```python
import torch

def _softmax(x, axis=-1):
    return torch.softmax(x, dim=axis)
def _gelu(x):
    return torch.nn.functional.gelu(x, approximate="tanh")
def _silu(x):
    return torch.nn.functional.silu(x)
def _sigmoid(x):
    return torch.sigmoid(x)
def _layernorm(x, axis=-1, eps=1e-5):
    m = x.mean(dim=axis, keepdim=True)
    v = x.var(dim=axis, keepdim=True, unbiased=False)
    return (x - m) / torch.sqrt(v + eps)
def _scatter_add(tgt, idx, src, axis=0):
    return tgt.clone().index_add_(axis, idx, src)
def _segment_sum(src, seg, n):
    out = torch.zeros((n,) + src.shape[1:], dtype=src.dtype)
    return out.index_add_(0, seg, src)
def _pad(x, pw):
    flat = tuple(v for pair in reversed(pw) for v in pair)
    return torch.nn.functional.pad(x, flat)

kernel_name = 'interleave_layers__sliding_local_dilated__dense_activation_squared_relu__prompt'

def setup_inputs():

    _w = (lambda a: (lambda t: t.to(torch.bfloat16) if t.is_floating_point() else t)(torch.as_tensor(a)))
    def _A_setup():
        B, S, D = 1, 2048, 1024
        return {
            "x": _w((lambda *s: torch.randn(*s, dtype=torch.bfloat16) * 0.05)(B, S, D)),
            "wqkv": _w((lambda *s: torch.randn(*s, dtype=torch.bfloat16) * 0.05)(D, 3 * D)),
            "wo": _w((lambda *s: torch.randn(*s, dtype=torch.bfloat16) * 0.05)(D, D)),
        }
    _A_in = _A_setup()

    def _B_setup():
        B, S, D, F = 1, 2048, 1024, 3584
        return {
            "x": _w((lambda *s: torch.randn(*s, dtype=torch.bfloat16) * 0.05)(B, S, D)),
            "w1": _w((lambda *s: torch.randn(*s, dtype=torch.bfloat16) * 0.05)(D, F)),
            "b1": _w((lambda *s: torch.randn(*s, dtype=torch.bfloat16) * 0.2)(F)),
            "w2": _w((lambda *s: torch.randn(*s, dtype=torch.bfloat16) * 0.05)(F, D)),
            "b2": _w((lambda *s: torch.randn(*s, dtype=torch.bfloat16) * 1.6)(D)),
        }
    _B_in = _B_setup()

    return {
        "A_x": _A_in["x"],
        "A_wqkv": _A_in["wqkv"],
        "A_wo": _A_in["wo"],
        "B_x": _B_in["x"],
        "B_w1": _B_in["w1"],
        "B_b1": _B_in["b1"],
        "B_w2": _B_in["w2"],
        "B_b2": _B_in["b2"],
    }

def reference(A_x, A_wqkv, A_wo, B_x, B_w1, B_b1, B_w2, B_b2):
    def _A_ref(x, wqkv, wo):
        B, S, D = x.shape
        H = 32; Dh = D // H
        W, dil = 8, 2
        qkv = (lambda x, *d: x.permute(*d))((x @ wqkv).reshape(B, S, 3, H, Dh), 2, 0, 3, 1, 4)
        q, k, v = qkv[0], qkv[1], qkv[2]
        scale = Dh ** -0.5
        pos = torch.arange(S)
        off = torch.arange(W) * dil
        idx = pos[:, None] - off[None, :]
        mask = idx >= 0
        idx_c = torch.where(mask, idx, 0).reshape(-1)
        kg = k[:, :, idx_c, :].reshape(B, H, S, W, Dh)
        vg = v[:, :, idx_c, :].reshape(B, H, S, W, Dh)
        logits = torch.einsum("bhsd,bhswd->bhsw", q, kg) * scale
        logits = torch.where(mask[None, None, :, :], logits, -1e9)
        attn = _softmax(logits, axis=-1)
        o = torch.einsum("bhsw,bhswd->bhsd", attn, vg)
        return (lambda x, *d: x.permute(*d))(o, 0, 2, 1, 3).reshape(B, S, D) @ wo

    def _B_ref(x, w1, b1, w2, b2):
        h = torch.einsum("bsd,df->bsf", x, w1) + b1
        zero = torch.zeros_like(h)
        r = torch.where(h > zero, h, zero)
        h = r * r
        out = torch.einsum("bsf,fd->bsd", h, w2) + b2
        return out

    x = A_x
    x = x + _A_ref(_layernorm(x, axis=-1), A_wqkv, A_wo)
    x = x + _B_ref(_layernorm(x, axis=-1), B_w1, B_b1, B_w2, B_b2)
    x = x + _A_ref(_layernorm(x, axis=-1), A_wqkv, A_wo)
    x = x + _B_ref(_layernorm(x, axis=-1), B_w1, B_b1, B_w2, B_b2)
    return x
```

```cpp
#include <hip/hip_runtime.h>
#include <stdint.h>

typedef __attribute__((ext_vector_type(16))) unsigned u32x16;
typedef __attribute__((ext_vector_type(8))) unsigned u32x8;
typedef __attribute__((ext_vector_type(4))) unsigned u32x4;
typedef __attribute__((ext_vector_type(2))) unsigned u32x2;
typedef __attribute__((ext_vector_type(2))) __bf16 bf2_t;
typedef unsigned short u16;
typedef __amdgpu_buffer_rsrc_t rsrc_t;

constexpr unsigned FLAGS = 4096, RB = 4194304, REC = 346112, SX = 344064, WS_END = 92798976, DBG = 1048576;
constexpr int LDS_BYTES = 163840;
#define NEGB 0xff61b1e6u
#define FW(p, s, w) (FLAGS + (unsigned)(((p) * 64 + (s)) * 128 + (w) * 8))
#define SPARE(i) ((i) < 2 ? 22 + (i) : 47)
#define blk0(s) (RB + (unsigned)(p * 64 + (s) - 2) * REC)
#define TA(seg, hf, sub) (unsigned)((seg) * 4096 + (8 * lg + (li >> 2)) * 128 + ((((hf) * 4 + (li & 3)) ^ ((li >> 1) & 4)) << 4) + (sub))
#define TO(seg, hf, half) (unsigned)((seg) * 4096 + (8 * lg + (li >> 2)) * 128 + ((((hf) * 4 + (half) * 2 + ((li & 3) >> 1)) ^ ((li >> 1) & 4)) << 4) + (li & 1) * 8)
#define BIAS(ptr, n, i) ((unsigned)__builtin_amdgcn_raw_buffer_load_b16(__builtin_amdgcn_make_buffer_rsrc((void*)(ptr), 0, (n) * 2, 0x00020000), (i) * 2, 0, 0) << 16)
#define SPIN_CAP 200000

struct Args {
  const u16 *x, *wqkv, *wo, *w1, *b1, *w2, *b2;
  u16* out;
  char* ws;
};

__device__ __forceinline__ float rows_sum(float v) {
  const auto a = __builtin_amdgcn_permlane16_swap(__float_as_uint(v), __float_as_uint(v), false, false);
  const float m = __uint_as_float(a[0]) + __uint_as_float(a[1]);
  const auto b = __builtin_amdgcn_permlane32_swap(__float_as_uint(m), __float_as_uint(m), false, false);
  return __uint_as_float(b[0]) + __uint_as_float(b[1]);
}

__global__ __launch_bounds__(256, 1) void df_kernel(Args m) {
  const int w = __builtin_amdgcn_readfirstlane(threadIdx.x >> 6);
  const int bid = blockIdx.x, xc = bid & 7, j_ = bid >> 3;
  const unsigned long long tb = xc == 0 ? 0x3332313003020100ull : xc == 1 ? 0x3736353407060504ull : xc == 2 ? 0x2c2b151413121110ull : xc == 3 ? 0x162d1d1c1b1a1918ull : xc == 4 ? 0x172e232221201f1eull : xc == 5 ? 0x2f2a292827262524ull : xc == 6 ? 0x3b3a39380b0a0908ull : 0x3f3e3d3c0f0e0d0cull;
  const int slot = (int)(tb >> ((j_ & 7) * 8)) & 63, p = j_ >> 3;
  if (slot == 22 || slot == 23 || slot == 47) {
    int lane;
    asm volatile("v_mbcnt_lo_u32_b32 %0, -1, 0\n\tv_mbcnt_hi_u32_b32 %0, -1, %0" : "=v"(lane));
    const int lg = lane >> 4, li = lane & 15;
    const unsigned l16 = lane * 16;
    const rsrc_t rS = __builtin_amdgcn_make_buffer_rsrc((void*)m.ws, 0, WS_END, 0x00020000);
    const rsrc_t rX = __builtin_amdgcn_make_buffer_rsrc((void*)m.x, 0, 2048 * 1024 * 2, 0x00020000);
    u32x2 tag = {0u, __builtin_amdgcn_raw_buffer_load_b32(rS, 0, 0, 16)};
    const int u = (slot == 47 ? 2 : slot - 22) * 4 + w;
    const unsigned fa = lane ? 0x80000000u : FW(p, slot, w);
    const bf2_t one2 = {(__bf16)1.0f, (__bf16)1.0f};
    for (int b = u; b < 32; b += 12) {
      u32x4 v[32];
#pragma unroll
      for (int k = 0; k < 32; ++k) v[k] = __builtin_amdgcn_raw_buffer_load_b128(rX, li * 2048 + lg * 16 + k * 64, (p * 32 + 31 - b) * 32768, 0);
      if (b == u) {
        tag[0] += 0x100u; tag[1] += 0x9e3779b9u;
        __builtin_amdgcn_raw_buffer_store_b64((u32x2){tag[0] + u, tag[1]}, rS, fa, 0, 16);
      }
      float s = 0.f, q = 0.f;
      const unsigned rb = RB + (unsigned)(p * 64 + b) * REC;
#pragma unroll
      for (int k = 0; k < 32; ++k) {
        __builtin_amdgcn_raw_buffer_store_b128(v[k], rS, l16 + k * 1024, rb, 16);
#pragma unroll
        for (int e = 0; e < 4; ++e) {
          const unsigned xe = v[k][e];
          const bf2_t x = __builtin_bit_cast(bf2_t, xe);
          s = __builtin_amdgcn_fdot2_f32_bf16(x, one2, s, false);
          q = __builtin_amdgcn_fdot2_f32_bf16(x, x, q, false);
        }
      }
      s = rows_sum(s); q = rows_sum(q);
      __builtin_amdgcn_raw_buffer_store_b64((u32x2){lg ? 0u : __float_as_uint(s), lg ? 0u : __float_as_uint(q)}, rS, lane * 8 + SX, rb, 16);
      asm volatile("s_waitcnt vmcnt(0)" ::: "memory");
      __builtin_amdgcn_raw_buffer_store_b64((u32x2){tag[0] + (b + 12 < 32 ? b + 12 : 100), tag[1]}, rS, fa, 0, 16);
    }
    if (slot == 47 && p == 3 && w == 3) {
      for (int n = 0; n < SPIN_CAP; ++n) {
        bool bad = false;
#pragma unroll
        for (int i = 0; i < 8; ++i) {
          const u32x4 f = __builtin_amdgcn_raw_buffer_load_b128(rS, FLAGS + (i * 32 + (lane >> 1)) * 128 + (lane & 1) * 16, 0, 16);
          bad = bad || f[1] != tag[1] || f[3] != tag[1] || f[0] - tag[0] > 255u || f[2] - tag[0] > 255u;
        }
        if (__builtin_amdgcn_ballot_w64(bad) == 0ull) break;
        __builtin_amdgcn_s_sleep(64);
      }
      __builtin_amdgcn_raw_buffer_store_b32(tag[1], rS, lane ? 0x80000000u : 0u, 0, 16);
    }
    return;
  }
  const int lane = threadIdx.x & 63, lg = lane >> 4, li = lane & 15;
  const unsigned l16 = lane * 16;
  u32x16 c0 = 0, c1 = 0, c2 = 0, s0 = 0;
  u32x8 v0 = 0, s1 = 0;
  const u16* wp; unsigned wn;
  const u16 *rQp = m.wqkv, *rOp = m.wo, *r1p = m.w1, *r2p = m.w2;
  constexpr unsigned rQn = 1024 * 3072 * 2, rOn = 1024 * 1024 * 2, r1n = 1024 * 3584 * 2, r2n = r1n;
  const int rot = p * 1, wr = (w + rot) & 3;
  if (slot < 16 && !(w & 1)) {
    s0[12] = 0 + (0); wp = rQp; wn = rQn;
    s0[13] = blk0(-3) + 32768u + (slot * 2 + (w >> 1)) * 1024 - (131072u + (w >> 1) * 2048);
    s0[15] = w * 8 * 6144 + slot * 128 + rot * 1572864;
    s1[2] = 262144u + (p * 32 + slot * 2 + (w >> 1)) * 4096 - (131072u + (w >> 1) * 2048);
    s1[3] = p ? 262144u + ((p - 1) * 32 + slot * 2 + (w >> 1)) * 4096 - (131072u + (w >> 1) * 2048) : 0x80000000u;
    s1[4] = p ? 0u : NEGB;
    s1[5] = w * 8 * 6144 + slot * 128 + 6291456u;
    v0[0] = (lane >> 3) * 6144 + (((lane & 7) ^ ((lane >> 2) & 4)) << 4) + 0u;
    v0[1] = (lane >> 3) * 6144 + (((lane & 7) ^ ((lane >> 2) & 4)) << 4) + 2048u;
    v0[2] = (lane >> 3) * 6144 + (((lane & 7) ^ ((lane >> 2) & 4)) << 4) + 4096u;
    v0[4] = TA(0, w >> 1, 0);
    v0[5] = TA(0, w >> 1, 8);
    v0[6] = TA(2, w >> 1, 0);
    c0[0] = l16;
    c0[1] = 131072u + (w >> 1) * 2048 + l16;
    c0[2] = 139264u + li * 8;
    c0[3] = 139264u + lg * 32;
    c0[4] = (((4 * lg + 0) ^ li) & 1) == 0 && (4 * lg + 0 <= li) ? 0u : NEGB;
    c0[5] = (((4 * lg + 1) ^ li) & 1) == 0 && (4 * lg + 1 <= li) ? 0u : NEGB;
    c0[6] = (((4 * lg + 2) ^ li) & 1) == 0 && (4 * lg + 2 <= li) ? 0u : NEGB;
    c0[7] = (((4 * lg + 3) ^ li) & 1) == 0 && (4 * lg + 3 <= li) ? 0u : NEGB;
    c0[8] = (((4 * lg + 0) ^ li) & 1) == 0 && (4 * lg + 0 > li) ? 0u : NEGB;
    c0[9] = (((4 * lg + 1) ^ li) & 1) == 0 && (4 * lg + 1 > li) ? 0u : NEGB;
    c0[10] = (((4 * lg + 2) ^ li) & 1) == 0 && (4 * lg + 2 > li) ? 0u : NEGB;
    c0[11] = (((4 * lg + 3) ^ li) & 1) == 0 && (4 * lg + 3 > li) ? 0u : NEGB;
    c0[12] = (4 * lg + 0 + (li & 1) <= li) ? ((li & 1) ? 0xffff0000u : 0xffffu) : 0u;
    c0[13] = (4 * lg + 2 + (li & 1) <= li) ? ((li & 1) ? 0xffff0000u : 0xffffu) : 0u;
    c0[14] = (4 * lg + 0 + (li & 1) > li) ? ((li & 1) ? 0xffff0000u : 0xffffu) : 0u;
    c0[15] = (4 * lg + 2 + (li & 1) > li) ? ((li & 1) ? 0xffff0000u : 0xffffu) : 0u;
  } else if (slot < 16) {
    s0[12] = 4 + (w >> 1); wp = rQp; wn = rQn;
    s0[13] = blk0(-3);
    s0[14] = (w >> 1) * 16384;
    s0[15] = w * 8 * 6144 + slot * 128 + rot * 1572864;
    s1[5] = w * 8 * 6144 + slot * 128 + 6291456u;
    v0[0] = (lane >> 3) * 6144 + (((lane & 7) ^ ((lane >> 2) & 4)) << 4) + 0u;
    v0[1] = (lane >> 3) * 6144 + (((lane & 7) ^ ((lane >> 2) & 4)) << 4) + 2048u;
    v0[2] = (lane >> 3) * 6144 + (((lane & 7) ^ ((lane >> 2) & 4)) << 4) + 4096u;
    v0[4] = TA(1, w >> 1, 0);
    v0[5] = TA(1, w >> 1, 8);
    v0[6] = TA(2, w >> 1, 8);
    c0[0] = l16;
    c0[1] = lane ? 0x80000000u : 0u;
    c0[2] = 142336u + lane * 4;
    c0[3] = 131072u + (w >> 1) * 2048 + l16;
    c0[4] = 139264u + li * 8;
    c0[5] = 139264u + lg * 32;
    c0[6] = 1384448u + ((0 + (w >> 1) * 16384 + rot * 8192) & 32767) + l16;
    c0[7] = 1384448u + ((4096 + (w >> 1) * 16384 + rot * 8192) & 32767) + l16;
    c0[8] = 1384448u + ((8192 + (w >> 1) * 16384 + rot * 8192) & 32767) + l16;
    c0[9] = 1384448u + ((12288 + (w >> 1) * 16384 + rot * 8192) & 32767) + l16;
    c0[10] = lane >= 16 && lane < 28 ? FW(p, SPARE((lane - 16) >> 2), lane & 3) : (lane >> 1) == 14 && p ? FW(p - 1, slot, (lane & 1) * 2) : FW(p, 43 + ((lane & 15) >> 2), lane & 3);
    c0[11] = lane >= 16 && lane < 28 ? 1u : (unsigned)-31;
    c0[12] = 1036288u + lane * 8;
  } else if (slot < 22) {
    s0[12] = 8 + (w < 2 ? w : 2); wp = rOp; wn = rOn;
    s0[13] = blk0(-2);
    s0[14] = w * 8192;
    s0[15] = w * 8 * 2048 + (slot - 16) * 384 + rot * 524288;
    s1[5] = w * 8 * 2048 + (slot - 16) * 384 + 2097152u;
    v0[0] = (lane >> 3) * 2048 + (((lane & 7) ^ ((lane >> 2) & 4)) << 4) + ((slot - 16) * 6 + 0 < 32 ? 0u : 0x80000000u);
    v0[1] = (lane >> 3) * 2048 + (((lane & 7) ^ ((lane >> 2) & 4)) << 4) + ((slot - 16) * 6 + 2 < 32 ? 128u : 0x80000000u);
    v0[2] = (lane >> 3) * 2048 + (((lane & 7) ^ ((lane >> 2) & 4)) << 4) + ((slot - 16) * 6 + 4 < 32 ? 256u : 0x80000000u);
    v0[4] = TA(((w >> 1) * 3 + (w & 1)) >> 1, ((w >> 1) * 3 + (w & 1)) & 1, 0);
    v0[5] = TA(((w >> 1) * 3 + (w & 1)) >> 1, ((w >> 1) * 3 + (w & 1)) & 1, 8);
    v0[6] = TO(((w >> 1) * 3 + 2) >> 1, ((w >> 1) * 3 + 2) & 1, w & 1);
    c0[0] = l16;
    c0[1] = lane ? 0x80000000u : 0u;
    c0[2] = 142336u + lane * 4;
    c0[3] = ((slot - 16) * 6 + (w >> 1) * 3 + (w & 1)) < 32 ? 411648u + ((slot - 16) * 6 + (w >> 1) * 3 + (w & 1)) * 1024 + l16 : 0x80000000u;
    c0[4] = ((slot - 16) * 6 + (w >> 1) * 3 + 2) < 32 && !(lg & 1) ? 411648u + ((slot - 16) * 6 + (w >> 1) * 3 + 2) * 1024 + ((w & 1) * 2 + (lg >> 1)) * 256 + li * 16 : 0x80000000u;
    c0[5] = 1071104u + wr * 8192 + l16;
    c0[6] = 1075200u + wr * 8192 + l16;
    c0[7] = FW(p, (lane & 31) >> 1, (lane & 1) * 2);
    c0[8] = 1u;
    c0[9] = ((slot - 16) * 6 + (w >> 1) * 3 + (w & 1)) < 32 ? 1384448u + ((slot - 16) * 6 + (w >> 1) * 3 + (w & 1)) * 1024 + l16 : 0x80000000u;
    c0[10] = ((slot - 16) * 6 + (w >> 1) * 3 + 2) < 32 ? 1384448u + ((slot - 16) * 6 + (w >> 1) * 3 + 2) * 1024 + ((w & 1) * 2 + (lg >> 1)) * 256 + li * 16 + (lg & 1) * 8 : 0x80000000u;
    c0[11] = 140288u + ((li * 2 + (lg >> 1)) * 8 + w * 2 + (lg & 1)) * 4;
    c0[12] = 140288u + l16;
    c0[13] = (lane & 1) ? 0x80000000u : 345088u + (slot - 16) * 128 + (lane >> 1) * 4;
  } else if (slot < 43) {
    s0[12] = 12 + (w < 2 ? w : 2); wp = r1p; wn = r1n;
    s0[13] = blk0(-2);
    s0[14] = w * 8192;
    s0[15] = w * 8 * 7168 + (slot - 24) * 384 + rot * 1835008;
    s1[5] = w * 8 * 7168 + (slot - 24) * 384 + 7340032u;
    v0[0] = (lane >> 3) * 7168 + (((lane & 7) ^ ((lane >> 2) & 4)) << 4) + ((slot - 24) * 6 + 0 < 112 ? 0u : 0x80000000u);
    v0[1] = (lane >> 3) * 7168 + (((lane & 7) ^ ((lane >> 2) & 4)) << 4) + ((slot - 24) * 6 + 2 < 112 ? 128u : 0x80000000u);
    v0[2] = (lane >> 3) * 7168 + (((lane & 7) ^ ((lane >> 2) & 4)) << 4) + ((slot - 24) * 6 + 4 < 112 ? 256u : 0x80000000u);
    v0[4] = TA(((w >> 1) * 3 + (w & 1)) >> 1, ((w >> 1) * 3 + (w & 1)) & 1, 0);
    v0[5] = TA(((w >> 1) * 3 + (w & 1)) >> 1, ((w >> 1) * 3 + (w & 1)) & 1, 8);
    v0[6] = TO(((w >> 1) * 3 + 2) >> 1, ((w >> 1) * 3 + 2) & 1, w & 1);
    c0[0] = l16;
    c0[1] = lane ? 0x80000000u : 0u;
    c0[2] = 142336u + lane * 4;
    c0[3] = ((slot - 24) * 6 + (w >> 1) * 3 + (w & 1)) < 112 ? 444416u + ((slot - 24) * 6 + (w >> 1) * 3 + (w & 1)) * 1024 + l16 : 0x80000000u;
    c0[4] = ((slot - 24) * 6 + (w >> 1) * 3 + 2) < 112 && !(lg & 1) ? 444416u + ((slot - 24) * 6 + (w >> 1) * 3 + 2) * 1024 + ((w & 1) * 2 + (lg >> 1)) * 256 + li * 16 : 0x80000000u;
    c0[5] = 1103872u + wr * 8192 + l16;
    c0[6] = 1107968u + wr * 8192 + l16;
    c0[7] = lane < 24 ? FW(p, 16 + (lane >> 2), lane & 3) : FW(p, 16, 0);
    c0[8] = 1u;
    c0[9] = 139264u + li * 8;
    c0[10] = 1037312u + lane * 8;
    c0[11] = lane < 32 ? 1037824u + lane * 8 : 0x80000000u;
    c0[12] = BIAS(m.b1, 3584, ((slot - 24) * 6 + (w >> 1) * 3 + (w & 1)) * 32 + lg * 8 + 0);
    c0[13] = BIAS(m.b1, 3584, ((slot - 24) * 6 + (w >> 1) * 3 + (w & 1)) * 32 + lg * 8 + 1);
    c0[14] = BIAS(m.b1, 3584, ((slot - 24) * 6 + (w >> 1) * 3 + (w & 1)) * 32 + lg * 8 + 2);
    c0[15] = BIAS(m.b1, 3584, ((slot - 24) * 6 + (w >> 1) * 3 + (w & 1)) * 32 + lg * 8 + 3);
    c1[0] = BIAS(m.b1, 3584, ((slot - 24) * 6 + (w >> 1) * 3 + (w & 1)) * 32 + lg * 8 + 4);
    c1[1] = BIAS(m.b1, 3584, ((slot - 24) * 6 + (w >> 1) * 3 + (w & 1)) * 32 + lg * 8 + 5);
    c1[2] = BIAS(m.b1, 3584, ((slot - 24) * 6 + (w >> 1) * 3 + (w & 1)) * 32 + lg * 8 + 6);
    c1[3] = BIAS(m.b1, 3584, ((slot - 24) * 6 + (w >> 1) * 3 + (w & 1)) * 32 + lg * 8 + 7);
    c1[4] = BIAS(m.b1, 3584, ((slot - 24) * 6 + (w >> 1) * 3 + 2) * 32 + (w & 1) * 16 + lg * 4 + 0);
    c1[5] = BIAS(m.b1, 3584, ((slot - 24) * 6 + (w >> 1) * 3 + 2) * 32 + (w & 1) * 16 + lg * 4 + 1);
    c1[6] = BIAS(m.b1, 3584, ((slot - 24) * 6 + (w >> 1) * 3 + 2) * 32 + (w & 1) * 16 + lg * 4 + 2);
    c1[7] = BIAS(m.b1, 3584, ((slot - 24) * 6 + (w >> 1) * 3 + 2) * 32 + (w & 1) * 16 + lg * 4 + 3);
  } else if (slot >= 48) {
    s0[12] = 16 + (w ? 1 : 0); wp = r2p; wn = r2n;
    s0[13] = blk0(-2);
    s0[14] = w * 6144;
    s0[15] = (((slot - 48) >> 2) * 768 + w * 8) * 2048 + (slot & 3) * 512 + rot * 393216;
    s1[5] = (((slot - 48) >> 2) * 768 + w * 8) * 2048 + (slot & 3) * 512 + 1572864u;
    v0[0] = (lane >> 3) * 2048 + (((lane & 7) ^ ((lane >> 2) & 4)) << 4) + 0u;
    v0[1] = (lane >> 3) * 2048 + (((lane & 7) ^ ((lane >> 2) & 4)) << 4) + 128u;
    v0[2] = (lane >> 3) * 2048 + (((lane & 7) ^ ((lane >> 2) & 4)) << 4) + 256u;
    v0[3] = (lane >> 3) * 2048 + (((lane & 7) ^ ((lane >> 2) & 4)) << 4) + 384u;
    v0[4] = TA(w, 0, 0);
    v0[5] = TA(w, 0, 8);
    v0[6] = TA(w, 1, 0);
    v0[7] = TA(w, 1, 8);
    c0[0] = l16;
    c0[1] = lane ? 0x80000000u : 0u;
    c0[2] = 142336u + lane * 4;
    c0[3] = 1136640u + ((slot - 48) >> 2) * 24576 + wr * 6144 + l16;
    c0[4] = 1140736u + ((slot - 48) >> 2) * 24576 + wr * 6144 + l16;
    c0[5] = FW(p, 24 + ((slot - 48) & ~3) + ((lane & 15) >> 2), lane & 3);
    c0[6] = 1u;
    c0[7] = 559104u + (slot - 48) * 8192 + w * 2048 + l16;
  } else if (true) {
    s0[12] = 20 + (w < 2 ? w : 2); wp = r2p; wn = r2n;
    s0[13] = blk0(-3);
    s0[14] = w * 4096;
    s0[15] = (3072 + w * 8) * 2048 + (slot - 43) * 512 + rot * 262144;
    s1[2] = (p * 32 + 31) * 32768;
    s1[5] = (3072 + w * 8) * 2048 + (slot - 43) * 512 + 1048576u;
    v0[0] = (lane >> 3) * 2048 + (((lane & 7) ^ ((lane >> 2) & 4)) << 4) + 0u;
    v0[1] = (lane >> 3) * 2048 + (((lane & 7) ^ ((lane >> 2) & 4)) << 4) + 128u;
    v0[2] = (lane >> 3) * 2048 + (((lane & 7) ^ ((lane >> 2) & 4)) << 4) + 256u;
    v0[3] = (lane >> 3) * 2048 + (((lane & 7) ^ ((lane >> 2) & 4)) << 4) + 384u;
    v0[4] = TA(w, 0, 0);
    v0[5] = TA(w, 0, 8);
    v0[6] = TA(w, 1, 0);
    v0[7] = TA(w, 1, 8);
    c0[0] = l16;
    c0[1] = lane ? 0x80000000u : 0u;
    c0[2] = 142336u + lane * 4;
    c0[3] = 1581056u + wr * 4096 + l16;
    c0[4] = (lane >> 4) == 1 ? FW(p, 48 + (lane & 12) + slot - 43, lane & 3) : lane < 12 ? FW(p, 40 + (lane >> 2), lane & 3) : FW(p, 40, 0);
    c0[5] = (lane >> 4) == 1 ? (unsigned)-1 : 1u;
    c0[6] = 905216u + (slot - 43) * 8192 + w * 2048 + l16;
    c0[7] = 937984u + (slot - 43) * 8192 + w * 2048 + l16;
    c0[8] = 970752u + (slot - 43) * 8192 + w * 2048 + l16;
    c0[9] = 1003520u + (slot - 43) * 8192 + w * 2048 + l16;
    c0[10] = 1103872u + (slot - 43) * 8192 + w * 2048 + l16;
    c0[11] = 11421696u + (slot - 43) * 8192 + w * 2048 + l16;
    c0[12] = (lane >> 3) * 2048 + (lane & 7) * 16 + (slot - 43) * 512 + w * 128;
    c0[13] = ((lane >> 3) + 8) * 2048 + (lane & 7) * 16 + (slot - 43) * 512 + w * 128;
    c0[14] = 131072u + w * 2048 + li * 128 + (((0 + lg) ^ (li & 7)) << 4);
    c0[15] = 131072u + w * 2048 + li * 128 + (((4 + lg) ^ (li & 7)) << 4);
    c1[0] = 131072u + w * 2048 + (lane >> 3) * 128 + (((lane ^ (lane >> 3)) & 7) << 4);
    c1[1] = 140288u + ((li * 2 + (lg >> 1)) * 8 + w * 2 + (lg & 1)) * 4;
    c1[2] = 140288u + l16;
    c1[3] = (lane & 1) ? 0x80000000u : 11419648u + (slot - 43) * 128 + (lane >> 1) * 4;
    c1[4] = BIAS(m.b2, 1024, (slot - 43) * 256 + w * 64 + 0 + lg * 8);
    c1[5] = BIAS(m.b2, 1024, (slot - 43) * 256 + w * 64 + 1 + lg * 8);
    c1[6] = BIAS(m.b2, 1024, (slot - 43) * 256 + w * 64 + 2 + lg * 8);
    c1[7] = BIAS(m.b2, 1024, (slot - 43) * 256 + w * 64 + 3 + lg * 8);
    c1[8] = BIAS(m.b2, 1024, (slot - 43) * 256 + w * 64 + 4 + lg * 8);
    c1[9] = BIAS(m.b2, 1024, (slot - 43) * 256 + w * 64 + 5 + lg * 8);
    c1[10] = BIAS(m.b2, 1024, (slot - 43) * 256 + w * 64 + 6 + lg * 8);
    c1[11] = BIAS(m.b2, 1024, (slot - 43) * 256 + w * 64 + 7 + lg * 8);
    c1[12] = BIAS(m.b2, 1024, (slot - 43) * 256 + w * 64 + 32 + lg * 8);
    c1[13] = BIAS(m.b2, 1024, (slot - 43) * 256 + w * 64 + 33 + lg * 8);
    c1[14] = BIAS(m.b2, 1024, (slot - 43) * 256 + w * 64 + 34 + lg * 8);
    c1[15] = BIAS(m.b2, 1024, (slot - 43) * 256 + w * 64 + 35 + lg * 8);
    c2[0] = BIAS(m.b2, 1024, (slot - 43) * 256 + w * 64 + 36 + lg * 8);
    c2[1] = BIAS(m.b2, 1024, (slot - 43) * 256 + w * 64 + 37 + lg * 8);
    c2[2] = BIAS(m.b2, 1024, (slot - 43) * 256 + w * 64 + 38 + lg * 8);
    c2[3] = BIAS(m.b2, 1024, (slot - 43) * 256 + w * 64 + 39 + lg * 8);
    c2[4] = (lg == (li >> 2) && ((li & 3) >> 1) + 0 == 0) ? ((li & 1) ? 0x3f800000u : 0x3f80u) : 0u;
    c2[5] = (lg == (li >> 2) && ((li & 3) >> 1) + 0 == 1) ? ((li & 1) ? 0x3f800000u : 0x3f80u) : 0u;
    c2[6] = (lg == (li >> 2) && ((li & 3) >> 1) + 0 == 2) ? ((li & 1) ? 0x3f800000u : 0x3f80u) : 0u;
    c2[7] = (lg == (li >> 2) && ((li & 3) >> 1) + 0 == 3) ? ((li & 1) ? 0x3f800000u : 0x3f80u) : 0u;
    c2[8] = (lg == (li >> 2) && ((li & 3) >> 1) + 2 == 0) ? ((li & 1) ? 0x3f800000u : 0x3f80u) : 0u;
    c2[9] = (lg == (li >> 2) && ((li & 3) >> 1) + 2 == 1) ? ((li & 1) ? 0x3f800000u : 0x3f80u) : 0u;
    c2[10] = (lg == (li >> 2) && ((li & 3) >> 1) + 2 == 2) ? ((li & 1) ? 0x3f800000u : 0x3f80u) : 0u;
    c2[11] = (lg == (li >> 2) && ((li & 3) >> 1) + 2 == 3) ? ((li & 1) ? 0x3f800000u : 0x3f80u) : 0u;
  }
  s0[0] = (unsigned)(size_t)m.ws; s0[1] = (unsigned)((size_t)m.ws >> 32) & 0xffffu; s0[2] = WS_END; s0[3] = 0x00020000;
  s0[4] = (unsigned)(size_t)wp; s0[5] = (unsigned)((size_t)wp >> 32) & 0xffffu; s0[6] = wn; s0[7] = 0x00020000;
  s0[8] = (unsigned)(size_t)m.out; s0[9] = (unsigned)((size_t)m.out >> 32) & 0xffffu; s0[10] = 2048 * 1024 * 2; s0[11] = 0x00020000;
  s1[0] = FW(p, slot, w); s1[1] = w * 1024; s1[7] = DBG + ((p * 64 + slot) * 4 + w) * 1024;
#pragma unroll
  for (int i = 0; i < 16; ++i) s0[i] = __builtin_amdgcn_readfirstlane(s0[i]);
#pragma unroll
  for (int i = 0; i < 8; ++i) s1[i] = __builtin_amdgcn_readfirstlane(s1[i]);
  asm volatile(
R"(
.macro mav d,a,b
v_mfma_f32_16x16x32_bf16 v[\d:\d+3], a[\a:\a+3], v[\b:\b+3], v[\d:\d+3]
.endm
.macro Ta d,a,o
ds_read_b64_tr_b16 a[\d:\d+1], v\a offset:\o
ds_read_b64_tr_b16 a[\d+2:\d+3], v\a offset:\o+512
.endm
.macro mva d,a,b
v_mfma_f32_16x16x32_bf16 v[\d:\d+3], v[\a:\a+3], a[\b:\b+3], v[\d:\d+3]
.endm
.macro mvv d,a,b
v_mfma_f32_16x16x32_bf16 v[\d:\d+3], v[\a:\a+3], v[\b:\b+3], v[\d:\d+3]
.endm
.macro Tv d,a,o
ds_read_b64_tr_b16 v[\d:\d+1], v\a offset:\o
ds_read_b64_tr_b16 v[\d+2:\d+3], v\a offset:\o+512
.endm
.macro D v,o
buffer_load_dwordx4 v\v, s[36:39], s49 offen offset:\o sc1 lds
.endm
.macro E v,o
s_add_u32 m0, s53, \o
s_nop 0
buffer_load_dwordx4 v\v, s[40:43], s51 offen lds
.endm
s_nop 4
s_mov_b32 s61, 200000
s_mov_b64 s[62:63], 255
s_mov_b32 s64, 0x3f803f80
s_mov_b32 s65, 0x3e8293ee
s_lshr_b32 s60, s48, 2
Lr0:
s_cmp_eq_u32 s60, 0
s_cbranch_scc0 Lr1
.macro R d,o
ds_read_b128 v[\d:\d+3], v80 offset:\o
.endm
buffer_load_dword v117, off, s[36:39], 0 sc1
s_getpc_b64 s[66:67]
LpQA:
v_lshlrev_b32 v8, 3, v80
s_and_b32 s60, s53, 2048
s_lshl_b32 s60, s60, 2
v_add_u32 v8, s60, v8
.set cn, 0
.rept 2
v_add_u32 v9, cn, v8
v_min_u32 v9, LeQA - LpQA - 4, v9
global_load_dword v10, v9, s[66:67]
.set cn, cn + 16384
.endr
E 0,0
E 1,4096
E 2,8192
s_add_u32 s51, s51, 196608
E 0,12288
E 1,16384
E 2,20480
s_add_u32 s51, s51, 196608
E 0,24576
E 1,28672
E 2,32768
s_add_u32 s51, s51, 196608
E 0,36864
E 1,40960
E 2,45056
s_add_u32 s51, s51, 196608
E 0,49152
E 1,53248
E 2,57344
s_add_u32 s51, s51, 196608
E 0,61440
E 1,65536
E 2,69632
s_add_u32 s51, s51, 196608
E 0,73728
E 1,77824
E 2,81920
s_add_u32 s51, s51, 196608
E 0,86016
E 1,90112
E 2,94208
s_add_u32 s51, s51, 196608
s_cmp_ge_u32 s51, s57
s_cselect_b32 s60, 6291456, 0
s_sub_u32 s51, s51, s60
E 0,98304
E 1,102400
E 2,106496
s_add_u32 s51, s51, 196608
E 0,110592
E 1,114688
E 2,118784
s_add_u32 s51, s51, 196608
E 0,122880
E 1,126976
E 2,131072
s_add_u32 s51, s51, 196608
E 0,135168
E 1,139264
E 2,143360
s_add_u32 s51, s51, 196608
v_mov_b32 v24, s64
v_mov_b32 v25, s64
v_mov_b32 v26, s64
v_mov_b32 v27, s64
s_waitcnt vmcnt(30)
s_barrier
v_mov_b32 v116, 0x100
v_add_u32 v117, 0x9e3779b9, v117
buffer_store_dwordx2 v[116:117], off, s[36:39], s52 sc1
Ta 0,4,0
Ta 4,5,0
Ta 8,6,0
Ta 12,4,12288
Ta 16,5,12288
Ta 20,6,12288
s_waitcnt lgkmcnt(0)
s_waitcnt vmcnt(25)
s_barrier
Ta 24,4,24576
Ta 28,5,24576
Ta 32,6,24576
Ta 36,4,36864
Ta 40,5,36864
Ta 44,6,36864
E 0,0
E 1,4096
E 2,8192
s_add_u32 s51, s51, 196608
E 0,12288
E 1,16384
E 2,20480
s_add_u32 s51, s51, 196608
v_mfma_f32_16x16x32_bf16 v[48:51], a[0:3], v[24:27], 0
v_mfma_f32_16x16x32_bf16 v[52:55], a[4:7], v[24:27], 0
v_mfma_f32_16x16x32_bf16 v[56:59], v[24:27], a[8:11], 0
mav 48,12,24
mav 52,16,24
mva 56,24,20
s_waitcnt lgkmcnt(0)
s_waitcnt vmcnt(25)
s_barrier
v_add_u32 v4, 49152, v4
v_add_u32 v5, 49152, v5
v_add_u32 v6, 49152, v6
Ta 48,4,0
Ta 52,5,0
Ta 56,6,0
Ta 60,4,12288
Ta 64,5,12288
Ta 68,6,12288
E 0,24576
E 1,28672
E 2,32768
s_add_u32 s51, s51, 196608
E 0,36864
E 1,40960
E 2,45056
s_add_u32 s51, s51, 196608
s_cmp_ge_u32 s51, s57
s_cselect_b32 s60, 6291456, 0
s_sub_u32 s51, s51, s60
mav 48,24,24
mav 52,28,24
mva 56,24,32
mav 48,36,24
mav 52,40,24
mva 56,24,44
s_waitcnt lgkmcnt(0)
s_waitcnt vmcnt(25)
s_barrier
Ta 72,4,24576
Ta 76,5,24576
Ta 80,6,24576
Ta 84,4,36864
Ta 88,5,36864
Ta 92,6,36864
E 0,49152
E 1,53248
E 2,57344
s_add_u32 s51, s51, 196608
E 0,61440
E 1,65536
E 2,69632
s_add_u32 s51, s51, 196608
mav 48,48,24
mav 52,52,24
mva 56,24,56
mav 48,60,24
mav 52,64,24
mva 56,24,68
s_waitcnt lgkmcnt(0)
s_waitcnt vmcnt(25)
s_barrier
v_add_u32 v4, 49152, v4
v_add_u32 v5, 49152, v5
v_add_u32 v6, 49152, v6
Ta 96,4,0
Ta 100,5,0
Ta 104,6,0
Ta 108,4,12288
Ta 112,5,12288
Ta 116,6,12288
E 0,73728
E 1,77824
E 2,81920
s_add_u32 s51, s51, 196608
E 0,86016
E 1,90112
E 2,94208
s_add_u32 s51, s51, 196608
mav 48,72,24
mav 52,76,24
mva 56,24,80
mav 48,84,24
mav 52,88,24
mva 56,24,92
s_waitcnt lgkmcnt(0)
s_waitcnt vmcnt(25)
s_barrier
Ta 120,4,24576
Ta 124,5,24576
Ta 128,6,24576
Ta 132,4,36864
Ta 136,5,36864
Ta 140,6,36864
E 0,98304
E 1,102400
E 2,106496
s_add_u32 s51, s51, 196608
E 0,110592
E 1,114688
E 2,118784
s_add_u32 s51, s51, 196608
mav 48,96,24
mav 52,100,24
mva 56,24,104
mav 48,108,24
mav 52,112,24
mva 56,24,116
s_waitcnt lgkmcnt(0)
s_waitcnt vmcnt(24)
s_barrier
v_add_u32 v4, -98304, v4
v_add_u32 v5, -98304, v5
v_add_u32 v6, -98304, v6
Ta 144,4,0
Ta 148,5,0
Ta 152,6,0
Ta 156,4,12288
Ta 160,5,12288
Ta 164,6,12288
E 0,122880
E 1,126976
E 2,131072
s_add_u32 s51, s51, 196608
E 0,135168
E 1,139264
E 2,143360
s_add_u32 s51, s51, 196608
s_cmp_ge_u32 s51, s57
s_cselect_b32 s60, 6291456, 0
s_sub_u32 s51, s51, s60
mav 48,120,24
mav 52,124,24
mva 56,24,128
mav 48,132,24
mav 52,136,24
mva 56,24,140
s_waitcnt lgkmcnt(0)
s_waitcnt vmcnt(24)
s_barrier
Ta 168,4,24576
Ta 172,5,24576
Ta 176,6,24576
Ta 180,4,36864
Ta 184,5,36864
Ta 188,6,36864
E 0,0
E 1,4096
E 2,8192
s_add_u32 s51, s51, 196608
E 0,12288
E 1,16384
E 2,20480
s_add_u32 s51, s51, 196608
mav 48,144,24
mav 52,148,24
mva 56,24,152
mav 48,156,24
mav 52,160,24
mva 56,24,164
s_waitcnt lgkmcnt(0)
s_waitcnt vmcnt(24)
s_barrier
v_add_u32 v4, 49152, v4
v_add_u32 v5, 49152, v5
v_add_u32 v6, 49152, v6
Ta 192,4,0
Ta 196,5,0
Ta 200,6,0
Ta 204,4,12288
Ta 208,5,12288
Ta 212,6,12288
E 0,24576
E 1,28672
E 2,32768
s_add_u32 s51, s51, 196608
E 0,36864
E 1,40960
E 2,45056
s_add_u32 s51, s51, 196608
mav 48,168,24
mav 52,172,24
mva 56,24,176
mav 48,180,24
mav 52,184,24
mva 56,24,188
s_waitcnt lgkmcnt(0)
s_waitcnt vmcnt(24)
s_barrier
Ta 216,4,24576
Ta 220,5,24576
Ta 224,6,24576
Ta 228,4,36864
Ta 232,5,36864
Ta 236,6,36864
E 0,49152
E 1,53248
E 2,57344
s_add_u32 s51, s51, 196608
E 0,61440
E 1,65536
E 2,69632
s_add_u32 s51, s51, 196608
mav 48,192,24
mav 52,196,24
mva 56,24,200
mav 48,204,24
mav 52,208,24
mva 56,24,212
s_waitcnt lgkmcnt(0)
s_waitcnt vmcnt(24)
s_barrier
v_add_u32 v4, 49152, v4
v_add_u32 v5, 49152, v5
v_add_u32 v6, 49152, v6
Ta 240,4,0
Ta 244,5,0
Ta 248,6,0
Ta 252,4,12288
Tv 128,5,12288
Tv 132,6,12288
E 0,73728
E 1,77824
E 2,81920
s_add_u32 s51, s51, 196608
E 0,86016
E 1,90112
E 2,94208
s_add_u32 s51, s51, 196608
s_cmp_ge_u32 s51, s57
s_cselect_b32 s60, 6291456, 0
s_sub_u32 s51, s51, s60
mav 48,216,24
mav 52,220,24
mva 56,24,224
mav 48,228,24
mav 52,232,24
mva 56,24,236
s_waitcnt lgkmcnt(0)
s_waitcnt vmcnt(24)
s_barrier
Tv 136,4,24576
Tv 140,5,24576
Tv 144,6,24576
Tv 148,4,36864
Tv 152,5,36864
Tv 156,6,36864
mav 48,240,24
mav 52,244,24
mva 56,24,248
mav 48,252,24
mvv 52,128,24
mvv 56,24,132
s_waitcnt lgkmcnt(0)
s_waitcnt vmcnt(18)
s_barrier
v_add_u32 v4, -98304, v4
v_add_u32 v5, -98304, v5
v_add_u32 v6, -98304, v6
Tv 160,4,0
Tv 164,5,0
Tv 168,6,0
Tv 172,4,12288
Tv 176,5,12288
Tv 180,6,12288
mvv 48,136,24
mvv 52,140,24
mvv 56,24,144
mvv 48,148,24
mvv 52,152,24
mvv 56,24,156
s_waitcnt lgkmcnt(0)
s_waitcnt vmcnt(12)
s_barrier
Tv 184,4,24576
Tv 188,5,24576
Tv 192,6,24576
Tv 196,4,36864
Tv 200,5,36864
Tv 204,6,36864
mvv 48,160,24
mvv 52,164,24
mvv 56,24,168
mvv 48,172,24
mvv 52,176,24
mvv 56,24,180
s_waitcnt lgkmcnt(0)
s_waitcnt vmcnt(6)
s_barrier
v_add_u32 v4, 49152, v4
v_add_u32 v5, 49152, v5
v_add_u32 v6, 49152, v6
Tv 208,4,0
Tv 212,5,0
Tv 216,6,0
Tv 220,4,12288
Tv 224,5,12288
Tv 228,6,12288
mvv 48,184,24
mvv 52,188,24
mvv 56,24,192
mvv 48,196,24
mvv 52,200,24
mvv 56,24,204
s_waitcnt lgkmcnt(0)
s_waitcnt vmcnt(0)
s_barrier
Tv 232,4,24576
Tv 236,5,24576
Tv 240,6,24576
Tv 244,4,36864
Tv 248,5,36864
Tv 252,6,36864
mvv 48,208,24
mvv 52,212,24
mvv 56,24,216
mvv 48,220,24
mvv 52,224,24
mvv 56,24,228
s_waitcnt lgkmcnt(0)
mvv 48,232,24
mvv 52,236,24
mvv 56,24,240
mvv 48,244,24
mvv 52,248,24
mvv 56,24,252
s_waitcnt vmcnt(0)
s_barrier
s_nop 7
v_mov_b32 v118, v56
s_and_b32 s60, s48, 3
LvQA:
s_add_u32 s49, s49, 346112
s_barrier
s_add_u32 s49, s49, 346112
s_barrier
s_add_u32 s49, s49, 346112
s_barrier
R 24,0
R 28,1024
R 32,2048
R 36,3072
s_waitcnt lgkmcnt(2)
v_mfma_f32_16x16x32_bf16 v[0:3], a[0:3], v[24:27], 0
R 40,4096
v_mfma_f32_16x16x32_bf16 v[4:7], a[4:7], v[24:27], 0
v_mfma_f32_16x16x32_bf16 v[8:11], v[24:27], a[8:11], 0
mav 0,12,28
R 44,5120
mav 4,16,28
mva 8,28,20
s_waitcnt lgkmcnt(2)
mav 0,24,32
R 24,6144
mav 4,28,32
mva 8,32,32
mav 0,36,36
R 28,7168
mav 4,40,36
mva 8,36,44
s_waitcnt lgkmcnt(2)
mav 0,48,40
R 32,8192
mav 4,52,40
mva 8,40,56
mav 0,60,44
R 36,9216
mav 4,64,44
mva 8,44,68
s_waitcnt lgkmcnt(2)
mav 0,72,24
R 40,10240
mav 4,76,24
mva 8,24,80
mav 0,84,28
R 44,11264
mav 4,88,28
mva 8,28,92
s_waitcnt lgkmcnt(2)
mav 0,96,32
R 24,12288
mav 4,100,32
mva 8,32,104
mav 0,108,36
R 28,13312
mav 4,112,36
mva 8,36,116
s_waitcnt lgkmcnt(2)
mav 0,120,40
R 32,14336
mav 4,124,40
mva 8,40,128
mav 0,132,44
R 36,15360
mav 4,136,44
mva 8,44,140
s_waitcnt lgkmcnt(2)
mav 0,144,24
R 40,16384
mav 4,148,24
mva 8,24,152
mav 0,156,28
R 24,17408
mav 4,160,28
mva 8,28,164
s_waitcnt lgkmcnt(2)
mav 0,168,32
R 28,18432
mav 4,172,32
mva 8,32,176
mav 0,180,36
R 32,19456
mav 4,184,36
mva 8,36,188
s_waitcnt lgkmcnt(2)
mav 0,192,40
R 36,20480
mav 4,196,40
mva 8,40,200
mav 0,204,24
R 40,21504
mav 4,208,24
mva 8,24,212
s_waitcnt lgkmcnt(2)
mav 0,216,28
R 24,22528
mav 4,220,28
mva 8,28,224
mav 0,228,32
R 28,23552
mav 4,232,32
mva 8,32,236
s_waitcnt lgkmcnt(2)
mav 0,240,36
R 32,24576
mav 4,244,36
mva 8,36,248
mav 0,252,40
R 36,25600
mvv 4,128,40
mvv 8,40,132
s_waitcnt lgkmcnt(2)
mvv 0,136,24
R 40,26624
mvv 4,140,24
mvv 8,24,144
mvv 0,148,28
R 24,27648
mvv 4,152,28
mvv 8,28,156
s_waitcnt lgkmcnt(2)
mvv 0,160,32
R 28,28672
mvv 4,164,32
mvv 8,32,168
mvv 0,172,36
R 32,29696
mvv 4,176,36
mvv 8,36,180
s_waitcnt lgkmcnt(2)
mvv 0,184,40
R 36,30720
mvv 4,188,40
mvv 8,40,192
mvv 0,196,24
R 40,31744
mvv 4,200,24
s_add_u32 s49, s49, 346112
mvv 8,24,204
s_barrier
s_waitcnt lgkmcnt(2)
mvv 0,208,28
R 24,32768
mvv 4,212,28
ds_read_b64 v[120:121], v82 offset:0
ds_read_b128 v[56:59], v83 offset:0
mvv 8,28,216
ds_read_b128 v[60:63], v83 offset:16
mvv 0,220,32
R 28,33792
mvv 4,224,32
mvv 8,32,228
s_waitcnt lgkmcnt(5)
mvv 0,232,36
R 32,34816
mvv 4,236,36
mvv 8,36,240
mvv 0,244,40
R 36,35840
mvv 4,248,40
mvv 8,40,252
s_waitcnt lgkmcnt(2)
v_mfma_f32_16x16x32_bf16 v[12:15], a[0:3], v[24:27], 0
R 40,36864
v_mul_f32 v57, v57, v118
v_mfma_f32_16x16x32_bf16 v[16:19], a[4:7], v[24:27], 0
v_mul_f32 v59, v59, v118
v_mul_f32 v61, v61, v118
v_mfma_f32_16x16x32_bf16 v[20:23], v[24:27], a[8:11], 0
v_fma_f32 v8, v8, v56, v57
v_fma_f32 v9, v9, v58, v59
mav 12,12,28
R 44,37888
v_fma_f32 v10, v10, v60, v61
mav 16,16,28
v_mul_f32 v63, v63, v118
v_fma_f32 v11, v11, v62, v63
mva 20,28,20
v_cvt_pk_bf16_f32 v96, v8, v9
s_waitcnt lgkmcnt(2)
mav 12,24,32
R 24,38912
v_cvt_pk_bf16_f32 v97, v10, v11
mav 16,28,32
v_mul_f32 v122, v121, v48
v_fma_f32 v0, v0, v120, v122
mva 20,32,32
v_mul_f32 v123, v121, v49
v_fma_f32 v1, v1, v120, v123
mav 12,36,36
R 28,39936
v_mul_f32 v122, v121, v50
mav 16,40,36
v_fma_f32 v2, v2, v120, v122
v_mul_f32 v123, v121, v51
mva 20,36,44
v_fma_f32 v3, v3, v120, v123
s_waitcnt lgkmcnt(2)
mav 12,48,40
R 32,40960
v_mul_f32 v122, v121, v52
mav 16,52,40
v_fma_f32 v4, v4, v120, v122
v_mul_f32 v123, v121, v53
mva 20,40,56
v_fma_f32 v5, v5, v120, v123
v_mul_f32 v122, v121, v54
mav 12,60,44
R 36,41984
v_fma_f32 v6, v6, v120, v122
mav 16,64,44
v_mul_f32 v123, v121, v55
v_fma_f32 v7, v7, v120, v123
mva 20,44,68
v_cvt_pk_bf16_f32 v64, v0, v1
s_waitcnt lgkmcnt(2)
mav 12,72,24
R 40,43008
v_cvt_pk_bf16_f32 v65, v2, v3
mav 16,76,24
v_cvt_pk_bf16_f32 v66, v4, v5
v_cvt_pk_bf16_f32 v67, v6, v7
mva 20,24,80
mav 12,84,28
R 44,44032
mav 16,88,28
mva 20,28,92
s_waitcnt lgkmcnt(2)
mav 12,96,32
R 24,45056
mav 16,100,32
mva 20,32,104
mav 12,108,36
R 28,46080
mav 16,112,36
mva 20,36,116
s_barrier
s_waitcnt lgkmcnt(2)
mav 12,120,40
ds_read_b128 v[68:71], v81 offset:0
ds_read_b64 v[102:103], v81 offset:1024
mav 16,124,40
R 32,47104
mva 20,40,128
mav 12,132,44
R 36,48128
mav 16,136,44
mva 20,44,140
s_waitcnt lgkmcnt(4)
mav 12,144,24
R 40,49152
mav 16,148,24
mva 20,24,152
mav 12,156,28
R 24,50176
mav 16,160,28
s_waitcnt lgkmcnt(2)
buffer_store_dwordx4 v[68:71], v81, s[36:39], s54 offen sc1
mva 20,28,164
buffer_store_dwordx2 v[96:97], v81, s[36:39], s54 offen offset:1024 sc1
buffer_store_dwordx2 v[102:103], v81, s[36:39], s54 offen offset:1032 sc1
mav 12,168,32
R 28,51200
mav 16,172,32
mva 20,32,176
mav 12,180,36
R 32,52224
mav 16,184,36
mva 20,36,188
s_waitcnt lgkmcnt(2)
mav 12,192,40
R 36,53248
mav 16,196,40
mva 20,40,200
mav 12,204,24
R 40,54272
mav 16,208,24
mva 20,24,212
s_waitcnt lgkmcnt(2)
mav 12,216,28
R 24,55296
mav 16,220,28
mva 20,28,224
mav 12,228,32
R 28,56320
mav 16,232,32
mva 20,32,236
s_waitcnt lgkmcnt(2)
mav 12,240,36
R 32,57344
mav 16,244,36
mva 20,36,248
mav 12,252,40
R 36,58368
mvv 16,128,40
mvv 20,40,132
s_waitcnt lgkmcnt(2)
mvv 12,136,24
R 40,59392
mvv 16,140,24
mvv 20,24,144
mvv 12,148,28
R 24,60416
mvv 16,152,28
mvv 20,28,156
s_waitcnt lgkmcnt(2)
mvv 12,160,32
R 28,61440
mvv 16,164,32
mvv 20,32,168
mvv 12,172,36
R 32,62464
mvv 16,176,36
mvv 20,36,180
s_waitcnt lgkmcnt(2)
mvv 12,184,40
R 36,63488
mvv 16,188,40
mvv 20,40,192
mvv 12,196,24
R 40,64512
v_xor_b32 v80, 0x10000, v80
mvv 16,200,24
s_add_u32 s49, s49, 346112
mvv 20,24,204
s_barrier
s_waitcnt lgkmcnt(2)
mvv 12,208,28
R 24,0
mvv 16,212,28
ds_read_b64 v[120:121], v82 offset:128
ds_read_b128 v[56:59], v83 offset:128
mvv 20,28,216
ds_read_b128 v[60:63], v83 offset:144
mvv 12,220,32
R 28,1024
mvv 16,224,32
mvv 20,32,228
s_waitcnt lgkmcnt(5)
mvv 12,232,36
R 32,2048
mvv 16,236,36
mvv 20,36,240
mvv 12,244,40
R 36,3072
mvv 16,248,40
mvv 20,40,252
s_waitcnt lgkmcnt(2)
v_mfma_f32_16x16x32_bf16 v[0:3], a[0:3], v[24:27], 0
R 40,4096
v_mul_f32 v57, v57, v118
v_mfma_f32_16x16x32_bf16 v[4:7], a[4:7], v[24:27], 0
v_mul_f32 v59, v59, v118
v_mul_f32 v61, v61, v118
v_mfma_f32_16x16x32_bf16 v[8:11], v[24:27], a[8:11], 0
v_fma_f32 v20, v20, v56, v57
v_fma_f32 v21, v21, v58, v59
mav 0,12,28
R 44,5120
v_fma_f32 v22, v22, v60, v61
mav 4,16,28
v_mul_f32 v63, v63, v118
v_fma_f32 v23, v23, v62, v63
mva 8,28,20
v_cvt_pk_bf16_f32 v98, v20, v21
s_waitcnt lgkmcnt(2)
mav 0,24,32
R 24,6144
v_cvt_pk_bf16_f32 v99, v22, v23
mav 4,28,32
v_mul_f32 v122, v121, v48
v_mfma_f32_16x16x32_bf16 v[56:59], v[68:71], v[64:67], v[84:87]
v_fma_f32 v12, v12, v120, v122
v_mul_f32 v123, v121, v49
mva 8,32,32
v_fma_f32 v13, v13, v120, v123
v_mul_f32 v122, v121, v50
mav 0,36,36
R 28,7168
v_fma_f32 v14, v14, v120, v122
mav 4,40,36
v_mul_f32 v123, v121, v51
v_fma_f32 v15, v15, v120, v123
mva 8,36,44
v_mul_f32 v122, v121, v52
s_waitcnt lgkmcnt(2)
mav 0,48,40
R 32,8192
v_fma_f32 v16, v16, v120, v122
mav 4,52,40
v_mul_f32 v123, v121, v53
v_fma_f32 v17, v17, v120, v123
mva 8,40,56
v_mul_f32 v122, v121, v54
v_fma_f32 v18, v18, v120, v122
mav 0,60,44
R 36,9216
v_mul_f32 v123, v121, v55
mav 4,64,44
v_fma_f32 v19, v19, v120, v123
mva 8,44,68
s_waitcnt lgkmcnt(2)
mav 0,72,24
R 40,10240
mav 4,76,24
mva 8,24,80
mav 0,84,28
R 44,11264
mav 4,88,28
mva 8,28,92
s_waitcnt lgkmcnt(2)
mav 0,96,32
R 24,12288
mav 4,100,32
mva 8,32,104
mav 0,108,36
R 28,13312
mav 4,112,36
mva 8,36,116
s_barrier
s_waitcnt lgkmcnt(2)
mav 0,120,40
ds_read_b128 v[72:75], v81 offset:0
ds_read_b64 v[104:105], v81 offset:1024
mav 4,124,40
R 32,14336
mva 8,40,128
mav 0,132,44
R 36,15360
mav 4,136,44
mva 8,44,140
s_waitcnt lgkmcnt(4)
mav 0,144,24
R 40,16384
mav 4,148,24
s_waitcnt lgkmcnt(3)
v_mfma_f32_16x16x32_bf16 v[60:63], v[72:75], v[64:67], v[88:91]
v_cvt_pk_bf16_f32 v64, v12, v13
v_cvt_pk_bf16_f32 v65, v14, v15
mva 8,24,152
v_cvt_pk_bf16_f32 v66, v16, v17
v_cvt_pk_bf16_f32 v67, v18, v19
mav 0,156,28
R 24,17408
mav 4,160,28
v_max3_f32 v112, v56, v57, v60
v_max3_f32 v113, v58, v59, v62
mva 8,28,164
v_max_f32 v112, v112, v61
s_waitcnt lgkmcnt(2)
mav 0,168,32
R 28,18432
v_max_f32 v113, v113, v63
mav 4,172,32
v_max_f32 v114, v112, v113
v_max_f32 v115, v112, v113
mva 8,32,176
mav 0,180,36
R 32,19456
v_permlane32_swap_b32 v114, v115
mav 4,184,36
v_max_f32 v108, v114, v115
v_max_f32 v109, v114, v115
mva 8,36,188
s_waitcnt lgkmcnt(2)
mav 0,192,40
R 36,20480
v_permlane16_swap_b32 v108, v109
mav 4,196,40
v_max_f32 v114, v108, v109
v_mul_f32 v114, s65, v114
mva 8,40,200
v_fma_f32 v112, v112, s65, -v114
v_fma_f32 v113, v113, s65, -v114
mav 0,204,24
R 40,21504
mav 4,208,24
v_exp_f32 v112, v112
mva 8,24,212
s_waitcnt lgkmcnt(2)
mav 0,216,28
R 24,22528
mav 4,220,28
v_exp_f32 v113, v113
mva 8,28,224
v_add_f32 v114, v112, v113
v_add_f32 v115, v112, v113
mav 0,228,32
R 28,23552
v_permlane32_swap_b32 v114, v115
mav 4,232,32
v_add_f32 v108, v114, v115
v_add_f32 v109, v114, v115
mva 8,32,236
s_waitcnt lgkmcnt(2)
mav 0,240,36
R 32,24576
v_permlane16_swap_b32 v108, v109
mav 4,244,36
v_add_f32 v114, v108, v109
mva 8,36,248
v_rcp_f32 v114, v114
mav 0,252,40
R 36,25600
v_mul_f32 v112, v112, v114
mvv 4,128,40
v_mul_f32 v113, v113, v114
v_cvt_pk_bf16_f32 v112, v112, v112
mvv 8,40,132
v_cvt_pk_bf16_f32 v113, v113, v113
s_waitcnt lgkmcnt(2)
mvv 0,136,24
R 40,26624
v_and_b32 v108, v112, v92
mvv 4,140,24
v_and_b32 v109, v113, v93
v_and_b32 v110, v112, v94
mvv 8,24,144
v_and_b32 v111, v113, v95
mvv 0,148,28
R 24,27648
v_mfma_f32_16x16x32_bf16 v[56:59], v[96:99], v[108:111], 0
v_mfma_f32_16x16x32_bf16 v[60:63], v[102:105], v[108:111], 0
mvv 4,152,28
mvv 8,28,156
s_waitcnt lgkmcnt(2)
mvv 0,160,32
R 28,28672
mvv 4,164,32
mvv 8,32,168
v_cvt_pk_bf16_f32 v108, v56, v57
v_cvt_pk_bf16_f32 v109, v58, v59
mvv 0,172,36
R 32,29696
v_cvt_pk_bf16_f32 v110, v60, v61
mvv 4,176,36
v_cvt_pk_bf16_f32 v111, v62, v63
buffer_store_dwordx4 v[108:111], v81, s[36:39], s49 offen sc1
mvv 8,36,180
s_waitcnt lgkmcnt(2)
mvv 0,184,40
R 36,30720
mvv 4,188,40
mvv 8,40,192
mvv 0,196,24
R 40,31744
mvv 4,200,24
s_add_u32 s49, s49, 346112
mvv 8,24,204
s_barrier
s_waitcnt lgkmcnt(2)
mvv 0,208,28
R 24,32768
mvv 4,212,28
ds_read_b64 v[120:121], v82 offset:0
ds_read_b128 v[56:59], v83 offset:0
mvv 8,28,216
ds_read_b128 v[60:63], v83 offset:16
mvv 0,220,32
R 28,33792
mvv 4,224,32
mvv 8,32,228
s_waitcnt lgkmcnt(5)
mvv 0,232,36
R 32,34816
mvv 4,236,36
mvv 8,36,240
mvv 0,244,40
R 36,35840
mvv 4,248,40
mvv 8,40,252
s_waitcnt lgkmcnt(2)
v_mfma_f32_16x16x32_bf16 v[12:15], a[0:3], v[24:27], 0
R 40,36864
v_mul_f32 v57, v57, v118
v_mfma_f32_16x16x32_bf16 v[16:19], a[4:7], v[24:27], 0
v_mul_f32 v59, v59, v118
v_mul_f32 v61, v61, v118
v_mfma_f32_16x16x32_bf16 v[20:23], v[24:27], a[8:11], 0
v_fma_f32 v8, v8, v56, v57
v_fma_f32 v9, v9, v58, v59
mav 12,12,28
R 44,37888
v_fma_f32 v10, v10, v60, v61
mav 16,16,28
v_mul_f32 v63, v63, v118
v_fma_f32 v11, v11, v62, v63
mva 20,28,20
v_cvt_pk_bf16_f32 v96, v8, v9
s_waitcnt lgkmcnt(2)
mav 12,24,32
R 24,38912
v_cvt_pk_bf16_f32 v97, v10, v11
mav 16,28,32
v_mul_f32 v122, v121, v48
v_mfma_f32_16x16x32_bf16 v[56:59], v[72:75], v[64:67], v[84:87]
v_fma_f32 v0, v0, v120, v122
v_mul_f32 v123, v121, v49
mva 20,32,32
v_fma_f32 v1, v1, v120, v123
v_mul_f32 v122, v121, v50
mav 12,36,36
R 28,39936
v_fma_f32 v2, v2, v120, v122
mav 16,40,36
v_mul_f32 v123, v121, v51
v_fma_f32 v3, v3, v120, v123
mva 20,36,44
v_mul_f32 v122, v121, v52
s_waitcnt lgkmcnt(2)
mav 12,48,40
R 32,40960
v_fma_f32 v4, v4, v120, v122
mav 16,52,40
v_mul_f32 v123, v121, v53
v_fma_f32 v5, v5, v120, v123
mva 20,40,56
v_mul_f32 v122, v121, v54
v_fma_f32 v6, v6, v120, v122
mav 12,60,44
R 36,41984
v_mul_f32 v123, v121, v55
mav 16,64,44
v_fma_f32 v7, v7, v120, v123
mva 20,44,68
s_waitcnt lgkmcnt(2)
mav 12,72,24
R 40,43008
mav 16,76,24
mva 20,24,80
mav 12,84,28
R 44,44032
mav 16,88,28
mva 20,28,92
s_waitcnt lgkmcnt(2)
mav 12,96,32
R 24,45056
v_add_u32 v116, 1, v116
mav 16,100,32
mva 20,32,104
mav 12,108,36
R 28,46080
mav 16,112,36
mva 20,36,116
s_barrier
s_waitcnt lgkmcnt(2)
mav 12,120,40
ds_read_b128 v[68:71], v81 offset:0
ds_read_b64 v[102:103], v81 offset:1024
mav 16,124,40
R 32,47104
mva 20,40,128
s_waitcnt vmcnt(0)
buffer_store_dwordx2 v[116:117], off, s[36:39], s52 sc1
mav 12,132,44
R 36,48128
mav 16,136,44
mva 20,44,140
s_waitcnt lgkmcnt(4)
mav 12,144,24
R 40,49152
mav 16,148,24
s_waitcnt lgkmcnt(3)
v_mfma_f32_16x16x32_bf16 v[60:63], v[68:71], v[64:67], v[88:91]
v_cvt_pk_bf16_f32 v64, v0, v1
v_cvt_pk_bf16_f32 v65, v2, v3
mva 20,24,152
v_cvt_pk_bf16_f32 v66, v4, v5
v_cvt_pk_bf16_f32 v67, v6, v7
mav 12,156,28
R 24,50176
mav 16,160,28
v_max3_f32 v112, v56, v57, v60
v_max3_f32 v113, v58, v59, v62
mva 20,28,164
v_max_f32 v112, v112, v61
s_waitcnt lgkmcnt(2)
mav 12,168,32
R 28,51200
v_max_f32 v113, v113, v63
mav 16,172,32
v_max_f32 v114, v112, v113
v_max_f32 v115, v112, v113
mva 20,32,176
mav 12,180,36
R 32,52224
v_permlane32_swap_b32 v114, v115
mav 16,184,36
v_max_f32 v108, v114, v115
v_max_f32 v109, v114, v115
mva 20,36,188
s_waitcnt lgkmcnt(2)
mav 12,192,40
R 36,53248
v_permlane16_swap_b32 v108, v109
mav 16,196,40
v_max_f32 v114, v108, v109
v_mul_f32 v114, s65, v114
mva 20,40,200
v_fma_f32 v112, v112, s65, -v114
v_fma_f32 v113, v113, s65, -v114
mav 12,204,24
R 40,54272
mav 16,208,24
v_exp_f32 v112, v112
mva 20,24,212
s_waitcnt lgkmcnt(2)
mav 12,216,28
R 24,55296
mav 16,220,28
v_exp_f32 v113, v113
mva 20,28,224
v_add_f32 v114, v112, v113
v_add_f32 v115, v112, v113
mav 12,228,32
R 28,56320
v_permlane32_swap_b32 v114, v115
mav 16,232,32
v_add_f32 v108, v114, v115
v_add_f32 v109, v114, v115
mva 20,32,236
s_waitcnt lgkmcnt(2)
mav 12,240,36
R 32,57344
v_permlane16_swap_b32 v108, v109
mav 16,244,36
v_add_f32 v114, v108, v109
mva 20,36,248
v_rcp_f32 v114, v114
mav 12,252,40
R 36,58368
v_mul_f32 v112, v112, v114
mvv 16,128,40
v_mul_f32 v113, v113, v114
v_cvt_pk_bf16_f32 v112, v112, v112
mvv 20,40,132
v_cvt_pk_bf16_f32 v113, v113, v113
s_waitcnt lgkmcnt(2)
mvv 12,136,24
R 40,59392
v_and_b32 v108, v112, v94
mvv 16,140,24
v_and_b32 v109, v113, v95
v_and_b32 v110, v112, v92
mvv 20,24,144
v_and_b32 v111, v113, v93
mvv 12,148,28
R 24,60416
v_mfma_f32_16x16x32_bf16 v[56:59], v[96:99], v[108:111], 0
v_mfma_f32_16x16x32_bf16 v[60:63], v[102:105], v[108:111], 0
mvv 16,152,28
mvv 20,28,156
s_waitcnt lgkmcnt(2)
mvv 12,160,32
R 28,61440
mvv 16,164,32
mvv 20,32,168
v_cvt_pk_bf16_f32 v108, v56, v57
v_cvt_pk_bf16_f32 v109, v58, v59
mvv 12,172,36
R 32,62464
v_cvt_pk_bf16_f32 v110, v60, v61
mvv 16,176,36
v_cvt_pk_bf16_f32 v111, v62, v63
buffer_store_dwordx4 v[108:111], v81, s[36:39], s49 offen sc1
mvv 20,36,180
s_waitcnt lgkmcnt(2)
mvv 12,184,40
R 36,63488
mvv 16,188,40
mvv 20,40,192
mvv 12,196,24
R 40,64512
v_xor_b32 v80, 0x10000, v80
mvv 16,200,24
s_add_u32 s49, s49, 346112
mvv 20,24,204
s_barrier
s_waitcnt lgkmcnt(2)
mvv 12,208,28
R 24,0
mvv 16,212,28
ds_read_b64 v[120:121], v82 offset:128
ds_read_b128 v[56:59], v83 offset:128
mvv 20,28,216
ds_read_b128 v[60:63], v83 offset:144
mvv 12,220,32
R 28,1024
mvv 16,224,32
mvv 20,32,228
s_waitcnt lgkmcnt(5)
mvv 12,232,36
R 32,2048
mvv 16,236,36
mvv 20,36,240
mvv 12,244,40
R 36,3072
mvv 16,248,40
mvv 20,40,252
s_mov_b32 s60, -13
.p2align 6
LQA_1:
s_waitcnt lgkmcnt(2)
v_mfma_f32_16x16x32_bf16 v[0:3], a[0:3], v[24:27], 0
R 40,4096
v_mul_f32 v57, v57, v118
v_mfma_f32_16x16x32_bf16 v[4:7], a[4:7], v[24:27], 0
v_mul_f32 v59, v59, v118
v_mul_f32 v61, v61, v118
v_mfma_f32_16x16x32_bf16 v[8:11], v[24:27], a[8:11], 0
v_fma_f32 v20, v20, v56, v57
v_fma_f32 v21, v21, v58, v59
mav 0,12,28
R 44,5120
v_fma_f32 v22, v22, v60, v61
mav 4,16,28
v_mul_f32 v63, v63, v118
v_fma_f32 v23, v23, v62, v63
mva 8,28,20
v_cvt_pk_bf16_f32 v98, v20, v21
s_waitcnt lgkmcnt(2)
mav 0,24,32
R 24,6144
v_cvt_pk_bf16_f32 v99, v22, v23
mav 4,28,32
v_mul_f32 v122, v121, v48
v_mfma_f32_16x16x32_bf16 v[56:59], v[68:71], v[64:67], v[84:87]
v_fma_f32 v12, v12, v120, v122
v_mul_f32 v123, v121, v49
mva 8,32,32
v_fma_f32 v13, v13, v120, v123
v_mul_f32 v122, v121, v50
mav 0,36,36
R 28,7168
v_fma_f32 v14, v14, v120, v122
mav 4,40,36
v_mul_f32 v123, v121, v51
v_fma_f32 v15, v15, v120, v123
mva 8,36,44
v_mul_f32 v122, v121, v52
s_waitcnt lgkmcnt(2)
mav 0,48,40
R 32,8192
v_fma_f32 v16, v16, v120, v122
mav 4,52,40
v_mul_f32 v123, v121, v53
v_fma_f32 v17, v17, v120, v123
mva 8,40,56
v_mul_f32 v122, v121, v54
v_fma_f32 v18, v18, v120, v122
mav 0,60,44
R 36,9216
v_mul_f32 v123, v121, v55
mav 4,64,44
v_fma_f32 v19, v19, v120, v123
mva 8,44,68
s_waitcnt lgkmcnt(2)
mav 0,72,24
R 40,10240
mav 4,76,24
mva 8,24,80
mav 0,84,28
R 44,11264
mav 4,88,28
mva 8,28,92
s_waitcnt lgkmcnt(2)
mav 0,96,32
R 24,12288
v_add_u32 v116, 1, v116
mav 4,100,32
mva 8,32,104
mav 0,108,36
R 28,13312
mav 4,112,36
mva 8,36,116
s_barrier
s_waitcnt lgkmcnt(2)
mav 0,120,40
ds_read_b128 v[72:75], v81 offset:0
ds_read_b64 v[104:105], v81 offset:1024
mav 4,124,40
R 32,14336
mva 8,40,128
s_waitcnt vmcnt(0)
buffer_store_dwordx2 v[116:117], off, s[36:39], s52 sc1
mav 0,132,44
R 36,15360
mav 4,136,44
mva 8,44,140
s_waitcnt lgkmcnt(4)
mav 0,144,24
R 40,16384
mav 4,148,24
s_waitcnt lgkmcnt(3)
v_mfma_f32_16x16x32_bf16 v[60:63], v[72:75], v[64:67], v[88:91]
v_cvt_pk_bf16_f32 v64, v12, v13
v_cvt_pk_bf16_f32 v65, v14, v15
mva 8,24,152
v_cvt_pk_bf16_f32 v66, v16, v17
v_cvt_pk_bf16_f32 v67, v18, v19
mav 0,156,28
R 24,17408
mav 4,160,28
v_max3_f32 v112, v56, v57, v60
v_max3_f32 v113, v58, v59, v62
mva 8,28,164
v_max_f32 v112, v112, v61
s_waitcnt lgkmcnt(2)
mav 0,168,32
R 28,18432
v_max_f32 v113, v113, v63
mav 4,172,32
v_max_f32 v114, v112, v113
v_max_f32 v115, v112, v113
mva 8,32,176
mav 0,180,36
R 32,19456
v_permlane32_swap_b32 v114, v115
mav 4,184,36
v_max_f32 v108, v114, v115
v_max_f32 v109, v114, v115
mva 8,36,188
s_waitcnt lgkmcnt(2)
mav 0,192,40
R 36,20480
v_permlane16_swap_b32 v108, v109
mav 4,196,40
v_max_f32 v114, v108, v109
v_mul_f32 v114, s65, v114
mva 8,40,200
v_fma_f32 v112, v112, s65, -v114
v_fma_f32 v113, v113, s65, -v114
mav 0,204,24
R 40,21504
mav 4,208,24
v_exp_f32 v112, v112
mva 8,24,212
s_waitcnt lgkmcnt(2)
mav 0,216,28
R 24,22528
mav 4,220,28
v_exp_f32 v113, v113
mva 8,28,224
v_add_f32 v114, v112, v113
v_add_f32 v115, v112, v113
mav 0,228,32
R 28,23552
v_permlane32_swap_b32 v114, v115
mav 4,232,32
v_add_f32 v108, v114, v115
v_add_f32 v109, v114, v115
mva 8,32,236
s_waitcnt lgkmcnt(2)
mav 0,240,36
R 32,24576
v_permlane16_swap_b32 v108, v109
mav 4,244,36
v_add_f32 v114, v108, v109
mva 8,36,248
v_rcp_f32 v114, v114
mav 0,252,40
R 36,25600
v_mul_f32 v112, v112, v114
mvv 4,128,40
v_mul_f32 v113, v113, v114
v_cvt_pk_bf16_f32 v112, v112, v112
mvv 8,40,132
v_cvt_pk_bf16_f32 v113, v113, v113
s_waitcnt lgkmcnt(2)
mvv 0,136,24
R 40,26624
v_and_b32 v108, v112, v92
mvv 4,140,24
v_and_b32 v109, v113, v93
v_and_b32 v110, v112, v94
mvv 8,24,144
v_and_b32 v111, v113, v95
mvv 0,148,28
R 24,27648
v_mfma_f32_16x16x32_bf16 v[56:59], v[96:99], v[108:111], 0
v_mfma_f32_16x16x32_bf16 v[60:63], v[102:105], v[108:111], 0
mvv 4,152,28
mvv 8,28,156
s_waitcnt lgkmcnt(2)
mvv 0,160,32
R 28,28672
mvv 4,164,32
mvv 8,32,168
v_cvt_pk_bf16_f32 v108, v56, v57
v_cvt_pk_bf16_f32 v109, v58, v59
mvv 0,172,36
R 32,29696
v_cvt_pk_bf16_f32 v110, v60, v61
mvv 4,176,36
v_cvt_pk_bf16_f32 v111, v62, v63
buffer_store_dwordx4 v[108:111], v81, s[36:39], s49 offen sc1
mvv 8,36,180
s_waitcnt lgkmcnt(2)
mvv 0,184,40
R 36,30720
mvv 4,188,40
mvv 8,40,192
mvv 0,196,24
R 40,31744
mvv 4,200,24
s_add_u32 s49, s49, 346112
mvv 8,24,204
s_barrier
s_waitcnt lgkmcnt(2)
mvv 0,208,28
R 24,32768
mvv 4,212,28
ds_read_b64 v[120:121], v82 offset:0
ds_read_b128 v[56:59], v83 offset:0
mvv 8,28,216
ds_read_b128 v[60:63], v83 offset:16
mvv 0,220,32
R 28,33792
mvv 4,224,32
mvv 8,32,228
s_waitcnt lgkmcnt(5)
mvv 0,232,36
R 32,34816
mvv 4,236,36
mvv 8,36,240
mvv 0,244,40
R 36,35840
mvv 4,248,40
mvv 8,40,252
s_waitcnt lgkmcnt(2)
v_mfma_f32_16x16x32_bf16 v[12:15], a[0:3], v[24:27], 0
R 40,36864
v_mul_f32 v57, v57, v118
v_mfma_f32_16x16x32_bf16 v[16:19], a[4:7], v[24:27], 0
v_mul_f32 v59, v59, v118
v_mul_f32 v61, v61, v118
v_mfma_f32_16x16x32_bf16 v[20:23], v[24:27], a[8:11], 0
v_fma_f32 v8, v8, v56, v57
v_fma_f32 v9, v9, v58, v59
mav 12,12,28
R 44,37888
v_fma_f32 v10, v10, v60, v61
mav 16,16,28
v_mul_f32 v63, v63, v118
v_fma_f32 v11, v11, v62, v63
mva 20,28,20
v_cvt_pk_bf16_f32 v96, v8, v9
s_waitcnt lgkmcnt(2)
mav 12,24,32
R 24,38912
v_cvt_pk_bf16_f32 v97, v10, v11
mav 16,28,32
v_mul_f32 v122, v121, v48
v_mfma_f32_16x16x32_bf16 v[56:59], v[72:75], v[64:67], v[84:87]
v_fma_f32 v0, v0, v120, v122
v_mul_f32 v123, v121, v49
mva 20,32,32
v_fma_f32 v1, v1, v120, v123
v_mul_f32 v122, v121, v50
mav 12,36,36
R 28,39936
v_fma_f32 v2, v2, v120, v122
mav 16,40,36
v_mul_f32 v123, v121, v51
v_fma_f32 v3, v3, v120, v123
mva 20,36,44
v_mul_f32 v122, v121, v52
s_waitcnt lgkmcnt(2)
mav 12,48,40
R 32,40960
v_fma_f32 v4, v4, v120, v122
mav 16,52,40
v_mul_f32 v123, v121, v53
v_fma_f32 v5, v5, v120, v123
mva 20,40,56
v_mul_f32 v122, v121, v54
v_fma_f32 v6, v6, v120, v122
mav 12,60,44
R 36,41984
v_mul_f32 v123, v121, v55
mav 16,64,44
v_fma_f32 v7, v7, v120, v123
mva 20,44,68
s_waitcnt lgkmcnt(2)
mav 12,72,24
R 40,43008
mav 16,76,24
mva 20,24,80
mav 12,84,28
R 44,44032
mav 16,88,28
mva 20,28,92
s_waitcnt lgkmcnt(2)
mav 12,96,32
R 24,45056
v_add_u32 v116, 1, v116
mav 16,100,32
mva 20,32,104
mav 12,108,36
R 28,46080
mav 16,112,36
mva 20,36,116
s_barrier
s_waitcnt lgkmcnt(2)
mav 12,120,40
ds_read_b128 v[68:71], v81 offset:0
ds_read_b64 v[102:103], v81 offset:1024
mav 16,124,40
R 32,47104
mva 20,40,128
s_waitcnt vmcnt(0)
buffer_store_dwordx2 v[116:117], off, s[36:39], s52 sc1
mav 12,132,44
R 36,48128
mav 16,136,44
mva 20,44,140
s_waitcnt lgkmcnt(4)
mav 12,144,24
R 40,49152
mav 16,148,24
s_waitcnt lgkmcnt(3)
v_mfma_f32_16x16x32_bf16 v[60:63], v[68:71], v[64:67], v[88:91]
v_cvt_pk_bf16_f32 v64, v0, v1
v_cvt_pk_bf16_f32 v65, v2, v3
mva 20,24,152
v_cvt_pk_bf16_f32 v66, v4, v5
v_cvt_pk_bf16_f32 v67, v6, v7
mav 12,156,28
R 24,50176
mav 16,160,28
v_max3_f32 v112, v56, v57, v60
v_max3_f32 v113, v58, v59, v62
mva 20,28,164
v_max_f32 v112, v112, v61
s_waitcnt lgkmcnt(2)
mav 12,168,32
R 28,51200
v_max_f32 v113, v113, v63
mav 16,172,32
v_max_f32 v114, v112, v113
v_max_f32 v115, v112, v113
mva 20,32,176
mav 12,180,36
R 32,52224
v_permlane32_swap_b32 v114, v115
mav 16,184,36
v_max_f32 v108, v114, v115
v_max_f32 v109, v114, v115
mva 20,36,188
s_waitcnt lgkmcnt(2)
mav 12,192,40
R 36,53248
v_permlane16_swap_b32 v108, v109
mav 16,196,40
v_max_f32 v114, v108, v109
v_mul_f32 v114, s65, v114
mva 20,40,200
v_fma_f32 v112, v112, s65, -v114
v_fma_f32 v113, v113, s65, -v114
mav 12,204,24
R 40,54272
mav 16,208,24
v_exp_f32 v112, v112
mva 20,24,212
s_waitcnt lgkmcnt(2)
mav 12,216,28
R 24,55296
mav 16,220,28
v_exp_f32 v113, v113
mva 20,28,224
v_add_f32 v114, v112, v113
v_add_f32 v115, v112, v113
mav 12,228,32
R 28,56320
v_permlane32_swap_b32 v114, v115
mav 16,232,32
v_add_f32 v108, v114, v115
v_add_f32 v109, v114, v115
mva 20,32,236
s_waitcnt lgkmcnt(2)
mav 12,240,36
R 32,57344
v_permlane16_swap_b32 v108, v109
mav 16,244,36
v_add_f32 v114, v108, v109
mva 20,36,248
v_rcp_f32 v114, v114
mav 12,252,40
R 36,58368
v_mul_f32 v112, v112, v114
mvv 16,128,40
v_mul_f32 v113, v113, v114
v_cvt_pk_bf16_f32 v112, v112, v112
mvv 20,40,132
v_cvt_pk_bf16_f32 v113, v113, v113
s_waitcnt lgkmcnt(2)
mvv 12,136,24
R 40,59392
v_and_b32 v108, v112, v94
mvv 16,140,24
v_and_b32 v109, v113, v95
v_and_b32 v110, v112, v92
mvv 20,24,144
v_and_b32 v111, v113, v93
mvv 12,148,28
R 24,60416
v_mfma_f32_16x16x32_bf16 v[56:59], v[96:99], v[108:111], 0
v_mfma_f32_16x16x32_bf16 v[60:63], v[102:105], v[108:111], 0
mvv 16,152,28
mvv 20,28,156
s_waitcnt lgkmcnt(2)
mvv 12,160,32
R 28,61440
mvv 16,164,32
mvv 20,32,168
v_cvt_pk_bf16_f32 v108, v56, v57
v_cvt_pk_bf16_f32 v109, v58, v59
mvv 12,172,36
R 32,62464
v_cvt_pk_bf16_f32 v110, v60, v61
mvv 16,176,36
v_cvt_pk_bf16_f32 v111, v62, v63
buffer_store_dwordx4 v[108:111], v81, s[36:39], s49 offen sc1
mvv 20,36,180
s_waitcnt lgkmcnt(2)
mvv 12,184,40
R 36,63488
mvv 16,188,40
mvv 20,40,192
mvv 12,196,24
R 40,64512
v_xor_b32 v80, 0x10000, v80
mvv 16,200,24
s_add_u32 s49, s49, 346112
mvv 20,24,204
s_barrier
s_waitcnt lgkmcnt(2)
mvv 12,208,28
R 24,0
mvv 16,212,28
ds_read_b64 v[120:121], v82 offset:128
ds_read_b128 v[56:59], v83 offset:128
mvv 20,28,216
ds_read_b128 v[60:63], v83 offset:144
mvv 12,220,32
R 28,1024
mvv 16,224,32
mvv 20,32,228
s_waitcnt lgkmcnt(5)
mvv 12,232,36
R 32,2048
mvv 16,236,36
mvv 20,36,240
mvv 12,244,40
R 36,3072
mvv 16,248,40
mvv 20,40,252
s_add_u32 s60, s60, 1
s_cbranch_scc0 LQA_1
s_waitcnt lgkmcnt(2)
v_mfma_f32_16x16x32_bf16 v[0:3], a[0:3], v[24:27], 0
R 40,4096
v_mul_f32 v57, v57, v118
v_mfma_f32_16x16x32_bf16 v[4:7], a[4:7], v[24:27], 0
v_mul_f32 v59, v59, v118
v_mul_f32 v61, v61, v118
v_mfma_f32_16x16x32_bf16 v[8:11], v[24:27], a[8:11], 0
v_fma_f32 v20, v20, v56, v57
v_fma_f32 v21, v21, v58, v59
mav 0,12,28
R 44,5120
v_fma_f32 v22, v22, v60, v61
mav 4,16,28
v_mul_f32 v63, v63, v118
v_fma_f32 v23, v23, v62, v63
mva 8,28,20
v_cvt_pk_bf16_f32 v98, v20, v21
s_waitcnt lgkmcnt(2)
mav 0,24,32
R 24,6144
v_cvt_pk_bf16_f32 v99, v22, v23
mav 4,28,32
v_mul_f32 v122, v121, v48
v_mfma_f32_16x16x32_bf16 v[56:59], v[68:71], v[64:67], v[84:87]
v_fma_f32 v12, v12, v120, v122
v_mul_f32 v123, v121, v49
mva 8,32,32
v_fma_f32 v13, v13, v120, v123
v_mul_f32 v122, v121, v50
mav 0,36,36
R 28,7168
v_fma_f32 v14, v14, v120, v122
mav 4,40,36
v_mul_f32 v123, v121, v51
v_fma_f32 v15, v15, v120, v123
mva 8,36,44
v_mul_f32 v122, v121, v52
s_waitcnt lgkmcnt(2)
mav 0,48,40
R 32,8192
v_fma_f32 v16, v16, v120, v122
mav 4,52,40
v_mul_f32 v123, v121, v53
v_fma_f32 v17, v17, v120, v123
mva 8,40,56
v_mul_f32 v122, v121, v54
v_fma_f32 v18, v18, v120, v122
mav 0,60,44
R 36,9216
v_mul_f32 v123, v121, v55
mav 4,64,44
v_fma_f32 v19, v19, v120, v123
mva 8,44,68
s_waitcnt lgkmcnt(2)
mav 0,72,24
R 40,10240
mav 4,76,24
mva 8,24,80
mav 0,84,28
R 44,11264
mav 4,88,28
mva 8,28,92
s_waitcnt lgkmcnt(2)
mav 0,96,32
R 24,12288
v_add_u32 v116, 1, v116
mav 4,100,32
mva 8,32,104
mav 0,108,36
R 28,13312
mav 4,112,36
mva 8,36,116
s_barrier
s_waitcnt lgkmcnt(2)
mav 0,120,40
ds_read_b128 v[72:75], v81 offset:0
ds_read_b64 v[104:105], v81 offset:1024
mav 4,124,40
R 32,14336
mva 8,40,128
s_waitcnt vmcnt(0)
buffer_store_dwordx2 v[116:117], off, s[36:39], s52 sc1
mav 0,132,44
R 36,15360
mav 4,136,44
mva 8,44,140
s_waitcnt lgkmcnt(4)
mav 0,144,24
R 40,16384
mav 4,148,24
s_waitcnt lgkmcnt(3)
v_mfma_f32_16x16x32_bf16 v[60:63], v[72:75], v[64:67], v[88:91]
v_cvt_pk_bf16_f32 v64, v12, v13
v_cvt_pk_bf16_f32 v65, v14, v15
mva 8,24,152
v_cvt_pk_bf16_f32 v66, v16, v17
v_cvt_pk_bf16_f32 v67, v18, v19
mav 0,156,28
R 24,17408
mav 4,160,28
v_max3_f32 v112, v56, v57, v60
v_max3_f32 v113, v58, v59, v62
mva 8,28,164
v_max_f32 v112, v112, v61
s_waitcnt lgkmcnt(2)
mav 0,168,32
R 28,18432
v_max_f32 v113, v113, v63
mav 4,172,32
v_max_f32 v114, v112, v113
v_max_f32 v115, v112, v113
mva 8,32,176
mav 0,180,36
R 32,19456
v_permlane32_swap_b32 v114, v115
mav 4,184,36
v_max_f32 v108, v114, v115
v_max_f32 v109, v114, v115
mva 8,36,188
s_waitcnt lgkmcnt(2)
mav 0,192,40
R 36,20480
v_permlane16_swap_b32 v108, v109
mav 4,196,40
v_max_f32 v114, v108, v109
v_mul_f32 v114, s65, v114
mva 8,40,200
v_fma_f32 v112, v112, s65, -v114
v_fma_f32 v113, v113, s65, -v114
mav 0,204,24
R 40,21504
mav 4,208,24
v_exp_f32 v112, v112
mva 8,24,212
s_waitcnt lgkmcnt(2)
mav 0,216,28
R 24,22528
mav 4,220,28
v_exp_f32 v113, v113
mva 8,28,224
v_add_f32 v114, v112, v113
v_add_f32 v115, v112, v113
mav 0,228,32
R 28,23552
v_permlane32_swap_b32 v114, v115
mav 4,232,32
v_add_f32 v108, v114, v115
v_add_f32 v109, v114, v115
mva 8,32,236
s_waitcnt lgkmcnt(2)
mav 0,240,36
R 32,24576
v_permlane16_swap_b32 v108, v109
mav 4,244,36
v_add_f32 v114, v108, v109
mva 8,36,248
v_rcp_f32 v114, v114
mav 0,252,40
R 36,25600
v_mul_f32 v112, v112, v114
mvv 4,128,40
v_mul_f32 v113, v113, v114
v_cvt_pk_bf16_f32 v112, v112, v112
mvv 8,40,132
v_cvt_pk_bf16_f32 v113, v113, v113
s_waitcnt lgkmcnt(2)
mvv 0,136,24
R 40,26624
v_and_b32 v108, v112, v92
mvv 4,140,24
v_and_b32 v109, v113, v93
v_and_b32 v110, v112, v94
mvv 8,24,144
v_and_b32 v111, v113, v95
mvv 0,148,28
R 24,27648
v_mfma_f32_16x16x32_bf16 v[56:59], v[96:99], v[108:111], 0
v_mfma_f32_16x16x32_bf16 v[60:63], v[102:105], v[108:111], 0
mvv 4,152,28
mvv 8,28,156
s_waitcnt lgkmcnt(2)
mvv 0,160,32
R 28,28672
mvv 4,164,32
mvv 8,32,168
v_cvt_pk_bf16_f32 v108, v56, v57
v_cvt_pk_bf16_f32 v109, v58, v59
mvv 0,172,36
R 32,29696
v_cvt_pk_bf16_f32 v110, v60, v61
mvv 4,176,36
v_cvt_pk_bf16_f32 v111, v62, v63
buffer_store_dwordx4 v[108:111], v81, s[36:39], s49 offen sc1
mvv 8,36,180
s_waitcnt lgkmcnt(2)
mvv 0,184,40
R 36,30720
mvv 4,188,40
mvv 8,40,192
mvv 0,196,24
R 40,31744
mvv 4,200,24
s_add_u32 s49, s49, 346112
mvv 8,24,204
s_barrier
s_waitcnt lgkmcnt(2)
mvv 0,208,28
R 24,32768
mvv 4,212,28
ds_read_b64 v[120:121], v82 offset:0
ds_read_b128 v[56:59], v83 offset:0
mvv 8,28,216
ds_read_b128 v[60:63], v83 offset:16
mvv 0,220,32
R 28,33792
mvv 4,224,32
mvv 8,32,228
s_waitcnt lgkmcnt(5)
mvv 0,232,36
R 32,34816
mvv 4,236,36
mvv 8,36,240
mvv 0,244,40
R 36,35840
mvv 4,248,40
mvv 8,40,252
s_waitcnt lgkmcnt(2)
v_mfma_f32_16x16x32_bf16 v[12:15], a[0:3], v[24:27], 0
R 40,36864
v_mul_f32 v57, v57, v118
v_mfma_f32_16x16x32_bf16 v[16:19], a[4:7], v[24:27], 0
v_mul_f32 v59, v59, v118
v_mul_f32 v61, v61, v118
v_mfma_f32_16x16x32_bf16 v[20:23], v[24:27], a[8:11], 0
v_fma_f32 v8, v8, v56, v57
v_fma_f32 v9, v9, v58, v59
mav 12,12,28
R 44,37888
v_fma_f32 v10, v10, v60, v61
mav 16,16,28
v_mul_f32 v63, v63, v118
v_fma_f32 v11, v11, v62, v63
mva 20,28,20
v_cvt_pk_bf16_f32 v96, v8, v9
s_waitcnt lgkmcnt(2)
mav 12,24,32
R 24,38912
v_cvt_pk_bf16_f32 v97, v10, v11
mav 16,28,32
v_mul_f32 v122, v121, v48
v_mfma_f32_16x16x32_bf16 v[56:59], v[72:75], v[64:67], v[84:87]
v_fma_f32 v0, v0, v120, v122
v_mul_f32 v123, v121, v49
mva 20,32,32
v_fma_f32 v1, v1, v120, v123
v_mul_f32 v122, v121, v50
mav 12,36,36
R 28,39936
v_fma_f32 v2, v2, v120, v122
mav 16,40,36
buffer_load_dwordx4 v[76:79], v81, s[36:39], s55 offen sc1
buffer_load_dwordx2 v[100:101], v81, s[36:39], s55 offen offset:1024 sc1
mva 20,36,44
buffer_load_dwordx2 v[106:107], v81, s[36:39], s55 offen offset:1032 sc1
s_waitcnt lgkmcnt(2)
mav 12,48,40
R 32,40960
v_mul_f32 v123, v121, v51
mav 16,52,40
v_fma_f32 v3, v3, v120, v123
v_mul_f32 v122, v121, v52
mva 20,40,56
v_fma_f32 v4, v4, v120, v122
v_mul_f32 v123, v121, v53
mav 12,60,44
R 36,41984
v_fma_f32 v5, v5, v120, v123
)"
R"(
mav 16,64,44
v_mul_f32 v122, v121, v54
v_fma_f32 v6, v6, v120, v122
mva 20,44,68
v_mul_f32 v123, v121, v55
s_waitcnt lgkmcnt(2)
mav 12,72,24
R 40,43008
v_fma_f32 v7, v7, v120, v123
mav 16,76,24
mva 20,24,80
mav 12,84,28
R 44,44032
mav 16,88,28
mva 20,28,92
s_waitcnt lgkmcnt(2)
mav 12,96,32
R 24,45056
v_add_u32 v116, 1, v116
mav 16,100,32
mva 20,32,104
mav 12,108,36
R 28,46080
mav 16,112,36
mva 20,36,116
s_barrier
s_waitcnt lgkmcnt(2)
mav 12,120,40
ds_read_b128 v[68:71], v81 offset:0
ds_read_b64 v[102:103], v81 offset:1024
mav 16,124,40
R 32,47104
mva 20,40,128
s_waitcnt vmcnt(3)
buffer_store_dwordx2 v[116:117], off, s[36:39], s52 sc1
mav 12,132,44
R 36,48128
mav 16,136,44
mva 20,44,140
s_waitcnt lgkmcnt(4)
mav 12,144,24
R 40,49152
mav 16,148,24
s_waitcnt lgkmcnt(3)
v_mfma_f32_16x16x32_bf16 v[60:63], v[68:71], v[64:67], v[88:91]
v_cvt_pk_bf16_f32 v64, v0, v1
v_cvt_pk_bf16_f32 v65, v2, v3
mva 20,24,152
v_cvt_pk_bf16_f32 v66, v4, v5
v_cvt_pk_bf16_f32 v67, v6, v7
mav 12,156,28
R 24,50176
mav 16,160,28
v_max3_f32 v112, v56, v57, v60
v_max3_f32 v113, v58, v59, v62
mva 20,28,164
v_max_f32 v112, v112, v61
s_waitcnt lgkmcnt(2)
mav 12,168,32
R 28,51200
v_max_f32 v113, v113, v63
mav 16,172,32
v_max_f32 v114, v112, v113
v_max_f32 v115, v112, v113
mva 20,32,176
mav 12,180,36
R 32,52224
v_permlane32_swap_b32 v114, v115
mav 16,184,36
v_max_f32 v108, v114, v115
v_max_f32 v109, v114, v115
mva 20,36,188
s_waitcnt lgkmcnt(2)
mav 12,192,40
R 36,53248
v_permlane16_swap_b32 v108, v109
mav 16,196,40
v_max_f32 v114, v108, v109
v_mul_f32 v114, s65, v114
mva 20,40,200
v_fma_f32 v112, v112, s65, -v114
v_fma_f32 v113, v113, s65, -v114
mav 12,204,24
R 40,54272
mav 16,208,24
v_exp_f32 v112, v112
mva 20,24,212
s_waitcnt lgkmcnt(2)
mav 12,216,28
R 24,55296
mav 16,220,28
v_exp_f32 v113, v113
mva 20,28,224
v_add_f32 v114, v112, v113
v_add_f32 v115, v112, v113
mav 12,228,32
R 28,56320
v_permlane32_swap_b32 v114, v115
mav 16,232,32
v_add_f32 v108, v114, v115
v_add_f32 v109, v114, v115
mva 20,32,236
s_waitcnt lgkmcnt(2)
mav 12,240,36
R 32,57344
v_permlane16_swap_b32 v108, v109
mav 16,244,36
v_add_f32 v114, v108, v109
mva 20,36,248
v_rcp_f32 v114, v114
mav 12,252,40
R 36,58368
v_mul_f32 v112, v112, v114
mvv 16,128,40
v_mul_f32 v113, v113, v114
v_cvt_pk_bf16_f32 v112, v112, v112
mvv 20,40,132
v_cvt_pk_bf16_f32 v113, v113, v113
s_waitcnt lgkmcnt(2)
mvv 12,136,24
R 40,59392
v_and_b32 v108, v112, v94
mvv 16,140,24
v_and_b32 v109, v113, v95
v_and_b32 v110, v112, v92
mvv 20,24,144
v_and_b32 v111, v113, v93
mvv 12,148,28
R 24,60416
v_mfma_f32_16x16x32_bf16 v[56:59], v[96:99], v[108:111], 0
v_mfma_f32_16x16x32_bf16 v[60:63], v[102:105], v[108:111], 0
mvv 16,152,28
mvv 20,28,156
s_waitcnt lgkmcnt(2)
mvv 12,160,32
R 28,61440
mvv 16,164,32
mvv 20,32,168
v_cvt_pk_bf16_f32 v108, v56, v57
v_cvt_pk_bf16_f32 v109, v58, v59
mvv 12,172,36
R 32,62464
v_cvt_pk_bf16_f32 v110, v60, v61
mvv 16,176,36
v_cvt_pk_bf16_f32 v111, v62, v63
buffer_store_dwordx4 v[108:111], v81, s[36:39], s49 offen sc1
mvv 20,36,180
s_waitcnt lgkmcnt(2)
mvv 12,184,40
R 36,63488
mvv 16,188,40
mvv 20,40,192
mvv 12,196,24
R 40,64512
v_xor_b32 v80, 0x10000, v80
mvv 16,200,24
s_add_u32 s49, s49, 346112
mvv 20,24,204
s_barrier
s_waitcnt lgkmcnt(2)
mvv 12,208,28
R 24,0
mvv 16,212,28
ds_read_b64 v[120:121], v82 offset:128
ds_read_b128 v[56:59], v83 offset:128
mvv 20,28,216
ds_read_b128 v[60:63], v83 offset:144
mvv 12,220,32
R 28,1024
mvv 16,224,32
mvv 20,32,228
s_waitcnt lgkmcnt(5)
mvv 12,232,36
R 32,2048
mvv 16,236,36
mvv 20,36,240
mvv 12,244,40
R 36,3072
mvv 16,248,40
mvv 20,40,252
s_waitcnt lgkmcnt(2)
v_mfma_f32_16x16x32_bf16 v[0:3], a[0:3], v[24:27], 0
R 40,4096
v_mul_f32 v57, v57, v118
v_mfma_f32_16x16x32_bf16 v[4:7], a[4:7], v[24:27], 0
v_mul_f32 v59, v59, v118
v_mul_f32 v61, v61, v118
v_mfma_f32_16x16x32_bf16 v[8:11], v[24:27], a[8:11], 0
v_fma_f32 v20, v20, v56, v57
v_fma_f32 v21, v21, v58, v59
mav 0,12,28
R 44,5120
v_fma_f32 v22, v22, v60, v61
mav 4,16,28
v_mul_f32 v63, v63, v118
v_fma_f32 v23, v23, v62, v63
mva 8,28,20
v_cvt_pk_bf16_f32 v98, v20, v21
s_waitcnt lgkmcnt(2)
mav 0,24,32
R 24,6144
v_cvt_pk_bf16_f32 v99, v22, v23
mav 4,28,32
v_mul_f32 v122, v121, v48
v_mfma_f32_16x16x32_bf16 v[56:59], v[68:71], v[64:67], v[84:87]
v_fma_f32 v12, v12, v120, v122
v_mul_f32 v123, v121, v49
mva 8,32,32
v_fma_f32 v13, v13, v120, v123
v_mul_f32 v122, v121, v50
mav 0,36,36
R 28,7168
v_fma_f32 v14, v14, v120, v122
mav 4,40,36
v_mul_f32 v123, v121, v51
v_fma_f32 v15, v15, v120, v123
mva 8,36,44
v_mul_f32 v122, v121, v52
s_waitcnt lgkmcnt(2)
mav 0,48,40
R 32,8192
v_fma_f32 v16, v16, v120, v122
mav 4,52,40
v_mul_f32 v123, v121, v53
v_fma_f32 v17, v17, v120, v123
mva 8,40,56
v_mul_f32 v122, v121, v54
v_fma_f32 v18, v18, v120, v122
mav 0,60,44
R 36,9216
v_mul_f32 v123, v121, v55
mav 4,64,44
v_fma_f32 v19, v19, v120, v123
mva 8,44,68
s_waitcnt lgkmcnt(2)
mav 0,72,24
R 40,10240
mav 4,76,24
mva 8,24,80
mav 0,84,28
R 44,11264
mav 4,88,28
mva 8,28,92
s_waitcnt lgkmcnt(2)
mav 0,96,32
R 24,12288
v_add_u32 v116, 1, v116
mav 4,100,32
mva 8,32,104
mav 0,108,36
R 28,13312
mav 4,112,36
mva 8,36,116
s_barrier
s_waitcnt lgkmcnt(2)
mav 0,120,40
ds_read_b128 v[72:75], v81 offset:0
ds_read_b64 v[104:105], v81 offset:1024
mav 4,124,40
R 32,14336
mva 8,40,128
s_waitcnt vmcnt(0)
buffer_store_dwordx2 v[116:117], off, s[36:39], s52 sc1
mav 0,132,44
R 36,15360
mav 4,136,44
mva 8,44,140
s_waitcnt lgkmcnt(4)
mav 0,144,24
R 40,16384
mav 4,148,24
s_waitcnt lgkmcnt(3)
v_mfma_f32_16x16x32_bf16 v[60:63], v[72:75], v[64:67], v[88:91]
v_cvt_pk_bf16_f32 v64, v12, v13
v_cvt_pk_bf16_f32 v65, v14, v15
mva 8,24,152
v_cvt_pk_bf16_f32 v66, v16, v17
v_cvt_pk_bf16_f32 v67, v18, v19
mav 0,156,28
R 24,17408
mav 4,160,28
v_max3_f32 v112, v56, v57, v60
v_max3_f32 v113, v58, v59, v62
mva 8,28,164
v_max_f32 v112, v112, v61
s_waitcnt lgkmcnt(2)
mav 0,168,32
R 28,18432
v_max_f32 v113, v113, v63
mav 4,172,32
v_max_f32 v114, v112, v113
v_max_f32 v115, v112, v113
mva 8,32,176
mav 0,180,36
R 32,19456
v_permlane32_swap_b32 v114, v115
mav 4,184,36
v_max_f32 v108, v114, v115
v_max_f32 v109, v114, v115
mva 8,36,188
s_waitcnt lgkmcnt(2)
mav 0,192,40
R 36,20480
v_permlane16_swap_b32 v108, v109
mav 4,196,40
v_max_f32 v114, v108, v109
v_mul_f32 v114, s65, v114
mva 8,40,200
v_fma_f32 v112, v112, s65, -v114
v_fma_f32 v113, v113, s65, -v114
mav 0,204,24
R 40,21504
mav 4,208,24
v_exp_f32 v112, v112
mva 8,24,212
s_waitcnt lgkmcnt(2)
mav 0,216,28
R 24,22528
mav 4,220,28
v_exp_f32 v113, v113
mva 8,28,224
v_add_f32 v114, v112, v113
v_add_f32 v115, v112, v113
mav 0,228,32
R 28,23552
v_permlane32_swap_b32 v114, v115
mav 4,232,32
v_add_f32 v108, v114, v115
v_add_f32 v109, v114, v115
mva 8,32,236
s_waitcnt lgkmcnt(2)
mav 0,240,36
R 32,24576
v_permlane16_swap_b32 v108, v109
mav 4,244,36
v_add_f32 v114, v108, v109
mva 8,36,248
v_rcp_f32 v114, v114
mav 0,252,40
R 36,25600
v_mul_f32 v112, v112, v114
mvv 4,128,40
v_mul_f32 v113, v113, v114
v_cvt_pk_bf16_f32 v112, v112, v112
mvv 8,40,132
v_cvt_pk_bf16_f32 v113, v113, v113
s_waitcnt lgkmcnt(2)
mvv 0,136,24
R 40,26624
v_and_b32 v108, v112, v92
mvv 4,140,24
v_and_b32 v109, v113, v93
v_and_b32 v110, v112, v94
mvv 8,24,144
v_and_b32 v111, v113, v95
mvv 0,148,28
R 24,27648
v_mfma_f32_16x16x32_bf16 v[56:59], v[96:99], v[108:111], 0
v_mfma_f32_16x16x32_bf16 v[60:63], v[102:105], v[108:111], 0
mvv 4,152,28
mvv 8,28,156
s_waitcnt lgkmcnt(2)
mvv 0,160,32
R 28,28672
mvv 4,164,32
mvv 8,32,168
v_cvt_pk_bf16_f32 v108, v56, v57
v_cvt_pk_bf16_f32 v109, v58, v59
mvv 0,172,36
R 32,29696
v_cvt_pk_bf16_f32 v110, v60, v61
mvv 4,176,36
v_cvt_pk_bf16_f32 v111, v62, v63
buffer_store_dwordx4 v[108:111], v81, s[36:39], s49 offen sc1
mvv 8,36,180
s_waitcnt lgkmcnt(2)
mvv 0,184,40
R 36,30720
mvv 4,188,40
mvv 8,40,192
mvv 0,196,24
R 40,31744
mvv 4,200,24
s_add_u32 s49, s49, 346112
mvv 8,24,204
s_barrier
s_waitcnt lgkmcnt(2)
mvv 0,208,28
R 24,32768
mvv 4,212,28
ds_read_b64 v[120:121], v82 offset:0
ds_read_b128 v[56:59], v83 offset:0
mvv 8,28,216
ds_read_b128 v[60:63], v83 offset:16
mvv 0,220,32
R 28,33792
mvv 4,224,32
mvv 8,32,228
s_waitcnt lgkmcnt(5)
mvv 0,232,36
R 32,34816
mvv 4,236,36
mvv 8,36,240
mvv 0,244,40
R 36,35840
mvv 4,248,40
mvv 8,40,252
s_waitcnt lgkmcnt(2)
v_mfma_f32_16x16x32_bf16 v[12:15], a[0:3], v[24:27], 0
R 40,36864
v_mul_f32 v57, v57, v118
v_mfma_f32_16x16x32_bf16 v[16:19], a[4:7], v[24:27], 0
v_mul_f32 v59, v59, v118
v_mul_f32 v61, v61, v118
v_mfma_f32_16x16x32_bf16 v[20:23], v[24:27], a[8:11], 0
v_fma_f32 v8, v8, v56, v57
v_fma_f32 v9, v9, v58, v59
mav 12,12,28
R 44,37888
v_fma_f32 v10, v10, v60, v61
mav 16,16,28
v_mul_f32 v63, v63, v118
v_fma_f32 v11, v11, v62, v63
mva 20,28,20
v_cvt_pk_bf16_f32 v96, v8, v9
s_waitcnt lgkmcnt(2)
mav 12,24,32
R 24,38912
v_cvt_pk_bf16_f32 v97, v10, v11
mav 16,28,32
v_min_f32 v60, s56, v88
v_mfma_f32_16x16x32_bf16 v[56:59], v[72:75], v[64:67], v[84:87]
v_min_f32 v61, s56, v89
v_min_f32 v62, s56, v90
mva 20,32,32
v_min_f32 v63, s56, v91
v_mul_f32 v122, v121, v48
mav 12,36,36
R 28,39936
mvv 60,76,64
v_fma_f32 v0, v0, v120, v122
v_mul_f32 v123, v121, v49
mav 16,40,36
v_fma_f32 v1, v1, v120, v123
v_mul_f32 v122, v121, v50
mva 20,36,44
v_fma_f32 v2, v2, v120, v122
s_waitcnt lgkmcnt(2)
mav 12,48,40
R 32,40960
v_max3_f32 v112, v56, v57, v60
mav 16,52,40
v_max3_f32 v113, v58, v59, v62
v_max_f32 v112, v112, v61
mva 20,40,56
v_max_f32 v113, v113, v63
v_max_f32 v114, v112, v113
mav 12,60,44
R 36,41984
v_max_f32 v115, v112, v113
mav 16,64,44
v_mul_f32 v123, v121, v51
v_permlane32_swap_b32 v114, v115
mva 20,44,68
v_max_f32 v108, v114, v115
s_waitcnt lgkmcnt(2)
mav 12,72,24
R 40,43008
v_max_f32 v109, v114, v115
mav 16,76,24
v_fma_f32 v3, v3, v120, v123
v_permlane16_swap_b32 v108, v109
mva 20,24,80
v_max_f32 v114, v108, v109
v_mul_f32 v114, s65, v114
mav 12,84,28
R 44,44032
v_fma_f32 v112, v112, s65, -v114
mav 16,88,28
v_fma_f32 v113, v113, s65, -v114
v_mul_f32 v122, v121, v52
mva 20,28,92
v_fma_f32 v4, v4, v120, v122
s_waitcnt lgkmcnt(2)
mav 12,96,32
R 24,45056
v_mul_f32 v123, v121, v53
mav 16,100,32
v_exp_f32 v112, v112
mva 20,32,104
v_exp_f32 v113, v113
mav 12,108,36
R 28,46080
v_add_f32 v114, v112, v113
mav 16,112,36
v_add_f32 v115, v112, v113
v_fma_f32 v5, v5, v120, v123
mva 20,36,116
s_barrier
s_waitcnt lgkmcnt(2)
mav 12,120,40
ds_read_b128 v[68:71], v81 offset:0
ds_read_b64 v[102:103], v81 offset:1024
mav 16,124,40
R 32,47104
v_permlane32_swap_b32 v114, v115
mva 20,40,128
v_add_f32 v108, v114, v115
v_add_f32 v109, v114, v115
mav 12,132,44
R 36,48128
v_permlane16_swap_b32 v108, v109
mav 16,136,44
v_add_f32 v114, v108, v109
v_mul_f32 v122, v121, v54
mva 20,44,140
v_fma_f32 v6, v6, v120, v122
s_waitcnt lgkmcnt(4)
mav 12,144,24
R 40,49152
v_mul_f32 v123, v121, v55
mav 16,148,24
v_rcp_f32 v114, v114
mva 20,24,152
v_mul_f32 v112, v112, v114
v_mul_f32 v113, v113, v114
mav 12,156,28
R 24,50176
v_cvt_pk_bf16_f32 v112, v112, v112
mav 16,160,28
v_cvt_pk_bf16_f32 v113, v113, v113
v_and_b32 v108, v112, v92
mva 20,28,164
v_and_b32 v109, v113, v93
s_waitcnt lgkmcnt(2)
mav 12,168,32
R 28,51200
v_and_b32 v110, v112, v94
mav 16,172,32
v_and_b32 v111, v113, v95
buffer_store_dwordx4 v[68:71], v81, s[36:39], s54 offen offset:2048 sc1
mva 20,32,176
v_mfma_f32_16x16x32_bf16 v[56:59], v[98:101], v[108:111], 0
v_mfma_f32_16x16x32_bf16 v[60:63], v[104:107], v[108:111], 0
buffer_store_dwordx2 v[96:97], v81, s[36:39], s54 offen offset:3072 sc1
buffer_store_dwordx2 v[102:103], v81, s[36:39], s54 offen offset:3080 sc1
mav 12,180,36
R 32,52224
v_fma_f32 v7, v7, v120, v123
mav 16,184,36
v_cvt_pk_bf16_f32 v64, v0, v1
v_cvt_pk_bf16_f32 v108, v56, v57
mva 20,36,188
v_cvt_pk_bf16_f32 v109, v58, v59
s_waitcnt lgkmcnt(2)
mav 12,192,40
R 36,53248
v_cvt_pk_bf16_f32 v110, v60, v61
mav 16,196,40
v_cvt_pk_bf16_f32 v111, v62, v63
buffer_store_dwordx4 v[108:111], v81, s[36:39], s49 offen sc1
mva 20,40,200
v_cvt_pk_bf16_f32 v65, v2, v3
v_cvt_pk_bf16_f32 v66, v4, v5
mav 12,204,24
R 40,54272
v_cvt_pk_bf16_f32 v67, v6, v7
mav 16,208,24
v_add_u32 v116, 1, v116
mva 20,24,212
s_waitcnt vmcnt(4)
buffer_store_dwordx2 v[116:117], off, s[36:39], s52 sc1
s_waitcnt lgkmcnt(2)
mav 12,216,28
R 24,55296
mav 16,220,28
mva 20,28,224
mav 12,228,32
R 28,56320
mav 16,232,32
mva 20,32,236
s_waitcnt lgkmcnt(2)
mav 12,240,36
R 32,57344
mav 16,244,36
mva 20,36,248
mav 12,252,40
R 36,58368
mvv 16,128,40
mvv 20,40,132
s_waitcnt lgkmcnt(2)
mvv 12,136,24
R 40,59392
mvv 16,140,24
mvv 20,24,144
mvv 12,148,28
R 24,60416
mvv 16,152,28
mvv 20,28,156
s_waitcnt lgkmcnt(2)
mvv 12,160,32
R 28,61440
mvv 16,164,32
mvv 20,32,168
mvv 12,172,36
R 32,62464
mvv 16,176,36
mvv 20,36,180
s_waitcnt lgkmcnt(2)
mvv 12,184,40
R 36,63488
mvv 16,188,40
mvv 20,40,192
mvv 12,196,24
R 40,64512
v_xor_b32 v80, 0x10000, v80
mvv 16,200,24
s_add_u32 s49, s49, 346112
mvv 20,24,204
s_barrier
s_waitcnt lgkmcnt(2)
mvv 12,208,28
R 24,0
mvv 16,212,28
ds_read_b64 v[120:121], v82 offset:128
ds_read_b128 v[56:59], v83 offset:128
mvv 20,28,216
ds_read_b128 v[60:63], v83 offset:144
mvv 12,220,32
R 28,1024
mvv 16,224,32
mvv 20,32,228
s_waitcnt lgkmcnt(5)
mvv 12,232,36
R 32,2048
mvv 16,236,36
mvv 20,36,240
mvv 12,244,40
R 36,3072
mvv 16,248,40
mvv 20,40,252
s_waitcnt lgkmcnt(2)
v_mfma_f32_16x16x32_bf16 v[0:3], a[0:3], v[24:27], 0
R 40,4096
v_mul_f32 v57, v57, v118
v_mfma_f32_16x16x32_bf16 v[4:7], a[4:7], v[24:27], 0
v_mul_f32 v59, v59, v118
v_mul_f32 v61, v61, v118
v_mfma_f32_16x16x32_bf16 v[8:11], v[24:27], a[8:11], 0
v_fma_f32 v20, v20, v56, v57
v_fma_f32 v21, v21, v58, v59
mav 0,12,28
R 44,5120
v_fma_f32 v22, v22, v60, v61
mav 4,16,28
v_mul_f32 v63, v63, v118
v_fma_f32 v23, v23, v62, v63
mva 8,28,20
v_cvt_pk_bf16_f32 v98, v20, v21
s_waitcnt lgkmcnt(2)
mav 0,24,32
R 24,6144
v_cvt_pk_bf16_f32 v99, v22, v23
mav 4,28,32
v_mul_f32 v122, v121, v48
v_mfma_f32_16x16x32_bf16 v[56:59], v[68:71], v[64:67], v[84:87]
v_fma_f32 v12, v12, v120, v122
v_mul_f32 v123, v121, v49
mva 8,32,32
v_fma_f32 v13, v13, v120, v123
v_mul_f32 v122, v121, v50
mav 0,36,36
R 28,7168
v_fma_f32 v14, v14, v120, v122
mav 4,40,36
v_mul_f32 v123, v121, v51
v_fma_f32 v15, v15, v120, v123
mva 8,36,44
v_mul_f32 v122, v121, v52
s_waitcnt lgkmcnt(2)
mav 0,48,40
R 32,8192
v_fma_f32 v16, v16, v120, v122
mav 4,52,40
v_mul_f32 v123, v121, v53
v_fma_f32 v17, v17, v120, v123
mva 8,40,56
v_mul_f32 v122, v121, v54
v_fma_f32 v18, v18, v120, v122
mav 0,60,44
R 36,9216
v_mul_f32 v123, v121, v55
mav 4,64,44
v_fma_f32 v19, v19, v120, v123
mva 8,44,68
s_waitcnt lgkmcnt(2)
mav 0,72,24
R 40,10240
mav 4,76,24
mva 8,24,80
mav 0,84,28
R 44,11264
mav 4,88,28
mva 8,28,92
s_waitcnt lgkmcnt(2)
mav 0,96,32
R 24,12288
v_add_u32 v116, 1, v116
mav 4,100,32
mva 8,32,104
mav 0,108,36
R 28,13312
mav 4,112,36
mva 8,36,116
s_barrier
s_waitcnt lgkmcnt(2)
mav 0,120,40
ds_read_b128 v[72:75], v81 offset:0
ds_read_b64 v[104:105], v81 offset:1024
mav 4,124,40
R 32,14336
mva 8,40,128
s_waitcnt vmcnt(1)
buffer_store_dwordx2 v[116:117], off, s[36:39], s52 sc1
mav 0,132,44
R 36,15360
mav 4,136,44
mva 8,44,140
s_waitcnt lgkmcnt(4)
mav 0,144,24
R 40,16384
mav 4,148,24
s_waitcnt lgkmcnt(3)
v_mfma_f32_16x16x32_bf16 v[60:63], v[72:75], v[64:67], v[88:91]
v_cvt_pk_bf16_f32 v64, v12, v13
v_cvt_pk_bf16_f32 v65, v14, v15
mva 8,24,152
v_cvt_pk_bf16_f32 v66, v16, v17
v_cvt_pk_bf16_f32 v67, v18, v19
mav 0,156,28
R 24,17408
mav 4,160,28
v_max3_f32 v112, v56, v57, v60
v_max3_f32 v113, v58, v59, v62
mva 8,28,164
v_max_f32 v112, v112, v61
s_waitcnt lgkmcnt(2)
mav 0,168,32
R 28,18432
v_max_f32 v113, v113, v63
mav 4,172,32
v_max_f32 v114, v112, v113
v_max_f32 v115, v112, v113
mva 8,32,176
mav 0,180,36
R 32,19456
v_permlane32_swap_b32 v114, v115
mav 4,184,36
v_max_f32 v108, v114, v115
v_max_f32 v109, v114, v115
mva 8,36,188
s_waitcnt lgkmcnt(2)
mav 0,192,40
R 36,20480
v_permlane16_swap_b32 v108, v109
mav 4,196,40
v_max_f32 v114, v108, v109
v_mul_f32 v114, s65, v114
mva 8,40,200
v_fma_f32 v112, v112, s65, -v114
v_fma_f32 v113, v113, s65, -v114
mav 0,204,24
R 40,21504
mav 4,208,24
v_exp_f32 v112, v112
mva 8,24,212
s_waitcnt lgkmcnt(2)
mav 0,216,28
R 24,22528
mav 4,220,28
v_exp_f32 v113, v113
mva 8,28,224
v_add_f32 v114, v112, v113
v_add_f32 v115, v112, v113
mav 0,228,32
R 28,23552
v_permlane32_swap_b32 v114, v115
mav 4,232,32
v_add_f32 v108, v114, v115
v_add_f32 v109, v114, v115
mva 8,32,236
s_waitcnt lgkmcnt(2)
mav 0,240,36
R 32,24576
v_permlane16_swap_b32 v108, v109
mav 4,244,36
v_add_f32 v114, v108, v109
mva 8,36,248
v_rcp_f32 v114, v114
mav 0,252,40
R 36,25600
v_mul_f32 v112, v112, v114
mvv 4,128,40
v_mul_f32 v113, v113, v114
v_cvt_pk_bf16_f32 v112, v112, v112
mvv 8,40,132
v_cvt_pk_bf16_f32 v113, v113, v113
s_waitcnt lgkmcnt(2)
mvv 0,136,24
R 40,26624
v_and_b32 v108, v112, v92
mvv 4,140,24
v_and_b32 v109, v113, v93
v_and_b32 v110, v112, v94
mvv 8,24,144
v_and_b32 v111, v113, v95
mvv 0,148,28
R 24,27648
v_mfma_f32_16x16x32_bf16 v[56:59], v[96:99], v[108:111], 0
v_mfma_f32_16x16x32_bf16 v[60:63], v[102:105], v[108:111], 0
mvv 4,152,28
mvv 8,28,156
s_waitcnt lgkmcnt(2)
mvv 0,160,32
R 28,28672
mvv 4,164,32
mvv 8,32,168
v_cvt_pk_bf16_f32 v108, v56, v57
v_cvt_pk_bf16_f32 v109, v58, v59
mvv 0,172,36
R 32,29696
v_cvt_pk_bf16_f32 v110, v60, v61
mvv 4,176,36
v_cvt_pk_bf16_f32 v111, v62, v63
buffer_store_dwordx4 v[108:111], v81, s[36:39], s49 offen sc1
mvv 8,36,180
s_waitcnt lgkmcnt(2)
mvv 0,184,40
R 36,30720
mvv 4,188,40
mvv 8,40,192
mvv 0,196,24
R 40,31744
mvv 4,200,24
s_add_u32 s49, s49, 346112
mvv 8,24,204
s_barrier
s_waitcnt lgkmcnt(2)
mvv 0,208,28
R 24,32768
mvv 4,212,28
ds_read_b64 v[120:121], v82 offset:0
ds_read_b128 v[56:59], v83 offset:0
mvv 8,28,216
ds_read_b128 v[60:63], v83 offset:16
mvv 0,220,32
R 28,33792
mvv 4,224,32
mvv 8,32,228
s_waitcnt lgkmcnt(5)
mvv 0,232,36
R 32,34816
mvv 4,236,36
mvv 8,36,240
mvv 0,244,40
R 36,35840
mvv 4,248,40
mvv 8,40,252
s_waitcnt lgkmcnt(2)
v_mfma_f32_16x16x32_bf16 v[12:15], a[0:3], v[24:27], 0
R 40,36864
v_mul_f32 v57, v57, v118
v_mfma_f32_16x16x32_bf16 v[16:19], a[4:7], v[24:27], 0
v_mul_f32 v59, v59, v118
v_mul_f32 v61, v61, v118
v_mfma_f32_16x16x32_bf16 v[20:23], v[24:27], a[8:11], 0
v_fma_f32 v8, v8, v56, v57
v_fma_f32 v9, v9, v58, v59
mav 12,12,28
R 44,37888
v_fma_f32 v10, v10, v60, v61
mav 16,16,28
v_mul_f32 v63, v63, v118
v_fma_f32 v11, v11, v62, v63
mva 20,28,20
v_cvt_pk_bf16_f32 v96, v8, v9
s_waitcnt lgkmcnt(2)
mav 12,24,32
R 24,38912
v_cvt_pk_bf16_f32 v97, v10, v11
mav 16,28,32
v_mul_f32 v122, v121, v48
v_mfma_f32_16x16x32_bf16 v[56:59], v[72:75], v[64:67], v[84:87]
v_fma_f32 v0, v0, v120, v122
v_mul_f32 v123, v121, v49
mva 20,32,32
v_fma_f32 v1, v1, v120, v123
v_mul_f32 v122, v121, v50
mav 12,36,36
R 28,39936
v_fma_f32 v2, v2, v120, v122
mav 16,40,36
v_mul_f32 v123, v121, v51
v_fma_f32 v3, v3, v120, v123
mva 20,36,44
v_mul_f32 v122, v121, v52
s_waitcnt lgkmcnt(2)
mav 12,48,40
R 32,40960
v_fma_f32 v4, v4, v120, v122
mav 16,52,40
v_mul_f32 v123, v121, v53
v_fma_f32 v5, v5, v120, v123
mva 20,40,56
v_mul_f32 v122, v121, v54
v_fma_f32 v6, v6, v120, v122
mav 12,60,44
R 36,41984
v_mul_f32 v123, v121, v55
mav 16,64,44
v_fma_f32 v7, v7, v120, v123
mva 20,44,68
s_waitcnt lgkmcnt(2)
mav 12,72,24
R 40,43008
mav 16,76,24
mva 20,24,80
mav 12,84,28
R 44,44032
mav 16,88,28
mva 20,28,92
s_waitcnt lgkmcnt(2)
mav 12,96,32
R 24,45056
v_add_u32 v116, 1, v116
mav 16,100,32
mva 20,32,104
mav 12,108,36
R 28,46080
mav 16,112,36
mva 20,36,116
s_barrier
s_waitcnt lgkmcnt(2)
mav 12,120,40
ds_read_b128 v[68:71], v81 offset:0
ds_read_b64 v[102:103], v81 offset:1024
mav 16,124,40
R 32,47104
mva 20,40,128
s_waitcnt vmcnt(0)
buffer_store_dwordx2 v[116:117], off, s[36:39], s52 sc1
mav 12,132,44
R 36,48128
mav 16,136,44
mva 20,44,140
s_waitcnt lgkmcnt(4)
mav 12,144,24
R 40,49152
mav 16,148,24
s_waitcnt lgkmcnt(3)
v_mfma_f32_16x16x32_bf16 v[60:63], v[68:71], v[64:67], v[88:91]
v_cvt_pk_bf16_f32 v64, v0, v1
v_cvt_pk_bf16_f32 v65, v2, v3
mva 20,24,152
v_cvt_pk_bf16_f32 v66, v4, v5
v_cvt_pk_bf16_f32 v67, v6, v7
mav 12,156,28
R 24,50176
mav 16,160,28
v_max3_f32 v112, v56, v57, v60
v_max3_f32 v113, v58, v59, v62
mva 20,28,164
v_max_f32 v112, v112, v61
s_waitcnt lgkmcnt(2)
mav 12,168,32
R 28,51200
v_max_f32 v113, v113, v63
mav 16,172,32
v_max_f32 v114, v112, v113
v_max_f32 v115, v112, v113
mva 20,32,176
mav 12,180,36
R 32,52224
v_permlane32_swap_b32 v114, v115
mav 16,184,36
v_max_f32 v108, v114, v115
v_max_f32 v109, v114, v115
mva 20,36,188
s_waitcnt lgkmcnt(2)
mav 12,192,40
R 36,53248
v_permlane16_swap_b32 v108, v109
mav 16,196,40
v_max_f32 v114, v108, v109
v_mul_f32 v114, s65, v114
mva 20,40,200
v_fma_f32 v112, v112, s65, -v114
v_fma_f32 v113, v113, s65, -v114
mav 12,204,24
R 40,54272
mav 16,208,24
v_exp_f32 v112, v112
mva 20,24,212
s_waitcnt lgkmcnt(2)
mav 12,216,28
R 24,55296
mav 16,220,28
v_exp_f32 v113, v113
mva 20,28,224
v_add_f32 v114, v112, v113
v_add_f32 v115, v112, v113
mav 12,228,32
R 28,56320
v_permlane32_swap_b32 v114, v115
mav 16,232,32
v_add_f32 v108, v114, v115
v_add_f32 v109, v114, v115
mva 20,32,236
s_waitcnt lgkmcnt(2)
mav 12,240,36
R 32,57344
v_permlane16_swap_b32 v108, v109
mav 16,244,36
v_add_f32 v114, v108, v109
mva 20,36,248
v_rcp_f32 v114, v114
mav 12,252,40
R 36,58368
v_mul_f32 v112, v112, v114
mvv 16,128,40
v_mul_f32 v113, v113, v114
v_cvt_pk_bf16_f32 v112, v112, v112
mvv 20,40,132
v_cvt_pk_bf16_f32 v113, v113, v113
s_waitcnt lgkmcnt(2)
mvv 12,136,24
R 40,59392
v_and_b32 v108, v112, v94
mvv 16,140,24
v_and_b32 v109, v113, v95
v_and_b32 v110, v112, v92
mvv 20,24,144
v_and_b32 v111, v113, v93
mvv 12,148,28
R 24,60416
v_mfma_f32_16x16x32_bf16 v[56:59], v[96:99], v[108:111], 0
v_mfma_f32_16x16x32_bf16 v[60:63], v[102:105], v[108:111], 0
mvv 16,152,28
mvv 20,28,156
s_waitcnt lgkmcnt(2)
mvv 12,160,32
R 28,61440
mvv 16,164,32
mvv 20,32,168
v_cvt_pk_bf16_f32 v108, v56, v57
v_cvt_pk_bf16_f32 v109, v58, v59
mvv 12,172,36
R 32,62464
v_cvt_pk_bf16_f32 v110, v60, v61
mvv 16,176,36
v_cvt_pk_bf16_f32 v111, v62, v63
buffer_store_dwordx4 v[108:111], v81, s[36:39], s49 offen sc1
mvv 20,36,180
s_waitcnt lgkmcnt(2)
mvv 12,184,40
R 36,63488
mvv 16,188,40
mvv 20,40,192
mvv 12,196,24
R 40,64512
v_xor_b32 v80, 0x10000, v80
mvv 16,200,24
s_add_u32 s49, s49, 346112
mvv 20,24,204
s_barrier
s_waitcnt lgkmcnt(2)
mvv 12,208,28
R 24,0
mvv 16,212,28
ds_read_b64 v[120:121], v82 offset:128
ds_read_b128 v[56:59], v83 offset:128
mvv 20,28,216
ds_read_b128 v[60:63], v83 offset:144
mvv 12,220,32
R 28,1024
mvv 16,224,32
mvv 20,32,228
s_waitcnt lgkmcnt(5)
mvv 12,232,36
R 32,2048
mvv 16,236,36
mvv 20,36,240
mvv 12,244,40
R 36,3072
mvv 16,248,40
mvv 20,40,252
s_mov_b32 s60, -13
.p2align 6
LQA_2:
s_waitcnt lgkmcnt(2)
v_mfma_f32_16x16x32_bf16 v[0:3], a[0:3], v[24:27], 0
R 40,4096
v_mul_f32 v57, v57, v118
v_mfma_f32_16x16x32_bf16 v[4:7], a[4:7], v[24:27], 0
v_mul_f32 v59, v59, v118
v_mul_f32 v61, v61, v118
v_mfma_f32_16x16x32_bf16 v[8:11], v[24:27], a[8:11], 0
v_fma_f32 v20, v20, v56, v57
v_fma_f32 v21, v21, v58, v59
mav 0,12,28
R 44,5120
v_fma_f32 v22, v22, v60, v61
mav 4,16,28
v_mul_f32 v63, v63, v118
v_fma_f32 v23, v23, v62, v63
mva 8,28,20
v_cvt_pk_bf16_f32 v98, v20, v21
s_waitcnt lgkmcnt(2)
mav 0,24,32
R 24,6144
v_cvt_pk_bf16_f32 v99, v22, v23
mav 4,28,32
v_mul_f32 v122, v121, v48
v_mfma_f32_16x16x32_bf16 v[56:59], v[68:71], v[64:67], v[84:87]
v_fma_f32 v12, v12, v120, v122
v_mul_f32 v123, v121, v49
mva 8,32,32
v_fma_f32 v13, v13, v120, v123
v_mul_f32 v122, v121, v50
mav 0,36,36
R 28,7168
v_fma_f32 v14, v14, v120, v122
mav 4,40,36
v_mul_f32 v123, v121, v51
v_fma_f32 v15, v15, v120, v123
mva 8,36,44
v_mul_f32 v122, v121, v52
s_waitcnt lgkmcnt(2)
mav 0,48,40
R 32,8192
v_fma_f32 v16, v16, v120, v122
mav 4,52,40
v_mul_f32 v123, v121, v53
v_fma_f32 v17, v17, v120, v123
mva 8,40,56
v_mul_f32 v122, v121, v54
v_fma_f32 v18, v18, v120, v122
mav 0,60,44
R 36,9216
v_mul_f32 v123, v121, v55
mav 4,64,44
v_fma_f32 v19, v19, v120, v123
mva 8,44,68
s_waitcnt lgkmcnt(2)
mav 0,72,24
R 40,10240
mav 4,76,24
mva 8,24,80
mav 0,84,28
R 44,11264
mav 4,88,28
mva 8,28,92
s_waitcnt lgkmcnt(2)
mav 0,96,32
R 24,12288
v_add_u32 v116, 1, v116
mav 4,100,32
mva 8,32,104
mav 0,108,36
R 28,13312
mav 4,112,36
mva 8,36,116
s_barrier
s_waitcnt lgkmcnt(2)
mav 0,120,40
ds_read_b128 v[72:75], v81 offset:0
ds_read_b64 v[104:105], v81 offset:1024
mav 4,124,40
R 32,14336
mva 8,40,128
s_waitcnt vmcnt(0)
buffer_store_dwordx2 v[116:117], off, s[36:39], s52 sc1
mav 0,132,44
R 36,15360
mav 4,136,44
mva 8,44,140
s_waitcnt lgkmcnt(4)
mav 0,144,24
R 40,16384
mav 4,148,24
s_waitcnt lgkmcnt(3)
v_mfma_f32_16x16x32_bf16 v[60:63], v[72:75], v[64:67], v[88:91]
v_cvt_pk_bf16_f32 v64, v12, v13
v_cvt_pk_bf16_f32 v65, v14, v15
mva 8,24,152
v_cvt_pk_bf16_f32 v66, v16, v17
v_cvt_pk_bf16_f32 v67, v18, v19
mav 0,156,28
R 24,17408
mav 4,160,28
v_max3_f32 v112, v56, v57, v60
v_max3_f32 v113, v58, v59, v62
mva 8,28,164
v_max_f32 v112, v112, v61
s_waitcnt lgkmcnt(2)
mav 0,168,32
R 28,18432
v_max_f32 v113, v113, v63
mav 4,172,32
v_max_f32 v114, v112, v113
v_max_f32 v115, v112, v113
mva 8,32,176
mav 0,180,36
R 32,19456
v_permlane32_swap_b32 v114, v115
mav 4,184,36
v_max_f32 v108, v114, v115
v_max_f32 v109, v114, v115
mva 8,36,188
s_waitcnt lgkmcnt(2)
mav 0,192,40
R 36,20480
v_permlane16_swap_b32 v108, v109
mav 4,196,40
v_max_f32 v114, v108, v109
v_mul_f32 v114, s65, v114
mva 8,40,200
v_fma_f32 v112, v112, s65, -v114
v_fma_f32 v113, v113, s65, -v114
mav 0,204,24
R 40,21504
mav 4,208,24
v_exp_f32 v112, v112
mva 8,24,212
s_waitcnt lgkmcnt(2)
mav 0,216,28
R 24,22528
mav 4,220,28
v_exp_f32 v113, v113
mva 8,28,224
v_add_f32 v114, v112, v113
v_add_f32 v115, v112, v113
mav 0,228,32
R 28,23552
v_permlane32_swap_b32 v114, v115
mav 4,232,32
v_add_f32 v108, v114, v115
v_add_f32 v109, v114, v115
mva 8,32,236
s_waitcnt lgkmcnt(2)
mav 0,240,36
R 32,24576
v_permlane16_swap_b32 v108, v109
mav 4,244,36
v_add_f32 v114, v108, v109
mva 8,36,248
v_rcp_f32 v114, v114
mav 0,252,40
R 36,25600
v_mul_f32 v112, v112, v114
mvv 4,128,40
v_mul_f32 v113, v113, v114
v_cvt_pk_bf16_f32 v112, v112, v112
mvv 8,40,132
v_cvt_pk_bf16_f32 v113, v113, v113
s_waitcnt lgkmcnt(2)
mvv 0,136,24
R 40,26624
v_and_b32 v108, v112, v92
mvv 4,140,24
v_and_b32 v109, v113, v93
v_and_b32 v110, v112, v94
mvv 8,24,144
v_and_b32 v111, v113, v95
mvv 0,148,28
R 24,27648
v_mfma_f32_16x16x32_bf16 v[56:59], v[96:99], v[108:111], 0
v_mfma_f32_16x16x32_bf16 v[60:63], v[102:105], v[108:111], 0
mvv 4,152,28
mvv 8,28,156
s_waitcnt lgkmcnt(2)
mvv 0,160,32
R 28,28672
mvv 4,164,32
mvv 8,32,168
v_cvt_pk_bf16_f32 v108, v56, v57
v_cvt_pk_bf16_f32 v109, v58, v59
mvv 0,172,36
R 32,29696
v_cvt_pk_bf16_f32 v110, v60, v61
mvv 4,176,36
v_cvt_pk_bf16_f32 v111, v62, v63
buffer_store_dwordx4 v[108:111], v81, s[36:39], s49 offen sc1
mvv 8,36,180
s_waitcnt lgkmcnt(2)
mvv 0,184,40
R 36,30720
mvv 4,188,40
mvv 8,40,192
mvv 0,196,24
R 40,31744
mvv 4,200,24
s_add_u32 s49, s49, 346112
mvv 8,24,204
s_barrier
s_waitcnt lgkmcnt(2)
mvv 0,208,28
R 24,32768
mvv 4,212,28
ds_read_b64 v[120:121], v82 offset:0
ds_read_b128 v[56:59], v83 offset:0
mvv 8,28,216
ds_read_b128 v[60:63], v83 offset:16
mvv 0,220,32
R 28,33792
mvv 4,224,32
mvv 8,32,228
s_waitcnt lgkmcnt(5)
mvv 0,232,36
R 32,34816
mvv 4,236,36
mvv 8,36,240
mvv 0,244,40
R 36,35840
mvv 4,248,40
mvv 8,40,252
s_waitcnt lgkmcnt(2)
v_mfma_f32_16x16x32_bf16 v[12:15], a[0:3], v[24:27], 0
R 40,36864
v_mul_f32 v57, v57, v118
v_mfma_f32_16x16x32_bf16 v[16:19], a[4:7], v[24:27], 0
v_mul_f32 v59, v59, v118
v_mul_f32 v61, v61, v118
v_mfma_f32_16x16x32_bf16 v[20:23], v[24:27], a[8:11], 0
v_fma_f32 v8, v8, v56, v57
v_fma_f32 v9, v9, v58, v59
mav 12,12,28
R 44,37888
v_fma_f32 v10, v10, v60, v61
mav 16,16,28
v_mul_f32 v63, v63, v118
v_fma_f32 v11, v11, v62, v63
mva 20,28,20
v_cvt_pk_bf16_f32 v96, v8, v9
s_waitcnt lgkmcnt(2)
mav 12,24,32
R 24,38912
v_cvt_pk_bf16_f32 v97, v10, v11
mav 16,28,32
v_mul_f32 v122, v121, v48
v_mfma_f32_16x16x32_bf16 v[56:59], v[72:75], v[64:67], v[84:87]
v_fma_f32 v0, v0, v120, v122
v_mul_f32 v123, v121, v49
mva 20,32,32
v_fma_f32 v1, v1, v120, v123
v_mul_f32 v122, v121, v50
mav 12,36,36
R 28,39936
v_fma_f32 v2, v2, v120, v122
mav 16,40,36
v_mul_f32 v123, v121, v51
v_fma_f32 v3, v3, v120, v123
mva 20,36,44
v_mul_f32 v122, v121, v52
s_waitcnt lgkmcnt(2)
mav 12,48,40
R 32,40960
v_fma_f32 v4, v4, v120, v122
mav 16,52,40
v_mul_f32 v123, v121, v53
v_fma_f32 v5, v5, v120, v123
mva 20,40,56
v_mul_f32 v122, v121, v54
v_fma_f32 v6, v6, v120, v122
mav 12,60,44
R 36,41984
v_mul_f32 v123, v121, v55
mav 16,64,44
v_fma_f32 v7, v7, v120, v123
mva 20,44,68
s_waitcnt lgkmcnt(2)
mav 12,72,24
R 40,43008
mav 16,76,24
mva 20,24,80
mav 12,84,28
R 44,44032
mav 16,88,28
mva 20,28,92
s_waitcnt lgkmcnt(2)
mav 12,96,32
R 24,45056
v_add_u32 v116, 1, v116
mav 16,100,32
mva 20,32,104
mav 12,108,36
R 28,46080
mav 16,112,36
mva 20,36,116
s_barrier
s_waitcnt lgkmcnt(2)
mav 12,120,40
ds_read_b128 v[68:71], v81 offset:0
ds_read_b64 v[102:103], v81 offset:1024
mav 16,124,40
R 32,47104
mva 20,40,128
s_waitcnt vmcnt(0)
buffer_store_dwordx2 v[116:117], off, s[36:39], s52 sc1
mav 12,132,44
R 36,48128
mav 16,136,44
mva 20,44,140
s_waitcnt lgkmcnt(4)
mav 12,144,24
R 40,49152
mav 16,148,24
s_waitcnt lgkmcnt(3)
v_mfma_f32_16x16x32_bf16 v[60:63], v[68:71], v[64:67], v[88:91]
v_cvt_pk_bf16_f32 v64, v0, v1
v_cvt_pk_bf16_f32 v65, v2, v3
mva 20,24,152
v_cvt_pk_bf16_f32 v66, v4, v5
v_cvt_pk_bf16_f32 v67, v6, v7
mav 12,156,28
R 24,50176
mav 16,160,28
v_max3_f32 v112, v56, v57, v60
v_max3_f32 v113, v58, v59, v62
mva 20,28,164
v_max_f32 v112, v112, v61
s_waitcnt lgkmcnt(2)
mav 12,168,32
R 28,51200
v_max_f32 v113, v113, v63
mav 16,172,32
v_max_f32 v114, v112, v113
v_max_f32 v115, v112, v113
mva 20,32,176
mav 12,180,36
R 32,52224
v_permlane32_swap_b32 v114, v115
mav 16,184,36
v_max_f32 v108, v114, v115
v_max_f32 v109, v114, v115
mva 20,36,188
s_waitcnt lgkmcnt(2)
mav 12,192,40
R 36,53248
v_permlane16_swap_b32 v108, v109
mav 16,196,40
v_max_f32 v114, v108, v109
v_mul_f32 v114, s65, v114
mva 20,40,200
v_fma_f32 v112, v112, s65, -v114
v_fma_f32 v113, v113, s65, -v114
mav 12,204,24
R 40,54272
mav 16,208,24
v_exp_f32 v112, v112
mva 20,24,212
s_waitcnt lgkmcnt(2)
mav 12,216,28
R 24,55296
mav 16,220,28
v_exp_f32 v113, v113
mva 20,28,224
v_add_f32 v114, v112, v113
v_add_f32 v115, v112, v113
mav 12,228,32
R 28,56320
v_permlane32_swap_b32 v114, v115
mav 16,232,32
v_add_f32 v108, v114, v115
v_add_f32 v109, v114, v115
mva 20,32,236
s_waitcnt lgkmcnt(2)
mav 12,240,36
R 32,57344
v_permlane16_swap_b32 v108, v109
mav 16,244,36
v_add_f32 v114, v108, v109
mva 20,36,248
v_rcp_f32 v114, v114
mav 12,252,40
R 36,58368
v_mul_f32 v112, v112, v114
mvv 16,128,40
v_mul_f32 v113, v113, v114
v_cvt_pk_bf16_f32 v112, v112, v112
mvv 20,40,132
v_cvt_pk_bf16_f32 v113, v113, v113
s_waitcnt lgkmcnt(2)
mvv 12,136,24
R 40,59392
v_and_b32 v108, v112, v94
mvv 16,140,24
v_and_b32 v109, v113, v95
v_and_b32 v110, v112, v92
mvv 20,24,144
v_and_b32 v111, v113, v93
mvv 12,148,28
R 24,60416
v_mfma_f32_16x16x32_bf16 v[56:59], v[96:99], v[108:111], 0
v_mfma_f32_16x16x32_bf16 v[60:63], v[102:105], v[108:111], 0
mvv 16,152,28
mvv 20,28,156
s_waitcnt lgkmcnt(2)
mvv 12,160,32
R 28,61440
mvv 16,164,32
mvv 20,32,168
v_cvt_pk_bf16_f32 v108, v56, v57
v_cvt_pk_bf16_f32 v109, v58, v59
mvv 12,172,36
R 32,62464
v_cvt_pk_bf16_f32 v110, v60, v61
mvv 16,176,36
v_cvt_pk_bf16_f32 v111, v62, v63
buffer_store_dwordx4 v[108:111], v81, s[36:39], s49 offen sc1
mvv 20,36,180
s_waitcnt lgkmcnt(2)
mvv 12,184,40
R 36,63488
mvv 16,188,40
mvv 20,40,192
mvv 12,196,24
R 40,64512
v_xor_b32 v80, 0x10000, v80
mvv 16,200,24
s_add_u32 s49, s49, 346112
mvv 20,24,204
s_barrier
s_waitcnt lgkmcnt(2)
mvv 12,208,28
R 24,0
mvv 16,212,28
ds_read_b64 v[120:121], v82 offset:128
ds_read_b128 v[56:59], v83 offset:128
mvv 20,28,216
ds_read_b128 v[60:63], v83 offset:144
mvv 12,220,32
R 28,1024
mvv 16,224,32
mvv 20,32,228
s_waitcnt lgkmcnt(5)
mvv 12,232,36
R 32,2048
mvv 16,236,36
mvv 20,36,240
mvv 12,244,40
R 36,3072
mvv 16,248,40
mvv 20,40,252
s_add_u32 s60, s60, 1
s_cbranch_scc0 LQA_2
s_waitcnt lgkmcnt(2)
v_mfma_f32_16x16x32_bf16 v[0:3], a[0:3], v[24:27], 0
R 40,4096
v_mul_f32 v57, v57, v118
v_mfma_f32_16x16x32_bf16 v[4:7], a[4:7], v[24:27], 0
v_mul_f32 v59, v59, v118
v_mul_f32 v61, v61, v118
v_mfma_f32_16x16x32_bf16 v[8:11], v[24:27], a[8:11], 0
v_fma_f32 v20, v20, v56, v57
v_fma_f32 v21, v21, v58, v59
mav 0,12,28
R 44,5120
v_fma_f32 v22, v22, v60, v61
mav 4,16,28
v_mul_f32 v63, v63, v118
v_fma_f32 v23, v23, v62, v63
mva 8,28,20
v_cvt_pk_bf16_f32 v98, v20, v21
s_waitcnt lgkmcnt(2)
mav 0,24,32
R 24,6144
v_cvt_pk_bf16_f32 v99, v22, v23
mav 4,28,32
v_mul_f32 v122, v121, v48
v_mfma_f32_16x16x32_bf16 v[56:59], v[68:71], v[64:67], v[84:87]
v_fma_f32 v12, v12, v120, v122
v_mul_f32 v123, v121, v49
mva 8,32,32
v_fma_f32 v13, v13, v120, v123
v_mul_f32 v122, v121, v50
mav 0,36,36
R 28,7168
v_fma_f32 v14, v14, v120, v122
mav 4,40,36
v_mul_f32 v123, v121, v51
v_fma_f32 v15, v15, v120, v123
mva 8,36,44
v_mul_f32 v122, v121, v52
s_waitcnt lgkmcnt(2)
mav 0,48,40
R 32,8192
v_fma_f32 v16, v16, v120, v122
mav 4,52,40
v_mul_f32 v123, v121, v53
v_fma_f32 v17, v17, v120, v123
mva 8,40,56
v_mul_f32 v122, v121, v54
v_fma_f32 v18, v18, v120, v122
mav 0,60,44
R 36,9216
v_mul_f32 v123, v121, v55
mav 4,64,44
v_fma_f32 v19, v19, v120, v123
mva 8,44,68
s_waitcnt lgkmcnt(2)
mav 0,72,24
R 40,10240
mav 4,76,24
mva 8,24,80
mav 0,84,28
R 44,11264
mav 4,88,28
mva 8,28,92
s_waitcnt lgkmcnt(2)
mav 0,96,32
R 24,12288
v_add_u32 v116, 1, v116
mav 4,100,32
mva 8,32,104
mav 0,108,36
R 28,13312
mav 4,112,36
mva 8,36,116
s_barrier
s_waitcnt lgkmcnt(2)
mav 0,120,40
ds_read_b128 v[72:75], v81 offset:0
ds_read_b64 v[104:105], v81 offset:1024
mav 4,124,40
R 32,14336
mva 8,40,128
s_waitcnt vmcnt(0)
buffer_store_dwordx2 v[116:117], off, s[36:39], s52 sc1
mav 0,132,44
R 36,15360
mav 4,136,44
mva 8,44,140
s_waitcnt lgkmcnt(4)
mav 0,144,24
R 40,16384
mav 4,148,24
s_waitcnt lgkmcnt(3)
v_mfma_f32_16x16x32_bf16 v[60:63], v[72:75], v[64:67], v[88:91]
v_cvt_pk_bf16_f32 v64, v12, v13
v_cvt_pk_bf16_f32 v65, v14, v15
mva 8,24,152
v_cvt_pk_bf16_f32 v66, v16, v17
v_cvt_pk_bf16_f32 v67, v18, v19
mav 0,156,28
R 24,17408
mav 4,160,28
v_max3_f32 v112, v56, v57, v60
v_max3_f32 v113, v58, v59, v62
mva 8,28,164
v_max_f32 v112, v112, v61
s_waitcnt lgkmcnt(2)
mav 0,168,32
R 28,18432
v_max_f32 v113, v113, v63
mav 4,172,32
v_max_f32 v114, v112, v113
v_max_f32 v115, v112, v113
mva 8,32,176
mav 0,180,36
R 32,19456
v_permlane32_swap_b32 v114, v115
mav 4,184,36
v_max_f32 v108, v114, v115
v_max_f32 v109, v114, v115
mva 8,36,188
s_waitcnt lgkmcnt(2)
mav 0,192,40
R 36,20480
v_permlane16_swap_b32 v108, v109
mav 4,196,40
v_max_f32 v114, v108, v109
v_mul_f32 v114, s65, v114
mva 8,40,200
v_fma_f32 v112, v112, s65, -v114
v_fma_f32 v113, v113, s65, -v114
mav 0,204,24
R 40,21504
mav 4,208,24
v_exp_f32 v112, v112
mva 8,24,212
s_waitcnt lgkmcnt(2)
mav 0,216,28
R 24,22528
mav 4,220,28
v_exp_f32 v113, v113
mva 8,28,224
v_add_f32 v114, v112, v113
v_add_f32 v115, v112, v113
mav 0,228,32
R 28,23552
v_permlane32_swap_b32 v114, v115
mav 4,232,32
v_add_f32 v108, v114, v115
v_add_f32 v109, v114, v115
mva 8,32,236
s_waitcnt lgkmcnt(2)
mav 0,240,36
R 32,24576
v_permlane16_swap_b32 v108, v109
mav 4,244,36
v_add_f32 v114, v108, v109
mva 8,36,248
v_rcp_f32 v114, v114
mav 0,252,40
R 36,25600
v_mul_f32 v112, v112, v114
mvv 4,128,40
v_mul_f32 v113, v113, v114
v_cvt_pk_bf16_f32 v112, v112, v112
mvv 8,40,132
v_cvt_pk_bf16_f32 v113, v113, v113
s_waitcnt lgkmcnt(2)
mvv 0,136,24
R 40,26624
v_and_b32 v108, v112, v92
mvv 4,140,24
v_and_b32 v109, v113, v93
v_and_b32 v110, v112, v94
mvv 8,24,144
v_and_b32 v111, v113, v95
mvv 0,148,28
R 24,27648
v_mfma_f32_16x16x32_bf16 v[56:59], v[96:99], v[108:111], 0
v_mfma_f32_16x16x32_bf16 v[60:63], v[102:105], v[108:111], 0
mvv 4,152,28
mvv 8,28,156
s_waitcnt lgkmcnt(2)
mvv 0,160,32
R 28,28672
mvv 4,164,32
mvv 8,32,168
v_cvt_pk_bf16_f32 v108, v56, v57
v_cvt_pk_bf16_f32 v109, v58, v59
mvv 0,172,36
R 32,29696
v_cvt_pk_bf16_f32 v110, v60, v61
mvv 4,176,36
v_cvt_pk_bf16_f32 v111, v62, v63
buffer_store_dwordx4 v[108:111], v81, s[36:39], s49 offen sc1
mvv 8,36,180
s_waitcnt lgkmcnt(2)
mvv 0,184,40
R 36,30720
mvv 4,188,40
mvv 8,40,192
mvv 0,196,24
R 40,31744
mvv 4,200,24
s_add_u32 s49, s49, 346112
mvv 8,24,204
s_barrier
s_waitcnt lgkmcnt(2)
mvv 0,208,28
R 24,32768
mvv 4,212,28
ds_read_b64 v[120:121], v82 offset:0
ds_read_b128 v[56:59], v83 offset:0
mvv 8,28,216
ds_read_b128 v[60:63], v83 offset:16
mvv 0,220,32
R 28,33792
mvv 4,224,32
mvv 8,32,228
s_waitcnt lgkmcnt(5)
mvv 0,232,36
R 32,34816
mvv 4,236,36
mvv 8,36,240
mvv 0,244,40
R 36,35840
mvv 4,248,40
mvv 8,40,252
s_waitcnt lgkmcnt(2)
v_mfma_f32_16x16x32_bf16 v[12:15], a[0:3], v[24:27], 0
R 40,36864
v_mul_f32 v57, v57, v118
v_mfma_f32_16x16x32_bf16 v[16:19], a[4:7], v[24:27], 0
v_mul_f32 v59, v59, v118
v_mul_f32 v61, v61, v118
v_mfma_f32_16x16x32_bf16 v[20:23], v[24:27], a[8:11], 0
v_fma_f32 v8, v8, v56, v57
v_fma_f32 v9, v9, v58, v59
mav 12,12,28
R 44,37888
v_fma_f32 v10, v10, v60, v61
mav 16,16,28
v_mul_f32 v63, v63, v118
v_fma_f32 v11, v11, v62, v63
mva 20,28,20
v_cvt_pk_bf16_f32 v96, v8, v9
s_waitcnt lgkmcnt(2)
mav 12,24,32
R 24,38912
v_cvt_pk_bf16_f32 v97, v10, v11
mav 16,28,32
v_mul_f32 v122, v121, v48
v_mfma_f32_16x16x32_bf16 v[56:59], v[72:75], v[64:67], v[84:87]
v_fma_f32 v0, v0, v120, v122
v_mul_f32 v123, v121, v49
mva 20,32,32
v_fma_f32 v1, v1, v120, v123
v_mul_f32 v122, v121, v50
mav 12,36,36
R 28,39936
v_fma_f32 v2, v2, v120, v122
mav 16,40,36
buffer_load_dwordx4 v[76:79], v81, s[36:39], s55 offen offset:2048 sc1
buffer_load_dwordx2 v[100:101], v81, s[36:39], s55 offen offset:3072 sc1
mva 20,36,44
buffer_load_dwordx2 v[106:107], v81, s[36:39], s55 offen offset:3080 sc1
s_waitcnt lgkmcnt(2)
mav 12,48,40
R 32,40960
v_mul_f32 v123, v121, v51
mav 16,52,40
v_fma_f32 v3, v3, v120, v123
v_mul_f32 v122, v121, v52
mva 20,40,56
v_fma_f32 v4, v4, v120, v122
v_mul_f32 v123, v121, v53
mav 12,60,44
R 36,41984
v_fma_f32 v5, v5, v120, v123
mav 16,64,44
v_mul_f32 v122, v121, v54
v_fma_f32 v6, v6, v120, v122
mva 20,44,68
v_mul_f32 v123, v121, v55
s_waitcnt lgkmcnt(2)
mav 12,72,24
R 40,43008
v_fma_f32 v7, v7, v120, v123
mav 16,76,24
mva 20,24,80
mav 12,84,28
R 44,44032
mav 16,88,28
mva 20,28,92
s_waitcnt lgkmcnt(2)
mav 12,96,32
R 24,45056
v_add_u32 v116, 1, v116
mav 16,100,32
mva 20,32,104
mav 12,108,36
R 28,46080
mav 16,112,36
mva 20,36,116
s_barrier
s_waitcnt lgkmcnt(2)
mav 12,120,40
ds_read_b128 v[68:71], v81 offset:0
ds_read_b64 v[102:103], v81 offset:1024
mav 16,124,40
R 32,47104
mva 20,40,128
s_waitcnt vmcnt(3)
buffer_store_dwordx2 v[116:117], off, s[36:39], s52 sc1
mav 12,132,44
R 36,48128
mav 16,136,44
mva 20,44,140
s_waitcnt lgkmcnt(4)
mav 12,144,24
R 40,49152
mav 16,148,24
s_waitcnt lgkmcnt(3)
v_mfma_f32_16x16x32_bf16 v[60:63], v[68:71], v[64:67], v[88:91]
v_cvt_pk_bf16_f32 v64, v0, v1
v_cvt_pk_bf16_f32 v65, v2, v3
mva 20,24,152
v_cvt_pk_bf16_f32 v66, v4, v5
v_cvt_pk_bf16_f32 v67, v6, v7
mav 12,156,28
R 24,50176
mav 16,160,28
v_max3_f32 v112, v56, v57, v60
v_max3_f32 v113, v58, v59, v62
mva 20,28,164
v_max_f32 v112, v112, v61
s_waitcnt lgkmcnt(2)
mav 12,168,32
R 28,51200
v_max_f32 v113, v113, v63
mav 16,172,32
v_max_f32 v114, v112, v113
v_max_f32 v115, v112, v113
mva 20,32,176
mav 12,180,36
R 32,52224
v_permlane32_swap_b32 v114, v115
mav 16,184,36
v_max_f32 v108, v114, v115
v_max_f32 v109, v114, v115
mva 20,36,188
s_waitcnt lgkmcnt(2)
mav 12,192,40
R 36,53248
v_permlane16_swap_b32 v108, v109
mav 16,196,40
v_max_f32 v114, v108, v109
v_mul_f32 v114, s65, v114
mva 20,40,200
v_fma_f32 v112, v112, s65, -v114
v_fma_f32 v113, v113, s65, -v114
mav 12,204,24
R 40,54272
mav 16,208,24
v_exp_f32 v112, v112
mva 20,24,212
s_waitcnt lgkmcnt(2)
mav 12,216,28
R 24,55296
mav 16,220,28
v_exp_f32 v113, v113
mva 20,28,224
v_add_f32 v114, v112, v113
v_add_f32 v115, v112, v113
mav 12,228,32
R 28,56320
v_permlane32_swap_b32 v114, v115
mav 16,232,32
v_add_f32 v108, v114, v115
v_add_f32 v109, v114, v115
mva 20,32,236
s_waitcnt lgkmcnt(2)
mav 12,240,36
R 32,57344
v_permlane16_swap_b32 v108, v109
mav 16,244,36
v_add_f32 v114, v108, v109
mva 20,36,248
v_rcp_f32 v114, v114
mav 12,252,40
R 36,58368
v_mul_f32 v112, v112, v114
mvv 16,128,40
v_mul_f32 v113, v113, v114
v_cvt_pk_bf16_f32 v112, v112, v112
mvv 20,40,132
v_cvt_pk_bf16_f32 v113, v113, v113
s_waitcnt lgkmcnt(2)
mvv 12,136,24
R 40,59392
v_and_b32 v108, v112, v94
mvv 16,140,24
v_and_b32 v109, v113, v95
v_and_b32 v110, v112, v92
mvv 20,24,144
v_and_b32 v111, v113, v93
mvv 12,148,28
R 24,60416
v_mfma_f32_16x16x32_bf16 v[56:59], v[96:99], v[108:111], 0
v_mfma_f32_16x16x32_bf16 v[60:63], v[102:105], v[108:111], 0
mvv 16,152,28
mvv 20,28,156
s_waitcnt lgkmcnt(2)
mvv 12,160,32
R 28,61440
mvv 16,164,32
mvv 20,32,168
v_cvt_pk_bf16_f32 v108, v56, v57
v_cvt_pk_bf16_f32 v109, v58, v59
mvv 12,172,36
R 32,62464
v_cvt_pk_bf16_f32 v110, v60, v61
mvv 16,176,36
v_cvt_pk_bf16_f32 v111, v62, v63
buffer_store_dwordx4 v[108:111], v81, s[36:39], s49 offen sc1
mvv 20,36,180
s_waitcnt lgkmcnt(2)
mvv 12,184,40
R 36,63488
mvv 16,188,40
mvv 20,40,192
mvv 12,196,24
R 40,64512
mvv 16,200,24
s_add_u32 s49, s49, 346112
mvv 20,24,204
s_barrier
s_waitcnt lgkmcnt(2)
mvv 12,208,28
mvv 16,212,28
ds_read_b64 v[120:121], v82 offset:128
ds_read_b128 v[56:59], v83 offset:128
mvv 20,28,216
ds_read_b128 v[60:63], v83 offset:144
mvv 12,220,32
)"
R"(
mvv 16,224,32
mvv 20,32,228
s_waitcnt lgkmcnt(3)
mvv 12,232,36
mvv 16,236,36
mvv 20,36,240
mvv 12,244,40
mvv 16,248,40
mvv 20,40,252
s_waitcnt lgkmcnt(0)
v_mul_f32 v57, v57, v118
s_nop 5
v_fma_f32 v20, v20, v56, v57
v_mul_f32 v59, v59, v118
v_fma_f32 v21, v21, v58, v59
v_mul_f32 v61, v61, v118
v_fma_f32 v22, v22, v60, v61
v_mul_f32 v63, v63, v118
v_fma_f32 v23, v23, v62, v63
v_cvt_pk_bf16_f32 v98, v20, v21
v_cvt_pk_bf16_f32 v99, v22, v23
s_nop 1
v_mfma_f32_16x16x32_bf16 v[56:59], v[68:71], v[64:67], v[84:87]
v_mul_f32 v122, v121, v48
v_fma_f32 v12, v12, v120, v122
v_mul_f32 v123, v121, v49
v_fma_f32 v13, v13, v120, v123
v_mul_f32 v122, v121, v50
v_fma_f32 v14, v14, v120, v122
v_mul_f32 v123, v121, v51
v_fma_f32 v15, v15, v120, v123
v_mul_f32 v122, v121, v52
v_fma_f32 v16, v16, v120, v122
v_mul_f32 v123, v121, v53
v_fma_f32 v17, v17, v120, v123
v_mul_f32 v122, v121, v54
v_fma_f32 v18, v18, v120, v122
v_mul_f32 v123, v121, v55
v_fma_f32 v19, v19, v120, v123
s_barrier
ds_read_b128 v[72:75], v81 offset:0
ds_read_b64 v[104:105], v81 offset:1024
s_waitcnt lgkmcnt(0)
v_mfma_f32_16x16x32_bf16 v[60:63], v[72:75], v[64:67], v[88:91]
s_nop 7
v_max3_f32 v112, v56, v57, v60
v_max3_f32 v113, v58, v59, v62
v_max_f32 v112, v112, v61
v_max_f32 v113, v113, v63
v_max_f32 v114, v112, v113
v_max_f32 v115, v112, v113
s_nop 1
v_permlane32_swap_b32 v114, v115
v_max_f32 v108, v114, v115
v_max_f32 v109, v114, v115
s_nop 1
v_permlane16_swap_b32 v108, v109
v_max_f32 v114, v108, v109
v_mul_f32 v114, s65, v114
v_fma_f32 v112, v112, s65, -v114
v_fma_f32 v113, v113, s65, -v114
v_exp_f32 v112, v112
v_exp_f32 v113, v113
s_nop 0
v_add_f32 v114, v112, v113
v_add_f32 v115, v112, v113
s_nop 1
v_permlane32_swap_b32 v114, v115
v_add_f32 v108, v114, v115
v_add_f32 v109, v114, v115
s_nop 1
v_permlane16_swap_b32 v108, v109
v_add_f32 v114, v108, v109
v_rcp_f32 v114, v114
s_nop 0
v_mul_f32 v112, v112, v114
v_mul_f32 v113, v113, v114
v_cvt_pk_bf16_f32 v112, v112, v112
v_cvt_pk_bf16_f32 v113, v113, v113
v_and_b32 v108, v112, v92
v_and_b32 v109, v113, v93
v_and_b32 v110, v112, v94
v_and_b32 v111, v113, v95
s_nop 1
v_mfma_f32_16x16x32_bf16 v[56:59], v[96:99], v[108:111], 0
v_mfma_f32_16x16x32_bf16 v[60:63], v[102:105], v[108:111], 0
s_nop 6
v_cvt_pk_bf16_f32 v108, v56, v57
v_cvt_pk_bf16_f32 v109, v58, v59
v_cvt_pk_bf16_f32 v110, v60, v61
v_cvt_pk_bf16_f32 v111, v62, v63
buffer_store_dwordx4 v[108:111], v81, s[36:39], s49 offen sc1
v_cvt_pk_bf16_f32 v64, v12, v13
v_cvt_pk_bf16_f32 v65, v14, v15
v_cvt_pk_bf16_f32 v66, v16, v17
v_cvt_pk_bf16_f32 v67, v18, v19
v_add_u32 v116, 1, v116
s_waitcnt vmcnt(1)
buffer_store_dwordx2 v[116:117], off, s[36:39], s52 sc1
v_mfma_f32_16x16x32_bf16 v[56:59], v[72:75], v[64:67], v[84:87]
s_add_u32 s49, s49, 346112
v_min_f32 v60, s56, v88
v_min_f32 v61, s56, v89
v_min_f32 v62, s56, v90
v_min_f32 v63, s56, v91
s_nop 1
mvv 60,76,64
s_nop 7
v_max3_f32 v112, v56, v57, v60
v_max3_f32 v113, v58, v59, v62
v_max_f32 v112, v112, v61
v_max_f32 v113, v113, v63
v_max_f32 v114, v112, v113
v_max_f32 v115, v112, v113
s_nop 1
v_permlane32_swap_b32 v114, v115
v_max_f32 v108, v114, v115
v_max_f32 v109, v114, v115
s_nop 1
v_permlane16_swap_b32 v108, v109
v_max_f32 v114, v108, v109
v_mul_f32 v114, s65, v114
v_fma_f32 v112, v112, s65, -v114
v_fma_f32 v113, v113, s65, -v114
v_exp_f32 v112, v112
v_exp_f32 v113, v113
s_nop 0
v_add_f32 v114, v112, v113
v_add_f32 v115, v112, v113
s_nop 1
v_permlane32_swap_b32 v114, v115
v_add_f32 v108, v114, v115
v_add_f32 v109, v114, v115
s_nop 1
v_permlane16_swap_b32 v108, v109
v_add_f32 v114, v108, v109
v_rcp_f32 v114, v114
s_nop 0
v_mul_f32 v112, v112, v114
v_mul_f32 v113, v113, v114
v_cvt_pk_bf16_f32 v112, v112, v112
v_cvt_pk_bf16_f32 v113, v113, v113
v_and_b32 v108, v112, v92
v_and_b32 v109, v113, v93
v_and_b32 v110, v112, v94
v_and_b32 v111, v113, v95
s_nop 1
v_mfma_f32_16x16x32_bf16 v[56:59], v[98:101], v[108:111], 0
v_mfma_f32_16x16x32_bf16 v[60:63], v[104:107], v[108:111], 0
s_nop 6
v_cvt_pk_bf16_f32 v108, v56, v57
v_cvt_pk_bf16_f32 v109, v58, v59
v_cvt_pk_bf16_f32 v110, v60, v61
v_cvt_pk_bf16_f32 v111, v62, v63
buffer_store_dwordx4 v[108:111], v81, s[36:39], s49 offen sc1
v_add_u32 v116, 1, v116
s_waitcnt vmcnt(2)
buffer_store_dwordx2 v[116:117], off, s[36:39], s52 sc1
v_add_u32 v116, 37, v116
s_waitcnt vmcnt(1)
buffer_store_dwordx2 v[116:117], off, s[36:39], s52 sc1
s_endpgm
LeQA:
.purgem R
Lr1:
s_cmp_eq_u32 s60, 1
s_cbranch_scc0 Lr2
.macro R d,o
ds_read_b128 v[\d:\d+3], v80 offset:\o
.endm
buffer_load_dword v79, off, s[36:39], 0 sc1
s_getpc_b64 s[66:67]
LpQB:
v_lshlrev_b32 v8, 3, v80
s_and_b32 s60, s53, 2048
s_lshl_b32 s60, s60, 2
v_add_u32 v8, s60, v8
.set cn, 0
.rept 2
v_add_u32 v9, cn, v8
v_min_u32 v9, LeQB - LpQB - 4, v9
global_load_dword v10, v9, s[66:67]
.set cn, cn + 16384
.endr
E 0,0
E 1,4096
E 2,8192
s_add_u32 s51, s51, 196608
E 0,12288
E 1,16384
E 2,20480
s_add_u32 s51, s51, 196608
E 0,24576
E 1,28672
E 2,32768
s_add_u32 s51, s51, 196608
E 0,36864
E 1,40960
E 2,45056
s_add_u32 s51, s51, 196608
E 0,49152
E 1,53248
E 2,57344
s_add_u32 s51, s51, 196608
E 0,61440
E 1,65536
E 2,69632
s_add_u32 s51, s51, 196608
E 0,73728
E 1,77824
E 2,81920
s_add_u32 s51, s51, 196608
E 0,86016
E 1,90112
E 2,94208
s_add_u32 s51, s51, 196608
s_cmp_ge_u32 s51, s57
s_cselect_b32 s60, 6291456, 0
s_sub_u32 s51, s51, s60
E 0,98304
E 1,102400
E 2,106496
s_add_u32 s51, s51, 196608
E 0,110592
E 1,114688
E 2,118784
s_add_u32 s51, s51, 196608
E 0,122880
E 1,126976
E 2,131072
s_add_u32 s51, s51, 196608
E 0,135168
E 1,139264
E 2,143360
s_add_u32 s51, s51, 196608
v_mov_b32 v24, s64
v_mov_b32 v25, s64
v_mov_b32 v26, s64
v_mov_b32 v27, s64
s_waitcnt vmcnt(30)
s_barrier
v_mov_b32 v78, 0x100
v_add_u32 v79, 0x9e3779b9, v79
buffer_store_dwordx2 v[78:79], v81, s[36:39], s52 offen sc1
v_add_u32 v91, v91, v78
Ta 0,4,0
Ta 4,5,0
Ta 8,6,0
Ta 12,4,12288
Ta 16,5,12288
Ta 20,6,12288
s_waitcnt lgkmcnt(0)
s_waitcnt vmcnt(25)
s_barrier
Ta 24,4,24576
Ta 28,5,24576
Ta 32,6,24576
Ta 36,4,36864
Ta 40,5,36864
Ta 44,6,36864
E 0,0
E 1,4096
E 2,8192
s_add_u32 s51, s51, 196608
E 0,12288
E 1,16384
E 2,20480
s_add_u32 s51, s51, 196608
v_mfma_f32_16x16x32_bf16 v[56:59], a[0:3], v[24:27], 0
v_mfma_f32_16x16x32_bf16 v[60:63], a[4:7], v[24:27], 0
v_mfma_f32_16x16x32_bf16 v[64:67], v[24:27], a[8:11], 0
mav 56,12,24
mav 60,16,24
mva 64,24,20
s_waitcnt lgkmcnt(0)
s_waitcnt vmcnt(25)
s_barrier
v_add_u32 v4, 49152, v4
v_add_u32 v5, 49152, v5
v_add_u32 v6, 49152, v6
Ta 48,4,0
Ta 52,5,0
Ta 56,6,0
Ta 60,4,12288
Ta 64,5,12288
Ta 68,6,12288
E 0,24576
E 1,28672
E 2,32768
s_add_u32 s51, s51, 196608
E 0,36864
E 1,40960
E 2,45056
s_add_u32 s51, s51, 196608
s_cmp_ge_u32 s51, s57
s_cselect_b32 s60, 6291456, 0
s_sub_u32 s51, s51, s60
mav 56,24,24
mav 60,28,24
mva 64,24,32
mav 56,36,24
mav 60,40,24
mva 64,24,44
s_waitcnt lgkmcnt(0)
s_waitcnt vmcnt(25)
s_barrier
Ta 72,4,24576
Ta 76,5,24576
Ta 80,6,24576
Ta 84,4,36864
Ta 88,5,36864
Ta 92,6,36864
E 0,49152
E 1,53248
E 2,57344
s_add_u32 s51, s51, 196608
E 0,61440
E 1,65536
E 2,69632
s_add_u32 s51, s51, 196608
mav 56,48,24
mav 60,52,24
mva 64,24,56
mav 56,60,24
mav 60,64,24
mva 64,24,68
s_waitcnt lgkmcnt(0)
s_waitcnt vmcnt(25)
s_barrier
v_add_u32 v4, 49152, v4
v_add_u32 v5, 49152, v5
v_add_u32 v6, 49152, v6
Ta 96,4,0
Ta 100,5,0
Ta 104,6,0
Ta 108,4,12288
Ta 112,5,12288
Ta 116,6,12288
E 0,73728
E 1,77824
E 2,81920
s_add_u32 s51, s51, 196608
E 0,86016
E 1,90112
E 2,94208
s_add_u32 s51, s51, 196608
mav 56,72,24
mav 60,76,24
mva 64,24,80
mav 56,84,24
mav 60,88,24
mva 64,24,92
s_waitcnt lgkmcnt(0)
s_waitcnt vmcnt(25)
s_barrier
Ta 120,4,24576
Ta 124,5,24576
Ta 128,6,24576
Ta 132,4,36864
Ta 136,5,36864
Ta 140,6,36864
E 0,98304
E 1,102400
E 2,106496
s_add_u32 s51, s51, 196608
E 0,110592
E 1,114688
E 2,118784
s_add_u32 s51, s51, 196608
mav 56,96,24
mav 60,100,24
mva 64,24,104
mav 56,108,24
mav 60,112,24
mva 64,24,116
s_waitcnt lgkmcnt(0)
s_waitcnt vmcnt(24)
s_barrier
v_add_u32 v4, -98304, v4
v_add_u32 v5, -98304, v5
v_add_u32 v6, -98304, v6
Ta 144,4,0
Ta 148,5,0
Ta 152,6,0
Ta 156,4,12288
Ta 160,5,12288
Ta 164,6,12288
E 0,122880
E 1,126976
E 2,131072
s_add_u32 s51, s51, 196608
E 0,135168
E 1,139264
E 2,143360
s_add_u32 s51, s51, 196608
s_cmp_ge_u32 s51, s57
s_cselect_b32 s60, 6291456, 0
s_sub_u32 s51, s51, s60
mav 56,120,24
mav 60,124,24
mva 64,24,128
mav 56,132,24
mav 60,136,24
mva 64,24,140
s_waitcnt lgkmcnt(0)
s_waitcnt vmcnt(24)
s_barrier
Ta 168,4,24576
Ta 172,5,24576
Ta 176,6,24576
Ta 180,4,36864
Ta 184,5,36864
Ta 188,6,36864
E 0,0
E 1,4096
E 2,8192
s_add_u32 s51, s51, 196608
E 0,12288
E 1,16384
E 2,20480
s_add_u32 s51, s51, 196608
mav 56,144,24
mav 60,148,24
mva 64,24,152
mav 56,156,24
mav 60,160,24
mva 64,24,164
s_waitcnt lgkmcnt(0)
s_waitcnt vmcnt(24)
s_barrier
v_add_u32 v4, 49152, v4
v_add_u32 v5, 49152, v5
v_add_u32 v6, 49152, v6
Ta 192,4,0
Ta 196,5,0
Ta 200,6,0
Ta 204,4,12288
Ta 208,5,12288
Ta 212,6,12288
E 0,24576
E 1,28672
E 2,32768
s_add_u32 s51, s51, 196608
E 0,36864
E 1,40960
E 2,45056
s_add_u32 s51, s51, 196608
mav 56,168,24
mav 60,172,24
mva 64,24,176
mav 56,180,24
mav 60,184,24
mva 64,24,188
s_waitcnt lgkmcnt(0)
s_waitcnt vmcnt(24)
s_barrier
Ta 216,4,24576
Ta 220,5,24576
Ta 224,6,24576
Ta 228,4,36864
Ta 232,5,36864
Ta 236,6,36864
E 0,49152
E 1,53248
E 2,57344
s_add_u32 s51, s51, 196608
E 0,61440
E 1,65536
E 2,69632
s_add_u32 s51, s51, 196608
mav 56,192,24
mav 60,196,24
mva 64,24,200
mav 56,204,24
mav 60,208,24
mva 64,24,212
s_waitcnt lgkmcnt(0)
s_waitcnt vmcnt(24)
s_barrier
v_add_u32 v4, 49152, v4
v_add_u32 v5, 49152, v5
v_add_u32 v6, 49152, v6
Ta 240,4,0
Ta 244,5,0
Ta 248,6,0
Ta 252,4,12288
Tv 128,5,12288
Tv 132,6,12288
E 0,73728
E 1,77824
E 2,81920
s_add_u32 s51, s51, 196608
E 0,86016
E 1,90112
E 2,94208
s_add_u32 s51, s51, 196608
s_cmp_ge_u32 s51, s57
s_cselect_b32 s60, 6291456, 0
s_sub_u32 s51, s51, s60
mav 56,216,24
mav 60,220,24
mva 64,24,224
mav 56,228,24
mav 60,232,24
mva 64,24,236
s_waitcnt lgkmcnt(0)
s_waitcnt vmcnt(24)
s_barrier
Tv 136,4,24576
Tv 140,5,24576
Tv 144,6,24576
Tv 148,4,36864
Tv 152,5,36864
Tv 156,6,36864
mav 56,240,24
mav 60,244,24
mva 64,24,248
mav 56,252,24
mvv 60,128,24
mvv 64,24,132
s_waitcnt lgkmcnt(0)
s_waitcnt vmcnt(18)
s_barrier
v_add_u32 v4, -98304, v4
v_add_u32 v5, -98304, v5
v_add_u32 v6, -98304, v6
Tv 160,4,0
Tv 164,5,0
Tv 168,6,0
Tv 172,4,12288
Tv 176,5,12288
Tv 180,6,12288
mvv 56,136,24
mvv 60,140,24
mvv 64,24,144
mvv 56,148,24
mvv 60,152,24
mvv 64,24,156
s_waitcnt lgkmcnt(0)
s_waitcnt vmcnt(12)
s_barrier
Tv 184,4,24576
Tv 188,5,24576
Tv 192,6,24576
Tv 196,4,36864
Tv 200,5,36864
Tv 204,6,36864
mvv 56,160,24
mvv 60,164,24
mvv 64,24,168
mvv 56,172,24
mvv 60,176,24
mvv 64,24,180
s_waitcnt lgkmcnt(0)
s_waitcnt vmcnt(6)
s_barrier
v_add_u32 v4, 49152, v4
v_add_u32 v5, 49152, v5
v_add_u32 v6, 49152, v6
Tv 208,4,0
Tv 212,5,0
Tv 216,6,0
Tv 220,4,12288
Tv 224,5,12288
Tv 228,6,12288
mvv 56,184,24
mvv 60,188,24
mvv 64,24,192
mvv 56,196,24
mvv 60,200,24
mvv 64,24,204
s_waitcnt lgkmcnt(0)
s_waitcnt vmcnt(0)
s_barrier
Tv 232,4,24576
Tv 236,5,24576
Tv 240,6,24576
Tv 244,4,36864
Tv 248,5,36864
Tv 252,6,36864
mvv 56,208,24
mvv 60,212,24
mvv 64,24,216
mvv 56,220,24
mvv 60,224,24
mvv 64,24,228
s_waitcnt lgkmcnt(0)
mvv 56,232,24
mvv 60,236,24
mvv 64,24,240
mvv 56,244,24
mvv 60,248,24
mvv 64,24,252
s_waitcnt vmcnt(0)
s_barrier
s_nop 7
v_mov_b32 v93, v64
s_and_b32 s60, s48, 3
s_cmp_eq_u32 s60, 1
s_cbranch_scc1 LvQB1
LvQB0:
buffer_load_dwordx2 v[98:99], v90, s[36:39], 0 offen sc1
s_waitcnt vmcnt(0)
ds_write_b32 v82, v98
v_sub_u32 v98, v98, v91
v_xor_b32 v99, v99, v79
v_cmp_lt_u64 vcc, s[62:63], v[98:99]
s_waitcnt lgkmcnt(0)
s_cbranch_vccnz CQB0_1
BQB0_1:
s_add_u32 s49, s49, 346112
s_barrier
s_mov_b32 m0, 142336
s_nop 0
buffer_load_dword v90, s[36:39], 0 offen sc1 lds
s_add_u32 m0, s50, 0
s_nop 0
D 86,0
D 86,1024
D 86,2048
D 86,3072
s_add_u32 m0, s50, 4096
s_nop 0
D 87,0
v_add_u32 v91, 1, v91
D 87,1024
D 87,2048
D 87,3072
s_add_u32 m0, s50, 8192
s_nop 0
D 88,0
D 88,1024
D 88,2048
D 88,3072
s_add_u32 m0, s50, 12288
s_nop 0
D 89,0
D 89,1024
ds_read_b32 v98, v82 offset:0
D 89,2048
D 89,3072
s_waitcnt lgkmcnt(0)
v_cmp_gt_i32 vcc, v91, v98
s_cbranch_vccnz CQB0_2
BQB0_2:
s_add_u32 s49, s49, 346112
s_barrier
s_mov_b32 m0, 142336
s_nop 0
buffer_load_dword v90, s[36:39], 0 offen sc1 lds
s_add_u32 m0, s50, 32768
s_nop 0
D 86,0
D 86,1024
D 86,2048
D 86,3072
s_add_u32 m0, s50, 36864
s_nop 0
D 87,0
v_add_u32 v91, 1, v91
D 87,1024
D 87,2048
D 87,3072
s_add_u32 m0, s50, 40960
s_nop 0
D 88,0
D 88,1024
D 88,2048
D 88,3072
s_add_u32 m0, s50, 45056
s_nop 0
D 89,0
D 89,1024
ds_read_b32 v98, v82 offset:0
D 89,2048
D 89,3072
s_xor_b32 s50, s50, 0x10000
s_waitcnt lgkmcnt(0)
v_cmp_gt_i32 vcc, v91, v98
s_waitcnt vmcnt(17)
s_cbranch_vccnz CQB0_3
BQB0_3:
s_add_u32 s49, s49, 346112
s_barrier
R 24,0
R 28,1024
s_mov_b32 m0, 142336
R 32,2048
buffer_load_dword v90, s[36:39], 0 offen sc1 lds
R 36,3072
s_waitcnt lgkmcnt(2)
v_mfma_f32_16x16x32_bf16 v[0:3], a[0:3], v[24:27], 0
R 40,4096
v_mfma_f32_16x16x32_bf16 v[4:7], a[4:7], v[24:27], 0
v_mfma_f32_16x16x32_bf16 v[8:11], v[24:27], a[8:11], 0
mav 0,12,28
R 44,5120
mav 4,16,28
mva 8,28,20
s_waitcnt lgkmcnt(2)
mav 0,24,32
R 48,6144
mav 4,28,32
mva 8,32,32
mav 0,36,36
R 52,7168
mav 4,40,36
mva 8,36,44
s_waitcnt lgkmcnt(2)
mav 0,48,40
R 24,8192
mav 4,52,40
mva 8,40,56
mav 0,60,44
R 28,9216
mav 4,64,44
mva 8,44,68
s_waitcnt lgkmcnt(2)
mav 0,72,48
R 32,10240
mav 4,76,48
mva 8,48,80
mav 0,84,52
R 36,11264
mav 4,88,52
mva 8,52,92
s_waitcnt lgkmcnt(2)
mav 0,96,24
R 40,12288
s_add_u32 m0, s50, 0
mav 4,100,24
mva 8,24,104
D 86,0
mav 0,108,28
R 44,13312
mav 4,112,28
mva 8,28,116
D 86,1024
s_waitcnt lgkmcnt(2)
mav 0,120,32
R 48,14336
mav 4,124,32
mva 8,32,128
mav 0,132,36
R 52,15360
D 86,2048
mav 4,136,36
mva 8,36,140
s_waitcnt lgkmcnt(2)
mav 0,144,40
R 24,16384
D 86,3072
mav 4,148,40
mva 8,40,152
s_add_u32 m0, s50, 4096
mav 0,156,44
R 28,17408
mav 4,160,44
D 87,0
v_add_u32 v91, 1, v91
mva 8,44,164
s_waitcnt lgkmcnt(2)
mav 0,168,48
R 32,18432
mav 4,172,48
D 87,1024
mva 8,48,176
mav 0,180,52
R 36,19456
mav 4,184,52
mva 8,52,188
D 87,2048
s_waitcnt lgkmcnt(2)
mav 0,192,24
R 40,20480
mav 4,196,24
mva 8,24,200
D 87,3072
mav 0,204,28
R 44,21504
mav 4,208,28
s_add_u32 m0, s50, 8192
mva 8,28,212
s_waitcnt lgkmcnt(2)
mav 0,216,32
R 48,22528
D 88,0
mav 4,220,32
mva 8,32,224
mav 0,228,36
R 52,23552
D 88,1024
mav 4,232,36
mva 8,36,236
s_waitcnt lgkmcnt(2)
mav 0,240,40
R 24,24576
mav 4,244,40
D 88,2048
mva 8,40,248
mav 0,252,44
R 28,25600
mvv 4,128,44
D 88,3072
mvv 8,44,132
s_waitcnt lgkmcnt(2)
mvv 0,136,48
R 32,26624
s_add_u32 m0, s50, 12288
mvv 4,140,48
mvv 8,48,144
D 89,0
mvv 0,148,52
R 36,27648
mvv 4,152,52
mvv 8,52,156
D 89,1024
s_waitcnt lgkmcnt(2)
mvv 0,160,24
R 40,28672
mvv 4,164,24
ds_read_b32 v98, v82 offset:0
mvv 8,24,168
mvv 0,172,28
R 44,29696
D 89,2048
mvv 4,176,28
mvv 8,28,180
s_waitcnt lgkmcnt(3)
mvv 0,184,32
R 48,30720
D 89,3072
mvv 4,188,32
mvv 8,32,192
mvv 0,196,36
R 52,31744
mvv 4,200,36
s_waitcnt lgkmcnt(2)
v_cmp_gt_i32 vcc, v91, v98
mvv 8,36,204
s_waitcnt vmcnt(17)
s_cbranch_vccnz CQB0_4
BQB0_4:
mvv 0,208,40
s_barrier
R 24,32768
mvv 4,212,40
s_add_u32 s49, s49, 346112
ds_read_b64 v[94:95], v84 offset:0
mvv 8,40,216
ds_read_b128 v[64:67], v85 offset:0
ds_read_b128 v[68:71], v85 offset:16
mvv 0,220,44
R 28,33792
mvv 4,224,44
s_mov_b32 m0, 142336
mvv 8,44,228
s_waitcnt lgkmcnt(5)
mvv 0,232,48
R 32,34816
buffer_load_dword v90, s[36:39], 0 offen sc1 lds
mvv 4,236,48
mvv 8,48,240
mvv 0,244,52
R 36,35840
mvv 4,248,52
mvv 8,52,252
s_waitcnt lgkmcnt(2)
v_mfma_f32_16x16x32_bf16 v[12:15], a[0:3], v[24:27], 0
R 40,36864
v_mul_f32 v96, v95, v56
v_mfma_f32_16x16x32_bf16 v[16:19], a[4:7], v[24:27], 0
v_fma_f32 v0, v0, v94, v96
v_mul_f32 v97, v95, v57
v_mfma_f32_16x16x32_bf16 v[20:23], v[24:27], a[8:11], 0
v_fma_f32 v1, v1, v94, v97
v_mul_f32 v96, v95, v58
mav 12,12,28
R 44,37888
v_fma_f32 v2, v2, v94, v96
mav 16,16,28
v_mul_f32 v97, v95, v59
v_fma_f32 v3, v3, v94, v97
mva 20,28,20
v_mul_f32 v96, v95, v60
s_waitcnt lgkmcnt(2)
mav 12,24,32
R 48,38912
v_fma_f32 v4, v4, v94, v96
mav 16,28,32
v_mul_f32 v97, v95, v61
v_fma_f32 v5, v5, v94, v97
mva 20,32,32
v_mul_f32 v96, v95, v62
v_fma_f32 v6, v6, v94, v96
mav 12,36,36
R 52,39936
v_mul_f32 v97, v95, v63
mav 16,40,36
v_fma_f32 v7, v7, v94, v97
v_cvt_pk_bf16_f32 v72, v0, v1
mva 20,36,44
v_cvt_pk_bf16_f32 v73, v2, v3
s_waitcnt lgkmcnt(2)
mav 12,48,40
R 24,40960
v_cvt_pk_bf16_f32 v74, v4, v5
mav 16,52,40
v_cvt_pk_bf16_f32 v75, v6, v7
ds_write_b128 v83, v[72:75] offset:0
mva 20,40,56
v_mul_f32 v65, v65, v93
v_fma_f32 v8, v8, v64, v65
mav 12,60,44
R 28,41984
v_mul_f32 v67, v67, v93
mav 16,64,44
v_fma_f32 v9, v9, v66, v67
v_mul_f32 v69, v69, v93
mva 20,44,68
v_fma_f32 v10, v10, v68, v69
s_waitcnt lgkmcnt(3)
mav 12,72,48
R 32,43008
v_mul_f32 v71, v71, v93
mav 16,76,48
v_fma_f32 v11, v11, v70, v71
v_cvt_pk_bf16_f32 v76, v8, v9
mva 20,48,80
v_cvt_pk_bf16_f32 v77, v10, v11
ds_write_b64 v83, v[76:77] offset:1024
mav 12,84,52
R 36,44032
mav 16,88,52
mva 20,52,92
s_waitcnt lgkmcnt(3)
mav 12,96,24
R 40,45056
s_add_u32 m0, s50, 32768
mav 16,100,24
s_waitcnt lgkmcnt(2)
s_barrier
mva 20,24,104
D 86,0
mav 12,108,28
R 44,46080
mav 16,112,28
mva 20,28,116
D 86,1024
mav 12,120,32
R 48,47104
mav 16,124,32
mva 20,32,128
s_waitcnt lgkmcnt(2)
mav 12,132,36
R 52,48128
D 86,2048
mav 16,136,36
mva 20,36,140
mav 12,144,40
R 24,49152
D 86,3072
mav 16,148,40
mva 20,40,152
s_add_u32 m0, s50, 36864
s_waitcnt lgkmcnt(2)
mav 12,156,44
R 28,50176
mav 16,160,44
D 87,0
v_add_u32 v91, 1, v91
mva 20,44,164
mav 12,168,48
R 32,51200
mav 16,172,48
D 87,1024
mva 20,48,176
s_waitcnt lgkmcnt(2)
mav 12,180,52
R 36,52224
mav 16,184,52
mva 20,52,188
D 87,2048
mav 12,192,24
R 40,53248
mav 16,196,24
mva 20,24,200
D 87,3072
s_waitcnt lgkmcnt(2)
mav 12,204,28
R 44,54272
mav 16,208,28
s_add_u32 m0, s50, 40960
mva 20,28,212
mav 12,216,32
R 48,55296
D 88,0
mav 16,220,32
mva 20,32,224
s_waitcnt lgkmcnt(2)
mav 12,228,36
R 52,56320
D 88,1024
mav 16,232,36
mva 20,36,236
mav 12,240,40
R 24,57344
mav 16,244,40
D 88,2048
mva 20,40,248
s_waitcnt lgkmcnt(2)
mav 12,252,44
R 28,58368
mvv 16,128,44
D 88,3072
mvv 20,44,132
mvv 12,136,48
R 32,59392
s_add_u32 m0, s50, 45056
mvv 16,140,48
mvv 20,48,144
D 89,0
s_waitcnt lgkmcnt(2)
mvv 12,148,52
R 36,60416
mvv 16,152,52
mvv 20,52,156
D 89,1024
mvv 12,160,24
R 40,61440
mvv 16,164,24
ds_read_b32 v98, v82 offset:0
mvv 20,24,168
s_waitcnt lgkmcnt(3)
mvv 12,172,28
R 44,62464
D 89,2048
mvv 16,176,28
mvv 20,28,180
mvv 12,184,32
R 48,63488
D 89,3072
mvv 16,188,32
s_xor_b32 s50, s50, 0x10000
mvv 20,32,192
s_waitcnt lgkmcnt(2)
mvv 12,196,36
R 52,64512
v_xor_b32 v80, 0x10000, v80
mvv 16,200,36
v_cmp_gt_i32 vcc, v91, v98
s_add_u32 s49, s49, 346112
mvv 20,36,204
s_waitcnt vmcnt(17)
s_cbranch_vccnz CQB0_5
BQB0_5:
mvv 12,208,40
s_barrier
R 24,0
mvv 16,212,40
ds_read_b64 v[94:95], v84 offset:128
ds_read_b128 v[64:67], v85 offset:128
mvv 20,40,216
ds_read_b128 v[68:71], v85 offset:144
s_waitcnt lgkmcnt(5)
mvv 12,220,44
R 28,1024
mvv 16,224,44
s_mov_b32 m0, 142336
mvv 20,44,228
mvv 12,232,48
R 32,2048
buffer_load_dword v90, s[36:39], 0 offen sc1 lds
mvv 16,236,48
mvv 20,48,240
s_waitcnt lgkmcnt(2)
mvv 12,244,52
R 36,3072
mvv 16,248,52
mvv 20,52,252
s_mov_b32 s60, -29
.p2align 6
LQB0_6:
v_mfma_f32_16x16x32_bf16 v[0:3], a[0:3], v[24:27], 0
R 40,4096
v_mul_f32 v96, v95, v56
v_mfma_f32_16x16x32_bf16 v[4:7], a[4:7], v[24:27], 0
v_mul_f32 v97, v95, v57
v_fma_f32 v12, v12, v94, v96
v_mfma_f32_16x16x32_bf16 v[8:11], v[24:27], a[8:11], 0
v_fma_f32 v13, v13, v94, v97
s_waitcnt lgkmcnt(2)
mav 0,12,28
R 44,5120
v_mul_f32 v96, v95, v58
mav 4,16,28
v_fma_f32 v14, v14, v94, v96
v_mul_f32 v97, v95, v59
mva 8,28,20
v_fma_f32 v15, v15, v94, v97
v_mul_f32 v96, v95, v60
mav 0,24,32
R 48,6144
v_fma_f32 v16, v16, v94, v96
mav 4,28,32
v_mul_f32 v97, v95, v61
v_fma_f32 v17, v17, v94, v97
mva 8,32,32
v_mul_f32 v96, v95, v62
s_waitcnt lgkmcnt(2)
mav 0,36,36
R 52,7168
v_fma_f32 v18, v18, v94, v96
mav 4,40,36
v_mul_f32 v97, v95, v63
v_fma_f32 v19, v19, v94, v97
mva 8,36,44
v_cvt_pk_bf16_f32 v72, v12, v13
v_cvt_pk_bf16_f32 v73, v14, v15
mav 0,48,40
R 24,8192
v_cvt_pk_bf16_f32 v74, v16, v17
mav 4,52,40
v_cvt_pk_bf16_f32 v75, v18, v19
ds_write_b128 v83, v[72:75] offset:0
mva 8,40,56
v_mul_f32 v65, v65, v93
s_waitcnt lgkmcnt(3)
mav 0,60,44
R 28,9216
v_fma_f32 v20, v20, v64, v65
mav 4,64,44
v_mul_f32 v67, v67, v93
v_fma_f32 v21, v21, v66, v67
mva 8,44,68
v_mul_f32 v69, v69, v93
v_fma_f32 v22, v22, v68, v69
mav 0,72,48
R 32,10240
v_mul_f32 v71, v71, v93
mav 4,76,48
v_fma_f32 v23, v23, v70, v71
v_cvt_pk_bf16_f32 v76, v20, v21
mva 8,48,80
v_cvt_pk_bf16_f32 v77, v22, v23
s_waitcnt lgkmcnt(2)
mav 0,84,52
R 36,11264
ds_write_b64 v83, v[76:77] offset:1024
mav 4,88,52
mva 8,52,92
mav 0,96,24
R 40,12288
s_add_u32 m0, s50, 0
mav 4,100,24
s_waitcnt lgkmcnt(1)
s_barrier
mva 8,24,104
D 86,0
mav 0,108,28
R 44,13312
mav 4,112,28
mva 8,28,116
D 86,1024
mav 0,120,32
R 48,14336
mav 4,124,32
mva 8,32,128
mav 0,132,36
R 52,15360
D 86,2048
mav 4,136,36
mva 8,36,140
s_waitcnt lgkmcnt(2)
mav 0,144,40
R 24,16384
D 86,3072
mav 4,148,40
mva 8,40,152
s_add_u32 m0, s50, 4096
mav 0,156,44
R 28,17408
mav 4,160,44
D 87,0
v_add_u32 v91, 1, v91
mva 8,44,164
s_waitcnt lgkmcnt(2)
mav 0,168,48
R 32,18432
mav 4,172,48
D 87,1024
mva 8,48,176
mav 0,180,52
R 36,19456
mav 4,184,52
mva 8,52,188
D 87,2048
s_waitcnt lgkmcnt(2)
mav 0,192,24
R 40,20480
mav 4,196,24
mva 8,24,200
D 87,3072
mav 0,204,28
R 44,21504
mav 4,208,28
s_add_u32 m0, s50, 8192
mva 8,28,212
s_waitcnt lgkmcnt(2)
mav 0,216,32
R 48,22528
D 88,0
mav 4,220,32
mva 8,32,224
mav 0,228,36
R 52,23552
D 88,1024
mav 4,232,36
mva 8,36,236
s_waitcnt lgkmcnt(2)
mav 0,240,40
R 24,24576
mav 4,244,40
D 88,2048
mva 8,40,248
mav 0,252,44
R 28,25600
mvv 4,128,44
D 88,3072
mvv 8,44,132
s_waitcnt lgkmcnt(2)
mvv 0,136,48
R 32,26624
s_add_u32 m0, s50, 12288
mvv 4,140,48
mvv 8,48,144
D 89,0
mvv 0,148,52
R 36,27648
mvv 4,152,52
mvv 8,52,156
D 89,1024
s_waitcnt lgkmcnt(2)
mvv 0,160,24
R 40,28672
mvv 4,164,24
ds_read_b32 v98, v82 offset:0
mvv 8,24,168
mvv 0,172,28
R 44,29696
D 89,2048
mvv 4,176,28
mvv 8,28,180
s_waitcnt lgkmcnt(3)
mvv 0,184,32
R 48,30720
D 89,3072
mvv 4,188,32
mvv 8,32,192
mvv 0,196,36
R 52,31744
mvv 4,200,36
s_waitcnt lgkmcnt(2)
v_cmp_gt_i32 vcc, v91, v98
mvv 8,36,204
s_waitcnt vmcnt(17)
s_cbranch_vccnz CQB0_7
BQB0_7:
mvv 0,208,40
s_barrier
R 24,32768
mvv 4,212,40
s_add_u32 s49, s49, 346112
ds_read_b64 v[94:95], v84 offset:0
mvv 8,40,216
ds_read_b128 v[64:67], v85 offset:0
ds_read_b128 v[68:71], v85 offset:16
mvv 0,220,44
R 28,33792
mvv 4,224,44
s_mov_b32 m0, 142336
mvv 8,44,228
s_waitcnt lgkmcnt(5)
mvv 0,232,48
R 32,34816
buffer_load_dword v90, s[36:39], 0 offen sc1 lds
mvv 4,236,48
mvv 8,48,240
mvv 0,244,52
R 36,35840
mvv 4,248,52
mvv 8,52,252
s_waitcnt lgkmcnt(2)
v_mfma_f32_16x16x32_bf16 v[12:15], a[0:3], v[24:27], 0
R 40,36864
v_mul_f32 v96, v95, v56
v_mfma_f32_16x16x32_bf16 v[16:19], a[4:7], v[24:27], 0
v_fma_f32 v0, v0, v94, v96
v_mul_f32 v97, v95, v57
v_mfma_f32_16x16x32_bf16 v[20:23], v[24:27], a[8:11], 0
v_fma_f32 v1, v1, v94, v97
v_mul_f32 v96, v95, v58
mav 12,12,28
R 44,37888
v_fma_f32 v2, v2, v94, v96
mav 16,16,28
v_mul_f32 v97, v95, v59
v_fma_f32 v3, v3, v94, v97
mva 20,28,20
v_mul_f32 v96, v95, v60
s_waitcnt lgkmcnt(2)
mav 12,24,32
R 48,38912
v_fma_f32 v4, v4, v94, v96
mav 16,28,32
v_mul_f32 v97, v95, v61
v_fma_f32 v5, v5, v94, v97
mva 20,32,32
v_mul_f32 v96, v95, v62
v_fma_f32 v6, v6, v94, v96
mav 12,36,36
R 52,39936
v_mul_f32 v97, v95, v63
mav 16,40,36
v_fma_f32 v7, v7, v94, v97
v_cvt_pk_bf16_f32 v72, v0, v1
mva 20,36,44
v_cvt_pk_bf16_f32 v73, v2, v3
s_waitcnt lgkmcnt(2)
mav 12,48,40
R 24,40960
v_cvt_pk_bf16_f32 v74, v4, v5
mav 16,52,40
v_cvt_pk_bf16_f32 v75, v6, v7
ds_write_b128 v83, v[72:75] offset:0
mva 20,40,56
v_mul_f32 v65, v65, v93
v_fma_f32 v8, v8, v64, v65
mav 12,60,44
R 28,41984
v_mul_f32 v67, v67, v93
mav 16,64,44
v_fma_f32 v9, v9, v66, v67
v_mul_f32 v69, v69, v93
mva 20,44,68
v_fma_f32 v10, v10, v68, v69
s_waitcnt lgkmcnt(3)
mav 12,72,48
R 32,43008
v_mul_f32 v71, v71, v93
mav 16,76,48
v_fma_f32 v11, v11, v70, v71
v_cvt_pk_bf16_f32 v76, v8, v9
mva 20,48,80
v_cvt_pk_bf16_f32 v77, v10, v11
ds_write_b64 v83, v[76:77] offset:1024
mav 12,84,52
R 36,44032
mav 16,88,52
mva 20,52,92
s_waitcnt lgkmcnt(3)
mav 12,96,24
R 40,45056
s_add_u32 m0, s50, 32768
mav 16,100,24
s_waitcnt lgkmcnt(2)
s_barrier
mva 20,24,104
D 86,0
mav 12,108,28
R 44,46080
mav 16,112,28
mva 20,28,116
D 86,1024
mav 12,120,32
R 48,47104
mav 16,124,32
mva 20,32,128
s_waitcnt lgkmcnt(2)
mav 12,132,36
R 52,48128
D 86,2048
mav 16,136,36
mva 20,36,140
mav 12,144,40
R 24,49152
D 86,3072
mav 16,148,40
mva 20,40,152
s_add_u32 m0, s50, 36864
s_waitcnt lgkmcnt(2)
mav 12,156,44
R 28,50176
mav 16,160,44
D 87,0
v_add_u32 v91, 1, v91
mva 20,44,164
mav 12,168,48
R 32,51200
mav 16,172,48
D 87,1024
mva 20,48,176
s_waitcnt lgkmcnt(2)
mav 12,180,52
R 36,52224
mav 16,184,52
mva 20,52,188
D 87,2048
mav 12,192,24
R 40,53248
mav 16,196,24
mva 20,24,200
D 87,3072
s_waitcnt lgkmcnt(2)
mav 12,204,28
R 44,54272
mav 16,208,28
s_add_u32 m0, s50, 40960
mva 20,28,212
mav 12,216,32
R 48,55296
D 88,0
mav 16,220,32
mva 20,32,224
s_waitcnt lgkmcnt(2)
mav 12,228,36
R 52,56320
D 88,1024
mav 16,232,36
mva 20,36,236
mav 12,240,40
R 24,57344
mav 16,244,40
D 88,2048
mva 20,40,248
s_waitcnt lgkmcnt(2)
mav 12,252,44
R 28,58368
mvv 16,128,44
D 88,3072
mvv 20,44,132
mvv 12,136,48
R 32,59392
s_add_u32 m0, s50, 45056
mvv 16,140,48
mvv 20,48,144
D 89,0
s_waitcnt lgkmcnt(2)
mvv 12,148,52
R 36,60416
mvv 16,152,52
mvv 20,52,156
D 89,1024
mvv 12,160,24
R 40,61440
mvv 16,164,24
ds_read_b32 v98, v82 offset:0
mvv 20,24,168
s_waitcnt lgkmcnt(3)
mvv 12,172,28
R 44,62464
D 89,2048
mvv 16,176,28
mvv 20,28,180
mvv 12,184,32
R 48,63488
D 89,3072
mvv 16,188,32
s_xor_b32 s50, s50, 0x10000
mvv 20,32,192
s_waitcnt lgkmcnt(2)
mvv 12,196,36
R 52,64512
v_xor_b32 v80, 0x10000, v80
mvv 16,200,36
v_cmp_gt_i32 vcc, v91, v98
s_add_u32 s49, s49, 346112
mvv 20,36,204
s_waitcnt vmcnt(17)
s_cbranch_vccnz CQB0_8
BQB0_8:
mvv 12,208,40
s_barrier
R 24,0
mvv 16,212,40
ds_read_b64 v[94:95], v84 offset:128
ds_read_b128 v[64:67], v85 offset:128
mvv 20,40,216
ds_read_b128 v[68:71], v85 offset:144
s_waitcnt lgkmcnt(5)
mvv 12,220,44
R 28,1024
mvv 16,224,44
s_mov_b32 m0, 142336
mvv 20,44,228
mvv 12,232,48
R 32,2048
buffer_load_dword v90, s[36:39], 0 offen sc1 lds
mvv 16,236,48
mvv 20,48,240
s_waitcnt lgkmcnt(2)
mvv 12,244,52
R 36,3072
mvv 16,248,52
mvv 20,52,252
s_add_u32 s60, s60, 1
s_cbranch_scc0 LQB0_6
v_mfma_f32_16x16x32_bf16 v[0:3], a[0:3], v[24:27], 0
R 40,4096
v_mul_f32 v96, v95, v56
v_mfma_f32_16x16x32_bf16 v[4:7], a[4:7], v[24:27], 0
v_mul_f32 v97, v95, v57
v_fma_f32 v12, v12, v94, v96
v_mfma_f32_16x16x32_bf16 v[8:11], v[24:27], a[8:11], 0
v_fma_f32 v13, v13, v94, v97
s_waitcnt lgkmcnt(2)
mav 0,12,28
R 44,5120
v_mul_f32 v96, v95, v58
mav 4,16,28
v_fma_f32 v14, v14, v94, v96
v_mul_f32 v97, v95, v59
mva 8,28,20
v_fma_f32 v15, v15, v94, v97
v_mul_f32 v96, v95, v60
mav 0,24,32
R 48,6144
v_fma_f32 v16, v16, v94, v96
mav 4,28,32
v_mul_f32 v97, v95, v61
v_fma_f32 v17, v17, v94, v97
mva 8,32,32
v_mul_f32 v96, v95, v62
s_waitcnt lgkmcnt(2)
mav 0,36,36
R 52,7168
v_fma_f32 v18, v18, v94, v96
mav 4,40,36
v_mul_f32 v97, v95, v63
v_fma_f32 v19, v19, v94, v97
mva 8,36,44
v_cvt_pk_bf16_f32 v72, v12, v13
v_cvt_pk_bf16_f32 v73, v14, v15
mav 0,48,40
R 24,8192
v_cvt_pk_bf16_f32 v74, v16, v17
mav 4,52,40
v_cvt_pk_bf16_f32 v75, v18, v19
ds_write_b128 v83, v[72:75] offset:0
mva 8,40,56
v_mul_f32 v65, v65, v93
s_waitcnt lgkmcnt(3)
mav 0,60,44
R 28,9216
v_fma_f32 v20, v20, v64, v65
mav 4,64,44
v_mul_f32 v67, v67, v93
v_fma_f32 v21, v21, v66, v67
mva 8,44,68
v_mul_f32 v69, v69, v93
v_fma_f32 v22, v22, v68, v69
mav 0,72,48
R 32,10240
v_mul_f32 v71, v71, v93
mav 4,76,48
v_fma_f32 v23, v23, v70, v71
v_cvt_pk_bf16_f32 v76, v20, v21
mva 8,48,80
v_cvt_pk_bf16_f32 v77, v22, v23
s_waitcnt lgkmcnt(2)
mav 0,84,52
R 36,11264
ds_write_b64 v83, v[76:77] offset:1024
mav 4,88,52
mva 8,52,92
mav 0,96,24
R 40,12288
s_add_u32 m0, s50, 0
mav 4,100,24
s_waitcnt lgkmcnt(1)
s_barrier
mva 8,24,104
D 86,0
mav 0,108,28
R 44,13312
mav 4,112,28
mva 8,28,116
D 86,1024
mav 0,120,32
R 48,14336
mav 4,124,32
mva 8,32,128
mav 0,132,36
R 52,15360
D 86,2048
mav 4,136,36
mva 8,36,140
s_waitcnt lgkmcnt(2)
mav 0,144,40
R 24,16384
D 86,3072
mav 4,148,40
mva 8,40,152
s_add_u32 m0, s50, 4096
mav 0,156,44
R 28,17408
mav 4,160,44
D 87,0
v_add_u32 v91, 1, v91
mva 8,44,164
s_waitcnt lgkmcnt(2)
mav 0,168,48
R 32,18432
mav 4,172,48
D 87,1024
mva 8,48,176
mav 0,180,52
R 36,19456
mav 4,184,52
mva 8,52,188
D 87,2048
s_waitcnt lgkmcnt(2)
mav 0,192,24
R 40,20480
mav 4,196,24
mva 8,24,200
D 87,3072
mav 0,204,28
R 44,21504
mav 4,208,28
s_add_u32 m0, s50, 8192
mva 8,28,212
s_waitcnt lgkmcnt(2)
mav 0,216,32
R 48,22528
D 88,0
mav 4,220,32
mva 8,32,224
mav 0,228,36
R 52,23552
D 88,1024
mav 4,232,36
mva 8,36,236
s_waitcnt lgkmcnt(2)
mav 0,240,40
R 24,24576
mav 4,244,40
D 88,2048
mva 8,40,248
mav 0,252,44
R 28,25600
mvv 4,128,44
D 88,3072
mvv 8,44,132
s_waitcnt lgkmcnt(2)
mvv 0,136,48
R 32,26624
s_add_u32 m0, s50, 12288
mvv 4,140,48
mvv 8,48,144
D 89,0
mvv 0,148,52
R 36,27648
mvv 4,152,52
mvv 8,52,156
D 89,1024
s_waitcnt lgkmcnt(2)
mvv 0,160,24
R 40,28672
mvv 4,164,24
ds_read_b32 v98, v82 offset:0
mvv 8,24,168
mvv 0,172,28
R 44,29696
D 89,2048
mvv 4,176,28
mvv 8,28,180
s_waitcnt lgkmcnt(3)
mvv 0,184,32
R 48,30720
D 89,3072
mvv 4,188,32
mvv 8,32,192
mvv 0,196,36
R 52,31744
mvv 4,200,36
s_waitcnt lgkmcnt(2)
v_cmp_gt_i32 vcc, v91, v98
mvv 8,36,204
s_waitcnt vmcnt(17)
s_cbranch_vccnz CQB0_9
BQB0_9:
mvv 0,208,40
s_barrier
R 24,32768
mvv 4,212,40
s_add_u32 s49, s49, 346112
ds_read_b64 v[94:95], v84 offset:0
mvv 8,40,216
ds_read_b128 v[64:67], v85 offset:0
ds_read_b128 v[68:71], v85 offset:16
mvv 0,220,44
R 28,33792
mvv 4,224,44
s_mov_b32 m0, 142336
mvv 8,44,228
s_waitcnt lgkmcnt(5)
mvv 0,232,48
R 32,34816
buffer_load_dword v90, s[36:39], 0 offen sc1 lds
mvv 4,236,48
mvv 8,48,240
mvv 0,244,52
R 36,35840
mvv 4,248,52
mvv 8,52,252
s_waitcnt lgkmcnt(2)
v_mfma_f32_16x16x32_bf16 v[12:15], a[0:3], v[24:27], 0
R 40,36864
v_mul_f32 v96, v95, v56
v_mfma_f32_16x16x32_bf16 v[16:19], a[4:7], v[24:27], 0
v_fma_f32 v0, v0, v94, v96
v_mul_f32 v97, v95, v57
v_mfma_f32_16x16x32_bf16 v[20:23], v[24:27], a[8:11], 0
v_fma_f32 v1, v1, v94, v97
v_mul_f32 v96, v95, v58
mav 12,12,28
R 44,37888
v_fma_f32 v2, v2, v94, v96
mav 16,16,28
v_mul_f32 v97, v95, v59
v_fma_f32 v3, v3, v94, v97
mva 20,28,20
v_mul_f32 v96, v95, v60
s_waitcnt lgkmcnt(2)
mav 12,24,32
R 48,38912
v_fma_f32 v4, v4, v94, v96
mav 16,28,32
v_mul_f32 v97, v95, v61
v_fma_f32 v5, v5, v94, v97
mva 20,32,32
v_mul_f32 v96, v95, v62
v_fma_f32 v6, v6, v94, v96
mav 12,36,36
R 52,39936
v_mul_f32 v97, v95, v63
mav 16,40,36
v_fma_f32 v7, v7, v94, v97
v_cvt_pk_bf16_f32 v72, v0, v1
mva 20,36,44
v_cvt_pk_bf16_f32 v73, v2, v3
s_waitcnt lgkmcnt(2)
mav 12,48,40
R 24,40960
v_cvt_pk_bf16_f32 v74, v4, v5
mav 16,52,40
v_cvt_pk_bf16_f32 v75, v6, v7
ds_write_b128 v83, v[72:75] offset:0
mva 20,40,56
v_mul_f32 v65, v65, v93
v_fma_f32 v8, v8, v64, v65
mav 12,60,44
R 28,41984
v_mul_f32 v67, v67, v93
mav 16,64,44
v_fma_f32 v9, v9, v66, v67
v_mul_f32 v69, v69, v93
mva 20,44,68
v_fma_f32 v10, v10, v68, v69
s_waitcnt lgkmcnt(3)
mav 12,72,48
R 32,43008
v_mul_f32 v71, v71, v93
mav 16,76,48
v_fma_f32 v11, v11, v70, v71
v_cvt_pk_bf16_f32 v76, v8, v9
mva 20,48,80
v_cvt_pk_bf16_f32 v77, v10, v11
ds_write_b64 v83, v[76:77] offset:1024
mav 12,84,52
R 36,44032
mav 16,88,52
mva 20,52,92
s_waitcnt lgkmcnt(3)
mav 12,96,24
R 40,45056
s_add_u32 m0, s50, 32768
mav 16,100,24
s_waitcnt lgkmcnt(2)
s_barrier
mva 20,24,104
D 86,0
mav 12,108,28
R 44,46080
mav 16,112,28
mva 20,28,116
D 86,1024
mav 12,120,32
R 48,47104
mav 16,124,32
mva 20,32,128
s_waitcnt lgkmcnt(2)
mav 12,132,36
R 52,48128
D 86,2048
mav 16,136,36
mva 20,36,140
mav 12,144,40
R 24,49152
D 86,3072
mav 16,148,40
mva 20,40,152
s_add_u32 m0, s50, 36864
s_waitcnt lgkmcnt(2)
mav 12,156,44
R 28,50176
mav 16,160,44
D 87,0
v_add_u32 v91, 1, v91
mva 20,44,164
mav 12,168,48
R 32,51200
mav 16,172,48
D 87,1024
mva 20,48,176
s_waitcnt lgkmcnt(2)
mav 12,180,52
R 36,52224
mav 16,184,52
mva 20,52,188
D 87,2048
mav 12,192,24
R 40,53248
mav 16,196,24
mva 20,24,200
D 87,3072
s_waitcnt lgkmcnt(2)
mav 12,204,28
R 44,54272
mav 16,208,28
s_add_u32 m0, s50, 40960
mva 20,28,212
mav 12,216,32
R 48,55296
D 88,0
mav 16,220,32
mva 20,32,224
s_waitcnt lgkmcnt(2)
mav 12,228,36
R 52,56320
D 88,1024
mav 16,232,36
mva 20,36,236
mav 12,240,40
R 24,57344
mav 16,244,40
D 88,2048
mva 20,40,248
s_waitcnt lgkmcnt(2)
mav 12,252,44
R 28,58368
mvv 16,128,44
D 88,3072
mvv 20,44,132
mvv 12,136,48
R 32,59392
s_add_u32 m0, s50, 45056
mvv 16,140,48
mvv 20,48,144
D 89,0
s_waitcnt lgkmcnt(2)
mvv 12,148,52
R 36,60416
mvv 16,152,52
mvv 20,52,156
D 89,1024
mvv 12,160,24
R 40,61440
mvv 16,164,24
ds_read_b32 v98, v82 offset:0
mvv 20,24,168
s_waitcnt lgkmcnt(3)
mvv 12,172,28
R 44,62464
D 89,2048
mvv 16,176,28
mvv 20,28,180
mvv 12,184,32
R 48,63488
D 89,3072
mvv 16,188,32
mvv 20,32,192
s_waitcnt lgkmcnt(2)
mvv 12,196,36
R 52,64512
v_xor_b32 v80, 0x10000, v80
mvv 16,200,36
v_cmp_gt_i32 vcc, v91, v98
s_add_u32 s49, s49, 346112
mvv 20,36,204
s_waitcnt vmcnt(17)
s_cbranch_vccnz CQB0_10
BQB0_10:
mvv 12,208,40
s_barrier
R 24,0
mvv 16,212,40
ds_read_b64 v[94:95], v84 offset:128
ds_read_b128 v[64:67], v85 offset:128
mvv 20,40,216
ds_read_b128 v[68:71], v85 offset:144
s_waitcnt lgkmcnt(5)
mvv 12,220,44
R 28,1024
mvv 16,224,44
mvv 20,44,228
mvv 12,232,48
R 32,2048
mvv 16,236,48
mvv 20,48,240
s_waitcnt lgkmcnt(2)
mvv 12,244,52
R 36,3072
mvv 16,248,52
mvv 20,52,252
v_mfma_f32_16x16x32_bf16 v[0:3], a[0:3], v[24:27], 0
R 40,4096
v_mul_f32 v96, v95, v56
v_mfma_f32_16x16x32_bf16 v[4:7], a[4:7], v[24:27], 0
v_mul_f32 v97, v95, v57
v_fma_f32 v12, v12, v94, v96
v_mfma_f32_16x16x32_bf16 v[8:11], v[24:27], a[8:11], 0
v_fma_f32 v13, v13, v94, v97
s_waitcnt lgkmcnt(2)
mav 0,12,28
R 44,5120
v_mul_f32 v96, v95, v58
mav 4,16,28
v_fma_f32 v14, v14, v94, v96
v_mul_f32 v97, v95, v59
mva 8,28,20
v_fma_f32 v15, v15, v94, v97
v_mul_f32 v96, v95, v60
mav 0,24,32
R 48,6144
v_fma_f32 v16, v16, v94, v96
mav 4,28,32
v_mul_f32 v97, v95, v61
v_fma_f32 v17, v17, v94, v97
mva 8,32,32
v_mul_f32 v96, v95, v62
s_waitcnt lgkmcnt(2)
mav 0,36,36
R 52,7168
v_fma_f32 v18, v18, v94, v96
mav 4,40,36
v_mul_f32 v97, v95, v63
v_fma_f32 v19, v19, v94, v97
mva 8,36,44
v_cvt_pk_bf16_f32 v72, v12, v13
v_cvt_pk_bf16_f32 v73, v14, v15
mav 0,48,40
R 24,8192
v_cvt_pk_bf16_f32 v74, v16, v17
mav 4,52,40
v_cvt_pk_bf16_f32 v75, v18, v19
ds_write_b128 v83, v[72:75] offset:0
mva 8,40,56
v_mul_f32 v65, v65, v93
s_waitcnt lgkmcnt(3)
mav 0,60,44
R 28,9216
v_fma_f32 v20, v20, v64, v65
mav 4,64,44
v_mul_f32 v67, v67, v93
v_fma_f32 v21, v21, v66, v67
mva 8,44,68
v_mul_f32 v69, v69, v93
v_fma_f32 v22, v22, v68, v69
mav 0,72,48
R 32,10240
v_mul_f32 v71, v71, v93
mav 4,76,48
v_fma_f32 v23, v23, v70, v71
v_cvt_pk_bf16_f32 v76, v20, v21
mva 8,48,80
v_cvt_pk_bf16_f32 v77, v22, v23
s_waitcnt lgkmcnt(2)
mav 0,84,52
R 36,11264
ds_write_b64 v83, v[76:77] offset:1024
mav 4,88,52
mva 8,52,92
mav 0,96,24
R 40,12288
mav 4,100,24
s_waitcnt lgkmcnt(1)
s_barrier
mva 8,24,104
)"
R"(
mav 0,108,28
R 44,13312
mav 4,112,28
mva 8,28,116
mav 0,120,32
R 48,14336
mav 4,124,32
mva 8,32,128
mav 0,132,36
R 52,15360
mav 4,136,36
mva 8,36,140
s_waitcnt lgkmcnt(2)
mav 0,144,40
R 24,16384
mav 4,148,40
mva 8,40,152
mav 0,156,44
R 28,17408
mav 4,160,44
mva 8,44,164
s_waitcnt lgkmcnt(2)
mav 0,168,48
R 32,18432
mav 4,172,48
mva 8,48,176
mav 0,180,52
R 36,19456
mav 4,184,52
mva 8,52,188
s_waitcnt lgkmcnt(2)
mav 0,192,24
R 40,20480
mav 4,196,24
mva 8,24,200
mav 0,204,28
R 44,21504
mav 4,208,28
mva 8,28,212
s_waitcnt lgkmcnt(2)
mav 0,216,32
R 48,22528
mav 4,220,32
mva 8,32,224
mav 0,228,36
R 52,23552
mav 4,232,36
mva 8,36,236
s_waitcnt lgkmcnt(2)
mav 0,240,40
R 24,24576
mav 4,244,40
mva 8,40,248
mav 0,252,44
R 28,25600
mvv 4,128,44
mvv 8,44,132
s_waitcnt lgkmcnt(2)
mvv 0,136,48
R 32,26624
mvv 4,140,48
mvv 8,48,144
mvv 0,148,52
R 36,27648
mvv 4,152,52
mvv 8,52,156
s_waitcnt lgkmcnt(2)
mvv 0,160,24
R 40,28672
mvv 4,164,24
mvv 8,24,168
mvv 0,172,28
R 44,29696
mvv 4,176,28
mvv 8,28,180
s_waitcnt lgkmcnt(2)
mvv 0,184,32
R 48,30720
mvv 4,188,32
mvv 8,32,192
mvv 0,196,36
R 52,31744
mvv 4,200,36
s_add_u32 s49, s49, 346112
mvv 8,36,204
s_waitcnt vmcnt(0)
s_barrier
s_waitcnt lgkmcnt(2)
mvv 0,208,40
R 24,32768
mvv 4,212,40
ds_read_b64 v[94:95], v84 offset:0
ds_read_b128 v[64:67], v85 offset:0
mvv 8,40,216
ds_read_b128 v[68:71], v85 offset:16
mvv 0,220,44
R 28,33792
mvv 4,224,44
mvv 8,44,228
s_waitcnt lgkmcnt(5)
mvv 0,232,48
R 32,34816
mvv 4,236,48
mvv 8,48,240
mvv 0,244,52
R 36,35840
mvv 4,248,52
mvv 8,52,252
s_waitcnt lgkmcnt(2)
v_mfma_f32_16x16x32_bf16 v[12:15], a[0:3], v[24:27], 0
R 40,36864
v_mul_f32 v96, v95, v56
v_mfma_f32_16x16x32_bf16 v[16:19], a[4:7], v[24:27], 0
v_fma_f32 v0, v0, v94, v96
v_mul_f32 v97, v95, v57
v_mfma_f32_16x16x32_bf16 v[20:23], v[24:27], a[8:11], 0
v_fma_f32 v1, v1, v94, v97
v_mul_f32 v96, v95, v58
mav 12,12,28
R 44,37888
v_fma_f32 v2, v2, v94, v96
mav 16,16,28
v_mul_f32 v97, v95, v59
v_fma_f32 v3, v3, v94, v97
mva 20,28,20
v_mul_f32 v96, v95, v60
s_waitcnt lgkmcnt(2)
mav 12,24,32
R 48,38912
v_fma_f32 v4, v4, v94, v96
mav 16,28,32
v_mul_f32 v97, v95, v61
v_fma_f32 v5, v5, v94, v97
mva 20,32,32
v_mul_f32 v96, v95, v62
v_fma_f32 v6, v6, v94, v96
mav 12,36,36
R 52,39936
v_mul_f32 v97, v95, v63
mav 16,40,36
v_fma_f32 v7, v7, v94, v97
v_cvt_pk_bf16_f32 v72, v0, v1
mva 20,36,44
v_cvt_pk_bf16_f32 v73, v2, v3
s_waitcnt lgkmcnt(2)
mav 12,48,40
R 24,40960
v_cvt_pk_bf16_f32 v74, v4, v5
mav 16,52,40
v_cvt_pk_bf16_f32 v75, v6, v7
ds_write_b128 v83, v[72:75] offset:0
mva 20,40,56
v_mul_f32 v65, v65, v93
v_fma_f32 v8, v8, v64, v65
mav 12,60,44
R 28,41984
v_mul_f32 v67, v67, v93
mav 16,64,44
v_fma_f32 v9, v9, v66, v67
v_mul_f32 v69, v69, v93
mva 20,44,68
v_fma_f32 v10, v10, v68, v69
s_waitcnt lgkmcnt(3)
mav 12,72,48
R 32,43008
v_mul_f32 v71, v71, v93
mav 16,76,48
v_fma_f32 v11, v11, v70, v71
v_cvt_pk_bf16_f32 v76, v8, v9
mva 20,48,80
v_cvt_pk_bf16_f32 v77, v10, v11
ds_write_b64 v83, v[76:77] offset:1024
mav 12,84,52
R 36,44032
mav 16,88,52
mva 20,52,92
s_waitcnt lgkmcnt(3)
mav 12,96,24
R 40,45056
mav 16,100,24
s_waitcnt lgkmcnt(2)
s_barrier
mva 20,24,104
mav 12,108,28
R 44,46080
mav 16,112,28
mva 20,28,116
mav 12,120,32
R 48,47104
mav 16,124,32
mva 20,32,128
s_waitcnt lgkmcnt(2)
mav 12,132,36
R 52,48128
mav 16,136,36
mva 20,36,140
mav 12,144,40
R 24,49152
mav 16,148,40
mva 20,40,152
s_waitcnt lgkmcnt(2)
mav 12,156,44
R 28,50176
mav 16,160,44
mva 20,44,164
mav 12,168,48
R 32,51200
mav 16,172,48
mva 20,48,176
s_waitcnt lgkmcnt(2)
mav 12,180,52
R 36,52224
mav 16,184,52
mva 20,52,188
mav 12,192,24
R 40,53248
mav 16,196,24
mva 20,24,200
s_waitcnt lgkmcnt(2)
mav 12,204,28
R 44,54272
mav 16,208,28
mva 20,28,212
mav 12,216,32
R 48,55296
mav 16,220,32
mva 20,32,224
s_waitcnt lgkmcnt(2)
mav 12,228,36
R 52,56320
mav 16,232,36
mva 20,36,236
mav 12,240,40
R 24,57344
mav 16,244,40
mva 20,40,248
s_waitcnt lgkmcnt(2)
mav 12,252,44
R 28,58368
mvv 16,128,44
mvv 20,44,132
mvv 12,136,48
R 32,59392
mvv 16,140,48
mvv 20,48,144
s_waitcnt lgkmcnt(2)
mvv 12,148,52
R 36,60416
mvv 16,152,52
mvv 20,52,156
mvv 12,160,24
R 40,61440
mvv 16,164,24
mvv 20,24,168
s_waitcnt lgkmcnt(2)
mvv 12,172,28
R 44,62464
mvv 16,176,28
mvv 20,28,180
mvv 12,184,32
R 48,63488
mvv 16,188,32
mvv 20,32,192
s_waitcnt lgkmcnt(2)
mvv 12,196,36
R 52,64512
mvv 16,200,36
mvv 20,36,204
s_barrier
mvv 12,208,40
mvv 16,212,40
ds_read_b64 v[94:95], v84 offset:128
ds_read_b128 v[64:67], v85 offset:128
mvv 20,40,216
ds_read_b128 v[68:71], v85 offset:144
s_waitcnt lgkmcnt(4)
mvv 12,220,44
mvv 16,224,44
mvv 20,44,228
mvv 12,232,48
mvv 16,236,48
mvv 20,48,240
s_waitcnt lgkmcnt(0)
mvv 12,244,52
mvv 16,248,52
mvv 20,52,252
v_mul_f32 v96, v95, v56
s_nop 4
v_fma_f32 v12, v12, v94, v96
v_mul_f32 v97, v95, v57
v_fma_f32 v13, v13, v94, v97
v_mul_f32 v96, v95, v58
v_fma_f32 v14, v14, v94, v96
v_mul_f32 v97, v95, v59
v_fma_f32 v15, v15, v94, v97
v_mul_f32 v96, v95, v60
v_fma_f32 v16, v16, v94, v96
v_mul_f32 v97, v95, v61
v_fma_f32 v17, v17, v94, v97
v_mul_f32 v96, v95, v62
v_fma_f32 v18, v18, v94, v96
v_mul_f32 v97, v95, v63
v_fma_f32 v19, v19, v94, v97
v_cvt_pk_bf16_f32 v72, v12, v13
v_cvt_pk_bf16_f32 v73, v14, v15
v_cvt_pk_bf16_f32 v74, v16, v17
v_cvt_pk_bf16_f32 v75, v18, v19
ds_write_b128 v83, v[72:75] offset:0
v_mul_f32 v65, v65, v93
v_fma_f32 v20, v20, v64, v65
v_mul_f32 v67, v67, v93
v_fma_f32 v21, v21, v66, v67
v_mul_f32 v69, v69, v93
v_fma_f32 v22, v22, v68, v69
v_mul_f32 v71, v71, v93
v_fma_f32 v23, v23, v70, v71
v_cvt_pk_bf16_f32 v76, v20, v21
v_cvt_pk_bf16_f32 v77, v22, v23
ds_write_b64 v83, v[76:77] offset:1024
s_waitcnt lgkmcnt(0)
s_barrier
s_endpgm
CQB0_1:
s_cmp_eq_u32 s61, 0
s_cbranch_scc1 BQB0_1
s_sub_u32 s61, s61, 1
s_sleep 8
buffer_load_dwordx2 v[98:99], v90, s[36:39], 0 offen sc1
s_waitcnt vmcnt(0)
ds_write_b32 v82, v98
v_sub_u32 v98, v98, v91
v_xor_b32 v99, v99, v79
v_cmp_lt_u64 vcc, s[62:63], v[98:99]
s_waitcnt lgkmcnt(0)
s_cbranch_vccnz CQB0_1
s_branch BQB0_1
CQB0_2:
s_mov_b32 s58, m0
s_mov_b32 m0, 142336
DQB0_2:
s_cmp_eq_u32 s61, 0
s_cbranch_scc1 EQB0_2
s_sub_u32 s61, s61, 1
buffer_load_dword v90, s[36:39], 0 offen sc1 lds
s_sleep 1
ds_read_b32 v98, v82
s_waitcnt lgkmcnt(0)
v_cmp_gt_i32 vcc, v91, v98
s_cbranch_vccnz DQB0_2
EQB0_2:
s_mov_b32 m0, s58
s_branch BQB0_2
CQB0_3:
s_mov_b32 s58, m0
s_mov_b32 m0, 142336
DQB0_3:
s_cmp_eq_u32 s61, 0
s_cbranch_scc1 EQB0_3
s_sub_u32 s61, s61, 1
buffer_load_dword v90, s[36:39], 0 offen sc1 lds
s_sleep 1
ds_read_b32 v98, v82
s_waitcnt lgkmcnt(0)
v_cmp_gt_i32 vcc, v91, v98
s_cbranch_vccnz DQB0_3
EQB0_3:
s_mov_b32 m0, s58
s_branch BQB0_3
CQB0_4:
s_mov_b32 s58, m0
s_mov_b32 m0, 142336
DQB0_4:
s_cmp_eq_u32 s61, 0
s_cbranch_scc1 EQB0_4
s_sub_u32 s61, s61, 1
buffer_load_dword v90, s[36:39], 0 offen sc1 lds
s_sleep 1
ds_read_b32 v98, v82
s_waitcnt lgkmcnt(0)
v_cmp_gt_i32 vcc, v91, v98
s_cbranch_vccnz DQB0_4
EQB0_4:
s_mov_b32 m0, s58
s_branch BQB0_4
CQB0_5:
s_mov_b32 s58, m0
s_mov_b32 m0, 142336
DQB0_5:
s_cmp_eq_u32 s61, 0
s_cbranch_scc1 EQB0_5
s_sub_u32 s61, s61, 1
buffer_load_dword v90, s[36:39], 0 offen sc1 lds
s_sleep 1
ds_read_b32 v98, v82
s_waitcnt lgkmcnt(0)
v_cmp_gt_i32 vcc, v91, v98
s_cbranch_vccnz DQB0_5
EQB0_5:
s_mov_b32 m0, s58
s_branch BQB0_5
CQB0_7:
s_mov_b32 s58, m0
s_mov_b32 m0, 142336
DQB0_7:
s_cmp_eq_u32 s61, 0
s_cbranch_scc1 EQB0_7
s_sub_u32 s61, s61, 1
buffer_load_dword v90, s[36:39], 0 offen sc1 lds
s_sleep 1
ds_read_b32 v98, v82
s_waitcnt lgkmcnt(0)
v_cmp_gt_i32 vcc, v91, v98
s_cbranch_vccnz DQB0_7
EQB0_7:
s_mov_b32 m0, s58
s_branch BQB0_7
CQB0_8:
s_mov_b32 s58, m0
s_mov_b32 m0, 142336
DQB0_8:
s_cmp_eq_u32 s61, 0
s_cbranch_scc1 EQB0_8
s_sub_u32 s61, s61, 1
buffer_load_dword v90, s[36:39], 0 offen sc1 lds
s_sleep 1
ds_read_b32 v98, v82
s_waitcnt lgkmcnt(0)
v_cmp_gt_i32 vcc, v91, v98
s_cbranch_vccnz DQB0_8
EQB0_8:
s_mov_b32 m0, s58
s_branch BQB0_8
CQB0_9:
s_mov_b32 s58, m0
s_mov_b32 m0, 142336
DQB0_9:
s_cmp_eq_u32 s61, 0
s_cbranch_scc1 EQB0_9
s_sub_u32 s61, s61, 1
buffer_load_dword v90, s[36:39], 0 offen sc1 lds
s_sleep 1
ds_read_b32 v98, v82
s_waitcnt lgkmcnt(0)
v_cmp_gt_i32 vcc, v91, v98
s_cbranch_vccnz DQB0_9
EQB0_9:
s_mov_b32 m0, s58
s_branch BQB0_9
CQB0_10:
s_mov_b32 s58, m0
s_mov_b32 m0, 142336
DQB0_10:
s_cmp_eq_u32 s61, 0
s_cbranch_scc1 EQB0_10
s_sub_u32 s61, s61, 1
buffer_load_dword v90, s[36:39], 0 offen sc1 lds
s_sleep 1
ds_read_b32 v98, v82
s_waitcnt lgkmcnt(0)
v_cmp_gt_i32 vcc, v91, v98
s_cbranch_vccnz DQB0_10
EQB0_10:
s_mov_b32 m0, s58
s_branch BQB0_10
LvQB1:
s_add_u32 s49, s49, 346112
s_barrier
s_add_u32 m0, s50, 0
s_nop 0
D 86,0
D 86,1024
D 86,2048
D 86,3072
s_add_u32 m0, s50, 4096
s_nop 0
D 87,0
D 87,1024
D 87,2048
D 87,3072
s_add_u32 m0, s50, 8192
s_nop 0
D 88,0
D 88,1024
D 88,2048
D 88,3072
s_add_u32 m0, s50, 12288
s_nop 0
D 89,0
D 89,1024
D 89,2048
D 89,3072
s_add_u32 s49, s49, 346112
s_barrier
s_add_u32 m0, s50, 32768
s_nop 0
D 86,0
D 86,1024
D 86,2048
D 86,3072
s_add_u32 m0, s50, 36864
s_nop 0
D 87,0
D 87,1024
D 87,2048
D 87,3072
s_add_u32 m0, s50, 40960
s_nop 0
D 88,0
D 88,1024
D 88,2048
D 88,3072
s_add_u32 m0, s50, 45056
s_nop 0
D 89,0
D 89,1024
D 89,2048
D 89,3072
s_xor_b32 s50, s50, 0x10000
s_add_u32 s49, s49, 346112
s_waitcnt vmcnt(16)
s_barrier
R 24,0
buffer_load_dwordx2 v[100:101], v92, s[36:39], s49 offen sc1
R 28,1024
R 32,2048
R 36,3072
s_waitcnt lgkmcnt(2)
v_mfma_f32_16x16x32_bf16 v[0:3], a[0:3], v[24:27], 0
R 40,4096
v_mfma_f32_16x16x32_bf16 v[4:7], a[4:7], v[24:27], 0
v_mfma_f32_16x16x32_bf16 v[8:11], v[24:27], a[8:11], 0
mav 0,12,28
R 44,5120
mav 4,16,28
mva 8,28,20
s_waitcnt lgkmcnt(2)
mav 0,24,32
R 48,6144
mav 4,28,32
mva 8,32,32
mav 0,36,36
R 52,7168
mav 4,40,36
mva 8,36,44
s_waitcnt lgkmcnt(2)
mav 0,48,40
R 24,8192
mav 4,52,40
mva 8,40,56
mav 0,60,44
R 28,9216
mav 4,64,44
mva 8,44,68
s_waitcnt lgkmcnt(2)
mav 0,72,48
R 32,10240
mav 4,76,48
mva 8,48,80
mav 0,84,52
R 36,11264
mav 4,88,52
mva 8,52,92
s_waitcnt lgkmcnt(2)
mav 0,96,24
R 40,12288
s_add_u32 m0, s50, 0
mav 4,100,24
mva 8,24,104
D 86,0
mav 0,108,28
R 44,13312
mav 4,112,28
mva 8,28,116
D 86,1024
s_waitcnt lgkmcnt(2)
mav 0,120,32
R 48,14336
mav 4,124,32
mva 8,32,128
mav 0,132,36
R 52,15360
D 86,2048
mav 4,136,36
mva 8,36,140
s_waitcnt lgkmcnt(2)
mav 0,144,40
R 24,16384
D 86,3072
mav 4,148,40
mva 8,40,152
s_add_u32 m0, s50, 4096
mav 0,156,44
R 28,17408
mav 4,160,44
D 87,0
mva 8,44,164
s_waitcnt lgkmcnt(2)
mav 0,168,48
R 32,18432
mav 4,172,48
D 87,1024
mva 8,48,176
mav 0,180,52
R 36,19456
mav 4,184,52
mva 8,52,188
D 87,2048
s_waitcnt lgkmcnt(2)
mav 0,192,24
R 40,20480
mav 4,196,24
mva 8,24,200
D 87,3072
mav 0,204,28
R 44,21504
mav 4,208,28
s_add_u32 m0, s50, 8192
mva 8,28,212
s_waitcnt lgkmcnt(2)
mav 0,216,32
R 48,22528
D 88,0
mav 4,220,32
s_waitcnt vmcnt(9)
v_permlane32_swap_b32 v100, v101
mva 8,32,224
v_add_f32 v102, v100, v101
v_add_f32 v103, v100, v101
mav 0,228,36
R 52,23552
D 88,1024
mav 4,232,36
v_permlane16_swap_b32 v102, v103
v_add_f32 v100, v102, v103
mva 8,36,236
v_add_f32 v101, v102, v103
s_waitcnt lgkmcnt(2)
mav 0,240,40
R 24,24576
v_permlane32_swap_b32 v100, v101
mav 4,244,40
D 88,2048
v_mul_f32 v102, 0x3a800000, v100
mva 8,40,248
v_mul_f32 v103, 0x3a800000, v101
v_fma_f32 v103, -v102, v102, v103
mav 0,252,44
R 28,25600
v_max_f32 v103, 0, v103
mvv 4,128,44
D 88,3072
v_add_f32 v103, 0x3727c5ac, v103
mvv 8,44,132
s_waitcnt lgkmcnt(2)
mvv 0,136,48
R 32,26624
s_add_u32 m0, s50, 12288
mvv 4,140,48
v_rsq_f32 v100, v103
mvv 8,48,144
D 89,0
v_mul_f32_e64 v101, -v102, v100
mvv 0,148,52
R 36,27648
ds_write_b64 v84, v[100:101] offset:0
mvv 4,152,52
mvv 8,52,156
D 89,1024
s_waitcnt lgkmcnt(3)
mvv 0,160,24
R 40,28672
mvv 4,164,24
mvv 8,24,168
mvv 0,172,28
R 44,29696
D 89,2048
mvv 4,176,28
mvv 8,28,180
s_waitcnt lgkmcnt(2)
mvv 0,184,32
R 48,30720
D 89,3072
mvv 4,188,32
mvv 8,32,192
mvv 0,196,36
R 52,31744
mvv 4,200,36
s_add_u32 s49, s49, 346112
mvv 8,36,204
s_barrier
s_waitcnt lgkmcnt(2)
mvv 0,208,40
R 24,32768
buffer_load_dwordx2 v[100:101], v92, s[36:39], s49 offen sc1
mvv 4,212,40
ds_read_b64 v[94:95], v84 offset:0
ds_read_b128 v[64:67], v85 offset:0
mvv 8,40,216
ds_read_b128 v[68:71], v85 offset:16
mvv 0,220,44
R 28,33792
mvv 4,224,44
mvv 8,44,228
s_waitcnt lgkmcnt(5)
mvv 0,232,48
R 32,34816
mvv 4,236,48
mvv 8,48,240
mvv 0,244,52
R 36,35840
mvv 4,248,52
mvv 8,52,252
s_waitcnt lgkmcnt(2)
v_mfma_f32_16x16x32_bf16 v[12:15], a[0:3], v[24:27], 0
R 40,36864
v_mul_f32 v96, v95, v56
v_mfma_f32_16x16x32_bf16 v[16:19], a[4:7], v[24:27], 0
v_fma_f32 v0, v0, v94, v96
v_mul_f32 v97, v95, v57
v_mfma_f32_16x16x32_bf16 v[20:23], v[24:27], a[8:11], 0
v_fma_f32 v1, v1, v94, v97
v_mul_f32 v96, v95, v58
mav 12,12,28
R 44,37888
v_fma_f32 v2, v2, v94, v96
mav 16,16,28
v_mul_f32 v97, v95, v59
v_fma_f32 v3, v3, v94, v97
mva 20,28,20
v_mul_f32 v96, v95, v60
s_waitcnt lgkmcnt(2)
mav 12,24,32
R 48,38912
v_fma_f32 v4, v4, v94, v96
mav 16,28,32
v_mul_f32 v97, v95, v61
v_fma_f32 v5, v5, v94, v97
mva 20,32,32
v_mul_f32 v96, v95, v62
v_fma_f32 v6, v6, v94, v96
mav 12,36,36
R 52,39936
v_mul_f32 v97, v95, v63
mav 16,40,36
v_fma_f32 v7, v7, v94, v97
v_cvt_pk_bf16_f32 v72, v0, v1
mva 20,36,44
v_cvt_pk_bf16_f32 v73, v2, v3
s_waitcnt lgkmcnt(2)
mav 12,48,40
R 24,40960
v_cvt_pk_bf16_f32 v74, v4, v5
mav 16,52,40
v_cvt_pk_bf16_f32 v75, v6, v7
ds_write_b128 v83, v[72:75] offset:0
mva 20,40,56
v_mul_f32 v65, v65, v93
v_fma_f32 v8, v8, v64, v65
mav 12,60,44
R 28,41984
v_mul_f32 v67, v67, v93
mav 16,64,44
v_fma_f32 v9, v9, v66, v67
v_mul_f32 v69, v69, v93
mva 20,44,68
v_fma_f32 v10, v10, v68, v69
s_waitcnt lgkmcnt(3)
mav 12,72,48
R 32,43008
v_mul_f32 v71, v71, v93
mav 16,76,48
v_fma_f32 v11, v11, v70, v71
v_cvt_pk_bf16_f32 v76, v8, v9
mva 20,48,80
v_cvt_pk_bf16_f32 v77, v10, v11
ds_write_b64 v83, v[76:77] offset:1024
mav 12,84,52
R 36,44032
mav 16,88,52
mva 20,52,92
s_waitcnt lgkmcnt(3)
mav 12,96,24
R 40,45056
s_add_u32 m0, s50, 32768
mav 16,100,24
s_waitcnt lgkmcnt(2)
s_barrier
mva 20,24,104
D 86,0
mav 12,108,28
R 44,46080
mav 16,112,28
mva 20,28,116
D 86,1024
mav 12,120,32
R 48,47104
mav 16,124,32
mva 20,32,128
s_waitcnt lgkmcnt(2)
mav 12,132,36
R 52,48128
D 86,2048
mav 16,136,36
mva 20,36,140
mav 12,144,40
R 24,49152
D 86,3072
mav 16,148,40
mva 20,40,152
s_add_u32 m0, s50, 36864
s_waitcnt lgkmcnt(2)
mav 12,156,44
R 28,50176
mav 16,160,44
D 87,0
mva 20,44,164
mav 12,168,48
R 32,51200
mav 16,172,48
D 87,1024
mva 20,48,176
s_waitcnt lgkmcnt(2)
mav 12,180,52
R 36,52224
mav 16,184,52
mva 20,52,188
D 87,2048
mav 12,192,24
R 40,53248
mav 16,196,24
mva 20,24,200
D 87,3072
s_waitcnt lgkmcnt(2)
mav 12,204,28
R 44,54272
mav 16,208,28
s_add_u32 m0, s50, 40960
mva 20,28,212
s_waitcnt vmcnt(8)
v_permlane32_swap_b32 v100, v101
mav 12,216,32
R 48,55296
D 88,0
mav 16,220,32
v_add_f32 v102, v100, v101
v_add_f32 v103, v100, v101
mva 20,32,224
s_waitcnt lgkmcnt(2)
mav 12,228,36
R 52,56320
D 88,1024
mav 16,232,36
v_permlane16_swap_b32 v102, v103
v_add_f32 v100, v102, v103
mva 20,36,236
v_add_f32 v101, v102, v103
mav 12,240,40
R 24,57344
v_permlane32_swap_b32 v100, v101
mav 16,244,40
D 88,2048
v_mul_f32 v102, 0x3a800000, v100
mva 20,40,248
v_mul_f32 v103, 0x3a800000, v101
s_waitcnt lgkmcnt(2)
mav 12,252,44
R 28,58368
v_fma_f32 v103, -v102, v102, v103
mvv 16,128,44
D 88,3072
v_max_f32 v103, 0, v103
mvv 20,44,132
v_add_f32 v103, 0x3727c5ac, v103
mvv 12,136,48
R 32,59392
s_add_u32 m0, s50, 45056
mvv 16,140,48
v_rsq_f32 v100, v103
mvv 20,48,144
D 89,0
s_waitcnt lgkmcnt(2)
mvv 12,148,52
R 36,60416
v_mul_f32_e64 v101, -v102, v100
mvv 16,152,52
ds_write_b64 v84, v[100:101] offset:128
mvv 20,52,156
D 89,1024
mvv 12,160,24
R 40,61440
mvv 16,164,24
mvv 20,24,168
s_waitcnt lgkmcnt(3)
mvv 12,172,28
R 44,62464
D 89,2048
mvv 16,176,28
mvv 20,28,180
mvv 12,184,32
R 48,63488
D 89,3072
mvv 16,188,32
s_xor_b32 s50, s50, 0x10000
mvv 20,32,192
s_waitcnt lgkmcnt(2)
mvv 12,196,36
R 52,64512
v_xor_b32 v80, 0x10000, v80
mvv 16,200,36
s_add_u32 s49, s49, 346112
mvv 20,36,204
s_barrier
mvv 12,208,40
R 24,0
buffer_load_dwordx2 v[100:101], v92, s[36:39], s49 offen sc1
mvv 16,212,40
ds_read_b64 v[94:95], v84 offset:128
ds_read_b128 v[64:67], v85 offset:128
mvv 20,40,216
ds_read_b128 v[68:71], v85 offset:144
s_waitcnt lgkmcnt(5)
mvv 12,220,44
R 28,1024
mvv 16,224,44
mvv 20,44,228
mvv 12,232,48
R 32,2048
mvv 16,236,48
mvv 20,48,240
s_waitcnt lgkmcnt(2)
mvv 12,244,52
R 36,3072
mvv 16,248,52
mvv 20,52,252
s_mov_b32 s60, -29
.p2align 6
LQB1_1:
v_mfma_f32_16x16x32_bf16 v[0:3], a[0:3], v[24:27], 0
R 40,4096
v_mul_f32 v96, v95, v56
v_mfma_f32_16x16x32_bf16 v[4:7], a[4:7], v[24:27], 0
v_mul_f32 v97, v95, v57
v_fma_f32 v12, v12, v94, v96
v_mfma_f32_16x16x32_bf16 v[8:11], v[24:27], a[8:11], 0
v_fma_f32 v13, v13, v94, v97
s_waitcnt lgkmcnt(2)
mav 0,12,28
R 44,5120
v_mul_f32 v96, v95, v58
mav 4,16,28
v_fma_f32 v14, v14, v94, v96
v_mul_f32 v97, v95, v59
mva 8,28,20
v_fma_f32 v15, v15, v94, v97
v_mul_f32 v96, v95, v60
mav 0,24,32
R 48,6144
v_fma_f32 v16, v16, v94, v96
mav 4,28,32
v_mul_f32 v97, v95, v61
v_fma_f32 v17, v17, v94, v97
mva 8,32,32
v_mul_f32 v96, v95, v62
s_waitcnt lgkmcnt(2)
mav 0,36,36
R 52,7168
v_fma_f32 v18, v18, v94, v96
mav 4,40,36
v_mul_f32 v97, v95, v63
v_fma_f32 v19, v19, v94, v97
mva 8,36,44
v_cvt_pk_bf16_f32 v72, v12, v13
v_cvt_pk_bf16_f32 v73, v14, v15
mav 0,48,40
R 24,8192
v_cvt_pk_bf16_f32 v74, v16, v17
mav 4,52,40
v_cvt_pk_bf16_f32 v75, v18, v19
ds_write_b128 v83, v[72:75] offset:0
mva 8,40,56
v_mul_f32 v65, v65, v93
s_waitcnt lgkmcnt(3)
mav 0,60,44
R 28,9216
v_fma_f32 v20, v20, v64, v65
mav 4,64,44
v_mul_f32 v67, v67, v93
v_fma_f32 v21, v21, v66, v67
mva 8,44,68
v_mul_f32 v69, v69, v93
v_fma_f32 v22, v22, v68, v69
mav 0,72,48
R 32,10240
v_mul_f32 v71, v71, v93
mav 4,76,48
v_fma_f32 v23, v23, v70, v71
v_cvt_pk_bf16_f32 v76, v20, v21
mva 8,48,80
v_cvt_pk_bf16_f32 v77, v22, v23
s_waitcnt lgkmcnt(2)
mav 0,84,52
R 36,11264
ds_write_b64 v83, v[76:77] offset:1024
mav 4,88,52
mva 8,52,92
mav 0,96,24
R 40,12288
s_add_u32 m0, s50, 0
mav 4,100,24
s_waitcnt lgkmcnt(1)
s_barrier
mva 8,24,104
D 86,0
mav 0,108,28
R 44,13312
mav 4,112,28
mva 8,28,116
D 86,1024
mav 0,120,32
R 48,14336
mav 4,124,32
mva 8,32,128
mav 0,132,36
R 52,15360
D 86,2048
mav 4,136,36
mva 8,36,140
s_waitcnt lgkmcnt(2)
mav 0,144,40
R 24,16384
D 86,3072
mav 4,148,40
mva 8,40,152
s_add_u32 m0, s50, 4096
mav 0,156,44
R 28,17408
mav 4,160,44
D 87,0
mva 8,44,164
s_waitcnt lgkmcnt(2)
mav 0,168,48
R 32,18432
mav 4,172,48
D 87,1024
mva 8,48,176
mav 0,180,52
R 36,19456
mav 4,184,52
mva 8,52,188
D 87,2048
s_waitcnt lgkmcnt(2)
mav 0,192,24
R 40,20480
mav 4,196,24
mva 8,24,200
D 87,3072
mav 0,204,28
R 44,21504
mav 4,208,28
s_add_u32 m0, s50, 8192
mva 8,28,212
s_waitcnt lgkmcnt(2)
mav 0,216,32
R 48,22528
D 88,0
mav 4,220,32
s_waitcnt vmcnt(9)
v_permlane32_swap_b32 v100, v101
mva 8,32,224
v_add_f32 v102, v100, v101
v_add_f32 v103, v100, v101
mav 0,228,36
R 52,23552
D 88,1024
mav 4,232,36
v_permlane16_swap_b32 v102, v103
v_add_f32 v100, v102, v103
mva 8,36,236
v_add_f32 v101, v102, v103
s_waitcnt lgkmcnt(2)
mav 0,240,40
R 24,24576
v_permlane32_swap_b32 v100, v101
mav 4,244,40
D 88,2048
v_mul_f32 v102, 0x3a800000, v100
mva 8,40,248
v_mul_f32 v103, 0x3a800000, v101
v_fma_f32 v103, -v102, v102, v103
mav 0,252,44
R 28,25600
v_max_f32 v103, 0, v103
mvv 4,128,44
D 88,3072
v_add_f32 v103, 0x3727c5ac, v103
mvv 8,44,132
s_waitcnt lgkmcnt(2)
mvv 0,136,48
R 32,26624
s_add_u32 m0, s50, 12288
mvv 4,140,48
v_rsq_f32 v100, v103
mvv 8,48,144
D 89,0
v_mul_f32_e64 v101, -v102, v100
mvv 0,148,52
R 36,27648
ds_write_b64 v84, v[100:101] offset:0
mvv 4,152,52
mvv 8,52,156
D 89,1024
s_waitcnt lgkmcnt(3)
mvv 0,160,24
R 40,28672
mvv 4,164,24
mvv 8,24,168
mvv 0,172,28
R 44,29696
D 89,2048
mvv 4,176,28
mvv 8,28,180
s_waitcnt lgkmcnt(2)
mvv 0,184,32
R 48,30720
D 89,3072
mvv 4,188,32
mvv 8,32,192
mvv 0,196,36
R 52,31744
mvv 4,200,36
s_add_u32 s49, s49, 346112
mvv 8,36,204
s_barrier
s_waitcnt lgkmcnt(2)
mvv 0,208,40
R 24,32768
buffer_load_dwordx2 v[100:101], v92, s[36:39], s49 offen sc1
mvv 4,212,40
ds_read_b64 v[94:95], v84 offset:0
ds_read_b128 v[64:67], v85 offset:0
mvv 8,40,216
ds_read_b128 v[68:71], v85 offset:16
mvv 0,220,44
R 28,33792
mvv 4,224,44
mvv 8,44,228
s_waitcnt lgkmcnt(5)
mvv 0,232,48
R 32,34816
mvv 4,236,48
mvv 8,48,240
mvv 0,244,52
R 36,35840
mvv 4,248,52
mvv 8,52,252
s_waitcnt lgkmcnt(2)
v_mfma_f32_16x16x32_bf16 v[12:15], a[0:3], v[24:27], 0
R 40,36864
v_mul_f32 v96, v95, v56
v_mfma_f32_16x16x32_bf16 v[16:19], a[4:7], v[24:27], 0
v_fma_f32 v0, v0, v94, v96
v_mul_f32 v97, v95, v57
v_mfma_f32_16x16x32_bf16 v[20:23], v[24:27], a[8:11], 0
v_fma_f32 v1, v1, v94, v97
v_mul_f32 v96, v95, v58
mav 12,12,28
R 44,37888
v_fma_f32 v2, v2, v94, v96
mav 16,16,28
v_mul_f32 v97, v95, v59
v_fma_f32 v3, v3, v94, v97
mva 20,28,20
v_mul_f32 v96, v95, v60
s_waitcnt lgkmcnt(2)
mav 12,24,32
R 48,38912
v_fma_f32 v4, v4, v94, v96
mav 16,28,32
v_mul_f32 v97, v95, v61
v_fma_f32 v5, v5, v94, v97
mva 20,32,32
v_mul_f32 v96, v95, v62
v_fma_f32 v6, v6, v94, v96
mav 12,36,36
R 52,39936
v_mul_f32 v97, v95, v63
mav 16,40,36
v_fma_f32 v7, v7, v94, v97
v_cvt_pk_bf16_f32 v72, v0, v1
mva 20,36,44
v_cvt_pk_bf16_f32 v73, v2, v3
s_waitcnt lgkmcnt(2)
mav 12,48,40
R 24,40960
v_cvt_pk_bf16_f32 v74, v4, v5
mav 16,52,40
v_cvt_pk_bf16_f32 v75, v6, v7
ds_write_b128 v83, v[72:75] offset:0
mva 20,40,56
v_mul_f32 v65, v65, v93
v_fma_f32 v8, v8, v64, v65
mav 12,60,44
R 28,41984
v_mul_f32 v67, v67, v93
mav 16,64,44
v_fma_f32 v9, v9, v66, v67
v_mul_f32 v69, v69, v93
mva 20,44,68
v_fma_f32 v10, v10, v68, v69
s_waitcnt lgkmcnt(3)
mav 12,72,48
R 32,43008
v_mul_f32 v71, v71, v93
mav 16,76,48
v_fma_f32 v11, v11, v70, v71
v_cvt_pk_bf16_f32 v76, v8, v9
mva 20,48,80
v_cvt_pk_bf16_f32 v77, v10, v11
ds_write_b64 v83, v[76:77] offset:1024
mav 12,84,52
R 36,44032
mav 16,88,52
mva 20,52,92
s_waitcnt lgkmcnt(3)
mav 12,96,24
R 40,45056
s_add_u32 m0, s50, 32768
mav 16,100,24
s_waitcnt lgkmcnt(2)
s_barrier
mva 20,24,104
D 86,0
mav 12,108,28
R 44,46080
mav 16,112,28
mva 20,28,116
D 86,1024
mav 12,120,32
R 48,47104
mav 16,124,32
mva 20,32,128
s_waitcnt lgkmcnt(2)
mav 12,132,36
R 52,48128
D 86,2048
mav 16,136,36
mva 20,36,140
mav 12,144,40
R 24,49152
D 86,3072
mav 16,148,40
mva 20,40,152
s_add_u32 m0, s50, 36864
s_waitcnt lgkmcnt(2)
mav 12,156,44
R 28,50176
mav 16,160,44
D 87,0
mva 20,44,164
mav 12,168,48
R 32,51200
mav 16,172,48
D 87,1024
mva 20,48,176
s_waitcnt lgkmcnt(2)
mav 12,180,52
R 36,52224
mav 16,184,52
mva 20,52,188
D 87,2048
mav 12,192,24
R 40,53248
mav 16,196,24
mva 20,24,200
D 87,3072
s_waitcnt lgkmcnt(2)
mav 12,204,28
R 44,54272
mav 16,208,28
s_add_u32 m0, s50, 40960
mva 20,28,212
s_waitcnt vmcnt(8)
v_permlane32_swap_b32 v100, v101
mav 12,216,32
R 48,55296
D 88,0
mav 16,220,32
v_add_f32 v102, v100, v101
v_add_f32 v103, v100, v101
mva 20,32,224
s_waitcnt lgkmcnt(2)
mav 12,228,36
R 52,56320
D 88,1024
mav 16,232,36
v_permlane16_swap_b32 v102, v103
v_add_f32 v100, v102, v103
mva 20,36,236
v_add_f32 v101, v102, v103
mav 12,240,40
R 24,57344
v_permlane32_swap_b32 v100, v101
mav 16,244,40
D 88,2048
v_mul_f32 v102, 0x3a800000, v100
mva 20,40,248
v_mul_f32 v103, 0x3a800000, v101
s_waitcnt lgkmcnt(2)
mav 12,252,44
R 28,58368
v_fma_f32 v103, -v102, v102, v103
mvv 16,128,44
D 88,3072
v_max_f32 v103, 0, v103
mvv 20,44,132
v_add_f32 v103, 0x3727c5ac, v103
mvv 12,136,48
R 32,59392
s_add_u32 m0, s50, 45056
mvv 16,140,48
v_rsq_f32 v100, v103
mvv 20,48,144
D 89,0
s_waitcnt lgkmcnt(2)
mvv 12,148,52
R 36,60416
v_mul_f32_e64 v101, -v102, v100
mvv 16,152,52
ds_write_b64 v84, v[100:101] offset:128
mvv 20,52,156
D 89,1024
mvv 12,160,24
R 40,61440
mvv 16,164,24
mvv 20,24,168
s_waitcnt lgkmcnt(3)
mvv 12,172,28
R 44,62464
D 89,2048
mvv 16,176,28
mvv 20,28,180
mvv 12,184,32
R 48,63488
D 89,3072
mvv 16,188,32
s_xor_b32 s50, s50, 0x10000
mvv 20,32,192
s_waitcnt lgkmcnt(2)
mvv 12,196,36
R 52,64512
v_xor_b32 v80, 0x10000, v80
mvv 16,200,36
s_add_u32 s49, s49, 346112
mvv 20,36,204
s_barrier
mvv 12,208,40
R 24,0
buffer_load_dwordx2 v[100:101], v92, s[36:39], s49 offen sc1
mvv 16,212,40
ds_read_b64 v[94:95], v84 offset:128
ds_read_b128 v[64:67], v85 offset:128
mvv 20,40,216
ds_read_b128 v[68:71], v85 offset:144
s_waitcnt lgkmcnt(5)
mvv 12,220,44
R 28,1024
mvv 16,224,44
mvv 20,44,228
mvv 12,232,48
R 32,2048
mvv 16,236,48
mvv 20,48,240
s_waitcnt lgkmcnt(2)
mvv 12,244,52
R 36,3072
mvv 16,248,52
mvv 20,52,252
s_add_u32 s60, s60, 1
s_cbranch_scc0 LQB1_1
v_mfma_f32_16x16x32_bf16 v[0:3], a[0:3], v[24:27], 0
R 40,4096
v_mul_f32 v96, v95, v56
v_mfma_f32_16x16x32_bf16 v[4:7], a[4:7], v[24:27], 0
v_mul_f32 v97, v95, v57
v_fma_f32 v12, v12, v94, v96
v_mfma_f32_16x16x32_bf16 v[8:11], v[24:27], a[8:11], 0
v_fma_f32 v13, v13, v94, v97
s_waitcnt lgkmcnt(2)
mav 0,12,28
R 44,5120
v_mul_f32 v96, v95, v58
mav 4,16,28
v_fma_f32 v14, v14, v94, v96
v_mul_f32 v97, v95, v59
mva 8,28,20
v_fma_f32 v15, v15, v94, v97
v_mul_f32 v96, v95, v60
mav 0,24,32
R 48,6144
v_fma_f32 v16, v16, v94, v96
mav 4,28,32
v_mul_f32 v97, v95, v61
v_fma_f32 v17, v17, v94, v97
mva 8,32,32
v_mul_f32 v96, v95, v62
s_waitcnt lgkmcnt(2)
mav 0,36,36
R 52,7168
v_fma_f32 v18, v18, v94, v96
mav 4,40,36
v_mul_f32 v97, v95, v63
v_fma_f32 v19, v19, v94, v97
mva 8,36,44
v_cvt_pk_bf16_f32 v72, v12, v13
v_cvt_pk_bf16_f32 v73, v14, v15
mav 0,48,40
R 24,8192
v_cvt_pk_bf16_f32 v74, v16, v17
mav 4,52,40
v_cvt_pk_bf16_f32 v75, v18, v19
ds_write_b128 v83, v[72:75] offset:0
mva 8,40,56
v_mul_f32 v65, v65, v93
s_waitcnt lgkmcnt(3)
mav 0,60,44
R 28,9216
v_fma_f32 v20, v20, v64, v65
mav 4,64,44
v_mul_f32 v67, v67, v93
v_fma_f32 v21, v21, v66, v67
mva 8,44,68
v_mul_f32 v69, v69, v93
v_fma_f32 v22, v22, v68, v69
mav 0,72,48
R 32,10240
v_mul_f32 v71, v71, v93
mav 4,76,48
v_fma_f32 v23, v23, v70, v71
v_cvt_pk_bf16_f32 v76, v20, v21
mva 8,48,80
v_cvt_pk_bf16_f32 v77, v22, v23
s_waitcnt lgkmcnt(2)
mav 0,84,52
R 36,11264
ds_write_b64 v83, v[76:77] offset:1024
mav 4,88,52
mva 8,52,92
mav 0,96,24
R 40,12288
s_add_u32 m0, s50, 0
mav 4,100,24
s_waitcnt lgkmcnt(1)
s_barrier
mva 8,24,104
D 86,0
mav 0,108,28
R 44,13312
mav 4,112,28
mva 8,28,116
D 86,1024
mav 0,120,32
R 48,14336
mav 4,124,32
mva 8,32,128
mav 0,132,36
R 52,15360
D 86,2048
mav 4,136,36
mva 8,36,140
s_waitcnt lgkmcnt(2)
mav 0,144,40
R 24,16384
D 86,3072
mav 4,148,40
mva 8,40,152
s_add_u32 m0, s50, 4096
mav 0,156,44
R 28,17408
mav 4,160,44
D 87,0
mva 8,44,164
s_waitcnt lgkmcnt(2)
mav 0,168,48
R 32,18432
mav 4,172,48
D 87,1024
mva 8,48,176
mav 0,180,52
R 36,19456
mav 4,184,52
mva 8,52,188
D 87,2048
s_waitcnt lgkmcnt(2)
mav 0,192,24
R 40,20480
mav 4,196,24
mva 8,24,200
D 87,3072
mav 0,204,28
R 44,21504
mav 4,208,28
s_add_u32 m0, s50, 8192
mva 8,28,212
s_waitcnt lgkmcnt(2)
mav 0,216,32
R 48,22528
D 88,0
mav 4,220,32
s_waitcnt vmcnt(9)
v_permlane32_swap_b32 v100, v101
mva 8,32,224
v_add_f32 v102, v100, v101
v_add_f32 v103, v100, v101
mav 0,228,36
R 52,23552
D 88,1024
mav 4,232,36
v_permlane16_swap_b32 v102, v103
v_add_f32 v100, v102, v103
mva 8,36,236
v_add_f32 v101, v102, v103
s_waitcnt lgkmcnt(2)
mav 0,240,40
R 24,24576
v_permlane32_swap_b32 v100, v101
mav 4,244,40
D 88,2048
v_mul_f32 v102, 0x3a800000, v100
mva 8,40,248
v_mul_f32 v103, 0x3a800000, v101
v_fma_f32 v103, -v102, v102, v103
mav 0,252,44
R 28,25600
v_max_f32 v103, 0, v103
mvv 4,128,44
D 88,3072
v_add_f32 v103, 0x3727c5ac, v103
mvv 8,44,132
s_waitcnt lgkmcnt(2)
mvv 0,136,48
R 32,26624
s_add_u32 m0, s50, 12288
mvv 4,140,48
v_rsq_f32 v100, v103
mvv 8,48,144
D 89,0
v_mul_f32_e64 v101, -v102, v100
mvv 0,148,52
R 36,27648
ds_write_b64 v84, v[100:101] offset:0
mvv 4,152,52
mvv 8,52,156
D 89,1024
s_waitcnt lgkmcnt(3)
mvv 0,160,24
R 40,28672
mvv 4,164,24
mvv 8,24,168
mvv 0,172,28
R 44,29696
D 89,2048
mvv 4,176,28
mvv 8,28,180
s_waitcnt lgkmcnt(2)
mvv 0,184,32
R 48,30720
D 89,3072
mvv 4,188,32
mvv 8,32,192
mvv 0,196,36
R 52,31744
mvv 4,200,36
s_add_u32 s49, s49, 346112
mvv 8,36,204
s_barrier
s_waitcnt lgkmcnt(2)
mvv 0,208,40
R 24,32768
buffer_load_dwordx2 v[100:101], v92, s[36:39], s49 offen sc1
mvv 4,212,40
ds_read_b64 v[94:95], v84 offset:0
ds_read_b128 v[64:67], v85 offset:0
mvv 8,40,216
ds_read_b128 v[68:71], v85 offset:16
mvv 0,220,44
R 28,33792
mvv 4,224,44
mvv 8,44,228
s_waitcnt lgkmcnt(5)
mvv 0,232,48
R 32,34816
mvv 4,236,48
mvv 8,48,240
mvv 0,244,52
R 36,35840
mvv 4,248,52
mvv 8,52,252
s_waitcnt lgkmcnt(2)
v_mfma_f32_16x16x32_bf16 v[12:15], a[0:3], v[24:27], 0
R 40,36864
v_mul_f32 v96, v95, v56
v_mfma_f32_16x16x32_bf16 v[16:19], a[4:7], v[24:27], 0
v_fma_f32 v0, v0, v94, v96
v_mul_f32 v97, v95, v57
v_mfma_f32_16x16x32_bf16 v[20:23], v[24:27], a[8:11], 0
v_fma_f32 v1, v1, v94, v97
v_mul_f32 v96, v95, v58
mav 12,12,28
R 44,37888
v_fma_f32 v2, v2, v94, v96
mav 16,16,28
v_mul_f32 v97, v95, v59
v_fma_f32 v3, v3, v94, v97
mva 20,28,20
v_mul_f32 v96, v95, v60
s_waitcnt lgkmcnt(2)
mav 12,24,32
R 48,38912
v_fma_f32 v4, v4, v94, v96
mav 16,28,32
v_mul_f32 v97, v95, v61
v_fma_f32 v5, v5, v94, v97
mva 20,32,32
v_mul_f32 v96, v95, v62
v_fma_f32 v6, v6, v94, v96
mav 12,36,36
R 52,39936
v_mul_f32 v97, v95, v63
mav 16,40,36
v_fma_f32 v7, v7, v94, v97
v_cvt_pk_bf16_f32 v72, v0, v1
mva 20,36,44
v_cvt_pk_bf16_f32 v73, v2, v3
s_waitcnt lgkmcnt(2)
mav 12,48,40
R 24,40960
v_cvt_pk_bf16_f32 v74, v4, v5
mav 16,52,40
v_cvt_pk_bf16_f32 v75, v6, v7
ds_write_b128 v83, v[72:75] offset:0
mva 20,40,56
v_mul_f32 v65, v65, v93
v_fma_f32 v8, v8, v64, v65
mav 12,60,44
R 28,41984
v_mul_f32 v67, v67, v93
mav 16,64,44
v_fma_f32 v9, v9, v66, v67
v_mul_f32 v69, v69, v93
mva 20,44,68
v_fma_f32 v10, v10, v68, v69
s_waitcnt lgkmcnt(3)
mav 12,72,48
R 32,43008
v_mul_f32 v71, v71, v93
mav 16,76,48
v_fma_f32 v11, v11, v70, v71
v_cvt_pk_bf16_f32 v76, v8, v9
mva 20,48,80
v_cvt_pk_bf16_f32 v77, v10, v11
ds_write_b64 v83, v[76:77] offset:1024
mav 12,84,52
R 36,44032
mav 16,88,52
mva 20,52,92
s_waitcnt lgkmcnt(3)
mav 12,96,24
R 40,45056
s_add_u32 m0, s50, 32768
mav 16,100,24
s_waitcnt lgkmcnt(2)
s_barrier
mva 20,24,104
D 86,0
mav 12,108,28
R 44,46080
mav 16,112,28
mva 20,28,116
D 86,1024
mav 12,120,32
R 48,47104
mav 16,124,32
mva 20,32,128
s_waitcnt lgkmcnt(2)
mav 12,132,36
R 52,48128
D 86,2048
mav 16,136,36
mva 20,36,140
mav 12,144,40
R 24,49152
D 86,3072
mav 16,148,40
mva 20,40,152
s_add_u32 m0, s50, 36864
s_waitcnt lgkmcnt(2)
mav 12,156,44
R 28,50176
mav 16,160,44
D 87,0
mva 20,44,164
mav 12,168,48
R 32,51200
mav 16,172,48
D 87,1024
mva 20,48,176
s_waitcnt lgkmcnt(2)
mav 12,180,52
R 36,52224
mav 16,184,52
mva 20,52,188
D 87,2048
mav 12,192,24
R 40,53248
mav 16,196,24
mva 20,24,200
D 87,3072
s_waitcnt lgkmcnt(2)
mav 12,204,28
R 44,54272
mav 16,208,28
s_add_u32 m0, s50, 40960
mva 20,28,212
s_waitcnt vmcnt(8)
v_permlane32_swap_b32 v100, v101
mav 12,216,32
R 48,55296
D 88,0
mav 16,220,32
v_add_f32 v102, v100, v101
v_add_f32 v103, v100, v101
mva 20,32,224
s_waitcnt lgkmcnt(2)
mav 12,228,36
R 52,56320
D 88,1024
mav 16,232,36
v_permlane16_swap_b32 v102, v103
v_add_f32 v100, v102, v103
mva 20,36,236
v_add_f32 v101, v102, v103
mav 12,240,40
R 24,57344
v_permlane32_swap_b32 v100, v101
mav 16,244,40
D 88,2048
v_mul_f32 v102, 0x3a800000, v100
mva 20,40,248
v_mul_f32 v103, 0x3a800000, v101
s_waitcnt lgkmcnt(2)
mav 12,252,44
R 28,58368
v_fma_f32 v103, -v102, v102, v103
mvv 16,128,44
D 88,3072
v_max_f32 v103, 0, v103
mvv 20,44,132
v_add_f32 v103, 0x3727c5ac, v103
mvv 12,136,48
R 32,59392
s_add_u32 m0, s50, 45056
mvv 16,140,48
v_rsq_f32 v100, v103
mvv 20,48,144
D 89,0
s_waitcnt lgkmcnt(2)
mvv 12,148,52
R 36,60416
v_mul_f32_e64 v101, -v102, v100
mvv 16,152,52
ds_write_b64 v84, v[100:101] offset:128
mvv 20,52,156
D 89,1024
mvv 12,160,24
R 40,61440
mvv 16,164,24
mvv 20,24,168
s_waitcnt lgkmcnt(3)
mvv 12,172,28
R 44,62464
D 89,2048
mvv 16,176,28
mvv 20,28,180
mvv 12,184,32
R 48,63488
D 89,3072
mvv 16,188,32
mvv 20,32,192
s_waitcnt lgkmcnt(2)
mvv 12,196,36
R 52,64512
v_xor_b32 v80, 0x10000, v80
mvv 16,200,36
s_add_u32 s49, s49, 346112
mvv 20,36,204
s_barrier
mvv 12,208,40
R 24,0
buffer_load_dwordx2 v[100:101], v92, s[36:39], s49 offen sc1
mvv 16,212,40
ds_read_b64 v[94:95], v84 offset:128
ds_read_b128 v[64:67], v85 offset:128
mvv 20,40,216
ds_read_b128 v[68:71], v85 offset:144
s_waitcnt lgkmcnt(5)
mvv 12,220,44
R 28,1024
mvv 16,224,44
mvv 20,44,228
mvv 12,232,48
R 32,2048
mvv 16,236,48
mvv 20,48,240
s_waitcnt lgkmcnt(2)
mvv 12,244,52
R 36,3072
mvv 16,248,52
mvv 20,52,252
v_mfma_f32_16x16x32_bf16 v[0:3], a[0:3], v[24:27], 0
R 40,4096
v_mul_f32 v96, v95, v56
v_mfma_f32_16x16x32_bf16 v[4:7], a[4:7], v[24:27], 0
v_mul_f32 v97, v95, v57
v_fma_f32 v12, v12, v94, v96
v_mfma_f32_16x16x32_bf16 v[8:11], v[24:27], a[8:11], 0
v_fma_f32 v13, v13, v94, v97
s_waitcnt lgkmcnt(2)
mav 0,12,28
R 44,5120
v_mul_f32 v96, v95, v58
mav 4,16,28
v_fma_f32 v14, v14, v94, v96
v_mul_f32 v97, v95, v59
mva 8,28,20
v_fma_f32 v15, v15, v94, v97
v_mul_f32 v96, v95, v60
mav 0,24,32
R 48,6144
v_fma_f32 v16, v16, v94, v96
mav 4,28,32
v_mul_f32 v97, v95, v61
v_fma_f32 v17, v17, v94, v97
mva 8,32,32
v_mul_f32 v96, v95, v62
s_waitcnt lgkmcnt(2)
mav 0,36,36
R 52,7168
v_fma_f32 v18, v18, v94, v96
mav 4,40,36
v_mul_f32 v97, v95, v63
v_fma_f32 v19, v19, v94, v97
mva 8,36,44
v_cvt_pk_bf16_f32 v72, v12, v13
v_cvt_pk_bf16_f32 v73, v14, v15
mav 0,48,40
R 24,8192
v_cvt_pk_bf16_f32 v74, v16, v17
mav 4,52,40
v_cvt_pk_bf16_f32 v75, v18, v19
ds_write_b128 v83, v[72:75] offset:0
mva 8,40,56
v_mul_f32 v65, v65, v93
s_waitcnt lgkmcnt(3)
mav 0,60,44
R 28,9216
v_fma_f32 v20, v20, v64, v65
mav 4,64,44
v_mul_f32 v67, v67, v93
v_fma_f32 v21, v21, v66, v67
mva 8,44,68
v_mul_f32 v69, v69, v93
v_fma_f32 v22, v22, v68, v69
mav 0,72,48
R 32,10240
v_mul_f32 v71, v71, v93
mav 4,76,48
v_fma_f32 v23, v23, v70, v71
v_cvt_pk_bf16_f32 v76, v20, v21
mva 8,48,80
v_cvt_pk_bf16_f32 v77, v22, v23
s_waitcnt lgkmcnt(2)
mav 0,84,52
R 36,11264
ds_write_b64 v83, v[76:77] offset:1024
mav 4,88,52
mva 8,52,92
mav 0,96,24
R 40,12288
mav 4,100,24
s_waitcnt lgkmcnt(1)
s_barrier
mva 8,24,104
mav 0,108,28
R 44,13312
mav 4,112,28
mva 8,28,116
mav 0,120,32
R 48,14336
mav 4,124,32
mva 8,32,128
mav 0,132,36
R 52,15360
mav 4,136,36
mva 8,36,140
s_waitcnt lgkmcnt(2)
mav 0,144,40
R 24,16384
mav 4,148,40
mva 8,40,152
mav 0,156,44
R 28,17408
mav 4,160,44
mva 8,44,164
s_waitcnt lgkmcnt(2)
mav 0,168,48
R 32,18432
mav 4,172,48
mva 8,48,176
mav 0,180,52
R 36,19456
mav 4,184,52
mva 8,52,188
s_waitcnt lgkmcnt(2)
mav 0,192,24
R 40,20480
mav 4,196,24
mva 8,24,200
s_waitcnt vmcnt(0)
v_permlane32_swap_b32 v100, v101
mav 0,204,28
R 44,21504
v_add_f32 v102, v100, v101
mav 4,208,28
v_add_f32 v103, v100, v101
mva 8,28,212
s_waitcnt lgkmcnt(2)
mav 0,216,32
R 48,22528
v_permlane16_swap_b32 v102, v103
mav 4,220,32
v_add_f32 v100, v102, v103
v_add_f32 v101, v102, v103
mva 8,32,224
mav 0,228,36
R 52,23552
v_permlane32_swap_b32 v100, v101
mav 4,232,36
v_mul_f32 v102, 0x3a800000, v100
v_mul_f32 v103, 0x3a800000, v101
mva 8,36,236
v_fma_f32 v103, -v102, v102, v103
s_waitcnt lgkmcnt(2)
mav 0,240,40
R 24,24576
v_max_f32 v103, 0, v103
mav 4,244,40
v_add_f32 v103, 0x3727c5ac, v103
mva 8,40,248
v_rsq_f32 v100, v103
mav 0,252,44
R 28,25600
v_mul_f32_e64 v101, -v102, v100
mvv 4,128,44
ds_write_b64 v84, v[100:101] offset:0
mvv 8,44,132
s_waitcnt lgkmcnt(3)
mvv 0,136,48
R 32,26624
mvv 4,140,48
mvv 8,48,144
mvv 0,148,52
R 36,27648
mvv 4,152,52
mvv 8,52,156
s_waitcnt lgkmcnt(2)
mvv 0,160,24
R 40,28672
mvv 4,164,24
mvv 8,24,168
mvv 0,172,28
R 44,29696
mvv 4,176,28
mvv 8,28,180
s_waitcnt lgkmcnt(2)
mvv 0,184,32
R 48,30720
mvv 4,188,32
mvv 8,32,192
mvv 0,196,36
R 52,31744
mvv 4,200,36
s_add_u32 s49, s49, 346112
mvv 8,36,204
s_barrier
s_waitcnt lgkmcnt(2)
mvv 0,208,40
R 24,32768
buffer_load_dwordx2 v[100:101], v92, s[36:39], s49 offen sc1
mvv 4,212,40
ds_read_b64 v[94:95], v84 offset:0
)"
R"(
ds_read_b128 v[64:67], v85 offset:0
mvv 8,40,216
ds_read_b128 v[68:71], v85 offset:16
mvv 0,220,44
R 28,33792
mvv 4,224,44
mvv 8,44,228
s_waitcnt lgkmcnt(5)
mvv 0,232,48
R 32,34816
mvv 4,236,48
mvv 8,48,240
mvv 0,244,52
R 36,35840
mvv 4,248,52
mvv 8,52,252
s_waitcnt lgkmcnt(2)
v_mfma_f32_16x16x32_bf16 v[12:15], a[0:3], v[24:27], 0
R 40,36864
v_mul_f32 v96, v95, v56
v_mfma_f32_16x16x32_bf16 v[16:19], a[4:7], v[24:27], 0
v_fma_f32 v0, v0, v94, v96
v_mul_f32 v97, v95, v57
v_mfma_f32_16x16x32_bf16 v[20:23], v[24:27], a[8:11], 0
v_fma_f32 v1, v1, v94, v97
v_mul_f32 v96, v95, v58
mav 12,12,28
R 44,37888
v_fma_f32 v2, v2, v94, v96
mav 16,16,28
v_mul_f32 v97, v95, v59
v_fma_f32 v3, v3, v94, v97
mva 20,28,20
v_mul_f32 v96, v95, v60
s_waitcnt lgkmcnt(2)
mav 12,24,32
R 48,38912
v_fma_f32 v4, v4, v94, v96
mav 16,28,32
v_mul_f32 v97, v95, v61
v_fma_f32 v5, v5, v94, v97
mva 20,32,32
v_mul_f32 v96, v95, v62
v_fma_f32 v6, v6, v94, v96
mav 12,36,36
R 52,39936
v_mul_f32 v97, v95, v63
mav 16,40,36
v_fma_f32 v7, v7, v94, v97
v_cvt_pk_bf16_f32 v72, v0, v1
mva 20,36,44
v_cvt_pk_bf16_f32 v73, v2, v3
s_waitcnt lgkmcnt(2)
mav 12,48,40
R 24,40960
v_cvt_pk_bf16_f32 v74, v4, v5
mav 16,52,40
v_cvt_pk_bf16_f32 v75, v6, v7
ds_write_b128 v83, v[72:75] offset:0
mva 20,40,56
v_mul_f32 v65, v65, v93
v_fma_f32 v8, v8, v64, v65
mav 12,60,44
R 28,41984
v_mul_f32 v67, v67, v93
mav 16,64,44
v_fma_f32 v9, v9, v66, v67
v_mul_f32 v69, v69, v93
mva 20,44,68
v_fma_f32 v10, v10, v68, v69
s_waitcnt lgkmcnt(3)
mav 12,72,48
R 32,43008
v_mul_f32 v71, v71, v93
mav 16,76,48
v_fma_f32 v11, v11, v70, v71
v_cvt_pk_bf16_f32 v76, v8, v9
mva 20,48,80
v_cvt_pk_bf16_f32 v77, v10, v11
ds_write_b64 v83, v[76:77] offset:1024
mav 12,84,52
R 36,44032
mav 16,88,52
mva 20,52,92
s_waitcnt lgkmcnt(3)
mav 12,96,24
R 40,45056
mav 16,100,24
s_waitcnt lgkmcnt(2)
s_barrier
mva 20,24,104
mav 12,108,28
R 44,46080
mav 16,112,28
mva 20,28,116
mav 12,120,32
R 48,47104
mav 16,124,32
mva 20,32,128
s_waitcnt lgkmcnt(2)
mav 12,132,36
R 52,48128
mav 16,136,36
mva 20,36,140
mav 12,144,40
R 24,49152
mav 16,148,40
mva 20,40,152
s_waitcnt lgkmcnt(2)
mav 12,156,44
R 28,50176
mav 16,160,44
mva 20,44,164
mav 12,168,48
R 32,51200
mav 16,172,48
mva 20,48,176
s_waitcnt lgkmcnt(2)
mav 12,180,52
R 36,52224
mav 16,184,52
mva 20,52,188
mav 12,192,24
R 40,53248
mav 16,196,24
mva 20,24,200
s_waitcnt lgkmcnt(2)
mav 12,204,28
R 44,54272
mav 16,208,28
s_waitcnt vmcnt(0)
v_permlane32_swap_b32 v100, v101
mva 20,28,212
v_add_f32 v102, v100, v101
v_add_f32 v103, v100, v101
mav 12,216,32
R 48,55296
v_permlane16_swap_b32 v102, v103
mav 16,220,32
v_add_f32 v100, v102, v103
v_add_f32 v101, v102, v103
mva 20,32,224
s_waitcnt lgkmcnt(2)
mav 12,228,36
R 52,56320
v_permlane32_swap_b32 v100, v101
mav 16,232,36
v_mul_f32 v102, 0x3a800000, v100
v_mul_f32 v103, 0x3a800000, v101
mva 20,36,236
v_fma_f32 v103, -v102, v102, v103
v_max_f32 v103, 0, v103
mav 12,240,40
R 24,57344
v_add_f32 v103, 0x3727c5ac, v103
mav 16,244,40
v_rsq_f32 v100, v103
mva 20,40,248
v_mul_f32_e64 v101, -v102, v100
s_waitcnt lgkmcnt(2)
mav 12,252,44
R 28,58368
ds_write_b64 v84, v[100:101] offset:128
mvv 16,128,44
mvv 20,44,132
mvv 12,136,48
R 32,59392
mvv 16,140,48
mvv 20,48,144
s_waitcnt lgkmcnt(3)
mvv 12,148,52
R 36,60416
mvv 16,152,52
mvv 20,52,156
mvv 12,160,24
R 40,61440
mvv 16,164,24
mvv 20,24,168
s_waitcnt lgkmcnt(2)
mvv 12,172,28
R 44,62464
mvv 16,176,28
mvv 20,28,180
mvv 12,184,32
R 48,63488
mvv 16,188,32
mvv 20,32,192
s_waitcnt lgkmcnt(2)
mvv 12,196,36
R 52,64512
mvv 16,200,36
mvv 20,36,204
s_barrier
mvv 12,208,40
mvv 16,212,40
ds_read_b64 v[94:95], v84 offset:128
ds_read_b128 v[64:67], v85 offset:128
mvv 20,40,216
ds_read_b128 v[68:71], v85 offset:144
s_waitcnt lgkmcnt(4)
mvv 12,220,44
mvv 16,224,44
mvv 20,44,228
mvv 12,232,48
mvv 16,236,48
mvv 20,48,240
s_waitcnt lgkmcnt(0)
mvv 12,244,52
mvv 16,248,52
mvv 20,52,252
v_mul_f32 v96, v95, v56
s_nop 4
v_fma_f32 v12, v12, v94, v96
v_mul_f32 v97, v95, v57
v_fma_f32 v13, v13, v94, v97
v_mul_f32 v96, v95, v58
v_fma_f32 v14, v14, v94, v96
v_mul_f32 v97, v95, v59
v_fma_f32 v15, v15, v94, v97
v_mul_f32 v96, v95, v60
v_fma_f32 v16, v16, v94, v96
v_mul_f32 v97, v95, v61
v_fma_f32 v17, v17, v94, v97
v_mul_f32 v96, v95, v62
v_fma_f32 v18, v18, v94, v96
v_mul_f32 v97, v95, v63
v_fma_f32 v19, v19, v94, v97
v_cvt_pk_bf16_f32 v72, v12, v13
v_cvt_pk_bf16_f32 v73, v14, v15
v_cvt_pk_bf16_f32 v74, v16, v17
v_cvt_pk_bf16_f32 v75, v18, v19
ds_write_b128 v83, v[72:75] offset:0
v_mul_f32 v65, v65, v93
v_fma_f32 v20, v20, v64, v65
v_mul_f32 v67, v67, v93
v_fma_f32 v21, v21, v66, v67
v_mul_f32 v69, v69, v93
v_fma_f32 v22, v22, v68, v69
v_mul_f32 v71, v71, v93
v_fma_f32 v23, v23, v70, v71
v_cvt_pk_bf16_f32 v76, v20, v21
v_cvt_pk_bf16_f32 v77, v22, v23
ds_write_b64 v83, v[76:77] offset:1024
s_waitcnt lgkmcnt(0)
s_barrier
s_endpgm
LeQB:
.purgem R
Lr2:
s_cmp_eq_u32 s60, 2
s_cbranch_scc0 Lr3
.macro R d,o
ds_read_b128 v[\d:\d+3], v80 offset:\o
.endm
buffer_load_dword v71, off, s[36:39], 0 sc1
s_getpc_b64 s[66:67]
LpO:
v_lshlrev_b32 v8, 3, v80
s_lshl_b32 s60, s53, 3
v_add_u32 v8, s60, v8
.set cn, 0
.rept 2
v_add_u32 v9, cn, v8
v_min_u32 v9, LeO - LpO - 4, v9
global_load_dword v10, v9, s[66:67]
.set cn, cn + 32768
.endr
E 0,0
E 1,4096
E 2,8192
s_add_u32 s51, s51, 65536
E 0,12288
E 1,16384
E 2,20480
s_add_u32 s51, s51, 65536
E 0,24576
E 1,28672
E 2,32768
s_add_u32 s51, s51, 65536
E 0,36864
E 1,40960
E 2,45056
s_add_u32 s51, s51, 65536
E 0,49152
E 1,53248
E 2,57344
s_add_u32 s51, s51, 65536
E 0,61440
E 1,65536
E 2,69632
s_add_u32 s51, s51, 65536
E 0,73728
E 1,77824
E 2,81920
s_add_u32 s51, s51, 65536
E 0,86016
E 1,90112
E 2,94208
s_add_u32 s51, s51, 65536
s_cmp_ge_u32 s51, s57
s_cselect_b32 s60, 2097152, 0
s_sub_u32 s51, s51, s60
E 0,98304
E 1,102400
E 2,106496
s_add_u32 s51, s51, 65536
E 0,110592
E 1,114688
E 2,118784
s_add_u32 s51, s51, 65536
E 0,122880
E 1,126976
E 2,131072
s_add_u32 s51, s51, 65536
E 0,135168
E 1,139264
E 2,143360
s_add_u32 s51, s51, 65536
s_waitcnt vmcnt(30)
s_barrier
v_mov_b32 v70, 0x100
v_add_u32 v71, 0x9e3779b9, v71
buffer_store_dwordx2 v[70:71], v81, s[36:39], s52 offen sc1
v_add_u32 v88, v88, v70
Ta 0,4,0
Ta 4,5,0
Ta 8,6,0
Ta 12,4,12288
Ta 16,5,12288
Ta 20,6,12288
s_waitcnt lgkmcnt(0)
s_waitcnt vmcnt(25)
s_barrier
Ta 24,4,24576
Ta 28,5,24576
Ta 32,6,24576
Ta 36,4,36864
Ta 40,5,36864
Ta 44,6,36864
E 0,0
E 1,4096
E 2,8192
s_add_u32 s51, s51, 65536
E 0,12288
E 1,16384
E 2,20480
s_add_u32 s51, s51, 65536
s_waitcnt lgkmcnt(0)
s_waitcnt vmcnt(25)
s_barrier
v_add_u32 v4, 49152, v4
v_add_u32 v5, 49152, v5
v_add_u32 v6, 49152, v6
Ta 48,4,0
Ta 52,5,0
Ta 56,6,0
Ta 60,4,12288
Ta 64,5,12288
Ta 68,6,12288
E 0,24576
E 1,28672
E 2,32768
s_add_u32 s51, s51, 65536
E 0,36864
E 1,40960
E 2,45056
s_add_u32 s51, s51, 65536
s_cmp_ge_u32 s51, s57
s_cselect_b32 s60, 2097152, 0
s_sub_u32 s51, s51, s60
s_waitcnt lgkmcnt(0)
s_waitcnt vmcnt(25)
s_barrier
Ta 72,4,24576
Ta 76,5,24576
Ta 80,6,24576
Ta 84,4,36864
Ta 88,5,36864
Ta 92,6,36864
E 0,49152
E 1,53248
E 2,57344
s_add_u32 s51, s51, 65536
E 0,61440
E 1,65536
E 2,69632
s_add_u32 s51, s51, 65536
s_waitcnt lgkmcnt(0)
s_waitcnt vmcnt(25)
s_barrier
v_add_u32 v4, 49152, v4
v_add_u32 v5, 49152, v5
v_add_u32 v6, 49152, v6
Ta 96,4,0
Ta 100,5,0
Ta 104,6,0
Ta 108,4,12288
Ta 112,5,12288
Ta 116,6,12288
E 0,73728
E 1,77824
E 2,81920
s_add_u32 s51, s51, 65536
E 0,86016
E 1,90112
E 2,94208
s_add_u32 s51, s51, 65536
s_waitcnt lgkmcnt(0)
s_waitcnt vmcnt(25)
s_barrier
Ta 120,4,24576
Ta 124,5,24576
Ta 128,6,24576
Ta 132,4,36864
Ta 136,5,36864
Ta 140,6,36864
E 0,98304
E 1,102400
E 2,106496
s_add_u32 s51, s51, 65536
E 0,110592
E 1,114688
E 2,118784
s_add_u32 s51, s51, 65536
s_waitcnt lgkmcnt(0)
s_waitcnt vmcnt(24)
s_barrier
v_add_u32 v4, -98304, v4
v_add_u32 v5, -98304, v5
v_add_u32 v6, -98304, v6
Ta 144,4,0
Ta 148,5,0
Ta 152,6,0
Ta 156,4,12288
Ta 160,5,12288
Ta 164,6,12288
E 0,122880
E 1,126976
E 2,131072
s_add_u32 s51, s51, 65536
E 0,135168
E 1,139264
E 2,143360
s_add_u32 s51, s51, 65536
s_cmp_ge_u32 s51, s57
s_cselect_b32 s60, 2097152, 0
s_sub_u32 s51, s51, s60
s_waitcnt lgkmcnt(0)
s_waitcnt vmcnt(24)
s_barrier
Ta 168,4,24576
Ta 172,5,24576
Ta 176,6,24576
Ta 180,4,36864
Ta 184,5,36864
Ta 188,6,36864
E 0,0
E 1,4096
E 2,8192
s_add_u32 s51, s51, 65536
E 0,12288
E 1,16384
E 2,20480
s_add_u32 s51, s51, 65536
s_waitcnt lgkmcnt(0)
s_waitcnt vmcnt(24)
s_barrier
v_add_u32 v4, 49152, v4
v_add_u32 v5, 49152, v5
v_add_u32 v6, 49152, v6
Ta 192,4,0
Ta 196,5,0
Ta 200,6,0
Ta 204,4,12288
Ta 208,5,12288
Ta 212,6,12288
E 0,24576
E 1,28672
E 2,32768
s_add_u32 s51, s51, 65536
E 0,36864
E 1,40960
E 2,45056
s_add_u32 s51, s51, 65536
s_waitcnt lgkmcnt(0)
s_waitcnt vmcnt(24)
s_barrier
Ta 216,4,24576
Ta 220,5,24576
Ta 224,6,24576
Ta 228,4,36864
Ta 232,5,36864
Ta 236,6,36864
E 0,49152
E 1,53248
E 2,57344
s_add_u32 s51, s51, 65536
E 0,61440
E 1,65536
E 2,69632
s_add_u32 s51, s51, 65536
s_waitcnt lgkmcnt(0)
s_waitcnt vmcnt(24)
s_barrier
v_add_u32 v4, 49152, v4
v_add_u32 v5, 49152, v5
v_add_u32 v6, 49152, v6
Ta 240,4,0
Ta 244,5,0
Ta 248,6,0
Ta 252,4,12288
Tv 128,5,12288
Tv 132,6,12288
E 0,73728
E 1,77824
E 2,81920
s_add_u32 s51, s51, 65536
E 0,86016
E 1,90112
E 2,94208
s_add_u32 s51, s51, 65536
s_cmp_ge_u32 s51, s57
s_cselect_b32 s60, 2097152, 0
s_sub_u32 s51, s51, s60
s_waitcnt lgkmcnt(0)
s_waitcnt vmcnt(24)
s_barrier
Tv 136,4,24576
Tv 140,5,24576
Tv 144,6,24576
Tv 148,4,36864
Tv 152,5,36864
Tv 156,6,36864
s_waitcnt lgkmcnt(0)
s_waitcnt vmcnt(18)
s_barrier
v_add_u32 v4, -98304, v4
v_add_u32 v5, -98304, v5
v_add_u32 v6, -98304, v6
Tv 160,4,0
Tv 164,5,0
Tv 168,6,0
Tv 172,4,12288
Tv 176,5,12288
Tv 180,6,12288
s_waitcnt lgkmcnt(0)
s_waitcnt vmcnt(12)
s_barrier
Tv 184,4,24576
Tv 188,5,24576
Tv 192,6,24576
Tv 196,4,36864
Tv 200,5,36864
Tv 204,6,36864
s_waitcnt lgkmcnt(0)
s_waitcnt vmcnt(6)
s_barrier
v_add_u32 v4, 49152, v4
v_add_u32 v5, 49152, v5
v_add_u32 v6, 49152, v6
Tv 208,4,0
Tv 212,5,0
Tv 216,6,0
Tv 220,4,12288
Tv 224,5,12288
Tv 228,6,12288
s_waitcnt lgkmcnt(0)
s_waitcnt vmcnt(0)
s_barrier
Tv 232,4,24576
Tv 236,5,24576
Tv 240,6,24576
Tv 244,4,36864
Tv 248,5,36864
Tv 252,6,36864
s_waitcnt lgkmcnt(0)
s_waitcnt vmcnt(0)
s_barrier
s_nop 7
s_and_b32 s60, s48, 3
s_cmp_eq_u32 s60, 1
s_cbranch_scc1 LvO1
s_cmp_eq_u32 s60, 2
s_cbranch_scc1 LvO2
LvO0:
buffer_load_dwordx2 v[74:75], v87, s[36:39], 0 offen sc1
s_waitcnt vmcnt(0)
ds_write_b32 v82, v74
v_sub_u32 v74, v74, v88
v_xor_b32 v75, v75, v71
v_cmp_lt_u64 vcc, s[62:63], v[74:75]
s_waitcnt lgkmcnt(0)
s_cbranch_vccnz CO0_1
BO0_1:
s_barrier
buffer_load_dwordx4 v[56:59], v89, s[36:39], s49 offen sc1
buffer_load_dwordx2 v[60:61], v90, s[36:39], s49 offen sc1
s_add_u32 s49, s49, 346112
s_add_u32 m0, s50, 0
s_nop 0
D 85,0
D 85,1024
D 85,2048
D 85,3072
s_add_u32 m0, s50, 4096
s_nop 0
D 86,0
D 86,1024
D 86,2048
D 86,3072
s_mov_b32 m0, 142336
s_nop 0
buffer_load_dword v87, s[36:39], 0 offen sc1 lds
v_add_u32 v88, 1, v88
buffer_load_dword v87, s[36:39], 0 offen sc1 lds
s_waitcnt vmcnt(11)
v_lshlrev_b32 v0, 16, v56
v_and_b32 v1, 0xffff0000, v56
v_lshlrev_b32 v2, 16, v57
v_and_b32 v3, 0xffff0000, v57
v_lshlrev_b32 v4, 16, v58
v_and_b32 v5, 0xffff0000, v58
v_lshlrev_b32 v6, 16, v59
v_and_b32 v7, 0xffff0000, v59
s_waitcnt vmcnt(10)
v_lshlrev_b32 v8, 16, v60
v_and_b32 v9, 0xffff0000, v60
v_lshlrev_b32 v10, 16, v61
v_and_b32 v11, 0xffff0000, v61
buffer_load_dword v87, s[36:39], 0 offen sc1 lds
buffer_load_dwordx4 v[56:59], v89, s[36:39], s49 offen sc1
buffer_load_dwordx2 v[60:61], v90, s[36:39], s49 offen sc1
ds_read_b32 v74, v82 offset:0
s_waitcnt lgkmcnt(0)
v_cmp_gt_i32 vcc, v88, v74
s_waitcnt vmcnt(5)
s_cbranch_vccnz CO0_2
BO0_2:
s_add_u32 s49, s49, 346112
s_barrier
s_add_u32 m0, s50, 32768
R 24,0
D 85,0
R 28,1024
D 85,1024
R 32,2048
R 36,3072
D 85,2048
s_waitcnt lgkmcnt(2)
mav 0,0,24
R 40,4096
mav 4,4,24
mav 8,8,24
D 85,3072
mav 0,12,28
R 44,5120
mav 4,16,28
mav 8,20,28
s_add_u32 m0, s50, 36864
s_waitcnt lgkmcnt(2)
mav 0,24,32
R 48,6144
mav 4,28,32
D 86,0
mav 8,32,32
mav 0,36,36
R 52,7168
mav 4,40,36
mav 8,44,36
D 86,1024
s_waitcnt lgkmcnt(2)
mav 0,48,40
R 24,8192
mav 4,52,40
mav 8,56,40
mav 0,60,44
R 28,9216
mav 4,64,44
D 86,2048
mav 8,68,44
s_waitcnt lgkmcnt(2)
mav 0,72,48
R 32,10240
mav 4,76,48
mav 8,80,48
D 86,3072
mav 0,84,52
R 36,11264
mav 4,88,52
mav 8,92,52
s_waitcnt lgkmcnt(2)
mav 0,96,24
R 40,12288
mav 4,100,24
mav 8,104,24
mav 0,108,28
R 44,13312
mav 4,112,28
mav 8,116,28
s_waitcnt lgkmcnt(2)
mav 0,120,32
R 48,14336
s_mov_b32 m0, 142336
mav 4,124,32
mav 8,128,32
buffer_load_dword v87, s[36:39], 0 offen sc1 lds
mav 0,132,36
R 52,15360
mav 4,136,36
mav 8,140,36
s_waitcnt lgkmcnt(2)
mav 0,144,40
R 24,16384
mav 4,148,40
mav 8,152,40
mav 0,156,44
R 28,17408
mav 4,160,44
v_add_u32 v88, 1, v88
mav 8,164,44
s_waitcnt lgkmcnt(2)
mav 0,168,48
R 32,18432
mav 4,172,48
mav 8,176,48
buffer_load_dword v87, s[36:39], 0 offen sc1 lds
mav 0,180,52
R 36,19456
mav 4,184,52
mav 8,188,52
s_waitcnt lgkmcnt(2)
mav 0,192,24
R 40,20480
mav 4,196,24
mav 8,200,24
s_waitcnt vmcnt(11)
v_lshlrev_b32 v12, 16, v56
mav 0,204,28
R 44,21504
v_and_b32 v13, 0xffff0000, v56
mav 4,208,28
v_lshlrev_b32 v14, 16, v57
v_and_b32 v15, 0xffff0000, v57
mav 8,212,28
v_lshlrev_b32 v16, 16, v58
s_waitcnt lgkmcnt(2)
mav 0,216,32
R 48,22528
v_and_b32 v17, 0xffff0000, v58
mav 4,220,32
v_lshlrev_b32 v18, 16, v59
v_and_b32 v19, 0xffff0000, v59
mav 8,224,32
buffer_load_dword v87, s[36:39], 0 offen sc1 lds
mav 0,228,36
R 52,23552
mav 4,232,36
s_waitcnt vmcnt(11)
v_lshlrev_b32 v20, 16, v60
mav 8,236,36
v_and_b32 v21, 0xffff0000, v60
s_waitcnt lgkmcnt(2)
mav 0,240,40
R 24,24576
v_lshlrev_b32 v22, 16, v61
mav 4,244,40
v_and_b32 v23, 0xffff0000, v61
mav 8,248,40
mav 0,252,44
R 28,25600
mvv 4,128,44
buffer_load_dwordx4 v[56:59], v89, s[36:39], s49 offen sc1
mvv 8,132,44
s_waitcnt lgkmcnt(2)
mvv 0,136,48
R 32,26624
mvv 4,140,48
mvv 8,144,48
buffer_load_dwordx2 v[60:61], v90, s[36:39], s49 offen sc1
mvv 0,148,52
R 36,27648
mvv 4,152,52
mvv 8,156,52
s_waitcnt lgkmcnt(2)
mvv 0,160,24
R 40,28672
mvv 4,164,24
ds_read_b32 v74, v82 offset:0
mvv 8,168,24
mvv 0,172,28
R 44,29696
mvv 4,176,28
mvv 8,180,28
s_waitcnt lgkmcnt(3)
mvv 0,184,32
R 48,30720
s_xor_b32 s50, s50, 0x10000
mvv 4,188,32
mvv 8,192,32
mvv 0,196,36
R 52,31744
mvv 4,200,36
s_waitcnt lgkmcnt(2)
v_cmp_gt_i32 vcc, v88, v74
mvv 8,204,36
s_waitcnt vmcnt(5)
s_cbranch_vccnz CO0_3
BO0_3:
mvv 0,208,40
s_barrier
R 24,32768
mvv 4,212,40
s_add_u32 s49, s49, 346112
s_add_u32 m0, s50, 0
mvv 8,216,40
D 85,0
mvv 0,220,44
R 28,33792
mvv 4,224,44
mvv 8,228,44
s_waitcnt lgkmcnt(2)
mvv 0,232,48
R 32,34816
D 85,1024
mvv 4,236,48
mvv 8,240,48
mvv 0,244,52
R 36,35840
mvv 4,248,52
D 85,2048
mvv 8,252,52
s_waitcnt lgkmcnt(2)
mav 12,0,24
R 40,36864
mav 16,4,24
v_cvt_pk_bf16_f32 v62, v0, v1
v_cvt_pk_bf16_f32 v63, v2, v3
mav 20,8,24
D 85,3072
v_cvt_pk_bf16_f32 v64, v4, v5
mav 12,12,28
R 44,37888
v_cvt_pk_bf16_f32 v65, v6, v7
mav 16,16,28
v_cvt_pk_bf16_f32 v66, v8, v9
v_cvt_pk_bf16_f32 v67, v10, v11
mav 20,20,28
s_add_u32 m0, s50, 4096
s_waitcnt lgkmcnt(2)
mav 12,24,32
R 48,38912
buffer_store_dwordx4 v[62:65], v83, s[36:39], s49 offen sc1
mav 16,28,32
v_cvt_pk_bf16_f32 v68, v8, v9
v_cvt_pk_bf16_f32 v69, v10, v11
mav 20,32,32
v_permlane16_swap_b32 v66, v68
v_permlane16_swap_b32 v67, v69
mav 12,36,36
R 52,39936
v_mul_f32 v73, v0, v0
mav 16,40,36
buffer_store_dwordx4 v[66:69], v84, s[36:39], s49 offen sc1
v_add_f32 v72, v1, v0
mav 20,44,36
v_fmac_f32 v73, v1, v1
s_waitcnt lgkmcnt(2)
mav 12,48,40
R 24,40960
v_add_f32 v72, v2, v72
mav 16,52,40
v_fmac_f32 v73, v2, v2
v_add_f32 v72, v3, v72
mav 20,56,40
D 86,0
v_fmac_f32 v73, v3, v3
mav 12,60,44
R 28,41984
v_add_f32 v72, v4, v72
mav 16,64,44
v_fmac_f32 v73, v4, v4
v_add_f32 v72, v5, v72
mav 20,68,44
v_fmac_f32 v73, v5, v5
s_waitcnt lgkmcnt(2)
mav 12,72,48
R 32,43008
D 86,1024
mav 16,76,48
v_add_f32 v72, v6, v72
v_fmac_f32 v73, v6, v6
mav 20,80,48
v_add_f32 v72, v7, v72
v_fmac_f32 v73, v7, v7
mav 12,84,52
R 36,44032
v_add_f32 v72, v8, v72
mav 16,88,52
D 86,2048
v_fmac_f32 v73, v8, v8
mav 20,92,52
v_add_f32 v72, v9, v72
s_waitcnt lgkmcnt(2)
mav 12,96,24
R 40,45056
v_fmac_f32 v73, v9, v9
mav 16,100,24
v_add_f32 v72, v10, v72
v_fmac_f32 v73, v10, v10
mav 20,104,24
D 86,3072
v_add_f32 v72, v11, v72
mav 12,108,28
R 44,46080
v_fmac_f32 v73, v11, v11
mav 16,112,28
mav 20,116,28
v_permlane32_swap_b32 v72, v73
s_waitcnt lgkmcnt(2)
mav 12,120,32
R 48,47104
s_mov_b32 m0, 142336
mav 16,124,32
v_add_f32 v72, v72, v73
ds_write_b32 v91, v72 offset:0
mav 20,128,32
buffer_load_dword v87, s[36:39], 0 offen sc1 lds
mav 12,132,36
R 52,48128
mav 16,136,36
mav 20,140,36
s_waitcnt lgkmcnt(3)
mav 12,144,40
R 24,49152
mav 16,148,40
mav 20,152,40
mav 12,156,44
R 28,50176
mav 16,160,44
v_add_u32 v88, 1, v88
mav 20,164,44
s_waitcnt lgkmcnt(2)
mav 12,168,48
R 32,51200
mav 16,172,48
mav 20,176,48
buffer_load_dword v87, s[36:39], 0 offen sc1 lds
mav 12,180,52
R 36,52224
mav 16,184,52
mav 20,188,52
s_waitcnt lgkmcnt(2)
mav 12,192,24
R 40,53248
mav 16,196,24
mav 20,200,24
s_waitcnt vmcnt(13)
v_lshlrev_b32 v0, 16, v56
mav 12,204,28
R 44,54272
v_and_b32 v1, 0xffff0000, v56
mav 16,208,28
v_lshlrev_b32 v2, 16, v57
v_and_b32 v3, 0xffff0000, v57
mav 20,212,28
v_lshlrev_b32 v4, 16, v58
s_waitcnt lgkmcnt(2)
mav 12,216,32
R 48,55296
v_and_b32 v5, 0xffff0000, v58
mav 16,220,32
v_lshlrev_b32 v6, 16, v59
v_and_b32 v7, 0xffff0000, v59
mav 20,224,32
buffer_load_dword v87, s[36:39], 0 offen sc1 lds
mav 12,228,36
R 52,56320
mav 16,232,36
s_waitcnt vmcnt(13)
v_lshlrev_b32 v8, 16, v60
mav 20,236,36
v_and_b32 v9, 0xffff0000, v60
s_waitcnt lgkmcnt(2)
mav 12,240,40
R 24,57344
v_lshlrev_b32 v10, 16, v61
mav 16,244,40
v_and_b32 v11, 0xffff0000, v61
mav 20,248,40
mav 12,252,44
R 28,58368
mvv 16,128,44
buffer_load_dwordx4 v[56:59], v89, s[36:39], s49 offen sc1
v_add_u32 v70, 1, v70
mvv 20,132,44
s_waitcnt lgkmcnt(2)
mvv 12,136,48
R 32,59392
mvv 16,140,48
mvv 20,144,48
buffer_load_dwordx2 v[60:61], v90, s[36:39], s49 offen sc1
mvv 12,148,52
R 36,60416
mvv 16,152,52
s_waitcnt vmcnt(9)
buffer_store_dwordx2 v[70:71], v81, s[36:39], s52 offen sc1
mvv 20,156,52
s_waitcnt lgkmcnt(2)
mvv 12,160,24
R 40,61440
mvv 16,164,24
ds_read_b32 v74, v82 offset:0
mvv 20,168,24
mvv 12,172,28
R 44,62464
mvv 16,176,28
mvv 20,180,28
s_waitcnt lgkmcnt(3)
mvv 12,184,32
R 48,63488
mvv 16,188,32
mvv 20,192,32
mvv 12,196,36
R 52,64512
v_xor_b32 v80, 0x10000, v80
mvv 16,200,36
s_waitcnt lgkmcnt(2)
v_cmp_gt_i32 vcc, v88, v74
mvv 20,204,36
s_waitcnt vmcnt(6)
s_cbranch_vccnz CO0_4
BO0_4:
mvv 12,208,40
s_barrier
R 24,0
mvv 16,212,40
s_add_u32 s49, s49, 346112
s_add_u32 m0, s50, 32768
mvv 20,216,40
D 85,0
mvv 12,220,44
R 28,1024
mvv 16,224,44
mvv 20,228,44
s_waitcnt lgkmcnt(2)
mvv 12,232,48
R 32,2048
D 85,1024
mvv 16,236,48
mvv 20,240,48
mvv 12,244,52
R 36,3072
mvv 16,248,52
D 85,2048
mvv 20,252,52
s_mov_b32 s60, -30
.p2align 6
LO0_5:
s_waitcnt lgkmcnt(2)
mav 0,0,24
R 40,4096
mav 4,4,24
v_cvt_pk_bf16_f32 v62, v12, v13
v_cvt_pk_bf16_f32 v63, v14, v15
mav 8,8,24
D 85,3072
v_cvt_pk_bf16_f32 v64, v16, v17
mav 0,12,28
R 44,5120
v_cvt_pk_bf16_f32 v65, v18, v19
mav 4,16,28
v_cvt_pk_bf16_f32 v66, v20, v21
v_cvt_pk_bf16_f32 v67, v22, v23
mav 8,20,28
s_add_u32 m0, s50, 36864
s_waitcnt lgkmcnt(2)
mav 0,24,32
R 48,6144
buffer_store_dwordx4 v[62:65], v83, s[36:39], s49 offen sc1
mav 4,28,32
v_cvt_pk_bf16_f32 v68, v20, v21
v_cvt_pk_bf16_f32 v69, v22, v23
mav 8,32,32
v_permlane16_swap_b32 v66, v68
v_permlane16_swap_b32 v67, v69
mav 0,36,36
R 52,7168
v_mul_f32 v73, v12, v12
mav 4,40,36
buffer_store_dwordx4 v[66:69], v84, s[36:39], s49 offen sc1
v_add_f32 v72, v13, v12
mav 8,44,36
v_fmac_f32 v73, v13, v13
s_waitcnt lgkmcnt(2)
mav 0,48,40
R 24,8192
v_add_f32 v72, v14, v72
mav 4,52,40
v_fmac_f32 v73, v14, v14
v_add_f32 v72, v15, v72
mav 8,56,40
D 86,0
v_fmac_f32 v73, v15, v15
mav 0,60,44
R 28,9216
v_add_f32 v72, v16, v72
mav 4,64,44
v_fmac_f32 v73, v16, v16
v_add_f32 v72, v17, v72
mav 8,68,44
v_fmac_f32 v73, v17, v17
s_waitcnt lgkmcnt(2)
mav 0,72,48
R 32,10240
D 86,1024
mav 4,76,48
v_add_f32 v72, v18, v72
v_fmac_f32 v73, v18, v18
mav 8,80,48
v_add_f32 v72, v19, v72
v_fmac_f32 v73, v19, v19
mav 0,84,52
R 36,11264
v_add_f32 v72, v20, v72
mav 4,88,52
D 86,2048
v_fmac_f32 v73, v20, v20
mav 8,92,52
v_add_f32 v72, v21, v72
s_waitcnt lgkmcnt(2)
mav 0,96,24
R 40,12288
v_fmac_f32 v73, v21, v21
mav 4,100,24
v_add_f32 v72, v22, v72
v_fmac_f32 v73, v22, v22
mav 8,104,24
D 86,3072
v_add_f32 v72, v23, v72
mav 0,108,28
R 44,13312
v_fmac_f32 v73, v23, v23
mav 4,112,28
mav 8,116,28
v_permlane32_swap_b32 v72, v73
s_waitcnt lgkmcnt(2)
mav 0,120,32
R 48,14336
s_mov_b32 m0, 142336
mav 4,124,32
v_add_f32 v72, v72, v73
ds_write_b32 v91, v72 offset:1024
mav 8,128,32
buffer_load_dword v87, s[36:39], 0 offen sc1 lds
mav 0,132,36
R 52,15360
mav 4,136,36
mav 8,140,36
s_waitcnt lgkmcnt(3)
mav 0,144,40
R 24,16384
mav 4,148,40
mav 8,152,40
mav 0,156,44
R 28,17408
mav 4,160,44
v_add_u32 v88, 1, v88
mav 8,164,44
s_waitcnt lgkmcnt(2)
mav 0,168,48
R 32,18432
mav 4,172,48
mav 8,176,48
buffer_load_dword v87, s[36:39], 0 offen sc1 lds
mav 0,180,52
R 36,19456
mav 4,184,52
mav 8,188,52
s_waitcnt lgkmcnt(2)
mav 0,192,24
R 40,20480
mav 4,196,24
mav 8,200,24
s_waitcnt vmcnt(14)
v_lshlrev_b32 v12, 16, v56
mav 0,204,28
R 44,21504
v_and_b32 v13, 0xffff0000, v56
mav 4,208,28
v_lshlrev_b32 v14, 16, v57
v_and_b32 v15, 0xffff0000, v57
mav 8,212,28
v_lshlrev_b32 v16, 16, v58
s_waitcnt lgkmcnt(2)
mav 0,216,32
R 48,22528
v_and_b32 v17, 0xffff0000, v58
mav 4,220,32
v_lshlrev_b32 v18, 16, v59
v_and_b32 v19, 0xffff0000, v59
mav 8,224,32
buffer_load_dword v87, s[36:39], 0 offen sc1 lds
mav 0,228,36
R 52,23552
mav 4,232,36
s_waitcnt vmcnt(14)
v_lshlrev_b32 v20, 16, v60
mav 8,236,36
v_and_b32 v21, 0xffff0000, v60
s_waitcnt lgkmcnt(2)
mav 0,240,40
R 24,24576
v_lshlrev_b32 v22, 16, v61
mav 4,244,40
v_and_b32 v23, 0xffff0000, v61
mav 8,248,40
mav 0,252,44
R 28,25600
mvv 4,128,44
buffer_load_dwordx4 v[56:59], v89, s[36:39], s49 offen sc1
v_add_u32 v70, 1, v70
mvv 8,132,44
s_waitcnt lgkmcnt(2)
mvv 0,136,48
R 32,26624
mvv 4,140,48
mvv 8,144,48
buffer_load_dwordx2 v[60:61], v90, s[36:39], s49 offen sc1
mvv 0,148,52
R 36,27648
mvv 4,152,52
s_waitcnt vmcnt(9)
buffer_store_dwordx2 v[70:71], v81, s[36:39], s52 offen sc1
mvv 8,156,52
s_waitcnt lgkmcnt(2)
mvv 0,160,24
R 40,28672
mvv 4,164,24
ds_read_b32 v74, v82 offset:0
mvv 8,168,24
mvv 0,172,28
R 44,29696
mvv 4,176,28
mvv 8,180,28
s_waitcnt lgkmcnt(3)
mvv 0,184,32
R 48,30720
s_xor_b32 s50, s50, 0x10000
mvv 4,188,32
mvv 8,192,32
mvv 0,196,36
R 52,31744
mvv 4,200,36
s_waitcnt lgkmcnt(2)
v_cmp_gt_i32 vcc, v88, v74
mvv 8,204,36
s_waitcnt vmcnt(6)
s_cbranch_vccnz CO0_6
BO0_6:
mvv 0,208,40
s_barrier
R 24,32768
mvv 4,212,40
s_add_u32 s49, s49, 346112
s_add_u32 m0, s50, 0
mvv 8,216,40
D 85,0
mvv 0,220,44
R 28,33792
mvv 4,224,44
mvv 8,228,44
s_waitcnt lgkmcnt(2)
mvv 0,232,48
R 32,34816
D 85,1024
mvv 4,236,48
mvv 8,240,48
mvv 0,244,52
R 36,35840
mvv 4,248,52
D 85,2048
mvv 8,252,52
s_waitcnt lgkmcnt(2)
mav 12,0,24
R 40,36864
mav 16,4,24
v_cvt_pk_bf16_f32 v62, v0, v1
v_cvt_pk_bf16_f32 v63, v2, v3
mav 20,8,24
D 85,3072
v_cvt_pk_bf16_f32 v64, v4, v5
mav 12,12,28
R 44,37888
v_cvt_pk_bf16_f32 v65, v6, v7
mav 16,16,28
v_cvt_pk_bf16_f32 v66, v8, v9
v_cvt_pk_bf16_f32 v67, v10, v11
mav 20,20,28
s_add_u32 m0, s50, 4096
s_waitcnt lgkmcnt(2)
mav 12,24,32
R 48,38912
buffer_store_dwordx4 v[62:65], v83, s[36:39], s49 offen sc1
mav 16,28,32
v_cvt_pk_bf16_f32 v68, v8, v9
v_cvt_pk_bf16_f32 v69, v10, v11
mav 20,32,32
v_permlane16_swap_b32 v66, v68
v_permlane16_swap_b32 v67, v69
mav 12,36,36
R 52,39936
v_mul_f32 v73, v0, v0
mav 16,40,36
buffer_store_dwordx4 v[66:69], v84, s[36:39], s49 offen sc1
v_add_f32 v72, v1, v0
mav 20,44,36
v_fmac_f32 v73, v1, v1
s_waitcnt lgkmcnt(2)
mav 12,48,40
R 24,40960
v_add_f32 v72, v2, v72
mav 16,52,40
v_fmac_f32 v73, v2, v2
v_add_f32 v72, v3, v72
mav 20,56,40
D 86,0
v_fmac_f32 v73, v3, v3
mav 12,60,44
R 28,41984
v_add_f32 v72, v4, v72
mav 16,64,44
v_fmac_f32 v73, v4, v4
v_add_f32 v72, v5, v72
mav 20,68,44
v_fmac_f32 v73, v5, v5
s_waitcnt lgkmcnt(2)
mav 12,72,48
R 32,43008
D 86,1024
mav 16,76,48
v_add_f32 v72, v6, v72
v_fmac_f32 v73, v6, v6
mav 20,80,48
v_add_f32 v72, v7, v72
v_fmac_f32 v73, v7, v7
mav 12,84,52
R 36,44032
v_add_f32 v72, v8, v72
mav 16,88,52
D 86,2048
v_fmac_f32 v73, v8, v8
mav 20,92,52
v_add_f32 v72, v9, v72
s_waitcnt lgkmcnt(2)
mav 12,96,24
R 40,45056
v_fmac_f32 v73, v9, v9
mav 16,100,24
v_add_f32 v72, v10, v72
v_fmac_f32 v73, v10, v10
mav 20,104,24
D 86,3072
v_add_f32 v72, v11, v72
mav 12,108,28
R 44,46080
v_fmac_f32 v73, v11, v11
mav 16,112,28
mav 20,116,28
v_permlane32_swap_b32 v72, v73
s_waitcnt lgkmcnt(2)
mav 12,120,32
R 48,47104
s_mov_b32 m0, 142336
mav 16,124,32
v_add_f32 v72, v72, v73
ds_write_b32 v91, v72 offset:0
mav 20,128,32
buffer_load_dword v87, s[36:39], 0 offen sc1 lds
mav 12,132,36
R 52,48128
mav 16,136,36
mav 20,140,36
s_waitcnt lgkmcnt(3)
mav 12,144,40
R 24,49152
mav 16,148,40
mav 20,152,40
mav 12,156,44
R 28,50176
mav 16,160,44
v_add_u32 v88, 1, v88
mav 20,164,44
s_waitcnt lgkmcnt(2)
mav 12,168,48
R 32,51200
mav 16,172,48
mav 20,176,48
buffer_load_dword v87, s[36:39], 0 offen sc1 lds
mav 12,180,52
R 36,52224
mav 16,184,52
mav 20,188,52
s_waitcnt lgkmcnt(2)
mav 12,192,24
R 40,53248
mav 16,196,24
mav 20,200,24
s_waitcnt vmcnt(14)
v_lshlrev_b32 v0, 16, v56
mav 12,204,28
R 44,54272
v_and_b32 v1, 0xffff0000, v56
mav 16,208,28
v_lshlrev_b32 v2, 16, v57
v_and_b32 v3, 0xffff0000, v57
mav 20,212,28
v_lshlrev_b32 v4, 16, v58
s_waitcnt lgkmcnt(2)
mav 12,216,32
R 48,55296
v_and_b32 v5, 0xffff0000, v58
mav 16,220,32
v_lshlrev_b32 v6, 16, v59
v_and_b32 v7, 0xffff0000, v59
mav 20,224,32
buffer_load_dword v87, s[36:39], 0 offen sc1 lds
mav 12,228,36
R 52,56320
mav 16,232,36
s_waitcnt vmcnt(14)
v_lshlrev_b32 v8, 16, v60
mav 20,236,36
v_and_b32 v9, 0xffff0000, v60
s_waitcnt lgkmcnt(2)
mav 12,240,40
R 24,57344
v_lshlrev_b32 v10, 16, v61
mav 16,244,40
v_and_b32 v11, 0xffff0000, v61
mav 20,248,40
mav 12,252,44
R 28,58368
mvv 16,128,44
buffer_load_dwordx4 v[56:59], v89, s[36:39], s49 offen sc1
v_add_u32 v70, 1, v70
mvv 20,132,44
s_waitcnt lgkmcnt(2)
mvv 12,136,48
R 32,59392
mvv 16,140,48
mvv 20,144,48
buffer_load_dwordx2 v[60:61], v90, s[36:39], s49 offen sc1
mvv 12,148,52
R 36,60416
mvv 16,152,52
s_waitcnt vmcnt(9)
buffer_store_dwordx2 v[70:71], v81, s[36:39], s52 offen sc1
mvv 20,156,52
s_waitcnt lgkmcnt(2)
mvv 12,160,24
R 40,61440
mvv 16,164,24
ds_read_b32 v74, v82 offset:0
mvv 20,168,24
mvv 12,172,28
R 44,62464
mvv 16,176,28
mvv 20,180,28
s_waitcnt lgkmcnt(3)
mvv 12,184,32
R 48,63488
mvv 16,188,32
mvv 20,192,32
mvv 12,196,36
R 52,64512
v_xor_b32 v80, 0x10000, v80
mvv 16,200,36
s_waitcnt lgkmcnt(2)
v_cmp_gt_i32 vcc, v88, v74
mvv 20,204,36
s_waitcnt vmcnt(6)
s_cbranch_vccnz CO0_7
BO0_7:
mvv 12,208,40
s_barrier
R 24,0
mvv 16,212,40
s_add_u32 s49, s49, 346112
s_add_u32 m0, s50, 32768
mvv 20,216,40
D 85,0
mvv 12,220,44
R 28,1024
mvv 16,224,44
mvv 20,228,44
s_waitcnt lgkmcnt(2)
mvv 12,232,48
R 32,2048
D 85,1024
mvv 16,236,48
mvv 20,240,48
mvv 12,244,52
R 36,3072
mvv 16,248,52
D 85,2048
mvv 20,252,52
s_add_u32 s60, s60, 1
s_cbranch_scc0 LO0_5
s_waitcnt lgkmcnt(2)
mav 0,0,24
R 40,4096
mav 4,4,24
v_cvt_pk_bf16_f32 v62, v12, v13
v_cvt_pk_bf16_f32 v63, v14, v15
mav 8,8,24
D 85,3072
v_cvt_pk_bf16_f32 v64, v16, v17
mav 0,12,28
R 44,5120
v_cvt_pk_bf16_f32 v65, v18, v19
mav 4,16,28
v_cvt_pk_bf16_f32 v66, v20, v21
v_cvt_pk_bf16_f32 v67, v22, v23
mav 8,20,28
s_add_u32 m0, s50, 36864
s_waitcnt lgkmcnt(2)
mav 0,24,32
R 48,6144
buffer_store_dwordx4 v[62:65], v83, s[36:39], s49 offen sc1
mav 4,28,32
v_cvt_pk_bf16_f32 v68, v20, v21
v_cvt_pk_bf16_f32 v69, v22, v23
mav 8,32,32
v_permlane16_swap_b32 v66, v68
v_permlane16_swap_b32 v67, v69
mav 0,36,36
R 52,7168
v_mul_f32 v73, v12, v12
mav 4,40,36
buffer_store_dwordx4 v[66:69], v84, s[36:39], s49 offen sc1
v_add_f32 v72, v13, v12
mav 8,44,36
v_fmac_f32 v73, v13, v13
s_waitcnt lgkmcnt(2)
mav 0,48,40
R 24,8192
v_add_f32 v72, v14, v72
mav 4,52,40
v_fmac_f32 v73, v14, v14
v_add_f32 v72, v15, v72
mav 8,56,40
D 86,0
v_fmac_f32 v73, v15, v15
mav 0,60,44
R 28,9216
v_add_f32 v72, v16, v72
mav 4,64,44
v_fmac_f32 v73, v16, v16
v_add_f32 v72, v17, v72
mav 8,68,44
v_fmac_f32 v73, v17, v17
s_waitcnt lgkmcnt(2)
mav 0,72,48
R 32,10240
D 86,1024
mav 4,76,48
v_add_f32 v72, v18, v72
v_fmac_f32 v73, v18, v18
mav 8,80,48
v_add_f32 v72, v19, v72
v_fmac_f32 v73, v19, v19
mav 0,84,52
R 36,11264
v_add_f32 v72, v20, v72
mav 4,88,52
D 86,2048
v_fmac_f32 v73, v20, v20
mav 8,92,52
v_add_f32 v72, v21, v72
s_waitcnt lgkmcnt(2)
mav 0,96,24
R 40,12288
v_fmac_f32 v73, v21, v21
mav 4,100,24
v_add_f32 v72, v22, v72
v_fmac_f32 v73, v22, v22
mav 8,104,24
D 86,3072
v_add_f32 v72, v23, v72
mav 0,108,28
R 44,13312
v_fmac_f32 v73, v23, v23
mav 4,112,28
mav 8,116,28
v_permlane32_swap_b32 v72, v73
s_waitcnt lgkmcnt(2)
mav 0,120,32
R 48,14336
v_add_f32 v72, v72, v73
mav 4,124,32
ds_write_b32 v91, v72 offset:1024
mav 8,128,32
mav 0,132,36
R 52,15360
mav 4,136,36
mav 8,140,36
s_waitcnt lgkmcnt(3)
mav 0,144,40
R 24,16384
mav 4,148,40
mav 8,152,40
mav 0,156,44
R 28,17408
mav 4,160,44
mav 8,164,44
s_waitcnt lgkmcnt(2)
mav 0,168,48
R 32,18432
mav 4,172,48
mav 8,176,48
mav 0,180,52
R 36,19456
mav 4,184,52
mav 8,188,52
s_waitcnt lgkmcnt(2)
mav 0,192,24
R 40,20480
mav 4,196,24
mav 8,200,24
s_waitcnt vmcnt(12)
v_lshlrev_b32 v12, 16, v56
mav 0,204,28
R 44,21504
v_and_b32 v13, 0xffff0000, v56
mav 4,208,28
v_lshlrev_b32 v14, 16, v57
v_and_b32 v15, 0xffff0000, v57
mav 8,212,28
v_lshlrev_b32 v16, 16, v58
s_waitcnt lgkmcnt(2)
mav 0,216,32
R 48,22528
v_and_b32 v17, 0xffff0000, v58
mav 4,220,32
v_lshlrev_b32 v18, 16, v59
v_and_b32 v19, 0xffff0000, v59
mav 8,224,32
s_waitcnt vmcnt(11)
v_lshlrev_b32 v20, 16, v60
mav 0,228,36
R 52,23552
v_and_b32 v21, 0xffff0000, v60
mav 4,232,36
v_lshlrev_b32 v22, 16, v61
v_and_b32 v23, 0xffff0000, v61
mav 8,236,36
s_waitcnt lgkmcnt(2)
mav 0,240,40
R 24,24576
mav 4,244,40
mav 8,248,40
mav 0,252,44
R 28,25600
mvv 4,128,44
v_add_u32 v70, 1, v70
mvv 8,132,44
s_waitcnt lgkmcnt(2)
mvv 0,136,48
R 32,26624
mvv 4,140,48
mvv 8,144,48
mvv 0,148,52
R 36,27648
mvv 4,152,52
s_waitcnt vmcnt(4)
buffer_store_dwordx2 v[70:71], v81, s[36:39], s52 offen sc1
mvv 8,156,52
s_waitcnt lgkmcnt(2)
mvv 0,160,24
R 40,28672
mvv 4,164,24
mvv 8,168,24
mvv 0,172,28
R 44,29696
mvv 4,176,28
mvv 8,180,28
s_waitcnt lgkmcnt(2)
mvv 0,184,32
R 48,30720
mvv 4,188,32
mvv 8,192,32
mvv 0,196,36
R 52,31744
mvv 4,200,36
s_add_u32 s49, s49, 346112
mvv 8,204,36
s_waitcnt vmcnt(1)
s_barrier
s_waitcnt lgkmcnt(2)
mvv 0,208,40
R 24,32768
mvv 4,212,40
mvv 8,216,40
mvv 0,220,44
R 28,33792
mvv 4,224,44
mvv 8,228,44
s_waitcnt lgkmcnt(2)
mvv 0,232,48
R 32,34816
mvv 4,236,48
mvv 8,240,48
mvv 0,244,52
R 36,35840
mvv 4,248,52
mvv 8,252,52
s_waitcnt lgkmcnt(2)
mav 12,0,24
R 40,36864
mav 16,4,24
mav 20,8,24
v_cvt_pk_bf16_f32 v62, v0, v1
v_cvt_pk_bf16_f32 v63, v2, v3
mav 12,12,28
R 44,37888
v_cvt_pk_bf16_f32 v64, v4, v5
mav 16,16,28
v_cvt_pk_bf16_f32 v65, v6, v7
buffer_store_dwordx4 v[62:65], v83, s[36:39], s49 offen sc1
mav 20,20,28
v_cvt_pk_bf16_f32 v66, v8, v9
s_waitcnt lgkmcnt(2)
mav 12,24,32
R 48,38912
v_cvt_pk_bf16_f32 v67, v10, v11
mav 16,28,32
v_cvt_pk_bf16_f32 v68, v8, v9
v_cvt_pk_bf16_f32 v69, v10, v11
mav 20,32,32
v_permlane16_swap_b32 v66, v68
v_permlane16_swap_b32 v67, v69
mav 12,36,36
R 52,39936
buffer_store_dwordx4 v[66:69], v84, s[36:39], s49 offen sc1
mav 16,40,36
v_mul_f32 v73, v0, v0
v_add_f32 v72, v1, v0
mav 20,44,36
v_fmac_f32 v73, v1, v1
s_waitcnt lgkmcnt(2)
mav 12,48,40
R 24,40960
v_add_f32 v72, v2, v72
mav 16,52,40
v_fmac_f32 v73, v2, v2
v_add_f32 v72, v3, v72
mav 20,56,40
v_fmac_f32 v73, v3, v3
v_add_f32 v72, v4, v72
mav 12,60,44
R 28,41984
v_fmac_f32 v73, v4, v4
mav 16,64,44
v_add_f32 v72, v5, v72
v_fmac_f32 v73, v5, v5
mav 20,68,44
v_add_f32 v72, v6, v72
s_waitcnt lgkmcnt(2)
mav 12,72,48
R 32,43008
v_fmac_f32 v73, v6, v6
mav 16,76,48
v_add_f32 v72, v7, v72
v_fmac_f32 v73, v7, v7
mav 20,80,48
v_add_f32 v72, v8, v72
v_fmac_f32 v73, v8, v8
mav 12,84,52
R 36,44032
v_add_f32 v72, v9, v72
mav 16,88,52
v_fmac_f32 v73, v9, v9
v_add_f32 v72, v10, v72
mav 20,92,52
v_fmac_f32 v73, v10, v10
s_waitcnt lgkmcnt(2)
mav 12,96,24
R 40,45056
v_add_f32 v72, v11, v72
mav 16,100,24
v_fmac_f32 v73, v11, v11
mav 20,104,24
mav 12,108,28
R 44,46080
v_permlane32_swap_b32 v72, v73
mav 16,112,28
v_add_f32 v72, v72, v73
ds_write_b32 v91, v72 offset:0
mav 20,116,28
s_waitcnt lgkmcnt(3)
mav 12,120,32
R 48,47104
mav 16,124,32
mav 20,128,32
mav 12,132,36
R 52,48128
mav 16,136,36
mav 20,140,36
s_waitcnt lgkmcnt(2)
mav 12,144,40
R 24,49152
mav 16,148,40
mav 20,152,40
mav 12,156,44
R 28,50176
mav 16,160,44
mav 20,164,44
s_waitcnt lgkmcnt(2)
mav 12,168,48
R 32,51200
mav 16,172,48
mav 20,176,48
mav 12,180,52
R 36,52224
mav 16,184,52
mav 20,188,52
s_waitcnt lgkmcnt(2)
mav 12,192,24
R 40,53248
mav 16,196,24
mav 20,200,24
mav 12,204,28
R 44,54272
mav 16,208,28
mav 20,212,28
s_waitcnt lgkmcnt(2)
mav 12,216,32
R 48,55296
mav 16,220,32
mav 20,224,32
mav 12,228,36
R 52,56320
mav 16,232,36
mav 20,236,36
s_waitcnt lgkmcnt(2)
mav 12,240,40
R 24,57344
mav 16,244,40
mav 20,248,40
mav 12,252,44
R 28,58368
mvv 16,128,44
v_add_u32 v70, 1, v70
mvv 20,132,44
s_waitcnt lgkmcnt(2)
mvv 12,136,48
R 32,59392
mvv 16,140,48
mvv 20,144,48
mvv 12,148,52
R 36,60416
mvv 16,152,52
s_waitcnt vmcnt(0)
buffer_store_dwordx2 v[70:71], v81, s[36:39], s52 offen sc1
mvv 20,156,52
s_waitcnt lgkmcnt(2)
mvv 12,160,24
R 40,61440
mvv 16,164,24
mvv 20,168,24
mvv 12,172,28
R 44,62464
mvv 16,176,28
mvv 20,180,28
s_waitcnt lgkmcnt(2)
mvv 12,184,32
R 48,63488
mvv 16,188,32
mvv 20,192,32
mvv 12,196,36
R 52,64512
mvv 16,200,36
s_add_u32 s49, s49, 346112
mvv 20,204,36
s_barrier
s_waitcnt lgkmcnt(2)
mvv 12,208,40
mvv 16,212,40
mvv 20,216,40
mvv 12,220,44
mvv 16,224,44
mvv 20,228,44
s_waitcnt lgkmcnt(0)
mvv 12,232,48
mvv 16,236,48
mvv 20,240,48
mvv 12,244,52
mvv 16,248,52
mvv 20,252,52
s_nop 5
v_cvt_pk_bf16_f32 v62, v12, v13
v_cvt_pk_bf16_f32 v63, v14, v15
v_cvt_pk_bf16_f32 v64, v16, v17
v_cvt_pk_bf16_f32 v65, v18, v19
buffer_store_dwordx4 v[62:65], v83, s[36:39], s49 offen sc1
v_cvt_pk_bf16_f32 v66, v20, v21
v_cvt_pk_bf16_f32 v67, v22, v23
v_cvt_pk_bf16_f32 v68, v20, v21
v_cvt_pk_bf16_f32 v69, v22, v23
s_nop 0
v_permlane16_swap_b32 v66, v68
v_permlane16_swap_b32 v67, v69
buffer_store_dwordx4 v[66:69], v84, s[36:39], s49 offen sc1
v_mul_f32 v73, v12, v12
v_add_f32 v72, v13, v12
v_fmac_f32 v73, v13, v13
v_add_f32 v72, v14, v72
v_fmac_f32 v73, v14, v14
v_add_f32 v72, v15, v72
v_fmac_f32 v73, v15, v15
v_add_f32 v72, v16, v72
v_fmac_f32 v73, v16, v16
v_add_f32 v72, v17, v72
v_fmac_f32 v73, v17, v17
v_add_f32 v72, v18, v72
v_fmac_f32 v73, v18, v18
v_add_f32 v72, v19, v72
v_fmac_f32 v73, v19, v19
v_add_f32 v72, v20, v72
v_fmac_f32 v73, v20, v20
v_add_f32 v72, v21, v72
v_fmac_f32 v73, v21, v21
v_add_f32 v72, v22, v72
v_fmac_f32 v73, v22, v22
v_add_f32 v72, v23, v72
v_fmac_f32 v73, v23, v23
s_nop 1
v_permlane32_swap_b32 v72, v73
v_add_f32 v72, v72, v73
ds_write_b32 v91, v72 offset:1024
v_add_u32 v70, 37, v70
s_waitcnt vmcnt(0)
buffer_store_dwordx2 v[70:71], v81, s[36:39], s52 offen sc1
s_add_u32 s49, s49, 346112
s_waitcnt lgkmcnt(0)
s_barrier
s_endpgm
CO0_1:
s_cmp_eq_u32 s61, 0
s_cbranch_scc1 BO0_1
s_sub_u32 s61, s61, 1
s_sleep 8
buffer_load_dwordx2 v[74:75], v87, s[36:39], 0 offen sc1
s_waitcnt vmcnt(0)
ds_write_b32 v82, v74
v_sub_u32 v74, v74, v88
v_xor_b32 v75, v75, v71
v_cmp_lt_u64 vcc, s[62:63], v[74:75]
s_waitcnt lgkmcnt(0)
s_cbranch_vccnz CO0_1
s_branch BO0_1
CO0_2:
s_mov_b32 s58, m0
s_mov_b32 m0, 142336
DO0_2:
s_cmp_eq_u32 s61, 0
s_cbranch_scc1 EO0_2
s_sub_u32 s61, s61, 1
buffer_load_dword v87, s[36:39], 0 offen sc1 lds
s_sleep 1
ds_read_b32 v74, v82
s_waitcnt lgkmcnt(0)
v_cmp_gt_i32 vcc, v88, v74
s_cbranch_vccnz DO0_2
EO0_2:
s_mov_b32 m0, s58
s_branch BO0_2
CO0_3:
s_mov_b32 s58, m0
s_mov_b32 m0, 142336
DO0_3:
s_cmp_eq_u32 s61, 0
s_cbranch_scc1 EO0_3
s_sub_u32 s61, s61, 1
buffer_load_dword v87, s[36:39], 0 offen sc1 lds
s_sleep 1
ds_read_b32 v74, v82
s_waitcnt lgkmcnt(0)
v_cmp_gt_i32 vcc, v88, v74
s_cbranch_vccnz DO0_3
EO0_3:
s_mov_b32 m0, s58
s_branch BO0_3
CO0_4:
s_mov_b32 s58, m0
s_mov_b32 m0, 142336
DO0_4:
s_cmp_eq_u32 s61, 0
s_cbranch_scc1 EO0_4
s_sub_u32 s61, s61, 1
)"
R"(
buffer_load_dword v87, s[36:39], 0 offen sc1 lds
s_sleep 1
ds_read_b32 v74, v82
s_waitcnt lgkmcnt(0)
v_cmp_gt_i32 vcc, v88, v74
s_cbranch_vccnz DO0_4
EO0_4:
s_mov_b32 m0, s58
s_branch BO0_4
CO0_6:
s_mov_b32 s58, m0
s_mov_b32 m0, 142336
DO0_6:
s_cmp_eq_u32 s61, 0
s_cbranch_scc1 EO0_6
s_sub_u32 s61, s61, 1
buffer_load_dword v87, s[36:39], 0 offen sc1 lds
s_sleep 1
ds_read_b32 v74, v82
s_waitcnt lgkmcnt(0)
v_cmp_gt_i32 vcc, v88, v74
s_cbranch_vccnz DO0_6
EO0_6:
s_mov_b32 m0, s58
s_branch BO0_6
CO0_7:
s_mov_b32 s58, m0
s_mov_b32 m0, 142336
DO0_7:
s_cmp_eq_u32 s61, 0
s_cbranch_scc1 EO0_7
s_sub_u32 s61, s61, 1
buffer_load_dword v87, s[36:39], 0 offen sc1 lds
s_sleep 1
ds_read_b32 v74, v82
s_waitcnt lgkmcnt(0)
v_cmp_gt_i32 vcc, v88, v74
s_cbranch_vccnz DO0_7
EO0_7:
s_mov_b32 m0, s58
s_branch BO0_7
LvO1:
s_barrier
buffer_load_dwordx4 v[56:59], v89, s[36:39], s49 offen sc1
buffer_load_dwordx2 v[60:61], v90, s[36:39], s49 offen sc1
s_add_u32 s49, s49, 346112
s_add_u32 m0, s50, 0
s_nop 0
D 85,0
D 85,1024
D 85,2048
D 85,3072
s_add_u32 m0, s50, 4096
s_nop 0
D 86,0
D 86,1024
D 86,2048
D 86,3072
s_waitcnt vmcnt(9)
v_lshlrev_b32 v0, 16, v56
v_and_b32 v1, 0xffff0000, v56
v_lshlrev_b32 v2, 16, v57
v_and_b32 v3, 0xffff0000, v57
v_lshlrev_b32 v4, 16, v58
v_and_b32 v5, 0xffff0000, v58
v_lshlrev_b32 v6, 16, v59
v_and_b32 v7, 0xffff0000, v59
s_waitcnt vmcnt(8)
v_lshlrev_b32 v8, 16, v60
v_and_b32 v9, 0xffff0000, v60
v_lshlrev_b32 v10, 16, v61
v_and_b32 v11, 0xffff0000, v61
buffer_load_dwordx4 v[56:59], v89, s[36:39], s49 offen sc1
buffer_load_dwordx2 v[60:61], v90, s[36:39], s49 offen sc1
s_add_u32 s49, s49, 346112
s_waitcnt vmcnt(2)
s_barrier
s_add_u32 m0, s50, 32768
R 24,0
D 85,0
R 28,1024
D 85,1024
R 32,2048
R 36,3072
D 85,2048
s_waitcnt lgkmcnt(2)
mav 0,0,24
R 40,4096
mav 4,4,24
mav 8,8,24
D 85,3072
mav 0,12,28
R 44,5120
mav 4,16,28
mav 8,20,28
s_add_u32 m0, s50, 36864
s_waitcnt lgkmcnt(2)
mav 0,24,32
R 48,6144
mav 4,28,32
D 86,0
mav 8,32,32
mav 0,36,36
R 52,7168
mav 4,40,36
mav 8,44,36
D 86,1024
s_waitcnt lgkmcnt(2)
mav 0,48,40
R 24,8192
mav 4,52,40
mav 8,56,40
mav 0,60,44
R 28,9216
mav 4,64,44
D 86,2048
mav 8,68,44
s_waitcnt lgkmcnt(2)
mav 0,72,48
R 32,10240
mav 4,76,48
mav 8,80,48
D 86,3072
mav 0,84,52
R 36,11264
mav 4,88,52
mav 8,92,52
s_waitcnt lgkmcnt(2)
mav 0,96,24
R 40,12288
mav 4,100,24
mav 8,104,24
mav 0,108,28
R 44,13312
mav 4,112,28
mav 8,116,28
s_waitcnt lgkmcnt(2)
mav 0,120,32
R 48,14336
mav 4,124,32
mav 8,128,32
mav 0,132,36
R 52,15360
mav 4,136,36
mav 8,140,36
s_waitcnt lgkmcnt(2)
mav 0,144,40
R 24,16384
mav 4,148,40
mav 8,152,40
mav 0,156,44
R 28,17408
mav 4,160,44
mav 8,164,44
s_waitcnt lgkmcnt(2)
mav 0,168,48
R 32,18432
mav 4,172,48
mav 8,176,48
mav 0,180,52
R 36,19456
mav 4,184,52
mav 8,188,52
s_waitcnt lgkmcnt(2)
mav 0,192,24
R 40,20480
mav 4,196,24
mav 8,200,24
s_waitcnt vmcnt(9)
v_lshlrev_b32 v12, 16, v56
mav 0,204,28
R 44,21504
v_and_b32 v13, 0xffff0000, v56
mav 4,208,28
v_lshlrev_b32 v14, 16, v57
v_and_b32 v15, 0xffff0000, v57
mav 8,212,28
v_lshlrev_b32 v16, 16, v58
s_waitcnt lgkmcnt(2)
mav 0,216,32
R 48,22528
v_and_b32 v17, 0xffff0000, v58
mav 4,220,32
v_lshlrev_b32 v18, 16, v59
v_and_b32 v19, 0xffff0000, v59
mav 8,224,32
s_waitcnt vmcnt(8)
v_lshlrev_b32 v20, 16, v60
mav 0,228,36
R 52,23552
v_and_b32 v21, 0xffff0000, v60
mav 4,232,36
v_lshlrev_b32 v22, 16, v61
v_and_b32 v23, 0xffff0000, v61
mav 8,236,36
s_waitcnt lgkmcnt(2)
mav 0,240,40
R 24,24576
mav 4,244,40
mav 8,248,40
mav 0,252,44
R 28,25600
mvv 4,128,44
buffer_load_dwordx4 v[56:59], v89, s[36:39], s49 offen sc1
mvv 8,132,44
s_waitcnt lgkmcnt(2)
mvv 0,136,48
R 32,26624
mvv 4,140,48
mvv 8,144,48
buffer_load_dwordx2 v[60:61], v90, s[36:39], s49 offen sc1
mvv 0,148,52
R 36,27648
mvv 4,152,52
mvv 8,156,52
s_waitcnt lgkmcnt(2)
mvv 0,160,24
R 40,28672
mvv 4,164,24
mvv 8,168,24
mvv 0,172,28
R 44,29696
mvv 4,176,28
mvv 8,180,28
s_waitcnt lgkmcnt(2)
mvv 0,184,32
R 48,30720
s_xor_b32 s50, s50, 0x10000
mvv 4,188,32
mvv 8,192,32
mvv 0,196,36
R 52,31744
mvv 4,200,36
s_add_u32 s49, s49, 346112
mvv 8,204,36
s_waitcnt vmcnt(2)
s_barrier
s_waitcnt lgkmcnt(2)
mvv 0,208,40
R 24,32768
s_add_u32 m0, s50, 0
mvv 4,212,40
D 85,0
mvv 8,216,40
mvv 0,220,44
R 28,33792
mvv 4,224,44
mvv 8,228,44
D 85,1024
s_waitcnt lgkmcnt(2)
mvv 0,232,48
R 32,34816
mvv 4,236,48
mvv 8,240,48
mvv 0,244,52
R 36,35840
mvv 4,248,52
D 85,2048
mvv 8,252,52
s_waitcnt lgkmcnt(2)
mav 12,0,24
R 40,36864
mav 16,4,24
v_cvt_pk_bf16_f32 v62, v0, v1
v_cvt_pk_bf16_f32 v63, v2, v3
mav 20,8,24
D 85,3072
v_cvt_pk_bf16_f32 v64, v4, v5
mav 12,12,28
R 44,37888
v_cvt_pk_bf16_f32 v65, v6, v7
mav 16,16,28
v_cvt_pk_bf16_f32 v66, v8, v9
v_cvt_pk_bf16_f32 v67, v10, v11
mav 20,20,28
s_add_u32 m0, s50, 4096
s_waitcnt lgkmcnt(2)
mav 12,24,32
R 48,38912
buffer_store_dwordx4 v[62:65], v83, s[36:39], s49 offen sc1
mav 16,28,32
v_cvt_pk_bf16_f32 v68, v8, v9
v_cvt_pk_bf16_f32 v69, v10, v11
mav 20,32,32
v_permlane16_swap_b32 v66, v68
v_permlane16_swap_b32 v67, v69
mav 12,36,36
R 52,39936
v_mul_f32 v73, v0, v0
mav 16,40,36
buffer_store_dwordx4 v[66:69], v84, s[36:39], s49 offen sc1
v_add_f32 v72, v1, v0
mav 20,44,36
v_fmac_f32 v73, v1, v1
s_waitcnt lgkmcnt(2)
mav 12,48,40
R 24,40960
v_add_f32 v72, v2, v72
mav 16,52,40
v_fmac_f32 v73, v2, v2
v_add_f32 v72, v3, v72
mav 20,56,40
D 86,0
v_fmac_f32 v73, v3, v3
mav 12,60,44
R 28,41984
v_add_f32 v72, v4, v72
mav 16,64,44
v_fmac_f32 v73, v4, v4
v_add_f32 v72, v5, v72
mav 20,68,44
v_fmac_f32 v73, v5, v5
s_waitcnt lgkmcnt(2)
mav 12,72,48
R 32,43008
D 86,1024
mav 16,76,48
v_add_f32 v72, v6, v72
v_fmac_f32 v73, v6, v6
mav 20,80,48
v_add_f32 v72, v7, v72
v_fmac_f32 v73, v7, v7
mav 12,84,52
R 36,44032
v_add_f32 v72, v8, v72
mav 16,88,52
D 86,2048
v_fmac_f32 v73, v8, v8
mav 20,92,52
v_add_f32 v72, v9, v72
s_waitcnt lgkmcnt(2)
mav 12,96,24
R 40,45056
v_fmac_f32 v73, v9, v9
mav 16,100,24
v_add_f32 v72, v10, v72
v_fmac_f32 v73, v10, v10
mav 20,104,24
D 86,3072
v_add_f32 v72, v11, v72
mav 12,108,28
R 44,46080
v_fmac_f32 v73, v11, v11
mav 16,112,28
mav 20,116,28
v_permlane32_swap_b32 v72, v73
s_waitcnt lgkmcnt(2)
mav 12,120,32
R 48,47104
v_add_f32 v72, v72, v73
mav 16,124,32
ds_write_b32 v91, v72 offset:0
mav 20,128,32
mav 12,132,36
R 52,48128
mav 16,136,36
mav 20,140,36
s_waitcnt lgkmcnt(3)
mav 12,144,40
R 24,49152
mav 16,148,40
mav 20,152,40
mav 12,156,44
R 28,50176
mav 16,160,44
mav 20,164,44
s_waitcnt lgkmcnt(2)
mav 12,168,48
R 32,51200
mav 16,172,48
mav 20,176,48
mav 12,180,52
R 36,52224
mav 16,184,52
mav 20,188,52
s_waitcnt lgkmcnt(2)
mav 12,192,24
R 40,53248
mav 16,196,24
mav 20,200,24
s_waitcnt vmcnt(11)
v_lshlrev_b32 v0, 16, v56
mav 12,204,28
R 44,54272
v_and_b32 v1, 0xffff0000, v56
mav 16,208,28
v_lshlrev_b32 v2, 16, v57
v_and_b32 v3, 0xffff0000, v57
mav 20,212,28
v_lshlrev_b32 v4, 16, v58
s_waitcnt lgkmcnt(2)
mav 12,216,32
R 48,55296
v_and_b32 v5, 0xffff0000, v58
mav 16,220,32
v_lshlrev_b32 v6, 16, v59
v_and_b32 v7, 0xffff0000, v59
mav 20,224,32
s_waitcnt vmcnt(10)
v_lshlrev_b32 v8, 16, v60
mav 12,228,36
R 52,56320
v_and_b32 v9, 0xffff0000, v60
mav 16,232,36
v_lshlrev_b32 v10, 16, v61
v_and_b32 v11, 0xffff0000, v61
mav 20,236,36
s_waitcnt lgkmcnt(2)
mav 12,240,40
R 24,57344
mav 16,244,40
mav 20,248,40
mav 12,252,44
R 28,58368
mvv 16,128,44
buffer_load_dwordx4 v[56:59], v89, s[36:39], s49 offen sc1
v_add_u32 v70, 1, v70
mvv 20,132,44
s_waitcnt lgkmcnt(2)
mvv 12,136,48
R 32,59392
mvv 16,140,48
mvv 20,144,48
buffer_load_dwordx2 v[60:61], v90, s[36:39], s49 offen sc1
mvv 12,148,52
R 36,60416
mvv 16,152,52
s_waitcnt vmcnt(6)
buffer_store_dwordx2 v[70:71], v81, s[36:39], s52 offen sc1
mvv 20,156,52
s_waitcnt lgkmcnt(2)
mvv 12,160,24
R 40,61440
mvv 16,164,24
mvv 20,168,24
mvv 12,172,28
R 44,62464
mvv 16,176,28
mvv 20,180,28
s_waitcnt lgkmcnt(2)
mvv 12,184,32
R 48,63488
mvv 16,188,32
mvv 20,192,32
mvv 12,196,36
R 52,64512
v_xor_b32 v80, 0x10000, v80
mvv 16,200,36
s_add_u32 s49, s49, 346112
mvv 20,204,36
s_waitcnt vmcnt(3)
s_barrier
s_waitcnt lgkmcnt(2)
mvv 12,208,40
R 24,0
s_add_u32 m0, s50, 32768
mvv 16,212,40
ds_read_b128 v[76:79], v92 offset:0
D 85,0
mvv 20,216,40
mvv 12,220,44
R 28,1024
mvv 16,224,44
mvv 20,228,44
D 85,1024
s_waitcnt lgkmcnt(3)
mvv 12,232,48
R 32,2048
mvv 16,236,48
mvv 20,240,48
s_waitcnt lgkmcnt(2)
v_add_f32 v76, v76, v77
mvv 12,244,52
R 36,3072
v_add_f32 v78, v78, v79
mvv 16,248,52
D 85,2048
v_add_f32 v76, v76, v78
mvv 20,252,52
mav 0,0,24
R 40,4096
v_add_f32_dpp v77, v76, v76 quad_perm:[1,0,3,2] row_mask:0xf bank_mask:0xf
mav 4,4,24
buffer_store_dword v77, v93, s[36:39], s49 offen sc1
v_cvt_pk_bf16_f32 v62, v12, v13
mav 8,8,24
D 85,3072
s_waitcnt lgkmcnt(2)
mav 0,12,28
R 44,5120
v_cvt_pk_bf16_f32 v63, v14, v15
mav 4,16,28
v_cvt_pk_bf16_f32 v64, v16, v17
v_cvt_pk_bf16_f32 v65, v18, v19
mav 8,20,28
s_add_u32 m0, s50, 36864
v_cvt_pk_bf16_f32 v66, v20, v21
mav 0,24,32
R 48,6144
buffer_store_dwordx4 v[62:65], v83, s[36:39], s49 offen sc1
mav 4,28,32
v_cvt_pk_bf16_f32 v67, v22, v23
v_cvt_pk_bf16_f32 v68, v20, v21
mav 8,32,32
v_cvt_pk_bf16_f32 v69, v22, v23
s_waitcnt lgkmcnt(2)
mav 0,36,36
R 52,7168
v_permlane16_swap_b32 v66, v68
mav 4,40,36
D 86,0
v_permlane16_swap_b32 v67, v69
mav 8,44,36
v_mul_f32 v73, v12, v12
v_add_f32 v72, v13, v12
mav 0,48,40
R 24,8192
v_fmac_f32 v73, v13, v13
mav 4,52,40
v_add_f32 v72, v14, v72
v_fmac_f32 v73, v14, v14
mav 8,56,40
buffer_store_dwordx4 v[66:69], v84, s[36:39], s49 offen sc1
s_waitcnt lgkmcnt(2)
mav 0,60,44
R 28,9216
v_add_f32 v72, v15, v72
mav 4,64,44
v_fmac_f32 v73, v15, v15
v_add_f32 v72, v16, v72
mav 8,68,44
v_fmac_f32 v73, v16, v16
v_add_f32 v72, v17, v72
mav 0,72,48
R 32,10240
D 86,1024
mav 4,76,48
v_fmac_f32 v73, v17, v17
v_add_f32 v72, v18, v72
mav 8,80,48
v_fmac_f32 v73, v18, v18
s_waitcnt lgkmcnt(2)
mav 0,84,52
R 36,11264
v_add_f32 v72, v19, v72
mav 4,88,52
D 86,2048
v_fmac_f32 v73, v19, v19
mav 8,92,52
v_add_f32 v72, v20, v72
v_fmac_f32 v73, v20, v20
mav 0,96,24
R 40,12288
v_add_f32 v72, v21, v72
mav 4,100,24
v_fmac_f32 v73, v21, v21
v_add_f32 v72, v22, v72
mav 8,104,24
D 86,3072
s_waitcnt lgkmcnt(2)
mav 0,108,28
R 44,13312
v_fmac_f32 v73, v22, v22
mav 4,112,28
v_add_f32 v72, v23, v72
v_fmac_f32 v73, v23, v23
mav 8,116,28
mav 0,120,32
R 48,14336
v_permlane32_swap_b32 v72, v73
mav 4,124,32
v_add_f32 v72, v72, v73
ds_write_b32 v91, v72 offset:1024
mav 8,128,32
s_waitcnt lgkmcnt(3)
mav 0,132,36
R 52,15360
mav 4,136,36
mav 8,140,36
mav 0,144,40
R 24,16384
mav 4,148,40
mav 8,152,40
s_waitcnt lgkmcnt(2)
mav 0,156,44
R 28,17408
mav 4,160,44
mav 8,164,44
mav 0,168,48
R 32,18432
mav 4,172,48
mav 8,176,48
s_waitcnt lgkmcnt(2)
mav 0,180,52
R 36,19456
mav 4,184,52
mav 8,188,52
mav 0,192,24
R 40,20480
mav 4,196,24
mav 8,200,24
s_waitcnt lgkmcnt(2)
mav 0,204,28
R 44,21504
mav 4,208,28
s_waitcnt vmcnt(13)
v_lshlrev_b32 v12, 16, v56
mav 8,212,28
v_and_b32 v13, 0xffff0000, v56
v_lshlrev_b32 v14, 16, v57
mav 0,216,32
R 48,22528
v_and_b32 v15, 0xffff0000, v57
mav 4,220,32
v_lshlrev_b32 v16, 16, v58
v_and_b32 v17, 0xffff0000, v58
mav 8,224,32
v_lshlrev_b32 v18, 16, v59
s_waitcnt lgkmcnt(2)
mav 0,228,36
R 52,23552
v_and_b32 v19, 0xffff0000, v59
mav 4,232,36
s_waitcnt vmcnt(12)
v_lshlrev_b32 v20, 16, v60
mav 8,236,36
v_and_b32 v21, 0xffff0000, v60
v_lshlrev_b32 v22, 16, v61
mav 0,240,40
R 24,24576
v_and_b32 v23, 0xffff0000, v61
mav 4,244,40
mav 8,248,40
s_waitcnt lgkmcnt(2)
mav 0,252,44
R 28,25600
mvv 4,128,44
buffer_load_dwordx4 v[56:59], v89, s[36:39], s49 offen sc1
v_add_u32 v70, 1, v70
mvv 8,132,44
mvv 0,136,48
R 32,26624
mvv 4,140,48
mvv 8,144,48
buffer_load_dwordx2 v[60:61], v90, s[36:39], s49 offen sc1
s_waitcnt lgkmcnt(2)
mvv 0,148,52
R 36,27648
mvv 4,152,52
s_waitcnt vmcnt(5)
buffer_store_dwordx2 v[70:71], v81, s[36:39], s52 offen sc1
mvv 8,156,52
mvv 0,160,24
R 40,28672
mvv 4,164,24
mvv 8,168,24
s_waitcnt lgkmcnt(2)
mvv 0,172,28
R 44,29696
mvv 4,176,28
mvv 8,180,28
mvv 0,184,32
R 48,30720
s_xor_b32 s50, s50, 0x10000
mvv 4,188,32
mvv 8,192,32
s_waitcnt lgkmcnt(2)
mvv 0,196,36
R 52,31744
mvv 4,200,36
s_add_u32 s49, s49, 346112
mvv 8,204,36
s_waitcnt vmcnt(3)
s_barrier
mvv 0,208,40
R 24,32768
s_add_u32 m0, s50, 0
mvv 4,212,40
ds_read_b128 v[76:79], v92 offset:1024
D 85,0
mvv 8,216,40
s_waitcnt lgkmcnt(3)
mvv 0,220,44
R 28,33792
mvv 4,224,44
mvv 8,228,44
D 85,1024
mvv 0,232,48
R 32,34816
mvv 4,236,48
mvv 8,240,48
s_waitcnt lgkmcnt(2)
mvv 0,244,52
R 36,35840
v_add_f32 v76, v76, v77
mvv 4,248,52
D 85,2048
v_add_f32 v78, v78, v79
mvv 8,252,52
v_add_f32 v76, v76, v78
mav 12,0,24
R 40,36864
v_add_f32_dpp v77, v76, v76 quad_perm:[1,0,3,2] row_mask:0xf bank_mask:0xf
mav 16,4,24
buffer_store_dword v77, v93, s[36:39], s49 offen sc1
v_cvt_pk_bf16_f32 v62, v0, v1
mav 20,8,24
D 85,3072
s_waitcnt lgkmcnt(2)
mav 12,12,28
R 44,37888
v_cvt_pk_bf16_f32 v63, v2, v3
mav 16,16,28
v_cvt_pk_bf16_f32 v64, v4, v5
v_cvt_pk_bf16_f32 v65, v6, v7
mav 20,20,28
s_add_u32 m0, s50, 4096
v_cvt_pk_bf16_f32 v66, v8, v9
mav 12,24,32
R 48,38912
buffer_store_dwordx4 v[62:65], v83, s[36:39], s49 offen sc1
mav 16,28,32
v_cvt_pk_bf16_f32 v67, v10, v11
v_cvt_pk_bf16_f32 v68, v8, v9
mav 20,32,32
v_cvt_pk_bf16_f32 v69, v10, v11
s_waitcnt lgkmcnt(2)
mav 12,36,36
R 52,39936
v_permlane16_swap_b32 v66, v68
mav 16,40,36
D 86,0
v_permlane16_swap_b32 v67, v69
mav 20,44,36
v_mul_f32 v73, v0, v0
v_add_f32 v72, v1, v0
mav 12,48,40
R 24,40960
v_fmac_f32 v73, v1, v1
mav 16,52,40
v_add_f32 v72, v2, v72
v_fmac_f32 v73, v2, v2
mav 20,56,40
buffer_store_dwordx4 v[66:69], v84, s[36:39], s49 offen sc1
s_waitcnt lgkmcnt(2)
mav 12,60,44
R 28,41984
v_add_f32 v72, v3, v72
mav 16,64,44
v_fmac_f32 v73, v3, v3
v_add_f32 v72, v4, v72
mav 20,68,44
v_fmac_f32 v73, v4, v4
v_add_f32 v72, v5, v72
mav 12,72,48
R 32,43008
D 86,1024
mav 16,76,48
v_fmac_f32 v73, v5, v5
v_add_f32 v72, v6, v72
mav 20,80,48
v_fmac_f32 v73, v6, v6
s_waitcnt lgkmcnt(2)
mav 12,84,52
R 36,44032
v_add_f32 v72, v7, v72
mav 16,88,52
D 86,2048
v_fmac_f32 v73, v7, v7
mav 20,92,52
v_add_f32 v72, v8, v72
v_fmac_f32 v73, v8, v8
mav 12,96,24
R 40,45056
v_add_f32 v72, v9, v72
mav 16,100,24
v_fmac_f32 v73, v9, v9
v_add_f32 v72, v10, v72
mav 20,104,24
D 86,3072
s_waitcnt lgkmcnt(2)
mav 12,108,28
R 44,46080
v_fmac_f32 v73, v10, v10
mav 16,112,28
v_add_f32 v72, v11, v72
v_fmac_f32 v73, v11, v11
mav 20,116,28
mav 12,120,32
R 48,47104
v_permlane32_swap_b32 v72, v73
mav 16,124,32
v_add_f32 v72, v72, v73
ds_write_b32 v91, v72 offset:0
mav 20,128,32
s_waitcnt lgkmcnt(3)
mav 12,132,36
R 52,48128
mav 16,136,36
mav 20,140,36
mav 12,144,40
R 24,49152
mav 16,148,40
mav 20,152,40
s_waitcnt lgkmcnt(2)
mav 12,156,44
R 28,50176
mav 16,160,44
mav 20,164,44
mav 12,168,48
R 32,51200
mav 16,172,48
mav 20,176,48
s_waitcnt lgkmcnt(2)
mav 12,180,52
R 36,52224
mav 16,184,52
mav 20,188,52
mav 12,192,24
R 40,53248
mav 16,196,24
mav 20,200,24
s_waitcnt lgkmcnt(2)
mav 12,204,28
R 44,54272
mav 16,208,28
s_waitcnt vmcnt(13)
v_lshlrev_b32 v0, 16, v56
mav 20,212,28
v_and_b32 v1, 0xffff0000, v56
v_lshlrev_b32 v2, 16, v57
mav 12,216,32
R 48,55296
v_and_b32 v3, 0xffff0000, v57
mav 16,220,32
v_lshlrev_b32 v4, 16, v58
v_and_b32 v5, 0xffff0000, v58
mav 20,224,32
v_lshlrev_b32 v6, 16, v59
s_waitcnt lgkmcnt(2)
mav 12,228,36
R 52,56320
v_and_b32 v7, 0xffff0000, v59
mav 16,232,36
s_waitcnt vmcnt(12)
v_lshlrev_b32 v8, 16, v60
mav 20,236,36
v_and_b32 v9, 0xffff0000, v60
v_lshlrev_b32 v10, 16, v61
mav 12,240,40
R 24,57344
v_and_b32 v11, 0xffff0000, v61
mav 16,244,40
mav 20,248,40
s_waitcnt lgkmcnt(2)
mav 12,252,44
R 28,58368
mvv 16,128,44
buffer_load_dwordx4 v[56:59], v89, s[36:39], s49 offen sc1
v_add_u32 v70, 1, v70
mvv 20,132,44
mvv 12,136,48
R 32,59392
mvv 16,140,48
mvv 20,144,48
buffer_load_dwordx2 v[60:61], v90, s[36:39], s49 offen sc1
s_waitcnt lgkmcnt(2)
mvv 12,148,52
R 36,60416
mvv 16,152,52
s_waitcnt vmcnt(5)
buffer_store_dwordx2 v[70:71], v81, s[36:39], s52 offen sc1
mvv 20,156,52
mvv 12,160,24
R 40,61440
mvv 16,164,24
mvv 20,168,24
s_waitcnt lgkmcnt(2)
mvv 12,172,28
R 44,62464
mvv 16,176,28
mvv 20,180,28
mvv 12,184,32
R 48,63488
mvv 16,188,32
mvv 20,192,32
s_waitcnt lgkmcnt(2)
mvv 12,196,36
R 52,64512
v_xor_b32 v80, 0x10000, v80
mvv 16,200,36
s_add_u32 s49, s49, 346112
mvv 20,204,36
s_waitcnt vmcnt(3)
s_barrier
mvv 12,208,40
R 24,0
s_add_u32 m0, s50, 32768
mvv 16,212,40
ds_read_b128 v[76:79], v92 offset:0
D 85,0
mvv 20,216,40
s_waitcnt lgkmcnt(3)
mvv 12,220,44
R 28,1024
mvv 16,224,44
mvv 20,228,44
D 85,1024
mvv 12,232,48
R 32,2048
mvv 16,236,48
mvv 20,240,48
s_waitcnt lgkmcnt(2)
mvv 12,244,52
R 36,3072
v_add_f32 v76, v76, v77
mvv 16,248,52
D 85,2048
v_add_f32 v78, v78, v79
mvv 20,252,52
s_mov_b32 s60, -29
.p2align 6
LO1_1:
mav 0,0,24
R 40,4096
v_add_f32 v76, v76, v78
mav 4,4,24
v_cvt_pk_bf16_f32 v62, v12, v13
v_add_f32_dpp v77, v76, v76 quad_perm:[1,0,3,2] row_mask:0xf bank_mask:0xf
mav 8,8,24
buffer_store_dword v77, v93, s[36:39], s49 offen sc1
s_waitcnt lgkmcnt(2)
mav 0,12,28
R 44,5120
D 85,3072
mav 4,16,28
v_cvt_pk_bf16_f32 v63, v14, v15
v_cvt_pk_bf16_f32 v64, v16, v17
mav 8,20,28
s_add_u32 m0, s50, 36864
v_cvt_pk_bf16_f32 v65, v18, v19
mav 0,24,32
R 48,6144
v_cvt_pk_bf16_f32 v66, v20, v21
mav 4,28,32
buffer_store_dwordx4 v[62:65], v83, s[36:39], s49 offen sc1
v_cvt_pk_bf16_f32 v67, v22, v23
mav 8,32,32
v_cvt_pk_bf16_f32 v68, v20, v21
s_waitcnt lgkmcnt(2)
mav 0,36,36
R 52,7168
v_cvt_pk_bf16_f32 v69, v22, v23
mav 4,40,36
v_permlane16_swap_b32 v66, v68
v_permlane16_swap_b32 v67, v69
mav 8,44,36
buffer_store_dwordx4 v[66:69], v84, s[36:39], s49 offen sc1
v_mul_f32 v73, v12, v12
mav 0,48,40
R 24,8192
v_add_f32 v72, v13, v12
mav 4,52,40
v_fmac_f32 v73, v13, v13
v_add_f32 v72, v14, v72
mav 8,56,40
v_fmac_f32 v73, v14, v14
s_waitcnt lgkmcnt(2)
mav 0,60,44
R 28,9216
D 86,0
mav 4,64,44
v_add_f32 v72, v15, v72
v_fmac_f32 v73, v15, v15
mav 8,68,44
v_add_f32 v72, v16, v72
v_fmac_f32 v73, v16, v16
mav 0,72,48
R 32,10240
v_add_f32 v72, v17, v72
mav 4,76,48
D 86,1024
v_fmac_f32 v73, v17, v17
mav 8,80,48
v_add_f32 v72, v18, v72
s_waitcnt lgkmcnt(2)
mav 0,84,52
R 36,11264
v_fmac_f32 v73, v18, v18
mav 4,88,52
v_add_f32 v72, v19, v72
v_fmac_f32 v73, v19, v19
mav 8,92,52
D 86,2048
v_add_f32 v72, v20, v72
mav 0,96,24
R 40,12288
v_fmac_f32 v73, v20, v20
mav 4,100,24
v_add_f32 v72, v21, v72
v_fmac_f32 v73, v21, v21
mav 8,104,24
v_add_f32 v72, v22, v72
s_waitcnt lgkmcnt(2)
mav 0,108,28
R 44,13312
D 86,3072
mav 4,112,28
v_fmac_f32 v73, v22, v22
v_add_f32 v72, v23, v72
mav 8,116,28
v_fmac_f32 v73, v23, v23
mav 0,120,32
R 48,14336
v_permlane32_swap_b32 v72, v73
mav 4,124,32
v_add_f32 v72, v72, v73
ds_write_b32 v91, v72 offset:1024
mav 8,128,32
s_waitcnt lgkmcnt(3)
mav 0,132,36
R 52,15360
mav 4,136,36
mav 8,140,36
mav 0,144,40
R 24,16384
mav 4,148,40
mav 8,152,40
s_waitcnt lgkmcnt(2)
mav 0,156,44
R 28,17408
mav 4,160,44
mav 8,164,44
mav 0,168,48
R 32,18432
mav 4,172,48
mav 8,176,48
s_waitcnt lgkmcnt(2)
mav 0,180,52
R 36,19456
mav 4,184,52
mav 8,188,52
mav 0,192,24
R 40,20480
mav 4,196,24
mav 8,200,24
s_waitcnt lgkmcnt(2)
mav 0,204,28
R 44,21504
mav 4,208,28
s_waitcnt vmcnt(13)
v_lshlrev_b32 v12, 16, v56
mav 8,212,28
v_and_b32 v13, 0xffff0000, v56
v_lshlrev_b32 v14, 16, v57
mav 0,216,32
R 48,22528
v_and_b32 v15, 0xffff0000, v57
mav 4,220,32
v_lshlrev_b32 v16, 16, v58
v_and_b32 v17, 0xffff0000, v58
mav 8,224,32
v_lshlrev_b32 v18, 16, v59
s_waitcnt lgkmcnt(2)
mav 0,228,36
R 52,23552
v_and_b32 v19, 0xffff0000, v59
mav 4,232,36
s_waitcnt vmcnt(12)
v_lshlrev_b32 v20, 16, v60
mav 8,236,36
v_and_b32 v21, 0xffff0000, v60
v_lshlrev_b32 v22, 16, v61
mav 0,240,40
R 24,24576
v_and_b32 v23, 0xffff0000, v61
mav 4,244,40
mav 8,248,40
s_waitcnt lgkmcnt(2)
mav 0,252,44
R 28,25600
mvv 4,128,44
buffer_load_dwordx4 v[56:59], v89, s[36:39], s49 offen sc1
v_add_u32 v70, 1, v70
mvv 8,132,44
mvv 0,136,48
R 32,26624
mvv 4,140,48
mvv 8,144,48
buffer_load_dwordx2 v[60:61], v90, s[36:39], s49 offen sc1
s_waitcnt lgkmcnt(2)
mvv 0,148,52
R 36,27648
mvv 4,152,52
s_waitcnt vmcnt(6)
buffer_store_dwordx2 v[70:71], v81, s[36:39], s52 offen sc1
mvv 8,156,52
mvv 0,160,24
R 40,28672
mvv 4,164,24
mvv 8,168,24
s_waitcnt lgkmcnt(2)
mvv 0,172,28
R 44,29696
mvv 4,176,28
mvv 8,180,28
mvv 0,184,32
R 48,30720
s_xor_b32 s50, s50, 0x10000
mvv 4,188,32
mvv 8,192,32
s_waitcnt lgkmcnt(2)
mvv 0,196,36
R 52,31744
mvv 4,200,36
s_add_u32 s49, s49, 346112
mvv 8,204,36
s_waitcnt vmcnt(3)
s_barrier
mvv 0,208,40
R 24,32768
s_add_u32 m0, s50, 0
mvv 4,212,40
ds_read_b128 v[76:79], v92 offset:1024
D 85,0
mvv 8,216,40
s_waitcnt lgkmcnt(3)
mvv 0,220,44
R 28,33792
mvv 4,224,44
mvv 8,228,44
D 85,1024
mvv 0,232,48
R 32,34816
mvv 4,236,48
mvv 8,240,48
s_waitcnt lgkmcnt(2)
mvv 0,244,52
R 36,35840
v_add_f32 v76, v76, v77
mvv 4,248,52
D 85,2048
v_add_f32 v78, v78, v79
mvv 8,252,52
v_add_f32 v76, v76, v78
mav 12,0,24
R 40,36864
v_add_f32_dpp v77, v76, v76 quad_perm:[1,0,3,2] row_mask:0xf bank_mask:0xf
mav 16,4,24
buffer_store_dword v77, v93, s[36:39], s49 offen sc1
v_cvt_pk_bf16_f32 v62, v0, v1
mav 20,8,24
D 85,3072
s_waitcnt lgkmcnt(2)
mav 12,12,28
R 44,37888
v_cvt_pk_bf16_f32 v63, v2, v3
mav 16,16,28
v_cvt_pk_bf16_f32 v64, v4, v5
v_cvt_pk_bf16_f32 v65, v6, v7
mav 20,20,28
s_add_u32 m0, s50, 4096
v_cvt_pk_bf16_f32 v66, v8, v9
mav 12,24,32
R 48,38912
buffer_store_dwordx4 v[62:65], v83, s[36:39], s49 offen sc1
mav 16,28,32
v_cvt_pk_bf16_f32 v67, v10, v11
v_cvt_pk_bf16_f32 v68, v8, v9
mav 20,32,32
v_cvt_pk_bf16_f32 v69, v10, v11
s_waitcnt lgkmcnt(2)
mav 12,36,36
R 52,39936
v_permlane16_swap_b32 v66, v68
mav 16,40,36
D 86,0
v_permlane16_swap_b32 v67, v69
mav 20,44,36
v_mul_f32 v73, v0, v0
v_add_f32 v72, v1, v0
mav 12,48,40
R 24,40960
v_fmac_f32 v73, v1, v1
mav 16,52,40
v_add_f32 v72, v2, v72
v_fmac_f32 v73, v2, v2
mav 20,56,40
buffer_store_dwordx4 v[66:69], v84, s[36:39], s49 offen sc1
s_waitcnt lgkmcnt(2)
mav 12,60,44
R 28,41984
v_add_f32 v72, v3, v72
mav 16,64,44
v_fmac_f32 v73, v3, v3
v_add_f32 v72, v4, v72
mav 20,68,44
v_fmac_f32 v73, v4, v4
v_add_f32 v72, v5, v72
mav 12,72,48
R 32,43008
D 86,1024
mav 16,76,48
v_fmac_f32 v73, v5, v5
v_add_f32 v72, v6, v72
mav 20,80,48
v_fmac_f32 v73, v6, v6
s_waitcnt lgkmcnt(2)
mav 12,84,52
R 36,44032
v_add_f32 v72, v7, v72
mav 16,88,52
D 86,2048
v_fmac_f32 v73, v7, v7
mav 20,92,52
v_add_f32 v72, v8, v72
v_fmac_f32 v73, v8, v8
mav 12,96,24
R 40,45056
v_add_f32 v72, v9, v72
mav 16,100,24
v_fmac_f32 v73, v9, v9
v_add_f32 v72, v10, v72
mav 20,104,24
D 86,3072
s_waitcnt lgkmcnt(2)
mav 12,108,28
R 44,46080
v_fmac_f32 v73, v10, v10
mav 16,112,28
v_add_f32 v72, v11, v72
v_fmac_f32 v73, v11, v11
mav 20,116,28
mav 12,120,32
R 48,47104
v_permlane32_swap_b32 v72, v73
mav 16,124,32
v_add_f32 v72, v72, v73
ds_write_b32 v91, v72 offset:0
mav 20,128,32
s_waitcnt lgkmcnt(3)
mav 12,132,36
R 52,48128
mav 16,136,36
mav 20,140,36
mav 12,144,40
R 24,49152
mav 16,148,40
mav 20,152,40
s_waitcnt lgkmcnt(2)
mav 12,156,44
R 28,50176
mav 16,160,44
mav 20,164,44
mav 12,168,48
R 32,51200
mav 16,172,48
mav 20,176,48
s_waitcnt lgkmcnt(2)
mav 12,180,52
R 36,52224
mav 16,184,52
mav 20,188,52
mav 12,192,24
R 40,53248
mav 16,196,24
mav 20,200,24
s_waitcnt lgkmcnt(2)
mav 12,204,28
R 44,54272
mav 16,208,28
s_waitcnt vmcnt(13)
v_lshlrev_b32 v0, 16, v56
mav 20,212,28
v_and_b32 v1, 0xffff0000, v56
v_lshlrev_b32 v2, 16, v57
mav 12,216,32
R 48,55296
v_and_b32 v3, 0xffff0000, v57
mav 16,220,32
v_lshlrev_b32 v4, 16, v58
v_and_b32 v5, 0xffff0000, v58
mav 20,224,32
v_lshlrev_b32 v6, 16, v59
s_waitcnt lgkmcnt(2)
mav 12,228,36
R 52,56320
v_and_b32 v7, 0xffff0000, v59
mav 16,232,36
s_waitcnt vmcnt(12)
v_lshlrev_b32 v8, 16, v60
mav 20,236,36
v_and_b32 v9, 0xffff0000, v60
v_lshlrev_b32 v10, 16, v61
mav 12,240,40
R 24,57344
v_and_b32 v11, 0xffff0000, v61
mav 16,244,40
mav 20,248,40
s_waitcnt lgkmcnt(2)
mav 12,252,44
R 28,58368
mvv 16,128,44
buffer_load_dwordx4 v[56:59], v89, s[36:39], s49 offen sc1
v_add_u32 v70, 1, v70
mvv 20,132,44
mvv 12,136,48
R 32,59392
mvv 16,140,48
mvv 20,144,48
buffer_load_dwordx2 v[60:61], v90, s[36:39], s49 offen sc1
s_waitcnt lgkmcnt(2)
mvv 12,148,52
R 36,60416
mvv 16,152,52
s_waitcnt vmcnt(5)
buffer_store_dwordx2 v[70:71], v81, s[36:39], s52 offen sc1
mvv 20,156,52
mvv 12,160,24
R 40,61440
mvv 16,164,24
mvv 20,168,24
s_waitcnt lgkmcnt(2)
mvv 12,172,28
R 44,62464
mvv 16,176,28
mvv 20,180,28
mvv 12,184,32
R 48,63488
mvv 16,188,32
mvv 20,192,32
s_waitcnt lgkmcnt(2)
mvv 12,196,36
R 52,64512
v_xor_b32 v80, 0x10000, v80
mvv 16,200,36
s_add_u32 s49, s49, 346112
mvv 20,204,36
s_waitcnt vmcnt(3)
s_barrier
mvv 12,208,40
R 24,0
s_add_u32 m0, s50, 32768
mvv 16,212,40
ds_read_b128 v[76:79], v92 offset:0
D 85,0
mvv 20,216,40
s_waitcnt lgkmcnt(3)
mvv 12,220,44
R 28,1024
mvv 16,224,44
mvv 20,228,44
D 85,1024
mvv 12,232,48
R 32,2048
mvv 16,236,48
mvv 20,240,48
s_waitcnt lgkmcnt(2)
mvv 12,244,52
R 36,3072
v_add_f32 v76, v76, v77
mvv 16,248,52
D 85,2048
v_add_f32 v78, v78, v79
mvv 20,252,52
s_add_u32 s60, s60, 1
s_cbranch_scc0 LO1_1
mav 0,0,24
R 40,4096
v_add_f32 v76, v76, v78
mav 4,4,24
v_cvt_pk_bf16_f32 v62, v12, v13
v_add_f32_dpp v77, v76, v76 quad_perm:[1,0,3,2] row_mask:0xf bank_mask:0xf
mav 8,8,24
buffer_store_dword v77, v93, s[36:39], s49 offen sc1
s_waitcnt lgkmcnt(2)
mav 0,12,28
R 44,5120
D 85,3072
mav 4,16,28
v_cvt_pk_bf16_f32 v63, v14, v15
v_cvt_pk_bf16_f32 v64, v16, v17
mav 8,20,28
s_add_u32 m0, s50, 36864
v_cvt_pk_bf16_f32 v65, v18, v19
mav 0,24,32
R 48,6144
v_cvt_pk_bf16_f32 v66, v20, v21
mav 4,28,32
buffer_store_dwordx4 v[62:65], v83, s[36:39], s49 offen sc1
v_cvt_pk_bf16_f32 v67, v22, v23
mav 8,32,32
v_cvt_pk_bf16_f32 v68, v20, v21
s_waitcnt lgkmcnt(2)
mav 0,36,36
R 52,7168
v_cvt_pk_bf16_f32 v69, v22, v23
mav 4,40,36
v_permlane16_swap_b32 v66, v68
v_permlane16_swap_b32 v67, v69
mav 8,44,36
buffer_store_dwordx4 v[66:69], v84, s[36:39], s49 offen sc1
v_mul_f32 v73, v12, v12
mav 0,48,40
R 24,8192
v_add_f32 v72, v13, v12
mav 4,52,40
v_fmac_f32 v73, v13, v13
v_add_f32 v72, v14, v72
mav 8,56,40
v_fmac_f32 v73, v14, v14
s_waitcnt lgkmcnt(2)
mav 0,60,44
R 28,9216
D 86,0
mav 4,64,44
v_add_f32 v72, v15, v72
v_fmac_f32 v73, v15, v15
mav 8,68,44
v_add_f32 v72, v16, v72
v_fmac_f32 v73, v16, v16
mav 0,72,48
R 32,10240
v_add_f32 v72, v17, v72
mav 4,76,48
D 86,1024
v_fmac_f32 v73, v17, v17
mav 8,80,48
v_add_f32 v72, v18, v72
s_waitcnt lgkmcnt(2)
mav 0,84,52
R 36,11264
v_fmac_f32 v73, v18, v18
mav 4,88,52
v_add_f32 v72, v19, v72
v_fmac_f32 v73, v19, v19
mav 8,92,52
D 86,2048
v_add_f32 v72, v20, v72
mav 0,96,24
R 40,12288
v_fmac_f32 v73, v20, v20
mav 4,100,24
v_add_f32 v72, v21, v72
v_fmac_f32 v73, v21, v21
mav 8,104,24
v_add_f32 v72, v22, v72
s_waitcnt lgkmcnt(2)
mav 0,108,28
R 44,13312
D 86,3072
mav 4,112,28
v_fmac_f32 v73, v22, v22
v_add_f32 v72, v23, v72
mav 8,116,28
v_fmac_f32 v73, v23, v23
mav 0,120,32
R 48,14336
v_permlane32_swap_b32 v72, v73
mav 4,124,32
v_add_f32 v72, v72, v73
ds_write_b32 v91, v72 offset:1024
mav 8,128,32
s_waitcnt lgkmcnt(3)
mav 0,132,36
R 52,15360
mav 4,136,36
mav 8,140,36
mav 0,144,40
R 24,16384
mav 4,148,40
mav 8,152,40
s_waitcnt lgkmcnt(2)
mav 0,156,44
R 28,17408
mav 4,160,44
mav 8,164,44
mav 0,168,48
R 32,18432
mav 4,172,48
mav 8,176,48
s_waitcnt lgkmcnt(2)
mav 0,180,52
R 36,19456
mav 4,184,52
mav 8,188,52
mav 0,192,24
R 40,20480
mav 4,196,24
mav 8,200,24
s_waitcnt lgkmcnt(2)
mav 0,204,28
R 44,21504
mav 4,208,28
s_waitcnt vmcnt(13)
v_lshlrev_b32 v12, 16, v56
mav 8,212,28
v_and_b32 v13, 0xffff0000, v56
v_lshlrev_b32 v14, 16, v57
mav 0,216,32
R 48,22528
v_and_b32 v15, 0xffff0000, v57
mav 4,220,32
v_lshlrev_b32 v16, 16, v58
v_and_b32 v17, 0xffff0000, v58
mav 8,224,32
v_lshlrev_b32 v18, 16, v59
s_waitcnt lgkmcnt(2)
mav 0,228,36
R 52,23552
v_and_b32 v19, 0xffff0000, v59
mav 4,232,36
s_waitcnt vmcnt(12)
v_lshlrev_b32 v20, 16, v60
mav 8,236,36
v_and_b32 v21, 0xffff0000, v60
v_lshlrev_b32 v22, 16, v61
mav 0,240,40
R 24,24576
v_and_b32 v23, 0xffff0000, v61
mav 4,244,40
mav 8,248,40
s_waitcnt lgkmcnt(2)
mav 0,252,44
R 28,25600
mvv 4,128,44
v_add_u32 v70, 1, v70
mvv 8,132,44
mvv 0,136,48
R 32,26624
mvv 4,140,48
mvv 8,144,48
s_waitcnt lgkmcnt(2)
mvv 0,148,52
R 36,27648
mvv 4,152,52
s_waitcnt vmcnt(4)
buffer_store_dwordx2 v[70:71], v81, s[36:39], s52 offen sc1
mvv 8,156,52
mvv 0,160,24
R 40,28672
mvv 4,164,24
mvv 8,168,24
s_waitcnt lgkmcnt(2)
mvv 0,172,28
R 44,29696
mvv 4,176,28
mvv 8,180,28
mvv 0,184,32
R 48,30720
mvv 4,188,32
mvv 8,192,32
s_waitcnt lgkmcnt(2)
mvv 0,196,36
R 52,31744
mvv 4,200,36
s_add_u32 s49, s49, 346112
mvv 8,204,36
s_waitcnt vmcnt(1)
s_barrier
mvv 0,208,40
R 24,32768
ds_read_b128 v[76:79], v92 offset:1024
mvv 4,212,40
mvv 8,216,40
s_waitcnt lgkmcnt(3)
mvv 0,220,44
R 28,33792
mvv 4,224,44
mvv 8,228,44
mvv 0,232,48
R 32,34816
mvv 4,236,48
s_waitcnt lgkmcnt(2)
v_add_f32 v76, v76, v77
mvv 8,240,48
v_add_f32 v78, v78, v79
v_add_f32 v76, v76, v78
mvv 0,244,52
R 36,35840
v_add_f32_dpp v77, v76, v76 quad_perm:[1,0,3,2] row_mask:0xf bank_mask:0xf
mvv 4,248,52
buffer_store_dword v77, v93, s[36:39], s49 offen sc1
mvv 8,252,52
mav 12,0,24
R 40,36864
mav 16,4,24
v_cvt_pk_bf16_f32 v62, v0, v1
v_cvt_pk_bf16_f32 v63, v2, v3
mav 20,8,24
v_cvt_pk_bf16_f32 v64, v4, v5
s_waitcnt lgkmcnt(2)
mav 12,12,28
R 44,37888
v_cvt_pk_bf16_f32 v65, v6, v7
mav 16,16,28
buffer_store_dwordx4 v[62:65], v83, s[36:39], s49 offen sc1
v_cvt_pk_bf16_f32 v66, v8, v9
mav 20,20,28
v_cvt_pk_bf16_f32 v67, v10, v11
v_cvt_pk_bf16_f32 v68, v8, v9
mav 12,24,32
R 48,38912
v_cvt_pk_bf16_f32 v69, v10, v11
mav 16,28,32
v_permlane16_swap_b32 v66, v68
v_permlane16_swap_b32 v67, v69
mav 20,32,32
buffer_store_dwordx4 v[66:69], v84, s[36:39], s49 offen sc1
s_waitcnt lgkmcnt(2)
mav 12,36,36
R 52,39936
v_mul_f32 v73, v0, v0
mav 16,40,36
v_add_f32 v72, v1, v0
v_fmac_f32 v73, v1, v1
mav 20,44,36
v_add_f32 v72, v2, v72
v_fmac_f32 v73, v2, v2
mav 12,48,40
R 24,40960
v_add_f32 v72, v3, v72
mav 16,52,40
v_fmac_f32 v73, v3, v3
v_add_f32 v72, v4, v72
mav 20,56,40
v_fmac_f32 v73, v4, v4
s_waitcnt lgkmcnt(2)
mav 12,60,44
R 28,41984
v_add_f32 v72, v5, v72
mav 16,64,44
v_fmac_f32 v73, v5, v5
v_add_f32 v72, v6, v72
mav 20,68,44
v_fmac_f32 v73, v6, v6
v_add_f32 v72, v7, v72
mav 12,72,48
R 32,43008
v_fmac_f32 v73, v7, v7
mav 16,76,48
v_add_f32 v72, v8, v72
v_fmac_f32 v73, v8, v8
mav 20,80,48
v_add_f32 v72, v9, v72
s_waitcnt lgkmcnt(2)
mav 12,84,52
R 36,44032
v_fmac_f32 v73, v9, v9
mav 16,88,52
v_add_f32 v72, v10, v72
v_fmac_f32 v73, v10, v10
mav 20,92,52
v_add_f32 v72, v11, v72
v_fmac_f32 v73, v11, v11
mav 12,96,24
R 40,45056
v_permlane32_swap_b32 v72, v73
mav 16,100,24
v_add_f32 v72, v72, v73
ds_write_b32 v91, v72 offset:0
mav 20,104,24
s_waitcnt lgkmcnt(3)
mav 12,108,28
R 44,46080
mav 16,112,28
mav 20,116,28
mav 12,120,32
R 48,47104
mav 16,124,32
mav 20,128,32
s_waitcnt lgkmcnt(2)
mav 12,132,36
R 52,48128
mav 16,136,36
mav 20,140,36
mav 12,144,40
R 24,49152
mav 16,148,40
mav 20,152,40
s_waitcnt lgkmcnt(2)
mav 12,156,44
R 28,50176
mav 16,160,44
mav 20,164,44
mav 12,168,48
R 32,51200
mav 16,172,48
mav 20,176,48
s_waitcnt lgkmcnt(2)
mav 12,180,52
R 36,52224
mav 16,184,52
mav 20,188,52
mav 12,192,24
R 40,53248
mav 16,196,24
mav 20,200,24
s_waitcnt lgkmcnt(2)
mav 12,204,28
R 44,54272
mav 16,208,28
mav 20,212,28
mav 12,216,32
R 48,55296
mav 16,220,32
mav 20,224,32
s_waitcnt lgkmcnt(2)
mav 12,228,36
R 52,56320
mav 16,232,36
mav 20,236,36
mav 12,240,40
R 24,57344
mav 16,244,40
mav 20,248,40
s_waitcnt lgkmcnt(2)
mav 12,252,44
R 28,58368
mvv 16,128,44
v_add_u32 v70, 1, v70
mvv 20,132,44
mvv 12,136,48
R 32,59392
mvv 16,140,48
mvv 20,144,48
s_waitcnt lgkmcnt(2)
mvv 12,148,52
R 36,60416
mvv 16,152,52
s_waitcnt vmcnt(0)
buffer_store_dwordx2 v[70:71], v81, s[36:39], s52 offen sc1
mvv 20,156,52
mvv 12,160,24
R 40,61440
mvv 16,164,24
mvv 20,168,24
s_waitcnt lgkmcnt(2)
mvv 12,172,28
R 44,62464
mvv 16,176,28
mvv 20,180,28
mvv 12,184,32
R 48,63488
mvv 16,188,32
mvv 20,192,32
s_waitcnt lgkmcnt(2)
mvv 12,196,36
R 52,64512
mvv 16,200,36
s_add_u32 s49, s49, 346112
mvv 20,204,36
s_barrier
mvv 12,208,40
ds_read_b128 v[76:79], v92 offset:0
mvv 16,212,40
mvv 20,216,40
s_waitcnt lgkmcnt(2)
mvv 12,220,44
mvv 16,224,44
mvv 20,228,44
mvv 12,232,48
mvv 16,236,48
s_waitcnt lgkmcnt(0)
v_add_f32 v76, v76, v77
mvv 20,240,48
v_add_f32 v78, v78, v79
v_add_f32 v76, v76, v78
mvv 12,244,52
mvv 16,248,52
v_add_f32_dpp v77, v76, v76 quad_perm:[1,0,3,2] row_mask:0xf bank_mask:0xf
buffer_store_dword v77, v93, s[36:39], s49 offen sc1
mvv 20,252,52
s_nop 3
v_cvt_pk_bf16_f32 v62, v12, v13
v_cvt_pk_bf16_f32 v63, v14, v15
v_cvt_pk_bf16_f32 v64, v16, v17
v_cvt_pk_bf16_f32 v65, v18, v19
buffer_store_dwordx4 v[62:65], v83, s[36:39], s49 offen sc1
v_cvt_pk_bf16_f32 v66, v20, v21
v_cvt_pk_bf16_f32 v67, v22, v23
v_cvt_pk_bf16_f32 v68, v20, v21
v_cvt_pk_bf16_f32 v69, v22, v23
s_nop 0
v_permlane16_swap_b32 v66, v68
v_permlane16_swap_b32 v67, v69
buffer_store_dwordx4 v[66:69], v84, s[36:39], s49 offen sc1
v_mul_f32 v73, v12, v12
v_add_f32 v72, v13, v12
v_fmac_f32 v73, v13, v13
v_add_f32 v72, v14, v72
v_fmac_f32 v73, v14, v14
v_add_f32 v72, v15, v72
v_fmac_f32 v73, v15, v15
v_add_f32 v72, v16, v72
v_fmac_f32 v73, v16, v16
v_add_f32 v72, v17, v72
v_fmac_f32 v73, v17, v17
v_add_f32 v72, v18, v72
v_fmac_f32 v73, v18, v18
v_add_f32 v72, v19, v72
v_fmac_f32 v73, v19, v19
v_add_f32 v72, v20, v72
v_fmac_f32 v73, v20, v20
v_add_f32 v72, v21, v72
v_fmac_f32 v73, v21, v21
v_add_f32 v72, v22, v72
v_fmac_f32 v73, v22, v22
v_add_f32 v72, v23, v72
v_fmac_f32 v73, v23, v23
s_nop 1
v_permlane32_swap_b32 v72, v73
v_add_f32 v72, v72, v73
ds_write_b32 v91, v72 offset:1024
s_add_u32 s49, s49, 346112
s_waitcnt lgkmcnt(0)
s_barrier
ds_read_b128 v[76:79], v92 offset:1024
s_waitcnt lgkmcnt(0)
v_add_f32 v76, v76, v77
v_add_f32 v78, v78, v79
v_add_f32 v76, v76, v78
s_nop 1
v_add_f32_dpp v77, v76, v76 quad_perm:[1,0,3,2] row_mask:0xf bank_mask:0xf
buffer_store_dword v77, v93, s[36:39], s49 offen sc1
v_add_u32 v70, 37, v70
s_waitcnt vmcnt(0)
buffer_store_dwordx2 v[70:71], v81, s[36:39], s52 offen sc1
s_endpgm
LvO2:
s_barrier
buffer_load_dwordx4 v[56:59], v89, s[36:39], s49 offen sc1
buffer_load_dwordx2 v[60:61], v90, s[36:39], s49 offen sc1
s_add_u32 s49, s49, 346112
s_add_u32 m0, s50, 0
s_nop 0
D 85,0
D 85,1024
D 85,2048
D 85,3072
s_add_u32 m0, s50, 4096
s_nop 0
D 86,0
D 86,1024
D 86,2048
D 86,3072
s_waitcnt vmcnt(9)
v_lshlrev_b32 v0, 16, v56
v_and_b32 v1, 0xffff0000, v56
v_lshlrev_b32 v2, 16, v57
v_and_b32 v3, 0xffff0000, v57
v_lshlrev_b32 v4, 16, v58
v_and_b32 v5, 0xffff0000, v58
v_lshlrev_b32 v6, 16, v59
v_and_b32 v7, 0xffff0000, v59
s_waitcnt vmcnt(8)
v_lshlrev_b32 v8, 16, v60
v_and_b32 v9, 0xffff0000, v60
v_lshlrev_b32 v10, 16, v61
v_and_b32 v11, 0xffff0000, v61
buffer_load_dwordx4 v[56:59], v89, s[36:39], s49 offen sc1
buffer_load_dwordx2 v[60:61], v90, s[36:39], s49 offen sc1
s_add_u32 s49, s49, 346112
s_waitcnt vmcnt(2)
s_barrier
s_add_u32 m0, s50, 32768
R 24,0
D 85,0
R 28,1024
D 85,1024
R 32,2048
R 36,3072
D 85,2048
s_waitcnt lgkmcnt(2)
mav 0,0,24
R 40,4096
mav 4,4,24
mav 8,8,24
D 85,3072
mav 0,12,28
R 44,5120
mav 4,16,28
mav 8,20,28
s_add_u32 m0, s50, 36864
s_waitcnt lgkmcnt(2)
mav 0,24,32
R 48,6144
mav 4,28,32
D 86,0
mav 8,32,32
mav 0,36,36
R 52,7168
mav 4,40,36
mav 8,44,36
D 86,1024
s_waitcnt lgkmcnt(2)
mav 0,48,40
R 24,8192
mav 4,52,40
mav 8,56,40
mav 0,60,44
R 28,9216
mav 4,64,44
D 86,2048
mav 8,68,44
s_waitcnt lgkmcnt(2)
mav 0,72,48
R 32,10240
mav 4,76,48
mav 8,80,48
D 86,3072
mav 0,84,52
R 36,11264
mav 4,88,52
mav 8,92,52
s_waitcnt lgkmcnt(2)
mav 0,96,24
R 40,12288
mav 4,100,24
mav 8,104,24
mav 0,108,28
R 44,13312
mav 4,112,28
mav 8,116,28
s_waitcnt lgkmcnt(2)
mav 0,120,32
R 48,14336
mav 4,124,32
mav 8,128,32
mav 0,132,36
R 52,15360
mav 4,136,36
mav 8,140,36
s_waitcnt lgkmcnt(2)
mav 0,144,40
R 24,16384
mav 4,148,40
mav 8,152,40
mav 0,156,44
R 28,17408
mav 4,160,44
mav 8,164,44
s_waitcnt lgkmcnt(2)
mav 0,168,48
R 32,18432
mav 4,172,48
mav 8,176,48
mav 0,180,52
R 36,19456
mav 4,184,52
mav 8,188,52
s_waitcnt lgkmcnt(2)
mav 0,192,24
R 40,20480
mav 4,196,24
mav 8,200,24
s_waitcnt vmcnt(9)
v_lshlrev_b32 v12, 16, v56
mav 0,204,28
R 44,21504
v_and_b32 v13, 0xffff0000, v56
mav 4,208,28
v_lshlrev_b32 v14, 16, v57
v_and_b32 v15, 0xffff0000, v57
mav 8,212,28
v_lshlrev_b32 v16, 16, v58
s_waitcnt lgkmcnt(2)
mav 0,216,32
R 48,22528
v_and_b32 v17, 0xffff0000, v58
mav 4,220,32
v_lshlrev_b32 v18, 16, v59
v_and_b32 v19, 0xffff0000, v59
mav 8,224,32
s_waitcnt vmcnt(8)
v_lshlrev_b32 v20, 16, v60
mav 0,228,36
R 52,23552
v_and_b32 v21, 0xffff0000, v60
mav 4,232,36
v_lshlrev_b32 v22, 16, v61
v_and_b32 v23, 0xffff0000, v61
mav 8,236,36
s_waitcnt lgkmcnt(2)
mav 0,240,40
R 24,24576
mav 4,244,40
mav 8,248,40
mav 0,252,44
R 28,25600
mvv 4,128,44
buffer_load_dwordx4 v[56:59], v89, s[36:39], s49 offen sc1
mvv 8,132,44
s_waitcnt lgkmcnt(2)
mvv 0,136,48
R 32,26624
mvv 4,140,48
mvv 8,144,48
buffer_load_dwordx2 v[60:61], v90, s[36:39], s49 offen sc1
mvv 0,148,52
)"
R"(
R 36,27648
mvv 4,152,52
mvv 8,156,52
s_waitcnt lgkmcnt(2)
mvv 0,160,24
R 40,28672
mvv 4,164,24
mvv 8,168,24
mvv 0,172,28
R 44,29696
mvv 4,176,28
mvv 8,180,28
s_waitcnt lgkmcnt(2)
mvv 0,184,32
R 48,30720
s_xor_b32 s50, s50, 0x10000
mvv 4,188,32
mvv 8,192,32
mvv 0,196,36
R 52,31744
mvv 4,200,36
s_add_u32 s49, s49, 346112
mvv 8,204,36
s_waitcnt vmcnt(2)
s_barrier
s_waitcnt lgkmcnt(2)
mvv 0,208,40
R 24,32768
s_add_u32 m0, s50, 0
mvv 4,212,40
D 85,0
mvv 8,216,40
mvv 0,220,44
R 28,33792
mvv 4,224,44
mvv 8,228,44
D 85,1024
s_waitcnt lgkmcnt(2)
mvv 0,232,48
R 32,34816
mvv 4,236,48
mvv 8,240,48
mvv 0,244,52
R 36,35840
mvv 4,248,52
D 85,2048
mvv 8,252,52
s_waitcnt lgkmcnt(2)
mav 12,0,24
R 40,36864
mav 16,4,24
v_cvt_pk_bf16_f32 v62, v0, v1
v_cvt_pk_bf16_f32 v63, v2, v3
mav 20,8,24
D 85,3072
v_cvt_pk_bf16_f32 v64, v4, v5
mav 12,12,28
R 44,37888
v_cvt_pk_bf16_f32 v65, v6, v7
mav 16,16,28
v_cvt_pk_bf16_f32 v66, v8, v9
v_cvt_pk_bf16_f32 v67, v10, v11
mav 20,20,28
s_add_u32 m0, s50, 4096
s_waitcnt lgkmcnt(2)
mav 12,24,32
R 48,38912
buffer_store_dwordx4 v[62:65], v83, s[36:39], s49 offen sc1
mav 16,28,32
v_cvt_pk_bf16_f32 v68, v8, v9
v_cvt_pk_bf16_f32 v69, v10, v11
mav 20,32,32
v_permlane16_swap_b32 v66, v68
v_permlane16_swap_b32 v67, v69
mav 12,36,36
R 52,39936
v_mul_f32 v73, v0, v0
mav 16,40,36
buffer_store_dwordx4 v[66:69], v84, s[36:39], s49 offen sc1
v_add_f32 v72, v1, v0
mav 20,44,36
v_fmac_f32 v73, v1, v1
s_waitcnt lgkmcnt(2)
mav 12,48,40
R 24,40960
v_add_f32 v72, v2, v72
mav 16,52,40
v_fmac_f32 v73, v2, v2
v_add_f32 v72, v3, v72
mav 20,56,40
D 86,0
v_fmac_f32 v73, v3, v3
mav 12,60,44
R 28,41984
v_add_f32 v72, v4, v72
mav 16,64,44
v_fmac_f32 v73, v4, v4
v_add_f32 v72, v5, v72
mav 20,68,44
v_fmac_f32 v73, v5, v5
s_waitcnt lgkmcnt(2)
mav 12,72,48
R 32,43008
D 86,1024
mav 16,76,48
v_add_f32 v72, v6, v72
v_fmac_f32 v73, v6, v6
mav 20,80,48
v_add_f32 v72, v7, v72
v_fmac_f32 v73, v7, v7
mav 12,84,52
R 36,44032
v_add_f32 v72, v8, v72
mav 16,88,52
D 86,2048
v_fmac_f32 v73, v8, v8
mav 20,92,52
v_add_f32 v72, v9, v72
s_waitcnt lgkmcnt(2)
mav 12,96,24
R 40,45056
v_fmac_f32 v73, v9, v9
mav 16,100,24
v_add_f32 v72, v10, v72
v_fmac_f32 v73, v10, v10
mav 20,104,24
D 86,3072
v_add_f32 v72, v11, v72
mav 12,108,28
R 44,46080
v_fmac_f32 v73, v11, v11
mav 16,112,28
mav 20,116,28
v_permlane32_swap_b32 v72, v73
s_waitcnt lgkmcnt(2)
mav 12,120,32
R 48,47104
v_add_f32 v72, v72, v73
mav 16,124,32
ds_write_b32 v91, v72 offset:0
mav 20,128,32
mav 12,132,36
R 52,48128
mav 16,136,36
mav 20,140,36
s_waitcnt lgkmcnt(3)
mav 12,144,40
R 24,49152
mav 16,148,40
mav 20,152,40
mav 12,156,44
R 28,50176
mav 16,160,44
mav 20,164,44
s_waitcnt lgkmcnt(2)
mav 12,168,48
R 32,51200
mav 16,172,48
mav 20,176,48
mav 12,180,52
R 36,52224
mav 16,184,52
mav 20,188,52
s_waitcnt lgkmcnt(2)
mav 12,192,24
R 40,53248
mav 16,196,24
mav 20,200,24
s_waitcnt vmcnt(11)
v_lshlrev_b32 v0, 16, v56
mav 12,204,28
R 44,54272
v_and_b32 v1, 0xffff0000, v56
mav 16,208,28
v_lshlrev_b32 v2, 16, v57
v_and_b32 v3, 0xffff0000, v57
mav 20,212,28
v_lshlrev_b32 v4, 16, v58
s_waitcnt lgkmcnt(2)
mav 12,216,32
R 48,55296
v_and_b32 v5, 0xffff0000, v58
mav 16,220,32
v_lshlrev_b32 v6, 16, v59
v_and_b32 v7, 0xffff0000, v59
mav 20,224,32
s_waitcnt vmcnt(10)
v_lshlrev_b32 v8, 16, v60
mav 12,228,36
R 52,56320
v_and_b32 v9, 0xffff0000, v60
mav 16,232,36
v_lshlrev_b32 v10, 16, v61
v_and_b32 v11, 0xffff0000, v61
mav 20,236,36
s_waitcnt lgkmcnt(2)
mav 12,240,40
R 24,57344
mav 16,244,40
mav 20,248,40
mav 12,252,44
R 28,58368
mvv 16,128,44
buffer_load_dwordx4 v[56:59], v89, s[36:39], s49 offen sc1
v_add_u32 v70, 1, v70
mvv 20,132,44
s_waitcnt lgkmcnt(2)
mvv 12,136,48
R 32,59392
mvv 16,140,48
mvv 20,144,48
buffer_load_dwordx2 v[60:61], v90, s[36:39], s49 offen sc1
mvv 12,148,52
R 36,60416
mvv 16,152,52
s_waitcnt vmcnt(6)
buffer_store_dwordx2 v[70:71], v81, s[36:39], s52 offen sc1
mvv 20,156,52
s_waitcnt lgkmcnt(2)
mvv 12,160,24
R 40,61440
mvv 16,164,24
mvv 20,168,24
mvv 12,172,28
R 44,62464
mvv 16,176,28
mvv 20,180,28
s_waitcnt lgkmcnt(2)
mvv 12,184,32
R 48,63488
mvv 16,188,32
mvv 20,192,32
mvv 12,196,36
R 52,64512
v_xor_b32 v80, 0x10000, v80
mvv 16,200,36
s_add_u32 s49, s49, 346112
mvv 20,204,36
s_waitcnt vmcnt(3)
s_barrier
s_waitcnt lgkmcnt(2)
mvv 12,208,40
R 24,0
s_add_u32 m0, s50, 32768
mvv 16,212,40
D 85,0
mvv 20,216,40
mvv 12,220,44
R 28,1024
mvv 16,224,44
mvv 20,228,44
D 85,1024
s_waitcnt lgkmcnt(2)
mvv 12,232,48
R 32,2048
mvv 16,236,48
mvv 20,240,48
mvv 12,244,52
R 36,3072
mvv 16,248,52
D 85,2048
mvv 20,252,52
s_mov_b32 s60, -30
.p2align 6
LO2_1:
s_waitcnt lgkmcnt(2)
mav 0,0,24
R 40,4096
mav 4,4,24
v_cvt_pk_bf16_f32 v62, v12, v13
v_cvt_pk_bf16_f32 v63, v14, v15
mav 8,8,24
D 85,3072
v_cvt_pk_bf16_f32 v64, v16, v17
mav 0,12,28
R 44,5120
v_cvt_pk_bf16_f32 v65, v18, v19
mav 4,16,28
v_cvt_pk_bf16_f32 v66, v20, v21
v_cvt_pk_bf16_f32 v67, v22, v23
mav 8,20,28
s_add_u32 m0, s50, 36864
s_waitcnt lgkmcnt(2)
mav 0,24,32
R 48,6144
buffer_store_dwordx4 v[62:65], v83, s[36:39], s49 offen sc1
mav 4,28,32
v_cvt_pk_bf16_f32 v68, v20, v21
v_cvt_pk_bf16_f32 v69, v22, v23
mav 8,32,32
v_permlane16_swap_b32 v66, v68
v_permlane16_swap_b32 v67, v69
mav 0,36,36
R 52,7168
v_mul_f32 v73, v12, v12
mav 4,40,36
buffer_store_dwordx4 v[66:69], v84, s[36:39], s49 offen sc1
v_add_f32 v72, v13, v12
mav 8,44,36
v_fmac_f32 v73, v13, v13
s_waitcnt lgkmcnt(2)
mav 0,48,40
R 24,8192
v_add_f32 v72, v14, v72
mav 4,52,40
v_fmac_f32 v73, v14, v14
v_add_f32 v72, v15, v72
mav 8,56,40
D 86,0
v_fmac_f32 v73, v15, v15
mav 0,60,44
R 28,9216
v_add_f32 v72, v16, v72
mav 4,64,44
v_fmac_f32 v73, v16, v16
v_add_f32 v72, v17, v72
mav 8,68,44
v_fmac_f32 v73, v17, v17
s_waitcnt lgkmcnt(2)
mav 0,72,48
R 32,10240
D 86,1024
mav 4,76,48
v_add_f32 v72, v18, v72
v_fmac_f32 v73, v18, v18
mav 8,80,48
v_add_f32 v72, v19, v72
v_fmac_f32 v73, v19, v19
mav 0,84,52
R 36,11264
v_add_f32 v72, v20, v72
mav 4,88,52
D 86,2048
v_fmac_f32 v73, v20, v20
mav 8,92,52
v_add_f32 v72, v21, v72
s_waitcnt lgkmcnt(2)
mav 0,96,24
R 40,12288
v_fmac_f32 v73, v21, v21
mav 4,100,24
v_add_f32 v72, v22, v72
v_fmac_f32 v73, v22, v22
mav 8,104,24
D 86,3072
v_add_f32 v72, v23, v72
mav 0,108,28
R 44,13312
v_fmac_f32 v73, v23, v23
mav 4,112,28
mav 8,116,28
v_permlane32_swap_b32 v72, v73
s_waitcnt lgkmcnt(2)
mav 0,120,32
R 48,14336
v_add_f32 v72, v72, v73
mav 4,124,32
ds_write_b32 v91, v72 offset:1024
mav 8,128,32
mav 0,132,36
R 52,15360
mav 4,136,36
mav 8,140,36
s_waitcnt lgkmcnt(3)
mav 0,144,40
R 24,16384
mav 4,148,40
mav 8,152,40
mav 0,156,44
R 28,17408
mav 4,160,44
mav 8,164,44
s_waitcnt lgkmcnt(2)
mav 0,168,48
R 32,18432
mav 4,172,48
mav 8,176,48
mav 0,180,52
R 36,19456
mav 4,184,52
mav 8,188,52
s_waitcnt lgkmcnt(2)
mav 0,192,24
R 40,20480
mav 4,196,24
mav 8,200,24
s_waitcnt vmcnt(12)
v_lshlrev_b32 v12, 16, v56
mav 0,204,28
R 44,21504
v_and_b32 v13, 0xffff0000, v56
mav 4,208,28
v_lshlrev_b32 v14, 16, v57
v_and_b32 v15, 0xffff0000, v57
mav 8,212,28
v_lshlrev_b32 v16, 16, v58
s_waitcnt lgkmcnt(2)
mav 0,216,32
R 48,22528
v_and_b32 v17, 0xffff0000, v58
mav 4,220,32
v_lshlrev_b32 v18, 16, v59
v_and_b32 v19, 0xffff0000, v59
mav 8,224,32
s_waitcnt vmcnt(11)
v_lshlrev_b32 v20, 16, v60
mav 0,228,36
R 52,23552
v_and_b32 v21, 0xffff0000, v60
mav 4,232,36
v_lshlrev_b32 v22, 16, v61
v_and_b32 v23, 0xffff0000, v61
mav 8,236,36
s_waitcnt lgkmcnt(2)
mav 0,240,40
R 24,24576
mav 4,244,40
mav 8,248,40
mav 0,252,44
R 28,25600
mvv 4,128,44
buffer_load_dwordx4 v[56:59], v89, s[36:39], s49 offen sc1
v_add_u32 v70, 1, v70
mvv 8,132,44
s_waitcnt lgkmcnt(2)
mvv 0,136,48
R 32,26624
mvv 4,140,48
mvv 8,144,48
buffer_load_dwordx2 v[60:61], v90, s[36:39], s49 offen sc1
mvv 0,148,52
R 36,27648
mvv 4,152,52
s_waitcnt vmcnt(6)
buffer_store_dwordx2 v[70:71], v81, s[36:39], s52 offen sc1
mvv 8,156,52
s_waitcnt lgkmcnt(2)
mvv 0,160,24
R 40,28672
mvv 4,164,24
mvv 8,168,24
mvv 0,172,28
R 44,29696
mvv 4,176,28
mvv 8,180,28
s_waitcnt lgkmcnt(2)
mvv 0,184,32
R 48,30720
s_xor_b32 s50, s50, 0x10000
mvv 4,188,32
mvv 8,192,32
mvv 0,196,36
R 52,31744
mvv 4,200,36
s_add_u32 s49, s49, 346112
mvv 8,204,36
s_waitcnt vmcnt(3)
s_barrier
s_waitcnt lgkmcnt(2)
mvv 0,208,40
R 24,32768
s_add_u32 m0, s50, 0
mvv 4,212,40
D 85,0
mvv 8,216,40
mvv 0,220,44
R 28,33792
mvv 4,224,44
mvv 8,228,44
D 85,1024
s_waitcnt lgkmcnt(2)
mvv 0,232,48
R 32,34816
mvv 4,236,48
mvv 8,240,48
mvv 0,244,52
R 36,35840
mvv 4,248,52
D 85,2048
mvv 8,252,52
s_waitcnt lgkmcnt(2)
mav 12,0,24
R 40,36864
mav 16,4,24
v_cvt_pk_bf16_f32 v62, v0, v1
v_cvt_pk_bf16_f32 v63, v2, v3
mav 20,8,24
D 85,3072
v_cvt_pk_bf16_f32 v64, v4, v5
mav 12,12,28
R 44,37888
v_cvt_pk_bf16_f32 v65, v6, v7
mav 16,16,28
v_cvt_pk_bf16_f32 v66, v8, v9
v_cvt_pk_bf16_f32 v67, v10, v11
mav 20,20,28
s_add_u32 m0, s50, 4096
s_waitcnt lgkmcnt(2)
mav 12,24,32
R 48,38912
buffer_store_dwordx4 v[62:65], v83, s[36:39], s49 offen sc1
mav 16,28,32
v_cvt_pk_bf16_f32 v68, v8, v9
v_cvt_pk_bf16_f32 v69, v10, v11
mav 20,32,32
v_permlane16_swap_b32 v66, v68
v_permlane16_swap_b32 v67, v69
mav 12,36,36
R 52,39936
v_mul_f32 v73, v0, v0
mav 16,40,36
buffer_store_dwordx4 v[66:69], v84, s[36:39], s49 offen sc1
v_add_f32 v72, v1, v0
mav 20,44,36
v_fmac_f32 v73, v1, v1
s_waitcnt lgkmcnt(2)
mav 12,48,40
R 24,40960
v_add_f32 v72, v2, v72
mav 16,52,40
v_fmac_f32 v73, v2, v2
v_add_f32 v72, v3, v72
mav 20,56,40
D 86,0
v_fmac_f32 v73, v3, v3
mav 12,60,44
R 28,41984
v_add_f32 v72, v4, v72
mav 16,64,44
v_fmac_f32 v73, v4, v4
v_add_f32 v72, v5, v72
mav 20,68,44
v_fmac_f32 v73, v5, v5
s_waitcnt lgkmcnt(2)
mav 12,72,48
R 32,43008
D 86,1024
mav 16,76,48
v_add_f32 v72, v6, v72
v_fmac_f32 v73, v6, v6
mav 20,80,48
v_add_f32 v72, v7, v72
v_fmac_f32 v73, v7, v7
mav 12,84,52
R 36,44032
v_add_f32 v72, v8, v72
mav 16,88,52
D 86,2048
v_fmac_f32 v73, v8, v8
mav 20,92,52
v_add_f32 v72, v9, v72
s_waitcnt lgkmcnt(2)
mav 12,96,24
R 40,45056
v_fmac_f32 v73, v9, v9
mav 16,100,24
v_add_f32 v72, v10, v72
v_fmac_f32 v73, v10, v10
mav 20,104,24
D 86,3072
v_add_f32 v72, v11, v72
mav 12,108,28
R 44,46080
v_fmac_f32 v73, v11, v11
mav 16,112,28
mav 20,116,28
v_permlane32_swap_b32 v72, v73
s_waitcnt lgkmcnt(2)
mav 12,120,32
R 48,47104
v_add_f32 v72, v72, v73
mav 16,124,32
ds_write_b32 v91, v72 offset:0
mav 20,128,32
mav 12,132,36
R 52,48128
mav 16,136,36
mav 20,140,36
s_waitcnt lgkmcnt(3)
mav 12,144,40
R 24,49152
mav 16,148,40
mav 20,152,40
mav 12,156,44
R 28,50176
mav 16,160,44
mav 20,164,44
s_waitcnt lgkmcnt(2)
mav 12,168,48
R 32,51200
mav 16,172,48
mav 20,176,48
mav 12,180,52
R 36,52224
mav 16,184,52
mav 20,188,52
s_waitcnt lgkmcnt(2)
mav 12,192,24
R 40,53248
mav 16,196,24
mav 20,200,24
s_waitcnt vmcnt(12)
v_lshlrev_b32 v0, 16, v56
mav 12,204,28
R 44,54272
v_and_b32 v1, 0xffff0000, v56
mav 16,208,28
v_lshlrev_b32 v2, 16, v57
v_and_b32 v3, 0xffff0000, v57
mav 20,212,28
v_lshlrev_b32 v4, 16, v58
s_waitcnt lgkmcnt(2)
mav 12,216,32
R 48,55296
v_and_b32 v5, 0xffff0000, v58
mav 16,220,32
v_lshlrev_b32 v6, 16, v59
v_and_b32 v7, 0xffff0000, v59
mav 20,224,32
s_waitcnt vmcnt(11)
v_lshlrev_b32 v8, 16, v60
mav 12,228,36
R 52,56320
v_and_b32 v9, 0xffff0000, v60
mav 16,232,36
v_lshlrev_b32 v10, 16, v61
v_and_b32 v11, 0xffff0000, v61
mav 20,236,36
s_waitcnt lgkmcnt(2)
mav 12,240,40
R 24,57344
mav 16,244,40
mav 20,248,40
mav 12,252,44
R 28,58368
mvv 16,128,44
buffer_load_dwordx4 v[56:59], v89, s[36:39], s49 offen sc1
v_add_u32 v70, 1, v70
mvv 20,132,44
s_waitcnt lgkmcnt(2)
mvv 12,136,48
R 32,59392
mvv 16,140,48
mvv 20,144,48
buffer_load_dwordx2 v[60:61], v90, s[36:39], s49 offen sc1
mvv 12,148,52
R 36,60416
mvv 16,152,52
s_waitcnt vmcnt(6)
buffer_store_dwordx2 v[70:71], v81, s[36:39], s52 offen sc1
mvv 20,156,52
s_waitcnt lgkmcnt(2)
mvv 12,160,24
R 40,61440
mvv 16,164,24
mvv 20,168,24
mvv 12,172,28
R 44,62464
mvv 16,176,28
mvv 20,180,28
s_waitcnt lgkmcnt(2)
mvv 12,184,32
R 48,63488
mvv 16,188,32
mvv 20,192,32
mvv 12,196,36
R 52,64512
v_xor_b32 v80, 0x10000, v80
mvv 16,200,36
s_add_u32 s49, s49, 346112
mvv 20,204,36
s_waitcnt vmcnt(3)
s_barrier
s_waitcnt lgkmcnt(2)
mvv 12,208,40
R 24,0
s_add_u32 m0, s50, 32768
mvv 16,212,40
D 85,0
mvv 20,216,40
mvv 12,220,44
R 28,1024
mvv 16,224,44
mvv 20,228,44
D 85,1024
s_waitcnt lgkmcnt(2)
mvv 12,232,48
R 32,2048
mvv 16,236,48
mvv 20,240,48
mvv 12,244,52
R 36,3072
mvv 16,248,52
D 85,2048
mvv 20,252,52
s_add_u32 s60, s60, 1
s_cbranch_scc0 LO2_1
s_waitcnt lgkmcnt(2)
mav 0,0,24
R 40,4096
mav 4,4,24
v_cvt_pk_bf16_f32 v62, v12, v13
v_cvt_pk_bf16_f32 v63, v14, v15
mav 8,8,24
D 85,3072
v_cvt_pk_bf16_f32 v64, v16, v17
mav 0,12,28
R 44,5120
v_cvt_pk_bf16_f32 v65, v18, v19
mav 4,16,28
v_cvt_pk_bf16_f32 v66, v20, v21
v_cvt_pk_bf16_f32 v67, v22, v23
mav 8,20,28
s_add_u32 m0, s50, 36864
s_waitcnt lgkmcnt(2)
mav 0,24,32
R 48,6144
buffer_store_dwordx4 v[62:65], v83, s[36:39], s49 offen sc1
mav 4,28,32
v_cvt_pk_bf16_f32 v68, v20, v21
v_cvt_pk_bf16_f32 v69, v22, v23
mav 8,32,32
v_permlane16_swap_b32 v66, v68
v_permlane16_swap_b32 v67, v69
mav 0,36,36
R 52,7168
v_mul_f32 v73, v12, v12
mav 4,40,36
buffer_store_dwordx4 v[66:69], v84, s[36:39], s49 offen sc1
v_add_f32 v72, v13, v12
mav 8,44,36
v_fmac_f32 v73, v13, v13
s_waitcnt lgkmcnt(2)
mav 0,48,40
R 24,8192
v_add_f32 v72, v14, v72
mav 4,52,40
v_fmac_f32 v73, v14, v14
v_add_f32 v72, v15, v72
mav 8,56,40
D 86,0
v_fmac_f32 v73, v15, v15
mav 0,60,44
R 28,9216
v_add_f32 v72, v16, v72
mav 4,64,44
v_fmac_f32 v73, v16, v16
v_add_f32 v72, v17, v72
mav 8,68,44
v_fmac_f32 v73, v17, v17
s_waitcnt lgkmcnt(2)
mav 0,72,48
R 32,10240
D 86,1024
mav 4,76,48
v_add_f32 v72, v18, v72
v_fmac_f32 v73, v18, v18
mav 8,80,48
v_add_f32 v72, v19, v72
v_fmac_f32 v73, v19, v19
mav 0,84,52
R 36,11264
v_add_f32 v72, v20, v72
mav 4,88,52
D 86,2048
v_fmac_f32 v73, v20, v20
mav 8,92,52
v_add_f32 v72, v21, v72
s_waitcnt lgkmcnt(2)
mav 0,96,24
R 40,12288
v_fmac_f32 v73, v21, v21
mav 4,100,24
v_add_f32 v72, v22, v72
v_fmac_f32 v73, v22, v22
mav 8,104,24
D 86,3072
v_add_f32 v72, v23, v72
mav 0,108,28
R 44,13312
v_fmac_f32 v73, v23, v23
mav 4,112,28
mav 8,116,28
v_permlane32_swap_b32 v72, v73
s_waitcnt lgkmcnt(2)
mav 0,120,32
R 48,14336
v_add_f32 v72, v72, v73
mav 4,124,32
ds_write_b32 v91, v72 offset:1024
mav 8,128,32
mav 0,132,36
R 52,15360
mav 4,136,36
mav 8,140,36
s_waitcnt lgkmcnt(3)
mav 0,144,40
R 24,16384
mav 4,148,40
mav 8,152,40
mav 0,156,44
R 28,17408
mav 4,160,44
mav 8,164,44
s_waitcnt lgkmcnt(2)
mav 0,168,48
R 32,18432
mav 4,172,48
mav 8,176,48
mav 0,180,52
R 36,19456
mav 4,184,52
mav 8,188,52
s_waitcnt lgkmcnt(2)
mav 0,192,24
R 40,20480
mav 4,196,24
mav 8,200,24
s_waitcnt vmcnt(12)
v_lshlrev_b32 v12, 16, v56
mav 0,204,28
R 44,21504
v_and_b32 v13, 0xffff0000, v56
mav 4,208,28
v_lshlrev_b32 v14, 16, v57
v_and_b32 v15, 0xffff0000, v57
mav 8,212,28
v_lshlrev_b32 v16, 16, v58
s_waitcnt lgkmcnt(2)
mav 0,216,32
R 48,22528
v_and_b32 v17, 0xffff0000, v58
mav 4,220,32
v_lshlrev_b32 v18, 16, v59
v_and_b32 v19, 0xffff0000, v59
mav 8,224,32
s_waitcnt vmcnt(11)
v_lshlrev_b32 v20, 16, v60
mav 0,228,36
R 52,23552
v_and_b32 v21, 0xffff0000, v60
mav 4,232,36
v_lshlrev_b32 v22, 16, v61
v_and_b32 v23, 0xffff0000, v61
mav 8,236,36
s_waitcnt lgkmcnt(2)
mav 0,240,40
R 24,24576
mav 4,244,40
mav 8,248,40
mav 0,252,44
R 28,25600
mvv 4,128,44
v_add_u32 v70, 1, v70
mvv 8,132,44
s_waitcnt lgkmcnt(2)
mvv 0,136,48
R 32,26624
mvv 4,140,48
mvv 8,144,48
mvv 0,148,52
R 36,27648
mvv 4,152,52
s_waitcnt vmcnt(4)
buffer_store_dwordx2 v[70:71], v81, s[36:39], s52 offen sc1
mvv 8,156,52
s_waitcnt lgkmcnt(2)
mvv 0,160,24
R 40,28672
mvv 4,164,24
mvv 8,168,24
mvv 0,172,28
R 44,29696
mvv 4,176,28
mvv 8,180,28
s_waitcnt lgkmcnt(2)
mvv 0,184,32
R 48,30720
mvv 4,188,32
mvv 8,192,32
mvv 0,196,36
R 52,31744
mvv 4,200,36
s_add_u32 s49, s49, 346112
mvv 8,204,36
s_waitcnt vmcnt(1)
s_barrier
s_waitcnt lgkmcnt(2)
mvv 0,208,40
R 24,32768
mvv 4,212,40
mvv 8,216,40
mvv 0,220,44
R 28,33792
mvv 4,224,44
mvv 8,228,44
s_waitcnt lgkmcnt(2)
mvv 0,232,48
R 32,34816
mvv 4,236,48
mvv 8,240,48
mvv 0,244,52
R 36,35840
mvv 4,248,52
mvv 8,252,52
s_waitcnt lgkmcnt(2)
mav 12,0,24
R 40,36864
mav 16,4,24
mav 20,8,24
v_cvt_pk_bf16_f32 v62, v0, v1
v_cvt_pk_bf16_f32 v63, v2, v3
mav 12,12,28
R 44,37888
v_cvt_pk_bf16_f32 v64, v4, v5
mav 16,16,28
v_cvt_pk_bf16_f32 v65, v6, v7
buffer_store_dwordx4 v[62:65], v83, s[36:39], s49 offen sc1
mav 20,20,28
v_cvt_pk_bf16_f32 v66, v8, v9
s_waitcnt lgkmcnt(2)
mav 12,24,32
R 48,38912
v_cvt_pk_bf16_f32 v67, v10, v11
mav 16,28,32
v_cvt_pk_bf16_f32 v68, v8, v9
v_cvt_pk_bf16_f32 v69, v10, v11
mav 20,32,32
v_permlane16_swap_b32 v66, v68
v_permlane16_swap_b32 v67, v69
mav 12,36,36
R 52,39936
buffer_store_dwordx4 v[66:69], v84, s[36:39], s49 offen sc1
mav 16,40,36
v_mul_f32 v73, v0, v0
v_add_f32 v72, v1, v0
mav 20,44,36
v_fmac_f32 v73, v1, v1
s_waitcnt lgkmcnt(2)
mav 12,48,40
R 24,40960
v_add_f32 v72, v2, v72
mav 16,52,40
v_fmac_f32 v73, v2, v2
v_add_f32 v72, v3, v72
mav 20,56,40
v_fmac_f32 v73, v3, v3
v_add_f32 v72, v4, v72
mav 12,60,44
R 28,41984
v_fmac_f32 v73, v4, v4
mav 16,64,44
v_add_f32 v72, v5, v72
v_fmac_f32 v73, v5, v5
mav 20,68,44
v_add_f32 v72, v6, v72
s_waitcnt lgkmcnt(2)
mav 12,72,48
R 32,43008
v_fmac_f32 v73, v6, v6
mav 16,76,48
v_add_f32 v72, v7, v72
v_fmac_f32 v73, v7, v7
mav 20,80,48
v_add_f32 v72, v8, v72
v_fmac_f32 v73, v8, v8
mav 12,84,52
R 36,44032
v_add_f32 v72, v9, v72
mav 16,88,52
v_fmac_f32 v73, v9, v9
v_add_f32 v72, v10, v72
mav 20,92,52
v_fmac_f32 v73, v10, v10
s_waitcnt lgkmcnt(2)
mav 12,96,24
R 40,45056
v_add_f32 v72, v11, v72
mav 16,100,24
v_fmac_f32 v73, v11, v11
mav 20,104,24
mav 12,108,28
R 44,46080
v_permlane32_swap_b32 v72, v73
mav 16,112,28
v_add_f32 v72, v72, v73
ds_write_b32 v91, v72 offset:0
mav 20,116,28
s_waitcnt lgkmcnt(3)
mav 12,120,32
R 48,47104
mav 16,124,32
mav 20,128,32
mav 12,132,36
R 52,48128
mav 16,136,36
mav 20,140,36
s_waitcnt lgkmcnt(2)
mav 12,144,40
R 24,49152
mav 16,148,40
mav 20,152,40
mav 12,156,44
R 28,50176
mav 16,160,44
mav 20,164,44
s_waitcnt lgkmcnt(2)
mav 12,168,48
R 32,51200
mav 16,172,48
mav 20,176,48
mav 12,180,52
R 36,52224
mav 16,184,52
mav 20,188,52
s_waitcnt lgkmcnt(2)
mav 12,192,24
R 40,53248
mav 16,196,24
mav 20,200,24
mav 12,204,28
R 44,54272
mav 16,208,28
mav 20,212,28
s_waitcnt lgkmcnt(2)
mav 12,216,32
R 48,55296
mav 16,220,32
mav 20,224,32
mav 12,228,36
R 52,56320
mav 16,232,36
mav 20,236,36
s_waitcnt lgkmcnt(2)
mav 12,240,40
R 24,57344
mav 16,244,40
mav 20,248,40
mav 12,252,44
R 28,58368
mvv 16,128,44
v_add_u32 v70, 1, v70
mvv 20,132,44
s_waitcnt lgkmcnt(2)
mvv 12,136,48
R 32,59392
mvv 16,140,48
mvv 20,144,48
mvv 12,148,52
R 36,60416
mvv 16,152,52
s_waitcnt vmcnt(0)
buffer_store_dwordx2 v[70:71], v81, s[36:39], s52 offen sc1
mvv 20,156,52
s_waitcnt lgkmcnt(2)
mvv 12,160,24
R 40,61440
mvv 16,164,24
mvv 20,168,24
mvv 12,172,28
R 44,62464
mvv 16,176,28
mvv 20,180,28
s_waitcnt lgkmcnt(2)
mvv 12,184,32
R 48,63488
mvv 16,188,32
mvv 20,192,32
mvv 12,196,36
R 52,64512
mvv 16,200,36
s_add_u32 s49, s49, 346112
mvv 20,204,36
s_barrier
s_waitcnt lgkmcnt(2)
mvv 12,208,40
mvv 16,212,40
mvv 20,216,40
mvv 12,220,44
mvv 16,224,44
mvv 20,228,44
s_waitcnt lgkmcnt(0)
mvv 12,232,48
mvv 16,236,48
mvv 20,240,48
mvv 12,244,52
mvv 16,248,52
mvv 20,252,52
s_nop 5
v_cvt_pk_bf16_f32 v62, v12, v13
v_cvt_pk_bf16_f32 v63, v14, v15
v_cvt_pk_bf16_f32 v64, v16, v17
v_cvt_pk_bf16_f32 v65, v18, v19
buffer_store_dwordx4 v[62:65], v83, s[36:39], s49 offen sc1
v_cvt_pk_bf16_f32 v66, v20, v21
v_cvt_pk_bf16_f32 v67, v22, v23
v_cvt_pk_bf16_f32 v68, v20, v21
v_cvt_pk_bf16_f32 v69, v22, v23
s_nop 0
v_permlane16_swap_b32 v66, v68
v_permlane16_swap_b32 v67, v69
buffer_store_dwordx4 v[66:69], v84, s[36:39], s49 offen sc1
v_mul_f32 v73, v12, v12
v_add_f32 v72, v13, v12
v_fmac_f32 v73, v13, v13
v_add_f32 v72, v14, v72
v_fmac_f32 v73, v14, v14
v_add_f32 v72, v15, v72
v_fmac_f32 v73, v15, v15
v_add_f32 v72, v16, v72
v_fmac_f32 v73, v16, v16
v_add_f32 v72, v17, v72
v_fmac_f32 v73, v17, v17
v_add_f32 v72, v18, v72
v_fmac_f32 v73, v18, v18
v_add_f32 v72, v19, v72
v_fmac_f32 v73, v19, v19
v_add_f32 v72, v20, v72
v_fmac_f32 v73, v20, v20
v_add_f32 v72, v21, v72
v_fmac_f32 v73, v21, v21
v_add_f32 v72, v22, v72
v_fmac_f32 v73, v22, v22
v_add_f32 v72, v23, v72
v_fmac_f32 v73, v23, v23
s_nop 1
v_permlane32_swap_b32 v72, v73
v_add_f32 v72, v72, v73
ds_write_b32 v91, v72 offset:1024
v_add_u32 v70, 37, v70
s_waitcnt vmcnt(0)
buffer_store_dwordx2 v[70:71], v81, s[36:39], s52 offen sc1
s_add_u32 s49, s49, 346112
s_waitcnt lgkmcnt(0)
s_barrier
s_endpgm
LeO:
.purgem R
Lr3:
s_cmp_eq_u32 s60, 3
s_cbranch_scc0 Lr4
.macro R d,o
ds_read_b128 v[\d:\d+3], v80 offset:\o
.endm
buffer_load_dword v77, off, s[36:39], 0 sc1
s_getpc_b64 s[66:67]
LpF:
v_lshlrev_b32 v8, 3, v80
s_lshl_b32 s60, s53, 3
v_add_u32 v8, s60, v8
.set cn, 0
.rept 2
v_add_u32 v9, cn, v8
v_min_u32 v9, LeF - LpF - 4, v9
global_load_dword v10, v9, s[66:67]
.set cn, cn + 32768
.endr
E 0,0
E 1,4096
E 2,8192
s_add_u32 s51, s51, 229376
E 0,12288
E 1,16384
E 2,20480
s_add_u32 s51, s51, 229376
E 0,24576
E 1,28672
E 2,32768
s_add_u32 s51, s51, 229376
E 0,36864
E 1,40960
E 2,45056
s_add_u32 s51, s51, 229376
E 0,49152
E 1,53248
E 2,57344
s_add_u32 s51, s51, 229376
E 0,61440
E 1,65536
E 2,69632
s_add_u32 s51, s51, 229376
E 0,73728
E 1,77824
E 2,81920
s_add_u32 s51, s51, 229376
E 0,86016
E 1,90112
E 2,94208
s_add_u32 s51, s51, 229376
s_cmp_ge_u32 s51, s57
s_cselect_b32 s60, 7340032, 0
s_sub_u32 s51, s51, s60
E 0,98304
E 1,102400
E 2,106496
s_add_u32 s51, s51, 229376
E 0,110592
E 1,114688
E 2,118784
s_add_u32 s51, s51, 229376
E 0,122880
E 1,126976
E 2,131072
s_add_u32 s51, s51, 229376
E 0,135168
E 1,139264
E 2,143360
s_add_u32 s51, s51, 229376
v_mov_b32 v24, s64
v_mov_b32 v25, s64
v_mov_b32 v26, s64
v_mov_b32 v27, s64
s_waitcnt vmcnt(30)
s_barrier
v_mov_b32 v76, 0x100
v_add_u32 v77, 0x9e3779b9, v77
buffer_store_dwordx2 v[76:77], v81, s[36:39], s52 offen sc1
v_add_u32 v88, v88, v76
Ta 0,4,0
Ta 4,5,0
Ta 8,6,0
Ta 12,4,12288
Ta 16,5,12288
Ta 20,6,12288
s_waitcnt lgkmcnt(0)
s_waitcnt vmcnt(25)
s_barrier
Ta 24,4,24576
Ta 28,5,24576
Ta 32,6,24576
Ta 36,4,36864
Ta 40,5,36864
Ta 44,6,36864
E 0,0
E 1,4096
E 2,8192
s_add_u32 s51, s51, 229376
E 0,12288
E 1,16384
E 2,20480
s_add_u32 s51, s51, 229376
v_mfma_f32_16x16x32_bf16 v[56:59], a[0:3], v[24:27], 0
v_mfma_f32_16x16x32_bf16 v[60:63], a[4:7], v[24:27], 0
v_mfma_f32_16x16x32_bf16 v[64:67], a[8:11], v[24:27], 0
mav 56,12,24
mav 60,16,24
mav 64,20,24
s_waitcnt lgkmcnt(0)
s_waitcnt vmcnt(25)
s_barrier
v_add_u32 v4, 49152, v4
v_add_u32 v5, 49152, v5
v_add_u32 v6, 49152, v6
Ta 48,4,0
Ta 52,5,0
Ta 56,6,0
Ta 60,4,12288
Ta 64,5,12288
Ta 68,6,12288
E 0,24576
E 1,28672
E 2,32768
s_add_u32 s51, s51, 229376
E 0,36864
E 1,40960
E 2,45056
s_add_u32 s51, s51, 229376
s_cmp_ge_u32 s51, s57
s_cselect_b32 s60, 7340032, 0
s_sub_u32 s51, s51, s60
mav 56,24,24
mav 60,28,24
mav 64,32,24
mav 56,36,24
mav 60,40,24
mav 64,44,24
s_waitcnt lgkmcnt(0)
s_waitcnt vmcnt(25)
s_barrier
Ta 72,4,24576
Ta 76,5,24576
Ta 80,6,24576
Ta 84,4,36864
Ta 88,5,36864
Ta 92,6,36864
E 0,49152
E 1,53248
E 2,57344
s_add_u32 s51, s51, 229376
E 0,61440
E 1,65536
E 2,69632
s_add_u32 s51, s51, 229376
mav 56,48,24
mav 60,52,24
mav 64,56,24
mav 56,60,24
mav 60,64,24
mav 64,68,24
s_waitcnt lgkmcnt(0)
s_waitcnt vmcnt(25)
s_barrier
v_add_u32 v4, 49152, v4
v_add_u32 v5, 49152, v5
v_add_u32 v6, 49152, v6
Ta 96,4,0
Ta 100,5,0
Ta 104,6,0
Ta 108,4,12288
Ta 112,5,12288
Ta 116,6,12288
E 0,73728
E 1,77824
E 2,81920
s_add_u32 s51, s51, 229376
E 0,86016
E 1,90112
E 2,94208
s_add_u32 s51, s51, 229376
mav 56,72,24
mav 60,76,24
mav 64,80,24
mav 56,84,24
mav 60,88,24
mav 64,92,24
s_waitcnt lgkmcnt(0)
s_waitcnt vmcnt(25)
s_barrier
Ta 120,4,24576
Ta 124,5,24576
Ta 128,6,24576
Ta 132,4,36864
Ta 136,5,36864
Ta 140,6,36864
E 0,98304
E 1,102400
E 2,106496
s_add_u32 s51, s51, 229376
E 0,110592
E 1,114688
E 2,118784
s_add_u32 s51, s51, 229376
mav 56,96,24
mav 60,100,24
mav 64,104,24
mav 56,108,24
mav 60,112,24
mav 64,116,24
s_waitcnt lgkmcnt(0)
s_waitcnt vmcnt(24)
s_barrier
v_add_u32 v4, -98304, v4
v_add_u32 v5, -98304, v5
v_add_u32 v6, -98304, v6
Ta 144,4,0
Ta 148,5,0
Ta 152,6,0
Ta 156,4,12288
Ta 160,5,12288
Ta 164,6,12288
E 0,122880
E 1,126976
E 2,131072
s_add_u32 s51, s51, 229376
E 0,135168
E 1,139264
E 2,143360
s_add_u32 s51, s51, 229376
s_cmp_ge_u32 s51, s57
s_cselect_b32 s60, 7340032, 0
s_sub_u32 s51, s51, s60
mav 56,120,24
mav 60,124,24
mav 64,128,24
mav 56,132,24
mav 60,136,24
mav 64,140,24
s_waitcnt lgkmcnt(0)
s_waitcnt vmcnt(24)
s_barrier
Ta 168,4,24576
Ta 172,5,24576
Ta 176,6,24576
Ta 180,4,36864
Ta 184,5,36864
Ta 188,6,36864
E 0,0
E 1,4096
E 2,8192
s_add_u32 s51, s51, 229376
E 0,12288
E 1,16384
E 2,20480
s_add_u32 s51, s51, 229376
mav 56,144,24
mav 60,148,24
mav 64,152,24
mav 56,156,24
mav 60,160,24
mav 64,164,24
s_waitcnt lgkmcnt(0)
s_waitcnt vmcnt(24)
s_barrier
v_add_u32 v4, 49152, v4
v_add_u32 v5, 49152, v5
v_add_u32 v6, 49152, v6
Ta 192,4,0
Ta 196,5,0
Ta 200,6,0
Ta 204,4,12288
Ta 208,5,12288
Ta 212,6,12288
E 0,24576
E 1,28672
E 2,32768
s_add_u32 s51, s51, 229376
E 0,36864
E 1,40960
E 2,45056
s_add_u32 s51, s51, 229376
mav 56,168,24
mav 60,172,24
mav 64,176,24
mav 56,180,24
mav 60,184,24
mav 64,188,24
s_waitcnt lgkmcnt(0)
s_waitcnt vmcnt(24)
s_barrier
Ta 216,4,24576
Ta 220,5,24576
Ta 224,6,24576
Ta 228,4,36864
Ta 232,5,36864
Ta 236,6,36864
E 0,49152
E 1,53248
E 2,57344
s_add_u32 s51, s51, 229376
E 0,61440
E 1,65536
E 2,69632
s_add_u32 s51, s51, 229376
mav 56,192,24
mav 60,196,24
mav 64,200,24
mav 56,204,24
mav 60,208,24
mav 64,212,24
s_waitcnt lgkmcnt(0)
s_waitcnt vmcnt(24)
s_barrier
v_add_u32 v4, 49152, v4
v_add_u32 v5, 49152, v5
v_add_u32 v6, 49152, v6
Ta 240,4,0
Ta 244,5,0
Ta 248,6,0
Ta 252,4,12288
Tv 128,5,12288
Tv 132,6,12288
E 0,73728
E 1,77824
E 2,81920
s_add_u32 s51, s51, 229376
E 0,86016
E 1,90112
E 2,94208
s_add_u32 s51, s51, 229376
s_cmp_ge_u32 s51, s57
s_cselect_b32 s60, 7340032, 0
s_sub_u32 s51, s51, s60
mav 56,216,24
mav 60,220,24
mav 64,224,24
mav 56,228,24
mav 60,232,24
mav 64,236,24
s_waitcnt lgkmcnt(0)
s_waitcnt vmcnt(24)
s_barrier
Tv 136,4,24576
Tv 140,5,24576
Tv 144,6,24576
Tv 148,4,36864
Tv 152,5,36864
Tv 156,6,36864
mav 56,240,24
mav 60,244,24
mav 64,248,24
mav 56,252,24
mvv 60,128,24
mvv 64,132,24
s_waitcnt lgkmcnt(0)
s_waitcnt vmcnt(18)
s_barrier
v_add_u32 v4, -98304, v4
v_add_u32 v5, -98304, v5
v_add_u32 v6, -98304, v6
Tv 160,4,0
Tv 164,5,0
Tv 168,6,0
Tv 172,4,12288
Tv 176,5,12288
Tv 180,6,12288
mvv 56,136,24
mvv 60,140,24
mvv 64,144,24
mvv 56,148,24
mvv 60,152,24
mvv 64,156,24
s_waitcnt lgkmcnt(0)
s_waitcnt vmcnt(12)
s_barrier
Tv 184,4,24576
Tv 188,5,24576
Tv 192,6,24576
Tv 196,4,36864
Tv 200,5,36864
Tv 204,6,36864
mvv 56,160,24
mvv 60,164,24
mvv 64,168,24
mvv 56,172,24
mvv 60,176,24
mvv 64,180,24
s_waitcnt lgkmcnt(0)
s_waitcnt vmcnt(6)
s_barrier
v_add_u32 v4, 49152, v4
v_add_u32 v5, 49152, v5
v_add_u32 v6, 49152, v6
Tv 208,4,0
Tv 212,5,0
Tv 216,6,0
Tv 220,4,12288
Tv 224,5,12288
Tv 228,6,12288
mvv 56,184,24
mvv 60,188,24
mvv 64,192,24
mvv 56,196,24
mvv 60,200,24
mvv 64,204,24
s_waitcnt lgkmcnt(0)
s_waitcnt vmcnt(0)
s_barrier
Tv 232,4,24576
Tv 236,5,24576
Tv 240,6,24576
Tv 244,4,36864
Tv 248,5,36864
Tv 252,6,36864
mvv 56,208,24
mvv 60,212,24
mvv 64,216,24
mvv 56,220,24
mvv 60,224,24
mvv 64,228,24
s_waitcnt lgkmcnt(0)
mvv 56,232,24
mvv 60,236,24
mvv 64,240,24
mvv 56,244,24
mvv 60,248,24
mvv 64,252,24
s_waitcnt vmcnt(0)
s_barrier
s_nop 7
s_and_b32 s60, s48, 3
s_cmp_eq_u32 s60, 1
s_cbranch_scc1 LvF1
s_cmp_eq_u32 s60, 2
s_cbranch_scc1 LvF2
LvF0:
buffer_load_dwordx2 v[106:107], v87, s[36:39], 0 offen sc1
s_waitcnt vmcnt(0)
ds_write_b32 v82, v106
v_sub_u32 v106, v106, v88
v_xor_b32 v107, v107, v77
v_cmp_lt_u64 vcc, s[62:63], v[106:107]
s_waitcnt lgkmcnt(0)
s_cbranch_vccnz CF0_1
BF0_1:
s_add_u32 s49, s49, 346112
s_barrier
s_add_u32 m0, s50, 0
s_nop 0
D 85,0
D 85,1024
D 85,2048
D 85,3072
s_add_u32 m0, s50, 4096
s_nop 0
D 86,0
D 86,1024
D 86,2048
D 86,3072
s_mov_b32 m0, 142336
s_nop 0
buffer_load_dword v87, s[36:39], 0 offen sc1 lds
v_add_u32 v88, 1, v88
buffer_load_dword v87, s[36:39], 0 offen sc1 lds
buffer_load_dword v87, s[36:39], 0 offen sc1 lds
ds_read_b32 v106, v82 offset:0
s_waitcnt lgkmcnt(0)
v_cmp_gt_i32 vcc, v88, v106
s_waitcnt vmcnt(3)
s_cbranch_vccnz CF0_2
BF0_2:
s_add_u32 s49, s49, 346112
s_barrier
R 24,0
s_add_u32 m0, s50, 32768
R 28,1024
D 85,0
R 32,2048
D 85,1024
R 36,3072
s_waitcnt lgkmcnt(2)
v_mfma_f32_16x16x32_bf16 v[0:3], a[0:3], v[24:27], 0
R 40,4096
D 85,2048
v_mfma_f32_16x16x32_bf16 v[4:7], a[4:7], v[24:27], 0
v_mfma_f32_16x16x32_bf16 v[8:11], a[8:11], v[24:27], 0
mav 0,12,28
R 44,5120
mav 4,16,28
D 85,3072
mav 8,20,28
s_waitcnt lgkmcnt(2)
mav 0,24,32
R 48,6144
mav 4,28,32
s_add_u32 m0, s50, 36864
mav 8,32,32
mav 0,36,36
R 52,7168
D 86,0
mav 4,40,36
mav 8,44,36
s_waitcnt lgkmcnt(2)
mav 0,48,40
R 24,8192
mav 4,52,40
D 86,1024
mav 8,56,40
mav 0,60,44
R 28,9216
mav 4,64,44
mav 8,68,44
s_waitcnt lgkmcnt(2)
mav 0,72,48
R 32,10240
D 86,2048
mav 4,76,48
mav 8,80,48
mav 0,84,52
R 36,11264
mav 4,88,52
D 86,3072
mav 8,92,52
s_waitcnt lgkmcnt(2)
mav 0,96,24
R 40,12288
mav 4,100,24
mav 8,104,24
mav 0,108,28
R 44,13312
mav 4,112,28
mav 8,116,28
s_waitcnt lgkmcnt(2)
mav 0,120,32
R 48,14336
s_mov_b32 m0, 142336
mav 4,124,32
mav 8,128,32
buffer_load_dword v87, s[36:39], 0 offen sc1 lds
mav 0,132,36
R 52,15360
mav 4,136,36
mav 8,140,36
s_waitcnt lgkmcnt(2)
mav 0,144,40
R 24,16384
mav 4,148,40
mav 8,152,40
mav 0,156,44
R 28,17408
mav 4,160,44
v_add_u32 v88, 1, v88
mav 8,164,44
s_waitcnt lgkmcnt(2)
mav 0,168,48
R 32,18432
mav 4,172,48
mav 8,176,48
buffer_load_dword v87, s[36:39], 0 offen sc1 lds
mav 0,180,52
R 36,19456
mav 4,184,52
mav 8,188,52
s_waitcnt lgkmcnt(2)
mav 0,192,24
R 40,20480
mav 4,196,24
mav 8,200,24
mav 0,204,28
R 44,21504
mav 4,208,28
mav 8,212,28
s_waitcnt lgkmcnt(2)
mav 0,216,32
R 48,22528
mav 4,220,32
mav 8,224,32
buffer_load_dword v87, s[36:39], 0 offen sc1 lds
mav 0,228,36
R 52,23552
mav 4,232,36
mav 8,236,36
s_waitcnt lgkmcnt(2)
mav 0,240,40
R 24,24576
mav 4,244,40
mav 8,248,40
mav 0,252,44
R 28,25600
mvv 4,128,44
mvv 8,132,44
s_waitcnt lgkmcnt(2)
mvv 0,136,48
R 32,26624
mvv 4,140,48
mvv 8,144,48
mvv 0,148,52
R 36,27648
mvv 4,152,52
mvv 8,156,52
s_waitcnt lgkmcnt(2)
mvv 0,160,24
R 40,28672
mvv 4,164,24
ds_read_b32 v106, v82 offset:0
mvv 8,168,24
mvv 0,172,28
R 44,29696
mvv 4,176,28
mvv 8,180,28
s_waitcnt lgkmcnt(3)
mvv 0,184,32
R 48,30720
s_xor_b32 s50, s50, 0x10000
mvv 4,188,32
mvv 8,192,32
mvv 0,196,36
R 52,31744
mvv 4,200,36
s_waitcnt lgkmcnt(2)
v_cmp_gt_i32 vcc, v88, v106
mvv 8,204,36
s_waitcnt vmcnt(3)
s_cbranch_vccnz CF0_3
BF0_3:
mvv 0,208,40
s_barrier
R 24,32768
mvv 4,212,40
s_add_u32 s49, s49, 346112
s_add_u32 m0, s50, 0
mvv 8,216,40
ds_read_b64 v[78:79], v89 offset:0
mvv 0,220,44
R 28,33792
D 85,0
mvv 4,224,44
mvv 8,228,44
s_waitcnt lgkmcnt(3)
mvv 0,232,48
R 32,34816
mvv 4,236,48
D 85,1024
mvv 8,240,48
mvv 0,244,52
R 36,35840
mvv 4,248,52
mvv 8,252,52
s_waitcnt lgkmcnt(2)
v_mfma_f32_16x16x32_bf16 v[12:15], a[0:3], v[24:27], 0
R 40,36864
D 85,2048
v_mfma_f32_16x16x32_bf16 v[16:19], a[4:7], v[24:27], 0
v_fma_f32 v104, v79, v56, v92
v_fma_f32 v0, v0, v78, v104
v_mfma_f32_16x16x32_bf16 v[20:23], a[8:11], v[24:27], 0
v_fma_f32 v105, v79, v57, v93
v_fma_f32 v1, v1, v78, v105
mav 12,12,28
R 44,37888
v_mul_f32_e64 v0, v0, |v0|
mav 16,16,28
D 85,3072
v_mul_f32_e64 v1, v1, |v1|
mav 20,20,28
v_cvt_pk_bf16_f32 v0, v0, v1
s_waitcnt lgkmcnt(2)
mav 12,24,32
R 48,38912
v_pk_max_i16 v68, v0, 0
mav 16,28,32
s_add_u32 m0, s50, 4096
v_fma_f32 v104, v79, v58, v94
mav 20,32,32
v_fma_f32 v2, v2, v78, v104
v_fma_f32 v105, v79, v59, v95
mav 12,36,36
R 52,39936
D 86,0
mav 16,40,36
v_fma_f32 v3, v3, v78, v105
v_mul_f32_e64 v2, v2, |v2|
mav 20,44,36
v_mul_f32_e64 v3, v3, |v3|
s_waitcnt lgkmcnt(2)
mav 12,48,40
R 24,40960
v_cvt_pk_bf16_f32 v2, v2, v3
mav 16,52,40
D 86,1024
v_pk_max_i16 v69, v2, 0
mav 20,56,40
v_fma_f32 v104, v79, v60, v96
v_fma_f32 v4, v4, v78, v104
mav 12,60,44
R 28,41984
v_fma_f32 v105, v79, v61, v97
mav 16,64,44
v_fma_f32 v5, v5, v78, v105
v_mul_f32_e64 v4, v4, |v4|
mav 20,68,44
v_mul_f32_e64 v5, v5, |v5|
s_waitcnt lgkmcnt(2)
mav 12,72,48
R 32,43008
D 86,2048
mav 16,76,48
v_cvt_pk_bf16_f32 v4, v4, v5
v_pk_max_i16 v70, v4, 0
mav 20,80,48
v_fma_f32 v104, v79, v62, v98
v_fma_f32 v6, v6, v78, v104
mav 12,84,52
R 36,44032
v_fma_f32 v105, v79, v63, v99
mav 16,88,52
D 86,3072
v_fma_f32 v7, v7, v78, v105
mav 20,92,52
v_mul_f32_e64 v6, v6, |v6|
s_waitcnt lgkmcnt(2)
mav 12,96,24
R 40,45056
v_mul_f32_e64 v7, v7, |v7|
mav 16,100,24
v_cvt_pk_bf16_f32 v6, v6, v7
v_pk_max_i16 v71, v6, 0
mav 20,104,24
buffer_store_dwordx4 v[68:71], v83, s[36:39], s49 offen sc1
v_fma_f32 v104, v79, v64, v100
mav 12,108,28
R 44,46080
v_fma_f32 v8, v8, v78, v104
mav 16,112,28
v_fma_f32 v105, v79, v65, v101
v_fma_f32 v9, v9, v78, v105
mav 20,116,28
v_mul_f32_e64 v8, v8, |v8|
s_waitcnt lgkmcnt(2)
mav 12,120,32
R 48,47104
s_mov_b32 m0, 142336
mav 16,124,32
v_mul_f32_e64 v9, v9, |v9|
v_cvt_pk_bf16_f32 v8, v8, v9
mav 20,128,32
buffer_load_dword v87, s[36:39], 0 offen sc1 lds
v_pk_max_i16 v72, v8, 0
mav 12,132,36
R 52,48128
v_pk_max_i16 v74, v8, 0
mav 16,136,36
v_fma_f32 v104, v79, v66, v102
v_fma_f32 v10, v10, v78, v104
mav 20,140,36
v_fma_f32 v105, v79, v67, v103
s_waitcnt lgkmcnt(2)
mav 12,144,40
R 24,49152
v_fma_f32 v11, v11, v78, v105
mav 16,148,40
v_mul_f32_e64 v10, v10, |v10|
v_mul_f32_e64 v11, v11, |v11|
mav 20,152,40
v_cvt_pk_bf16_f32 v10, v10, v11
v_pk_max_i16 v73, v10, 0
mav 12,156,44
R 28,50176
v_pk_max_i16 v75, v10, 0
mav 16,160,44
v_permlane16_swap_b32 v72, v74
v_permlane16_swap_b32 v73, v75
mav 20,164,44
buffer_store_dwordx4 v[72:75], v84, s[36:39], s49 offen sc1
s_waitcnt lgkmcnt(2)
mav 12,168,48
R 32,51200
v_add_u32 v88, 1, v88
mav 16,172,48
mav 20,176,48
buffer_load_dword v87, s[36:39], 0 offen sc1 lds
mav 12,180,52
R 36,52224
mav 16,184,52
mav 20,188,52
s_waitcnt lgkmcnt(2)
mav 12,192,24
R 40,53248
mav 16,196,24
mav 20,200,24
mav 12,204,28
R 44,54272
mav 16,208,28
mav 20,212,28
s_waitcnt lgkmcnt(2)
mav 12,216,32
R 48,55296
mav 16,220,32
mav 20,224,32
buffer_load_dword v87, s[36:39], 0 offen sc1 lds
mav 12,228,36
R 52,56320
mav 16,232,36
mav 20,236,36
s_waitcnt lgkmcnt(2)
mav 12,240,40
R 24,57344
mav 16,244,40
mav 20,248,40
mav 12,252,44
R 28,58368
mvv 16,128,44
mvv 20,132,44
s_waitcnt lgkmcnt(2)
mvv 12,136,48
R 32,59392
mvv 16,140,48
mvv 20,144,48
mvv 12,148,52
R 36,60416
mvv 16,152,52
mvv 20,156,52
s_waitcnt lgkmcnt(2)
mvv 12,160,24
R 40,61440
mvv 16,164,24
ds_read_b32 v106, v82 offset:0
mvv 20,168,24
v_add_u32 v76, 1, v76
mvv 12,172,28
R 44,62464
mvv 16,176,28
mvv 20,180,28
s_waitcnt lgkmcnt(3)
mvv 12,184,32
R 48,63488
mvv 16,188,32
mvv 20,192,32
s_waitcnt vmcnt(2)
buffer_store_dwordx2 v[76:77], v81, s[36:39], s52 offen sc1
mvv 12,196,36
R 52,64512
v_xor_b32 v80, 0x10000, v80
mvv 16,200,36
s_waitcnt lgkmcnt(2)
v_cmp_gt_i32 vcc, v88, v106
mvv 20,204,36
s_cbranch_vccnz CF0_4
BF0_4:
s_barrier
mvv 12,208,40
R 24,0
s_add_u32 s49, s49, 346112
mvv 16,212,40
s_add_u32 m0, s50, 32768
ds_read_b64 v[78:79], v89 offset:128
mvv 20,216,40
mvv 12,220,44
R 28,1024
D 85,0
mvv 16,224,44
)"
R"(
mvv 20,228,44
s_waitcnt lgkmcnt(3)
mvv 12,232,48
R 32,2048
mvv 16,236,48
D 85,1024
mvv 20,240,48
mvv 12,244,52
R 36,3072
mvv 16,248,52
mvv 20,252,52
s_mov_b32 s60, -30
.p2align 6
LF0_5:
s_waitcnt lgkmcnt(2)
v_mfma_f32_16x16x32_bf16 v[0:3], a[0:3], v[24:27], 0
R 40,4096
D 85,2048
v_mfma_f32_16x16x32_bf16 v[4:7], a[4:7], v[24:27], 0
v_fma_f32 v104, v79, v56, v92
v_fma_f32 v12, v12, v78, v104
v_mfma_f32_16x16x32_bf16 v[8:11], a[8:11], v[24:27], 0
v_fma_f32 v105, v79, v57, v93
v_fma_f32 v13, v13, v78, v105
mav 0,12,28
R 44,5120
v_mul_f32_e64 v12, v12, |v12|
mav 4,16,28
D 85,3072
v_mul_f32_e64 v13, v13, |v13|
mav 8,20,28
v_cvt_pk_bf16_f32 v12, v12, v13
s_waitcnt lgkmcnt(2)
mav 0,24,32
R 48,6144
v_pk_max_i16 v68, v12, 0
mav 4,28,32
s_add_u32 m0, s50, 36864
v_fma_f32 v104, v79, v58, v94
mav 8,32,32
v_fma_f32 v14, v14, v78, v104
v_fma_f32 v105, v79, v59, v95
mav 0,36,36
R 52,7168
D 86,0
mav 4,40,36
v_fma_f32 v15, v15, v78, v105
v_mul_f32_e64 v14, v14, |v14|
mav 8,44,36
v_mul_f32_e64 v15, v15, |v15|
s_waitcnt lgkmcnt(2)
mav 0,48,40
R 24,8192
v_cvt_pk_bf16_f32 v14, v14, v15
mav 4,52,40
D 86,1024
v_pk_max_i16 v69, v14, 0
mav 8,56,40
v_fma_f32 v104, v79, v60, v96
v_fma_f32 v16, v16, v78, v104
mav 0,60,44
R 28,9216
v_fma_f32 v105, v79, v61, v97
mav 4,64,44
v_fma_f32 v17, v17, v78, v105
v_mul_f32_e64 v16, v16, |v16|
mav 8,68,44
v_mul_f32_e64 v17, v17, |v17|
s_waitcnt lgkmcnt(2)
mav 0,72,48
R 32,10240
D 86,2048
mav 4,76,48
v_cvt_pk_bf16_f32 v16, v16, v17
v_pk_max_i16 v70, v16, 0
mav 8,80,48
v_fma_f32 v104, v79, v62, v98
v_fma_f32 v18, v18, v78, v104
mav 0,84,52
R 36,11264
v_fma_f32 v105, v79, v63, v99
mav 4,88,52
D 86,3072
v_fma_f32 v19, v19, v78, v105
mav 8,92,52
v_mul_f32_e64 v18, v18, |v18|
s_waitcnt lgkmcnt(2)
mav 0,96,24
R 40,12288
v_mul_f32_e64 v19, v19, |v19|
mav 4,100,24
v_cvt_pk_bf16_f32 v18, v18, v19
v_pk_max_i16 v71, v18, 0
mav 8,104,24
buffer_store_dwordx4 v[68:71], v83, s[36:39], s49 offen sc1
v_fma_f32 v104, v79, v64, v100
mav 0,108,28
R 44,13312
v_fma_f32 v20, v20, v78, v104
mav 4,112,28
v_fma_f32 v105, v79, v65, v101
v_fma_f32 v21, v21, v78, v105
mav 8,116,28
v_mul_f32_e64 v20, v20, |v20|
s_waitcnt lgkmcnt(2)
mav 0,120,32
R 48,14336
s_mov_b32 m0, 142336
mav 4,124,32
v_mul_f32_e64 v21, v21, |v21|
v_cvt_pk_bf16_f32 v20, v20, v21
mav 8,128,32
buffer_load_dword v87, s[36:39], 0 offen sc1 lds
v_pk_max_i16 v72, v20, 0
mav 0,132,36
R 52,15360
v_pk_max_i16 v74, v20, 0
mav 4,136,36
v_fma_f32 v104, v79, v66, v102
v_fma_f32 v22, v22, v78, v104
mav 8,140,36
v_fma_f32 v105, v79, v67, v103
s_waitcnt lgkmcnt(2)
mav 0,144,40
R 24,16384
v_fma_f32 v23, v23, v78, v105
mav 4,148,40
v_mul_f32_e64 v22, v22, |v22|
v_mul_f32_e64 v23, v23, |v23|
mav 8,152,40
v_cvt_pk_bf16_f32 v22, v22, v23
v_pk_max_i16 v73, v22, 0
mav 0,156,44
R 28,17408
v_pk_max_i16 v75, v22, 0
mav 4,160,44
v_permlane16_swap_b32 v72, v74
v_permlane16_swap_b32 v73, v75
mav 8,164,44
buffer_store_dwordx4 v[72:75], v84, s[36:39], s49 offen sc1
s_waitcnt lgkmcnt(2)
mav 0,168,48
R 32,18432
v_add_u32 v88, 1, v88
mav 4,172,48
mav 8,176,48
buffer_load_dword v87, s[36:39], 0 offen sc1 lds
mav 0,180,52
R 36,19456
mav 4,184,52
mav 8,188,52
s_waitcnt lgkmcnt(2)
mav 0,192,24
R 40,20480
mav 4,196,24
mav 8,200,24
mav 0,204,28
R 44,21504
mav 4,208,28
mav 8,212,28
s_waitcnt lgkmcnt(2)
mav 0,216,32
R 48,22528
mav 4,220,32
mav 8,224,32
buffer_load_dword v87, s[36:39], 0 offen sc1 lds
mav 0,228,36
R 52,23552
mav 4,232,36
mav 8,236,36
s_waitcnt lgkmcnt(2)
mav 0,240,40
R 24,24576
mav 4,244,40
mav 8,248,40
mav 0,252,44
R 28,25600
mvv 4,128,44
mvv 8,132,44
s_waitcnt lgkmcnt(2)
mvv 0,136,48
R 32,26624
mvv 4,140,48
mvv 8,144,48
mvv 0,148,52
R 36,27648
mvv 4,152,52
mvv 8,156,52
s_waitcnt lgkmcnt(2)
mvv 0,160,24
R 40,28672
mvv 4,164,24
ds_read_b32 v106, v82 offset:0
mvv 8,168,24
v_add_u32 v76, 1, v76
mvv 0,172,28
R 44,29696
mvv 4,176,28
mvv 8,180,28
s_waitcnt lgkmcnt(3)
mvv 0,184,32
R 48,30720
s_xor_b32 s50, s50, 0x10000
mvv 4,188,32
mvv 8,192,32
s_waitcnt vmcnt(2)
buffer_store_dwordx2 v[76:77], v81, s[36:39], s52 offen sc1
mvv 0,196,36
R 52,31744
mvv 4,200,36
s_waitcnt lgkmcnt(2)
v_cmp_gt_i32 vcc, v88, v106
mvv 8,204,36
s_cbranch_vccnz CF0_6
BF0_6:
s_barrier
mvv 0,208,40
R 24,32768
s_add_u32 s49, s49, 346112
mvv 4,212,40
s_add_u32 m0, s50, 0
ds_read_b64 v[78:79], v89 offset:0
mvv 8,216,40
mvv 0,220,44
R 28,33792
D 85,0
mvv 4,224,44
mvv 8,228,44
s_waitcnt lgkmcnt(3)
mvv 0,232,48
R 32,34816
mvv 4,236,48
D 85,1024
mvv 8,240,48
mvv 0,244,52
R 36,35840
mvv 4,248,52
mvv 8,252,52
s_waitcnt lgkmcnt(2)
v_mfma_f32_16x16x32_bf16 v[12:15], a[0:3], v[24:27], 0
R 40,36864
D 85,2048
v_mfma_f32_16x16x32_bf16 v[16:19], a[4:7], v[24:27], 0
v_fma_f32 v104, v79, v56, v92
v_fma_f32 v0, v0, v78, v104
v_mfma_f32_16x16x32_bf16 v[20:23], a[8:11], v[24:27], 0
v_fma_f32 v105, v79, v57, v93
v_fma_f32 v1, v1, v78, v105
mav 12,12,28
R 44,37888
v_mul_f32_e64 v0, v0, |v0|
mav 16,16,28
D 85,3072
v_mul_f32_e64 v1, v1, |v1|
mav 20,20,28
v_cvt_pk_bf16_f32 v0, v0, v1
s_waitcnt lgkmcnt(2)
mav 12,24,32
R 48,38912
v_pk_max_i16 v68, v0, 0
mav 16,28,32
s_add_u32 m0, s50, 4096
v_fma_f32 v104, v79, v58, v94
mav 20,32,32
v_fma_f32 v2, v2, v78, v104
v_fma_f32 v105, v79, v59, v95
mav 12,36,36
R 52,39936
D 86,0
mav 16,40,36
v_fma_f32 v3, v3, v78, v105
v_mul_f32_e64 v2, v2, |v2|
mav 20,44,36
v_mul_f32_e64 v3, v3, |v3|
s_waitcnt lgkmcnt(2)
mav 12,48,40
R 24,40960
v_cvt_pk_bf16_f32 v2, v2, v3
mav 16,52,40
D 86,1024
v_pk_max_i16 v69, v2, 0
mav 20,56,40
v_fma_f32 v104, v79, v60, v96
v_fma_f32 v4, v4, v78, v104
mav 12,60,44
R 28,41984
v_fma_f32 v105, v79, v61, v97
mav 16,64,44
v_fma_f32 v5, v5, v78, v105
v_mul_f32_e64 v4, v4, |v4|
mav 20,68,44
v_mul_f32_e64 v5, v5, |v5|
s_waitcnt lgkmcnt(2)
mav 12,72,48
R 32,43008
D 86,2048
mav 16,76,48
v_cvt_pk_bf16_f32 v4, v4, v5
v_pk_max_i16 v70, v4, 0
mav 20,80,48
v_fma_f32 v104, v79, v62, v98
v_fma_f32 v6, v6, v78, v104
mav 12,84,52
R 36,44032
v_fma_f32 v105, v79, v63, v99
mav 16,88,52
D 86,3072
v_fma_f32 v7, v7, v78, v105
mav 20,92,52
v_mul_f32_e64 v6, v6, |v6|
s_waitcnt lgkmcnt(2)
mav 12,96,24
R 40,45056
v_mul_f32_e64 v7, v7, |v7|
mav 16,100,24
v_cvt_pk_bf16_f32 v6, v6, v7
v_pk_max_i16 v71, v6, 0
mav 20,104,24
buffer_store_dwordx4 v[68:71], v83, s[36:39], s49 offen sc1
v_fma_f32 v104, v79, v64, v100
mav 12,108,28
R 44,46080
v_fma_f32 v8, v8, v78, v104
mav 16,112,28
v_fma_f32 v105, v79, v65, v101
v_fma_f32 v9, v9, v78, v105
mav 20,116,28
v_mul_f32_e64 v8, v8, |v8|
s_waitcnt lgkmcnt(2)
mav 12,120,32
R 48,47104
s_mov_b32 m0, 142336
mav 16,124,32
v_mul_f32_e64 v9, v9, |v9|
v_cvt_pk_bf16_f32 v8, v8, v9
mav 20,128,32
buffer_load_dword v87, s[36:39], 0 offen sc1 lds
v_pk_max_i16 v72, v8, 0
mav 12,132,36
R 52,48128
v_pk_max_i16 v74, v8, 0
mav 16,136,36
v_fma_f32 v104, v79, v66, v102
v_fma_f32 v10, v10, v78, v104
mav 20,140,36
v_fma_f32 v105, v79, v67, v103
s_waitcnt lgkmcnt(2)
mav 12,144,40
R 24,49152
v_fma_f32 v11, v11, v78, v105
mav 16,148,40
v_mul_f32_e64 v10, v10, |v10|
v_mul_f32_e64 v11, v11, |v11|
mav 20,152,40
v_cvt_pk_bf16_f32 v10, v10, v11
v_pk_max_i16 v73, v10, 0
mav 12,156,44
R 28,50176
v_pk_max_i16 v75, v10, 0
mav 16,160,44
v_permlane16_swap_b32 v72, v74
v_permlane16_swap_b32 v73, v75
mav 20,164,44
buffer_store_dwordx4 v[72:75], v84, s[36:39], s49 offen sc1
s_waitcnt lgkmcnt(2)
mav 12,168,48
R 32,51200
v_add_u32 v88, 1, v88
mav 16,172,48
mav 20,176,48
buffer_load_dword v87, s[36:39], 0 offen sc1 lds
mav 12,180,52
R 36,52224
mav 16,184,52
mav 20,188,52
s_waitcnt lgkmcnt(2)
mav 12,192,24
R 40,53248
mav 16,196,24
mav 20,200,24
mav 12,204,28
R 44,54272
mav 16,208,28
mav 20,212,28
s_waitcnt lgkmcnt(2)
mav 12,216,32
R 48,55296
mav 16,220,32
mav 20,224,32
buffer_load_dword v87, s[36:39], 0 offen sc1 lds
mav 12,228,36
R 52,56320
mav 16,232,36
mav 20,236,36
s_waitcnt lgkmcnt(2)
mav 12,240,40
R 24,57344
mav 16,244,40
mav 20,248,40
mav 12,252,44
R 28,58368
mvv 16,128,44
mvv 20,132,44
s_waitcnt lgkmcnt(2)
mvv 12,136,48
R 32,59392
mvv 16,140,48
mvv 20,144,48
mvv 12,148,52
R 36,60416
mvv 16,152,52
mvv 20,156,52
s_waitcnt lgkmcnt(2)
mvv 12,160,24
R 40,61440
mvv 16,164,24
ds_read_b32 v106, v82 offset:0
mvv 20,168,24
v_add_u32 v76, 1, v76
mvv 12,172,28
R 44,62464
mvv 16,176,28
mvv 20,180,28
s_waitcnt lgkmcnt(3)
mvv 12,184,32
R 48,63488
mvv 16,188,32
mvv 20,192,32
s_waitcnt vmcnt(2)
buffer_store_dwordx2 v[76:77], v81, s[36:39], s52 offen sc1
mvv 12,196,36
R 52,64512
v_xor_b32 v80, 0x10000, v80
mvv 16,200,36
s_waitcnt lgkmcnt(2)
v_cmp_gt_i32 vcc, v88, v106
mvv 20,204,36
s_cbranch_vccnz CF0_7
BF0_7:
s_barrier
mvv 12,208,40
R 24,0
s_add_u32 s49, s49, 346112
mvv 16,212,40
s_add_u32 m0, s50, 32768
ds_read_b64 v[78:79], v89 offset:128
mvv 20,216,40
mvv 12,220,44
R 28,1024
D 85,0
mvv 16,224,44
mvv 20,228,44
s_waitcnt lgkmcnt(3)
mvv 12,232,48
R 32,2048
mvv 16,236,48
D 85,1024
mvv 20,240,48
mvv 12,244,52
R 36,3072
mvv 16,248,52
mvv 20,252,52
s_add_u32 s60, s60, 1
s_cbranch_scc0 LF0_5
s_waitcnt lgkmcnt(2)
v_mfma_f32_16x16x32_bf16 v[0:3], a[0:3], v[24:27], 0
R 40,4096
D 85,2048
v_mfma_f32_16x16x32_bf16 v[4:7], a[4:7], v[24:27], 0
v_fma_f32 v104, v79, v56, v92
v_fma_f32 v12, v12, v78, v104
v_mfma_f32_16x16x32_bf16 v[8:11], a[8:11], v[24:27], 0
v_fma_f32 v105, v79, v57, v93
v_fma_f32 v13, v13, v78, v105
mav 0,12,28
R 44,5120
v_mul_f32_e64 v12, v12, |v12|
mav 4,16,28
D 85,3072
v_mul_f32_e64 v13, v13, |v13|
mav 8,20,28
v_cvt_pk_bf16_f32 v12, v12, v13
s_waitcnt lgkmcnt(2)
mav 0,24,32
R 48,6144
v_pk_max_i16 v68, v12, 0
mav 4,28,32
s_add_u32 m0, s50, 36864
v_fma_f32 v104, v79, v58, v94
mav 8,32,32
v_fma_f32 v14, v14, v78, v104
v_fma_f32 v105, v79, v59, v95
mav 0,36,36
R 52,7168
D 86,0
mav 4,40,36
v_fma_f32 v15, v15, v78, v105
v_mul_f32_e64 v14, v14, |v14|
mav 8,44,36
v_mul_f32_e64 v15, v15, |v15|
s_waitcnt lgkmcnt(2)
mav 0,48,40
R 24,8192
v_cvt_pk_bf16_f32 v14, v14, v15
mav 4,52,40
D 86,1024
v_pk_max_i16 v69, v14, 0
mav 8,56,40
v_fma_f32 v104, v79, v60, v96
v_fma_f32 v16, v16, v78, v104
mav 0,60,44
R 28,9216
v_fma_f32 v105, v79, v61, v97
mav 4,64,44
v_fma_f32 v17, v17, v78, v105
v_mul_f32_e64 v16, v16, |v16|
mav 8,68,44
v_mul_f32_e64 v17, v17, |v17|
s_waitcnt lgkmcnt(2)
mav 0,72,48
R 32,10240
D 86,2048
mav 4,76,48
v_cvt_pk_bf16_f32 v16, v16, v17
v_pk_max_i16 v70, v16, 0
mav 8,80,48
v_fma_f32 v104, v79, v62, v98
v_fma_f32 v18, v18, v78, v104
mav 0,84,52
R 36,11264
v_fma_f32 v105, v79, v63, v99
mav 4,88,52
D 86,3072
v_fma_f32 v19, v19, v78, v105
mav 8,92,52
v_mul_f32_e64 v18, v18, |v18|
s_waitcnt lgkmcnt(2)
mav 0,96,24
R 40,12288
v_mul_f32_e64 v19, v19, |v19|
mav 4,100,24
v_cvt_pk_bf16_f32 v18, v18, v19
v_pk_max_i16 v71, v18, 0
mav 8,104,24
buffer_store_dwordx4 v[68:71], v83, s[36:39], s49 offen sc1
v_fma_f32 v104, v79, v64, v100
mav 0,108,28
R 44,13312
v_fma_f32 v20, v20, v78, v104
mav 4,112,28
v_fma_f32 v105, v79, v65, v101
v_fma_f32 v21, v21, v78, v105
mav 8,116,28
v_mul_f32_e64 v20, v20, |v20|
s_waitcnt lgkmcnt(2)
mav 0,120,32
R 48,14336
s_mov_b32 m0, 142336
mav 4,124,32
v_mul_f32_e64 v21, v21, |v21|
v_cvt_pk_bf16_f32 v20, v20, v21
mav 8,128,32
buffer_load_dword v87, s[36:39], 0 offen sc1 lds
v_pk_max_i16 v72, v20, 0
mav 0,132,36
R 52,15360
v_pk_max_i16 v74, v20, 0
mav 4,136,36
v_fma_f32 v104, v79, v66, v102
v_fma_f32 v22, v22, v78, v104
mav 8,140,36
v_fma_f32 v105, v79, v67, v103
s_waitcnt lgkmcnt(2)
mav 0,144,40
R 24,16384
v_fma_f32 v23, v23, v78, v105
mav 4,148,40
v_mul_f32_e64 v22, v22, |v22|
v_mul_f32_e64 v23, v23, |v23|
mav 8,152,40
v_cvt_pk_bf16_f32 v22, v22, v23
v_pk_max_i16 v73, v22, 0
mav 0,156,44
R 28,17408
v_pk_max_i16 v75, v22, 0
mav 4,160,44
v_permlane16_swap_b32 v72, v74
v_permlane16_swap_b32 v73, v75
mav 8,164,44
buffer_store_dwordx4 v[72:75], v84, s[36:39], s49 offen sc1
s_waitcnt lgkmcnt(2)
mav 0,168,48
R 32,18432
v_add_u32 v88, 1, v88
mav 4,172,48
mav 8,176,48
buffer_load_dword v87, s[36:39], 0 offen sc1 lds
mav 0,180,52
R 36,19456
mav 4,184,52
mav 8,188,52
s_waitcnt lgkmcnt(2)
mav 0,192,24
R 40,20480
mav 4,196,24
mav 8,200,24
mav 0,204,28
R 44,21504
mav 4,208,28
mav 8,212,28
s_waitcnt lgkmcnt(2)
mav 0,216,32
R 48,22528
mav 4,220,32
mav 8,224,32
buffer_load_dword v87, s[36:39], 0 offen sc1 lds
mav 0,228,36
R 52,23552
mav 4,232,36
mav 8,236,36
s_waitcnt lgkmcnt(2)
mav 0,240,40
R 24,24576
mav 4,244,40
mav 8,248,40
mav 0,252,44
R 28,25600
mvv 4,128,44
mvv 8,132,44
s_waitcnt lgkmcnt(2)
mvv 0,136,48
R 32,26624
mvv 4,140,48
mvv 8,144,48
mvv 0,148,52
R 36,27648
mvv 4,152,52
mvv 8,156,52
s_waitcnt lgkmcnt(2)
mvv 0,160,24
R 40,28672
mvv 4,164,24
ds_read_b32 v106, v82 offset:0
mvv 8,168,24
v_add_u32 v76, 1, v76
mvv 0,172,28
R 44,29696
mvv 4,176,28
mvv 8,180,28
s_waitcnt lgkmcnt(3)
mvv 0,184,32
R 48,30720
mvv 4,188,32
mvv 8,192,32
s_waitcnt vmcnt(2)
buffer_store_dwordx2 v[76:77], v81, s[36:39], s52 offen sc1
mvv 0,196,36
R 52,31744
mvv 4,200,36
s_waitcnt lgkmcnt(2)
v_cmp_gt_i32 vcc, v88, v106
mvv 8,204,36
s_cbranch_vccnz CF0_8
BF0_8:
s_barrier
mvv 0,208,40
R 24,32768
s_add_u32 s49, s49, 346112
mvv 4,212,40
ds_read_b64 v[78:79], v89 offset:0
mvv 8,216,40
mvv 0,220,44
R 28,33792
mvv 4,224,44
mvv 8,228,44
s_waitcnt lgkmcnt(3)
mvv 0,232,48
R 32,34816
mvv 4,236,48
mvv 8,240,48
mvv 0,244,52
R 36,35840
mvv 4,248,52
mvv 8,252,52
s_waitcnt lgkmcnt(2)
v_mfma_f32_16x16x32_bf16 v[12:15], a[0:3], v[24:27], 0
R 40,36864
v_fma_f32 v104, v79, v56, v92
v_mfma_f32_16x16x32_bf16 v[16:19], a[4:7], v[24:27], 0
v_fma_f32 v0, v0, v78, v104
v_fma_f32 v105, v79, v57, v93
v_mfma_f32_16x16x32_bf16 v[20:23], a[8:11], v[24:27], 0
v_fma_f32 v1, v1, v78, v105
v_mul_f32_e64 v0, v0, |v0|
mav 12,12,28
R 44,37888
v_mul_f32_e64 v1, v1, |v1|
mav 16,16,28
v_cvt_pk_bf16_f32 v0, v0, v1
v_pk_max_i16 v68, v0, 0
mav 20,20,28
v_fma_f32 v104, v79, v58, v94
s_waitcnt lgkmcnt(2)
mav 12,24,32
R 48,38912
v_fma_f32 v2, v2, v78, v104
mav 16,28,32
v_fma_f32 v105, v79, v59, v95
v_fma_f32 v3, v3, v78, v105
mav 20,32,32
v_mul_f32_e64 v2, v2, |v2|
v_mul_f32_e64 v3, v3, |v3|
mav 12,36,36
R 52,39936
v_cvt_pk_bf16_f32 v2, v2, v3
mav 16,40,36
v_pk_max_i16 v69, v2, 0
v_fma_f32 v104, v79, v60, v96
mav 20,44,36
v_fma_f32 v4, v4, v78, v104
s_waitcnt lgkmcnt(2)
mav 12,48,40
R 24,40960
v_fma_f32 v105, v79, v61, v97
mav 16,52,40
v_fma_f32 v5, v5, v78, v105
v_mul_f32_e64 v4, v4, |v4|
mav 20,56,40
v_mul_f32_e64 v5, v5, |v5|
v_cvt_pk_bf16_f32 v4, v4, v5
mav 12,60,44
R 28,41984
v_pk_max_i16 v70, v4, 0
mav 16,64,44
v_fma_f32 v104, v79, v62, v98
v_fma_f32 v6, v6, v78, v104
mav 20,68,44
v_fma_f32 v105, v79, v63, v99
s_waitcnt lgkmcnt(2)
mav 12,72,48
R 32,43008
v_fma_f32 v7, v7, v78, v105
mav 16,76,48
v_mul_f32_e64 v6, v6, |v6|
v_mul_f32_e64 v7, v7, |v7|
mav 20,80,48
v_cvt_pk_bf16_f32 v6, v6, v7
v_pk_max_i16 v71, v6, 0
mav 12,84,52
R 36,44032
buffer_store_dwordx4 v[68:71], v83, s[36:39], s49 offen sc1
mav 16,88,52
v_fma_f32 v104, v79, v64, v100
v_fma_f32 v8, v8, v78, v104
mav 20,92,52
v_fma_f32 v105, v79, v65, v101
s_waitcnt lgkmcnt(2)
mav 12,96,24
R 40,45056
v_fma_f32 v9, v9, v78, v105
mav 16,100,24
v_mul_f32_e64 v8, v8, |v8|
v_mul_f32_e64 v9, v9, |v9|
mav 20,104,24
v_cvt_pk_bf16_f32 v8, v8, v9
v_pk_max_i16 v72, v8, 0
mav 12,108,28
R 44,46080
v_pk_max_i16 v74, v8, 0
mav 16,112,28
v_fma_f32 v104, v79, v66, v102
v_fma_f32 v10, v10, v78, v104
mav 20,116,28
v_fma_f32 v105, v79, v67, v103
s_waitcnt lgkmcnt(2)
mav 12,120,32
R 48,47104
v_fma_f32 v11, v11, v78, v105
mav 16,124,32
v_mul_f32_e64 v10, v10, |v10|
v_mul_f32_e64 v11, v11, |v11|
mav 20,128,32
v_cvt_pk_bf16_f32 v10, v10, v11
v_pk_max_i16 v73, v10, 0
mav 12,132,36
R 52,48128
v_pk_max_i16 v75, v10, 0
mav 16,136,36
v_permlane16_swap_b32 v72, v74
v_permlane16_swap_b32 v73, v75
mav 20,140,36
buffer_store_dwordx4 v[72:75], v84, s[36:39], s49 offen sc1
s_waitcnt lgkmcnt(2)
mav 12,144,40
R 24,49152
mav 16,148,40
mav 20,152,40
mav 12,156,44
R 28,50176
mav 16,160,44
mav 20,164,44
s_waitcnt lgkmcnt(2)
mav 12,168,48
R 32,51200
mav 16,172,48
mav 20,176,48
mav 12,180,52
R 36,52224
mav 16,184,52
mav 20,188,52
s_waitcnt lgkmcnt(2)
mav 12,192,24
R 40,53248
mav 16,196,24
mav 20,200,24
mav 12,204,28
R 44,54272
mav 16,208,28
mav 20,212,28
s_waitcnt lgkmcnt(2)
mav 12,216,32
R 48,55296
mav 16,220,32
mav 20,224,32
mav 12,228,36
R 52,56320
mav 16,232,36
mav 20,236,36
s_waitcnt lgkmcnt(2)
mav 12,240,40
R 24,57344
mav 16,244,40
mav 20,248,40
mav 12,252,44
R 28,58368
mvv 16,128,44
mvv 20,132,44
s_waitcnt lgkmcnt(2)
mvv 12,136,48
R 32,59392
mvv 16,140,48
mvv 20,144,48
mvv 12,148,52
R 36,60416
mvv 16,152,52
mvv 20,156,52
s_waitcnt lgkmcnt(2)
mvv 12,160,24
R 40,61440
mvv 16,164,24
mvv 20,168,24
v_add_u32 v76, 1, v76
mvv 12,172,28
R 44,62464
mvv 16,176,28
mvv 20,180,28
s_waitcnt lgkmcnt(2)
mvv 12,184,32
R 48,63488
mvv 16,188,32
mvv 20,192,32
s_waitcnt vmcnt(0)
buffer_store_dwordx2 v[76:77], v81, s[36:39], s52 offen sc1
mvv 12,196,36
R 52,64512
mvv 16,200,36
s_add_u32 s49, s49, 346112
mvv 20,204,36
s_barrier
s_waitcnt lgkmcnt(2)
mvv 12,208,40
mvv 16,212,40
ds_read_b64 v[78:79], v89 offset:128
mvv 20,216,40
mvv 12,220,44
mvv 16,224,44
mvv 20,228,44
s_waitcnt lgkmcnt(1)
mvv 12,232,48
mvv 16,236,48
mvv 20,240,48
mvv 12,244,52
mvv 16,248,52
mvv 20,252,52
s_waitcnt lgkmcnt(0)
v_fma_f32 v104, v79, v56, v92
s_nop 3
v_fma_f32 v12, v12, v78, v104
v_fma_f32 v105, v79, v57, v93
v_fma_f32 v13, v13, v78, v105
v_mul_f32_e64 v12, v12, |v12|
v_mul_f32_e64 v13, v13, |v13|
v_cvt_pk_bf16_f32 v12, v12, v13
v_pk_max_i16 v68, v12, 0
v_fma_f32 v104, v79, v58, v94
v_fma_f32 v14, v14, v78, v104
v_fma_f32 v105, v79, v59, v95
v_fma_f32 v15, v15, v78, v105
v_mul_f32_e64 v14, v14, |v14|
v_mul_f32_e64 v15, v15, |v15|
v_cvt_pk_bf16_f32 v14, v14, v15
v_pk_max_i16 v69, v14, 0
v_fma_f32 v104, v79, v60, v96
v_fma_f32 v16, v16, v78, v104
v_fma_f32 v105, v79, v61, v97
v_fma_f32 v17, v17, v78, v105
v_mul_f32_e64 v16, v16, |v16|
v_mul_f32_e64 v17, v17, |v17|
v_cvt_pk_bf16_f32 v16, v16, v17
v_pk_max_i16 v70, v16, 0
v_fma_f32 v104, v79, v62, v98
v_fma_f32 v18, v18, v78, v104
v_fma_f32 v105, v79, v63, v99
v_fma_f32 v19, v19, v78, v105
v_mul_f32_e64 v18, v18, |v18|
v_mul_f32_e64 v19, v19, |v19|
v_cvt_pk_bf16_f32 v18, v18, v19
v_pk_max_i16 v71, v18, 0
buffer_store_dwordx4 v[68:71], v83, s[36:39], s49 offen sc1
v_fma_f32 v104, v79, v64, v100
v_fma_f32 v20, v20, v78, v104
v_fma_f32 v105, v79, v65, v101
v_fma_f32 v21, v21, v78, v105
v_mul_f32_e64 v20, v20, |v20|
v_mul_f32_e64 v21, v21, |v21|
v_cvt_pk_bf16_f32 v20, v20, v21
v_pk_max_i16 v72, v20, 0
v_pk_max_i16 v74, v20, 0
v_fma_f32 v104, v79, v66, v102
v_fma_f32 v22, v22, v78, v104
v_fma_f32 v105, v79, v67, v103
v_fma_f32 v23, v23, v78, v105
v_mul_f32_e64 v22, v22, |v22|
v_mul_f32_e64 v23, v23, |v23|
v_cvt_pk_bf16_f32 v22, v22, v23
v_pk_max_i16 v73, v22, 0
v_pk_max_i16 v75, v22, 0
v_permlane16_swap_b32 v72, v74
s_nop 0
v_permlane16_swap_b32 v73, v75
buffer_store_dwordx4 v[72:75], v84, s[36:39], s49 offen sc1
v_add_u32 v76, 37, v76
s_waitcnt vmcnt(0)
buffer_store_dwordx2 v[76:77], v81, s[36:39], s52 offen sc1
s_endpgm
CF0_1:
s_cmp_eq_u32 s61, 0
s_cbranch_scc1 BF0_1
s_sub_u32 s61, s61, 1
s_sleep 8
buffer_load_dwordx2 v[106:107], v87, s[36:39], 0 offen sc1
s_waitcnt vmcnt(0)
ds_write_b32 v82, v106
v_sub_u32 v106, v106, v88
v_xor_b32 v107, v107, v77
v_cmp_lt_u64 vcc, s[62:63], v[106:107]
s_waitcnt lgkmcnt(0)
s_cbranch_vccnz CF0_1
s_branch BF0_1
CF0_2:
s_mov_b32 s58, m0
s_mov_b32 m0, 142336
DF0_2:
s_cmp_eq_u32 s61, 0
s_cbranch_scc1 EF0_2
s_sub_u32 s61, s61, 1
buffer_load_dword v87, s[36:39], 0 offen sc1 lds
s_sleep 1
ds_read_b32 v106, v82
s_waitcnt lgkmcnt(0)
v_cmp_gt_i32 vcc, v88, v106
s_cbranch_vccnz DF0_2
EF0_2:
s_mov_b32 m0, s58
s_branch BF0_2
CF0_3:
s_mov_b32 s58, m0
s_mov_b32 m0, 142336
DF0_3:
s_cmp_eq_u32 s61, 0
s_cbranch_scc1 EF0_3
s_sub_u32 s61, s61, 1
buffer_load_dword v87, s[36:39], 0 offen sc1 lds
s_sleep 1
ds_read_b32 v106, v82
s_waitcnt lgkmcnt(0)
v_cmp_gt_i32 vcc, v88, v106
s_cbranch_vccnz DF0_3
EF0_3:
s_mov_b32 m0, s58
s_branch BF0_3
CF0_4:
s_mov_b32 s58, m0
s_mov_b32 m0, 142336
DF0_4:
s_cmp_eq_u32 s61, 0
s_cbranch_scc1 EF0_4
s_sub_u32 s61, s61, 1
buffer_load_dword v87, s[36:39], 0 offen sc1 lds
s_sleep 1
ds_read_b32 v106, v82
s_waitcnt lgkmcnt(0)
v_cmp_gt_i32 vcc, v88, v106
s_cbranch_vccnz DF0_4
EF0_4:
s_mov_b32 m0, s58
s_branch BF0_4
CF0_6:
s_mov_b32 s58, m0
s_mov_b32 m0, 142336
DF0_6:
s_cmp_eq_u32 s61, 0
s_cbranch_scc1 EF0_6
s_sub_u32 s61, s61, 1
buffer_load_dword v87, s[36:39], 0 offen sc1 lds
s_sleep 1
ds_read_b32 v106, v82
s_waitcnt lgkmcnt(0)
v_cmp_gt_i32 vcc, v88, v106
s_cbranch_vccnz DF0_6
EF0_6:
s_mov_b32 m0, s58
s_branch BF0_6
CF0_7:
s_mov_b32 s58, m0
s_mov_b32 m0, 142336
DF0_7:
s_cmp_eq_u32 s61, 0
s_cbranch_scc1 EF0_7
s_sub_u32 s61, s61, 1
buffer_load_dword v87, s[36:39], 0 offen sc1 lds
s_sleep 1
ds_read_b32 v106, v82
s_waitcnt lgkmcnt(0)
v_cmp_gt_i32 vcc, v88, v106
s_cbranch_vccnz DF0_7
EF0_7:
s_mov_b32 m0, s58
s_branch BF0_7
CF0_8:
s_mov_b32 s58, m0
s_mov_b32 m0, 142336
DF0_8:
s_cmp_eq_u32 s61, 0
s_cbranch_scc1 EF0_8
s_sub_u32 s61, s61, 1
buffer_load_dword v87, s[36:39], 0 offen sc1 lds
s_sleep 1
ds_read_b32 v106, v82
s_waitcnt lgkmcnt(0)
v_cmp_gt_i32 vcc, v88, v106
s_cbranch_vccnz DF0_8
EF0_8:
s_mov_b32 m0, s58
s_branch BF0_8
LvF1:
s_add_u32 s49, s49, 346112
s_barrier
s_add_u32 m0, s50, 0
s_nop 0
D 85,0
D 85,1024
D 85,2048
D 85,3072
s_add_u32 m0, s50, 4096
s_nop 0
D 86,0
D 86,1024
D 86,2048
D 86,3072
s_add_u32 s49, s49, 346112
s_waitcnt vmcnt(0)
s_barrier
R 24,0
buffer_load_dwordx2 v[108:109], v90, s[36:39], s49 offen sc1
buffer_load_dwordx2 v[110:111], v91, s[36:39], s49 offen sc1
s_add_u32 m0, s50, 32768
R 28,1024
D 85,0
R 32,2048
D 85,1024
R 36,3072
s_waitcnt lgkmcnt(2)
v_mfma_f32_16x16x32_bf16 v[0:3], a[0:3], v[24:27], 0
R 40,4096
D 85,2048
v_mfma_f32_16x16x32_bf16 v[4:7], a[4:7], v[24:27], 0
v_mfma_f32_16x16x32_bf16 v[8:11], a[8:11], v[24:27], 0
mav 0,12,28
R 44,5120
mav 4,16,28
D 85,3072
mav 8,20,28
s_waitcnt lgkmcnt(2)
mav 0,24,32
R 48,6144
mav 4,28,32
s_add_u32 m0, s50, 36864
mav 8,32,32
mav 0,36,36
R 52,7168
D 86,0
mav 4,40,36
mav 8,44,36
s_waitcnt lgkmcnt(2)
mav 0,48,40
R 24,8192
mav 4,52,40
D 86,1024
mav 8,56,40
mav 0,60,44
R 28,9216
mav 4,64,44
mav 8,68,44
s_waitcnt lgkmcnt(2)
mav 0,72,48
R 32,10240
D 86,2048
mav 4,76,48
mav 8,80,48
mav 0,84,52
R 36,11264
mav 4,88,52
D 86,3072
mav 8,92,52
s_waitcnt lgkmcnt(2)
mav 0,96,24
R 40,12288
mav 4,100,24
mav 8,104,24
mav 0,108,28
R 44,13312
mav 4,112,28
mav 8,116,28
s_waitcnt lgkmcnt(2)
mav 0,120,32
R 48,14336
mav 4,124,32
mav 8,128,32
mav 0,132,36
R 52,15360
mav 4,136,36
mav 8,140,36
s_waitcnt lgkmcnt(2)
mav 0,144,40
R 24,16384
mav 4,148,40
mav 8,152,40
mav 0,156,44
R 28,17408
mav 4,160,44
mav 8,164,44
s_waitcnt lgkmcnt(2)
mav 0,168,48
R 32,18432
mav 4,172,48
mav 8,176,48
mav 0,180,52
R 36,19456
mav 4,184,52
mav 8,188,52
s_waitcnt lgkmcnt(2)
mav 0,192,24
R 40,20480
mav 4,196,24
mav 8,200,24
s_waitcnt vmcnt(8)
v_add_f32 v108, v108, v110
mav 0,204,28
R 44,21504
v_add_f32 v109, v109, v111
mav 4,208,28
mav 8,212,28
v_permlane32_swap_b32 v108, v109
s_waitcnt lgkmcnt(2)
mav 0,216,32
R 48,22528
v_add_f32 v110, v108, v109
mav 4,220,32
v_add_f32 v111, v108, v109
mav 8,224,32
mav 0,228,36
R 52,23552
v_permlane16_swap_b32 v110, v111
mav 4,232,36
v_add_f32 v108, v110, v111
v_add_f32 v109, v110, v111
mav 8,236,36
s_waitcnt lgkmcnt(2)
mav 0,240,40
R 24,24576
v_permlane32_swap_b32 v108, v109
mav 4,244,40
v_mul_f32 v110, 0x3a800000, v108
v_mul_f32 v111, 0x3a800000, v109
mav 8,248,40
v_fma_f32 v111, -v110, v110, v111
v_max_f32 v111, 0, v111
mav 0,252,44
R 28,25600
v_add_f32 v111, 0x3727c5ac, v111
mvv 4,128,44
v_rsq_f32 v108, v111
mvv 8,132,44
v_mul_f32_e64 v109, -v110, v108
s_waitcnt lgkmcnt(2)
mvv 0,136,48
R 32,26624
ds_write_b64 v89, v[108:109] offset:0
mvv 4,140,48
mvv 8,144,48
mvv 0,148,52
R 36,27648
mvv 4,152,52
mvv 8,156,52
s_waitcnt lgkmcnt(3)
mvv 0,160,24
R 40,28672
mvv 4,164,24
mvv 8,168,24
mvv 0,172,28
R 44,29696
mvv 4,176,28
mvv 8,180,28
s_waitcnt lgkmcnt(2)
mvv 0,184,32
R 48,30720
s_xor_b32 s50, s50, 0x10000
mvv 4,188,32
mvv 8,192,32
mvv 0,196,36
R 52,31744
mvv 4,200,36
s_add_u32 s49, s49, 346112
mvv 8,204,36
s_waitcnt vmcnt(0)
s_barrier
s_waitcnt lgkmcnt(2)
mvv 0,208,40
R 24,32768
buffer_load_dwordx2 v[108:109], v90, s[36:39], s49 offen sc1
mvv 4,212,40
buffer_load_dwordx2 v[110:111], v91, s[36:39], s49 offen sc1
s_add_u32 m0, s50, 0
mvv 8,216,40
ds_read_b64 v[78:79], v89 offset:0
mvv 0,220,44
R 28,33792
D 85,0
mvv 4,224,44
mvv 8,228,44
s_waitcnt lgkmcnt(3)
mvv 0,232,48
R 32,34816
mvv 4,236,48
D 85,1024
mvv 8,240,48
mvv 0,244,52
R 36,35840
mvv 4,248,52
mvv 8,252,52
s_waitcnt lgkmcnt(2)
v_mfma_f32_16x16x32_bf16 v[12:15], a[0:3], v[24:27], 0
R 40,36864
D 85,2048
v_mfma_f32_16x16x32_bf16 v[16:19], a[4:7], v[24:27], 0
v_fma_f32 v104, v79, v56, v92
v_fma_f32 v0, v0, v78, v104
v_mfma_f32_16x16x32_bf16 v[20:23], a[8:11], v[24:27], 0
v_fma_f32 v105, v79, v57, v93
v_fma_f32 v1, v1, v78, v105
mav 12,12,28
R 44,37888
v_mul_f32_e64 v0, v0, |v0|
mav 16,16,28
D 85,3072
v_mul_f32_e64 v1, v1, |v1|
mav 20,20,28
v_cvt_pk_bf16_f32 v0, v0, v1
s_waitcnt lgkmcnt(2)
mav 12,24,32
R 48,38912
v_pk_max_i16 v68, v0, 0
mav 16,28,32
s_add_u32 m0, s50, 4096
v_fma_f32 v104, v79, v58, v94
mav 20,32,32
v_fma_f32 v2, v2, v78, v104
v_fma_f32 v105, v79, v59, v95
mav 12,36,36
R 52,39936
D 86,0
mav 16,40,36
v_fma_f32 v3, v3, v78, v105
v_mul_f32_e64 v2, v2, |v2|
mav 20,44,36
v_mul_f32_e64 v3, v3, |v3|
s_waitcnt lgkmcnt(2)
mav 12,48,40
R 24,40960
v_cvt_pk_bf16_f32 v2, v2, v3
mav 16,52,40
D 86,1024
v_pk_max_i16 v69, v2, 0
mav 20,56,40
v_fma_f32 v104, v79, v60, v96
v_fma_f32 v4, v4, v78, v104
mav 12,60,44
R 28,41984
v_fma_f32 v105, v79, v61, v97
mav 16,64,44
v_fma_f32 v5, v5, v78, v105
v_mul_f32_e64 v4, v4, |v4|
mav 20,68,44
v_mul_f32_e64 v5, v5, |v5|
s_waitcnt lgkmcnt(2)
mav 12,72,48
R 32,43008
D 86,2048
mav 16,76,48
v_cvt_pk_bf16_f32 v4, v4, v5
v_pk_max_i16 v70, v4, 0
mav 20,80,48
v_fma_f32 v104, v79, v62, v98
v_fma_f32 v6, v6, v78, v104
mav 12,84,52
R 36,44032
v_fma_f32 v105, v79, v63, v99
mav 16,88,52
D 86,3072
v_fma_f32 v7, v7, v78, v105
mav 20,92,52
v_mul_f32_e64 v6, v6, |v6|
s_waitcnt lgkmcnt(2)
mav 12,96,24
R 40,45056
v_mul_f32_e64 v7, v7, |v7|
mav 16,100,24
v_cvt_pk_bf16_f32 v6, v6, v7
v_pk_max_i16 v71, v6, 0
mav 20,104,24
buffer_store_dwordx4 v[68:71], v83, s[36:39], s49 offen sc1
v_fma_f32 v104, v79, v64, v100
mav 12,108,28
R 44,46080
v_fma_f32 v8, v8, v78, v104
mav 16,112,28
v_fma_f32 v105, v79, v65, v101
v_fma_f32 v9, v9, v78, v105
mav 20,116,28
v_mul_f32_e64 v8, v8, |v8|
s_waitcnt lgkmcnt(2)
mav 12,120,32
R 48,47104
v_mul_f32_e64 v9, v9, |v9|
mav 16,124,32
v_cvt_pk_bf16_f32 v8, v8, v9
v_pk_max_i16 v72, v8, 0
mav 20,128,32
v_pk_max_i16 v74, v8, 0
v_fma_f32 v104, v79, v66, v102
mav 12,132,36
R 52,48128
v_fma_f32 v10, v10, v78, v104
mav 16,136,36
v_fma_f32 v105, v79, v67, v103
v_fma_f32 v11, v11, v78, v105
mav 20,140,36
v_mul_f32_e64 v10, v10, |v10|
s_waitcnt lgkmcnt(2)
mav 12,144,40
R 24,49152
v_mul_f32_e64 v11, v11, |v11|
mav 16,148,40
v_cvt_pk_bf16_f32 v10, v10, v11
v_pk_max_i16 v73, v10, 0
mav 20,152,40
v_pk_max_i16 v75, v10, 0
v_permlane16_swap_b32 v72, v74
mav 12,156,44
R 28,50176
v_permlane16_swap_b32 v73, v75
mav 16,160,44
buffer_store_dwordx4 v[72:75], v84, s[36:39], s49 offen sc1
mav 20,164,44
s_waitcnt lgkmcnt(2)
mav 12,168,48
R 32,51200
mav 16,172,48
mav 20,176,48
mav 12,180,52
R 36,52224
mav 16,184,52
mav 20,188,52
s_waitcnt lgkmcnt(2)
mav 12,192,24
R 40,53248
mav 16,196,24
mav 20,200,24
s_waitcnt vmcnt(10)
v_add_f32 v108, v108, v110
mav 12,204,28
R 44,54272
v_add_f32 v109, v109, v111
mav 16,208,28
mav 20,212,28
v_permlane32_swap_b32 v108, v109
s_waitcnt lgkmcnt(2)
mav 12,216,32
R 48,55296
v_add_f32 v110, v108, v109
mav 16,220,32
v_add_f32 v111, v108, v109
mav 20,224,32
mav 12,228,36
R 52,56320
v_permlane16_swap_b32 v110, v111
mav 16,232,36
v_add_f32 v108, v110, v111
v_add_f32 v109, v110, v111
mav 20,236,36
s_waitcnt lgkmcnt(2)
mav 12,240,40
R 24,57344
v_permlane32_swap_b32 v108, v109
mav 16,244,40
v_mul_f32 v110, 0x3a800000, v108
v_mul_f32 v111, 0x3a800000, v109
mav 20,248,40
v_fma_f32 v111, -v110, v110, v111
v_max_f32 v111, 0, v111
mav 12,252,44
R 28,58368
v_add_f32 v111, 0x3727c5ac, v111
mvv 16,128,44
v_rsq_f32 v108, v111
mvv 20,132,44
v_mul_f32_e64 v109, -v110, v108
s_waitcnt lgkmcnt(2)
mvv 12,136,48
R 32,59392
ds_write_b64 v89, v[108:109] offset:128
mvv 16,140,48
mvv 20,144,48
mvv 12,148,52
R 36,60416
mvv 16,152,52
mvv 20,156,52
s_waitcnt lgkmcnt(3)
mvv 12,160,24
R 40,61440
mvv 16,164,24
mvv 20,168,24
v_add_u32 v76, 1, v76
mvv 12,172,28
R 44,62464
mvv 16,176,28
mvv 20,180,28
s_waitcnt lgkmcnt(2)
mvv 12,184,32
R 48,63488
mvv 16,188,32
mvv 20,192,32
s_waitcnt vmcnt(0)
buffer_store_dwordx2 v[76:77], v81, s[36:39], s52 offen sc1
mvv 12,196,36
R 52,64512
v_xor_b32 v80, 0x10000, v80
mvv 16,200,36
s_add_u32 s49, s49, 346112
mvv 20,204,36
s_barrier
s_waitcnt lgkmcnt(2)
mvv 12,208,40
R 24,0
buffer_load_dwordx2 v[108:109], v90, s[36:39], s49 offen sc1
mvv 16,212,40
buffer_load_dwordx2 v[110:111], v91, s[36:39], s49 offen sc1
s_add_u32 m0, s50, 32768
mvv 20,216,40
ds_read_b64 v[78:79], v89 offset:128
mvv 12,220,44
R 28,1024
D 85,0
mvv 16,224,44
mvv 20,228,44
s_waitcnt lgkmcnt(3)
mvv 12,232,48
R 32,2048
mvv 16,236,48
D 85,1024
mvv 20,240,48
mvv 12,244,52
R 36,3072
mvv 16,248,52
mvv 20,252,52
s_mov_b32 s60, -30
.p2align 6
LF1_1:
s_waitcnt lgkmcnt(2)
v_mfma_f32_16x16x32_bf16 v[0:3], a[0:3], v[24:27], 0
R 40,4096
D 85,2048
v_mfma_f32_16x16x32_bf16 v[4:7], a[4:7], v[24:27], 0
v_fma_f32 v104, v79, v56, v92
v_fma_f32 v12, v12, v78, v104
v_mfma_f32_16x16x32_bf16 v[8:11], a[8:11], v[24:27], 0
v_fma_f32 v105, v79, v57, v93
v_fma_f32 v13, v13, v78, v105
mav 0,12,28
R 44,5120
v_mul_f32_e64 v12, v12, |v12|
mav 4,16,28
D 85,3072
v_mul_f32_e64 v13, v13, |v13|
mav 8,20,28
v_cvt_pk_bf16_f32 v12, v12, v13
s_waitcnt lgkmcnt(2)
mav 0,24,32
R 48,6144
v_pk_max_i16 v68, v12, 0
mav 4,28,32
s_add_u32 m0, s50, 36864
v_fma_f32 v104, v79, v58, v94
mav 8,32,32
v_fma_f32 v14, v14, v78, v104
v_fma_f32 v105, v79, v59, v95
mav 0,36,36
R 52,7168
D 86,0
mav 4,40,36
v_fma_f32 v15, v15, v78, v105
v_mul_f32_e64 v14, v14, |v14|
mav 8,44,36
v_mul_f32_e64 v15, v15, |v15|
s_waitcnt lgkmcnt(2)
mav 0,48,40
R 24,8192
v_cvt_pk_bf16_f32 v14, v14, v15
mav 4,52,40
D 86,1024
v_pk_max_i16 v69, v14, 0
mav 8,56,40
v_fma_f32 v104, v79, v60, v96
v_fma_f32 v16, v16, v78, v104
mav 0,60,44
R 28,9216
v_fma_f32 v105, v79, v61, v97
mav 4,64,44
v_fma_f32 v17, v17, v78, v105
v_mul_f32_e64 v16, v16, |v16|
mav 8,68,44
v_mul_f32_e64 v17, v17, |v17|
s_waitcnt lgkmcnt(2)
mav 0,72,48
R 32,10240
D 86,2048
mav 4,76,48
v_cvt_pk_bf16_f32 v16, v16, v17
v_pk_max_i16 v70, v16, 0
mav 8,80,48
v_fma_f32 v104, v79, v62, v98
v_fma_f32 v18, v18, v78, v104
mav 0,84,52
R 36,11264
v_fma_f32 v105, v79, v63, v99
mav 4,88,52
D 86,3072
v_fma_f32 v19, v19, v78, v105
mav 8,92,52
v_mul_f32_e64 v18, v18, |v18|
s_waitcnt lgkmcnt(2)
mav 0,96,24
R 40,12288
v_mul_f32_e64 v19, v19, |v19|
mav 4,100,24
v_cvt_pk_bf16_f32 v18, v18, v19
v_pk_max_i16 v71, v18, 0
mav 8,104,24
buffer_store_dwordx4 v[68:71], v83, s[36:39], s49 offen sc1
v_fma_f32 v104, v79, v64, v100
mav 0,108,28
R 44,13312
v_fma_f32 v20, v20, v78, v104
mav 4,112,28
v_fma_f32 v105, v79, v65, v101
v_fma_f32 v21, v21, v78, v105
mav 8,116,28
v_mul_f32_e64 v20, v20, |v20|
s_waitcnt lgkmcnt(2)
mav 0,120,32
R 48,14336
v_mul_f32_e64 v21, v21, |v21|
mav 4,124,32
v_cvt_pk_bf16_f32 v20, v20, v21
v_pk_max_i16 v72, v20, 0
mav 8,128,32
v_pk_max_i16 v74, v20, 0
v_fma_f32 v104, v79, v66, v102
mav 0,132,36
R 52,15360
v_fma_f32 v22, v22, v78, v104
mav 4,136,36
v_fma_f32 v105, v79, v67, v103
v_fma_f32 v23, v23, v78, v105
mav 8,140,36
v_mul_f32_e64 v22, v22, |v22|
s_waitcnt lgkmcnt(2)
mav 0,144,40
R 24,16384
v_mul_f32_e64 v23, v23, |v23|
mav 4,148,40
v_cvt_pk_bf16_f32 v22, v22, v23
v_pk_max_i16 v73, v22, 0
mav 8,152,40
v_pk_max_i16 v75, v22, 0
v_permlane16_swap_b32 v72, v74
mav 0,156,44
R 28,17408
v_permlane16_swap_b32 v73, v75
mav 4,160,44
buffer_store_dwordx4 v[72:75], v84, s[36:39], s49 offen sc1
mav 8,164,44
s_waitcnt lgkmcnt(2)
mav 0,168,48
R 32,18432
mav 4,172,48
mav 8,176,48
mav 0,180,52
R 36,19456
mav 4,184,52
mav 8,188,52
s_waitcnt lgkmcnt(2)
mav 0,192,24
R 40,20480
mav 4,196,24
mav 8,200,24
s_waitcnt vmcnt(10)
v_add_f32 v108, v108, v110
mav 0,204,28
R 44,21504
v_add_f32 v109, v109, v111
mav 4,208,28
mav 8,212,28
v_permlane32_swap_b32 v108, v109
s_waitcnt lgkmcnt(2)
mav 0,216,32
R 48,22528
v_add_f32 v110, v108, v109
mav 4,220,32
v_add_f32 v111, v108, v109
mav 8,224,32
mav 0,228,36
R 52,23552
v_permlane16_swap_b32 v110, v111
mav 4,232,36
v_add_f32 v108, v110, v111
v_add_f32 v109, v110, v111
mav 8,236,36
s_waitcnt lgkmcnt(2)
mav 0,240,40
R 24,24576
v_permlane32_swap_b32 v108, v109
mav 4,244,40
v_mul_f32 v110, 0x3a800000, v108
v_mul_f32 v111, 0x3a800000, v109
mav 8,248,40
v_fma_f32 v111, -v110, v110, v111
v_max_f32 v111, 0, v111
mav 0,252,44
R 28,25600
v_add_f32 v111, 0x3727c5ac, v111
mvv 4,128,44
v_rsq_f32 v108, v111
mvv 8,132,44
v_mul_f32_e64 v109, -v110, v108
s_waitcnt lgkmcnt(2)
mvv 0,136,48
R 32,26624
ds_write_b64 v89, v[108:109] offset:0
mvv 4,140,48
mvv 8,144,48
mvv 0,148,52
R 36,27648
mvv 4,152,52
mvv 8,156,52
s_waitcnt lgkmcnt(3)
mvv 0,160,24
R 40,28672
mvv 4,164,24
mvv 8,168,24
v_add_u32 v76, 1, v76
mvv 0,172,28
R 44,29696
mvv 4,176,28
mvv 8,180,28
s_waitcnt lgkmcnt(2)
mvv 0,184,32
R 48,30720
s_xor_b32 s50, s50, 0x10000
mvv 4,188,32
mvv 8,192,32
s_waitcnt vmcnt(0)
buffer_store_dwordx2 v[76:77], v81, s[36:39], s52 offen sc1
mvv 0,196,36
R 52,31744
mvv 4,200,36
s_add_u32 s49, s49, 346112
mvv 8,204,36
s_barrier
s_waitcnt lgkmcnt(2)
mvv 0,208,40
R 24,32768
buffer_load_dwordx2 v[108:109], v90, s[36:39], s49 offen sc1
mvv 4,212,40
buffer_load_dwordx2 v[110:111], v91, s[36:39], s49 offen sc1
s_add_u32 m0, s50, 0
mvv 8,216,40
ds_read_b64 v[78:79], v89 offset:0
mvv 0,220,44
R 28,33792
D 85,0
mvv 4,224,44
mvv 8,228,44
s_waitcnt lgkmcnt(3)
mvv 0,232,48
R 32,34816
mvv 4,236,48
D 85,1024
mvv 8,240,48
mvv 0,244,52
R 36,35840
mvv 4,248,52
mvv 8,252,52
s_waitcnt lgkmcnt(2)
v_mfma_f32_16x16x32_bf16 v[12:15], a[0:3], v[24:27], 0
R 40,36864
D 85,2048
v_mfma_f32_16x16x32_bf16 v[16:19], a[4:7], v[24:27], 0
v_fma_f32 v104, v79, v56, v92
v_fma_f32 v0, v0, v78, v104
v_mfma_f32_16x16x32_bf16 v[20:23], a[8:11], v[24:27], 0
v_fma_f32 v105, v79, v57, v93
v_fma_f32 v1, v1, v78, v105
mav 12,12,28
R 44,37888
v_mul_f32_e64 v0, v0, |v0|
mav 16,16,28
D 85,3072
v_mul_f32_e64 v1, v1, |v1|
mav 20,20,28
v_cvt_pk_bf16_f32 v0, v0, v1
s_waitcnt lgkmcnt(2)
mav 12,24,32
R 48,38912
v_pk_max_i16 v68, v0, 0
mav 16,28,32
s_add_u32 m0, s50, 4096
v_fma_f32 v104, v79, v58, v94
mav 20,32,32
v_fma_f32 v2, v2, v78, v104
v_fma_f32 v105, v79, v59, v95
mav 12,36,36
R 52,39936
D 86,0
mav 16,40,36
v_fma_f32 v3, v3, v78, v105
v_mul_f32_e64 v2, v2, |v2|
mav 20,44,36
v_mul_f32_e64 v3, v3, |v3|
s_waitcnt lgkmcnt(2)
mav 12,48,40
R 24,40960
v_cvt_pk_bf16_f32 v2, v2, v3
mav 16,52,40
D 86,1024
v_pk_max_i16 v69, v2, 0
mav 20,56,40
v_fma_f32 v104, v79, v60, v96
v_fma_f32 v4, v4, v78, v104
mav 12,60,44
R 28,41984
v_fma_f32 v105, v79, v61, v97
mav 16,64,44
v_fma_f32 v5, v5, v78, v105
v_mul_f32_e64 v4, v4, |v4|
mav 20,68,44
v_mul_f32_e64 v5, v5, |v5|
s_waitcnt lgkmcnt(2)
mav 12,72,48
R 32,43008
D 86,2048
mav 16,76,48
v_cvt_pk_bf16_f32 v4, v4, v5
v_pk_max_i16 v70, v4, 0
mav 20,80,48
v_fma_f32 v104, v79, v62, v98
v_fma_f32 v6, v6, v78, v104
mav 12,84,52
R 36,44032
v_fma_f32 v105, v79, v63, v99
mav 16,88,52
D 86,3072
v_fma_f32 v7, v7, v78, v105
mav 20,92,52
v_mul_f32_e64 v6, v6, |v6|
s_waitcnt lgkmcnt(2)
mav 12,96,24
R 40,45056
v_mul_f32_e64 v7, v7, |v7|
mav 16,100,24
v_cvt_pk_bf16_f32 v6, v6, v7
v_pk_max_i16 v71, v6, 0
mav 20,104,24
buffer_store_dwordx4 v[68:71], v83, s[36:39], s49 offen sc1
v_fma_f32 v104, v79, v64, v100
mav 12,108,28
R 44,46080
v_fma_f32 v8, v8, v78, v104
mav 16,112,28
v_fma_f32 v105, v79, v65, v101
v_fma_f32 v9, v9, v78, v105
mav 20,116,28
v_mul_f32_e64 v8, v8, |v8|
s_waitcnt lgkmcnt(2)
mav 12,120,32
R 48,47104
v_mul_f32_e64 v9, v9, |v9|
mav 16,124,32
v_cvt_pk_bf16_f32 v8, v8, v9
v_pk_max_i16 v72, v8, 0
mav 20,128,32
v_pk_max_i16 v74, v8, 0
v_fma_f32 v104, v79, v66, v102
mav 12,132,36
R 52,48128
v_fma_f32 v10, v10, v78, v104
mav 16,136,36
v_fma_f32 v105, v79, v67, v103
v_fma_f32 v11, v11, v78, v105
mav 20,140,36
v_mul_f32_e64 v10, v10, |v10|
s_waitcnt lgkmcnt(2)
mav 12,144,40
R 24,49152
v_mul_f32_e64 v11, v11, |v11|
mav 16,148,40
v_cvt_pk_bf16_f32 v10, v10, v11
v_pk_max_i16 v73, v10, 0
mav 20,152,40
v_pk_max_i16 v75, v10, 0
v_permlane16_swap_b32 v72, v74
mav 12,156,44
R 28,50176
v_permlane16_swap_b32 v73, v75
mav 16,160,44
buffer_store_dwordx4 v[72:75], v84, s[36:39], s49 offen sc1
mav 20,164,44
s_waitcnt lgkmcnt(2)
mav 12,168,48
R 32,51200
mav 16,172,48
mav 20,176,48
mav 12,180,52
R 36,52224
mav 16,184,52
mav 20,188,52
s_waitcnt lgkmcnt(2)
mav 12,192,24
R 40,53248
mav 16,196,24
mav 20,200,24
s_waitcnt vmcnt(10)
v_add_f32 v108, v108, v110
mav 12,204,28
R 44,54272
v_add_f32 v109, v109, v111
mav 16,208,28
mav 20,212,28
v_permlane32_swap_b32 v108, v109
s_waitcnt lgkmcnt(2)
mav 12,216,32
R 48,55296
v_add_f32 v110, v108, v109
mav 16,220,32
v_add_f32 v111, v108, v109
mav 20,224,32
mav 12,228,36
R 52,56320
v_permlane16_swap_b32 v110, v111
mav 16,232,36
v_add_f32 v108, v110, v111
v_add_f32 v109, v110, v111
mav 20,236,36
s_waitcnt lgkmcnt(2)
mav 12,240,40
R 24,57344
v_permlane32_swap_b32 v108, v109
mav 16,244,40
v_mul_f32 v110, 0x3a800000, v108
v_mul_f32 v111, 0x3a800000, v109
mav 20,248,40
v_fma_f32 v111, -v110, v110, v111
v_max_f32 v111, 0, v111
mav 12,252,44
R 28,58368
v_add_f32 v111, 0x3727c5ac, v111
mvv 16,128,44
v_rsq_f32 v108, v111
mvv 20,132,44
v_mul_f32_e64 v109, -v110, v108
s_waitcnt lgkmcnt(2)
mvv 12,136,48
R 32,59392
ds_write_b64 v89, v[108:109] offset:128
mvv 16,140,48
mvv 20,144,48
mvv 12,148,52
R 36,60416
mvv 16,152,52
mvv 20,156,52
s_waitcnt lgkmcnt(3)
mvv 12,160,24
R 40,61440
mvv 16,164,24
mvv 20,168,24
v_add_u32 v76, 1, v76
mvv 12,172,28
R 44,62464
mvv 16,176,28
mvv 20,180,28
s_waitcnt lgkmcnt(2)
mvv 12,184,32
R 48,63488
mvv 16,188,32
mvv 20,192,32
s_waitcnt vmcnt(0)
buffer_store_dwordx2 v[76:77], v81, s[36:39], s52 offen sc1
mvv 12,196,36
R 52,64512
v_xor_b32 v80, 0x10000, v80
mvv 16,200,36
s_add_u32 s49, s49, 346112
mvv 20,204,36
s_barrier
s_waitcnt lgkmcnt(2)
mvv 12,208,40
R 24,0
buffer_load_dwordx2 v[108:109], v90, s[36:39], s49 offen sc1
mvv 16,212,40
buffer_load_dwordx2 v[110:111], v91, s[36:39], s49 offen sc1
s_add_u32 m0, s50, 32768
mvv 20,216,40
ds_read_b64 v[78:79], v89 offset:128
mvv 12,220,44
R 28,1024
D 85,0
mvv 16,224,44
mvv 20,228,44
s_waitcnt lgkmcnt(3)
mvv 12,232,48
R 32,2048
mvv 16,236,48
D 85,1024
mvv 20,240,48
mvv 12,244,52
R 36,3072
mvv 16,248,52
mvv 20,252,52
s_add_u32 s60, s60, 1
s_cbranch_scc0 LF1_1
s_waitcnt lgkmcnt(2)
v_mfma_f32_16x16x32_bf16 v[0:3], a[0:3], v[24:27], 0
R 40,4096
D 85,2048
v_mfma_f32_16x16x32_bf16 v[4:7], a[4:7], v[24:27], 0
v_fma_f32 v104, v79, v56, v92
v_fma_f32 v12, v12, v78, v104
v_mfma_f32_16x16x32_bf16 v[8:11], a[8:11], v[24:27], 0
v_fma_f32 v105, v79, v57, v93
v_fma_f32 v13, v13, v78, v105
mav 0,12,28
R 44,5120
v_mul_f32_e64 v12, v12, |v12|
mav 4,16,28
D 85,3072
v_mul_f32_e64 v13, v13, |v13|
mav 8,20,28
v_cvt_pk_bf16_f32 v12, v12, v13
s_waitcnt lgkmcnt(2)
mav 0,24,32
R 48,6144
v_pk_max_i16 v68, v12, 0
mav 4,28,32
s_add_u32 m0, s50, 36864
v_fma_f32 v104, v79, v58, v94
mav 8,32,32
v_fma_f32 v14, v14, v78, v104
v_fma_f32 v105, v79, v59, v95
mav 0,36,36
)"
R"(
R 52,7168
D 86,0
mav 4,40,36
v_fma_f32 v15, v15, v78, v105
v_mul_f32_e64 v14, v14, |v14|
mav 8,44,36
v_mul_f32_e64 v15, v15, |v15|
s_waitcnt lgkmcnt(2)
mav 0,48,40
R 24,8192
v_cvt_pk_bf16_f32 v14, v14, v15
mav 4,52,40
D 86,1024
v_pk_max_i16 v69, v14, 0
mav 8,56,40
v_fma_f32 v104, v79, v60, v96
v_fma_f32 v16, v16, v78, v104
mav 0,60,44
R 28,9216
v_fma_f32 v105, v79, v61, v97
mav 4,64,44
v_fma_f32 v17, v17, v78, v105
v_mul_f32_e64 v16, v16, |v16|
mav 8,68,44
v_mul_f32_e64 v17, v17, |v17|
s_waitcnt lgkmcnt(2)
mav 0,72,48
R 32,10240
D 86,2048
mav 4,76,48
v_cvt_pk_bf16_f32 v16, v16, v17
v_pk_max_i16 v70, v16, 0
mav 8,80,48
v_fma_f32 v104, v79, v62, v98
v_fma_f32 v18, v18, v78, v104
mav 0,84,52
R 36,11264
v_fma_f32 v105, v79, v63, v99
mav 4,88,52
D 86,3072
v_fma_f32 v19, v19, v78, v105
mav 8,92,52
v_mul_f32_e64 v18, v18, |v18|
s_waitcnt lgkmcnt(2)
mav 0,96,24
R 40,12288
v_mul_f32_e64 v19, v19, |v19|
mav 4,100,24
v_cvt_pk_bf16_f32 v18, v18, v19
v_pk_max_i16 v71, v18, 0
mav 8,104,24
buffer_store_dwordx4 v[68:71], v83, s[36:39], s49 offen sc1
v_fma_f32 v104, v79, v64, v100
mav 0,108,28
R 44,13312
v_fma_f32 v20, v20, v78, v104
mav 4,112,28
v_fma_f32 v105, v79, v65, v101
v_fma_f32 v21, v21, v78, v105
mav 8,116,28
v_mul_f32_e64 v20, v20, |v20|
s_waitcnt lgkmcnt(2)
mav 0,120,32
R 48,14336
v_mul_f32_e64 v21, v21, |v21|
mav 4,124,32
v_cvt_pk_bf16_f32 v20, v20, v21
v_pk_max_i16 v72, v20, 0
mav 8,128,32
v_pk_max_i16 v74, v20, 0
v_fma_f32 v104, v79, v66, v102
mav 0,132,36
R 52,15360
v_fma_f32 v22, v22, v78, v104
mav 4,136,36
v_fma_f32 v105, v79, v67, v103
v_fma_f32 v23, v23, v78, v105
mav 8,140,36
v_mul_f32_e64 v22, v22, |v22|
s_waitcnt lgkmcnt(2)
mav 0,144,40
R 24,16384
v_mul_f32_e64 v23, v23, |v23|
mav 4,148,40
v_cvt_pk_bf16_f32 v22, v22, v23
v_pk_max_i16 v73, v22, 0
mav 8,152,40
v_pk_max_i16 v75, v22, 0
v_permlane16_swap_b32 v72, v74
mav 0,156,44
R 28,17408
v_permlane16_swap_b32 v73, v75
mav 4,160,44
buffer_store_dwordx4 v[72:75], v84, s[36:39], s49 offen sc1
mav 8,164,44
s_waitcnt lgkmcnt(2)
mav 0,168,48
R 32,18432
mav 4,172,48
mav 8,176,48
mav 0,180,52
R 36,19456
mav 4,184,52
mav 8,188,52
s_waitcnt lgkmcnt(2)
mav 0,192,24
R 40,20480
mav 4,196,24
mav 8,200,24
s_waitcnt vmcnt(10)
v_add_f32 v108, v108, v110
mav 0,204,28
R 44,21504
v_add_f32 v109, v109, v111
mav 4,208,28
mav 8,212,28
v_permlane32_swap_b32 v108, v109
s_waitcnt lgkmcnt(2)
mav 0,216,32
R 48,22528
v_add_f32 v110, v108, v109
mav 4,220,32
v_add_f32 v111, v108, v109
mav 8,224,32
mav 0,228,36
R 52,23552
v_permlane16_swap_b32 v110, v111
mav 4,232,36
v_add_f32 v108, v110, v111
v_add_f32 v109, v110, v111
mav 8,236,36
s_waitcnt lgkmcnt(2)
mav 0,240,40
R 24,24576
v_permlane32_swap_b32 v108, v109
mav 4,244,40
v_mul_f32 v110, 0x3a800000, v108
v_mul_f32 v111, 0x3a800000, v109
mav 8,248,40
v_fma_f32 v111, -v110, v110, v111
v_max_f32 v111, 0, v111
mav 0,252,44
R 28,25600
v_add_f32 v111, 0x3727c5ac, v111
mvv 4,128,44
v_rsq_f32 v108, v111
mvv 8,132,44
v_mul_f32_e64 v109, -v110, v108
s_waitcnt lgkmcnt(2)
mvv 0,136,48
R 32,26624
ds_write_b64 v89, v[108:109] offset:0
mvv 4,140,48
mvv 8,144,48
mvv 0,148,52
R 36,27648
mvv 4,152,52
mvv 8,156,52
s_waitcnt lgkmcnt(3)
mvv 0,160,24
R 40,28672
mvv 4,164,24
mvv 8,168,24
v_add_u32 v76, 1, v76
mvv 0,172,28
R 44,29696
mvv 4,176,28
mvv 8,180,28
s_waitcnt lgkmcnt(2)
mvv 0,184,32
R 48,30720
mvv 4,188,32
mvv 8,192,32
s_waitcnt vmcnt(0)
buffer_store_dwordx2 v[76:77], v81, s[36:39], s52 offen sc1
mvv 0,196,36
R 52,31744
mvv 4,200,36
s_add_u32 s49, s49, 346112
mvv 8,204,36
s_barrier
s_waitcnt lgkmcnt(2)
mvv 0,208,40
R 24,32768
buffer_load_dwordx2 v[108:109], v90, s[36:39], s49 offen sc1
mvv 4,212,40
buffer_load_dwordx2 v[110:111], v91, s[36:39], s49 offen sc1
ds_read_b64 v[78:79], v89 offset:0
mvv 8,216,40
mvv 0,220,44
R 28,33792
mvv 4,224,44
mvv 8,228,44
s_waitcnt lgkmcnt(3)
mvv 0,232,48
R 32,34816
mvv 4,236,48
mvv 8,240,48
mvv 0,244,52
R 36,35840
mvv 4,248,52
mvv 8,252,52
s_waitcnt lgkmcnt(2)
v_mfma_f32_16x16x32_bf16 v[12:15], a[0:3], v[24:27], 0
R 40,36864
v_fma_f32 v104, v79, v56, v92
v_mfma_f32_16x16x32_bf16 v[16:19], a[4:7], v[24:27], 0
v_fma_f32 v0, v0, v78, v104
v_fma_f32 v105, v79, v57, v93
v_mfma_f32_16x16x32_bf16 v[20:23], a[8:11], v[24:27], 0
v_fma_f32 v1, v1, v78, v105
v_mul_f32_e64 v0, v0, |v0|
mav 12,12,28
R 44,37888
v_mul_f32_e64 v1, v1, |v1|
mav 16,16,28
v_cvt_pk_bf16_f32 v0, v0, v1
v_pk_max_i16 v68, v0, 0
mav 20,20,28
v_fma_f32 v104, v79, v58, v94
s_waitcnt lgkmcnt(2)
mav 12,24,32
R 48,38912
v_fma_f32 v2, v2, v78, v104
mav 16,28,32
v_fma_f32 v105, v79, v59, v95
v_fma_f32 v3, v3, v78, v105
mav 20,32,32
v_mul_f32_e64 v2, v2, |v2|
v_mul_f32_e64 v3, v3, |v3|
mav 12,36,36
R 52,39936
v_cvt_pk_bf16_f32 v2, v2, v3
mav 16,40,36
v_pk_max_i16 v69, v2, 0
v_fma_f32 v104, v79, v60, v96
mav 20,44,36
v_fma_f32 v4, v4, v78, v104
s_waitcnt lgkmcnt(2)
mav 12,48,40
R 24,40960
v_fma_f32 v105, v79, v61, v97
mav 16,52,40
v_fma_f32 v5, v5, v78, v105
v_mul_f32_e64 v4, v4, |v4|
mav 20,56,40
v_mul_f32_e64 v5, v5, |v5|
v_cvt_pk_bf16_f32 v4, v4, v5
mav 12,60,44
R 28,41984
v_pk_max_i16 v70, v4, 0
mav 16,64,44
v_fma_f32 v104, v79, v62, v98
v_fma_f32 v6, v6, v78, v104
mav 20,68,44
v_fma_f32 v105, v79, v63, v99
s_waitcnt lgkmcnt(2)
mav 12,72,48
R 32,43008
v_fma_f32 v7, v7, v78, v105
mav 16,76,48
v_mul_f32_e64 v6, v6, |v6|
v_mul_f32_e64 v7, v7, |v7|
mav 20,80,48
v_cvt_pk_bf16_f32 v6, v6, v7
v_pk_max_i16 v71, v6, 0
mav 12,84,52
R 36,44032
buffer_store_dwordx4 v[68:71], v83, s[36:39], s49 offen sc1
mav 16,88,52
v_fma_f32 v104, v79, v64, v100
v_fma_f32 v8, v8, v78, v104
mav 20,92,52
v_fma_f32 v105, v79, v65, v101
s_waitcnt lgkmcnt(2)
mav 12,96,24
R 40,45056
v_fma_f32 v9, v9, v78, v105
mav 16,100,24
v_mul_f32_e64 v8, v8, |v8|
v_mul_f32_e64 v9, v9, |v9|
mav 20,104,24
v_cvt_pk_bf16_f32 v8, v8, v9
v_pk_max_i16 v72, v8, 0
mav 12,108,28
R 44,46080
v_pk_max_i16 v74, v8, 0
mav 16,112,28
v_fma_f32 v104, v79, v66, v102
v_fma_f32 v10, v10, v78, v104
mav 20,116,28
v_fma_f32 v105, v79, v67, v103
s_waitcnt lgkmcnt(2)
mav 12,120,32
R 48,47104
v_fma_f32 v11, v11, v78, v105
mav 16,124,32
v_mul_f32_e64 v10, v10, |v10|
v_mul_f32_e64 v11, v11, |v11|
mav 20,128,32
v_cvt_pk_bf16_f32 v10, v10, v11
v_pk_max_i16 v73, v10, 0
mav 12,132,36
R 52,48128
v_pk_max_i16 v75, v10, 0
mav 16,136,36
v_permlane16_swap_b32 v72, v74
v_permlane16_swap_b32 v73, v75
mav 20,140,36
buffer_store_dwordx4 v[72:75], v84, s[36:39], s49 offen sc1
s_waitcnt lgkmcnt(2)
mav 12,144,40
R 24,49152
mav 16,148,40
mav 20,152,40
mav 12,156,44
R 28,50176
mav 16,160,44
mav 20,164,44
s_waitcnt lgkmcnt(2)
mav 12,168,48
R 32,51200
mav 16,172,48
mav 20,176,48
mav 12,180,52
R 36,52224
mav 16,184,52
mav 20,188,52
s_waitcnt lgkmcnt(2)
mav 12,192,24
R 40,53248
mav 16,196,24
mav 20,200,24
s_waitcnt vmcnt(2)
v_add_f32 v108, v108, v110
mav 12,204,28
R 44,54272
v_add_f32 v109, v109, v111
mav 16,208,28
mav 20,212,28
v_permlane32_swap_b32 v108, v109
s_waitcnt lgkmcnt(2)
mav 12,216,32
R 48,55296
v_add_f32 v110, v108, v109
mav 16,220,32
v_add_f32 v111, v108, v109
mav 20,224,32
mav 12,228,36
R 52,56320
v_permlane16_swap_b32 v110, v111
mav 16,232,36
v_add_f32 v108, v110, v111
v_add_f32 v109, v110, v111
mav 20,236,36
s_waitcnt lgkmcnt(2)
mav 12,240,40
R 24,57344
v_permlane32_swap_b32 v108, v109
mav 16,244,40
v_mul_f32 v110, 0x3a800000, v108
v_mul_f32 v111, 0x3a800000, v109
mav 20,248,40
v_fma_f32 v111, -v110, v110, v111
v_max_f32 v111, 0, v111
mav 12,252,44
R 28,58368
v_add_f32 v111, 0x3727c5ac, v111
mvv 16,128,44
v_rsq_f32 v108, v111
mvv 20,132,44
v_mul_f32_e64 v109, -v110, v108
s_waitcnt lgkmcnt(2)
mvv 12,136,48
R 32,59392
ds_write_b64 v89, v[108:109] offset:128
mvv 16,140,48
mvv 20,144,48
mvv 12,148,52
R 36,60416
mvv 16,152,52
mvv 20,156,52
s_waitcnt lgkmcnt(3)
mvv 12,160,24
R 40,61440
mvv 16,164,24
mvv 20,168,24
v_add_u32 v76, 1, v76
mvv 12,172,28
R 44,62464
mvv 16,176,28
mvv 20,180,28
s_waitcnt lgkmcnt(2)
mvv 12,184,32
R 48,63488
mvv 16,188,32
mvv 20,192,32
s_waitcnt vmcnt(0)
buffer_store_dwordx2 v[76:77], v81, s[36:39], s52 offen sc1
mvv 12,196,36
R 52,64512
mvv 16,200,36
s_add_u32 s49, s49, 346112
mvv 20,204,36
s_barrier
s_waitcnt lgkmcnt(2)
mvv 12,208,40
mvv 16,212,40
ds_read_b64 v[78:79], v89 offset:128
mvv 20,216,40
mvv 12,220,44
mvv 16,224,44
mvv 20,228,44
s_waitcnt lgkmcnt(1)
mvv 12,232,48
mvv 16,236,48
mvv 20,240,48
mvv 12,244,52
mvv 16,248,52
mvv 20,252,52
s_waitcnt lgkmcnt(0)
v_fma_f32 v104, v79, v56, v92
s_nop 3
v_fma_f32 v12, v12, v78, v104
v_fma_f32 v105, v79, v57, v93
v_fma_f32 v13, v13, v78, v105
v_mul_f32_e64 v12, v12, |v12|
v_mul_f32_e64 v13, v13, |v13|
v_cvt_pk_bf16_f32 v12, v12, v13
v_pk_max_i16 v68, v12, 0
v_fma_f32 v104, v79, v58, v94
v_fma_f32 v14, v14, v78, v104
v_fma_f32 v105, v79, v59, v95
v_fma_f32 v15, v15, v78, v105
v_mul_f32_e64 v14, v14, |v14|
v_mul_f32_e64 v15, v15, |v15|
v_cvt_pk_bf16_f32 v14, v14, v15
v_pk_max_i16 v69, v14, 0
v_fma_f32 v104, v79, v60, v96
v_fma_f32 v16, v16, v78, v104
v_fma_f32 v105, v79, v61, v97
v_fma_f32 v17, v17, v78, v105
v_mul_f32_e64 v16, v16, |v16|
v_mul_f32_e64 v17, v17, |v17|
v_cvt_pk_bf16_f32 v16, v16, v17
v_pk_max_i16 v70, v16, 0
v_fma_f32 v104, v79, v62, v98
v_fma_f32 v18, v18, v78, v104
v_fma_f32 v105, v79, v63, v99
v_fma_f32 v19, v19, v78, v105
v_mul_f32_e64 v18, v18, |v18|
v_mul_f32_e64 v19, v19, |v19|
v_cvt_pk_bf16_f32 v18, v18, v19
v_pk_max_i16 v71, v18, 0
buffer_store_dwordx4 v[68:71], v83, s[36:39], s49 offen sc1
v_fma_f32 v104, v79, v64, v100
v_fma_f32 v20, v20, v78, v104
v_fma_f32 v105, v79, v65, v101
v_fma_f32 v21, v21, v78, v105
v_mul_f32_e64 v20, v20, |v20|
v_mul_f32_e64 v21, v21, |v21|
v_cvt_pk_bf16_f32 v20, v20, v21
v_pk_max_i16 v72, v20, 0
v_pk_max_i16 v74, v20, 0
v_fma_f32 v104, v79, v66, v102
v_fma_f32 v22, v22, v78, v104
v_fma_f32 v105, v79, v67, v103
v_fma_f32 v23, v23, v78, v105
v_mul_f32_e64 v22, v22, |v22|
v_mul_f32_e64 v23, v23, |v23|
v_cvt_pk_bf16_f32 v22, v22, v23
v_pk_max_i16 v73, v22, 0
v_pk_max_i16 v75, v22, 0
v_permlane16_swap_b32 v72, v74
s_nop 0
v_permlane16_swap_b32 v73, v75
buffer_store_dwordx4 v[72:75], v84, s[36:39], s49 offen sc1
v_add_u32 v76, 37, v76
s_waitcnt vmcnt(0)
buffer_store_dwordx2 v[76:77], v81, s[36:39], s52 offen sc1
s_endpgm
LvF2:
s_add_u32 s49, s49, 346112
s_barrier
s_add_u32 m0, s50, 0
s_nop 0
D 85,0
D 85,1024
D 85,2048
D 85,3072
s_add_u32 m0, s50, 4096
s_nop 0
D 86,0
D 86,1024
D 86,2048
D 86,3072
s_add_u32 s49, s49, 346112
s_waitcnt vmcnt(0)
s_barrier
R 24,0
s_add_u32 m0, s50, 32768
R 28,1024
D 85,0
R 32,2048
D 85,1024
R 36,3072
s_waitcnt lgkmcnt(2)
v_mfma_f32_16x16x32_bf16 v[0:3], a[0:3], v[24:27], 0
R 40,4096
D 85,2048
v_mfma_f32_16x16x32_bf16 v[4:7], a[4:7], v[24:27], 0
v_mfma_f32_16x16x32_bf16 v[8:11], a[8:11], v[24:27], 0
mav 0,12,28
R 44,5120
mav 4,16,28
D 85,3072
mav 8,20,28
s_waitcnt lgkmcnt(2)
mav 0,24,32
R 48,6144
mav 4,28,32
s_add_u32 m0, s50, 36864
mav 8,32,32
mav 0,36,36
R 52,7168
D 86,0
mav 4,40,36
mav 8,44,36
s_waitcnt lgkmcnt(2)
mav 0,48,40
R 24,8192
mav 4,52,40
D 86,1024
mav 8,56,40
mav 0,60,44
R 28,9216
mav 4,64,44
mav 8,68,44
s_waitcnt lgkmcnt(2)
mav 0,72,48
R 32,10240
D 86,2048
mav 4,76,48
mav 8,80,48
mav 0,84,52
R 36,11264
mav 4,88,52
D 86,3072
mav 8,92,52
s_waitcnt lgkmcnt(2)
mav 0,96,24
R 40,12288
mav 4,100,24
mav 8,104,24
mav 0,108,28
R 44,13312
mav 4,112,28
mav 8,116,28
s_waitcnt lgkmcnt(2)
mav 0,120,32
R 48,14336
mav 4,124,32
mav 8,128,32
mav 0,132,36
R 52,15360
mav 4,136,36
mav 8,140,36
s_waitcnt lgkmcnt(2)
mav 0,144,40
R 24,16384
mav 4,148,40
mav 8,152,40
mav 0,156,44
R 28,17408
mav 4,160,44
mav 8,164,44
s_waitcnt lgkmcnt(2)
mav 0,168,48
R 32,18432
mav 4,172,48
mav 8,176,48
mav 0,180,52
R 36,19456
mav 4,184,52
mav 8,188,52
s_waitcnt lgkmcnt(2)
mav 0,192,24
R 40,20480
mav 4,196,24
mav 8,200,24
mav 0,204,28
R 44,21504
mav 4,208,28
mav 8,212,28
s_waitcnt lgkmcnt(2)
mav 0,216,32
R 48,22528
mav 4,220,32
mav 8,224,32
mav 0,228,36
R 52,23552
mav 4,232,36
mav 8,236,36
s_waitcnt lgkmcnt(2)
mav 0,240,40
R 24,24576
mav 4,244,40
mav 8,248,40
mav 0,252,44
R 28,25600
mvv 4,128,44
mvv 8,132,44
s_waitcnt lgkmcnt(2)
mvv 0,136,48
R 32,26624
mvv 4,140,48
mvv 8,144,48
mvv 0,148,52
R 36,27648
mvv 4,152,52
mvv 8,156,52
s_waitcnt lgkmcnt(2)
mvv 0,160,24
R 40,28672
mvv 4,164,24
mvv 8,168,24
mvv 0,172,28
R 44,29696
mvv 4,176,28
mvv 8,180,28
s_waitcnt lgkmcnt(2)
mvv 0,184,32
R 48,30720
s_xor_b32 s50, s50, 0x10000
mvv 4,188,32
mvv 8,192,32
mvv 0,196,36
R 52,31744
mvv 4,200,36
s_add_u32 s49, s49, 346112
mvv 8,204,36
s_waitcnt vmcnt(0)
s_barrier
s_waitcnt lgkmcnt(2)
mvv 0,208,40
R 24,32768
mvv 4,212,40
s_add_u32 m0, s50, 0
ds_read_b64 v[78:79], v89 offset:0
mvv 8,216,40
mvv 0,220,44
R 28,33792
D 85,0
mvv 4,224,44
mvv 8,228,44
s_waitcnt lgkmcnt(3)
mvv 0,232,48
R 32,34816
mvv 4,236,48
D 85,1024
mvv 8,240,48
mvv 0,244,52
R 36,35840
mvv 4,248,52
mvv 8,252,52
s_waitcnt lgkmcnt(2)
v_mfma_f32_16x16x32_bf16 v[12:15], a[0:3], v[24:27], 0
R 40,36864
D 85,2048
v_mfma_f32_16x16x32_bf16 v[16:19], a[4:7], v[24:27], 0
v_fma_f32 v104, v79, v56, v92
v_fma_f32 v0, v0, v78, v104
v_mfma_f32_16x16x32_bf16 v[20:23], a[8:11], v[24:27], 0
v_fma_f32 v105, v79, v57, v93
v_fma_f32 v1, v1, v78, v105
mav 12,12,28
R 44,37888
v_mul_f32_e64 v0, v0, |v0|
mav 16,16,28
D 85,3072
v_mul_f32_e64 v1, v1, |v1|
mav 20,20,28
v_cvt_pk_bf16_f32 v0, v0, v1
s_waitcnt lgkmcnt(2)
mav 12,24,32
R 48,38912
v_pk_max_i16 v68, v0, 0
mav 16,28,32
s_add_u32 m0, s50, 4096
v_fma_f32 v104, v79, v58, v94
mav 20,32,32
v_fma_f32 v2, v2, v78, v104
v_fma_f32 v105, v79, v59, v95
mav 12,36,36
R 52,39936
D 86,0
mav 16,40,36
v_fma_f32 v3, v3, v78, v105
v_mul_f32_e64 v2, v2, |v2|
mav 20,44,36
v_mul_f32_e64 v3, v3, |v3|
s_waitcnt lgkmcnt(2)
mav 12,48,40
R 24,40960
v_cvt_pk_bf16_f32 v2, v2, v3
mav 16,52,40
D 86,1024
v_pk_max_i16 v69, v2, 0
mav 20,56,40
v_fma_f32 v104, v79, v60, v96
v_fma_f32 v4, v4, v78, v104
mav 12,60,44
R 28,41984
v_fma_f32 v105, v79, v61, v97
mav 16,64,44
v_fma_f32 v5, v5, v78, v105
v_mul_f32_e64 v4, v4, |v4|
mav 20,68,44
v_mul_f32_e64 v5, v5, |v5|
s_waitcnt lgkmcnt(2)
mav 12,72,48
R 32,43008
D 86,2048
mav 16,76,48
v_cvt_pk_bf16_f32 v4, v4, v5
v_pk_max_i16 v70, v4, 0
mav 20,80,48
v_fma_f32 v104, v79, v62, v98
v_fma_f32 v6, v6, v78, v104
mav 12,84,52
R 36,44032
v_fma_f32 v105, v79, v63, v99
mav 16,88,52
D 86,3072
v_fma_f32 v7, v7, v78, v105
mav 20,92,52
v_mul_f32_e64 v6, v6, |v6|
s_waitcnt lgkmcnt(2)
mav 12,96,24
R 40,45056
v_mul_f32_e64 v7, v7, |v7|
mav 16,100,24
v_cvt_pk_bf16_f32 v6, v6, v7
v_pk_max_i16 v71, v6, 0
mav 20,104,24
buffer_store_dwordx4 v[68:71], v83, s[36:39], s49 offen sc1
v_fma_f32 v104, v79, v64, v100
mav 12,108,28
R 44,46080
v_fma_f32 v8, v8, v78, v104
mav 16,112,28
v_fma_f32 v105, v79, v65, v101
v_fma_f32 v9, v9, v78, v105
mav 20,116,28
v_mul_f32_e64 v8, v8, |v8|
s_waitcnt lgkmcnt(2)
mav 12,120,32
R 48,47104
v_mul_f32_e64 v9, v9, |v9|
mav 16,124,32
v_cvt_pk_bf16_f32 v8, v8, v9
v_pk_max_i16 v72, v8, 0
mav 20,128,32
v_pk_max_i16 v74, v8, 0
v_fma_f32 v104, v79, v66, v102
mav 12,132,36
R 52,48128
v_fma_f32 v10, v10, v78, v104
mav 16,136,36
v_fma_f32 v105, v79, v67, v103
v_fma_f32 v11, v11, v78, v105
mav 20,140,36
v_mul_f32_e64 v10, v10, |v10|
s_waitcnt lgkmcnt(2)
mav 12,144,40
R 24,49152
v_mul_f32_e64 v11, v11, |v11|
mav 16,148,40
v_cvt_pk_bf16_f32 v10, v10, v11
v_pk_max_i16 v73, v10, 0
mav 20,152,40
v_pk_max_i16 v75, v10, 0
v_permlane16_swap_b32 v72, v74
mav 12,156,44
R 28,50176
v_permlane16_swap_b32 v73, v75
mav 16,160,44
buffer_store_dwordx4 v[72:75], v84, s[36:39], s49 offen sc1
mav 20,164,44
s_waitcnt lgkmcnt(2)
mav 12,168,48
R 32,51200
mav 16,172,48
mav 20,176,48
mav 12,180,52
R 36,52224
mav 16,184,52
mav 20,188,52
s_waitcnt lgkmcnt(2)
mav 12,192,24
R 40,53248
mav 16,196,24
mav 20,200,24
mav 12,204,28
R 44,54272
mav 16,208,28
mav 20,212,28
s_waitcnt lgkmcnt(2)
mav 12,216,32
R 48,55296
mav 16,220,32
mav 20,224,32
mav 12,228,36
R 52,56320
mav 16,232,36
mav 20,236,36
s_waitcnt lgkmcnt(2)
mav 12,240,40
R 24,57344
mav 16,244,40
mav 20,248,40
mav 12,252,44
R 28,58368
mvv 16,128,44
mvv 20,132,44
s_waitcnt lgkmcnt(2)
mvv 12,136,48
R 32,59392
mvv 16,140,48
mvv 20,144,48
mvv 12,148,52
R 36,60416
mvv 16,152,52
mvv 20,156,52
s_waitcnt lgkmcnt(2)
mvv 12,160,24
R 40,61440
mvv 16,164,24
mvv 20,168,24
v_add_u32 v76, 1, v76
mvv 12,172,28
R 44,62464
mvv 16,176,28
mvv 20,180,28
s_waitcnt lgkmcnt(2)
mvv 12,184,32
R 48,63488
mvv 16,188,32
mvv 20,192,32
s_waitcnt vmcnt(0)
buffer_store_dwordx2 v[76:77], v81, s[36:39], s52 offen sc1
mvv 12,196,36
R 52,64512
v_xor_b32 v80, 0x10000, v80
mvv 16,200,36
s_add_u32 s49, s49, 346112
mvv 20,204,36
s_barrier
s_waitcnt lgkmcnt(2)
mvv 12,208,40
R 24,0
mvv 16,212,40
s_add_u32 m0, s50, 32768
ds_read_b64 v[78:79], v89 offset:128
mvv 20,216,40
mvv 12,220,44
R 28,1024
D 85,0
mvv 16,224,44
mvv 20,228,44
s_waitcnt lgkmcnt(3)
mvv 12,232,48
R 32,2048
mvv 16,236,48
D 85,1024
mvv 20,240,48
mvv 12,244,52
R 36,3072
mvv 16,248,52
mvv 20,252,52
s_mov_b32 s60, -30
.p2align 6
LF2_1:
s_waitcnt lgkmcnt(2)
v_mfma_f32_16x16x32_bf16 v[0:3], a[0:3], v[24:27], 0
R 40,4096
D 85,2048
v_mfma_f32_16x16x32_bf16 v[4:7], a[4:7], v[24:27], 0
v_fma_f32 v104, v79, v56, v92
v_fma_f32 v12, v12, v78, v104
v_mfma_f32_16x16x32_bf16 v[8:11], a[8:11], v[24:27], 0
v_fma_f32 v105, v79, v57, v93
v_fma_f32 v13, v13, v78, v105
mav 0,12,28
R 44,5120
v_mul_f32_e64 v12, v12, |v12|
mav 4,16,28
D 85,3072
v_mul_f32_e64 v13, v13, |v13|
mav 8,20,28
v_cvt_pk_bf16_f32 v12, v12, v13
s_waitcnt lgkmcnt(2)
mav 0,24,32
R 48,6144
v_pk_max_i16 v68, v12, 0
mav 4,28,32
s_add_u32 m0, s50, 36864
v_fma_f32 v104, v79, v58, v94
mav 8,32,32
v_fma_f32 v14, v14, v78, v104
v_fma_f32 v105, v79, v59, v95
mav 0,36,36
R 52,7168
D 86,0
mav 4,40,36
v_fma_f32 v15, v15, v78, v105
v_mul_f32_e64 v14, v14, |v14|
mav 8,44,36
v_mul_f32_e64 v15, v15, |v15|
s_waitcnt lgkmcnt(2)
mav 0,48,40
R 24,8192
v_cvt_pk_bf16_f32 v14, v14, v15
mav 4,52,40
D 86,1024
v_pk_max_i16 v69, v14, 0
mav 8,56,40
v_fma_f32 v104, v79, v60, v96
v_fma_f32 v16, v16, v78, v104
mav 0,60,44
R 28,9216
v_fma_f32 v105, v79, v61, v97
mav 4,64,44
v_fma_f32 v17, v17, v78, v105
v_mul_f32_e64 v16, v16, |v16|
mav 8,68,44
v_mul_f32_e64 v17, v17, |v17|
s_waitcnt lgkmcnt(2)
mav 0,72,48
R 32,10240
D 86,2048
mav 4,76,48
v_cvt_pk_bf16_f32 v16, v16, v17
v_pk_max_i16 v70, v16, 0
mav 8,80,48
v_fma_f32 v104, v79, v62, v98
v_fma_f32 v18, v18, v78, v104
mav 0,84,52
R 36,11264
v_fma_f32 v105, v79, v63, v99
mav 4,88,52
D 86,3072
v_fma_f32 v19, v19, v78, v105
mav 8,92,52
v_mul_f32_e64 v18, v18, |v18|
s_waitcnt lgkmcnt(2)
mav 0,96,24
R 40,12288
v_mul_f32_e64 v19, v19, |v19|
mav 4,100,24
v_cvt_pk_bf16_f32 v18, v18, v19
v_pk_max_i16 v71, v18, 0
mav 8,104,24
buffer_store_dwordx4 v[68:71], v83, s[36:39], s49 offen sc1
v_fma_f32 v104, v79, v64, v100
mav 0,108,28
R 44,13312
v_fma_f32 v20, v20, v78, v104
mav 4,112,28
v_fma_f32 v105, v79, v65, v101
v_fma_f32 v21, v21, v78, v105
mav 8,116,28
v_mul_f32_e64 v20, v20, |v20|
s_waitcnt lgkmcnt(2)
mav 0,120,32
R 48,14336
v_mul_f32_e64 v21, v21, |v21|
mav 4,124,32
v_cvt_pk_bf16_f32 v20, v20, v21
v_pk_max_i16 v72, v20, 0
mav 8,128,32
v_pk_max_i16 v74, v20, 0
v_fma_f32 v104, v79, v66, v102
mav 0,132,36
R 52,15360
v_fma_f32 v22, v22, v78, v104
mav 4,136,36
v_fma_f32 v105, v79, v67, v103
v_fma_f32 v23, v23, v78, v105
mav 8,140,36
v_mul_f32_e64 v22, v22, |v22|
s_waitcnt lgkmcnt(2)
mav 0,144,40
R 24,16384
v_mul_f32_e64 v23, v23, |v23|
mav 4,148,40
v_cvt_pk_bf16_f32 v22, v22, v23
v_pk_max_i16 v73, v22, 0
mav 8,152,40
v_pk_max_i16 v75, v22, 0
v_permlane16_swap_b32 v72, v74
mav 0,156,44
R 28,17408
v_permlane16_swap_b32 v73, v75
mav 4,160,44
buffer_store_dwordx4 v[72:75], v84, s[36:39], s49 offen sc1
mav 8,164,44
s_waitcnt lgkmcnt(2)
mav 0,168,48
R 32,18432
mav 4,172,48
mav 8,176,48
mav 0,180,52
R 36,19456
mav 4,184,52
mav 8,188,52
s_waitcnt lgkmcnt(2)
mav 0,192,24
R 40,20480
mav 4,196,24
mav 8,200,24
mav 0,204,28
R 44,21504
mav 4,208,28
mav 8,212,28
s_waitcnt lgkmcnt(2)
mav 0,216,32
R 48,22528
mav 4,220,32
mav 8,224,32
mav 0,228,36
R 52,23552
mav 4,232,36
mav 8,236,36
s_waitcnt lgkmcnt(2)
mav 0,240,40
R 24,24576
mav 4,244,40
mav 8,248,40
mav 0,252,44
R 28,25600
mvv 4,128,44
mvv 8,132,44
s_waitcnt lgkmcnt(2)
mvv 0,136,48
R 32,26624
mvv 4,140,48
mvv 8,144,48
mvv 0,148,52
R 36,27648
mvv 4,152,52
mvv 8,156,52
s_waitcnt lgkmcnt(2)
mvv 0,160,24
R 40,28672
mvv 4,164,24
mvv 8,168,24
v_add_u32 v76, 1, v76
mvv 0,172,28
R 44,29696
mvv 4,176,28
mvv 8,180,28
s_waitcnt lgkmcnt(2)
mvv 0,184,32
R 48,30720
s_xor_b32 s50, s50, 0x10000
mvv 4,188,32
mvv 8,192,32
s_waitcnt vmcnt(0)
buffer_store_dwordx2 v[76:77], v81, s[36:39], s52 offen sc1
mvv 0,196,36
R 52,31744
mvv 4,200,36
s_add_u32 s49, s49, 346112
mvv 8,204,36
s_barrier
s_waitcnt lgkmcnt(2)
mvv 0,208,40
R 24,32768
mvv 4,212,40
s_add_u32 m0, s50, 0
ds_read_b64 v[78:79], v89 offset:0
mvv 8,216,40
mvv 0,220,44
R 28,33792
D 85,0
mvv 4,224,44
mvv 8,228,44
s_waitcnt lgkmcnt(3)
mvv 0,232,48
R 32,34816
mvv 4,236,48
D 85,1024
mvv 8,240,48
mvv 0,244,52
R 36,35840
mvv 4,248,52
mvv 8,252,52
s_waitcnt lgkmcnt(2)
v_mfma_f32_16x16x32_bf16 v[12:15], a[0:3], v[24:27], 0
R 40,36864
D 85,2048
v_mfma_f32_16x16x32_bf16 v[16:19], a[4:7], v[24:27], 0
v_fma_f32 v104, v79, v56, v92
v_fma_f32 v0, v0, v78, v104
v_mfma_f32_16x16x32_bf16 v[20:23], a[8:11], v[24:27], 0
v_fma_f32 v105, v79, v57, v93
v_fma_f32 v1, v1, v78, v105
mav 12,12,28
R 44,37888
v_mul_f32_e64 v0, v0, |v0|
mav 16,16,28
D 85,3072
v_mul_f32_e64 v1, v1, |v1|
mav 20,20,28
v_cvt_pk_bf16_f32 v0, v0, v1
s_waitcnt lgkmcnt(2)
mav 12,24,32
R 48,38912
v_pk_max_i16 v68, v0, 0
mav 16,28,32
s_add_u32 m0, s50, 4096
v_fma_f32 v104, v79, v58, v94
mav 20,32,32
v_fma_f32 v2, v2, v78, v104
v_fma_f32 v105, v79, v59, v95
mav 12,36,36
R 52,39936
D 86,0
mav 16,40,36
v_fma_f32 v3, v3, v78, v105
v_mul_f32_e64 v2, v2, |v2|
mav 20,44,36
v_mul_f32_e64 v3, v3, |v3|
s_waitcnt lgkmcnt(2)
mav 12,48,40
R 24,40960
v_cvt_pk_bf16_f32 v2, v2, v3
mav 16,52,40
D 86,1024
v_pk_max_i16 v69, v2, 0
mav 20,56,40
v_fma_f32 v104, v79, v60, v96
v_fma_f32 v4, v4, v78, v104
mav 12,60,44
R 28,41984
v_fma_f32 v105, v79, v61, v97
mav 16,64,44
v_fma_f32 v5, v5, v78, v105
v_mul_f32_e64 v4, v4, |v4|
mav 20,68,44
v_mul_f32_e64 v5, v5, |v5|
s_waitcnt lgkmcnt(2)
mav 12,72,48
R 32,43008
D 86,2048
mav 16,76,48
v_cvt_pk_bf16_f32 v4, v4, v5
v_pk_max_i16 v70, v4, 0
mav 20,80,48
v_fma_f32 v104, v79, v62, v98
v_fma_f32 v6, v6, v78, v104
mav 12,84,52
R 36,44032
v_fma_f32 v105, v79, v63, v99
mav 16,88,52
D 86,3072
v_fma_f32 v7, v7, v78, v105
mav 20,92,52
v_mul_f32_e64 v6, v6, |v6|
s_waitcnt lgkmcnt(2)
mav 12,96,24
R 40,45056
v_mul_f32_e64 v7, v7, |v7|
mav 16,100,24
v_cvt_pk_bf16_f32 v6, v6, v7
v_pk_max_i16 v71, v6, 0
mav 20,104,24
buffer_store_dwordx4 v[68:71], v83, s[36:39], s49 offen sc1
v_fma_f32 v104, v79, v64, v100
mav 12,108,28
R 44,46080
v_fma_f32 v8, v8, v78, v104
mav 16,112,28
v_fma_f32 v105, v79, v65, v101
v_fma_f32 v9, v9, v78, v105
mav 20,116,28
v_mul_f32_e64 v8, v8, |v8|
s_waitcnt lgkmcnt(2)
mav 12,120,32
R 48,47104
v_mul_f32_e64 v9, v9, |v9|
mav 16,124,32
v_cvt_pk_bf16_f32 v8, v8, v9
v_pk_max_i16 v72, v8, 0
mav 20,128,32
v_pk_max_i16 v74, v8, 0
v_fma_f32 v104, v79, v66, v102
mav 12,132,36
R 52,48128
v_fma_f32 v10, v10, v78, v104
mav 16,136,36
v_fma_f32 v105, v79, v67, v103
v_fma_f32 v11, v11, v78, v105
mav 20,140,36
v_mul_f32_e64 v10, v10, |v10|
s_waitcnt lgkmcnt(2)
mav 12,144,40
R 24,49152
v_mul_f32_e64 v11, v11, |v11|
mav 16,148,40
v_cvt_pk_bf16_f32 v10, v10, v11
v_pk_max_i16 v73, v10, 0
mav 20,152,40
v_pk_max_i16 v75, v10, 0
v_permlane16_swap_b32 v72, v74
mav 12,156,44
R 28,50176
v_permlane16_swap_b32 v73, v75
mav 16,160,44
buffer_store_dwordx4 v[72:75], v84, s[36:39], s49 offen sc1
mav 20,164,44
s_waitcnt lgkmcnt(2)
mav 12,168,48
R 32,51200
mav 16,172,48
mav 20,176,48
mav 12,180,52
R 36,52224
mav 16,184,52
mav 20,188,52
s_waitcnt lgkmcnt(2)
mav 12,192,24
R 40,53248
mav 16,196,24
mav 20,200,24
mav 12,204,28
R 44,54272
mav 16,208,28
mav 20,212,28
s_waitcnt lgkmcnt(2)
mav 12,216,32
R 48,55296
mav 16,220,32
mav 20,224,32
mav 12,228,36
R 52,56320
mav 16,232,36
mav 20,236,36
s_waitcnt lgkmcnt(2)
mav 12,240,40
R 24,57344
mav 16,244,40
mav 20,248,40
mav 12,252,44
R 28,58368
mvv 16,128,44
mvv 20,132,44
s_waitcnt lgkmcnt(2)
mvv 12,136,48
R 32,59392
mvv 16,140,48
mvv 20,144,48
mvv 12,148,52
R 36,60416
mvv 16,152,52
mvv 20,156,52
s_waitcnt lgkmcnt(2)
mvv 12,160,24
R 40,61440
mvv 16,164,24
mvv 20,168,24
v_add_u32 v76, 1, v76
mvv 12,172,28
R 44,62464
mvv 16,176,28
mvv 20,180,28
s_waitcnt lgkmcnt(2)
mvv 12,184,32
R 48,63488
mvv 16,188,32
mvv 20,192,32
s_waitcnt vmcnt(0)
buffer_store_dwordx2 v[76:77], v81, s[36:39], s52 offen sc1
mvv 12,196,36
R 52,64512
v_xor_b32 v80, 0x10000, v80
mvv 16,200,36
s_add_u32 s49, s49, 346112
mvv 20,204,36
s_barrier
s_waitcnt lgkmcnt(2)
mvv 12,208,40
R 24,0
mvv 16,212,40
s_add_u32 m0, s50, 32768
ds_read_b64 v[78:79], v89 offset:128
mvv 20,216,40
mvv 12,220,44
R 28,1024
D 85,0
mvv 16,224,44
mvv 20,228,44
s_waitcnt lgkmcnt(3)
mvv 12,232,48
R 32,2048
mvv 16,236,48
D 85,1024
mvv 20,240,48
mvv 12,244,52
R 36,3072
mvv 16,248,52
mvv 20,252,52
s_add_u32 s60, s60, 1
s_cbranch_scc0 LF2_1
s_waitcnt lgkmcnt(2)
v_mfma_f32_16x16x32_bf16 v[0:3], a[0:3], v[24:27], 0
R 40,4096
D 85,2048
v_mfma_f32_16x16x32_bf16 v[4:7], a[4:7], v[24:27], 0
v_fma_f32 v104, v79, v56, v92
v_fma_f32 v12, v12, v78, v104
v_mfma_f32_16x16x32_bf16 v[8:11], a[8:11], v[24:27], 0
v_fma_f32 v105, v79, v57, v93
v_fma_f32 v13, v13, v78, v105
mav 0,12,28
R 44,5120
v_mul_f32_e64 v12, v12, |v12|
mav 4,16,28
D 85,3072
v_mul_f32_e64 v13, v13, |v13|
mav 8,20,28
v_cvt_pk_bf16_f32 v12, v12, v13
s_waitcnt lgkmcnt(2)
mav 0,24,32
R 48,6144
v_pk_max_i16 v68, v12, 0
mav 4,28,32
s_add_u32 m0, s50, 36864
v_fma_f32 v104, v79, v58, v94
mav 8,32,32
v_fma_f32 v14, v14, v78, v104
v_fma_f32 v105, v79, v59, v95
mav 0,36,36
R 52,7168
D 86,0
mav 4,40,36
v_fma_f32 v15, v15, v78, v105
v_mul_f32_e64 v14, v14, |v14|
mav 8,44,36
v_mul_f32_e64 v15, v15, |v15|
s_waitcnt lgkmcnt(2)
mav 0,48,40
R 24,8192
v_cvt_pk_bf16_f32 v14, v14, v15
mav 4,52,40
D 86,1024
v_pk_max_i16 v69, v14, 0
mav 8,56,40
v_fma_f32 v104, v79, v60, v96
v_fma_f32 v16, v16, v78, v104
mav 0,60,44
R 28,9216
v_fma_f32 v105, v79, v61, v97
mav 4,64,44
v_fma_f32 v17, v17, v78, v105
v_mul_f32_e64 v16, v16, |v16|
mav 8,68,44
v_mul_f32_e64 v17, v17, |v17|
s_waitcnt lgkmcnt(2)
mav 0,72,48
R 32,10240
D 86,2048
mav 4,76,48
v_cvt_pk_bf16_f32 v16, v16, v17
v_pk_max_i16 v70, v16, 0
mav 8,80,48
v_fma_f32 v104, v79, v62, v98
v_fma_f32 v18, v18, v78, v104
mav 0,84,52
R 36,11264
v_fma_f32 v105, v79, v63, v99
mav 4,88,52
D 86,3072
v_fma_f32 v19, v19, v78, v105
mav 8,92,52
v_mul_f32_e64 v18, v18, |v18|
s_waitcnt lgkmcnt(2)
mav 0,96,24
R 40,12288
v_mul_f32_e64 v19, v19, |v19|
mav 4,100,24
v_cvt_pk_bf16_f32 v18, v18, v19
v_pk_max_i16 v71, v18, 0
mav 8,104,24
buffer_store_dwordx4 v[68:71], v83, s[36:39], s49 offen sc1
v_fma_f32 v104, v79, v64, v100
mav 0,108,28
R 44,13312
v_fma_f32 v20, v20, v78, v104
mav 4,112,28
v_fma_f32 v105, v79, v65, v101
v_fma_f32 v21, v21, v78, v105
mav 8,116,28
v_mul_f32_e64 v20, v20, |v20|
s_waitcnt lgkmcnt(2)
mav 0,120,32
R 48,14336
v_mul_f32_e64 v21, v21, |v21|
mav 4,124,32
v_cvt_pk_bf16_f32 v20, v20, v21
v_pk_max_i16 v72, v20, 0
mav 8,128,32
v_pk_max_i16 v74, v20, 0
v_fma_f32 v104, v79, v66, v102
mav 0,132,36
R 52,15360
v_fma_f32 v22, v22, v78, v104
mav 4,136,36
v_fma_f32 v105, v79, v67, v103
v_fma_f32 v23, v23, v78, v105
mav 8,140,36
v_mul_f32_e64 v22, v22, |v22|
s_waitcnt lgkmcnt(2)
mav 0,144,40
R 24,16384
v_mul_f32_e64 v23, v23, |v23|
mav 4,148,40
v_cvt_pk_bf16_f32 v22, v22, v23
v_pk_max_i16 v73, v22, 0
mav 8,152,40
v_pk_max_i16 v75, v22, 0
v_permlane16_swap_b32 v72, v74
mav 0,156,44
R 28,17408
v_permlane16_swap_b32 v73, v75
mav 4,160,44
buffer_store_dwordx4 v[72:75], v84, s[36:39], s49 offen sc1
mav 8,164,44
s_waitcnt lgkmcnt(2)
mav 0,168,48
R 32,18432
mav 4,172,48
mav 8,176,48
mav 0,180,52
R 36,19456
mav 4,184,52
mav 8,188,52
s_waitcnt lgkmcnt(2)
mav 0,192,24
R 40,20480
mav 4,196,24
mav 8,200,24
mav 0,204,28
R 44,21504
mav 4,208,28
mav 8,212,28
s_waitcnt lgkmcnt(2)
mav 0,216,32
R 48,22528
mav 4,220,32
mav 8,224,32
mav 0,228,36
R 52,23552
mav 4,232,36
mav 8,236,36
s_waitcnt lgkmcnt(2)
mav 0,240,40
R 24,24576
mav 4,244,40
mav 8,248,40
mav 0,252,44
R 28,25600
mvv 4,128,44
mvv 8,132,44
s_waitcnt lgkmcnt(2)
mvv 0,136,48
R 32,26624
mvv 4,140,48
mvv 8,144,48
mvv 0,148,52
R 36,27648
mvv 4,152,52
mvv 8,156,52
s_waitcnt lgkmcnt(2)
mvv 0,160,24
R 40,28672
mvv 4,164,24
mvv 8,168,24
v_add_u32 v76, 1, v76
mvv 0,172,28
R 44,29696
mvv 4,176,28
mvv 8,180,28
s_waitcnt lgkmcnt(2)
mvv 0,184,32
R 48,30720
mvv 4,188,32
mvv 8,192,32
s_waitcnt vmcnt(0)
buffer_store_dwordx2 v[76:77], v81, s[36:39], s52 offen sc1
mvv 0,196,36
R 52,31744
mvv 4,200,36
s_add_u32 s49, s49, 346112
mvv 8,204,36
s_barrier
s_waitcnt lgkmcnt(2)
mvv 0,208,40
R 24,32768
mvv 4,212,40
ds_read_b64 v[78:79], v89 offset:0
mvv 8,216,40
mvv 0,220,44
R 28,33792
mvv 4,224,44
mvv 8,228,44
s_waitcnt lgkmcnt(3)
mvv 0,232,48
R 32,34816
mvv 4,236,48
mvv 8,240,48
mvv 0,244,52
R 36,35840
mvv 4,248,52
mvv 8,252,52
s_waitcnt lgkmcnt(2)
v_mfma_f32_16x16x32_bf16 v[12:15], a[0:3], v[24:27], 0
R 40,36864
v_fma_f32 v104, v79, v56, v92
v_mfma_f32_16x16x32_bf16 v[16:19], a[4:7], v[24:27], 0
v_fma_f32 v0, v0, v78, v104
v_fma_f32 v105, v79, v57, v93
v_mfma_f32_16x16x32_bf16 v[20:23], a[8:11], v[24:27], 0
v_fma_f32 v1, v1, v78, v105
v_mul_f32_e64 v0, v0, |v0|
mav 12,12,28
R 44,37888
v_mul_f32_e64 v1, v1, |v1|
mav 16,16,28
v_cvt_pk_bf16_f32 v0, v0, v1
v_pk_max_i16 v68, v0, 0
mav 20,20,28
v_fma_f32 v104, v79, v58, v94
s_waitcnt lgkmcnt(2)
mav 12,24,32
R 48,38912
v_fma_f32 v2, v2, v78, v104
mav 16,28,32
v_fma_f32 v105, v79, v59, v95
v_fma_f32 v3, v3, v78, v105
mav 20,32,32
v_mul_f32_e64 v2, v2, |v2|
v_mul_f32_e64 v3, v3, |v3|
mav 12,36,36
R 52,39936
v_cvt_pk_bf16_f32 v2, v2, v3
mav 16,40,36
v_pk_max_i16 v69, v2, 0
v_fma_f32 v104, v79, v60, v96
mav 20,44,36
v_fma_f32 v4, v4, v78, v104
s_waitcnt lgkmcnt(2)
mav 12,48,40
R 24,40960
v_fma_f32 v105, v79, v61, v97
mav 16,52,40
v_fma_f32 v5, v5, v78, v105
v_mul_f32_e64 v4, v4, |v4|
mav 20,56,40
v_mul_f32_e64 v5, v5, |v5|
v_cvt_pk_bf16_f32 v4, v4, v5
mav 12,60,44
R 28,41984
v_pk_max_i16 v70, v4, 0
mav 16,64,44
v_fma_f32 v104, v79, v62, v98
v_fma_f32 v6, v6, v78, v104
mav 20,68,44
v_fma_f32 v105, v79, v63, v99
s_waitcnt lgkmcnt(2)
mav 12,72,48
R 32,43008
v_fma_f32 v7, v7, v78, v105
mav 16,76,48
v_mul_f32_e64 v6, v6, |v6|
v_mul_f32_e64 v7, v7, |v7|
mav 20,80,48
v_cvt_pk_bf16_f32 v6, v6, v7
v_pk_max_i16 v71, v6, 0
mav 12,84,52
R 36,44032
buffer_store_dwordx4 v[68:71], v83, s[36:39], s49 offen sc1
mav 16,88,52
v_fma_f32 v104, v79, v64, v100
v_fma_f32 v8, v8, v78, v104
mav 20,92,52
v_fma_f32 v105, v79, v65, v101
s_waitcnt lgkmcnt(2)
mav 12,96,24
R 40,45056
v_fma_f32 v9, v9, v78, v105
mav 16,100,24
v_mul_f32_e64 v8, v8, |v8|
v_mul_f32_e64 v9, v9, |v9|
mav 20,104,24
v_cvt_pk_bf16_f32 v8, v8, v9
v_pk_max_i16 v72, v8, 0
mav 12,108,28
R 44,46080
v_pk_max_i16 v74, v8, 0
mav 16,112,28
v_fma_f32 v104, v79, v66, v102
v_fma_f32 v10, v10, v78, v104
mav 20,116,28
v_fma_f32 v105, v79, v67, v103
s_waitcnt lgkmcnt(2)
mav 12,120,32
R 48,47104
v_fma_f32 v11, v11, v78, v105
mav 16,124,32
v_mul_f32_e64 v10, v10, |v10|
v_mul_f32_e64 v11, v11, |v11|
mav 20,128,32
v_cvt_pk_bf16_f32 v10, v10, v11
v_pk_max_i16 v73, v10, 0
mav 12,132,36
R 52,48128
v_pk_max_i16 v75, v10, 0
mav 16,136,36
v_permlane16_swap_b32 v72, v74
v_permlane16_swap_b32 v73, v75
mav 20,140,36
buffer_store_dwordx4 v[72:75], v84, s[36:39], s49 offen sc1
s_waitcnt lgkmcnt(2)
mav 12,144,40
R 24,49152
mav 16,148,40
mav 20,152,40
mav 12,156,44
R 28,50176
mav 16,160,44
mav 20,164,44
s_waitcnt lgkmcnt(2)
mav 12,168,48
R 32,51200
mav 16,172,48
mav 20,176,48
mav 12,180,52
R 36,52224
mav 16,184,52
mav 20,188,52
s_waitcnt lgkmcnt(2)
mav 12,192,24
R 40,53248
mav 16,196,24
mav 20,200,24
mav 12,204,28
R 44,54272
mav 16,208,28
mav 20,212,28
s_waitcnt lgkmcnt(2)
mav 12,216,32
R 48,55296
mav 16,220,32
mav 20,224,32
mav 12,228,36
R 52,56320
mav 16,232,36
mav 20,236,36
s_waitcnt lgkmcnt(2)
mav 12,240,40
R 24,57344
mav 16,244,40
mav 20,248,40
mav 12,252,44
R 28,58368
mvv 16,128,44
mvv 20,132,44
s_waitcnt lgkmcnt(2)
mvv 12,136,48
R 32,59392
mvv 16,140,48
mvv 20,144,48
mvv 12,148,52
R 36,60416
mvv 16,152,52
mvv 20,156,52
s_waitcnt lgkmcnt(2)
mvv 12,160,24
R 40,61440
mvv 16,164,24
mvv 20,168,24
v_add_u32 v76, 1, v76
mvv 12,172,28
R 44,62464
mvv 16,176,28
mvv 20,180,28
s_waitcnt lgkmcnt(2)
mvv 12,184,32
R 48,63488
mvv 16,188,32
mvv 20,192,32
s_waitcnt vmcnt(0)
buffer_store_dwordx2 v[76:77], v81, s[36:39], s52 offen sc1
mvv 12,196,36
R 52,64512
mvv 16,200,36
s_add_u32 s49, s49, 346112
mvv 20,204,36
s_barrier
s_waitcnt lgkmcnt(2)
mvv 12,208,40
mvv 16,212,40
ds_read_b64 v[78:79], v89 offset:128
mvv 20,216,40
mvv 12,220,44
mvv 16,224,44
mvv 20,228,44
s_waitcnt lgkmcnt(1)
mvv 12,232,48
mvv 16,236,48
mvv 20,240,48
mvv 12,244,52
mvv 16,248,52
mvv 20,252,52
s_waitcnt lgkmcnt(0)
v_fma_f32 v104, v79, v56, v92
s_nop 3
v_fma_f32 v12, v12, v78, v104
v_fma_f32 v105, v79, v57, v93
v_fma_f32 v13, v13, v78, v105
v_mul_f32_e64 v12, v12, |v12|
v_mul_f32_e64 v13, v13, |v13|
v_cvt_pk_bf16_f32 v12, v12, v13
v_pk_max_i16 v68, v12, 0
v_fma_f32 v104, v79, v58, v94
v_fma_f32 v14, v14, v78, v104
v_fma_f32 v105, v79, v59, v95
v_fma_f32 v15, v15, v78, v105
v_mul_f32_e64 v14, v14, |v14|
v_mul_f32_e64 v15, v15, |v15|
v_cvt_pk_bf16_f32 v14, v14, v15
v_pk_max_i16 v69, v14, 0
v_fma_f32 v104, v79, v60, v96
v_fma_f32 v16, v16, v78, v104
v_fma_f32 v105, v79, v61, v97
v_fma_f32 v17, v17, v78, v105
v_mul_f32_e64 v16, v16, |v16|
v_mul_f32_e64 v17, v17, |v17|
v_cvt_pk_bf16_f32 v16, v16, v17
v_pk_max_i16 v70, v16, 0
v_fma_f32 v104, v79, v62, v98
v_fma_f32 v18, v18, v78, v104
v_fma_f32 v105, v79, v63, v99
v_fma_f32 v19, v19, v78, v105
v_mul_f32_e64 v18, v18, |v18|
v_mul_f32_e64 v19, v19, |v19|
v_cvt_pk_bf16_f32 v18, v18, v19
v_pk_max_i16 v71, v18, 0
buffer_store_dwordx4 v[68:71], v83, s[36:39], s49 offen sc1
v_fma_f32 v104, v79, v64, v100
v_fma_f32 v20, v20, v78, v104
v_fma_f32 v105, v79, v65, v101
v_fma_f32 v21, v21, v78, v105
v_mul_f32_e64 v20, v20, |v20|
v_mul_f32_e64 v21, v21, |v21|
v_cvt_pk_bf16_f32 v20, v20, v21
v_pk_max_i16 v72, v20, 0
v_pk_max_i16 v74, v20, 0
v_fma_f32 v104, v79, v66, v102
v_fma_f32 v22, v22, v78, v104
v_fma_f32 v105, v79, v67, v103
v_fma_f32 v23, v23, v78, v105
v_mul_f32_e64 v22, v22, |v22|
v_mul_f32_e64 v23, v23, |v23|
v_cvt_pk_bf16_f32 v22, v22, v23
v_pk_max_i16 v73, v22, 0
v_pk_max_i16 v75, v22, 0
v_permlane16_swap_b32 v72, v74
s_nop 0
v_permlane16_swap_b32 v73, v75
buffer_store_dwordx4 v[72:75], v84, s[36:39], s49 offen sc1
v_add_u32 v76, 37, v76
s_waitcnt vmcnt(0)
buffer_store_dwordx2 v[76:77], v81, s[36:39], s52 offen sc1
s_endpgm
LeF:
.purgem R
Lr4:
s_cmp_eq_u32 s60, 4
s_cbranch_scc0 Lr5
.macro R d,o
ds_read_b128 v[\d:\d+3], v80 offset:\o
.endm
buffer_load_dword v73, off, s[36:39], 0 sc1
s_getpc_b64 s[66:67]
LpG:
v_lshlrev_b32 v8, 3, v80
s_lshl_b32 s60, s53, 3
v_add_u32 v8, s60, v8
.set cn, 0
.rept 2
v_add_u32 v9, cn, v8
v_min_u32 v9, LeG - LpG - 4, v9
global_load_dword v10, v9, s[66:67]
.set cn, cn + 32768
.endr
E 0,0
E 1,4096
E 2,8192
E 3,12288
s_add_u32 s51, s51, 65536
E 0,16384
E 1,20480
E 2,24576
E 3,28672
s_add_u32 s51, s51, 65536
E 0,32768
E 1,36864
E 2,40960
E 3,45056
s_add_u32 s51, s51, 65536
E 0,49152
E 1,53248
E 2,57344
E 3,61440
s_add_u32 s51, s51, 65536
E 0,65536
E 1,69632
E 2,73728
E 3,77824
s_add_u32 s51, s51, 65536
E 0,81920
E 1,86016
E 2,90112
E 3,94208
s_add_u32 s51, s51, 65536
s_cmp_ge_u32 s51, s57
s_cselect_b32 s60, 1572864, 0
s_sub_u32 s51, s51, s60
E 0,98304
E 1,102400
E 2,106496
E 3,110592
s_add_u32 s51, s51, 65536
E 0,114688
E 1,118784
E 2,122880
E 3,126976
s_add_u32 s51, s51, 65536
E 0,131072
E 1,135168
E 2,139264
E 3,143360
s_add_u32 s51, s51, 65536
E 0,147456
E 1,151552
E 2,155648
E 3,159744
s_add_u32 s51, s51, 65536
s_waitcnt vmcnt(32)
s_barrier
v_mov_b32 v72, 0x100
v_add_u32 v73, 0x9e3779b9, v73
buffer_store_dwordx2 v[72:73], v81, s[36:39], s52 offen sc1
v_add_u32 v86, v86, v72
Ta 0,4,0
Ta 4,5,0
Ta 8,6,0
Ta 12,7,0
Ta 16,4,16384
Ta 20,5,16384
Ta 24,6,16384
Ta 28,7,16384
s_waitcnt lgkmcnt(0)
s_waitcnt vmcnt(25)
s_barrier
Ta 32,4,32768
Ta 36,5,32768
Ta 40,6,32768
Ta 44,7,32768
Ta 48,4,49152
Ta 52,5,49152
Ta 56,6,49152
Ta 60,7,49152
E 0,0
E 1,4096
E 2,8192
E 3,12288
s_add_u32 s51, s51, 65536
E 0,16384
E 1,20480
E 2,24576
E 3,28672
s_add_u32 s51, s51, 65536
s_cmp_ge_u32 s51, s57
s_cselect_b32 s60, 1572864, 0
s_sub_u32 s51, s51, s60
s_waitcnt lgkmcnt(0)
s_waitcnt vmcnt(25)
s_barrier
v_add_u32 v4, 65536, v4
v_add_u32 v5, 65536, v5
v_add_u32 v6, 65536, v6
v_add_u32 v7, 65536, v7
Ta 64,4,0
Ta 68,5,0
Ta 72,6,0
Ta 76,7,0
Ta 80,4,16384
Ta 84,5,16384
Ta 88,6,16384
Ta 92,7,16384
E 0,32768
E 1,36864
E 2,40960
E 3,45056
s_add_u32 s51, s51, 65536
E 0,49152
E 1,53248
E 2,57344
E 3,61440
s_add_u32 s51, s51, 65536
s_waitcnt lgkmcnt(0)
s_waitcnt vmcnt(25)
s_barrier
Ta 96,4,32768
Ta 100,5,32768
Ta 104,6,32768
Ta 108,7,32768
Ta 112,4,49152
Ta 116,5,49152
Ta 120,6,49152
Ta 124,7,49152
E 0,65536
E 1,69632
E 2,73728
E 3,77824
s_add_u32 s51, s51, 65536
E 0,81920
E 1,86016
E 2,90112
E 3,94208
s_add_u32 s51, s51, 65536
s_waitcnt lgkmcnt(0)
s_waitcnt vmcnt(25)
s_barrier
v_add_u32 v4, 65536, v4
v_add_u32 v5, 65536, v5
v_add_u32 v6, 65536, v6
v_add_u32 v7, 65536, v7
Ta 128,4,0
Ta 132,5,0
Ta 136,6,0
Ta 140,7,0
Ta 144,4,16384
Ta 148,5,16384
Ta 152,6,16384
Ta 156,7,16384
E 0,98304
E 1,102400
E 2,106496
E 3,110592
s_add_u32 s51, s51, 65536
E 0,114688
E 1,118784
E 2,122880
E 3,126976
s_add_u32 s51, s51, 65536
s_cmp_ge_u32 s51, s57
s_cselect_b32 s60, 1572864, 0
s_sub_u32 s51, s51, s60
s_waitcnt lgkmcnt(0)
s_waitcnt vmcnt(24)
s_barrier
v_add_u32 v4, -131072, v4
v_add_u32 v5, -131072, v5
v_add_u32 v6, -131072, v6
v_add_u32 v7, -131072, v7
Ta 160,4,0
Ta 164,5,0
Ta 168,6,0
Ta 172,7,0
Ta 176,4,16384
Ta 180,5,16384
Ta 184,6,16384
Ta 188,7,16384
E 0,131072
E 1,135168
E 2,139264
E 3,143360
s_add_u32 s51, s51, 65536
E 0,147456
E 1,151552
E 2,155648
E 3,159744
s_add_u32 s51, s51, 65536
s_waitcnt lgkmcnt(0)
s_waitcnt vmcnt(24)
)"
R"(
s_barrier
Ta 192,4,32768
Ta 196,5,32768
Ta 200,6,32768
Ta 204,7,32768
Ta 208,4,49152
Ta 212,5,49152
Ta 216,6,49152
Ta 220,7,49152
E 0,0
E 1,4096
E 2,8192
E 3,12288
s_add_u32 s51, s51, 65536
E 0,16384
E 1,20480
E 2,24576
E 3,28672
s_add_u32 s51, s51, 65536
s_waitcnt lgkmcnt(0)
s_waitcnt vmcnt(24)
s_barrier
v_add_u32 v4, 65536, v4
v_add_u32 v5, 65536, v5
v_add_u32 v6, 65536, v6
v_add_u32 v7, 65536, v7
Ta 224,4,0
Ta 228,5,0
Ta 232,6,0
Ta 236,7,0
Ta 240,4,16384
Ta 244,5,16384
Ta 248,6,16384
Ta 252,7,16384
E 0,32768
E 1,36864
E 2,40960
E 3,45056
s_add_u32 s51, s51, 65536
E 0,49152
E 1,53248
E 2,57344
E 3,61440
s_add_u32 s51, s51, 65536
s_cmp_ge_u32 s51, s57
s_cselect_b32 s60, 1572864, 0
s_sub_u32 s51, s51, s60
s_waitcnt lgkmcnt(0)
s_waitcnt vmcnt(24)
s_barrier
Tv 128,4,32768
Tv 132,5,32768
Tv 136,6,32768
Tv 140,7,32768
Tv 144,4,49152
Tv 148,5,49152
Tv 152,6,49152
Tv 156,7,49152
s_waitcnt lgkmcnt(0)
s_waitcnt vmcnt(16)
s_barrier
v_add_u32 v4, 65536, v4
v_add_u32 v5, 65536, v5
v_add_u32 v6, 65536, v6
v_add_u32 v7, 65536, v7
Tv 160,4,0
Tv 164,5,0
Tv 168,6,0
Tv 172,7,0
Tv 176,4,16384
Tv 180,5,16384
Tv 184,6,16384
Tv 188,7,16384
s_waitcnt lgkmcnt(0)
s_waitcnt vmcnt(8)
s_barrier
v_add_u32 v4, -131072, v4
v_add_u32 v5, -131072, v5
v_add_u32 v6, -131072, v6
v_add_u32 v7, -131072, v7
Tv 192,4,0
Tv 196,5,0
Tv 200,6,0
Tv 204,7,0
Tv 208,4,16384
Tv 212,5,16384
Tv 216,6,16384
Tv 220,7,16384
s_waitcnt lgkmcnt(0)
s_waitcnt vmcnt(0)
s_barrier
Tv 224,4,32768
Tv 228,5,32768
Tv 232,6,32768
Tv 236,7,32768
Tv 240,4,49152
Tv 244,5,49152
Tv 248,6,49152
Tv 252,7,49152
s_waitcnt lgkmcnt(0)
s_waitcnt vmcnt(0)
s_barrier
s_nop 7
s_and_b32 s60, s48, 3
s_cmp_eq_u32 s60, 1
s_cbranch_scc1 LvG1
LvG0:
buffer_load_dwordx2 v[74:75], v85, s[36:39], 0 offen sc1
s_waitcnt vmcnt(0)
ds_write_b32 v82, v74
v_sub_u32 v74, v74, v86
v_xor_b32 v75, v75, v73
v_cmp_lt_u64 vcc, s[62:63], v[74:75]
s_waitcnt lgkmcnt(0)
s_cbranch_vccnz CG0_1
BG0_1:
s_add_u32 s49, s49, 346112
s_barrier
s_add_u32 m0, s50, 0
s_nop 0
D 83,0
D 83,1024
D 83,2048
D 83,3072
s_add_u32 m0, s50, 4096
s_nop 0
D 84,0
D 84,1024
s_mov_b32 m0, 142336
s_nop 0
buffer_load_dword v85, s[36:39], 0 offen sc1 lds
v_add_u32 v86, 1, v86
buffer_load_dword v85, s[36:39], 0 offen sc1 lds
buffer_load_dword v85, s[36:39], 0 offen sc1 lds
ds_read_b32 v74, v82 offset:0
s_waitcnt lgkmcnt(0)
v_cmp_gt_i32 vcc, v86, v74
s_waitcnt vmcnt(3)
s_cbranch_vccnz CG0_2
BG0_2:
s_add_u32 s49, s49, 346112
s_barrier
s_add_u32 m0, s50, 32768
R 32,0
D 83,0
R 36,1024
D 83,1024
R 40,2048
D 83,2048
s_waitcnt lgkmcnt(1)
v_mfma_f32_16x16x32_bf16 v[0:3], a[0:3], v[32:35], 0
R 44,3072
v_mfma_f32_16x16x32_bf16 v[4:7], a[4:7], v[32:35], 0
v_mfma_f32_16x16x32_bf16 v[8:11], a[8:11], v[32:35], 0
D 83,3072
v_mfma_f32_16x16x32_bf16 v[12:15], a[12:15], v[32:35], 0
mav 0,16,36
R 48,4096
mav 4,20,36
s_add_u32 m0, s50, 36864
mav 8,24,36
mav 12,28,36
D 84,0
s_waitcnt lgkmcnt(1)
mav 0,32,40
R 52,5120
mav 4,36,40
mav 8,40,40
mav 12,44,40
D 84,1024
mav 0,48,44
R 56,6144
mav 4,52,44
mav 8,56,44
mav 12,60,44
s_waitcnt lgkmcnt(1)
mav 0,64,48
R 60,7168
mav 4,68,48
mav 8,72,48
mav 12,76,48
mav 0,80,52
R 32,8192
mav 4,84,52
mav 8,88,52
mav 12,92,52
s_waitcnt lgkmcnt(1)
mav 0,96,56
R 36,9216
mav 4,100,56
mav 8,104,56
mav 12,108,56
mav 0,112,60
R 40,10240
mav 4,116,60
mav 8,120,60
s_mov_b32 m0, 142336
mav 12,124,60
s_waitcnt lgkmcnt(1)
mav 0,128,32
R 44,11264
buffer_load_dword v85, s[36:39], 0 offen sc1 lds
mav 4,132,32
mav 8,136,32
mav 12,140,32
mav 0,144,36
R 48,12288
mav 4,148,36
mav 8,152,36
mav 12,156,36
s_waitcnt lgkmcnt(1)
mav 0,160,40
R 52,13312
v_add_u32 v86, 1, v86
mav 4,164,40
mav 8,168,40
mav 12,172,40
mav 0,176,44
R 56,14336
buffer_load_dword v85, s[36:39], 0 offen sc1 lds
mav 4,180,44
mav 8,184,44
mav 12,188,44
s_waitcnt lgkmcnt(1)
mav 0,192,48
R 60,15360
mav 4,196,48
mav 8,200,48
mav 12,204,48
mav 0,208,52
R 32,16384
mav 4,212,52
mav 8,216,52
mav 12,220,52
s_waitcnt lgkmcnt(1)
mav 0,224,56
R 36,17408
buffer_load_dword v85, s[36:39], 0 offen sc1 lds
mav 4,228,56
mav 8,232,56
mav 12,236,56
mav 0,240,60
R 40,18432
mav 4,244,60
mav 8,248,60
mav 12,252,60
s_waitcnt lgkmcnt(1)
mvv 0,128,32
R 44,19456
mvv 4,132,32
mvv 8,136,32
mvv 12,140,32
mvv 0,144,36
R 48,20480
mvv 4,148,36
mvv 8,152,36
mvv 12,156,36
s_waitcnt lgkmcnt(1)
mvv 0,160,40
R 52,21504
mvv 4,164,40
ds_read_b32 v74, v82 offset:0
mvv 8,168,40
mvv 12,172,40
mvv 0,176,44
R 56,22528
mvv 4,180,44
mvv 8,184,44
s_xor_b32 s50, s50, 0x10000
mvv 12,188,44
s_waitcnt lgkmcnt(1)
mvv 0,192,48
R 60,23552
mvv 4,196,48
v_cmp_gt_i32 vcc, v86, v74
mvv 8,200,48
s_waitcnt vmcnt(3)
s_cbranch_vccnz CG0_3
BG0_3:
mvv 12,204,48
s_barrier
s_add_u32 s49, s49, 346112
mvv 0,208,52
R 32,32768
s_add_u32 m0, s50, 0
mvv 4,212,52
D 83,0
mvv 8,216,52
mvv 12,220,52
s_waitcnt lgkmcnt(1)
mvv 0,224,56
R 36,33792
mvv 4,228,56
D 83,1024
mvv 8,232,56
mvv 12,236,56
mvv 0,240,60
R 40,34816
mvv 4,244,60
mvv 8,248,60
D 83,2048
mvv 12,252,60
s_waitcnt lgkmcnt(1)
v_mfma_f32_16x16x32_bf16 v[16:19], a[0:3], v[32:35], 0
R 44,35840
v_cvt_pk_bf16_f32 v64, v0, v1
v_mfma_f32_16x16x32_bf16 v[20:23], a[4:7], v[32:35], 0
v_cvt_pk_bf16_f32 v65, v2, v3
v_cvt_pk_bf16_f32 v66, v4, v5
v_mfma_f32_16x16x32_bf16 v[24:27], a[8:11], v[32:35], 0
D 83,3072
v_cvt_pk_bf16_f32 v67, v6, v7
v_mfma_f32_16x16x32_bf16 v[28:31], a[12:15], v[32:35], 0
v_cvt_pk_bf16_f32 v68, v8, v9
v_cvt_pk_bf16_f32 v69, v10, v11
mav 16,16,36
R 48,36864
v_cvt_pk_bf16_f32 v70, v12, v13
mav 20,20,36
s_add_u32 m0, s50, 4096
v_cvt_pk_bf16_f32 v71, v14, v15
mav 24,24,36
buffer_store_dwordx4 v[64:67], v87, s[36:39], s49 offen sc1
mav 28,28,36
s_waitcnt lgkmcnt(1)
mav 16,32,40
R 52,37888
mav 20,36,40
mav 24,40,40
buffer_store_dwordx4 v[68:71], v87, s[36:39], s49 offen offset:1024 sc1
mav 28,44,40
mav 16,48,44
R 56,38912
mav 20,52,44
mav 24,56,44
D 84,0
mav 28,60,44
s_waitcnt lgkmcnt(1)
mav 16,64,48
R 60,39936
mav 20,68,48
mav 24,72,48
D 84,1024
mav 28,76,48
mav 16,80,52
R 32,40960
mav 20,84,52
mav 24,88,52
mav 28,92,52
s_waitcnt lgkmcnt(1)
mav 16,96,56
R 36,41984
mav 20,100,56
mav 24,104,56
mav 28,108,56
mav 16,112,60
R 40,43008
mav 20,116,60
mav 24,120,60
s_mov_b32 m0, 142336
mav 28,124,60
s_waitcnt lgkmcnt(1)
mav 16,128,32
R 44,44032
buffer_load_dword v85, s[36:39], 0 offen sc1 lds
mav 20,132,32
mav 24,136,32
mav 28,140,32
mav 16,144,36
R 48,45056
mav 20,148,36
mav 24,152,36
mav 28,156,36
s_waitcnt lgkmcnt(1)
mav 16,160,40
R 52,46080
v_add_u32 v86, 1, v86
mav 20,164,40
mav 24,168,40
mav 28,172,40
mav 16,176,44
R 56,47104
buffer_load_dword v85, s[36:39], 0 offen sc1 lds
mav 20,180,44
mav 24,184,44
mav 28,188,44
s_waitcnt lgkmcnt(1)
mav 16,192,48
R 60,48128
mav 20,196,48
mav 24,200,48
mav 28,204,48
mav 16,208,52
R 32,49152
mav 20,212,52
mav 24,216,52
v_add_u32 v72, 1, v72
mav 28,220,52
s_waitcnt lgkmcnt(1)
mav 16,224,56
R 36,50176
buffer_load_dword v85, s[36:39], 0 offen sc1 lds
mav 20,228,56
mav 24,232,56
mav 28,236,56
mav 16,240,60
R 40,51200
mav 20,244,60
s_waitcnt vmcnt(5)
buffer_store_dwordx2 v[72:73], v81, s[36:39], s52 offen sc1
mav 24,248,60
mav 28,252,60
s_waitcnt lgkmcnt(1)
mvv 16,128,32
R 44,52224
mvv 20,132,32
mvv 24,136,32
mvv 28,140,32
mvv 16,144,36
R 48,53248
mvv 20,148,36
mvv 24,152,36
mvv 28,156,36
s_waitcnt lgkmcnt(1)
mvv 16,160,40
R 52,54272
mvv 20,164,40
ds_read_b32 v74, v82 offset:0
mvv 24,168,40
mvv 28,172,40
mvv 16,176,44
R 56,55296
mvv 20,180,44
mvv 24,184,44
mvv 28,188,44
s_waitcnt lgkmcnt(1)
mvv 16,192,48
R 60,56320
mvv 20,196,48
v_xor_b32 v80, 0x10000, v80
v_cmp_gt_i32 vcc, v86, v74
mvv 24,200,48
s_waitcnt vmcnt(4)
s_cbranch_vccnz CG0_4
BG0_4:
mvv 28,204,48
s_barrier
s_add_u32 s49, s49, 346112
mvv 16,208,52
R 32,0
s_add_u32 m0, s50, 32768
mvv 20,212,52
D 83,0
mvv 24,216,52
mvv 28,220,52
s_waitcnt lgkmcnt(1)
mvv 16,224,56
R 36,1024
mvv 20,228,56
D 83,1024
mvv 24,232,56
mvv 28,236,56
mvv 16,240,60
R 40,2048
mvv 20,244,60
mvv 24,248,60
D 83,2048
mvv 28,252,60
s_mov_b32 s60, -30
.p2align 6
LG0_5:
s_waitcnt lgkmcnt(1)
v_mfma_f32_16x16x32_bf16 v[0:3], a[0:3], v[32:35], 0
R 44,3072
v_cvt_pk_bf16_f32 v64, v16, v17
v_mfma_f32_16x16x32_bf16 v[4:7], a[4:7], v[32:35], 0
v_cvt_pk_bf16_f32 v65, v18, v19
v_cvt_pk_bf16_f32 v66, v20, v21
v_mfma_f32_16x16x32_bf16 v[8:11], a[8:11], v[32:35], 0
D 83,3072
v_cvt_pk_bf16_f32 v67, v22, v23
v_mfma_f32_16x16x32_bf16 v[12:15], a[12:15], v[32:35], 0
v_cvt_pk_bf16_f32 v68, v24, v25
v_cvt_pk_bf16_f32 v69, v26, v27
mav 0,16,36
R 48,4096
v_cvt_pk_bf16_f32 v70, v28, v29
mav 4,20,36
s_add_u32 m0, s50, 36864
v_cvt_pk_bf16_f32 v71, v30, v31
mav 8,24,36
buffer_store_dwordx4 v[64:67], v87, s[36:39], s49 offen sc1
mav 12,28,36
s_waitcnt lgkmcnt(1)
mav 0,32,40
R 52,5120
mav 4,36,40
mav 8,40,40
buffer_store_dwordx4 v[68:71], v87, s[36:39], s49 offen offset:1024 sc1
mav 12,44,40
mav 0,48,44
R 56,6144
mav 4,52,44
mav 8,56,44
D 84,0
mav 12,60,44
s_waitcnt lgkmcnt(1)
mav 0,64,48
R 60,7168
mav 4,68,48
mav 8,72,48
D 84,1024
mav 12,76,48
mav 0,80,52
R 32,8192
mav 4,84,52
mav 8,88,52
mav 12,92,52
s_waitcnt lgkmcnt(1)
mav 0,96,56
R 36,9216
mav 4,100,56
mav 8,104,56
mav 12,108,56
mav 0,112,60
R 40,10240
mav 4,116,60
mav 8,120,60
s_mov_b32 m0, 142336
mav 12,124,60
s_waitcnt lgkmcnt(1)
mav 0,128,32
R 44,11264
buffer_load_dword v85, s[36:39], 0 offen sc1 lds
mav 4,132,32
mav 8,136,32
mav 12,140,32
mav 0,144,36
R 48,12288
mav 4,148,36
mav 8,152,36
mav 12,156,36
s_waitcnt lgkmcnt(1)
mav 0,160,40
R 52,13312
v_add_u32 v86, 1, v86
mav 4,164,40
mav 8,168,40
mav 12,172,40
mav 0,176,44
R 56,14336
buffer_load_dword v85, s[36:39], 0 offen sc1 lds
mav 4,180,44
mav 8,184,44
mav 12,188,44
s_waitcnt lgkmcnt(1)
mav 0,192,48
R 60,15360
mav 4,196,48
mav 8,200,48
mav 12,204,48
mav 0,208,52
R 32,16384
mav 4,212,52
mav 8,216,52
v_add_u32 v72, 1, v72
mav 12,220,52
s_waitcnt lgkmcnt(1)
mav 0,224,56
R 36,17408
buffer_load_dword v85, s[36:39], 0 offen sc1 lds
mav 4,228,56
mav 8,232,56
mav 12,236,56
mav 0,240,60
R 40,18432
mav 4,244,60
s_waitcnt vmcnt(5)
buffer_store_dwordx2 v[72:73], v81, s[36:39], s52 offen sc1
mav 8,248,60
mav 12,252,60
s_waitcnt lgkmcnt(1)
mvv 0,128,32
R 44,19456
mvv 4,132,32
mvv 8,136,32
mvv 12,140,32
mvv 0,144,36
R 48,20480
mvv 4,148,36
mvv 8,152,36
mvv 12,156,36
s_waitcnt lgkmcnt(1)
mvv 0,160,40
R 52,21504
mvv 4,164,40
ds_read_b32 v74, v82 offset:0
mvv 8,168,40
mvv 12,172,40
mvv 0,176,44
R 56,22528
mvv 4,180,44
mvv 8,184,44
s_xor_b32 s50, s50, 0x10000
mvv 12,188,44
s_waitcnt lgkmcnt(1)
mvv 0,192,48
R 60,23552
mvv 4,196,48
v_cmp_gt_i32 vcc, v86, v74
mvv 8,200,48
s_waitcnt vmcnt(4)
s_cbranch_vccnz CG0_6
BG0_6:
mvv 12,204,48
s_barrier
s_add_u32 s49, s49, 346112
mvv 0,208,52
R 32,32768
s_add_u32 m0, s50, 0
mvv 4,212,52
D 83,0
mvv 8,216,52
mvv 12,220,52
s_waitcnt lgkmcnt(1)
mvv 0,224,56
R 36,33792
mvv 4,228,56
D 83,1024
mvv 8,232,56
mvv 12,236,56
mvv 0,240,60
R 40,34816
mvv 4,244,60
mvv 8,248,60
D 83,2048
mvv 12,252,60
s_waitcnt lgkmcnt(1)
v_mfma_f32_16x16x32_bf16 v[16:19], a[0:3], v[32:35], 0
R 44,35840
v_cvt_pk_bf16_f32 v64, v0, v1
v_mfma_f32_16x16x32_bf16 v[20:23], a[4:7], v[32:35], 0
v_cvt_pk_bf16_f32 v65, v2, v3
v_cvt_pk_bf16_f32 v66, v4, v5
v_mfma_f32_16x16x32_bf16 v[24:27], a[8:11], v[32:35], 0
D 83,3072
v_cvt_pk_bf16_f32 v67, v6, v7
v_mfma_f32_16x16x32_bf16 v[28:31], a[12:15], v[32:35], 0
v_cvt_pk_bf16_f32 v68, v8, v9
v_cvt_pk_bf16_f32 v69, v10, v11
mav 16,16,36
R 48,36864
v_cvt_pk_bf16_f32 v70, v12, v13
mav 20,20,36
s_add_u32 m0, s50, 4096
v_cvt_pk_bf16_f32 v71, v14, v15
mav 24,24,36
buffer_store_dwordx4 v[64:67], v87, s[36:39], s49 offen sc1
mav 28,28,36
s_waitcnt lgkmcnt(1)
mav 16,32,40
R 52,37888
mav 20,36,40
mav 24,40,40
buffer_store_dwordx4 v[68:71], v87, s[36:39], s49 offen offset:1024 sc1
mav 28,44,40
mav 16,48,44
R 56,38912
mav 20,52,44
mav 24,56,44
D 84,0
mav 28,60,44
s_waitcnt lgkmcnt(1)
mav 16,64,48
R 60,39936
mav 20,68,48
mav 24,72,48
D 84,1024
mav 28,76,48
mav 16,80,52
R 32,40960
mav 20,84,52
mav 24,88,52
mav 28,92,52
s_waitcnt lgkmcnt(1)
mav 16,96,56
R 36,41984
mav 20,100,56
mav 24,104,56
mav 28,108,56
mav 16,112,60
R 40,43008
mav 20,116,60
mav 24,120,60
s_mov_b32 m0, 142336
mav 28,124,60
s_waitcnt lgkmcnt(1)
mav 16,128,32
R 44,44032
buffer_load_dword v85, s[36:39], 0 offen sc1 lds
mav 20,132,32
mav 24,136,32
mav 28,140,32
mav 16,144,36
R 48,45056
mav 20,148,36
mav 24,152,36
mav 28,156,36
s_waitcnt lgkmcnt(1)
mav 16,160,40
R 52,46080
v_add_u32 v86, 1, v86
mav 20,164,40
mav 24,168,40
mav 28,172,40
mav 16,176,44
R 56,47104
buffer_load_dword v85, s[36:39], 0 offen sc1 lds
mav 20,180,44
mav 24,184,44
mav 28,188,44
s_waitcnt lgkmcnt(1)
mav 16,192,48
R 60,48128
mav 20,196,48
mav 24,200,48
mav 28,204,48
mav 16,208,52
R 32,49152
mav 20,212,52
mav 24,216,52
v_add_u32 v72, 1, v72
mav 28,220,52
s_waitcnt lgkmcnt(1)
mav 16,224,56
R 36,50176
buffer_load_dword v85, s[36:39], 0 offen sc1 lds
mav 20,228,56
mav 24,232,56
mav 28,236,56
mav 16,240,60
R 40,51200
mav 20,244,60
s_waitcnt vmcnt(5)
buffer_store_dwordx2 v[72:73], v81, s[36:39], s52 offen sc1
mav 24,248,60
mav 28,252,60
s_waitcnt lgkmcnt(1)
mvv 16,128,32
R 44,52224
mvv 20,132,32
mvv 24,136,32
mvv 28,140,32
mvv 16,144,36
R 48,53248
mvv 20,148,36
mvv 24,152,36
mvv 28,156,36
s_waitcnt lgkmcnt(1)
mvv 16,160,40
R 52,54272
mvv 20,164,40
ds_read_b32 v74, v82 offset:0
mvv 24,168,40
mvv 28,172,40
mvv 16,176,44
R 56,55296
mvv 20,180,44
mvv 24,184,44
mvv 28,188,44
s_waitcnt lgkmcnt(1)
mvv 16,192,48
R 60,56320
mvv 20,196,48
v_xor_b32 v80, 0x10000, v80
v_cmp_gt_i32 vcc, v86, v74
mvv 24,200,48
s_waitcnt vmcnt(4)
s_cbranch_vccnz CG0_7
BG0_7:
mvv 28,204,48
s_barrier
s_add_u32 s49, s49, 346112
mvv 16,208,52
R 32,0
s_add_u32 m0, s50, 32768
mvv 20,212,52
D 83,0
mvv 24,216,52
mvv 28,220,52
s_waitcnt lgkmcnt(1)
mvv 16,224,56
R 36,1024
mvv 20,228,56
D 83,1024
mvv 24,232,56
mvv 28,236,56
mvv 16,240,60
R 40,2048
mvv 20,244,60
mvv 24,248,60
D 83,2048
mvv 28,252,60
s_add_u32 s60, s60, 1
s_cbranch_scc0 LG0_5
s_waitcnt lgkmcnt(1)
v_mfma_f32_16x16x32_bf16 v[0:3], a[0:3], v[32:35], 0
R 44,3072
v_cvt_pk_bf16_f32 v64, v16, v17
v_mfma_f32_16x16x32_bf16 v[4:7], a[4:7], v[32:35], 0
v_cvt_pk_bf16_f32 v65, v18, v19
v_cvt_pk_bf16_f32 v66, v20, v21
v_mfma_f32_16x16x32_bf16 v[8:11], a[8:11], v[32:35], 0
D 83,3072
v_cvt_pk_bf16_f32 v67, v22, v23
v_mfma_f32_16x16x32_bf16 v[12:15], a[12:15], v[32:35], 0
v_cvt_pk_bf16_f32 v68, v24, v25
v_cvt_pk_bf16_f32 v69, v26, v27
mav 0,16,36
R 48,4096
v_cvt_pk_bf16_f32 v70, v28, v29
mav 4,20,36
s_add_u32 m0, s50, 36864
v_cvt_pk_bf16_f32 v71, v30, v31
mav 8,24,36
buffer_store_dwordx4 v[64:67], v87, s[36:39], s49 offen sc1
mav 12,28,36
s_waitcnt lgkmcnt(1)
mav 0,32,40
R 52,5120
mav 4,36,40
mav 8,40,40
buffer_store_dwordx4 v[68:71], v87, s[36:39], s49 offen offset:1024 sc1
mav 12,44,40
mav 0,48,44
R 56,6144
mav 4,52,44
mav 8,56,44
D 84,0
mav 12,60,44
s_waitcnt lgkmcnt(1)
mav 0,64,48
R 60,7168
mav 4,68,48
mav 8,72,48
D 84,1024
mav 12,76,48
mav 0,80,52
R 32,8192
mav 4,84,52
mav 8,88,52
mav 12,92,52
s_waitcnt lgkmcnt(1)
mav 0,96,56
R 36,9216
mav 4,100,56
mav 8,104,56
mav 12,108,56
mav 0,112,60
R 40,10240
mav 4,116,60
mav 8,120,60
mav 12,124,60
s_waitcnt lgkmcnt(1)
mav 0,128,32
R 44,11264
mav 4,132,32
mav 8,136,32
mav 12,140,32
mav 0,144,36
R 48,12288
mav 4,148,36
mav 8,152,36
mav 12,156,36
s_waitcnt lgkmcnt(1)
mav 0,160,40
R 52,13312
mav 4,164,40
mav 8,168,40
mav 12,172,40
mav 0,176,44
R 56,14336
mav 4,180,44
mav 8,184,44
mav 12,188,44
s_waitcnt lgkmcnt(1)
mav 0,192,48
R 60,15360
mav 4,196,48
mav 8,200,48
mav 12,204,48
mav 0,208,52
R 32,16384
mav 4,212,52
mav 8,216,52
v_add_u32 v72, 1, v72
mav 12,220,52
s_waitcnt lgkmcnt(1)
mav 0,224,56
R 36,17408
mav 4,228,56
mav 8,232,56
mav 12,236,56
mav 0,240,60
R 40,18432
mav 4,244,60
s_waitcnt vmcnt(2)
buffer_store_dwordx2 v[72:73], v81, s[36:39], s52 offen sc1
mav 8,248,60
mav 12,252,60
s_waitcnt lgkmcnt(1)
mvv 0,128,32
R 44,19456
mvv 4,132,32
mvv 8,136,32
mvv 12,140,32
mvv 0,144,36
R 48,20480
mvv 4,148,36
mvv 8,152,36
mvv 12,156,36
s_waitcnt lgkmcnt(1)
mvv 0,160,40
R 52,21504
mvv 4,164,40
mvv 8,168,40
mvv 12,172,40
mvv 0,176,44
R 56,22528
mvv 4,180,44
mvv 8,184,44
mvv 12,188,44
s_waitcnt lgkmcnt(1)
mvv 0,192,48
R 60,23552
mvv 4,196,48
mvv 8,200,48
s_add_u32 s49, s49, 346112
mvv 12,204,48
s_waitcnt vmcnt(1)
s_barrier
mvv 0,208,52
R 32,32768
mvv 4,212,52
mvv 8,216,52
mvv 12,220,52
s_waitcnt lgkmcnt(1)
mvv 0,224,56
R 36,33792
mvv 4,228,56
mvv 8,232,56
mvv 12,236,56
mvv 0,240,60
R 40,34816
mvv 4,244,60
mvv 8,248,60
mvv 12,252,60
s_waitcnt lgkmcnt(1)
v_mfma_f32_16x16x32_bf16 v[16:19], a[0:3], v[32:35], 0
R 44,35840
v_mfma_f32_16x16x32_bf16 v[20:23], a[4:7], v[32:35], 0
v_cvt_pk_bf16_f32 v64, v0, v1
v_cvt_pk_bf16_f32 v65, v2, v3
v_mfma_f32_16x16x32_bf16 v[24:27], a[8:11], v[32:35], 0
v_cvt_pk_bf16_f32 v66, v4, v5
v_cvt_pk_bf16_f32 v67, v6, v7
v_mfma_f32_16x16x32_bf16 v[28:31], a[12:15], v[32:35], 0
buffer_store_dwordx4 v[64:67], v87, s[36:39], s49 offen sc1
v_cvt_pk_bf16_f32 v68, v8, v9
mav 16,16,36
R 48,36864
v_cvt_pk_bf16_f32 v69, v10, v11
mav 20,20,36
v_cvt_pk_bf16_f32 v70, v12, v13
v_cvt_pk_bf16_f32 v71, v14, v15
mav 24,24,36
mav 28,28,36
buffer_store_dwordx4 v[68:71], v87, s[36:39], s49 offen offset:1024 sc1
s_waitcnt lgkmcnt(1)
mav 16,32,40
R 52,37888
mav 20,36,40
mav 24,40,40
mav 28,44,40
mav 16,48,44
R 56,38912
mav 20,52,44
mav 24,56,44
mav 28,60,44
s_waitcnt lgkmcnt(1)
mav 16,64,48
R 60,39936
mav 20,68,48
mav 24,72,48
mav 28,76,48
mav 16,80,52
R 32,40960
mav 20,84,52
mav 24,88,52
mav 28,92,52
s_waitcnt lgkmcnt(1)
mav 16,96,56
R 36,41984
mav 20,100,56
mav 24,104,56
mav 28,108,56
mav 16,112,60
R 40,43008
mav 20,116,60
mav 24,120,60
mav 28,124,60
s_waitcnt lgkmcnt(1)
mav 16,128,32
R 44,44032
mav 20,132,32
mav 24,136,32
mav 28,140,32
mav 16,144,36
R 48,45056
mav 20,148,36
mav 24,152,36
mav 28,156,36
s_waitcnt lgkmcnt(1)
mav 16,160,40
R 52,46080
mav 20,164,40
mav 24,168,40
mav 28,172,40
mav 16,176,44
R 56,47104
mav 20,180,44
mav 24,184,44
mav 28,188,44
s_waitcnt lgkmcnt(1)
mav 16,192,48
R 60,48128
mav 20,196,48
mav 24,200,48
mav 28,204,48
mav 16,208,52
R 32,49152
mav 20,212,52
mav 24,216,52
v_add_u32 v72, 1, v72
mav 28,220,52
s_waitcnt lgkmcnt(1)
mav 16,224,56
R 36,50176
mav 20,228,56
mav 24,232,56
mav 28,236,56
mav 16,240,60
R 40,51200
mav 20,244,60
s_waitcnt vmcnt(0)
buffer_store_dwordx2 v[72:73], v81, s[36:39], s52 offen sc1
mav 24,248,60
mav 28,252,60
s_waitcnt lgkmcnt(1)
mvv 16,128,32
R 44,52224
mvv 20,132,32
mvv 24,136,32
mvv 28,140,32
mvv 16,144,36
R 48,53248
mvv 20,148,36
mvv 24,152,36
mvv 28,156,36
s_waitcnt lgkmcnt(1)
mvv 16,160,40
R 52,54272
mvv 20,164,40
mvv 24,168,40
mvv 28,172,40
mvv 16,176,44
R 56,55296
mvv 20,180,44
mvv 24,184,44
mvv 28,188,44
s_waitcnt lgkmcnt(1)
mvv 16,192,48
R 60,56320
mvv 20,196,48
mvv 24,200,48
s_add_u32 s49, s49, 346112
mvv 28,204,48
mvv 16,208,52
mvv 20,212,52
mvv 24,216,52
mvv 28,220,52
s_waitcnt lgkmcnt(0)
mvv 16,224,56
mvv 20,228,56
mvv 24,232,56
mvv 28,236,56
mvv 16,240,60
mvv 20,244,60
mvv 24,248,60
mvv 28,252,60
s_nop 4
v_cvt_pk_bf16_f32 v64, v16, v17
v_cvt_pk_bf16_f32 v65, v18, v19
v_cvt_pk_bf16_f32 v66, v20, v21
v_cvt_pk_bf16_f32 v67, v22, v23
buffer_store_dwordx4 v[64:67], v87, s[36:39], s49 offen sc1
v_cvt_pk_bf16_f32 v68, v24, v25
v_cvt_pk_bf16_f32 v69, v26, v27
v_cvt_pk_bf16_f32 v70, v28, v29
v_cvt_pk_bf16_f32 v71, v30, v31
buffer_store_dwordx4 v[68:71], v87, s[36:39], s49 offen offset:1024 sc1
v_add_u32 v72, 37, v72
s_waitcnt vmcnt(0)
buffer_store_dwordx2 v[72:73], v81, s[36:39], s52 offen sc1
s_endpgm
CG0_1:
s_cmp_eq_u32 s61, 0
s_cbranch_scc1 BG0_1
s_sub_u32 s61, s61, 1
s_sleep 8
buffer_load_dwordx2 v[74:75], v85, s[36:39], 0 offen sc1
s_waitcnt vmcnt(0)
ds_write_b32 v82, v74
v_sub_u32 v74, v74, v86
v_xor_b32 v75, v75, v73
v_cmp_lt_u64 vcc, s[62:63], v[74:75]
s_waitcnt lgkmcnt(0)
s_cbranch_vccnz CG0_1
s_branch BG0_1
CG0_2:
s_mov_b32 s58, m0
s_mov_b32 m0, 142336
DG0_2:
s_cmp_eq_u32 s61, 0
s_cbranch_scc1 EG0_2
s_sub_u32 s61, s61, 1
buffer_load_dword v85, s[36:39], 0 offen sc1 lds
s_sleep 1
ds_read_b32 v74, v82
s_waitcnt lgkmcnt(0)
v_cmp_gt_i32 vcc, v86, v74
s_cbranch_vccnz DG0_2
EG0_2:
s_mov_b32 m0, s58
s_branch BG0_2
CG0_3:
s_mov_b32 s58, m0
s_mov_b32 m0, 142336
DG0_3:
s_cmp_eq_u32 s61, 0
s_cbranch_scc1 EG0_3
s_sub_u32 s61, s61, 1
buffer_load_dword v85, s[36:39], 0 offen sc1 lds
s_sleep 1
ds_read_b32 v74, v82
s_waitcnt lgkmcnt(0)
v_cmp_gt_i32 vcc, v86, v74
s_cbranch_vccnz DG0_3
EG0_3:
s_mov_b32 m0, s58
s_branch BG0_3
CG0_4:
s_mov_b32 s58, m0
s_mov_b32 m0, 142336
DG0_4:
s_cmp_eq_u32 s61, 0
s_cbranch_scc1 EG0_4
s_sub_u32 s61, s61, 1
buffer_load_dword v85, s[36:39], 0 offen sc1 lds
s_sleep 1
ds_read_b32 v74, v82
s_waitcnt lgkmcnt(0)
v_cmp_gt_i32 vcc, v86, v74
s_cbranch_vccnz DG0_4
EG0_4:
s_mov_b32 m0, s58
s_branch BG0_4
CG0_6:
s_mov_b32 s58, m0
s_mov_b32 m0, 142336
DG0_6:
s_cmp_eq_u32 s61, 0
s_cbranch_scc1 EG0_6
s_sub_u32 s61, s61, 1
buffer_load_dword v85, s[36:39], 0 offen sc1 lds
s_sleep 1
ds_read_b32 v74, v82
s_waitcnt lgkmcnt(0)
v_cmp_gt_i32 vcc, v86, v74
s_cbranch_vccnz DG0_6
EG0_6:
s_mov_b32 m0, s58
s_branch BG0_6
CG0_7:
s_mov_b32 s58, m0
s_mov_b32 m0, 142336
DG0_7:
s_cmp_eq_u32 s61, 0
s_cbranch_scc1 EG0_7
s_sub_u32 s61, s61, 1
buffer_load_dword v85, s[36:39], 0 offen sc1 lds
s_sleep 1
ds_read_b32 v74, v82
s_waitcnt lgkmcnt(0)
v_cmp_gt_i32 vcc, v86, v74
s_cbranch_vccnz DG0_7
EG0_7:
s_mov_b32 m0, s58
s_branch BG0_7
LvG1:
s_add_u32 s49, s49, 346112
s_barrier
s_add_u32 m0, s50, 0
s_nop 0
D 83,0
D 83,1024
D 83,2048
D 83,3072
s_add_u32 m0, s50, 4096
s_nop 0
D 84,0
D 84,1024
s_add_u32 s49, s49, 346112
s_waitcnt vmcnt(0)
s_barrier
s_add_u32 m0, s50, 32768
R 32,0
D 83,0
R 36,1024
D 83,1024
R 40,2048
D 83,2048
s_waitcnt lgkmcnt(1)
v_mfma_f32_16x16x32_bf16 v[0:3], a[0:3], v[32:35], 0
R 44,3072
v_mfma_f32_16x16x32_bf16 v[4:7], a[4:7], v[32:35], 0
v_mfma_f32_16x16x32_bf16 v[8:11], a[8:11], v[32:35], 0
D 83,3072
v_mfma_f32_16x16x32_bf16 v[12:15], a[12:15], v[32:35], 0
mav 0,16,36
R 48,4096
mav 4,20,36
s_add_u32 m0, s50, 36864
mav 8,24,36
mav 12,28,36
D 84,0
s_waitcnt lgkmcnt(1)
mav 0,32,40
R 52,5120
mav 4,36,40
mav 8,40,40
mav 12,44,40
D 84,1024
mav 0,48,44
R 56,6144
mav 4,52,44
mav 8,56,44
mav 12,60,44
s_waitcnt lgkmcnt(1)
mav 0,64,48
R 60,7168
mav 4,68,48
mav 8,72,48
mav 12,76,48
mav 0,80,52
R 32,8192
mav 4,84,52
mav 8,88,52
mav 12,92,52
s_waitcnt lgkmcnt(1)
mav 0,96,56
R 36,9216
mav 4,100,56
mav 8,104,56
mav 12,108,56
mav 0,112,60
R 40,10240
mav 4,116,60
mav 8,120,60
mav 12,124,60
s_waitcnt lgkmcnt(1)
mav 0,128,32
R 44,11264
mav 4,132,32
mav 8,136,32
mav 12,140,32
mav 0,144,36
R 48,12288
mav 4,148,36
mav 8,152,36
mav 12,156,36
s_waitcnt lgkmcnt(1)
mav 0,160,40
R 52,13312
mav 4,164,40
mav 8,168,40
mav 12,172,40
mav 0,176,44
R 56,14336
mav 4,180,44
mav 8,184,44
mav 12,188,44
s_waitcnt lgkmcnt(1)
mav 0,192,48
R 60,15360
mav 4,196,48
mav 8,200,48
mav 12,204,48
mav 0,208,52
R 32,16384
mav 4,212,52
mav 8,216,52
mav 12,220,52
s_waitcnt lgkmcnt(1)
mav 0,224,56
R 36,17408
mav 4,228,56
mav 8,232,56
mav 12,236,56
mav 0,240,60
R 40,18432
mav 4,244,60
mav 8,248,60
mav 12,252,60
s_waitcnt lgkmcnt(1)
mvv 0,128,32
R 44,19456
mvv 4,132,32
mvv 8,136,32
mvv 12,140,32
mvv 0,144,36
R 48,20480
mvv 4,148,36
mvv 8,152,36
mvv 12,156,36
s_waitcnt lgkmcnt(1)
mvv 0,160,40
R 52,21504
mvv 4,164,40
mvv 8,168,40
mvv 12,172,40
mvv 0,176,44
R 56,22528
mvv 4,180,44
mvv 8,184,44
s_xor_b32 s50, s50, 0x10000
mvv 12,188,44
s_waitcnt lgkmcnt(1)
mvv 0,192,48
R 60,23552
mvv 4,196,48
mvv 8,200,48
s_add_u32 s49, s49, 346112
mvv 12,204,48
s_waitcnt vmcnt(0)
s_barrier
mvv 0,208,52
R 32,32768
s_add_u32 m0, s50, 0
mvv 4,212,52
D 83,0
mvv 8,216,52
mvv 12,220,52
s_waitcnt lgkmcnt(1)
mvv 0,224,56
R 36,33792
mvv 4,228,56
D 83,1024
mvv 8,232,56
mvv 12,236,56
mvv 0,240,60
R 40,34816
mvv 4,244,60
mvv 8,248,60
D 83,2048
mvv 12,252,60
s_waitcnt lgkmcnt(1)
v_mfma_f32_16x16x32_bf16 v[16:19], a[0:3], v[32:35], 0
R 44,35840
v_cvt_pk_bf16_f32 v64, v0, v1
v_mfma_f32_16x16x32_bf16 v[20:23], a[4:7], v[32:35], 0
v_cvt_pk_bf16_f32 v65, v2, v3
v_cvt_pk_bf16_f32 v66, v4, v5
v_mfma_f32_16x16x32_bf16 v[24:27], a[8:11], v[32:35], 0
D 83,3072
v_cvt_pk_bf16_f32 v67, v6, v7
v_mfma_f32_16x16x32_bf16 v[28:31], a[12:15], v[32:35], 0
v_cvt_pk_bf16_f32 v68, v8, v9
v_cvt_pk_bf16_f32 v69, v10, v11
mav 16,16,36
R 48,36864
v_cvt_pk_bf16_f32 v70, v12, v13
mav 20,20,36
s_add_u32 m0, s50, 4096
v_cvt_pk_bf16_f32 v71, v14, v15
mav 24,24,36
buffer_store_dwordx4 v[64:67], v87, s[36:39], s49 offen sc1
mav 28,28,36
s_waitcnt lgkmcnt(1)
mav 16,32,40
R 52,37888
mav 20,36,40
mav 24,40,40
buffer_store_dwordx4 v[68:71], v87, s[36:39], s49 offen offset:1024 sc1
mav 28,44,40
mav 16,48,44
R 56,38912
mav 20,52,44
mav 24,56,44
D 84,0
mav 28,60,44
s_waitcnt lgkmcnt(1)
mav 16,64,48
R 60,39936
mav 20,68,48
mav 24,72,48
D 84,1024
mav 28,76,48
mav 16,80,52
R 32,40960
mav 20,84,52
mav 24,88,52
mav 28,92,52
s_waitcnt lgkmcnt(1)
mav 16,96,56
R 36,41984
mav 20,100,56
mav 24,104,56
mav 28,108,56
mav 16,112,60
R 40,43008
mav 20,116,60
mav 24,120,60
mav 28,124,60
s_waitcnt lgkmcnt(1)
mav 16,128,32
R 44,44032
mav 20,132,32
mav 24,136,32
mav 28,140,32
mav 16,144,36
R 48,45056
mav 20,148,36
mav 24,152,36
mav 28,156,36
s_waitcnt lgkmcnt(1)
mav 16,160,40
R 52,46080
mav 20,164,40
mav 24,168,40
mav 28,172,40
mav 16,176,44
R 56,47104
mav 20,180,44
mav 24,184,44
mav 28,188,44
s_waitcnt lgkmcnt(1)
mav 16,192,48
R 60,48128
mav 20,196,48
mav 24,200,48
mav 28,204,48
mav 16,208,52
R 32,49152
mav 20,212,52
mav 24,216,52
v_add_u32 v72, 1, v72
mav 28,220,52
s_waitcnt lgkmcnt(1)
mav 16,224,56
R 36,50176
mav 20,228,56
mav 24,232,56
mav 28,236,56
mav 16,240,60
R 40,51200
mav 20,244,60
s_waitcnt vmcnt(2)
buffer_store_dwordx2 v[72:73], v81, s[36:39], s52 offen sc1
mav 24,248,60
mav 28,252,60
s_waitcnt lgkmcnt(1)
mvv 16,128,32
R 44,52224
mvv 20,132,32
mvv 24,136,32
mvv 28,140,32
mvv 16,144,36
R 48,53248
mvv 20,148,36
mvv 24,152,36
mvv 28,156,36
s_waitcnt lgkmcnt(1)
mvv 16,160,40
R 52,54272
mvv 20,164,40
mvv 24,168,40
mvv 28,172,40
mvv 16,176,44
R 56,55296
mvv 20,180,44
mvv 24,184,44
mvv 28,188,44
s_waitcnt lgkmcnt(1)
mvv 16,192,48
R 60,56320
mvv 20,196,48
v_xor_b32 v80, 0x10000, v80
mvv 24,200,48
s_add_u32 s49, s49, 346112
mvv 28,204,48
s_waitcnt vmcnt(1)
s_barrier
mvv 16,208,52
R 32,0
s_add_u32 m0, s50, 32768
mvv 20,212,52
D 83,0
mvv 24,216,52
mvv 28,220,52
s_waitcnt lgkmcnt(1)
mvv 16,224,56
R 36,1024
mvv 20,228,56
D 83,1024
mvv 24,232,56
mvv 28,236,56
mvv 16,240,60
R 40,2048
mvv 20,244,60
mvv 24,248,60
D 83,2048
mvv 28,252,60
s_mov_b32 s60, -30
.p2align 6
LG1_1:
s_waitcnt lgkmcnt(1)
v_mfma_f32_16x16x32_bf16 v[0:3], a[0:3], v[32:35], 0
R 44,3072
v_cvt_pk_bf16_f32 v64, v16, v17
v_mfma_f32_16x16x32_bf16 v[4:7], a[4:7], v[32:35], 0
v_cvt_pk_bf16_f32 v65, v18, v19
v_cvt_pk_bf16_f32 v66, v20, v21
v_mfma_f32_16x16x32_bf16 v[8:11], a[8:11], v[32:35], 0
D 83,3072
v_cvt_pk_bf16_f32 v67, v22, v23
v_mfma_f32_16x16x32_bf16 v[12:15], a[12:15], v[32:35], 0
v_cvt_pk_bf16_f32 v68, v24, v25
v_cvt_pk_bf16_f32 v69, v26, v27
mav 0,16,36
R 48,4096
v_cvt_pk_bf16_f32 v70, v28, v29
mav 4,20,36
s_add_u32 m0, s50, 36864
v_cvt_pk_bf16_f32 v71, v30, v31
mav 8,24,36
buffer_store_dwordx4 v[64:67], v87, s[36:39], s49 offen sc1
mav 12,28,36
s_waitcnt lgkmcnt(1)
mav 0,32,40
R 52,5120
mav 4,36,40
mav 8,40,40
buffer_store_dwordx4 v[68:71], v87, s[36:39], s49 offen offset:1024 sc1
mav 12,44,40
mav 0,48,44
R 56,6144
mav 4,52,44
mav 8,56,44
D 84,0
mav 12,60,44
s_waitcnt lgkmcnt(1)
mav 0,64,48
R 60,7168
mav 4,68,48
mav 8,72,48
D 84,1024
mav 12,76,48
mav 0,80,52
R 32,8192
mav 4,84,52
mav 8,88,52
mav 12,92,52
s_waitcnt lgkmcnt(1)
mav 0,96,56
R 36,9216
mav 4,100,56
mav 8,104,56
mav 12,108,56
mav 0,112,60
R 40,10240
mav 4,116,60
mav 8,120,60
mav 12,124,60
s_waitcnt lgkmcnt(1)
mav 0,128,32
R 44,11264
mav 4,132,32
mav 8,136,32
mav 12,140,32
mav 0,144,36
R 48,12288
mav 4,148,36
mav 8,152,36
mav 12,156,36
s_waitcnt lgkmcnt(1)
mav 0,160,40
R 52,13312
mav 4,164,40
mav 8,168,40
mav 12,172,40
mav 0,176,44
R 56,14336
mav 4,180,44
mav 8,184,44
mav 12,188,44
s_waitcnt lgkmcnt(1)
mav 0,192,48
R 60,15360
mav 4,196,48
mav 8,200,48
mav 12,204,48
mav 0,208,52
R 32,16384
mav 4,212,52
mav 8,216,52
v_add_u32 v72, 1, v72
mav 12,220,52
s_waitcnt lgkmcnt(1)
mav 0,224,56
R 36,17408
mav 4,228,56
mav 8,232,56
mav 12,236,56
mav 0,240,60
R 40,18432
mav 4,244,60
s_waitcnt vmcnt(2)
buffer_store_dwordx2 v[72:73], v81, s[36:39], s52 offen sc1
mav 8,248,60
mav 12,252,60
s_waitcnt lgkmcnt(1)
mvv 0,128,32
R 44,19456
mvv 4,132,32
mvv 8,136,32
mvv 12,140,32
mvv 0,144,36
R 48,20480
mvv 4,148,36
mvv 8,152,36
mvv 12,156,36
s_waitcnt lgkmcnt(1)
mvv 0,160,40
R 52,21504
mvv 4,164,40
mvv 8,168,40
mvv 12,172,40
mvv 0,176,44
R 56,22528
mvv 4,180,44
mvv 8,184,44
s_xor_b32 s50, s50, 0x10000
mvv 12,188,44
s_waitcnt lgkmcnt(1)
mvv 0,192,48
R 60,23552
mvv 4,196,48
mvv 8,200,48
s_add_u32 s49, s49, 346112
mvv 12,204,48
s_waitcnt vmcnt(1)
s_barrier
mvv 0,208,52
R 32,32768
s_add_u32 m0, s50, 0
mvv 4,212,52
D 83,0
mvv 8,216,52
mvv 12,220,52
s_waitcnt lgkmcnt(1)
mvv 0,224,56
R 36,33792
mvv 4,228,56
D 83,1024
mvv 8,232,56
mvv 12,236,56
mvv 0,240,60
R 40,34816
mvv 4,244,60
mvv 8,248,60
D 83,2048
mvv 12,252,60
s_waitcnt lgkmcnt(1)
v_mfma_f32_16x16x32_bf16 v[16:19], a[0:3], v[32:35], 0
R 44,35840
v_cvt_pk_bf16_f32 v64, v0, v1
v_mfma_f32_16x16x32_bf16 v[20:23], a[4:7], v[32:35], 0
v_cvt_pk_bf16_f32 v65, v2, v3
v_cvt_pk_bf16_f32 v66, v4, v5
v_mfma_f32_16x16x32_bf16 v[24:27], a[8:11], v[32:35], 0
D 83,3072
v_cvt_pk_bf16_f32 v67, v6, v7
v_mfma_f32_16x16x32_bf16 v[28:31], a[12:15], v[32:35], 0
v_cvt_pk_bf16_f32 v68, v8, v9
v_cvt_pk_bf16_f32 v69, v10, v11
mav 16,16,36
R 48,36864
v_cvt_pk_bf16_f32 v70, v12, v13
mav 20,20,36
s_add_u32 m0, s50, 4096
v_cvt_pk_bf16_f32 v71, v14, v15
mav 24,24,36
buffer_store_dwordx4 v[64:67], v87, s[36:39], s49 offen sc1
mav 28,28,36
s_waitcnt lgkmcnt(1)
mav 16,32,40
R 52,37888
mav 20,36,40
mav 24,40,40
buffer_store_dwordx4 v[68:71], v87, s[36:39], s49 offen offset:1024 sc1
mav 28,44,40
mav 16,48,44
R 56,38912
mav 20,52,44
mav 24,56,44
D 84,0
mav 28,60,44
s_waitcnt lgkmcnt(1)
mav 16,64,48
R 60,39936
mav 20,68,48
mav 24,72,48
D 84,1024
mav 28,76,48
mav 16,80,52
R 32,40960
mav 20,84,52
mav 24,88,52
mav 28,92,52
s_waitcnt lgkmcnt(1)
mav 16,96,56
R 36,41984
mav 20,100,56
mav 24,104,56
mav 28,108,56
mav 16,112,60
R 40,43008
mav 20,116,60
mav 24,120,60
mav 28,124,60
s_waitcnt lgkmcnt(1)
mav 16,128,32
R 44,44032
mav 20,132,32
mav 24,136,32
mav 28,140,32
mav 16,144,36
R 48,45056
mav 20,148,36
mav 24,152,36
mav 28,156,36
s_waitcnt lgkmcnt(1)
mav 16,160,40
R 52,46080
mav 20,164,40
mav 24,168,40
mav 28,172,40
mav 16,176,44
R 56,47104
mav 20,180,44
mav 24,184,44
mav 28,188,44
s_waitcnt lgkmcnt(1)
mav 16,192,48
R 60,48128
mav 20,196,48
mav 24,200,48
mav 28,204,48
mav 16,208,52
R 32,49152
mav 20,212,52
mav 24,216,52
v_add_u32 v72, 1, v72
mav 28,220,52
s_waitcnt lgkmcnt(1)
mav 16,224,56
R 36,50176
mav 20,228,56
mav 24,232,56
mav 28,236,56
mav 16,240,60
R 40,51200
mav 20,244,60
s_waitcnt vmcnt(2)
buffer_store_dwordx2 v[72:73], v81, s[36:39], s52 offen sc1
mav 24,248,60
mav 28,252,60
s_waitcnt lgkmcnt(1)
mvv 16,128,32
R 44,52224
mvv 20,132,32
mvv 24,136,32
mvv 28,140,32
mvv 16,144,36
R 48,53248
mvv 20,148,36
mvv 24,152,36
mvv 28,156,36
s_waitcnt lgkmcnt(1)
mvv 16,160,40
R 52,54272
mvv 20,164,40
mvv 24,168,40
mvv 28,172,40
mvv 16,176,44
R 56,55296
mvv 20,180,44
mvv 24,184,44
mvv 28,188,44
s_waitcnt lgkmcnt(1)
mvv 16,192,48
R 60,56320
mvv 20,196,48
v_xor_b32 v80, 0x10000, v80
mvv 24,200,48
s_add_u32 s49, s49, 346112
mvv 28,204,48
s_waitcnt vmcnt(1)
s_barrier
mvv 16,208,52
R 32,0
s_add_u32 m0, s50, 32768
mvv 20,212,52
D 83,0
mvv 24,216,52
mvv 28,220,52
s_waitcnt lgkmcnt(1)
mvv 16,224,56
R 36,1024
mvv 20,228,56
D 83,1024
mvv 24,232,56
mvv 28,236,56
mvv 16,240,60
R 40,2048
mvv 20,244,60
mvv 24,248,60
D 83,2048
mvv 28,252,60
s_add_u32 s60, s60, 1
s_cbranch_scc0 LG1_1
s_waitcnt lgkmcnt(1)
v_mfma_f32_16x16x32_bf16 v[0:3], a[0:3], v[32:35], 0
R 44,3072
v_cvt_pk_bf16_f32 v64, v16, v17
v_mfma_f32_16x16x32_bf16 v[4:7], a[4:7], v[32:35], 0
v_cvt_pk_bf16_f32 v65, v18, v19
v_cvt_pk_bf16_f32 v66, v20, v21
v_mfma_f32_16x16x32_bf16 v[8:11], a[8:11], v[32:35], 0
D 83,3072
v_cvt_pk_bf16_f32 v67, v22, v23
v_mfma_f32_16x16x32_bf16 v[12:15], a[12:15], v[32:35], 0
v_cvt_pk_bf16_f32 v68, v24, v25
v_cvt_pk_bf16_f32 v69, v26, v27
mav 0,16,36
R 48,4096
v_cvt_pk_bf16_f32 v70, v28, v29
mav 4,20,36
s_add_u32 m0, s50, 36864
v_cvt_pk_bf16_f32 v71, v30, v31
mav 8,24,36
buffer_store_dwordx4 v[64:67], v87, s[36:39], s49 offen sc1
mav 12,28,36
s_waitcnt lgkmcnt(1)
mav 0,32,40
R 52,5120
mav 4,36,40
mav 8,40,40
buffer_store_dwordx4 v[68:71], v87, s[36:39], s49 offen offset:1024 sc1
mav 12,44,40
mav 0,48,44
R 56,6144
mav 4,52,44
mav 8,56,44
D 84,0
mav 12,60,44
s_waitcnt lgkmcnt(1)
mav 0,64,48
R 60,7168
mav 4,68,48
mav 8,72,48
D 84,1024
mav 12,76,48
mav 0,80,52
R 32,8192
mav 4,84,52
mav 8,88,52
mav 12,92,52
s_waitcnt lgkmcnt(1)
mav 0,96,56
R 36,9216
mav 4,100,56
mav 8,104,56
mav 12,108,56
mav 0,112,60
R 40,10240
mav 4,116,60
mav 8,120,60
mav 12,124,60
s_waitcnt lgkmcnt(1)
mav 0,128,32
R 44,11264
mav 4,132,32
mav 8,136,32
mav 12,140,32
mav 0,144,36
R 48,12288
mav 4,148,36
mav 8,152,36
mav 12,156,36
s_waitcnt lgkmcnt(1)
mav 0,160,40
R 52,13312
mav 4,164,40
mav 8,168,40
mav 12,172,40
mav 0,176,44
R 56,14336
mav 4,180,44
mav 8,184,44
mav 12,188,44
s_waitcnt lgkmcnt(1)
mav 0,192,48
R 60,15360
mav 4,196,48
mav 8,200,48
mav 12,204,48
mav 0,208,52
R 32,16384
mav 4,212,52
mav 8,216,52
v_add_u32 v72, 1, v72
mav 12,220,52
s_waitcnt lgkmcnt(1)
mav 0,224,56
R 36,17408
mav 4,228,56
mav 8,232,56
mav 12,236,56
mav 0,240,60
R 40,18432
mav 4,244,60
s_waitcnt vmcnt(2)
buffer_store_dwordx2 v[72:73], v81, s[36:39], s52 offen sc1
mav 8,248,60
mav 12,252,60
s_waitcnt lgkmcnt(1)
mvv 0,128,32
R 44,19456
mvv 4,132,32
mvv 8,136,32
mvv 12,140,32
mvv 0,144,36
R 48,20480
mvv 4,148,36
mvv 8,152,36
mvv 12,156,36
s_waitcnt lgkmcnt(1)
mvv 0,160,40
R 52,21504
mvv 4,164,40
mvv 8,168,40
mvv 12,172,40
mvv 0,176,44
R 56,22528
mvv 4,180,44
mvv 8,184,44
mvv 12,188,44
s_waitcnt lgkmcnt(1)
mvv 0,192,48
R 60,23552
mvv 4,196,48
mvv 8,200,48
s_add_u32 s49, s49, 346112
mvv 12,204,48
s_waitcnt vmcnt(1)
s_barrier
mvv 0,208,52
R 32,32768
mvv 4,212,52
mvv 8,216,52
mvv 12,220,52
s_waitcnt lgkmcnt(1)
mvv 0,224,56
R 36,33792
mvv 4,228,56
mvv 8,232,56
mvv 12,236,56
mvv 0,240,60
)"
R"(
R 40,34816
mvv 4,244,60
mvv 8,248,60
mvv 12,252,60
s_waitcnt lgkmcnt(1)
v_mfma_f32_16x16x32_bf16 v[16:19], a[0:3], v[32:35], 0
R 44,35840
v_mfma_f32_16x16x32_bf16 v[20:23], a[4:7], v[32:35], 0
v_cvt_pk_bf16_f32 v64, v0, v1
v_cvt_pk_bf16_f32 v65, v2, v3
v_mfma_f32_16x16x32_bf16 v[24:27], a[8:11], v[32:35], 0
v_cvt_pk_bf16_f32 v66, v4, v5
v_cvt_pk_bf16_f32 v67, v6, v7
v_mfma_f32_16x16x32_bf16 v[28:31], a[12:15], v[32:35], 0
buffer_store_dwordx4 v[64:67], v87, s[36:39], s49 offen sc1
v_cvt_pk_bf16_f32 v68, v8, v9
mav 16,16,36
R 48,36864
v_cvt_pk_bf16_f32 v69, v10, v11
mav 20,20,36
v_cvt_pk_bf16_f32 v70, v12, v13
v_cvt_pk_bf16_f32 v71, v14, v15
mav 24,24,36
mav 28,28,36
buffer_store_dwordx4 v[68:71], v87, s[36:39], s49 offen offset:1024 sc1
s_waitcnt lgkmcnt(1)
mav 16,32,40
R 52,37888
mav 20,36,40
mav 24,40,40
mav 28,44,40
mav 16,48,44
R 56,38912
mav 20,52,44
mav 24,56,44
mav 28,60,44
s_waitcnt lgkmcnt(1)
mav 16,64,48
R 60,39936
mav 20,68,48
mav 24,72,48
mav 28,76,48
mav 16,80,52
R 32,40960
mav 20,84,52
mav 24,88,52
mav 28,92,52
s_waitcnt lgkmcnt(1)
mav 16,96,56
R 36,41984
mav 20,100,56
mav 24,104,56
mav 28,108,56
mav 16,112,60
R 40,43008
mav 20,116,60
mav 24,120,60
mav 28,124,60
s_waitcnt lgkmcnt(1)
mav 16,128,32
R 44,44032
mav 20,132,32
mav 24,136,32
mav 28,140,32
mav 16,144,36
R 48,45056
mav 20,148,36
mav 24,152,36
mav 28,156,36
s_waitcnt lgkmcnt(1)
mav 16,160,40
R 52,46080
mav 20,164,40
mav 24,168,40
mav 28,172,40
mav 16,176,44
R 56,47104
mav 20,180,44
mav 24,184,44
mav 28,188,44
s_waitcnt lgkmcnt(1)
mav 16,192,48
R 60,48128
mav 20,196,48
mav 24,200,48
mav 28,204,48
mav 16,208,52
R 32,49152
mav 20,212,52
mav 24,216,52
v_add_u32 v72, 1, v72
mav 28,220,52
s_waitcnt lgkmcnt(1)
mav 16,224,56
R 36,50176
mav 20,228,56
mav 24,232,56
mav 28,236,56
mav 16,240,60
R 40,51200
mav 20,244,60
s_waitcnt vmcnt(0)
buffer_store_dwordx2 v[72:73], v81, s[36:39], s52 offen sc1
mav 24,248,60
mav 28,252,60
s_waitcnt lgkmcnt(1)
mvv 16,128,32
R 44,52224
mvv 20,132,32
mvv 24,136,32
mvv 28,140,32
mvv 16,144,36
R 48,53248
mvv 20,148,36
mvv 24,152,36
mvv 28,156,36
s_waitcnt lgkmcnt(1)
mvv 16,160,40
R 52,54272
mvv 20,164,40
mvv 24,168,40
mvv 28,172,40
mvv 16,176,44
R 56,55296
mvv 20,180,44
mvv 24,184,44
mvv 28,188,44
s_waitcnt lgkmcnt(1)
mvv 16,192,48
R 60,56320
mvv 20,196,48
mvv 24,200,48
s_add_u32 s49, s49, 346112
mvv 28,204,48
mvv 16,208,52
mvv 20,212,52
mvv 24,216,52
mvv 28,220,52
s_waitcnt lgkmcnt(0)
mvv 16,224,56
mvv 20,228,56
mvv 24,232,56
mvv 28,236,56
mvv 16,240,60
mvv 20,244,60
mvv 24,248,60
mvv 28,252,60
s_nop 4
v_cvt_pk_bf16_f32 v64, v16, v17
v_cvt_pk_bf16_f32 v65, v18, v19
v_cvt_pk_bf16_f32 v66, v20, v21
v_cvt_pk_bf16_f32 v67, v22, v23
buffer_store_dwordx4 v[64:67], v87, s[36:39], s49 offen sc1
v_cvt_pk_bf16_f32 v68, v24, v25
v_cvt_pk_bf16_f32 v69, v26, v27
v_cvt_pk_bf16_f32 v70, v28, v29
v_cvt_pk_bf16_f32 v71, v30, v31
buffer_store_dwordx4 v[68:71], v87, s[36:39], s49 offen offset:1024 sc1
v_add_u32 v72, 37, v72
s_waitcnt vmcnt(0)
buffer_store_dwordx2 v[72:73], v81, s[36:39], s52 offen sc1
s_endpgm
LeG:
.purgem R
Lr5:
.macro R d,o
ds_read_b128 v[\d:\d+3], v80 offset:\o
.endm
buffer_load_dword v73, off, s[36:39], 0 sc1
s_getpc_b64 s[66:67]
LpR:
v_lshlrev_b32 v8, 3, v80
s_lshl_b32 s60, s53, 3
v_add_u32 v8, s60, v8
.set cn, 0
.rept 2
v_add_u32 v9, cn, v8
v_min_u32 v9, LeR - LpR - 4, v9
global_load_dword v10, v9, s[66:67]
.set cn, cn + 32768
.endr
E 0,0
E 1,4096
E 2,8192
E 3,12288
s_add_u32 s51, s51, 65536
E 0,16384
E 1,20480
E 2,24576
E 3,28672
s_add_u32 s51, s51, 65536
E 0,32768
E 1,36864
E 2,40960
E 3,45056
s_add_u32 s51, s51, 65536
E 0,49152
E 1,53248
E 2,57344
E 3,61440
s_add_u32 s51, s51, 65536
s_cmp_ge_u32 s51, s57
s_cselect_b32 s60, 1048576, 0
s_sub_u32 s51, s51, s60
E 0,65536
E 1,69632
E 2,73728
E 3,77824
s_add_u32 s51, s51, 65536
E 0,81920
E 1,86016
E 2,90112
E 3,94208
s_add_u32 s51, s51, 65536
E 0,98304
E 1,102400
E 2,106496
E 3,110592
s_add_u32 s51, s51, 65536
E 0,114688
E 1,118784
E 2,122880
E 3,126976
s_add_u32 s51, s51, 65536
s_cmp_ge_u32 s51, s57
s_cselect_b32 s60, 1048576, 0
s_sub_u32 s51, s51, s60
E 0,131072
E 1,135168
E 2,139264
E 3,143360
s_add_u32 s51, s51, 65536
E 0,147456
E 1,151552
E 2,155648
E 3,159744
s_add_u32 s51, s51, 65536
s_waitcnt vmcnt(32)
s_barrier
v_mov_b32 v72, 0x100
v_add_u32 v73, 0x9e3779b9, v73
buffer_store_dwordx2 v[72:73], v81, s[36:39], s52 offen sc1
v_add_u32 v85, v85, v72
Ta 0,4,0
Ta 4,5,0
Ta 8,6,0
Ta 12,7,0
Ta 16,4,16384
Ta 20,5,16384
Ta 24,6,16384
Ta 28,7,16384
s_waitcnt lgkmcnt(0)
s_waitcnt vmcnt(25)
s_barrier
Ta 32,4,32768
Ta 36,5,32768
Ta 40,6,32768
Ta 44,7,32768
Ta 48,4,49152
Ta 52,5,49152
Ta 56,6,49152
Ta 60,7,49152
E 0,0
E 1,4096
E 2,8192
E 3,12288
s_add_u32 s51, s51, 65536
E 0,16384
E 1,20480
E 2,24576
E 3,28672
s_add_u32 s51, s51, 65536
s_cmp_ge_u32 s51, s57
s_cselect_b32 s60, 1048576, 0
s_sub_u32 s51, s51, s60
s_waitcnt lgkmcnt(0)
s_waitcnt vmcnt(25)
s_barrier
v_add_u32 v4, 65536, v4
v_add_u32 v5, 65536, v5
v_add_u32 v6, 65536, v6
v_add_u32 v7, 65536, v7
Ta 64,4,0
Ta 68,5,0
Ta 72,6,0
Ta 76,7,0
Ta 80,4,16384
Ta 84,5,16384
Ta 88,6,16384
Ta 92,7,16384
E 0,32768
E 1,36864
E 2,40960
E 3,45056
s_add_u32 s51, s51, 65536
E 0,49152
E 1,53248
E 2,57344
E 3,61440
s_add_u32 s51, s51, 65536
s_waitcnt lgkmcnt(0)
s_waitcnt vmcnt(25)
s_barrier
Ta 96,4,32768
Ta 100,5,32768
Ta 104,6,32768
Ta 108,7,32768
Ta 112,4,49152
Ta 116,5,49152
Ta 120,6,49152
Ta 124,7,49152
E 0,65536
E 1,69632
E 2,73728
E 3,77824
s_add_u32 s51, s51, 65536
E 0,81920
E 1,86016
E 2,90112
E 3,94208
s_add_u32 s51, s51, 65536
s_cmp_ge_u32 s51, s57
s_cselect_b32 s60, 1048576, 0
s_sub_u32 s51, s51, s60
s_waitcnt lgkmcnt(0)
s_waitcnt vmcnt(25)
s_barrier
v_add_u32 v4, 65536, v4
v_add_u32 v5, 65536, v5
v_add_u32 v6, 65536, v6
v_add_u32 v7, 65536, v7
Ta 128,4,0
Ta 132,5,0
Ta 136,6,0
Ta 140,7,0
Ta 144,4,16384
Ta 148,5,16384
Ta 152,6,16384
Ta 156,7,16384
s_waitcnt lgkmcnt(0)
s_waitcnt vmcnt(16)
s_barrier
v_add_u32 v4, -131072, v4
v_add_u32 v5, -131072, v5
v_add_u32 v6, -131072, v6
v_add_u32 v7, -131072, v7
Ta 160,4,0
Ta 164,5,0
Ta 168,6,0
Ta 172,7,0
Ta 176,4,16384
Ta 180,5,16384
Ta 184,6,16384
Ta 188,7,16384
s_waitcnt lgkmcnt(0)
s_waitcnt vmcnt(8)
s_barrier
Ta 192,4,32768
Ta 196,5,32768
Ta 200,6,32768
Ta 204,7,32768
Ta 208,4,49152
Ta 212,5,49152
Ta 216,6,49152
Ta 220,7,49152
s_waitcnt lgkmcnt(0)
s_waitcnt vmcnt(0)
s_barrier
v_add_u32 v4, 65536, v4
v_add_u32 v5, 65536, v5
v_add_u32 v6, 65536, v6
v_add_u32 v7, 65536, v7
Ta 224,4,0
Ta 228,5,0
Ta 232,6,0
Ta 236,7,0
Ta 240,4,16384
Ta 244,5,16384
Ta 248,6,16384
Ta 252,7,16384
s_waitcnt lgkmcnt(0)
s_waitcnt vmcnt(0)
s_barrier
s_nop 7
s_and_b32 s60, s48, 3
s_cmp_eq_u32 s60, 1
s_cbranch_scc1 LvR1
s_cmp_eq_u32 s60, 2
s_cbranch_scc1 LvR2
LvR0:
buffer_load_dwordx2 v[76:77], v84, s[36:39], 0 offen sc1
s_waitcnt vmcnt(0)
ds_write_b32 v82, v76
v_sub_u32 v76, v76, v85
v_xor_b32 v77, v77, v73
v_cmp_lt_u64 vcc, s[62:63], v[76:77]
s_waitcnt lgkmcnt(0)
s_cbranch_vccnz CR0_1
BR0_1:
s_add_u32 s49, s49, 346112
s_barrier
s_add_u32 m0, s50, 0
s_nop 0
D 83,0
D 83,1024
D 83,2048
D 83,3072
s_mov_b32 m0, 142336
v_add_u32 v85, 1, v85
buffer_load_dword v84, s[36:39], 0 offen sc1 lds
buffer_load_dword v84, s[36:39], 0 offen sc1 lds
ds_read_b32 v76, v82 offset:0
s_waitcnt lgkmcnt(0)
v_cmp_gt_i32 vcc, v85, v76
s_cbranch_vccnz CR0_2
BR0_2:
s_add_u32 s49, s49, 346112
s_barrier
s_add_u32 m0, s50, 32768
s_nop 0
D 83,0
D 83,1024
D 83,2048
D 83,3072
s_mov_b32 m0, 142336
v_add_u32 v85, 1, v85
buffer_load_dword v84, s[36:39], 0 offen sc1 lds
buffer_load_dwordx4 v[124:127], v90, s[36:39], s49 offen sc1
buffer_load_dwordx4 v[128:131], v90, s[36:39], s49 offen offset:1024 sc1
buffer_load_dword v84, s[36:39], 0 offen sc1 lds
ds_read_b32 v76, v82 offset:0
s_xor_b32 s50, s50, 0x10000
s_waitcnt lgkmcnt(0)
v_cmp_gt_i32 vcc, v85, v76
s_waitcnt vmcnt(10)
s_cbranch_vccnz CR0_3
BR0_3:
s_add_u32 s49, s49, 346112
s_barrier
R 32,0
buffer_load_dwordx4 v[132:135], v86, s[36:39], s49 offen sc1
R 36,1024
buffer_load_dwordx4 v[136:139], v86, s[36:39], s49 offen offset:1024 sc1
R 40,2048
buffer_load_dwordx4 v[140:143], v87, s[36:39], s49 offen sc1
s_waitcnt lgkmcnt(1)
v_mfma_f32_16x16x32_bf16 v[0:3], a[0:3], v[32:35], v[100:103]
R 44,3072
v_mfma_f32_16x16x32_bf16 v[4:7], a[4:7], v[32:35], v[104:107]
v_mfma_f32_16x16x32_bf16 v[8:11], a[8:11], v[32:35], v[108:111]
buffer_load_dwordx4 v[144:147], v87, s[36:39], s49 offen offset:1024 sc1
v_mfma_f32_16x16x32_bf16 v[12:15], a[12:15], v[32:35], v[112:115]
mav 0,16,36
R 48,4096
mav 4,20,36
mav 8,24,36
mav 12,28,36
buffer_load_dwordx4 v[148:151], v88, s[36:39], s49 offen sc1
s_waitcnt lgkmcnt(1)
mav 0,32,40
R 52,5120
mav 4,36,40
mav 8,40,40
mav 12,44,40
buffer_load_dwordx4 v[152:155], v88, s[36:39], s49 offen offset:1024 sc1
mav 0,48,44
R 56,6144
mav 4,52,44
mav 8,56,44
mav 12,60,44
s_waitcnt lgkmcnt(1)
mav 0,64,48
R 60,7168
buffer_load_dwordx4 v[156:159], v89, s[36:39], s49 offen sc1
mav 4,68,48
mav 8,72,48
mav 12,76,48
mav 0,80,52
R 32,8192
buffer_load_dwordx4 v[160:163], v89, s[36:39], s49 offen offset:1024 sc1
mav 4,84,52
mav 8,88,52
mav 12,92,52
s_add_u32 m0, s50, 0
s_waitcnt lgkmcnt(1)
mav 0,96,56
R 36,9216
mav 4,100,56
D 83,0
mav 8,104,56
mav 12,108,56
mav 0,112,60
R 40,10240
mav 4,116,60
D 83,1024
mav 8,120,60
mav 12,124,60
s_waitcnt lgkmcnt(1)
mav 0,128,32
R 44,11264
mav 4,132,32
mav 8,136,32
D 83,2048
mav 12,140,32
mav 0,144,36
R 48,12288
mav 4,148,36
mav 8,152,36
D 83,3072
mav 12,156,36
s_waitcnt lgkmcnt(1)
mav 0,160,40
R 52,13312
s_mov_b32 m0, 142336
mav 4,164,40
v_add_u32 v85, 1, v85
mav 8,168,40
buffer_load_dword v84, s[36:39], 0 offen sc1 lds
mav 12,172,40
buffer_load_dwordx4 v[164:167], v90, s[36:39], s49 offen sc1
mav 0,176,44
R 56,14336
mav 4,180,44
mav 8,184,44
mav 12,188,44
buffer_load_dwordx4 v[168:171], v90, s[36:39], s49 offen offset:1024 sc1
s_waitcnt lgkmcnt(1)
mav 0,192,48
R 60,15360
mav 4,196,48
mav 8,200,48
mav 12,204,48
mav 0,208,52
mav 4,212,52
mav 8,216,52
buffer_load_dword v84, s[36:39], 0 offen sc1 lds
mav 12,220,52
s_waitcnt lgkmcnt(0)
mav 0,224,56
mav 4,228,56
mav 8,232,56
mav 12,236,56
mav 0,240,60
mav 4,244,60
ds_read_b32 v76, v82 offset:0
mav 8,248,60
mav 12,252,60
s_waitcnt vmcnt(18)
mvv 0,116,124
mvv 4,120,124
s_waitcnt vmcnt(17)
mvv 8,116,128
mvv 12,120,128
s_waitcnt vmcnt(15)
mvv 0,116,132
mvv 4,120,132
s_waitcnt lgkmcnt(0)
v_cmp_gt_i32 vcc, v85, v76
s_waitcnt vmcnt(14)
mvv 8,116,136
s_cbranch_vccnz CR0_4
BR0_4:
s_add_u32 s49, s49, 346112
mvv 12,120,136
s_barrier
s_waitcnt vmcnt(13)
mvv 0,116,140
R 32,32768
mvv 4,120,140
buffer_load_dwordx4 v[172:175], v86, s[36:39], s49 offen sc1
s_waitcnt vmcnt(13)
mvv 8,116,144
mvv 12,120,144
s_waitcnt vmcnt(12)
mvv 0,116,148
R 36,33792
mvv 4,120,148
buffer_load_dwordx4 v[176:179], v86, s[36:39], s49 offen offset:1024 sc1
s_waitcnt vmcnt(12)
mvv 8,116,152
mvv 12,120,152
s_waitcnt vmcnt(11)
mvv 0,116,156
R 40,34816
mvv 4,120,156
s_waitcnt vmcnt(10)
mvv 8,116,160
buffer_load_dwordx4 v[180:183], v87, s[36:39], s49 offen sc1
mvv 12,120,160
s_waitcnt lgkmcnt(1)
v_mfma_f32_16x16x32_bf16 v[16:19], a[0:3], v[32:35], v[100:103]
R 44,35840
v_cvt_pk_bf16_f32 v64, v0, v1
v_mfma_f32_16x16x32_bf16 v[20:23], a[4:7], v[32:35], v[104:107]
v_cvt_pk_bf16_f32 v65, v2, v3
v_cvt_pk_bf16_f32 v66, v4, v5
v_mfma_f32_16x16x32_bf16 v[24:27], a[8:11], v[32:35], v[108:111]
buffer_load_dwordx4 v[184:187], v87, s[36:39], s49 offen offset:1024 sc1
v_cvt_pk_bf16_f32 v67, v6, v7
v_mfma_f32_16x16x32_bf16 v[28:31], a[12:15], v[32:35], v[112:115]
v_cvt_pk_bf16_f32 v68, v8, v9
v_cvt_pk_bf16_f32 v69, v10, v11
mav 16,16,36
R 48,36864
v_cvt_pk_bf16_f32 v70, v12, v13
mav 20,20,36
v_cvt_pk_bf16_f32 v71, v14, v15
v_mul_f32 v75, v0, v0
mav 24,24,36
buffer_store_dwordx4 v[64:67], v91, s[36:39], s49 offen sc1
v_add_f32 v74, v1, v0
mav 28,28,36
v_fmac_f32 v75, v1, v1
s_waitcnt lgkmcnt(1)
mav 16,32,40
R 52,37888
v_add_f32 v74, v2, v74
mav 20,36,40
v_fmac_f32 v75, v2, v2
v_add_f32 v74, v3, v74
mav 24,40,40
buffer_store_dwordx4 v[68:71], v91, s[36:39], s49 offen offset:1024 sc1
v_fmac_f32 v75, v3, v3
mav 28,44,40
v_add_f32 v74, v4, v74
v_fmac_f32 v75, v4, v4
mav 16,48,44
R 56,38912
v_add_f32 v74, v5, v74
mav 20,52,44
v_fmac_f32 v75, v5, v5
v_add_f32 v74, v6, v74
mav 24,56,44
buffer_load_dwordx4 v[188:191], v88, s[36:39], s49 offen sc1
v_fmac_f32 v75, v6, v6
mav 28,60,44
v_add_f32 v74, v7, v74
s_waitcnt lgkmcnt(1)
mav 16,64,48
R 60,39936
v_fmac_f32 v75, v7, v7
mav 20,68,48
v_add_f32 v74, v8, v74
v_fmac_f32 v75, v8, v8
mav 24,72,48
buffer_load_dwordx4 v[192:195], v88, s[36:39], s49 offen offset:1024 sc1
v_add_f32 v74, v9, v74
mav 28,76,48
v_fmac_f32 v75, v9, v9
v_add_f32 v74, v10, v74
mav 16,80,52
R 32,40960
v_fmac_f32 v75, v10, v10
mav 20,84,52
v_add_f32 v74, v11, v74
v_fmac_f32 v75, v11, v11
mav 24,88,52
buffer_load_dwordx4 v[196:199], v89, s[36:39], s49 offen sc1
v_add_f32 v74, v12, v74
mav 28,92,52
s_add_u32 m0, s50, 32768
s_waitcnt lgkmcnt(1)
mav 16,96,56
R 36,41984
v_fmac_f32 v75, v12, v12
mav 20,100,56
v_add_f32 v74, v13, v74
v_fmac_f32 v75, v13, v13
mav 24,104,56
buffer_load_dwordx4 v[200:203], v89, s[36:39], s49 offen offset:1024 sc1
v_add_f32 v74, v14, v74
mav 28,108,56
v_fmac_f32 v75, v14, v14
v_add_f32 v74, v15, v74
mav 16,112,60
R 40,43008
v_fmac_f32 v75, v15, v15
mav 20,116,60
mav 24,120,60
D 83,0
v_permlane32_swap_b32 v74, v75
mav 28,124,60
v_add_f32 v74, v74, v75
s_waitcnt lgkmcnt(1)
mav 16,128,32
R 44,44032
ds_write_b32 v97, v74 offset:0
mav 20,132,32
mav 24,136,32
D 83,1024
mav 28,140,32
mav 16,144,36
R 48,45056
mav 20,148,36
mav 24,152,36
D 83,2048
mav 28,156,36
s_waitcnt lgkmcnt(1)
mav 16,160,40
R 52,46080
v_add_u32 v85, 1, v85
mav 20,164,40
mav 24,168,40
D 83,3072
s_mov_b32 m0, 142336
mav 28,172,40
buffer_load_dword v84, s[36:39], 0 offen sc1 lds
mav 16,176,44
R 56,47104
mav 20,180,44
mav 24,184,44
buffer_load_dwordx4 v[124:127], v90, s[36:39], s49 offen sc1
mav 28,188,44
s_waitcnt lgkmcnt(1)
mav 16,192,48
R 60,48128
mav 20,196,48
mav 24,200,48
buffer_load_dwordx4 v[128:131], v90, s[36:39], s49 offen offset:1024 sc1
mav 28,204,48
mav 16,208,52
mav 20,212,52
mav 24,216,52
buffer_load_dword v84, s[36:39], 0 offen sc1 lds
v_add_u32 v72, 1, v72
mav 28,220,52
s_waitcnt lgkmcnt(0)
mav 16,224,56
mav 20,228,56
mav 24,232,56
mav 28,236,56
mav 16,240,60
s_waitcnt vmcnt(12)
buffer_store_dwordx2 v[72:73], v81, s[36:39], s52 offen sc1
mav 20,244,60
ds_read_b32 v76, v82 offset:0
mav 24,248,60
mav 28,252,60
mvv 16,116,164
mvv 20,120,164
mvv 24,116,168
s_xor_b32 s50, s50, 0x10000
mvv 28,120,168
mvv 16,116,172
mvv 20,120,172
v_xor_b32 v80, 0x10000, v80
mvv 24,116,176
s_waitcnt lgkmcnt(0)
v_cmp_gt_i32 vcc, v85, v76
mvv 28,120,176
s_cbranch_vccnz CR0_5
BR0_5:
s_barrier
mvv 16,116,180
R 32,0
s_add_u32 s49, s49, 346112
mvv 20,120,180
buffer_load_dwordx4 v[132:135], v86, s[36:39], s49 offen sc1
mvv 24,116,184
mvv 28,120,184
s_waitcnt vmcnt(13)
mvv 16,116,188
R 36,1024
mvv 20,120,188
buffer_load_dwordx4 v[136:139], v86, s[36:39], s49 offen offset:1024 sc1
s_waitcnt vmcnt(13)
mvv 24,116,192
mvv 28,120,192
s_waitcnt vmcnt(12)
mvv 16,116,196
R 40,2048
mvv 20,120,196
s_waitcnt vmcnt(11)
mvv 24,116,200
buffer_load_dwordx4 v[140:143], v87, s[36:39], s49 offen sc1
mvv 28,120,200
s_mov_b32 s60, -15
.p2align 6
LR0_6:
s_waitcnt lgkmcnt(1)
v_mfma_f32_16x16x32_bf16 v[0:3], a[0:3], v[32:35], v[100:103]
R 44,3072
v_cvt_pk_bf16_f32 v64, v16, v17
v_mfma_f32_16x16x32_bf16 v[4:7], a[4:7], v[32:35], v[104:107]
v_cvt_pk_bf16_f32 v65, v18, v19
v_cvt_pk_bf16_f32 v66, v20, v21
v_mfma_f32_16x16x32_bf16 v[8:11], a[8:11], v[32:35], v[108:111]
buffer_load_dwordx4 v[144:147], v87, s[36:39], s49 offen offset:1024 sc1
v_cvt_pk_bf16_f32 v67, v22, v23
v_mfma_f32_16x16x32_bf16 v[12:15], a[12:15], v[32:35], v[112:115]
v_cvt_pk_bf16_f32 v68, v24, v25
v_cvt_pk_bf16_f32 v69, v26, v27
mav 0,16,36
R 48,4096
v_cvt_pk_bf16_f32 v70, v28, v29
mav 4,20,36
v_cvt_pk_bf16_f32 v71, v30, v31
v_mul_f32 v75, v16, v16
mav 8,24,36
buffer_store_dwordx4 v[64:67], v91, s[36:39], s49 offen sc1
v_add_f32 v74, v17, v16
mav 12,28,36
v_fmac_f32 v75, v17, v17
s_waitcnt lgkmcnt(1)
mav 0,32,40
R 52,5120
v_add_f32 v74, v18, v74
mav 4,36,40
v_fmac_f32 v75, v18, v18
v_add_f32 v74, v19, v74
mav 8,40,40
buffer_store_dwordx4 v[68:71], v91, s[36:39], s49 offen offset:1024 sc1
v_fmac_f32 v75, v19, v19
mav 12,44,40
v_add_f32 v74, v20, v74
v_fmac_f32 v75, v20, v20
mav 0,48,44
R 56,6144
v_add_f32 v74, v21, v74
mav 4,52,44
v_fmac_f32 v75, v21, v21
v_add_f32 v74, v22, v74
mav 8,56,44
buffer_load_dwordx4 v[148:151], v88, s[36:39], s49 offen sc1
v_fmac_f32 v75, v22, v22
mav 12,60,44
v_add_f32 v74, v23, v74
s_waitcnt lgkmcnt(1)
mav 0,64,48
R 60,7168
v_fmac_f32 v75, v23, v23
mav 4,68,48
v_add_f32 v74, v24, v74
v_fmac_f32 v75, v24, v24
mav 8,72,48
buffer_load_dwordx4 v[152:155], v88, s[36:39], s49 offen offset:1024 sc1
v_add_f32 v74, v25, v74
mav 12,76,48
v_fmac_f32 v75, v25, v25
v_add_f32 v74, v26, v74
mav 0,80,52
R 32,8192
v_fmac_f32 v75, v26, v26
mav 4,84,52
v_add_f32 v74, v27, v74
v_fmac_f32 v75, v27, v27
mav 8,88,52
buffer_load_dwordx4 v[156:159], v89, s[36:39], s49 offen sc1
v_add_f32 v74, v28, v74
mav 12,92,52
s_add_u32 m0, s50, 0
s_waitcnt lgkmcnt(1)
mav 0,96,56
R 36,9216
v_fmac_f32 v75, v28, v28
mav 4,100,56
v_add_f32 v74, v29, v74
v_fmac_f32 v75, v29, v29
mav 8,104,56
buffer_load_dwordx4 v[160:163], v89, s[36:39], s49 offen offset:1024 sc1
v_add_f32 v74, v30, v74
mav 12,108,56
v_fmac_f32 v75, v30, v30
v_add_f32 v74, v31, v74
mav 0,112,60
R 40,10240
v_fmac_f32 v75, v31, v31
mav 4,116,60
mav 8,120,60
D 83,0
v_permlane32_swap_b32 v74, v75
mav 12,124,60
v_add_f32 v74, v74, v75
s_waitcnt lgkmcnt(1)
mav 0,128,32
R 44,11264
ds_write_b32 v97, v74 offset:1024
mav 4,132,32
mav 8,136,32
D 83,1024
mav 12,140,32
mav 0,144,36
R 48,12288
mav 4,148,36
mav 8,152,36
D 83,2048
mav 12,156,36
s_waitcnt lgkmcnt(1)
mav 0,160,40
R 52,13312
v_add_u32 v85, 1, v85
mav 4,164,40
mav 8,168,40
D 83,3072
s_mov_b32 m0, 142336
mav 12,172,40
buffer_load_dword v84, s[36:39], 0 offen sc1 lds
mav 0,176,44
R 56,14336
mav 4,180,44
mav 8,184,44
buffer_load_dwordx4 v[164:167], v90, s[36:39], s49 offen sc1
mav 12,188,44
s_waitcnt lgkmcnt(1)
mav 0,192,48
R 60,15360
mav 4,196,48
mav 8,200,48
buffer_load_dwordx4 v[168:171], v90, s[36:39], s49 offen offset:1024 sc1
mav 12,204,48
mav 0,208,52
mav 4,212,52
mav 8,216,52
buffer_load_dword v84, s[36:39], 0 offen sc1 lds
v_add_u32 v72, 1, v72
mav 12,220,52
s_waitcnt lgkmcnt(0)
mav 0,224,56
mav 4,228,56
mav 8,232,56
mav 12,236,56
mav 0,240,60
s_waitcnt vmcnt(12)
buffer_store_dwordx2 v[72:73], v81, s[36:39], s52 offen sc1
mav 4,244,60
ds_read_b32 v76, v82 offset:0
mav 8,248,60
mav 12,252,60
mvv 0,116,124
mvv 4,120,124
mvv 8,116,128
mvv 12,120,128
mvv 0,116,132
mvv 4,120,132
s_waitcnt lgkmcnt(0)
v_cmp_gt_i32 vcc, v85, v76
mvv 8,116,136
s_cbranch_vccnz CR0_7
BR0_7:
s_add_u32 s49, s49, 346112
mvv 12,120,136
s_barrier
mvv 0,116,140
R 32,32768
mvv 4,120,140
buffer_load_dwordx4 v[172:175], v86, s[36:39], s49 offen sc1
mvv 8,116,144
mvv 12,120,144
s_waitcnt vmcnt(13)
mvv 0,116,148
R 36,33792
mvv 4,120,148
buffer_load_dwordx4 v[176:179], v86, s[36:39], s49 offen offset:1024 sc1
s_waitcnt vmcnt(13)
mvv 8,116,152
mvv 12,120,152
s_waitcnt vmcnt(12)
mvv 0,116,156
R 40,34816
mvv 4,120,156
s_waitcnt vmcnt(11)
mvv 8,116,160
buffer_load_dwordx4 v[180:183], v87, s[36:39], s49 offen sc1
mvv 12,120,160
s_waitcnt lgkmcnt(1)
v_mfma_f32_16x16x32_bf16 v[16:19], a[0:3], v[32:35], v[100:103]
R 44,35840
v_cvt_pk_bf16_f32 v64, v0, v1
v_mfma_f32_16x16x32_bf16 v[20:23], a[4:7], v[32:35], v[104:107]
v_cvt_pk_bf16_f32 v65, v2, v3
v_cvt_pk_bf16_f32 v66, v4, v5
v_mfma_f32_16x16x32_bf16 v[24:27], a[8:11], v[32:35], v[108:111]
buffer_load_dwordx4 v[184:187], v87, s[36:39], s49 offen offset:1024 sc1
v_cvt_pk_bf16_f32 v67, v6, v7
v_mfma_f32_16x16x32_bf16 v[28:31], a[12:15], v[32:35], v[112:115]
v_cvt_pk_bf16_f32 v68, v8, v9
v_cvt_pk_bf16_f32 v69, v10, v11
mav 16,16,36
R 48,36864
v_cvt_pk_bf16_f32 v70, v12, v13
mav 20,20,36
v_cvt_pk_bf16_f32 v71, v14, v15
v_mul_f32 v75, v0, v0
mav 24,24,36
buffer_store_dwordx4 v[64:67], v91, s[36:39], s49 offen sc1
v_add_f32 v74, v1, v0
mav 28,28,36
v_fmac_f32 v75, v1, v1
s_waitcnt lgkmcnt(1)
mav 16,32,40
R 52,37888
v_add_f32 v74, v2, v74
mav 20,36,40
v_fmac_f32 v75, v2, v2
v_add_f32 v74, v3, v74
mav 24,40,40
buffer_store_dwordx4 v[68:71], v91, s[36:39], s49 offen offset:1024 sc1
v_fmac_f32 v75, v3, v3
mav 28,44,40
v_add_f32 v74, v4, v74
v_fmac_f32 v75, v4, v4
mav 16,48,44
R 56,38912
v_add_f32 v74, v5, v74
mav 20,52,44
v_fmac_f32 v75, v5, v5
v_add_f32 v74, v6, v74
mav 24,56,44
buffer_load_dwordx4 v[188:191], v88, s[36:39], s49 offen sc1
v_fmac_f32 v75, v6, v6
mav 28,60,44
v_add_f32 v74, v7, v74
s_waitcnt lgkmcnt(1)
mav 16,64,48
R 60,39936
v_fmac_f32 v75, v7, v7
mav 20,68,48
v_add_f32 v74, v8, v74
v_fmac_f32 v75, v8, v8
mav 24,72,48
buffer_load_dwordx4 v[192:195], v88, s[36:39], s49 offen offset:1024 sc1
v_add_f32 v74, v9, v74
mav 28,76,48
v_fmac_f32 v75, v9, v9
v_add_f32 v74, v10, v74
mav 16,80,52
R 32,40960
v_fmac_f32 v75, v10, v10
mav 20,84,52
v_add_f32 v74, v11, v74
v_fmac_f32 v75, v11, v11
mav 24,88,52
buffer_load_dwordx4 v[196:199], v89, s[36:39], s49 offen sc1
v_add_f32 v74, v12, v74
mav 28,92,52
s_add_u32 m0, s50, 32768
s_waitcnt lgkmcnt(1)
mav 16,96,56
R 36,41984
v_fmac_f32 v75, v12, v12
mav 20,100,56
v_add_f32 v74, v13, v74
v_fmac_f32 v75, v13, v13
mav 24,104,56
buffer_load_dwordx4 v[200:203], v89, s[36:39], s49 offen offset:1024 sc1
v_add_f32 v74, v14, v74
mav 28,108,56
v_fmac_f32 v75, v14, v14
v_add_f32 v74, v15, v74
mav 16,112,60
R 40,43008
v_fmac_f32 v75, v15, v15
mav 20,116,60
mav 24,120,60
D 83,0
v_permlane32_swap_b32 v74, v75
mav 28,124,60
v_add_f32 v74, v74, v75
s_waitcnt lgkmcnt(1)
mav 16,128,32
R 44,44032
ds_write_b32 v97, v74 offset:0
mav 20,132,32
mav 24,136,32
D 83,1024
mav 28,140,32
mav 16,144,36
R 48,45056
mav 20,148,36
mav 24,152,36
D 83,2048
mav 28,156,36
s_waitcnt lgkmcnt(1)
mav 16,160,40
R 52,46080
v_add_u32 v85, 1, v85
mav 20,164,40
mav 24,168,40
D 83,3072
s_mov_b32 m0, 142336
mav 28,172,40
buffer_load_dword v84, s[36:39], 0 offen sc1 lds
mav 16,176,44
R 56,47104
mav 20,180,44
mav 24,184,44
buffer_load_dwordx4 v[124:127], v90, s[36:39], s49 offen sc1
mav 28,188,44
s_waitcnt lgkmcnt(1)
mav 16,192,48
R 60,48128
mav 20,196,48
mav 24,200,48
buffer_load_dwordx4 v[128:131], v90, s[36:39], s49 offen offset:1024 sc1
mav 28,204,48
mav 16,208,52
mav 20,212,52
mav 24,216,52
buffer_load_dword v84, s[36:39], 0 offen sc1 lds
v_add_u32 v72, 1, v72
mav 28,220,52
s_waitcnt lgkmcnt(0)
mav 16,224,56
mav 20,228,56
mav 24,232,56
mav 28,236,56
mav 16,240,60
s_waitcnt vmcnt(12)
buffer_store_dwordx2 v[72:73], v81, s[36:39], s52 offen sc1
mav 20,244,60
ds_read_b32 v76, v82 offset:0
mav 24,248,60
mav 28,252,60
mvv 16,116,164
mvv 20,120,164
mvv 24,116,168
s_xor_b32 s50, s50, 0x10000
mvv 28,120,168
mvv 16,116,172
mvv 20,120,172
v_xor_b32 v80, 0x10000, v80
mvv 24,116,176
s_waitcnt lgkmcnt(0)
v_cmp_gt_i32 vcc, v85, v76
mvv 28,120,176
s_cbranch_vccnz CR0_8
BR0_8:
s_barrier
mvv 16,116,180
R 32,0
s_add_u32 s49, s49, 346112
mvv 20,120,180
buffer_load_dwordx4 v[132:135], v86, s[36:39], s49 offen sc1
mvv 24,116,184
mvv 28,120,184
s_waitcnt vmcnt(13)
mvv 16,116,188
R 36,1024
mvv 20,120,188
buffer_load_dwordx4 v[136:139], v86, s[36:39], s49 offen offset:1024 sc1
s_waitcnt vmcnt(13)
mvv 24,116,192
mvv 28,120,192
s_waitcnt vmcnt(12)
mvv 16,116,196
R 40,2048
mvv 20,120,196
s_waitcnt vmcnt(11)
mvv 24,116,200
buffer_load_dwordx4 v[140:143], v87, s[36:39], s49 offen sc1
mvv 28,120,200
s_add_u32 s60, s60, 1
s_cbranch_scc0 LR0_6
s_waitcnt lgkmcnt(1)
v_mfma_f32_16x16x32_bf16 v[0:3], a[0:3], v[32:35], v[100:103]
R 44,3072
v_cvt_pk_bf16_f32 v64, v16, v17
v_mfma_f32_16x16x32_bf16 v[4:7], a[4:7], v[32:35], v[104:107]
v_cvt_pk_bf16_f32 v65, v18, v19
v_cvt_pk_bf16_f32 v66, v20, v21
v_mfma_f32_16x16x32_bf16 v[8:11], a[8:11], v[32:35], v[108:111]
buffer_load_dwordx4 v[144:147], v87, s[36:39], s49 offen offset:1024 sc1
v_cvt_pk_bf16_f32 v67, v22, v23
v_mfma_f32_16x16x32_bf16 v[12:15], a[12:15], v[32:35], v[112:115]
v_cvt_pk_bf16_f32 v68, v24, v25
v_cvt_pk_bf16_f32 v69, v26, v27
mav 0,16,36
R 48,4096
v_cvt_pk_bf16_f32 v70, v28, v29
mav 4,20,36
v_cvt_pk_bf16_f32 v71, v30, v31
v_mul_f32 v75, v16, v16
mav 8,24,36
buffer_store_dwordx4 v[64:67], v91, s[36:39], s49 offen sc1
v_add_f32 v74, v17, v16
mav 12,28,36
v_fmac_f32 v75, v17, v17
s_waitcnt lgkmcnt(1)
mav 0,32,40
R 52,5120
v_add_f32 v74, v18, v74
mav 4,36,40
v_fmac_f32 v75, v18, v18
v_add_f32 v74, v19, v74
mav 8,40,40
buffer_store_dwordx4 v[68:71], v91, s[36:39], s49 offen offset:1024 sc1
v_fmac_f32 v75, v19, v19
mav 12,44,40
v_add_f32 v74, v20, v74
v_fmac_f32 v75, v20, v20
mav 0,48,44
R 56,6144
v_add_f32 v74, v21, v74
mav 4,52,44
v_fmac_f32 v75, v21, v21
v_add_f32 v74, v22, v74
mav 8,56,44
buffer_load_dwordx4 v[148:151], v88, s[36:39], s49 offen sc1
v_fmac_f32 v75, v22, v22
mav 12,60,44
v_add_f32 v74, v23, v74
s_waitcnt lgkmcnt(1)
mav 0,64,48
R 60,7168
v_fmac_f32 v75, v23, v23
mav 4,68,48
v_add_f32 v74, v24, v74
v_fmac_f32 v75, v24, v24
mav 8,72,48
buffer_load_dwordx4 v[152:155], v88, s[36:39], s49 offen offset:1024 sc1
v_add_f32 v74, v25, v74
mav 12,76,48
v_fmac_f32 v75, v25, v25
v_add_f32 v74, v26, v74
mav 0,80,52
R 32,8192
v_fmac_f32 v75, v26, v26
mav 4,84,52
v_add_f32 v74, v27, v74
v_fmac_f32 v75, v27, v27
mav 8,88,52
buffer_load_dwordx4 v[156:159], v89, s[36:39], s49 offen sc1
v_add_f32 v74, v28, v74
mav 12,92,52
s_add_u32 m0, s50, 0
s_waitcnt lgkmcnt(1)
mav 0,96,56
R 36,9216
v_fmac_f32 v75, v28, v28
mav 4,100,56
v_add_f32 v74, v29, v74
v_fmac_f32 v75, v29, v29
mav 8,104,56
buffer_load_dwordx4 v[160:163], v89, s[36:39], s49 offen offset:1024 sc1
v_add_f32 v74, v30, v74
mav 12,108,56
v_fmac_f32 v75, v30, v30
v_add_f32 v74, v31, v74
mav 0,112,60
R 40,10240
v_fmac_f32 v75, v31, v31
mav 4,116,60
mav 8,120,60
D 83,0
v_permlane32_swap_b32 v74, v75
mav 12,124,60
v_add_f32 v74, v74, v75
s_waitcnt lgkmcnt(1)
mav 0,128,32
R 44,11264
ds_write_b32 v97, v74 offset:1024
mav 4,132,32
mav 8,136,32
D 83,1024
mav 12,140,32
mav 0,144,36
R 48,12288
mav 4,148,36
mav 8,152,36
D 83,2048
mav 12,156,36
s_waitcnt lgkmcnt(1)
mav 0,160,40
R 52,13312
v_add_u32 v85, 1, v85
mav 4,164,40
mav 8,168,40
D 83,3072
s_mov_b32 m0, 142336
mav 12,172,40
buffer_load_dword v84, s[36:39], 0 offen sc1 lds
mav 0,176,44
R 56,14336
mav 4,180,44
mav 8,184,44
buffer_load_dwordx4 v[164:167], v90, s[36:39], s49 offen sc1
mav 12,188,44
s_waitcnt lgkmcnt(1)
mav 0,192,48
R 60,15360
mav 4,196,48
mav 8,200,48
buffer_load_dwordx4 v[168:171], v90, s[36:39], s49 offen offset:1024 sc1
mav 12,204,48
mav 0,208,52
mav 4,212,52
mav 8,216,52
buffer_load_dword v84, s[36:39], 0 offen sc1 lds
v_add_u32 v72, 69, v72
mav 12,220,52
s_waitcnt lgkmcnt(0)
mav 0,224,56
mav 4,228,56
mav 8,232,56
mav 12,236,56
mav 0,240,60
s_waitcnt vmcnt(12)
buffer_store_dwordx2 v[72:73], v81, s[36:39], s52 offen sc1
mav 4,244,60
ds_read_b32 v76, v82 offset:0
mav 8,248,60
mav 12,252,60
mvv 0,116,124
mvv 4,120,124
mvv 8,116,128
mvv 12,120,128
mvv 0,116,132
mvv 4,120,132
s_waitcnt lgkmcnt(0)
v_cmp_gt_i32 vcc, v85, v76
mvv 8,116,136
s_cbranch_vccnz CR0_9
BR0_9:
s_add_u32 s49, s49, 346112
mvv 12,120,136
s_barrier
mvv 0,116,140
R 32,32768
mvv 4,120,140
buffer_load_dwordx4 v[172:175], v86, s[36:39], s49 offen sc1
mvv 8,116,144
mvv 12,120,144
s_waitcnt vmcnt(13)
mvv 0,116,148
R 36,33792
mvv 4,120,148
buffer_load_dwordx4 v[176:179], v86, s[36:39], s49 offen offset:1024 sc1
s_waitcnt vmcnt(13)
mvv 8,116,152
mvv 12,120,152
s_waitcnt vmcnt(12)
mvv 0,116,156
R 40,34816
mvv 4,120,156
s_waitcnt vmcnt(11)
mvv 8,116,160
buffer_load_dwordx4 v[180:183], v87, s[36:39], s49 offen sc1
mvv 12,120,160
s_waitcnt lgkmcnt(1)
v_mfma_f32_16x16x32_bf16 v[16:19], a[0:3], v[32:35], v[100:103]
R 44,35840
v_cvt_pk_bf16_f32 v64, v0, v1
v_mfma_f32_16x16x32_bf16 v[20:23], a[4:7], v[32:35], v[104:107]
v_cvt_pk_bf16_f32 v65, v2, v3
v_cvt_pk_bf16_f32 v66, v4, v5
v_mfma_f32_16x16x32_bf16 v[24:27], a[8:11], v[32:35], v[108:111]
buffer_load_dwordx4 v[184:187], v87, s[36:39], s49 offen offset:1024 sc1
v_cvt_pk_bf16_f32 v67, v6, v7
v_mfma_f32_16x16x32_bf16 v[28:31], a[12:15], v[32:35], v[112:115]
ds_write_b128 v94, v[64:67] offset:0
v_cvt_pk_bf16_f32 v68, v8, v9
mav 16,16,36
R 48,36864
v_cvt_pk_bf16_f32 v69, v10, v11
mav 20,20,36
v_cvt_pk_bf16_f32 v70, v12, v13
v_cvt_pk_bf16_f32 v71, v14, v15
mav 24,24,36
ds_write_b128 v95, v[68:71] offset:0
mav 28,28,36
buffer_load_dwordx4 v[188:191], v88, s[36:39], s49 offen sc1
s_waitcnt lgkmcnt(3)
mav 16,32,40
R 52,37888
mav 20,36,40
s_waitcnt lgkmcnt(1)
ds_read_b128 v[64:67], v96 offset:0
mav 24,40,40
ds_read_b128 v[68:71], v96 offset:1024
mav 28,44,40
buffer_load_dwordx4 v[192:195], v88, s[36:39], s49 offen offset:1024 sc1
mav 16,48,44
R 56,38912
mav 20,52,44
mav 24,56,44
mav 28,60,44
mav 16,64,48
R 60,39936
buffer_load_dwordx4 v[196:199], v89, s[36:39], s49 offen sc1
mav 20,68,48
mav 24,72,48
mav 28,76,48
s_waitcnt lgkmcnt(1)
mav 16,80,52
R 32,40960
buffer_store_dwordx4 v[64:67], v92, s[44:47], s54 offen
mav 20,84,52
mav 24,88,52
mav 28,92,52
s_add_u32 m0, s50, 32768
mav 16,96,56
R 36,41984
buffer_store_dwordx4 v[68:71], v93, s[44:47], s54 offen
mav 20,100,56
mav 24,104,56
mav 28,108,56
s_waitcnt lgkmcnt(1)
mav 16,112,60
R 40,43008
buffer_load_dwordx4 v[200:203], v89, s[36:39], s49 offen offset:1024 sc1
mav 20,116,60
mav 24,120,60
mav 28,124,60
mav 16,128,32
R 44,44032
D 83,0
mav 20,132,32
mav 24,136,32
mav 28,140,32
s_waitcnt lgkmcnt(1)
mav 16,144,36
R 48,45056
D 83,1024
mav 20,148,36
mav 24,152,36
mav 28,156,36
mav 16,160,40
R 52,46080
D 83,2048
mav 20,164,40
s_sub_u32 s54, s54, 0x8000
v_add_u32 v85, 1, v85
mav 24,168,40
mav 28,172,40
s_waitcnt lgkmcnt(1)
mav 16,176,44
R 56,47104
D 83,3072
mav 20,180,44
s_mov_b32 m0, 142336
mav 24,184,44
buffer_load_dword v84, s[36:39], 0 offen sc1 lds
mav 28,188,44
mav 16,192,48
R 60,48128
buffer_load_dwordx4 v[124:127], v90, s[36:39], s49 offen sc1
mav 20,196,48
mav 24,200,48
mav 28,204,48
s_waitcnt lgkmcnt(1)
mav 16,208,52
buffer_load_dwordx4 v[128:131], v90, s[36:39], s49 offen offset:1024 sc1
mav 20,212,52
mav 24,216,52
buffer_load_dword v84, s[36:39], 0 offen sc1 lds
mav 28,220,52
mav 16,224,56
mav 20,228,56
mav 24,232,56
mav 28,236,56
s_waitcnt lgkmcnt(0)
mav 16,240,60
mav 20,244,60
ds_read_b32 v76, v82 offset:0
mav 24,248,60
mav 28,252,60
s_waitcnt vmcnt(21)
mvv 16,116,164
mvv 20,120,164
s_waitcnt vmcnt(20)
mvv 24,116,168
s_xor_b32 s50, s50, 0x10000
mvv 28,120,168
s_waitcnt vmcnt(17)
mvv 16,116,172
mvv 20,120,172
v_xor_b32 v80, 0x10000, v80
s_waitcnt vmcnt(16)
mvv 24,116,176
s_waitcnt lgkmcnt(0)
v_cmp_gt_i32 vcc, v85, v76
mvv 28,120,176
s_cbranch_vccnz CR0_10
BR0_10:
s_waitcnt vmcnt(15)
mvv 16,116,180
s_barrier
R 32,0
mvv 20,120,180
s_add_u32 s49, s49, 346112
s_waitcnt vmcnt(14)
mvv 24,116,184
buffer_load_dwordx4 v[132:135], v86, s[36:39], s49 offen sc1
mvv 28,120,184
s_waitcnt vmcnt(14)
mvv 16,116,188
R 36,1024
mvv 20,120,188
s_waitcnt vmcnt(13)
mvv 24,116,192
buffer_load_dwordx4 v[136:139], v86, s[36:39], s49 offen offset:1024 sc1
mvv 28,120,192
s_waitcnt vmcnt(13)
mvv 16,116,196
R 40,2048
mvv 20,120,196
s_waitcnt vmcnt(10)
mvv 24,116,200
buffer_load_dwordx4 v[140:143], v87, s[36:39], s49 offen sc1
mvv 28,120,200
s_mov_b32 s60, -13
.p2align 6
LR0_11:
s_waitcnt lgkmcnt(1)
v_mfma_f32_16x16x32_bf16 v[0:3], a[0:3], v[32:35], v[100:103]
R 44,3072
v_cvt_pk_bf16_f32 v64, v16, v17
v_mfma_f32_16x16x32_bf16 v[4:7], a[4:7], v[32:35], v[104:107]
v_cvt_pk_bf16_f32 v65, v18, v19
v_cvt_pk_bf16_f32 v66, v20, v21
v_mfma_f32_16x16x32_bf16 v[8:11], a[8:11], v[32:35], v[108:111]
buffer_load_dwordx4 v[144:147], v87, s[36:39], s49 offen offset:1024 sc1
v_cvt_pk_bf16_f32 v67, v22, v23
v_mfma_f32_16x16x32_bf16 v[12:15], a[12:15], v[32:35], v[112:115]
ds_write_b128 v94, v[64:67] offset:0
v_cvt_pk_bf16_f32 v68, v24, v25
mav 0,16,36
R 48,4096
v_cvt_pk_bf16_f32 v69, v26, v27
mav 4,20,36
v_cvt_pk_bf16_f32 v70, v28, v29
v_cvt_pk_bf16_f32 v71, v30, v31
mav 8,24,36
ds_write_b128 v95, v[68:71] offset:0
mav 12,28,36
buffer_load_dwordx4 v[148:151], v88, s[36:39], s49 offen sc1
s_waitcnt lgkmcnt(3)
mav 0,32,40
R 52,5120
mav 4,36,40
s_waitcnt lgkmcnt(1)
ds_read_b128 v[64:67], v96 offset:0
mav 8,40,40
ds_read_b128 v[68:71], v96 offset:1024
mav 12,44,40
buffer_load_dwordx4 v[152:155], v88, s[36:39], s49 offen offset:1024 sc1
mav 0,48,44
R 56,6144
mav 4,52,44
mav 8,56,44
mav 12,60,44
mav 0,64,48
R 60,7168
buffer_load_dwordx4 v[156:159], v89, s[36:39], s49 offen sc1
mav 4,68,48
mav 8,72,48
mav 12,76,48
s_waitcnt lgkmcnt(1)
mav 0,80,52
R 32,8192
buffer_store_dwordx4 v[64:67], v92, s[44:47], s54 offen
mav 4,84,52
mav 8,88,52
mav 12,92,52
s_add_u32 m0, s50, 0
mav 0,96,56
R 36,9216
buffer_store_dwordx4 v[68:71], v93, s[44:47], s54 offen
mav 4,100,56
mav 8,104,56
mav 12,108,56
s_waitcnt lgkmcnt(1)
mav 0,112,60
R 40,10240
buffer_load_dwordx4 v[160:163], v89, s[36:39], s49 offen offset:1024 sc1
mav 4,116,60
mav 8,120,60
mav 12,124,60
mav 0,128,32
R 44,11264
D 83,0
mav 4,132,32
mav 8,136,32
mav 12,140,32
s_waitcnt lgkmcnt(1)
mav 0,144,36
R 48,12288
D 83,1024
mav 4,148,36
mav 8,152,36
mav 12,156,36
mav 0,160,40
R 52,13312
D 83,2048
mav 4,164,40
s_sub_u32 s54, s54, 0x8000
v_add_u32 v85, 1, v85
mav 8,168,40
mav 12,172,40
s_waitcnt lgkmcnt(1)
mav 0,176,44
R 56,14336
D 83,3072
mav 4,180,44
s_mov_b32 m0, 142336
mav 8,184,44
buffer_load_dword v84, s[36:39], 0 offen sc1 lds
mav 12,188,44
mav 0,192,48
R 60,15360
buffer_load_dwordx4 v[164:167], v90, s[36:39], s49 offen sc1
mav 4,196,48
mav 8,200,48
mav 12,204,48
s_waitcnt lgkmcnt(1)
mav 0,208,52
buffer_load_dwordx4 v[168:171], v90, s[36:39], s49 offen offset:1024 sc1
mav 4,212,52
mav 8,216,52
buffer_load_dword v84, s[36:39], 0 offen sc1 lds
mav 12,220,52
mav 0,224,56
mav 4,228,56
mav 8,232,56
mav 12,236,56
s_waitcnt lgkmcnt(0)
mav 0,240,60
mav 4,244,60
ds_read_b32 v76, v82 offset:0
mav 8,248,60
mav 12,252,60
s_waitcnt vmcnt(20)
mvv 0,116,124
mvv 4,120,124
s_waitcnt vmcnt(19)
mvv 8,116,128
mvv 12,120,128
s_waitcnt vmcnt(17)
mvv 0,116,132
mvv 4,120,132
s_waitcnt lgkmcnt(0)
v_cmp_gt_i32 vcc, v85, v76
s_waitcnt vmcnt(16)
mvv 8,116,136
s_cbranch_vccnz CR0_12
BR0_12:
s_add_u32 s49, s49, 346112
mvv 12,120,136
s_barrier
s_waitcnt vmcnt(15)
mvv 0,116,140
R 32,32768
mvv 4,120,140
buffer_load_dwordx4 v[172:175], v86, s[36:39], s49 offen sc1
s_waitcnt vmcnt(15)
mvv 8,116,144
mvv 12,120,144
s_waitcnt vmcnt(14)
mvv 0,116,148
R 36,33792
mvv 4,120,148
buffer_load_dwordx4 v[176:179], v86, s[36:39], s49 offen offset:1024 sc1
s_waitcnt vmcnt(14)
mvv 8,116,152
mvv 12,120,152
s_waitcnt vmcnt(13)
mvv 0,116,156
R 40,34816
mvv 4,120,156
s_waitcnt vmcnt(10)
mvv 8,116,160
buffer_load_dwordx4 v[180:183], v87, s[36:39], s49 offen sc1
mvv 12,120,160
s_waitcnt lgkmcnt(1)
v_mfma_f32_16x16x32_bf16 v[16:19], a[0:3], v[32:35], v[100:103]
R 44,35840
v_cvt_pk_bf16_f32 v64, v0, v1
v_mfma_f32_16x16x32_bf16 v[20:23], a[4:7], v[32:35], v[104:107]
v_cvt_pk_bf16_f32 v65, v2, v3
v_cvt_pk_bf16_f32 v66, v4, v5
v_mfma_f32_16x16x32_bf16 v[24:27], a[8:11], v[32:35], v[108:111]
buffer_load_dwordx4 v[184:187], v87, s[36:39], s49 offen offset:1024 sc1
v_cvt_pk_bf16_f32 v67, v6, v7
v_mfma_f32_16x16x32_bf16 v[28:31], a[12:15], v[32:35], v[112:115]
ds_write_b128 v94, v[64:67] offset:0
v_cvt_pk_bf16_f32 v68, v8, v9
mav 16,16,36
R 48,36864
v_cvt_pk_bf16_f32 v69, v10, v11
mav 20,20,36
v_cvt_pk_bf16_f32 v70, v12, v13
v_cvt_pk_bf16_f32 v71, v14, v15
mav 24,24,36
ds_write_b128 v95, v[68:71] offset:0
mav 28,28,36
buffer_load_dwordx4 v[188:191], v88, s[36:39], s49 offen sc1
s_waitcnt lgkmcnt(3)
mav 16,32,40
R 52,37888
mav 20,36,40
s_waitcnt lgkmcnt(1)
ds_read_b128 v[64:67], v96 offset:0
mav 24,40,40
ds_read_b128 v[68:71], v96 offset:1024
mav 28,44,40
buffer_load_dwordx4 v[192:195], v88, s[36:39], s49 offen offset:1024 sc1
mav 16,48,44
R 56,38912
mav 20,52,44
mav 24,56,44
mav 28,60,44
mav 16,64,48
R 60,39936
buffer_load_dwordx4 v[196:199], v89, s[36:39], s49 offen sc1
mav 20,68,48
mav 24,72,48
mav 28,76,48
s_waitcnt lgkmcnt(1)
mav 16,80,52
R 32,40960
buffer_store_dwordx4 v[64:67], v92, s[44:47], s54 offen
mav 20,84,52
mav 24,88,52
mav 28,92,52
s_add_u32 m0, s50, 32768
mav 16,96,56
R 36,41984
buffer_store_dwordx4 v[68:71], v93, s[44:47], s54 offen
mav 20,100,56
mav 24,104,56
mav 28,108,56
s_waitcnt lgkmcnt(1)
mav 16,112,60
R 40,43008
buffer_load_dwordx4 v[200:203], v89, s[36:39], s49 offen offset:1024 sc1
mav 20,116,60
mav 24,120,60
mav 28,124,60
mav 16,128,32
R 44,44032
D 83,0
mav 20,132,32
mav 24,136,32
mav 28,140,32
s_waitcnt lgkmcnt(1)
mav 16,144,36
R 48,45056
D 83,1024
mav 20,148,36
mav 24,152,36
mav 28,156,36
mav 16,160,40
R 52,46080
D 83,2048
mav 20,164,40
s_sub_u32 s54, s54, 0x8000
v_add_u32 v85, 1, v85
mav 24,168,40
mav 28,172,40
s_waitcnt lgkmcnt(1)
mav 16,176,44
R 56,47104
D 83,3072
mav 20,180,44
s_mov_b32 m0, 142336
mav 24,184,44
buffer_load_dword v84, s[36:39], 0 offen sc1 lds
mav 28,188,44
mav 16,192,48
R 60,48128
buffer_load_dwordx4 v[124:127], v90, s[36:39], s49 offen sc1
mav 20,196,48
mav 24,200,48
mav 28,204,48
s_waitcnt lgkmcnt(1)
mav 16,208,52
buffer_load_dwordx4 v[128:131], v90, s[36:39], s49 offen offset:1024 sc1
mav 20,212,52
mav 24,216,52
buffer_load_dword v84, s[36:39], 0 offen sc1 lds
mav 28,220,52
mav 16,224,56
mav 20,228,56
mav 24,232,56
mav 28,236,56
s_waitcnt lgkmcnt(0)
mav 16,240,60
mav 20,244,60
ds_read_b32 v76, v82 offset:0
mav 24,248,60
mav 28,252,60
s_waitcnt vmcnt(20)
mvv 16,116,164
mvv 20,120,164
s_waitcnt vmcnt(19)
mvv 24,116,168
s_xor_b32 s50, s50, 0x10000
mvv 28,120,168
s_waitcnt vmcnt(17)
mvv 16,116,172
mvv 20,120,172
v_xor_b32 v80, 0x10000, v80
s_waitcnt vmcnt(16)
mvv 24,116,176
s_waitcnt lgkmcnt(0)
v_cmp_gt_i32 vcc, v85, v76
mvv 28,120,176
s_cbranch_vccnz CR0_13
BR0_13:
s_waitcnt vmcnt(15)
mvv 16,116,180
s_barrier
R 32,0
mvv 20,120,180
s_add_u32 s49, s49, 346112
s_waitcnt vmcnt(14)
mvv 24,116,184
buffer_load_dwordx4 v[132:135], v86, s[36:39], s49 offen sc1
mvv 28,120,184
s_waitcnt vmcnt(14)
mvv 16,116,188
R 36,1024
mvv 20,120,188
s_waitcnt vmcnt(13)
mvv 24,116,192
buffer_load_dwordx4 v[136:139], v86, s[36:39], s49 offen offset:1024 sc1
mvv 28,120,192
s_waitcnt vmcnt(13)
mvv 16,116,196
R 40,2048
mvv 20,120,196
s_waitcnt vmcnt(10)
mvv 24,116,200
buffer_load_dwordx4 v[140:143], v87, s[36:39], s49 offen sc1
mvv 28,120,200
s_add_u32 s60, s60, 1
s_cbranch_scc0 LR0_11
s_waitcnt lgkmcnt(1)
v_mfma_f32_16x16x32_bf16 v[0:3], a[0:3], v[32:35], v[100:103]
R 44,3072
v_cvt_pk_bf16_f32 v64, v16, v17
v_mfma_f32_16x16x32_bf16 v[4:7], a[4:7], v[32:35], v[104:107]
v_cvt_pk_bf16_f32 v65, v18, v19
v_cvt_pk_bf16_f32 v66, v20, v21
v_mfma_f32_16x16x32_bf16 v[8:11], a[8:11], v[32:35], v[108:111]
buffer_load_dwordx4 v[144:147], v87, s[36:39], s49 offen offset:1024 sc1
v_cvt_pk_bf16_f32 v67, v22, v23
v_mfma_f32_16x16x32_bf16 v[12:15], a[12:15], v[32:35], v[112:115]
ds_write_b128 v94, v[64:67] offset:0
v_cvt_pk_bf16_f32 v68, v24, v25
mav 0,16,36
R 48,4096
v_cvt_pk_bf16_f32 v69, v26, v27
mav 4,20,36
v_cvt_pk_bf16_f32 v70, v28, v29
v_cvt_pk_bf16_f32 v71, v30, v31
mav 8,24,36
ds_write_b128 v95, v[68:71] offset:0
mav 12,28,36
buffer_load_dwordx4 v[148:151], v88, s[36:39], s49 offen sc1
s_waitcnt lgkmcnt(3)
mav 0,32,40
R 52,5120
mav 4,36,40
s_waitcnt lgkmcnt(1)
ds_read_b128 v[64:67], v96 offset:0
mav 8,40,40
ds_read_b128 v[68:71], v96 offset:1024
mav 12,44,40
buffer_load_dwordx4 v[152:155], v88, s[36:39], s49 offen offset:1024 sc1
mav 0,48,44
R 56,6144
mav 4,52,44
mav 8,56,44
mav 12,60,44
mav 0,64,48
R 60,7168
buffer_load_dwordx4 v[156:159], v89, s[36:39], s49 offen sc1
mav 4,68,48
mav 8,72,48
mav 12,76,48
s_waitcnt lgkmcnt(1)
mav 0,80,52
R 32,8192
buffer_store_dwordx4 v[64:67], v92, s[44:47], s54 offen
mav 4,84,52
mav 8,88,52
mav 12,92,52
s_add_u32 m0, s50, 0
mav 0,96,56
R 36,9216
buffer_store_dwordx4 v[68:71], v93, s[44:47], s54 offen
mav 4,100,56
mav 8,104,56
mav 12,108,56
s_waitcnt lgkmcnt(1)
mav 0,112,60
R 40,10240
buffer_load_dwordx4 v[160:163], v89, s[36:39], s49 offen offset:1024 sc1
mav 4,116,60
mav 8,120,60
mav 12,124,60
mav 0,128,32
R 44,11264
D 83,0
mav 4,132,32
mav 8,136,32
mav 12,140,32
s_waitcnt lgkmcnt(1)
mav 0,144,36
R 48,12288
D 83,1024
mav 4,148,36
mav 8,152,36
mav 12,156,36
mav 0,160,40
R 52,13312
D 83,2048
mav 4,164,40
s_sub_u32 s54, s54, 0x8000
v_add_u32 v85, 1, v85
mav 8,168,40
mav 12,172,40
s_waitcnt lgkmcnt(1)
mav 0,176,44
R 56,14336
D 83,3072
mav 4,180,44
s_mov_b32 m0, 142336
mav 8,184,44
buffer_load_dword v84, s[36:39], 0 offen sc1 lds
mav 12,188,44
mav 0,192,48
R 60,15360
buffer_load_dwordx4 v[164:167], v90, s[36:39], s49 offen sc1
mav 4,196,48
mav 8,200,48
mav 12,204,48
s_waitcnt lgkmcnt(1)
mav 0,208,52
buffer_load_dwordx4 v[168:171], v90, s[36:39], s49 offen offset:1024 sc1
mav 4,212,52
mav 8,216,52
buffer_load_dword v84, s[36:39], 0 offen sc1 lds
mav 12,220,52
mav 0,224,56
mav 4,228,56
mav 8,232,56
mav 12,236,56
s_waitcnt lgkmcnt(0)
mav 0,240,60
mav 4,244,60
ds_read_b32 v76, v82 offset:0
mav 8,248,60
mav 12,252,60
s_waitcnt vmcnt(20)
mvv 0,116,124
mvv 4,120,124
s_waitcnt vmcnt(19)
mvv 8,116,128
mvv 12,120,128
s_waitcnt vmcnt(17)
mvv 0,116,132
mvv 4,120,132
s_waitcnt lgkmcnt(0)
v_cmp_gt_i32 vcc, v85, v76
s_waitcnt vmcnt(16)
mvv 8,116,136
s_cbranch_vccnz CR0_14
BR0_14:
s_add_u32 s49, s49, 346112
mvv 12,120,136
s_barrier
s_waitcnt vmcnt(15)
mvv 0,116,140
R 32,32768
mvv 4,120,140
buffer_load_dwordx4 v[172:175], v86, s[36:39], s49 offen sc1
s_waitcnt vmcnt(15)
mvv 8,116,144
mvv 12,120,144
s_waitcnt vmcnt(14)
mvv 0,116,148
R 36,33792
mvv 4,120,148
buffer_load_dwordx4 v[176:179], v86, s[36:39], s49 offen offset:1024 sc1
s_waitcnt vmcnt(14)
mvv 8,116,152
mvv 12,120,152
s_waitcnt vmcnt(13)
mvv 0,116,156
R 40,34816
mvv 4,120,156
s_waitcnt vmcnt(10)
mvv 8,116,160
buffer_load_dwordx4 v[180:183], v87, s[36:39], s49 offen sc1
mvv 12,120,160
s_waitcnt lgkmcnt(1)
v_mfma_f32_16x16x32_bf16 v[16:19], a[0:3], v[32:35], v[100:103]
R 44,35840
v_cvt_pk_bf16_f32 v64, v0, v1
v_mfma_f32_16x16x32_bf16 v[20:23], a[4:7], v[32:35], v[104:107]
v_cvt_pk_bf16_f32 v65, v2, v3
v_cvt_pk_bf16_f32 v66, v4, v5
v_mfma_f32_16x16x32_bf16 v[24:27], a[8:11], v[32:35], v[108:111]
buffer_load_dwordx4 v[184:187], v87, s[36:39], s49 offen offset:1024 sc1
v_cvt_pk_bf16_f32 v67, v6, v7
v_mfma_f32_16x16x32_bf16 v[28:31], a[12:15], v[32:35], v[112:115]
ds_write_b128 v94, v[64:67] offset:0
v_cvt_pk_bf16_f32 v68, v8, v9
)"
R"(
mav 16,16,36
R 48,36864
v_cvt_pk_bf16_f32 v69, v10, v11
mav 20,20,36
v_cvt_pk_bf16_f32 v70, v12, v13
v_cvt_pk_bf16_f32 v71, v14, v15
mav 24,24,36
ds_write_b128 v95, v[68:71] offset:0
mav 28,28,36
buffer_load_dwordx4 v[188:191], v88, s[36:39], s49 offen sc1
s_waitcnt lgkmcnt(3)
mav 16,32,40
R 52,37888
mav 20,36,40
s_waitcnt lgkmcnt(1)
ds_read_b128 v[64:67], v96 offset:0
mav 24,40,40
ds_read_b128 v[68:71], v96 offset:1024
mav 28,44,40
buffer_load_dwordx4 v[192:195], v88, s[36:39], s49 offen offset:1024 sc1
mav 16,48,44
R 56,38912
mav 20,52,44
mav 24,56,44
mav 28,60,44
mav 16,64,48
R 60,39936
buffer_load_dwordx4 v[196:199], v89, s[36:39], s49 offen sc1
mav 20,68,48
mav 24,72,48
mav 28,76,48
s_waitcnt lgkmcnt(1)
mav 16,80,52
R 32,40960
buffer_store_dwordx4 v[64:67], v92, s[44:47], s54 offen
mav 20,84,52
mav 24,88,52
mav 28,92,52
s_add_u32 m0, s50, 32768
mav 16,96,56
R 36,41984
buffer_store_dwordx4 v[68:71], v93, s[44:47], s54 offen
mav 20,100,56
mav 24,104,56
mav 28,108,56
s_waitcnt lgkmcnt(1)
mav 16,112,60
R 40,43008
buffer_load_dwordx4 v[200:203], v89, s[36:39], s49 offen offset:1024 sc1
mav 20,116,60
mav 24,120,60
mav 28,124,60
mav 16,128,32
R 44,44032
D 83,0
mav 20,132,32
mav 24,136,32
mav 28,140,32
s_waitcnt lgkmcnt(1)
mav 16,144,36
R 48,45056
D 83,1024
mav 20,148,36
mav 24,152,36
mav 28,156,36
mav 16,160,40
R 52,46080
D 83,2048
mav 20,164,40
s_sub_u32 s54, s54, 0x8000
v_add_u32 v85, 1, v85
mav 24,168,40
mav 28,172,40
s_waitcnt lgkmcnt(1)
mav 16,176,44
R 56,47104
D 83,3072
mav 20,180,44
s_mov_b32 m0, 142336
mav 24,184,44
buffer_load_dword v84, s[36:39], 0 offen sc1 lds
mav 28,188,44
mav 16,192,48
R 60,48128
buffer_load_dwordx4 v[124:127], v90, s[36:39], s49 offen sc1
mav 20,196,48
mav 24,200,48
mav 28,204,48
s_waitcnt lgkmcnt(1)
mav 16,208,52
buffer_load_dwordx4 v[128:131], v90, s[36:39], s49 offen offset:1024 sc1
mav 20,212,52
mav 24,216,52
buffer_load_dword v84, s[36:39], 0 offen sc1 lds
mav 28,220,52
mav 16,224,56
mav 20,228,56
mav 24,232,56
mav 28,236,56
s_waitcnt lgkmcnt(0)
mav 16,240,60
mav 20,244,60
ds_read_b32 v76, v82 offset:0
mav 24,248,60
mav 28,252,60
s_waitcnt vmcnt(20)
mvv 16,116,164
mvv 20,120,164
s_waitcnt vmcnt(19)
mvv 24,116,168
mvv 28,120,168
s_waitcnt vmcnt(17)
mvv 16,116,172
mvv 20,120,172
v_xor_b32 v80, 0x10000, v80
s_waitcnt vmcnt(16)
mvv 24,116,176
s_waitcnt lgkmcnt(0)
v_cmp_gt_i32 vcc, v85, v76
mvv 28,120,176
s_cbranch_vccnz CR0_15
BR0_15:
s_waitcnt vmcnt(15)
mvv 16,116,180
s_barrier
R 32,0
mvv 20,120,180
s_add_u32 s49, s49, 346112
s_waitcnt vmcnt(14)
mvv 24,116,184
buffer_load_dwordx4 v[132:135], v86, s[36:39], s49 offen sc1
mvv 28,120,184
s_waitcnt vmcnt(14)
mvv 16,116,188
R 36,1024
mvv 20,120,188
s_waitcnt vmcnt(13)
mvv 24,116,192
buffer_load_dwordx4 v[136:139], v86, s[36:39], s49 offen offset:1024 sc1
mvv 28,120,192
s_waitcnt vmcnt(13)
mvv 16,116,196
R 40,2048
mvv 20,120,196
s_waitcnt vmcnt(10)
mvv 24,116,200
buffer_load_dwordx4 v[140:143], v87, s[36:39], s49 offen sc1
mvv 28,120,200
s_waitcnt lgkmcnt(1)
v_mfma_f32_16x16x32_bf16 v[0:3], a[0:3], v[32:35], v[100:103]
R 44,3072
v_cvt_pk_bf16_f32 v64, v16, v17
v_mfma_f32_16x16x32_bf16 v[4:7], a[4:7], v[32:35], v[104:107]
v_cvt_pk_bf16_f32 v65, v18, v19
v_cvt_pk_bf16_f32 v66, v20, v21
v_mfma_f32_16x16x32_bf16 v[8:11], a[8:11], v[32:35], v[108:111]
buffer_load_dwordx4 v[144:147], v87, s[36:39], s49 offen offset:1024 sc1
v_cvt_pk_bf16_f32 v67, v22, v23
v_mfma_f32_16x16x32_bf16 v[12:15], a[12:15], v[32:35], v[112:115]
ds_write_b128 v94, v[64:67] offset:0
v_cvt_pk_bf16_f32 v68, v24, v25
mav 0,16,36
R 48,4096
v_cvt_pk_bf16_f32 v69, v26, v27
mav 4,20,36
v_cvt_pk_bf16_f32 v70, v28, v29
v_cvt_pk_bf16_f32 v71, v30, v31
mav 8,24,36
ds_write_b128 v95, v[68:71] offset:0
mav 12,28,36
buffer_load_dwordx4 v[148:151], v88, s[36:39], s49 offen sc1
s_waitcnt lgkmcnt(3)
mav 0,32,40
R 52,5120
mav 4,36,40
s_waitcnt lgkmcnt(1)
ds_read_b128 v[64:67], v96 offset:0
mav 8,40,40
ds_read_b128 v[68:71], v96 offset:1024
mav 12,44,40
buffer_load_dwordx4 v[152:155], v88, s[36:39], s49 offen offset:1024 sc1
mav 0,48,44
R 56,6144
mav 4,52,44
mav 8,56,44
mav 12,60,44
mav 0,64,48
R 60,7168
buffer_load_dwordx4 v[156:159], v89, s[36:39], s49 offen sc1
mav 4,68,48
mav 8,72,48
mav 12,76,48
s_waitcnt lgkmcnt(1)
mav 0,80,52
R 32,8192
buffer_store_dwordx4 v[64:67], v92, s[44:47], s54 offen
mav 4,84,52
mav 8,88,52
mav 12,92,52
mav 0,96,56
R 36,9216
buffer_store_dwordx4 v[68:71], v93, s[44:47], s54 offen
mav 4,100,56
mav 8,104,56
mav 12,108,56
s_waitcnt lgkmcnt(1)
mav 0,112,60
R 40,10240
buffer_load_dwordx4 v[160:163], v89, s[36:39], s49 offen offset:1024 sc1
mav 4,116,60
mav 8,120,60
mav 12,124,60
mav 0,128,32
R 44,11264
mav 4,132,32
mav 8,136,32
mav 12,140,32
s_waitcnt lgkmcnt(1)
mav 0,144,36
R 48,12288
mav 4,148,36
mav 8,152,36
mav 12,156,36
mav 0,160,40
R 52,13312
s_mov_b32 m0, 142336
mav 4,164,40
s_sub_u32 s54, s54, 0x8000
v_add_u32 v85, 1, v85
mav 8,168,40
buffer_load_dword v84, s[36:39], 0 offen sc1 lds
mav 12,172,40
buffer_load_dwordx4 v[164:167], v90, s[36:39], s49 offen sc1
s_waitcnt lgkmcnt(1)
mav 0,176,44
R 56,14336
mav 4,180,44
mav 8,184,44
mav 12,188,44
buffer_load_dwordx4 v[168:171], v90, s[36:39], s49 offen offset:1024 sc1
mav 0,192,48
R 60,15360
mav 4,196,48
mav 8,200,48
mav 12,204,48
s_waitcnt lgkmcnt(1)
mav 0,208,52
mav 4,212,52
mav 8,216,52
buffer_load_dword v84, s[36:39], 0 offen sc1 lds
mav 12,220,52
mav 0,224,56
mav 4,228,56
mav 8,232,56
mav 12,236,56
s_waitcnt lgkmcnt(0)
mav 0,240,60
mav 4,244,60
ds_read_b32 v76, v82 offset:0
mav 8,248,60
mav 12,252,60
s_waitcnt vmcnt(16)
mvv 0,116,124
mvv 4,120,124
s_waitcnt vmcnt(15)
mvv 8,116,128
mvv 12,120,128
s_waitcnt vmcnt(13)
mvv 0,116,132
mvv 4,120,132
s_waitcnt lgkmcnt(0)
v_cmp_gt_i32 vcc, v85, v76
s_waitcnt vmcnt(12)
mvv 8,116,136
s_cbranch_vccnz CR0_16
BR0_16:
s_add_u32 s49, s49, 346112
mvv 12,120,136
s_barrier
s_waitcnt vmcnt(11)
mvv 0,116,140
R 32,32768
mvv 4,120,140
buffer_load_dwordx4 v[172:175], v86, s[36:39], s49 offen sc1
s_waitcnt vmcnt(11)
mvv 8,116,144
mvv 12,120,144
s_waitcnt vmcnt(10)
mvv 0,116,148
R 36,33792
mvv 4,120,148
buffer_load_dwordx4 v[176:179], v86, s[36:39], s49 offen offset:1024 sc1
s_waitcnt vmcnt(10)
mvv 8,116,152
mvv 12,120,152
s_waitcnt vmcnt(9)
mvv 0,116,156
R 40,34816
mvv 4,120,156
s_waitcnt vmcnt(6)
mvv 8,116,160
buffer_load_dwordx4 v[180:183], v87, s[36:39], s49 offen sc1
mvv 12,120,160
s_waitcnt lgkmcnt(1)
v_mfma_f32_16x16x32_bf16 v[16:19], a[0:3], v[32:35], v[100:103]
R 44,35840
v_cvt_pk_bf16_f32 v64, v0, v1
v_mfma_f32_16x16x32_bf16 v[20:23], a[4:7], v[32:35], v[104:107]
v_cvt_pk_bf16_f32 v65, v2, v3
v_cvt_pk_bf16_f32 v66, v4, v5
v_mfma_f32_16x16x32_bf16 v[24:27], a[8:11], v[32:35], v[108:111]
buffer_load_dwordx4 v[184:187], v87, s[36:39], s49 offen offset:1024 sc1
v_cvt_pk_bf16_f32 v67, v6, v7
v_mfma_f32_16x16x32_bf16 v[28:31], a[12:15], v[32:35], v[112:115]
ds_write_b128 v94, v[64:67] offset:0
v_cvt_pk_bf16_f32 v68, v8, v9
mav 16,16,36
R 48,36864
v_cvt_pk_bf16_f32 v69, v10, v11
mav 20,20,36
v_cvt_pk_bf16_f32 v70, v12, v13
v_cvt_pk_bf16_f32 v71, v14, v15
mav 24,24,36
ds_write_b128 v95, v[68:71] offset:0
mav 28,28,36
buffer_load_dwordx4 v[188:191], v88, s[36:39], s49 offen sc1
s_waitcnt lgkmcnt(3)
mav 16,32,40
R 52,37888
mav 20,36,40
s_waitcnt lgkmcnt(1)
ds_read_b128 v[64:67], v96 offset:0
mav 24,40,40
ds_read_b128 v[68:71], v96 offset:1024
mav 28,44,40
buffer_load_dwordx4 v[192:195], v88, s[36:39], s49 offen offset:1024 sc1
mav 16,48,44
R 56,38912
mav 20,52,44
mav 24,56,44
mav 28,60,44
mav 16,64,48
R 60,39936
buffer_load_dwordx4 v[196:199], v89, s[36:39], s49 offen sc1
mav 20,68,48
mav 24,72,48
mav 28,76,48
s_waitcnt lgkmcnt(1)
mav 16,80,52
R 32,40960
buffer_store_dwordx4 v[64:67], v92, s[44:47], s54 offen
mav 20,84,52
mav 24,88,52
mav 28,92,52
mav 16,96,56
R 36,41984
buffer_store_dwordx4 v[68:71], v93, s[44:47], s54 offen
mav 20,100,56
mav 24,104,56
mav 28,108,56
s_waitcnt lgkmcnt(1)
mav 16,112,60
R 40,43008
buffer_load_dwordx4 v[200:203], v89, s[36:39], s49 offen offset:1024 sc1
mav 20,116,60
mav 24,120,60
mav 28,124,60
mav 16,128,32
R 44,44032
mav 20,132,32
mav 24,136,32
mav 28,140,32
s_waitcnt lgkmcnt(1)
mav 16,144,36
R 48,45056
mav 20,148,36
mav 24,152,36
mav 28,156,36
mav 16,160,40
R 52,46080
s_sub_u32 s54, s54, 0x8000
mav 20,164,40
mav 24,168,40
mav 28,172,40
s_waitcnt lgkmcnt(1)
mav 16,176,44
R 56,47104
mav 20,180,44
mav 24,184,44
mav 28,188,44
mav 16,192,48
R 60,48128
mav 20,196,48
mav 24,200,48
mav 28,204,48
s_waitcnt lgkmcnt(1)
mav 16,208,52
mav 20,212,52
mav 24,216,52
mav 28,220,52
mav 16,224,56
mav 20,228,56
mav 24,232,56
mav 28,236,56
s_waitcnt lgkmcnt(0)
mav 16,240,60
mav 20,244,60
mav 24,248,60
mav 28,252,60
s_waitcnt vmcnt(12)
mvv 16,116,164
mvv 20,120,164
s_waitcnt vmcnt(11)
mvv 24,116,168
mvv 28,120,168
s_waitcnt vmcnt(9)
mvv 16,116,172
mvv 20,120,172
s_waitcnt vmcnt(8)
mvv 24,116,176
mvv 28,120,176
s_waitcnt vmcnt(7)
mvv 16,116,180
mvv 20,120,180
s_waitcnt vmcnt(6)
mvv 24,116,184
mvv 28,120,184
s_waitcnt vmcnt(5)
mvv 16,116,188
mvv 20,120,188
s_waitcnt vmcnt(4)
mvv 24,116,192
mvv 28,120,192
s_waitcnt vmcnt(3)
mvv 16,116,196
mvv 20,120,196
s_waitcnt vmcnt(0)
mvv 24,116,200
mvv 28,120,200
s_nop 3
v_cvt_pk_bf16_f32 v64, v16, v17
v_cvt_pk_bf16_f32 v65, v18, v19
v_cvt_pk_bf16_f32 v66, v20, v21
v_cvt_pk_bf16_f32 v67, v22, v23
ds_write_b128 v94, v[64:67] offset:0
v_cvt_pk_bf16_f32 v68, v24, v25
v_cvt_pk_bf16_f32 v69, v26, v27
v_cvt_pk_bf16_f32 v70, v28, v29
v_cvt_pk_bf16_f32 v71, v30, v31
ds_write_b128 v95, v[68:71] offset:0
s_waitcnt lgkmcnt(0)
ds_read_b128 v[64:67], v96 offset:0
ds_read_b128 v[68:71], v96 offset:1024
s_waitcnt lgkmcnt(0)
buffer_store_dwordx4 v[64:67], v92, s[44:47], s54 offen
buffer_store_dwordx4 v[68:71], v93, s[44:47], s54 offen
s_endpgm
CR0_1:
s_cmp_eq_u32 s61, 0
s_cbranch_scc1 BR0_1
s_sub_u32 s61, s61, 1
s_sleep 8
buffer_load_dwordx2 v[76:77], v84, s[36:39], 0 offen sc1
s_waitcnt vmcnt(0)
ds_write_b32 v82, v76
v_sub_u32 v76, v76, v85
v_xor_b32 v77, v77, v73
v_cmp_lt_u64 vcc, s[62:63], v[76:77]
s_waitcnt lgkmcnt(0)
s_cbranch_vccnz CR0_1
s_branch BR0_1
CR0_2:
s_mov_b32 s58, m0
s_mov_b32 m0, 142336
DR0_2:
s_cmp_eq_u32 s61, 0
s_cbranch_scc1 ER0_2
s_sub_u32 s61, s61, 1
buffer_load_dword v84, s[36:39], 0 offen sc1 lds
s_sleep 1
ds_read_b32 v76, v82
s_waitcnt lgkmcnt(0)
v_cmp_gt_i32 vcc, v85, v76
s_cbranch_vccnz DR0_2
ER0_2:
s_mov_b32 m0, s58
s_branch BR0_2
CR0_3:
s_mov_b32 s58, m0
s_mov_b32 m0, 142336
DR0_3:
s_cmp_eq_u32 s61, 0
s_cbranch_scc1 ER0_3
s_sub_u32 s61, s61, 1
buffer_load_dword v84, s[36:39], 0 offen sc1 lds
s_sleep 1
ds_read_b32 v76, v82
s_waitcnt lgkmcnt(0)
v_cmp_gt_i32 vcc, v85, v76
s_cbranch_vccnz DR0_3
ER0_3:
s_mov_b32 m0, s58
s_branch BR0_3
CR0_4:
s_mov_b32 s58, m0
s_mov_b32 m0, 142336
DR0_4:
s_cmp_eq_u32 s61, 0
s_cbranch_scc1 ER0_4
s_sub_u32 s61, s61, 1
buffer_load_dword v84, s[36:39], 0 offen sc1 lds
s_sleep 1
ds_read_b32 v76, v82
s_waitcnt lgkmcnt(0)
v_cmp_gt_i32 vcc, v85, v76
s_cbranch_vccnz DR0_4
ER0_4:
s_mov_b32 m0, s58
s_branch BR0_4
CR0_5:
s_mov_b32 s58, m0
s_mov_b32 m0, 142336
DR0_5:
s_cmp_eq_u32 s61, 0
s_cbranch_scc1 ER0_5
s_sub_u32 s61, s61, 1
buffer_load_dword v84, s[36:39], 0 offen sc1 lds
s_sleep 1
ds_read_b32 v76, v82
s_waitcnt lgkmcnt(0)
v_cmp_gt_i32 vcc, v85, v76
s_cbranch_vccnz DR0_5
ER0_5:
s_mov_b32 m0, s58
s_branch BR0_5
CR0_7:
s_mov_b32 s58, m0
s_mov_b32 m0, 142336
DR0_7:
s_cmp_eq_u32 s61, 0
s_cbranch_scc1 ER0_7
s_sub_u32 s61, s61, 1
buffer_load_dword v84, s[36:39], 0 offen sc1 lds
s_sleep 1
ds_read_b32 v76, v82
s_waitcnt lgkmcnt(0)
v_cmp_gt_i32 vcc, v85, v76
s_cbranch_vccnz DR0_7
ER0_7:
s_mov_b32 m0, s58
s_branch BR0_7
CR0_8:
s_mov_b32 s58, m0
s_mov_b32 m0, 142336
DR0_8:
s_cmp_eq_u32 s61, 0
s_cbranch_scc1 ER0_8
s_sub_u32 s61, s61, 1
buffer_load_dword v84, s[36:39], 0 offen sc1 lds
s_sleep 1
ds_read_b32 v76, v82
s_waitcnt lgkmcnt(0)
v_cmp_gt_i32 vcc, v85, v76
s_cbranch_vccnz DR0_8
ER0_8:
s_mov_b32 m0, s58
s_branch BR0_8
CR0_9:
s_mov_b32 s58, m0
s_mov_b32 m0, 142336
DR0_9:
s_cmp_eq_u32 s61, 0
s_cbranch_scc1 ER0_9
s_sub_u32 s61, s61, 1
buffer_load_dword v84, s[36:39], 0 offen sc1 lds
s_sleep 1
ds_read_b32 v76, v82
s_waitcnt lgkmcnt(0)
v_cmp_gt_i32 vcc, v85, v76
s_cbranch_vccnz DR0_9
ER0_9:
s_mov_b32 m0, s58
s_branch BR0_9
CR0_10:
s_mov_b32 s58, m0
s_mov_b32 m0, 142336
DR0_10:
s_cmp_eq_u32 s61, 0
s_cbranch_scc1 ER0_10
s_sub_u32 s61, s61, 1
buffer_load_dword v84, s[36:39], 0 offen sc1 lds
s_sleep 1
ds_read_b32 v76, v82
s_waitcnt lgkmcnt(0)
v_cmp_gt_i32 vcc, v85, v76
s_cbranch_vccnz DR0_10
ER0_10:
s_mov_b32 m0, s58
s_branch BR0_10
CR0_12:
s_mov_b32 s58, m0
s_mov_b32 m0, 142336
DR0_12:
s_cmp_eq_u32 s61, 0
s_cbranch_scc1 ER0_12
s_sub_u32 s61, s61, 1
buffer_load_dword v84, s[36:39], 0 offen sc1 lds
s_sleep 1
ds_read_b32 v76, v82
s_waitcnt lgkmcnt(0)
v_cmp_gt_i32 vcc, v85, v76
s_cbranch_vccnz DR0_12
ER0_12:
s_mov_b32 m0, s58
s_branch BR0_12
CR0_13:
s_mov_b32 s58, m0
s_mov_b32 m0, 142336
DR0_13:
s_cmp_eq_u32 s61, 0
s_cbranch_scc1 ER0_13
s_sub_u32 s61, s61, 1
buffer_load_dword v84, s[36:39], 0 offen sc1 lds
s_sleep 1
ds_read_b32 v76, v82
s_waitcnt lgkmcnt(0)
v_cmp_gt_i32 vcc, v85, v76
s_cbranch_vccnz DR0_13
ER0_13:
s_mov_b32 m0, s58
s_branch BR0_13
CR0_14:
s_mov_b32 s58, m0
s_mov_b32 m0, 142336
DR0_14:
s_cmp_eq_u32 s61, 0
s_cbranch_scc1 ER0_14
s_sub_u32 s61, s61, 1
buffer_load_dword v84, s[36:39], 0 offen sc1 lds
s_sleep 1
ds_read_b32 v76, v82
s_waitcnt lgkmcnt(0)
v_cmp_gt_i32 vcc, v85, v76
s_cbranch_vccnz DR0_14
ER0_14:
s_mov_b32 m0, s58
s_branch BR0_14
CR0_15:
s_mov_b32 s58, m0
s_mov_b32 m0, 142336
DR0_15:
s_cmp_eq_u32 s61, 0
s_cbranch_scc1 ER0_15
s_sub_u32 s61, s61, 1
buffer_load_dword v84, s[36:39], 0 offen sc1 lds
s_sleep 1
ds_read_b32 v76, v82
s_waitcnt lgkmcnt(0)
v_cmp_gt_i32 vcc, v85, v76
s_cbranch_vccnz DR0_15
ER0_15:
s_mov_b32 m0, s58
s_branch BR0_15
CR0_16:
s_mov_b32 s58, m0
s_mov_b32 m0, 142336
DR0_16:
s_cmp_eq_u32 s61, 0
s_cbranch_scc1 ER0_16
s_sub_u32 s61, s61, 1
buffer_load_dword v84, s[36:39], 0 offen sc1 lds
s_sleep 1
ds_read_b32 v76, v82
s_waitcnt lgkmcnt(0)
v_cmp_gt_i32 vcc, v85, v76
s_cbranch_vccnz DR0_16
ER0_16:
s_mov_b32 m0, s58
s_branch BR0_16
LvR1:
s_add_u32 s49, s49, 346112
s_barrier
s_add_u32 m0, s50, 0
s_nop 0
D 83,0
D 83,1024
D 83,2048
D 83,3072
s_add_u32 s49, s49, 346112
s_barrier
s_add_u32 m0, s50, 32768
s_nop 0
D 83,0
D 83,1024
D 83,2048
D 83,3072
buffer_load_dwordx4 v[124:127], v90, s[36:39], s49 offen sc1
buffer_load_dwordx4 v[128:131], v90, s[36:39], s49 offen offset:1024 sc1
s_xor_b32 s50, s50, 0x10000
s_add_u32 s49, s49, 346112
s_waitcnt vmcnt(6)
s_barrier
R 32,0
buffer_load_dwordx4 v[132:135], v86, s[36:39], s49 offen sc1
R 36,1024
buffer_load_dwordx4 v[136:139], v86, s[36:39], s49 offen offset:1024 sc1
R 40,2048
buffer_load_dwordx4 v[140:143], v87, s[36:39], s49 offen sc1
s_waitcnt lgkmcnt(1)
v_mfma_f32_16x16x32_bf16 v[0:3], a[0:3], v[32:35], v[100:103]
R 44,3072
v_mfma_f32_16x16x32_bf16 v[4:7], a[4:7], v[32:35], v[104:107]
v_mfma_f32_16x16x32_bf16 v[8:11], a[8:11], v[32:35], v[108:111]
buffer_load_dwordx4 v[144:147], v87, s[36:39], s49 offen offset:1024 sc1
v_mfma_f32_16x16x32_bf16 v[12:15], a[12:15], v[32:35], v[112:115]
mav 0,16,36
R 48,4096
mav 4,20,36
mav 8,24,36
mav 12,28,36
buffer_load_dwordx4 v[148:151], v88, s[36:39], s49 offen sc1
s_waitcnt lgkmcnt(1)
mav 0,32,40
R 52,5120
mav 4,36,40
mav 8,40,40
mav 12,44,40
buffer_load_dwordx4 v[152:155], v88, s[36:39], s49 offen offset:1024 sc1
mav 0,48,44
R 56,6144
mav 4,52,44
mav 8,56,44
mav 12,60,44
s_waitcnt lgkmcnt(1)
mav 0,64,48
R 60,7168
buffer_load_dwordx4 v[156:159], v89, s[36:39], s49 offen sc1
mav 4,68,48
mav 8,72,48
mav 12,76,48
mav 0,80,52
R 32,8192
buffer_load_dwordx4 v[160:163], v89, s[36:39], s49 offen offset:1024 sc1
mav 4,84,52
mav 8,88,52
mav 12,92,52
s_add_u32 m0, s50, 0
s_waitcnt lgkmcnt(1)
mav 0,96,56
R 36,9216
mav 4,100,56
D 83,0
mav 8,104,56
mav 12,108,56
mav 0,112,60
R 40,10240
mav 4,116,60
D 83,1024
mav 8,120,60
mav 12,124,60
s_waitcnt lgkmcnt(1)
mav 0,128,32
R 44,11264
mav 4,132,32
mav 8,136,32
D 83,2048
mav 12,140,32
mav 0,144,36
R 48,12288
mav 4,148,36
mav 8,152,36
D 83,3072
mav 12,156,36
s_waitcnt lgkmcnt(1)
mav 0,160,40
R 52,13312
mav 4,164,40
mav 8,168,40
mav 12,172,40
buffer_load_dwordx4 v[164:167], v90, s[36:39], s49 offen sc1
mav 0,176,44
R 56,14336
mav 4,180,44
mav 8,184,44
mav 12,188,44
buffer_load_dwordx4 v[168:171], v90, s[36:39], s49 offen offset:1024 sc1
s_waitcnt lgkmcnt(1)
mav 0,192,48
R 60,15360
mav 4,196,48
mav 8,200,48
mav 12,204,48
mav 0,208,52
mav 4,212,52
mav 8,216,52
mav 12,220,52
s_waitcnt lgkmcnt(0)
mav 0,224,56
mav 4,228,56
mav 8,232,56
mav 12,236,56
mav 0,240,60
mav 4,244,60
mav 8,248,60
mav 12,252,60
s_waitcnt vmcnt(15)
mvv 0,116,124
mvv 4,120,124
s_waitcnt vmcnt(14)
mvv 8,116,128
mvv 12,120,128
s_waitcnt vmcnt(13)
mvv 0,116,132
mvv 4,120,132
s_waitcnt vmcnt(12)
mvv 8,116,136
s_add_u32 s49, s49, 346112
mvv 12,120,136
s_barrier
s_waitcnt vmcnt(11)
mvv 0,116,140
R 32,32768
mvv 4,120,140
buffer_load_dwordx4 v[172:175], v86, s[36:39], s49 offen sc1
s_waitcnt vmcnt(11)
mvv 8,116,144
mvv 12,120,144
s_waitcnt vmcnt(10)
mvv 0,116,148
R 36,33792
mvv 4,120,148
buffer_load_dwordx4 v[176:179], v86, s[36:39], s49 offen offset:1024 sc1
s_waitcnt vmcnt(10)
mvv 8,116,152
mvv 12,120,152
s_waitcnt vmcnt(9)
mvv 0,116,156
R 40,34816
mvv 4,120,156
s_waitcnt vmcnt(8)
mvv 8,116,160
buffer_load_dwordx4 v[180:183], v87, s[36:39], s49 offen sc1
mvv 12,120,160
s_waitcnt lgkmcnt(1)
v_mfma_f32_16x16x32_bf16 v[16:19], a[0:3], v[32:35], v[100:103]
R 44,35840
v_cvt_pk_bf16_f32 v64, v0, v1
v_mfma_f32_16x16x32_bf16 v[20:23], a[4:7], v[32:35], v[104:107]
v_cvt_pk_bf16_f32 v65, v2, v3
v_cvt_pk_bf16_f32 v66, v4, v5
v_mfma_f32_16x16x32_bf16 v[24:27], a[8:11], v[32:35], v[108:111]
buffer_load_dwordx4 v[184:187], v87, s[36:39], s49 offen offset:1024 sc1
v_cvt_pk_bf16_f32 v67, v6, v7
v_mfma_f32_16x16x32_bf16 v[28:31], a[12:15], v[32:35], v[112:115]
v_cvt_pk_bf16_f32 v68, v8, v9
v_cvt_pk_bf16_f32 v69, v10, v11
mav 16,16,36
R 48,36864
v_cvt_pk_bf16_f32 v70, v12, v13
mav 20,20,36
v_cvt_pk_bf16_f32 v71, v14, v15
v_mul_f32 v75, v0, v0
mav 24,24,36
buffer_store_dwordx4 v[64:67], v91, s[36:39], s49 offen sc1
v_add_f32 v74, v1, v0
mav 28,28,36
v_fmac_f32 v75, v1, v1
s_waitcnt lgkmcnt(1)
mav 16,32,40
R 52,37888
v_add_f32 v74, v2, v74
mav 20,36,40
v_fmac_f32 v75, v2, v2
v_add_f32 v74, v3, v74
mav 24,40,40
buffer_store_dwordx4 v[68:71], v91, s[36:39], s49 offen offset:1024 sc1
v_fmac_f32 v75, v3, v3
mav 28,44,40
v_add_f32 v74, v4, v74
v_fmac_f32 v75, v4, v4
mav 16,48,44
R 56,38912
v_add_f32 v74, v5, v74
mav 20,52,44
v_fmac_f32 v75, v5, v5
v_add_f32 v74, v6, v74
mav 24,56,44
buffer_load_dwordx4 v[188:191], v88, s[36:39], s49 offen sc1
v_fmac_f32 v75, v6, v6
mav 28,60,44
v_add_f32 v74, v7, v74
s_waitcnt lgkmcnt(1)
mav 16,64,48
R 60,39936
v_fmac_f32 v75, v7, v7
mav 20,68,48
v_add_f32 v74, v8, v74
v_fmac_f32 v75, v8, v8
mav 24,72,48
buffer_load_dwordx4 v[192:195], v88, s[36:39], s49 offen offset:1024 sc1
v_add_f32 v74, v9, v74
mav 28,76,48
v_fmac_f32 v75, v9, v9
v_add_f32 v74, v10, v74
mav 16,80,52
R 32,40960
v_fmac_f32 v75, v10, v10
mav 20,84,52
v_add_f32 v74, v11, v74
v_fmac_f32 v75, v11, v11
mav 24,88,52
buffer_load_dwordx4 v[196:199], v89, s[36:39], s49 offen sc1
v_add_f32 v74, v12, v74
mav 28,92,52
s_add_u32 m0, s50, 32768
s_waitcnt lgkmcnt(1)
mav 16,96,56
R 36,41984
v_fmac_f32 v75, v12, v12
mav 20,100,56
v_add_f32 v74, v13, v74
v_fmac_f32 v75, v13, v13
mav 24,104,56
buffer_load_dwordx4 v[200:203], v89, s[36:39], s49 offen offset:1024 sc1
v_add_f32 v74, v14, v74
mav 28,108,56
v_fmac_f32 v75, v14, v14
v_add_f32 v74, v15, v74
mav 16,112,60
R 40,43008
v_fmac_f32 v75, v15, v15
mav 20,116,60
mav 24,120,60
D 83,0
v_permlane32_swap_b32 v74, v75
mav 28,124,60
v_add_f32 v74, v74, v75
s_waitcnt lgkmcnt(1)
mav 16,128,32
R 44,44032
ds_write_b32 v97, v74 offset:0
mav 20,132,32
mav 24,136,32
D 83,1024
mav 28,140,32
mav 16,144,36
R 48,45056
mav 20,148,36
mav 24,152,36
D 83,2048
mav 28,156,36
s_waitcnt lgkmcnt(1)
mav 16,160,40
R 52,46080
mav 20,164,40
mav 24,168,40
D 83,3072
mav 28,172,40
mav 16,176,44
R 56,47104
mav 20,180,44
mav 24,184,44
buffer_load_dwordx4 v[124:127], v90, s[36:39], s49 offen sc1
mav 28,188,44
s_waitcnt lgkmcnt(1)
mav 16,192,48
R 60,48128
mav 20,196,48
mav 24,200,48
buffer_load_dwordx4 v[128:131], v90, s[36:39], s49 offen offset:1024 sc1
mav 28,204,48
mav 16,208,52
mav 20,212,52
mav 24,216,52
v_add_u32 v72, 1, v72
mav 28,220,52
s_waitcnt lgkmcnt(0)
mav 16,224,56
mav 20,228,56
mav 24,232,56
mav 28,236,56
mav 16,240,60
s_waitcnt vmcnt(10)
buffer_store_dwordx2 v[72:73], v81, s[36:39], s52 offen sc1
mav 20,244,60
mav 24,248,60
mav 28,252,60
mvv 16,116,164
mvv 20,120,164
mvv 24,116,168
s_xor_b32 s50, s50, 0x10000
mvv 28,120,168
mvv 16,116,172
mvv 20,120,172
v_xor_b32 v80, 0x10000, v80
mvv 24,116,176
s_add_u32 s49, s49, 346112
mvv 28,120,176
s_barrier
mvv 16,116,180
R 32,0
ds_read_b128 v[204:207], v98 offset:0
mvv 20,120,180
buffer_load_dwordx4 v[132:135], v86, s[36:39], s49 offen sc1
mvv 24,116,184
mvv 28,120,184
s_waitcnt vmcnt(11)
mvv 16,116,188
R 36,1024
mvv 20,120,188
buffer_load_dwordx4 v[136:139], v86, s[36:39], s49 offen offset:1024 sc1
s_waitcnt vmcnt(11)
mvv 24,116,192
mvv 28,120,192
s_waitcnt vmcnt(10)
mvv 16,116,196
R 40,2048
mvv 20,120,196
s_waitcnt vmcnt(9)
mvv 24,116,200
buffer_load_dwordx4 v[140:143], v87, s[36:39], s49 offen sc1
mvv 28,120,200
s_mov_b32 s60, -15
.p2align 6
LR1_1:
s_waitcnt lgkmcnt(1)
v_mfma_f32_16x16x32_bf16 v[0:3], a[0:3], v[32:35], v[100:103]
R 44,3072
v_add_f32 v204, v204, v205
v_mfma_f32_16x16x32_bf16 v[4:7], a[4:7], v[32:35], v[104:107]
v_add_f32 v206, v206, v207
v_add_f32 v204, v204, v206
v_mfma_f32_16x16x32_bf16 v[8:11], a[8:11], v[32:35], v[108:111]
buffer_load_dwordx4 v[144:147], v87, s[36:39], s49 offen offset:1024 sc1
v_add_f32_dpp v205, v204, v204 quad_perm:[1,0,3,2] row_mask:0xf bank_mask:0xf
v_mfma_f32_16x16x32_bf16 v[12:15], a[12:15], v[32:35], v[112:115]
buffer_store_dword v205, v99, s[36:39], s49 offen sc1
v_cvt_pk_bf16_f32 v64, v16, v17
mav 0,16,36
R 48,4096
v_cvt_pk_bf16_f32 v65, v18, v19
mav 4,20,36
v_cvt_pk_bf16_f32 v66, v20, v21
v_cvt_pk_bf16_f32 v67, v22, v23
mav 8,24,36
buffer_store_dwordx4 v[64:67], v91, s[36:39], s49 offen sc1
v_cvt_pk_bf16_f32 v68, v24, v25
mav 12,28,36
v_cvt_pk_bf16_f32 v69, v26, v27
s_waitcnt lgkmcnt(1)
mav 0,32,40
R 52,5120
v_cvt_pk_bf16_f32 v70, v28, v29
mav 4,36,40
v_cvt_pk_bf16_f32 v71, v30, v31
v_mul_f32 v75, v16, v16
mav 8,40,40
buffer_store_dwordx4 v[68:71], v91, s[36:39], s49 offen offset:1024 sc1
v_add_f32 v74, v17, v16
mav 12,44,40
v_fmac_f32 v75, v17, v17
v_add_f32 v74, v18, v74
mav 0,48,44
R 56,6144
v_fmac_f32 v75, v18, v18
mav 4,52,44
v_add_f32 v74, v19, v74
v_fmac_f32 v75, v19, v19
mav 8,56,44
buffer_load_dwordx4 v[148:151], v88, s[36:39], s49 offen sc1
v_add_f32 v74, v20, v74
mav 12,60,44
v_fmac_f32 v75, v20, v20
s_waitcnt lgkmcnt(1)
mav 0,64,48
R 60,7168
v_add_f32 v74, v21, v74
mav 4,68,48
v_fmac_f32 v75, v21, v21
v_add_f32 v74, v22, v74
mav 8,72,48
buffer_load_dwordx4 v[152:155], v88, s[36:39], s49 offen offset:1024 sc1
v_fmac_f32 v75, v22, v22
mav 12,76,48
v_add_f32 v74, v23, v74
v_fmac_f32 v75, v23, v23
mav 0,80,52
R 32,8192
v_add_f32 v74, v24, v74
mav 4,84,52
v_fmac_f32 v75, v24, v24
v_add_f32 v74, v25, v74
mav 8,88,52
buffer_load_dwordx4 v[156:159], v89, s[36:39], s49 offen sc1
v_fmac_f32 v75, v25, v25
mav 12,92,52
s_add_u32 m0, s50, 0
s_waitcnt lgkmcnt(1)
mav 0,96,56
R 36,9216
v_add_f32 v74, v26, v74
mav 4,100,56
v_fmac_f32 v75, v26, v26
v_add_f32 v74, v27, v74
mav 8,104,56
buffer_load_dwordx4 v[160:163], v89, s[36:39], s49 offen offset:1024 sc1
v_fmac_f32 v75, v27, v27
mav 12,108,56
v_add_f32 v74, v28, v74
v_fmac_f32 v75, v28, v28
mav 0,112,60
R 40,10240
v_add_f32 v74, v29, v74
mav 4,116,60
v_fmac_f32 v75, v29, v29
v_add_f32 v74, v30, v74
mav 8,120,60
D 83,0
v_fmac_f32 v75, v30, v30
mav 12,124,60
v_add_f32 v74, v31, v74
s_waitcnt lgkmcnt(1)
mav 0,128,32
R 44,11264
v_fmac_f32 v75, v31, v31
mav 4,132,32
mav 8,136,32
D 83,1024
v_permlane32_swap_b32 v74, v75
mav 12,140,32
v_add_f32 v74, v74, v75
ds_write_b32 v97, v74 offset:1024
mav 0,144,36
R 48,12288
mav 4,148,36
mav 8,152,36
D 83,2048
mav 12,156,36
s_waitcnt lgkmcnt(2)
mav 0,160,40
R 52,13312
mav 4,164,40
mav 8,168,40
D 83,3072
mav 12,172,40
mav 0,176,44
R 56,14336
mav 4,180,44
mav 8,184,44
buffer_load_dwordx4 v[164:167], v90, s[36:39], s49 offen sc1
mav 12,188,44
s_waitcnt lgkmcnt(1)
mav 0,192,48
R 60,15360
mav 4,196,48
mav 8,200,48
buffer_load_dwordx4 v[168:171], v90, s[36:39], s49 offen offset:1024 sc1
mav 12,204,48
mav 0,208,52
mav 4,212,52
mav 8,216,52
v_add_u32 v72, 1, v72
mav 12,220,52
s_waitcnt lgkmcnt(0)
mav 0,224,56
mav 4,228,56
mav 8,232,56
mav 12,236,56
mav 0,240,60
s_waitcnt vmcnt(10)
buffer_store_dwordx2 v[72:73], v81, s[36:39], s52 offen sc1
mav 4,244,60
mav 8,248,60
mav 12,252,60
mvv 0,116,124
mvv 4,120,124
mvv 8,116,128
mvv 12,120,128
mvv 0,116,132
mvv 4,120,132
mvv 8,116,136
s_add_u32 s49, s49, 346112
mvv 12,120,136
s_barrier
mvv 0,116,140
R 32,32768
ds_read_b128 v[204:207], v98 offset:1024
mvv 4,120,140
buffer_load_dwordx4 v[172:175], v86, s[36:39], s49 offen sc1
mvv 8,116,144
mvv 12,120,144
s_waitcnt vmcnt(11)
mvv 0,116,148
R 36,33792
mvv 4,120,148
buffer_load_dwordx4 v[176:179], v86, s[36:39], s49 offen offset:1024 sc1
s_waitcnt vmcnt(11)
mvv 8,116,152
mvv 12,120,152
s_waitcnt vmcnt(10)
mvv 0,116,156
R 40,34816
mvv 4,120,156
s_waitcnt vmcnt(9)
mvv 8,116,160
buffer_load_dwordx4 v[180:183], v87, s[36:39], s49 offen sc1
mvv 12,120,160
s_waitcnt lgkmcnt(1)
v_mfma_f32_16x16x32_bf16 v[16:19], a[0:3], v[32:35], v[100:103]
R 44,35840
v_add_f32 v204, v204, v205
v_mfma_f32_16x16x32_bf16 v[20:23], a[4:7], v[32:35], v[104:107]
v_add_f32 v206, v206, v207
v_add_f32 v204, v204, v206
v_mfma_f32_16x16x32_bf16 v[24:27], a[8:11], v[32:35], v[108:111]
buffer_load_dwordx4 v[184:187], v87, s[36:39], s49 offen offset:1024 sc1
v_add_f32_dpp v205, v204, v204 quad_perm:[1,0,3,2] row_mask:0xf bank_mask:0xf
v_mfma_f32_16x16x32_bf16 v[28:31], a[12:15], v[32:35], v[112:115]
buffer_store_dword v205, v99, s[36:39], s49 offen sc1
v_cvt_pk_bf16_f32 v64, v0, v1
mav 16,16,36
R 48,36864
v_cvt_pk_bf16_f32 v65, v2, v3
mav 20,20,36
v_cvt_pk_bf16_f32 v66, v4, v5
v_cvt_pk_bf16_f32 v67, v6, v7
mav 24,24,36
buffer_store_dwordx4 v[64:67], v91, s[36:39], s49 offen sc1
v_cvt_pk_bf16_f32 v68, v8, v9
mav 28,28,36
v_cvt_pk_bf16_f32 v69, v10, v11
s_waitcnt lgkmcnt(1)
mav 16,32,40
R 52,37888
v_cvt_pk_bf16_f32 v70, v12, v13
mav 20,36,40
v_cvt_pk_bf16_f32 v71, v14, v15
v_mul_f32 v75, v0, v0
mav 24,40,40
buffer_store_dwordx4 v[68:71], v91, s[36:39], s49 offen offset:1024 sc1
v_add_f32 v74, v1, v0
mav 28,44,40
v_fmac_f32 v75, v1, v1
v_add_f32 v74, v2, v74
mav 16,48,44
R 56,38912
v_fmac_f32 v75, v2, v2
mav 20,52,44
v_add_f32 v74, v3, v74
v_fmac_f32 v75, v3, v3
mav 24,56,44
buffer_load_dwordx4 v[188:191], v88, s[36:39], s49 offen sc1
v_add_f32 v74, v4, v74
mav 28,60,44
v_fmac_f32 v75, v4, v4
s_waitcnt lgkmcnt(1)
mav 16,64,48
R 60,39936
v_add_f32 v74, v5, v74
mav 20,68,48
v_fmac_f32 v75, v5, v5
v_add_f32 v74, v6, v74
mav 24,72,48
buffer_load_dwordx4 v[192:195], v88, s[36:39], s49 offen offset:1024 sc1
v_fmac_f32 v75, v6, v6
mav 28,76,48
v_add_f32 v74, v7, v74
v_fmac_f32 v75, v7, v7
mav 16,80,52
R 32,40960
v_add_f32 v74, v8, v74
mav 20,84,52
v_fmac_f32 v75, v8, v8
v_add_f32 v74, v9, v74
mav 24,88,52
buffer_load_dwordx4 v[196:199], v89, s[36:39], s49 offen sc1
v_fmac_f32 v75, v9, v9
mav 28,92,52
s_add_u32 m0, s50, 32768
s_waitcnt lgkmcnt(1)
mav 16,96,56
R 36,41984
v_add_f32 v74, v10, v74
mav 20,100,56
v_fmac_f32 v75, v10, v10
v_add_f32 v74, v11, v74
mav 24,104,56
buffer_load_dwordx4 v[200:203], v89, s[36:39], s49 offen offset:1024 sc1
v_fmac_f32 v75, v11, v11
mav 28,108,56
v_add_f32 v74, v12, v74
v_fmac_f32 v75, v12, v12
mav 16,112,60
R 40,43008
v_add_f32 v74, v13, v74
mav 20,116,60
v_fmac_f32 v75, v13, v13
v_add_f32 v74, v14, v74
mav 24,120,60
D 83,0
v_fmac_f32 v75, v14, v14
mav 28,124,60
v_add_f32 v74, v15, v74
s_waitcnt lgkmcnt(1)
mav 16,128,32
R 44,44032
v_fmac_f32 v75, v15, v15
mav 20,132,32
mav 24,136,32
D 83,1024
v_permlane32_swap_b32 v74, v75
mav 28,140,32
v_add_f32 v74, v74, v75
ds_write_b32 v97, v74 offset:0
mav 16,144,36
R 48,45056
mav 20,148,36
mav 24,152,36
D 83,2048
mav 28,156,36
s_waitcnt lgkmcnt(2)
mav 16,160,40
R 52,46080
mav 20,164,40
mav 24,168,40
D 83,3072
mav 28,172,40
mav 16,176,44
R 56,47104
mav 20,180,44
mav 24,184,44
buffer_load_dwordx4 v[124:127], v90, s[36:39], s49 offen sc1
mav 28,188,44
s_waitcnt lgkmcnt(1)
mav 16,192,48
R 60,48128
mav 20,196,48
mav 24,200,48
buffer_load_dwordx4 v[128:131], v90, s[36:39], s49 offen offset:1024 sc1
mav 28,204,48
mav 16,208,52
mav 20,212,52
mav 24,216,52
v_add_u32 v72, 1, v72
mav 28,220,52
s_waitcnt lgkmcnt(0)
mav 16,224,56
mav 20,228,56
mav 24,232,56
mav 28,236,56
mav 16,240,60
s_waitcnt vmcnt(10)
buffer_store_dwordx2 v[72:73], v81, s[36:39], s52 offen sc1
mav 20,244,60
mav 24,248,60
mav 28,252,60
mvv 16,116,164
mvv 20,120,164
mvv 24,116,168
s_xor_b32 s50, s50, 0x10000
mvv 28,120,168
mvv 16,116,172
mvv 20,120,172
v_xor_b32 v80, 0x10000, v80
mvv 24,116,176
s_add_u32 s49, s49, 346112
mvv 28,120,176
s_barrier
mvv 16,116,180
R 32,0
ds_read_b128 v[204:207], v98 offset:0
mvv 20,120,180
buffer_load_dwordx4 v[132:135], v86, s[36:39], s49 offen sc1
mvv 24,116,184
mvv 28,120,184
s_waitcnt vmcnt(11)
mvv 16,116,188
R 36,1024
mvv 20,120,188
buffer_load_dwordx4 v[136:139], v86, s[36:39], s49 offen offset:1024 sc1
s_waitcnt vmcnt(11)
mvv 24,116,192
mvv 28,120,192
s_waitcnt vmcnt(10)
mvv 16,116,196
R 40,2048
mvv 20,120,196
s_waitcnt vmcnt(9)
mvv 24,116,200
buffer_load_dwordx4 v[140:143], v87, s[36:39], s49 offen sc1
mvv 28,120,200
s_add_u32 s60, s60, 1
s_cbranch_scc0 LR1_1
s_waitcnt lgkmcnt(1)
v_mfma_f32_16x16x32_bf16 v[0:3], a[0:3], v[32:35], v[100:103]
R 44,3072
v_add_f32 v204, v204, v205
v_mfma_f32_16x16x32_bf16 v[4:7], a[4:7], v[32:35], v[104:107]
v_add_f32 v206, v206, v207
v_add_f32 v204, v204, v206
v_mfma_f32_16x16x32_bf16 v[8:11], a[8:11], v[32:35], v[108:111]
buffer_load_dwordx4 v[144:147], v87, s[36:39], s49 offen offset:1024 sc1
v_add_f32_dpp v205, v204, v204 quad_perm:[1,0,3,2] row_mask:0xf bank_mask:0xf
v_mfma_f32_16x16x32_bf16 v[12:15], a[12:15], v[32:35], v[112:115]
buffer_store_dword v205, v99, s[36:39], s49 offen sc1
v_cvt_pk_bf16_f32 v64, v16, v17
mav 0,16,36
R 48,4096
v_cvt_pk_bf16_f32 v65, v18, v19
mav 4,20,36
v_cvt_pk_bf16_f32 v66, v20, v21
v_cvt_pk_bf16_f32 v67, v22, v23
mav 8,24,36
buffer_store_dwordx4 v[64:67], v91, s[36:39], s49 offen sc1
v_cvt_pk_bf16_f32 v68, v24, v25
mav 12,28,36
v_cvt_pk_bf16_f32 v69, v26, v27
s_waitcnt lgkmcnt(1)
mav 0,32,40
R 52,5120
v_cvt_pk_bf16_f32 v70, v28, v29
mav 4,36,40
v_cvt_pk_bf16_f32 v71, v30, v31
v_mul_f32 v75, v16, v16
mav 8,40,40
buffer_store_dwordx4 v[68:71], v91, s[36:39], s49 offen offset:1024 sc1
v_add_f32 v74, v17, v16
mav 12,44,40
v_fmac_f32 v75, v17, v17
v_add_f32 v74, v18, v74
mav 0,48,44
R 56,6144
v_fmac_f32 v75, v18, v18
mav 4,52,44
v_add_f32 v74, v19, v74
v_fmac_f32 v75, v19, v19
mav 8,56,44
buffer_load_dwordx4 v[148:151], v88, s[36:39], s49 offen sc1
v_add_f32 v74, v20, v74
mav 12,60,44
v_fmac_f32 v75, v20, v20
s_waitcnt lgkmcnt(1)
mav 0,64,48
R 60,7168
v_add_f32 v74, v21, v74
mav 4,68,48
v_fmac_f32 v75, v21, v21
v_add_f32 v74, v22, v74
mav 8,72,48
buffer_load_dwordx4 v[152:155], v88, s[36:39], s49 offen offset:1024 sc1
v_fmac_f32 v75, v22, v22
mav 12,76,48
v_add_f32 v74, v23, v74
v_fmac_f32 v75, v23, v23
mav 0,80,52
R 32,8192
v_add_f32 v74, v24, v74
mav 4,84,52
v_fmac_f32 v75, v24, v24
v_add_f32 v74, v25, v74
mav 8,88,52
buffer_load_dwordx4 v[156:159], v89, s[36:39], s49 offen sc1
v_fmac_f32 v75, v25, v25
mav 12,92,52
s_add_u32 m0, s50, 0
s_waitcnt lgkmcnt(1)
mav 0,96,56
R 36,9216
v_add_f32 v74, v26, v74
mav 4,100,56
v_fmac_f32 v75, v26, v26
v_add_f32 v74, v27, v74
mav 8,104,56
buffer_load_dwordx4 v[160:163], v89, s[36:39], s49 offen offset:1024 sc1
v_fmac_f32 v75, v27, v27
mav 12,108,56
v_add_f32 v74, v28, v74
v_fmac_f32 v75, v28, v28
mav 0,112,60
R 40,10240
v_add_f32 v74, v29, v74
mav 4,116,60
v_fmac_f32 v75, v29, v29
v_add_f32 v74, v30, v74
mav 8,120,60
D 83,0
v_fmac_f32 v75, v30, v30
mav 12,124,60
v_add_f32 v74, v31, v74
s_waitcnt lgkmcnt(1)
mav 0,128,32
R 44,11264
v_fmac_f32 v75, v31, v31
mav 4,132,32
mav 8,136,32
D 83,1024
v_permlane32_swap_b32 v74, v75
mav 12,140,32
v_add_f32 v74, v74, v75
ds_write_b32 v97, v74 offset:1024
mav 0,144,36
R 48,12288
mav 4,148,36
mav 8,152,36
D 83,2048
mav 12,156,36
s_waitcnt lgkmcnt(2)
mav 0,160,40
R 52,13312
mav 4,164,40
mav 8,168,40
D 83,3072
mav 12,172,40
mav 0,176,44
R 56,14336
mav 4,180,44
mav 8,184,44
buffer_load_dwordx4 v[164:167], v90, s[36:39], s49 offen sc1
mav 12,188,44
s_waitcnt lgkmcnt(1)
mav 0,192,48
R 60,15360
mav 4,196,48
mav 8,200,48
buffer_load_dwordx4 v[168:171], v90, s[36:39], s49 offen offset:1024 sc1
mav 12,204,48
mav 0,208,52
mav 4,212,52
mav 8,216,52
v_add_u32 v72, 1, v72
mav 12,220,52
s_waitcnt lgkmcnt(0)
mav 0,224,56
mav 4,228,56
mav 8,232,56
mav 12,236,56
mav 0,240,60
s_waitcnt vmcnt(10)
buffer_store_dwordx2 v[72:73], v81, s[36:39], s52 offen sc1
mav 4,244,60
mav 8,248,60
mav 12,252,60
mvv 0,116,124
mvv 4,120,124
mvv 8,116,128
mvv 12,120,128
mvv 0,116,132
mvv 4,120,132
mvv 8,116,136
s_add_u32 s49, s49, 346112
mvv 12,120,136
s_barrier
mvv 0,116,140
R 32,32768
ds_read_b128 v[204:207], v98 offset:1024
mvv 4,120,140
buffer_load_dwordx4 v[172:175], v86, s[36:39], s49 offen sc1
mvv 8,116,144
mvv 12,120,144
s_waitcnt vmcnt(11)
mvv 0,116,148
R 36,33792
mvv 4,120,148
buffer_load_dwordx4 v[176:179], v86, s[36:39], s49 offen offset:1024 sc1
s_waitcnt vmcnt(11)
mvv 8,116,152
mvv 12,120,152
s_waitcnt vmcnt(10)
mvv 0,116,156
R 40,34816
mvv 4,120,156
s_waitcnt vmcnt(9)
mvv 8,116,160
buffer_load_dwordx4 v[180:183], v87, s[36:39], s49 offen sc1
mvv 12,120,160
s_waitcnt lgkmcnt(1)
v_mfma_f32_16x16x32_bf16 v[16:19], a[0:3], v[32:35], v[100:103]
R 44,35840
v_add_f32 v204, v204, v205
v_mfma_f32_16x16x32_bf16 v[20:23], a[4:7], v[32:35], v[104:107]
v_add_f32 v206, v206, v207
v_add_f32 v204, v204, v206
v_mfma_f32_16x16x32_bf16 v[24:27], a[8:11], v[32:35], v[108:111]
buffer_load_dwordx4 v[184:187], v87, s[36:39], s49 offen offset:1024 sc1
v_add_f32_dpp v205, v204, v204 quad_perm:[1,0,3,2] row_mask:0xf bank_mask:0xf
v_mfma_f32_16x16x32_bf16 v[28:31], a[12:15], v[32:35], v[112:115]
buffer_store_dword v205, v99, s[36:39], s49 offen sc1
v_cvt_pk_bf16_f32 v64, v0, v1
mav 16,16,36
R 48,36864
v_cvt_pk_bf16_f32 v65, v2, v3
mav 20,20,36
v_cvt_pk_bf16_f32 v66, v4, v5
v_cvt_pk_bf16_f32 v67, v6, v7
mav 24,24,36
ds_write_b128 v94, v[64:67] offset:0
v_cvt_pk_bf16_f32 v68, v8, v9
mav 28,28,36
buffer_load_dwordx4 v[188:191], v88, s[36:39], s49 offen sc1
s_waitcnt lgkmcnt(2)
mav 16,32,40
R 52,37888
v_cvt_pk_bf16_f32 v69, v10, v11
mav 20,36,40
v_cvt_pk_bf16_f32 v70, v12, v13
v_cvt_pk_bf16_f32 v71, v14, v15
mav 24,40,40
ds_write_b128 v95, v[68:71] offset:0
mav 28,44,40
buffer_load_dwordx4 v[192:195], v88, s[36:39], s49 offen offset:1024 sc1
mav 16,48,44
R 56,38912
mav 20,52,44
s_waitcnt lgkmcnt(1)
ds_read_b128 v[64:67], v96 offset:0
mav 24,56,44
ds_read_b128 v[68:71], v96 offset:1024
mav 28,60,44
mav 16,64,48
R 60,39936
buffer_load_dwordx4 v[196:199], v89, s[36:39], s49 offen sc1
mav 20,68,48
mav 24,72,48
mav 28,76,48
mav 16,80,52
R 32,40960
buffer_load_dwordx4 v[200:203], v89, s[36:39], s49 offen offset:1024 sc1
mav 20,84,52
mav 24,88,52
mav 28,92,52
s_add_u32 m0, s50, 32768
s_waitcnt lgkmcnt(1)
mav 16,96,56
R 36,41984
buffer_store_dwordx4 v[64:67], v92, s[44:47], s54 offen
mav 20,100,56
mav 24,104,56
mav 28,108,56
mav 16,112,60
R 40,43008
buffer_store_dwordx4 v[68:71], v93, s[44:47], s54 offen
mav 20,116,60
mav 24,120,60
mav 28,124,60
s_waitcnt lgkmcnt(1)
mav 16,128,32
R 44,44032
D 83,0
mav 20,132,32
mav 24,136,32
mav 28,140,32
mav 16,144,36
R 48,45056
D 83,1024
mav 20,148,36
mav 24,152,36
mav 28,156,36
s_waitcnt lgkmcnt(1)
mav 16,160,40
R 52,46080
D 83,2048
mav 20,164,40
s_sub_u32 s54, s54, 0x8000
mav 24,168,40
mav 28,172,40
mav 16,176,44
R 56,47104
D 83,3072
mav 20,180,44
mav 24,184,44
mav 28,188,44
s_waitcnt lgkmcnt(1)
mav 16,192,48
R 60,48128
buffer_load_dwordx4 v[124:127], v90, s[36:39], s49 offen sc1
mav 20,196,48
mav 24,200,48
mav 28,204,48
mav 16,208,52
buffer_load_dwordx4 v[128:131], v90, s[36:39], s49 offen offset:1024 sc1
mav 20,212,52
mav 24,216,52
v_add_u32 v72, 68, v72
mav 28,220,52
s_waitcnt lgkmcnt(0)
mav 16,224,56
mav 20,228,56
mav 24,232,56
mav 28,236,56
mav 16,240,60
s_waitcnt vmcnt(12)
buffer_store_dwordx2 v[72:73], v81, s[36:39], s52 offen sc1
mav 20,244,60
mav 24,248,60
mav 28,252,60
mvv 16,116,164
mvv 20,120,164
mvv 24,116,168
s_xor_b32 s50, s50, 0x10000
mvv 28,120,168
mvv 16,116,172
mvv 20,120,172
v_xor_b32 v80, 0x10000, v80
mvv 24,116,176
s_add_u32 s49, s49, 346112
mvv 28,120,176
s_barrier
mvv 16,116,180
R 32,0
mvv 20,120,180
buffer_load_dwordx4 v[132:135], v86, s[36:39], s49 offen sc1
mvv 24,116,184
mvv 28,120,184
s_waitcnt vmcnt(13)
mvv 16,116,188
R 36,1024
mvv 20,120,188
buffer_load_dwordx4 v[136:139], v86, s[36:39], s49 offen offset:1024 sc1
s_waitcnt vmcnt(13)
mvv 24,116,192
mvv 28,120,192
s_waitcnt vmcnt(12)
mvv 16,116,196
R 40,2048
mvv 20,120,196
s_waitcnt vmcnt(11)
mvv 24,116,200
buffer_load_dwordx4 v[140:143], v87, s[36:39], s49 offen sc1
mvv 28,120,200
s_waitcnt lgkmcnt(1)
v_mfma_f32_16x16x32_bf16 v[0:3], a[0:3], v[32:35], v[100:103]
R 44,3072
v_cvt_pk_bf16_f32 v64, v16, v17
v_mfma_f32_16x16x32_bf16 v[4:7], a[4:7], v[32:35], v[104:107]
v_cvt_pk_bf16_f32 v65, v18, v19
v_cvt_pk_bf16_f32 v66, v20, v21
v_mfma_f32_16x16x32_bf16 v[8:11], a[8:11], v[32:35], v[108:111]
buffer_load_dwordx4 v[144:147], v87, s[36:39], s49 offen offset:1024 sc1
v_cvt_pk_bf16_f32 v67, v22, v23
v_mfma_f32_16x16x32_bf16 v[12:15], a[12:15], v[32:35], v[112:115]
ds_write_b128 v94, v[64:67] offset:0
v_cvt_pk_bf16_f32 v68, v24, v25
mav 0,16,36
R 48,4096
v_cvt_pk_bf16_f32 v69, v26, v27
mav 4,20,36
v_cvt_pk_bf16_f32 v70, v28, v29
v_cvt_pk_bf16_f32 v71, v30, v31
mav 8,24,36
ds_write_b128 v95, v[68:71] offset:0
mav 12,28,36
buffer_load_dwordx4 v[148:151], v88, s[36:39], s49 offen sc1
s_waitcnt lgkmcnt(3)
mav 0,32,40
R 52,5120
mav 4,36,40
s_waitcnt lgkmcnt(1)
ds_read_b128 v[64:67], v96 offset:0
mav 8,40,40
ds_read_b128 v[68:71], v96 offset:1024
mav 12,44,40
buffer_load_dwordx4 v[152:155], v88, s[36:39], s49 offen offset:1024 sc1
mav 0,48,44
R 56,6144
mav 4,52,44
mav 8,56,44
mav 12,60,44
mav 0,64,48
R 60,7168
buffer_load_dwordx4 v[156:159], v89, s[36:39], s49 offen sc1
mav 4,68,48
mav 8,72,48
mav 12,76,48
s_waitcnt lgkmcnt(1)
mav 0,80,52
R 32,8192
buffer_store_dwordx4 v[64:67], v92, s[44:47], s54 offen
mav 4,84,52
mav 8,88,52
mav 12,92,52
s_add_u32 m0, s50, 0
mav 0,96,56
R 36,9216
buffer_store_dwordx4 v[68:71], v93, s[44:47], s54 offen
mav 4,100,56
mav 8,104,56
mav 12,108,56
s_waitcnt lgkmcnt(1)
mav 0,112,60
R 40,10240
buffer_load_dwordx4 v[160:163], v89, s[36:39], s49 offen offset:1024 sc1
mav 4,116,60
mav 8,120,60
mav 12,124,60
mav 0,128,32
R 44,11264
D 83,0
mav 4,132,32
mav 8,136,32
mav 12,140,32
s_waitcnt lgkmcnt(1)
mav 0,144,36
R 48,12288
D 83,1024
mav 4,148,36
mav 8,152,36
mav 12,156,36
mav 0,160,40
R 52,13312
D 83,2048
mav 4,164,40
s_sub_u32 s54, s54, 0x8000
mav 8,168,40
mav 12,172,40
s_waitcnt lgkmcnt(1)
mav 0,176,44
R 56,14336
D 83,3072
mav 4,180,44
mav 8,184,44
mav 12,188,44
mav 0,192,48
R 60,15360
buffer_load_dwordx4 v[164:167], v90, s[36:39], s49 offen sc1
mav 4,196,48
mav 8,200,48
mav 12,204,48
s_waitcnt lgkmcnt(1)
mav 0,208,52
buffer_load_dwordx4 v[168:171], v90, s[36:39], s49 offen offset:1024 sc1
mav 4,212,52
mav 8,216,52
mav 12,220,52
mav 0,224,56
mav 4,228,56
mav 8,232,56
mav 12,236,56
s_waitcnt lgkmcnt(0)
mav 0,240,60
mav 4,244,60
mav 8,248,60
mav 12,252,60
s_waitcnt vmcnt(18)
mvv 0,116,124
mvv 4,120,124
s_waitcnt vmcnt(17)
mvv 8,116,128
mvv 12,120,128
s_waitcnt vmcnt(15)
mvv 0,116,132
mvv 4,120,132
s_waitcnt vmcnt(14)
mvv 8,116,136
s_add_u32 s49, s49, 346112
mvv 12,120,136
s_barrier
s_waitcnt vmcnt(13)
mvv 0,116,140
R 32,32768
mvv 4,120,140
buffer_load_dwordx4 v[172:175], v86, s[36:39], s49 offen sc1
s_waitcnt vmcnt(13)
mvv 8,116,144
mvv 12,120,144
s_waitcnt vmcnt(12)
mvv 0,116,148
R 36,33792
mvv 4,120,148
buffer_load_dwordx4 v[176:179], v86, s[36:39], s49 offen offset:1024 sc1
s_waitcnt vmcnt(12)
mvv 8,116,152
mvv 12,120,152
s_waitcnt vmcnt(11)
mvv 0,116,156
R 40,34816
mvv 4,120,156
s_waitcnt vmcnt(8)
mvv 8,116,160
buffer_load_dwordx4 v[180:183], v87, s[36:39], s49 offen sc1
mvv 12,120,160
s_waitcnt lgkmcnt(1)
v_mfma_f32_16x16x32_bf16 v[16:19], a[0:3], v[32:35], v[100:103]
R 44,35840
v_cvt_pk_bf16_f32 v64, v0, v1
v_mfma_f32_16x16x32_bf16 v[20:23], a[4:7], v[32:35], v[104:107]
v_cvt_pk_bf16_f32 v65, v2, v3
v_cvt_pk_bf16_f32 v66, v4, v5
v_mfma_f32_16x16x32_bf16 v[24:27], a[8:11], v[32:35], v[108:111]
buffer_load_dwordx4 v[184:187], v87, s[36:39], s49 offen offset:1024 sc1
v_cvt_pk_bf16_f32 v67, v6, v7
v_mfma_f32_16x16x32_bf16 v[28:31], a[12:15], v[32:35], v[112:115]
ds_write_b128 v94, v[64:67] offset:0
v_cvt_pk_bf16_f32 v68, v8, v9
mav 16,16,36
R 48,36864
v_cvt_pk_bf16_f32 v69, v10, v11
mav 20,20,36
v_cvt_pk_bf16_f32 v70, v12, v13
v_cvt_pk_bf16_f32 v71, v14, v15
mav 24,24,36
ds_write_b128 v95, v[68:71] offset:0
mav 28,28,36
buffer_load_dwordx4 v[188:191], v88, s[36:39], s49 offen sc1
s_waitcnt lgkmcnt(3)
mav 16,32,40
R 52,37888
mav 20,36,40
s_waitcnt lgkmcnt(1)
ds_read_b128 v[64:67], v96 offset:0
mav 24,40,40
ds_read_b128 v[68:71], v96 offset:1024
mav 28,44,40
buffer_load_dwordx4 v[192:195], v88, s[36:39], s49 offen offset:1024 sc1
mav 16,48,44
R 56,38912
mav 20,52,44
mav 24,56,44
mav 28,60,44
mav 16,64,48
R 60,39936
buffer_load_dwordx4 v[196:199], v89, s[36:39], s49 offen sc1
mav 20,68,48
mav 24,72,48
mav 28,76,48
s_waitcnt lgkmcnt(1)
mav 16,80,52
R 32,40960
buffer_store_dwordx4 v[64:67], v92, s[44:47], s54 offen
mav 20,84,52
mav 24,88,52
mav 28,92,52
s_add_u32 m0, s50, 32768
mav 16,96,56
R 36,41984
buffer_store_dwordx4 v[68:71], v93, s[44:47], s54 offen
mav 20,100,56
mav 24,104,56
mav 28,108,56
s_waitcnt lgkmcnt(1)
mav 16,112,60
R 40,43008
buffer_load_dwordx4 v[200:203], v89, s[36:39], s49 offen offset:1024 sc1
mav 20,116,60
mav 24,120,60
mav 28,124,60
mav 16,128,32
R 44,44032
D 83,0
mav 20,132,32
mav 24,136,32
mav 28,140,32
s_waitcnt lgkmcnt(1)
mav 16,144,36
R 48,45056
D 83,1024
mav 20,148,36
mav 24,152,36
mav 28,156,36
mav 16,160,40
R 52,46080
D 83,2048
mav 20,164,40
s_sub_u32 s54, s54, 0x8000
mav 24,168,40
mav 28,172,40
s_waitcnt lgkmcnt(1)
mav 16,176,44
R 56,47104
D 83,3072
mav 20,180,44
mav 24,184,44
mav 28,188,44
mav 16,192,48
R 60,48128
buffer_load_dwordx4 v[124:127], v90, s[36:39], s49 offen sc1
mav 20,196,48
mav 24,200,48
mav 28,204,48
s_waitcnt lgkmcnt(1)
mav 16,208,52
buffer_load_dwordx4 v[128:131], v90, s[36:39], s49 offen offset:1024 sc1
mav 20,212,52
mav 24,216,52
mav 28,220,52
mav 16,224,56
mav 20,228,56
mav 24,232,56
mav 28,236,56
s_waitcnt lgkmcnt(0)
mav 16,240,60
mav 20,244,60
mav 24,248,60
mav 28,252,60
s_waitcnt vmcnt(17)
mvv 16,116,164
mvv 20,120,164
s_waitcnt vmcnt(16)
mvv 24,116,168
s_xor_b32 s50, s50, 0x10000
mvv 28,120,168
)"
R"(
s_waitcnt vmcnt(15)
mvv 16,116,172
mvv 20,120,172
v_xor_b32 v80, 0x10000, v80
s_waitcnt vmcnt(14)
mvv 24,116,176
s_add_u32 s49, s49, 346112
mvv 28,120,176
s_barrier
s_waitcnt vmcnt(13)
mvv 16,116,180
R 32,0
mvv 20,120,180
buffer_load_dwordx4 v[132:135], v86, s[36:39], s49 offen sc1
s_waitcnt vmcnt(13)
mvv 24,116,184
mvv 28,120,184
s_waitcnt vmcnt(12)
mvv 16,116,188
R 36,1024
mvv 20,120,188
buffer_load_dwordx4 v[136:139], v86, s[36:39], s49 offen offset:1024 sc1
s_waitcnt vmcnt(12)
mvv 24,116,192
mvv 28,120,192
s_waitcnt vmcnt(11)
mvv 16,116,196
R 40,2048
mvv 20,120,196
s_waitcnt vmcnt(8)
mvv 24,116,200
buffer_load_dwordx4 v[140:143], v87, s[36:39], s49 offen sc1
mvv 28,120,200
s_mov_b32 s60, -12
.p2align 6
LR1_2:
s_waitcnt lgkmcnt(1)
v_mfma_f32_16x16x32_bf16 v[0:3], a[0:3], v[32:35], v[100:103]
R 44,3072
v_cvt_pk_bf16_f32 v64, v16, v17
v_mfma_f32_16x16x32_bf16 v[4:7], a[4:7], v[32:35], v[104:107]
v_cvt_pk_bf16_f32 v65, v18, v19
v_cvt_pk_bf16_f32 v66, v20, v21
v_mfma_f32_16x16x32_bf16 v[8:11], a[8:11], v[32:35], v[108:111]
buffer_load_dwordx4 v[144:147], v87, s[36:39], s49 offen offset:1024 sc1
v_cvt_pk_bf16_f32 v67, v22, v23
v_mfma_f32_16x16x32_bf16 v[12:15], a[12:15], v[32:35], v[112:115]
ds_write_b128 v94, v[64:67] offset:0
v_cvt_pk_bf16_f32 v68, v24, v25
mav 0,16,36
R 48,4096
v_cvt_pk_bf16_f32 v69, v26, v27
mav 4,20,36
v_cvt_pk_bf16_f32 v70, v28, v29
v_cvt_pk_bf16_f32 v71, v30, v31
mav 8,24,36
ds_write_b128 v95, v[68:71] offset:0
mav 12,28,36
buffer_load_dwordx4 v[148:151], v88, s[36:39], s49 offen sc1
s_waitcnt lgkmcnt(3)
mav 0,32,40
R 52,5120
mav 4,36,40
s_waitcnt lgkmcnt(1)
ds_read_b128 v[64:67], v96 offset:0
mav 8,40,40
ds_read_b128 v[68:71], v96 offset:1024
mav 12,44,40
buffer_load_dwordx4 v[152:155], v88, s[36:39], s49 offen offset:1024 sc1
mav 0,48,44
R 56,6144
mav 4,52,44
mav 8,56,44
mav 12,60,44
mav 0,64,48
R 60,7168
buffer_load_dwordx4 v[156:159], v89, s[36:39], s49 offen sc1
mav 4,68,48
mav 8,72,48
mav 12,76,48
s_waitcnt lgkmcnt(1)
mav 0,80,52
R 32,8192
buffer_store_dwordx4 v[64:67], v92, s[44:47], s54 offen
mav 4,84,52
mav 8,88,52
mav 12,92,52
s_add_u32 m0, s50, 0
mav 0,96,56
R 36,9216
buffer_store_dwordx4 v[68:71], v93, s[44:47], s54 offen
mav 4,100,56
mav 8,104,56
mav 12,108,56
s_waitcnt lgkmcnt(1)
mav 0,112,60
R 40,10240
buffer_load_dwordx4 v[160:163], v89, s[36:39], s49 offen offset:1024 sc1
mav 4,116,60
mav 8,120,60
mav 12,124,60
mav 0,128,32
R 44,11264
D 83,0
mav 4,132,32
mav 8,136,32
mav 12,140,32
s_waitcnt lgkmcnt(1)
mav 0,144,36
R 48,12288
D 83,1024
mav 4,148,36
mav 8,152,36
mav 12,156,36
mav 0,160,40
R 52,13312
D 83,2048
mav 4,164,40
s_sub_u32 s54, s54, 0x8000
mav 8,168,40
mav 12,172,40
s_waitcnt lgkmcnt(1)
mav 0,176,44
R 56,14336
D 83,3072
mav 4,180,44
mav 8,184,44
mav 12,188,44
mav 0,192,48
R 60,15360
buffer_load_dwordx4 v[164:167], v90, s[36:39], s49 offen sc1
mav 4,196,48
mav 8,200,48
mav 12,204,48
s_waitcnt lgkmcnt(1)
mav 0,208,52
buffer_load_dwordx4 v[168:171], v90, s[36:39], s49 offen offset:1024 sc1
mav 4,212,52
mav 8,216,52
mav 12,220,52
mav 0,224,56
mav 4,228,56
mav 8,232,56
mav 12,236,56
s_waitcnt lgkmcnt(0)
mav 0,240,60
mav 4,244,60
mav 8,248,60
mav 12,252,60
s_waitcnt vmcnt(17)
mvv 0,116,124
mvv 4,120,124
s_waitcnt vmcnt(16)
mvv 8,116,128
mvv 12,120,128
s_waitcnt vmcnt(15)
mvv 0,116,132
mvv 4,120,132
s_waitcnt vmcnt(14)
mvv 8,116,136
s_add_u32 s49, s49, 346112
mvv 12,120,136
s_barrier
s_waitcnt vmcnt(13)
mvv 0,116,140
R 32,32768
mvv 4,120,140
buffer_load_dwordx4 v[172:175], v86, s[36:39], s49 offen sc1
s_waitcnt vmcnt(13)
mvv 8,116,144
mvv 12,120,144
s_waitcnt vmcnt(12)
mvv 0,116,148
R 36,33792
mvv 4,120,148
buffer_load_dwordx4 v[176:179], v86, s[36:39], s49 offen offset:1024 sc1
s_waitcnt vmcnt(12)
mvv 8,116,152
mvv 12,120,152
s_waitcnt vmcnt(11)
mvv 0,116,156
R 40,34816
mvv 4,120,156
s_waitcnt vmcnt(8)
mvv 8,116,160
buffer_load_dwordx4 v[180:183], v87, s[36:39], s49 offen sc1
mvv 12,120,160
s_waitcnt lgkmcnt(1)
v_mfma_f32_16x16x32_bf16 v[16:19], a[0:3], v[32:35], v[100:103]
R 44,35840
v_cvt_pk_bf16_f32 v64, v0, v1
v_mfma_f32_16x16x32_bf16 v[20:23], a[4:7], v[32:35], v[104:107]
v_cvt_pk_bf16_f32 v65, v2, v3
v_cvt_pk_bf16_f32 v66, v4, v5
v_mfma_f32_16x16x32_bf16 v[24:27], a[8:11], v[32:35], v[108:111]
buffer_load_dwordx4 v[184:187], v87, s[36:39], s49 offen offset:1024 sc1
v_cvt_pk_bf16_f32 v67, v6, v7
v_mfma_f32_16x16x32_bf16 v[28:31], a[12:15], v[32:35], v[112:115]
ds_write_b128 v94, v[64:67] offset:0
v_cvt_pk_bf16_f32 v68, v8, v9
mav 16,16,36
R 48,36864
v_cvt_pk_bf16_f32 v69, v10, v11
mav 20,20,36
v_cvt_pk_bf16_f32 v70, v12, v13
v_cvt_pk_bf16_f32 v71, v14, v15
mav 24,24,36
ds_write_b128 v95, v[68:71] offset:0
mav 28,28,36
buffer_load_dwordx4 v[188:191], v88, s[36:39], s49 offen sc1
s_waitcnt lgkmcnt(3)
mav 16,32,40
R 52,37888
mav 20,36,40
s_waitcnt lgkmcnt(1)
ds_read_b128 v[64:67], v96 offset:0
mav 24,40,40
ds_read_b128 v[68:71], v96 offset:1024
mav 28,44,40
buffer_load_dwordx4 v[192:195], v88, s[36:39], s49 offen offset:1024 sc1
mav 16,48,44
R 56,38912
mav 20,52,44
mav 24,56,44
mav 28,60,44
mav 16,64,48
R 60,39936
buffer_load_dwordx4 v[196:199], v89, s[36:39], s49 offen sc1
mav 20,68,48
mav 24,72,48
mav 28,76,48
s_waitcnt lgkmcnt(1)
mav 16,80,52
R 32,40960
buffer_store_dwordx4 v[64:67], v92, s[44:47], s54 offen
mav 20,84,52
mav 24,88,52
mav 28,92,52
s_add_u32 m0, s50, 32768
mav 16,96,56
R 36,41984
buffer_store_dwordx4 v[68:71], v93, s[44:47], s54 offen
mav 20,100,56
mav 24,104,56
mav 28,108,56
s_waitcnt lgkmcnt(1)
mav 16,112,60
R 40,43008
buffer_load_dwordx4 v[200:203], v89, s[36:39], s49 offen offset:1024 sc1
mav 20,116,60
mav 24,120,60
mav 28,124,60
mav 16,128,32
R 44,44032
D 83,0
mav 20,132,32
mav 24,136,32
mav 28,140,32
s_waitcnt lgkmcnt(1)
mav 16,144,36
R 48,45056
D 83,1024
mav 20,148,36
mav 24,152,36
mav 28,156,36
mav 16,160,40
R 52,46080
D 83,2048
mav 20,164,40
s_sub_u32 s54, s54, 0x8000
mav 24,168,40
mav 28,172,40
s_waitcnt lgkmcnt(1)
mav 16,176,44
R 56,47104
D 83,3072
mav 20,180,44
mav 24,184,44
mav 28,188,44
mav 16,192,48
R 60,48128
buffer_load_dwordx4 v[124:127], v90, s[36:39], s49 offen sc1
mav 20,196,48
mav 24,200,48
mav 28,204,48
s_waitcnt lgkmcnt(1)
mav 16,208,52
buffer_load_dwordx4 v[128:131], v90, s[36:39], s49 offen offset:1024 sc1
mav 20,212,52
mav 24,216,52
mav 28,220,52
mav 16,224,56
mav 20,228,56
mav 24,232,56
mav 28,236,56
s_waitcnt lgkmcnt(0)
mav 16,240,60
mav 20,244,60
mav 24,248,60
mav 28,252,60
s_waitcnt vmcnt(17)
mvv 16,116,164
mvv 20,120,164
s_waitcnt vmcnt(16)
mvv 24,116,168
s_xor_b32 s50, s50, 0x10000
mvv 28,120,168
s_waitcnt vmcnt(15)
mvv 16,116,172
mvv 20,120,172
v_xor_b32 v80, 0x10000, v80
s_waitcnt vmcnt(14)
mvv 24,116,176
s_add_u32 s49, s49, 346112
mvv 28,120,176
s_barrier
s_waitcnt vmcnt(13)
mvv 16,116,180
R 32,0
mvv 20,120,180
buffer_load_dwordx4 v[132:135], v86, s[36:39], s49 offen sc1
s_waitcnt vmcnt(13)
mvv 24,116,184
mvv 28,120,184
s_waitcnt vmcnt(12)
mvv 16,116,188
R 36,1024
mvv 20,120,188
buffer_load_dwordx4 v[136:139], v86, s[36:39], s49 offen offset:1024 sc1
s_waitcnt vmcnt(12)
mvv 24,116,192
mvv 28,120,192
s_waitcnt vmcnt(11)
mvv 16,116,196
R 40,2048
mvv 20,120,196
s_waitcnt vmcnt(8)
mvv 24,116,200
buffer_load_dwordx4 v[140:143], v87, s[36:39], s49 offen sc1
mvv 28,120,200
s_add_u32 s60, s60, 1
s_cbranch_scc0 LR1_2
s_waitcnt lgkmcnt(1)
v_mfma_f32_16x16x32_bf16 v[0:3], a[0:3], v[32:35], v[100:103]
R 44,3072
v_cvt_pk_bf16_f32 v64, v16, v17
v_mfma_f32_16x16x32_bf16 v[4:7], a[4:7], v[32:35], v[104:107]
v_cvt_pk_bf16_f32 v65, v18, v19
v_cvt_pk_bf16_f32 v66, v20, v21
v_mfma_f32_16x16x32_bf16 v[8:11], a[8:11], v[32:35], v[108:111]
buffer_load_dwordx4 v[144:147], v87, s[36:39], s49 offen offset:1024 sc1
v_cvt_pk_bf16_f32 v67, v22, v23
v_mfma_f32_16x16x32_bf16 v[12:15], a[12:15], v[32:35], v[112:115]
ds_write_b128 v94, v[64:67] offset:0
v_cvt_pk_bf16_f32 v68, v24, v25
mav 0,16,36
R 48,4096
v_cvt_pk_bf16_f32 v69, v26, v27
mav 4,20,36
v_cvt_pk_bf16_f32 v70, v28, v29
v_cvt_pk_bf16_f32 v71, v30, v31
mav 8,24,36
ds_write_b128 v95, v[68:71] offset:0
mav 12,28,36
buffer_load_dwordx4 v[148:151], v88, s[36:39], s49 offen sc1
s_waitcnt lgkmcnt(3)
mav 0,32,40
R 52,5120
mav 4,36,40
s_waitcnt lgkmcnt(1)
ds_read_b128 v[64:67], v96 offset:0
mav 8,40,40
ds_read_b128 v[68:71], v96 offset:1024
mav 12,44,40
buffer_load_dwordx4 v[152:155], v88, s[36:39], s49 offen offset:1024 sc1
mav 0,48,44
R 56,6144
mav 4,52,44
mav 8,56,44
mav 12,60,44
mav 0,64,48
R 60,7168
buffer_load_dwordx4 v[156:159], v89, s[36:39], s49 offen sc1
mav 4,68,48
mav 8,72,48
mav 12,76,48
s_waitcnt lgkmcnt(1)
mav 0,80,52
R 32,8192
buffer_store_dwordx4 v[64:67], v92, s[44:47], s54 offen
mav 4,84,52
mav 8,88,52
mav 12,92,52
s_add_u32 m0, s50, 0
mav 0,96,56
R 36,9216
buffer_store_dwordx4 v[68:71], v93, s[44:47], s54 offen
mav 4,100,56
mav 8,104,56
mav 12,108,56
s_waitcnt lgkmcnt(1)
mav 0,112,60
R 40,10240
buffer_load_dwordx4 v[160:163], v89, s[36:39], s49 offen offset:1024 sc1
mav 4,116,60
mav 8,120,60
mav 12,124,60
mav 0,128,32
R 44,11264
D 83,0
mav 4,132,32
mav 8,136,32
mav 12,140,32
s_waitcnt lgkmcnt(1)
mav 0,144,36
R 48,12288
D 83,1024
mav 4,148,36
mav 8,152,36
mav 12,156,36
mav 0,160,40
R 52,13312
D 83,2048
mav 4,164,40
s_sub_u32 s54, s54, 0x8000
mav 8,168,40
mav 12,172,40
s_waitcnt lgkmcnt(1)
mav 0,176,44
R 56,14336
D 83,3072
mav 4,180,44
mav 8,184,44
mav 12,188,44
mav 0,192,48
R 60,15360
buffer_load_dwordx4 v[164:167], v90, s[36:39], s49 offen sc1
mav 4,196,48
mav 8,200,48
mav 12,204,48
s_waitcnt lgkmcnt(1)
mav 0,208,52
buffer_load_dwordx4 v[168:171], v90, s[36:39], s49 offen offset:1024 sc1
mav 4,212,52
mav 8,216,52
mav 12,220,52
mav 0,224,56
mav 4,228,56
mav 8,232,56
mav 12,236,56
s_waitcnt lgkmcnt(0)
mav 0,240,60
mav 4,244,60
mav 8,248,60
mav 12,252,60
s_waitcnt vmcnt(17)
mvv 0,116,124
mvv 4,120,124
s_waitcnt vmcnt(16)
mvv 8,116,128
mvv 12,120,128
s_waitcnt vmcnt(15)
mvv 0,116,132
mvv 4,120,132
s_waitcnt vmcnt(14)
mvv 8,116,136
s_add_u32 s49, s49, 346112
mvv 12,120,136
s_barrier
s_waitcnt vmcnt(13)
mvv 0,116,140
R 32,32768
mvv 4,120,140
buffer_load_dwordx4 v[172:175], v86, s[36:39], s49 offen sc1
s_waitcnt vmcnt(13)
mvv 8,116,144
mvv 12,120,144
s_waitcnt vmcnt(12)
mvv 0,116,148
R 36,33792
mvv 4,120,148
buffer_load_dwordx4 v[176:179], v86, s[36:39], s49 offen offset:1024 sc1
s_waitcnt vmcnt(12)
mvv 8,116,152
mvv 12,120,152
s_waitcnt vmcnt(11)
mvv 0,116,156
R 40,34816
mvv 4,120,156
s_waitcnt vmcnt(8)
mvv 8,116,160
buffer_load_dwordx4 v[180:183], v87, s[36:39], s49 offen sc1
mvv 12,120,160
s_waitcnt lgkmcnt(1)
v_mfma_f32_16x16x32_bf16 v[16:19], a[0:3], v[32:35], v[100:103]
R 44,35840
v_cvt_pk_bf16_f32 v64, v0, v1
v_mfma_f32_16x16x32_bf16 v[20:23], a[4:7], v[32:35], v[104:107]
v_cvt_pk_bf16_f32 v65, v2, v3
v_cvt_pk_bf16_f32 v66, v4, v5
v_mfma_f32_16x16x32_bf16 v[24:27], a[8:11], v[32:35], v[108:111]
buffer_load_dwordx4 v[184:187], v87, s[36:39], s49 offen offset:1024 sc1
v_cvt_pk_bf16_f32 v67, v6, v7
v_mfma_f32_16x16x32_bf16 v[28:31], a[12:15], v[32:35], v[112:115]
ds_write_b128 v94, v[64:67] offset:0
v_cvt_pk_bf16_f32 v68, v8, v9
mav 16,16,36
R 48,36864
v_cvt_pk_bf16_f32 v69, v10, v11
mav 20,20,36
v_cvt_pk_bf16_f32 v70, v12, v13
v_cvt_pk_bf16_f32 v71, v14, v15
mav 24,24,36
ds_write_b128 v95, v[68:71] offset:0
mav 28,28,36
buffer_load_dwordx4 v[188:191], v88, s[36:39], s49 offen sc1
s_waitcnt lgkmcnt(3)
mav 16,32,40
R 52,37888
mav 20,36,40
s_waitcnt lgkmcnt(1)
ds_read_b128 v[64:67], v96 offset:0
mav 24,40,40
ds_read_b128 v[68:71], v96 offset:1024
mav 28,44,40
buffer_load_dwordx4 v[192:195], v88, s[36:39], s49 offen offset:1024 sc1
mav 16,48,44
R 56,38912
mav 20,52,44
mav 24,56,44
mav 28,60,44
mav 16,64,48
R 60,39936
buffer_load_dwordx4 v[196:199], v89, s[36:39], s49 offen sc1
mav 20,68,48
mav 24,72,48
mav 28,76,48
s_waitcnt lgkmcnt(1)
mav 16,80,52
R 32,40960
buffer_store_dwordx4 v[64:67], v92, s[44:47], s54 offen
mav 20,84,52
mav 24,88,52
mav 28,92,52
s_add_u32 m0, s50, 32768
mav 16,96,56
R 36,41984
buffer_store_dwordx4 v[68:71], v93, s[44:47], s54 offen
mav 20,100,56
mav 24,104,56
mav 28,108,56
s_waitcnt lgkmcnt(1)
mav 16,112,60
R 40,43008
buffer_load_dwordx4 v[200:203], v89, s[36:39], s49 offen offset:1024 sc1
mav 20,116,60
mav 24,120,60
mav 28,124,60
mav 16,128,32
R 44,44032
D 83,0
mav 20,132,32
mav 24,136,32
mav 28,140,32
s_waitcnt lgkmcnt(1)
mav 16,144,36
R 48,45056
D 83,1024
mav 20,148,36
mav 24,152,36
mav 28,156,36
mav 16,160,40
R 52,46080
D 83,2048
mav 20,164,40
s_sub_u32 s54, s54, 0x8000
mav 24,168,40
mav 28,172,40
s_waitcnt lgkmcnt(1)
mav 16,176,44
R 56,47104
D 83,3072
mav 20,180,44
mav 24,184,44
mav 28,188,44
mav 16,192,48
R 60,48128
buffer_load_dwordx4 v[124:127], v90, s[36:39], s49 offen sc1
mav 20,196,48
mav 24,200,48
mav 28,204,48
s_waitcnt lgkmcnt(1)
mav 16,208,52
buffer_load_dwordx4 v[128:131], v90, s[36:39], s49 offen offset:1024 sc1
mav 20,212,52
mav 24,216,52
mav 28,220,52
mav 16,224,56
mav 20,228,56
mav 24,232,56
mav 28,236,56
s_waitcnt lgkmcnt(0)
mav 16,240,60
mav 20,244,60
mav 24,248,60
mav 28,252,60
s_waitcnt vmcnt(17)
mvv 16,116,164
mvv 20,120,164
s_waitcnt vmcnt(16)
mvv 24,116,168
mvv 28,120,168
s_waitcnt vmcnt(15)
mvv 16,116,172
mvv 20,120,172
v_xor_b32 v80, 0x10000, v80
s_waitcnt vmcnt(14)
mvv 24,116,176
s_add_u32 s49, s49, 346112
mvv 28,120,176
s_barrier
s_waitcnt vmcnt(13)
mvv 16,116,180
R 32,0
mvv 20,120,180
buffer_load_dwordx4 v[132:135], v86, s[36:39], s49 offen sc1
s_waitcnt vmcnt(13)
mvv 24,116,184
mvv 28,120,184
s_waitcnt vmcnt(12)
mvv 16,116,188
R 36,1024
mvv 20,120,188
buffer_load_dwordx4 v[136:139], v86, s[36:39], s49 offen offset:1024 sc1
s_waitcnt vmcnt(12)
mvv 24,116,192
mvv 28,120,192
s_waitcnt vmcnt(11)
mvv 16,116,196
R 40,2048
mvv 20,120,196
s_waitcnt vmcnt(8)
mvv 24,116,200
buffer_load_dwordx4 v[140:143], v87, s[36:39], s49 offen sc1
mvv 28,120,200
s_waitcnt lgkmcnt(1)
v_mfma_f32_16x16x32_bf16 v[0:3], a[0:3], v[32:35], v[100:103]
R 44,3072
v_cvt_pk_bf16_f32 v64, v16, v17
v_mfma_f32_16x16x32_bf16 v[4:7], a[4:7], v[32:35], v[104:107]
v_cvt_pk_bf16_f32 v65, v18, v19
v_cvt_pk_bf16_f32 v66, v20, v21
v_mfma_f32_16x16x32_bf16 v[8:11], a[8:11], v[32:35], v[108:111]
buffer_load_dwordx4 v[144:147], v87, s[36:39], s49 offen offset:1024 sc1
v_cvt_pk_bf16_f32 v67, v22, v23
v_mfma_f32_16x16x32_bf16 v[12:15], a[12:15], v[32:35], v[112:115]
ds_write_b128 v94, v[64:67] offset:0
v_cvt_pk_bf16_f32 v68, v24, v25
mav 0,16,36
R 48,4096
v_cvt_pk_bf16_f32 v69, v26, v27
mav 4,20,36
v_cvt_pk_bf16_f32 v70, v28, v29
v_cvt_pk_bf16_f32 v71, v30, v31
mav 8,24,36
ds_write_b128 v95, v[68:71] offset:0
mav 12,28,36
buffer_load_dwordx4 v[148:151], v88, s[36:39], s49 offen sc1
s_waitcnt lgkmcnt(3)
mav 0,32,40
R 52,5120
mav 4,36,40
s_waitcnt lgkmcnt(1)
ds_read_b128 v[64:67], v96 offset:0
mav 8,40,40
ds_read_b128 v[68:71], v96 offset:1024
mav 12,44,40
buffer_load_dwordx4 v[152:155], v88, s[36:39], s49 offen offset:1024 sc1
mav 0,48,44
R 56,6144
mav 4,52,44
mav 8,56,44
mav 12,60,44
mav 0,64,48
R 60,7168
buffer_load_dwordx4 v[156:159], v89, s[36:39], s49 offen sc1
mav 4,68,48
mav 8,72,48
mav 12,76,48
s_waitcnt lgkmcnt(1)
mav 0,80,52
R 32,8192
buffer_store_dwordx4 v[64:67], v92, s[44:47], s54 offen
mav 4,84,52
mav 8,88,52
mav 12,92,52
mav 0,96,56
R 36,9216
buffer_store_dwordx4 v[68:71], v93, s[44:47], s54 offen
mav 4,100,56
mav 8,104,56
mav 12,108,56
s_waitcnt lgkmcnt(1)
mav 0,112,60
R 40,10240
buffer_load_dwordx4 v[160:163], v89, s[36:39], s49 offen offset:1024 sc1
mav 4,116,60
mav 8,120,60
mav 12,124,60
mav 0,128,32
R 44,11264
mav 4,132,32
mav 8,136,32
mav 12,140,32
s_waitcnt lgkmcnt(1)
mav 0,144,36
R 48,12288
mav 4,148,36
mav 8,152,36
mav 12,156,36
mav 0,160,40
R 52,13312
s_sub_u32 s54, s54, 0x8000
mav 4,164,40
mav 8,168,40
mav 12,172,40
buffer_load_dwordx4 v[164:167], v90, s[36:39], s49 offen sc1
s_waitcnt lgkmcnt(1)
mav 0,176,44
R 56,14336
mav 4,180,44
mav 8,184,44
mav 12,188,44
buffer_load_dwordx4 v[168:171], v90, s[36:39], s49 offen offset:1024 sc1
mav 0,192,48
R 60,15360
mav 4,196,48
mav 8,200,48
mav 12,204,48
s_waitcnt lgkmcnt(1)
mav 0,208,52
mav 4,212,52
mav 8,216,52
mav 12,220,52
mav 0,224,56
mav 4,228,56
mav 8,232,56
mav 12,236,56
s_waitcnt lgkmcnt(0)
mav 0,240,60
mav 4,244,60
mav 8,248,60
mav 12,252,60
s_waitcnt vmcnt(13)
mvv 0,116,124
mvv 4,120,124
s_waitcnt vmcnt(12)
mvv 8,116,128
mvv 12,120,128
s_waitcnt vmcnt(11)
mvv 0,116,132
mvv 4,120,132
s_waitcnt vmcnt(10)
mvv 8,116,136
s_add_u32 s49, s49, 346112
mvv 12,120,136
s_barrier
s_waitcnt vmcnt(9)
mvv 0,116,140
R 32,32768
mvv 4,120,140
buffer_load_dwordx4 v[172:175], v86, s[36:39], s49 offen sc1
s_waitcnt vmcnt(9)
mvv 8,116,144
mvv 12,120,144
s_waitcnt vmcnt(8)
mvv 0,116,148
R 36,33792
mvv 4,120,148
buffer_load_dwordx4 v[176:179], v86, s[36:39], s49 offen offset:1024 sc1
s_waitcnt vmcnt(8)
mvv 8,116,152
mvv 12,120,152
s_waitcnt vmcnt(7)
mvv 0,116,156
R 40,34816
mvv 4,120,156
s_waitcnt vmcnt(4)
mvv 8,116,160
buffer_load_dwordx4 v[180:183], v87, s[36:39], s49 offen sc1
mvv 12,120,160
s_waitcnt lgkmcnt(1)
v_mfma_f32_16x16x32_bf16 v[16:19], a[0:3], v[32:35], v[100:103]
R 44,35840
v_cvt_pk_bf16_f32 v64, v0, v1
v_mfma_f32_16x16x32_bf16 v[20:23], a[4:7], v[32:35], v[104:107]
v_cvt_pk_bf16_f32 v65, v2, v3
v_cvt_pk_bf16_f32 v66, v4, v5
v_mfma_f32_16x16x32_bf16 v[24:27], a[8:11], v[32:35], v[108:111]
buffer_load_dwordx4 v[184:187], v87, s[36:39], s49 offen offset:1024 sc1
v_cvt_pk_bf16_f32 v67, v6, v7
v_mfma_f32_16x16x32_bf16 v[28:31], a[12:15], v[32:35], v[112:115]
ds_write_b128 v94, v[64:67] offset:0
v_cvt_pk_bf16_f32 v68, v8, v9
mav 16,16,36
R 48,36864
v_cvt_pk_bf16_f32 v69, v10, v11
mav 20,20,36
v_cvt_pk_bf16_f32 v70, v12, v13
v_cvt_pk_bf16_f32 v71, v14, v15
mav 24,24,36
ds_write_b128 v95, v[68:71] offset:0
mav 28,28,36
buffer_load_dwordx4 v[188:191], v88, s[36:39], s49 offen sc1
s_waitcnt lgkmcnt(3)
mav 16,32,40
R 52,37888
mav 20,36,40
s_waitcnt lgkmcnt(1)
ds_read_b128 v[64:67], v96 offset:0
mav 24,40,40
ds_read_b128 v[68:71], v96 offset:1024
mav 28,44,40
buffer_load_dwordx4 v[192:195], v88, s[36:39], s49 offen offset:1024 sc1
mav 16,48,44
R 56,38912
mav 20,52,44
mav 24,56,44
mav 28,60,44
mav 16,64,48
R 60,39936
buffer_load_dwordx4 v[196:199], v89, s[36:39], s49 offen sc1
mav 20,68,48
mav 24,72,48
mav 28,76,48
s_waitcnt lgkmcnt(1)
mav 16,80,52
R 32,40960
buffer_store_dwordx4 v[64:67], v92, s[44:47], s54 offen
mav 20,84,52
mav 24,88,52
mav 28,92,52
mav 16,96,56
R 36,41984
buffer_store_dwordx4 v[68:71], v93, s[44:47], s54 offen
mav 20,100,56
mav 24,104,56
mav 28,108,56
s_waitcnt lgkmcnt(1)
mav 16,112,60
R 40,43008
buffer_load_dwordx4 v[200:203], v89, s[36:39], s49 offen offset:1024 sc1
mav 20,116,60
mav 24,120,60
mav 28,124,60
mav 16,128,32
R 44,44032
mav 20,132,32
mav 24,136,32
mav 28,140,32
s_waitcnt lgkmcnt(1)
mav 16,144,36
R 48,45056
mav 20,148,36
mav 24,152,36
mav 28,156,36
mav 16,160,40
R 52,46080
s_sub_u32 s54, s54, 0x8000
mav 20,164,40
mav 24,168,40
mav 28,172,40
s_waitcnt lgkmcnt(1)
mav 16,176,44
R 56,47104
mav 20,180,44
mav 24,184,44
mav 28,188,44
mav 16,192,48
R 60,48128
mav 20,196,48
mav 24,200,48
mav 28,204,48
s_waitcnt lgkmcnt(1)
mav 16,208,52
mav 20,212,52
mav 24,216,52
mav 28,220,52
mav 16,224,56
mav 20,228,56
mav 24,232,56
mav 28,236,56
s_waitcnt lgkmcnt(0)
mav 16,240,60
mav 20,244,60
mav 24,248,60
mav 28,252,60
s_waitcnt vmcnt(11)
mvv 16,116,164
mvv 20,120,164
s_waitcnt vmcnt(10)
mvv 24,116,168
mvv 28,120,168
s_waitcnt vmcnt(9)
mvv 16,116,172
mvv 20,120,172
s_waitcnt vmcnt(8)
mvv 24,116,176
mvv 28,120,176
s_waitcnt vmcnt(7)
mvv 16,116,180
mvv 20,120,180
s_waitcnt vmcnt(6)
mvv 24,116,184
mvv 28,120,184
s_waitcnt vmcnt(5)
mvv 16,116,188
mvv 20,120,188
s_waitcnt vmcnt(4)
mvv 24,116,192
mvv 28,120,192
s_waitcnt vmcnt(3)
mvv 16,116,196
mvv 20,120,196
s_waitcnt vmcnt(0)
mvv 24,116,200
mvv 28,120,200
s_nop 3
v_cvt_pk_bf16_f32 v64, v16, v17
v_cvt_pk_bf16_f32 v65, v18, v19
v_cvt_pk_bf16_f32 v66, v20, v21
v_cvt_pk_bf16_f32 v67, v22, v23
ds_write_b128 v94, v[64:67] offset:0
v_cvt_pk_bf16_f32 v68, v24, v25
v_cvt_pk_bf16_f32 v69, v26, v27
v_cvt_pk_bf16_f32 v70, v28, v29
v_cvt_pk_bf16_f32 v71, v30, v31
ds_write_b128 v95, v[68:71] offset:0
s_waitcnt lgkmcnt(0)
ds_read_b128 v[64:67], v96 offset:0
ds_read_b128 v[68:71], v96 offset:1024
s_waitcnt lgkmcnt(0)
buffer_store_dwordx4 v[64:67], v92, s[44:47], s54 offen
buffer_store_dwordx4 v[68:71], v93, s[44:47], s54 offen
s_endpgm
LvR2:
s_add_u32 s49, s49, 346112
s_barrier
s_add_u32 m0, s50, 0
s_nop 0
D 83,0
D 83,1024
D 83,2048
D 83,3072
s_add_u32 s49, s49, 346112
s_barrier
s_add_u32 m0, s50, 32768
s_nop 0
D 83,0
D 83,1024
D 83,2048
D 83,3072
buffer_load_dwordx4 v[124:127], v90, s[36:39], s49 offen sc1
buffer_load_dwordx4 v[128:131], v90, s[36:39], s49 offen offset:1024 sc1
s_xor_b32 s50, s50, 0x10000
s_add_u32 s49, s49, 346112
s_waitcnt vmcnt(6)
s_barrier
R 32,0
buffer_load_dwordx4 v[132:135], v86, s[36:39], s49 offen sc1
R 36,1024
buffer_load_dwordx4 v[136:139], v86, s[36:39], s49 offen offset:1024 sc1
R 40,2048
buffer_load_dwordx4 v[140:143], v87, s[36:39], s49 offen sc1
s_waitcnt lgkmcnt(1)
v_mfma_f32_16x16x32_bf16 v[0:3], a[0:3], v[32:35], v[100:103]
R 44,3072
v_mfma_f32_16x16x32_bf16 v[4:7], a[4:7], v[32:35], v[104:107]
v_mfma_f32_16x16x32_bf16 v[8:11], a[8:11], v[32:35], v[108:111]
buffer_load_dwordx4 v[144:147], v87, s[36:39], s49 offen offset:1024 sc1
v_mfma_f32_16x16x32_bf16 v[12:15], a[12:15], v[32:35], v[112:115]
mav 0,16,36
R 48,4096
mav 4,20,36
mav 8,24,36
mav 12,28,36
buffer_load_dwordx4 v[148:151], v88, s[36:39], s49 offen sc1
s_waitcnt lgkmcnt(1)
mav 0,32,40
R 52,5120
mav 4,36,40
mav 8,40,40
mav 12,44,40
buffer_load_dwordx4 v[152:155], v88, s[36:39], s49 offen offset:1024 sc1
mav 0,48,44
R 56,6144
mav 4,52,44
mav 8,56,44
mav 12,60,44
s_waitcnt lgkmcnt(1)
mav 0,64,48
R 60,7168
buffer_load_dwordx4 v[156:159], v89, s[36:39], s49 offen sc1
mav 4,68,48
mav 8,72,48
mav 12,76,48
mav 0,80,52
R 32,8192
buffer_load_dwordx4 v[160:163], v89, s[36:39], s49 offen offset:1024 sc1
mav 4,84,52
mav 8,88,52
mav 12,92,52
s_add_u32 m0, s50, 0
s_waitcnt lgkmcnt(1)
mav 0,96,56
R 36,9216
mav 4,100,56
D 83,0
mav 8,104,56
mav 12,108,56
mav 0,112,60
R 40,10240
mav 4,116,60
D 83,1024
mav 8,120,60
mav 12,124,60
s_waitcnt lgkmcnt(1)
mav 0,128,32
R 44,11264
mav 4,132,32
mav 8,136,32
D 83,2048
mav 12,140,32
mav 0,144,36
R 48,12288
mav 4,148,36
mav 8,152,36
D 83,3072
mav 12,156,36
s_waitcnt lgkmcnt(1)
mav 0,160,40
R 52,13312
mav 4,164,40
mav 8,168,40
mav 12,172,40
buffer_load_dwordx4 v[164:167], v90, s[36:39], s49 offen sc1
mav 0,176,44
R 56,14336
mav 4,180,44
mav 8,184,44
mav 12,188,44
buffer_load_dwordx4 v[168:171], v90, s[36:39], s49 offen offset:1024 sc1
s_waitcnt lgkmcnt(1)
mav 0,192,48
R 60,15360
mav 4,196,48
mav 8,200,48
mav 12,204,48
mav 0,208,52
mav 4,212,52
mav 8,216,52
mav 12,220,52
s_waitcnt lgkmcnt(0)
mav 0,224,56
mav 4,228,56
mav 8,232,56
mav 12,236,56
mav 0,240,60
mav 4,244,60
mav 8,248,60
mav 12,252,60
s_waitcnt vmcnt(15)
mvv 0,116,124
mvv 4,120,124
s_waitcnt vmcnt(14)
mvv 8,116,128
mvv 12,120,128
s_waitcnt vmcnt(13)
mvv 0,116,132
mvv 4,120,132
s_waitcnt vmcnt(12)
mvv 8,116,136
s_add_u32 s49, s49, 346112
mvv 12,120,136
s_barrier
s_waitcnt vmcnt(11)
mvv 0,116,140
R 32,32768
mvv 4,120,140
buffer_load_dwordx4 v[172:175], v86, s[36:39], s49 offen sc1
s_waitcnt vmcnt(11)
mvv 8,116,144
mvv 12,120,144
s_waitcnt vmcnt(10)
mvv 0,116,148
R 36,33792
mvv 4,120,148
buffer_load_dwordx4 v[176:179], v86, s[36:39], s49 offen offset:1024 sc1
s_waitcnt vmcnt(10)
mvv 8,116,152
mvv 12,120,152
s_waitcnt vmcnt(9)
mvv 0,116,156
R 40,34816
mvv 4,120,156
s_waitcnt vmcnt(8)
mvv 8,116,160
buffer_load_dwordx4 v[180:183], v87, s[36:39], s49 offen sc1
mvv 12,120,160
s_waitcnt lgkmcnt(1)
v_mfma_f32_16x16x32_bf16 v[16:19], a[0:3], v[32:35], v[100:103]
R 44,35840
v_cvt_pk_bf16_f32 v64, v0, v1
v_mfma_f32_16x16x32_bf16 v[20:23], a[4:7], v[32:35], v[104:107]
v_cvt_pk_bf16_f32 v65, v2, v3
v_cvt_pk_bf16_f32 v66, v4, v5
v_mfma_f32_16x16x32_bf16 v[24:27], a[8:11], v[32:35], v[108:111]
buffer_load_dwordx4 v[184:187], v87, s[36:39], s49 offen offset:1024 sc1
v_cvt_pk_bf16_f32 v67, v6, v7
v_mfma_f32_16x16x32_bf16 v[28:31], a[12:15], v[32:35], v[112:115]
v_cvt_pk_bf16_f32 v68, v8, v9
v_cvt_pk_bf16_f32 v69, v10, v11
mav 16,16,36
R 48,36864
v_cvt_pk_bf16_f32 v70, v12, v13
mav 20,20,36
v_cvt_pk_bf16_f32 v71, v14, v15
v_mul_f32 v75, v0, v0
mav 24,24,36
buffer_store_dwordx4 v[64:67], v91, s[36:39], s49 offen sc1
v_add_f32 v74, v1, v0
mav 28,28,36
v_fmac_f32 v75, v1, v1
s_waitcnt lgkmcnt(1)
mav 16,32,40
R 52,37888
v_add_f32 v74, v2, v74
mav 20,36,40
v_fmac_f32 v75, v2, v2
v_add_f32 v74, v3, v74
mav 24,40,40
buffer_store_dwordx4 v[68:71], v91, s[36:39], s49 offen offset:1024 sc1
v_fmac_f32 v75, v3, v3
mav 28,44,40
v_add_f32 v74, v4, v74
v_fmac_f32 v75, v4, v4
mav 16,48,44
R 56,38912
v_add_f32 v74, v5, v74
mav 20,52,44
v_fmac_f32 v75, v5, v5
v_add_f32 v74, v6, v74
mav 24,56,44
buffer_load_dwordx4 v[188:191], v88, s[36:39], s49 offen sc1
v_fmac_f32 v75, v6, v6
mav 28,60,44
v_add_f32 v74, v7, v74
s_waitcnt lgkmcnt(1)
mav 16,64,48
R 60,39936
v_fmac_f32 v75, v7, v7
mav 20,68,48
v_add_f32 v74, v8, v74
v_fmac_f32 v75, v8, v8
mav 24,72,48
buffer_load_dwordx4 v[192:195], v88, s[36:39], s49 offen offset:1024 sc1
v_add_f32 v74, v9, v74
mav 28,76,48
v_fmac_f32 v75, v9, v9
v_add_f32 v74, v10, v74
mav 16,80,52
R 32,40960
v_fmac_f32 v75, v10, v10
mav 20,84,52
v_add_f32 v74, v11, v74
v_fmac_f32 v75, v11, v11
mav 24,88,52
buffer_load_dwordx4 v[196:199], v89, s[36:39], s49 offen sc1
v_add_f32 v74, v12, v74
mav 28,92,52
s_add_u32 m0, s50, 32768
s_waitcnt lgkmcnt(1)
mav 16,96,56
R 36,41984
v_fmac_f32 v75, v12, v12
mav 20,100,56
v_add_f32 v74, v13, v74
v_fmac_f32 v75, v13, v13
mav 24,104,56
buffer_load_dwordx4 v[200:203], v89, s[36:39], s49 offen offset:1024 sc1
v_add_f32 v74, v14, v74
mav 28,108,56
v_fmac_f32 v75, v14, v14
v_add_f32 v74, v15, v74
mav 16,112,60
R 40,43008
v_fmac_f32 v75, v15, v15
mav 20,116,60
mav 24,120,60
D 83,0
v_permlane32_swap_b32 v74, v75
mav 28,124,60
v_add_f32 v74, v74, v75
s_waitcnt lgkmcnt(1)
mav 16,128,32
R 44,44032
ds_write_b32 v97, v74 offset:0
mav 20,132,32
mav 24,136,32
D 83,1024
mav 28,140,32
mav 16,144,36
R 48,45056
mav 20,148,36
mav 24,152,36
D 83,2048
mav 28,156,36
s_waitcnt lgkmcnt(1)
mav 16,160,40
R 52,46080
mav 20,164,40
mav 24,168,40
D 83,3072
mav 28,172,40
mav 16,176,44
R 56,47104
mav 20,180,44
mav 24,184,44
buffer_load_dwordx4 v[124:127], v90, s[36:39], s49 offen sc1
mav 28,188,44
s_waitcnt lgkmcnt(1)
mav 16,192,48
R 60,48128
mav 20,196,48
mav 24,200,48
buffer_load_dwordx4 v[128:131], v90, s[36:39], s49 offen offset:1024 sc1
mav 28,204,48
mav 16,208,52
mav 20,212,52
mav 24,216,52
v_add_u32 v72, 1, v72
mav 28,220,52
s_waitcnt lgkmcnt(0)
mav 16,224,56
mav 20,228,56
mav 24,232,56
mav 28,236,56
mav 16,240,60
s_waitcnt vmcnt(10)
buffer_store_dwordx2 v[72:73], v81, s[36:39], s52 offen sc1
mav 20,244,60
mav 24,248,60
mav 28,252,60
mvv 16,116,164
mvv 20,120,164
mvv 24,116,168
s_xor_b32 s50, s50, 0x10000
mvv 28,120,168
mvv 16,116,172
mvv 20,120,172
v_xor_b32 v80, 0x10000, v80
mvv 24,116,176
s_add_u32 s49, s49, 346112
mvv 28,120,176
s_barrier
mvv 16,116,180
R 32,0
mvv 20,120,180
buffer_load_dwordx4 v[132:135], v86, s[36:39], s49 offen sc1
mvv 24,116,184
mvv 28,120,184
s_waitcnt vmcnt(11)
mvv 16,116,188
R 36,1024
mvv 20,120,188
buffer_load_dwordx4 v[136:139], v86, s[36:39], s49 offen offset:1024 sc1
s_waitcnt vmcnt(11)
mvv 24,116,192
mvv 28,120,192
s_waitcnt vmcnt(10)
mvv 16,116,196
R 40,2048
mvv 20,120,196
s_waitcnt vmcnt(9)
mvv 24,116,200
buffer_load_dwordx4 v[140:143], v87, s[36:39], s49 offen sc1
mvv 28,120,200
s_mov_b32 s60, -15
.p2align 6
LR2_1:
s_waitcnt lgkmcnt(1)
v_mfma_f32_16x16x32_bf16 v[0:3], a[0:3], v[32:35], v[100:103]
R 44,3072
v_cvt_pk_bf16_f32 v64, v16, v17
v_mfma_f32_16x16x32_bf16 v[4:7], a[4:7], v[32:35], v[104:107]
v_cvt_pk_bf16_f32 v65, v18, v19
v_cvt_pk_bf16_f32 v66, v20, v21
v_mfma_f32_16x16x32_bf16 v[8:11], a[8:11], v[32:35], v[108:111]
buffer_load_dwordx4 v[144:147], v87, s[36:39], s49 offen offset:1024 sc1
v_cvt_pk_bf16_f32 v67, v22, v23
v_mfma_f32_16x16x32_bf16 v[12:15], a[12:15], v[32:35], v[112:115]
v_cvt_pk_bf16_f32 v68, v24, v25
v_cvt_pk_bf16_f32 v69, v26, v27
mav 0,16,36
R 48,4096
v_cvt_pk_bf16_f32 v70, v28, v29
mav 4,20,36
v_cvt_pk_bf16_f32 v71, v30, v31
v_mul_f32 v75, v16, v16
mav 8,24,36
buffer_store_dwordx4 v[64:67], v91, s[36:39], s49 offen sc1
v_add_f32 v74, v17, v16
mav 12,28,36
v_fmac_f32 v75, v17, v17
s_waitcnt lgkmcnt(1)
mav 0,32,40
R 52,5120
v_add_f32 v74, v18, v74
mav 4,36,40
v_fmac_f32 v75, v18, v18
v_add_f32 v74, v19, v74
mav 8,40,40
buffer_store_dwordx4 v[68:71], v91, s[36:39], s49 offen offset:1024 sc1
v_fmac_f32 v75, v19, v19
mav 12,44,40
v_add_f32 v74, v20, v74
v_fmac_f32 v75, v20, v20
mav 0,48,44
R 56,6144
v_add_f32 v74, v21, v74
mav 4,52,44
v_fmac_f32 v75, v21, v21
v_add_f32 v74, v22, v74
mav 8,56,44
buffer_load_dwordx4 v[148:151], v88, s[36:39], s49 offen sc1
v_fmac_f32 v75, v22, v22
mav 12,60,44
v_add_f32 v74, v23, v74
s_waitcnt lgkmcnt(1)
mav 0,64,48
R 60,7168
v_fmac_f32 v75, v23, v23
mav 4,68,48
v_add_f32 v74, v24, v74
v_fmac_f32 v75, v24, v24
mav 8,72,48
buffer_load_dwordx4 v[152:155], v88, s[36:39], s49 offen offset:1024 sc1
v_add_f32 v74, v25, v74
mav 12,76,48
v_fmac_f32 v75, v25, v25
v_add_f32 v74, v26, v74
mav 0,80,52
R 32,8192
v_fmac_f32 v75, v26, v26
mav 4,84,52
v_add_f32 v74, v27, v74
v_fmac_f32 v75, v27, v27
mav 8,88,52
buffer_load_dwordx4 v[156:159], v89, s[36:39], s49 offen sc1
v_add_f32 v74, v28, v74
mav 12,92,52
s_add_u32 m0, s50, 0
s_waitcnt lgkmcnt(1)
mav 0,96,56
R 36,9216
v_fmac_f32 v75, v28, v28
mav 4,100,56
v_add_f32 v74, v29, v74
v_fmac_f32 v75, v29, v29
mav 8,104,56
buffer_load_dwordx4 v[160:163], v89, s[36:39], s49 offen offset:1024 sc1
v_add_f32 v74, v30, v74
mav 12,108,56
v_fmac_f32 v75, v30, v30
v_add_f32 v74, v31, v74
mav 0,112,60
R 40,10240
v_fmac_f32 v75, v31, v31
mav 4,116,60
mav 8,120,60
D 83,0
v_permlane32_swap_b32 v74, v75
mav 12,124,60
v_add_f32 v74, v74, v75
s_waitcnt lgkmcnt(1)
mav 0,128,32
R 44,11264
ds_write_b32 v97, v74 offset:1024
mav 4,132,32
mav 8,136,32
D 83,1024
mav 12,140,32
mav 0,144,36
R 48,12288
mav 4,148,36
mav 8,152,36
D 83,2048
mav 12,156,36
s_waitcnt lgkmcnt(1)
mav 0,160,40
R 52,13312
mav 4,164,40
mav 8,168,40
D 83,3072
mav 12,172,40
mav 0,176,44
R 56,14336
mav 4,180,44
mav 8,184,44
buffer_load_dwordx4 v[164:167], v90, s[36:39], s49 offen sc1
mav 12,188,44
s_waitcnt lgkmcnt(1)
mav 0,192,48
R 60,15360
mav 4,196,48
mav 8,200,48
buffer_load_dwordx4 v[168:171], v90, s[36:39], s49 offen offset:1024 sc1
mav 12,204,48
mav 0,208,52
mav 4,212,52
mav 8,216,52
v_add_u32 v72, 1, v72
mav 12,220,52
s_waitcnt lgkmcnt(0)
mav 0,224,56
mav 4,228,56
mav 8,232,56
mav 12,236,56
mav 0,240,60
s_waitcnt vmcnt(10)
buffer_store_dwordx2 v[72:73], v81, s[36:39], s52 offen sc1
mav 4,244,60
mav 8,248,60
mav 12,252,60
mvv 0,116,124
mvv 4,120,124
mvv 8,116,128
mvv 12,120,128
mvv 0,116,132
mvv 4,120,132
mvv 8,116,136
s_add_u32 s49, s49, 346112
mvv 12,120,136
s_barrier
mvv 0,116,140
R 32,32768
mvv 4,120,140
buffer_load_dwordx4 v[172:175], v86, s[36:39], s49 offen sc1
mvv 8,116,144
mvv 12,120,144
s_waitcnt vmcnt(11)
mvv 0,116,148
R 36,33792
mvv 4,120,148
buffer_load_dwordx4 v[176:179], v86, s[36:39], s49 offen offset:1024 sc1
s_waitcnt vmcnt(11)
mvv 8,116,152
mvv 12,120,152
s_waitcnt vmcnt(10)
mvv 0,116,156
R 40,34816
mvv 4,120,156
s_waitcnt vmcnt(9)
mvv 8,116,160
buffer_load_dwordx4 v[180:183], v87, s[36:39], s49 offen sc1
mvv 12,120,160
s_waitcnt lgkmcnt(1)
v_mfma_f32_16x16x32_bf16 v[16:19], a[0:3], v[32:35], v[100:103]
R 44,35840
v_cvt_pk_bf16_f32 v64, v0, v1
v_mfma_f32_16x16x32_bf16 v[20:23], a[4:7], v[32:35], v[104:107]
v_cvt_pk_bf16_f32 v65, v2, v3
v_cvt_pk_bf16_f32 v66, v4, v5
v_mfma_f32_16x16x32_bf16 v[24:27], a[8:11], v[32:35], v[108:111]
buffer_load_dwordx4 v[184:187], v87, s[36:39], s49 offen offset:1024 sc1
v_cvt_pk_bf16_f32 v67, v6, v7
v_mfma_f32_16x16x32_bf16 v[28:31], a[12:15], v[32:35], v[112:115]
v_cvt_pk_bf16_f32 v68, v8, v9
v_cvt_pk_bf16_f32 v69, v10, v11
mav 16,16,36
R 48,36864
v_cvt_pk_bf16_f32 v70, v12, v13
mav 20,20,36
v_cvt_pk_bf16_f32 v71, v14, v15
v_mul_f32 v75, v0, v0
mav 24,24,36
buffer_store_dwordx4 v[64:67], v91, s[36:39], s49 offen sc1
v_add_f32 v74, v1, v0
mav 28,28,36
v_fmac_f32 v75, v1, v1
s_waitcnt lgkmcnt(1)
mav 16,32,40
R 52,37888
v_add_f32 v74, v2, v74
mav 20,36,40
v_fmac_f32 v75, v2, v2
v_add_f32 v74, v3, v74
mav 24,40,40
buffer_store_dwordx4 v[68:71], v91, s[36:39], s49 offen offset:1024 sc1
v_fmac_f32 v75, v3, v3
mav 28,44,40
v_add_f32 v74, v4, v74
v_fmac_f32 v75, v4, v4
mav 16,48,44
R 56,38912
v_add_f32 v74, v5, v74
mav 20,52,44
v_fmac_f32 v75, v5, v5
v_add_f32 v74, v6, v74
mav 24,56,44
buffer_load_dwordx4 v[188:191], v88, s[36:39], s49 offen sc1
v_fmac_f32 v75, v6, v6
mav 28,60,44
v_add_f32 v74, v7, v74
s_waitcnt lgkmcnt(1)
mav 16,64,48
R 60,39936
v_fmac_f32 v75, v7, v7
mav 20,68,48
v_add_f32 v74, v8, v74
v_fmac_f32 v75, v8, v8
mav 24,72,48
buffer_load_dwordx4 v[192:195], v88, s[36:39], s49 offen offset:1024 sc1
v_add_f32 v74, v9, v74
mav 28,76,48
v_fmac_f32 v75, v9, v9
v_add_f32 v74, v10, v74
mav 16,80,52
R 32,40960
v_fmac_f32 v75, v10, v10
mav 20,84,52
v_add_f32 v74, v11, v74
v_fmac_f32 v75, v11, v11
mav 24,88,52
buffer_load_dwordx4 v[196:199], v89, s[36:39], s49 offen sc1
v_add_f32 v74, v12, v74
mav 28,92,52
s_add_u32 m0, s50, 32768
s_waitcnt lgkmcnt(1)
mav 16,96,56
R 36,41984
v_fmac_f32 v75, v12, v12
mav 20,100,56
v_add_f32 v74, v13, v74
v_fmac_f32 v75, v13, v13
mav 24,104,56
buffer_load_dwordx4 v[200:203], v89, s[36:39], s49 offen offset:1024 sc1
v_add_f32 v74, v14, v74
mav 28,108,56
v_fmac_f32 v75, v14, v14
v_add_f32 v74, v15, v74
mav 16,112,60
R 40,43008
v_fmac_f32 v75, v15, v15
mav 20,116,60
mav 24,120,60
D 83,0
v_permlane32_swap_b32 v74, v75
mav 28,124,60
v_add_f32 v74, v74, v75
s_waitcnt lgkmcnt(1)
mav 16,128,32
R 44,44032
ds_write_b32 v97, v74 offset:0
mav 20,132,32
mav 24,136,32
D 83,1024
mav 28,140,32
mav 16,144,36
R 48,45056
mav 20,148,36
mav 24,152,36
D 83,2048
mav 28,156,36
s_waitcnt lgkmcnt(1)
mav 16,160,40
R 52,46080
mav 20,164,40
mav 24,168,40
D 83,3072
mav 28,172,40
mav 16,176,44
R 56,47104
mav 20,180,44
mav 24,184,44
buffer_load_dwordx4 v[124:127], v90, s[36:39], s49 offen sc1
mav 28,188,44
s_waitcnt lgkmcnt(1)
mav 16,192,48
R 60,48128
mav 20,196,48
mav 24,200,48
buffer_load_dwordx4 v[128:131], v90, s[36:39], s49 offen offset:1024 sc1
mav 28,204,48
mav 16,208,52
mav 20,212,52
mav 24,216,52
v_add_u32 v72, 1, v72
mav 28,220,52
s_waitcnt lgkmcnt(0)
mav 16,224,56
mav 20,228,56
mav 24,232,56
mav 28,236,56
mav 16,240,60
s_waitcnt vmcnt(10)
buffer_store_dwordx2 v[72:73], v81, s[36:39], s52 offen sc1
mav 20,244,60
mav 24,248,60
mav 28,252,60
mvv 16,116,164
mvv 20,120,164
mvv 24,116,168
s_xor_b32 s50, s50, 0x10000
mvv 28,120,168
mvv 16,116,172
mvv 20,120,172
v_xor_b32 v80, 0x10000, v80
mvv 24,116,176
s_add_u32 s49, s49, 346112
mvv 28,120,176
s_barrier
mvv 16,116,180
R 32,0
mvv 20,120,180
buffer_load_dwordx4 v[132:135], v86, s[36:39], s49 offen sc1
mvv 24,116,184
mvv 28,120,184
s_waitcnt vmcnt(11)
mvv 16,116,188
R 36,1024
mvv 20,120,188
buffer_load_dwordx4 v[136:139], v86, s[36:39], s49 offen offset:1024 sc1
s_waitcnt vmcnt(11)
mvv 24,116,192
mvv 28,120,192
s_waitcnt vmcnt(10)
mvv 16,116,196
R 40,2048
mvv 20,120,196
s_waitcnt vmcnt(9)
mvv 24,116,200
buffer_load_dwordx4 v[140:143], v87, s[36:39], s49 offen sc1
mvv 28,120,200
s_add_u32 s60, s60, 1
s_cbranch_scc0 LR2_1
s_waitcnt lgkmcnt(1)
v_mfma_f32_16x16x32_bf16 v[0:3], a[0:3], v[32:35], v[100:103]
R 44,3072
v_cvt_pk_bf16_f32 v64, v16, v17
v_mfma_f32_16x16x32_bf16 v[4:7], a[4:7], v[32:35], v[104:107]
v_cvt_pk_bf16_f32 v65, v18, v19
v_cvt_pk_bf16_f32 v66, v20, v21
v_mfma_f32_16x16x32_bf16 v[8:11], a[8:11], v[32:35], v[108:111]
buffer_load_dwordx4 v[144:147], v87, s[36:39], s49 offen offset:1024 sc1
v_cvt_pk_bf16_f32 v67, v22, v23
v_mfma_f32_16x16x32_bf16 v[12:15], a[12:15], v[32:35], v[112:115]
v_cvt_pk_bf16_f32 v68, v24, v25
v_cvt_pk_bf16_f32 v69, v26, v27
mav 0,16,36
R 48,4096
v_cvt_pk_bf16_f32 v70, v28, v29
mav 4,20,36
v_cvt_pk_bf16_f32 v71, v30, v31
v_mul_f32 v75, v16, v16
mav 8,24,36
buffer_store_dwordx4 v[64:67], v91, s[36:39], s49 offen sc1
v_add_f32 v74, v17, v16
mav 12,28,36
v_fmac_f32 v75, v17, v17
s_waitcnt lgkmcnt(1)
mav 0,32,40
R 52,5120
v_add_f32 v74, v18, v74
mav 4,36,40
v_fmac_f32 v75, v18, v18
v_add_f32 v74, v19, v74
mav 8,40,40
buffer_store_dwordx4 v[68:71], v91, s[36:39], s49 offen offset:1024 sc1
v_fmac_f32 v75, v19, v19
mav 12,44,40
v_add_f32 v74, v20, v74
v_fmac_f32 v75, v20, v20
mav 0,48,44
R 56,6144
v_add_f32 v74, v21, v74
mav 4,52,44
v_fmac_f32 v75, v21, v21
v_add_f32 v74, v22, v74
mav 8,56,44
buffer_load_dwordx4 v[148:151], v88, s[36:39], s49 offen sc1
v_fmac_f32 v75, v22, v22
mav 12,60,44
v_add_f32 v74, v23, v74
s_waitcnt lgkmcnt(1)
mav 0,64,48
R 60,7168
v_fmac_f32 v75, v23, v23
mav 4,68,48
v_add_f32 v74, v24, v74
v_fmac_f32 v75, v24, v24
mav 8,72,48
buffer_load_dwordx4 v[152:155], v88, s[36:39], s49 offen offset:1024 sc1
v_add_f32 v74, v25, v74
mav 12,76,48
v_fmac_f32 v75, v25, v25
v_add_f32 v74, v26, v74
mav 0,80,52
R 32,8192
v_fmac_f32 v75, v26, v26
mav 4,84,52
v_add_f32 v74, v27, v74
v_fmac_f32 v75, v27, v27
mav 8,88,52
buffer_load_dwordx4 v[156:159], v89, s[36:39], s49 offen sc1
v_add_f32 v74, v28, v74
mav 12,92,52
s_add_u32 m0, s50, 0
s_waitcnt lgkmcnt(1)
mav 0,96,56
R 36,9216
v_fmac_f32 v75, v28, v28
mav 4,100,56
v_add_f32 v74, v29, v74
v_fmac_f32 v75, v29, v29
mav 8,104,56
buffer_load_dwordx4 v[160:163], v89, s[36:39], s49 offen offset:1024 sc1
v_add_f32 v74, v30, v74
mav 12,108,56
v_fmac_f32 v75, v30, v30
v_add_f32 v74, v31, v74
mav 0,112,60
R 40,10240
v_fmac_f32 v75, v31, v31
mav 4,116,60
mav 8,120,60
D 83,0
v_permlane32_swap_b32 v74, v75
mav 12,124,60
v_add_f32 v74, v74, v75
s_waitcnt lgkmcnt(1)
mav 0,128,32
R 44,11264
ds_write_b32 v97, v74 offset:1024
mav 4,132,32
mav 8,136,32
D 83,1024
mav 12,140,32
mav 0,144,36
R 48,12288
mav 4,148,36
mav 8,152,36
D 83,2048
mav 12,156,36
s_waitcnt lgkmcnt(1)
mav 0,160,40
R 52,13312
mav 4,164,40
mav 8,168,40
D 83,3072
mav 12,172,40
mav 0,176,44
R 56,14336
mav 4,180,44
mav 8,184,44
buffer_load_dwordx4 v[164:167], v90, s[36:39], s49 offen sc1
mav 12,188,44
s_waitcnt lgkmcnt(1)
mav 0,192,48
R 60,15360
mav 4,196,48
mav 8,200,48
buffer_load_dwordx4 v[168:171], v90, s[36:39], s49 offen offset:1024 sc1
mav 12,204,48
mav 0,208,52
mav 4,212,52
mav 8,216,52
v_add_u32 v72, 69, v72
mav 12,220,52
s_waitcnt lgkmcnt(0)
mav 0,224,56
mav 4,228,56
mav 8,232,56
mav 12,236,56
mav 0,240,60
s_waitcnt vmcnt(10)
buffer_store_dwordx2 v[72:73], v81, s[36:39], s52 offen sc1
mav 4,244,60
mav 8,248,60
mav 12,252,60
mvv 0,116,124
mvv 4,120,124
mvv 8,116,128
mvv 12,120,128
mvv 0,116,132
mvv 4,120,132
mvv 8,116,136
s_add_u32 s49, s49, 346112
mvv 12,120,136
s_barrier
mvv 0,116,140
R 32,32768
mvv 4,120,140
buffer_load_dwordx4 v[172:175], v86, s[36:39], s49 offen sc1
mvv 8,116,144
mvv 12,120,144
s_waitcnt vmcnt(11)
mvv 0,116,148
R 36,33792
mvv 4,120,148
buffer_load_dwordx4 v[176:179], v86, s[36:39], s49 offen offset:1024 sc1
s_waitcnt vmcnt(11)
mvv 8,116,152
mvv 12,120,152
s_waitcnt vmcnt(10)
mvv 0,116,156
R 40,34816
mvv 4,120,156
s_waitcnt vmcnt(9)
mvv 8,116,160
buffer_load_dwordx4 v[180:183], v87, s[36:39], s49 offen sc1
mvv 12,120,160
s_waitcnt lgkmcnt(1)
v_mfma_f32_16x16x32_bf16 v[16:19], a[0:3], v[32:35], v[100:103]
R 44,35840
v_cvt_pk_bf16_f32 v64, v0, v1
v_mfma_f32_16x16x32_bf16 v[20:23], a[4:7], v[32:35], v[104:107]
v_cvt_pk_bf16_f32 v65, v2, v3
v_cvt_pk_bf16_f32 v66, v4, v5
v_mfma_f32_16x16x32_bf16 v[24:27], a[8:11], v[32:35], v[108:111]
buffer_load_dwordx4 v[184:187], v87, s[36:39], s49 offen offset:1024 sc1
v_cvt_pk_bf16_f32 v67, v6, v7
v_mfma_f32_16x16x32_bf16 v[28:31], a[12:15], v[32:35], v[112:115]
ds_write_b128 v94, v[64:67] offset:0
v_cvt_pk_bf16_f32 v68, v8, v9
mav 16,16,36
R 48,36864
v_cvt_pk_bf16_f32 v69, v10, v11
mav 20,20,36
v_cvt_pk_bf16_f32 v70, v12, v13
v_cvt_pk_bf16_f32 v71, v14, v15
mav 24,24,36
ds_write_b128 v95, v[68:71] offset:0
mav 28,28,36
buffer_load_dwordx4 v[188:191], v88, s[36:39], s49 offen sc1
s_waitcnt lgkmcnt(3)
mav 16,32,40
R 52,37888
mav 20,36,40
s_waitcnt lgkmcnt(1)
ds_read_b128 v[64:67], v96 offset:0
mav 24,40,40
ds_read_b128 v[68:71], v96 offset:1024
mav 28,44,40
buffer_load_dwordx4 v[192:195], v88, s[36:39], s49 offen offset:1024 sc1
mav 16,48,44
R 56,38912
mav 20,52,44
mav 24,56,44
mav 28,60,44
mav 16,64,48
R 60,39936
buffer_load_dwordx4 v[196:199], v89, s[36:39], s49 offen sc1
mav 20,68,48
mav 24,72,48
mav 28,76,48
s_waitcnt lgkmcnt(1)
mav 16,80,52
R 32,40960
buffer_store_dwordx4 v[64:67], v92, s[44:47], s54 offen
mav 20,84,52
mav 24,88,52
mav 28,92,52
s_add_u32 m0, s50, 32768
mav 16,96,56
R 36,41984
buffer_store_dwordx4 v[68:71], v93, s[44:47], s54 offen
mav 20,100,56
mav 24,104,56
mav 28,108,56
s_waitcnt lgkmcnt(1)
mav 16,112,60
R 40,43008
buffer_load_dwordx4 v[200:203], v89, s[36:39], s49 offen offset:1024 sc1
mav 20,116,60
mav 24,120,60
mav 28,124,60
mav 16,128,32
R 44,44032
D 83,0
mav 20,132,32
mav 24,136,32
mav 28,140,32
s_waitcnt lgkmcnt(1)
mav 16,144,36
R 48,45056
D 83,1024
mav 20,148,36
mav 24,152,36
mav 28,156,36
mav 16,160,40
R 52,46080
D 83,2048
mav 20,164,40
s_sub_u32 s54, s54, 0x8000
mav 24,168,40
mav 28,172,40
s_waitcnt lgkmcnt(1)
mav 16,176,44
R 56,47104
D 83,3072
mav 20,180,44
mav 24,184,44
mav 28,188,44
mav 16,192,48
R 60,48128
buffer_load_dwordx4 v[124:127], v90, s[36:39], s49 offen sc1
mav 20,196,48
mav 24,200,48
mav 28,204,48
s_waitcnt lgkmcnt(1)
mav 16,208,52
buffer_load_dwordx4 v[128:131], v90, s[36:39], s49 offen offset:1024 sc1
mav 20,212,52
mav 24,216,52
mav 28,220,52
mav 16,224,56
mav 20,228,56
mav 24,232,56
mav 28,236,56
s_waitcnt lgkmcnt(0)
mav 16,240,60
mav 20,244,60
mav 24,248,60
mav 28,252,60
s_waitcnt vmcnt(18)
mvv 16,116,164
mvv 20,120,164
s_waitcnt vmcnt(17)
mvv 24,116,168
s_xor_b32 s50, s50, 0x10000
mvv 28,120,168
s_waitcnt vmcnt(15)
mvv 16,116,172
mvv 20,120,172
v_xor_b32 v80, 0x10000, v80
s_waitcnt vmcnt(14)
mvv 24,116,176
s_add_u32 s49, s49, 346112
mvv 28,120,176
s_barrier
s_waitcnt vmcnt(13)
mvv 16,116,180
R 32,0
mvv 20,120,180
buffer_load_dwordx4 v[132:135], v86, s[36:39], s49 offen sc1
s_waitcnt vmcnt(13)
mvv 24,116,184
mvv 28,120,184
s_waitcnt vmcnt(12)
mvv 16,116,188
R 36,1024
mvv 20,120,188
buffer_load_dwordx4 v[136:139], v86, s[36:39], s49 offen offset:1024 sc1
s_waitcnt vmcnt(12)
mvv 24,116,192
mvv 28,120,192
s_waitcnt vmcnt(11)
mvv 16,116,196
R 40,2048
mvv 20,120,196
s_waitcnt vmcnt(8)
mvv 24,116,200
buffer_load_dwordx4 v[140:143], v87, s[36:39], s49 offen sc1
mvv 28,120,200
s_mov_b32 s60, -13
.p2align 6
LR2_2:
s_waitcnt lgkmcnt(1)
v_mfma_f32_16x16x32_bf16 v[0:3], a[0:3], v[32:35], v[100:103]
R 44,3072
v_cvt_pk_bf16_f32 v64, v16, v17
v_mfma_f32_16x16x32_bf16 v[4:7], a[4:7], v[32:35], v[104:107]
v_cvt_pk_bf16_f32 v65, v18, v19
v_cvt_pk_bf16_f32 v66, v20, v21
v_mfma_f32_16x16x32_bf16 v[8:11], a[8:11], v[32:35], v[108:111]
buffer_load_dwordx4 v[144:147], v87, s[36:39], s49 offen offset:1024 sc1
v_cvt_pk_bf16_f32 v67, v22, v23
v_mfma_f32_16x16x32_bf16 v[12:15], a[12:15], v[32:35], v[112:115]
ds_write_b128 v94, v[64:67] offset:0
v_cvt_pk_bf16_f32 v68, v24, v25
mav 0,16,36
R 48,4096
v_cvt_pk_bf16_f32 v69, v26, v27
mav 4,20,36
v_cvt_pk_bf16_f32 v70, v28, v29
v_cvt_pk_bf16_f32 v71, v30, v31
mav 8,24,36
ds_write_b128 v95, v[68:71] offset:0
mav 12,28,36
)"
R"(
buffer_load_dwordx4 v[148:151], v88, s[36:39], s49 offen sc1
s_waitcnt lgkmcnt(3)
mav 0,32,40
R 52,5120
mav 4,36,40
s_waitcnt lgkmcnt(1)
ds_read_b128 v[64:67], v96 offset:0
mav 8,40,40
ds_read_b128 v[68:71], v96 offset:1024
mav 12,44,40
buffer_load_dwordx4 v[152:155], v88, s[36:39], s49 offen offset:1024 sc1
mav 0,48,44
R 56,6144
mav 4,52,44
mav 8,56,44
mav 12,60,44
mav 0,64,48
R 60,7168
buffer_load_dwordx4 v[156:159], v89, s[36:39], s49 offen sc1
mav 4,68,48
mav 8,72,48
mav 12,76,48
s_waitcnt lgkmcnt(1)
mav 0,80,52
R 32,8192
buffer_store_dwordx4 v[64:67], v92, s[44:47], s54 offen
mav 4,84,52
mav 8,88,52
mav 12,92,52
s_add_u32 m0, s50, 0
mav 0,96,56
R 36,9216
buffer_store_dwordx4 v[68:71], v93, s[44:47], s54 offen
mav 4,100,56
mav 8,104,56
mav 12,108,56
s_waitcnt lgkmcnt(1)
mav 0,112,60
R 40,10240
buffer_load_dwordx4 v[160:163], v89, s[36:39], s49 offen offset:1024 sc1
mav 4,116,60
mav 8,120,60
mav 12,124,60
mav 0,128,32
R 44,11264
D 83,0
mav 4,132,32
mav 8,136,32
mav 12,140,32
s_waitcnt lgkmcnt(1)
mav 0,144,36
R 48,12288
D 83,1024
mav 4,148,36
mav 8,152,36
mav 12,156,36
mav 0,160,40
R 52,13312
D 83,2048
mav 4,164,40
s_sub_u32 s54, s54, 0x8000
mav 8,168,40
mav 12,172,40
s_waitcnt lgkmcnt(1)
mav 0,176,44
R 56,14336
D 83,3072
mav 4,180,44
mav 8,184,44
mav 12,188,44
mav 0,192,48
R 60,15360
buffer_load_dwordx4 v[164:167], v90, s[36:39], s49 offen sc1
mav 4,196,48
mav 8,200,48
mav 12,204,48
s_waitcnt lgkmcnt(1)
mav 0,208,52
buffer_load_dwordx4 v[168:171], v90, s[36:39], s49 offen offset:1024 sc1
mav 4,212,52
mav 8,216,52
mav 12,220,52
mav 0,224,56
mav 4,228,56
mav 8,232,56
mav 12,236,56
s_waitcnt lgkmcnt(0)
mav 0,240,60
mav 4,244,60
mav 8,248,60
mav 12,252,60
s_waitcnt vmcnt(17)
mvv 0,116,124
mvv 4,120,124
s_waitcnt vmcnt(16)
mvv 8,116,128
mvv 12,120,128
s_waitcnt vmcnt(15)
mvv 0,116,132
mvv 4,120,132
s_waitcnt vmcnt(14)
mvv 8,116,136
s_add_u32 s49, s49, 346112
mvv 12,120,136
s_barrier
s_waitcnt vmcnt(13)
mvv 0,116,140
R 32,32768
mvv 4,120,140
buffer_load_dwordx4 v[172:175], v86, s[36:39], s49 offen sc1
s_waitcnt vmcnt(13)
mvv 8,116,144
mvv 12,120,144
s_waitcnt vmcnt(12)
mvv 0,116,148
R 36,33792
mvv 4,120,148
buffer_load_dwordx4 v[176:179], v86, s[36:39], s49 offen offset:1024 sc1
s_waitcnt vmcnt(12)
mvv 8,116,152
mvv 12,120,152
s_waitcnt vmcnt(11)
mvv 0,116,156
R 40,34816
mvv 4,120,156
s_waitcnt vmcnt(8)
mvv 8,116,160
buffer_load_dwordx4 v[180:183], v87, s[36:39], s49 offen sc1
mvv 12,120,160
s_waitcnt lgkmcnt(1)
v_mfma_f32_16x16x32_bf16 v[16:19], a[0:3], v[32:35], v[100:103]
R 44,35840
v_cvt_pk_bf16_f32 v64, v0, v1
v_mfma_f32_16x16x32_bf16 v[20:23], a[4:7], v[32:35], v[104:107]
v_cvt_pk_bf16_f32 v65, v2, v3
v_cvt_pk_bf16_f32 v66, v4, v5
v_mfma_f32_16x16x32_bf16 v[24:27], a[8:11], v[32:35], v[108:111]
buffer_load_dwordx4 v[184:187], v87, s[36:39], s49 offen offset:1024 sc1
v_cvt_pk_bf16_f32 v67, v6, v7
v_mfma_f32_16x16x32_bf16 v[28:31], a[12:15], v[32:35], v[112:115]
ds_write_b128 v94, v[64:67] offset:0
v_cvt_pk_bf16_f32 v68, v8, v9
mav 16,16,36
R 48,36864
v_cvt_pk_bf16_f32 v69, v10, v11
mav 20,20,36
v_cvt_pk_bf16_f32 v70, v12, v13
v_cvt_pk_bf16_f32 v71, v14, v15
mav 24,24,36
ds_write_b128 v95, v[68:71] offset:0
mav 28,28,36
buffer_load_dwordx4 v[188:191], v88, s[36:39], s49 offen sc1
s_waitcnt lgkmcnt(3)
mav 16,32,40
R 52,37888
mav 20,36,40
s_waitcnt lgkmcnt(1)
ds_read_b128 v[64:67], v96 offset:0
mav 24,40,40
ds_read_b128 v[68:71], v96 offset:1024
mav 28,44,40
buffer_load_dwordx4 v[192:195], v88, s[36:39], s49 offen offset:1024 sc1
mav 16,48,44
R 56,38912
mav 20,52,44
mav 24,56,44
mav 28,60,44
mav 16,64,48
R 60,39936
buffer_load_dwordx4 v[196:199], v89, s[36:39], s49 offen sc1
mav 20,68,48
mav 24,72,48
mav 28,76,48
s_waitcnt lgkmcnt(1)
mav 16,80,52
R 32,40960
buffer_store_dwordx4 v[64:67], v92, s[44:47], s54 offen
mav 20,84,52
mav 24,88,52
mav 28,92,52
s_add_u32 m0, s50, 32768
mav 16,96,56
R 36,41984
buffer_store_dwordx4 v[68:71], v93, s[44:47], s54 offen
mav 20,100,56
mav 24,104,56
mav 28,108,56
s_waitcnt lgkmcnt(1)
mav 16,112,60
R 40,43008
buffer_load_dwordx4 v[200:203], v89, s[36:39], s49 offen offset:1024 sc1
mav 20,116,60
mav 24,120,60
mav 28,124,60
mav 16,128,32
R 44,44032
D 83,0
mav 20,132,32
mav 24,136,32
mav 28,140,32
s_waitcnt lgkmcnt(1)
mav 16,144,36
R 48,45056
D 83,1024
mav 20,148,36
mav 24,152,36
mav 28,156,36
mav 16,160,40
R 52,46080
D 83,2048
mav 20,164,40
s_sub_u32 s54, s54, 0x8000
mav 24,168,40
mav 28,172,40
s_waitcnt lgkmcnt(1)
mav 16,176,44
R 56,47104
D 83,3072
mav 20,180,44
mav 24,184,44
mav 28,188,44
mav 16,192,48
R 60,48128
buffer_load_dwordx4 v[124:127], v90, s[36:39], s49 offen sc1
mav 20,196,48
mav 24,200,48
mav 28,204,48
s_waitcnt lgkmcnt(1)
mav 16,208,52
buffer_load_dwordx4 v[128:131], v90, s[36:39], s49 offen offset:1024 sc1
mav 20,212,52
mav 24,216,52
mav 28,220,52
mav 16,224,56
mav 20,228,56
mav 24,232,56
mav 28,236,56
s_waitcnt lgkmcnt(0)
mav 16,240,60
mav 20,244,60
mav 24,248,60
mav 28,252,60
s_waitcnt vmcnt(17)
mvv 16,116,164
mvv 20,120,164
s_waitcnt vmcnt(16)
mvv 24,116,168
s_xor_b32 s50, s50, 0x10000
mvv 28,120,168
s_waitcnt vmcnt(15)
mvv 16,116,172
mvv 20,120,172
v_xor_b32 v80, 0x10000, v80
s_waitcnt vmcnt(14)
mvv 24,116,176
s_add_u32 s49, s49, 346112
mvv 28,120,176
s_barrier
s_waitcnt vmcnt(13)
mvv 16,116,180
R 32,0
mvv 20,120,180
buffer_load_dwordx4 v[132:135], v86, s[36:39], s49 offen sc1
s_waitcnt vmcnt(13)
mvv 24,116,184
mvv 28,120,184
s_waitcnt vmcnt(12)
mvv 16,116,188
R 36,1024
mvv 20,120,188
buffer_load_dwordx4 v[136:139], v86, s[36:39], s49 offen offset:1024 sc1
s_waitcnt vmcnt(12)
mvv 24,116,192
mvv 28,120,192
s_waitcnt vmcnt(11)
mvv 16,116,196
R 40,2048
mvv 20,120,196
s_waitcnt vmcnt(8)
mvv 24,116,200
buffer_load_dwordx4 v[140:143], v87, s[36:39], s49 offen sc1
mvv 28,120,200
s_add_u32 s60, s60, 1
s_cbranch_scc0 LR2_2
s_waitcnt lgkmcnt(1)
v_mfma_f32_16x16x32_bf16 v[0:3], a[0:3], v[32:35], v[100:103]
R 44,3072
v_cvt_pk_bf16_f32 v64, v16, v17
v_mfma_f32_16x16x32_bf16 v[4:7], a[4:7], v[32:35], v[104:107]
v_cvt_pk_bf16_f32 v65, v18, v19
v_cvt_pk_bf16_f32 v66, v20, v21
v_mfma_f32_16x16x32_bf16 v[8:11], a[8:11], v[32:35], v[108:111]
buffer_load_dwordx4 v[144:147], v87, s[36:39], s49 offen offset:1024 sc1
v_cvt_pk_bf16_f32 v67, v22, v23
v_mfma_f32_16x16x32_bf16 v[12:15], a[12:15], v[32:35], v[112:115]
ds_write_b128 v94, v[64:67] offset:0
v_cvt_pk_bf16_f32 v68, v24, v25
mav 0,16,36
R 48,4096
v_cvt_pk_bf16_f32 v69, v26, v27
mav 4,20,36
v_cvt_pk_bf16_f32 v70, v28, v29
v_cvt_pk_bf16_f32 v71, v30, v31
mav 8,24,36
ds_write_b128 v95, v[68:71] offset:0
mav 12,28,36
buffer_load_dwordx4 v[148:151], v88, s[36:39], s49 offen sc1
s_waitcnt lgkmcnt(3)
mav 0,32,40
R 52,5120
mav 4,36,40
s_waitcnt lgkmcnt(1)
ds_read_b128 v[64:67], v96 offset:0
mav 8,40,40
ds_read_b128 v[68:71], v96 offset:1024
mav 12,44,40
buffer_load_dwordx4 v[152:155], v88, s[36:39], s49 offen offset:1024 sc1
mav 0,48,44
R 56,6144
mav 4,52,44
mav 8,56,44
mav 12,60,44
mav 0,64,48
R 60,7168
buffer_load_dwordx4 v[156:159], v89, s[36:39], s49 offen sc1
mav 4,68,48
mav 8,72,48
mav 12,76,48
s_waitcnt lgkmcnt(1)
mav 0,80,52
R 32,8192
buffer_store_dwordx4 v[64:67], v92, s[44:47], s54 offen
mav 4,84,52
mav 8,88,52
mav 12,92,52
s_add_u32 m0, s50, 0
mav 0,96,56
R 36,9216
buffer_store_dwordx4 v[68:71], v93, s[44:47], s54 offen
mav 4,100,56
mav 8,104,56
mav 12,108,56
s_waitcnt lgkmcnt(1)
mav 0,112,60
R 40,10240
buffer_load_dwordx4 v[160:163], v89, s[36:39], s49 offen offset:1024 sc1
mav 4,116,60
mav 8,120,60
mav 12,124,60
mav 0,128,32
R 44,11264
D 83,0
mav 4,132,32
mav 8,136,32
mav 12,140,32
s_waitcnt lgkmcnt(1)
mav 0,144,36
R 48,12288
D 83,1024
mav 4,148,36
mav 8,152,36
mav 12,156,36
mav 0,160,40
R 52,13312
D 83,2048
mav 4,164,40
s_sub_u32 s54, s54, 0x8000
mav 8,168,40
mav 12,172,40
s_waitcnt lgkmcnt(1)
mav 0,176,44
R 56,14336
D 83,3072
mav 4,180,44
mav 8,184,44
mav 12,188,44
mav 0,192,48
R 60,15360
buffer_load_dwordx4 v[164:167], v90, s[36:39], s49 offen sc1
mav 4,196,48
mav 8,200,48
mav 12,204,48
s_waitcnt lgkmcnt(1)
mav 0,208,52
buffer_load_dwordx4 v[168:171], v90, s[36:39], s49 offen offset:1024 sc1
mav 4,212,52
mav 8,216,52
mav 12,220,52
mav 0,224,56
mav 4,228,56
mav 8,232,56
mav 12,236,56
s_waitcnt lgkmcnt(0)
mav 0,240,60
mav 4,244,60
mav 8,248,60
mav 12,252,60
s_waitcnt vmcnt(17)
mvv 0,116,124
mvv 4,120,124
s_waitcnt vmcnt(16)
mvv 8,116,128
mvv 12,120,128
s_waitcnt vmcnt(15)
mvv 0,116,132
mvv 4,120,132
s_waitcnt vmcnt(14)
mvv 8,116,136
s_add_u32 s49, s49, 346112
mvv 12,120,136
s_barrier
s_waitcnt vmcnt(13)
mvv 0,116,140
R 32,32768
mvv 4,120,140
buffer_load_dwordx4 v[172:175], v86, s[36:39], s49 offen sc1
s_waitcnt vmcnt(13)
mvv 8,116,144
mvv 12,120,144
s_waitcnt vmcnt(12)
mvv 0,116,148
R 36,33792
mvv 4,120,148
buffer_load_dwordx4 v[176:179], v86, s[36:39], s49 offen offset:1024 sc1
s_waitcnt vmcnt(12)
mvv 8,116,152
mvv 12,120,152
s_waitcnt vmcnt(11)
mvv 0,116,156
R 40,34816
mvv 4,120,156
s_waitcnt vmcnt(8)
mvv 8,116,160
buffer_load_dwordx4 v[180:183], v87, s[36:39], s49 offen sc1
mvv 12,120,160
s_waitcnt lgkmcnt(1)
v_mfma_f32_16x16x32_bf16 v[16:19], a[0:3], v[32:35], v[100:103]
R 44,35840
v_cvt_pk_bf16_f32 v64, v0, v1
v_mfma_f32_16x16x32_bf16 v[20:23], a[4:7], v[32:35], v[104:107]
v_cvt_pk_bf16_f32 v65, v2, v3
v_cvt_pk_bf16_f32 v66, v4, v5
v_mfma_f32_16x16x32_bf16 v[24:27], a[8:11], v[32:35], v[108:111]
buffer_load_dwordx4 v[184:187], v87, s[36:39], s49 offen offset:1024 sc1
v_cvt_pk_bf16_f32 v67, v6, v7
v_mfma_f32_16x16x32_bf16 v[28:31], a[12:15], v[32:35], v[112:115]
ds_write_b128 v94, v[64:67] offset:0
v_cvt_pk_bf16_f32 v68, v8, v9
mav 16,16,36
R 48,36864
v_cvt_pk_bf16_f32 v69, v10, v11
mav 20,20,36
v_cvt_pk_bf16_f32 v70, v12, v13
v_cvt_pk_bf16_f32 v71, v14, v15
mav 24,24,36
ds_write_b128 v95, v[68:71] offset:0
mav 28,28,36
buffer_load_dwordx4 v[188:191], v88, s[36:39], s49 offen sc1
s_waitcnt lgkmcnt(3)
mav 16,32,40
R 52,37888
mav 20,36,40
s_waitcnt lgkmcnt(1)
ds_read_b128 v[64:67], v96 offset:0
mav 24,40,40
ds_read_b128 v[68:71], v96 offset:1024
mav 28,44,40
buffer_load_dwordx4 v[192:195], v88, s[36:39], s49 offen offset:1024 sc1
mav 16,48,44
R 56,38912
mav 20,52,44
mav 24,56,44
mav 28,60,44
mav 16,64,48
R 60,39936
buffer_load_dwordx4 v[196:199], v89, s[36:39], s49 offen sc1
mav 20,68,48
mav 24,72,48
mav 28,76,48
s_waitcnt lgkmcnt(1)
mav 16,80,52
R 32,40960
buffer_store_dwordx4 v[64:67], v92, s[44:47], s54 offen
mav 20,84,52
mav 24,88,52
mav 28,92,52
s_add_u32 m0, s50, 32768
mav 16,96,56
R 36,41984
buffer_store_dwordx4 v[68:71], v93, s[44:47], s54 offen
mav 20,100,56
mav 24,104,56
mav 28,108,56
s_waitcnt lgkmcnt(1)
mav 16,112,60
R 40,43008
buffer_load_dwordx4 v[200:203], v89, s[36:39], s49 offen offset:1024 sc1
mav 20,116,60
mav 24,120,60
mav 28,124,60
mav 16,128,32
R 44,44032
D 83,0
mav 20,132,32
mav 24,136,32
mav 28,140,32
s_waitcnt lgkmcnt(1)
mav 16,144,36
R 48,45056
D 83,1024
mav 20,148,36
mav 24,152,36
mav 28,156,36
mav 16,160,40
R 52,46080
D 83,2048
mav 20,164,40
s_sub_u32 s54, s54, 0x8000
mav 24,168,40
mav 28,172,40
s_waitcnt lgkmcnt(1)
mav 16,176,44
R 56,47104
D 83,3072
mav 20,180,44
mav 24,184,44
mav 28,188,44
mav 16,192,48
R 60,48128
buffer_load_dwordx4 v[124:127], v90, s[36:39], s49 offen sc1
mav 20,196,48
mav 24,200,48
mav 28,204,48
s_waitcnt lgkmcnt(1)
mav 16,208,52
buffer_load_dwordx4 v[128:131], v90, s[36:39], s49 offen offset:1024 sc1
mav 20,212,52
mav 24,216,52
mav 28,220,52
mav 16,224,56
mav 20,228,56
mav 24,232,56
mav 28,236,56
s_waitcnt lgkmcnt(0)
mav 16,240,60
mav 20,244,60
mav 24,248,60
mav 28,252,60
s_waitcnt vmcnt(17)
mvv 16,116,164
mvv 20,120,164
s_waitcnt vmcnt(16)
mvv 24,116,168
mvv 28,120,168
s_waitcnt vmcnt(15)
mvv 16,116,172
mvv 20,120,172
v_xor_b32 v80, 0x10000, v80
s_waitcnt vmcnt(14)
mvv 24,116,176
s_add_u32 s49, s49, 346112
mvv 28,120,176
s_barrier
s_waitcnt vmcnt(13)
mvv 16,116,180
R 32,0
mvv 20,120,180
buffer_load_dwordx4 v[132:135], v86, s[36:39], s49 offen sc1
s_waitcnt vmcnt(13)
mvv 24,116,184
mvv 28,120,184
s_waitcnt vmcnt(12)
mvv 16,116,188
R 36,1024
mvv 20,120,188
buffer_load_dwordx4 v[136:139], v86, s[36:39], s49 offen offset:1024 sc1
s_waitcnt vmcnt(12)
mvv 24,116,192
mvv 28,120,192
s_waitcnt vmcnt(11)
mvv 16,116,196
R 40,2048
mvv 20,120,196
s_waitcnt vmcnt(8)
mvv 24,116,200
buffer_load_dwordx4 v[140:143], v87, s[36:39], s49 offen sc1
mvv 28,120,200
s_waitcnt lgkmcnt(1)
v_mfma_f32_16x16x32_bf16 v[0:3], a[0:3], v[32:35], v[100:103]
R 44,3072
v_cvt_pk_bf16_f32 v64, v16, v17
v_mfma_f32_16x16x32_bf16 v[4:7], a[4:7], v[32:35], v[104:107]
v_cvt_pk_bf16_f32 v65, v18, v19
v_cvt_pk_bf16_f32 v66, v20, v21
v_mfma_f32_16x16x32_bf16 v[8:11], a[8:11], v[32:35], v[108:111]
buffer_load_dwordx4 v[144:147], v87, s[36:39], s49 offen offset:1024 sc1
v_cvt_pk_bf16_f32 v67, v22, v23
v_mfma_f32_16x16x32_bf16 v[12:15], a[12:15], v[32:35], v[112:115]
ds_write_b128 v94, v[64:67] offset:0
v_cvt_pk_bf16_f32 v68, v24, v25
mav 0,16,36
R 48,4096
v_cvt_pk_bf16_f32 v69, v26, v27
mav 4,20,36
v_cvt_pk_bf16_f32 v70, v28, v29
v_cvt_pk_bf16_f32 v71, v30, v31
mav 8,24,36
ds_write_b128 v95, v[68:71] offset:0
mav 12,28,36
buffer_load_dwordx4 v[148:151], v88, s[36:39], s49 offen sc1
s_waitcnt lgkmcnt(3)
mav 0,32,40
R 52,5120
mav 4,36,40
s_waitcnt lgkmcnt(1)
ds_read_b128 v[64:67], v96 offset:0
mav 8,40,40
ds_read_b128 v[68:71], v96 offset:1024
mav 12,44,40
buffer_load_dwordx4 v[152:155], v88, s[36:39], s49 offen offset:1024 sc1
mav 0,48,44
R 56,6144
mav 4,52,44
mav 8,56,44
mav 12,60,44
mav 0,64,48
R 60,7168
buffer_load_dwordx4 v[156:159], v89, s[36:39], s49 offen sc1
mav 4,68,48
mav 8,72,48
mav 12,76,48
s_waitcnt lgkmcnt(1)
mav 0,80,52
R 32,8192
buffer_store_dwordx4 v[64:67], v92, s[44:47], s54 offen
mav 4,84,52
mav 8,88,52
mav 12,92,52
mav 0,96,56
R 36,9216
buffer_store_dwordx4 v[68:71], v93, s[44:47], s54 offen
mav 4,100,56
mav 8,104,56
mav 12,108,56
s_waitcnt lgkmcnt(1)
mav 0,112,60
R 40,10240
buffer_load_dwordx4 v[160:163], v89, s[36:39], s49 offen offset:1024 sc1
mav 4,116,60
mav 8,120,60
mav 12,124,60
mav 0,128,32
R 44,11264
mav 4,132,32
mav 8,136,32
mav 12,140,32
s_waitcnt lgkmcnt(1)
mav 0,144,36
R 48,12288
mav 4,148,36
mav 8,152,36
mav 12,156,36
mav 0,160,40
R 52,13312
s_sub_u32 s54, s54, 0x8000
mav 4,164,40
mav 8,168,40
mav 12,172,40
buffer_load_dwordx4 v[164:167], v90, s[36:39], s49 offen sc1
s_waitcnt lgkmcnt(1)
mav 0,176,44
R 56,14336
mav 4,180,44
mav 8,184,44
mav 12,188,44
buffer_load_dwordx4 v[168:171], v90, s[36:39], s49 offen offset:1024 sc1
mav 0,192,48
R 60,15360
mav 4,196,48
mav 8,200,48
mav 12,204,48
s_waitcnt lgkmcnt(1)
mav 0,208,52
mav 4,212,52
mav 8,216,52
mav 12,220,52
mav 0,224,56
mav 4,228,56
mav 8,232,56
mav 12,236,56
s_waitcnt lgkmcnt(0)
mav 0,240,60
mav 4,244,60
mav 8,248,60
mav 12,252,60
s_waitcnt vmcnt(13)
mvv 0,116,124
mvv 4,120,124
s_waitcnt vmcnt(12)
mvv 8,116,128
mvv 12,120,128
s_waitcnt vmcnt(11)
mvv 0,116,132
mvv 4,120,132
s_waitcnt vmcnt(10)
mvv 8,116,136
s_add_u32 s49, s49, 346112
mvv 12,120,136
s_barrier
s_waitcnt vmcnt(9)
mvv 0,116,140
R 32,32768
mvv 4,120,140
buffer_load_dwordx4 v[172:175], v86, s[36:39], s49 offen sc1
s_waitcnt vmcnt(9)
mvv 8,116,144
mvv 12,120,144
s_waitcnt vmcnt(8)
mvv 0,116,148
R 36,33792
mvv 4,120,148
buffer_load_dwordx4 v[176:179], v86, s[36:39], s49 offen offset:1024 sc1
s_waitcnt vmcnt(8)
mvv 8,116,152
mvv 12,120,152
s_waitcnt vmcnt(7)
mvv 0,116,156
R 40,34816
mvv 4,120,156
s_waitcnt vmcnt(4)
mvv 8,116,160
buffer_load_dwordx4 v[180:183], v87, s[36:39], s49 offen sc1
mvv 12,120,160
s_waitcnt lgkmcnt(1)
v_mfma_f32_16x16x32_bf16 v[16:19], a[0:3], v[32:35], v[100:103]
R 44,35840
v_cvt_pk_bf16_f32 v64, v0, v1
v_mfma_f32_16x16x32_bf16 v[20:23], a[4:7], v[32:35], v[104:107]
v_cvt_pk_bf16_f32 v65, v2, v3
v_cvt_pk_bf16_f32 v66, v4, v5
v_mfma_f32_16x16x32_bf16 v[24:27], a[8:11], v[32:35], v[108:111]
buffer_load_dwordx4 v[184:187], v87, s[36:39], s49 offen offset:1024 sc1
v_cvt_pk_bf16_f32 v67, v6, v7
v_mfma_f32_16x16x32_bf16 v[28:31], a[12:15], v[32:35], v[112:115]
ds_write_b128 v94, v[64:67] offset:0
v_cvt_pk_bf16_f32 v68, v8, v9
mav 16,16,36
R 48,36864
v_cvt_pk_bf16_f32 v69, v10, v11
mav 20,20,36
v_cvt_pk_bf16_f32 v70, v12, v13
v_cvt_pk_bf16_f32 v71, v14, v15
mav 24,24,36
ds_write_b128 v95, v[68:71] offset:0
mav 28,28,36
buffer_load_dwordx4 v[188:191], v88, s[36:39], s49 offen sc1
s_waitcnt lgkmcnt(3)
mav 16,32,40
R 52,37888
mav 20,36,40
s_waitcnt lgkmcnt(1)
ds_read_b128 v[64:67], v96 offset:0
mav 24,40,40
ds_read_b128 v[68:71], v96 offset:1024
mav 28,44,40
buffer_load_dwordx4 v[192:195], v88, s[36:39], s49 offen offset:1024 sc1
mav 16,48,44
R 56,38912
mav 20,52,44
mav 24,56,44
mav 28,60,44
mav 16,64,48
R 60,39936
buffer_load_dwordx4 v[196:199], v89, s[36:39], s49 offen sc1
mav 20,68,48
mav 24,72,48
mav 28,76,48
s_waitcnt lgkmcnt(1)
mav 16,80,52
R 32,40960
buffer_store_dwordx4 v[64:67], v92, s[44:47], s54 offen
mav 20,84,52
mav 24,88,52
mav 28,92,52
mav 16,96,56
R 36,41984
buffer_store_dwordx4 v[68:71], v93, s[44:47], s54 offen
mav 20,100,56
mav 24,104,56
mav 28,108,56
s_waitcnt lgkmcnt(1)
mav 16,112,60
R 40,43008
buffer_load_dwordx4 v[200:203], v89, s[36:39], s49 offen offset:1024 sc1
mav 20,116,60
mav 24,120,60
mav 28,124,60
mav 16,128,32
R 44,44032
mav 20,132,32
mav 24,136,32
mav 28,140,32
s_waitcnt lgkmcnt(1)
mav 16,144,36
R 48,45056
mav 20,148,36
mav 24,152,36
mav 28,156,36
mav 16,160,40
R 52,46080
s_sub_u32 s54, s54, 0x8000
mav 20,164,40
mav 24,168,40
mav 28,172,40
s_waitcnt lgkmcnt(1)
mav 16,176,44
R 56,47104
mav 20,180,44
mav 24,184,44
mav 28,188,44
mav 16,192,48
R 60,48128
mav 20,196,48
mav 24,200,48
mav 28,204,48
s_waitcnt lgkmcnt(1)
mav 16,208,52
mav 20,212,52
mav 24,216,52
mav 28,220,52
mav 16,224,56
mav 20,228,56
mav 24,232,56
mav 28,236,56
s_waitcnt lgkmcnt(0)
mav 16,240,60
mav 20,244,60
mav 24,248,60
mav 28,252,60
s_waitcnt vmcnt(11)
mvv 16,116,164
mvv 20,120,164
s_waitcnt vmcnt(10)
mvv 24,116,168
mvv 28,120,168
s_waitcnt vmcnt(9)
mvv 16,116,172
mvv 20,120,172
s_waitcnt vmcnt(8)
mvv 24,116,176
mvv 28,120,176
s_waitcnt vmcnt(7)
mvv 16,116,180
mvv 20,120,180
s_waitcnt vmcnt(6)
mvv 24,116,184
mvv 28,120,184
s_waitcnt vmcnt(5)
mvv 16,116,188
mvv 20,120,188
s_waitcnt vmcnt(4)
mvv 24,116,192
mvv 28,120,192
s_waitcnt vmcnt(3)
mvv 16,116,196
mvv 20,120,196
s_waitcnt vmcnt(0)
mvv 24,116,200
mvv 28,120,200
s_nop 3
v_cvt_pk_bf16_f32 v64, v16, v17
v_cvt_pk_bf16_f32 v65, v18, v19
v_cvt_pk_bf16_f32 v66, v20, v21
v_cvt_pk_bf16_f32 v67, v22, v23
ds_write_b128 v94, v[64:67] offset:0
v_cvt_pk_bf16_f32 v68, v24, v25
v_cvt_pk_bf16_f32 v69, v26, v27
v_cvt_pk_bf16_f32 v70, v28, v29
v_cvt_pk_bf16_f32 v71, v30, v31
ds_write_b128 v95, v[68:71] offset:0
s_waitcnt lgkmcnt(0)
ds_read_b128 v[64:67], v96 offset:0
ds_read_b128 v[68:71], v96 offset:1024
s_waitcnt lgkmcnt(0)
buffer_store_dwordx4 v[64:67], v92, s[44:47], s54 offen
buffer_store_dwordx4 v[68:71], v93, s[44:47], s54 offen
s_endpgm
LeR:
.purgem R
.purgem mav
.purgem Ta
.purgem mva
.purgem mvv
.purgem Tv
.purgem D
.purgem E
)"
    : "+{v[0:7]}"(v0), "+{v[80:95]}"(c0), "+{v[96:111]}"(c1), "+{v[112:127]}"(c2), "+{s[36:51]}"(s0), "+{s[52:59]}"(s1)
    :
    : "v8", "v9", "v10", "v11", "v12", "v13", "v14", "v15", "v16", "v17", "v18", "v19", "v20", "v21", "v22", "v23", "v24", "v25", "v26", "v27", "v28", "v29", "v30", "v31", "v32", "v33", "v34", "v35", "v36", "v37", "v38", "v39", "v40", "v41", "v42", "v43", "v44", "v45", "v46", "v47", "v48", "v49", "v50", "v51", "v52", "v53", "v54", "v55", "v56", "v57", "v58", "v59", "v60", "v61", "v62", "v63", "v64", "v65", "v66", "v67", "v68", "v69", "v70", "v71", "v72", "v73", "v74", "v75", "v76", "v77", "v78", "v79", "v128", "v129", "v130", "v131", "v132", "v133", "v134", "v135", "v136", "v137", "v138", "v139", "v140", "v141", "v142", "v143", "v144", "v145", "v146", "v147", "v148", "v149", "v150", "v151", "v152", "v153", "v154", "v155", "v156", "v157", "v158", "v159", "v160", "v161", "v162", "v163", "v164", "v165", "v166", "v167", "v168", "v169", "v170", "v171", "v172", "v173", "v174", "v175", "v176", "v177", "v178", "v179", "v180", "v181", "v182", "v183", "v184", "v185", "v186", "v187", "v188", "v189", "v190", "v191", "v192", "v193", "v194", "v195", "v196", "v197", "v198", "v199", "v200", "v201", "v202", "v203", "v204", "v205", "v206", "v207", "v208", "v209", "v210", "v211", "v212", "v213", "v214", "v215", "v216", "v217", "v218", "v219", "v220", "v221", "v222", "v223", "v224", "v225", "v226", "v227", "v228", "v229", "v230", "v231", "v232", "v233", "v234", "v235", "v236", "v237", "v238", "v239", "v240", "v241", "v242", "v243", "v244", "v245", "v246", "v247", "v248", "v249", "v250", "v251", "v252", "v253", "v254", "v255", "a0", "a1", "a2", "a3", "a4", "a5", "a6", "a7", "a8", "a9", "a10", "a11", "a12", "a13", "a14", "a15", "a16", "a17", "a18", "a19", "a20", "a21", "a22", "a23", "a24", "a25", "a26", "a27", "a28", "a29", "a30", "a31", "a32", "a33", "a34", "a35", "a36", "a37", "a38", "a39", "a40", "a41", "a42", "a43", "a44", "a45", "a46", "a47", "a48", "a49", "a50", "a51", "a52", "a53", "a54", "a55", "a56", "a57", "a58", "a59", "a60", "a61", "a62", "a63", "a64", "a65", "a66", "a67", "a68", "a69", "a70", "a71", "a72", "a73", "a74", "a75", "a76", "a77", "a78", "a79", "a80", "a81", "a82", "a83", "a84", "a85", "a86", "a87", "a88", "a89", "a90", "a91", "a92", "a93", "a94", "a95", "a96", "a97", "a98", "a99", "a100", "a101", "a102", "a103", "a104", "a105", "a106", "a107", "a108", "a109", "a110", "a111", "a112", "a113", "a114", "a115", "a116", "a117", "a118", "a119", "a120", "a121", "a122", "a123", "a124", "a125", "a126", "a127", "a128", "a129", "a130", "a131", "a132", "a133", "a134", "a135", "a136", "a137", "a138", "a139", "a140", "a141", "a142", "a143", "a144", "a145", "a146", "a147", "a148", "a149", "a150", "a151", "a152", "a153", "a154", "a155", "a156", "a157", "a158", "a159", "a160", "a161", "a162", "a163", "a164", "a165", "a166", "a167", "a168", "a169", "a170", "a171", "a172", "a173", "a174", "a175", "a176", "a177", "a178", "a179", "a180", "a181", "a182", "a183", "a184", "a185", "a186", "a187", "a188", "a189", "a190", "a191", "a192", "a193", "a194", "a195", "a196", "a197", "a198", "a199", "a200", "a201", "a202", "a203", "a204", "a205", "a206", "a207", "a208", "a209", "a210", "a211", "a212", "a213", "a214", "a215", "a216", "a217", "a218", "a219", "a220", "a221", "a222", "a223", "a224", "a225", "a226", "a227", "a228", "a229", "a230", "a231", "a232", "a233", "a234", "a235", "a236", "a237", "a238", "a239", "a240", "a241", "a242", "a243", "a244", "a245", "a246", "a247", "a248", "a249", "a250", "a251", "a252", "a253", "a254", "a255", "s60", "s61", "s62", "s63", "s64", "s65", "s66", "s67", "s68", "s69", "s70", "s71", "s72", "s73", "s74", "s75", "s76", "s77", "s78", "s79", "s80", "s81", "s82", "s83", "s84", "s85", "s86", "s87", "s88", "s89", "s90", "s91", "s92", "s93", "s94", "s95", "s96", "s97", "s98", "s99", "vcc", "scc", "memory");
}

extern "C" void kernel_launch(void* const* d_in, const int* in_sizes, int n_in, void* d_out, int out_size,
                              void* d_ws, size_t ws_size, hipStream_t stream) {
  Args m{};
  m.x = (const u16*)d_in[0]; m.wqkv = (const u16*)d_in[1]; m.wo = (const u16*)d_in[2]; m.w1 = (const u16*)d_in[4];
  m.b1 = (const u16*)d_in[5]; m.w2 = (const u16*)d_in[6]; m.b2 = (const u16*)d_in[7];
  m.out = (u16*)d_out; m.ws = (char*)d_ws;
  (void)hipFuncSetAttribute((const void*)df_kernel, hipFuncAttributeMaxDynamicSharedMemorySize, LDS_BYTES);
  hipLaunchKernelGGL(df_kernel, dim3(256), dim3(256), LDS_BYTES, stream, m);
}
```

```cpp
#include <hip/hip_runtime.h>
#include <stdint.h>

typedef __attribute__((ext_vector_type(16))) unsigned u32x16;
typedef __attribute__((ext_vector_type(8))) unsigned u32x8;
typedef __attribute__((ext_vector_type(4))) unsigned u32x4;
typedef __attribute__((ext_vector_type(2))) unsigned u32x2;
typedef __attribute__((ext_vector_type(2))) __bf16 bf2_t;
typedef unsigned short u16;
typedef __amdgpu_buffer_rsrc_t rsrc_t;
typedef __attribute__((address_space(3))) void lds_t;

constexpr unsigned FLAGS = 4096, RB = 4194304, REC = 346112, SX = 344064, WS_END = 92798976, DBG = 1048576;
constexpr int LDS_BYTES = 163840;
#define NEGB 0xff61b1e6u
#define FW(p, s, w) (FLAGS + (unsigned)(((p) * 64 + (s)) * 128 + (w) * 8))
#define SPARE(i) ((i) < 2 ? 22 + (i) : 47)
#define blk0(s) (RB + (unsigned)(p * 64 + (s) - 2) * REC)
#define TA(seg, hf, sub) (unsigned)((seg) * 4096 + (8 * lg + (li >> 2)) * 128 + ((((hf) * 4 + (li & 3)) ^ ((li >> 1) & 4)) << 4) + (sub))
#define TO(seg, hf, half) (unsigned)((seg) * 4096 + (8 * lg + (li >> 2)) * 128 + ((((hf) * 4 + (half) * 2 + ((li & 3) >> 1)) ^ ((li >> 1) & 4)) << 4) + (li & 1) * 8)
#define BIAS(ptr, n, i) ((unsigned)__builtin_amdgcn_raw_buffer_load_b16(__builtin_amdgcn_make_buffer_rsrc((void*)(ptr), 0, (n) * 2, 0x00020000), (i) * 2, 0, 0) << 16)
#define SPIN_CAP 200000

struct Args {
  const u16 *x, *wqkv, *wo, *w1, *w2;
  char* ws;
  u16* out;
  const u16 *b1, *b2;
};

__device__ __forceinline__ float rows_sum(float v) {
  const auto a = __builtin_amdgcn_permlane16_swap(__float_as_uint(v), __float_as_uint(v), false, false);
  const float m = __uint_as_float(a[0]) + __uint_as_float(a[1]);
  const auto b = __builtin_amdgcn_permlane32_swap(__float_as_uint(m), __float_as_uint(m), false, false);
  return __uint_as_float(b[0]) + __uint_as_float(b[1]);
}

__device__ __forceinline__ const u16* arg_b2() {
  auto ka = (const __attribute__((address_space(4))) Args*)__builtin_amdgcn_kernarg_segment_ptr();
  asm volatile("" : "+s"(ka));
  return ka->b2;
}

__global__ __launch_bounds__(256, 1) void df_kernel(Args m) {
  const int w = __builtin_amdgcn_readfirstlane(threadIdx.x >> 6);
  const int bid = blockIdx.x, xc = bid & 7, j_ = bid >> 3;
  {
    const unsigned tid = threadIdx.x, kd = (0x32765410u >> (xc * 4)) & 15;
    lds_t* const sink = (lds_t*)(size_t)(65536 + w * 1024);
    if (w & 1) {
      unsigned long long pc; unsigned o = w == 1 ? (tid & 63) * 128 : (tid & 63) * 4096 + j_ * 128;
      asm volatile("s_mov_b32 m0, %2\n\ts_getpc_b64 %0\nLbeg:\n\tv_min_u32 %1, Lfin - Lbeg - 4, %1\n\tglobal_load_lds_dword %1, %0" : "=&s"(pc), "+v"(o) : "s"(65536 + w * 1024) : "memory");
    }
#define QLINE(l) (((l) >> 2) * 2048 + ((l) & 3) * 128 + kd * 512)
    if (kd < 4) {
      const rsrc_t rT = __builtin_amdgcn_make_buffer_rsrc((void*)(w == 3 ? m.x : m.wqkv), 0, w == 3 ? 2048 * 1024 * 2 : 1024 * 3072 * 2, 0x00020000);
      const unsigned lx = j_ * 64 + (tid & 63), lh = ((j_ >> 3) * 4 + (j_ & 3)) * 192 + tid;
      __builtin_amdgcn_raw_ptr_buffer_load_lds(rT, sink, 4, w == 3 ? (lx >> 9) * 1048576 + (lx & 511) * 128 + 30 * 32768 : QLINE(w * 2048 + lx), 0, 0, 0);
      if ((j_ & 4) && w < 3) {
        __builtin_amdgcn_raw_ptr_buffer_load_lds(rT, sink, 4, QLINE(lh), 6144 * 512, 0, 0);
        __builtin_amdgcn_raw_ptr_buffer_load_lds(rT, sink, 4, QLINE(lh), 9216 * 512, 0, 0);
      }
    } else if (kd == 4) {
      const rsrc_t rT = __builtin_amdgcn_make_buffer_rsrc((void*)m.wo, 0, 1024 * 1024 * 2, 0x00020000);
      __builtin_amdgcn_raw_ptr_buffer_load_lds(rT, sink, 4, (j_ * 256 + tid) * 128, 0, 0, 0);
      __builtin_amdgcn_raw_ptr_buffer_load_lds(rT, sink, 4, (j_ * 256 + tid) * 128, 1048576, 0, 0);
    } else if (kd < 8 && w == 0) {
      const rsrc_t rT = __builtin_amdgcn_make_buffer_rsrc((void*)m.x, 0, 2048 * 1024 * 2, 0x00020000);
      __builtin_amdgcn_raw_ptr_buffer_load_lds(rT, sink, 4, (j_ >> 3) * 1048576 + ((j_ & 7) * 64 + tid) * 128, (40 - 2 * kd) * 32768, 0, 0);
    }
  }
  const unsigned long long tb = xc == 0 ? 0x3332313003020100ull : xc == 1 ? 0x3736353407060504ull : xc == 2 ? 0x2c2b151413121110ull : xc == 3 ? 0x162d1d1c1b1a1918ull : xc == 4 ? 0x172e232221201f1eull : xc == 5 ? 0x2f2a292827262524ull : xc == 6 ? 0x3b3a39380b0a0908ull : 0x3f3e3d3c0f0e0d0cull;
  const int slot = (int)(tb >> ((j_ & 7) * 8)) & 63, p = j_ >> 3;
  if (slot == 22 || slot == 23 || slot == 47) {
    int lane;
    asm volatile("v_mbcnt_lo_u32_b32 %0, -1, 0\n\tv_mbcnt_hi_u32_b32 %0, -1, %0" : "=v"(lane));
    const int lg = lane >> 4, li = lane & 15;
    const unsigned l16 = lane * 16;
    const rsrc_t rS = __builtin_amdgcn_make_buffer_rsrc((void*)m.ws, 0, WS_END, 0x00020000);
    const rsrc_t rX = __builtin_amdgcn_make_buffer_rsrc((void*)m.x, 0, 2048 * 1024 * 2, 0x00020000);
    u32x2 tag = {0u, __builtin_amdgcn_raw_buffer_load_b32(rS, 0, 0, 16)};
    const int u = slot == 47 ? 4 : (slot - 22) * 2;
    const unsigned fa = lane ? 0x80000000u : FW(p, slot, w);
    const bf2_t one2 = {(__bf16)1.0f, (__bf16)1.0f};
    const unsigned go = lg * 2048 + w * 512 + ((li ^ (lg * 4)) << 4), lb = 73728 + w * 20480, lo = lb + (li >> 2) * 2064 + (li & 3) * 256 + lg * 16;
    auto fetch = [&](int b) {
#pragma unroll
      for (int i = 0; i < 16; ++i)
        __builtin_amdgcn_raw_ptr_buffer_load_lds(rX, (lds_t*)(size_t)(lb + (i >> 1) * 2064 + (i & 1) * 1024), 16, go + (((i >> 1) ^ 4) * 8192 + (i & 1) * 256), (p * 32 + 30 - b) * 32768, 0, 0);
    };
    fetch(u);
    tag[0] += 0x100u; tag[1] += 0x9e3779b9u;
    for (int b = u; b < 32; b += 6) {
      asm volatile("s_waitcnt vmcnt(16)" ::: "memory");
      __builtin_amdgcn_raw_buffer_store_b64((u32x2){tag[0] + b, tag[1]}, rS, fa, 0, 16);
      asm volatile("s_waitcnt vmcnt(1)" ::: "memory");
      u32x4 v[16];
#pragma unroll
      for (int k = 0; k < 16; ++k) v[k] = *(const __attribute__((address_space(3))) u32x4*)(size_t)(lo + (k >> 3) * 8256 + ((k >> 2) & 1) * 1024 + (((k ^ li) & 3) << 6));
      const unsigned rb = RB + (unsigned)(p * 64 + b) * REC;
#pragma unroll
      for (int k = 0; k < 16; ++k) __builtin_amdgcn_raw_buffer_store_b128(v[k], rS, l16 + w * 8192 + (k & 7) * 1024, rb + (k >> 3) * REC, 16);
#pragma unroll
      for (int h = 0; h < 2; ++h) {
        float s = 0.f, q = 0.f;
#pragma unroll
        for (int k = 0; k < 8; ++k) {
#pragma unroll
          for (int e = 0; e < 4; ++e) {
            const unsigned xe = v[h * 8 + k][e];
            const bf2_t x = __builtin_bit_cast(bf2_t, xe);
            s = __builtin_amdgcn_fdot2_f32_bf16(x, one2, s, false);
            q = __builtin_amdgcn_fdot2_f32_bf16(x, x, q, false);
          }
        }
        s = rows_sum(s); q = rows_sum(q);
        __builtin_amdgcn_raw_buffer_store_b64((u32x2){__float_as_uint(s), __float_as_uint(q)}, rS, lg ? 0x80000000u : w * 128 + li * 8 + SX, rb + h * REC, 16);
      }
      asm volatile("" ::: "memory");
      if (b + 6 < 32) fetch(b + 6);
    }
    asm volatile("s_waitcnt vmcnt(0)\nLfin:" ::: "memory");
    __builtin_amdgcn_raw_buffer_store_b64((u32x2){tag[0] + 100, tag[1]}, rS, fa, 0, 16);
    if (slot == 47 && p == 3 && w == 3) {
      for (int n = 0; n < SPIN_CAP; ++n) {
        bool bad = false;
#pragma unroll
        for (int i = 0; i < 8; ++i) {
          const u32x4 f = __builtin_amdgcn_raw_buffer_load_b128(rS, FLAGS + (i * 32 + (lane >> 1)) * 128 + (lane & 1) * 16, 0, 16);
          bad = bad || f[1] != tag[1] || f[3] != tag[1] || f[0] - tag[0] > 255u || f[2] - tag[0] > 255u;
        }
        if (__builtin_amdgcn_ballot_w64(bad) == 0ull) break;
        __builtin_amdgcn_s_sleep(64);
      }
      __builtin_amdgcn_raw_buffer_store_b32(tag[1], rS, lane ? 0x80000000u : 0u, 0, 16);
    }
    return;
  }
  const int lane = threadIdx.x & 63, lg = lane >> 4, li = lane & 15;
  const unsigned l16 = lane * 16;
  u32x16 c0 = 0, c1 = 0, c2 = 0, s0 = 0;
  u32x8 v0 = 0, s1 = 0;
  const u16 *wp, *op = m.out; unsigned wn;
  const u16 *rQp = m.wqkv, *rOp = m.wo, *r1p = m.w1, *r2p = m.w2;
  constexpr unsigned rQn = 1024 * 3072 * 2, rOn = 1024 * 1024 * 2, r1n = 1024 * 3584 * 2, r2n = r1n;
  const int rot = p * 1, wr = (w + rot) & 3;
  if (slot < 16 && !(w & 1)) {
    s0[12] = 0 + (0); wp = rQp; wn = rQn;
    v0[3] = (p * 32 + 30) * 32768 + (w * 4 + lg) * 2048 + ((li ^ (lg * 4)) << 4); v0[7] = 2048 + (li >> 2) * 8208 + (li & 3) * 256 + lg * 16; op = m.x;
    s0[13] = blk0(0) + 32768u + (slot * 2 + (w >> 1)) * 1024 - (131072u + (w >> 1) * 2048);
    s0[15] = w * 8 * 6144 + slot * 128;
    s1[2] = 262144u + (p * 32 + slot * 2 + (w >> 1)) * 4096 - (131072u + (w >> 1) * 2048);
    s1[3] = p ? 262144u + ((p - 1) * 32 + slot * 2 + (w >> 1)) * 4096 - (131072u + (w >> 1) * 2048) : 0x80000000u;
    s1[4] = p ? 0u : NEGB;
    v0[0] = (lane >> 3) * 6144 + (((lane & 7) ^ ((lane >> 2) & 4)) << 4) + 0u;
    v0[1] = (lane >> 3) * 6144 + (((lane & 7) ^ ((lane >> 2) & 4)) << 4) + 2048u;
    v0[2] = (lane >> 3) * 6144 + (((lane & 7) ^ ((lane >> 2) & 4)) << 4) + 4096u;
    v0[4] = TA(0, w >> 1, 0);
    v0[5] = TA(0, w >> 1, 8);
    v0[6] = TA(2, w >> 1, 0);
    c0[0] = l16;
    c0[1] = 131072u + (w >> 1) * 2048 + l16;
    c0[2] = 139264u + li * 8;
    c0[3] = 139264u + lg * 32;
    c0[4] = (((4 * lg + 0) ^ li) & 1) == 0 && (4 * lg + 0 <= li) ? 0u : NEGB;
    c0[5] = (((4 * lg + 1) ^ li) & 1) == 0 && (4 * lg + 1 <= li) ? 0u : NEGB;
    c0[6] = (((4 * lg + 2) ^ li) & 1) == 0 && (4 * lg + 2 <= li) ? 0u : NEGB;
    c0[7] = (((4 * lg + 3) ^ li) & 1) == 0 && (4 * lg + 3 <= li) ? 0u : NEGB;
    c0[8] = (((4 * lg + 0) ^ li) & 1) == 0 && (4 * lg + 0 > li) ? 0u : NEGB;
    c0[9] = (((4 * lg + 1) ^ li) & 1) == 0 && (4 * lg + 1 > li) ? 0u : NEGB;
    c0[10] = (((4 * lg + 2) ^ li) & 1) == 0 && (4 * lg + 2 > li) ? 0u : NEGB;
    c0[11] = (((4 * lg + 3) ^ li) & 1) == 0 && (4 * lg + 3 > li) ? 0u : NEGB;
    c0[12] = (4 * lg + 0 + (li & 1) <= li) ? ((li & 1) ? 0xffff0000u : 0xffffu) : 0u;
    c0[13] = (4 * lg + 2 + (li & 1) <= li) ? ((li & 1) ? 0xffff0000u : 0xffffu) : 0u;
    c0[14] = (4 * lg + 0 + (li & 1) > li) ? ((li & 1) ? 0xffff0000u : 0xffffu) : 0u;
    c0[15] = (4 * lg + 2 + (li & 1) > li) ? ((li & 1) ? 0xffff0000u : 0xffffu) : 0u;
  } else if (slot < 16) {
    s0[12] = 4 + (w >> 1); wp = rQp; wn = rQn;
    v0[3] = (p * 32 + 30) * 32768 + (w * 4 + lg) * 2048 + ((li ^ (lg * 4)) << 4); v0[7] = 2048 + (li >> 2) * 8208 + (li & 3) * 256 + lg * 16; op = m.x;
    s0[13] = blk0(0);
    s0[14] = 65536 + (w >> 1) * 4096;
    s0[15] = w * 8 * 6144 + slot * 128;
    s1[2] = blk0(0) + (w >> 1) * 346112u;
    s1[3] = (w >> 1) * 128;
    v0[0] = (lane >> 3) * 6144 + (((lane & 7) ^ ((lane >> 2) & 4)) << 4) + 0u;
    v0[1] = (lane >> 3) * 6144 + (((lane & 7) ^ ((lane >> 2) & 4)) << 4) + 2048u;
    v0[2] = (lane >> 3) * 6144 + (((lane & 7) ^ ((lane >> 2) & 4)) << 4) + 4096u;
    v0[4] = TA(1, w >> 1, 0);
    v0[5] = TA(1, w >> 1, 8);
    v0[6] = TA(2, w >> 1, 8);
    c0[0] = l16;
    c0[1] = lane ? 0x80000000u : 0u;
    c0[2] = 142336u + lane * 4;
    c0[3] = 131072u + (w >> 1) * 2048 + l16;
    c0[4] = 139264u + li * 8;
    c0[5] = 139264u + lg * 32;
    c0[6] = 1384448u + 0 + (w >> 1) * 4096 + l16;
    c0[7] = 1384448u + 8192 + (w >> 1) * 4096 + l16;
    c0[8] = 1384448u + 16384 + (w >> 1) * 4096 + l16;
    c0[9] = 1384448u + 24576 + (w >> 1) * 4096 + l16;
    c0[10] = lane >= 16 && lane < 28 ? FW(p, SPARE((lane - 16) >> 2), lane & 3) : (lane >> 1) == 14 && p ? FW(p - 1, slot, (lane & 1) * 2) : FW(p, 43 + ((lane & 15) >> 2), lane & 3);
    c0[11] = lane >= 16 && lane < 28 ? 6u : (unsigned)-26;
    c0[12] = 1036288u + lane * 8;
  } else if (slot < 22) {
    s0[12] = 8 + (w < 2 ? w : 2); wp = rOp; wn = rOn;
    s0[13] = blk0(-2);
    s0[14] = w * 8192;
    s0[15] = w * 8 * 2048 + (slot - 16) * 384 + rot * 524288;
    s1[5] = w * 8 * 2048 + (slot - 16) * 384 + 2097152u;
    v0[0] = (lane >> 3) * 2048 + (((lane & 7) ^ ((lane >> 2) & 4)) << 4) + ((slot - 16) * 6 + 0 < 32 ? 0u : 0x80000000u);
    v0[1] = (lane >> 3) * 2048 + (((lane & 7) ^ ((lane >> 2) & 4)) << 4) + ((slot - 16) * 6 + 2 < 32 ? 128u : 0x80000000u);
    v0[2] = (lane >> 3) * 2048 + (((lane & 7) ^ ((lane >> 2) & 4)) << 4) + ((slot - 16) * 6 + 4 < 32 ? 256u : 0x80000000u);
    v0[4] = TA(((w >> 1) * 3 + (w & 1)) >> 1, ((w >> 1) * 3 + (w & 1)) & 1, 0);
    v0[5] = TA(((w >> 1) * 3 + (w & 1)) >> 1, ((w >> 1) * 3 + (w & 1)) & 1, 8);
    v0[6] = TO(((w >> 1) * 3 + 2) >> 1, ((w >> 1) * 3 + 2) & 1, w & 1);
    c0[0] = l16;
    c0[1] = lane ? 0x80000000u : 0u;
    c0[2] = 142336u + lane * 4;
    c0[3] = ((slot - 16) * 6 + (w >> 1) * 3 + (w & 1)) < 32 ? 411648u + ((slot - 16) * 6 + (w >> 1) * 3 + (w & 1)) * 1024 + l16 : 0x80000000u;
    c0[4] = ((slot - 16) * 6 + (w >> 1) * 3 + 2) < 32 && !(lg & 1) ? 411648u + ((slot - 16) * 6 + (w >> 1) * 3 + 2) * 1024 + ((w & 1) * 2 + (lg >> 1)) * 256 + li * 16 : 0x80000000u;
    c0[5] = 1071104u + wr * 8192 + l16;
    c0[6] = 1075200u + wr * 8192 + l16;
    c0[7] = FW(p, (lane & 31) >> 1, (lane & 1) * 2);
    c0[8] = 1u;
    c0[9] = ((slot - 16) * 6 + (w >> 1) * 3 + (w & 1)) < 32 ? 1384448u + ((slot - 16) * 6 + (w >> 1) * 3 + (w & 1)) * 1024 + l16 : 0x80000000u;
    c0[10] = ((slot - 16) * 6 + (w >> 1) * 3 + 2) < 32 ? 1384448u + ((slot - 16) * 6 + (w >> 1) * 3 + 2) * 1024 + ((w & 1) * 2 + (lg >> 1)) * 256 + li * 16 + (lg & 1) * 8 : 0x80000000u;
    c0[11] = 140288u + ((li * 2 + (lg >> 1)) * 8 + w * 2 + (lg & 1)) * 4;
    c0[12] = 140288u + l16;
    c0[13] = (lane & 1) ? 0x80000000u : 345088u + (slot - 16) * 128 + (lane >> 1) * 4;
  } else if (slot < 43) {
    s0[12] = 12 + (w < 2 ? w : 2); wp = r1p; wn = r1n;
    s0[13] = blk0(-2);
    s0[14] = w * 8192;
    s0[15] = w * 8 * 7168 + (slot - 24) * 384 + rot * 1835008;
    s1[5] = w * 8 * 7168 + (slot - 24) * 384 + 7340032u;
    v0[0] = (lane >> 3) * 7168 + (((lane & 7) ^ ((lane >> 2) & 4)) << 4) + ((slot - 24) * 6 + 0 < 112 ? 0u : 0x80000000u);
    v0[1] = (lane >> 3) * 7168 + (((lane & 7) ^ ((lane >> 2) & 4)) << 4) + ((slot - 24) * 6 + 2 < 112 ? 128u : 0x80000000u);
    v0[2] = (lane >> 3) * 7168 + (((lane & 7) ^ ((lane >> 2) & 4)) << 4) + ((slot - 24) * 6 + 4 < 112 ? 256u : 0x80000000u);
    v0[4] = TA(((w >> 1) * 3 + (w & 1)) >> 1, ((w >> 1) * 3 + (w & 1)) & 1, 0);
    v0[5] = TA(((w >> 1) * 3 + (w & 1)) >> 1, ((w >> 1) * 3 + (w & 1)) & 1, 8);
    v0[6] = TO(((w >> 1) * 3 + 2) >> 1, ((w >> 1) * 3 + 2) & 1, w & 1);
    c0[0] = l16;
    c0[1] = lane ? 0x80000000u : 0u;
    c0[2] = 142336u + lane * 4;
    c0[3] = ((slot - 24) * 6 + (w >> 1) * 3 + (w & 1)) < 112 ? 444416u + ((slot - 24) * 6 + (w >> 1) * 3 + (w & 1)) * 1024 + l16 : 0x80000000u;
    c0[4] = ((slot - 24) * 6 + (w >> 1) * 3 + 2) < 112 && !(lg & 1) ? 444416u + ((slot - 24) * 6 + (w >> 1) * 3 + 2) * 1024 + ((w & 1) * 2 + (lg >> 1)) * 256 + li * 16 : 0x80000000u;
    c0[5] = 1103872u + wr * 8192 + l16;
    c0[6] = 1107968u + wr * 8192 + l16;
    c0[7] = lane < 24 ? FW(p, 16 + (lane >> 2), lane & 3) : FW(p, 16, 0);
    c0[8] = 1u;
    c0[9] = 139264u + li * 8;
    c0[10] = 1037312u + lane * 8;
    c0[11] = lane < 32 ? 1037824u + lane * 8 : 0x80000000u;
    c0[12] = BIAS(m.b1, 3584, ((slot - 24) * 6 + (w >> 1) * 3 + (w & 1)) * 32 + lg * 8 + 0);
    c0[13] = BIAS(m.b1, 3584, ((slot - 24) * 6 + (w >> 1) * 3 + (w & 1)) * 32 + lg * 8 + 1);
    c0[14] = BIAS(m.b1, 3584, ((slot - 24) * 6 + (w >> 1) * 3 + (w & 1)) * 32 + lg * 8 + 2);
    c0[15] = BIAS(m.b1, 3584, ((slot - 24) * 6 + (w >> 1) * 3 + (w & 1)) * 32 + lg * 8 + 3);
    c1[0] = BIAS(m.b1, 3584, ((slot - 24) * 6 + (w >> 1) * 3 + (w & 1)) * 32 + lg * 8 + 4);
    c1[1] = BIAS(m.b1, 3584, ((slot - 24) * 6 + (w >> 1) * 3 + (w & 1)) * 32 + lg * 8 + 5);
    c1[2] = BIAS(m.b1, 3584, ((slot - 24) * 6 + (w >> 1) * 3 + (w & 1)) * 32 + lg * 8 + 6);
    c1[3] = BIAS(m.b1, 3584, ((slot - 24) * 6 + (w >> 1) * 3 + (w & 1)) * 32 + lg * 8 + 7);
    c1[4] = BIAS(m.b1, 3584, ((slot - 24) * 6 + (w >> 1) * 3 + 2) * 32 + (w & 1) * 16 + lg * 4 + 0);
    c1[5] = BIAS(m.b1, 3584, ((slot - 24) * 6 + (w >> 1) * 3 + 2) * 32 + (w & 1) * 16 + lg * 4 + 1);
    c1[6] = BIAS(m.b1, 3584, ((slot - 24) * 6 + (w >> 1) * 3 + 2) * 32 + (w & 1) * 16 + lg * 4 + 2);
    c1[7] = BIAS(m.b1, 3584, ((slot - 24) * 6 + (w >> 1) * 3 + 2) * 32 + (w & 1) * 16 + lg * 4 + 3);
  } else if (slot >= 48) {
    s0[12] = 16 + (w ? 1 : 0); wp = r2p; wn = r2n;
    s0[13] = blk0(-2);
    s0[14] = w * 6144;
    s0[15] = (((slot - 48) >> 2) * 768 + w * 8) * 2048 + (slot & 3) * 512 + rot * 393216;
    s1[5] = (((slot - 48) >> 2) * 768 + w * 8) * 2048 + (slot & 3) * 512 + 1572864u;
    v0[0] = (lane >> 3) * 2048 + (((lane & 7) ^ ((lane >> 2) & 4)) << 4) + 0u;
    v0[1] = (lane >> 3) * 2048 + (((lane & 7) ^ ((lane >> 2) & 4)) << 4) + 128u;
    v0[2] = (lane >> 3) * 2048 + (((lane & 7) ^ ((lane >> 2) & 4)) << 4) + 256u;
    v0[3] = (lane >> 3) * 2048 + (((lane & 7) ^ ((lane >> 2) & 4)) << 4) + 384u;
    v0[4] = TA(w, 0, 0);
    v0[5] = TA(w, 0, 8);
    v0[6] = TA(w, 1, 0);
    v0[7] = TA(w, 1, 8);
    c0[0] = l16;
    c0[1] = lane ? 0x80000000u : 0u;
    c0[2] = 142336u + lane * 4;
    c0[3] = 1136640u + ((slot - 48) >> 2) * 24576 + wr * 6144 + l16;
    c0[4] = 1140736u + ((slot - 48) >> 2) * 24576 + wr * 6144 + l16;
    c0[5] = FW(p, 24 + ((slot - 48) & ~3) + ((lane & 15) >> 2), lane & 3);
    c0[6] = 1u;
    c0[7] = 559104u + (slot - 48) * 8192 + w * 2048 + l16;
  } else if (true) {
    s0[12] = 20 + (w < 2 ? w : 2); wp = r2p; wn = r2n;
    const u16* const b2p = arg_b2();
    s0[13] = blk0(-3);
    s0[14] = w * 4096;
    s0[15] = (3072 + w * 8) * 2048 + (slot - 43) * 512 + rot * 262144;
    s1[2] = (p * 32 + 31) * 32768;
    s1[5] = (3072 + w * 8) * 2048 + (slot - 43) * 512 + 1048576u;
    v0[0] = (lane >> 3) * 2048 + (((lane & 7) ^ ((lane >> 2) & 4)) << 4) + 0u;
    v0[1] = (lane >> 3) * 2048 + (((lane & 7) ^ ((lane >> 2) & 4)) << 4) + 128u;
    v0[2] = (lane >> 3) * 2048 + (((lane & 7) ^ ((lane >> 2) & 4)) << 4) + 256u;
    v0[3] = (lane >> 3) * 2048 + (((lane & 7) ^ ((lane >> 2) & 4)) << 4) + 384u;
    v0[4] = TA(w, 0, 0);
    v0[5] = TA(w, 0, 8);
    v0[6] = TA(w, 1, 0);
    v0[7] = TA(w, 1, 8);
    c0[0] = l16;
    c0[1] = lane ? 0x80000000u : 0u;
    c0[2] = 142336u + lane * 4;
    c0[3] = 1581056u + wr * 4096 + l16;
    c0[4] = (lane >> 4) == 1 ? FW(p, 48 + (lane & 12) + slot - 43, lane & 3) : lane < 12 ? FW(p, 40 + (lane >> 2), lane & 3) : FW(p, 40, 0);
    c0[5] = (lane >> 4) == 1 ? (unsigned)-1 : 1u;
    c0[6] = 905216u + (slot - 43) * 8192 + w * 2048 + l16;
    c0[7] = 937984u + (slot - 43) * 8192 + w * 2048 + l16;
    c0[8] = 970752u + (slot - 43) * 8192 + w * 2048 + l16;
    c0[9] = 1003520u + (slot - 43) * 8192 + w * 2048 + l16;
    c0[10] = 1103872u + (slot - 43) * 8192 + w * 2048 + l16;
    c0[11] = 11421696u + (slot - 43) * 8192 + w * 2048 + l16;
    c0[12] = (lane >> 3) * 2048 + (lane & 7) * 16 + (slot - 43) * 512 + w * 128;
    c0[13] = ((lane >> 3) + 8) * 2048 + (lane & 7) * 16 + (slot - 43) * 512 + w * 128;
    c0[14] = 131072u + w * 2048 + li * 128 + (((0 + lg) ^ (li & 7)) << 4);
    c0[15] = 131072u + w * 2048 + li * 128 + (((4 + lg) ^ (li & 7)) << 4);
    c1[0] = 131072u + w * 2048 + (lane >> 3) * 128 + (((lane ^ (lane >> 3)) & 7) << 4);
    c1[1] = 140288u + ((li * 2 + (lg >> 1)) * 8 + w * 2 + (lg & 1)) * 4;
    c1[2] = 140288u + l16;
    c1[3] = (lane & 1) ? 0x80000000u : 11419648u + (slot - 43) * 128 + (lane >> 1) * 4;
    c1[4] = BIAS(b2p, 1024, (slot - 43) * 256 + w * 64 + 0 + lg * 8);
    c1[5] = BIAS(b2p, 1024, (slot - 43) * 256 + w * 64 + 1 + lg * 8);
    c1[6] = BIAS(b2p, 1024, (slot - 43) * 256 + w * 64 + 2 + lg * 8);
    c1[7] = BIAS(b2p, 1024, (slot - 43) * 256 + w * 64 + 3 + lg * 8);
    c1[8] = BIAS(b2p, 1024, (slot - 43) * 256 + w * 64 + 4 + lg * 8);
    c1[9] = BIAS(b2p, 1024, (slot - 43) * 256 + w * 64 + 5 + lg * 8);
    c1[10] = BIAS(b2p, 1024, (slot - 43) * 256 + w * 64 + 6 + lg * 8);
    c1[11] = BIAS(b2p, 1024, (slot - 43) * 256 + w * 64 + 7 + lg * 8);
    c1[12] = BIAS(b2p, 1024, (slot - 43) * 256 + w * 64 + 32 + lg * 8);
    c1[13] = BIAS(b2p, 1024, (slot - 43) * 256 + w * 64 + 33 + lg * 8);
    c1[14] = BIAS(b2p, 1024, (slot - 43) * 256 + w * 64 + 34 + lg * 8);
    c1[15] = BIAS(b2p, 1024, (slot - 43) * 256 + w * 64 + 35 + lg * 8);
    c2[0] = BIAS(b2p, 1024, (slot - 43) * 256 + w * 64 + 36 + lg * 8);
    c2[1] = BIAS(b2p, 1024, (slot - 43) * 256 + w * 64 + 37 + lg * 8);
    c2[2] = BIAS(b2p, 1024, (slot - 43) * 256 + w * 64 + 38 + lg * 8);
    c2[3] = BIAS(b2p, 1024, (slot - 43) * 256 + w * 64 + 39 + lg * 8);
    c2[4] = (lg == (li >> 2) && ((li & 3) >> 1) + 0 == 0) ? ((li & 1) ? 0x3f800000u : 0x3f80u) : 0u;
    c2[5] = (lg == (li >> 2) && ((li & 3) >> 1) + 0 == 1) ? ((li & 1) ? 0x3f800000u : 0x3f80u) : 0u;
    c2[6] = (lg == (li >> 2) && ((li & 3) >> 1) + 0 == 2) ? ((li & 1) ? 0x3f800000u : 0x3f80u) : 0u;
    c2[7] = (lg == (li >> 2) && ((li & 3) >> 1) + 0 == 3) ? ((li & 1) ? 0x3f800000u : 0x3f80u) : 0u;
    c2[8] = (lg == (li >> 2) && ((li & 3) >> 1) + 2 == 0) ? ((li & 1) ? 0x3f800000u : 0x3f80u) : 0u;
    c2[9] = (lg == (li >> 2) && ((li & 3) >> 1) + 2 == 1) ? ((li & 1) ? 0x3f800000u : 0x3f80u) : 0u;
    c2[10] = (lg == (li >> 2) && ((li & 3) >> 1) + 2 == 2) ? ((li & 1) ? 0x3f800000u : 0x3f80u) : 0u;
    c2[11] = (lg == (li >> 2) && ((li & 3) >> 1) + 2 == 3) ? ((li & 1) ? 0x3f800000u : 0x3f80u) : 0u;
  }
  s0[0] = (unsigned)(size_t)m.ws; s0[1] = (unsigned)((size_t)m.ws >> 32) & 0xffffu; s0[2] = WS_END; s0[3] = 0x00020000;
  s0[4] = (unsigned)(size_t)wp; s0[5] = (unsigned)((size_t)wp >> 32) & 0xffffu; s0[6] = wn; s0[7] = 0x00020000;
  s0[8] = (unsigned)(size_t)op; s0[9] = (unsigned)((size_t)op >> 32) & 0xffffu; s0[10] = 2048 * 1024 * 2; s0[11] = 0x00020000;
  s1[0] = FW(p, slot, w); s1[1] = w * 1024; s1[7] = DBG + ((p * 64 + slot) * 4 + w) * 1024;
#pragma unroll
  for (int i = 0; i < 16; ++i) s0[i] = __builtin_amdgcn_readfirstlane(s0[i]);
#pragma unroll
  for (int i = 0; i < 8; ++i) s1[i] = __builtin_amdgcn_readfirstlane(s1[i]);
  asm volatile(
R"(
.macro mav d,a,b
v_mfma_f32_16x16x32_bf16 v[\d:\d+3], a[\a:\a+3], v[\b:\b+3], v[\d:\d+3]
.endm
.macro Ta d,a,o
ds_read_b64_tr_b16 a[\d:\d+1], v\a offset:\o
ds_read_b64_tr_b16 a[\d+2:\d+3], v\a offset:\o+512
.endm
.macro mva d,a,b
v_mfma_f32_16x16x32_bf16 v[\d:\d+3], v[\a:\a+3], a[\b:\b+3], v[\d:\d+3]
.endm
.macro mvv d,a,b
v_mfma_f32_16x16x32_bf16 v[\d:\d+3], v[\a:\a+3], v[\b:\b+3], v[\d:\d+3]
.endm
.macro Tv d,a,o
ds_read_b64_tr_b16 v[\d:\d+1], v\a offset:\o
ds_read_b64_tr_b16 v[\d+2:\d+3], v\a offset:\o+512
.endm
.macro R d,o
ds_read_b128 v[\d:\d+3], v80 offset:\o
.endm
.macro D v,o
buffer_load_dwordx4 v\v, s[36:39], s49 offen offset:\o sc1 lds
.endm
.macro E v,o
s_add_u32 m0, s53, \o
s_nop 0
buffer_load_dwordx4 v\v, s[40:43], s51 offen lds
.endm
s_nop 4
s_mov_b32 s61, 200000
s_mov_b64 s[62:63], 255
s_mov_b32 s64, 0x3f803f80
s_mov_b32 s65, 0x3e8293ee
s_lshr_b32 s60, s48, 2
Lr0:
s_cmp_eq_u32 s60, 0
s_cbranch_scc0 Lr1
buffer_load_dword v79, off, s[36:39], 0 sc1
s_lshr_b32 s66, s53, 10
s_mul_i32 s66, s66, 8208
s_mov_b32 s67, 0x8000
E 0,110592
E 1,114688
s_add_u32 m0, s66, 2048
s_nop 0
buffer_load_dwordx4 v3, s[44:47], s67 offen offset:0 lds
E 2,118784
s_add_u32 s51, s51, 196608
E 0,122880
s_add_u32 m0, s66, 6144
s_nop 0
buffer_load_dwordx4 v3, s[44:47], 0 offen offset:0 lds
E 1,126976
E 2,131072
s_add_u32 s51, s51, 196608
E 0,86016
E 1,90112
E 2,94208
s_add_u32 s51, s51, 196608
E 0,98304
E 1,102400
E 2,106496
s_add_u32 s51, s51, 196608
v_lshlrev_b32 v12, 2, v80
v_and_b32 v12, 0xc0, v12
v_xor_b32 v13, 0, v12
v_add_u32 v8, v7, v13
v_xor_b32 v13, 64, v12
v_add_u32 v9, v7, v13
v_xor_b32 v13, 128, v12
v_add_u32 v10, v7, v13
v_xor_b32 v13, 192, v12
v_add_u32 v11, v7, v13
v_mov_b32 v110, s64
v_mov_b32 v111, s64
v_mov_b32 v112, s64
v_mov_b32 v113, s64
s_waitcnt vmcnt(6)
s_barrier
v_add_u32 v4, 69632, v4
v_add_u32 v5, 69632, v5
v_add_u32 v6, 69632, v6
Ta 0,4,40960
Ta 4,5,40960
Ta 8,6,40960
Ta 12,4,53248
Ta 16,5,53248
Ta 20,6,53248
ds_read_b128 v[38:41], v8 offset:0
ds_read_b128 v[42:45], v9 offset:0
ds_read_b128 v[46:49], v8 offset:4096
ds_read_b128 v[50:53], v9 offset:4096
v_mov_b32 v78, 0x100
v_add_u32 v79, 0x9e3779b9, v79
buffer_store_dwordx2 v[78:79], off, s[36:39], s52 sc1
E 0,61440
E 1,65536
s_add_u32 m0, s66, 2816
s_nop 0
buffer_load_dwordx4 v3, s[44:47], s67 offen offset:256 lds
E 2,69632
s_add_u32 s51, s51, 196608
E 0,73728
s_add_u32 m0, s66, 6912
s_nop 0
buffer_load_dwordx4 v3, s[44:47], 0 offen offset:256 lds
E 1,77824
E 2,81920
s_add_u32 s51, s51, 196608
E 0,36864
E 1,40960
E 2,45056
s_add_u32 s51, s51, 196608
E 0,49152
E 1,53248
E 2,57344
s_add_u32 s51, s51, 196608
s_waitcnt lgkmcnt(0)
s_waitcnt vmcnt(15)
s_barrier
E 0,110592
Ta 24,4,16384
v_mfma_f32_16x16x32_bf16 v[62:65], a[0:3], v[110:113], 0
v_mfma_f32_16x16x32_bf16 v[66:69], a[4:7], v[110:113], 0
Ta 28,5,16384
v_mfma_f32_16x16x32_bf16 v[70:73], v[110:113], a[8:11], 0
Ta 32,6,16384
E 1,114688
v_mfma_f32_16x16x32_bf16 v[14:17], a[0:3], v[38:41], 0
Ta 36,4,28672
v_mfma_f32_16x16x32_bf16 v[18:21], a[4:7], v[38:41], 0
Ta 40,5,28672
s_add_u32 m0, s66, 3584
s_nop 0
buffer_load_dwordx4 v3, s[44:47], s67 offen offset:512 lds
v_mfma_f32_16x16x32_bf16 v[22:25], v[38:41], a[8:11], 0
Ta 44,6,28672
v_mfma_f32_16x16x32_bf16 v[26:29], a[0:3], v[46:49], 0
ds_read_b128 v[54:57], v10 offset:0
E 2,118784
s_add_u32 s51, s51, 196608
v_mfma_f32_16x16x32_bf16 v[30:33], a[4:7], v[46:49], 0
ds_read_b128 v[58:61], v11 offset:0
v_mfma_f32_16x16x32_bf16 v[34:37], v[46:49], a[8:11], 0
ds_read_b128 v[96:99], v10 offset:4096
E 0,122880
mav 62,12,110
ds_read_b128 v[100:103], v11 offset:4096
mav 66,16,110
mva 70,110,20
s_add_u32 m0, s66, 7680
s_nop 0
buffer_load_dwordx4 v3, s[44:47], 0 offen offset:512 lds
mav 14,12,42
mav 18,16,42
E 1,126976
mva 22,42,20
mav 26,12,50
E 2,131072
s_add_u32 s51, s51, 196608
mav 30,16,50
mva 34,50,20
s_waitcnt lgkmcnt(0)
s_waitcnt vmcnt(14)
s_barrier
v_add_u32 v4, -49152, v4
v_add_u32 v5, -49152, v5
v_add_u32 v6, -49152, v6
E 0,86016
Ta 48,4,40960
mav 62,24,110
mav 66,28,110
Ta 52,5,40960
mva 70,110,32
Ta 56,6,40960
E 1,90112
mav 14,24,54
Ta 60,4,53248
mav 18,28,54
Ta 64,5,53248
mva 22,54,32
Ta 68,6,53248
E 2,94208
s_add_u32 s51, s51, 196608
mav 26,24,96
ds_read_b128 v[38:41], v8 offset:1024
mav 30,28,96
ds_read_b128 v[42:45], v9 offset:1024
mva 34,96,32
ds_read_b128 v[46:49], v8 offset:5120
E 0,98304
mav 62,36,110
ds_read_b128 v[50:53], v9 offset:5120
mav 66,40,110
mva 70,110,44
E 1,102400
mav 14,36,58
mav 18,40,58
mva 22,58,44
E 2,106496
s_add_u32 s51, s51, 196608
mav 26,36,100
mav 30,40,100
mva 34,100,44
s_waitcnt lgkmcnt(0)
s_waitcnt vmcnt(14)
s_barrier
E 0,61440
Ta 72,4,16384
mav 62,48,110
mav 66,52,110
Ta 76,5,16384
mva 70,110,56
Ta 80,6,16384
E 1,65536
mav 14,48,38
Ta 84,4,28672
mav 18,52,38
Ta 88,5,28672
s_add_u32 m0, s66, 4352
s_nop 0
buffer_load_dwordx4 v3, s[44:47], s67 offen offset:768 lds
mva 22,38,56
Ta 92,6,28672
mav 26,48,46
ds_read_b128 v[54:57], v10 offset:1024
E 2,69632
s_add_u32 s51, s51, 196608
mav 30,52,46
ds_read_b128 v[58:61], v11 offset:1024
mva 34,46,56
ds_read_b128 v[96:99], v10 offset:5120
E 0,73728
mav 62,60,110
ds_read_b128 v[100:103], v11 offset:5120
mav 66,64,110
mva 70,110,68
s_add_u32 m0, s66, 8448
s_nop 0
buffer_load_dwordx4 v3, s[44:47], 0 offen offset:768 lds
mav 14,60,42
mav 18,64,42
E 1,77824
mva 22,42,68
mav 26,60,50
E 2,81920
s_add_u32 s51, s51, 196608
mav 30,64,50
mva 34,50,68
s_waitcnt lgkmcnt(0)
s_waitcnt vmcnt(14)
s_barrier
v_add_u32 v4, 49152, v4
v_add_u32 v5, 49152, v5
v_add_u32 v6, 49152, v6
E 0,36864
Ta 96,4,40960
mav 62,72,110
mav 66,76,110
Ta 100,5,40960
mva 70,110,80
Ta 104,6,40960
E 1,40960
mav 14,72,54
Ta 108,4,53248
mav 18,76,54
Ta 112,5,53248
mva 22,54,80
Ta 116,6,53248
E 2,45056
s_add_u32 s51, s51, 196608
mav 26,72,96
ds_read_b128 v[38:41], v8 offset:2048
mav 30,76,96
ds_read_b128 v[42:45], v9 offset:2048
mva 34,96,80
ds_read_b128 v[46:49], v8 offset:6144
E 0,49152
mav 62,84,110
ds_read_b128 v[50:53], v9 offset:6144
mav 66,88,110
mva 70,110,92
E 1,53248
mav 14,84,58
mav 18,88,58
mva 22,58,92
E 2,57344
s_add_u32 s51, s51, 196608
mav 26,84,100
mav 30,88,100
mva 34,100,92
s_waitcnt lgkmcnt(0)
s_waitcnt vmcnt(14)
s_barrier
E 0,110592
Ta 120,4,16384
mav 62,96,110
mav 66,100,110
Ta 124,5,16384
mva 70,110,104
Ta 128,6,16384
E 1,114688
mav 14,96,38
Ta 132,4,28672
mav 18,100,38
Ta 136,5,28672
s_add_u32 m0, s66, 1024
s_nop 0
buffer_load_dwordx4 v3, s[44:47], s67 offen offset:1024 lds
mva 22,38,104
Ta 140,6,28672
mav 26,96,46
ds_read_b128 v[54:57], v10 offset:2048
E 2,118784
s_add_u32 s51, s51, 196608
mav 30,100,46
ds_read_b128 v[58:61], v11 offset:2048
mva 34,46,104
ds_read_b128 v[96:99], v10 offset:6144
E 0,122880
mav 62,108,110
ds_read_b128 v[100:103], v11 offset:6144
mav 66,112,110
mva 70,110,116
s_add_u32 m0, s66, 5120
s_nop 0
buffer_load_dwordx4 v3, s[44:47], 0 offen offset:1024 lds
mav 14,108,42
mav 18,112,42
E 1,126976
mva 22,42,116
mav 26,108,50
E 2,131072
s_add_u32 s51, s51, 196608
mav 30,112,50
mva 34,50,116
s_waitcnt lgkmcnt(0)
s_waitcnt vmcnt(14)
s_barrier
v_add_u32 v4, -49152, v4
v_add_u32 v5, -49152, v5
v_add_u32 v6, -49152, v6
E 0,86016
Ta 144,4,40960
mav 62,120,110
mav 66,124,110
Ta 148,5,40960
mva 70,110,128
Ta 152,6,40960
E 1,90112
mav 14,120,54
Ta 156,4,53248
mav 18,124,54
Ta 160,5,53248
mva 22,54,128
Ta 164,6,53248
E 2,94208
s_add_u32 s51, s51, 196608
mav 26,120,96
ds_read_b128 v[38:41], v8 offset:3072
mav 30,124,96
ds_read_b128 v[42:45], v9 offset:3072
mva 34,96,128
ds_read_b128 v[46:49], v8 offset:7168
E 0,98304
mav 62,132,110
ds_read_b128 v[50:53], v9 offset:7168
mav 66,136,110
mva 70,110,140
E 1,102400
mav 14,132,58
mav 18,136,58
mva 22,58,140
E 2,106496
s_add_u32 s51, s51, 196608
mav 26,132,100
mav 30,136,100
mva 34,100,140
s_waitcnt lgkmcnt(0)
s_waitcnt vmcnt(14)
s_barrier
E 0,61440
Ta 168,4,16384
mav 62,144,110
mav 66,148,110
Ta 172,5,16384
mva 70,110,152
Ta 176,6,16384
E 1,65536
mav 14,144,38
Ta 180,4,28672
mav 18,148,38
Ta 184,5,28672
s_add_u32 m0, s66, 1792
s_nop 0
buffer_load_dwordx4 v3, s[44:47], s67 offen offset:1280 lds
mva 22,38,152
Ta 188,6,28672
mav 26,144,46
ds_read_b128 v[54:57], v10 offset:3072
E 2,69632
s_add_u32 s51, s51, 196608
mav 30,148,46
ds_read_b128 v[58:61], v11 offset:3072
mva 34,46,152
ds_read_b128 v[96:99], v10 offset:7168
E 0,73728
mav 62,156,110
ds_read_b128 v[100:103], v11 offset:7168
mav 66,160,110
mva 70,110,164
s_add_u32 m0, s66, 5888
s_nop 0
buffer_load_dwordx4 v3, s[44:47], 0 offen offset:1280 lds
mav 14,156,42
mav 18,160,42
E 1,77824
mva 22,42,164
mav 26,156,50
E 2,81920
s_add_u32 s51, s51, 196608
mav 30,160,50
mva 34,50,164
s_waitcnt lgkmcnt(0)
s_waitcnt vmcnt(14)
s_barrier
v_add_u32 v4, 49152, v4
v_add_u32 v5, 49152, v5
v_add_u32 v6, 49152, v6
E 0,36864
Ta 192,4,40960
mav 62,168,110
mav 66,172,110
Ta 196,5,40960
mva 70,110,176
Ta 200,6,40960
E 1,40960
mav 14,168,54
Ta 204,4,53248
mav 18,172,54
Ta 208,5,53248
mva 22,54,176
Ta 212,6,53248
E 2,45056
s_add_u32 s51, s51, 196608
mav 26,168,96
ds_read_b128 v[38:41], v8 offset:0
mav 30,172,96
ds_read_b128 v[42:45], v9 offset:0
mva 34,96,176
ds_read_b128 v[46:49], v8 offset:4096
E 0,49152
mav 62,180,110
ds_read_b128 v[50:53], v9 offset:4096
mav 66,184,110
mva 70,110,188
E 1,53248
mav 14,180,58
mav 18,184,58
mva 22,58,188
E 2,57344
s_add_u32 s51, s51, 196608
mav 26,180,100
mav 30,184,100
mva 34,100,188
s_waitcnt lgkmcnt(0)
s_waitcnt vmcnt(14)
s_barrier
E 0,110592
Ta 216,4,16384
mav 62,192,110
mav 66,196,110
Ta 220,5,16384
mva 70,110,200
Ta 224,6,16384
E 1,114688
mav 14,192,38
Ta 228,4,28672
mav 18,196,38
Ta 232,5,28672
s_add_u32 m0, s66, 2560
s_nop 0
buffer_load_dwordx4 v3, s[44:47], s67 offen offset:1536 lds
mva 22,38,200
Ta 236,6,28672
mav 26,192,46
ds_read_b128 v[54:57], v10 offset:0
E 2,118784
s_add_u32 s51, s51, 196608
mav 30,196,46
ds_read_b128 v[58:61], v11 offset:0
mva 34,46,200
ds_read_b128 v[96:99], v10 offset:4096
E 0,122880
mav 62,204,110
ds_read_b128 v[100:103], v11 offset:4096
mav 66,208,110
mva 70,110,212
s_add_u32 m0, s66, 6656
s_nop 0
buffer_load_dwordx4 v3, s[44:47], 0 offen offset:1536 lds
mav 14,204,42
mav 18,208,42
E 1,126976
mva 22,42,212
mav 26,204,50
E 2,131072
s_add_u32 s51, s51, 196608
mav 30,208,50
mva 34,50,212
s_waitcnt lgkmcnt(0)
s_waitcnt vmcnt(14)
s_barrier
v_add_u32 v4, -49152, v4
v_add_u32 v5, -49152, v5
v_add_u32 v6, -49152, v6
E 0,86016
Ta 240,4,40960
mav 62,216,110
mav 66,220,110
Ta 244,5,40960
mva 70,110,224
Ta 248,6,40960
E 1,90112
mav 14,216,54
Ta 252,4,53248
mav 18,220,54
Tv 128,5,53248
mva 22,54,224
Tv 132,6,53248
E 2,94208
s_add_u32 s51, s51, 196608
mav 26,216,96
ds_read_b128 v[38:41], v8 offset:1024
mav 30,220,96
ds_read_b128 v[42:45], v9 offset:1024
mva 34,96,224
ds_read_b128 v[46:49], v8 offset:5120
E 0,98304
mav 62,228,110
ds_read_b128 v[50:53], v9 offset:5120
mav 66,232,110
mva 70,110,236
E 1,102400
mav 14,228,58
mav 18,232,58
mva 22,58,236
E 2,106496
s_add_u32 s51, s51, 196608
mav 26,228,100
mav 30,232,100
mva 34,100,236
s_waitcnt lgkmcnt(0)
s_waitcnt vmcnt(14)
s_barrier
E 0,61440
Tv 136,4,16384
mav 62,240,110
mav 66,244,110
Tv 140,5,16384
mva 70,110,248
Tv 144,6,16384
E 1,65536
mav 14,240,38
Tv 148,4,28672
mav 18,244,38
Tv 152,5,28672
s_add_u32 m0, s66, 3328
s_nop 0
buffer_load_dwordx4 v3, s[44:47], s67 offen offset:1792 lds
mva 22,38,248
Tv 156,6,28672
mav 26,240,46
ds_read_b128 v[54:57], v10 offset:1024
E 2,69632
s_add_u32 s51, s51, 196608
mav 30,244,46
ds_read_b128 v[58:61], v11 offset:1024
mva 34,46,248
ds_read_b128 v[96:99], v10 offset:5120
E 0,73728
mav 62,252,110
ds_read_b128 v[100:103], v11 offset:5120
mvv 66,128,110
mvv 70,110,132
s_add_u32 m0, s66, 7424
s_nop 0
buffer_load_dwordx4 v3, s[44:47], 0 offen offset:1792 lds
mav 14,252,42
mvv 18,128,42
E 1,77824
mvv 22,42,132
mav 26,252,50
E 2,81920
s_add_u32 s51, s51, 196608
mvv 30,128,50
mvv 34,50,132
s_waitcnt lgkmcnt(0)
s_waitcnt vmcnt(14)
s_barrier
v_add_u32 v4, 49152, v4
v_add_u32 v5, 49152, v5
v_add_u32 v6, 49152, v6
E 0,36864
Tv 160,4,40960
mvv 62,136,110
mvv 66,140,110
Tv 164,5,40960
mvv 70,110,144
Tv 168,6,40960
E 1,40960
mvv 14,136,54
Tv 172,4,53248
mvv 18,140,54
Tv 176,5,53248
mvv 22,54,144
Tv 180,6,53248
E 2,45056
s_add_u32 s51, s51, 196608
mvv 26,136,96
ds_read_b128 v[38:41], v8 offset:2048
mvv 30,140,96
ds_read_b128 v[42:45], v9 offset:2048
mvv 34,96,144
ds_read_b128 v[46:49], v8 offset:6144
E 0,49152
mvv 62,148,110
ds_read_b128 v[50:53], v9 offset:6144
mvv 66,152,110
mvv 70,110,156
E 1,53248
mvv 14,148,58
mvv 18,152,58
mvv 22,58,156
E 2,57344
s_add_u32 s51, s51, 196608
mvv 26,148,100
mvv 30,152,100
mvv 34,100,156
s_waitcnt lgkmcnt(0)
s_waitcnt vmcnt(14)
s_barrier
Tv 184,4,16384
mvv 62,160,110
mvv 66,164,110
Tv 188,5,16384
mvv 70,110,168
Tv 192,6,16384
mvv 14,160,38
Tv 196,4,28672
mvv 18,164,38
Tv 200,5,28672
mvv 22,38,168
Tv 204,6,28672
mvv 26,160,46
ds_read_b128 v[54:57], v10 offset:2048
mvv 30,164,46
ds_read_b128 v[58:61], v11 offset:2048
mvv 34,46,168
ds_read_b128 v[96:99], v10 offset:6144
mvv 62,172,110
ds_read_b128 v[100:103], v11 offset:6144
mvv 66,176,110
mvv 70,110,180
mvv 14,172,42
mvv 18,176,42
mvv 22,42,180
mvv 26,172,50
mvv 30,176,50
mvv 34,50,180
s_waitcnt lgkmcnt(0)
s_waitcnt vmcnt(6)
s_barrier
v_add_u32 v4, -49152, v4
v_add_u32 v5, -49152, v5
v_add_u32 v6, -49152, v6
Tv 208,4,40960
mvv 62,184,110
mvv 66,188,110
Tv 212,5,40960
mvv 70,110,192
Tv 216,6,40960
mvv 14,184,54
Tv 220,4,53248
mvv 18,188,54
Tv 224,5,53248
mvv 22,54,192
Tv 228,6,53248
mvv 26,184,96
ds_read_b128 v[38:41], v8 offset:3072
mvv 30,188,96
ds_read_b128 v[42:45], v9 offset:3072
mvv 34,96,192
ds_read_b128 v[46:49], v8 offset:7168
mvv 62,196,110
ds_read_b128 v[50:53], v9 offset:7168
mvv 66,200,110
mvv 70,110,204
mvv 14,196,58
mvv 18,200,58
mvv 22,58,204
mvv 26,196,100
mvv 30,200,100
mvv 34,100,204
s_waitcnt lgkmcnt(0)
s_waitcnt vmcnt(0)
s_barrier
Tv 232,4,16384
mvv 62,208,110
mvv 66,212,110
Tv 236,5,16384
mvv 70,110,216
Tv 240,6,16384
mvv 14,208,38
Tv 244,4,28672
mvv 18,212,38
Tv 248,5,28672
mvv 22,38,216
Tv 252,6,28672
mvv 26,208,46
ds_read_b128 v[54:57], v10 offset:3072
mvv 30,212,46
ds_read_b128 v[58:61], v11 offset:3072
mvv 34,46,216
ds_read_b128 v[96:99], v10 offset:7168
mvv 62,220,110
ds_read_b128 v[100:103], v11 offset:7168
mvv 66,224,110
mvv 70,110,228
mvv 14,220,42
mvv 18,224,42
mvv 22,42,228
mvv 26,220,50
mvv 30,224,50
mvv 34,50,228
s_waitcnt lgkmcnt(0)
mvv 62,232,110
mvv 66,236,110
mvv 70,110,240
mvv 14,232,54
mvv 18,236,54
mvv 22,54,240
mvv 26,232,96
mvv 30,236,96
mvv 34,96,240
mvv 62,244,110
mvv 66,248,110
mvv 70,110,252
mvv 14,244,58
mvv 18,248,58
mvv 22,58,252
mvv 26,244,100
mvv 30,248,100
mvv 34,100,252
s_barrier
s_nop 7
v_mov_b32 v118, v70
s_and_b32 s60, s48, 3
LvQA:
v_xor_b32 v80, 0x10000, v80
ds_read_b64 v[120:121], v82 offset:0
ds_read_b128 v[70:73], v83 offset:0
ds_read_b128 v[74:77], v83 offset:16
s_add_u32 s49, s49, 346112
R 38,32768
R 42,33792
R 46,34816
R 50,35840
s_waitcnt lgkmcnt(2)
v_mul_f32 v71, v71, v118
v_fma_f32 v22, v22, v70, v71
v_mul_f32 v73, v73, v118
v_fma_f32 v23, v23, v72, v73
v_mul_f32 v75, v75, v118
v_fma_f32 v24, v24, v74, v75
v_mul_f32 v77, v77, v118
v_fma_f32 v25, v25, v76, v77
v_cvt_pk_bf16_f32 v8, v22, v23
v_cvt_pk_bf16_f32 v9, v24, v25
v_mul_f32 v122, v121, v62
v_fma_f32 v14, v14, v120, v122
v_mul_f32 v123, v121, v63
v_fma_f32 v15, v15, v120, v123
v_mul_f32 v122, v121, v64
v_fma_f32 v16, v16, v120, v122
v_mul_f32 v123, v121, v65
v_fma_f32 v17, v17, v120, v123
v_mul_f32 v122, v121, v66
v_fma_f32 v18, v18, v120, v122
v_mul_f32 v123, v121, v67
v_fma_f32 v19, v19, v120, v123
v_mul_f32 v122, v121, v68
v_fma_f32 v20, v20, v120, v122
v_mul_f32 v123, v121, v69
v_fma_f32 v21, v21, v120, v123
v_cvt_pk_bf16_f32 v0, v14, v15
v_cvt_pk_bf16_f32 v1, v16, v17
v_cvt_pk_bf16_f32 v2, v18, v19
v_cvt_pk_bf16_f32 v3, v20, v21
s_add_u32 s49, s49, 346112
s_barrier
ds_read_b128 v[96:99], v81 offset:0
ds_read_b64 v[104:105], v81 offset:1024
ds_read_b64 v[120:121], v82 offset:128
ds_read_b128 v[70:73], v83 offset:128
ds_read_b128 v[74:77], v83 offset:144
v_mfma_f32_16x16x32_bf16 v[14:17], a[0:3], v[38:41], 0
R 54,36864
v_mfma_f32_16x16x32_bf16 v[18:21], a[4:7], v[38:41], 0
s_waitcnt lgkmcnt(1)
v_mul_f32 v71, v71, v118
v_mfma_f32_16x16x32_bf16 v[22:25], v[38:41], a[8:11], 0
v_fma_f32 v34, v34, v70, v71
v_mul_f32 v73, v73, v118
mav 14,12,42
R 58,37888
v_fma_f32 v35, v35, v72, v73
mav 18,16,42
v_mul_f32 v75, v75, v118
v_fma_f32 v36, v36, v74, v75
mva 22,42,20
v_mul_f32 v77, v77, v118
v_fma_f32 v37, v37, v76, v77
mav 14,24,46
R 38,38912
v_cvt_pk_bf16_f32 v10, v34, v35
mav 18,28,46
v_cvt_pk_bf16_f32 v11, v36, v37
v_mul_f32 v122, v121, v62
mva 22,46,32
v_mfma_f32_16x16x32_bf16 v[70:73], v[96:99], v[0:3], v[84:87]
v_fma_f32 v26, v26, v120, v122
v_mul_f32 v123, v121, v63
mav 14,36,50
R 42,39936
v_fma_f32 v27, v27, v120, v123
mav 18,40,50
v_mul_f32 v122, v121, v64
v_fma_f32 v28, v28, v120, v122
mva 22,50,44
v_mul_f32 v123, v121, v65
s_waitcnt lgkmcnt(2)
mav 14,48,54
R 46,40960
v_fma_f32 v29, v29, v120, v123
mav 18,52,54
v_mul_f32 v122, v121, v66
v_fma_f32 v30, v30, v120, v122
mva 22,54,56
v_mul_f32 v123, v121, v67
v_fma_f32 v31, v31, v120, v123
mav 14,60,58
R 50,41984
v_mul_f32 v122, v121, v68
mav 18,64,58
v_fma_f32 v32, v32, v120, v122
v_mul_f32 v123, v121, v69
mva 22,58,68
v_fma_f32 v33, v33, v120, v123
s_waitcnt lgkmcnt(2)
mav 14,72,38
R 54,43008
mav 18,76,38
buffer_store_dwordx4 v[96:99], v81, s[36:39], s54 offen sc1
buffer_store_dwordx2 v[8:9], v81, s[36:39], s54 offen offset:1024 sc1
mva 22,38,80
buffer_store_dwordx2 v[104:105], v81, s[36:39], s54 offen offset:1032 sc1
mav 14,84,42
R 58,44032
mav 18,88,42
mva 22,42,92
s_waitcnt lgkmcnt(2)
mav 14,96,46
R 38,45056
mav 18,100,46
s_barrier
ds_read_b128 v[100:103], v81 offset:4096
mva 22,46,104
ds_read_b64 v[106:107], v81 offset:5120
mav 14,108,50
R 42,46080
mav 18,112,50
mva 22,50,116
s_waitcnt lgkmcnt(4)
mav 14,120,54
R 46,47104
mav 18,124,54
mva 22,54,128
s_waitcnt lgkmcnt(3)
v_mfma_f32_16x16x32_bf16 v[74:77], v[100:103], v[0:3], v[88:91]
v_cvt_pk_bf16_f32 v0, v26, v27
v_cvt_pk_bf16_f32 v1, v28, v29
mav 14,132,58
R 50,48128
v_cvt_pk_bf16_f32 v2, v30, v31
mav 18,136,58
v_cvt_pk_bf16_f32 v3, v32, v33
mva 22,58,140
v_max3_f32 v114, v70, v71, v74
v_max3_f32 v115, v72, v73, v76
mav 14,144,38
R 54,49152
v_max_f32 v114, v114, v75
mav 18,148,38
v_max_f32 v115, v115, v77
v_max_f32 v116, v114, v115
mva 22,38,152
v_max_f32 v117, v114, v115
s_waitcnt lgkmcnt(2)
mav 14,156,42
R 38,50176
v_permlane32_swap_b32 v116, v117
mav 18,160,42
v_max_f32 v110, v116, v117
v_max_f32 v111, v116, v117
mva 22,42,164
mav 14,168,46
R 42,51200
v_permlane16_swap_b32 v110, v111
mav 18,172,46
v_max_f32 v116, v110, v111
v_mul_f32 v116, s65, v116
mva 22,46,176
v_fma_f32 v114, v114, s65, -v116
s_waitcnt lgkmcnt(2)
mav 14,180,50
R 46,52224
v_fma_f32 v115, v115, s65, -v116
mav 18,184,50
v_exp_f32 v114, v114
mva 22,50,188
v_exp_f32 v115, v115
mav 14,192,54
R 50,53248
v_add_f32 v116, v114, v115
mav 18,196,54
v_add_f32 v117, v114, v115
mva 22,54,200
s_waitcnt lgkmcnt(2)
mav 14,204,38
R 54,54272
v_permlane32_swap_b32 v116, v117
mav 18,208,38
v_add_f32 v110, v116, v117
v_add_f32 v111, v116, v117
mva 22,38,212
mav 14,216,42
R 38,55296
v_permlane16_swap_b32 v110, v111
mav 18,220,42
v_add_f32 v116, v110, v111
mva 22,42,224
s_waitcnt lgkmcnt(2)
mav 14,228,46
R 42,56320
mav 18,232,46
v_rcp_f32 v116, v116
mva 22,46,236
v_mul_f32 v114, v114, v116
v_mul_f32 v115, v115, v116
mav 14,240,50
R 46,57344
v_cvt_pk_bf16_f32 v114, v114, v114
mav 18,244,50
v_cvt_pk_bf16_f32 v115, v115, v115
v_and_b32 v110, v114, v92
mva 22,50,248
v_and_b32 v111, v115, v93
s_waitcnt lgkmcnt(2)
mav 14,252,54
R 50,58368
v_and_b32 v112, v114, v94
mvv 18,128,54
v_and_b32 v113, v115, v95
mvv 22,54,132
mvv 14,136,38
R 54,59392
v_mfma_f32_16x16x32_bf16 v[70:73], v[8:11], v[110:113], 0
v_mfma_f32_16x16x32_bf16 v[74:77], v[104:107], v[110:113], 0
mvv 18,140,38
mvv 22,38,144
s_waitcnt lgkmcnt(2)
mvv 14,148,42
R 38,60416
mvv 18,152,42
mvv 22,42,156
v_cvt_pk_bf16_f32 v110, v70, v71
v_cvt_pk_bf16_f32 v111, v72, v73
mvv 14,160,46
R 42,61440
v_cvt_pk_bf16_f32 v112, v74, v75
mvv 18,164,46
v_cvt_pk_bf16_f32 v113, v76, v77
buffer_store_dwordx4 v[110:113], v81, s[36:39], s49 offen sc1
mvv 22,46,168
s_add_u32 s49, s49, 346112
s_waitcnt lgkmcnt(2)
mvv 14,172,50
R 46,62464
mvv 18,176,50
mvv 22,50,180
mvv 14,184,54
R 50,63488
mvv 18,188,54
mvv 22,54,192
s_waitcnt lgkmcnt(2)
mvv 14,196,38
R 54,64512
mvv 18,200,38
mvv 22,38,204
mvv 14,208,42
R 38,0
mvv 18,212,42
ds_read_b64 v[120:121], v82 offset:0
ds_read_b128 v[70:73], v83 offset:0
mvv 22,42,216
ds_read_b128 v[74:77], v83 offset:16
s_waitcnt lgkmcnt(5)
mvv 14,220,46
R 42,1024
mvv 18,224,46
mvv 22,46,228
mvv 14,232,50
R 46,2048
mvv 18,236,50
mvv 22,50,240
s_waitcnt lgkmcnt(2)
mvv 14,244,54
R 50,3072
mvv 18,248,54
mvv 22,54,252
v_mfma_f32_16x16x32_bf16 v[26:29], a[0:3], v[38:41], 0
R 54,4096
v_mul_f32 v71, v71, v118
v_mfma_f32_16x16x32_bf16 v[30:33], a[4:7], v[38:41], 0
v_mul_f32 v73, v73, v118
v_mul_f32 v75, v75, v118
v_mfma_f32_16x16x32_bf16 v[34:37], v[38:41], a[8:11], 0
v_mul_f32 v77, v77, v118
s_waitcnt lgkmcnt(2)
mav 26,12,42
R 58,5120
v_fma_f32 v22, v22, v70, v71
mav 30,16,42
v_fma_f32 v23, v23, v72, v73
v_fma_f32 v24, v24, v74, v75
mva 34,42,20
v_fma_f32 v25, v25, v76, v77
v_cvt_pk_bf16_f32 v8, v22, v23
mav 26,24,46
R 38,6144
v_cvt_pk_bf16_f32 v9, v24, v25
mav 30,28,46
v_mul_f32 v122, v121, v62
v_mfma_f32_16x16x32_bf16 v[70:73], v[100:103], v[0:3], v[84:87]
v_fma_f32 v14, v14, v120, v122
v_mul_f32 v123, v121, v63
mva 34,46,32
v_fma_f32 v15, v15, v120, v123
s_waitcnt lgkmcnt(2)
mav 26,36,50
R 42,7168
v_mul_f32 v122, v121, v64
mav 30,40,50
v_fma_f32 v16, v16, v120, v122
v_mul_f32 v123, v121, v65
mva 34,50,44
v_fma_f32 v17, v17, v120, v123
v_mul_f32 v122, v121, v66
mav 26,48,54
R 46,8192
v_fma_f32 v18, v18, v120, v122
mav 30,52,54
v_mul_f32 v123, v121, v67
v_fma_f32 v19, v19, v120, v123
mva 34,54,56
v_mul_f32 v122, v121, v68
s_waitcnt lgkmcnt(2)
mav 26,60,58
R 50,9216
v_fma_f32 v20, v20, v120, v122
mav 30,64,58
v_mul_f32 v123, v121, v69
v_fma_f32 v21, v21, v120, v123
mva 34,58,68
mav 26,72,38
R 54,10240
mav 30,76,38
mva 34,38,80
s_waitcnt lgkmcnt(2)
mav 26,84,42
R 58,11264
mav 30,88,42
mva 34,42,92
mav 26,96,46
R 38,12288
v_add_u32 v78, 1, v78
mav 30,100,46
s_barrier
ds_read_b128 v[96:99], v81 offset:0
mva 34,46,104
ds_read_b64 v[104:105], v81 offset:1024
s_waitcnt lgkmcnt(4)
mav 26,108,50
R 42,13312
mav 30,112,50
mva 34,50,116
mav 26,120,54
R 46,14336
mav 30,124,54
s_waitcnt vmcnt(0)
buffer_store_dwordx2 v[78:79], off, s[36:39], s52 sc1
mva 34,54,128
s_waitcnt lgkmcnt(3)
v_mfma_f32_16x16x32_bf16 v[74:77], v[96:99], v[0:3], v[88:91]
v_cvt_pk_bf16_f32 v0, v14, v15
v_cvt_pk_bf16_f32 v1, v16, v17
mav 26,132,58
R 50,15360
v_cvt_pk_bf16_f32 v2, v18, v19
mav 30,136,58
v_cvt_pk_bf16_f32 v3, v20, v21
mva 34,58,140
v_max3_f32 v114, v70, v71, v74
v_max3_f32 v115, v72, v73, v76
mav 26,144,38
R 54,16384
v_max_f32 v114, v114, v75
mav 30,148,38
v_max_f32 v115, v115, v77
v_max_f32 v116, v114, v115
mva 34,38,152
v_max_f32 v117, v114, v115
s_waitcnt lgkmcnt(2)
mav 26,156,42
R 38,17408
v_permlane32_swap_b32 v116, v117
mav 30,160,42
v_max_f32 v110, v116, v117
v_max_f32 v111, v116, v117
mva 34,42,164
mav 26,168,46
R 42,18432
v_permlane16_swap_b32 v110, v111
mav 30,172,46
v_max_f32 v116, v110, v111
v_mul_f32 v116, s65, v116
mva 34,46,176
v_fma_f32 v114, v114, s65, -v116
s_waitcnt lgkmcnt(2)
mav 26,180,50
R 46,19456
v_fma_f32 v115, v115, s65, -v116
mav 30,184,50
v_exp_f32 v114, v114
mva 34,50,188
v_exp_f32 v115, v115
mav 26,192,54
R 50,20480
v_add_f32 v116, v114, v115
mav 30,196,54
v_add_f32 v117, v114, v115
mva 34,54,200
s_waitcnt lgkmcnt(2)
mav 26,204,38
R 54,21504
v_permlane32_swap_b32 v116, v117
mav 30,208,38
v_add_f32 v110, v116, v117
v_add_f32 v111, v116, v117
mva 34,38,212
mav 26,216,42
R 38,22528
v_permlane16_swap_b32 v110, v111
mav 30,220,42
v_add_f32 v116, v110, v111
mva 34,42,224
s_waitcnt lgkmcnt(2)
mav 26,228,46
R 42,23552
mav 30,232,46
v_rcp_f32 v116, v116
mva 34,46,236
v_mul_f32 v114, v114, v116
v_mul_f32 v115, v115, v116
mav 26,240,50
R 46,24576
v_cvt_pk_bf16_f32 v114, v114, v114
mav 30,244,50
v_cvt_pk_bf16_f32 v115, v115, v115
v_and_b32 v110, v114, v94
mva 34,50,248
v_and_b32 v111, v115, v95
s_waitcnt lgkmcnt(2)
mav 26,252,54
R 50,25600
v_and_b32 v112, v114, v92
mvv 30,128,54
v_and_b32 v113, v115, v93
mvv 34,54,132
mvv 26,136,38
R 54,26624
v_mfma_f32_16x16x32_bf16 v[70:73], v[8:11], v[110:113], 0
v_mfma_f32_16x16x32_bf16 v[74:77], v[104:107], v[110:113], 0
mvv 30,140,38
mvv 34,38,144
s_waitcnt lgkmcnt(2)
mvv 26,148,42
R 38,27648
mvv 30,152,42
mvv 34,42,156
v_cvt_pk_bf16_f32 v110, v70, v71
v_cvt_pk_bf16_f32 v111, v72, v73
mvv 26,160,46
R 42,28672
v_cvt_pk_bf16_f32 v112, v74, v75
mvv 30,164,46
v_cvt_pk_bf16_f32 v113, v76, v77
buffer_store_dwordx4 v[110:113], v81, s[36:39], s49 offen sc1
mvv 34,46,168
s_add_u32 s49, s49, 346112
s_waitcnt lgkmcnt(2)
mvv 26,172,50
R 46,29696
mvv 30,176,50
mvv 34,50,180
mvv 26,184,54
R 50,30720
mvv 30,188,54
mvv 34,54,192
s_waitcnt lgkmcnt(2)
mvv 26,196,38
R 54,31744
v_xor_b32 v80, 0x10000, v80
mvv 30,200,38
mvv 34,38,204
mvv 26,208,42
R 38,32768
mvv 30,212,42
ds_read_b64 v[120:121], v82 offset:128
ds_read_b128 v[70:73], v83 offset:128
mvv 34,42,216
ds_read_b128 v[74:77], v83 offset:144
s_waitcnt lgkmcnt(5)
mvv 26,220,46
R 42,33792
mvv 30,224,46
mvv 34,46,228
mvv 26,232,50
R 46,34816
mvv 30,236,50
mvv 34,50,240
s_waitcnt lgkmcnt(2)
mvv 26,244,54
R 50,35840
mvv 30,248,54
mvv 34,54,252
s_mov_b32 s60, -13
.p2align 6
LQA_1:
v_mfma_f32_16x16x32_bf16 v[14:17], a[0:3], v[38:41], 0
R 54,36864
v_mul_f32 v71, v71, v118
v_mfma_f32_16x16x32_bf16 v[18:21], a[4:7], v[38:41], 0
v_mul_f32 v73, v73, v118
v_mul_f32 v75, v75, v118
v_mfma_f32_16x16x32_bf16 v[22:25], v[38:41], a[8:11], 0
v_mul_f32 v77, v77, v118
s_waitcnt lgkmcnt(2)
mav 14,12,42
R 58,37888
v_fma_f32 v34, v34, v70, v71
mav 18,16,42
v_fma_f32 v35, v35, v72, v73
v_fma_f32 v36, v36, v74, v75
mva 22,42,20
v_fma_f32 v37, v37, v76, v77
v_cvt_pk_bf16_f32 v10, v34, v35
mav 14,24,46
R 38,38912
v_cvt_pk_bf16_f32 v11, v36, v37
mav 18,28,46
v_mul_f32 v122, v121, v62
v_mfma_f32_16x16x32_bf16 v[70:73], v[96:99], v[0:3], v[84:87]
v_fma_f32 v26, v26, v120, v122
v_mul_f32 v123, v121, v63
mva 22,46,32
v_fma_f32 v27, v27, v120, v123
s_waitcnt lgkmcnt(2)
mav 14,36,50
R 42,39936
v_mul_f32 v122, v121, v64
mav 18,40,50
v_fma_f32 v28, v28, v120, v122
v_mul_f32 v123, v121, v65
mva 22,50,44
v_fma_f32 v29, v29, v120, v123
v_mul_f32 v122, v121, v66
mav 14,48,54
R 46,40960
v_fma_f32 v30, v30, v120, v122
mav 18,52,54
v_mul_f32 v123, v121, v67
v_fma_f32 v31, v31, v120, v123
mva 22,54,56
v_mul_f32 v122, v121, v68
s_waitcnt lgkmcnt(2)
mav 14,60,58
R 50,41984
v_fma_f32 v32, v32, v120, v122
mav 18,64,58
v_mul_f32 v123, v121, v69
v_fma_f32 v33, v33, v120, v123
mva 22,58,68
mav 14,72,38
R 54,43008
mav 18,76,38
mva 22,38,80
s_waitcnt lgkmcnt(2)
mav 14,84,42
R 58,44032
mav 18,88,42
mva 22,42,92
mav 14,96,46
R 38,45056
v_add_u32 v78, 1, v78
mav 18,100,46
s_barrier
ds_read_b128 v[100:103], v81 offset:4096
mva 22,46,104
ds_read_b64 v[106:107], v81 offset:5120
s_waitcnt lgkmcnt(4)
mav 14,108,50
R 42,46080
mav 18,112,50
mva 22,50,116
mav 14,120,54
R 46,47104
mav 18,124,54
s_waitcnt vmcnt(0)
buffer_store_dwordx2 v[78:79], off, s[36:39], s52 sc1
mva 22,54,128
s_waitcnt lgkmcnt(3)
v_mfma_f32_16x16x32_bf16 v[74:77], v[100:103], v[0:3], v[88:91]
v_cvt_pk_bf16_f32 v0, v26, v27
v_cvt_pk_bf16_f32 v1, v28, v29
mav 14,132,58
R 50,48128
v_cvt_pk_bf16_f32 v2, v30, v31
mav 18,136,58
v_cvt_pk_bf16_f32 v3, v32, v33
mva 22,58,140
v_max3_f32 v114, v70, v71, v74
v_max3_f32 v115, v72, v73, v76
mav 14,144,38
R 54,49152
v_max_f32 v114, v114, v75
mav 18,148,38
v_max_f32 v115, v115, v77
v_max_f32 v116, v114, v115
mva 22,38,152
v_max_f32 v117, v114, v115
s_waitcnt lgkmcnt(2)
mav 14,156,42
R 38,50176
v_permlane32_swap_b32 v116, v117
mav 18,160,42
v_max_f32 v110, v116, v117
v_max_f32 v111, v116, v117
mva 22,42,164
mav 14,168,46
R 42,51200
v_permlane16_swap_b32 v110, v111
mav 18,172,46
v_max_f32 v116, v110, v111
v_mul_f32 v116, s65, v116
mva 22,46,176
v_fma_f32 v114, v114, s65, -v116
s_waitcnt lgkmcnt(2)
mav 14,180,50
R 46,52224
v_fma_f32 v115, v115, s65, -v116
mav 18,184,50
v_exp_f32 v114, v114
mva 22,50,188
v_exp_f32 v115, v115
mav 14,192,54
R 50,53248
v_add_f32 v116, v114, v115
mav 18,196,54
v_add_f32 v117, v114, v115
mva 22,54,200
s_waitcnt lgkmcnt(2)
mav 14,204,38
R 54,54272
v_permlane32_swap_b32 v116, v117
mav 18,208,38
v_add_f32 v110, v116, v117
v_add_f32 v111, v116, v117
mva 22,38,212
mav 14,216,42
R 38,55296
v_permlane16_swap_b32 v110, v111
mav 18,220,42
v_add_f32 v116, v110, v111
mva 22,42,224
s_waitcnt lgkmcnt(2)
mav 14,228,46
R 42,56320
mav 18,232,46
v_rcp_f32 v116, v116
mva 22,46,236
v_mul_f32 v114, v114, v116
v_mul_f32 v115, v115, v116
mav 14,240,50
R 46,57344
v_cvt_pk_bf16_f32 v114, v114, v114
mav 18,244,50
v_cvt_pk_bf16_f32 v115, v115, v115
v_and_b32 v110, v114, v92
mva 22,50,248
v_and_b32 v111, v115, v93
s_waitcnt lgkmcnt(2)
mav 14,252,54
R 50,58368
v_and_b32 v112, v114, v94
mvv 18,128,54
v_and_b32 v113, v115, v95
mvv 22,54,132
mvv 14,136,38
R 54,59392
v_mfma_f32_16x16x32_bf16 v[70:73], v[8:11], v[110:113], 0
v_mfma_f32_16x16x32_bf16 v[74:77], v[104:107], v[110:113], 0
mvv 18,140,38
mvv 22,38,144
s_waitcnt lgkmcnt(2)
mvv 14,148,42
R 38,60416
mvv 18,152,42
mvv 22,42,156
v_cvt_pk_bf16_f32 v110, v70, v71
v_cvt_pk_bf16_f32 v111, v72, v73
mvv 14,160,46
R 42,61440
v_cvt_pk_bf16_f32 v112, v74, v75
mvv 18,164,46
v_cvt_pk_bf16_f32 v113, v76, v77
buffer_store_dwordx4 v[110:113], v81, s[36:39], s49 offen sc1
mvv 22,46,168
s_add_u32 s49, s49, 346112
s_waitcnt lgkmcnt(2)
mvv 14,172,50
R 46,62464
mvv 18,176,50
mvv 22,50,180
mvv 14,184,54
R 50,63488
mvv 18,188,54
mvv 22,54,192
s_waitcnt lgkmcnt(2)
mvv 14,196,38
R 54,64512
mvv 18,200,38
mvv 22,38,204
mvv 14,208,42
R 38,0
mvv 18,212,42
ds_read_b64 v[120:121], v82 offset:0
ds_read_b128 v[70:73], v83 offset:0
mvv 22,42,216
ds_read_b128 v[74:77], v83 offset:16
s_waitcnt lgkmcnt(5)
mvv 14,220,46
R 42,1024
mvv 18,224,46
mvv 22,46,228
mvv 14,232,50
R 46,2048
mvv 18,236,50
mvv 22,50,240
s_waitcnt lgkmcnt(2)
mvv 14,244,54
R 50,3072
mvv 18,248,54
mvv 22,54,252
v_mfma_f32_16x16x32_bf16 v[26:29], a[0:3], v[38:41], 0
R 54,4096
v_mul_f32 v71, v71, v118
v_mfma_f32_16x16x32_bf16 v[30:33], a[4:7], v[38:41], 0
v_mul_f32 v73, v73, v118
v_mul_f32 v75, v75, v118
v_mfma_f32_16x16x32_bf16 v[34:37], v[38:41], a[8:11], 0
v_mul_f32 v77, v77, v118
s_waitcnt lgkmcnt(2)
mav 26,12,42
R 58,5120
v_fma_f32 v22, v22, v70, v71
mav 30,16,42
v_fma_f32 v23, v23, v72, v73
v_fma_f32 v24, v24, v74, v75
mva 34,42,20
v_fma_f32 v25, v25, v76, v77
v_cvt_pk_bf16_f32 v8, v22, v23
mav 26,24,46
R 38,6144
v_cvt_pk_bf16_f32 v9, v24, v25
mav 30,28,46
v_mul_f32 v122, v121, v62
v_mfma_f32_16x16x32_bf16 v[70:73], v[100:103], v[0:3], v[84:87]
v_fma_f32 v14, v14, v120, v122
v_mul_f32 v123, v121, v63
mva 34,46,32
v_fma_f32 v15, v15, v120, v123
s_waitcnt lgkmcnt(2)
mav 26,36,50
R 42,7168
v_mul_f32 v122, v121, v64
mav 30,40,50
v_fma_f32 v16, v16, v120, v122
v_mul_f32 v123, v121, v65
mva 34,50,44
v_fma_f32 v17, v17, v120, v123
v_mul_f32 v122, v121, v66
mav 26,48,54
R 46,8192
v_fma_f32 v18, v18, v120, v122
mav 30,52,54
v_mul_f32 v123, v121, v67
v_fma_f32 v19, v19, v120, v123
mva 34,54,56
v_mul_f32 v122, v121, v68
s_waitcnt lgkmcnt(2)
mav 26,60,58
R 50,9216
v_fma_f32 v20, v20, v120, v122
mav 30,64,58
v_mul_f32 v123, v121, v69
v_fma_f32 v21, v21, v120, v123
mva 34,58,68
mav 26,72,38
R 54,10240
mav 30,76,38
mva 34,38,80
s_waitcnt lgkmcnt(2)
mav 26,84,42
R 58,11264
mav 30,88,42
mva 34,42,92
mav 26,96,46
R 38,12288
v_add_u32 v78, 1, v78
mav 30,100,46
s_barrier
ds_read_b128 v[96:99], v81 offset:0
mva 34,46,104
ds_read_b64 v[104:105], v81 offset:1024
s_waitcnt lgkmcnt(4)
mav 26,108,50
R 42,13312
mav 30,112,50
mva 34,50,116
mav 26,120,54
R 46,14336
mav 30,124,54
s_waitcnt vmcnt(0)
buffer_store_dwordx2 v[78:79], off, s[36:39], s52 sc1
mva 34,54,128
s_waitcnt lgkmcnt(3)
v_mfma_f32_16x16x32_bf16 v[74:77], v[96:99], v[0:3], v[88:91]
v_cvt_pk_bf16_f32 v0, v14, v15
v_cvt_pk_bf16_f32 v1, v16, v17
mav 26,132,58
R 50,15360
v_cvt_pk_bf16_f32 v2, v18, v19
mav 30,136,58
v_cvt_pk_bf16_f32 v3, v20, v21
mva 34,58,140
v_max3_f32 v114, v70, v71, v74
v_max3_f32 v115, v72, v73, v76
mav 26,144,38
R 54,16384
v_max_f32 v114, v114, v75
mav 30,148,38
v_max_f32 v115, v115, v77
v_max_f32 v116, v114, v115
mva 34,38,152
v_max_f32 v117, v114, v115
s_waitcnt lgkmcnt(2)
mav 26,156,42
R 38,17408
v_permlane32_swap_b32 v116, v117
mav 30,160,42
v_max_f32 v110, v116, v117
v_max_f32 v111, v116, v117
mva 34,42,164
mav 26,168,46
R 42,18432
v_permlane16_swap_b32 v110, v111
mav 30,172,46
v_max_f32 v116, v110, v111
v_mul_f32 v116, s65, v116
mva 34,46,176
v_fma_f32 v114, v114, s65, -v116
s_waitcnt lgkmcnt(2)
mav 26,180,50
R 46,19456
v_fma_f32 v115, v115, s65, -v116
mav 30,184,50
v_exp_f32 v114, v114
mva 34,50,188
v_exp_f32 v115, v115
mav 26,192,54
R 50,20480
v_add_f32 v116, v114, v115
mav 30,196,54
v_add_f32 v117, v114, v115
mva 34,54,200
s_waitcnt lgkmcnt(2)
mav 26,204,38
R 54,21504
v_permlane32_swap_b32 v116, v117
mav 30,208,38
v_add_f32 v110, v116, v117
v_add_f32 v111, v116, v117
mva 34,38,212
mav 26,216,42
R 38,22528
v_permlane16_swap_b32 v110, v111
mav 30,220,42
v_add_f32 v116, v110, v111
mva 34,42,224
s_waitcnt lgkmcnt(2)
mav 26,228,46
R 42,23552
mav 30,232,46
v_rcp_f32 v116, v116
mva 34,46,236
v_mul_f32 v114, v114, v116
v_mul_f32 v115, v115, v116
mav 26,240,50
R 46,24576
v_cvt_pk_bf16_f32 v114, v114, v114
mav 30,244,50
v_cvt_pk_bf16_f32 v115, v115, v115
v_and_b32 v110, v114, v94
mva 34,50,248
v_and_b32 v111, v115, v95
s_waitcnt lgkmcnt(2)
mav 26,252,54
R 50,25600
v_and_b32 v112, v114, v92
mvv 30,128,54
v_and_b32 v113, v115, v93
mvv 34,54,132
mvv 26,136,38
R 54,26624
v_mfma_f32_16x16x32_bf16 v[70:73], v[8:11], v[110:113], 0
v_mfma_f32_16x16x32_bf16 v[74:77], v[104:107], v[110:113], 0
mvv 30,140,38
mvv 34,38,144
s_waitcnt lgkmcnt(2)
mvv 26,148,42
R 38,27648
mvv 30,152,42
mvv 34,42,156
v_cvt_pk_bf16_f32 v110, v70, v71
v_cvt_pk_bf16_f32 v111, v72, v73
mvv 26,160,46
R 42,28672
v_cvt_pk_bf16_f32 v112, v74, v75
mvv 30,164,46
v_cvt_pk_bf16_f32 v113, v76, v77
buffer_store_dwordx4 v[110:113], v81, s[36:39], s49 offen sc1
mvv 34,46,168
s_add_u32 s49, s49, 346112
s_waitcnt lgkmcnt(2)
mvv 26,172,50
R 46,29696
mvv 30,176,50
mvv 34,50,180
mvv 26,184,54
R 50,30720
mvv 30,188,54
mvv 34,54,192
s_waitcnt lgkmcnt(2)
mvv 26,196,38
R 54,31744
v_xor_b32 v80, 0x10000, v80
mvv 30,200,38
mvv 34,38,204
mvv 26,208,42
R 38,32768
mvv 30,212,42
ds_read_b64 v[120:121], v82 offset:128
ds_read_b128 v[70:73], v83 offset:128
mvv 34,42,216
ds_read_b128 v[74:77], v83 offset:144
s_waitcnt lgkmcnt(5)
mvv 26,220,46
R 42,33792
mvv 30,224,46
mvv 34,46,228
mvv 26,232,50
R 46,34816
mvv 30,236,50
mvv 34,50,240
s_waitcnt lgkmcnt(2)
mvv 26,244,54
R 50,35840
mvv 30,248,54
mvv 34,54,252
s_add_u32 s60, s60, 1
s_cbranch_scc0 LQA_1
v_mfma_f32_16x16x32_bf16 v[14:17], a[0:3], v[38:41], 0
R 54,36864
v_mul_f32 v71, v71, v118
v_mfma_f32_16x16x32_bf16 v[18:21], a[4:7], v[38:41], 0
v_mul_f32 v73, v73, v118
v_mul_f32 v75, v75, v118
v_mfma_f32_16x16x32_bf16 v[22:25], v[38:41], a[8:11], 0
v_mul_f32 v77, v77, v118
s_waitcnt lgkmcnt(2)
mav 14,12,42
R 58,37888
v_fma_f32 v34, v34, v70, v71
mav 18,16,42
v_fma_f32 v35, v35, v72, v73
v_fma_f32 v36, v36, v74, v75
mva 22,42,20
v_fma_f32 v37, v37, v76, v77
v_cvt_pk_bf16_f32 v10, v34, v35
mav 14,24,46
R 38,38912
v_cvt_pk_bf16_f32 v11, v36, v37
mav 18,28,46
v_mul_f32 v122, v121, v62
v_mfma_f32_16x16x32_bf16 v[70:73], v[96:99], v[0:3], v[84:87]
v_fma_f32 v26, v26, v120, v122
v_mul_f32 v123, v121, v63
mva 22,46,32
v_fma_f32 v27, v27, v120, v123
s_waitcnt lgkmcnt(2)
mav 14,36,50
R 42,39936
v_mul_f32 v122, v121, v64
mav 18,40,50
v_fma_f32 v28, v28, v120, v122
v_mul_f32 v123, v121, v65
mva 22,50,44
v_fma_f32 v29, v29, v120, v123
v_mul_f32 v122, v121, v66
mav 14,48,54
R 46,40960
v_fma_f32 v30, v30, v120, v122
mav 18,52,54
v_mul_f32 v123, v121, v67
v_fma_f32 v31, v31, v120, v123
mva 22,54,56
v_mul_f32 v122, v121, v68
s_waitcnt lgkmcnt(2)
mav 14,60,58
R 50,41984
v_fma_f32 v32, v32, v120, v122
mav 18,64,58
v_mul_f32 v123, v121, v69
v_fma_f32 v33, v33, v120, v123
mva 22,58,68
mav 14,72,38
R 54,43008
mav 18,76,38
mva 22,38,80
s_waitcnt lgkmcnt(2)
mav 14,84,42
R 58,44032
mav 18,88,42
mva 22,42,92
mav 14,96,46
R 38,45056
v_add_u32 v78, 1, v78
mav 18,100,46
s_barrier
ds_read_b128 v[100:103], v81 offset:4096
mva 22,46,104
ds_read_b64 v[106:107], v81 offset:5120
s_waitcnt lgkmcnt(4)
mav 14,108,50
R 42,46080
mav 18,112,50
mva 22,50,116
mav 14,120,54
R 46,47104
mav 18,124,54
s_waitcnt vmcnt(0)
buffer_store_dwordx2 v[78:79], off, s[36:39], s52 sc1
mva 22,54,128
s_waitcnt lgkmcnt(3)
v_mfma_f32_16x16x32_bf16 v[74:77], v[100:103], v[0:3], v[88:91]
v_cvt_pk_bf16_f32 v0, v26, v27
v_cvt_pk_bf16_f32 v1, v28, v29
mav 14,132,58
R 50,48128
v_cvt_pk_bf16_f32 v2, v30, v31
mav 18,136,58
v_cvt_pk_bf16_f32 v3, v32, v33
mva 22,58,140
v_max3_f32 v114, v70, v71, v74
v_max3_f32 v115, v72, v73, v76
mav 14,144,38
R 54,49152
v_max_f32 v114, v114, v75
mav 18,148,38
v_max_f32 v115, v115, v77
v_max_f32 v116, v114, v115
mva 22,38,152
v_max_f32 v117, v114, v115
s_waitcnt lgkmcnt(2)
mav 14,156,42
R 38,50176
v_permlane32_swap_b32 v116, v117
mav 18,160,42
v_max_f32 v110, v116, v117
v_max_f32 v111, v116, v117
mva 22,42,164
mav 14,168,46
R 42,51200
v_permlane16_swap_b32 v110, v111
mav 18,172,46
v_max_f32 v116, v110, v111
v_mul_f32 v116, s65, v116
mva 22,46,176
v_fma_f32 v114, v114, s65, -v116
s_waitcnt lgkmcnt(2)
mav 14,180,50
R 46,52224
v_fma_f32 v115, v115, s65, -v116
mav 18,184,50
v_exp_f32 v114, v114
mva 22,50,188
v_exp_f32 v115, v115
mav 14,192,54
R 50,53248
v_add_f32 v116, v114, v115
mav 18,196,54
v_add_f32 v117, v114, v115
mva 22,54,200
s_waitcnt lgkmcnt(2)
mav 14,204,38
R 54,54272
v_permlane32_swap_b32 v116, v117
mav 18,208,38
v_add_f32 v110, v116, v117
v_add_f32 v111, v116, v117
mva 22,38,212
mav 14,216,42
R 38,55296
v_permlane16_swap_b32 v110, v111
mav 18,220,42
v_add_f32 v116, v110, v111
mva 22,42,224
s_waitcnt lgkmcnt(2)
mav 14,228,46
R 42,56320
mav 18,232,46
v_rcp_f32 v116, v116
mva 22,46,236
v_mul_f32 v114, v114, v116
v_mul_f32 v115, v115, v116
mav 14,240,50
R 46,57344
v_cvt_pk_bf16_f32 v114, v114, v114
mav 18,244,50
v_cvt_pk_bf16_f32 v115, v115, v115
v_and_b32 v110, v114, v92
mva 22,50,248
v_and_b32 v111, v115, v93
s_waitcnt lgkmcnt(2)
mav 14,252,54
R 50,58368
v_and_b32 v112, v114, v94
mvv 18,128,54
v_and_b32 v113, v115, v95
mvv 22,54,132
mvv 14,136,38
R 54,59392
v_mfma_f32_16x16x32_bf16 v[70:73], v[8:11], v[110:113], 0
v_mfma_f32_16x16x32_bf16 v[74:77], v[104:107], v[110:113], 0
mvv 18,140,38
mvv 22,38,144
s_waitcnt lgkmcnt(2)
mvv 14,148,42
R 38,60416
mvv 18,152,42
mvv 22,42,156
v_cvt_pk_bf16_f32 v110, v70, v71
v_cvt_pk_bf16_f32 v111, v72, v73
mvv 14,160,46
R 42,61440
v_cvt_pk_bf16_f32 v112, v74, v75
mvv 18,164,46
v_cvt_pk_bf16_f32 v113, v76, v77
buffer_store_dwordx4 v[110:113], v81, s[36:39], s49 offen sc1
mvv 22,46,168
s_add_u32 s49, s49, 346112
s_waitcnt lgkmcnt(2)
mvv 14,172,50
R 46,62464
mvv 18,176,50
mvv 22,50,180
mvv 14,184,54
R 50,63488
mvv 18,188,54
mvv 22,54,192
s_waitcnt lgkmcnt(2)
mvv 14,196,38
R 54,64512
mvv 18,200,38
mvv 22,38,204
mvv 14,208,42
R 38,0
mvv 18,212,42
ds_read_b64 v[120:121], v82 offset:0
ds_read_b128 v[70:73], v83 offset:0
mvv 22,42,216
ds_read_b128 v[74:77], v83 offset:16
s_waitcnt lgkmcnt(5)
mvv 14,220,46
R 42,1024
mvv 18,224,46
mvv 22,46,228
mvv 14,232,50
R 46,2048
mvv 18,236,50
mvv 22,50,240
s_waitcnt lgkmcnt(2)
mvv 14,244,54
R 50,3072
mvv 18,248,54
mvv 22,54,252
v_mfma_f32_16x16x32_bf16 v[26:29], a[0:3], v[38:41], 0
R 54,4096
v_mul_f32 v71, v71, v118
v_mfma_f32_16x16x32_bf16 v[30:33], a[4:7], v[38:41], 0
v_mul_f32 v73, v73, v118
v_mul_f32 v75, v75, v118
v_mfma_f32_16x16x32_bf16 v[34:37], v[38:41], a[8:11], 0
v_mul_f32 v77, v77, v118
s_waitcnt lgkmcnt(2)
mav 26,12,42
R 58,5120
v_fma_f32 v22, v22, v70, v71
mav 30,16,42
v_fma_f32 v23, v23, v72, v73
v_fma_f32 v24, v24, v74, v75
mva 34,42,20
v_fma_f32 v25, v25, v76, v77
v_cvt_pk_bf16_f32 v8, v22, v23
mav 26,24,46
R 38,6144
v_cvt_pk_bf16_f32 v9, v24, v25
mav 30,28,46
v_mul_f32 v122, v121, v62
v_mfma_f32_16x16x32_bf16 v[70:73], v[100:103], v[0:3], v[84:87]
v_fma_f32 v14, v14, v120, v122
v_mul_f32 v123, v121, v63
mva 34,46,32
v_fma_f32 v15, v15, v120, v123
s_waitcnt lgkmcnt(2)
mav 26,36,50
R 42,7168
v_mul_f32 v122, v121, v64
mav 30,40,50
buffer_load_dwordx4 v[4:7], v81, s[36:39], s55 offen sc1
buffer_load_dwordx2 v[12:13], v81, s[36:39], s55 offen offset:1024 sc1
mva 34,50,44
buffer_load_dwordx2 v[108:109], v81, s[36:39], s55 offen offset:1032 sc1
v_fma_f32 v16, v16, v120, v122
mav 26,48,54
R 46,8192
v_mul_f32 v123, v121, v65
mav 30,52,54
v_fma_f32 v17, v17, v120, v123
v_mul_f32 v122, v121, v66
mva 34,54,56
v_fma_f32 v18, v18, v120, v122
s_waitcnt lgkmcnt(2)
mav 26,60,58
R 50,9216
v_mul_f32 v123, v121, v67
mav 30,64,58
v_fma_f32 v19, v19, v120, v123
v_mul_f32 v122, v121, v68
mva 34,58,68
v_fma_f32 v20, v20, v120, v122
v_mul_f32 v123, v121, v69
mav 26,72,38
R 54,10240
v_fma_f32 v21, v21, v120, v123
mav 30,76,38
mva 34,38,80
s_waitcnt lgkmcnt(2)
)"
R"(
mav 26,84,42
R 58,11264
mav 30,88,42
mva 34,42,92
mav 26,96,46
R 38,12288
v_add_u32 v78, 1, v78
mav 30,100,46
s_barrier
ds_read_b128 v[96:99], v81 offset:0
mva 34,46,104
ds_read_b64 v[104:105], v81 offset:1024
s_waitcnt lgkmcnt(4)
mav 26,108,50
R 42,13312
mav 30,112,50
mva 34,50,116
mav 26,120,54
R 46,14336
mav 30,124,54
s_waitcnt vmcnt(3)
buffer_store_dwordx2 v[78:79], off, s[36:39], s52 sc1
mva 34,54,128
s_waitcnt lgkmcnt(3)
v_mfma_f32_16x16x32_bf16 v[74:77], v[96:99], v[0:3], v[88:91]
v_cvt_pk_bf16_f32 v0, v14, v15
v_cvt_pk_bf16_f32 v1, v16, v17
mav 26,132,58
R 50,15360
v_cvt_pk_bf16_f32 v2, v18, v19
mav 30,136,58
v_cvt_pk_bf16_f32 v3, v20, v21
mva 34,58,140
v_max3_f32 v114, v70, v71, v74
v_max3_f32 v115, v72, v73, v76
mav 26,144,38
R 54,16384
v_max_f32 v114, v114, v75
mav 30,148,38
v_max_f32 v115, v115, v77
v_max_f32 v116, v114, v115
mva 34,38,152
v_max_f32 v117, v114, v115
s_waitcnt lgkmcnt(2)
mav 26,156,42
R 38,17408
v_permlane32_swap_b32 v116, v117
mav 30,160,42
v_max_f32 v110, v116, v117
v_max_f32 v111, v116, v117
mva 34,42,164
mav 26,168,46
R 42,18432
v_permlane16_swap_b32 v110, v111
mav 30,172,46
v_max_f32 v116, v110, v111
v_mul_f32 v116, s65, v116
mva 34,46,176
v_fma_f32 v114, v114, s65, -v116
s_waitcnt lgkmcnt(2)
mav 26,180,50
R 46,19456
v_fma_f32 v115, v115, s65, -v116
mav 30,184,50
v_exp_f32 v114, v114
mva 34,50,188
v_exp_f32 v115, v115
mav 26,192,54
R 50,20480
v_add_f32 v116, v114, v115
mav 30,196,54
v_add_f32 v117, v114, v115
mva 34,54,200
s_waitcnt lgkmcnt(2)
mav 26,204,38
R 54,21504
v_permlane32_swap_b32 v116, v117
mav 30,208,38
v_add_f32 v110, v116, v117
v_add_f32 v111, v116, v117
mva 34,38,212
mav 26,216,42
R 38,22528
v_permlane16_swap_b32 v110, v111
mav 30,220,42
v_add_f32 v116, v110, v111
mva 34,42,224
s_waitcnt lgkmcnt(2)
mav 26,228,46
R 42,23552
mav 30,232,46
v_rcp_f32 v116, v116
mva 34,46,236
v_mul_f32 v114, v114, v116
v_mul_f32 v115, v115, v116
mav 26,240,50
R 46,24576
v_cvt_pk_bf16_f32 v114, v114, v114
mav 30,244,50
v_cvt_pk_bf16_f32 v115, v115, v115
v_and_b32 v110, v114, v94
mva 34,50,248
v_and_b32 v111, v115, v95
s_waitcnt lgkmcnt(2)
mav 26,252,54
R 50,25600
v_and_b32 v112, v114, v92
mvv 30,128,54
v_and_b32 v113, v115, v93
mvv 34,54,132
mvv 26,136,38
R 54,26624
v_mfma_f32_16x16x32_bf16 v[70:73], v[8:11], v[110:113], 0
v_mfma_f32_16x16x32_bf16 v[74:77], v[104:107], v[110:113], 0
mvv 30,140,38
mvv 34,38,144
s_waitcnt lgkmcnt(2)
mvv 26,148,42
R 38,27648
mvv 30,152,42
mvv 34,42,156
v_cvt_pk_bf16_f32 v110, v70, v71
v_cvt_pk_bf16_f32 v111, v72, v73
mvv 26,160,46
R 42,28672
v_cvt_pk_bf16_f32 v112, v74, v75
mvv 30,164,46
v_cvt_pk_bf16_f32 v113, v76, v77
buffer_store_dwordx4 v[110:113], v81, s[36:39], s49 offen sc1
mvv 34,46,168
s_add_u32 s49, s49, 346112
s_waitcnt lgkmcnt(2)
mvv 26,172,50
R 46,29696
mvv 30,176,50
mvv 34,50,180
mvv 26,184,54
R 50,30720
mvv 30,188,54
mvv 34,54,192
s_waitcnt lgkmcnt(2)
mvv 26,196,38
R 54,31744
v_xor_b32 v80, 0x10000, v80
mvv 30,200,38
mvv 34,38,204
mvv 26,208,42
R 38,32768
mvv 30,212,42
ds_read_b64 v[120:121], v82 offset:128
ds_read_b128 v[70:73], v83 offset:128
mvv 34,42,216
ds_read_b128 v[74:77], v83 offset:144
s_waitcnt lgkmcnt(5)
mvv 26,220,46
R 42,33792
mvv 30,224,46
mvv 34,46,228
mvv 26,232,50
R 46,34816
mvv 30,236,50
mvv 34,50,240
s_waitcnt lgkmcnt(2)
mvv 26,244,54
R 50,35840
mvv 30,248,54
mvv 34,54,252
v_mfma_f32_16x16x32_bf16 v[14:17], a[0:3], v[38:41], 0
R 54,36864
v_mul_f32 v71, v71, v118
v_mfma_f32_16x16x32_bf16 v[18:21], a[4:7], v[38:41], 0
v_mul_f32 v73, v73, v118
v_mul_f32 v75, v75, v118
v_mfma_f32_16x16x32_bf16 v[22:25], v[38:41], a[8:11], 0
v_mul_f32 v77, v77, v118
s_waitcnt lgkmcnt(2)
mav 14,12,42
R 58,37888
v_fma_f32 v34, v34, v70, v71
mav 18,16,42
v_fma_f32 v35, v35, v72, v73
v_fma_f32 v36, v36, v74, v75
mva 22,42,20
v_fma_f32 v37, v37, v76, v77
v_cvt_pk_bf16_f32 v10, v34, v35
mav 14,24,46
R 38,38912
v_cvt_pk_bf16_f32 v11, v36, v37
mav 18,28,46
v_mul_f32 v122, v121, v62
v_mfma_f32_16x16x32_bf16 v[70:73], v[96:99], v[0:3], v[84:87]
v_fma_f32 v26, v26, v120, v122
v_mul_f32 v123, v121, v63
mva 22,46,32
v_fma_f32 v27, v27, v120, v123
s_waitcnt lgkmcnt(2)
mav 14,36,50
R 42,39936
v_mul_f32 v122, v121, v64
mav 18,40,50
v_fma_f32 v28, v28, v120, v122
v_mul_f32 v123, v121, v65
mva 22,50,44
v_fma_f32 v29, v29, v120, v123
v_mul_f32 v122, v121, v66
mav 14,48,54
R 46,40960
v_fma_f32 v30, v30, v120, v122
mav 18,52,54
v_mul_f32 v123, v121, v67
v_fma_f32 v31, v31, v120, v123
mva 22,54,56
v_mul_f32 v122, v121, v68
s_waitcnt lgkmcnt(2)
mav 14,60,58
R 50,41984
v_fma_f32 v32, v32, v120, v122
mav 18,64,58
v_mul_f32 v123, v121, v69
v_fma_f32 v33, v33, v120, v123
mva 22,58,68
mav 14,72,38
R 54,43008
mav 18,76,38
mva 22,38,80
s_waitcnt lgkmcnt(2)
mav 14,84,42
R 58,44032
mav 18,88,42
mva 22,42,92
mav 14,96,46
R 38,45056
v_add_u32 v78, 1, v78
mav 18,100,46
s_barrier
ds_read_b128 v[100:103], v81 offset:4096
mva 22,46,104
ds_read_b64 v[106:107], v81 offset:5120
s_waitcnt lgkmcnt(4)
mav 14,108,50
R 42,46080
mav 18,112,50
mva 22,50,116
mav 14,120,54
R 46,47104
mav 18,124,54
s_waitcnt vmcnt(0)
buffer_store_dwordx2 v[78:79], off, s[36:39], s52 sc1
mva 22,54,128
s_waitcnt lgkmcnt(3)
v_mfma_f32_16x16x32_bf16 v[74:77], v[100:103], v[0:3], v[88:91]
v_cvt_pk_bf16_f32 v0, v26, v27
v_cvt_pk_bf16_f32 v1, v28, v29
mav 14,132,58
R 50,48128
v_cvt_pk_bf16_f32 v2, v30, v31
mav 18,136,58
v_cvt_pk_bf16_f32 v3, v32, v33
mva 22,58,140
v_max3_f32 v114, v70, v71, v74
v_max3_f32 v115, v72, v73, v76
mav 14,144,38
R 54,49152
v_max_f32 v114, v114, v75
mav 18,148,38
v_max_f32 v115, v115, v77
v_max_f32 v116, v114, v115
mva 22,38,152
v_max_f32 v117, v114, v115
s_waitcnt lgkmcnt(2)
mav 14,156,42
R 38,50176
v_permlane32_swap_b32 v116, v117
mav 18,160,42
v_max_f32 v110, v116, v117
v_max_f32 v111, v116, v117
mva 22,42,164
mav 14,168,46
R 42,51200
v_permlane16_swap_b32 v110, v111
mav 18,172,46
v_max_f32 v116, v110, v111
v_mul_f32 v116, s65, v116
mva 22,46,176
v_fma_f32 v114, v114, s65, -v116
s_waitcnt lgkmcnt(2)
mav 14,180,50
R 46,52224
v_fma_f32 v115, v115, s65, -v116
mav 18,184,50
v_exp_f32 v114, v114
mva 22,50,188
v_exp_f32 v115, v115
mav 14,192,54
R 50,53248
v_add_f32 v116, v114, v115
mav 18,196,54
v_add_f32 v117, v114, v115
mva 22,54,200
s_waitcnt lgkmcnt(2)
mav 14,204,38
R 54,54272
v_permlane32_swap_b32 v116, v117
mav 18,208,38
v_add_f32 v110, v116, v117
v_add_f32 v111, v116, v117
mva 22,38,212
mav 14,216,42
R 38,55296
v_permlane16_swap_b32 v110, v111
mav 18,220,42
v_add_f32 v116, v110, v111
mva 22,42,224
s_waitcnt lgkmcnt(2)
mav 14,228,46
R 42,56320
mav 18,232,46
v_rcp_f32 v116, v116
mva 22,46,236
v_mul_f32 v114, v114, v116
v_mul_f32 v115, v115, v116
mav 14,240,50
R 46,57344
v_cvt_pk_bf16_f32 v114, v114, v114
mav 18,244,50
v_cvt_pk_bf16_f32 v115, v115, v115
v_and_b32 v110, v114, v92
mva 22,50,248
v_and_b32 v111, v115, v93
s_waitcnt lgkmcnt(2)
mav 14,252,54
R 50,58368
v_and_b32 v112, v114, v94
mvv 18,128,54
v_and_b32 v113, v115, v95
mvv 22,54,132
mvv 14,136,38
R 54,59392
v_mfma_f32_16x16x32_bf16 v[70:73], v[8:11], v[110:113], 0
v_mfma_f32_16x16x32_bf16 v[74:77], v[104:107], v[110:113], 0
mvv 18,140,38
mvv 22,38,144
s_waitcnt lgkmcnt(2)
mvv 14,148,42
R 38,60416
mvv 18,152,42
mvv 22,42,156
v_cvt_pk_bf16_f32 v110, v70, v71
v_cvt_pk_bf16_f32 v111, v72, v73
mvv 14,160,46
R 42,61440
v_cvt_pk_bf16_f32 v112, v74, v75
mvv 18,164,46
v_cvt_pk_bf16_f32 v113, v76, v77
buffer_store_dwordx4 v[110:113], v81, s[36:39], s49 offen sc1
mvv 22,46,168
s_add_u32 s49, s49, 346112
s_waitcnt lgkmcnt(2)
mvv 14,172,50
R 46,62464
mvv 18,176,50
mvv 22,50,180
mvv 14,184,54
R 50,63488
mvv 18,188,54
mvv 22,54,192
s_waitcnt lgkmcnt(2)
mvv 14,196,38
R 54,64512
mvv 18,200,38
mvv 22,38,204
mvv 14,208,42
R 38,0
mvv 18,212,42
ds_read_b64 v[120:121], v82 offset:0
ds_read_b128 v[70:73], v83 offset:0
mvv 22,42,216
ds_read_b128 v[74:77], v83 offset:16
s_waitcnt lgkmcnt(5)
mvv 14,220,46
R 42,1024
mvv 18,224,46
mvv 22,46,228
mvv 14,232,50
R 46,2048
mvv 18,236,50
mvv 22,50,240
s_waitcnt lgkmcnt(2)
mvv 14,244,54
R 50,3072
mvv 18,248,54
mvv 22,54,252
v_mfma_f32_16x16x32_bf16 v[26:29], a[0:3], v[38:41], 0
R 54,4096
v_mul_f32 v71, v71, v118
v_mfma_f32_16x16x32_bf16 v[30:33], a[4:7], v[38:41], 0
v_mul_f32 v73, v73, v118
v_mul_f32 v75, v75, v118
v_mfma_f32_16x16x32_bf16 v[34:37], v[38:41], a[8:11], 0
v_mul_f32 v77, v77, v118
s_waitcnt lgkmcnt(2)
mav 26,12,42
R 58,5120
v_fma_f32 v22, v22, v70, v71
mav 30,16,42
v_fma_f32 v23, v23, v72, v73
v_fma_f32 v24, v24, v74, v75
mva 34,42,20
v_fma_f32 v25, v25, v76, v77
v_cvt_pk_bf16_f32 v8, v22, v23
mav 26,24,46
R 38,6144
v_cvt_pk_bf16_f32 v9, v24, v25
mav 30,28,46
v_min_f32 v74, s56, v88
v_mfma_f32_16x16x32_bf16 v[70:73], v[100:103], v[0:3], v[84:87]
v_min_f32 v75, s56, v89
v_min_f32 v76, s56, v90
mva 34,46,32
v_min_f32 v77, s56, v91
s_waitcnt lgkmcnt(2)
mav 26,36,50
R 42,7168
mvv 74,4,0
v_mul_f32 v122, v121, v62
v_fma_f32 v14, v14, v120, v122
mav 30,40,50
v_mul_f32 v123, v121, v63
v_fma_f32 v15, v15, v120, v123
mva 34,50,44
v_mul_f32 v122, v121, v64
v_fma_f32 v16, v16, v120, v122
mav 26,48,54
R 46,8192
v_max3_f32 v114, v70, v71, v74
mav 30,52,54
v_max3_f32 v115, v72, v73, v76
v_max_f32 v114, v114, v75
mva 34,54,56
v_max_f32 v115, v115, v77
s_waitcnt lgkmcnt(2)
mav 26,60,58
R 50,9216
v_max_f32 v116, v114, v115
mav 30,64,58
v_max_f32 v117, v114, v115
v_mul_f32 v123, v121, v65
mva 34,58,68
v_permlane32_swap_b32 v116, v117
v_max_f32 v110, v116, v117
mav 26,72,38
R 54,10240
v_max_f32 v111, v116, v117
mav 30,76,38
v_fma_f32 v17, v17, v120, v123
v_permlane16_swap_b32 v110, v111
mva 34,38,80
v_max_f32 v116, v110, v111
s_waitcnt lgkmcnt(2)
mav 26,84,42
R 58,11264
v_mul_f32 v116, s65, v116
mav 30,88,42
v_fma_f32 v114, v114, s65, -v116
v_fma_f32 v115, v115, s65, -v116
mva 34,42,92
v_exp_f32 v114, v114
mav 26,96,46
R 38,12288
v_mul_f32 v122, v121, v66
mav 30,100,46
s_barrier
ds_read_b128 v[96:99], v81 offset:0
mva 34,46,104
ds_read_b64 v[104:105], v81 offset:1024
s_waitcnt lgkmcnt(4)
mav 26,108,50
R 42,13312
v_fma_f32 v18, v18, v120, v122
mav 30,112,50
v_exp_f32 v115, v115
mva 34,50,116
v_add_f32 v116, v114, v115
v_add_f32 v117, v114, v115
mav 26,120,54
R 46,14336
v_permlane32_swap_b32 v116, v117
mav 30,124,54
v_add_f32 v110, v116, v117
v_add_f32 v111, v116, v117
mva 34,54,128
v_mul_f32 v123, v121, v67
s_waitcnt lgkmcnt(2)
mav 26,132,58
R 50,15360
v_permlane16_swap_b32 v110, v111
mav 30,136,58
v_add_f32 v116, v110, v111
v_fma_f32 v19, v19, v120, v123
mva 34,58,140
v_rcp_f32 v116, v116
mav 26,144,38
R 54,16384
v_mul_f32 v114, v114, v116
mav 30,148,38
v_mul_f32 v115, v115, v116
v_cvt_pk_bf16_f32 v114, v114, v114
mva 34,38,152
v_cvt_pk_bf16_f32 v115, v115, v115
s_waitcnt lgkmcnt(2)
mav 26,156,42
R 38,17408
v_and_b32 v110, v114, v92
mav 30,160,42
v_and_b32 v111, v115, v93
v_and_b32 v112, v114, v94
mva 34,42,164
v_and_b32 v113, v115, v95
buffer_store_dwordx4 v[96:99], v81, s[36:39], s54 offen offset:2048 sc1
mav 26,168,46
R 42,18432
v_mfma_f32_16x16x32_bf16 v[70:73], v[10:13], v[110:113], 0
v_mfma_f32_16x16x32_bf16 v[74:77], v[106:109], v[110:113], 0
buffer_store_dwordx2 v[8:9], v81, s[36:39], s54 offen offset:3072 sc1
buffer_store_dwordx2 v[104:105], v81, s[36:39], s54 offen offset:3080 sc1
mav 30,172,46
v_mul_f32 v122, v121, v68
v_fma_f32 v20, v20, v120, v122
mva 34,46,176
v_mul_f32 v123, v121, v69
s_waitcnt lgkmcnt(2)
mav 26,180,50
R 46,19456
v_cvt_pk_bf16_f32 v110, v70, v71
mav 30,184,50
v_cvt_pk_bf16_f32 v111, v72, v73
v_cvt_pk_bf16_f32 v112, v74, v75
mva 34,50,188
v_cvt_pk_bf16_f32 v113, v76, v77
buffer_store_dwordx4 v[110:113], v81, s[36:39], s49 offen sc1
mav 26,192,54
R 50,20480
s_add_u32 s49, s49, 346112
mav 30,196,54
v_fma_f32 v21, v21, v120, v123
v_cvt_pk_bf16_f32 v0, v14, v15
mva 34,54,200
v_cvt_pk_bf16_f32 v1, v16, v17
s_waitcnt lgkmcnt(2)
mav 26,204,38
R 54,21504
v_cvt_pk_bf16_f32 v2, v18, v19
mav 30,208,38
v_cvt_pk_bf16_f32 v3, v20, v21
v_add_u32 v78, 1, v78
mva 34,38,212
s_waitcnt vmcnt(4)
buffer_store_dwordx2 v[78:79], off, s[36:39], s52 sc1
mav 26,216,42
R 38,22528
mav 30,220,42
mva 34,42,224
s_waitcnt lgkmcnt(2)
mav 26,228,46
R 42,23552
mav 30,232,46
mva 34,46,236
mav 26,240,50
R 46,24576
mav 30,244,50
mva 34,50,248
s_waitcnt lgkmcnt(2)
mav 26,252,54
R 50,25600
mvv 30,128,54
mvv 34,54,132
mvv 26,136,38
R 54,26624
mvv 30,140,38
mvv 34,38,144
s_waitcnt lgkmcnt(2)
mvv 26,148,42
R 38,27648
mvv 30,152,42
mvv 34,42,156
mvv 26,160,46
R 42,28672
mvv 30,164,46
mvv 34,46,168
s_waitcnt lgkmcnt(2)
mvv 26,172,50
R 46,29696
mvv 30,176,50
mvv 34,50,180
mvv 26,184,54
R 50,30720
mvv 30,188,54
mvv 34,54,192
s_waitcnt lgkmcnt(2)
mvv 26,196,38
R 54,31744
v_xor_b32 v80, 0x10000, v80
mvv 30,200,38
mvv 34,38,204
mvv 26,208,42
R 38,32768
mvv 30,212,42
ds_read_b64 v[120:121], v82 offset:128
ds_read_b128 v[70:73], v83 offset:128
mvv 34,42,216
ds_read_b128 v[74:77], v83 offset:144
s_waitcnt lgkmcnt(5)
mvv 26,220,46
R 42,33792
mvv 30,224,46
mvv 34,46,228
mvv 26,232,50
R 46,34816
mvv 30,236,50
mvv 34,50,240
s_waitcnt lgkmcnt(2)
mvv 26,244,54
R 50,35840
mvv 30,248,54
mvv 34,54,252
v_mfma_f32_16x16x32_bf16 v[14:17], a[0:3], v[38:41], 0
R 54,36864
v_mul_f32 v71, v71, v118
v_mfma_f32_16x16x32_bf16 v[18:21], a[4:7], v[38:41], 0
v_mul_f32 v73, v73, v118
v_mul_f32 v75, v75, v118
v_mfma_f32_16x16x32_bf16 v[22:25], v[38:41], a[8:11], 0
v_mul_f32 v77, v77, v118
s_waitcnt lgkmcnt(2)
mav 14,12,42
R 58,37888
v_fma_f32 v34, v34, v70, v71
mav 18,16,42
v_fma_f32 v35, v35, v72, v73
v_fma_f32 v36, v36, v74, v75
mva 22,42,20
v_fma_f32 v37, v37, v76, v77
v_cvt_pk_bf16_f32 v10, v34, v35
mav 14,24,46
R 38,38912
v_cvt_pk_bf16_f32 v11, v36, v37
mav 18,28,46
v_mul_f32 v122, v121, v62
v_mfma_f32_16x16x32_bf16 v[70:73], v[96:99], v[0:3], v[84:87]
v_fma_f32 v26, v26, v120, v122
v_mul_f32 v123, v121, v63
mva 22,46,32
v_fma_f32 v27, v27, v120, v123
s_waitcnt lgkmcnt(2)
mav 14,36,50
R 42,39936
v_mul_f32 v122, v121, v64
mav 18,40,50
v_fma_f32 v28, v28, v120, v122
v_mul_f32 v123, v121, v65
mva 22,50,44
v_fma_f32 v29, v29, v120, v123
v_mul_f32 v122, v121, v66
mav 14,48,54
R 46,40960
v_fma_f32 v30, v30, v120, v122
mav 18,52,54
v_mul_f32 v123, v121, v67
v_fma_f32 v31, v31, v120, v123
mva 22,54,56
v_mul_f32 v122, v121, v68
s_waitcnt lgkmcnt(2)
mav 14,60,58
R 50,41984
v_fma_f32 v32, v32, v120, v122
mav 18,64,58
v_mul_f32 v123, v121, v69
v_fma_f32 v33, v33, v120, v123
mva 22,58,68
mav 14,72,38
R 54,43008
mav 18,76,38
mva 22,38,80
s_waitcnt lgkmcnt(2)
mav 14,84,42
R 58,44032
mav 18,88,42
mva 22,42,92
mav 14,96,46
R 38,45056
v_add_u32 v78, 1, v78
mav 18,100,46
s_barrier
ds_read_b128 v[100:103], v81 offset:4096
mva 22,46,104
ds_read_b64 v[106:107], v81 offset:5120
s_waitcnt lgkmcnt(4)
mav 14,108,50
R 42,46080
mav 18,112,50
mva 22,50,116
mav 14,120,54
R 46,47104
mav 18,124,54
s_waitcnt vmcnt(1)
buffer_store_dwordx2 v[78:79], off, s[36:39], s52 sc1
mva 22,54,128
s_waitcnt lgkmcnt(3)
v_mfma_f32_16x16x32_bf16 v[74:77], v[100:103], v[0:3], v[88:91]
v_cvt_pk_bf16_f32 v0, v26, v27
v_cvt_pk_bf16_f32 v1, v28, v29
mav 14,132,58
R 50,48128
v_cvt_pk_bf16_f32 v2, v30, v31
mav 18,136,58
v_cvt_pk_bf16_f32 v3, v32, v33
mva 22,58,140
v_max3_f32 v114, v70, v71, v74
v_max3_f32 v115, v72, v73, v76
mav 14,144,38
R 54,49152
v_max_f32 v114, v114, v75
mav 18,148,38
v_max_f32 v115, v115, v77
v_max_f32 v116, v114, v115
mva 22,38,152
v_max_f32 v117, v114, v115
s_waitcnt lgkmcnt(2)
mav 14,156,42
R 38,50176
v_permlane32_swap_b32 v116, v117
mav 18,160,42
v_max_f32 v110, v116, v117
v_max_f32 v111, v116, v117
mva 22,42,164
mav 14,168,46
R 42,51200
v_permlane16_swap_b32 v110, v111
mav 18,172,46
v_max_f32 v116, v110, v111
v_mul_f32 v116, s65, v116
mva 22,46,176
v_fma_f32 v114, v114, s65, -v116
s_waitcnt lgkmcnt(2)
mav 14,180,50
R 46,52224
v_fma_f32 v115, v115, s65, -v116
mav 18,184,50
v_exp_f32 v114, v114
mva 22,50,188
v_exp_f32 v115, v115
mav 14,192,54
R 50,53248
v_add_f32 v116, v114, v115
mav 18,196,54
v_add_f32 v117, v114, v115
mva 22,54,200
s_waitcnt lgkmcnt(2)
mav 14,204,38
R 54,54272
v_permlane32_swap_b32 v116, v117
mav 18,208,38
v_add_f32 v110, v116, v117
v_add_f32 v111, v116, v117
mva 22,38,212
mav 14,216,42
R 38,55296
v_permlane16_swap_b32 v110, v111
mav 18,220,42
v_add_f32 v116, v110, v111
mva 22,42,224
s_waitcnt lgkmcnt(2)
mav 14,228,46
R 42,56320
mav 18,232,46
v_rcp_f32 v116, v116
mva 22,46,236
v_mul_f32 v114, v114, v116
v_mul_f32 v115, v115, v116
mav 14,240,50
R 46,57344
v_cvt_pk_bf16_f32 v114, v114, v114
mav 18,244,50
v_cvt_pk_bf16_f32 v115, v115, v115
v_and_b32 v110, v114, v92
mva 22,50,248
v_and_b32 v111, v115, v93
s_waitcnt lgkmcnt(2)
mav 14,252,54
R 50,58368
v_and_b32 v112, v114, v94
mvv 18,128,54
v_and_b32 v113, v115, v95
mvv 22,54,132
mvv 14,136,38
R 54,59392
v_mfma_f32_16x16x32_bf16 v[70:73], v[8:11], v[110:113], 0
v_mfma_f32_16x16x32_bf16 v[74:77], v[104:107], v[110:113], 0
mvv 18,140,38
mvv 22,38,144
s_waitcnt lgkmcnt(2)
mvv 14,148,42
R 38,60416
mvv 18,152,42
mvv 22,42,156
v_cvt_pk_bf16_f32 v110, v70, v71
v_cvt_pk_bf16_f32 v111, v72, v73
mvv 14,160,46
R 42,61440
v_cvt_pk_bf16_f32 v112, v74, v75
mvv 18,164,46
v_cvt_pk_bf16_f32 v113, v76, v77
buffer_store_dwordx4 v[110:113], v81, s[36:39], s49 offen sc1
mvv 22,46,168
s_add_u32 s49, s49, 346112
s_waitcnt lgkmcnt(2)
mvv 14,172,50
R 46,62464
mvv 18,176,50
mvv 22,50,180
mvv 14,184,54
R 50,63488
mvv 18,188,54
mvv 22,54,192
s_waitcnt lgkmcnt(2)
mvv 14,196,38
R 54,64512
mvv 18,200,38
mvv 22,38,204
mvv 14,208,42
R 38,0
mvv 18,212,42
ds_read_b64 v[120:121], v82 offset:0
ds_read_b128 v[70:73], v83 offset:0
mvv 22,42,216
ds_read_b128 v[74:77], v83 offset:16
s_waitcnt lgkmcnt(5)
mvv 14,220,46
R 42,1024
mvv 18,224,46
mvv 22,46,228
mvv 14,232,50
R 46,2048
mvv 18,236,50
mvv 22,50,240
s_waitcnt lgkmcnt(2)
mvv 14,244,54
R 50,3072
mvv 18,248,54
mvv 22,54,252
v_mfma_f32_16x16x32_bf16 v[26:29], a[0:3], v[38:41], 0
R 54,4096
v_mul_f32 v71, v71, v118
v_mfma_f32_16x16x32_bf16 v[30:33], a[4:7], v[38:41], 0
v_mul_f32 v73, v73, v118
v_mul_f32 v75, v75, v118
v_mfma_f32_16x16x32_bf16 v[34:37], v[38:41], a[8:11], 0
v_mul_f32 v77, v77, v118
s_waitcnt lgkmcnt(2)
mav 26,12,42
R 58,5120
v_fma_f32 v22, v22, v70, v71
mav 30,16,42
v_fma_f32 v23, v23, v72, v73
v_fma_f32 v24, v24, v74, v75
mva 34,42,20
v_fma_f32 v25, v25, v76, v77
v_cvt_pk_bf16_f32 v8, v22, v23
mav 26,24,46
R 38,6144
v_cvt_pk_bf16_f32 v9, v24, v25
mav 30,28,46
v_mul_f32 v122, v121, v62
v_mfma_f32_16x16x32_bf16 v[70:73], v[100:103], v[0:3], v[84:87]
v_fma_f32 v14, v14, v120, v122
v_mul_f32 v123, v121, v63
mva 34,46,32
v_fma_f32 v15, v15, v120, v123
s_waitcnt lgkmcnt(2)
mav 26,36,50
R 42,7168
v_mul_f32 v122, v121, v64
mav 30,40,50
v_fma_f32 v16, v16, v120, v122
v_mul_f32 v123, v121, v65
mva 34,50,44
v_fma_f32 v17, v17, v120, v123
v_mul_f32 v122, v121, v66
mav 26,48,54
R 46,8192
v_fma_f32 v18, v18, v120, v122
mav 30,52,54
v_mul_f32 v123, v121, v67
v_fma_f32 v19, v19, v120, v123
mva 34,54,56
v_mul_f32 v122, v121, v68
s_waitcnt lgkmcnt(2)
mav 26,60,58
R 50,9216
v_fma_f32 v20, v20, v120, v122
mav 30,64,58
v_mul_f32 v123, v121, v69
v_fma_f32 v21, v21, v120, v123
mva 34,58,68
mav 26,72,38
R 54,10240
mav 30,76,38
mva 34,38,80
s_waitcnt lgkmcnt(2)
mav 26,84,42
R 58,11264
mav 30,88,42
mva 34,42,92
mav 26,96,46
R 38,12288
v_add_u32 v78, 1, v78
mav 30,100,46
s_barrier
ds_read_b128 v[96:99], v81 offset:0
mva 34,46,104
ds_read_b64 v[104:105], v81 offset:1024
s_waitcnt lgkmcnt(4)
mav 26,108,50
R 42,13312
mav 30,112,50
mva 34,50,116
mav 26,120,54
R 46,14336
mav 30,124,54
s_waitcnt vmcnt(0)
buffer_store_dwordx2 v[78:79], off, s[36:39], s52 sc1
mva 34,54,128
s_waitcnt lgkmcnt(3)
v_mfma_f32_16x16x32_bf16 v[74:77], v[96:99], v[0:3], v[88:91]
v_cvt_pk_bf16_f32 v0, v14, v15
v_cvt_pk_bf16_f32 v1, v16, v17
mav 26,132,58
R 50,15360
v_cvt_pk_bf16_f32 v2, v18, v19
mav 30,136,58
v_cvt_pk_bf16_f32 v3, v20, v21
mva 34,58,140
v_max3_f32 v114, v70, v71, v74
v_max3_f32 v115, v72, v73, v76
mav 26,144,38
R 54,16384
v_max_f32 v114, v114, v75
mav 30,148,38
v_max_f32 v115, v115, v77
v_max_f32 v116, v114, v115
mva 34,38,152
v_max_f32 v117, v114, v115
s_waitcnt lgkmcnt(2)
mav 26,156,42
R 38,17408
v_permlane32_swap_b32 v116, v117
mav 30,160,42
v_max_f32 v110, v116, v117
v_max_f32 v111, v116, v117
mva 34,42,164
mav 26,168,46
R 42,18432
v_permlane16_swap_b32 v110, v111
mav 30,172,46
v_max_f32 v116, v110, v111
v_mul_f32 v116, s65, v116
mva 34,46,176
v_fma_f32 v114, v114, s65, -v116
s_waitcnt lgkmcnt(2)
mav 26,180,50
R 46,19456
v_fma_f32 v115, v115, s65, -v116
mav 30,184,50
v_exp_f32 v114, v114
mva 34,50,188
v_exp_f32 v115, v115
mav 26,192,54
R 50,20480
v_add_f32 v116, v114, v115
mav 30,196,54
v_add_f32 v117, v114, v115
mva 34,54,200
s_waitcnt lgkmcnt(2)
mav 26,204,38
R 54,21504
v_permlane32_swap_b32 v116, v117
mav 30,208,38
v_add_f32 v110, v116, v117
v_add_f32 v111, v116, v117
mva 34,38,212
mav 26,216,42
R 38,22528
v_permlane16_swap_b32 v110, v111
mav 30,220,42
v_add_f32 v116, v110, v111
mva 34,42,224
s_waitcnt lgkmcnt(2)
mav 26,228,46
R 42,23552
mav 30,232,46
v_rcp_f32 v116, v116
mva 34,46,236
v_mul_f32 v114, v114, v116
v_mul_f32 v115, v115, v116
mav 26,240,50
R 46,24576
v_cvt_pk_bf16_f32 v114, v114, v114
mav 30,244,50
v_cvt_pk_bf16_f32 v115, v115, v115
v_and_b32 v110, v114, v94
mva 34,50,248
v_and_b32 v111, v115, v95
s_waitcnt lgkmcnt(2)
mav 26,252,54
R 50,25600
v_and_b32 v112, v114, v92
mvv 30,128,54
v_and_b32 v113, v115, v93
mvv 34,54,132
mvv 26,136,38
R 54,26624
v_mfma_f32_16x16x32_bf16 v[70:73], v[8:11], v[110:113], 0
v_mfma_f32_16x16x32_bf16 v[74:77], v[104:107], v[110:113], 0
mvv 30,140,38
mvv 34,38,144
s_waitcnt lgkmcnt(2)
mvv 26,148,42
R 38,27648
mvv 30,152,42
mvv 34,42,156
v_cvt_pk_bf16_f32 v110, v70, v71
v_cvt_pk_bf16_f32 v111, v72, v73
mvv 26,160,46
R 42,28672
v_cvt_pk_bf16_f32 v112, v74, v75
mvv 30,164,46
v_cvt_pk_bf16_f32 v113, v76, v77
buffer_store_dwordx4 v[110:113], v81, s[36:39], s49 offen sc1
mvv 34,46,168
s_add_u32 s49, s49, 346112
s_waitcnt lgkmcnt(2)
mvv 26,172,50
R 46,29696
mvv 30,176,50
mvv 34,50,180
mvv 26,184,54
R 50,30720
mvv 30,188,54
mvv 34,54,192
s_waitcnt lgkmcnt(2)
mvv 26,196,38
R 54,31744
v_xor_b32 v80, 0x10000, v80
mvv 30,200,38
mvv 34,38,204
mvv 26,208,42
R 38,32768
mvv 30,212,42
ds_read_b64 v[120:121], v82 offset:128
ds_read_b128 v[70:73], v83 offset:128
mvv 34,42,216
ds_read_b128 v[74:77], v83 offset:144
s_waitcnt lgkmcnt(5)
mvv 26,220,46
R 42,33792
mvv 30,224,46
mvv 34,46,228
mvv 26,232,50
R 46,34816
mvv 30,236,50
mvv 34,50,240
s_waitcnt lgkmcnt(2)
mvv 26,244,54
R 50,35840
mvv 30,248,54
mvv 34,54,252
s_mov_b32 s60, -13
.p2align 6
LQA_2:
v_mfma_f32_16x16x32_bf16 v[14:17], a[0:3], v[38:41], 0
R 54,36864
v_mul_f32 v71, v71, v118
v_mfma_f32_16x16x32_bf16 v[18:21], a[4:7], v[38:41], 0
v_mul_f32 v73, v73, v118
v_mul_f32 v75, v75, v118
v_mfma_f32_16x16x32_bf16 v[22:25], v[38:41], a[8:11], 0
v_mul_f32 v77, v77, v118
s_waitcnt lgkmcnt(2)
mav 14,12,42
R 58,37888
v_fma_f32 v34, v34, v70, v71
mav 18,16,42
v_fma_f32 v35, v35, v72, v73
v_fma_f32 v36, v36, v74, v75
mva 22,42,20
v_fma_f32 v37, v37, v76, v77
v_cvt_pk_bf16_f32 v10, v34, v35
mav 14,24,46
R 38,38912
v_cvt_pk_bf16_f32 v11, v36, v37
mav 18,28,46
v_mul_f32 v122, v121, v62
v_mfma_f32_16x16x32_bf16 v[70:73], v[96:99], v[0:3], v[84:87]
v_fma_f32 v26, v26, v120, v122
v_mul_f32 v123, v121, v63
mva 22,46,32
v_fma_f32 v27, v27, v120, v123
s_waitcnt lgkmcnt(2)
mav 14,36,50
R 42,39936
v_mul_f32 v122, v121, v64
mav 18,40,50
v_fma_f32 v28, v28, v120, v122
v_mul_f32 v123, v121, v65
mva 22,50,44
v_fma_f32 v29, v29, v120, v123
v_mul_f32 v122, v121, v66
mav 14,48,54
R 46,40960
v_fma_f32 v30, v30, v120, v122
mav 18,52,54
v_mul_f32 v123, v121, v67
v_fma_f32 v31, v31, v120, v123
mva 22,54,56
v_mul_f32 v122, v121, v68
s_waitcnt lgkmcnt(2)
mav 14,60,58
R 50,41984
v_fma_f32 v32, v32, v120, v122
mav 18,64,58
v_mul_f32 v123, v121, v69
v_fma_f32 v33, v33, v120, v123
mva 22,58,68
mav 14,72,38
R 54,43008
mav 18,76,38
mva 22,38,80
s_waitcnt lgkmcnt(2)
mav 14,84,42
R 58,44032
mav 18,88,42
mva 22,42,92
mav 14,96,46
R 38,45056
v_add_u32 v78, 1, v78
mav 18,100,46
s_barrier
ds_read_b128 v[100:103], v81 offset:4096
mva 22,46,104
ds_read_b64 v[106:107], v81 offset:5120
s_waitcnt lgkmcnt(4)
mav 14,108,50
R 42,46080
mav 18,112,50
mva 22,50,116
mav 14,120,54
R 46,47104
mav 18,124,54
s_waitcnt vmcnt(0)
buffer_store_dwordx2 v[78:79], off, s[36:39], s52 sc1
mva 22,54,128
s_waitcnt lgkmcnt(3)
v_mfma_f32_16x16x32_bf16 v[74:77], v[100:103], v[0:3], v[88:91]
v_cvt_pk_bf16_f32 v0, v26, v27
v_cvt_pk_bf16_f32 v1, v28, v29
mav 14,132,58
R 50,48128
v_cvt_pk_bf16_f32 v2, v30, v31
mav 18,136,58
v_cvt_pk_bf16_f32 v3, v32, v33
mva 22,58,140
v_max3_f32 v114, v70, v71, v74
v_max3_f32 v115, v72, v73, v76
mav 14,144,38
R 54,49152
v_max_f32 v114, v114, v75
mav 18,148,38
v_max_f32 v115, v115, v77
v_max_f32 v116, v114, v115
mva 22,38,152
v_max_f32 v117, v114, v115
s_waitcnt lgkmcnt(2)
mav 14,156,42
R 38,50176
v_permlane32_swap_b32 v116, v117
mav 18,160,42
v_max_f32 v110, v116, v117
v_max_f32 v111, v116, v117
mva 22,42,164
mav 14,168,46
R 42,51200
v_permlane16_swap_b32 v110, v111
mav 18,172,46
v_max_f32 v116, v110, v111
v_mul_f32 v116, s65, v116
mva 22,46,176
v_fma_f32 v114, v114, s65, -v116
s_waitcnt lgkmcnt(2)
mav 14,180,50
R 46,52224
v_fma_f32 v115, v115, s65, -v116
mav 18,184,50
v_exp_f32 v114, v114
mva 22,50,188
v_exp_f32 v115, v115
mav 14,192,54
R 50,53248
v_add_f32 v116, v114, v115
mav 18,196,54
v_add_f32 v117, v114, v115
mva 22,54,200
s_waitcnt lgkmcnt(2)
mav 14,204,38
R 54,54272
v_permlane32_swap_b32 v116, v117
mav 18,208,38
v_add_f32 v110, v116, v117
v_add_f32 v111, v116, v117
mva 22,38,212
mav 14,216,42
R 38,55296
v_permlane16_swap_b32 v110, v111
mav 18,220,42
v_add_f32 v116, v110, v111
mva 22,42,224
s_waitcnt lgkmcnt(2)
mav 14,228,46
R 42,56320
mav 18,232,46
v_rcp_f32 v116, v116
mva 22,46,236
v_mul_f32 v114, v114, v116
v_mul_f32 v115, v115, v116
mav 14,240,50
R 46,57344
v_cvt_pk_bf16_f32 v114, v114, v114
mav 18,244,50
v_cvt_pk_bf16_f32 v115, v115, v115
v_and_b32 v110, v114, v92
mva 22,50,248
v_and_b32 v111, v115, v93
s_waitcnt lgkmcnt(2)
mav 14,252,54
R 50,58368
v_and_b32 v112, v114, v94
mvv 18,128,54
v_and_b32 v113, v115, v95
mvv 22,54,132
mvv 14,136,38
R 54,59392
v_mfma_f32_16x16x32_bf16 v[70:73], v[8:11], v[110:113], 0
v_mfma_f32_16x16x32_bf16 v[74:77], v[104:107], v[110:113], 0
mvv 18,140,38
mvv 22,38,144
s_waitcnt lgkmcnt(2)
mvv 14,148,42
R 38,60416
mvv 18,152,42
mvv 22,42,156
v_cvt_pk_bf16_f32 v110, v70, v71
v_cvt_pk_bf16_f32 v111, v72, v73
mvv 14,160,46
R 42,61440
v_cvt_pk_bf16_f32 v112, v74, v75
mvv 18,164,46
v_cvt_pk_bf16_f32 v113, v76, v77
buffer_store_dwordx4 v[110:113], v81, s[36:39], s49 offen sc1
mvv 22,46,168
s_add_u32 s49, s49, 346112
s_waitcnt lgkmcnt(2)
mvv 14,172,50
R 46,62464
mvv 18,176,50
mvv 22,50,180
mvv 14,184,54
R 50,63488
mvv 18,188,54
mvv 22,54,192
s_waitcnt lgkmcnt(2)
mvv 14,196,38
R 54,64512
mvv 18,200,38
mvv 22,38,204
mvv 14,208,42
R 38,0
mvv 18,212,42
ds_read_b64 v[120:121], v82 offset:0
ds_read_b128 v[70:73], v83 offset:0
mvv 22,42,216
ds_read_b128 v[74:77], v83 offset:16
s_waitcnt lgkmcnt(5)
mvv 14,220,46
R 42,1024
mvv 18,224,46
mvv 22,46,228
mvv 14,232,50
R 46,2048
mvv 18,236,50
mvv 22,50,240
s_waitcnt lgkmcnt(2)
mvv 14,244,54
R 50,3072
mvv 18,248,54
mvv 22,54,252
v_mfma_f32_16x16x32_bf16 v[26:29], a[0:3], v[38:41], 0
R 54,4096
v_mul_f32 v71, v71, v118
v_mfma_f32_16x16x32_bf16 v[30:33], a[4:7], v[38:41], 0
v_mul_f32 v73, v73, v118
v_mul_f32 v75, v75, v118
v_mfma_f32_16x16x32_bf16 v[34:37], v[38:41], a[8:11], 0
v_mul_f32 v77, v77, v118
s_waitcnt lgkmcnt(2)
mav 26,12,42
R 58,5120
v_fma_f32 v22, v22, v70, v71
mav 30,16,42
v_fma_f32 v23, v23, v72, v73
v_fma_f32 v24, v24, v74, v75
mva 34,42,20
v_fma_f32 v25, v25, v76, v77
v_cvt_pk_bf16_f32 v8, v22, v23
mav 26,24,46
R 38,6144
v_cvt_pk_bf16_f32 v9, v24, v25
mav 30,28,46
v_mul_f32 v122, v121, v62
v_mfma_f32_16x16x32_bf16 v[70:73], v[100:103], v[0:3], v[84:87]
v_fma_f32 v14, v14, v120, v122
v_mul_f32 v123, v121, v63
mva 34,46,32
v_fma_f32 v15, v15, v120, v123
s_waitcnt lgkmcnt(2)
mav 26,36,50
R 42,7168
v_mul_f32 v122, v121, v64
mav 30,40,50
v_fma_f32 v16, v16, v120, v122
v_mul_f32 v123, v121, v65
mva 34,50,44
v_fma_f32 v17, v17, v120, v123
v_mul_f32 v122, v121, v66
mav 26,48,54
R 46,8192
v_fma_f32 v18, v18, v120, v122
mav 30,52,54
v_mul_f32 v123, v121, v67
v_fma_f32 v19, v19, v120, v123
mva 34,54,56
v_mul_f32 v122, v121, v68
s_waitcnt lgkmcnt(2)
mav 26,60,58
R 50,9216
v_fma_f32 v20, v20, v120, v122
mav 30,64,58
v_mul_f32 v123, v121, v69
v_fma_f32 v21, v21, v120, v123
mva 34,58,68
mav 26,72,38
R 54,10240
mav 30,76,38
mva 34,38,80
s_waitcnt lgkmcnt(2)
mav 26,84,42
R 58,11264
mav 30,88,42
mva 34,42,92
mav 26,96,46
R 38,12288
v_add_u32 v78, 1, v78
mav 30,100,46
s_barrier
ds_read_b128 v[96:99], v81 offset:0
mva 34,46,104
ds_read_b64 v[104:105], v81 offset:1024
s_waitcnt lgkmcnt(4)
mav 26,108,50
R 42,13312
mav 30,112,50
mva 34,50,116
mav 26,120,54
R 46,14336
mav 30,124,54
s_waitcnt vmcnt(0)
buffer_store_dwordx2 v[78:79], off, s[36:39], s52 sc1
mva 34,54,128
s_waitcnt lgkmcnt(3)
v_mfma_f32_16x16x32_bf16 v[74:77], v[96:99], v[0:3], v[88:91]
v_cvt_pk_bf16_f32 v0, v14, v15
v_cvt_pk_bf16_f32 v1, v16, v17
mav 26,132,58
R 50,15360
v_cvt_pk_bf16_f32 v2, v18, v19
mav 30,136,58
v_cvt_pk_bf16_f32 v3, v20, v21
mva 34,58,140
v_max3_f32 v114, v70, v71, v74
v_max3_f32 v115, v72, v73, v76
mav 26,144,38
R 54,16384
v_max_f32 v114, v114, v75
mav 30,148,38
v_max_f32 v115, v115, v77
v_max_f32 v116, v114, v115
mva 34,38,152
v_max_f32 v117, v114, v115
s_waitcnt lgkmcnt(2)
mav 26,156,42
R 38,17408
v_permlane32_swap_b32 v116, v117
mav 30,160,42
v_max_f32 v110, v116, v117
v_max_f32 v111, v116, v117
mva 34,42,164
mav 26,168,46
R 42,18432
v_permlane16_swap_b32 v110, v111
mav 30,172,46
v_max_f32 v116, v110, v111
v_mul_f32 v116, s65, v116
mva 34,46,176
v_fma_f32 v114, v114, s65, -v116
s_waitcnt lgkmcnt(2)
mav 26,180,50
R 46,19456
v_fma_f32 v115, v115, s65, -v116
mav 30,184,50
v_exp_f32 v114, v114
mva 34,50,188
v_exp_f32 v115, v115
mav 26,192,54
R 50,20480
v_add_f32 v116, v114, v115
mav 30,196,54
v_add_f32 v117, v114, v115
mva 34,54,200
s_waitcnt lgkmcnt(2)
mav 26,204,38
R 54,21504
v_permlane32_swap_b32 v116, v117
mav 30,208,38
v_add_f32 v110, v116, v117
v_add_f32 v111, v116, v117
mva 34,38,212
mav 26,216,42
R 38,22528
v_permlane16_swap_b32 v110, v111
mav 30,220,42
v_add_f32 v116, v110, v111
mva 34,42,224
s_waitcnt lgkmcnt(2)
mav 26,228,46
R 42,23552
mav 30,232,46
v_rcp_f32 v116, v116
mva 34,46,236
v_mul_f32 v114, v114, v116
v_mul_f32 v115, v115, v116
mav 26,240,50
R 46,24576
v_cvt_pk_bf16_f32 v114, v114, v114
mav 30,244,50
v_cvt_pk_bf16_f32 v115, v115, v115
v_and_b32 v110, v114, v94
mva 34,50,248
v_and_b32 v111, v115, v95
s_waitcnt lgkmcnt(2)
mav 26,252,54
R 50,25600
v_and_b32 v112, v114, v92
mvv 30,128,54
v_and_b32 v113, v115, v93
mvv 34,54,132
mvv 26,136,38
R 54,26624
v_mfma_f32_16x16x32_bf16 v[70:73], v[8:11], v[110:113], 0
v_mfma_f32_16x16x32_bf16 v[74:77], v[104:107], v[110:113], 0
mvv 30,140,38
mvv 34,38,144
s_waitcnt lgkmcnt(2)
mvv 26,148,42
R 38,27648
mvv 30,152,42
mvv 34,42,156
v_cvt_pk_bf16_f32 v110, v70, v71
v_cvt_pk_bf16_f32 v111, v72, v73
mvv 26,160,46
R 42,28672
v_cvt_pk_bf16_f32 v112, v74, v75
mvv 30,164,46
v_cvt_pk_bf16_f32 v113, v76, v77
buffer_store_dwordx4 v[110:113], v81, s[36:39], s49 offen sc1
mvv 34,46,168
s_add_u32 s49, s49, 346112
s_waitcnt lgkmcnt(2)
mvv 26,172,50
R 46,29696
mvv 30,176,50
mvv 34,50,180
mvv 26,184,54
R 50,30720
mvv 30,188,54
mvv 34,54,192
s_waitcnt lgkmcnt(2)
mvv 26,196,38
R 54,31744
v_xor_b32 v80, 0x10000, v80
mvv 30,200,38
mvv 34,38,204
mvv 26,208,42
R 38,32768
mvv 30,212,42
ds_read_b64 v[120:121], v82 offset:128
ds_read_b128 v[70:73], v83 offset:128
mvv 34,42,216
ds_read_b128 v[74:77], v83 offset:144
s_waitcnt lgkmcnt(5)
mvv 26,220,46
R 42,33792
mvv 30,224,46
mvv 34,46,228
mvv 26,232,50
R 46,34816
mvv 30,236,50
mvv 34,50,240
s_waitcnt lgkmcnt(2)
mvv 26,244,54
R 50,35840
mvv 30,248,54
mvv 34,54,252
s_add_u32 s60, s60, 1
s_cbranch_scc0 LQA_2
v_mfma_f32_16x16x32_bf16 v[14:17], a[0:3], v[38:41], 0
R 54,36864
v_mul_f32 v71, v71, v118
v_mfma_f32_16x16x32_bf16 v[18:21], a[4:7], v[38:41], 0
v_mul_f32 v73, v73, v118
v_mul_f32 v75, v75, v118
v_mfma_f32_16x16x32_bf16 v[22:25], v[38:41], a[8:11], 0
v_mul_f32 v77, v77, v118
s_waitcnt lgkmcnt(2)
mav 14,12,42
R 58,37888
v_fma_f32 v34, v34, v70, v71
mav 18,16,42
v_fma_f32 v35, v35, v72, v73
v_fma_f32 v36, v36, v74, v75
mva 22,42,20
v_fma_f32 v37, v37, v76, v77
v_cvt_pk_bf16_f32 v10, v34, v35
mav 14,24,46
R 38,38912
v_cvt_pk_bf16_f32 v11, v36, v37
mav 18,28,46
v_mul_f32 v122, v121, v62
v_mfma_f32_16x16x32_bf16 v[70:73], v[96:99], v[0:3], v[84:87]
v_fma_f32 v26, v26, v120, v122
v_mul_f32 v123, v121, v63
mva 22,46,32
v_fma_f32 v27, v27, v120, v123
s_waitcnt lgkmcnt(2)
mav 14,36,50
R 42,39936
v_mul_f32 v122, v121, v64
mav 18,40,50
v_fma_f32 v28, v28, v120, v122
v_mul_f32 v123, v121, v65
mva 22,50,44
v_fma_f32 v29, v29, v120, v123
v_mul_f32 v122, v121, v66
mav 14,48,54
R 46,40960
v_fma_f32 v30, v30, v120, v122
mav 18,52,54
v_mul_f32 v123, v121, v67
v_fma_f32 v31, v31, v120, v123
mva 22,54,56
v_mul_f32 v122, v121, v68
s_waitcnt lgkmcnt(2)
mav 14,60,58
R 50,41984
v_fma_f32 v32, v32, v120, v122
mav 18,64,58
v_mul_f32 v123, v121, v69
v_fma_f32 v33, v33, v120, v123
mva 22,58,68
mav 14,72,38
R 54,43008
mav 18,76,38
mva 22,38,80
s_waitcnt lgkmcnt(2)
mav 14,84,42
R 58,44032
mav 18,88,42
mva 22,42,92
mav 14,96,46
R 38,45056
v_add_u32 v78, 1, v78
mav 18,100,46
s_barrier
ds_read_b128 v[100:103], v81 offset:4096
mva 22,46,104
ds_read_b64 v[106:107], v81 offset:5120
s_waitcnt lgkmcnt(4)
mav 14,108,50
R 42,46080
mav 18,112,50
mva 22,50,116
mav 14,120,54
R 46,47104
mav 18,124,54
s_waitcnt vmcnt(0)
buffer_store_dwordx2 v[78:79], off, s[36:39], s52 sc1
mva 22,54,128
s_waitcnt lgkmcnt(3)
v_mfma_f32_16x16x32_bf16 v[74:77], v[100:103], v[0:3], v[88:91]
v_cvt_pk_bf16_f32 v0, v26, v27
v_cvt_pk_bf16_f32 v1, v28, v29
mav 14,132,58
R 50,48128
v_cvt_pk_bf16_f32 v2, v30, v31
mav 18,136,58
v_cvt_pk_bf16_f32 v3, v32, v33
mva 22,58,140
v_max3_f32 v114, v70, v71, v74
v_max3_f32 v115, v72, v73, v76
mav 14,144,38
R 54,49152
v_max_f32 v114, v114, v75
mav 18,148,38
v_max_f32 v115, v115, v77
v_max_f32 v116, v114, v115
mva 22,38,152
v_max_f32 v117, v114, v115
s_waitcnt lgkmcnt(2)
mav 14,156,42
R 38,50176
v_permlane32_swap_b32 v116, v117
mav 18,160,42
v_max_f32 v110, v116, v117
v_max_f32 v111, v116, v117
mva 22,42,164
mav 14,168,46
R 42,51200
v_permlane16_swap_b32 v110, v111
mav 18,172,46
v_max_f32 v116, v110, v111
v_mul_f32 v116, s65, v116
mva 22,46,176
v_fma_f32 v114, v114, s65, -v116
s_waitcnt lgkmcnt(2)
mav 14,180,50
R 46,52224
v_fma_f32 v115, v115, s65, -v116
mav 18,184,50
v_exp_f32 v114, v114
mva 22,50,188
v_exp_f32 v115, v115
mav 14,192,54
R 50,53248
v_add_f32 v116, v114, v115
mav 18,196,54
v_add_f32 v117, v114, v115
mva 22,54,200
s_waitcnt lgkmcnt(2)
mav 14,204,38
R 54,54272
v_permlane32_swap_b32 v116, v117
mav 18,208,38
v_add_f32 v110, v116, v117
v_add_f32 v111, v116, v117
mva 22,38,212
mav 14,216,42
R 38,55296
v_permlane16_swap_b32 v110, v111
mav 18,220,42
v_add_f32 v116, v110, v111
mva 22,42,224
s_waitcnt lgkmcnt(2)
mav 14,228,46
R 42,56320
mav 18,232,46
v_rcp_f32 v116, v116
mva 22,46,236
v_mul_f32 v114, v114, v116
v_mul_f32 v115, v115, v116
mav 14,240,50
R 46,57344
v_cvt_pk_bf16_f32 v114, v114, v114
mav 18,244,50
v_cvt_pk_bf16_f32 v115, v115, v115
v_and_b32 v110, v114, v92
mva 22,50,248
v_and_b32 v111, v115, v93
s_waitcnt lgkmcnt(2)
mav 14,252,54
R 50,58368
v_and_b32 v112, v114, v94
mvv 18,128,54
v_and_b32 v113, v115, v95
mvv 22,54,132
mvv 14,136,38
R 54,59392
v_mfma_f32_16x16x32_bf16 v[70:73], v[8:11], v[110:113], 0
v_mfma_f32_16x16x32_bf16 v[74:77], v[104:107], v[110:113], 0
mvv 18,140,38
mvv 22,38,144
s_waitcnt lgkmcnt(2)
mvv 14,148,42
R 38,60416
mvv 18,152,42
mvv 22,42,156
v_cvt_pk_bf16_f32 v110, v70, v71
v_cvt_pk_bf16_f32 v111, v72, v73
mvv 14,160,46
R 42,61440
v_cvt_pk_bf16_f32 v112, v74, v75
mvv 18,164,46
v_cvt_pk_bf16_f32 v113, v76, v77
buffer_store_dwordx4 v[110:113], v81, s[36:39], s49 offen sc1
mvv 22,46,168
s_add_u32 s49, s49, 346112
s_waitcnt lgkmcnt(2)
mvv 14,172,50
R 46,62464
mvv 18,176,50
mvv 22,50,180
mvv 14,184,54
R 50,63488
mvv 18,188,54
mvv 22,54,192
s_waitcnt lgkmcnt(2)
mvv 14,196,38
R 54,64512
mvv 18,200,38
mvv 22,38,204
mvv 14,208,42
R 38,0
mvv 18,212,42
ds_read_b64 v[120:121], v82 offset:0
ds_read_b128 v[70:73], v83 offset:0
mvv 22,42,216
ds_read_b128 v[74:77], v83 offset:16
s_waitcnt lgkmcnt(5)
mvv 14,220,46
R 42,1024
mvv 18,224,46
mvv 22,46,228
mvv 14,232,50
R 46,2048
mvv 18,236,50
mvv 22,50,240
s_waitcnt lgkmcnt(2)
mvv 14,244,54
R 50,3072
mvv 18,248,54
mvv 22,54,252
v_mfma_f32_16x16x32_bf16 v[26:29], a[0:3], v[38:41], 0
R 54,4096
v_mul_f32 v71, v71, v118
v_mfma_f32_16x16x32_bf16 v[30:33], a[4:7], v[38:41], 0
v_mul_f32 v73, v73, v118
v_mul_f32 v75, v75, v118
v_mfma_f32_16x16x32_bf16 v[34:37], v[38:41], a[8:11], 0
v_mul_f32 v77, v77, v118
s_waitcnt lgkmcnt(2)
mav 26,12,42
R 58,5120
v_fma_f32 v22, v22, v70, v71
mav 30,16,42
v_fma_f32 v23, v23, v72, v73
v_fma_f32 v24, v24, v74, v75
mva 34,42,20
v_fma_f32 v25, v25, v76, v77
v_cvt_pk_bf16_f32 v8, v22, v23
mav 26,24,46
R 38,6144
v_cvt_pk_bf16_f32 v9, v24, v25
mav 30,28,46
v_mul_f32 v122, v121, v62
v_mfma_f32_16x16x32_bf16 v[70:73], v[100:103], v[0:3], v[84:87]
v_fma_f32 v14, v14, v120, v122
v_mul_f32 v123, v121, v63
mva 34,46,32
v_fma_f32 v15, v15, v120, v123
s_waitcnt lgkmcnt(2)
mav 26,36,50
R 42,7168
v_mul_f32 v122, v121, v64
mav 30,40,50
buffer_load_dwordx4 v[4:7], v81, s[36:39], s55 offen offset:2048 sc1
buffer_load_dwordx2 v[12:13], v81, s[36:39], s55 offen offset:3072 sc1
mva 34,50,44
buffer_load_dwordx2 v[108:109], v81, s[36:39], s55 offen offset:3080 sc1
v_fma_f32 v16, v16, v120, v122
mav 26,48,54
R 46,8192
v_mul_f32 v123, v121, v65
mav 30,52,54
v_fma_f32 v17, v17, v120, v123
v_mul_f32 v122, v121, v66
mva 34,54,56
v_fma_f32 v18, v18, v120, v122
s_waitcnt lgkmcnt(2)
mav 26,60,58
R 50,9216
v_mul_f32 v123, v121, v67
mav 30,64,58
v_fma_f32 v19, v19, v120, v123
v_mul_f32 v122, v121, v68
mva 34,58,68
v_fma_f32 v20, v20, v120, v122
v_mul_f32 v123, v121, v69
mav 26,72,38
R 54,10240
v_fma_f32 v21, v21, v120, v123
mav 30,76,38
mva 34,38,80
s_waitcnt lgkmcnt(2)
mav 26,84,42
R 58,11264
mav 30,88,42
mva 34,42,92
mav 26,96,46
R 38,12288
v_add_u32 v78, 1, v78
mav 30,100,46
s_barrier
ds_read_b128 v[96:99], v81 offset:0
mva 34,46,104
ds_read_b64 v[104:105], v81 offset:1024
s_waitcnt lgkmcnt(4)
mav 26,108,50
R 42,13312
mav 30,112,50
mva 34,50,116
mav 26,120,54
R 46,14336
mav 30,124,54
s_waitcnt vmcnt(3)
buffer_store_dwordx2 v[78:79], off, s[36:39], s52 sc1
mva 34,54,128
s_waitcnt lgkmcnt(3)
v_mfma_f32_16x16x32_bf16 v[74:77], v[96:99], v[0:3], v[88:91]
v_cvt_pk_bf16_f32 v0, v14, v15
v_cvt_pk_bf16_f32 v1, v16, v17
mav 26,132,58
R 50,15360
v_cvt_pk_bf16_f32 v2, v18, v19
mav 30,136,58
v_cvt_pk_bf16_f32 v3, v20, v21
mva 34,58,140
v_max3_f32 v114, v70, v71, v74
v_max3_f32 v115, v72, v73, v76
mav 26,144,38
R 54,16384
v_max_f32 v114, v114, v75
mav 30,148,38
v_max_f32 v115, v115, v77
v_max_f32 v116, v114, v115
mva 34,38,152
v_max_f32 v117, v114, v115
s_waitcnt lgkmcnt(2)
mav 26,156,42
R 38,17408
v_permlane32_swap_b32 v116, v117
mav 30,160,42
v_max_f32 v110, v116, v117
v_max_f32 v111, v116, v117
mva 34,42,164
mav 26,168,46
R 42,18432
v_permlane16_swap_b32 v110, v111
mav 30,172,46
v_max_f32 v116, v110, v111
v_mul_f32 v116, s65, v116
mva 34,46,176
v_fma_f32 v114, v114, s65, -v116
s_waitcnt lgkmcnt(2)
mav 26,180,50
R 46,19456
v_fma_f32 v115, v115, s65, -v116
mav 30,184,50
v_exp_f32 v114, v114
mva 34,50,188
v_exp_f32 v115, v115
mav 26,192,54
R 50,20480
v_add_f32 v116, v114, v115
mav 30,196,54
v_add_f32 v117, v114, v115
mva 34,54,200
s_waitcnt lgkmcnt(2)
mav 26,204,38
R 54,21504
v_permlane32_swap_b32 v116, v117
mav 30,208,38
v_add_f32 v110, v116, v117
v_add_f32 v111, v116, v117
mva 34,38,212
mav 26,216,42
R 38,22528
v_permlane16_swap_b32 v110, v111
mav 30,220,42
v_add_f32 v116, v110, v111
mva 34,42,224
s_waitcnt lgkmcnt(2)
mav 26,228,46
R 42,23552
mav 30,232,46
v_rcp_f32 v116, v116
mva 34,46,236
v_mul_f32 v114, v114, v116
v_mul_f32 v115, v115, v116
mav 26,240,50
R 46,24576
v_cvt_pk_bf16_f32 v114, v114, v114
mav 30,244,50
v_cvt_pk_bf16_f32 v115, v115, v115
v_and_b32 v110, v114, v94
mva 34,50,248
v_and_b32 v111, v115, v95
s_waitcnt lgkmcnt(2)
mav 26,252,54
R 50,25600
v_and_b32 v112, v114, v92
mvv 30,128,54
v_and_b32 v113, v115, v93
mvv 34,54,132
mvv 26,136,38
R 54,26624
v_mfma_f32_16x16x32_bf16 v[70:73], v[8:11], v[110:113], 0
v_mfma_f32_16x16x32_bf16 v[74:77], v[104:107], v[110:113], 0
mvv 30,140,38
mvv 34,38,144
s_waitcnt lgkmcnt(2)
mvv 26,148,42
R 38,27648
mvv 30,152,42
mvv 34,42,156
v_cvt_pk_bf16_f32 v110, v70, v71
v_cvt_pk_bf16_f32 v111, v72, v73
mvv 26,160,46
R 42,28672
v_cvt_pk_bf16_f32 v112, v74, v75
mvv 30,164,46
v_cvt_pk_bf16_f32 v113, v76, v77
buffer_store_dwordx4 v[110:113], v81, s[36:39], s49 offen sc1
mvv 34,46,168
s_add_u32 s49, s49, 346112
s_waitcnt lgkmcnt(2)
mvv 26,172,50
R 46,29696
mvv 30,176,50
mvv 34,50,180
mvv 26,184,54
R 50,30720
mvv 30,188,54
mvv 34,54,192
s_waitcnt lgkmcnt(2)
mvv 26,196,38
R 54,31744
mvv 30,200,38
mvv 34,38,204
mvv 26,208,42
mvv 30,212,42
ds_read_b64 v[120:121], v82 offset:128
ds_read_b128 v[70:73], v83 offset:128
mvv 34,42,216
ds_read_b128 v[74:77], v83 offset:144
s_waitcnt lgkmcnt(4)
mvv 26,220,46
mvv 30,224,46
mvv 34,46,228
mvv 26,232,50
mvv 30,236,50
mvv 34,50,240
s_waitcnt lgkmcnt(0)
mvv 26,244,54
mvv 30,248,54
mvv 34,54,252
v_mul_f32 v71, v71, v118
s_nop 6
v_fma_f32 v34, v34, v70, v71
v_mul_f32 v73, v73, v118
v_fma_f32 v35, v35, v72, v73
v_mul_f32 v75, v75, v118
v_fma_f32 v36, v36, v74, v75
v_mul_f32 v77, v77, v118
v_fma_f32 v37, v37, v76, v77
v_cvt_pk_bf16_f32 v10, v34, v35
v_cvt_pk_bf16_f32 v11, v36, v37
s_nop 1
v_mfma_f32_16x16x32_bf16 v[70:73], v[96:99], v[0:3], v[84:87]
v_mul_f32 v122, v121, v62
v_fma_f32 v26, v26, v120, v122
v_mul_f32 v123, v121, v63
v_fma_f32 v27, v27, v120, v123
v_mul_f32 v122, v121, v64
v_fma_f32 v28, v28, v120, v122
)"
R"(
v_mul_f32 v123, v121, v65
v_fma_f32 v29, v29, v120, v123
v_mul_f32 v122, v121, v66
v_fma_f32 v30, v30, v120, v122
v_mul_f32 v123, v121, v67
v_fma_f32 v31, v31, v120, v123
v_mul_f32 v122, v121, v68
v_fma_f32 v32, v32, v120, v122
v_mul_f32 v123, v121, v69
v_fma_f32 v33, v33, v120, v123
s_barrier
ds_read_b128 v[100:103], v81 offset:4096
ds_read_b64 v[106:107], v81 offset:5120
s_waitcnt lgkmcnt(0)
v_mfma_f32_16x16x32_bf16 v[74:77], v[100:103], v[0:3], v[88:91]
s_nop 7
v_max3_f32 v114, v70, v71, v74
v_max3_f32 v115, v72, v73, v76
v_max_f32 v114, v114, v75
v_max_f32 v115, v115, v77
v_max_f32 v116, v114, v115
v_max_f32 v117, v114, v115
s_nop 1
v_permlane32_swap_b32 v116, v117
v_max_f32 v110, v116, v117
v_max_f32 v111, v116, v117
s_nop 1
v_permlane16_swap_b32 v110, v111
v_max_f32 v116, v110, v111
v_mul_f32 v116, s65, v116
v_fma_f32 v114, v114, s65, -v116
v_fma_f32 v115, v115, s65, -v116
v_exp_f32 v114, v114
v_exp_f32 v115, v115
s_nop 0
v_add_f32 v116, v114, v115
v_add_f32 v117, v114, v115
s_nop 1
v_permlane32_swap_b32 v116, v117
v_add_f32 v110, v116, v117
v_add_f32 v111, v116, v117
s_nop 1
v_permlane16_swap_b32 v110, v111
v_add_f32 v116, v110, v111
v_rcp_f32 v116, v116
s_nop 0
v_mul_f32 v114, v114, v116
v_mul_f32 v115, v115, v116
v_cvt_pk_bf16_f32 v114, v114, v114
v_cvt_pk_bf16_f32 v115, v115, v115
v_and_b32 v110, v114, v92
v_and_b32 v111, v115, v93
v_and_b32 v112, v114, v94
v_and_b32 v113, v115, v95
s_nop 1
v_mfma_f32_16x16x32_bf16 v[70:73], v[8:11], v[110:113], 0
v_mfma_f32_16x16x32_bf16 v[74:77], v[104:107], v[110:113], 0
s_nop 6
v_cvt_pk_bf16_f32 v110, v70, v71
v_cvt_pk_bf16_f32 v111, v72, v73
v_cvt_pk_bf16_f32 v112, v74, v75
v_cvt_pk_bf16_f32 v113, v76, v77
buffer_store_dwordx4 v[110:113], v81, s[36:39], s49 offen sc1
s_add_u32 s49, s49, 346112
v_cvt_pk_bf16_f32 v0, v26, v27
v_cvt_pk_bf16_f32 v1, v28, v29
v_cvt_pk_bf16_f32 v2, v30, v31
v_cvt_pk_bf16_f32 v3, v32, v33
v_add_u32 v78, 1, v78
s_waitcnt vmcnt(1)
buffer_store_dwordx2 v[78:79], off, s[36:39], s52 sc1
v_mfma_f32_16x16x32_bf16 v[70:73], v[100:103], v[0:3], v[84:87]
v_min_f32 v74, s56, v88
v_min_f32 v75, s56, v89
v_min_f32 v76, s56, v90
v_min_f32 v77, s56, v91
s_nop 1
mvv 74,4,0
s_nop 7
v_max3_f32 v114, v70, v71, v74
v_max3_f32 v115, v72, v73, v76
v_max_f32 v114, v114, v75
v_max_f32 v115, v115, v77
v_max_f32 v116, v114, v115
v_max_f32 v117, v114, v115
s_nop 1
v_permlane32_swap_b32 v116, v117
v_max_f32 v110, v116, v117
v_max_f32 v111, v116, v117
s_nop 1
v_permlane16_swap_b32 v110, v111
v_max_f32 v116, v110, v111
v_mul_f32 v116, s65, v116
v_fma_f32 v114, v114, s65, -v116
v_fma_f32 v115, v115, s65, -v116
v_exp_f32 v114, v114
v_exp_f32 v115, v115
s_nop 0
v_add_f32 v116, v114, v115
v_add_f32 v117, v114, v115
s_nop 1
v_permlane32_swap_b32 v116, v117
v_add_f32 v110, v116, v117
v_add_f32 v111, v116, v117
s_nop 1
v_permlane16_swap_b32 v110, v111
v_add_f32 v116, v110, v111
v_rcp_f32 v116, v116
s_nop 0
v_mul_f32 v114, v114, v116
v_mul_f32 v115, v115, v116
v_cvt_pk_bf16_f32 v114, v114, v114
v_cvt_pk_bf16_f32 v115, v115, v115
v_and_b32 v110, v114, v92
v_and_b32 v111, v115, v93
v_and_b32 v112, v114, v94
v_and_b32 v113, v115, v95
s_nop 1
v_mfma_f32_16x16x32_bf16 v[70:73], v[10:13], v[110:113], 0
v_mfma_f32_16x16x32_bf16 v[74:77], v[106:109], v[110:113], 0
s_nop 6
v_cvt_pk_bf16_f32 v110, v70, v71
v_cvt_pk_bf16_f32 v111, v72, v73
v_cvt_pk_bf16_f32 v112, v74, v75
v_cvt_pk_bf16_f32 v113, v76, v77
buffer_store_dwordx4 v[110:113], v81, s[36:39], s49 offen sc1
v_add_u32 v78, 1, v78
s_waitcnt vmcnt(2)
buffer_store_dwordx2 v[78:79], off, s[36:39], s52 sc1
v_add_u32 v78, 37, v78
s_waitcnt vmcnt(1)
buffer_store_dwordx2 v[78:79], off, s[36:39], s52 sc1
s_endpgm
Lr1:
s_cmp_eq_u32 s60, 1
s_cbranch_scc0 Lr2
buffer_load_dword v115, off, s[36:39], 0 sc1
s_lshr_b32 s66, s53, 10
s_mul_i32 s66, s66, 8208
s_mov_b32 s67, 0x8000
E 0,110592
E 1,114688
s_add_u32 m0, s66, 2048
s_nop 0
buffer_load_dwordx4 v3, s[44:47], s67 offen offset:0 lds
E 2,118784
s_add_u32 s51, s51, 196608
E 0,122880
s_add_u32 m0, s66, 6144
s_nop 0
buffer_load_dwordx4 v3, s[44:47], 0 offen offset:0 lds
E 1,126976
E 2,131072
s_add_u32 s51, s51, 196608
E 0,86016
E 1,90112
E 2,94208
s_add_u32 s51, s51, 196608
E 0,98304
E 1,102400
E 2,106496
s_add_u32 s51, s51, 196608
v_lshlrev_b32 v12, 2, v80
v_and_b32 v12, 0xc0, v12
v_xor_b32 v13, 0, v12
v_add_u32 v8, v7, v13
v_xor_b32 v13, 64, v12
v_add_u32 v9, v7, v13
v_xor_b32 v13, 128, v12
v_add_u32 v10, v7, v13
v_xor_b32 v13, 192, v12
v_add_u32 v11, v7, v13
v_mov_b32 v110, s64
v_mov_b32 v111, s64
v_mov_b32 v112, s64
v_mov_b32 v113, s64
s_waitcnt vmcnt(6)
s_barrier
v_add_u32 v4, 69632, v4
v_add_u32 v5, 69632, v5
v_add_u32 v6, 69632, v6
Ta 0,4,40960
Ta 4,5,40960
Ta 8,6,40960
Ta 12,4,53248
Ta 16,5,53248
Ta 20,6,53248
ds_read_b128 v[46:49], v8 offset:0
ds_read_b128 v[50:53], v9 offset:0
ds_read_b128 v[54:57], v8 offset:4096
ds_read_b128 v[58:61], v9 offset:4096
v_mov_b32 v114, 0x100
v_add_u32 v115, 0x9e3779b9, v115
buffer_store_dwordx2 v[114:115], v81, s[36:39], s52 offen sc1
v_add_u32 v91, v91, v114
E 0,61440
E 1,65536
s_add_u32 m0, s66, 2816
s_nop 0
buffer_load_dwordx4 v3, s[44:47], s67 offen offset:256 lds
E 2,69632
s_add_u32 s51, s51, 196608
E 0,73728
s_add_u32 m0, s66, 6912
s_nop 0
buffer_load_dwordx4 v3, s[44:47], 0 offen offset:256 lds
E 1,77824
E 2,81920
s_add_u32 s51, s51, 196608
E 0,36864
E 1,40960
E 2,45056
s_add_u32 s51, s51, 196608
E 0,49152
E 1,53248
E 2,57344
s_add_u32 s51, s51, 196608
s_waitcnt lgkmcnt(0)
s_waitcnt vmcnt(15)
s_barrier
E 0,110592
Ta 24,4,16384
v_mfma_f32_16x16x32_bf16 v[94:97], a[0:3], v[110:113], 0
v_mfma_f32_16x16x32_bf16 v[98:101], a[4:7], v[110:113], 0
Ta 28,5,16384
v_mfma_f32_16x16x32_bf16 v[102:105], v[110:113], a[8:11], 0
Ta 32,6,16384
E 1,114688
v_mfma_f32_16x16x32_bf16 v[22:25], a[0:3], v[46:49], 0
Ta 36,4,28672
v_mfma_f32_16x16x32_bf16 v[26:29], a[4:7], v[46:49], 0
Ta 40,5,28672
s_add_u32 m0, s66, 3584
s_nop 0
buffer_load_dwordx4 v3, s[44:47], s67 offen offset:512 lds
v_mfma_f32_16x16x32_bf16 v[30:33], v[46:49], a[8:11], 0
Ta 44,6,28672
v_mfma_f32_16x16x32_bf16 v[34:37], a[0:3], v[54:57], 0
ds_read_b128 v[62:65], v10 offset:0
E 2,118784
s_add_u32 s51, s51, 196608
v_mfma_f32_16x16x32_bf16 v[38:41], a[4:7], v[54:57], 0
ds_read_b128 v[66:69], v11 offset:0
v_mfma_f32_16x16x32_bf16 v[42:45], v[54:57], a[8:11], 0
ds_read_b128 v[70:73], v10 offset:4096
E 0,122880
mav 94,12,110
ds_read_b128 v[74:77], v11 offset:4096
mav 98,16,110
mva 102,110,20
s_add_u32 m0, s66, 7680
s_nop 0
buffer_load_dwordx4 v3, s[44:47], 0 offen offset:512 lds
mav 22,12,50
mav 26,16,50
E 1,126976
mva 30,50,20
mav 34,12,58
E 2,131072
s_add_u32 s51, s51, 196608
mav 38,16,58
mva 42,58,20
s_waitcnt lgkmcnt(0)
s_waitcnt vmcnt(14)
s_barrier
v_add_u32 v4, -49152, v4
v_add_u32 v5, -49152, v5
v_add_u32 v6, -49152, v6
E 0,86016
Ta 48,4,40960
mav 94,24,110
mav 98,28,110
Ta 52,5,40960
mva 102,110,32
Ta 56,6,40960
E 1,90112
mav 22,24,62
Ta 60,4,53248
mav 26,28,62
Ta 64,5,53248
mva 30,62,32
Ta 68,6,53248
E 2,94208
s_add_u32 s51, s51, 196608
mav 34,24,70
ds_read_b128 v[46:49], v8 offset:1024
mav 38,28,70
ds_read_b128 v[50:53], v9 offset:1024
mva 42,70,32
ds_read_b128 v[54:57], v8 offset:5120
E 0,98304
mav 94,36,110
ds_read_b128 v[58:61], v9 offset:5120
mav 98,40,110
mva 102,110,44
E 1,102400
mav 22,36,66
mav 26,40,66
mva 30,66,44
E 2,106496
s_add_u32 s51, s51, 196608
mav 34,36,74
mav 38,40,74
mva 42,74,44
s_waitcnt lgkmcnt(0)
s_waitcnt vmcnt(14)
s_barrier
E 0,61440
Ta 72,4,16384
mav 94,48,110
mav 98,52,110
Ta 76,5,16384
mva 102,110,56
Ta 80,6,16384
E 1,65536
mav 22,48,46
Ta 84,4,28672
mav 26,52,46
Ta 88,5,28672
s_add_u32 m0, s66, 4352
s_nop 0
buffer_load_dwordx4 v3, s[44:47], s67 offen offset:768 lds
mva 30,46,56
Ta 92,6,28672
mav 34,48,54
ds_read_b128 v[62:65], v10 offset:1024
E 2,69632
s_add_u32 s51, s51, 196608
mav 38,52,54
ds_read_b128 v[66:69], v11 offset:1024
mva 42,54,56
ds_read_b128 v[70:73], v10 offset:5120
E 0,73728
mav 94,60,110
ds_read_b128 v[74:77], v11 offset:5120
mav 98,64,110
mva 102,110,68
s_add_u32 m0, s66, 8448
s_nop 0
buffer_load_dwordx4 v3, s[44:47], 0 offen offset:768 lds
mav 22,60,50
mav 26,64,50
E 1,77824
mva 30,50,68
mav 34,60,58
E 2,81920
s_add_u32 s51, s51, 196608
mav 38,64,58
mva 42,58,68
s_waitcnt lgkmcnt(0)
s_waitcnt vmcnt(14)
s_barrier
v_add_u32 v4, 49152, v4
v_add_u32 v5, 49152, v5
v_add_u32 v6, 49152, v6
E 0,36864
Ta 96,4,40960
mav 94,72,110
mav 98,76,110
Ta 100,5,40960
mva 102,110,80
Ta 104,6,40960
E 1,40960
mav 22,72,62
Ta 108,4,53248
mav 26,76,62
Ta 112,5,53248
mva 30,62,80
Ta 116,6,53248
E 2,45056
s_add_u32 s51, s51, 196608
mav 34,72,70
ds_read_b128 v[46:49], v8 offset:2048
mav 38,76,70
ds_read_b128 v[50:53], v9 offset:2048
mva 42,70,80
ds_read_b128 v[54:57], v8 offset:6144
E 0,49152
mav 94,84,110
ds_read_b128 v[58:61], v9 offset:6144
mav 98,88,110
mva 102,110,92
E 1,53248
mav 22,84,66
mav 26,88,66
mva 30,66,92
E 2,57344
s_add_u32 s51, s51, 196608
mav 34,84,74
mav 38,88,74
mva 42,74,92
s_waitcnt lgkmcnt(0)
s_waitcnt vmcnt(14)
s_barrier
E 0,110592
Ta 120,4,16384
mav 94,96,110
mav 98,100,110
Ta 124,5,16384
mva 102,110,104
Ta 128,6,16384
E 1,114688
mav 22,96,46
Ta 132,4,28672
mav 26,100,46
Ta 136,5,28672
s_add_u32 m0, s66, 1024
s_nop 0
buffer_load_dwordx4 v3, s[44:47], s67 offen offset:1024 lds
mva 30,46,104
Ta 140,6,28672
mav 34,96,54
ds_read_b128 v[62:65], v10 offset:2048
E 2,118784
s_add_u32 s51, s51, 196608
mav 38,100,54
ds_read_b128 v[66:69], v11 offset:2048
mva 42,54,104
ds_read_b128 v[70:73], v10 offset:6144
E 0,122880
mav 94,108,110
ds_read_b128 v[74:77], v11 offset:6144
mav 98,112,110
mva 102,110,116
s_add_u32 m0, s66, 5120
s_nop 0
buffer_load_dwordx4 v3, s[44:47], 0 offen offset:1024 lds
mav 22,108,50
mav 26,112,50
E 1,126976
mva 30,50,116
mav 34,108,58
E 2,131072
s_add_u32 s51, s51, 196608
mav 38,112,58
mva 42,58,116
s_waitcnt lgkmcnt(0)
s_waitcnt vmcnt(14)
s_barrier
v_add_u32 v4, -49152, v4
v_add_u32 v5, -49152, v5
v_add_u32 v6, -49152, v6
E 0,86016
Ta 144,4,40960
mav 94,120,110
mav 98,124,110
Ta 148,5,40960
mva 102,110,128
Ta 152,6,40960
E 1,90112
mav 22,120,62
Ta 156,4,53248
mav 26,124,62
Ta 160,5,53248
mva 30,62,128
Ta 164,6,53248
E 2,94208
s_add_u32 s51, s51, 196608
mav 34,120,70
ds_read_b128 v[46:49], v8 offset:3072
mav 38,124,70
ds_read_b128 v[50:53], v9 offset:3072
mva 42,70,128
ds_read_b128 v[54:57], v8 offset:7168
E 0,98304
mav 94,132,110
ds_read_b128 v[58:61], v9 offset:7168
mav 98,136,110
mva 102,110,140
E 1,102400
mav 22,132,66
mav 26,136,66
mva 30,66,140
E 2,106496
s_add_u32 s51, s51, 196608
mav 34,132,74
mav 38,136,74
mva 42,74,140
s_waitcnt lgkmcnt(0)
s_waitcnt vmcnt(14)
s_barrier
E 0,61440
Ta 168,4,16384
mav 94,144,110
mav 98,148,110
Ta 172,5,16384
mva 102,110,152
Ta 176,6,16384
E 1,65536
mav 22,144,46
Ta 180,4,28672
mav 26,148,46
Ta 184,5,28672
s_add_u32 m0, s66, 1792
s_nop 0
buffer_load_dwordx4 v3, s[44:47], s67 offen offset:1280 lds
mva 30,46,152
Ta 188,6,28672
mav 34,144,54
ds_read_b128 v[62:65], v10 offset:3072
E 2,69632
s_add_u32 s51, s51, 196608
mav 38,148,54
ds_read_b128 v[66:69], v11 offset:3072
mva 42,54,152
ds_read_b128 v[70:73], v10 offset:7168
E 0,73728
mav 94,156,110
ds_read_b128 v[74:77], v11 offset:7168
mav 98,160,110
mva 102,110,164
s_add_u32 m0, s66, 5888
s_nop 0
buffer_load_dwordx4 v3, s[44:47], 0 offen offset:1280 lds
mav 22,156,50
mav 26,160,50
E 1,77824
mva 30,50,164
mav 34,156,58
E 2,81920
s_add_u32 s51, s51, 196608
mav 38,160,58
mva 42,58,164
s_waitcnt lgkmcnt(0)
s_waitcnt vmcnt(14)
s_barrier
v_add_u32 v4, 49152, v4
v_add_u32 v5, 49152, v5
v_add_u32 v6, 49152, v6
E 0,36864
Ta 192,4,40960
mav 94,168,110
mav 98,172,110
Ta 196,5,40960
mva 102,110,176
Ta 200,6,40960
E 1,40960
mav 22,168,62
Ta 204,4,53248
mav 26,172,62
Ta 208,5,53248
mva 30,62,176
Ta 212,6,53248
E 2,45056
s_add_u32 s51, s51, 196608
mav 34,168,70
ds_read_b128 v[46:49], v8 offset:0
mav 38,172,70
ds_read_b128 v[50:53], v9 offset:0
mva 42,70,176
ds_read_b128 v[54:57], v8 offset:4096
E 0,49152
mav 94,180,110
ds_read_b128 v[58:61], v9 offset:4096
mav 98,184,110
mva 102,110,188
E 1,53248
mav 22,180,66
mav 26,184,66
mva 30,66,188
E 2,57344
s_add_u32 s51, s51, 196608
mav 34,180,74
mav 38,184,74
mva 42,74,188
s_waitcnt lgkmcnt(0)
s_waitcnt vmcnt(14)
s_barrier
E 0,110592
Ta 216,4,16384
mav 94,192,110
mav 98,196,110
Ta 220,5,16384
mva 102,110,200
Ta 224,6,16384
E 1,114688
mav 22,192,46
Ta 228,4,28672
mav 26,196,46
Ta 232,5,28672
s_add_u32 m0, s66, 2560
s_nop 0
buffer_load_dwordx4 v3, s[44:47], s67 offen offset:1536 lds
mva 30,46,200
Ta 236,6,28672
mav 34,192,54
ds_read_b128 v[62:65], v10 offset:0
E 2,118784
s_add_u32 s51, s51, 196608
mav 38,196,54
ds_read_b128 v[66:69], v11 offset:0
mva 42,54,200
ds_read_b128 v[70:73], v10 offset:4096
E 0,122880
mav 94,204,110
ds_read_b128 v[74:77], v11 offset:4096
mav 98,208,110
mva 102,110,212
s_add_u32 m0, s66, 6656
s_nop 0
buffer_load_dwordx4 v3, s[44:47], 0 offen offset:1536 lds
mav 22,204,50
mav 26,208,50
E 1,126976
mva 30,50,212
mav 34,204,58
E 2,131072
s_add_u32 s51, s51, 196608
mav 38,208,58
mva 42,58,212
s_waitcnt lgkmcnt(0)
s_waitcnt vmcnt(14)
s_barrier
v_add_u32 v4, -49152, v4
v_add_u32 v5, -49152, v5
v_add_u32 v6, -49152, v6
E 0,86016
Ta 240,4,40960
mav 94,216,110
mav 98,220,110
Ta 244,5,40960
mva 102,110,224
Ta 248,6,40960
E 1,90112
mav 22,216,62
Ta 252,4,53248
mav 26,220,62
Tv 128,5,53248
mva 30,62,224
E 2,94208
s_add_u32 s51, s51, 196608
Tv 132,6,53248
mav 34,216,70
ds_read_b128 v[46:49], v8 offset:1024
mav 38,220,70
ds_read_b128 v[50:53], v9 offset:1024
E 0,98304
mva 42,70,224
ds_read_b128 v[54:57], v8 offset:5120
mav 94,228,110
ds_read_b128 v[58:61], v9 offset:5120
mav 98,232,110
E 1,102400
mva 102,110,236
mav 22,228,66
E 2,106496
s_add_u32 s51, s51, 196608
mav 26,232,66
mva 30,66,236
mav 34,228,74
buffer_load_dwordx2 v[122:123], v90, s[36:39], 0 offen sc1
mav 38,232,74
mva 42,74,236
s_waitcnt lgkmcnt(0)
s_waitcnt vmcnt(15)
s_barrier
E 0,61440
Tv 136,4,16384
mav 94,240,110
mav 98,244,110
Tv 140,5,16384
mva 102,110,248
Tv 144,6,16384
E 1,65536
mav 22,240,46
Tv 148,4,28672
mav 26,244,46
Tv 152,5,28672
s_add_u32 m0, s66, 3328
s_nop 0
buffer_load_dwordx4 v3, s[44:47], s67 offen offset:1792 lds
mva 30,46,248
Tv 156,6,28672
mav 34,240,54
ds_read_b128 v[62:65], v10 offset:1024
E 2,69632
s_add_u32 s51, s51, 196608
mav 38,244,54
ds_read_b128 v[66:69], v11 offset:1024
mva 42,54,248
ds_read_b128 v[70:73], v10 offset:5120
E 0,73728
mav 94,252,110
ds_read_b128 v[74:77], v11 offset:5120
mvv 98,128,110
mvv 102,110,132
s_add_u32 m0, s66, 7424
s_nop 0
buffer_load_dwordx4 v3, s[44:47], 0 offen offset:1792 lds
mav 22,252,50
mvv 26,128,50
E 1,77824
mvv 30,50,132
mav 34,252,58
E 2,81920
s_add_u32 s51, s51, 196608
mvv 38,128,58
mvv 42,58,132
s_waitcnt lgkmcnt(0)
s_waitcnt vmcnt(15)
s_barrier
v_add_u32 v4, 49152, v4
v_add_u32 v5, 49152, v5
v_add_u32 v6, 49152, v6
E 0,36864
Tv 160,4,40960
mvv 94,136,110
mvv 98,140,110
Tv 164,5,40960
mvv 102,110,144
Tv 168,6,40960
E 1,40960
mvv 22,136,62
Tv 172,4,53248
mvv 26,140,62
Tv 176,5,53248
mvv 30,62,144
Tv 180,6,53248
E 2,45056
s_add_u32 s51, s51, 196608
mvv 34,136,70
ds_read_b128 v[46:49], v8 offset:2048
mvv 38,140,70
ds_read_b128 v[50:53], v9 offset:2048
mvv 42,70,144
ds_read_b128 v[54:57], v8 offset:6144
E 0,49152
mvv 94,148,110
ds_read_b128 v[58:61], v9 offset:6144
mvv 98,152,110
mvv 102,110,156
E 1,53248
mvv 22,148,66
mvv 26,152,66
mvv 30,66,156
E 2,57344
s_add_u32 s51, s51, 196608
mvv 34,148,74
mvv 38,152,74
mvv 42,74,156
s_waitcnt lgkmcnt(0)
s_waitcnt vmcnt(15)
s_barrier
Tv 184,4,16384
Tv 188,5,16384
Tv 192,6,16384
Tv 196,4,28672
Tv 200,5,28672
Tv 204,6,28672
ds_read_b128 v[62:65], v10 offset:2048
ds_read_b128 v[66:69], v11 offset:2048
ds_read_b128 v[70:73], v10 offset:6144
ds_read_b128 v[74:77], v11 offset:6144
s_waitcnt vmcnt(14)
ds_write_b32 v82, v122
v_sub_u32 v122, v122, v91
v_xor_b32 v123, v123, v115
v_cmp_lt_u64 vcc, s[62:63], v[122:123]
s_cbranch_vccnz Cq
Bq:
v_add_u32 v91, 1, v91
s_add_u32 s60, s49, 346112
s_add_u32 s56, s54, 692224
v_add_u32 v12, s55, v84
buffer_load_dwordx2 v[14:15], v92, s[36:39], s54 offen sc1
mvv 94,160,110
mvv 98,164,110
buffer_load_dwordx2 v[18:19], v92, s[36:39], s56 offen sc1
mvv 102,110,168
mvv 22,160,46
s_add_u32 m0, s50, 49152
s_nop 0
buffer_load_dwordx4 v88, s[36:39], s49 offen offset:0 sc1 lds
mvv 26,164,46
mvv 30,46,168
buffer_load_dwordx4 v88, s[36:39], s49 offen offset:1024 sc1 lds
mvv 34,160,54
mvv 38,164,54
buffer_load_dwordx4 v88, s[36:39], s49 offen offset:2048 sc1 lds
mvv 42,54,168
buffer_load_dwordx4 v88, s[36:39], s49 offen offset:3072 sc1 lds
mvv 94,172,110
mvv 98,176,110
s_add_u32 m0, s50, 57344
s_nop 0
buffer_load_dwordx4 v89, s[36:39], s49 offen offset:0 sc1 lds
mvv 102,110,180
mvv 22,172,50
buffer_load_dwordx4 v89, s[36:39], s49 offen offset:1024 sc1 lds
mvv 26,176,50
mvv 30,50,180
buffer_load_dwordx4 v89, s[36:39], s49 offen offset:2048 sc1 lds
mvv 34,172,58
mvv 38,176,58
buffer_load_dwordx4 v89, s[36:39], s49 offen offset:3072 sc1 lds
mvv 42,58,180
s_waitcnt lgkmcnt(0)
s_waitcnt vmcnt(16)
s_barrier
v_add_u32 v4, -49152, v4
v_add_u32 v5, -49152, v5
v_add_u32 v6, -49152, v6
s_add_u32 m0, s50, 32768
s_nop 0
buffer_load_dwordx4 v86, s[36:39], s49 offen offset:0 sc1 lds
Tv 208,4,40960
mvv 94,184,110
mvv 98,188,110
Tv 212,5,40960
mvv 102,110,192
Tv 216,6,40960
buffer_load_dwordx4 v86, s[36:39], s49 offen offset:1024 sc1 lds
mvv 22,184,62
Tv 220,4,53248
mvv 26,188,62
Tv 224,5,53248
buffer_load_dwordx4 v86, s[36:39], s49 offen offset:2048 sc1 lds
mvv 30,62,192
Tv 228,6,53248
mvv 34,184,70
ds_read_b128 v[46:49], v8 offset:3072
buffer_load_dwordx4 v86, s[36:39], s49 offen offset:3072 sc1 lds
mvv 38,188,70
ds_read_b128 v[50:53], v9 offset:3072
mvv 42,70,192
ds_read_b128 v[54:57], v8 offset:7168
s_add_u32 m0, s50, 40960
s_nop 0
buffer_load_dwordx4 v87, s[36:39], s49 offen offset:0 sc1 lds
mvv 94,196,110
ds_read_b128 v[58:61], v9 offset:7168
mvv 98,200,110
mvv 102,110,204
buffer_load_dwordx4 v87, s[36:39], s49 offen offset:1024 sc1 lds
mvv 22,196,66
mvv 26,200,66
buffer_load_dwordx4 v87, s[36:39], s49 offen offset:2048 sc1 lds
mvv 30,66,204
mvv 34,196,74
buffer_load_dwordx4 v87, s[36:39], s49 offen offset:3072 sc1 lds
mvv 38,200,74
mvv 42,74,204
s_waitcnt lgkmcnt(0)
s_waitcnt vmcnt(18)
s_barrier
s_waitcnt vmcnt(16)
s_add_u32 m0, s50, 0
s_nop 0
buffer_load_dwordx4 v86, s[36:39], s60 offen offset:0 sc1 lds
Tv 232,4,16384
mvv 94,208,110
v_permlane32_swap_b32 v14, v15
mvv 98,212,110
Tv 236,5,16384
v_add_f32 v16, v14, v15
mvv 102,110,216
Tv 240,6,16384
v_add_f32 v17, v14, v15
buffer_load_dwordx4 v86, s[36:39], s60 offen offset:1024 sc1 lds
mvv 22,208,46
Tv 244,4,28672
s_nop 1
v_permlane16_swap_b32 v16, v17
mvv 26,212,46
Tv 248,5,28672
v_add_f32 v14, v16, v17
buffer_load_dwordx4 v86, s[36:39], s60 offen offset:2048 sc1 lds
mvv 30,46,216
Tv 252,6,28672
v_add_f32 v15, v16, v17
mvv 34,208,54
ds_read_b128 v[62:65], v10 offset:3072
s_nop 1
v_permlane32_swap_b32 v14, v15
buffer_load_dwordx4 v86, s[36:39], s60 offen offset:3072 sc1 lds
mvv 38,212,54
v_mul_f32 v16, 0x3a800000, v14
ds_read_b128 v[66:69], v11 offset:3072
mvv 42,54,216
ds_read_b128 v[70:73], v10 offset:7168
v_mul_f32 v17, 0x3a800000, v15
s_add_u32 m0, s50, 8192
s_nop 0
buffer_load_dwordx4 v87, s[36:39], s60 offen offset:0 sc1 lds
mvv 94,220,110
ds_read_b128 v[74:77], v11 offset:7168
v_fma_f32 v17, -v16, v16, v17
mvv 98,224,110
v_max_f32 v17, 0, v17
mvv 102,110,228
buffer_load_dwordx4 v87, s[36:39], s60 offen offset:1024 sc1 lds
v_add_f32 v17, 0x3727c5ac, v17
mvv 22,220,50
v_rsq_f32 v14, v17
mvv 26,224,50
buffer_load_dwordx4 v87, s[36:39], s60 offen offset:2048 sc1 lds
mvv 30,50,228
s_nop 0
v_mul_f32_e64 v15, -v16, v14
mvv 34,220,58
ds_write_b64 v12, v[14:15]
buffer_load_dwordx4 v87, s[36:39], s60 offen offset:3072 sc1 lds
mvv 38,224,58
mvv 42,58,228
s_waitcnt lgkmcnt(0)
s_add_u32 m0, s50, 16384
s_nop 0
buffer_load_dwordx4 v88, s[36:39], s60 offen offset:0 sc1 lds
mvv 94,232,110
v_permlane32_swap_b32 v18, v19
mvv 98,236,110
v_add_f32 v20, v18, v19
mvv 102,110,240
buffer_load_dwordx4 v88, s[36:39], s60 offen offset:1024 sc1 lds
v_add_f32 v21, v18, v19
mvv 22,232,62
s_nop 1
v_permlane16_swap_b32 v20, v21
mvv 26,236,62
buffer_load_dwordx4 v88, s[36:39], s60 offen offset:2048 sc1 lds
mvv 30,62,240
v_add_f32 v18, v20, v21
mvv 34,232,70
v_add_f32 v19, v20, v21
buffer_load_dwordx4 v88, s[36:39], s60 offen offset:3072 sc1 lds
mvv 38,236,70
s_nop 1
v_permlane32_swap_b32 v18, v19
mvv 42,70,240
s_add_u32 m0, s50, 24576
s_nop 0
buffer_load_dwordx4 v89, s[36:39], s60 offen offset:0 sc1 lds
mvv 94,244,110
v_mul_f32 v20, 0x3a800000, v18
mvv 98,248,110
v_mul_f32 v21, 0x3a800000, v19
mvv 102,110,252
buffer_load_dwordx4 v89, s[36:39], s60 offen offset:1024 sc1 lds
v_fma_f32 v21, -v20, v20, v21
mvv 22,244,66
v_max_f32 v21, 0, v21
mvv 26,248,66
buffer_load_dwordx4 v89, s[36:39], s60 offen offset:2048 sc1 lds
mvv 30,66,252
v_add_f32 v21, 0x3727c5ac, v21
mvv 34,244,74
v_rsq_f32 v18, v21
buffer_load_dwordx4 v89, s[36:39], s60 offen offset:3072 sc1 lds
mvv 38,248,74
s_nop 0
v_mul_f32_e64 v19, -v20, v18
mvv 42,74,252
s_waitcnt vmcnt(16)
s_barrier
s_nop 7
v_mov_b32 v116, v102
s_and_b32 s60, s48, 3
s_cmp_eq_u32 s60, 1
s_cbranch_scc1 LvQB1
LvQB0:
v_xor_b32 v80, 0x10000, v80
ds_read_b64 v[118:119], v84 offset:0
ds_read_b128 v[102:105], v85 offset:0
ds_read_b128 v[106:109], v85 offset:16
s_add_u32 s49, s49, 346112
R 46,32768
R 50,33792
R 54,34816
R 58,35840
s_waitcnt lgkmcnt(2)
v_mul_f32 v120, v119, v94
v_fma_f32 v22, v22, v118, v120
v_mul_f32 v121, v119, v95
v_fma_f32 v23, v23, v118, v121
v_mul_f32 v120, v119, v96
v_fma_f32 v24, v24, v118, v120
v_mul_f32 v121, v119, v97
v_fma_f32 v25, v25, v118, v121
v_mul_f32 v120, v119, v98
v_fma_f32 v26, v26, v118, v120
v_mul_f32 v121, v119, v99
v_fma_f32 v27, v27, v118, v121
v_mul_f32 v120, v119, v100
v_fma_f32 v28, v28, v118, v120
v_mul_f32 v121, v119, v101
v_fma_f32 v29, v29, v118, v121
v_cvt_pk_bf16_f32 v110, v22, v23
v_cvt_pk_bf16_f32 v111, v24, v25
v_cvt_pk_bf16_f32 v112, v26, v27
v_cvt_pk_bf16_f32 v113, v28, v29
ds_write_b128 v83, v[110:113] offset:0
v_mul_f32 v103, v103, v116
v_fma_f32 v30, v30, v102, v103
v_mul_f32 v105, v105, v116
v_fma_f32 v31, v31, v104, v105
v_mul_f32 v107, v107, v116
v_fma_f32 v32, v32, v106, v107
v_mul_f32 v109, v109, v116
v_fma_f32 v33, v33, v108, v109
v_cvt_pk_bf16_f32 v78, v30, v31
v_cvt_pk_bf16_f32 v79, v32, v33
ds_write_b64 v83, v[78:79] offset:1024
s_xor_b32 s50, s50, 0x10000
s_add_u32 s49, s49, 346112
s_waitcnt lgkmcnt(0)
s_barrier
ds_write_b64 v84, v[18:19] offset:0
ds_read_b64 v[118:119], v84 offset:128
ds_read_b128 v[102:105], v85 offset:128
ds_read_b128 v[106:109], v85 offset:144
v_mfma_f32_16x16x32_bf16 v[22:25], a[0:3], v[46:49], 0
R 62,36864
v_mfma_f32_16x16x32_bf16 v[26:29], a[4:7], v[46:49], 0
s_waitcnt lgkmcnt(1)
v_mul_f32 v120, v119, v94
v_mfma_f32_16x16x32_bf16 v[30:33], v[46:49], a[8:11], 0
v_fma_f32 v34, v34, v118, v120
v_mul_f32 v121, v119, v95
mav 22,12,50
R 66,37888
v_fma_f32 v35, v35, v118, v121
mav 26,16,50
v_mul_f32 v120, v119, v96
v_fma_f32 v36, v36, v118, v120
mva 30,50,20
v_mul_f32 v121, v119, v97
v_fma_f32 v37, v37, v118, v121
mav 22,24,54
R 70,38912
v_mul_f32 v120, v119, v98
mav 26,28,54
v_fma_f32 v38, v38, v118, v120
v_mul_f32 v121, v119, v99
mva 30,54,32
v_fma_f32 v39, v39, v118, v121
v_mul_f32 v120, v119, v100
mav 22,36,58
R 74,39936
v_fma_f32 v40, v40, v118, v120
mav 26,40,58
v_mul_f32 v121, v119, v101
v_fma_f32 v41, v41, v118, v121
mva 30,58,44
v_cvt_pk_bf16_f32 v110, v34, v35
s_waitcnt lgkmcnt(2)
mav 22,48,62
R 46,40960
v_cvt_pk_bf16_f32 v111, v36, v37
mav 26,52,62
v_cvt_pk_bf16_f32 v112, v38, v39
v_cvt_pk_bf16_f32 v113, v40, v41
mva 30,62,56
ds_write_b128 v83, v[110:113] offset:4096
mav 22,60,66
R 50,41984
v_mul_f32 v103, v103, v116
mav 26,64,66
v_fma_f32 v42, v42, v102, v103
v_mul_f32 v105, v105, v116
mva 30,66,68
v_fma_f32 v43, v43, v104, v105
s_waitcnt lgkmcnt(3)
mav 22,72,70
R 54,43008
v_mul_f32 v107, v107, v116
mav 26,76,70
v_fma_f32 v44, v44, v106, v107
v_mul_f32 v109, v109, v116
mva 30,70,80
v_fma_f32 v45, v45, v108, v109
v_cvt_pk_bf16_f32 v78, v42, v43
mav 22,84,74
R 58,44032
v_cvt_pk_bf16_f32 v79, v44, v45
mav 26,88,74
ds_write_b64 v83, v[78:79] offset:5120
mva 30,74,92
s_waitcnt lgkmcnt(3)
mav 22,96,46
R 62,45056
s_mov_b32 m0, 142336
mav 26,100,46
s_waitcnt vmcnt(0) lgkmcnt(1)
s_barrier
mva 30,46,104
buffer_load_dword v90, s[36:39], 0 offen sc1 lds
mav 22,108,50
R 66,46080
s_add_u32 m0, s50, 32768
mav 26,112,50
mva 30,50,116
D 86,0
mav 22,120,54
R 70,47104
mav 26,124,54
D 86,1024
mva 30,54,128
mav 22,132,58
R 74,48128
D 86,2048
mav 26,136,58
mva 30,58,140
D 86,3072
s_waitcnt lgkmcnt(2)
mav 22,144,62
R 46,49152
s_add_u32 m0, s50, 40960
mav 26,148,62
D 87,0
mva 30,62,152
mav 22,156,66
R 50,50176
D 87,1024
mav 26,160,66
mva 30,66,164
D 87,2048
s_waitcnt lgkmcnt(2)
mav 22,168,70
R 54,51200
mav 26,172,70
D 87,3072
s_add_u32 m0, s50, 49152
mva 30,70,176
mav 22,180,74
R 58,52224
D 88,0
mav 26,184,74
mva 30,74,188
D 88,1024
s_waitcnt lgkmcnt(2)
mav 22,192,46
R 62,53248
mav 26,196,46
D 88,2048
mva 30,46,200
mav 22,204,50
R 66,54272
D 88,3072
mav 26,208,50
s_add_u32 m0, s50, 57344
mva 30,50,212
D 89,0
s_waitcnt lgkmcnt(2)
mav 22,216,54
R 70,55296
mav 26,220,54
D 89,1024
mva 30,54,224
mav 22,228,58
R 74,56320
D 89,2048
mav 26,232,58
mva 30,58,236
D 89,3072
s_waitcnt lgkmcnt(2)
mav 22,240,62
R 46,57344
s_add_u32 s49, s49, 346112
mav 26,244,62
s_add_u32 m0, s50, 0
mva 30,62,248
D 86,0
mav 22,252,66
R 50,58368
mvv 26,128,66
D 86,1024
mvv 30,66,132
s_waitcnt lgkmcnt(2)
mvv 22,136,70
R 54,59392
D 86,2048
mvv 26,140,70
mvv 30,70,144
D 86,3072
s_add_u32 m0, s50, 8192
mvv 22,148,74
R 58,60416
mvv 26,152,74
D 87,0
mvv 30,74,156
s_waitcnt lgkmcnt(2)
mvv 22,160,46
R 62,61440
D 87,1024
mvv 26,164,46
mvv 30,46,168
D 87,2048
mvv 22,172,50
R 66,62464
mvv 26,176,50
D 87,3072
s_add_u32 m0, s50, 16384
mvv 30,50,180
s_waitcnt lgkmcnt(2)
mvv 22,184,54
R 70,63488
D 88,0
mvv 26,188,54
mvv 30,54,192
D 88,1024
mvv 22,196,58
R 74,64512
mvv 26,200,58
D 88,2048
mvv 30,58,204
s_waitcnt lgkmcnt(2)
mvv 22,208,62
R 46,0
D 88,3072
mvv 26,212,62
ds_read_b32 v122, v82 offset:0
s_add_u32 m0, s50, 24576
mvv 30,62,216
ds_read_b64 v[118:119], v84 offset:0
ds_read_b128 v[102:105], v85 offset:0
mvv 22,220,66
R 50,1024
ds_read_b128 v[106:109], v85 offset:16
mvv 26,224,66
D 89,0
mvv 30,66,228
s_waitcnt lgkmcnt(6)
mvv 22,232,70
R 54,2048
D 89,1024
mvv 26,236,70
mvv 30,70,240
D 89,2048
mvv 22,244,74
R 58,3072
mvv 26,248,74
s_waitcnt lgkmcnt(2)
v_cmp_gt_i32 vcc, v91, v122
mvv 30,74,252
s_cbranch_vccnz CQB0_1
BQB0_1:
D 89,3072
v_mfma_f32_16x16x32_bf16 v[34:37], a[0:3], v[46:49], 0
R 62,4096
v_mul_f32 v120, v119, v94
v_mfma_f32_16x16x32_bf16 v[38:41], a[4:7], v[46:49], 0
v_fma_f32 v22, v22, v118, v120
v_mul_f32 v121, v119, v95
v_mfma_f32_16x16x32_bf16 v[42:45], v[46:49], a[8:11], 0
v_fma_f32 v23, v23, v118, v121
v_mul_f32 v120, v119, v96
mav 34,12,50
R 66,5120
v_fma_f32 v24, v24, v118, v120
mav 38,16,50
v_mul_f32 v121, v119, v97
v_fma_f32 v25, v25, v118, v121
mva 42,50,20
v_mul_f32 v120, v119, v98
s_waitcnt lgkmcnt(2)
mav 34,24,54
R 70,6144
v_fma_f32 v26, v26, v118, v120
mav 38,28,54
v_mul_f32 v121, v119, v99
v_fma_f32 v27, v27, v118, v121
mva 42,54,32
v_mul_f32 v120, v119, v100
v_fma_f32 v28, v28, v118, v120
mav 34,36,58
R 74,7168
v_mul_f32 v121, v119, v101
mav 38,40,58
v_fma_f32 v29, v29, v118, v121
v_cvt_pk_bf16_f32 v110, v22, v23
mva 42,58,44
v_cvt_pk_bf16_f32 v111, v24, v25
s_waitcnt lgkmcnt(2)
mav 34,48,62
R 46,8192
v_cvt_pk_bf16_f32 v112, v26, v27
mav 38,52,62
v_cvt_pk_bf16_f32 v113, v28, v29
v_mul_f32 v103, v103, v116
mva 42,62,56
ds_write_b128 v83, v[110:113] offset:0
mav 34,60,66
R 50,9216
v_fma_f32 v30, v30, v102, v103
mav 38,64,66
v_mul_f32 v105, v105, v116
v_fma_f32 v31, v31, v104, v105
mva 42,66,68
v_mul_f32 v107, v107, v116
s_waitcnt lgkmcnt(3)
mav 34,72,70
R 54,10240
v_fma_f32 v32, v32, v106, v107
mav 38,76,70
v_mul_f32 v109, v109, v116
v_fma_f32 v33, v33, v108, v109
mva 42,70,80
s_xor_b32 s50, s50, 0x10000
v_cvt_pk_bf16_f32 v78, v30, v31
mav 34,84,74
R 58,11264
v_cvt_pk_bf16_f32 v79, v32, v33
mav 38,88,74
ds_write_b64 v83, v[78:79] offset:1024
mva 42,74,92
s_waitcnt lgkmcnt(3)
mav 34,96,46
R 62,12288
s_mov_b32 m0, 142336
mav 38,100,46
s_waitcnt vmcnt(16) lgkmcnt(1)
s_barrier
mva 42,46,104
s_add_u32 s49, s49, 346112
buffer_load_dword v90, s[36:39], 0 offen sc1 lds
mav 34,108,50
R 66,13312
s_add_u32 m0, s50, 32768
mav 38,112,50
mva 42,50,116
D 86,0
mav 34,120,54
R 70,14336
mav 38,124,54
D 86,1024
mva 42,54,128
mav 34,132,58
R 74,15360
mav 38,136,58
D 86,2048
mva 42,58,140
s_waitcnt lgkmcnt(2)
mav 34,144,62
R 46,16384
D 86,3072
mav 38,148,62
s_add_u32 m0, s50, 40960
mva 42,62,152
mav 34,156,66
R 50,17408
D 87,0
mav 38,160,66
v_add_u32 v91, 1, v91
mva 42,66,164
D 87,1024
s_waitcnt lgkmcnt(2)
mav 34,168,70
R 54,18432
mav 38,172,70
mva 42,70,176
D 87,2048
mav 34,180,74
R 58,19456
mav 38,184,74
D 87,3072
mva 42,74,188
s_add_u32 m0, s50, 49152
s_waitcnt lgkmcnt(2)
mav 34,192,46
R 62,20480
mav 38,196,46
D 88,0
mva 42,46,200
mav 34,204,50
R 66,21504
D 88,1024
mav 38,208,50
mva 42,50,212
s_waitcnt lgkmcnt(2)
mav 34,216,54
R 70,22528
D 88,2048
mav 38,220,54
mva 42,54,224
D 88,3072
mav 34,228,58
R 74,23552
s_add_u32 m0, s50, 57344
mav 38,232,58
mva 42,58,236
D 89,0
s_waitcnt lgkmcnt(2)
mav 34,240,62
R 46,24576
mav 38,244,62
D 89,1024
mva 42,62,248
mav 34,252,66
R 50,25600
mvv 38,128,66
D 89,2048
mvv 42,66,132
s_waitcnt lgkmcnt(2)
mvv 34,136,70
R 54,26624
D 89,3072
mvv 38,140,70
mvv 42,70,144
mvv 34,148,74
R 58,27648
mvv 38,152,74
mvv 42,74,156
s_waitcnt lgkmcnt(2)
mvv 34,160,46
R 62,28672
mvv 38,164,46
mvv 42,46,168
mvv 34,172,50
R 66,29696
mvv 38,176,50
mvv 42,50,180
s_waitcnt lgkmcnt(2)
mvv 34,184,54
R 70,30720
mvv 38,188,54
mvv 42,54,192
mvv 34,196,58
R 74,31744
v_xor_b32 v80, 0x10000, v80
mvv 38,200,58
mvv 42,58,204
s_waitcnt lgkmcnt(2)
mvv 34,208,62
R 46,32768
mvv 38,212,62
ds_read_b32 v122, v82 offset:0
ds_read_b64 v[118:119], v84 offset:128
mvv 42,62,216
ds_read_b128 v[102:105], v85 offset:128
ds_read_b128 v[106:109], v85 offset:144
mvv 34,220,66
R 50,33792
mvv 38,224,66
mvv 42,66,228
s_waitcnt lgkmcnt(6)
mvv 34,232,70
R 54,34816
mvv 38,236,70
mvv 42,70,240
mvv 34,244,74
R 58,35840
mvv 38,248,74
s_waitcnt lgkmcnt(2)
v_cmp_gt_i32 vcc, v91, v122
mvv 42,74,252
s_mov_b32 s60, -28
.p2align 6
LQB0_2:
v_mfma_f32_16x16x32_bf16 v[22:25], a[0:3], v[46:49], 0
s_cbranch_vccnz CQB0_3
BQB0_3:
R 62,36864
v_mfma_f32_16x16x32_bf16 v[26:29], a[4:7], v[46:49], 0
v_mul_f32 v120, v119, v94
v_fma_f32 v34, v34, v118, v120
v_mfma_f32_16x16x32_bf16 v[30:33], v[46:49], a[8:11], 0
v_mul_f32 v121, v119, v95
v_fma_f32 v35, v35, v118, v121
mav 22,12,50
R 66,37888
v_mul_f32 v120, v119, v96
mav 26,16,50
v_fma_f32 v36, v36, v118, v120
v_mul_f32 v121, v119, v97
mva 30,50,20
v_fma_f32 v37, v37, v118, v121
s_waitcnt lgkmcnt(2)
mav 22,24,54
R 70,38912
v_mul_f32 v120, v119, v98
mav 26,28,54
v_fma_f32 v38, v38, v118, v120
v_mul_f32 v121, v119, v99
mva 30,54,32
v_fma_f32 v39, v39, v118, v121
v_mul_f32 v120, v119, v100
mav 22,36,58
R 74,39936
v_fma_f32 v40, v40, v118, v120
mav 26,40,58
v_mul_f32 v121, v119, v101
v_fma_f32 v41, v41, v118, v121
mva 30,58,44
v_cvt_pk_bf16_f32 v110, v34, v35
s_waitcnt lgkmcnt(2)
mav 22,48,62
R 46,40960
v_cvt_pk_bf16_f32 v111, v36, v37
mav 26,52,62
v_cvt_pk_bf16_f32 v112, v38, v39
v_cvt_pk_bf16_f32 v113, v40, v41
mva 30,62,56
ds_write_b128 v83, v[110:113] offset:4096
mav 22,60,66
R 50,41984
v_mul_f32 v103, v103, v116
mav 26,64,66
v_fma_f32 v42, v42, v102, v103
v_mul_f32 v105, v105, v116
mva 30,66,68
v_fma_f32 v43, v43, v104, v105
s_waitcnt lgkmcnt(3)
mav 22,72,70
R 54,43008
v_mul_f32 v107, v107, v116
mav 26,76,70
v_fma_f32 v44, v44, v106, v107
v_mul_f32 v109, v109, v116
mva 30,70,80
v_fma_f32 v45, v45, v108, v109
v_cvt_pk_bf16_f32 v78, v42, v43
mav 22,84,74
R 58,44032
v_cvt_pk_bf16_f32 v79, v44, v45
mav 26,88,74
ds_write_b64 v83, v[78:79] offset:5120
mva 30,74,92
s_waitcnt lgkmcnt(3)
mav 22,96,46
R 62,45056
s_mov_b32 m0, 142336
mav 26,100,46
s_waitcnt vmcnt(17) lgkmcnt(1)
s_barrier
mva 30,46,104
s_add_u32 s49, s49, 346112
buffer_load_dword v90, s[36:39], 0 offen sc1 lds
mav 22,108,50
R 66,46080
s_add_u32 m0, s50, 0
mav 26,112,50
mva 30,50,116
D 86,0
mav 22,120,54
R 70,47104
mav 26,124,54
D 86,1024
mva 30,54,128
mav 22,132,58
R 74,48128
mav 26,136,58
D 86,2048
mva 30,58,140
s_waitcnt lgkmcnt(2)
mav 22,144,62
R 46,49152
D 86,3072
mav 26,148,62
s_add_u32 m0, s50, 8192
mva 30,62,152
mav 22,156,66
R 50,50176
D 87,0
mav 26,160,66
v_add_u32 v91, 1, v91
mva 30,66,164
D 87,1024
s_waitcnt lgkmcnt(2)
mav 22,168,70
R 54,51200
mav 26,172,70
mva 30,70,176
D 87,2048
mav 22,180,74
R 58,52224
mav 26,184,74
D 87,3072
mva 30,74,188
s_add_u32 m0, s50, 16384
s_waitcnt lgkmcnt(2)
mav 22,192,46
R 62,53248
mav 26,196,46
D 88,0
mva 30,46,200
mav 22,204,50
R 66,54272
D 88,1024
mav 26,208,50
mva 30,50,212
s_waitcnt lgkmcnt(2)
mav 22,216,54
R 70,55296
D 88,2048
mav 26,220,54
mva 30,54,224
D 88,3072
mav 22,228,58
R 74,56320
s_add_u32 m0, s50, 24576
mav 26,232,58
mva 30,58,236
D 89,0
s_waitcnt lgkmcnt(2)
mav 22,240,62
R 46,57344
mav 26,244,62
D 89,1024
mva 30,62,248
mav 22,252,66
R 50,58368
mvv 26,128,66
D 89,2048
mvv 30,66,132
s_waitcnt lgkmcnt(2)
mvv 22,136,70
R 54,59392
D 89,3072
mvv 26,140,70
mvv 30,70,144
mvv 22,148,74
R 58,60416
mvv 26,152,74
mvv 30,74,156
s_waitcnt lgkmcnt(2)
mvv 22,160,46
R 62,61440
mvv 26,164,46
mvv 30,46,168
mvv 22,172,50
R 66,62464
mvv 26,176,50
mvv 30,50,180
s_waitcnt lgkmcnt(2)
mvv 22,184,54
R 70,63488
mvv 26,188,54
mvv 30,54,192
mvv 22,196,58
R 74,64512
mvv 26,200,58
mvv 30,58,204
s_waitcnt lgkmcnt(2)
mvv 22,208,62
R 46,0
mvv 26,212,62
ds_read_b32 v122, v82 offset:0
ds_read_b64 v[118:119], v84 offset:0
mvv 30,62,216
ds_read_b128 v[102:105], v85 offset:0
ds_read_b128 v[106:109], v85 offset:16
mvv 22,220,66
R 50,1024
mvv 26,224,66
mvv 30,66,228
s_waitcnt lgkmcnt(6)
mvv 22,232,70
R 54,2048
mvv 26,236,70
mvv 30,70,240
mvv 22,244,74
R 58,3072
mvv 26,248,74
s_waitcnt lgkmcnt(2)
v_cmp_gt_i32 vcc, v91, v122
mvv 30,74,252
s_cbranch_vccnz CQB0_4
BQB0_4:
v_mfma_f32_16x16x32_bf16 v[34:37], a[0:3], v[46:49], 0
R 62,4096
v_mul_f32 v120, v119, v94
v_mfma_f32_16x16x32_bf16 v[38:41], a[4:7], v[46:49], 0
v_fma_f32 v22, v22, v118, v120
v_mul_f32 v121, v119, v95
v_mfma_f32_16x16x32_bf16 v[42:45], v[46:49], a[8:11], 0
v_fma_f32 v23, v23, v118, v121
v_mul_f32 v120, v119, v96
mav 34,12,50
R 66,5120
v_fma_f32 v24, v24, v118, v120
mav 38,16,50
v_mul_f32 v121, v119, v97
v_fma_f32 v25, v25, v118, v121
mva 42,50,20
v_mul_f32 v120, v119, v98
s_waitcnt lgkmcnt(2)
mav 34,24,54
R 70,6144
v_fma_f32 v26, v26, v118, v120
mav 38,28,54
v_mul_f32 v121, v119, v99
v_fma_f32 v27, v27, v118, v121
mva 42,54,32
v_mul_f32 v120, v119, v100
v_fma_f32 v28, v28, v118, v120
mav 34,36,58
R 74,7168
v_mul_f32 v121, v119, v101
mav 38,40,58
v_fma_f32 v29, v29, v118, v121
v_cvt_pk_bf16_f32 v110, v22, v23
mva 42,58,44
v_cvt_pk_bf16_f32 v111, v24, v25
s_waitcnt lgkmcnt(2)
mav 34,48,62
R 46,8192
v_cvt_pk_bf16_f32 v112, v26, v27
mav 38,52,62
v_cvt_pk_bf16_f32 v113, v28, v29
v_mul_f32 v103, v103, v116
mva 42,62,56
ds_write_b128 v83, v[110:113] offset:0
mav 34,60,66
R 50,9216
v_fma_f32 v30, v30, v102, v103
mav 38,64,66
v_mul_f32 v105, v105, v116
v_fma_f32 v31, v31, v104, v105
mva 42,66,68
v_mul_f32 v107, v107, v116
s_waitcnt lgkmcnt(3)
mav 34,72,70
R 54,10240
v_fma_f32 v32, v32, v106, v107
mav 38,76,70
v_mul_f32 v109, v109, v116
v_fma_f32 v33, v33, v108, v109
mva 42,70,80
s_xor_b32 s50, s50, 0x10000
v_cvt_pk_bf16_f32 v78, v30, v31
mav 34,84,74
R 58,11264
v_cvt_pk_bf16_f32 v79, v32, v33
mav 38,88,74
ds_write_b64 v83, v[78:79] offset:1024
mva 42,74,92
s_waitcnt lgkmcnt(3)
mav 34,96,46
R 62,12288
s_mov_b32 m0, 142336
mav 38,100,46
s_waitcnt vmcnt(17) lgkmcnt(1)
s_barrier
mva 42,46,104
s_add_u32 s49, s49, 346112
buffer_load_dword v90, s[36:39], 0 offen sc1 lds
mav 34,108,50
R 66,13312
s_add_u32 m0, s50, 32768
mav 38,112,50
mva 42,50,116
D 86,0
mav 34,120,54
R 70,14336
mav 38,124,54
D 86,1024
mva 42,54,128
mav 34,132,58
R 74,15360
mav 38,136,58
D 86,2048
mva 42,58,140
s_waitcnt lgkmcnt(2)
mav 34,144,62
R 46,16384
D 86,3072
mav 38,148,62
s_add_u32 m0, s50, 40960
mva 42,62,152
mav 34,156,66
R 50,17408
D 87,0
mav 38,160,66
v_add_u32 v91, 1, v91
mva 42,66,164
D 87,1024
s_waitcnt lgkmcnt(2)
mav 34,168,70
R 54,18432
mav 38,172,70
mva 42,70,176
D 87,2048
mav 34,180,74
R 58,19456
mav 38,184,74
D 87,3072
mva 42,74,188
s_add_u32 m0, s50, 49152
s_waitcnt lgkmcnt(2)
mav 34,192,46
R 62,20480
mav 38,196,46
D 88,0
mva 42,46,200
mav 34,204,50
R 66,21504
D 88,1024
mav 38,208,50
mva 42,50,212
s_waitcnt lgkmcnt(2)
mav 34,216,54
R 70,22528
D 88,2048
mav 38,220,54
mva 42,54,224
D 88,3072
mav 34,228,58
R 74,23552
s_add_u32 m0, s50, 57344
mav 38,232,58
mva 42,58,236
D 89,0
s_waitcnt lgkmcnt(2)
mav 34,240,62
R 46,24576
mav 38,244,62
D 89,1024
mva 42,62,248
mav 34,252,66
R 50,25600
mvv 38,128,66
D 89,2048
mvv 42,66,132
s_waitcnt lgkmcnt(2)
mvv 34,136,70
R 54,26624
D 89,3072
mvv 38,140,70
mvv 42,70,144
mvv 34,148,74
R 58,27648
mvv 38,152,74
mvv 42,74,156
s_waitcnt lgkmcnt(2)
mvv 34,160,46
R 62,28672
mvv 38,164,46
mvv 42,46,168
mvv 34,172,50
R 66,29696
mvv 38,176,50
mvv 42,50,180
s_waitcnt lgkmcnt(2)
mvv 34,184,54
R 70,30720
mvv 38,188,54
mvv 42,54,192
mvv 34,196,58
R 74,31744
v_xor_b32 v80, 0x10000, v80
mvv 38,200,58
mvv 42,58,204
s_waitcnt lgkmcnt(2)
mvv 34,208,62
R 46,32768
mvv 38,212,62
ds_read_b32 v122, v82 offset:0
ds_read_b64 v[118:119], v84 offset:128
mvv 42,62,216
ds_read_b128 v[102:105], v85 offset:128
ds_read_b128 v[106:109], v85 offset:144
mvv 34,220,66
R 50,33792
mvv 38,224,66
mvv 42,66,228
s_waitcnt lgkmcnt(6)
mvv 34,232,70
R 54,34816
mvv 38,236,70
mvv 42,70,240
mvv 34,244,74
R 58,35840
mvv 38,248,74
s_waitcnt lgkmcnt(2)
v_cmp_gt_i32 vcc, v91, v122
mvv 42,74,252
s_add_u32 s60, s60, 1
s_cbranch_scc0 LQB0_2
v_mfma_f32_16x16x32_bf16 v[22:25], a[0:3], v[46:49], 0
s_cbranch_vccnz CQB0_5
BQB0_5:
R 62,36864
v_mfma_f32_16x16x32_bf16 v[26:29], a[4:7], v[46:49], 0
v_mul_f32 v120, v119, v94
v_fma_f32 v34, v34, v118, v120
v_mfma_f32_16x16x32_bf16 v[30:33], v[46:49], a[8:11], 0
v_mul_f32 v121, v119, v95
v_fma_f32 v35, v35, v118, v121
mav 22,12,50
R 66,37888
v_mul_f32 v120, v119, v96
mav 26,16,50
v_fma_f32 v36, v36, v118, v120
v_mul_f32 v121, v119, v97
mva 30,50,20
v_fma_f32 v37, v37, v118, v121
s_waitcnt lgkmcnt(2)
mav 22,24,54
R 70,38912
v_mul_f32 v120, v119, v98
mav 26,28,54
v_fma_f32 v38, v38, v118, v120
v_mul_f32 v121, v119, v99
mva 30,54,32
v_fma_f32 v39, v39, v118, v121
v_mul_f32 v120, v119, v100
mav 22,36,58
R 74,39936
v_fma_f32 v40, v40, v118, v120
mav 26,40,58
v_mul_f32 v121, v119, v101
v_fma_f32 v41, v41, v118, v121
mva 30,58,44
v_cvt_pk_bf16_f32 v110, v34, v35
s_waitcnt lgkmcnt(2)
mav 22,48,62
R 46,40960
v_cvt_pk_bf16_f32 v111, v36, v37
mav 26,52,62
v_cvt_pk_bf16_f32 v112, v38, v39
v_cvt_pk_bf16_f32 v113, v40, v41
mva 30,62,56
ds_write_b128 v83, v[110:113] offset:4096
mav 22,60,66
R 50,41984
v_mul_f32 v103, v103, v116
mav 26,64,66
v_fma_f32 v42, v42, v102, v103
v_mul_f32 v105, v105, v116
mva 30,66,68
v_fma_f32 v43, v43, v104, v105
s_waitcnt lgkmcnt(3)
mav 22,72,70
R 54,43008
v_mul_f32 v107, v107, v116
mav 26,76,70
v_fma_f32 v44, v44, v106, v107
v_mul_f32 v109, v109, v116
mva 30,70,80
v_fma_f32 v45, v45, v108, v109
v_cvt_pk_bf16_f32 v78, v42, v43
mav 22,84,74
R 58,44032
v_cvt_pk_bf16_f32 v79, v44, v45
mav 26,88,74
ds_write_b64 v83, v[78:79] offset:5120
mva 30,74,92
s_waitcnt lgkmcnt(3)
mav 22,96,46
R 62,45056
s_mov_b32 m0, 142336
mav 26,100,46
s_waitcnt vmcnt(17) lgkmcnt(1)
s_barrier
mva 30,46,104
s_add_u32 s49, s49, 346112
buffer_load_dword v90, s[36:39], 0 offen sc1 lds
mav 22,108,50
R 66,46080
s_add_u32 m0, s50, 0
mav 26,112,50
mva 30,50,116
D 86,0
mav 22,120,54
R 70,47104
mav 26,124,54
D 86,1024
mva 30,54,128
mav 22,132,58
R 74,48128
mav 26,136,58
D 86,2048
mva 30,58,140
s_waitcnt lgkmcnt(2)
mav 22,144,62
R 46,49152
D 86,3072
mav 26,148,62
s_add_u32 m0, s50, 8192
mva 30,62,152
mav 22,156,66
R 50,50176
D 87,0
mav 26,160,66
v_add_u32 v91, 1, v91
mva 30,66,164
D 87,1024
s_waitcnt lgkmcnt(2)
mav 22,168,70
R 54,51200
mav 26,172,70
mva 30,70,176
D 87,2048
mav 22,180,74
R 58,52224
mav 26,184,74
D 87,3072
mva 30,74,188
s_add_u32 m0, s50, 16384
s_waitcnt lgkmcnt(2)
mav 22,192,46
R 62,53248
mav 26,196,46
D 88,0
mva 30,46,200
mav 22,204,50
R 66,54272
D 88,1024
mav 26,208,50
mva 30,50,212
)"
R"(
s_waitcnt lgkmcnt(2)
mav 22,216,54
R 70,55296
D 88,2048
mav 26,220,54
mva 30,54,224
D 88,3072
mav 22,228,58
R 74,56320
s_add_u32 m0, s50, 24576
mav 26,232,58
mva 30,58,236
D 89,0
s_waitcnt lgkmcnt(2)
mav 22,240,62
R 46,57344
mav 26,244,62
D 89,1024
mva 30,62,248
mav 22,252,66
R 50,58368
mvv 26,128,66
D 89,2048
mvv 30,66,132
s_waitcnt lgkmcnt(2)
mvv 22,136,70
R 54,59392
D 89,3072
mvv 26,140,70
mvv 30,70,144
mvv 22,148,74
R 58,60416
mvv 26,152,74
mvv 30,74,156
s_waitcnt lgkmcnt(2)
mvv 22,160,46
R 62,61440
mvv 26,164,46
mvv 30,46,168
mvv 22,172,50
R 66,62464
mvv 26,176,50
mvv 30,50,180
s_waitcnt lgkmcnt(2)
mvv 22,184,54
R 70,63488
mvv 26,188,54
mvv 30,54,192
mvv 22,196,58
R 74,64512
mvv 26,200,58
mvv 30,58,204
s_waitcnt lgkmcnt(2)
mvv 22,208,62
R 46,0
mvv 26,212,62
ds_read_b32 v122, v82 offset:0
ds_read_b64 v[118:119], v84 offset:0
mvv 30,62,216
ds_read_b128 v[102:105], v85 offset:0
ds_read_b128 v[106:109], v85 offset:16
mvv 22,220,66
R 50,1024
mvv 26,224,66
mvv 30,66,228
s_waitcnt lgkmcnt(6)
mvv 22,232,70
R 54,2048
mvv 26,236,70
mvv 30,70,240
mvv 22,244,74
R 58,3072
mvv 26,248,74
s_waitcnt lgkmcnt(2)
v_cmp_gt_i32 vcc, v91, v122
mvv 30,74,252
s_cbranch_vccnz CQB0_6
BQB0_6:
v_mfma_f32_16x16x32_bf16 v[34:37], a[0:3], v[46:49], 0
R 62,4096
v_mul_f32 v120, v119, v94
v_mfma_f32_16x16x32_bf16 v[38:41], a[4:7], v[46:49], 0
v_fma_f32 v22, v22, v118, v120
v_mul_f32 v121, v119, v95
v_mfma_f32_16x16x32_bf16 v[42:45], v[46:49], a[8:11], 0
v_fma_f32 v23, v23, v118, v121
v_mul_f32 v120, v119, v96
mav 34,12,50
R 66,5120
v_fma_f32 v24, v24, v118, v120
mav 38,16,50
v_mul_f32 v121, v119, v97
v_fma_f32 v25, v25, v118, v121
mva 42,50,20
v_mul_f32 v120, v119, v98
s_waitcnt lgkmcnt(2)
mav 34,24,54
R 70,6144
v_fma_f32 v26, v26, v118, v120
mav 38,28,54
v_mul_f32 v121, v119, v99
v_fma_f32 v27, v27, v118, v121
mva 42,54,32
v_mul_f32 v120, v119, v100
v_fma_f32 v28, v28, v118, v120
mav 34,36,58
R 74,7168
v_mul_f32 v121, v119, v101
mav 38,40,58
v_fma_f32 v29, v29, v118, v121
v_cvt_pk_bf16_f32 v110, v22, v23
mva 42,58,44
v_cvt_pk_bf16_f32 v111, v24, v25
s_waitcnt lgkmcnt(2)
mav 34,48,62
R 46,8192
v_cvt_pk_bf16_f32 v112, v26, v27
mav 38,52,62
v_cvt_pk_bf16_f32 v113, v28, v29
v_mul_f32 v103, v103, v116
mva 42,62,56
ds_write_b128 v83, v[110:113] offset:0
mav 34,60,66
R 50,9216
v_fma_f32 v30, v30, v102, v103
mav 38,64,66
v_mul_f32 v105, v105, v116
v_fma_f32 v31, v31, v104, v105
mva 42,66,68
v_mul_f32 v107, v107, v116
s_waitcnt lgkmcnt(3)
mav 34,72,70
R 54,10240
v_fma_f32 v32, v32, v106, v107
mav 38,76,70
v_mul_f32 v109, v109, v116
v_fma_f32 v33, v33, v108, v109
mva 42,70,80
v_cvt_pk_bf16_f32 v78, v30, v31
v_cvt_pk_bf16_f32 v79, v32, v33
mav 34,84,74
R 58,11264
mav 38,88,74
ds_write_b64 v83, v[78:79] offset:1024
mva 42,74,92
s_waitcnt lgkmcnt(3)
mav 34,96,46
R 62,12288
s_add_u32 s49, s49, 346112
mav 38,100,46
s_waitcnt vmcnt(17) lgkmcnt(1)
s_barrier
mva 42,46,104
mav 34,108,50
R 66,13312
mav 38,112,50
mva 42,50,116
mav 34,120,54
R 70,14336
mav 38,124,54
mva 42,54,128
mav 34,132,58
R 74,15360
mav 38,136,58
mva 42,58,140
s_waitcnt lgkmcnt(2)
mav 34,144,62
R 46,16384
mav 38,148,62
mva 42,62,152
mav 34,156,66
R 50,17408
mav 38,160,66
mva 42,66,164
s_waitcnt lgkmcnt(2)
mav 34,168,70
R 54,18432
mav 38,172,70
mva 42,70,176
mav 34,180,74
R 58,19456
mav 38,184,74
mva 42,74,188
s_waitcnt lgkmcnt(2)
mav 34,192,46
R 62,20480
mav 38,196,46
mva 42,46,200
mav 34,204,50
R 66,21504
mav 38,208,50
mva 42,50,212
s_waitcnt lgkmcnt(2)
mav 34,216,54
R 70,22528
mav 38,220,54
mva 42,54,224
mav 34,228,58
R 74,23552
mav 38,232,58
mva 42,58,236
s_waitcnt lgkmcnt(2)
mav 34,240,62
R 46,24576
mav 38,244,62
mva 42,62,248
mav 34,252,66
R 50,25600
mvv 38,128,66
mvv 42,66,132
s_waitcnt lgkmcnt(2)
mvv 34,136,70
R 54,26624
mvv 38,140,70
mvv 42,70,144
mvv 34,148,74
R 58,27648
mvv 38,152,74
mvv 42,74,156
s_waitcnt lgkmcnt(2)
mvv 34,160,46
R 62,28672
mvv 38,164,46
mvv 42,46,168
mvv 34,172,50
R 66,29696
mvv 38,176,50
mvv 42,50,180
s_waitcnt lgkmcnt(2)
mvv 34,184,54
R 70,30720
mvv 38,188,54
mvv 42,54,192
mvv 34,196,58
R 74,31744
v_xor_b32 v80, 0x10000, v80
mvv 38,200,58
mvv 42,58,204
s_waitcnt lgkmcnt(2)
mvv 34,208,62
R 46,32768
mvv 38,212,62
ds_read_b64 v[118:119], v84 offset:128
ds_read_b128 v[102:105], v85 offset:128
mvv 42,62,216
ds_read_b128 v[106:109], v85 offset:144
mvv 34,220,66
R 50,33792
mvv 38,224,66
mvv 42,66,228
s_waitcnt lgkmcnt(5)
mvv 34,232,70
R 54,34816
mvv 38,236,70
mvv 42,70,240
mvv 34,244,74
R 58,35840
mvv 38,248,74
mvv 42,74,252
s_waitcnt lgkmcnt(2)
v_mfma_f32_16x16x32_bf16 v[22:25], a[0:3], v[46:49], 0
R 62,36864
v_mul_f32 v120, v119, v94
v_mfma_f32_16x16x32_bf16 v[26:29], a[4:7], v[46:49], 0
v_fma_f32 v34, v34, v118, v120
v_mul_f32 v121, v119, v95
v_mfma_f32_16x16x32_bf16 v[30:33], v[46:49], a[8:11], 0
v_fma_f32 v35, v35, v118, v121
v_mul_f32 v120, v119, v96
mav 22,12,50
R 66,37888
v_fma_f32 v36, v36, v118, v120
mav 26,16,50
v_mul_f32 v121, v119, v97
v_fma_f32 v37, v37, v118, v121
mva 30,50,20
v_mul_f32 v120, v119, v98
s_waitcnt lgkmcnt(2)
mav 22,24,54
R 70,38912
v_fma_f32 v38, v38, v118, v120
mav 26,28,54
v_mul_f32 v121, v119, v99
v_fma_f32 v39, v39, v118, v121
mva 30,54,32
v_mul_f32 v120, v119, v100
v_fma_f32 v40, v40, v118, v120
mav 22,36,58
R 74,39936
v_mul_f32 v121, v119, v101
mav 26,40,58
v_fma_f32 v41, v41, v118, v121
v_cvt_pk_bf16_f32 v110, v34, v35
mva 30,58,44
v_cvt_pk_bf16_f32 v111, v36, v37
s_waitcnt lgkmcnt(2)
mav 22,48,62
R 46,40960
v_cvt_pk_bf16_f32 v112, v38, v39
mav 26,52,62
v_cvt_pk_bf16_f32 v113, v40, v41
v_mul_f32 v103, v103, v116
mva 30,62,56
ds_write_b128 v83, v[110:113] offset:4096
mav 22,60,66
R 50,41984
v_fma_f32 v42, v42, v102, v103
mav 26,64,66
v_mul_f32 v105, v105, v116
v_fma_f32 v43, v43, v104, v105
mva 30,66,68
v_mul_f32 v107, v107, v116
s_waitcnt lgkmcnt(3)
mav 22,72,70
R 54,43008
v_fma_f32 v44, v44, v106, v107
mav 26,76,70
v_mul_f32 v109, v109, v116
v_fma_f32 v45, v45, v108, v109
mva 30,70,80
v_cvt_pk_bf16_f32 v78, v42, v43
v_cvt_pk_bf16_f32 v79, v44, v45
mav 22,84,74
R 58,44032
mav 26,88,74
ds_write_b64 v83, v[78:79] offset:5120
mva 30,74,92
s_waitcnt lgkmcnt(3)
mav 22,96,46
R 62,45056
s_add_u32 s49, s49, 346112
mav 26,100,46
s_waitcnt vmcnt(0) lgkmcnt(1)
s_barrier
mva 30,46,104
mav 22,108,50
R 66,46080
mav 26,112,50
mva 30,50,116
mav 22,120,54
R 70,47104
mav 26,124,54
mva 30,54,128
mav 22,132,58
R 74,48128
mav 26,136,58
mva 30,58,140
s_waitcnt lgkmcnt(2)
mav 22,144,62
R 46,49152
mav 26,148,62
mva 30,62,152
mav 22,156,66
R 50,50176
mav 26,160,66
mva 30,66,164
s_waitcnt lgkmcnt(2)
mav 22,168,70
R 54,51200
mav 26,172,70
mva 30,70,176
mav 22,180,74
R 58,52224
mav 26,184,74
mva 30,74,188
s_waitcnt lgkmcnt(2)
mav 22,192,46
R 62,53248
mav 26,196,46
mva 30,46,200
mav 22,204,50
R 66,54272
mav 26,208,50
mva 30,50,212
s_waitcnt lgkmcnt(2)
mav 22,216,54
R 70,55296
mav 26,220,54
mva 30,54,224
mav 22,228,58
R 74,56320
mav 26,232,58
mva 30,58,236
s_waitcnt lgkmcnt(2)
mav 22,240,62
R 46,57344
mav 26,244,62
mva 30,62,248
mav 22,252,66
R 50,58368
mvv 26,128,66
mvv 30,66,132
s_waitcnt lgkmcnt(2)
mvv 22,136,70
R 54,59392
mvv 26,140,70
mvv 30,70,144
mvv 22,148,74
R 58,60416
mvv 26,152,74
mvv 30,74,156
s_waitcnt lgkmcnt(2)
mvv 22,160,46
R 62,61440
mvv 26,164,46
mvv 30,46,168
mvv 22,172,50
R 66,62464
mvv 26,176,50
mvv 30,50,180
s_waitcnt lgkmcnt(2)
mvv 22,184,54
R 70,63488
mvv 26,188,54
mvv 30,54,192
mvv 22,196,58
R 74,64512
mvv 26,200,58
mvv 30,58,204
s_waitcnt lgkmcnt(2)
mvv 22,208,62
R 46,0
mvv 26,212,62
ds_read_b64 v[118:119], v84 offset:0
ds_read_b128 v[102:105], v85 offset:0
mvv 30,62,216
ds_read_b128 v[106:109], v85 offset:16
mvv 22,220,66
R 50,1024
mvv 26,224,66
mvv 30,66,228
s_waitcnt lgkmcnt(5)
mvv 22,232,70
R 54,2048
mvv 26,236,70
mvv 30,70,240
mvv 22,244,74
R 58,3072
mvv 26,248,74
mvv 30,74,252
s_waitcnt lgkmcnt(2)
v_mfma_f32_16x16x32_bf16 v[34:37], a[0:3], v[46:49], 0
R 62,4096
v_mul_f32 v120, v119, v94
v_mfma_f32_16x16x32_bf16 v[38:41], a[4:7], v[46:49], 0
v_fma_f32 v22, v22, v118, v120
v_mul_f32 v121, v119, v95
v_mfma_f32_16x16x32_bf16 v[42:45], v[46:49], a[8:11], 0
v_fma_f32 v23, v23, v118, v121
v_mul_f32 v120, v119, v96
mav 34,12,50
R 66,5120
v_fma_f32 v24, v24, v118, v120
mav 38,16,50
v_mul_f32 v121, v119, v97
v_fma_f32 v25, v25, v118, v121
mva 42,50,20
v_mul_f32 v120, v119, v98
s_waitcnt lgkmcnt(2)
mav 34,24,54
R 70,6144
v_fma_f32 v26, v26, v118, v120
mav 38,28,54
v_mul_f32 v121, v119, v99
v_fma_f32 v27, v27, v118, v121
mva 42,54,32
v_mul_f32 v120, v119, v100
v_fma_f32 v28, v28, v118, v120
mav 34,36,58
R 74,7168
v_mul_f32 v121, v119, v101
mav 38,40,58
v_fma_f32 v29, v29, v118, v121
v_cvt_pk_bf16_f32 v110, v22, v23
mva 42,58,44
v_cvt_pk_bf16_f32 v111, v24, v25
s_waitcnt lgkmcnt(2)
mav 34,48,62
R 46,8192
v_cvt_pk_bf16_f32 v112, v26, v27
mav 38,52,62
v_cvt_pk_bf16_f32 v113, v28, v29
v_mul_f32 v103, v103, v116
mva 42,62,56
ds_write_b128 v83, v[110:113] offset:0
mav 34,60,66
R 50,9216
v_fma_f32 v30, v30, v102, v103
mav 38,64,66
v_mul_f32 v105, v105, v116
v_fma_f32 v31, v31, v104, v105
mva 42,66,68
v_mul_f32 v107, v107, v116
s_waitcnt lgkmcnt(3)
mav 34,72,70
R 54,10240
v_fma_f32 v32, v32, v106, v107
mav 38,76,70
v_mul_f32 v109, v109, v116
v_fma_f32 v33, v33, v108, v109
mva 42,70,80
v_cvt_pk_bf16_f32 v78, v30, v31
v_cvt_pk_bf16_f32 v79, v32, v33
mav 34,84,74
R 58,11264
mav 38,88,74
ds_write_b64 v83, v[78:79] offset:1024
mva 42,74,92
s_waitcnt lgkmcnt(3)
mav 34,96,46
R 62,12288
mav 38,100,46
s_waitcnt lgkmcnt(1)
s_barrier
mva 42,46,104
mav 34,108,50
R 66,13312
mav 38,112,50
mva 42,50,116
mav 34,120,54
R 70,14336
mav 38,124,54
mva 42,54,128
mav 34,132,58
R 74,15360
mav 38,136,58
mva 42,58,140
s_waitcnt lgkmcnt(2)
mav 34,144,62
R 46,16384
mav 38,148,62
mva 42,62,152
mav 34,156,66
R 50,17408
mav 38,160,66
mva 42,66,164
s_waitcnt lgkmcnt(2)
mav 34,168,70
R 54,18432
mav 38,172,70
mva 42,70,176
mav 34,180,74
R 58,19456
mav 38,184,74
mva 42,74,188
s_waitcnt lgkmcnt(2)
mav 34,192,46
R 62,20480
mav 38,196,46
mva 42,46,200
mav 34,204,50
R 66,21504
mav 38,208,50
mva 42,50,212
s_waitcnt lgkmcnt(2)
mav 34,216,54
R 70,22528
mav 38,220,54
mva 42,54,224
mav 34,228,58
R 74,23552
mav 38,232,58
mva 42,58,236
s_waitcnt lgkmcnt(2)
mav 34,240,62
R 46,24576
mav 38,244,62
mva 42,62,248
mav 34,252,66
R 50,25600
mvv 38,128,66
mvv 42,66,132
s_waitcnt lgkmcnt(2)
mvv 34,136,70
R 54,26624
mvv 38,140,70
mvv 42,70,144
mvv 34,148,74
R 58,27648
mvv 38,152,74
mvv 42,74,156
s_waitcnt lgkmcnt(2)
mvv 34,160,46
R 62,28672
mvv 38,164,46
mvv 42,46,168
mvv 34,172,50
R 66,29696
mvv 38,176,50
mvv 42,50,180
s_waitcnt lgkmcnt(2)
mvv 34,184,54
R 70,30720
mvv 38,188,54
mvv 42,54,192
mvv 34,196,58
R 74,31744
mvv 38,200,58
mvv 42,58,204
s_waitcnt lgkmcnt(2)
mvv 34,208,62
mvv 38,212,62
ds_read_b64 v[118:119], v84 offset:128
ds_read_b128 v[102:105], v85 offset:128
mvv 42,62,216
ds_read_b128 v[106:109], v85 offset:144
mvv 34,220,66
mvv 38,224,66
mvv 42,66,228
s_waitcnt lgkmcnt(3)
mvv 34,232,70
mvv 38,236,70
mvv 42,70,240
mvv 34,244,74
mvv 38,248,74
mvv 42,74,252
s_waitcnt lgkmcnt(0)
v_mul_f32 v120, v119, v94
s_nop 3
v_fma_f32 v34, v34, v118, v120
v_mul_f32 v121, v119, v95
v_fma_f32 v35, v35, v118, v121
v_mul_f32 v120, v119, v96
v_fma_f32 v36, v36, v118, v120
v_mul_f32 v121, v119, v97
v_fma_f32 v37, v37, v118, v121
v_mul_f32 v120, v119, v98
v_fma_f32 v38, v38, v118, v120
v_mul_f32 v121, v119, v99
v_fma_f32 v39, v39, v118, v121
v_mul_f32 v120, v119, v100
v_fma_f32 v40, v40, v118, v120
v_mul_f32 v121, v119, v101
v_fma_f32 v41, v41, v118, v121
v_cvt_pk_bf16_f32 v110, v34, v35
v_cvt_pk_bf16_f32 v111, v36, v37
v_cvt_pk_bf16_f32 v112, v38, v39
v_cvt_pk_bf16_f32 v113, v40, v41
ds_write_b128 v83, v[110:113] offset:4096
v_mul_f32 v103, v103, v116
v_fma_f32 v42, v42, v102, v103
v_mul_f32 v105, v105, v116
v_fma_f32 v43, v43, v104, v105
v_mul_f32 v107, v107, v116
v_fma_f32 v44, v44, v106, v107
v_mul_f32 v109, v109, v116
v_fma_f32 v45, v45, v108, v109
v_cvt_pk_bf16_f32 v78, v42, v43
v_cvt_pk_bf16_f32 v79, v44, v45
ds_write_b64 v83, v[78:79] offset:5120
s_waitcnt lgkmcnt(0)
s_barrier
s_endpgm
CQB0_1:
s_mov_b32 s58, m0
s_mov_b32 m0, 142336
DQB0_1:
s_cmp_eq_u32 s61, 0
s_cbranch_scc1 EQB0_1
s_sub_u32 s61, s61, 1
buffer_load_dword v90, s[36:39], 0 offen sc1 lds
s_sleep 1
ds_read_b32 v122, v82
s_waitcnt lgkmcnt(0)
v_cmp_gt_i32 vcc, v91, v122
s_cbranch_vccnz DQB0_1
EQB0_1:
s_mov_b32 m0, s58
s_branch BQB0_1
CQB0_3:
s_mov_b32 s58, m0
s_mov_b32 m0, 142336
DQB0_3:
s_cmp_eq_u32 s61, 0
s_cbranch_scc1 EQB0_3
s_sub_u32 s61, s61, 1
buffer_load_dword v90, s[36:39], 0 offen sc1 lds
s_sleep 1
ds_read_b32 v122, v82
s_waitcnt lgkmcnt(0)
v_cmp_gt_i32 vcc, v91, v122
s_cbranch_vccnz DQB0_3
EQB0_3:
s_mov_b32 m0, s58
s_branch BQB0_3
CQB0_4:
s_mov_b32 s58, m0
s_mov_b32 m0, 142336
DQB0_4:
s_cmp_eq_u32 s61, 0
s_cbranch_scc1 EQB0_4
s_sub_u32 s61, s61, 1
buffer_load_dword v90, s[36:39], 0 offen sc1 lds
s_sleep 1
ds_read_b32 v122, v82
s_waitcnt lgkmcnt(0)
v_cmp_gt_i32 vcc, v91, v122
s_cbranch_vccnz DQB0_4
EQB0_4:
s_mov_b32 m0, s58
s_branch BQB0_4
CQB0_5:
s_mov_b32 s58, m0
s_mov_b32 m0, 142336
DQB0_5:
s_cmp_eq_u32 s61, 0
s_cbranch_scc1 EQB0_5
s_sub_u32 s61, s61, 1
buffer_load_dword v90, s[36:39], 0 offen sc1 lds
s_sleep 1
ds_read_b32 v122, v82
s_waitcnt lgkmcnt(0)
v_cmp_gt_i32 vcc, v91, v122
s_cbranch_vccnz DQB0_5
EQB0_5:
s_mov_b32 m0, s58
s_branch BQB0_5
CQB0_6:
s_mov_b32 s58, m0
s_mov_b32 m0, 142336
DQB0_6:
s_cmp_eq_u32 s61, 0
s_cbranch_scc1 EQB0_6
s_sub_u32 s61, s61, 1
buffer_load_dword v90, s[36:39], 0 offen sc1 lds
s_sleep 1
ds_read_b32 v122, v82
s_waitcnt lgkmcnt(0)
v_cmp_gt_i32 vcc, v91, v122
s_cbranch_vccnz DQB0_6
EQB0_6:
s_mov_b32 m0, s58
s_branch BQB0_6
LvQB1:
v_xor_b32 v80, 0x10000, v80
ds_read_b64 v[118:119], v84 offset:0
ds_read_b128 v[102:105], v85 offset:0
ds_read_b128 v[106:109], v85 offset:16
s_add_u32 s49, s49, 346112
R 46,32768
R 50,33792
R 54,34816
R 58,35840
s_waitcnt lgkmcnt(2)
v_mul_f32 v120, v119, v94
v_fma_f32 v22, v22, v118, v120
v_mul_f32 v121, v119, v95
v_fma_f32 v23, v23, v118, v121
v_mul_f32 v120, v119, v96
v_fma_f32 v24, v24, v118, v120
v_mul_f32 v121, v119, v97
v_fma_f32 v25, v25, v118, v121
v_mul_f32 v120, v119, v98
v_fma_f32 v26, v26, v118, v120
v_mul_f32 v121, v119, v99
v_fma_f32 v27, v27, v118, v121
v_mul_f32 v120, v119, v100
v_fma_f32 v28, v28, v118, v120
v_mul_f32 v121, v119, v101
v_fma_f32 v29, v29, v118, v121
v_cvt_pk_bf16_f32 v110, v22, v23
v_cvt_pk_bf16_f32 v111, v24, v25
v_cvt_pk_bf16_f32 v112, v26, v27
v_cvt_pk_bf16_f32 v113, v28, v29
ds_write_b128 v83, v[110:113] offset:0
v_mul_f32 v103, v103, v116
v_fma_f32 v30, v30, v102, v103
v_mul_f32 v105, v105, v116
v_fma_f32 v31, v31, v104, v105
v_mul_f32 v107, v107, v116
v_fma_f32 v32, v32, v106, v107
v_mul_f32 v109, v109, v116
v_fma_f32 v33, v33, v108, v109
v_cvt_pk_bf16_f32 v78, v30, v31
v_cvt_pk_bf16_f32 v79, v32, v33
ds_write_b64 v83, v[78:79] offset:1024
s_xor_b32 s50, s50, 0x10000
s_add_u32 s49, s49, 346112
s_waitcnt lgkmcnt(0)
s_barrier
ds_read_b64 v[118:119], v84 offset:128
ds_read_b128 v[102:105], v85 offset:128
ds_read_b128 v[106:109], v85 offset:144
v_mfma_f32_16x16x32_bf16 v[22:25], a[0:3], v[46:49], 0
R 62,36864
v_mfma_f32_16x16x32_bf16 v[26:29], a[4:7], v[46:49], 0
s_waitcnt lgkmcnt(1)
v_mul_f32 v120, v119, v94
v_mfma_f32_16x16x32_bf16 v[30:33], v[46:49], a[8:11], 0
v_fma_f32 v34, v34, v118, v120
v_mul_f32 v121, v119, v95
mav 22,12,50
R 66,37888
v_fma_f32 v35, v35, v118, v121
mav 26,16,50
v_mul_f32 v120, v119, v96
v_fma_f32 v36, v36, v118, v120
mva 30,50,20
v_mul_f32 v121, v119, v97
v_fma_f32 v37, v37, v118, v121
mav 22,24,54
R 70,38912
v_mul_f32 v120, v119, v98
mav 26,28,54
v_fma_f32 v38, v38, v118, v120
v_mul_f32 v121, v119, v99
mva 30,54,32
v_fma_f32 v39, v39, v118, v121
v_mul_f32 v120, v119, v100
mav 22,36,58
R 74,39936
v_fma_f32 v40, v40, v118, v120
mav 26,40,58
v_mul_f32 v121, v119, v101
v_fma_f32 v41, v41, v118, v121
mva 30,58,44
v_cvt_pk_bf16_f32 v110, v34, v35
s_waitcnt lgkmcnt(2)
mav 22,48,62
R 46,40960
v_cvt_pk_bf16_f32 v111, v36, v37
mav 26,52,62
v_cvt_pk_bf16_f32 v112, v38, v39
v_cvt_pk_bf16_f32 v113, v40, v41
mva 30,62,56
ds_write_b128 v83, v[110:113] offset:4096
mav 22,60,66
R 50,41984
v_mul_f32 v103, v103, v116
mav 26,64,66
v_fma_f32 v42, v42, v102, v103
v_mul_f32 v105, v105, v116
mva 30,66,68
v_fma_f32 v43, v43, v104, v105
s_waitcnt lgkmcnt(3)
mav 22,72,70
R 54,43008
v_mul_f32 v107, v107, v116
mav 26,76,70
v_fma_f32 v44, v44, v106, v107
v_mul_f32 v109, v109, v116
mva 30,70,80
v_fma_f32 v45, v45, v108, v109
v_cvt_pk_bf16_f32 v78, v42, v43
mav 22,84,74
R 58,44032
v_cvt_pk_bf16_f32 v79, v44, v45
mav 26,88,74
ds_write_b64 v83, v[78:79] offset:5120
mva 30,74,92
s_waitcnt lgkmcnt(3)
mav 22,96,46
R 62,45056
mav 26,100,46
s_waitcnt vmcnt(0) lgkmcnt(1)
s_barrier
mva 30,46,104
ds_write_b64 v84, v[18:19] offset:128
mav 22,108,50
R 66,46080
s_add_u32 m0, s50, 32768
mav 26,112,50
mva 30,50,116
D 86,0
mav 22,120,54
R 70,47104
mav 26,124,54
D 86,1024
mva 30,54,128
mav 22,132,58
R 74,48128
D 86,2048
mav 26,136,58
mva 30,58,140
D 86,3072
s_waitcnt lgkmcnt(2)
mav 22,144,62
R 46,49152
s_add_u32 m0, s50, 40960
mav 26,148,62
D 87,0
mva 30,62,152
mav 22,156,66
R 50,50176
D 87,1024
mav 26,160,66
mva 30,66,164
D 87,2048
s_waitcnt lgkmcnt(2)
mav 22,168,70
R 54,51200
mav 26,172,70
D 87,3072
s_add_u32 m0, s50, 49152
mva 30,70,176
mav 22,180,74
R 58,52224
D 88,0
mav 26,184,74
mva 30,74,188
D 88,1024
s_waitcnt lgkmcnt(2)
mav 22,192,46
R 62,53248
mav 26,196,46
D 88,2048
mva 30,46,200
mav 22,204,50
R 66,54272
D 88,3072
mav 26,208,50
s_add_u32 m0, s50, 57344
mva 30,50,212
D 89,0
s_waitcnt lgkmcnt(2)
mav 22,216,54
R 70,55296
mav 26,220,54
D 89,1024
mva 30,54,224
mav 22,228,58
R 74,56320
D 89,2048
mav 26,232,58
mva 30,58,236
D 89,3072
s_waitcnt lgkmcnt(2)
mav 22,240,62
R 46,57344
s_add_u32 s49, s49, 346112
mav 26,244,62
s_add_u32 m0, s50, 0
mva 30,62,248
D 86,0
mav 22,252,66
R 50,58368
mvv 26,128,66
D 86,1024
mvv 30,66,132
s_waitcnt lgkmcnt(2)
mvv 22,136,70
R 54,59392
D 86,2048
mvv 26,140,70
mvv 30,70,144
D 86,3072
s_add_u32 m0, s50, 8192
mvv 22,148,74
R 58,60416
mvv 26,152,74
D 87,0
mvv 30,74,156
s_waitcnt lgkmcnt(2)
mvv 22,160,46
R 62,61440
D 87,1024
mvv 26,164,46
mvv 30,46,168
D 87,2048
mvv 22,172,50
R 66,62464
mvv 26,176,50
D 87,3072
s_add_u32 m0, s50, 16384
mvv 30,50,180
s_waitcnt lgkmcnt(2)
mvv 22,184,54
R 70,63488
D 88,0
mvv 26,188,54
mvv 30,54,192
D 88,1024
mvv 22,196,58
R 74,64512
mvv 26,200,58
D 88,2048
mvv 30,58,204
s_waitcnt lgkmcnt(2)
mvv 22,208,62
R 46,0
D 88,3072
mvv 26,212,62
s_add_u32 m0, s50, 24576
ds_read_b64 v[118:119], v84 offset:0
mvv 30,62,216
ds_read_b128 v[102:105], v85 offset:0
ds_read_b128 v[106:109], v85 offset:16
mvv 22,220,66
R 50,1024
D 89,0
mvv 26,224,66
mvv 30,66,228
D 89,1024
s_waitcnt lgkmcnt(5)
mvv 22,232,70
R 54,2048
mvv 26,236,70
D 89,2048
mvv 30,70,240
mvv 22,244,74
R 58,3072
D 89,3072
mvv 26,248,74
mvv 30,74,252
s_waitcnt lgkmcnt(2)
v_mfma_f32_16x16x32_bf16 v[34:37], a[0:3], v[46:49], 0
R 62,4096
v_mul_f32 v120, v119, v94
v_mfma_f32_16x16x32_bf16 v[38:41], a[4:7], v[46:49], 0
v_fma_f32 v22, v22, v118, v120
v_mul_f32 v121, v119, v95
v_mfma_f32_16x16x32_bf16 v[42:45], v[46:49], a[8:11], 0
v_fma_f32 v23, v23, v118, v121
v_mul_f32 v120, v119, v96
mav 34,12,50
R 66,5120
v_fma_f32 v24, v24, v118, v120
mav 38,16,50
v_mul_f32 v121, v119, v97
v_fma_f32 v25, v25, v118, v121
mva 42,50,20
v_mul_f32 v120, v119, v98
s_waitcnt lgkmcnt(2)
mav 34,24,54
R 70,6144
v_fma_f32 v26, v26, v118, v120
mav 38,28,54
v_mul_f32 v121, v119, v99
v_fma_f32 v27, v27, v118, v121
mva 42,54,32
v_mul_f32 v120, v119, v100
v_fma_f32 v28, v28, v118, v120
mav 34,36,58
R 74,7168
v_mul_f32 v121, v119, v101
mav 38,40,58
v_fma_f32 v29, v29, v118, v121
v_cvt_pk_bf16_f32 v110, v22, v23
mva 42,58,44
v_cvt_pk_bf16_f32 v111, v24, v25
s_waitcnt lgkmcnt(2)
mav 34,48,62
R 46,8192
v_cvt_pk_bf16_f32 v112, v26, v27
mav 38,52,62
v_cvt_pk_bf16_f32 v113, v28, v29
v_mul_f32 v103, v103, v116
mva 42,62,56
ds_write_b128 v83, v[110:113] offset:0
mav 34,60,66
R 50,9216
v_fma_f32 v30, v30, v102, v103
mav 38,64,66
v_mul_f32 v105, v105, v116
v_fma_f32 v31, v31, v104, v105
mva 42,66,68
v_mul_f32 v107, v107, v116
s_waitcnt lgkmcnt(3)
mav 34,72,70
R 54,10240
v_fma_f32 v32, v32, v106, v107
mav 38,76,70
v_mul_f32 v109, v109, v116
v_fma_f32 v33, v33, v108, v109
mva 42,70,80
s_xor_b32 s50, s50, 0x10000
v_cvt_pk_bf16_f32 v78, v30, v31
mav 34,84,74
R 58,11264
v_cvt_pk_bf16_f32 v79, v32, v33
mav 38,88,74
ds_write_b64 v83, v[78:79] offset:1024
mva 42,74,92
s_waitcnt lgkmcnt(3)
mav 34,96,46
R 62,12288
s_add_u32 s49, s49, 346112
mav 38,100,46
s_waitcnt vmcnt(16) lgkmcnt(1)
s_barrier
mva 42,46,104
buffer_load_dwordx2 v[14:15], v92, s[36:39], s49 offen sc1
mav 34,108,50
R 66,13312
s_add_u32 m0, s50, 32768
mav 38,112,50
mva 42,50,116
D 86,0
mav 34,120,54
R 70,14336
mav 38,124,54
D 86,1024
mva 42,54,128
mav 34,132,58
R 74,15360
mav 38,136,58
D 86,2048
mva 42,58,140
s_waitcnt lgkmcnt(2)
mav 34,144,62
R 46,16384
D 86,3072
mav 38,148,62
s_add_u32 m0, s50, 40960
mva 42,62,152
mav 34,156,66
R 50,17408
D 87,0
mav 38,160,66
mva 42,66,164
D 87,1024
s_waitcnt lgkmcnt(2)
mav 34,168,70
R 54,18432
mav 38,172,70
mva 42,70,176
D 87,2048
mav 34,180,74
R 58,19456
mav 38,184,74
D 87,3072
mva 42,74,188
s_add_u32 m0, s50, 49152
s_waitcnt lgkmcnt(2)
mav 34,192,46
R 62,20480
mav 38,196,46
D 88,0
mva 42,46,200
mav 34,204,50
R 66,21504
D 88,1024
mav 38,208,50
mva 42,50,212
s_waitcnt lgkmcnt(2)
mav 34,216,54
R 70,22528
D 88,2048
mav 38,220,54
mva 42,54,224
D 88,3072
mav 34,228,58
R 74,23552
s_add_u32 m0, s50, 57344
mav 38,232,58
mva 42,58,236
D 89,0
s_waitcnt lgkmcnt(2)
mav 34,240,62
R 46,24576
mav 38,244,62
D 89,1024
mva 42,62,248
mav 34,252,66
R 50,25600
mvv 38,128,66
D 89,2048
mvv 42,66,132
s_waitcnt lgkmcnt(2)
mvv 34,136,70
R 54,26624
D 89,3072
mvv 38,140,70
mvv 42,70,144
mvv 34,148,74
R 58,27648
mvv 38,152,74
mvv 42,74,156
s_waitcnt lgkmcnt(2)
mvv 34,160,46
R 62,28672
mvv 38,164,46
mvv 42,46,168
s_waitcnt vmcnt(16)
v_permlane32_swap_b32 v14, v15
mvv 34,172,50
R 66,29696
v_add_f32 v16, v14, v15
mvv 38,176,50
v_add_f32 v17, v14, v15
mvv 42,50,180
s_waitcnt lgkmcnt(2)
mvv 34,184,54
R 70,30720
v_permlane16_swap_b32 v16, v17
mvv 38,188,54
v_add_f32 v14, v16, v17
v_add_f32 v15, v16, v17
mvv 42,54,192
mvv 34,196,58
R 74,31744
v_xor_b32 v80, 0x10000, v80
mvv 38,200,58
v_permlane32_swap_b32 v14, v15
v_mul_f32 v16, 0x3a800000, v14
mvv 42,58,204
v_mul_f32 v17, 0x3a800000, v15
s_waitcnt lgkmcnt(2)
mvv 34,208,62
R 46,32768
v_fma_f32 v17, -v16, v16, v17
mvv 38,212,62
ds_read_b64 v[118:119], v84 offset:128
ds_read_b128 v[102:105], v85 offset:128
mvv 42,62,216
ds_read_b128 v[106:109], v85 offset:144
v_max_f32 v17, 0, v17
mvv 34,220,66
R 50,33792
v_add_f32 v17, 0x3727c5ac, v17
mvv 38,224,66
v_rsq_f32 v14, v17
mvv 42,66,228
v_mul_f32_e64 v15, -v16, v14
s_waitcnt lgkmcnt(5)
mvv 34,232,70
R 54,34816
mvv 38,236,70
mvv 42,70,240
mvv 34,244,74
R 58,35840
mvv 38,248,74
mvv 42,74,252
s_waitcnt lgkmcnt(2)
v_mfma_f32_16x16x32_bf16 v[22:25], a[0:3], v[46:49], 0
R 62,36864
v_mul_f32 v120, v119, v94
v_mfma_f32_16x16x32_bf16 v[26:29], a[4:7], v[46:49], 0
v_fma_f32 v34, v34, v118, v120
v_mul_f32 v121, v119, v95
v_mfma_f32_16x16x32_bf16 v[30:33], v[46:49], a[8:11], 0
v_fma_f32 v35, v35, v118, v121
v_mul_f32 v120, v119, v96
mav 22,12,50
R 66,37888
v_fma_f32 v36, v36, v118, v120
mav 26,16,50
v_mul_f32 v121, v119, v97
v_fma_f32 v37, v37, v118, v121
mva 30,50,20
v_mul_f32 v120, v119, v98
s_waitcnt lgkmcnt(2)
mav 22,24,54
R 70,38912
v_fma_f32 v38, v38, v118, v120
mav 26,28,54
v_mul_f32 v121, v119, v99
v_fma_f32 v39, v39, v118, v121
mva 30,54,32
v_mul_f32 v120, v119, v100
v_fma_f32 v40, v40, v118, v120
mav 22,36,58
R 74,39936
v_mul_f32 v121, v119, v101
mav 26,40,58
v_fma_f32 v41, v41, v118, v121
v_cvt_pk_bf16_f32 v110, v34, v35
mva 30,58,44
v_cvt_pk_bf16_f32 v111, v36, v37
s_waitcnt lgkmcnt(2)
mav 22,48,62
R 46,40960
v_cvt_pk_bf16_f32 v112, v38, v39
mav 26,52,62
v_cvt_pk_bf16_f32 v113, v40, v41
v_mul_f32 v103, v103, v116
mva 30,62,56
ds_write_b128 v83, v[110:113] offset:4096
mav 22,60,66
R 50,41984
v_fma_f32 v42, v42, v102, v103
mav 26,64,66
v_mul_f32 v105, v105, v116
v_fma_f32 v43, v43, v104, v105
mva 30,66,68
v_mul_f32 v107, v107, v116
s_waitcnt lgkmcnt(3)
mav 22,72,70
R 54,43008
v_fma_f32 v44, v44, v106, v107
mav 26,76,70
v_mul_f32 v109, v109, v116
v_fma_f32 v45, v45, v108, v109
mva 30,70,80
v_cvt_pk_bf16_f32 v78, v42, v43
v_cvt_pk_bf16_f32 v79, v44, v45
mav 22,84,74
R 58,44032
mav 26,88,74
ds_write_b64 v83, v[78:79] offset:5120
mva 30,74,92
s_waitcnt lgkmcnt(3)
mav 22,96,46
R 62,45056
s_add_u32 s49, s49, 346112
mav 26,100,46
ds_write_b64 v84, v[14:15] offset:0
mva 30,46,104
mav 22,108,50
R 66,46080
s_add_u32 m0, s50, 0
mav 26,112,50
s_waitcnt lgkmcnt(1)
s_barrier
mva 30,50,116
buffer_load_dwordx2 v[14:15], v92, s[36:39], s49 offen sc1
D 86,0
mav 22,120,54
R 70,47104
mav 26,124,54
D 86,1024
mva 30,54,128
mav 22,132,58
R 74,48128
mav 26,136,58
D 86,2048
mva 30,58,140
mav 22,144,62
R 46,49152
D 86,3072
mav 26,148,62
s_add_u32 m0, s50, 8192
mva 30,62,152
s_waitcnt lgkmcnt(2)
mav 22,156,66
R 50,50176
D 87,0
mav 26,160,66
mva 30,66,164
D 87,1024
mav 22,168,70
R 54,51200
mav 26,172,70
mva 30,70,176
D 87,2048
s_waitcnt lgkmcnt(2)
mav 22,180,74
R 58,52224
mav 26,184,74
D 87,3072
mva 30,74,188
s_add_u32 m0, s50, 16384
mav 22,192,46
R 62,53248
mav 26,196,46
D 88,0
mva 30,46,200
s_waitcnt lgkmcnt(2)
mav 22,204,50
R 66,54272
D 88,1024
mav 26,208,50
mva 30,50,212
mav 22,216,54
R 70,55296
D 88,2048
mav 26,220,54
mva 30,54,224
D 88,3072
s_waitcnt lgkmcnt(2)
mav 22,228,58
R 74,56320
s_add_u32 m0, s50, 24576
mav 26,232,58
mva 30,58,236
D 89,0
mav 22,240,62
R 46,57344
mav 26,244,62
D 89,1024
mva 30,62,248
s_waitcnt lgkmcnt(2)
mav 22,252,66
R 50,58368
mvv 26,128,66
D 89,2048
mvv 30,66,132
mvv 22,136,70
R 54,59392
D 89,3072
mvv 26,140,70
mvv 30,70,144
s_waitcnt lgkmcnt(2)
mvv 22,148,74
R 58,60416
mvv 26,152,74
mvv 30,74,156
mvv 22,160,46
R 62,61440
mvv 26,164,46
mvv 30,46,168
s_waitcnt lgkmcnt(2)
mvv 22,172,50
R 66,62464
mvv 26,176,50
s_waitcnt vmcnt(16)
v_permlane32_swap_b32 v14, v15
mvv 30,50,180
v_add_f32 v16, v14, v15
v_add_f32 v17, v14, v15
mvv 22,184,54
R 70,63488
v_permlane16_swap_b32 v16, v17
mvv 26,188,54
v_add_f32 v14, v16, v17
v_add_f32 v15, v16, v17
mvv 30,54,192
s_waitcnt lgkmcnt(2)
mvv 22,196,58
R 74,64512
v_permlane32_swap_b32 v14, v15
mvv 26,200,58
v_mul_f32 v16, 0x3a800000, v14
v_mul_f32 v17, 0x3a800000, v15
mvv 30,58,204
v_fma_f32 v17, -v16, v16, v17
v_max_f32 v17, 0, v17
mvv 22,208,62
R 46,0
v_add_f32 v17, 0x3727c5ac, v17
mvv 26,212,62
ds_read_b64 v[118:119], v84 offset:0
ds_read_b128 v[102:105], v85 offset:0
mvv 30,62,216
ds_read_b128 v[106:109], v85 offset:16
s_waitcnt lgkmcnt(5)
mvv 22,220,66
R 50,1024
mvv 26,224,66
v_rsq_f32 v14, v17
mvv 30,66,228
v_mul_f32_e64 v15, -v16, v14
mvv 22,232,70
R 54,2048
mvv 26,236,70
mvv 30,70,240
s_waitcnt lgkmcnt(2)
mvv 22,244,74
R 58,3072
mvv 26,248,74
mvv 30,74,252
v_mfma_f32_16x16x32_bf16 v[34:37], a[0:3], v[46:49], 0
R 62,4096
v_mul_f32 v120, v119, v94
v_mfma_f32_16x16x32_bf16 v[38:41], a[4:7], v[46:49], 0
v_mul_f32 v121, v119, v95
v_fma_f32 v22, v22, v118, v120
v_mfma_f32_16x16x32_bf16 v[42:45], v[46:49], a[8:11], 0
v_fma_f32 v23, v23, v118, v121
s_waitcnt lgkmcnt(2)
mav 34,12,50
R 66,5120
v_mul_f32 v120, v119, v96
mav 38,16,50
v_fma_f32 v24, v24, v118, v120
v_mul_f32 v121, v119, v97
mva 42,50,20
v_fma_f32 v25, v25, v118, v121
v_mul_f32 v120, v119, v98
mav 34,24,54
R 70,6144
v_fma_f32 v26, v26, v118, v120
mav 38,28,54
v_mul_f32 v121, v119, v99
v_fma_f32 v27, v27, v118, v121
mva 42,54,32
v_mul_f32 v120, v119, v100
s_waitcnt lgkmcnt(2)
mav 34,36,58
R 74,7168
v_fma_f32 v28, v28, v118, v120
mav 38,40,58
v_mul_f32 v121, v119, v101
v_fma_f32 v29, v29, v118, v121
mva 42,58,44
v_cvt_pk_bf16_f32 v110, v22, v23
v_cvt_pk_bf16_f32 v111, v24, v25
mav 34,48,62
R 46,8192
v_cvt_pk_bf16_f32 v112, v26, v27
mav 38,52,62
v_cvt_pk_bf16_f32 v113, v28, v29
v_mul_f32 v103, v103, v116
mva 42,62,56
v_fma_f32 v30, v30, v102, v103
s_waitcnt lgkmcnt(2)
mav 34,60,66
R 50,9216
v_mul_f32 v105, v105, v116
mav 38,64,66
ds_write_b128 v83, v[110:113] offset:0
mva 42,66,68
v_fma_f32 v31, v31, v104, v105
v_mul_f32 v107, v107, v116
mav 34,72,70
R 54,10240
v_fma_f32 v32, v32, v106, v107
mav 38,76,70
v_mul_f32 v109, v109, v116
v_fma_f32 v33, v33, v108, v109
mva 42,70,80
s_xor_b32 s50, s50, 0x10000
s_waitcnt lgkmcnt(3)
mav 34,84,74
R 58,11264
v_cvt_pk_bf16_f32 v78, v30, v31
mav 38,88,74
ds_write_b64 v84, v[14:15] offset:128
mva 42,74,92
v_cvt_pk_bf16_f32 v79, v32, v33
mav 34,96,46
R 62,12288
s_add_u32 s49, s49, 346112
mav 38,100,46
ds_write_b64 v83, v[78:79] offset:1024
mva 42,46,104
s_waitcnt lgkmcnt(4)
mav 34,108,50
R 66,13312
s_add_u32 m0, s50, 32768
mav 38,112,50
s_waitcnt lgkmcnt(1)
s_barrier
mva 42,50,116
buffer_load_dwordx2 v[14:15], v92, s[36:39], s49 offen sc1
D 86,0
mav 34,120,54
R 70,14336
mav 38,124,54
D 86,1024
mva 42,54,128
mav 34,132,58
R 74,15360
mav 38,136,58
D 86,2048
mva 42,58,140
mav 34,144,62
R 46,16384
D 86,3072
mav 38,148,62
s_add_u32 m0, s50, 40960
mva 42,62,152
s_waitcnt lgkmcnt(2)
mav 34,156,66
R 50,17408
D 87,0
mav 38,160,66
mva 42,66,164
D 87,1024
mav 34,168,70
R 54,18432
mav 38,172,70
mva 42,70,176
D 87,2048
s_waitcnt lgkmcnt(2)
mav 34,180,74
R 58,19456
mav 38,184,74
D 87,3072
mva 42,74,188
s_add_u32 m0, s50, 49152
mav 34,192,46
R 62,20480
mav 38,196,46
D 88,0
mva 42,46,200
s_waitcnt lgkmcnt(2)
mav 34,204,50
R 66,21504
D 88,1024
mav 38,208,50
mva 42,50,212
mav 34,216,54
R 70,22528
D 88,2048
mav 38,220,54
mva 42,54,224
D 88,3072
s_waitcnt lgkmcnt(2)
mav 34,228,58
R 74,23552
s_add_u32 m0, s50, 57344
mav 38,232,58
mva 42,58,236
D 89,0
mav 34,240,62
R 46,24576
mav 38,244,62
D 89,1024
mva 42,62,248
s_waitcnt lgkmcnt(2)
mav 34,252,66
R 50,25600
mvv 38,128,66
D 89,2048
mvv 42,66,132
mvv 34,136,70
R 54,26624
D 89,3072
mvv 38,140,70
mvv 42,70,144
s_waitcnt lgkmcnt(2)
mvv 34,148,74
R 58,27648
mvv 38,152,74
mvv 42,74,156
mvv 34,160,46
R 62,28672
mvv 38,164,46
mvv 42,46,168
s_waitcnt lgkmcnt(2)
mvv 34,172,50
R 66,29696
mvv 38,176,50
s_waitcnt vmcnt(16)
v_permlane32_swap_b32 v14, v15
mvv 42,50,180
v_add_f32 v16, v14, v15
v_add_f32 v17, v14, v15
mvv 34,184,54
R 70,30720
v_permlane16_swap_b32 v16, v17
mvv 38,188,54
v_add_f32 v14, v16, v17
v_add_f32 v15, v16, v17
mvv 42,54,192
s_waitcnt lgkmcnt(2)
mvv 34,196,58
R 74,31744
v_xor_b32 v80, 0x10000, v80
mvv 38,200,58
v_permlane32_swap_b32 v14, v15
v_mul_f32 v16, 0x3a800000, v14
mvv 42,58,204
v_mul_f32 v17, 0x3a800000, v15
v_fma_f32 v17, -v16, v16, v17
mvv 34,208,62
R 46,32768
v_max_f32 v17, 0, v17
mvv 38,212,62
ds_read_b64 v[118:119], v84 offset:128
ds_read_b128 v[102:105], v85 offset:128
mvv 42,62,216
ds_read_b128 v[106:109], v85 offset:144
s_waitcnt lgkmcnt(5)
mvv 34,220,66
R 50,33792
v_add_f32 v17, 0x3727c5ac, v17
mvv 38,224,66
v_rsq_f32 v14, v17
mvv 42,66,228
v_mul_f32_e64 v15, -v16, v14
mvv 34,232,70
R 54,34816
mvv 38,236,70
mvv 42,70,240
s_waitcnt lgkmcnt(2)
mvv 34,244,74
R 58,35840
mvv 38,248,74
mvv 42,74,252
s_mov_b32 s60, -27
.p2align 6
LQB1_1:
v_mfma_f32_16x16x32_bf16 v[22:25], a[0:3], v[46:49], 0
R 62,36864
v_mul_f32 v120, v119, v94
v_mfma_f32_16x16x32_bf16 v[26:29], a[4:7], v[46:49], 0
v_mul_f32 v121, v119, v95
v_fma_f32 v34, v34, v118, v120
v_mfma_f32_16x16x32_bf16 v[30:33], v[46:49], a[8:11], 0
v_fma_f32 v35, v35, v118, v121
s_waitcnt lgkmcnt(2)
mav 22,12,50
R 66,37888
v_mul_f32 v120, v119, v96
mav 26,16,50
v_fma_f32 v36, v36, v118, v120
v_mul_f32 v121, v119, v97
mva 30,50,20
v_fma_f32 v37, v37, v118, v121
v_mul_f32 v120, v119, v98
mav 22,24,54
R 70,38912
v_fma_f32 v38, v38, v118, v120
mav 26,28,54
v_mul_f32 v121, v119, v99
v_fma_f32 v39, v39, v118, v121
mva 30,54,32
v_mul_f32 v120, v119, v100
s_waitcnt lgkmcnt(2)
mav 22,36,58
R 74,39936
v_fma_f32 v40, v40, v118, v120
mav 26,40,58
v_mul_f32 v121, v119, v101
v_fma_f32 v41, v41, v118, v121
mva 30,58,44
v_cvt_pk_bf16_f32 v110, v34, v35
v_cvt_pk_bf16_f32 v111, v36, v37
mav 22,48,62
R 46,40960
v_cvt_pk_bf16_f32 v112, v38, v39
mav 26,52,62
v_cvt_pk_bf16_f32 v113, v40, v41
v_mul_f32 v103, v103, v116
mva 30,62,56
v_fma_f32 v42, v42, v102, v103
s_waitcnt lgkmcnt(2)
mav 22,60,66
R 50,41984
v_mul_f32 v105, v105, v116
mav 26,64,66
ds_write_b128 v83, v[110:113] offset:4096
mva 30,66,68
v_fma_f32 v43, v43, v104, v105
v_mul_f32 v107, v107, v116
mav 22,72,70
R 54,43008
v_fma_f32 v44, v44, v106, v107
mav 26,76,70
v_mul_f32 v109, v109, v116
v_fma_f32 v45, v45, v108, v109
mva 30,70,80
v_cvt_pk_bf16_f32 v78, v42, v43
s_waitcnt lgkmcnt(3)
mav 22,84,74
R 58,44032
v_cvt_pk_bf16_f32 v79, v44, v45
mav 26,88,74
ds_write_b64 v83, v[78:79] offset:5120
mva 30,74,92
ds_write_b64 v84, v[14:15] offset:0
mav 22,96,46
R 62,45056
s_add_u32 s49, s49, 346112
mav 26,100,46
mva 30,46,104
s_waitcnt lgkmcnt(1)
s_barrier
mav 22,108,50
buffer_load_dwordx2 v[14:15], v92, s[36:39], s49 offen sc1
R 66,46080
mav 26,112,50
s_add_u32 m0, s50, 0
mva 30,50,116
D 86,0
mav 22,120,54
R 70,47104
mav 26,124,54
D 86,1024
mva 30,54,128
mav 22,132,58
R 74,48128
mav 26,136,58
D 86,2048
mva 30,58,140
s_waitcnt lgkmcnt(2)
mav 22,144,62
R 46,49152
D 86,3072
mav 26,148,62
s_add_u32 m0, s50, 8192
mva 30,62,152
mav 22,156,66
R 50,50176
D 87,0
mav 26,160,66
mva 30,66,164
D 87,1024
s_waitcnt lgkmcnt(2)
mav 22,168,70
R 54,51200
mav 26,172,70
mva 30,70,176
D 87,2048
mav 22,180,74
R 58,52224
mav 26,184,74
D 87,3072
mva 30,74,188
s_add_u32 m0, s50, 16384
s_waitcnt lgkmcnt(2)
mav 22,192,46
R 62,53248
mav 26,196,46
D 88,0
mva 30,46,200
mav 22,204,50
R 66,54272
D 88,1024
mav 26,208,50
mva 30,50,212
s_waitcnt lgkmcnt(2)
mav 22,216,54
R 70,55296
D 88,2048
mav 26,220,54
mva 30,54,224
D 88,3072
mav 22,228,58
R 74,56320
s_add_u32 m0, s50, 24576
mav 26,232,58
mva 30,58,236
D 89,0
s_waitcnt lgkmcnt(2)
mav 22,240,62
R 46,57344
mav 26,244,62
D 89,1024
mva 30,62,248
mav 22,252,66
R 50,58368
mvv 26,128,66
D 89,2048
mvv 30,66,132
s_waitcnt lgkmcnt(2)
mvv 22,136,70
R 54,59392
D 89,3072
mvv 26,140,70
mvv 30,70,144
mvv 22,148,74
R 58,60416
mvv 26,152,74
mvv 30,74,156
s_waitcnt lgkmcnt(2)
mvv 22,160,46
R 62,61440
mvv 26,164,46
mvv 30,46,168
s_waitcnt vmcnt(16)
v_permlane32_swap_b32 v14, v15
mvv 22,172,50
R 66,62464
v_add_f32 v16, v14, v15
mvv 26,176,50
v_add_f32 v17, v14, v15
mvv 30,50,180
s_waitcnt lgkmcnt(2)
mvv 22,184,54
R 70,63488
v_permlane16_swap_b32 v16, v17
mvv 26,188,54
v_add_f32 v14, v16, v17
v_add_f32 v15, v16, v17
mvv 30,54,192
mvv 22,196,58
R 74,64512
v_permlane32_swap_b32 v14, v15
mvv 26,200,58
v_mul_f32 v16, 0x3a800000, v14
v_mul_f32 v17, 0x3a800000, v15
mvv 30,58,204
v_fma_f32 v17, -v16, v16, v17
s_waitcnt lgkmcnt(2)
mvv 22,208,62
R 46,0
v_max_f32 v17, 0, v17
mvv 26,212,62
ds_read_b64 v[118:119], v84 offset:0
ds_read_b128 v[102:105], v85 offset:0
mvv 30,62,216
ds_read_b128 v[106:109], v85 offset:16
v_add_f32 v17, 0x3727c5ac, v17
mvv 22,220,66
R 50,1024
mvv 26,224,66
v_rsq_f32 v14, v17
mvv 30,66,228
v_mul_f32_e64 v15, -v16, v14
s_waitcnt lgkmcnt(5)
mvv 22,232,70
R 54,2048
mvv 26,236,70
mvv 30,70,240
mvv 22,244,74
R 58,3072
mvv 26,248,74
mvv 30,74,252
s_waitcnt lgkmcnt(2)
v_mfma_f32_16x16x32_bf16 v[34:37], a[0:3], v[46:49], 0
R 62,4096
v_mul_f32 v120, v119, v94
v_mfma_f32_16x16x32_bf16 v[38:41], a[4:7], v[46:49], 0
v_fma_f32 v22, v22, v118, v120
v_mul_f32 v121, v119, v95
v_mfma_f32_16x16x32_bf16 v[42:45], v[46:49], a[8:11], 0
v_fma_f32 v23, v23, v118, v121
v_mul_f32 v120, v119, v96
mav 34,12,50
R 66,5120
v_fma_f32 v24, v24, v118, v120
mav 38,16,50
v_mul_f32 v121, v119, v97
v_fma_f32 v25, v25, v118, v121
mva 42,50,20
v_mul_f32 v120, v119, v98
s_waitcnt lgkmcnt(2)
mav 34,24,54
R 70,6144
v_fma_f32 v26, v26, v118, v120
mav 38,28,54
v_mul_f32 v121, v119, v99
v_fma_f32 v27, v27, v118, v121
mva 42,54,32
v_mul_f32 v120, v119, v100
v_fma_f32 v28, v28, v118, v120
mav 34,36,58
R 74,7168
v_mul_f32 v121, v119, v101
mav 38,40,58
v_fma_f32 v29, v29, v118, v121
v_cvt_pk_bf16_f32 v110, v22, v23
mva 42,58,44
v_cvt_pk_bf16_f32 v111, v24, v25
s_waitcnt lgkmcnt(2)
mav 34,48,62
R 46,8192
v_cvt_pk_bf16_f32 v112, v26, v27
mav 38,52,62
v_cvt_pk_bf16_f32 v113, v28, v29
v_mul_f32 v103, v103, v116
mva 42,62,56
ds_write_b128 v83, v[110:113] offset:0
mav 34,60,66
R 50,9216
v_fma_f32 v30, v30, v102, v103
mav 38,64,66
v_mul_f32 v105, v105, v116
v_fma_f32 v31, v31, v104, v105
mva 42,66,68
v_mul_f32 v107, v107, v116
s_waitcnt lgkmcnt(3)
mav 34,72,70
R 54,10240
v_fma_f32 v32, v32, v106, v107
mav 38,76,70
v_mul_f32 v109, v109, v116
v_fma_f32 v33, v33, v108, v109
mva 42,70,80
s_xor_b32 s50, s50, 0x10000
v_cvt_pk_bf16_f32 v78, v30, v31
mav 34,84,74
R 58,11264
v_cvt_pk_bf16_f32 v79, v32, v33
mav 38,88,74
ds_write_b64 v83, v[78:79] offset:1024
mva 42,74,92
s_waitcnt lgkmcnt(3)
mav 34,96,46
R 62,12288
s_add_u32 s49, s49, 346112
mav 38,100,46
ds_write_b64 v84, v[14:15] offset:128
mva 42,46,104
mav 34,108,50
R 66,13312
s_add_u32 m0, s50, 32768
mav 38,112,50
s_waitcnt lgkmcnt(1)
s_barrier
mva 42,50,116
buffer_load_dwordx2 v[14:15], v92, s[36:39], s49 offen sc1
D 86,0
mav 34,120,54
R 70,14336
mav 38,124,54
D 86,1024
mva 42,54,128
mav 34,132,58
R 74,15360
mav 38,136,58
D 86,2048
mva 42,58,140
mav 34,144,62
R 46,16384
D 86,3072
mav 38,148,62
s_add_u32 m0, s50, 40960
mva 42,62,152
s_waitcnt lgkmcnt(2)
mav 34,156,66
R 50,17408
D 87,0
mav 38,160,66
mva 42,66,164
D 87,1024
mav 34,168,70
R 54,18432
mav 38,172,70
mva 42,70,176
D 87,2048
s_waitcnt lgkmcnt(2)
mav 34,180,74
R 58,19456
mav 38,184,74
D 87,3072
mva 42,74,188
)"
R"(
s_add_u32 m0, s50, 49152
mav 34,192,46
R 62,20480
mav 38,196,46
D 88,0
mva 42,46,200
s_waitcnt lgkmcnt(2)
mav 34,204,50
R 66,21504
D 88,1024
mav 38,208,50
mva 42,50,212
mav 34,216,54
R 70,22528
D 88,2048
mav 38,220,54
mva 42,54,224
D 88,3072
s_waitcnt lgkmcnt(2)
mav 34,228,58
R 74,23552
s_add_u32 m0, s50, 57344
mav 38,232,58
mva 42,58,236
D 89,0
mav 34,240,62
R 46,24576
mav 38,244,62
D 89,1024
mva 42,62,248
s_waitcnt lgkmcnt(2)
mav 34,252,66
R 50,25600
mvv 38,128,66
D 89,2048
mvv 42,66,132
mvv 34,136,70
R 54,26624
D 89,3072
mvv 38,140,70
mvv 42,70,144
s_waitcnt lgkmcnt(2)
mvv 34,148,74
R 58,27648
mvv 38,152,74
mvv 42,74,156
mvv 34,160,46
R 62,28672
mvv 38,164,46
mvv 42,46,168
s_waitcnt lgkmcnt(2)
mvv 34,172,50
R 66,29696
mvv 38,176,50
s_waitcnt vmcnt(16)
v_permlane32_swap_b32 v14, v15
mvv 42,50,180
v_add_f32 v16, v14, v15
v_add_f32 v17, v14, v15
mvv 34,184,54
R 70,30720
v_permlane16_swap_b32 v16, v17
mvv 38,188,54
v_add_f32 v14, v16, v17
v_add_f32 v15, v16, v17
mvv 42,54,192
s_waitcnt lgkmcnt(2)
mvv 34,196,58
R 74,31744
v_xor_b32 v80, 0x10000, v80
mvv 38,200,58
v_permlane32_swap_b32 v14, v15
v_mul_f32 v16, 0x3a800000, v14
mvv 42,58,204
v_mul_f32 v17, 0x3a800000, v15
v_fma_f32 v17, -v16, v16, v17
mvv 34,208,62
R 46,32768
v_max_f32 v17, 0, v17
mvv 38,212,62
ds_read_b64 v[118:119], v84 offset:128
ds_read_b128 v[102:105], v85 offset:128
mvv 42,62,216
ds_read_b128 v[106:109], v85 offset:144
s_waitcnt lgkmcnt(5)
mvv 34,220,66
R 50,33792
v_add_f32 v17, 0x3727c5ac, v17
mvv 38,224,66
v_rsq_f32 v14, v17
mvv 42,66,228
v_mul_f32_e64 v15, -v16, v14
mvv 34,232,70
R 54,34816
mvv 38,236,70
mvv 42,70,240
s_waitcnt lgkmcnt(2)
mvv 34,244,74
R 58,35840
mvv 38,248,74
mvv 42,74,252
s_add_u32 s60, s60, 1
s_cbranch_scc0 LQB1_1
v_mfma_f32_16x16x32_bf16 v[22:25], a[0:3], v[46:49], 0
R 62,36864
v_mul_f32 v120, v119, v94
v_mfma_f32_16x16x32_bf16 v[26:29], a[4:7], v[46:49], 0
v_mul_f32 v121, v119, v95
v_fma_f32 v34, v34, v118, v120
v_mfma_f32_16x16x32_bf16 v[30:33], v[46:49], a[8:11], 0
v_fma_f32 v35, v35, v118, v121
s_waitcnt lgkmcnt(2)
mav 22,12,50
R 66,37888
v_mul_f32 v120, v119, v96
mav 26,16,50
v_fma_f32 v36, v36, v118, v120
v_mul_f32 v121, v119, v97
mva 30,50,20
v_fma_f32 v37, v37, v118, v121
v_mul_f32 v120, v119, v98
mav 22,24,54
R 70,38912
v_fma_f32 v38, v38, v118, v120
mav 26,28,54
v_mul_f32 v121, v119, v99
v_fma_f32 v39, v39, v118, v121
mva 30,54,32
v_mul_f32 v120, v119, v100
s_waitcnt lgkmcnt(2)
mav 22,36,58
R 74,39936
v_fma_f32 v40, v40, v118, v120
mav 26,40,58
v_mul_f32 v121, v119, v101
v_fma_f32 v41, v41, v118, v121
mva 30,58,44
v_cvt_pk_bf16_f32 v110, v34, v35
v_cvt_pk_bf16_f32 v111, v36, v37
mav 22,48,62
R 46,40960
v_cvt_pk_bf16_f32 v112, v38, v39
mav 26,52,62
v_cvt_pk_bf16_f32 v113, v40, v41
v_mul_f32 v103, v103, v116
mva 30,62,56
v_fma_f32 v42, v42, v102, v103
s_waitcnt lgkmcnt(2)
mav 22,60,66
R 50,41984
v_mul_f32 v105, v105, v116
mav 26,64,66
ds_write_b128 v83, v[110:113] offset:4096
mva 30,66,68
v_fma_f32 v43, v43, v104, v105
v_mul_f32 v107, v107, v116
mav 22,72,70
R 54,43008
v_fma_f32 v44, v44, v106, v107
mav 26,76,70
v_mul_f32 v109, v109, v116
v_fma_f32 v45, v45, v108, v109
mva 30,70,80
v_cvt_pk_bf16_f32 v78, v42, v43
s_waitcnt lgkmcnt(3)
mav 22,84,74
R 58,44032
v_cvt_pk_bf16_f32 v79, v44, v45
mav 26,88,74
ds_write_b64 v83, v[78:79] offset:5120
mva 30,74,92
ds_write_b64 v84, v[14:15] offset:0
mav 22,96,46
R 62,45056
s_add_u32 s49, s49, 346112
mav 26,100,46
mva 30,46,104
s_waitcnt lgkmcnt(1)
s_barrier
mav 22,108,50
buffer_load_dwordx2 v[14:15], v92, s[36:39], s49 offen sc1
R 66,46080
mav 26,112,50
s_add_u32 m0, s50, 0
mva 30,50,116
D 86,0
mav 22,120,54
R 70,47104
mav 26,124,54
D 86,1024
mva 30,54,128
mav 22,132,58
R 74,48128
mav 26,136,58
D 86,2048
mva 30,58,140
s_waitcnt lgkmcnt(2)
mav 22,144,62
R 46,49152
D 86,3072
mav 26,148,62
s_add_u32 m0, s50, 8192
mva 30,62,152
mav 22,156,66
R 50,50176
D 87,0
mav 26,160,66
mva 30,66,164
D 87,1024
s_waitcnt lgkmcnt(2)
mav 22,168,70
R 54,51200
mav 26,172,70
mva 30,70,176
D 87,2048
mav 22,180,74
R 58,52224
mav 26,184,74
D 87,3072
mva 30,74,188
s_add_u32 m0, s50, 16384
s_waitcnt lgkmcnt(2)
mav 22,192,46
R 62,53248
mav 26,196,46
D 88,0
mva 30,46,200
mav 22,204,50
R 66,54272
D 88,1024
mav 26,208,50
mva 30,50,212
s_waitcnt lgkmcnt(2)
mav 22,216,54
R 70,55296
D 88,2048
mav 26,220,54
mva 30,54,224
D 88,3072
mav 22,228,58
R 74,56320
s_add_u32 m0, s50, 24576
mav 26,232,58
mva 30,58,236
D 89,0
s_waitcnt lgkmcnt(2)
mav 22,240,62
R 46,57344
mav 26,244,62
D 89,1024
mva 30,62,248
mav 22,252,66
R 50,58368
mvv 26,128,66
D 89,2048
mvv 30,66,132
s_waitcnt lgkmcnt(2)
mvv 22,136,70
R 54,59392
D 89,3072
mvv 26,140,70
mvv 30,70,144
mvv 22,148,74
R 58,60416
mvv 26,152,74
mvv 30,74,156
s_waitcnt lgkmcnt(2)
mvv 22,160,46
R 62,61440
mvv 26,164,46
mvv 30,46,168
s_waitcnt vmcnt(16)
v_permlane32_swap_b32 v14, v15
mvv 22,172,50
R 66,62464
v_add_f32 v16, v14, v15
mvv 26,176,50
v_add_f32 v17, v14, v15
mvv 30,50,180
s_waitcnt lgkmcnt(2)
mvv 22,184,54
R 70,63488
v_permlane16_swap_b32 v16, v17
mvv 26,188,54
v_add_f32 v14, v16, v17
v_add_f32 v15, v16, v17
mvv 30,54,192
mvv 22,196,58
R 74,64512
v_permlane32_swap_b32 v14, v15
mvv 26,200,58
v_mul_f32 v16, 0x3a800000, v14
v_mul_f32 v17, 0x3a800000, v15
mvv 30,58,204
v_fma_f32 v17, -v16, v16, v17
s_waitcnt lgkmcnt(2)
mvv 22,208,62
R 46,0
v_max_f32 v17, 0, v17
mvv 26,212,62
ds_read_b64 v[118:119], v84 offset:0
ds_read_b128 v[102:105], v85 offset:0
mvv 30,62,216
ds_read_b128 v[106:109], v85 offset:16
v_add_f32 v17, 0x3727c5ac, v17
mvv 22,220,66
R 50,1024
mvv 26,224,66
v_rsq_f32 v14, v17
mvv 30,66,228
v_mul_f32_e64 v15, -v16, v14
s_waitcnt lgkmcnt(5)
mvv 22,232,70
R 54,2048
mvv 26,236,70
mvv 30,70,240
mvv 22,244,74
R 58,3072
mvv 26,248,74
mvv 30,74,252
s_waitcnt lgkmcnt(2)
v_mfma_f32_16x16x32_bf16 v[34:37], a[0:3], v[46:49], 0
R 62,4096
v_mul_f32 v120, v119, v94
v_mfma_f32_16x16x32_bf16 v[38:41], a[4:7], v[46:49], 0
v_fma_f32 v22, v22, v118, v120
v_mul_f32 v121, v119, v95
v_mfma_f32_16x16x32_bf16 v[42:45], v[46:49], a[8:11], 0
v_fma_f32 v23, v23, v118, v121
v_mul_f32 v120, v119, v96
mav 34,12,50
R 66,5120
v_fma_f32 v24, v24, v118, v120
mav 38,16,50
v_mul_f32 v121, v119, v97
v_fma_f32 v25, v25, v118, v121
mva 42,50,20
v_mul_f32 v120, v119, v98
s_waitcnt lgkmcnt(2)
mav 34,24,54
R 70,6144
v_fma_f32 v26, v26, v118, v120
mav 38,28,54
v_mul_f32 v121, v119, v99
v_fma_f32 v27, v27, v118, v121
mva 42,54,32
v_mul_f32 v120, v119, v100
v_fma_f32 v28, v28, v118, v120
mav 34,36,58
R 74,7168
v_mul_f32 v121, v119, v101
mav 38,40,58
v_fma_f32 v29, v29, v118, v121
v_cvt_pk_bf16_f32 v110, v22, v23
mva 42,58,44
v_cvt_pk_bf16_f32 v111, v24, v25
s_waitcnt lgkmcnt(2)
mav 34,48,62
R 46,8192
v_cvt_pk_bf16_f32 v112, v26, v27
mav 38,52,62
v_cvt_pk_bf16_f32 v113, v28, v29
v_mul_f32 v103, v103, v116
mva 42,62,56
ds_write_b128 v83, v[110:113] offset:0
mav 34,60,66
R 50,9216
v_fma_f32 v30, v30, v102, v103
mav 38,64,66
v_mul_f32 v105, v105, v116
v_fma_f32 v31, v31, v104, v105
mva 42,66,68
v_mul_f32 v107, v107, v116
s_waitcnt lgkmcnt(3)
mav 34,72,70
R 54,10240
v_fma_f32 v32, v32, v106, v107
mav 38,76,70
v_mul_f32 v109, v109, v116
v_fma_f32 v33, v33, v108, v109
mva 42,70,80
v_cvt_pk_bf16_f32 v78, v30, v31
v_cvt_pk_bf16_f32 v79, v32, v33
mav 34,84,74
R 58,11264
mav 38,88,74
ds_write_b64 v83, v[78:79] offset:1024
mva 42,74,92
s_waitcnt lgkmcnt(3)
mav 34,96,46
R 62,12288
s_add_u32 s49, s49, 346112
mav 38,100,46
ds_write_b64 v84, v[14:15] offset:128
mva 42,46,104
mav 34,108,50
R 66,13312
mav 38,112,50
s_waitcnt lgkmcnt(1)
s_barrier
mva 42,50,116
buffer_load_dwordx2 v[14:15], v92, s[36:39], s49 offen sc1
mav 34,120,54
R 70,14336
mav 38,124,54
mva 42,54,128
mav 34,132,58
R 74,15360
mav 38,136,58
mva 42,58,140
mav 34,144,62
R 46,16384
mav 38,148,62
mva 42,62,152
s_waitcnt lgkmcnt(2)
mav 34,156,66
R 50,17408
mav 38,160,66
mva 42,66,164
mav 34,168,70
R 54,18432
mav 38,172,70
mva 42,70,176
s_waitcnt lgkmcnt(2)
mav 34,180,74
R 58,19456
mav 38,184,74
mva 42,74,188
mav 34,192,46
R 62,20480
mav 38,196,46
mva 42,46,200
s_waitcnt lgkmcnt(2)
mav 34,204,50
R 66,21504
mav 38,208,50
mva 42,50,212
mav 34,216,54
R 70,22528
mav 38,220,54
mva 42,54,224
s_waitcnt lgkmcnt(2)
mav 34,228,58
R 74,23552
mav 38,232,58
mva 42,58,236
mav 34,240,62
R 46,24576
mav 38,244,62
mva 42,62,248
s_waitcnt lgkmcnt(2)
mav 34,252,66
R 50,25600
mvv 38,128,66
mvv 42,66,132
mvv 34,136,70
R 54,26624
mvv 38,140,70
mvv 42,70,144
s_waitcnt lgkmcnt(2)
mvv 34,148,74
R 58,27648
mvv 38,152,74
mvv 42,74,156
mvv 34,160,46
R 62,28672
mvv 38,164,46
mvv 42,46,168
s_waitcnt lgkmcnt(2)
mvv 34,172,50
R 66,29696
mvv 38,176,50
s_waitcnt vmcnt(0)
v_permlane32_swap_b32 v14, v15
mvv 42,50,180
v_add_f32 v16, v14, v15
v_add_f32 v17, v14, v15
mvv 34,184,54
R 70,30720
v_permlane16_swap_b32 v16, v17
mvv 38,188,54
v_add_f32 v14, v16, v17
v_add_f32 v15, v16, v17
mvv 42,54,192
s_waitcnt lgkmcnt(2)
mvv 34,196,58
R 74,31744
v_xor_b32 v80, 0x10000, v80
mvv 38,200,58
v_permlane32_swap_b32 v14, v15
v_mul_f32 v16, 0x3a800000, v14
mvv 42,58,204
v_mul_f32 v17, 0x3a800000, v15
v_fma_f32 v17, -v16, v16, v17
mvv 34,208,62
R 46,32768
v_max_f32 v17, 0, v17
mvv 38,212,62
ds_read_b64 v[118:119], v84 offset:128
ds_read_b128 v[102:105], v85 offset:128
mvv 42,62,216
ds_read_b128 v[106:109], v85 offset:144
s_waitcnt lgkmcnt(5)
mvv 34,220,66
R 50,33792
v_add_f32 v17, 0x3727c5ac, v17
mvv 38,224,66
v_rsq_f32 v14, v17
mvv 42,66,228
v_mul_f32_e64 v15, -v16, v14
mvv 34,232,70
R 54,34816
mvv 38,236,70
mvv 42,70,240
s_waitcnt lgkmcnt(2)
mvv 34,244,74
R 58,35840
mvv 38,248,74
mvv 42,74,252
v_mfma_f32_16x16x32_bf16 v[22:25], a[0:3], v[46:49], 0
R 62,36864
v_mul_f32 v120, v119, v94
v_mfma_f32_16x16x32_bf16 v[26:29], a[4:7], v[46:49], 0
v_mul_f32 v121, v119, v95
v_fma_f32 v34, v34, v118, v120
v_mfma_f32_16x16x32_bf16 v[30:33], v[46:49], a[8:11], 0
v_fma_f32 v35, v35, v118, v121
s_waitcnt lgkmcnt(2)
mav 22,12,50
R 66,37888
v_mul_f32 v120, v119, v96
mav 26,16,50
v_fma_f32 v36, v36, v118, v120
v_mul_f32 v121, v119, v97
mva 30,50,20
v_fma_f32 v37, v37, v118, v121
v_mul_f32 v120, v119, v98
mav 22,24,54
R 70,38912
v_fma_f32 v38, v38, v118, v120
mav 26,28,54
v_mul_f32 v121, v119, v99
v_fma_f32 v39, v39, v118, v121
mva 30,54,32
v_mul_f32 v120, v119, v100
s_waitcnt lgkmcnt(2)
mav 22,36,58
R 74,39936
v_fma_f32 v40, v40, v118, v120
mav 26,40,58
v_mul_f32 v121, v119, v101
v_fma_f32 v41, v41, v118, v121
mva 30,58,44
v_cvt_pk_bf16_f32 v110, v34, v35
v_cvt_pk_bf16_f32 v111, v36, v37
mav 22,48,62
R 46,40960
v_cvt_pk_bf16_f32 v112, v38, v39
mav 26,52,62
v_cvt_pk_bf16_f32 v113, v40, v41
v_mul_f32 v103, v103, v116
mva 30,62,56
v_fma_f32 v42, v42, v102, v103
s_waitcnt lgkmcnt(2)
mav 22,60,66
R 50,41984
v_mul_f32 v105, v105, v116
mav 26,64,66
ds_write_b128 v83, v[110:113] offset:4096
mva 30,66,68
v_fma_f32 v43, v43, v104, v105
v_mul_f32 v107, v107, v116
mav 22,72,70
R 54,43008
v_fma_f32 v44, v44, v106, v107
mav 26,76,70
v_mul_f32 v109, v109, v116
v_fma_f32 v45, v45, v108, v109
mva 30,70,80
v_cvt_pk_bf16_f32 v78, v42, v43
s_waitcnt lgkmcnt(3)
mav 22,84,74
R 58,44032
v_cvt_pk_bf16_f32 v79, v44, v45
mav 26,88,74
ds_write_b64 v83, v[78:79] offset:5120
mva 30,74,92
ds_write_b64 v84, v[14:15] offset:0
mav 22,96,46
R 62,45056
s_add_u32 s49, s49, 346112
mav 26,100,46
mva 30,46,104
s_waitcnt lgkmcnt(1)
s_barrier
mav 22,108,50
buffer_load_dwordx2 v[14:15], v92, s[36:39], s49 offen sc1
R 66,46080
mav 26,112,50
mva 30,50,116
mav 22,120,54
R 70,47104
mav 26,124,54
mva 30,54,128
mav 22,132,58
R 74,48128
mav 26,136,58
mva 30,58,140
s_waitcnt lgkmcnt(2)
mav 22,144,62
R 46,49152
mav 26,148,62
mva 30,62,152
mav 22,156,66
R 50,50176
mav 26,160,66
mva 30,66,164
s_waitcnt lgkmcnt(2)
mav 22,168,70
R 54,51200
mav 26,172,70
mva 30,70,176
mav 22,180,74
R 58,52224
mav 26,184,74
mva 30,74,188
s_waitcnt lgkmcnt(2)
mav 22,192,46
R 62,53248
mav 26,196,46
mva 30,46,200
mav 22,204,50
R 66,54272
mav 26,208,50
mva 30,50,212
s_waitcnt lgkmcnt(2)
mav 22,216,54
R 70,55296
mav 26,220,54
mva 30,54,224
mav 22,228,58
R 74,56320
mav 26,232,58
mva 30,58,236
s_waitcnt lgkmcnt(2)
mav 22,240,62
R 46,57344
mav 26,244,62
mva 30,62,248
mav 22,252,66
R 50,58368
mvv 26,128,66
mvv 30,66,132
s_waitcnt lgkmcnt(2)
mvv 22,136,70
R 54,59392
mvv 26,140,70
mvv 30,70,144
mvv 22,148,74
R 58,60416
mvv 26,152,74
mvv 30,74,156
s_waitcnt lgkmcnt(2)
mvv 22,160,46
R 62,61440
mvv 26,164,46
mvv 30,46,168
s_waitcnt vmcnt(0)
v_permlane32_swap_b32 v14, v15
mvv 22,172,50
R 66,62464
v_add_f32 v16, v14, v15
mvv 26,176,50
v_add_f32 v17, v14, v15
mvv 30,50,180
s_waitcnt lgkmcnt(2)
mvv 22,184,54
R 70,63488
v_permlane16_swap_b32 v16, v17
mvv 26,188,54
v_add_f32 v14, v16, v17
v_add_f32 v15, v16, v17
mvv 30,54,192
mvv 22,196,58
R 74,64512
v_permlane32_swap_b32 v14, v15
mvv 26,200,58
v_mul_f32 v16, 0x3a800000, v14
v_mul_f32 v17, 0x3a800000, v15
mvv 30,58,204
v_fma_f32 v17, -v16, v16, v17
s_waitcnt lgkmcnt(2)
mvv 22,208,62
R 46,0
v_max_f32 v17, 0, v17
mvv 26,212,62
ds_read_b64 v[118:119], v84 offset:0
ds_read_b128 v[102:105], v85 offset:0
mvv 30,62,216
ds_read_b128 v[106:109], v85 offset:16
v_add_f32 v17, 0x3727c5ac, v17
mvv 22,220,66
R 50,1024
mvv 26,224,66
v_rsq_f32 v14, v17
mvv 30,66,228
v_mul_f32_e64 v15, -v16, v14
s_waitcnt lgkmcnt(5)
mvv 22,232,70
R 54,2048
mvv 26,236,70
mvv 30,70,240
mvv 22,244,74
R 58,3072
mvv 26,248,74
mvv 30,74,252
s_waitcnt lgkmcnt(2)
v_mfma_f32_16x16x32_bf16 v[34:37], a[0:3], v[46:49], 0
R 62,4096
v_mul_f32 v120, v119, v94
v_mfma_f32_16x16x32_bf16 v[38:41], a[4:7], v[46:49], 0
v_fma_f32 v22, v22, v118, v120
v_mul_f32 v121, v119, v95
v_mfma_f32_16x16x32_bf16 v[42:45], v[46:49], a[8:11], 0
v_fma_f32 v23, v23, v118, v121
v_mul_f32 v120, v119, v96
mav 34,12,50
R 66,5120
v_fma_f32 v24, v24, v118, v120
mav 38,16,50
v_mul_f32 v121, v119, v97
v_fma_f32 v25, v25, v118, v121
mva 42,50,20
v_mul_f32 v120, v119, v98
s_waitcnt lgkmcnt(2)
mav 34,24,54
R 70,6144
v_fma_f32 v26, v26, v118, v120
mav 38,28,54
v_mul_f32 v121, v119, v99
v_fma_f32 v27, v27, v118, v121
mva 42,54,32
v_mul_f32 v120, v119, v100
v_fma_f32 v28, v28, v118, v120
mav 34,36,58
R 74,7168
v_mul_f32 v121, v119, v101
mav 38,40,58
v_fma_f32 v29, v29, v118, v121
v_cvt_pk_bf16_f32 v110, v22, v23
mva 42,58,44
v_cvt_pk_bf16_f32 v111, v24, v25
s_waitcnt lgkmcnt(2)
mav 34,48,62
R 46,8192
v_cvt_pk_bf16_f32 v112, v26, v27
mav 38,52,62
v_cvt_pk_bf16_f32 v113, v28, v29
v_mul_f32 v103, v103, v116
mva 42,62,56
ds_write_b128 v83, v[110:113] offset:0
mav 34,60,66
R 50,9216
v_fma_f32 v30, v30, v102, v103
mav 38,64,66
v_mul_f32 v105, v105, v116
v_fma_f32 v31, v31, v104, v105
mva 42,66,68
v_mul_f32 v107, v107, v116
s_waitcnt lgkmcnt(3)
mav 34,72,70
R 54,10240
v_fma_f32 v32, v32, v106, v107
mav 38,76,70
v_mul_f32 v109, v109, v116
v_fma_f32 v33, v33, v108, v109
mva 42,70,80
v_cvt_pk_bf16_f32 v78, v30, v31
v_cvt_pk_bf16_f32 v79, v32, v33
mav 34,84,74
R 58,11264
mav 38,88,74
ds_write_b64 v83, v[78:79] offset:1024
mva 42,74,92
s_waitcnt lgkmcnt(3)
mav 34,96,46
R 62,12288
mav 38,100,46
ds_write_b64 v84, v[14:15] offset:128
mva 42,46,104
mav 34,108,50
R 66,13312
mav 38,112,50
s_waitcnt lgkmcnt(1)
s_barrier
mva 42,50,116
mav 34,120,54
R 70,14336
mav 38,124,54
mva 42,54,128
mav 34,132,58
R 74,15360
mav 38,136,58
mva 42,58,140
mav 34,144,62
R 46,16384
mav 38,148,62
mva 42,62,152
s_waitcnt lgkmcnt(2)
mav 34,156,66
R 50,17408
mav 38,160,66
mva 42,66,164
mav 34,168,70
R 54,18432
mav 38,172,70
mva 42,70,176
s_waitcnt lgkmcnt(2)
mav 34,180,74
R 58,19456
mav 38,184,74
mva 42,74,188
mav 34,192,46
R 62,20480
mav 38,196,46
mva 42,46,200
s_waitcnt lgkmcnt(2)
mav 34,204,50
R 66,21504
mav 38,208,50
mva 42,50,212
mav 34,216,54
R 70,22528
mav 38,220,54
mva 42,54,224
s_waitcnt lgkmcnt(2)
mav 34,228,58
R 74,23552
mav 38,232,58
mva 42,58,236
mav 34,240,62
R 46,24576
mav 38,244,62
mva 42,62,248
s_waitcnt lgkmcnt(2)
mav 34,252,66
R 50,25600
mvv 38,128,66
mvv 42,66,132
mvv 34,136,70
R 54,26624
mvv 38,140,70
mvv 42,70,144
s_waitcnt lgkmcnt(2)
mvv 34,148,74
R 58,27648
mvv 38,152,74
mvv 42,74,156
mvv 34,160,46
R 62,28672
mvv 38,164,46
mvv 42,46,168
s_waitcnt lgkmcnt(2)
mvv 34,172,50
R 66,29696
mvv 38,176,50
mvv 42,50,180
mvv 34,184,54
R 70,30720
mvv 38,188,54
mvv 42,54,192
s_waitcnt lgkmcnt(2)
mvv 34,196,58
R 74,31744
mvv 38,200,58
mvv 42,58,204
mvv 34,208,62
mvv 38,212,62
ds_read_b64 v[118:119], v84 offset:128
ds_read_b128 v[102:105], v85 offset:128
mvv 42,62,216
ds_read_b128 v[106:109], v85 offset:144
s_waitcnt lgkmcnt(4)
mvv 34,220,66
mvv 38,224,66
mvv 42,66,228
mvv 34,232,70
mvv 38,236,70
mvv 42,70,240
s_waitcnt lgkmcnt(0)
mvv 34,244,74
mvv 38,248,74
mvv 42,74,252
v_mul_f32 v120, v119, v94
s_nop 4
v_fma_f32 v34, v34, v118, v120
v_mul_f32 v121, v119, v95
v_fma_f32 v35, v35, v118, v121
v_mul_f32 v120, v119, v96
v_fma_f32 v36, v36, v118, v120
v_mul_f32 v121, v119, v97
v_fma_f32 v37, v37, v118, v121
v_mul_f32 v120, v119, v98
v_fma_f32 v38, v38, v118, v120
v_mul_f32 v121, v119, v99
v_fma_f32 v39, v39, v118, v121
v_mul_f32 v120, v119, v100
v_fma_f32 v40, v40, v118, v120
v_mul_f32 v121, v119, v101
v_fma_f32 v41, v41, v118, v121
v_cvt_pk_bf16_f32 v110, v34, v35
v_cvt_pk_bf16_f32 v111, v36, v37
v_cvt_pk_bf16_f32 v112, v38, v39
v_cvt_pk_bf16_f32 v113, v40, v41
ds_write_b128 v83, v[110:113] offset:4096
v_mul_f32 v103, v103, v116
v_fma_f32 v42, v42, v102, v103
v_mul_f32 v105, v105, v116
v_fma_f32 v43, v43, v104, v105
v_mul_f32 v107, v107, v116
v_fma_f32 v44, v44, v106, v107
v_mul_f32 v109, v109, v116
v_fma_f32 v45, v45, v108, v109
v_cvt_pk_bf16_f32 v78, v42, v43
v_cvt_pk_bf16_f32 v79, v44, v45
ds_write_b64 v83, v[78:79] offset:5120
s_waitcnt lgkmcnt(0)
s_barrier
s_endpgm
Cq:
s_cmp_eq_u32 s61, 0
s_cbranch_scc1 Bq
s_sub_u32 s61, s61, 1
s_sleep 2
buffer_load_dwordx2 v[122:123], v90, s[36:39], 0 offen sc1
s_waitcnt vmcnt(0)
ds_write_b32 v82, v122
v_sub_u32 v122, v122, v91
v_xor_b32 v123, v123, v115
v_cmp_lt_u64 vcc, s[62:63], v[122:123]
s_cbranch_vccnz Cq
s_branch Bq
Lr2:
s_cmp_eq_u32 s60, 2
s_cbranch_scc0 Lr3
buffer_load_dword v71, off, s[36:39], 0 sc1
E 0,65536
E 1,69632
E 2,73728
s_add_u32 s51, s51, 65536
E 0,77824
E 1,81920
E 2,86016
s_add_u32 s51, s51, 65536
E 0,90112
E 1,94208
E 2,98304
s_add_u32 s51, s51, 65536
E 0,102400
E 1,106496
E 2,110592
s_add_u32 s51, s51, 65536
s_waitcnt vmcnt(6)
s_barrier
v_add_u32 v4, 65536, v4
v_add_u32 v5, 65536, v5
v_add_u32 v6, 65536, v6
Ta 0,4,0
Ta 4,5,0
Ta 8,6,0
Ta 12,4,12288
Ta 16,5,12288
Ta 20,6,12288
v_mov_b32 v70, 0x100
v_add_u32 v71, 0x9e3779b9, v71
buffer_store_dwordx2 v[70:71], v81, s[36:39], s52 offen sc1
v_add_u32 v88, v88, v70
E 0,114688
E 1,118784
E 2,122880
s_add_u32 s51, s51, 65536
E 0,126976
E 1,131072
E 2,135168
s_add_u32 s51, s51, 65536
E 0,139264
E 1,143360
E 2,147456
s_add_u32 s51, s51, 65536
E 0,151552
E 1,155648
E 2,159744
s_add_u32 s51, s51, 65536
s_cmp_ge_u32 s51, s57
s_cselect_b32 s60, 2097152, 0
s_sub_u32 s51, s51, s60
s_waitcnt lgkmcnt(0)
s_waitcnt vmcnt(13)
s_barrier
E 0,65536
Ta 24,4,24576
Ta 28,5,24576
E 1,69632
Ta 32,6,24576
Ta 36,4,36864
E 2,73728
s_add_u32 s51, s51, 65536
Ta 40,5,36864
E 0,77824
Ta 44,6,36864
E 1,81920
E 2,86016
s_add_u32 s51, s51, 65536
s_waitcnt lgkmcnt(0)
s_waitcnt vmcnt(12)
s_barrier
v_add_u32 v4, 49152, v4
v_add_u32 v5, 49152, v5
v_add_u32 v6, 49152, v6
E 0,90112
Ta 48,4,0
Ta 52,5,0
E 1,94208
Ta 56,6,0
Ta 60,4,12288
E 2,98304
s_add_u32 s51, s51, 65536
Ta 64,5,12288
E 0,102400
Ta 68,6,12288
E 1,106496
E 2,110592
s_add_u32 s51, s51, 65536
s_waitcnt lgkmcnt(0)
s_waitcnt vmcnt(12)
s_barrier
E 0,114688
Ta 72,4,24576
Ta 76,5,24576
E 1,118784
Ta 80,6,24576
Ta 84,4,36864
E 2,122880
s_add_u32 s51, s51, 65536
Ta 88,5,36864
E 0,126976
Ta 92,6,36864
E 1,131072
E 2,135168
s_add_u32 s51, s51, 65536
s_waitcnt lgkmcnt(0)
s_waitcnt vmcnt(12)
s_barrier
v_add_u32 v4, -49152, v4
v_add_u32 v5, -49152, v5
v_add_u32 v6, -49152, v6
E 0,139264
Ta 96,4,0
Ta 100,5,0
E 1,143360
Ta 104,6,0
Ta 108,4,12288
E 2,147456
s_add_u32 s51, s51, 65536
Ta 112,5,12288
E 0,151552
Ta 116,6,12288
E 1,155648
E 2,159744
s_add_u32 s51, s51, 65536
s_cmp_ge_u32 s51, s57
s_cselect_b32 s60, 2097152, 0
s_sub_u32 s51, s51, s60
s_waitcnt lgkmcnt(0)
s_waitcnt vmcnt(12)
s_barrier
E 0,65536
Ta 120,4,24576
Ta 124,5,24576
E 1,69632
Ta 128,6,24576
Ta 132,4,36864
E 2,73728
s_add_u32 s51, s51, 65536
Ta 136,5,36864
E 0,77824
Ta 140,6,36864
E 1,81920
E 2,86016
s_add_u32 s51, s51, 65536
s_waitcnt lgkmcnt(0)
s_waitcnt vmcnt(12)
s_barrier
v_add_u32 v4, 49152, v4
v_add_u32 v5, 49152, v5
v_add_u32 v6, 49152, v6
E 0,90112
Ta 144,4,0
Ta 148,5,0
E 1,94208
Ta 152,6,0
Ta 156,4,12288
E 2,98304
s_add_u32 s51, s51, 65536
Ta 160,5,12288
E 0,102400
Ta 164,6,12288
E 1,106496
E 2,110592
s_add_u32 s51, s51, 65536
s_waitcnt lgkmcnt(0)
s_waitcnt vmcnt(12)
s_barrier
E 0,114688
Ta 168,4,24576
Ta 172,5,24576
E 1,118784
Ta 176,6,24576
Ta 180,4,36864
E 2,122880
s_add_u32 s51, s51, 65536
Ta 184,5,36864
E 0,126976
Ta 188,6,36864
E 1,131072
E 2,135168
s_add_u32 s51, s51, 65536
s_waitcnt lgkmcnt(0)
s_waitcnt vmcnt(12)
s_barrier
v_add_u32 v4, -49152, v4
v_add_u32 v5, -49152, v5
v_add_u32 v6, -49152, v6
E 0,139264
Ta 192,4,0
Ta 196,5,0
E 1,143360
Ta 200,6,0
Ta 204,4,12288
E 2,147456
s_add_u32 s51, s51, 65536
Ta 208,5,12288
E 0,151552
Ta 212,6,12288
E 1,155648
E 2,159744
s_add_u32 s51, s51, 65536
s_cmp_ge_u32 s51, s57
s_cselect_b32 s60, 2097152, 0
s_sub_u32 s51, s51, s60
s_waitcnt lgkmcnt(0)
s_waitcnt vmcnt(12)
s_barrier
E 0,65536
Ta 216,4,24576
Ta 220,5,24576
E 1,69632
Ta 224,6,24576
Ta 228,4,36864
E 2,73728
s_add_u32 s51, s51, 65536
Ta 232,5,36864
E 0,77824
Ta 236,6,36864
E 1,81920
E 2,86016
s_add_u32 s51, s51, 65536
s_waitcnt lgkmcnt(0)
s_waitcnt vmcnt(12)
s_barrier
v_add_u32 v4, 49152, v4
v_add_u32 v5, 49152, v5
v_add_u32 v6, 49152, v6
E 0,90112
Ta 240,4,0
Ta 244,5,0
E 1,94208
Ta 248,6,0
Ta 252,4,12288
E 2,98304
s_add_u32 s51, s51, 65536
Tv 128,5,12288
E 0,102400
Tv 132,6,12288
E 1,106496
E 2,110592
s_add_u32 s51, s51, 65536
s_waitcnt lgkmcnt(0)
s_waitcnt vmcnt(12)
s_barrier
E 0,114688
Tv 136,4,24576
Tv 140,5,24576
E 1,118784
Tv 144,6,24576
Tv 148,4,36864
E 2,122880
s_add_u32 s51, s51, 65536
Tv 152,5,36864
E 0,126976
Tv 156,6,36864
E 1,131072
E 2,135168
s_add_u32 s51, s51, 65536
s_waitcnt lgkmcnt(0)
s_waitcnt vmcnt(12)
s_barrier
v_add_u32 v4, -49152, v4
v_add_u32 v5, -49152, v5
v_add_u32 v6, -49152, v6
E 0,139264
Tv 160,4,0
Tv 164,5,0
E 1,143360
Tv 168,6,0
Tv 172,4,12288
E 2,147456
s_add_u32 s51, s51, 65536
Tv 176,5,12288
E 0,151552
Tv 180,6,12288
E 1,155648
E 2,159744
s_add_u32 s51, s51, 65536
s_cmp_ge_u32 s51, s57
s_cselect_b32 s60, 2097152, 0
s_sub_u32 s51, s51, s60
s_waitcnt lgkmcnt(0)
s_waitcnt vmcnt(12)
s_barrier
Tv 184,4,24576
Tv 188,5,24576
Tv 192,6,24576
Tv 196,4,36864
Tv 200,5,36864
Tv 204,6,36864
s_waitcnt lgkmcnt(0)
s_waitcnt vmcnt(6)
s_barrier
v_add_u32 v4, 49152, v4
v_add_u32 v5, 49152, v5
v_add_u32 v6, 49152, v6
Tv 208,4,0
Tv 212,5,0
Tv 216,6,0
Tv 220,4,12288
Tv 224,5,12288
Tv 228,6,12288
s_waitcnt lgkmcnt(0)
s_waitcnt vmcnt(0)
s_barrier
Tv 232,4,24576
Tv 236,5,24576
Tv 240,6,24576
Tv 244,4,36864
Tv 248,5,36864
Tv 252,6,36864
s_waitcnt lgkmcnt(0)
s_waitcnt vmcnt(0)
s_barrier
s_nop 7
s_and_b32 s60, s48, 3
s_cmp_eq_u32 s60, 1
s_cbranch_scc1 LvO1
s_cmp_eq_u32 s60, 2
s_cbranch_scc1 LvO2
LvO0:
buffer_load_dwordx2 v[74:75], v87, s[36:39], 0 offen sc1
s_waitcnt vmcnt(0)
ds_write_b32 v82, v74
v_sub_u32 v74, v74, v88
v_xor_b32 v75, v75, v71
v_cmp_lt_u64 vcc, s[62:63], v[74:75]
s_waitcnt lgkmcnt(0)
s_cbranch_vccnz CO0_1
BO0_1:
s_barrier
buffer_load_dwordx4 v[56:59], v89, s[36:39], s49 offen sc1
buffer_load_dwordx2 v[60:61], v90, s[36:39], s49 offen sc1
s_add_u32 s49, s49, 346112
s_add_u32 m0, s50, 0
s_nop 0
D 85,0
D 85,1024
D 85,2048
D 85,3072
s_add_u32 m0, s50, 4096
s_nop 0
D 86,0
D 86,1024
D 86,2048
D 86,3072
s_mov_b32 m0, 142336
s_nop 0
buffer_load_dword v87, s[36:39], 0 offen sc1 lds
v_add_u32 v88, 1, v88
buffer_load_dword v87, s[36:39], 0 offen sc1 lds
s_waitcnt vmcnt(11)
v_lshlrev_b32 v0, 16, v56
v_and_b32 v1, 0xffff0000, v56
v_lshlrev_b32 v2, 16, v57
v_and_b32 v3, 0xffff0000, v57
v_lshlrev_b32 v4, 16, v58
v_and_b32 v5, 0xffff0000, v58
v_lshlrev_b32 v6, 16, v59
v_and_b32 v7, 0xffff0000, v59
s_waitcnt vmcnt(10)
v_lshlrev_b32 v8, 16, v60
v_and_b32 v9, 0xffff0000, v60
v_lshlrev_b32 v10, 16, v61
v_and_b32 v11, 0xffff0000, v61
buffer_load_dword v87, s[36:39], 0 offen sc1 lds
buffer_load_dwordx4 v[56:59], v89, s[36:39], s49 offen sc1
buffer_load_dwordx2 v[60:61], v90, s[36:39], s49 offen sc1
ds_read_b32 v74, v82 offset:0
s_waitcnt lgkmcnt(0)
v_cmp_gt_i32 vcc, v88, v74
s_waitcnt vmcnt(5)
s_cbranch_vccnz CO0_2
BO0_2:
s_add_u32 s49, s49, 346112
s_barrier
s_add_u32 m0, s50, 32768
R 24,0
D 85,0
R 28,1024
D 85,1024
R 32,2048
R 36,3072
D 85,2048
s_waitcnt lgkmcnt(2)
mav 0,0,24
R 40,4096
mav 4,4,24
mav 8,8,24
D 85,3072
mav 0,12,28
R 44,5120
mav 4,16,28
mav 8,20,28
s_add_u32 m0, s50, 36864
s_waitcnt lgkmcnt(2)
mav 0,24,32
R 48,6144
mav 4,28,32
D 86,0
mav 8,32,32
mav 0,36,36
R 52,7168
mav 4,40,36
mav 8,44,36
D 86,1024
s_waitcnt lgkmcnt(2)
mav 0,48,40
R 24,8192
mav 4,52,40
mav 8,56,40
mav 0,60,44
R 28,9216
mav 4,64,44
D 86,2048
mav 8,68,44
s_waitcnt lgkmcnt(2)
mav 0,72,48
R 32,10240
mav 4,76,48
mav 8,80,48
D 86,3072
mav 0,84,52
R 36,11264
mav 4,88,52
mav 8,92,52
s_waitcnt lgkmcnt(2)
mav 0,96,24
R 40,12288
mav 4,100,24
mav 8,104,24
mav 0,108,28
R 44,13312
mav 4,112,28
mav 8,116,28
s_waitcnt lgkmcnt(2)
mav 0,120,32
R 48,14336
s_mov_b32 m0, 142336
mav 4,124,32
mav 8,128,32
buffer_load_dword v87, s[36:39], 0 offen sc1 lds
mav 0,132,36
R 52,15360
mav 4,136,36
mav 8,140,36
s_waitcnt lgkmcnt(2)
mav 0,144,40
R 24,16384
mav 4,148,40
mav 8,152,40
mav 0,156,44
R 28,17408
mav 4,160,44
v_add_u32 v88, 1, v88
mav 8,164,44
s_waitcnt lgkmcnt(2)
mav 0,168,48
R 32,18432
mav 4,172,48
mav 8,176,48
buffer_load_dword v87, s[36:39], 0 offen sc1 lds
mav 0,180,52
R 36,19456
mav 4,184,52
mav 8,188,52
s_waitcnt lgkmcnt(2)
mav 0,192,24
R 40,20480
mav 4,196,24
mav 8,200,24
s_waitcnt vmcnt(11)
v_lshlrev_b32 v12, 16, v56
mav 0,204,28
R 44,21504
v_and_b32 v13, 0xffff0000, v56
mav 4,208,28
v_lshlrev_b32 v14, 16, v57
v_and_b32 v15, 0xffff0000, v57
mav 8,212,28
v_lshlrev_b32 v16, 16, v58
s_waitcnt lgkmcnt(2)
mav 0,216,32
R 48,22528
v_and_b32 v17, 0xffff0000, v58
mav 4,220,32
v_lshlrev_b32 v18, 16, v59
v_and_b32 v19, 0xffff0000, v59
mav 8,224,32
buffer_load_dword v87, s[36:39], 0 offen sc1 lds
mav 0,228,36
R 52,23552
mav 4,232,36
s_waitcnt vmcnt(11)
v_lshlrev_b32 v20, 16, v60
mav 8,236,36
v_and_b32 v21, 0xffff0000, v60
s_waitcnt lgkmcnt(2)
mav 0,240,40
R 24,24576
v_lshlrev_b32 v22, 16, v61
mav 4,244,40
v_and_b32 v23, 0xffff0000, v61
mav 8,248,40
mav 0,252,44
R 28,25600
mvv 4,128,44
buffer_load_dwordx4 v[56:59], v89, s[36:39], s49 offen sc1
mvv 8,132,44
s_waitcnt lgkmcnt(2)
mvv 0,136,48
R 32,26624
mvv 4,140,48
mvv 8,144,48
buffer_load_dwordx2 v[60:61], v90, s[36:39], s49 offen sc1
mvv 0,148,52
R 36,27648
mvv 4,152,52
mvv 8,156,52
s_waitcnt lgkmcnt(2)
mvv 0,160,24
R 40,28672
mvv 4,164,24
ds_read_b32 v74, v82 offset:0
mvv 8,168,24
mvv 0,172,28
R 44,29696
mvv 4,176,28
mvv 8,180,28
s_waitcnt lgkmcnt(3)
mvv 0,184,32
R 48,30720
s_xor_b32 s50, s50, 0x10000
mvv 4,188,32
mvv 8,192,32
mvv 0,196,36
R 52,31744
mvv 4,200,36
s_waitcnt lgkmcnt(2)
v_cmp_gt_i32 vcc, v88, v74
mvv 8,204,36
s_waitcnt vmcnt(5)
s_cbranch_vccnz CO0_3
BO0_3:
mvv 0,208,40
s_barrier
R 24,32768
mvv 4,212,40
s_add_u32 s49, s49, 346112
s_add_u32 m0, s50, 0
mvv 8,216,40
D 85,0
mvv 0,220,44
R 28,33792
mvv 4,224,44
mvv 8,228,44
s_waitcnt lgkmcnt(2)
mvv 0,232,48
R 32,34816
D 85,1024
mvv 4,236,48
mvv 8,240,48
mvv 0,244,52
R 36,35840
mvv 4,248,52
D 85,2048
mvv 8,252,52
s_waitcnt lgkmcnt(2)
mav 12,0,24
R 40,36864
mav 16,4,24
v_cvt_pk_bf16_f32 v62, v0, v1
v_cvt_pk_bf16_f32 v63, v2, v3
mav 20,8,24
D 85,3072
v_cvt_pk_bf16_f32 v64, v4, v5
mav 12,12,28
R 44,37888
v_cvt_pk_bf16_f32 v65, v6, v7
mav 16,16,28
v_cvt_pk_bf16_f32 v66, v8, v9
v_cvt_pk_bf16_f32 v67, v10, v11
mav 20,20,28
s_add_u32 m0, s50, 4096
s_waitcnt lgkmcnt(2)
mav 12,24,32
R 48,38912
buffer_store_dwordx4 v[62:65], v83, s[36:39], s49 offen sc1
mav 16,28,32
v_cvt_pk_bf16_f32 v68, v8, v9
v_cvt_pk_bf16_f32 v69, v10, v11
mav 20,32,32
v_permlane16_swap_b32 v66, v68
v_permlane16_swap_b32 v67, v69
mav 12,36,36
R 52,39936
v_mul_f32 v73, v0, v0
mav 16,40,36
buffer_store_dwordx4 v[66:69], v84, s[36:39], s49 offen sc1
v_add_f32 v72, v1, v0
mav 20,44,36
v_fmac_f32 v73, v1, v1
s_waitcnt lgkmcnt(2)
mav 12,48,40
R 24,40960
v_add_f32 v72, v2, v72
mav 16,52,40
v_fmac_f32 v73, v2, v2
v_add_f32 v72, v3, v72
mav 20,56,40
D 86,0
v_fmac_f32 v73, v3, v3
mav 12,60,44
R 28,41984
v_add_f32 v72, v4, v72
mav 16,64,44
v_fmac_f32 v73, v4, v4
v_add_f32 v72, v5, v72
mav 20,68,44
v_fmac_f32 v73, v5, v5
s_waitcnt lgkmcnt(2)
mav 12,72,48
R 32,43008
D 86,1024
mav 16,76,48
v_add_f32 v72, v6, v72
v_fmac_f32 v73, v6, v6
mav 20,80,48
v_add_f32 v72, v7, v72
v_fmac_f32 v73, v7, v7
mav 12,84,52
R 36,44032
v_add_f32 v72, v8, v72
mav 16,88,52
D 86,2048
v_fmac_f32 v73, v8, v8
mav 20,92,52
v_add_f32 v72, v9, v72
s_waitcnt lgkmcnt(2)
mav 12,96,24
R 40,45056
v_fmac_f32 v73, v9, v9
mav 16,100,24
v_add_f32 v72, v10, v72
v_fmac_f32 v73, v10, v10
mav 20,104,24
D 86,3072
v_add_f32 v72, v11, v72
mav 12,108,28
R 44,46080
v_fmac_f32 v73, v11, v11
mav 16,112,28
mav 20,116,28
v_permlane32_swap_b32 v72, v73
s_waitcnt lgkmcnt(2)
mav 12,120,32
R 48,47104
s_mov_b32 m0, 142336
mav 16,124,32
v_add_f32 v72, v72, v73
mav 20,128,32
buffer_load_dword v87, s[36:39], 0 offen sc1 lds
mav 12,132,36
R 52,48128
mav 16,136,36
mav 20,140,36
s_waitcnt lgkmcnt(2)
mav 12,144,40
R 24,49152
mav 16,148,40
mav 20,152,40
mav 12,156,44
R 28,50176
mav 16,160,44
v_add_u32 v88, 1, v88
mav 20,164,44
s_waitcnt lgkmcnt(2)
mav 12,168,48
R 32,51200
mav 16,172,48
mav 20,176,48
buffer_load_dword v87, s[36:39], 0 offen sc1 lds
mav 12,180,52
R 36,52224
mav 16,184,52
mav 20,188,52
s_waitcnt lgkmcnt(2)
mav 12,192,24
R 40,53248
mav 16,196,24
mav 20,200,24
s_waitcnt vmcnt(13)
v_lshlrev_b32 v0, 16, v56
mav 12,204,28
R 44,54272
v_and_b32 v1, 0xffff0000, v56
mav 16,208,28
v_lshlrev_b32 v2, 16, v57
v_and_b32 v3, 0xffff0000, v57
mav 20,212,28
v_lshlrev_b32 v4, 16, v58
s_waitcnt lgkmcnt(2)
mav 12,216,32
R 48,55296
v_and_b32 v5, 0xffff0000, v58
mav 16,220,32
v_lshlrev_b32 v6, 16, v59
v_and_b32 v7, 0xffff0000, v59
mav 20,224,32
buffer_load_dword v87, s[36:39], 0 offen sc1 lds
mav 12,228,36
R 52,56320
mav 16,232,36
s_waitcnt vmcnt(13)
v_lshlrev_b32 v8, 16, v60
mav 20,236,36
v_and_b32 v9, 0xffff0000, v60
s_waitcnt lgkmcnt(2)
mav 12,240,40
R 24,57344
v_lshlrev_b32 v10, 16, v61
mav 16,244,40
v_and_b32 v11, 0xffff0000, v61
mav 20,248,40
mav 12,252,44
R 28,58368
mvv 16,128,44
buffer_load_dwordx4 v[56:59], v89, s[36:39], s49 offen sc1
v_add_u32 v70, 1, v70
mvv 20,132,44
s_waitcnt lgkmcnt(2)
mvv 12,136,48
R 32,59392
mvv 16,140,48
mvv 20,144,48
buffer_load_dwordx2 v[60:61], v90, s[36:39], s49 offen sc1
mvv 12,148,52
R 36,60416
mvv 16,152,52
s_waitcnt vmcnt(9)
buffer_store_dwordx2 v[70:71], v81, s[36:39], s52 offen sc1
mvv 20,156,52
s_waitcnt lgkmcnt(2)
mvv 12,160,24
R 40,61440
mvv 16,164,24
ds_read_b32 v74, v82 offset:0
mvv 20,168,24
ds_write_b32 v91, v72 offset:0
mvv 12,172,28
R 44,62464
mvv 16,176,28
mvv 20,180,28
s_waitcnt lgkmcnt(4)
mvv 12,184,32
R 48,63488
mvv 16,188,32
mvv 20,192,32
mvv 12,196,36
R 52,64512
v_xor_b32 v80, 0x10000, v80
mvv 16,200,36
s_waitcnt lgkmcnt(2)
v_cmp_gt_i32 vcc, v88, v74
mvv 20,204,36
s_waitcnt vmcnt(6)
s_cbranch_vccnz CO0_4
BO0_4:
mvv 12,208,40
s_barrier
R 24,0
mvv 16,212,40
s_add_u32 s49, s49, 346112
s_add_u32 m0, s50, 32768
mvv 20,216,40
D 85,0
mvv 12,220,44
R 28,1024
mvv 16,224,44
mvv 20,228,44
s_waitcnt lgkmcnt(2)
mvv 12,232,48
R 32,2048
D 85,1024
mvv 16,236,48
mvv 20,240,48
mvv 12,244,52
R 36,3072
mvv 16,248,52
D 85,2048
mvv 20,252,52
s_mov_b32 s60, -30
.p2align 6
LO0_5:
s_waitcnt lgkmcnt(2)
mav 0,0,24
R 40,4096
mav 4,4,24
v_cvt_pk_bf16_f32 v62, v12, v13
v_cvt_pk_bf16_f32 v63, v14, v15
mav 8,8,24
D 85,3072
v_cvt_pk_bf16_f32 v64, v16, v17
mav 0,12,28
R 44,5120
v_cvt_pk_bf16_f32 v65, v18, v19
mav 4,16,28
v_cvt_pk_bf16_f32 v66, v20, v21
v_cvt_pk_bf16_f32 v67, v22, v23
mav 8,20,28
s_add_u32 m0, s50, 36864
s_waitcnt lgkmcnt(2)
mav 0,24,32
R 48,6144
buffer_store_dwordx4 v[62:65], v83, s[36:39], s49 offen sc1
mav 4,28,32
v_cvt_pk_bf16_f32 v68, v20, v21
v_cvt_pk_bf16_f32 v69, v22, v23
mav 8,32,32
v_permlane16_swap_b32 v66, v68
v_permlane16_swap_b32 v67, v69
mav 0,36,36
R 52,7168
v_mul_f32 v73, v12, v12
mav 4,40,36
buffer_store_dwordx4 v[66:69], v84, s[36:39], s49 offen sc1
v_add_f32 v72, v13, v12
mav 8,44,36
v_fmac_f32 v73, v13, v13
s_waitcnt lgkmcnt(2)
mav 0,48,40
R 24,8192
v_add_f32 v72, v14, v72
mav 4,52,40
v_fmac_f32 v73, v14, v14
v_add_f32 v72, v15, v72
mav 8,56,40
D 86,0
v_fmac_f32 v73, v15, v15
mav 0,60,44
R 28,9216
v_add_f32 v72, v16, v72
mav 4,64,44
v_fmac_f32 v73, v16, v16
v_add_f32 v72, v17, v72
mav 8,68,44
v_fmac_f32 v73, v17, v17
s_waitcnt lgkmcnt(2)
mav 0,72,48
R 32,10240
D 86,1024
mav 4,76,48
v_add_f32 v72, v18, v72
v_fmac_f32 v73, v18, v18
mav 8,80,48
v_add_f32 v72, v19, v72
v_fmac_f32 v73, v19, v19
mav 0,84,52
R 36,11264
v_add_f32 v72, v20, v72
mav 4,88,52
D 86,2048
v_fmac_f32 v73, v20, v20
mav 8,92,52
v_add_f32 v72, v21, v72
s_waitcnt lgkmcnt(2)
mav 0,96,24
R 40,12288
v_fmac_f32 v73, v21, v21
mav 4,100,24
v_add_f32 v72, v22, v72
v_fmac_f32 v73, v22, v22
mav 8,104,24
D 86,3072
v_add_f32 v72, v23, v72
mav 0,108,28
R 44,13312
v_fmac_f32 v73, v23, v23
mav 4,112,28
mav 8,116,28
v_permlane32_swap_b32 v72, v73
s_waitcnt lgkmcnt(2)
mav 0,120,32
R 48,14336
s_mov_b32 m0, 142336
mav 4,124,32
v_add_f32 v72, v72, v73
mav 8,128,32
buffer_load_dword v87, s[36:39], 0 offen sc1 lds
mav 0,132,36
R 52,15360
mav 4,136,36
mav 8,140,36
s_waitcnt lgkmcnt(2)
mav 0,144,40
R 24,16384
mav 4,148,40
mav 8,152,40
mav 0,156,44
R 28,17408
mav 4,160,44
v_add_u32 v88, 1, v88
mav 8,164,44
s_waitcnt lgkmcnt(2)
mav 0,168,48
R 32,18432
mav 4,172,48
mav 8,176,48
buffer_load_dword v87, s[36:39], 0 offen sc1 lds
mav 0,180,52
R 36,19456
mav 4,184,52
mav 8,188,52
s_waitcnt lgkmcnt(2)
mav 0,192,24
R 40,20480
mav 4,196,24
mav 8,200,24
s_waitcnt vmcnt(14)
v_lshlrev_b32 v12, 16, v56
mav 0,204,28
R 44,21504
v_and_b32 v13, 0xffff0000, v56
mav 4,208,28
v_lshlrev_b32 v14, 16, v57
v_and_b32 v15, 0xffff0000, v57
mav 8,212,28
v_lshlrev_b32 v16, 16, v58
s_waitcnt lgkmcnt(2)
mav 0,216,32
R 48,22528
v_and_b32 v17, 0xffff0000, v58
mav 4,220,32
v_lshlrev_b32 v18, 16, v59
v_and_b32 v19, 0xffff0000, v59
mav 8,224,32
buffer_load_dword v87, s[36:39], 0 offen sc1 lds
mav 0,228,36
R 52,23552
mav 4,232,36
s_waitcnt vmcnt(14)
v_lshlrev_b32 v20, 16, v60
mav 8,236,36
v_and_b32 v21, 0xffff0000, v60
s_waitcnt lgkmcnt(2)
mav 0,240,40
R 24,24576
v_lshlrev_b32 v22, 16, v61
mav 4,244,40
v_and_b32 v23, 0xffff0000, v61
mav 8,248,40
mav 0,252,44
R 28,25600
mvv 4,128,44
buffer_load_dwordx4 v[56:59], v89, s[36:39], s49 offen sc1
v_add_u32 v70, 1, v70
mvv 8,132,44
s_waitcnt lgkmcnt(2)
mvv 0,136,48
R 32,26624
mvv 4,140,48
mvv 8,144,48
buffer_load_dwordx2 v[60:61], v90, s[36:39], s49 offen sc1
mvv 0,148,52
R 36,27648
mvv 4,152,52
s_waitcnt vmcnt(9)
buffer_store_dwordx2 v[70:71], v81, s[36:39], s52 offen sc1
mvv 8,156,52
s_waitcnt lgkmcnt(2)
mvv 0,160,24
R 40,28672
mvv 4,164,24
ds_read_b32 v74, v82 offset:0
mvv 8,168,24
ds_write_b32 v91, v72 offset:1024
mvv 0,172,28
R 44,29696
mvv 4,176,28
mvv 8,180,28
s_waitcnt lgkmcnt(4)
mvv 0,184,32
R 48,30720
s_xor_b32 s50, s50, 0x10000
mvv 4,188,32
mvv 8,192,32
mvv 0,196,36
R 52,31744
mvv 4,200,36
s_waitcnt lgkmcnt(2)
v_cmp_gt_i32 vcc, v88, v74
mvv 8,204,36
s_waitcnt vmcnt(6)
s_cbranch_vccnz CO0_6
BO0_6:
mvv 0,208,40
s_barrier
R 24,32768
mvv 4,212,40
s_add_u32 s49, s49, 346112
s_add_u32 m0, s50, 0
mvv 8,216,40
D 85,0
mvv 0,220,44
R 28,33792
mvv 4,224,44
mvv 8,228,44
s_waitcnt lgkmcnt(2)
mvv 0,232,48
R 32,34816
D 85,1024
mvv 4,236,48
mvv 8,240,48
mvv 0,244,52
R 36,35840
mvv 4,248,52
D 85,2048
mvv 8,252,52
s_waitcnt lgkmcnt(2)
mav 12,0,24
R 40,36864
mav 16,4,24
v_cvt_pk_bf16_f32 v62, v0, v1
v_cvt_pk_bf16_f32 v63, v2, v3
mav 20,8,24
D 85,3072
v_cvt_pk_bf16_f32 v64, v4, v5
mav 12,12,28
R 44,37888
v_cvt_pk_bf16_f32 v65, v6, v7
mav 16,16,28
v_cvt_pk_bf16_f32 v66, v8, v9
v_cvt_pk_bf16_f32 v67, v10, v11
mav 20,20,28
s_add_u32 m0, s50, 4096
s_waitcnt lgkmcnt(2)
mav 12,24,32
R 48,38912
buffer_store_dwordx4 v[62:65], v83, s[36:39], s49 offen sc1
mav 16,28,32
v_cvt_pk_bf16_f32 v68, v8, v9
v_cvt_pk_bf16_f32 v69, v10, v11
mav 20,32,32
v_permlane16_swap_b32 v66, v68
v_permlane16_swap_b32 v67, v69
mav 12,36,36
R 52,39936
v_mul_f32 v73, v0, v0
mav 16,40,36
buffer_store_dwordx4 v[66:69], v84, s[36:39], s49 offen sc1
v_add_f32 v72, v1, v0
mav 20,44,36
v_fmac_f32 v73, v1, v1
s_waitcnt lgkmcnt(2)
)"
R"(
mav 12,48,40
R 24,40960
v_add_f32 v72, v2, v72
mav 16,52,40
v_fmac_f32 v73, v2, v2
v_add_f32 v72, v3, v72
mav 20,56,40
D 86,0
v_fmac_f32 v73, v3, v3
mav 12,60,44
R 28,41984
v_add_f32 v72, v4, v72
mav 16,64,44
v_fmac_f32 v73, v4, v4
v_add_f32 v72, v5, v72
mav 20,68,44
v_fmac_f32 v73, v5, v5
s_waitcnt lgkmcnt(2)
mav 12,72,48
R 32,43008
D 86,1024
mav 16,76,48
v_add_f32 v72, v6, v72
v_fmac_f32 v73, v6, v6
mav 20,80,48
v_add_f32 v72, v7, v72
v_fmac_f32 v73, v7, v7
mav 12,84,52
R 36,44032
v_add_f32 v72, v8, v72
mav 16,88,52
D 86,2048
v_fmac_f32 v73, v8, v8
mav 20,92,52
v_add_f32 v72, v9, v72
s_waitcnt lgkmcnt(2)
mav 12,96,24
R 40,45056
v_fmac_f32 v73, v9, v9
mav 16,100,24
v_add_f32 v72, v10, v72
v_fmac_f32 v73, v10, v10
mav 20,104,24
D 86,3072
v_add_f32 v72, v11, v72
mav 12,108,28
R 44,46080
v_fmac_f32 v73, v11, v11
mav 16,112,28
mav 20,116,28
v_permlane32_swap_b32 v72, v73
s_waitcnt lgkmcnt(2)
mav 12,120,32
R 48,47104
s_mov_b32 m0, 142336
mav 16,124,32
v_add_f32 v72, v72, v73
mav 20,128,32
buffer_load_dword v87, s[36:39], 0 offen sc1 lds
mav 12,132,36
R 52,48128
mav 16,136,36
mav 20,140,36
s_waitcnt lgkmcnt(2)
mav 12,144,40
R 24,49152
mav 16,148,40
mav 20,152,40
mav 12,156,44
R 28,50176
mav 16,160,44
v_add_u32 v88, 1, v88
mav 20,164,44
s_waitcnt lgkmcnt(2)
mav 12,168,48
R 32,51200
mav 16,172,48
mav 20,176,48
buffer_load_dword v87, s[36:39], 0 offen sc1 lds
mav 12,180,52
R 36,52224
mav 16,184,52
mav 20,188,52
s_waitcnt lgkmcnt(2)
mav 12,192,24
R 40,53248
mav 16,196,24
mav 20,200,24
s_waitcnt vmcnt(14)
v_lshlrev_b32 v0, 16, v56
mav 12,204,28
R 44,54272
v_and_b32 v1, 0xffff0000, v56
mav 16,208,28
v_lshlrev_b32 v2, 16, v57
v_and_b32 v3, 0xffff0000, v57
mav 20,212,28
v_lshlrev_b32 v4, 16, v58
s_waitcnt lgkmcnt(2)
mav 12,216,32
R 48,55296
v_and_b32 v5, 0xffff0000, v58
mav 16,220,32
v_lshlrev_b32 v6, 16, v59
v_and_b32 v7, 0xffff0000, v59
mav 20,224,32
buffer_load_dword v87, s[36:39], 0 offen sc1 lds
mav 12,228,36
R 52,56320
mav 16,232,36
s_waitcnt vmcnt(14)
v_lshlrev_b32 v8, 16, v60
mav 20,236,36
v_and_b32 v9, 0xffff0000, v60
s_waitcnt lgkmcnt(2)
mav 12,240,40
R 24,57344
v_lshlrev_b32 v10, 16, v61
mav 16,244,40
v_and_b32 v11, 0xffff0000, v61
mav 20,248,40
mav 12,252,44
R 28,58368
mvv 16,128,44
buffer_load_dwordx4 v[56:59], v89, s[36:39], s49 offen sc1
v_add_u32 v70, 1, v70
mvv 20,132,44
s_waitcnt lgkmcnt(2)
mvv 12,136,48
R 32,59392
mvv 16,140,48
mvv 20,144,48
buffer_load_dwordx2 v[60:61], v90, s[36:39], s49 offen sc1
mvv 12,148,52
R 36,60416
mvv 16,152,52
s_waitcnt vmcnt(9)
buffer_store_dwordx2 v[70:71], v81, s[36:39], s52 offen sc1
mvv 20,156,52
s_waitcnt lgkmcnt(2)
mvv 12,160,24
R 40,61440
mvv 16,164,24
ds_read_b32 v74, v82 offset:0
mvv 20,168,24
ds_write_b32 v91, v72 offset:0
mvv 12,172,28
R 44,62464
mvv 16,176,28
mvv 20,180,28
s_waitcnt lgkmcnt(4)
mvv 12,184,32
R 48,63488
mvv 16,188,32
mvv 20,192,32
mvv 12,196,36
R 52,64512
v_xor_b32 v80, 0x10000, v80
mvv 16,200,36
s_waitcnt lgkmcnt(2)
v_cmp_gt_i32 vcc, v88, v74
mvv 20,204,36
s_waitcnt vmcnt(6)
s_cbranch_vccnz CO0_7
BO0_7:
mvv 12,208,40
s_barrier
R 24,0
mvv 16,212,40
s_add_u32 s49, s49, 346112
s_add_u32 m0, s50, 32768
mvv 20,216,40
D 85,0
mvv 12,220,44
R 28,1024
mvv 16,224,44
mvv 20,228,44
s_waitcnt lgkmcnt(2)
mvv 12,232,48
R 32,2048
D 85,1024
mvv 16,236,48
mvv 20,240,48
mvv 12,244,52
R 36,3072
mvv 16,248,52
D 85,2048
mvv 20,252,52
s_add_u32 s60, s60, 1
s_cbranch_scc0 LO0_5
s_waitcnt lgkmcnt(2)
mav 0,0,24
R 40,4096
mav 4,4,24
v_cvt_pk_bf16_f32 v62, v12, v13
v_cvt_pk_bf16_f32 v63, v14, v15
mav 8,8,24
D 85,3072
v_cvt_pk_bf16_f32 v64, v16, v17
mav 0,12,28
R 44,5120
v_cvt_pk_bf16_f32 v65, v18, v19
mav 4,16,28
v_cvt_pk_bf16_f32 v66, v20, v21
v_cvt_pk_bf16_f32 v67, v22, v23
mav 8,20,28
s_add_u32 m0, s50, 36864
s_waitcnt lgkmcnt(2)
mav 0,24,32
R 48,6144
buffer_store_dwordx4 v[62:65], v83, s[36:39], s49 offen sc1
mav 4,28,32
v_cvt_pk_bf16_f32 v68, v20, v21
v_cvt_pk_bf16_f32 v69, v22, v23
mav 8,32,32
v_permlane16_swap_b32 v66, v68
v_permlane16_swap_b32 v67, v69
mav 0,36,36
R 52,7168
v_mul_f32 v73, v12, v12
mav 4,40,36
buffer_store_dwordx4 v[66:69], v84, s[36:39], s49 offen sc1
v_add_f32 v72, v13, v12
mav 8,44,36
v_fmac_f32 v73, v13, v13
s_waitcnt lgkmcnt(2)
mav 0,48,40
R 24,8192
v_add_f32 v72, v14, v72
mav 4,52,40
v_fmac_f32 v73, v14, v14
v_add_f32 v72, v15, v72
mav 8,56,40
D 86,0
v_fmac_f32 v73, v15, v15
mav 0,60,44
R 28,9216
v_add_f32 v72, v16, v72
mav 4,64,44
v_fmac_f32 v73, v16, v16
v_add_f32 v72, v17, v72
mav 8,68,44
v_fmac_f32 v73, v17, v17
s_waitcnt lgkmcnt(2)
mav 0,72,48
R 32,10240
D 86,1024
mav 4,76,48
v_add_f32 v72, v18, v72
v_fmac_f32 v73, v18, v18
mav 8,80,48
v_add_f32 v72, v19, v72
v_fmac_f32 v73, v19, v19
mav 0,84,52
R 36,11264
v_add_f32 v72, v20, v72
mav 4,88,52
D 86,2048
v_fmac_f32 v73, v20, v20
mav 8,92,52
v_add_f32 v72, v21, v72
s_waitcnt lgkmcnt(2)
mav 0,96,24
R 40,12288
v_fmac_f32 v73, v21, v21
mav 4,100,24
v_add_f32 v72, v22, v72
v_fmac_f32 v73, v22, v22
mav 8,104,24
D 86,3072
v_add_f32 v72, v23, v72
mav 0,108,28
R 44,13312
v_fmac_f32 v73, v23, v23
mav 4,112,28
mav 8,116,28
v_permlane32_swap_b32 v72, v73
s_waitcnt lgkmcnt(2)
mav 0,120,32
R 48,14336
v_add_f32 v72, v72, v73
mav 4,124,32
mav 8,128,32
mav 0,132,36
R 52,15360
mav 4,136,36
mav 8,140,36
s_waitcnt lgkmcnt(2)
mav 0,144,40
R 24,16384
mav 4,148,40
mav 8,152,40
mav 0,156,44
R 28,17408
mav 4,160,44
mav 8,164,44
s_waitcnt lgkmcnt(2)
mav 0,168,48
R 32,18432
mav 4,172,48
mav 8,176,48
mav 0,180,52
R 36,19456
mav 4,184,52
mav 8,188,52
s_waitcnt lgkmcnt(2)
mav 0,192,24
R 40,20480
mav 4,196,24
mav 8,200,24
s_waitcnt vmcnt(12)
v_lshlrev_b32 v12, 16, v56
mav 0,204,28
R 44,21504
v_and_b32 v13, 0xffff0000, v56
mav 4,208,28
v_lshlrev_b32 v14, 16, v57
v_and_b32 v15, 0xffff0000, v57
mav 8,212,28
v_lshlrev_b32 v16, 16, v58
s_waitcnt lgkmcnt(2)
mav 0,216,32
R 48,22528
v_and_b32 v17, 0xffff0000, v58
mav 4,220,32
v_lshlrev_b32 v18, 16, v59
v_and_b32 v19, 0xffff0000, v59
mav 8,224,32
s_waitcnt vmcnt(11)
v_lshlrev_b32 v20, 16, v60
mav 0,228,36
R 52,23552
v_and_b32 v21, 0xffff0000, v60
mav 4,232,36
v_lshlrev_b32 v22, 16, v61
v_and_b32 v23, 0xffff0000, v61
mav 8,236,36
s_waitcnt lgkmcnt(2)
mav 0,240,40
R 24,24576
mav 4,244,40
mav 8,248,40
mav 0,252,44
R 28,25600
mvv 4,128,44
v_add_u32 v70, 1, v70
mvv 8,132,44
s_waitcnt lgkmcnt(2)
mvv 0,136,48
R 32,26624
mvv 4,140,48
mvv 8,144,48
mvv 0,148,52
R 36,27648
mvv 4,152,52
s_waitcnt vmcnt(4)
buffer_store_dwordx2 v[70:71], v81, s[36:39], s52 offen sc1
mvv 8,156,52
s_waitcnt lgkmcnt(2)
mvv 0,160,24
R 40,28672
mvv 4,164,24
mvv 8,168,24
ds_write_b32 v91, v72 offset:1024
mvv 0,172,28
R 44,29696
mvv 4,176,28
mvv 8,180,28
s_waitcnt lgkmcnt(3)
mvv 0,184,32
R 48,30720
mvv 4,188,32
mvv 8,192,32
mvv 0,196,36
R 52,31744
mvv 4,200,36
s_add_u32 s49, s49, 346112
mvv 8,204,36
s_waitcnt vmcnt(1) lgkmcnt(2)
s_barrier
mvv 0,208,40
R 24,32768
mvv 4,212,40
mvv 8,216,40
mvv 0,220,44
R 28,33792
mvv 4,224,44
mvv 8,228,44
s_waitcnt lgkmcnt(2)
mvv 0,232,48
R 32,34816
mvv 4,236,48
mvv 8,240,48
mvv 0,244,52
R 36,35840
mvv 4,248,52
mvv 8,252,52
s_waitcnt lgkmcnt(2)
mav 12,0,24
R 40,36864
mav 16,4,24
mav 20,8,24
v_cvt_pk_bf16_f32 v62, v0, v1
v_cvt_pk_bf16_f32 v63, v2, v3
mav 12,12,28
R 44,37888
v_cvt_pk_bf16_f32 v64, v4, v5
mav 16,16,28
v_cvt_pk_bf16_f32 v65, v6, v7
buffer_store_dwordx4 v[62:65], v83, s[36:39], s49 offen sc1
mav 20,20,28
v_cvt_pk_bf16_f32 v66, v8, v9
s_waitcnt lgkmcnt(2)
mav 12,24,32
R 48,38912
v_cvt_pk_bf16_f32 v67, v10, v11
mav 16,28,32
v_cvt_pk_bf16_f32 v68, v8, v9
v_cvt_pk_bf16_f32 v69, v10, v11
mav 20,32,32
v_permlane16_swap_b32 v66, v68
v_permlane16_swap_b32 v67, v69
mav 12,36,36
R 52,39936
buffer_store_dwordx4 v[66:69], v84, s[36:39], s49 offen sc1
mav 16,40,36
v_mul_f32 v73, v0, v0
v_add_f32 v72, v1, v0
mav 20,44,36
v_fmac_f32 v73, v1, v1
s_waitcnt lgkmcnt(2)
mav 12,48,40
R 24,40960
v_add_f32 v72, v2, v72
mav 16,52,40
v_fmac_f32 v73, v2, v2
v_add_f32 v72, v3, v72
mav 20,56,40
v_fmac_f32 v73, v3, v3
v_add_f32 v72, v4, v72
mav 12,60,44
R 28,41984
v_fmac_f32 v73, v4, v4
mav 16,64,44
v_add_f32 v72, v5, v72
v_fmac_f32 v73, v5, v5
mav 20,68,44
v_add_f32 v72, v6, v72
s_waitcnt lgkmcnt(2)
mav 12,72,48
R 32,43008
v_fmac_f32 v73, v6, v6
mav 16,76,48
v_add_f32 v72, v7, v72
v_fmac_f32 v73, v7, v7
mav 20,80,48
v_add_f32 v72, v8, v72
v_fmac_f32 v73, v8, v8
mav 12,84,52
R 36,44032
v_add_f32 v72, v9, v72
mav 16,88,52
v_fmac_f32 v73, v9, v9
v_add_f32 v72, v10, v72
mav 20,92,52
v_fmac_f32 v73, v10, v10
s_waitcnt lgkmcnt(2)
mav 12,96,24
R 40,45056
v_add_f32 v72, v11, v72
mav 16,100,24
v_fmac_f32 v73, v11, v11
mav 20,104,24
mav 12,108,28
R 44,46080
v_permlane32_swap_b32 v72, v73
mav 16,112,28
v_add_f32 v72, v72, v73
mav 20,116,28
s_waitcnt lgkmcnt(2)
mav 12,120,32
R 48,47104
mav 16,124,32
mav 20,128,32
mav 12,132,36
R 52,48128
mav 16,136,36
mav 20,140,36
s_waitcnt lgkmcnt(2)
mav 12,144,40
R 24,49152
mav 16,148,40
mav 20,152,40
mav 12,156,44
R 28,50176
mav 16,160,44
mav 20,164,44
s_waitcnt lgkmcnt(2)
mav 12,168,48
R 32,51200
mav 16,172,48
mav 20,176,48
mav 12,180,52
R 36,52224
mav 16,184,52
mav 20,188,52
s_waitcnt lgkmcnt(2)
mav 12,192,24
R 40,53248
mav 16,196,24
mav 20,200,24
mav 12,204,28
R 44,54272
mav 16,208,28
mav 20,212,28
s_waitcnt lgkmcnt(2)
mav 12,216,32
R 48,55296
mav 16,220,32
mav 20,224,32
mav 12,228,36
R 52,56320
mav 16,232,36
mav 20,236,36
s_waitcnt lgkmcnt(2)
mav 12,240,40
R 24,57344
mav 16,244,40
mav 20,248,40
mav 12,252,44
R 28,58368
mvv 16,128,44
v_add_u32 v70, 1, v70
mvv 20,132,44
s_waitcnt lgkmcnt(2)
mvv 12,136,48
R 32,59392
mvv 16,140,48
mvv 20,144,48
mvv 12,148,52
R 36,60416
mvv 16,152,52
s_waitcnt vmcnt(0)
buffer_store_dwordx2 v[70:71], v81, s[36:39], s52 offen sc1
mvv 20,156,52
s_waitcnt lgkmcnt(2)
mvv 12,160,24
R 40,61440
mvv 16,164,24
mvv 20,168,24
ds_write_b32 v91, v72 offset:0
mvv 12,172,28
R 44,62464
mvv 16,176,28
mvv 20,180,28
s_waitcnt lgkmcnt(3)
mvv 12,184,32
R 48,63488
mvv 16,188,32
mvv 20,192,32
mvv 12,196,36
R 52,64512
mvv 16,200,36
s_add_u32 s49, s49, 346112
mvv 20,204,36
s_waitcnt lgkmcnt(2)
s_barrier
mvv 12,208,40
mvv 16,212,40
mvv 20,216,40
mvv 12,220,44
mvv 16,224,44
mvv 20,228,44
s_waitcnt lgkmcnt(0)
mvv 12,232,48
mvv 16,236,48
mvv 20,240,48
mvv 12,244,52
mvv 16,248,52
mvv 20,252,52
s_nop 5
v_cvt_pk_bf16_f32 v62, v12, v13
v_cvt_pk_bf16_f32 v63, v14, v15
v_cvt_pk_bf16_f32 v64, v16, v17
v_cvt_pk_bf16_f32 v65, v18, v19
buffer_store_dwordx4 v[62:65], v83, s[36:39], s49 offen sc1
v_cvt_pk_bf16_f32 v66, v20, v21
v_cvt_pk_bf16_f32 v67, v22, v23
v_cvt_pk_bf16_f32 v68, v20, v21
v_cvt_pk_bf16_f32 v69, v22, v23
s_nop 0
v_permlane16_swap_b32 v66, v68
v_permlane16_swap_b32 v67, v69
buffer_store_dwordx4 v[66:69], v84, s[36:39], s49 offen sc1
v_mul_f32 v73, v12, v12
v_add_f32 v72, v13, v12
v_fmac_f32 v73, v13, v13
v_add_f32 v72, v14, v72
v_fmac_f32 v73, v14, v14
v_add_f32 v72, v15, v72
v_fmac_f32 v73, v15, v15
v_add_f32 v72, v16, v72
v_fmac_f32 v73, v16, v16
v_add_f32 v72, v17, v72
v_fmac_f32 v73, v17, v17
v_add_f32 v72, v18, v72
v_fmac_f32 v73, v18, v18
v_add_f32 v72, v19, v72
v_fmac_f32 v73, v19, v19
v_add_f32 v72, v20, v72
v_fmac_f32 v73, v20, v20
v_add_f32 v72, v21, v72
v_fmac_f32 v73, v21, v21
v_add_f32 v72, v22, v72
v_fmac_f32 v73, v22, v22
v_add_f32 v72, v23, v72
v_fmac_f32 v73, v23, v23
s_nop 1
v_permlane32_swap_b32 v72, v73
v_add_f32 v72, v72, v73
v_add_u32 v70, 37, v70
s_waitcnt vmcnt(0)
buffer_store_dwordx2 v[70:71], v81, s[36:39], s52 offen sc1
ds_write_b32 v91, v72 offset:1024
s_add_u32 s49, s49, 346112
s_waitcnt lgkmcnt(0)
s_barrier
s_endpgm
CO0_1:
s_cmp_eq_u32 s61, 0
s_cbranch_scc1 BO0_1
s_sub_u32 s61, s61, 1
s_sleep 8
buffer_load_dwordx2 v[74:75], v87, s[36:39], 0 offen sc1
s_waitcnt vmcnt(0)
ds_write_b32 v82, v74
v_sub_u32 v74, v74, v88
v_xor_b32 v75, v75, v71
v_cmp_lt_u64 vcc, s[62:63], v[74:75]
s_waitcnt lgkmcnt(0)
s_cbranch_vccnz CO0_1
s_branch BO0_1
CO0_2:
s_mov_b32 s58, m0
s_mov_b32 m0, 142336
DO0_2:
s_cmp_eq_u32 s61, 0
s_cbranch_scc1 EO0_2
s_sub_u32 s61, s61, 1
buffer_load_dword v87, s[36:39], 0 offen sc1 lds
s_sleep 1
ds_read_b32 v74, v82
s_waitcnt lgkmcnt(0)
v_cmp_gt_i32 vcc, v88, v74
s_cbranch_vccnz DO0_2
EO0_2:
s_mov_b32 m0, s58
s_branch BO0_2
CO0_3:
s_mov_b32 s58, m0
s_mov_b32 m0, 142336
DO0_3:
s_cmp_eq_u32 s61, 0
s_cbranch_scc1 EO0_3
s_sub_u32 s61, s61, 1
buffer_load_dword v87, s[36:39], 0 offen sc1 lds
s_sleep 1
ds_read_b32 v74, v82
s_waitcnt lgkmcnt(0)
v_cmp_gt_i32 vcc, v88, v74
s_cbranch_vccnz DO0_3
EO0_3:
s_mov_b32 m0, s58
s_branch BO0_3
CO0_4:
s_mov_b32 s58, m0
s_mov_b32 m0, 142336
DO0_4:
s_cmp_eq_u32 s61, 0
s_cbranch_scc1 EO0_4
s_sub_u32 s61, s61, 1
buffer_load_dword v87, s[36:39], 0 offen sc1 lds
s_sleep 1
ds_read_b32 v74, v82
s_waitcnt lgkmcnt(0)
v_cmp_gt_i32 vcc, v88, v74
s_cbranch_vccnz DO0_4
EO0_4:
s_mov_b32 m0, s58
s_branch BO0_4
CO0_6:
s_mov_b32 s58, m0
s_mov_b32 m0, 142336
DO0_6:
s_cmp_eq_u32 s61, 0
s_cbranch_scc1 EO0_6
s_sub_u32 s61, s61, 1
buffer_load_dword v87, s[36:39], 0 offen sc1 lds
s_sleep 1
ds_read_b32 v74, v82
s_waitcnt lgkmcnt(0)
v_cmp_gt_i32 vcc, v88, v74
s_cbranch_vccnz DO0_6
EO0_6:
s_mov_b32 m0, s58
s_branch BO0_6
CO0_7:
s_mov_b32 s58, m0
s_mov_b32 m0, 142336
DO0_7:
s_cmp_eq_u32 s61, 0
s_cbranch_scc1 EO0_7
s_sub_u32 s61, s61, 1
buffer_load_dword v87, s[36:39], 0 offen sc1 lds
s_sleep 1
ds_read_b32 v74, v82
s_waitcnt lgkmcnt(0)
v_cmp_gt_i32 vcc, v88, v74
s_cbranch_vccnz DO0_7
EO0_7:
s_mov_b32 m0, s58
s_branch BO0_7
LvO1:
s_barrier
buffer_load_dwordx4 v[56:59], v89, s[36:39], s49 offen sc1
buffer_load_dwordx2 v[60:61], v90, s[36:39], s49 offen sc1
s_add_u32 s49, s49, 346112
s_add_u32 m0, s50, 0
s_nop 0
D 85,0
D 85,1024
D 85,2048
D 85,3072
s_add_u32 m0, s50, 4096
s_nop 0
D 86,0
D 86,1024
D 86,2048
D 86,3072
s_waitcnt vmcnt(9)
v_lshlrev_b32 v0, 16, v56
v_and_b32 v1, 0xffff0000, v56
v_lshlrev_b32 v2, 16, v57
v_and_b32 v3, 0xffff0000, v57
v_lshlrev_b32 v4, 16, v58
v_and_b32 v5, 0xffff0000, v58
v_lshlrev_b32 v6, 16, v59
v_and_b32 v7, 0xffff0000, v59
s_waitcnt vmcnt(8)
v_lshlrev_b32 v8, 16, v60
v_and_b32 v9, 0xffff0000, v60
v_lshlrev_b32 v10, 16, v61
v_and_b32 v11, 0xffff0000, v61
buffer_load_dwordx4 v[56:59], v89, s[36:39], s49 offen sc1
buffer_load_dwordx2 v[60:61], v90, s[36:39], s49 offen sc1
s_add_u32 s49, s49, 346112
s_waitcnt vmcnt(2)
s_barrier
s_add_u32 m0, s50, 32768
R 24,0
D 85,0
R 28,1024
D 85,1024
R 32,2048
R 36,3072
D 85,2048
s_waitcnt lgkmcnt(2)
mav 0,0,24
R 40,4096
mav 4,4,24
mav 8,8,24
D 85,3072
mav 0,12,28
R 44,5120
mav 4,16,28
mav 8,20,28
s_add_u32 m0, s50, 36864
s_waitcnt lgkmcnt(2)
mav 0,24,32
R 48,6144
mav 4,28,32
D 86,0
mav 8,32,32
mav 0,36,36
R 52,7168
mav 4,40,36
mav 8,44,36
D 86,1024
s_waitcnt lgkmcnt(2)
mav 0,48,40
R 24,8192
mav 4,52,40
mav 8,56,40
mav 0,60,44
R 28,9216
mav 4,64,44
D 86,2048
mav 8,68,44
s_waitcnt lgkmcnt(2)
mav 0,72,48
R 32,10240
mav 4,76,48
mav 8,80,48
D 86,3072
mav 0,84,52
R 36,11264
mav 4,88,52
mav 8,92,52
s_waitcnt lgkmcnt(2)
mav 0,96,24
R 40,12288
mav 4,100,24
mav 8,104,24
mav 0,108,28
R 44,13312
mav 4,112,28
mav 8,116,28
s_waitcnt lgkmcnt(2)
mav 0,120,32
R 48,14336
mav 4,124,32
mav 8,128,32
mav 0,132,36
R 52,15360
mav 4,136,36
mav 8,140,36
s_waitcnt lgkmcnt(2)
mav 0,144,40
R 24,16384
mav 4,148,40
mav 8,152,40
mav 0,156,44
R 28,17408
mav 4,160,44
mav 8,164,44
s_waitcnt lgkmcnt(2)
mav 0,168,48
R 32,18432
mav 4,172,48
mav 8,176,48
mav 0,180,52
R 36,19456
mav 4,184,52
mav 8,188,52
s_waitcnt lgkmcnt(2)
mav 0,192,24
R 40,20480
mav 4,196,24
mav 8,200,24
s_waitcnt vmcnt(9)
v_lshlrev_b32 v12, 16, v56
mav 0,204,28
R 44,21504
v_and_b32 v13, 0xffff0000, v56
mav 4,208,28
v_lshlrev_b32 v14, 16, v57
v_and_b32 v15, 0xffff0000, v57
mav 8,212,28
v_lshlrev_b32 v16, 16, v58
s_waitcnt lgkmcnt(2)
mav 0,216,32
R 48,22528
v_and_b32 v17, 0xffff0000, v58
mav 4,220,32
v_lshlrev_b32 v18, 16, v59
v_and_b32 v19, 0xffff0000, v59
mav 8,224,32
s_waitcnt vmcnt(8)
v_lshlrev_b32 v20, 16, v60
mav 0,228,36
R 52,23552
v_and_b32 v21, 0xffff0000, v60
mav 4,232,36
v_lshlrev_b32 v22, 16, v61
v_and_b32 v23, 0xffff0000, v61
mav 8,236,36
s_waitcnt lgkmcnt(2)
mav 0,240,40
R 24,24576
mav 4,244,40
mav 8,248,40
mav 0,252,44
R 28,25600
mvv 4,128,44
buffer_load_dwordx4 v[56:59], v89, s[36:39], s49 offen sc1
mvv 8,132,44
s_waitcnt lgkmcnt(2)
mvv 0,136,48
R 32,26624
mvv 4,140,48
mvv 8,144,48
buffer_load_dwordx2 v[60:61], v90, s[36:39], s49 offen sc1
mvv 0,148,52
R 36,27648
mvv 4,152,52
mvv 8,156,52
s_waitcnt lgkmcnt(2)
mvv 0,160,24
R 40,28672
mvv 4,164,24
mvv 8,168,24
mvv 0,172,28
R 44,29696
mvv 4,176,28
mvv 8,180,28
s_waitcnt lgkmcnt(2)
mvv 0,184,32
R 48,30720
s_xor_b32 s50, s50, 0x10000
mvv 4,188,32
mvv 8,192,32
mvv 0,196,36
R 52,31744
mvv 4,200,36
s_add_u32 s49, s49, 346112
mvv 8,204,36
s_waitcnt vmcnt(2)
s_barrier
s_waitcnt lgkmcnt(2)
mvv 0,208,40
R 24,32768
s_add_u32 m0, s50, 0
mvv 4,212,40
D 85,0
mvv 8,216,40
mvv 0,220,44
R 28,33792
mvv 4,224,44
mvv 8,228,44
D 85,1024
s_waitcnt lgkmcnt(2)
mvv 0,232,48
R 32,34816
mvv 4,236,48
mvv 8,240,48
mvv 0,244,52
R 36,35840
mvv 4,248,52
D 85,2048
mvv 8,252,52
s_waitcnt lgkmcnt(2)
mav 12,0,24
R 40,36864
mav 16,4,24
v_cvt_pk_bf16_f32 v62, v0, v1
v_cvt_pk_bf16_f32 v63, v2, v3
mav 20,8,24
D 85,3072
v_cvt_pk_bf16_f32 v64, v4, v5
mav 12,12,28
R 44,37888
v_cvt_pk_bf16_f32 v65, v6, v7
mav 16,16,28
v_cvt_pk_bf16_f32 v66, v8, v9
v_cvt_pk_bf16_f32 v67, v10, v11
mav 20,20,28
s_add_u32 m0, s50, 4096
s_waitcnt lgkmcnt(2)
mav 12,24,32
R 48,38912
buffer_store_dwordx4 v[62:65], v83, s[36:39], s49 offen sc1
mav 16,28,32
v_cvt_pk_bf16_f32 v68, v8, v9
v_cvt_pk_bf16_f32 v69, v10, v11
mav 20,32,32
v_permlane16_swap_b32 v66, v68
v_permlane16_swap_b32 v67, v69
mav 12,36,36
R 52,39936
v_mul_f32 v73, v0, v0
mav 16,40,36
buffer_store_dwordx4 v[66:69], v84, s[36:39], s49 offen sc1
v_add_f32 v72, v1, v0
mav 20,44,36
v_fmac_f32 v73, v1, v1
s_waitcnt lgkmcnt(2)
mav 12,48,40
R 24,40960
v_add_f32 v72, v2, v72
mav 16,52,40
v_fmac_f32 v73, v2, v2
v_add_f32 v72, v3, v72
mav 20,56,40
D 86,0
v_fmac_f32 v73, v3, v3
mav 12,60,44
R 28,41984
v_add_f32 v72, v4, v72
mav 16,64,44
v_fmac_f32 v73, v4, v4
v_add_f32 v72, v5, v72
mav 20,68,44
v_fmac_f32 v73, v5, v5
s_waitcnt lgkmcnt(2)
mav 12,72,48
R 32,43008
D 86,1024
mav 16,76,48
v_add_f32 v72, v6, v72
v_fmac_f32 v73, v6, v6
mav 20,80,48
v_add_f32 v72, v7, v72
v_fmac_f32 v73, v7, v7
mav 12,84,52
R 36,44032
v_add_f32 v72, v8, v72
mav 16,88,52
D 86,2048
v_fmac_f32 v73, v8, v8
mav 20,92,52
v_add_f32 v72, v9, v72
s_waitcnt lgkmcnt(2)
mav 12,96,24
R 40,45056
v_fmac_f32 v73, v9, v9
mav 16,100,24
v_add_f32 v72, v10, v72
v_fmac_f32 v73, v10, v10
mav 20,104,24
D 86,3072
v_add_f32 v72, v11, v72
mav 12,108,28
R 44,46080
v_fmac_f32 v73, v11, v11
mav 16,112,28
mav 20,116,28
v_permlane32_swap_b32 v72, v73
s_waitcnt lgkmcnt(2)
mav 12,120,32
R 48,47104
v_add_f32 v72, v72, v73
mav 16,124,32
mav 20,128,32
mav 12,132,36
R 52,48128
mav 16,136,36
mav 20,140,36
s_waitcnt lgkmcnt(2)
mav 12,144,40
R 24,49152
mav 16,148,40
mav 20,152,40
mav 12,156,44
R 28,50176
mav 16,160,44
mav 20,164,44
s_waitcnt lgkmcnt(2)
mav 12,168,48
R 32,51200
mav 16,172,48
mav 20,176,48
mav 12,180,52
R 36,52224
mav 16,184,52
mav 20,188,52
s_waitcnt lgkmcnt(2)
mav 12,192,24
R 40,53248
mav 16,196,24
mav 20,200,24
s_waitcnt vmcnt(11)
v_lshlrev_b32 v0, 16, v56
mav 12,204,28
R 44,54272
v_and_b32 v1, 0xffff0000, v56
mav 16,208,28
v_lshlrev_b32 v2, 16, v57
v_and_b32 v3, 0xffff0000, v57
mav 20,212,28
v_lshlrev_b32 v4, 16, v58
s_waitcnt lgkmcnt(2)
mav 12,216,32
R 48,55296
v_and_b32 v5, 0xffff0000, v58
mav 16,220,32
v_lshlrev_b32 v6, 16, v59
v_and_b32 v7, 0xffff0000, v59
mav 20,224,32
s_waitcnt vmcnt(10)
v_lshlrev_b32 v8, 16, v60
mav 12,228,36
R 52,56320
v_and_b32 v9, 0xffff0000, v60
mav 16,232,36
v_lshlrev_b32 v10, 16, v61
v_and_b32 v11, 0xffff0000, v61
mav 20,236,36
s_waitcnt lgkmcnt(2)
mav 12,240,40
R 24,57344
mav 16,244,40
mav 20,248,40
mav 12,252,44
R 28,58368
mvv 16,128,44
buffer_load_dwordx4 v[56:59], v89, s[36:39], s49 offen sc1
v_add_u32 v70, 1, v70
mvv 20,132,44
s_waitcnt lgkmcnt(2)
mvv 12,136,48
R 32,59392
mvv 16,140,48
mvv 20,144,48
buffer_load_dwordx2 v[60:61], v90, s[36:39], s49 offen sc1
mvv 12,148,52
R 36,60416
mvv 16,152,52
s_waitcnt vmcnt(6)
buffer_store_dwordx2 v[70:71], v81, s[36:39], s52 offen sc1
mvv 20,156,52
s_waitcnt lgkmcnt(2)
mvv 12,160,24
R 40,61440
mvv 16,164,24
mvv 20,168,24
ds_write_b32 v91, v72 offset:0
mvv 12,172,28
R 44,62464
mvv 16,176,28
mvv 20,180,28
s_waitcnt lgkmcnt(3)
mvv 12,184,32
R 48,63488
mvv 16,188,32
mvv 20,192,32
mvv 12,196,36
R 52,64512
v_xor_b32 v80, 0x10000, v80
mvv 16,200,36
s_add_u32 s49, s49, 346112
mvv 20,204,36
s_waitcnt vmcnt(3) lgkmcnt(2)
s_barrier
mvv 12,208,40
R 24,0
s_add_u32 m0, s50, 32768
mvv 16,212,40
ds_read_b128 v[76:79], v92 offset:0
D 85,0
mvv 20,216,40
mvv 12,220,44
R 28,1024
mvv 16,224,44
mvv 20,228,44
D 85,1024
s_waitcnt lgkmcnt(3)
mvv 12,232,48
R 32,2048
mvv 16,236,48
mvv 20,240,48
s_waitcnt lgkmcnt(2)
v_add_f32 v76, v76, v77
mvv 12,244,52
R 36,3072
v_add_f32 v78, v78, v79
mvv 16,248,52
D 85,2048
v_add_f32 v76, v76, v78
mvv 20,252,52
s_mov_b32 s60, -30
.p2align 6
LO1_1:
mav 0,0,24
R 40,4096
v_add_f32_dpp v77, v76, v76 quad_perm:[1,0,3,2] row_mask:0xf bank_mask:0xf
mav 4,4,24
buffer_store_dword v77, v93, s[36:39], s49 offen sc1
v_cvt_pk_bf16_f32 v62, v12, v13
mav 8,8,24
D 85,3072
s_waitcnt lgkmcnt(2)
mav 0,12,28
R 44,5120
v_cvt_pk_bf16_f32 v63, v14, v15
mav 4,16,28
v_cvt_pk_bf16_f32 v64, v16, v17
v_cvt_pk_bf16_f32 v65, v18, v19
mav 8,20,28
s_add_u32 m0, s50, 36864
v_cvt_pk_bf16_f32 v66, v20, v21
mav 0,24,32
R 48,6144
buffer_store_dwordx4 v[62:65], v83, s[36:39], s49 offen sc1
mav 4,28,32
v_cvt_pk_bf16_f32 v67, v22, v23
v_cvt_pk_bf16_f32 v68, v20, v21
mav 8,32,32
v_cvt_pk_bf16_f32 v69, v22, v23
s_waitcnt lgkmcnt(2)
mav 0,36,36
R 52,7168
v_permlane16_swap_b32 v66, v68
mav 4,40,36
D 86,0
v_permlane16_swap_b32 v67, v69
mav 8,44,36
v_mul_f32 v73, v12, v12
v_add_f32 v72, v13, v12
mav 0,48,40
R 24,8192
v_fmac_f32 v73, v13, v13
mav 4,52,40
v_add_f32 v72, v14, v72
v_fmac_f32 v73, v14, v14
mav 8,56,40
buffer_store_dwordx4 v[66:69], v84, s[36:39], s49 offen sc1
s_waitcnt lgkmcnt(2)
mav 0,60,44
R 28,9216
v_add_f32 v72, v15, v72
mav 4,64,44
v_fmac_f32 v73, v15, v15
v_add_f32 v72, v16, v72
mav 8,68,44
v_fmac_f32 v73, v16, v16
v_add_f32 v72, v17, v72
mav 0,72,48
R 32,10240
D 86,1024
mav 4,76,48
v_fmac_f32 v73, v17, v17
v_add_f32 v72, v18, v72
mav 8,80,48
v_fmac_f32 v73, v18, v18
s_waitcnt lgkmcnt(2)
mav 0,84,52
R 36,11264
v_add_f32 v72, v19, v72
mav 4,88,52
D 86,2048
v_fmac_f32 v73, v19, v19
mav 8,92,52
v_add_f32 v72, v20, v72
v_fmac_f32 v73, v20, v20
mav 0,96,24
R 40,12288
v_add_f32 v72, v21, v72
mav 4,100,24
v_fmac_f32 v73, v21, v21
v_add_f32 v72, v22, v72
mav 8,104,24
D 86,3072
s_waitcnt lgkmcnt(2)
mav 0,108,28
R 44,13312
v_fmac_f32 v73, v22, v22
mav 4,112,28
v_add_f32 v72, v23, v72
v_fmac_f32 v73, v23, v23
mav 8,116,28
mav 0,120,32
R 48,14336
v_permlane32_swap_b32 v72, v73
mav 4,124,32
v_add_f32 v72, v72, v73
mav 8,128,32
s_waitcnt lgkmcnt(2)
mav 0,132,36
R 52,15360
mav 4,136,36
mav 8,140,36
mav 0,144,40
R 24,16384
mav 4,148,40
mav 8,152,40
s_waitcnt lgkmcnt(2)
mav 0,156,44
R 28,17408
mav 4,160,44
mav 8,164,44
mav 0,168,48
R 32,18432
mav 4,172,48
mav 8,176,48
s_waitcnt lgkmcnt(2)
mav 0,180,52
R 36,19456
mav 4,184,52
mav 8,188,52
mav 0,192,24
R 40,20480
mav 4,196,24
mav 8,200,24
s_waitcnt lgkmcnt(2)
mav 0,204,28
R 44,21504
mav 4,208,28
s_waitcnt vmcnt(13)
v_lshlrev_b32 v12, 16, v56
mav 8,212,28
v_and_b32 v13, 0xffff0000, v56
v_lshlrev_b32 v14, 16, v57
mav 0,216,32
R 48,22528
v_and_b32 v15, 0xffff0000, v57
mav 4,220,32
v_lshlrev_b32 v16, 16, v58
v_and_b32 v17, 0xffff0000, v58
mav 8,224,32
v_lshlrev_b32 v18, 16, v59
s_waitcnt lgkmcnt(2)
mav 0,228,36
R 52,23552
v_and_b32 v19, 0xffff0000, v59
mav 4,232,36
s_waitcnt vmcnt(12)
v_lshlrev_b32 v20, 16, v60
mav 8,236,36
v_and_b32 v21, 0xffff0000, v60
v_lshlrev_b32 v22, 16, v61
mav 0,240,40
R 24,24576
v_and_b32 v23, 0xffff0000, v61
mav 4,244,40
mav 8,248,40
s_waitcnt lgkmcnt(2)
mav 0,252,44
R 28,25600
mvv 4,128,44
buffer_load_dwordx4 v[56:59], v89, s[36:39], s49 offen sc1
v_add_u32 v70, 1, v70
mvv 8,132,44
mvv 0,136,48
R 32,26624
mvv 4,140,48
mvv 8,144,48
buffer_load_dwordx2 v[60:61], v90, s[36:39], s49 offen sc1
s_waitcnt lgkmcnt(2)
mvv 0,148,52
R 36,27648
mvv 4,152,52
s_waitcnt vmcnt(5)
buffer_store_dwordx2 v[70:71], v81, s[36:39], s52 offen sc1
mvv 8,156,52
mvv 0,160,24
R 40,28672
mvv 4,164,24
mvv 8,168,24
s_waitcnt lgkmcnt(2)
mvv 0,172,28
R 44,29696
mvv 4,176,28
ds_write_b32 v91, v72 offset:1024
mvv 8,180,28
mvv 0,184,32
R 48,30720
s_xor_b32 s50, s50, 0x10000
mvv 4,188,32
mvv 8,192,32
s_waitcnt lgkmcnt(3)
mvv 0,196,36
R 52,31744
mvv 4,200,36
s_add_u32 s49, s49, 346112
mvv 8,204,36
s_waitcnt vmcnt(3) lgkmcnt(2)
s_barrier
mvv 0,208,40
R 24,32768
s_add_u32 m0, s50, 0
mvv 4,212,40
ds_read_b128 v[76:79], v92 offset:1024
D 85,0
mvv 8,216,40
mvv 0,220,44
R 28,33792
mvv 4,224,44
mvv 8,228,44
D 85,1024
s_waitcnt lgkmcnt(3)
mvv 0,232,48
R 32,34816
mvv 4,236,48
mvv 8,240,48
s_waitcnt lgkmcnt(2)
v_add_f32 v76, v76, v77
mvv 0,244,52
R 36,35840
v_add_f32 v78, v78, v79
mvv 4,248,52
D 85,2048
v_add_f32 v76, v76, v78
mvv 8,252,52
mav 12,0,24
R 40,36864
v_add_f32_dpp v77, v76, v76 quad_perm:[1,0,3,2] row_mask:0xf bank_mask:0xf
mav 16,4,24
buffer_store_dword v77, v93, s[36:39], s49 offen sc1
v_cvt_pk_bf16_f32 v62, v0, v1
mav 20,8,24
D 85,3072
s_waitcnt lgkmcnt(2)
mav 12,12,28
R 44,37888
v_cvt_pk_bf16_f32 v63, v2, v3
mav 16,16,28
v_cvt_pk_bf16_f32 v64, v4, v5
v_cvt_pk_bf16_f32 v65, v6, v7
mav 20,20,28
s_add_u32 m0, s50, 4096
v_cvt_pk_bf16_f32 v66, v8, v9
mav 12,24,32
R 48,38912
buffer_store_dwordx4 v[62:65], v83, s[36:39], s49 offen sc1
mav 16,28,32
v_cvt_pk_bf16_f32 v67, v10, v11
v_cvt_pk_bf16_f32 v68, v8, v9
mav 20,32,32
v_cvt_pk_bf16_f32 v69, v10, v11
s_waitcnt lgkmcnt(2)
mav 12,36,36
R 52,39936
v_permlane16_swap_b32 v66, v68
mav 16,40,36
D 86,0
v_permlane16_swap_b32 v67, v69
mav 20,44,36
v_mul_f32 v73, v0, v0
v_add_f32 v72, v1, v0
mav 12,48,40
R 24,40960
v_fmac_f32 v73, v1, v1
mav 16,52,40
v_add_f32 v72, v2, v72
v_fmac_f32 v73, v2, v2
mav 20,56,40
buffer_store_dwordx4 v[66:69], v84, s[36:39], s49 offen sc1
s_waitcnt lgkmcnt(2)
mav 12,60,44
R 28,41984
v_add_f32 v72, v3, v72
mav 16,64,44
v_fmac_f32 v73, v3, v3
v_add_f32 v72, v4, v72
mav 20,68,44
v_fmac_f32 v73, v4, v4
v_add_f32 v72, v5, v72
mav 12,72,48
R 32,43008
D 86,1024
mav 16,76,48
v_fmac_f32 v73, v5, v5
v_add_f32 v72, v6, v72
mav 20,80,48
v_fmac_f32 v73, v6, v6
s_waitcnt lgkmcnt(2)
mav 12,84,52
R 36,44032
v_add_f32 v72, v7, v72
mav 16,88,52
D 86,2048
v_fmac_f32 v73, v7, v7
mav 20,92,52
v_add_f32 v72, v8, v72
v_fmac_f32 v73, v8, v8
mav 12,96,24
R 40,45056
v_add_f32 v72, v9, v72
mav 16,100,24
v_fmac_f32 v73, v9, v9
v_add_f32 v72, v10, v72
mav 20,104,24
D 86,3072
s_waitcnt lgkmcnt(2)
mav 12,108,28
R 44,46080
v_fmac_f32 v73, v10, v10
mav 16,112,28
v_add_f32 v72, v11, v72
v_fmac_f32 v73, v11, v11
mav 20,116,28
mav 12,120,32
R 48,47104
v_permlane32_swap_b32 v72, v73
mav 16,124,32
v_add_f32 v72, v72, v73
mav 20,128,32
s_waitcnt lgkmcnt(2)
mav 12,132,36
R 52,48128
mav 16,136,36
mav 20,140,36
mav 12,144,40
R 24,49152
mav 16,148,40
mav 20,152,40
s_waitcnt lgkmcnt(2)
mav 12,156,44
R 28,50176
mav 16,160,44
mav 20,164,44
mav 12,168,48
R 32,51200
mav 16,172,48
mav 20,176,48
s_waitcnt lgkmcnt(2)
mav 12,180,52
R 36,52224
mav 16,184,52
mav 20,188,52
mav 12,192,24
R 40,53248
mav 16,196,24
mav 20,200,24
s_waitcnt lgkmcnt(2)
mav 12,204,28
R 44,54272
mav 16,208,28
s_waitcnt vmcnt(13)
v_lshlrev_b32 v0, 16, v56
mav 20,212,28
v_and_b32 v1, 0xffff0000, v56
v_lshlrev_b32 v2, 16, v57
mav 12,216,32
R 48,55296
v_and_b32 v3, 0xffff0000, v57
mav 16,220,32
v_lshlrev_b32 v4, 16, v58
v_and_b32 v5, 0xffff0000, v58
mav 20,224,32
v_lshlrev_b32 v6, 16, v59
s_waitcnt lgkmcnt(2)
mav 12,228,36
R 52,56320
v_and_b32 v7, 0xffff0000, v59
mav 16,232,36
s_waitcnt vmcnt(12)
v_lshlrev_b32 v8, 16, v60
mav 20,236,36
v_and_b32 v9, 0xffff0000, v60
v_lshlrev_b32 v10, 16, v61
mav 12,240,40
R 24,57344
v_and_b32 v11, 0xffff0000, v61
mav 16,244,40
mav 20,248,40
s_waitcnt lgkmcnt(2)
mav 12,252,44
R 28,58368
mvv 16,128,44
buffer_load_dwordx4 v[56:59], v89, s[36:39], s49 offen sc1
v_add_u32 v70, 1, v70
mvv 20,132,44
mvv 12,136,48
R 32,59392
mvv 16,140,48
mvv 20,144,48
buffer_load_dwordx2 v[60:61], v90, s[36:39], s49 offen sc1
s_waitcnt lgkmcnt(2)
mvv 12,148,52
R 36,60416
mvv 16,152,52
s_waitcnt vmcnt(5)
buffer_store_dwordx2 v[70:71], v81, s[36:39], s52 offen sc1
mvv 20,156,52
mvv 12,160,24
R 40,61440
mvv 16,164,24
mvv 20,168,24
s_waitcnt lgkmcnt(2)
mvv 12,172,28
R 44,62464
mvv 16,176,28
ds_write_b32 v91, v72 offset:0
mvv 20,180,28
mvv 12,184,32
R 48,63488
mvv 16,188,32
mvv 20,192,32
s_waitcnt lgkmcnt(3)
mvv 12,196,36
R 52,64512
v_xor_b32 v80, 0x10000, v80
mvv 16,200,36
s_add_u32 s49, s49, 346112
mvv 20,204,36
s_waitcnt vmcnt(3) lgkmcnt(2)
s_barrier
mvv 12,208,40
R 24,0
s_add_u32 m0, s50, 32768
mvv 16,212,40
ds_read_b128 v[76:79], v92 offset:0
D 85,0
mvv 20,216,40
mvv 12,220,44
R 28,1024
mvv 16,224,44
mvv 20,228,44
D 85,1024
s_waitcnt lgkmcnt(3)
mvv 12,232,48
R 32,2048
mvv 16,236,48
mvv 20,240,48
s_waitcnt lgkmcnt(2)
v_add_f32 v76, v76, v77
mvv 12,244,52
R 36,3072
v_add_f32 v78, v78, v79
mvv 16,248,52
D 85,2048
v_add_f32 v76, v76, v78
mvv 20,252,52
s_add_u32 s60, s60, 1
s_cbranch_scc0 LO1_1
mav 0,0,24
R 40,4096
v_add_f32_dpp v77, v76, v76 quad_perm:[1,0,3,2] row_mask:0xf bank_mask:0xf
mav 4,4,24
buffer_store_dword v77, v93, s[36:39], s49 offen sc1
v_cvt_pk_bf16_f32 v62, v12, v13
mav 8,8,24
D 85,3072
s_waitcnt lgkmcnt(2)
mav 0,12,28
R 44,5120
v_cvt_pk_bf16_f32 v63, v14, v15
mav 4,16,28
v_cvt_pk_bf16_f32 v64, v16, v17
v_cvt_pk_bf16_f32 v65, v18, v19
mav 8,20,28
s_add_u32 m0, s50, 36864
v_cvt_pk_bf16_f32 v66, v20, v21
mav 0,24,32
R 48,6144
buffer_store_dwordx4 v[62:65], v83, s[36:39], s49 offen sc1
mav 4,28,32
v_cvt_pk_bf16_f32 v67, v22, v23
v_cvt_pk_bf16_f32 v68, v20, v21
mav 8,32,32
v_cvt_pk_bf16_f32 v69, v22, v23
s_waitcnt lgkmcnt(2)
mav 0,36,36
R 52,7168
v_permlane16_swap_b32 v66, v68
mav 4,40,36
D 86,0
v_permlane16_swap_b32 v67, v69
mav 8,44,36
v_mul_f32 v73, v12, v12
v_add_f32 v72, v13, v12
mav 0,48,40
R 24,8192
v_fmac_f32 v73, v13, v13
mav 4,52,40
v_add_f32 v72, v14, v72
v_fmac_f32 v73, v14, v14
mav 8,56,40
buffer_store_dwordx4 v[66:69], v84, s[36:39], s49 offen sc1
s_waitcnt lgkmcnt(2)
mav 0,60,44
R 28,9216
v_add_f32 v72, v15, v72
mav 4,64,44
v_fmac_f32 v73, v15, v15
v_add_f32 v72, v16, v72
mav 8,68,44
v_fmac_f32 v73, v16, v16
v_add_f32 v72, v17, v72
mav 0,72,48
R 32,10240
D 86,1024
mav 4,76,48
v_fmac_f32 v73, v17, v17
v_add_f32 v72, v18, v72
mav 8,80,48
v_fmac_f32 v73, v18, v18
s_waitcnt lgkmcnt(2)
mav 0,84,52
R 36,11264
v_add_f32 v72, v19, v72
mav 4,88,52
D 86,2048
v_fmac_f32 v73, v19, v19
mav 8,92,52
v_add_f32 v72, v20, v72
v_fmac_f32 v73, v20, v20
mav 0,96,24
R 40,12288
v_add_f32 v72, v21, v72
mav 4,100,24
v_fmac_f32 v73, v21, v21
v_add_f32 v72, v22, v72
mav 8,104,24
D 86,3072
s_waitcnt lgkmcnt(2)
mav 0,108,28
R 44,13312
v_fmac_f32 v73, v22, v22
mav 4,112,28
v_add_f32 v72, v23, v72
v_fmac_f32 v73, v23, v23
mav 8,116,28
mav 0,120,32
R 48,14336
v_permlane32_swap_b32 v72, v73
mav 4,124,32
v_add_f32 v72, v72, v73
mav 8,128,32
s_waitcnt lgkmcnt(2)
mav 0,132,36
R 52,15360
mav 4,136,36
mav 8,140,36
mav 0,144,40
R 24,16384
mav 4,148,40
mav 8,152,40
s_waitcnt lgkmcnt(2)
mav 0,156,44
R 28,17408
mav 4,160,44
mav 8,164,44
mav 0,168,48
R 32,18432
mav 4,172,48
mav 8,176,48
s_waitcnt lgkmcnt(2)
mav 0,180,52
R 36,19456
mav 4,184,52
mav 8,188,52
mav 0,192,24
R 40,20480
mav 4,196,24
mav 8,200,24
s_waitcnt lgkmcnt(2)
mav 0,204,28
R 44,21504
mav 4,208,28
s_waitcnt vmcnt(13)
v_lshlrev_b32 v12, 16, v56
mav 8,212,28
v_and_b32 v13, 0xffff0000, v56
v_lshlrev_b32 v14, 16, v57
mav 0,216,32
R 48,22528
v_and_b32 v15, 0xffff0000, v57
mav 4,220,32
v_lshlrev_b32 v16, 16, v58
v_and_b32 v17, 0xffff0000, v58
mav 8,224,32
v_lshlrev_b32 v18, 16, v59
s_waitcnt lgkmcnt(2)
mav 0,228,36
R 52,23552
v_and_b32 v19, 0xffff0000, v59
mav 4,232,36
s_waitcnt vmcnt(12)
v_lshlrev_b32 v20, 16, v60
mav 8,236,36
v_and_b32 v21, 0xffff0000, v60
v_lshlrev_b32 v22, 16, v61
mav 0,240,40
R 24,24576
v_and_b32 v23, 0xffff0000, v61
mav 4,244,40
mav 8,248,40
s_waitcnt lgkmcnt(2)
mav 0,252,44
R 28,25600
mvv 4,128,44
v_add_u32 v70, 1, v70
mvv 8,132,44
mvv 0,136,48
R 32,26624
mvv 4,140,48
mvv 8,144,48
s_waitcnt lgkmcnt(2)
mvv 0,148,52
R 36,27648
mvv 4,152,52
s_waitcnt vmcnt(3)
buffer_store_dwordx2 v[70:71], v81, s[36:39], s52 offen sc1
mvv 8,156,52
mvv 0,160,24
R 40,28672
mvv 4,164,24
mvv 8,168,24
s_waitcnt lgkmcnt(2)
mvv 0,172,28
R 44,29696
mvv 4,176,28
ds_write_b32 v91, v72 offset:1024
mvv 8,180,28
mvv 0,184,32
R 48,30720
mvv 4,188,32
mvv 8,192,32
s_waitcnt lgkmcnt(3)
mvv 0,196,36
R 52,31744
mvv 4,200,36
s_add_u32 s49, s49, 346112
mvv 8,204,36
s_waitcnt vmcnt(1) lgkmcnt(2)
s_barrier
mvv 0,208,40
R 24,32768
ds_read_b128 v[76:79], v92 offset:1024
mvv 4,212,40
mvv 8,216,40
mvv 0,220,44
R 28,33792
mvv 4,224,44
mvv 8,228,44
s_waitcnt lgkmcnt(3)
mvv 0,232,48
R 32,34816
mvv 4,236,48
s_waitcnt lgkmcnt(2)
v_add_f32 v76, v76, v77
mvv 8,240,48
v_add_f32 v78, v78, v79
v_add_f32 v76, v76, v78
mvv 0,244,52
R 36,35840
v_add_f32_dpp v77, v76, v76 quad_perm:[1,0,3,2] row_mask:0xf bank_mask:0xf
mvv 4,248,52
buffer_store_dword v77, v93, s[36:39], s49 offen sc1
mvv 8,252,52
mav 12,0,24
R 40,36864
mav 16,4,24
v_cvt_pk_bf16_f32 v62, v0, v1
v_cvt_pk_bf16_f32 v63, v2, v3
mav 20,8,24
v_cvt_pk_bf16_f32 v64, v4, v5
s_waitcnt lgkmcnt(2)
mav 12,12,28
R 44,37888
v_cvt_pk_bf16_f32 v65, v6, v7
mav 16,16,28
buffer_store_dwordx4 v[62:65], v83, s[36:39], s49 offen sc1
v_cvt_pk_bf16_f32 v66, v8, v9
mav 20,20,28
v_cvt_pk_bf16_f32 v67, v10, v11
v_cvt_pk_bf16_f32 v68, v8, v9
mav 12,24,32
R 48,38912
v_cvt_pk_bf16_f32 v69, v10, v11
mav 16,28,32
v_permlane16_swap_b32 v66, v68
v_permlane16_swap_b32 v67, v69
mav 20,32,32
buffer_store_dwordx4 v[66:69], v84, s[36:39], s49 offen sc1
s_waitcnt lgkmcnt(2)
mav 12,36,36
R 52,39936
v_mul_f32 v73, v0, v0
mav 16,40,36
v_add_f32 v72, v1, v0
v_fmac_f32 v73, v1, v1
mav 20,44,36
v_add_f32 v72, v2, v72
v_fmac_f32 v73, v2, v2
mav 12,48,40
R 24,40960
v_add_f32 v72, v3, v72
mav 16,52,40
v_fmac_f32 v73, v3, v3
v_add_f32 v72, v4, v72
mav 20,56,40
v_fmac_f32 v73, v4, v4
s_waitcnt lgkmcnt(2)
mav 12,60,44
R 28,41984
v_add_f32 v72, v5, v72
mav 16,64,44
v_fmac_f32 v73, v5, v5
v_add_f32 v72, v6, v72
mav 20,68,44
v_fmac_f32 v73, v6, v6
v_add_f32 v72, v7, v72
mav 12,72,48
R 32,43008
v_fmac_f32 v73, v7, v7
mav 16,76,48
v_add_f32 v72, v8, v72
v_fmac_f32 v73, v8, v8
mav 20,80,48
v_add_f32 v72, v9, v72
s_waitcnt lgkmcnt(2)
mav 12,84,52
R 36,44032
v_fmac_f32 v73, v9, v9
mav 16,88,52
v_add_f32 v72, v10, v72
v_fmac_f32 v73, v10, v10
mav 20,92,52
v_add_f32 v72, v11, v72
v_fmac_f32 v73, v11, v11
mav 12,96,24
R 40,45056
v_permlane32_swap_b32 v72, v73
mav 16,100,24
v_add_f32 v72, v72, v73
mav 20,104,24
s_waitcnt lgkmcnt(2)
mav 12,108,28
R 44,46080
mav 16,112,28
mav 20,116,28
mav 12,120,32
R 48,47104
mav 16,124,32
mav 20,128,32
s_waitcnt lgkmcnt(2)
mav 12,132,36
R 52,48128
mav 16,136,36
mav 20,140,36
mav 12,144,40
R 24,49152
mav 16,148,40
mav 20,152,40
s_waitcnt lgkmcnt(2)
mav 12,156,44
R 28,50176
mav 16,160,44
mav 20,164,44
mav 12,168,48
R 32,51200
mav 16,172,48
mav 20,176,48
s_waitcnt lgkmcnt(2)
mav 12,180,52
R 36,52224
mav 16,184,52
mav 20,188,52
mav 12,192,24
R 40,53248
mav 16,196,24
mav 20,200,24
s_waitcnt lgkmcnt(2)
mav 12,204,28
R 44,54272
mav 16,208,28
mav 20,212,28
mav 12,216,32
R 48,55296
mav 16,220,32
mav 20,224,32
s_waitcnt lgkmcnt(2)
mav 12,228,36
R 52,56320
mav 16,232,36
mav 20,236,36
mav 12,240,40
R 24,57344
mav 16,244,40
mav 20,248,40
s_waitcnt lgkmcnt(2)
mav 12,252,44
R 28,58368
mvv 16,128,44
v_add_u32 v70, 1, v70
mvv 20,132,44
mvv 12,136,48
R 32,59392
mvv 16,140,48
mvv 20,144,48
s_waitcnt lgkmcnt(2)
mvv 12,148,52
R 36,60416
mvv 16,152,52
s_waitcnt vmcnt(0)
buffer_store_dwordx2 v[70:71], v81, s[36:39], s52 offen sc1
mvv 20,156,52
mvv 12,160,24
R 40,61440
mvv 16,164,24
mvv 20,168,24
s_waitcnt lgkmcnt(2)
mvv 12,172,28
R 44,62464
mvv 16,176,28
ds_write_b32 v91, v72 offset:0
mvv 20,180,28
mvv 12,184,32
R 48,63488
mvv 16,188,32
mvv 20,192,32
s_waitcnt lgkmcnt(3)
mvv 12,196,36
R 52,64512
mvv 16,200,36
s_add_u32 s49, s49, 346112
mvv 20,204,36
s_waitcnt lgkmcnt(2)
s_barrier
mvv 12,208,40
ds_read_b128 v[76:79], v92 offset:0
mvv 16,212,40
mvv 20,216,40
mvv 12,220,44
mvv 16,224,44
mvv 20,228,44
s_waitcnt lgkmcnt(1)
)"
R"(
mvv 12,232,48
mvv 16,236,48
s_waitcnt lgkmcnt(0)
v_add_f32 v76, v76, v77
mvv 20,240,48
v_add_f32 v78, v78, v79
v_add_f32 v76, v76, v78
mvv 12,244,52
mvv 16,248,52
v_add_f32_dpp v77, v76, v76 quad_perm:[1,0,3,2] row_mask:0xf bank_mask:0xf
buffer_store_dword v77, v93, s[36:39], s49 offen sc1
mvv 20,252,52
s_nop 3
v_cvt_pk_bf16_f32 v62, v12, v13
v_cvt_pk_bf16_f32 v63, v14, v15
v_cvt_pk_bf16_f32 v64, v16, v17
v_cvt_pk_bf16_f32 v65, v18, v19
buffer_store_dwordx4 v[62:65], v83, s[36:39], s49 offen sc1
v_cvt_pk_bf16_f32 v66, v20, v21
v_cvt_pk_bf16_f32 v67, v22, v23
v_cvt_pk_bf16_f32 v68, v20, v21
v_cvt_pk_bf16_f32 v69, v22, v23
s_nop 0
v_permlane16_swap_b32 v66, v68
v_permlane16_swap_b32 v67, v69
buffer_store_dwordx4 v[66:69], v84, s[36:39], s49 offen sc1
v_mul_f32 v73, v12, v12
v_add_f32 v72, v13, v12
v_fmac_f32 v73, v13, v13
v_add_f32 v72, v14, v72
v_fmac_f32 v73, v14, v14
v_add_f32 v72, v15, v72
v_fmac_f32 v73, v15, v15
v_add_f32 v72, v16, v72
v_fmac_f32 v73, v16, v16
v_add_f32 v72, v17, v72
v_fmac_f32 v73, v17, v17
v_add_f32 v72, v18, v72
v_fmac_f32 v73, v18, v18
v_add_f32 v72, v19, v72
v_fmac_f32 v73, v19, v19
v_add_f32 v72, v20, v72
v_fmac_f32 v73, v20, v20
v_add_f32 v72, v21, v72
v_fmac_f32 v73, v21, v21
v_add_f32 v72, v22, v72
v_fmac_f32 v73, v22, v22
v_add_f32 v72, v23, v72
v_fmac_f32 v73, v23, v23
s_nop 1
v_permlane32_swap_b32 v72, v73
v_add_f32 v72, v72, v73
ds_write_b32 v91, v72 offset:1024
s_add_u32 s49, s49, 346112
s_waitcnt lgkmcnt(0)
s_barrier
ds_read_b128 v[76:79], v92 offset:1024
s_waitcnt lgkmcnt(0)
v_add_f32 v76, v76, v77
v_add_f32 v78, v78, v79
v_add_f32 v76, v76, v78
s_nop 1
v_add_f32_dpp v77, v76, v76 quad_perm:[1,0,3,2] row_mask:0xf bank_mask:0xf
buffer_store_dword v77, v93, s[36:39], s49 offen sc1
v_add_u32 v70, 37, v70
s_waitcnt vmcnt(0)
buffer_store_dwordx2 v[70:71], v81, s[36:39], s52 offen sc1
s_endpgm
LvO2:
s_barrier
buffer_load_dwordx4 v[56:59], v89, s[36:39], s49 offen sc1
buffer_load_dwordx2 v[60:61], v90, s[36:39], s49 offen sc1
s_add_u32 s49, s49, 346112
s_add_u32 m0, s50, 0
s_nop 0
D 85,0
D 85,1024
D 85,2048
D 85,3072
s_add_u32 m0, s50, 4096
s_nop 0
D 86,0
D 86,1024
D 86,2048
D 86,3072
s_waitcnt vmcnt(9)
v_lshlrev_b32 v0, 16, v56
v_and_b32 v1, 0xffff0000, v56
v_lshlrev_b32 v2, 16, v57
v_and_b32 v3, 0xffff0000, v57
v_lshlrev_b32 v4, 16, v58
v_and_b32 v5, 0xffff0000, v58
v_lshlrev_b32 v6, 16, v59
v_and_b32 v7, 0xffff0000, v59
s_waitcnt vmcnt(8)
v_lshlrev_b32 v8, 16, v60
v_and_b32 v9, 0xffff0000, v60
v_lshlrev_b32 v10, 16, v61
v_and_b32 v11, 0xffff0000, v61
buffer_load_dwordx4 v[56:59], v89, s[36:39], s49 offen sc1
buffer_load_dwordx2 v[60:61], v90, s[36:39], s49 offen sc1
s_add_u32 s49, s49, 346112
s_waitcnt vmcnt(2)
s_barrier
s_add_u32 m0, s50, 32768
R 24,0
D 85,0
R 28,1024
D 85,1024
R 32,2048
R 36,3072
D 85,2048
s_waitcnt lgkmcnt(2)
mav 0,0,24
R 40,4096
mav 4,4,24
mav 8,8,24
D 85,3072
mav 0,12,28
R 44,5120
mav 4,16,28
mav 8,20,28
s_add_u32 m0, s50, 36864
s_waitcnt lgkmcnt(2)
mav 0,24,32
R 48,6144
mav 4,28,32
D 86,0
mav 8,32,32
mav 0,36,36
R 52,7168
mav 4,40,36
mav 8,44,36
D 86,1024
s_waitcnt lgkmcnt(2)
mav 0,48,40
R 24,8192
mav 4,52,40
mav 8,56,40
mav 0,60,44
R 28,9216
mav 4,64,44
D 86,2048
mav 8,68,44
s_waitcnt lgkmcnt(2)
mav 0,72,48
R 32,10240
mav 4,76,48
mav 8,80,48
D 86,3072
mav 0,84,52
R 36,11264
mav 4,88,52
mav 8,92,52
s_waitcnt lgkmcnt(2)
mav 0,96,24
R 40,12288
mav 4,100,24
mav 8,104,24
mav 0,108,28
R 44,13312
mav 4,112,28
mav 8,116,28
s_waitcnt lgkmcnt(2)
mav 0,120,32
R 48,14336
mav 4,124,32
mav 8,128,32
mav 0,132,36
R 52,15360
mav 4,136,36
mav 8,140,36
s_waitcnt lgkmcnt(2)
mav 0,144,40
R 24,16384
mav 4,148,40
mav 8,152,40
mav 0,156,44
R 28,17408
mav 4,160,44
mav 8,164,44
s_waitcnt lgkmcnt(2)
mav 0,168,48
R 32,18432
mav 4,172,48
mav 8,176,48
mav 0,180,52
R 36,19456
mav 4,184,52
mav 8,188,52
s_waitcnt lgkmcnt(2)
mav 0,192,24
R 40,20480
mav 4,196,24
mav 8,200,24
s_waitcnt vmcnt(9)
v_lshlrev_b32 v12, 16, v56
mav 0,204,28
R 44,21504
v_and_b32 v13, 0xffff0000, v56
mav 4,208,28
v_lshlrev_b32 v14, 16, v57
v_and_b32 v15, 0xffff0000, v57
mav 8,212,28
v_lshlrev_b32 v16, 16, v58
s_waitcnt lgkmcnt(2)
mav 0,216,32
R 48,22528
v_and_b32 v17, 0xffff0000, v58
mav 4,220,32
v_lshlrev_b32 v18, 16, v59
v_and_b32 v19, 0xffff0000, v59
mav 8,224,32
s_waitcnt vmcnt(8)
v_lshlrev_b32 v20, 16, v60
mav 0,228,36
R 52,23552
v_and_b32 v21, 0xffff0000, v60
mav 4,232,36
v_lshlrev_b32 v22, 16, v61
v_and_b32 v23, 0xffff0000, v61
mav 8,236,36
s_waitcnt lgkmcnt(2)
mav 0,240,40
R 24,24576
mav 4,244,40
mav 8,248,40
mav 0,252,44
R 28,25600
mvv 4,128,44
buffer_load_dwordx4 v[56:59], v89, s[36:39], s49 offen sc1
mvv 8,132,44
s_waitcnt lgkmcnt(2)
mvv 0,136,48
R 32,26624
mvv 4,140,48
mvv 8,144,48
buffer_load_dwordx2 v[60:61], v90, s[36:39], s49 offen sc1
mvv 0,148,52
R 36,27648
mvv 4,152,52
mvv 8,156,52
s_waitcnt lgkmcnt(2)
mvv 0,160,24
R 40,28672
mvv 4,164,24
mvv 8,168,24
mvv 0,172,28
R 44,29696
mvv 4,176,28
mvv 8,180,28
s_waitcnt lgkmcnt(2)
mvv 0,184,32
R 48,30720
s_xor_b32 s50, s50, 0x10000
mvv 4,188,32
mvv 8,192,32
mvv 0,196,36
R 52,31744
mvv 4,200,36
s_add_u32 s49, s49, 346112
mvv 8,204,36
s_waitcnt vmcnt(2)
s_barrier
s_waitcnt lgkmcnt(2)
mvv 0,208,40
R 24,32768
s_add_u32 m0, s50, 0
mvv 4,212,40
D 85,0
mvv 8,216,40
mvv 0,220,44
R 28,33792
mvv 4,224,44
mvv 8,228,44
D 85,1024
s_waitcnt lgkmcnt(2)
mvv 0,232,48
R 32,34816
mvv 4,236,48
mvv 8,240,48
mvv 0,244,52
R 36,35840
mvv 4,248,52
D 85,2048
mvv 8,252,52
s_waitcnt lgkmcnt(2)
mav 12,0,24
R 40,36864
mav 16,4,24
v_cvt_pk_bf16_f32 v62, v0, v1
v_cvt_pk_bf16_f32 v63, v2, v3
mav 20,8,24
D 85,3072
v_cvt_pk_bf16_f32 v64, v4, v5
mav 12,12,28
R 44,37888
v_cvt_pk_bf16_f32 v65, v6, v7
mav 16,16,28
v_cvt_pk_bf16_f32 v66, v8, v9
v_cvt_pk_bf16_f32 v67, v10, v11
mav 20,20,28
s_add_u32 m0, s50, 4096
s_waitcnt lgkmcnt(2)
mav 12,24,32
R 48,38912
buffer_store_dwordx4 v[62:65], v83, s[36:39], s49 offen sc1
mav 16,28,32
v_cvt_pk_bf16_f32 v68, v8, v9
v_cvt_pk_bf16_f32 v69, v10, v11
mav 20,32,32
v_permlane16_swap_b32 v66, v68
v_permlane16_swap_b32 v67, v69
mav 12,36,36
R 52,39936
v_mul_f32 v73, v0, v0
mav 16,40,36
buffer_store_dwordx4 v[66:69], v84, s[36:39], s49 offen sc1
v_add_f32 v72, v1, v0
mav 20,44,36
v_fmac_f32 v73, v1, v1
s_waitcnt lgkmcnt(2)
mav 12,48,40
R 24,40960
v_add_f32 v72, v2, v72
mav 16,52,40
v_fmac_f32 v73, v2, v2
v_add_f32 v72, v3, v72
mav 20,56,40
D 86,0
v_fmac_f32 v73, v3, v3
mav 12,60,44
R 28,41984
v_add_f32 v72, v4, v72
mav 16,64,44
v_fmac_f32 v73, v4, v4
v_add_f32 v72, v5, v72
mav 20,68,44
v_fmac_f32 v73, v5, v5
s_waitcnt lgkmcnt(2)
mav 12,72,48
R 32,43008
D 86,1024
mav 16,76,48
v_add_f32 v72, v6, v72
v_fmac_f32 v73, v6, v6
mav 20,80,48
v_add_f32 v72, v7, v72
v_fmac_f32 v73, v7, v7
mav 12,84,52
R 36,44032
v_add_f32 v72, v8, v72
mav 16,88,52
D 86,2048
v_fmac_f32 v73, v8, v8
mav 20,92,52
v_add_f32 v72, v9, v72
s_waitcnt lgkmcnt(2)
mav 12,96,24
R 40,45056
v_fmac_f32 v73, v9, v9
mav 16,100,24
v_add_f32 v72, v10, v72
v_fmac_f32 v73, v10, v10
mav 20,104,24
D 86,3072
v_add_f32 v72, v11, v72
mav 12,108,28
R 44,46080
v_fmac_f32 v73, v11, v11
mav 16,112,28
mav 20,116,28
v_permlane32_swap_b32 v72, v73
s_waitcnt lgkmcnt(2)
mav 12,120,32
R 48,47104
v_add_f32 v72, v72, v73
mav 16,124,32
mav 20,128,32
mav 12,132,36
R 52,48128
mav 16,136,36
mav 20,140,36
s_waitcnt lgkmcnt(2)
mav 12,144,40
R 24,49152
mav 16,148,40
mav 20,152,40
mav 12,156,44
R 28,50176
mav 16,160,44
mav 20,164,44
s_waitcnt lgkmcnt(2)
mav 12,168,48
R 32,51200
mav 16,172,48
mav 20,176,48
mav 12,180,52
R 36,52224
mav 16,184,52
mav 20,188,52
s_waitcnt lgkmcnt(2)
mav 12,192,24
R 40,53248
mav 16,196,24
mav 20,200,24
s_waitcnt vmcnt(11)
v_lshlrev_b32 v0, 16, v56
mav 12,204,28
R 44,54272
v_and_b32 v1, 0xffff0000, v56
mav 16,208,28
v_lshlrev_b32 v2, 16, v57
v_and_b32 v3, 0xffff0000, v57
mav 20,212,28
v_lshlrev_b32 v4, 16, v58
s_waitcnt lgkmcnt(2)
mav 12,216,32
R 48,55296
v_and_b32 v5, 0xffff0000, v58
mav 16,220,32
v_lshlrev_b32 v6, 16, v59
v_and_b32 v7, 0xffff0000, v59
mav 20,224,32
s_waitcnt vmcnt(10)
v_lshlrev_b32 v8, 16, v60
mav 12,228,36
R 52,56320
v_and_b32 v9, 0xffff0000, v60
mav 16,232,36
v_lshlrev_b32 v10, 16, v61
v_and_b32 v11, 0xffff0000, v61
mav 20,236,36
s_waitcnt lgkmcnt(2)
mav 12,240,40
R 24,57344
mav 16,244,40
mav 20,248,40
mav 12,252,44
R 28,58368
mvv 16,128,44
buffer_load_dwordx4 v[56:59], v89, s[36:39], s49 offen sc1
v_add_u32 v70, 1, v70
mvv 20,132,44
s_waitcnt lgkmcnt(2)
mvv 12,136,48
R 32,59392
mvv 16,140,48
mvv 20,144,48
buffer_load_dwordx2 v[60:61], v90, s[36:39], s49 offen sc1
mvv 12,148,52
R 36,60416
mvv 16,152,52
s_waitcnt vmcnt(6)
buffer_store_dwordx2 v[70:71], v81, s[36:39], s52 offen sc1
mvv 20,156,52
s_waitcnt lgkmcnt(2)
mvv 12,160,24
R 40,61440
mvv 16,164,24
mvv 20,168,24
ds_write_b32 v91, v72 offset:0
mvv 12,172,28
R 44,62464
mvv 16,176,28
mvv 20,180,28
s_waitcnt lgkmcnt(3)
mvv 12,184,32
R 48,63488
mvv 16,188,32
mvv 20,192,32
mvv 12,196,36
R 52,64512
v_xor_b32 v80, 0x10000, v80
mvv 16,200,36
s_add_u32 s49, s49, 346112
mvv 20,204,36
s_waitcnt vmcnt(3) lgkmcnt(2)
s_barrier
mvv 12,208,40
R 24,0
s_add_u32 m0, s50, 32768
mvv 16,212,40
D 85,0
mvv 20,216,40
mvv 12,220,44
R 28,1024
mvv 16,224,44
mvv 20,228,44
D 85,1024
s_waitcnt lgkmcnt(2)
mvv 12,232,48
R 32,2048
mvv 16,236,48
mvv 20,240,48
mvv 12,244,52
R 36,3072
mvv 16,248,52
D 85,2048
mvv 20,252,52
s_mov_b32 s60, -30
.p2align 6
LO2_1:
s_waitcnt lgkmcnt(2)
mav 0,0,24
R 40,4096
mav 4,4,24
v_cvt_pk_bf16_f32 v62, v12, v13
v_cvt_pk_bf16_f32 v63, v14, v15
mav 8,8,24
D 85,3072
v_cvt_pk_bf16_f32 v64, v16, v17
mav 0,12,28
R 44,5120
v_cvt_pk_bf16_f32 v65, v18, v19
mav 4,16,28
v_cvt_pk_bf16_f32 v66, v20, v21
v_cvt_pk_bf16_f32 v67, v22, v23
mav 8,20,28
s_add_u32 m0, s50, 36864
s_waitcnt lgkmcnt(2)
mav 0,24,32
R 48,6144
buffer_store_dwordx4 v[62:65], v83, s[36:39], s49 offen sc1
mav 4,28,32
v_cvt_pk_bf16_f32 v68, v20, v21
v_cvt_pk_bf16_f32 v69, v22, v23
mav 8,32,32
v_permlane16_swap_b32 v66, v68
v_permlane16_swap_b32 v67, v69
mav 0,36,36
R 52,7168
v_mul_f32 v73, v12, v12
mav 4,40,36
buffer_store_dwordx4 v[66:69], v84, s[36:39], s49 offen sc1
v_add_f32 v72, v13, v12
mav 8,44,36
v_fmac_f32 v73, v13, v13
s_waitcnt lgkmcnt(2)
mav 0,48,40
R 24,8192
v_add_f32 v72, v14, v72
mav 4,52,40
v_fmac_f32 v73, v14, v14
v_add_f32 v72, v15, v72
mav 8,56,40
D 86,0
v_fmac_f32 v73, v15, v15
mav 0,60,44
R 28,9216
v_add_f32 v72, v16, v72
mav 4,64,44
v_fmac_f32 v73, v16, v16
v_add_f32 v72, v17, v72
mav 8,68,44
v_fmac_f32 v73, v17, v17
s_waitcnt lgkmcnt(2)
mav 0,72,48
R 32,10240
D 86,1024
mav 4,76,48
v_add_f32 v72, v18, v72
v_fmac_f32 v73, v18, v18
mav 8,80,48
v_add_f32 v72, v19, v72
v_fmac_f32 v73, v19, v19
mav 0,84,52
R 36,11264
v_add_f32 v72, v20, v72
mav 4,88,52
D 86,2048
v_fmac_f32 v73, v20, v20
mav 8,92,52
v_add_f32 v72, v21, v72
s_waitcnt lgkmcnt(2)
mav 0,96,24
R 40,12288
v_fmac_f32 v73, v21, v21
mav 4,100,24
v_add_f32 v72, v22, v72
v_fmac_f32 v73, v22, v22
mav 8,104,24
D 86,3072
v_add_f32 v72, v23, v72
mav 0,108,28
R 44,13312
v_fmac_f32 v73, v23, v23
mav 4,112,28
mav 8,116,28
v_permlane32_swap_b32 v72, v73
s_waitcnt lgkmcnt(2)
mav 0,120,32
R 48,14336
v_add_f32 v72, v72, v73
mav 4,124,32
mav 8,128,32
mav 0,132,36
R 52,15360
mav 4,136,36
mav 8,140,36
s_waitcnt lgkmcnt(2)
mav 0,144,40
R 24,16384
mav 4,148,40
mav 8,152,40
mav 0,156,44
R 28,17408
mav 4,160,44
mav 8,164,44
s_waitcnt lgkmcnt(2)
mav 0,168,48
R 32,18432
mav 4,172,48
mav 8,176,48
mav 0,180,52
R 36,19456
mav 4,184,52
mav 8,188,52
s_waitcnt lgkmcnt(2)
mav 0,192,24
R 40,20480
mav 4,196,24
mav 8,200,24
s_waitcnt vmcnt(12)
v_lshlrev_b32 v12, 16, v56
mav 0,204,28
R 44,21504
v_and_b32 v13, 0xffff0000, v56
mav 4,208,28
v_lshlrev_b32 v14, 16, v57
v_and_b32 v15, 0xffff0000, v57
mav 8,212,28
v_lshlrev_b32 v16, 16, v58
s_waitcnt lgkmcnt(2)
mav 0,216,32
R 48,22528
v_and_b32 v17, 0xffff0000, v58
mav 4,220,32
v_lshlrev_b32 v18, 16, v59
v_and_b32 v19, 0xffff0000, v59
mav 8,224,32
s_waitcnt vmcnt(11)
v_lshlrev_b32 v20, 16, v60
mav 0,228,36
R 52,23552
v_and_b32 v21, 0xffff0000, v60
mav 4,232,36
v_lshlrev_b32 v22, 16, v61
v_and_b32 v23, 0xffff0000, v61
mav 8,236,36
s_waitcnt lgkmcnt(2)
mav 0,240,40
R 24,24576
mav 4,244,40
mav 8,248,40
mav 0,252,44
R 28,25600
mvv 4,128,44
buffer_load_dwordx4 v[56:59], v89, s[36:39], s49 offen sc1
v_add_u32 v70, 1, v70
mvv 8,132,44
s_waitcnt lgkmcnt(2)
mvv 0,136,48
R 32,26624
mvv 4,140,48
mvv 8,144,48
buffer_load_dwordx2 v[60:61], v90, s[36:39], s49 offen sc1
mvv 0,148,52
R 36,27648
mvv 4,152,52
s_waitcnt vmcnt(6)
buffer_store_dwordx2 v[70:71], v81, s[36:39], s52 offen sc1
mvv 8,156,52
s_waitcnt lgkmcnt(2)
mvv 0,160,24
R 40,28672
mvv 4,164,24
mvv 8,168,24
ds_write_b32 v91, v72 offset:1024
mvv 0,172,28
R 44,29696
mvv 4,176,28
mvv 8,180,28
s_waitcnt lgkmcnt(3)
mvv 0,184,32
R 48,30720
s_xor_b32 s50, s50, 0x10000
mvv 4,188,32
mvv 8,192,32
mvv 0,196,36
R 52,31744
mvv 4,200,36
s_add_u32 s49, s49, 346112
mvv 8,204,36
s_waitcnt vmcnt(3) lgkmcnt(2)
s_barrier
mvv 0,208,40
R 24,32768
s_add_u32 m0, s50, 0
mvv 4,212,40
D 85,0
mvv 8,216,40
mvv 0,220,44
R 28,33792
mvv 4,224,44
mvv 8,228,44
D 85,1024
s_waitcnt lgkmcnt(2)
mvv 0,232,48
R 32,34816
mvv 4,236,48
mvv 8,240,48
mvv 0,244,52
R 36,35840
mvv 4,248,52
D 85,2048
mvv 8,252,52
s_waitcnt lgkmcnt(2)
mav 12,0,24
R 40,36864
mav 16,4,24
v_cvt_pk_bf16_f32 v62, v0, v1
v_cvt_pk_bf16_f32 v63, v2, v3
mav 20,8,24
D 85,3072
v_cvt_pk_bf16_f32 v64, v4, v5
mav 12,12,28
R 44,37888
v_cvt_pk_bf16_f32 v65, v6, v7
mav 16,16,28
v_cvt_pk_bf16_f32 v66, v8, v9
v_cvt_pk_bf16_f32 v67, v10, v11
mav 20,20,28
s_add_u32 m0, s50, 4096
s_waitcnt lgkmcnt(2)
mav 12,24,32
R 48,38912
buffer_store_dwordx4 v[62:65], v83, s[36:39], s49 offen sc1
mav 16,28,32
v_cvt_pk_bf16_f32 v68, v8, v9
v_cvt_pk_bf16_f32 v69, v10, v11
mav 20,32,32
v_permlane16_swap_b32 v66, v68
v_permlane16_swap_b32 v67, v69
mav 12,36,36
R 52,39936
v_mul_f32 v73, v0, v0
mav 16,40,36
buffer_store_dwordx4 v[66:69], v84, s[36:39], s49 offen sc1
v_add_f32 v72, v1, v0
mav 20,44,36
v_fmac_f32 v73, v1, v1
s_waitcnt lgkmcnt(2)
mav 12,48,40
R 24,40960
v_add_f32 v72, v2, v72
mav 16,52,40
v_fmac_f32 v73, v2, v2
v_add_f32 v72, v3, v72
mav 20,56,40
D 86,0
v_fmac_f32 v73, v3, v3
mav 12,60,44
R 28,41984
v_add_f32 v72, v4, v72
mav 16,64,44
v_fmac_f32 v73, v4, v4
v_add_f32 v72, v5, v72
mav 20,68,44
v_fmac_f32 v73, v5, v5
s_waitcnt lgkmcnt(2)
mav 12,72,48
R 32,43008
D 86,1024
mav 16,76,48
v_add_f32 v72, v6, v72
v_fmac_f32 v73, v6, v6
mav 20,80,48
v_add_f32 v72, v7, v72
v_fmac_f32 v73, v7, v7
mav 12,84,52
R 36,44032
v_add_f32 v72, v8, v72
mav 16,88,52
D 86,2048
v_fmac_f32 v73, v8, v8
mav 20,92,52
v_add_f32 v72, v9, v72
s_waitcnt lgkmcnt(2)
mav 12,96,24
R 40,45056
v_fmac_f32 v73, v9, v9
mav 16,100,24
v_add_f32 v72, v10, v72
v_fmac_f32 v73, v10, v10
mav 20,104,24
D 86,3072
v_add_f32 v72, v11, v72
mav 12,108,28
R 44,46080
v_fmac_f32 v73, v11, v11
mav 16,112,28
mav 20,116,28
v_permlane32_swap_b32 v72, v73
s_waitcnt lgkmcnt(2)
mav 12,120,32
R 48,47104
v_add_f32 v72, v72, v73
mav 16,124,32
mav 20,128,32
mav 12,132,36
R 52,48128
mav 16,136,36
mav 20,140,36
s_waitcnt lgkmcnt(2)
mav 12,144,40
R 24,49152
mav 16,148,40
mav 20,152,40
mav 12,156,44
R 28,50176
mav 16,160,44
mav 20,164,44
s_waitcnt lgkmcnt(2)
mav 12,168,48
R 32,51200
mav 16,172,48
mav 20,176,48
mav 12,180,52
R 36,52224
mav 16,184,52
mav 20,188,52
s_waitcnt lgkmcnt(2)
mav 12,192,24
R 40,53248
mav 16,196,24
mav 20,200,24
s_waitcnt vmcnt(12)
v_lshlrev_b32 v0, 16, v56
mav 12,204,28
R 44,54272
v_and_b32 v1, 0xffff0000, v56
mav 16,208,28
v_lshlrev_b32 v2, 16, v57
v_and_b32 v3, 0xffff0000, v57
mav 20,212,28
v_lshlrev_b32 v4, 16, v58
s_waitcnt lgkmcnt(2)
mav 12,216,32
R 48,55296
v_and_b32 v5, 0xffff0000, v58
mav 16,220,32
v_lshlrev_b32 v6, 16, v59
v_and_b32 v7, 0xffff0000, v59
mav 20,224,32
s_waitcnt vmcnt(11)
v_lshlrev_b32 v8, 16, v60
mav 12,228,36
R 52,56320
v_and_b32 v9, 0xffff0000, v60
mav 16,232,36
v_lshlrev_b32 v10, 16, v61
v_and_b32 v11, 0xffff0000, v61
mav 20,236,36
s_waitcnt lgkmcnt(2)
mav 12,240,40
R 24,57344
mav 16,244,40
mav 20,248,40
mav 12,252,44
R 28,58368
mvv 16,128,44
buffer_load_dwordx4 v[56:59], v89, s[36:39], s49 offen sc1
v_add_u32 v70, 1, v70
mvv 20,132,44
s_waitcnt lgkmcnt(2)
mvv 12,136,48
R 32,59392
mvv 16,140,48
mvv 20,144,48
buffer_load_dwordx2 v[60:61], v90, s[36:39], s49 offen sc1
mvv 12,148,52
R 36,60416
mvv 16,152,52
s_waitcnt vmcnt(6)
buffer_store_dwordx2 v[70:71], v81, s[36:39], s52 offen sc1
mvv 20,156,52
s_waitcnt lgkmcnt(2)
mvv 12,160,24
R 40,61440
mvv 16,164,24
mvv 20,168,24
ds_write_b32 v91, v72 offset:0
mvv 12,172,28
R 44,62464
mvv 16,176,28
mvv 20,180,28
s_waitcnt lgkmcnt(3)
mvv 12,184,32
R 48,63488
mvv 16,188,32
mvv 20,192,32
mvv 12,196,36
R 52,64512
v_xor_b32 v80, 0x10000, v80
mvv 16,200,36
s_add_u32 s49, s49, 346112
mvv 20,204,36
s_waitcnt vmcnt(3) lgkmcnt(2)
s_barrier
mvv 12,208,40
R 24,0
s_add_u32 m0, s50, 32768
mvv 16,212,40
D 85,0
mvv 20,216,40
mvv 12,220,44
R 28,1024
mvv 16,224,44
mvv 20,228,44
D 85,1024
s_waitcnt lgkmcnt(2)
mvv 12,232,48
R 32,2048
mvv 16,236,48
mvv 20,240,48
mvv 12,244,52
R 36,3072
mvv 16,248,52
D 85,2048
mvv 20,252,52
s_add_u32 s60, s60, 1
s_cbranch_scc0 LO2_1
s_waitcnt lgkmcnt(2)
mav 0,0,24
R 40,4096
mav 4,4,24
v_cvt_pk_bf16_f32 v62, v12, v13
v_cvt_pk_bf16_f32 v63, v14, v15
mav 8,8,24
D 85,3072
v_cvt_pk_bf16_f32 v64, v16, v17
mav 0,12,28
R 44,5120
v_cvt_pk_bf16_f32 v65, v18, v19
mav 4,16,28
v_cvt_pk_bf16_f32 v66, v20, v21
v_cvt_pk_bf16_f32 v67, v22, v23
mav 8,20,28
s_add_u32 m0, s50, 36864
s_waitcnt lgkmcnt(2)
mav 0,24,32
R 48,6144
buffer_store_dwordx4 v[62:65], v83, s[36:39], s49 offen sc1
mav 4,28,32
v_cvt_pk_bf16_f32 v68, v20, v21
v_cvt_pk_bf16_f32 v69, v22, v23
mav 8,32,32
v_permlane16_swap_b32 v66, v68
v_permlane16_swap_b32 v67, v69
mav 0,36,36
R 52,7168
v_mul_f32 v73, v12, v12
mav 4,40,36
buffer_store_dwordx4 v[66:69], v84, s[36:39], s49 offen sc1
v_add_f32 v72, v13, v12
mav 8,44,36
v_fmac_f32 v73, v13, v13
s_waitcnt lgkmcnt(2)
mav 0,48,40
R 24,8192
v_add_f32 v72, v14, v72
mav 4,52,40
v_fmac_f32 v73, v14, v14
v_add_f32 v72, v15, v72
mav 8,56,40
D 86,0
v_fmac_f32 v73, v15, v15
mav 0,60,44
R 28,9216
v_add_f32 v72, v16, v72
mav 4,64,44
v_fmac_f32 v73, v16, v16
v_add_f32 v72, v17, v72
mav 8,68,44
v_fmac_f32 v73, v17, v17
s_waitcnt lgkmcnt(2)
mav 0,72,48
R 32,10240
D 86,1024
mav 4,76,48
v_add_f32 v72, v18, v72
v_fmac_f32 v73, v18, v18
mav 8,80,48
v_add_f32 v72, v19, v72
v_fmac_f32 v73, v19, v19
mav 0,84,52
R 36,11264
v_add_f32 v72, v20, v72
mav 4,88,52
D 86,2048
v_fmac_f32 v73, v20, v20
mav 8,92,52
v_add_f32 v72, v21, v72
s_waitcnt lgkmcnt(2)
mav 0,96,24
R 40,12288
v_fmac_f32 v73, v21, v21
mav 4,100,24
v_add_f32 v72, v22, v72
v_fmac_f32 v73, v22, v22
mav 8,104,24
D 86,3072
v_add_f32 v72, v23, v72
mav 0,108,28
R 44,13312
v_fmac_f32 v73, v23, v23
mav 4,112,28
mav 8,116,28
v_permlane32_swap_b32 v72, v73
s_waitcnt lgkmcnt(2)
mav 0,120,32
R 48,14336
v_add_f32 v72, v72, v73
mav 4,124,32
mav 8,128,32
mav 0,132,36
R 52,15360
mav 4,136,36
mav 8,140,36
s_waitcnt lgkmcnt(2)
mav 0,144,40
R 24,16384
mav 4,148,40
mav 8,152,40
mav 0,156,44
R 28,17408
mav 4,160,44
mav 8,164,44
s_waitcnt lgkmcnt(2)
mav 0,168,48
R 32,18432
mav 4,172,48
mav 8,176,48
mav 0,180,52
R 36,19456
mav 4,184,52
mav 8,188,52
s_waitcnt lgkmcnt(2)
mav 0,192,24
R 40,20480
mav 4,196,24
mav 8,200,24
s_waitcnt vmcnt(12)
v_lshlrev_b32 v12, 16, v56
mav 0,204,28
R 44,21504
v_and_b32 v13, 0xffff0000, v56
mav 4,208,28
v_lshlrev_b32 v14, 16, v57
v_and_b32 v15, 0xffff0000, v57
mav 8,212,28
v_lshlrev_b32 v16, 16, v58
s_waitcnt lgkmcnt(2)
mav 0,216,32
R 48,22528
v_and_b32 v17, 0xffff0000, v58
mav 4,220,32
v_lshlrev_b32 v18, 16, v59
v_and_b32 v19, 0xffff0000, v59
mav 8,224,32
s_waitcnt vmcnt(11)
v_lshlrev_b32 v20, 16, v60
mav 0,228,36
R 52,23552
v_and_b32 v21, 0xffff0000, v60
mav 4,232,36
v_lshlrev_b32 v22, 16, v61
v_and_b32 v23, 0xffff0000, v61
mav 8,236,36
s_waitcnt lgkmcnt(2)
mav 0,240,40
R 24,24576
mav 4,244,40
mav 8,248,40
mav 0,252,44
R 28,25600
mvv 4,128,44
v_add_u32 v70, 1, v70
mvv 8,132,44
s_waitcnt lgkmcnt(2)
mvv 0,136,48
R 32,26624
mvv 4,140,48
mvv 8,144,48
mvv 0,148,52
R 36,27648
mvv 4,152,52
s_waitcnt vmcnt(4)
buffer_store_dwordx2 v[70:71], v81, s[36:39], s52 offen sc1
mvv 8,156,52
s_waitcnt lgkmcnt(2)
mvv 0,160,24
R 40,28672
mvv 4,164,24
mvv 8,168,24
ds_write_b32 v91, v72 offset:1024
mvv 0,172,28
R 44,29696
mvv 4,176,28
mvv 8,180,28
s_waitcnt lgkmcnt(3)
mvv 0,184,32
R 48,30720
mvv 4,188,32
mvv 8,192,32
mvv 0,196,36
R 52,31744
mvv 4,200,36
s_add_u32 s49, s49, 346112
mvv 8,204,36
s_waitcnt vmcnt(1) lgkmcnt(2)
s_barrier
mvv 0,208,40
R 24,32768
mvv 4,212,40
mvv 8,216,40
mvv 0,220,44
R 28,33792
mvv 4,224,44
mvv 8,228,44
s_waitcnt lgkmcnt(2)
mvv 0,232,48
R 32,34816
mvv 4,236,48
mvv 8,240,48
mvv 0,244,52
R 36,35840
mvv 4,248,52
mvv 8,252,52
s_waitcnt lgkmcnt(2)
mav 12,0,24
R 40,36864
mav 16,4,24
mav 20,8,24
v_cvt_pk_bf16_f32 v62, v0, v1
v_cvt_pk_bf16_f32 v63, v2, v3
mav 12,12,28
R 44,37888
v_cvt_pk_bf16_f32 v64, v4, v5
mav 16,16,28
v_cvt_pk_bf16_f32 v65, v6, v7
buffer_store_dwordx4 v[62:65], v83, s[36:39], s49 offen sc1
mav 20,20,28
v_cvt_pk_bf16_f32 v66, v8, v9
s_waitcnt lgkmcnt(2)
mav 12,24,32
R 48,38912
v_cvt_pk_bf16_f32 v67, v10, v11
mav 16,28,32
v_cvt_pk_bf16_f32 v68, v8, v9
v_cvt_pk_bf16_f32 v69, v10, v11
mav 20,32,32
v_permlane16_swap_b32 v66, v68
v_permlane16_swap_b32 v67, v69
mav 12,36,36
R 52,39936
buffer_store_dwordx4 v[66:69], v84, s[36:39], s49 offen sc1
mav 16,40,36
v_mul_f32 v73, v0, v0
v_add_f32 v72, v1, v0
mav 20,44,36
v_fmac_f32 v73, v1, v1
s_waitcnt lgkmcnt(2)
mav 12,48,40
R 24,40960
v_add_f32 v72, v2, v72
mav 16,52,40
v_fmac_f32 v73, v2, v2
v_add_f32 v72, v3, v72
mav 20,56,40
v_fmac_f32 v73, v3, v3
v_add_f32 v72, v4, v72
mav 12,60,44
R 28,41984
v_fmac_f32 v73, v4, v4
mav 16,64,44
v_add_f32 v72, v5, v72
v_fmac_f32 v73, v5, v5
mav 20,68,44
v_add_f32 v72, v6, v72
s_waitcnt lgkmcnt(2)
mav 12,72,48
R 32,43008
v_fmac_f32 v73, v6, v6
mav 16,76,48
v_add_f32 v72, v7, v72
v_fmac_f32 v73, v7, v7
mav 20,80,48
v_add_f32 v72, v8, v72
v_fmac_f32 v73, v8, v8
mav 12,84,52
R 36,44032
v_add_f32 v72, v9, v72
mav 16,88,52
v_fmac_f32 v73, v9, v9
v_add_f32 v72, v10, v72
mav 20,92,52
v_fmac_f32 v73, v10, v10
s_waitcnt lgkmcnt(2)
mav 12,96,24
R 40,45056
v_add_f32 v72, v11, v72
mav 16,100,24
v_fmac_f32 v73, v11, v11
mav 20,104,24
mav 12,108,28
R 44,46080
v_permlane32_swap_b32 v72, v73
mav 16,112,28
v_add_f32 v72, v72, v73
mav 20,116,28
s_waitcnt lgkmcnt(2)
mav 12,120,32
R 48,47104
mav 16,124,32
mav 20,128,32
mav 12,132,36
R 52,48128
mav 16,136,36
mav 20,140,36
s_waitcnt lgkmcnt(2)
mav 12,144,40
R 24,49152
mav 16,148,40
mav 20,152,40
mav 12,156,44
R 28,50176
mav 16,160,44
mav 20,164,44
s_waitcnt lgkmcnt(2)
mav 12,168,48
R 32,51200
mav 16,172,48
mav 20,176,48
mav 12,180,52
R 36,52224
mav 16,184,52
mav 20,188,52
s_waitcnt lgkmcnt(2)
mav 12,192,24
R 40,53248
mav 16,196,24
mav 20,200,24
mav 12,204,28
R 44,54272
mav 16,208,28
mav 20,212,28
s_waitcnt lgkmcnt(2)
mav 12,216,32
R 48,55296
mav 16,220,32
mav 20,224,32
mav 12,228,36
R 52,56320
mav 16,232,36
mav 20,236,36
s_waitcnt lgkmcnt(2)
mav 12,240,40
R 24,57344
mav 16,244,40
mav 20,248,40
mav 12,252,44
R 28,58368
mvv 16,128,44
v_add_u32 v70, 1, v70
mvv 20,132,44
s_waitcnt lgkmcnt(2)
mvv 12,136,48
R 32,59392
mvv 16,140,48
mvv 20,144,48
mvv 12,148,52
R 36,60416
mvv 16,152,52
s_waitcnt vmcnt(0)
buffer_store_dwordx2 v[70:71], v81, s[36:39], s52 offen sc1
mvv 20,156,52
s_waitcnt lgkmcnt(2)
mvv 12,160,24
R 40,61440
mvv 16,164,24
mvv 20,168,24
ds_write_b32 v91, v72 offset:0
mvv 12,172,28
R 44,62464
mvv 16,176,28
mvv 20,180,28
s_waitcnt lgkmcnt(3)
mvv 12,184,32
R 48,63488
mvv 16,188,32
mvv 20,192,32
mvv 12,196,36
R 52,64512
mvv 16,200,36
s_add_u32 s49, s49, 346112
mvv 20,204,36
s_waitcnt lgkmcnt(2)
s_barrier
mvv 12,208,40
mvv 16,212,40
mvv 20,216,40
mvv 12,220,44
mvv 16,224,44
mvv 20,228,44
s_waitcnt lgkmcnt(0)
mvv 12,232,48
mvv 16,236,48
mvv 20,240,48
mvv 12,244,52
mvv 16,248,52
mvv 20,252,52
s_nop 5
v_cvt_pk_bf16_f32 v62, v12, v13
v_cvt_pk_bf16_f32 v63, v14, v15
v_cvt_pk_bf16_f32 v64, v16, v17
v_cvt_pk_bf16_f32 v65, v18, v19
buffer_store_dwordx4 v[62:65], v83, s[36:39], s49 offen sc1
v_cvt_pk_bf16_f32 v66, v20, v21
v_cvt_pk_bf16_f32 v67, v22, v23
v_cvt_pk_bf16_f32 v68, v20, v21
v_cvt_pk_bf16_f32 v69, v22, v23
s_nop 0
v_permlane16_swap_b32 v66, v68
v_permlane16_swap_b32 v67, v69
buffer_store_dwordx4 v[66:69], v84, s[36:39], s49 offen sc1
v_mul_f32 v73, v12, v12
v_add_f32 v72, v13, v12
v_fmac_f32 v73, v13, v13
v_add_f32 v72, v14, v72
v_fmac_f32 v73, v14, v14
v_add_f32 v72, v15, v72
v_fmac_f32 v73, v15, v15
v_add_f32 v72, v16, v72
v_fmac_f32 v73, v16, v16
v_add_f32 v72, v17, v72
v_fmac_f32 v73, v17, v17
v_add_f32 v72, v18, v72
v_fmac_f32 v73, v18, v18
v_add_f32 v72, v19, v72
v_fmac_f32 v73, v19, v19
v_add_f32 v72, v20, v72
v_fmac_f32 v73, v20, v20
v_add_f32 v72, v21, v72
v_fmac_f32 v73, v21, v21
v_add_f32 v72, v22, v72
v_fmac_f32 v73, v22, v22
v_add_f32 v72, v23, v72
v_fmac_f32 v73, v23, v23
s_nop 1
v_permlane32_swap_b32 v72, v73
v_add_f32 v72, v72, v73
v_add_u32 v70, 37, v70
s_waitcnt vmcnt(0)
buffer_store_dwordx2 v[70:71], v81, s[36:39], s52 offen sc1
ds_write_b32 v91, v72 offset:1024
s_add_u32 s49, s49, 346112
s_waitcnt lgkmcnt(0)
s_barrier
s_endpgm
Lr3:
s_cmp_eq_u32 s60, 3
s_cbranch_scc0 Lr4
buffer_load_dword v77, off, s[36:39], 0 sc1
s_waitcnt vmcnt(0)
v_mov_b32 v76, 0x100
v_add_u32 v77, 0x9e3779b9, v77
buffer_store_dwordx2 v[76:77], v81, s[36:39], s52 offen sc1
v_add_u32 v88, v88, v76
s_sleep 32
E 0,65536
E 1,69632
E 2,73728
s_add_u32 s51, s51, 229376
E 0,77824
E 1,81920
E 2,86016
s_add_u32 s51, s51, 229376
E 0,90112
E 1,94208
E 2,98304
s_add_u32 s51, s51, 229376
E 0,102400
E 1,106496
E 2,110592
s_add_u32 s51, s51, 229376
v_mov_b32 v24, s64
v_mov_b32 v25, s64
v_mov_b32 v26, s64
v_mov_b32 v27, s64
s_waitcnt vmcnt(6)
s_barrier
v_add_u32 v4, 65536, v4
v_add_u32 v5, 65536, v5
v_add_u32 v6, 65536, v6
Ta 0,4,0
Ta 4,5,0
Ta 8,6,0
Ta 12,4,12288
Ta 16,5,12288
Ta 20,6,12288
E 0,114688
E 1,118784
E 2,122880
s_add_u32 s51, s51, 229376
E 0,126976
E 1,131072
E 2,135168
s_add_u32 s51, s51, 229376
E 0,139264
E 1,143360
E 2,147456
s_add_u32 s51, s51, 229376
E 0,151552
E 1,155648
E 2,159744
s_add_u32 s51, s51, 229376
s_cmp_ge_u32 s51, s57
s_cselect_b32 s60, 7340032, 0
s_sub_u32 s51, s51, s60
s_waitcnt lgkmcnt(0)
s_waitcnt vmcnt(12)
s_barrier
E 0,65536
Ta 24,4,24576
v_mfma_f32_16x16x32_bf16 v[56:59], a[0:3], v[24:27], 0
Ta 28,5,24576
E 1,69632
v_mfma_f32_16x16x32_bf16 v[60:63], a[4:7], v[24:27], 0
Ta 32,6,24576
Ta 36,4,36864
E 2,73728
s_add_u32 s51, s51, 229376
v_mfma_f32_16x16x32_bf16 v[64:67], a[8:11], v[24:27], 0
Ta 40,5,36864
E 0,77824
Ta 44,6,36864
mav 56,12,24
E 1,81920
mav 60,16,24
E 2,86016
s_add_u32 s51, s51, 229376
mav 64,20,24
s_waitcnt lgkmcnt(0)
s_waitcnt vmcnt(12)
s_barrier
v_add_u32 v4, 49152, v4
v_add_u32 v5, 49152, v5
v_add_u32 v6, 49152, v6
E 0,90112
Ta 48,4,0
mav 56,24,24
Ta 52,5,0
E 1,94208
mav 60,28,24
Ta 56,6,0
Ta 60,4,12288
E 2,98304
s_add_u32 s51, s51, 229376
mav 64,32,24
Ta 64,5,12288
E 0,102400
Ta 68,6,12288
mav 56,36,24
E 1,106496
mav 60,40,24
E 2,110592
s_add_u32 s51, s51, 229376
mav 64,44,24
s_waitcnt lgkmcnt(0)
s_waitcnt vmcnt(12)
s_barrier
E 0,114688
Ta 72,4,24576
mav 56,48,24
Ta 76,5,24576
E 1,118784
mav 60,52,24
Ta 80,6,24576
Ta 84,4,36864
E 2,122880
s_add_u32 s51, s51, 229376
mav 64,56,24
Ta 88,5,36864
E 0,126976
Ta 92,6,36864
mav 56,60,24
E 1,131072
mav 60,64,24
E 2,135168
s_add_u32 s51, s51, 229376
mav 64,68,24
s_waitcnt lgkmcnt(0)
s_waitcnt vmcnt(12)
s_barrier
v_add_u32 v4, -49152, v4
v_add_u32 v5, -49152, v5
v_add_u32 v6, -49152, v6
E 0,139264
Ta 96,4,0
mav 56,72,24
Ta 100,5,0
E 1,143360
mav 60,76,24
Ta 104,6,0
Ta 108,4,12288
E 2,147456
s_add_u32 s51, s51, 229376
mav 64,80,24
Ta 112,5,12288
E 0,151552
Ta 116,6,12288
mav 56,84,24
E 1,155648
mav 60,88,24
E 2,159744
s_add_u32 s51, s51, 229376
s_cmp_ge_u32 s51, s57
s_cselect_b32 s60, 7340032, 0
s_sub_u32 s51, s51, s60
mav 64,92,24
s_waitcnt lgkmcnt(0)
s_waitcnt vmcnt(12)
s_barrier
E 0,65536
Ta 120,4,24576
mav 56,96,24
Ta 124,5,24576
E 1,69632
mav 60,100,24
Ta 128,6,24576
Ta 132,4,36864
E 2,73728
s_add_u32 s51, s51, 229376
mav 64,104,24
Ta 136,5,36864
E 0,77824
Ta 140,6,36864
mav 56,108,24
E 1,81920
mav 60,112,24
E 2,86016
s_add_u32 s51, s51, 229376
mav 64,116,24
s_waitcnt lgkmcnt(0)
s_waitcnt vmcnt(12)
s_barrier
v_add_u32 v4, 49152, v4
v_add_u32 v5, 49152, v5
v_add_u32 v6, 49152, v6
E 0,90112
Ta 144,4,0
mav 56,120,24
Ta 148,5,0
E 1,94208
mav 60,124,24
Ta 152,6,0
Ta 156,4,12288
E 2,98304
s_add_u32 s51, s51, 229376
mav 64,128,24
Ta 160,5,12288
E 0,102400
Ta 164,6,12288
mav 56,132,24
E 1,106496
mav 60,136,24
E 2,110592
s_add_u32 s51, s51, 229376
mav 64,140,24
s_waitcnt lgkmcnt(0)
s_waitcnt vmcnt(12)
s_barrier
E 0,114688
Ta 168,4,24576
mav 56,144,24
Ta 172,5,24576
E 1,118784
mav 60,148,24
Ta 176,6,24576
Ta 180,4,36864
E 2,122880
s_add_u32 s51, s51, 229376
mav 64,152,24
Ta 184,5,36864
E 0,126976
Ta 188,6,36864
mav 56,156,24
E 1,131072
mav 60,160,24
E 2,135168
s_add_u32 s51, s51, 229376
mav 64,164,24
s_waitcnt lgkmcnt(0)
s_waitcnt vmcnt(12)
s_barrier
v_add_u32 v4, -49152, v4
v_add_u32 v5, -49152, v5
v_add_u32 v6, -49152, v6
E 0,139264
Ta 192,4,0
mav 56,168,24
Ta 196,5,0
E 1,143360
mav 60,172,24
Ta 200,6,0
Ta 204,4,12288
E 2,147456
s_add_u32 s51, s51, 229376
mav 64,176,24
Ta 208,5,12288
E 0,151552
Ta 212,6,12288
mav 56,180,24
E 1,155648
mav 60,184,24
E 2,159744
s_add_u32 s51, s51, 229376
s_cmp_ge_u32 s51, s57
s_cselect_b32 s60, 7340032, 0
s_sub_u32 s51, s51, s60
mav 64,188,24
s_waitcnt lgkmcnt(0)
s_waitcnt vmcnt(12)
s_barrier
E 0,65536
Ta 216,4,24576
mav 56,192,24
Ta 220,5,24576
E 1,69632
mav 60,196,24
Ta 224,6,24576
Ta 228,4,36864
E 2,73728
s_add_u32 s51, s51, 229376
mav 64,200,24
Ta 232,5,36864
E 0,77824
Ta 236,6,36864
mav 56,204,24
E 1,81920
mav 60,208,24
E 2,86016
s_add_u32 s51, s51, 229376
mav 64,212,24
s_waitcnt lgkmcnt(0)
s_waitcnt vmcnt(12)
s_barrier
v_add_u32 v4, 49152, v4
v_add_u32 v5, 49152, v5
v_add_u32 v6, 49152, v6
E 0,90112
Ta 240,4,0
mav 56,216,24
Ta 244,5,0
E 1,94208
mav 60,220,24
Ta 248,6,0
Ta 252,4,12288
E 2,98304
s_add_u32 s51, s51, 229376
mav 64,224,24
Tv 128,5,12288
E 0,102400
Tv 132,6,12288
mav 56,228,24
E 1,106496
mav 60,232,24
E 2,110592
s_add_u32 s51, s51, 229376
mav 64,236,24
s_waitcnt lgkmcnt(0)
s_waitcnt vmcnt(12)
s_barrier
E 0,114688
Tv 136,4,24576
mav 56,240,24
Tv 140,5,24576
E 1,118784
mav 60,244,24
Tv 144,6,24576
Tv 148,4,36864
E 2,122880
s_add_u32 s51, s51, 229376
mav 64,248,24
Tv 152,5,36864
E 0,126976
Tv 156,6,36864
mav 56,252,24
E 1,131072
mvv 60,128,24
E 2,135168
s_add_u32 s51, s51, 229376
mvv 64,132,24
s_waitcnt lgkmcnt(0)
s_waitcnt vmcnt(12)
s_barrier
v_add_u32 v4, -49152, v4
v_add_u32 v5, -49152, v5
v_add_u32 v6, -49152, v6
E 0,139264
Tv 160,4,0
mvv 56,136,24
Tv 164,5,0
E 1,143360
mvv 60,140,24
Tv 168,6,0
Tv 172,4,12288
E 2,147456
s_add_u32 s51, s51, 229376
mvv 64,144,24
Tv 176,5,12288
E 0,151552
Tv 180,6,12288
mvv 56,148,24
E 1,155648
mvv 60,152,24
E 2,159744
s_add_u32 s51, s51, 229376
s_cmp_ge_u32 s51, s57
s_cselect_b32 s60, 7340032, 0
s_sub_u32 s51, s51, s60
mvv 64,156,24
s_waitcnt lgkmcnt(0)
s_waitcnt vmcnt(12)
s_barrier
Tv 184,4,24576
mvv 56,160,24
Tv 188,5,24576
mvv 60,164,24
Tv 192,6,24576
Tv 196,4,36864
mvv 64,168,24
Tv 200,5,36864
Tv 204,6,36864
mvv 56,172,24
mvv 60,176,24
mvv 64,180,24
s_waitcnt lgkmcnt(0)
s_waitcnt vmcnt(6)
s_barrier
v_add_u32 v4, 49152, v4
v_add_u32 v5, 49152, v5
v_add_u32 v6, 49152, v6
Tv 208,4,0
mvv 56,184,24
Tv 212,5,0
mvv 60,188,24
Tv 216,6,0
Tv 220,4,12288
mvv 64,192,24
Tv 224,5,12288
Tv 228,6,12288
mvv 56,196,24
mvv 60,200,24
mvv 64,204,24
s_waitcnt lgkmcnt(0)
s_waitcnt vmcnt(0)
s_barrier
Tv 232,4,24576
mvv 56,208,24
Tv 236,5,24576
mvv 60,212,24
Tv 240,6,24576
Tv 244,4,36864
mvv 64,216,24
Tv 248,5,36864
Tv 252,6,36864
mvv 56,220,24
mvv 60,224,24
mvv 64,228,24
s_waitcnt lgkmcnt(0)
mvv 56,232,24
mvv 60,236,24
mvv 64,240,24
mvv 56,244,24
mvv 60,248,24
mvv 64,252,24
s_waitcnt vmcnt(0)
s_barrier
s_nop 7
s_and_b32 s60, s48, 3
s_cmp_eq_u32 s60, 1
s_cbranch_scc1 LvF1
s_cmp_eq_u32 s60, 2
s_cbranch_scc1 LvF2
LvF0:
buffer_load_dwordx2 v[106:107], v87, s[36:39], 0 offen sc1
s_waitcnt vmcnt(0)
ds_write_b32 v82, v106
v_sub_u32 v106, v106, v88
v_xor_b32 v107, v107, v77
v_cmp_lt_u64 vcc, s[62:63], v[106:107]
s_waitcnt lgkmcnt(0)
s_cbranch_vccnz CF0_1
BF0_1:
s_add_u32 s49, s49, 346112
s_barrier
s_add_u32 m0, s50, 0
s_nop 0
D 85,0
D 85,1024
D 85,2048
D 85,3072
s_add_u32 m0, s50, 4096
s_nop 0
D 86,0
D 86,1024
D 86,2048
D 86,3072
s_mov_b32 m0, 142336
s_nop 0
buffer_load_dword v87, s[36:39], 0 offen sc1 lds
v_add_u32 v88, 1, v88
buffer_load_dword v87, s[36:39], 0 offen sc1 lds
buffer_load_dword v87, s[36:39], 0 offen sc1 lds
ds_read_b32 v106, v82 offset:0
s_waitcnt lgkmcnt(0)
v_cmp_gt_i32 vcc, v88, v106
s_waitcnt vmcnt(3)
s_cbranch_vccnz CF0_2
BF0_2:
s_add_u32 s49, s49, 346112
s_barrier
R 24,0
s_add_u32 m0, s50, 32768
R 28,1024
D 85,0
R 32,2048
D 85,1024
R 36,3072
s_waitcnt lgkmcnt(2)
v_mfma_f32_16x16x32_bf16 v[0:3], a[0:3], v[24:27], 0
R 40,4096
D 85,2048
v_mfma_f32_16x16x32_bf16 v[4:7], a[4:7], v[24:27], 0
v_mfma_f32_16x16x32_bf16 v[8:11], a[8:11], v[24:27], 0
mav 0,12,28
R 44,5120
mav 4,16,28
D 85,3072
mav 8,20,28
s_waitcnt lgkmcnt(2)
mav 0,24,32
R 48,6144
mav 4,28,32
s_add_u32 m0, s50, 36864
mav 8,32,32
mav 0,36,36
R 52,7168
D 86,0
mav 4,40,36
mav 8,44,36
s_waitcnt lgkmcnt(2)
mav 0,48,40
R 24,8192
mav 4,52,40
D 86,1024
mav 8,56,40
mav 0,60,44
R 28,9216
mav 4,64,44
mav 8,68,44
s_waitcnt lgkmcnt(2)
mav 0,72,48
R 32,10240
D 86,2048
mav 4,76,48
mav 8,80,48
mav 0,84,52
R 36,11264
mav 4,88,52
D 86,3072
mav 8,92,52
s_waitcnt lgkmcnt(2)
mav 0,96,24
R 40,12288
mav 4,100,24
mav 8,104,24
mav 0,108,28
R 44,13312
mav 4,112,28
mav 8,116,28
s_waitcnt lgkmcnt(2)
mav 0,120,32
R 48,14336
s_mov_b32 m0, 142336
mav 4,124,32
mav 8,128,32
buffer_load_dword v87, s[36:39], 0 offen sc1 lds
mav 0,132,36
R 52,15360
mav 4,136,36
mav 8,140,36
s_waitcnt lgkmcnt(2)
mav 0,144,40
R 24,16384
mav 4,148,40
mav 8,152,40
mav 0,156,44
R 28,17408
mav 4,160,44
v_add_u32 v88, 1, v88
mav 8,164,44
s_waitcnt lgkmcnt(2)
mav 0,168,48
R 32,18432
mav 4,172,48
mav 8,176,48
buffer_load_dword v87, s[36:39], 0 offen sc1 lds
mav 0,180,52
R 36,19456
mav 4,184,52
mav 8,188,52
s_waitcnt lgkmcnt(2)
mav 0,192,24
R 40,20480
mav 4,196,24
mav 8,200,24
mav 0,204,28
R 44,21504
mav 4,208,28
mav 8,212,28
s_waitcnt lgkmcnt(2)
mav 0,216,32
R 48,22528
mav 4,220,32
mav 8,224,32
buffer_load_dword v87, s[36:39], 0 offen sc1 lds
mav 0,228,36
R 52,23552
mav 4,232,36
mav 8,236,36
s_waitcnt lgkmcnt(2)
mav 0,240,40
R 24,24576
mav 4,244,40
mav 8,248,40
mav 0,252,44
R 28,25600
mvv 4,128,44
mvv 8,132,44
s_waitcnt lgkmcnt(2)
mvv 0,136,48
R 32,26624
mvv 4,140,48
mvv 8,144,48
mvv 0,148,52
R 36,27648
mvv 4,152,52
mvv 8,156,52
s_waitcnt lgkmcnt(2)
mvv 0,160,24
R 40,28672
mvv 4,164,24
ds_read_b32 v106, v82 offset:0
mvv 8,168,24
mvv 0,172,28
R 44,29696
mvv 4,176,28
mvv 8,180,28
s_waitcnt lgkmcnt(3)
mvv 0,184,32
R 48,30720
s_xor_b32 s50, s50, 0x10000
mvv 4,188,32
mvv 8,192,32
mvv 0,196,36
R 52,31744
mvv 4,200,36
s_waitcnt lgkmcnt(2)
v_cmp_gt_i32 vcc, v88, v106
mvv 8,204,36
s_waitcnt vmcnt(3)
s_cbranch_vccnz CF0_3
BF0_3:
mvv 0,208,40
s_barrier
R 24,32768
mvv 4,212,40
s_add_u32 s49, s49, 346112
s_add_u32 m0, s50, 0
mvv 8,216,40
ds_read_b64 v[78:79], v89 offset:0
mvv 0,220,44
R 28,33792
D 85,0
mvv 4,224,44
mvv 8,228,44
)"
R"(
s_waitcnt lgkmcnt(3)
mvv 0,232,48
R 32,34816
mvv 4,236,48
D 85,1024
mvv 8,240,48
mvv 0,244,52
R 36,35840
mvv 4,248,52
mvv 8,252,52
s_waitcnt lgkmcnt(2)
v_mfma_f32_16x16x32_bf16 v[12:15], a[0:3], v[24:27], 0
R 40,36864
D 85,2048
v_mfma_f32_16x16x32_bf16 v[16:19], a[4:7], v[24:27], 0
v_fma_f32 v104, v79, v56, v92
v_fma_f32 v0, v0, v78, v104
v_mfma_f32_16x16x32_bf16 v[20:23], a[8:11], v[24:27], 0
v_fma_f32 v105, v79, v57, v93
v_fma_f32 v1, v1, v78, v105
mav 12,12,28
R 44,37888
v_mul_f32_e64 v0, v0, |v0|
mav 16,16,28
D 85,3072
v_mul_f32_e64 v1, v1, |v1|
mav 20,20,28
v_cvt_pk_bf16_f32 v0, v0, v1
s_waitcnt lgkmcnt(2)
mav 12,24,32
R 48,38912
v_pk_max_i16 v68, v0, 0
mav 16,28,32
s_add_u32 m0, s50, 4096
v_fma_f32 v104, v79, v58, v94
mav 20,32,32
v_fma_f32 v2, v2, v78, v104
v_fma_f32 v105, v79, v59, v95
mav 12,36,36
R 52,39936
D 86,0
mav 16,40,36
v_fma_f32 v3, v3, v78, v105
v_mul_f32_e64 v2, v2, |v2|
mav 20,44,36
v_mul_f32_e64 v3, v3, |v3|
s_waitcnt lgkmcnt(2)
mav 12,48,40
R 24,40960
v_cvt_pk_bf16_f32 v2, v2, v3
mav 16,52,40
D 86,1024
v_pk_max_i16 v69, v2, 0
mav 20,56,40
v_fma_f32 v104, v79, v60, v96
v_fma_f32 v4, v4, v78, v104
mav 12,60,44
R 28,41984
v_fma_f32 v105, v79, v61, v97
mav 16,64,44
v_fma_f32 v5, v5, v78, v105
v_mul_f32_e64 v4, v4, |v4|
mav 20,68,44
v_mul_f32_e64 v5, v5, |v5|
s_waitcnt lgkmcnt(2)
mav 12,72,48
R 32,43008
D 86,2048
mav 16,76,48
v_cvt_pk_bf16_f32 v4, v4, v5
v_pk_max_i16 v70, v4, 0
mav 20,80,48
v_fma_f32 v104, v79, v62, v98
v_fma_f32 v6, v6, v78, v104
mav 12,84,52
R 36,44032
v_fma_f32 v105, v79, v63, v99
mav 16,88,52
D 86,3072
v_fma_f32 v7, v7, v78, v105
mav 20,92,52
v_mul_f32_e64 v6, v6, |v6|
s_waitcnt lgkmcnt(2)
mav 12,96,24
R 40,45056
v_mul_f32_e64 v7, v7, |v7|
mav 16,100,24
v_cvt_pk_bf16_f32 v6, v6, v7
v_pk_max_i16 v71, v6, 0
mav 20,104,24
buffer_store_dwordx4 v[68:71], v83, s[36:39], s49 offen sc1
v_fma_f32 v104, v79, v64, v100
mav 12,108,28
R 44,46080
v_fma_f32 v8, v8, v78, v104
mav 16,112,28
v_fma_f32 v105, v79, v65, v101
v_fma_f32 v9, v9, v78, v105
mav 20,116,28
v_mul_f32_e64 v8, v8, |v8|
s_waitcnt lgkmcnt(2)
mav 12,120,32
R 48,47104
s_mov_b32 m0, 142336
mav 16,124,32
v_mul_f32_e64 v9, v9, |v9|
v_cvt_pk_bf16_f32 v8, v8, v9
mav 20,128,32
buffer_load_dword v87, s[36:39], 0 offen sc1 lds
v_pk_max_i16 v72, v8, 0
mav 12,132,36
R 52,48128
v_pk_max_i16 v74, v8, 0
mav 16,136,36
v_fma_f32 v104, v79, v66, v102
v_fma_f32 v10, v10, v78, v104
mav 20,140,36
v_fma_f32 v105, v79, v67, v103
s_waitcnt lgkmcnt(2)
mav 12,144,40
R 24,49152
v_fma_f32 v11, v11, v78, v105
mav 16,148,40
v_mul_f32_e64 v10, v10, |v10|
v_mul_f32_e64 v11, v11, |v11|
mav 20,152,40
v_cvt_pk_bf16_f32 v10, v10, v11
v_pk_max_i16 v73, v10, 0
mav 12,156,44
R 28,50176
v_pk_max_i16 v75, v10, 0
mav 16,160,44
v_permlane16_swap_b32 v72, v74
v_permlane16_swap_b32 v73, v75
mav 20,164,44
buffer_store_dwordx4 v[72:75], v84, s[36:39], s49 offen sc1
s_waitcnt lgkmcnt(2)
mav 12,168,48
R 32,51200
v_add_u32 v88, 1, v88
mav 16,172,48
mav 20,176,48
buffer_load_dword v87, s[36:39], 0 offen sc1 lds
mav 12,180,52
R 36,52224
mav 16,184,52
mav 20,188,52
s_waitcnt lgkmcnt(2)
mav 12,192,24
R 40,53248
mav 16,196,24
mav 20,200,24
mav 12,204,28
R 44,54272
mav 16,208,28
mav 20,212,28
s_waitcnt lgkmcnt(2)
mav 12,216,32
R 48,55296
mav 16,220,32
mav 20,224,32
buffer_load_dword v87, s[36:39], 0 offen sc1 lds
mav 12,228,36
R 52,56320
mav 16,232,36
mav 20,236,36
s_waitcnt lgkmcnt(2)
mav 12,240,40
R 24,57344
mav 16,244,40
mav 20,248,40
mav 12,252,44
R 28,58368
mvv 16,128,44
mvv 20,132,44
s_waitcnt lgkmcnt(2)
mvv 12,136,48
R 32,59392
mvv 16,140,48
mvv 20,144,48
mvv 12,148,52
R 36,60416
mvv 16,152,52
mvv 20,156,52
s_waitcnt lgkmcnt(2)
mvv 12,160,24
R 40,61440
mvv 16,164,24
ds_read_b32 v106, v82 offset:0
mvv 20,168,24
mvv 12,172,28
R 44,62464
mvv 16,176,28
mvv 20,180,28
s_waitcnt lgkmcnt(3)
mvv 12,184,32
R 48,63488
mvv 16,188,32
mvv 20,192,32
mvv 12,196,36
R 52,64512
v_xor_b32 v80, 0x10000, v80
mvv 16,200,36
s_waitcnt lgkmcnt(2)
v_cmp_gt_i32 vcc, v88, v106
mvv 20,204,36
s_waitcnt vmcnt(5)
s_cbranch_vccnz CF0_4
BF0_4:
mvv 12,208,40
s_barrier
R 24,0
mvv 16,212,40
s_add_u32 s49, s49, 346112
s_add_u32 m0, s50, 32768
mvv 20,216,40
ds_read_b64 v[78:79], v89 offset:128
mvv 12,220,44
R 28,1024
D 85,0
mvv 16,224,44
mvv 20,228,44
s_waitcnt lgkmcnt(3)
mvv 12,232,48
R 32,2048
mvv 16,236,48
D 85,1024
mvv 20,240,48
mvv 12,244,52
R 36,3072
mvv 16,248,52
mvv 20,252,52
s_waitcnt lgkmcnt(2)
v_mfma_f32_16x16x32_bf16 v[0:3], a[0:3], v[24:27], 0
R 40,4096
D 85,2048
v_mfma_f32_16x16x32_bf16 v[4:7], a[4:7], v[24:27], 0
v_fma_f32 v104, v79, v56, v92
v_fma_f32 v12, v12, v78, v104
v_mfma_f32_16x16x32_bf16 v[8:11], a[8:11], v[24:27], 0
v_fma_f32 v105, v79, v57, v93
v_fma_f32 v13, v13, v78, v105
mav 0,12,28
R 44,5120
v_mul_f32_e64 v12, v12, |v12|
mav 4,16,28
D 85,3072
v_mul_f32_e64 v13, v13, |v13|
mav 8,20,28
v_cvt_pk_bf16_f32 v12, v12, v13
s_waitcnt lgkmcnt(2)
mav 0,24,32
R 48,6144
v_pk_max_i16 v68, v12, 0
mav 4,28,32
s_add_u32 m0, s50, 36864
v_fma_f32 v104, v79, v58, v94
mav 8,32,32
v_fma_f32 v14, v14, v78, v104
v_fma_f32 v105, v79, v59, v95
mav 0,36,36
R 52,7168
D 86,0
mav 4,40,36
v_fma_f32 v15, v15, v78, v105
v_mul_f32_e64 v14, v14, |v14|
mav 8,44,36
v_mul_f32_e64 v15, v15, |v15|
s_waitcnt lgkmcnt(2)
mav 0,48,40
R 24,8192
v_cvt_pk_bf16_f32 v14, v14, v15
mav 4,52,40
D 86,1024
v_pk_max_i16 v69, v14, 0
mav 8,56,40
v_fma_f32 v104, v79, v60, v96
v_fma_f32 v16, v16, v78, v104
mav 0,60,44
R 28,9216
v_fma_f32 v105, v79, v61, v97
mav 4,64,44
v_fma_f32 v17, v17, v78, v105
v_mul_f32_e64 v16, v16, |v16|
mav 8,68,44
v_mul_f32_e64 v17, v17, |v17|
s_waitcnt lgkmcnt(2)
mav 0,72,48
R 32,10240
D 86,2048
mav 4,76,48
v_cvt_pk_bf16_f32 v16, v16, v17
v_pk_max_i16 v70, v16, 0
mav 8,80,48
v_fma_f32 v104, v79, v62, v98
v_fma_f32 v18, v18, v78, v104
mav 0,84,52
R 36,11264
v_fma_f32 v105, v79, v63, v99
mav 4,88,52
D 86,3072
v_fma_f32 v19, v19, v78, v105
mav 8,92,52
v_mul_f32_e64 v18, v18, |v18|
s_waitcnt lgkmcnt(2)
mav 0,96,24
R 40,12288
v_mul_f32_e64 v19, v19, |v19|
mav 4,100,24
v_cvt_pk_bf16_f32 v18, v18, v19
v_pk_max_i16 v71, v18, 0
mav 8,104,24
buffer_store_dwordx4 v[68:71], v83, s[36:39], s49 offen sc1
v_fma_f32 v104, v79, v64, v100
mav 0,108,28
R 44,13312
v_fma_f32 v20, v20, v78, v104
mav 4,112,28
v_fma_f32 v105, v79, v65, v101
v_fma_f32 v21, v21, v78, v105
mav 8,116,28
v_mul_f32_e64 v20, v20, |v20|
s_waitcnt lgkmcnt(2)
mav 0,120,32
R 48,14336
s_mov_b32 m0, 142336
mav 4,124,32
v_mul_f32_e64 v21, v21, |v21|
v_cvt_pk_bf16_f32 v20, v20, v21
mav 8,128,32
buffer_load_dword v87, s[36:39], 0 offen sc1 lds
v_pk_max_i16 v72, v20, 0
mav 0,132,36
R 52,15360
v_pk_max_i16 v74, v20, 0
mav 4,136,36
v_fma_f32 v104, v79, v66, v102
v_fma_f32 v22, v22, v78, v104
mav 8,140,36
v_fma_f32 v105, v79, v67, v103
s_waitcnt lgkmcnt(2)
mav 0,144,40
R 24,16384
v_fma_f32 v23, v23, v78, v105
mav 4,148,40
v_mul_f32_e64 v22, v22, |v22|
v_mul_f32_e64 v23, v23, |v23|
mav 8,152,40
v_cvt_pk_bf16_f32 v22, v22, v23
v_pk_max_i16 v73, v22, 0
mav 0,156,44
R 28,17408
v_pk_max_i16 v75, v22, 0
mav 4,160,44
v_permlane16_swap_b32 v72, v74
v_permlane16_swap_b32 v73, v75
mav 8,164,44
buffer_store_dwordx4 v[72:75], v84, s[36:39], s49 offen sc1
s_waitcnt lgkmcnt(2)
mav 0,168,48
R 32,18432
v_add_u32 v76, 1, v76
mav 4,172,48
s_waitcnt vmcnt(13)
buffer_store_dwordx2 v[76:77], v81, s[36:39], s52 offen sc1
mav 8,176,48
buffer_load_dword v87, s[36:39], 0 offen sc1 lds
v_add_u32 v88, 1, v88
mav 0,180,52
R 36,19456
mav 4,184,52
mav 8,188,52
s_waitcnt lgkmcnt(2)
mav 0,192,24
R 40,20480
mav 4,196,24
mav 8,200,24
mav 0,204,28
R 44,21504
mav 4,208,28
mav 8,212,28
s_waitcnt lgkmcnt(2)
mav 0,216,32
R 48,22528
mav 4,220,32
mav 8,224,32
buffer_load_dword v87, s[36:39], 0 offen sc1 lds
mav 0,228,36
R 52,23552
mav 4,232,36
mav 8,236,36
s_waitcnt lgkmcnt(2)
mav 0,240,40
R 24,24576
mav 4,244,40
mav 8,248,40
mav 0,252,44
R 28,25600
mvv 4,128,44
mvv 8,132,44
s_waitcnt lgkmcnt(2)
mvv 0,136,48
R 32,26624
mvv 4,140,48
mvv 8,144,48
mvv 0,148,52
R 36,27648
mvv 4,152,52
mvv 8,156,52
s_waitcnt lgkmcnt(2)
mvv 0,160,24
R 40,28672
mvv 4,164,24
ds_read_b32 v106, v82 offset:0
mvv 8,168,24
mvv 0,172,28
R 44,29696
mvv 4,176,28
mvv 8,180,28
s_waitcnt lgkmcnt(3)
mvv 0,184,32
R 48,30720
s_xor_b32 s50, s50, 0x10000
mvv 4,188,32
mvv 8,192,32
mvv 0,196,36
R 52,31744
mvv 4,200,36
s_waitcnt lgkmcnt(2)
v_cmp_gt_i32 vcc, v88, v106
mvv 8,204,36
s_waitcnt vmcnt(6)
s_cbranch_vccnz CF0_5
BF0_5:
mvv 0,208,40
s_barrier
R 24,32768
mvv 4,212,40
s_add_u32 s49, s49, 346112
s_add_u32 m0, s50, 0
mvv 8,216,40
ds_read_b64 v[78:79], v89 offset:0
mvv 0,220,44
R 28,33792
D 85,0
mvv 4,224,44
mvv 8,228,44
s_waitcnt lgkmcnt(3)
mvv 0,232,48
R 32,34816
mvv 4,236,48
D 85,1024
mvv 8,240,48
mvv 0,244,52
R 36,35840
mvv 4,248,52
mvv 8,252,52
s_waitcnt lgkmcnt(2)
v_mfma_f32_16x16x32_bf16 v[12:15], a[0:3], v[24:27], 0
R 40,36864
D 85,2048
v_mfma_f32_16x16x32_bf16 v[16:19], a[4:7], v[24:27], 0
v_fma_f32 v104, v79, v56, v92
v_fma_f32 v0, v0, v78, v104
v_mfma_f32_16x16x32_bf16 v[20:23], a[8:11], v[24:27], 0
v_fma_f32 v105, v79, v57, v93
v_fma_f32 v1, v1, v78, v105
mav 12,12,28
R 44,37888
v_mul_f32_e64 v0, v0, |v0|
mav 16,16,28
D 85,3072
v_mul_f32_e64 v1, v1, |v1|
mav 20,20,28
v_cvt_pk_bf16_f32 v0, v0, v1
s_waitcnt lgkmcnt(2)
mav 12,24,32
R 48,38912
v_pk_max_i16 v68, v0, 0
mav 16,28,32
s_add_u32 m0, s50, 4096
v_fma_f32 v104, v79, v58, v94
mav 20,32,32
v_fma_f32 v2, v2, v78, v104
v_fma_f32 v105, v79, v59, v95
mav 12,36,36
R 52,39936
D 86,0
mav 16,40,36
v_fma_f32 v3, v3, v78, v105
v_mul_f32_e64 v2, v2, |v2|
mav 20,44,36
v_mul_f32_e64 v3, v3, |v3|
s_waitcnt lgkmcnt(2)
mav 12,48,40
R 24,40960
v_cvt_pk_bf16_f32 v2, v2, v3
mav 16,52,40
D 86,1024
v_pk_max_i16 v69, v2, 0
mav 20,56,40
v_fma_f32 v104, v79, v60, v96
v_fma_f32 v4, v4, v78, v104
mav 12,60,44
R 28,41984
v_fma_f32 v105, v79, v61, v97
mav 16,64,44
v_fma_f32 v5, v5, v78, v105
v_mul_f32_e64 v4, v4, |v4|
mav 20,68,44
v_mul_f32_e64 v5, v5, |v5|
s_waitcnt lgkmcnt(2)
mav 12,72,48
R 32,43008
D 86,2048
mav 16,76,48
v_cvt_pk_bf16_f32 v4, v4, v5
v_pk_max_i16 v70, v4, 0
mav 20,80,48
v_fma_f32 v104, v79, v62, v98
v_fma_f32 v6, v6, v78, v104
mav 12,84,52
R 36,44032
v_fma_f32 v105, v79, v63, v99
mav 16,88,52
D 86,3072
v_fma_f32 v7, v7, v78, v105
mav 20,92,52
v_mul_f32_e64 v6, v6, |v6|
s_waitcnt lgkmcnt(2)
mav 12,96,24
R 40,45056
v_mul_f32_e64 v7, v7, |v7|
mav 16,100,24
v_cvt_pk_bf16_f32 v6, v6, v7
v_pk_max_i16 v71, v6, 0
mav 20,104,24
buffer_store_dwordx4 v[68:71], v83, s[36:39], s49 offen sc1
v_fma_f32 v104, v79, v64, v100
mav 12,108,28
R 44,46080
v_fma_f32 v8, v8, v78, v104
mav 16,112,28
v_fma_f32 v105, v79, v65, v101
v_fma_f32 v9, v9, v78, v105
mav 20,116,28
v_mul_f32_e64 v8, v8, |v8|
s_waitcnt lgkmcnt(2)
mav 12,120,32
R 48,47104
s_mov_b32 m0, 142336
mav 16,124,32
v_mul_f32_e64 v9, v9, |v9|
v_cvt_pk_bf16_f32 v8, v8, v9
mav 20,128,32
buffer_load_dword v87, s[36:39], 0 offen sc1 lds
v_pk_max_i16 v72, v8, 0
mav 12,132,36
R 52,48128
v_pk_max_i16 v74, v8, 0
mav 16,136,36
v_fma_f32 v104, v79, v66, v102
v_fma_f32 v10, v10, v78, v104
mav 20,140,36
v_fma_f32 v105, v79, v67, v103
s_waitcnt lgkmcnt(2)
mav 12,144,40
R 24,49152
v_fma_f32 v11, v11, v78, v105
mav 16,148,40
v_mul_f32_e64 v10, v10, |v10|
v_mul_f32_e64 v11, v11, |v11|
mav 20,152,40
v_cvt_pk_bf16_f32 v10, v10, v11
v_pk_max_i16 v73, v10, 0
mav 12,156,44
R 28,50176
v_pk_max_i16 v75, v10, 0
mav 16,160,44
v_permlane16_swap_b32 v72, v74
v_permlane16_swap_b32 v73, v75
mav 20,164,44
buffer_store_dwordx4 v[72:75], v84, s[36:39], s49 offen sc1
s_waitcnt lgkmcnt(2)
mav 12,168,48
R 32,51200
v_add_u32 v76, 1, v76
mav 16,172,48
s_waitcnt vmcnt(14)
buffer_store_dwordx2 v[76:77], v81, s[36:39], s52 offen sc1
mav 20,176,48
buffer_load_dword v87, s[36:39], 0 offen sc1 lds
v_add_u32 v88, 1, v88
mav 12,180,52
R 36,52224
mav 16,184,52
mav 20,188,52
s_waitcnt lgkmcnt(2)
mav 12,192,24
R 40,53248
mav 16,196,24
mav 20,200,24
mav 12,204,28
R 44,54272
mav 16,208,28
mav 20,212,28
s_waitcnt lgkmcnt(2)
mav 12,216,32
R 48,55296
mav 16,220,32
mav 20,224,32
buffer_load_dword v87, s[36:39], 0 offen sc1 lds
mav 12,228,36
R 52,56320
mav 16,232,36
mav 20,236,36
s_waitcnt lgkmcnt(2)
mav 12,240,40
R 24,57344
mav 16,244,40
mav 20,248,40
mav 12,252,44
R 28,58368
mvv 16,128,44
mvv 20,132,44
s_waitcnt lgkmcnt(2)
mvv 12,136,48
R 32,59392
mvv 16,140,48
mvv 20,144,48
mvv 12,148,52
R 36,60416
mvv 16,152,52
mvv 20,156,52
s_waitcnt lgkmcnt(2)
mvv 12,160,24
R 40,61440
mvv 16,164,24
ds_read_b32 v106, v82 offset:0
mvv 20,168,24
mvv 12,172,28
R 44,62464
mvv 16,176,28
mvv 20,180,28
s_waitcnt lgkmcnt(3)
mvv 12,184,32
R 48,63488
mvv 16,188,32
mvv 20,192,32
mvv 12,196,36
R 52,64512
v_xor_b32 v80, 0x10000, v80
mvv 16,200,36
s_waitcnt lgkmcnt(2)
v_cmp_gt_i32 vcc, v88, v106
mvv 20,204,36
s_waitcnt vmcnt(6)
s_cbranch_vccnz CF0_6
BF0_6:
mvv 12,208,40
s_barrier
R 24,0
mvv 16,212,40
s_add_u32 s49, s49, 346112
s_add_u32 m0, s50, 32768
mvv 20,216,40
ds_read_b64 v[78:79], v89 offset:128
mvv 12,220,44
R 28,1024
D 85,0
mvv 16,224,44
mvv 20,228,44
s_waitcnt lgkmcnt(3)
mvv 12,232,48
R 32,2048
mvv 16,236,48
D 85,1024
mvv 20,240,48
mvv 12,244,52
R 36,3072
mvv 16,248,52
mvv 20,252,52
s_mov_b32 s60, -29
.p2align 6
LF0_7:
s_waitcnt lgkmcnt(2)
v_mfma_f32_16x16x32_bf16 v[0:3], a[0:3], v[24:27], 0
R 40,4096
D 85,2048
v_mfma_f32_16x16x32_bf16 v[4:7], a[4:7], v[24:27], 0
v_fma_f32 v104, v79, v56, v92
v_fma_f32 v12, v12, v78, v104
v_mfma_f32_16x16x32_bf16 v[8:11], a[8:11], v[24:27], 0
v_fma_f32 v105, v79, v57, v93
v_fma_f32 v13, v13, v78, v105
mav 0,12,28
R 44,5120
v_mul_f32_e64 v12, v12, |v12|
mav 4,16,28
D 85,3072
v_mul_f32_e64 v13, v13, |v13|
mav 8,20,28
v_cvt_pk_bf16_f32 v12, v12, v13
s_waitcnt lgkmcnt(2)
mav 0,24,32
R 48,6144
v_pk_max_i16 v68, v12, 0
mav 4,28,32
s_add_u32 m0, s50, 36864
v_fma_f32 v104, v79, v58, v94
mav 8,32,32
v_fma_f32 v14, v14, v78, v104
v_fma_f32 v105, v79, v59, v95
mav 0,36,36
R 52,7168
D 86,0
mav 4,40,36
v_fma_f32 v15, v15, v78, v105
v_mul_f32_e64 v14, v14, |v14|
mav 8,44,36
v_mul_f32_e64 v15, v15, |v15|
s_waitcnt lgkmcnt(2)
mav 0,48,40
R 24,8192
v_cvt_pk_bf16_f32 v14, v14, v15
mav 4,52,40
D 86,1024
v_pk_max_i16 v69, v14, 0
mav 8,56,40
v_fma_f32 v104, v79, v60, v96
v_fma_f32 v16, v16, v78, v104
mav 0,60,44
R 28,9216
v_fma_f32 v105, v79, v61, v97
mav 4,64,44
v_fma_f32 v17, v17, v78, v105
v_mul_f32_e64 v16, v16, |v16|
mav 8,68,44
v_mul_f32_e64 v17, v17, |v17|
s_waitcnt lgkmcnt(2)
mav 0,72,48
R 32,10240
D 86,2048
mav 4,76,48
v_cvt_pk_bf16_f32 v16, v16, v17
v_pk_max_i16 v70, v16, 0
mav 8,80,48
v_fma_f32 v104, v79, v62, v98
v_fma_f32 v18, v18, v78, v104
mav 0,84,52
R 36,11264
v_fma_f32 v105, v79, v63, v99
mav 4,88,52
D 86,3072
v_fma_f32 v19, v19, v78, v105
mav 8,92,52
v_mul_f32_e64 v18, v18, |v18|
s_waitcnt lgkmcnt(2)
mav 0,96,24
R 40,12288
v_mul_f32_e64 v19, v19, |v19|
mav 4,100,24
v_cvt_pk_bf16_f32 v18, v18, v19
v_pk_max_i16 v71, v18, 0
mav 8,104,24
buffer_store_dwordx4 v[68:71], v83, s[36:39], s49 offen sc1
v_fma_f32 v104, v79, v64, v100
mav 0,108,28
R 44,13312
v_fma_f32 v20, v20, v78, v104
mav 4,112,28
v_fma_f32 v105, v79, v65, v101
v_fma_f32 v21, v21, v78, v105
mav 8,116,28
v_mul_f32_e64 v20, v20, |v20|
s_waitcnt lgkmcnt(2)
mav 0,120,32
R 48,14336
s_mov_b32 m0, 142336
mav 4,124,32
v_mul_f32_e64 v21, v21, |v21|
v_cvt_pk_bf16_f32 v20, v20, v21
mav 8,128,32
buffer_load_dword v87, s[36:39], 0 offen sc1 lds
v_pk_max_i16 v72, v20, 0
mav 0,132,36
R 52,15360
v_pk_max_i16 v74, v20, 0
mav 4,136,36
v_fma_f32 v104, v79, v66, v102
v_fma_f32 v22, v22, v78, v104
mav 8,140,36
v_fma_f32 v105, v79, v67, v103
s_waitcnt lgkmcnt(2)
mav 0,144,40
R 24,16384
v_fma_f32 v23, v23, v78, v105
mav 4,148,40
v_mul_f32_e64 v22, v22, |v22|
v_mul_f32_e64 v23, v23, |v23|
mav 8,152,40
v_cvt_pk_bf16_f32 v22, v22, v23
v_pk_max_i16 v73, v22, 0
mav 0,156,44
R 28,17408
v_pk_max_i16 v75, v22, 0
mav 4,160,44
v_permlane16_swap_b32 v72, v74
v_permlane16_swap_b32 v73, v75
mav 8,164,44
buffer_store_dwordx4 v[72:75], v84, s[36:39], s49 offen sc1
s_waitcnt lgkmcnt(2)
mav 0,168,48
R 32,18432
v_add_u32 v76, 1, v76
mav 4,172,48
s_waitcnt vmcnt(14)
buffer_store_dwordx2 v[76:77], v81, s[36:39], s52 offen sc1
mav 8,176,48
buffer_load_dword v87, s[36:39], 0 offen sc1 lds
v_add_u32 v88, 1, v88
mav 0,180,52
R 36,19456
mav 4,184,52
mav 8,188,52
s_waitcnt lgkmcnt(2)
mav 0,192,24
R 40,20480
mav 4,196,24
mav 8,200,24
mav 0,204,28
R 44,21504
mav 4,208,28
mav 8,212,28
s_waitcnt lgkmcnt(2)
mav 0,216,32
R 48,22528
mav 4,220,32
mav 8,224,32
buffer_load_dword v87, s[36:39], 0 offen sc1 lds
mav 0,228,36
R 52,23552
mav 4,232,36
mav 8,236,36
s_waitcnt lgkmcnt(2)
mav 0,240,40
R 24,24576
mav 4,244,40
mav 8,248,40
mav 0,252,44
R 28,25600
mvv 4,128,44
mvv 8,132,44
s_waitcnt lgkmcnt(2)
mvv 0,136,48
R 32,26624
mvv 4,140,48
mvv 8,144,48
mvv 0,148,52
R 36,27648
mvv 4,152,52
mvv 8,156,52
s_waitcnt lgkmcnt(2)
mvv 0,160,24
R 40,28672
mvv 4,164,24
ds_read_b32 v106, v82 offset:0
mvv 8,168,24
mvv 0,172,28
R 44,29696
mvv 4,176,28
mvv 8,180,28
s_waitcnt lgkmcnt(3)
mvv 0,184,32
R 48,30720
s_xor_b32 s50, s50, 0x10000
mvv 4,188,32
mvv 8,192,32
mvv 0,196,36
R 52,31744
mvv 4,200,36
s_waitcnt lgkmcnt(2)
v_cmp_gt_i32 vcc, v88, v106
mvv 8,204,36
s_waitcnt vmcnt(6)
s_cbranch_vccnz CF0_8
BF0_8:
mvv 0,208,40
s_barrier
R 24,32768
mvv 4,212,40
s_add_u32 s49, s49, 346112
s_add_u32 m0, s50, 0
mvv 8,216,40
ds_read_b64 v[78:79], v89 offset:0
mvv 0,220,44
R 28,33792
D 85,0
mvv 4,224,44
mvv 8,228,44
s_waitcnt lgkmcnt(3)
mvv 0,232,48
R 32,34816
mvv 4,236,48
D 85,1024
mvv 8,240,48
mvv 0,244,52
R 36,35840
mvv 4,248,52
mvv 8,252,52
s_waitcnt lgkmcnt(2)
v_mfma_f32_16x16x32_bf16 v[12:15], a[0:3], v[24:27], 0
R 40,36864
D 85,2048
v_mfma_f32_16x16x32_bf16 v[16:19], a[4:7], v[24:27], 0
v_fma_f32 v104, v79, v56, v92
v_fma_f32 v0, v0, v78, v104
v_mfma_f32_16x16x32_bf16 v[20:23], a[8:11], v[24:27], 0
v_fma_f32 v105, v79, v57, v93
v_fma_f32 v1, v1, v78, v105
mav 12,12,28
R 44,37888
v_mul_f32_e64 v0, v0, |v0|
mav 16,16,28
D 85,3072
v_mul_f32_e64 v1, v1, |v1|
mav 20,20,28
v_cvt_pk_bf16_f32 v0, v0, v1
s_waitcnt lgkmcnt(2)
mav 12,24,32
R 48,38912
v_pk_max_i16 v68, v0, 0
mav 16,28,32
s_add_u32 m0, s50, 4096
v_fma_f32 v104, v79, v58, v94
mav 20,32,32
v_fma_f32 v2, v2, v78, v104
v_fma_f32 v105, v79, v59, v95
mav 12,36,36
R 52,39936
D 86,0
mav 16,40,36
v_fma_f32 v3, v3, v78, v105
v_mul_f32_e64 v2, v2, |v2|
mav 20,44,36
v_mul_f32_e64 v3, v3, |v3|
s_waitcnt lgkmcnt(2)
mav 12,48,40
R 24,40960
v_cvt_pk_bf16_f32 v2, v2, v3
mav 16,52,40
D 86,1024
v_pk_max_i16 v69, v2, 0
mav 20,56,40
v_fma_f32 v104, v79, v60, v96
v_fma_f32 v4, v4, v78, v104
mav 12,60,44
R 28,41984
v_fma_f32 v105, v79, v61, v97
mav 16,64,44
v_fma_f32 v5, v5, v78, v105
v_mul_f32_e64 v4, v4, |v4|
mav 20,68,44
v_mul_f32_e64 v5, v5, |v5|
s_waitcnt lgkmcnt(2)
mav 12,72,48
R 32,43008
D 86,2048
mav 16,76,48
v_cvt_pk_bf16_f32 v4, v4, v5
v_pk_max_i16 v70, v4, 0
mav 20,80,48
v_fma_f32 v104, v79, v62, v98
v_fma_f32 v6, v6, v78, v104
mav 12,84,52
R 36,44032
v_fma_f32 v105, v79, v63, v99
mav 16,88,52
D 86,3072
v_fma_f32 v7, v7, v78, v105
mav 20,92,52
v_mul_f32_e64 v6, v6, |v6|
s_waitcnt lgkmcnt(2)
mav 12,96,24
R 40,45056
v_mul_f32_e64 v7, v7, |v7|
mav 16,100,24
v_cvt_pk_bf16_f32 v6, v6, v7
v_pk_max_i16 v71, v6, 0
mav 20,104,24
buffer_store_dwordx4 v[68:71], v83, s[36:39], s49 offen sc1
v_fma_f32 v104, v79, v64, v100
mav 12,108,28
R 44,46080
v_fma_f32 v8, v8, v78, v104
mav 16,112,28
v_fma_f32 v105, v79, v65, v101
v_fma_f32 v9, v9, v78, v105
mav 20,116,28
v_mul_f32_e64 v8, v8, |v8|
s_waitcnt lgkmcnt(2)
mav 12,120,32
R 48,47104
s_mov_b32 m0, 142336
mav 16,124,32
v_mul_f32_e64 v9, v9, |v9|
v_cvt_pk_bf16_f32 v8, v8, v9
mav 20,128,32
buffer_load_dword v87, s[36:39], 0 offen sc1 lds
v_pk_max_i16 v72, v8, 0
mav 12,132,36
R 52,48128
v_pk_max_i16 v74, v8, 0
mav 16,136,36
v_fma_f32 v104, v79, v66, v102
v_fma_f32 v10, v10, v78, v104
mav 20,140,36
v_fma_f32 v105, v79, v67, v103
s_waitcnt lgkmcnt(2)
mav 12,144,40
R 24,49152
v_fma_f32 v11, v11, v78, v105
mav 16,148,40
v_mul_f32_e64 v10, v10, |v10|
v_mul_f32_e64 v11, v11, |v11|
mav 20,152,40
v_cvt_pk_bf16_f32 v10, v10, v11
v_pk_max_i16 v73, v10, 0
mav 12,156,44
R 28,50176
v_pk_max_i16 v75, v10, 0
mav 16,160,44
v_permlane16_swap_b32 v72, v74
v_permlane16_swap_b32 v73, v75
mav 20,164,44
buffer_store_dwordx4 v[72:75], v84, s[36:39], s49 offen sc1
s_waitcnt lgkmcnt(2)
mav 12,168,48
R 32,51200
v_add_u32 v76, 1, v76
mav 16,172,48
s_waitcnt vmcnt(14)
buffer_store_dwordx2 v[76:77], v81, s[36:39], s52 offen sc1
mav 20,176,48
buffer_load_dword v87, s[36:39], 0 offen sc1 lds
v_add_u32 v88, 1, v88
mav 12,180,52
R 36,52224
mav 16,184,52
mav 20,188,52
s_waitcnt lgkmcnt(2)
mav 12,192,24
R 40,53248
mav 16,196,24
mav 20,200,24
mav 12,204,28
R 44,54272
mav 16,208,28
mav 20,212,28
s_waitcnt lgkmcnt(2)
mav 12,216,32
R 48,55296
mav 16,220,32
mav 20,224,32
buffer_load_dword v87, s[36:39], 0 offen sc1 lds
mav 12,228,36
R 52,56320
mav 16,232,36
mav 20,236,36
s_waitcnt lgkmcnt(2)
mav 12,240,40
R 24,57344
mav 16,244,40
mav 20,248,40
mav 12,252,44
R 28,58368
mvv 16,128,44
mvv 20,132,44
s_waitcnt lgkmcnt(2)
mvv 12,136,48
R 32,59392
mvv 16,140,48
mvv 20,144,48
mvv 12,148,52
R 36,60416
mvv 16,152,52
mvv 20,156,52
s_waitcnt lgkmcnt(2)
mvv 12,160,24
R 40,61440
mvv 16,164,24
ds_read_b32 v106, v82 offset:0
mvv 20,168,24
mvv 12,172,28
R 44,62464
mvv 16,176,28
mvv 20,180,28
s_waitcnt lgkmcnt(3)
mvv 12,184,32
R 48,63488
mvv 16,188,32
mvv 20,192,32
mvv 12,196,36
R 52,64512
v_xor_b32 v80, 0x10000, v80
mvv 16,200,36
s_waitcnt lgkmcnt(2)
v_cmp_gt_i32 vcc, v88, v106
mvv 20,204,36
s_waitcnt vmcnt(6)
s_cbranch_vccnz CF0_9
BF0_9:
mvv 12,208,40
s_barrier
R 24,0
mvv 16,212,40
s_add_u32 s49, s49, 346112
s_add_u32 m0, s50, 32768
mvv 20,216,40
ds_read_b64 v[78:79], v89 offset:128
mvv 12,220,44
R 28,1024
D 85,0
mvv 16,224,44
mvv 20,228,44
s_waitcnt lgkmcnt(3)
mvv 12,232,48
R 32,2048
mvv 16,236,48
D 85,1024
mvv 20,240,48
mvv 12,244,52
R 36,3072
mvv 16,248,52
mvv 20,252,52
s_add_u32 s60, s60, 1
s_cbranch_scc0 LF0_7
s_waitcnt lgkmcnt(2)
v_mfma_f32_16x16x32_bf16 v[0:3], a[0:3], v[24:27], 0
R 40,4096
D 85,2048
v_mfma_f32_16x16x32_bf16 v[4:7], a[4:7], v[24:27], 0
v_fma_f32 v104, v79, v56, v92
v_fma_f32 v12, v12, v78, v104
v_mfma_f32_16x16x32_bf16 v[8:11], a[8:11], v[24:27], 0
v_fma_f32 v105, v79, v57, v93
v_fma_f32 v13, v13, v78, v105
mav 0,12,28
R 44,5120
v_mul_f32_e64 v12, v12, |v12|
mav 4,16,28
D 85,3072
v_mul_f32_e64 v13, v13, |v13|
mav 8,20,28
v_cvt_pk_bf16_f32 v12, v12, v13
s_waitcnt lgkmcnt(2)
mav 0,24,32
R 48,6144
v_pk_max_i16 v68, v12, 0
mav 4,28,32
s_add_u32 m0, s50, 36864
v_fma_f32 v104, v79, v58, v94
mav 8,32,32
v_fma_f32 v14, v14, v78, v104
v_fma_f32 v105, v79, v59, v95
mav 0,36,36
R 52,7168
D 86,0
mav 4,40,36
v_fma_f32 v15, v15, v78, v105
v_mul_f32_e64 v14, v14, |v14|
mav 8,44,36
v_mul_f32_e64 v15, v15, |v15|
s_waitcnt lgkmcnt(2)
mav 0,48,40
R 24,8192
v_cvt_pk_bf16_f32 v14, v14, v15
mav 4,52,40
D 86,1024
v_pk_max_i16 v69, v14, 0
mav 8,56,40
v_fma_f32 v104, v79, v60, v96
v_fma_f32 v16, v16, v78, v104
mav 0,60,44
R 28,9216
v_fma_f32 v105, v79, v61, v97
mav 4,64,44
v_fma_f32 v17, v17, v78, v105
v_mul_f32_e64 v16, v16, |v16|
mav 8,68,44
v_mul_f32_e64 v17, v17, |v17|
s_waitcnt lgkmcnt(2)
mav 0,72,48
R 32,10240
D 86,2048
mav 4,76,48
v_cvt_pk_bf16_f32 v16, v16, v17
v_pk_max_i16 v70, v16, 0
mav 8,80,48
v_fma_f32 v104, v79, v62, v98
v_fma_f32 v18, v18, v78, v104
mav 0,84,52
R 36,11264
v_fma_f32 v105, v79, v63, v99
mav 4,88,52
D 86,3072
v_fma_f32 v19, v19, v78, v105
mav 8,92,52
v_mul_f32_e64 v18, v18, |v18|
s_waitcnt lgkmcnt(2)
mav 0,96,24
R 40,12288
v_mul_f32_e64 v19, v19, |v19|
mav 4,100,24
v_cvt_pk_bf16_f32 v18, v18, v19
v_pk_max_i16 v71, v18, 0
mav 8,104,24
buffer_store_dwordx4 v[68:71], v83, s[36:39], s49 offen sc1
v_fma_f32 v104, v79, v64, v100
mav 0,108,28
R 44,13312
v_fma_f32 v20, v20, v78, v104
mav 4,112,28
v_fma_f32 v105, v79, v65, v101
v_fma_f32 v21, v21, v78, v105
mav 8,116,28
v_mul_f32_e64 v20, v20, |v20|
s_waitcnt lgkmcnt(2)
mav 0,120,32
R 48,14336
s_mov_b32 m0, 142336
mav 4,124,32
v_mul_f32_e64 v21, v21, |v21|
v_cvt_pk_bf16_f32 v20, v20, v21
mav 8,128,32
buffer_load_dword v87, s[36:39], 0 offen sc1 lds
v_pk_max_i16 v72, v20, 0
mav 0,132,36
R 52,15360
v_pk_max_i16 v74, v20, 0
mav 4,136,36
v_fma_f32 v104, v79, v66, v102
v_fma_f32 v22, v22, v78, v104
mav 8,140,36
v_fma_f32 v105, v79, v67, v103
s_waitcnt lgkmcnt(2)
mav 0,144,40
R 24,16384
v_fma_f32 v23, v23, v78, v105
mav 4,148,40
v_mul_f32_e64 v22, v22, |v22|
v_mul_f32_e64 v23, v23, |v23|
mav 8,152,40
v_cvt_pk_bf16_f32 v22, v22, v23
v_pk_max_i16 v73, v22, 0
mav 0,156,44
R 28,17408
v_pk_max_i16 v75, v22, 0
mav 4,160,44
v_permlane16_swap_b32 v72, v74
v_permlane16_swap_b32 v73, v75
mav 8,164,44
buffer_store_dwordx4 v[72:75], v84, s[36:39], s49 offen sc1
s_waitcnt lgkmcnt(2)
mav 0,168,48
R 32,18432
v_add_u32 v76, 1, v76
mav 4,172,48
s_waitcnt vmcnt(14)
buffer_store_dwordx2 v[76:77], v81, s[36:39], s52 offen sc1
mav 8,176,48
buffer_load_dword v87, s[36:39], 0 offen sc1 lds
v_add_u32 v88, 1, v88
mav 0,180,52
R 36,19456
mav 4,184,52
mav 8,188,52
s_waitcnt lgkmcnt(2)
mav 0,192,24
R 40,20480
mav 4,196,24
mav 8,200,24
mav 0,204,28
R 44,21504
mav 4,208,28
mav 8,212,28
s_waitcnt lgkmcnt(2)
mav 0,216,32
R 48,22528
mav 4,220,32
mav 8,224,32
buffer_load_dword v87, s[36:39], 0 offen sc1 lds
mav 0,228,36
R 52,23552
mav 4,232,36
mav 8,236,36
s_waitcnt lgkmcnt(2)
mav 0,240,40
R 24,24576
mav 4,244,40
mav 8,248,40
mav 0,252,44
R 28,25600
mvv 4,128,44
mvv 8,132,44
s_waitcnt lgkmcnt(2)
mvv 0,136,48
R 32,26624
mvv 4,140,48
mvv 8,144,48
mvv 0,148,52
R 36,27648
mvv 4,152,52
mvv 8,156,52
s_waitcnt lgkmcnt(2)
mvv 0,160,24
R 40,28672
mvv 4,164,24
ds_read_b32 v106, v82 offset:0
mvv 8,168,24
mvv 0,172,28
R 44,29696
mvv 4,176,28
mvv 8,180,28
s_waitcnt lgkmcnt(3)
mvv 0,184,32
R 48,30720
mvv 4,188,32
mvv 8,192,32
mvv 0,196,36
R 52,31744
mvv 4,200,36
s_waitcnt lgkmcnt(2)
v_cmp_gt_i32 vcc, v88, v106
mvv 8,204,36
s_waitcnt vmcnt(6)
s_cbranch_vccnz CF0_10
BF0_10:
mvv 0,208,40
s_barrier
R 24,32768
mvv 4,212,40
s_add_u32 s49, s49, 346112
ds_read_b64 v[78:79], v89 offset:0
mvv 8,216,40
mvv 0,220,44
R 28,33792
mvv 4,224,44
mvv 8,228,44
s_waitcnt lgkmcnt(3)
mvv 0,232,48
R 32,34816
mvv 4,236,48
mvv 8,240,48
mvv 0,244,52
R 36,35840
mvv 4,248,52
mvv 8,252,52
s_waitcnt lgkmcnt(2)
v_mfma_f32_16x16x32_bf16 v[12:15], a[0:3], v[24:27], 0
R 40,36864
v_fma_f32 v104, v79, v56, v92
v_mfma_f32_16x16x32_bf16 v[16:19], a[4:7], v[24:27], 0
v_fma_f32 v0, v0, v78, v104
v_fma_f32 v105, v79, v57, v93
v_mfma_f32_16x16x32_bf16 v[20:23], a[8:11], v[24:27], 0
v_fma_f32 v1, v1, v78, v105
v_mul_f32_e64 v0, v0, |v0|
mav 12,12,28
R 44,37888
v_mul_f32_e64 v1, v1, |v1|
mav 16,16,28
v_cvt_pk_bf16_f32 v0, v0, v1
v_pk_max_i16 v68, v0, 0
mav 20,20,28
v_fma_f32 v104, v79, v58, v94
s_waitcnt lgkmcnt(2)
mav 12,24,32
R 48,38912
v_fma_f32 v2, v2, v78, v104
mav 16,28,32
v_fma_f32 v105, v79, v59, v95
v_fma_f32 v3, v3, v78, v105
mav 20,32,32
v_mul_f32_e64 v2, v2, |v2|
v_mul_f32_e64 v3, v3, |v3|
mav 12,36,36
R 52,39936
v_cvt_pk_bf16_f32 v2, v2, v3
mav 16,40,36
v_pk_max_i16 v69, v2, 0
v_fma_f32 v104, v79, v60, v96
mav 20,44,36
v_fma_f32 v4, v4, v78, v104
s_waitcnt lgkmcnt(2)
mav 12,48,40
R 24,40960
v_fma_f32 v105, v79, v61, v97
mav 16,52,40
v_fma_f32 v5, v5, v78, v105
v_mul_f32_e64 v4, v4, |v4|
mav 20,56,40
v_mul_f32_e64 v5, v5, |v5|
v_cvt_pk_bf16_f32 v4, v4, v5
mav 12,60,44
R 28,41984
v_pk_max_i16 v70, v4, 0
mav 16,64,44
v_fma_f32 v104, v79, v62, v98
v_fma_f32 v6, v6, v78, v104
mav 20,68,44
v_fma_f32 v105, v79, v63, v99
s_waitcnt lgkmcnt(2)
mav 12,72,48
R 32,43008
v_fma_f32 v7, v7, v78, v105
mav 16,76,48
v_mul_f32_e64 v6, v6, |v6|
v_mul_f32_e64 v7, v7, |v7|
mav 20,80,48
v_cvt_pk_bf16_f32 v6, v6, v7
v_pk_max_i16 v71, v6, 0
mav 12,84,52
R 36,44032
buffer_store_dwordx4 v[68:71], v83, s[36:39], s49 offen sc1
mav 16,88,52
v_fma_f32 v104, v79, v64, v100
v_fma_f32 v8, v8, v78, v104
mav 20,92,52
v_fma_f32 v105, v79, v65, v101
s_waitcnt lgkmcnt(2)
mav 12,96,24
R 40,45056
v_fma_f32 v9, v9, v78, v105
mav 16,100,24
v_mul_f32_e64 v8, v8, |v8|
v_mul_f32_e64 v9, v9, |v9|
mav 20,104,24
v_cvt_pk_bf16_f32 v8, v8, v9
v_pk_max_i16 v72, v8, 0
mav 12,108,28
R 44,46080
v_pk_max_i16 v74, v8, 0
mav 16,112,28
v_fma_f32 v104, v79, v66, v102
v_fma_f32 v10, v10, v78, v104
mav 20,116,28
v_fma_f32 v105, v79, v67, v103
s_waitcnt lgkmcnt(2)
mav 12,120,32
R 48,47104
v_fma_f32 v11, v11, v78, v105
mav 16,124,32
v_mul_f32_e64 v10, v10, |v10|
v_mul_f32_e64 v11, v11, |v11|
mav 20,128,32
v_cvt_pk_bf16_f32 v10, v10, v11
v_pk_max_i16 v73, v10, 0
mav 12,132,36
R 52,48128
v_pk_max_i16 v75, v10, 0
mav 16,136,36
v_permlane16_swap_b32 v72, v74
v_permlane16_swap_b32 v73, v75
mav 20,140,36
buffer_store_dwordx4 v[72:75], v84, s[36:39], s49 offen sc1
s_waitcnt lgkmcnt(2)
mav 12,144,40
R 24,49152
v_add_u32 v76, 1, v76
mav 16,148,40
s_waitcnt vmcnt(5)
buffer_store_dwordx2 v[76:77], v81, s[36:39], s52 offen sc1
mav 20,152,40
mav 12,156,44
R 28,50176
mav 16,160,44
mav 20,164,44
s_waitcnt lgkmcnt(2)
mav 12,168,48
R 32,51200
mav 16,172,48
mav 20,176,48
mav 12,180,52
R 36,52224
mav 16,184,52
mav 20,188,52
s_waitcnt lgkmcnt(2)
mav 12,192,24
R 40,53248
mav 16,196,24
mav 20,200,24
mav 12,204,28
R 44,54272
mav 16,208,28
mav 20,212,28
s_waitcnt lgkmcnt(2)
mav 12,216,32
R 48,55296
mav 16,220,32
mav 20,224,32
mav 12,228,36
R 52,56320
mav 16,232,36
mav 20,236,36
s_waitcnt lgkmcnt(2)
mav 12,240,40
R 24,57344
mav 16,244,40
mav 20,248,40
mav 12,252,44
R 28,58368
mvv 16,128,44
mvv 20,132,44
s_waitcnt lgkmcnt(2)
mvv 12,136,48
R 32,59392
mvv 16,140,48
mvv 20,144,48
mvv 12,148,52
R 36,60416
mvv 16,152,52
mvv 20,156,52
s_waitcnt lgkmcnt(2)
mvv 12,160,24
R 40,61440
mvv 16,164,24
mvv 20,168,24
mvv 12,172,28
R 44,62464
mvv 16,176,28
mvv 20,180,28
s_waitcnt lgkmcnt(2)
mvv 12,184,32
R 48,63488
mvv 16,188,32
mvv 20,192,32
mvv 12,196,36
R 52,64512
mvv 16,200,36
s_add_u32 s49, s49, 346112
mvv 20,204,36
s_barrier
s_waitcnt lgkmcnt(2)
mvv 12,208,40
mvv 16,212,40
ds_read_b64 v[78:79], v89 offset:128
mvv 20,216,40
mvv 12,220,44
mvv 16,224,44
mvv 20,228,44
s_waitcnt lgkmcnt(1)
mvv 12,232,48
mvv 16,236,48
mvv 20,240,48
mvv 12,244,52
mvv 16,248,52
mvv 20,252,52
s_waitcnt lgkmcnt(0)
v_fma_f32 v104, v79, v56, v92
s_nop 3
v_fma_f32 v12, v12, v78, v104
v_fma_f32 v105, v79, v57, v93
v_fma_f32 v13, v13, v78, v105
v_mul_f32_e64 v12, v12, |v12|
v_mul_f32_e64 v13, v13, |v13|
v_cvt_pk_bf16_f32 v12, v12, v13
v_pk_max_i16 v68, v12, 0
v_fma_f32 v104, v79, v58, v94
v_fma_f32 v14, v14, v78, v104
v_fma_f32 v105, v79, v59, v95
v_fma_f32 v15, v15, v78, v105
v_mul_f32_e64 v14, v14, |v14|
v_mul_f32_e64 v15, v15, |v15|
v_cvt_pk_bf16_f32 v14, v14, v15
v_pk_max_i16 v69, v14, 0
v_fma_f32 v104, v79, v60, v96
v_fma_f32 v16, v16, v78, v104
v_fma_f32 v105, v79, v61, v97
v_fma_f32 v17, v17, v78, v105
v_mul_f32_e64 v16, v16, |v16|
v_mul_f32_e64 v17, v17, |v17|
v_cvt_pk_bf16_f32 v16, v16, v17
v_pk_max_i16 v70, v16, 0
v_fma_f32 v104, v79, v62, v98
v_fma_f32 v18, v18, v78, v104
v_fma_f32 v105, v79, v63, v99
v_fma_f32 v19, v19, v78, v105
v_mul_f32_e64 v18, v18, |v18|
v_mul_f32_e64 v19, v19, |v19|
v_cvt_pk_bf16_f32 v18, v18, v19
v_pk_max_i16 v71, v18, 0
buffer_store_dwordx4 v[68:71], v83, s[36:39], s49 offen sc1
v_fma_f32 v104, v79, v64, v100
v_fma_f32 v20, v20, v78, v104
v_fma_f32 v105, v79, v65, v101
v_fma_f32 v21, v21, v78, v105
v_mul_f32_e64 v20, v20, |v20|
v_mul_f32_e64 v21, v21, |v21|
v_cvt_pk_bf16_f32 v20, v20, v21
v_pk_max_i16 v72, v20, 0
v_pk_max_i16 v74, v20, 0
v_fma_f32 v104, v79, v66, v102
v_fma_f32 v22, v22, v78, v104
v_fma_f32 v105, v79, v67, v103
v_fma_f32 v23, v23, v78, v105
v_mul_f32_e64 v22, v22, |v22|
v_mul_f32_e64 v23, v23, |v23|
v_cvt_pk_bf16_f32 v22, v22, v23
v_pk_max_i16 v73, v22, 0
v_pk_max_i16 v75, v22, 0
v_permlane16_swap_b32 v72, v74
s_nop 0
v_permlane16_swap_b32 v73, v75
buffer_store_dwordx4 v[72:75], v84, s[36:39], s49 offen sc1
v_add_u32 v76, 1, v76
s_waitcnt vmcnt(3)
buffer_store_dwordx2 v[76:77], v81, s[36:39], s52 offen sc1
v_add_u32 v76, 37, v76
s_waitcnt vmcnt(1)
buffer_store_dwordx2 v[76:77], v81, s[36:39], s52 offen sc1
s_endpgm
CF0_1:
s_cmp_eq_u32 s61, 0
s_cbranch_scc1 BF0_1
s_sub_u32 s61, s61, 1
s_sleep 8
buffer_load_dwordx2 v[106:107], v87, s[36:39], 0 offen sc1
s_waitcnt vmcnt(0)
ds_write_b32 v82, v106
v_sub_u32 v106, v106, v88
v_xor_b32 v107, v107, v77
v_cmp_lt_u64 vcc, s[62:63], v[106:107]
s_waitcnt lgkmcnt(0)
s_cbranch_vccnz CF0_1
s_branch BF0_1
CF0_2:
s_mov_b32 s58, m0
s_mov_b32 m0, 142336
DF0_2:
s_cmp_eq_u32 s61, 0
s_cbranch_scc1 EF0_2
s_sub_u32 s61, s61, 1
buffer_load_dword v87, s[36:39], 0 offen sc1 lds
s_sleep 1
ds_read_b32 v106, v82
s_waitcnt lgkmcnt(0)
v_cmp_gt_i32 vcc, v88, v106
s_cbranch_vccnz DF0_2
EF0_2:
s_mov_b32 m0, s58
s_branch BF0_2
CF0_3:
s_mov_b32 s58, m0
s_mov_b32 m0, 142336
DF0_3:
s_cmp_eq_u32 s61, 0
s_cbranch_scc1 EF0_3
s_sub_u32 s61, s61, 1
buffer_load_dword v87, s[36:39], 0 offen sc1 lds
s_sleep 1
ds_read_b32 v106, v82
s_waitcnt lgkmcnt(0)
v_cmp_gt_i32 vcc, v88, v106
s_cbranch_vccnz DF0_3
EF0_3:
s_mov_b32 m0, s58
s_branch BF0_3
CF0_4:
s_mov_b32 s58, m0
s_mov_b32 m0, 142336
DF0_4:
s_cmp_eq_u32 s61, 0
s_cbranch_scc1 EF0_4
s_sub_u32 s61, s61, 1
buffer_load_dword v87, s[36:39], 0 offen sc1 lds
s_sleep 1
ds_read_b32 v106, v82
s_waitcnt lgkmcnt(0)
v_cmp_gt_i32 vcc, v88, v106
s_cbranch_vccnz DF0_4
EF0_4:
s_mov_b32 m0, s58
s_branch BF0_4
CF0_5:
s_mov_b32 s58, m0
s_mov_b32 m0, 142336
DF0_5:
s_cmp_eq_u32 s61, 0
s_cbranch_scc1 EF0_5
s_sub_u32 s61, s61, 1
buffer_load_dword v87, s[36:39], 0 offen sc1 lds
s_sleep 1
ds_read_b32 v106, v82
s_waitcnt lgkmcnt(0)
v_cmp_gt_i32 vcc, v88, v106
s_cbranch_vccnz DF0_5
EF0_5:
s_mov_b32 m0, s58
s_branch BF0_5
CF0_6:
s_mov_b32 s58, m0
s_mov_b32 m0, 142336
DF0_6:
s_cmp_eq_u32 s61, 0
s_cbranch_scc1 EF0_6
s_sub_u32 s61, s61, 1
buffer_load_dword v87, s[36:39], 0 offen sc1 lds
s_sleep 1
ds_read_b32 v106, v82
s_waitcnt lgkmcnt(0)
v_cmp_gt_i32 vcc, v88, v106
s_cbranch_vccnz DF0_6
EF0_6:
s_mov_b32 m0, s58
s_branch BF0_6
CF0_8:
s_mov_b32 s58, m0
s_mov_b32 m0, 142336
DF0_8:
s_cmp_eq_u32 s61, 0
s_cbranch_scc1 EF0_8
s_sub_u32 s61, s61, 1
buffer_load_dword v87, s[36:39], 0 offen sc1 lds
s_sleep 1
ds_read_b32 v106, v82
s_waitcnt lgkmcnt(0)
v_cmp_gt_i32 vcc, v88, v106
s_cbranch_vccnz DF0_8
EF0_8:
s_mov_b32 m0, s58
s_branch BF0_8
CF0_9:
s_mov_b32 s58, m0
s_mov_b32 m0, 142336
DF0_9:
s_cmp_eq_u32 s61, 0
s_cbranch_scc1 EF0_9
s_sub_u32 s61, s61, 1
buffer_load_dword v87, s[36:39], 0 offen sc1 lds
s_sleep 1
ds_read_b32 v106, v82
s_waitcnt lgkmcnt(0)
v_cmp_gt_i32 vcc, v88, v106
s_cbranch_vccnz DF0_9
EF0_9:
s_mov_b32 m0, s58
s_branch BF0_9
CF0_10:
s_mov_b32 s58, m0
s_mov_b32 m0, 142336
DF0_10:
s_cmp_eq_u32 s61, 0
s_cbranch_scc1 EF0_10
s_sub_u32 s61, s61, 1
buffer_load_dword v87, s[36:39], 0 offen sc1 lds
s_sleep 1
ds_read_b32 v106, v82
s_waitcnt lgkmcnt(0)
v_cmp_gt_i32 vcc, v88, v106
s_cbranch_vccnz DF0_10
EF0_10:
s_mov_b32 m0, s58
s_branch BF0_10
LvF1:
s_add_u32 s49, s49, 346112
s_barrier
s_add_u32 m0, s50, 0
s_nop 0
D 85,0
D 85,1024
D 85,2048
D 85,3072
s_add_u32 m0, s50, 4096
s_nop 0
D 86,0
D 86,1024
D 86,2048
D 86,3072
s_add_u32 s49, s49, 346112
s_waitcnt vmcnt(0)
s_barrier
R 24,0
buffer_load_dwordx2 v[108:109], v90, s[36:39], s49 offen sc1
buffer_load_dwordx2 v[110:111], v91, s[36:39], s49 offen sc1
s_add_u32 m0, s50, 32768
R 28,1024
D 85,0
R 32,2048
D 85,1024
R 36,3072
s_waitcnt lgkmcnt(2)
v_mfma_f32_16x16x32_bf16 v[0:3], a[0:3], v[24:27], 0
R 40,4096
D 85,2048
v_mfma_f32_16x16x32_bf16 v[4:7], a[4:7], v[24:27], 0
v_mfma_f32_16x16x32_bf16 v[8:11], a[8:11], v[24:27], 0
mav 0,12,28
R 44,5120
mav 4,16,28
D 85,3072
mav 8,20,28
s_waitcnt lgkmcnt(2)
mav 0,24,32
R 48,6144
mav 4,28,32
s_add_u32 m0, s50, 36864
mav 8,32,32
mav 0,36,36
R 52,7168
D 86,0
mav 4,40,36
mav 8,44,36
s_waitcnt lgkmcnt(2)
mav 0,48,40
R 24,8192
mav 4,52,40
D 86,1024
mav 8,56,40
mav 0,60,44
R 28,9216
mav 4,64,44
mav 8,68,44
s_waitcnt lgkmcnt(2)
mav 0,72,48
R 32,10240
D 86,2048
mav 4,76,48
mav 8,80,48
mav 0,84,52
R 36,11264
mav 4,88,52
D 86,3072
mav 8,92,52
s_waitcnt lgkmcnt(2)
mav 0,96,24
R 40,12288
mav 4,100,24
mav 8,104,24
mav 0,108,28
R 44,13312
mav 4,112,28
mav 8,116,28
s_waitcnt lgkmcnt(2)
mav 0,120,32
R 48,14336
mav 4,124,32
mav 8,128,32
mav 0,132,36
R 52,15360
mav 4,136,36
mav 8,140,36
s_waitcnt lgkmcnt(2)
mav 0,144,40
R 24,16384
mav 4,148,40
mav 8,152,40
mav 0,156,44
R 28,17408
mav 4,160,44
mav 8,164,44
s_waitcnt lgkmcnt(2)
mav 0,168,48
R 32,18432
mav 4,172,48
mav 8,176,48
mav 0,180,52
R 36,19456
mav 4,184,52
mav 8,188,52
s_waitcnt lgkmcnt(2)
mav 0,192,24
R 40,20480
mav 4,196,24
mav 8,200,24
s_waitcnt vmcnt(8)
v_add_f32 v108, v108, v110
mav 0,204,28
R 44,21504
v_add_f32 v109, v109, v111
mav 4,208,28
mav 8,212,28
v_permlane32_swap_b32 v108, v109
s_waitcnt lgkmcnt(2)
mav 0,216,32
R 48,22528
v_add_f32 v110, v108, v109
mav 4,220,32
v_add_f32 v111, v108, v109
mav 8,224,32
mav 0,228,36
R 52,23552
v_permlane16_swap_b32 v110, v111
mav 4,232,36
v_add_f32 v108, v110, v111
v_add_f32 v109, v110, v111
mav 8,236,36
s_waitcnt lgkmcnt(2)
mav 0,240,40
R 24,24576
v_permlane32_swap_b32 v108, v109
mav 4,244,40
v_mul_f32 v110, 0x3a800000, v108
v_mul_f32 v111, 0x3a800000, v109
mav 8,248,40
v_fma_f32 v111, -v110, v110, v111
v_max_f32 v111, 0, v111
mav 0,252,44
R 28,25600
v_add_f32 v111, 0x3727c5ac, v111
mvv 4,128,44
v_rsq_f32 v108, v111
mvv 8,132,44
v_mul_f32_e64 v109, -v110, v108
s_waitcnt lgkmcnt(2)
mvv 0,136,48
R 32,26624
mvv 4,140,48
ds_write_b64 v89, v[108:109] offset:0
mvv 8,144,48
mvv 0,148,52
R 36,27648
mvv 4,152,52
mvv 8,156,52
s_waitcnt lgkmcnt(3)
mvv 0,160,24
R 40,28672
mvv 4,164,24
mvv 8,168,24
mvv 0,172,28
R 44,29696
mvv 4,176,28
mvv 8,180,28
s_waitcnt lgkmcnt(2)
mvv 0,184,32
R 48,30720
s_xor_b32 s50, s50, 0x10000
mvv 4,188,32
mvv 8,192,32
mvv 0,196,36
R 52,31744
mvv 4,200,36
s_add_u32 s49, s49, 346112
mvv 8,204,36
s_waitcnt vmcnt(0)
s_barrier
s_waitcnt lgkmcnt(2)
mvv 0,208,40
R 24,32768
buffer_load_dwordx2 v[108:109], v90, s[36:39], s49 offen sc1
mvv 4,212,40
buffer_load_dwordx2 v[110:111], v91, s[36:39], s49 offen sc1
s_add_u32 m0, s50, 0
mvv 8,216,40
ds_read_b64 v[78:79], v89 offset:0
mvv 0,220,44
R 28,33792
D 85,0
mvv 4,224,44
mvv 8,228,44
s_waitcnt lgkmcnt(3)
mvv 0,232,48
R 32,34816
mvv 4,236,48
D 85,1024
mvv 8,240,48
mvv 0,244,52
R 36,35840
mvv 4,248,52
mvv 8,252,52
s_waitcnt lgkmcnt(2)
v_mfma_f32_16x16x32_bf16 v[12:15], a[0:3], v[24:27], 0
R 40,36864
D 85,2048
v_mfma_f32_16x16x32_bf16 v[16:19], a[4:7], v[24:27], 0
v_fma_f32 v104, v79, v56, v92
v_fma_f32 v0, v0, v78, v104
v_mfma_f32_16x16x32_bf16 v[20:23], a[8:11], v[24:27], 0
v_fma_f32 v105, v79, v57, v93
v_fma_f32 v1, v1, v78, v105
mav 12,12,28
R 44,37888
v_mul_f32_e64 v0, v0, |v0|
mav 16,16,28
D 85,3072
v_mul_f32_e64 v1, v1, |v1|
mav 20,20,28
v_cvt_pk_bf16_f32 v0, v0, v1
s_waitcnt lgkmcnt(2)
mav 12,24,32
R 48,38912
v_pk_max_i16 v68, v0, 0
mav 16,28,32
s_add_u32 m0, s50, 4096
v_fma_f32 v104, v79, v58, v94
mav 20,32,32
v_fma_f32 v2, v2, v78, v104
v_fma_f32 v105, v79, v59, v95
mav 12,36,36
R 52,39936
)"
R"(
D 86,0
mav 16,40,36
v_fma_f32 v3, v3, v78, v105
v_mul_f32_e64 v2, v2, |v2|
mav 20,44,36
v_mul_f32_e64 v3, v3, |v3|
s_waitcnt lgkmcnt(2)
mav 12,48,40
R 24,40960
v_cvt_pk_bf16_f32 v2, v2, v3
mav 16,52,40
D 86,1024
v_pk_max_i16 v69, v2, 0
mav 20,56,40
v_fma_f32 v104, v79, v60, v96
v_fma_f32 v4, v4, v78, v104
mav 12,60,44
R 28,41984
v_fma_f32 v105, v79, v61, v97
mav 16,64,44
v_fma_f32 v5, v5, v78, v105
v_mul_f32_e64 v4, v4, |v4|
mav 20,68,44
v_mul_f32_e64 v5, v5, |v5|
s_waitcnt lgkmcnt(2)
mav 12,72,48
R 32,43008
D 86,2048
mav 16,76,48
v_cvt_pk_bf16_f32 v4, v4, v5
v_pk_max_i16 v70, v4, 0
mav 20,80,48
v_fma_f32 v104, v79, v62, v98
v_fma_f32 v6, v6, v78, v104
mav 12,84,52
R 36,44032
v_fma_f32 v105, v79, v63, v99
mav 16,88,52
D 86,3072
v_fma_f32 v7, v7, v78, v105
mav 20,92,52
v_mul_f32_e64 v6, v6, |v6|
s_waitcnt lgkmcnt(2)
mav 12,96,24
R 40,45056
v_mul_f32_e64 v7, v7, |v7|
mav 16,100,24
v_cvt_pk_bf16_f32 v6, v6, v7
v_pk_max_i16 v71, v6, 0
mav 20,104,24
buffer_store_dwordx4 v[68:71], v83, s[36:39], s49 offen sc1
v_fma_f32 v104, v79, v64, v100
mav 12,108,28
R 44,46080
v_fma_f32 v8, v8, v78, v104
mav 16,112,28
v_fma_f32 v105, v79, v65, v101
v_fma_f32 v9, v9, v78, v105
mav 20,116,28
v_mul_f32_e64 v8, v8, |v8|
s_waitcnt lgkmcnt(2)
mav 12,120,32
R 48,47104
v_mul_f32_e64 v9, v9, |v9|
mav 16,124,32
v_cvt_pk_bf16_f32 v8, v8, v9
v_pk_max_i16 v72, v8, 0
mav 20,128,32
v_pk_max_i16 v74, v8, 0
v_fma_f32 v104, v79, v66, v102
mav 12,132,36
R 52,48128
v_fma_f32 v10, v10, v78, v104
mav 16,136,36
v_fma_f32 v105, v79, v67, v103
v_fma_f32 v11, v11, v78, v105
mav 20,140,36
v_mul_f32_e64 v10, v10, |v10|
s_waitcnt lgkmcnt(2)
mav 12,144,40
R 24,49152
v_mul_f32_e64 v11, v11, |v11|
mav 16,148,40
v_cvt_pk_bf16_f32 v10, v10, v11
v_pk_max_i16 v73, v10, 0
mav 20,152,40
v_pk_max_i16 v75, v10, 0
v_permlane16_swap_b32 v72, v74
mav 12,156,44
R 28,50176
v_permlane16_swap_b32 v73, v75
mav 16,160,44
buffer_store_dwordx4 v[72:75], v84, s[36:39], s49 offen sc1
mav 20,164,44
s_waitcnt lgkmcnt(2)
mav 12,168,48
R 32,51200
mav 16,172,48
mav 20,176,48
mav 12,180,52
R 36,52224
mav 16,184,52
mav 20,188,52
s_waitcnt lgkmcnt(2)
mav 12,192,24
R 40,53248
mav 16,196,24
mav 20,200,24
s_waitcnt vmcnt(10)
v_add_f32 v108, v108, v110
mav 12,204,28
R 44,54272
v_add_f32 v109, v109, v111
mav 16,208,28
mav 20,212,28
v_permlane32_swap_b32 v108, v109
s_waitcnt lgkmcnt(2)
mav 12,216,32
R 48,55296
v_add_f32 v110, v108, v109
mav 16,220,32
v_add_f32 v111, v108, v109
mav 20,224,32
mav 12,228,36
R 52,56320
v_permlane16_swap_b32 v110, v111
mav 16,232,36
v_add_f32 v108, v110, v111
v_add_f32 v109, v110, v111
mav 20,236,36
s_waitcnt lgkmcnt(2)
mav 12,240,40
R 24,57344
v_permlane32_swap_b32 v108, v109
mav 16,244,40
v_mul_f32 v110, 0x3a800000, v108
v_mul_f32 v111, 0x3a800000, v109
mav 20,248,40
v_fma_f32 v111, -v110, v110, v111
v_max_f32 v111, 0, v111
mav 12,252,44
R 28,58368
v_add_f32 v111, 0x3727c5ac, v111
mvv 16,128,44
v_rsq_f32 v108, v111
mvv 20,132,44
v_mul_f32_e64 v109, -v110, v108
s_waitcnt lgkmcnt(2)
mvv 12,136,48
R 32,59392
mvv 16,140,48
ds_write_b64 v89, v[108:109] offset:128
mvv 20,144,48
mvv 12,148,52
R 36,60416
mvv 16,152,52
mvv 20,156,52
s_waitcnt lgkmcnt(3)
mvv 12,160,24
R 40,61440
mvv 16,164,24
mvv 20,168,24
mvv 12,172,28
R 44,62464
mvv 16,176,28
mvv 20,180,28
s_waitcnt lgkmcnt(2)
mvv 12,184,32
R 48,63488
mvv 16,188,32
mvv 20,192,32
mvv 12,196,36
R 52,64512
v_xor_b32 v80, 0x10000, v80
mvv 16,200,36
s_add_u32 s49, s49, 346112
mvv 20,204,36
s_waitcnt vmcnt(2)
s_barrier
s_waitcnt lgkmcnt(2)
mvv 12,208,40
R 24,0
buffer_load_dwordx2 v[108:109], v90, s[36:39], s49 offen sc1
mvv 16,212,40
buffer_load_dwordx2 v[110:111], v91, s[36:39], s49 offen sc1
s_add_u32 m0, s50, 32768
mvv 20,216,40
ds_read_b64 v[78:79], v89 offset:128
mvv 12,220,44
R 28,1024
D 85,0
mvv 16,224,44
mvv 20,228,44
s_waitcnt lgkmcnt(3)
mvv 12,232,48
R 32,2048
mvv 16,236,48
D 85,1024
mvv 20,240,48
mvv 12,244,52
R 36,3072
mvv 16,248,52
mvv 20,252,52
s_waitcnt lgkmcnt(2)
v_mfma_f32_16x16x32_bf16 v[0:3], a[0:3], v[24:27], 0
R 40,4096
D 85,2048
v_mfma_f32_16x16x32_bf16 v[4:7], a[4:7], v[24:27], 0
v_fma_f32 v104, v79, v56, v92
v_fma_f32 v12, v12, v78, v104
v_mfma_f32_16x16x32_bf16 v[8:11], a[8:11], v[24:27], 0
v_fma_f32 v105, v79, v57, v93
v_fma_f32 v13, v13, v78, v105
mav 0,12,28
R 44,5120
v_mul_f32_e64 v12, v12, |v12|
mav 4,16,28
D 85,3072
v_mul_f32_e64 v13, v13, |v13|
mav 8,20,28
v_cvt_pk_bf16_f32 v12, v12, v13
s_waitcnt lgkmcnt(2)
mav 0,24,32
R 48,6144
v_pk_max_i16 v68, v12, 0
mav 4,28,32
s_add_u32 m0, s50, 36864
v_fma_f32 v104, v79, v58, v94
mav 8,32,32
v_fma_f32 v14, v14, v78, v104
v_fma_f32 v105, v79, v59, v95
mav 0,36,36
R 52,7168
D 86,0
mav 4,40,36
v_fma_f32 v15, v15, v78, v105
v_mul_f32_e64 v14, v14, |v14|
mav 8,44,36
v_mul_f32_e64 v15, v15, |v15|
s_waitcnt lgkmcnt(2)
mav 0,48,40
R 24,8192
v_cvt_pk_bf16_f32 v14, v14, v15
mav 4,52,40
D 86,1024
v_pk_max_i16 v69, v14, 0
mav 8,56,40
v_fma_f32 v104, v79, v60, v96
v_fma_f32 v16, v16, v78, v104
mav 0,60,44
R 28,9216
v_fma_f32 v105, v79, v61, v97
mav 4,64,44
v_fma_f32 v17, v17, v78, v105
v_mul_f32_e64 v16, v16, |v16|
mav 8,68,44
v_mul_f32_e64 v17, v17, |v17|
s_waitcnt lgkmcnt(2)
mav 0,72,48
R 32,10240
D 86,2048
mav 4,76,48
v_cvt_pk_bf16_f32 v16, v16, v17
v_pk_max_i16 v70, v16, 0
mav 8,80,48
v_fma_f32 v104, v79, v62, v98
v_fma_f32 v18, v18, v78, v104
mav 0,84,52
R 36,11264
v_fma_f32 v105, v79, v63, v99
mav 4,88,52
D 86,3072
v_fma_f32 v19, v19, v78, v105
mav 8,92,52
v_mul_f32_e64 v18, v18, |v18|
s_waitcnt lgkmcnt(2)
mav 0,96,24
R 40,12288
v_mul_f32_e64 v19, v19, |v19|
mav 4,100,24
v_cvt_pk_bf16_f32 v18, v18, v19
v_pk_max_i16 v71, v18, 0
mav 8,104,24
buffer_store_dwordx4 v[68:71], v83, s[36:39], s49 offen sc1
v_fma_f32 v104, v79, v64, v100
mav 0,108,28
R 44,13312
v_fma_f32 v20, v20, v78, v104
mav 4,112,28
v_fma_f32 v105, v79, v65, v101
v_fma_f32 v21, v21, v78, v105
mav 8,116,28
v_mul_f32_e64 v20, v20, |v20|
s_waitcnt lgkmcnt(2)
mav 0,120,32
R 48,14336
v_mul_f32_e64 v21, v21, |v21|
mav 4,124,32
v_cvt_pk_bf16_f32 v20, v20, v21
v_pk_max_i16 v72, v20, 0
mav 8,128,32
v_pk_max_i16 v74, v20, 0
v_fma_f32 v104, v79, v66, v102
mav 0,132,36
R 52,15360
v_fma_f32 v22, v22, v78, v104
mav 4,136,36
v_fma_f32 v105, v79, v67, v103
v_fma_f32 v23, v23, v78, v105
mav 8,140,36
v_mul_f32_e64 v22, v22, |v22|
s_waitcnt lgkmcnt(2)
mav 0,144,40
R 24,16384
v_mul_f32_e64 v23, v23, |v23|
mav 4,148,40
v_cvt_pk_bf16_f32 v22, v22, v23
v_pk_max_i16 v73, v22, 0
mav 8,152,40
v_pk_max_i16 v75, v22, 0
v_permlane16_swap_b32 v72, v74
mav 0,156,44
R 28,17408
v_permlane16_swap_b32 v73, v75
mav 4,160,44
buffer_store_dwordx4 v[72:75], v84, s[36:39], s49 offen sc1
v_add_u32 v76, 1, v76
mav 8,164,44
s_waitcnt vmcnt(12)
buffer_store_dwordx2 v[76:77], v81, s[36:39], s52 offen sc1
s_waitcnt lgkmcnt(2)
mav 0,168,48
R 32,18432
mav 4,172,48
mav 8,176,48
mav 0,180,52
R 36,19456
mav 4,184,52
mav 8,188,52
s_waitcnt lgkmcnt(2)
mav 0,192,24
R 40,20480
mav 4,196,24
mav 8,200,24
s_waitcnt vmcnt(11)
v_add_f32 v108, v108, v110
mav 0,204,28
R 44,21504
v_add_f32 v109, v109, v111
mav 4,208,28
mav 8,212,28
v_permlane32_swap_b32 v108, v109
s_waitcnt lgkmcnt(2)
mav 0,216,32
R 48,22528
v_add_f32 v110, v108, v109
mav 4,220,32
v_add_f32 v111, v108, v109
mav 8,224,32
mav 0,228,36
R 52,23552
v_permlane16_swap_b32 v110, v111
mav 4,232,36
v_add_f32 v108, v110, v111
v_add_f32 v109, v110, v111
mav 8,236,36
s_waitcnt lgkmcnt(2)
mav 0,240,40
R 24,24576
v_permlane32_swap_b32 v108, v109
mav 4,244,40
v_mul_f32 v110, 0x3a800000, v108
v_mul_f32 v111, 0x3a800000, v109
mav 8,248,40
v_fma_f32 v111, -v110, v110, v111
v_max_f32 v111, 0, v111
mav 0,252,44
R 28,25600
v_add_f32 v111, 0x3727c5ac, v111
mvv 4,128,44
v_rsq_f32 v108, v111
mvv 8,132,44
v_mul_f32_e64 v109, -v110, v108
s_waitcnt lgkmcnt(2)
mvv 0,136,48
R 32,26624
mvv 4,140,48
ds_write_b64 v89, v[108:109] offset:0
mvv 8,144,48
mvv 0,148,52
R 36,27648
mvv 4,152,52
mvv 8,156,52
s_waitcnt lgkmcnt(3)
mvv 0,160,24
R 40,28672
mvv 4,164,24
mvv 8,168,24
mvv 0,172,28
R 44,29696
mvv 4,176,28
mvv 8,180,28
s_waitcnt lgkmcnt(2)
mvv 0,184,32
R 48,30720
s_xor_b32 s50, s50, 0x10000
mvv 4,188,32
mvv 8,192,32
mvv 0,196,36
R 52,31744
mvv 4,200,36
s_add_u32 s49, s49, 346112
mvv 8,204,36
s_waitcnt vmcnt(3)
s_barrier
s_waitcnt lgkmcnt(2)
mvv 0,208,40
R 24,32768
buffer_load_dwordx2 v[108:109], v90, s[36:39], s49 offen sc1
mvv 4,212,40
buffer_load_dwordx2 v[110:111], v91, s[36:39], s49 offen sc1
s_add_u32 m0, s50, 0
mvv 8,216,40
ds_read_b64 v[78:79], v89 offset:0
mvv 0,220,44
R 28,33792
D 85,0
mvv 4,224,44
mvv 8,228,44
s_waitcnt lgkmcnt(3)
mvv 0,232,48
R 32,34816
mvv 4,236,48
D 85,1024
mvv 8,240,48
mvv 0,244,52
R 36,35840
mvv 4,248,52
mvv 8,252,52
s_waitcnt lgkmcnt(2)
v_mfma_f32_16x16x32_bf16 v[12:15], a[0:3], v[24:27], 0
R 40,36864
D 85,2048
v_mfma_f32_16x16x32_bf16 v[16:19], a[4:7], v[24:27], 0
v_fma_f32 v104, v79, v56, v92
v_fma_f32 v0, v0, v78, v104
v_mfma_f32_16x16x32_bf16 v[20:23], a[8:11], v[24:27], 0
v_fma_f32 v105, v79, v57, v93
v_fma_f32 v1, v1, v78, v105
mav 12,12,28
R 44,37888
v_mul_f32_e64 v0, v0, |v0|
mav 16,16,28
D 85,3072
v_mul_f32_e64 v1, v1, |v1|
mav 20,20,28
v_cvt_pk_bf16_f32 v0, v0, v1
s_waitcnt lgkmcnt(2)
mav 12,24,32
R 48,38912
v_pk_max_i16 v68, v0, 0
mav 16,28,32
s_add_u32 m0, s50, 4096
v_fma_f32 v104, v79, v58, v94
mav 20,32,32
v_fma_f32 v2, v2, v78, v104
v_fma_f32 v105, v79, v59, v95
mav 12,36,36
R 52,39936
D 86,0
mav 16,40,36
v_fma_f32 v3, v3, v78, v105
v_mul_f32_e64 v2, v2, |v2|
mav 20,44,36
v_mul_f32_e64 v3, v3, |v3|
s_waitcnt lgkmcnt(2)
mav 12,48,40
R 24,40960
v_cvt_pk_bf16_f32 v2, v2, v3
mav 16,52,40
D 86,1024
v_pk_max_i16 v69, v2, 0
mav 20,56,40
v_fma_f32 v104, v79, v60, v96
v_fma_f32 v4, v4, v78, v104
mav 12,60,44
R 28,41984
v_fma_f32 v105, v79, v61, v97
mav 16,64,44
v_fma_f32 v5, v5, v78, v105
v_mul_f32_e64 v4, v4, |v4|
mav 20,68,44
v_mul_f32_e64 v5, v5, |v5|
s_waitcnt lgkmcnt(2)
mav 12,72,48
R 32,43008
D 86,2048
mav 16,76,48
v_cvt_pk_bf16_f32 v4, v4, v5
v_pk_max_i16 v70, v4, 0
mav 20,80,48
v_fma_f32 v104, v79, v62, v98
v_fma_f32 v6, v6, v78, v104
mav 12,84,52
R 36,44032
v_fma_f32 v105, v79, v63, v99
mav 16,88,52
D 86,3072
v_fma_f32 v7, v7, v78, v105
mav 20,92,52
v_mul_f32_e64 v6, v6, |v6|
s_waitcnt lgkmcnt(2)
mav 12,96,24
R 40,45056
v_mul_f32_e64 v7, v7, |v7|
mav 16,100,24
v_cvt_pk_bf16_f32 v6, v6, v7
v_pk_max_i16 v71, v6, 0
mav 20,104,24
buffer_store_dwordx4 v[68:71], v83, s[36:39], s49 offen sc1
v_fma_f32 v104, v79, v64, v100
mav 12,108,28
R 44,46080
v_fma_f32 v8, v8, v78, v104
mav 16,112,28
v_fma_f32 v105, v79, v65, v101
v_fma_f32 v9, v9, v78, v105
mav 20,116,28
v_mul_f32_e64 v8, v8, |v8|
s_waitcnt lgkmcnt(2)
mav 12,120,32
R 48,47104
v_mul_f32_e64 v9, v9, |v9|
mav 16,124,32
v_cvt_pk_bf16_f32 v8, v8, v9
v_pk_max_i16 v72, v8, 0
mav 20,128,32
v_pk_max_i16 v74, v8, 0
v_fma_f32 v104, v79, v66, v102
mav 12,132,36
R 52,48128
v_fma_f32 v10, v10, v78, v104
mav 16,136,36
v_fma_f32 v105, v79, v67, v103
v_fma_f32 v11, v11, v78, v105
mav 20,140,36
v_mul_f32_e64 v10, v10, |v10|
s_waitcnt lgkmcnt(2)
mav 12,144,40
R 24,49152
v_mul_f32_e64 v11, v11, |v11|
mav 16,148,40
v_cvt_pk_bf16_f32 v10, v10, v11
v_pk_max_i16 v73, v10, 0
mav 20,152,40
v_pk_max_i16 v75, v10, 0
v_permlane16_swap_b32 v72, v74
mav 12,156,44
R 28,50176
v_permlane16_swap_b32 v73, v75
mav 16,160,44
buffer_store_dwordx4 v[72:75], v84, s[36:39], s49 offen sc1
v_add_u32 v76, 1, v76
mav 20,164,44
s_waitcnt vmcnt(13)
buffer_store_dwordx2 v[76:77], v81, s[36:39], s52 offen sc1
s_waitcnt lgkmcnt(2)
mav 12,168,48
R 32,51200
mav 16,172,48
mav 20,176,48
mav 12,180,52
R 36,52224
mav 16,184,52
mav 20,188,52
s_waitcnt lgkmcnt(2)
mav 12,192,24
R 40,53248
mav 16,196,24
mav 20,200,24
s_waitcnt vmcnt(11)
v_add_f32 v108, v108, v110
mav 12,204,28
R 44,54272
v_add_f32 v109, v109, v111
mav 16,208,28
mav 20,212,28
v_permlane32_swap_b32 v108, v109
s_waitcnt lgkmcnt(2)
mav 12,216,32
R 48,55296
v_add_f32 v110, v108, v109
mav 16,220,32
v_add_f32 v111, v108, v109
mav 20,224,32
mav 12,228,36
R 52,56320
v_permlane16_swap_b32 v110, v111
mav 16,232,36
v_add_f32 v108, v110, v111
v_add_f32 v109, v110, v111
mav 20,236,36
s_waitcnt lgkmcnt(2)
mav 12,240,40
R 24,57344
v_permlane32_swap_b32 v108, v109
mav 16,244,40
v_mul_f32 v110, 0x3a800000, v108
v_mul_f32 v111, 0x3a800000, v109
mav 20,248,40
v_fma_f32 v111, -v110, v110, v111
v_max_f32 v111, 0, v111
mav 12,252,44
R 28,58368
v_add_f32 v111, 0x3727c5ac, v111
mvv 16,128,44
v_rsq_f32 v108, v111
mvv 20,132,44
v_mul_f32_e64 v109, -v110, v108
s_waitcnt lgkmcnt(2)
mvv 12,136,48
R 32,59392
mvv 16,140,48
ds_write_b64 v89, v[108:109] offset:128
mvv 20,144,48
mvv 12,148,52
R 36,60416
mvv 16,152,52
mvv 20,156,52
s_waitcnt lgkmcnt(3)
mvv 12,160,24
R 40,61440
mvv 16,164,24
mvv 20,168,24
mvv 12,172,28
R 44,62464
mvv 16,176,28
mvv 20,180,28
s_waitcnt lgkmcnt(2)
mvv 12,184,32
R 48,63488
mvv 16,188,32
mvv 20,192,32
mvv 12,196,36
R 52,64512
v_xor_b32 v80, 0x10000, v80
mvv 16,200,36
s_add_u32 s49, s49, 346112
mvv 20,204,36
s_waitcnt vmcnt(3)
s_barrier
s_waitcnt lgkmcnt(2)
mvv 12,208,40
R 24,0
buffer_load_dwordx2 v[108:109], v90, s[36:39], s49 offen sc1
mvv 16,212,40
buffer_load_dwordx2 v[110:111], v91, s[36:39], s49 offen sc1
s_add_u32 m0, s50, 32768
mvv 20,216,40
ds_read_b64 v[78:79], v89 offset:128
mvv 12,220,44
R 28,1024
D 85,0
mvv 16,224,44
mvv 20,228,44
s_waitcnt lgkmcnt(3)
mvv 12,232,48
R 32,2048
mvv 16,236,48
D 85,1024
mvv 20,240,48
mvv 12,244,52
R 36,3072
mvv 16,248,52
mvv 20,252,52
s_mov_b32 s60, -29
.p2align 6
LF1_1:
s_waitcnt lgkmcnt(2)
v_mfma_f32_16x16x32_bf16 v[0:3], a[0:3], v[24:27], 0
R 40,4096
D 85,2048
v_mfma_f32_16x16x32_bf16 v[4:7], a[4:7], v[24:27], 0
v_fma_f32 v104, v79, v56, v92
v_fma_f32 v12, v12, v78, v104
v_mfma_f32_16x16x32_bf16 v[8:11], a[8:11], v[24:27], 0
v_fma_f32 v105, v79, v57, v93
v_fma_f32 v13, v13, v78, v105
mav 0,12,28
R 44,5120
v_mul_f32_e64 v12, v12, |v12|
mav 4,16,28
D 85,3072
v_mul_f32_e64 v13, v13, |v13|
mav 8,20,28
v_cvt_pk_bf16_f32 v12, v12, v13
s_waitcnt lgkmcnt(2)
mav 0,24,32
R 48,6144
v_pk_max_i16 v68, v12, 0
mav 4,28,32
s_add_u32 m0, s50, 36864
v_fma_f32 v104, v79, v58, v94
mav 8,32,32
v_fma_f32 v14, v14, v78, v104
v_fma_f32 v105, v79, v59, v95
mav 0,36,36
R 52,7168
D 86,0
mav 4,40,36
v_fma_f32 v15, v15, v78, v105
v_mul_f32_e64 v14, v14, |v14|
mav 8,44,36
v_mul_f32_e64 v15, v15, |v15|
s_waitcnt lgkmcnt(2)
mav 0,48,40
R 24,8192
v_cvt_pk_bf16_f32 v14, v14, v15
mav 4,52,40
D 86,1024
v_pk_max_i16 v69, v14, 0
mav 8,56,40
v_fma_f32 v104, v79, v60, v96
v_fma_f32 v16, v16, v78, v104
mav 0,60,44
R 28,9216
v_fma_f32 v105, v79, v61, v97
mav 4,64,44
v_fma_f32 v17, v17, v78, v105
v_mul_f32_e64 v16, v16, |v16|
mav 8,68,44
v_mul_f32_e64 v17, v17, |v17|
s_waitcnt lgkmcnt(2)
mav 0,72,48
R 32,10240
D 86,2048
mav 4,76,48
v_cvt_pk_bf16_f32 v16, v16, v17
v_pk_max_i16 v70, v16, 0
mav 8,80,48
v_fma_f32 v104, v79, v62, v98
v_fma_f32 v18, v18, v78, v104
mav 0,84,52
R 36,11264
v_fma_f32 v105, v79, v63, v99
mav 4,88,52
D 86,3072
v_fma_f32 v19, v19, v78, v105
mav 8,92,52
v_mul_f32_e64 v18, v18, |v18|
s_waitcnt lgkmcnt(2)
mav 0,96,24
R 40,12288
v_mul_f32_e64 v19, v19, |v19|
mav 4,100,24
v_cvt_pk_bf16_f32 v18, v18, v19
v_pk_max_i16 v71, v18, 0
mav 8,104,24
buffer_store_dwordx4 v[68:71], v83, s[36:39], s49 offen sc1
v_fma_f32 v104, v79, v64, v100
mav 0,108,28
R 44,13312
v_fma_f32 v20, v20, v78, v104
mav 4,112,28
v_fma_f32 v105, v79, v65, v101
v_fma_f32 v21, v21, v78, v105
mav 8,116,28
v_mul_f32_e64 v20, v20, |v20|
s_waitcnt lgkmcnt(2)
mav 0,120,32
R 48,14336
v_mul_f32_e64 v21, v21, |v21|
mav 4,124,32
v_cvt_pk_bf16_f32 v20, v20, v21
v_pk_max_i16 v72, v20, 0
mav 8,128,32
v_pk_max_i16 v74, v20, 0
v_fma_f32 v104, v79, v66, v102
mav 0,132,36
R 52,15360
v_fma_f32 v22, v22, v78, v104
mav 4,136,36
v_fma_f32 v105, v79, v67, v103
v_fma_f32 v23, v23, v78, v105
mav 8,140,36
v_mul_f32_e64 v22, v22, |v22|
s_waitcnt lgkmcnt(2)
mav 0,144,40
R 24,16384
v_mul_f32_e64 v23, v23, |v23|
mav 4,148,40
v_cvt_pk_bf16_f32 v22, v22, v23
v_pk_max_i16 v73, v22, 0
mav 8,152,40
v_pk_max_i16 v75, v22, 0
v_permlane16_swap_b32 v72, v74
mav 0,156,44
R 28,17408
v_permlane16_swap_b32 v73, v75
mav 4,160,44
buffer_store_dwordx4 v[72:75], v84, s[36:39], s49 offen sc1
v_add_u32 v76, 1, v76
mav 8,164,44
s_waitcnt vmcnt(13)
buffer_store_dwordx2 v[76:77], v81, s[36:39], s52 offen sc1
s_waitcnt lgkmcnt(2)
mav 0,168,48
R 32,18432
mav 4,172,48
mav 8,176,48
mav 0,180,52
R 36,19456
mav 4,184,52
mav 8,188,52
s_waitcnt lgkmcnt(2)
mav 0,192,24
R 40,20480
mav 4,196,24
mav 8,200,24
s_waitcnt vmcnt(11)
v_add_f32 v108, v108, v110
mav 0,204,28
R 44,21504
v_add_f32 v109, v109, v111
mav 4,208,28
mav 8,212,28
v_permlane32_swap_b32 v108, v109
s_waitcnt lgkmcnt(2)
mav 0,216,32
R 48,22528
v_add_f32 v110, v108, v109
mav 4,220,32
v_add_f32 v111, v108, v109
mav 8,224,32
mav 0,228,36
R 52,23552
v_permlane16_swap_b32 v110, v111
mav 4,232,36
v_add_f32 v108, v110, v111
v_add_f32 v109, v110, v111
mav 8,236,36
s_waitcnt lgkmcnt(2)
mav 0,240,40
R 24,24576
v_permlane32_swap_b32 v108, v109
mav 4,244,40
v_mul_f32 v110, 0x3a800000, v108
v_mul_f32 v111, 0x3a800000, v109
mav 8,248,40
v_fma_f32 v111, -v110, v110, v111
v_max_f32 v111, 0, v111
mav 0,252,44
R 28,25600
v_add_f32 v111, 0x3727c5ac, v111
mvv 4,128,44
v_rsq_f32 v108, v111
mvv 8,132,44
v_mul_f32_e64 v109, -v110, v108
s_waitcnt lgkmcnt(2)
mvv 0,136,48
R 32,26624
mvv 4,140,48
ds_write_b64 v89, v[108:109] offset:0
mvv 8,144,48
mvv 0,148,52
R 36,27648
mvv 4,152,52
mvv 8,156,52
s_waitcnt lgkmcnt(3)
mvv 0,160,24
R 40,28672
mvv 4,164,24
mvv 8,168,24
mvv 0,172,28
R 44,29696
mvv 4,176,28
mvv 8,180,28
s_waitcnt lgkmcnt(2)
mvv 0,184,32
R 48,30720
s_xor_b32 s50, s50, 0x10000
mvv 4,188,32
mvv 8,192,32
mvv 0,196,36
R 52,31744
mvv 4,200,36
s_add_u32 s49, s49, 346112
mvv 8,204,36
s_waitcnt vmcnt(3)
s_barrier
s_waitcnt lgkmcnt(2)
mvv 0,208,40
R 24,32768
buffer_load_dwordx2 v[108:109], v90, s[36:39], s49 offen sc1
mvv 4,212,40
buffer_load_dwordx2 v[110:111], v91, s[36:39], s49 offen sc1
s_add_u32 m0, s50, 0
mvv 8,216,40
ds_read_b64 v[78:79], v89 offset:0
mvv 0,220,44
R 28,33792
D 85,0
mvv 4,224,44
mvv 8,228,44
s_waitcnt lgkmcnt(3)
mvv 0,232,48
R 32,34816
mvv 4,236,48
D 85,1024
mvv 8,240,48
mvv 0,244,52
R 36,35840
mvv 4,248,52
mvv 8,252,52
s_waitcnt lgkmcnt(2)
v_mfma_f32_16x16x32_bf16 v[12:15], a[0:3], v[24:27], 0
R 40,36864
D 85,2048
v_mfma_f32_16x16x32_bf16 v[16:19], a[4:7], v[24:27], 0
v_fma_f32 v104, v79, v56, v92
v_fma_f32 v0, v0, v78, v104
v_mfma_f32_16x16x32_bf16 v[20:23], a[8:11], v[24:27], 0
v_fma_f32 v105, v79, v57, v93
v_fma_f32 v1, v1, v78, v105
mav 12,12,28
R 44,37888
v_mul_f32_e64 v0, v0, |v0|
mav 16,16,28
D 85,3072
v_mul_f32_e64 v1, v1, |v1|
mav 20,20,28
v_cvt_pk_bf16_f32 v0, v0, v1
s_waitcnt lgkmcnt(2)
mav 12,24,32
R 48,38912
v_pk_max_i16 v68, v0, 0
mav 16,28,32
s_add_u32 m0, s50, 4096
v_fma_f32 v104, v79, v58, v94
mav 20,32,32
v_fma_f32 v2, v2, v78, v104
v_fma_f32 v105, v79, v59, v95
mav 12,36,36
R 52,39936
D 86,0
mav 16,40,36
v_fma_f32 v3, v3, v78, v105
v_mul_f32_e64 v2, v2, |v2|
mav 20,44,36
v_mul_f32_e64 v3, v3, |v3|
s_waitcnt lgkmcnt(2)
mav 12,48,40
R 24,40960
v_cvt_pk_bf16_f32 v2, v2, v3
mav 16,52,40
D 86,1024
v_pk_max_i16 v69, v2, 0
mav 20,56,40
v_fma_f32 v104, v79, v60, v96
v_fma_f32 v4, v4, v78, v104
mav 12,60,44
R 28,41984
v_fma_f32 v105, v79, v61, v97
mav 16,64,44
v_fma_f32 v5, v5, v78, v105
v_mul_f32_e64 v4, v4, |v4|
mav 20,68,44
v_mul_f32_e64 v5, v5, |v5|
s_waitcnt lgkmcnt(2)
mav 12,72,48
R 32,43008
D 86,2048
mav 16,76,48
v_cvt_pk_bf16_f32 v4, v4, v5
v_pk_max_i16 v70, v4, 0
mav 20,80,48
v_fma_f32 v104, v79, v62, v98
v_fma_f32 v6, v6, v78, v104
mav 12,84,52
R 36,44032
v_fma_f32 v105, v79, v63, v99
mav 16,88,52
D 86,3072
v_fma_f32 v7, v7, v78, v105
mav 20,92,52
v_mul_f32_e64 v6, v6, |v6|
s_waitcnt lgkmcnt(2)
mav 12,96,24
R 40,45056
v_mul_f32_e64 v7, v7, |v7|
mav 16,100,24
v_cvt_pk_bf16_f32 v6, v6, v7
v_pk_max_i16 v71, v6, 0
mav 20,104,24
buffer_store_dwordx4 v[68:71], v83, s[36:39], s49 offen sc1
v_fma_f32 v104, v79, v64, v100
mav 12,108,28
R 44,46080
v_fma_f32 v8, v8, v78, v104
mav 16,112,28
v_fma_f32 v105, v79, v65, v101
v_fma_f32 v9, v9, v78, v105
mav 20,116,28
v_mul_f32_e64 v8, v8, |v8|
s_waitcnt lgkmcnt(2)
mav 12,120,32
R 48,47104
v_mul_f32_e64 v9, v9, |v9|
mav 16,124,32
v_cvt_pk_bf16_f32 v8, v8, v9
v_pk_max_i16 v72, v8, 0
mav 20,128,32
v_pk_max_i16 v74, v8, 0
v_fma_f32 v104, v79, v66, v102
mav 12,132,36
R 52,48128
v_fma_f32 v10, v10, v78, v104
mav 16,136,36
v_fma_f32 v105, v79, v67, v103
v_fma_f32 v11, v11, v78, v105
mav 20,140,36
v_mul_f32_e64 v10, v10, |v10|
s_waitcnt lgkmcnt(2)
mav 12,144,40
R 24,49152
v_mul_f32_e64 v11, v11, |v11|
mav 16,148,40
v_cvt_pk_bf16_f32 v10, v10, v11
v_pk_max_i16 v73, v10, 0
mav 20,152,40
v_pk_max_i16 v75, v10, 0
v_permlane16_swap_b32 v72, v74
mav 12,156,44
R 28,50176
v_permlane16_swap_b32 v73, v75
mav 16,160,44
buffer_store_dwordx4 v[72:75], v84, s[36:39], s49 offen sc1
v_add_u32 v76, 1, v76
mav 20,164,44
s_waitcnt vmcnt(13)
buffer_store_dwordx2 v[76:77], v81, s[36:39], s52 offen sc1
s_waitcnt lgkmcnt(2)
mav 12,168,48
R 32,51200
mav 16,172,48
mav 20,176,48
mav 12,180,52
R 36,52224
mav 16,184,52
mav 20,188,52
s_waitcnt lgkmcnt(2)
mav 12,192,24
R 40,53248
mav 16,196,24
mav 20,200,24
s_waitcnt vmcnt(11)
v_add_f32 v108, v108, v110
mav 12,204,28
R 44,54272
v_add_f32 v109, v109, v111
mav 16,208,28
mav 20,212,28
v_permlane32_swap_b32 v108, v109
s_waitcnt lgkmcnt(2)
mav 12,216,32
R 48,55296
v_add_f32 v110, v108, v109
mav 16,220,32
v_add_f32 v111, v108, v109
mav 20,224,32
mav 12,228,36
R 52,56320
v_permlane16_swap_b32 v110, v111
mav 16,232,36
v_add_f32 v108, v110, v111
v_add_f32 v109, v110, v111
mav 20,236,36
s_waitcnt lgkmcnt(2)
mav 12,240,40
R 24,57344
v_permlane32_swap_b32 v108, v109
mav 16,244,40
v_mul_f32 v110, 0x3a800000, v108
v_mul_f32 v111, 0x3a800000, v109
mav 20,248,40
v_fma_f32 v111, -v110, v110, v111
v_max_f32 v111, 0, v111
mav 12,252,44
R 28,58368
v_add_f32 v111, 0x3727c5ac, v111
mvv 16,128,44
v_rsq_f32 v108, v111
mvv 20,132,44
v_mul_f32_e64 v109, -v110, v108
s_waitcnt lgkmcnt(2)
mvv 12,136,48
R 32,59392
mvv 16,140,48
ds_write_b64 v89, v[108:109] offset:128
mvv 20,144,48
mvv 12,148,52
R 36,60416
mvv 16,152,52
mvv 20,156,52
s_waitcnt lgkmcnt(3)
mvv 12,160,24
R 40,61440
mvv 16,164,24
mvv 20,168,24
mvv 12,172,28
R 44,62464
mvv 16,176,28
mvv 20,180,28
s_waitcnt lgkmcnt(2)
mvv 12,184,32
R 48,63488
mvv 16,188,32
mvv 20,192,32
mvv 12,196,36
R 52,64512
v_xor_b32 v80, 0x10000, v80
mvv 16,200,36
s_add_u32 s49, s49, 346112
mvv 20,204,36
s_waitcnt vmcnt(3)
s_barrier
s_waitcnt lgkmcnt(2)
mvv 12,208,40
R 24,0
buffer_load_dwordx2 v[108:109], v90, s[36:39], s49 offen sc1
mvv 16,212,40
buffer_load_dwordx2 v[110:111], v91, s[36:39], s49 offen sc1
s_add_u32 m0, s50, 32768
mvv 20,216,40
ds_read_b64 v[78:79], v89 offset:128
mvv 12,220,44
R 28,1024
D 85,0
mvv 16,224,44
mvv 20,228,44
s_waitcnt lgkmcnt(3)
mvv 12,232,48
R 32,2048
mvv 16,236,48
D 85,1024
mvv 20,240,48
mvv 12,244,52
R 36,3072
mvv 16,248,52
mvv 20,252,52
s_add_u32 s60, s60, 1
s_cbranch_scc0 LF1_1
s_waitcnt lgkmcnt(2)
v_mfma_f32_16x16x32_bf16 v[0:3], a[0:3], v[24:27], 0
R 40,4096
D 85,2048
v_mfma_f32_16x16x32_bf16 v[4:7], a[4:7], v[24:27], 0
v_fma_f32 v104, v79, v56, v92
v_fma_f32 v12, v12, v78, v104
v_mfma_f32_16x16x32_bf16 v[8:11], a[8:11], v[24:27], 0
v_fma_f32 v105, v79, v57, v93
v_fma_f32 v13, v13, v78, v105
mav 0,12,28
R 44,5120
v_mul_f32_e64 v12, v12, |v12|
mav 4,16,28
D 85,3072
v_mul_f32_e64 v13, v13, |v13|
mav 8,20,28
v_cvt_pk_bf16_f32 v12, v12, v13
s_waitcnt lgkmcnt(2)
mav 0,24,32
R 48,6144
v_pk_max_i16 v68, v12, 0
mav 4,28,32
s_add_u32 m0, s50, 36864
v_fma_f32 v104, v79, v58, v94
mav 8,32,32
v_fma_f32 v14, v14, v78, v104
v_fma_f32 v105, v79, v59, v95
mav 0,36,36
R 52,7168
D 86,0
mav 4,40,36
v_fma_f32 v15, v15, v78, v105
v_mul_f32_e64 v14, v14, |v14|
mav 8,44,36
v_mul_f32_e64 v15, v15, |v15|
s_waitcnt lgkmcnt(2)
mav 0,48,40
R 24,8192
v_cvt_pk_bf16_f32 v14, v14, v15
mav 4,52,40
D 86,1024
v_pk_max_i16 v69, v14, 0
mav 8,56,40
v_fma_f32 v104, v79, v60, v96
v_fma_f32 v16, v16, v78, v104
mav 0,60,44
R 28,9216
v_fma_f32 v105, v79, v61, v97
mav 4,64,44
v_fma_f32 v17, v17, v78, v105
v_mul_f32_e64 v16, v16, |v16|
mav 8,68,44
v_mul_f32_e64 v17, v17, |v17|
s_waitcnt lgkmcnt(2)
mav 0,72,48
R 32,10240
D 86,2048
mav 4,76,48
v_cvt_pk_bf16_f32 v16, v16, v17
v_pk_max_i16 v70, v16, 0
mav 8,80,48
v_fma_f32 v104, v79, v62, v98
v_fma_f32 v18, v18, v78, v104
mav 0,84,52
R 36,11264
v_fma_f32 v105, v79, v63, v99
mav 4,88,52
D 86,3072
v_fma_f32 v19, v19, v78, v105
mav 8,92,52
v_mul_f32_e64 v18, v18, |v18|
s_waitcnt lgkmcnt(2)
mav 0,96,24
R 40,12288
v_mul_f32_e64 v19, v19, |v19|
mav 4,100,24
v_cvt_pk_bf16_f32 v18, v18, v19
v_pk_max_i16 v71, v18, 0
mav 8,104,24
buffer_store_dwordx4 v[68:71], v83, s[36:39], s49 offen sc1
v_fma_f32 v104, v79, v64, v100
mav 0,108,28
R 44,13312
v_fma_f32 v20, v20, v78, v104
mav 4,112,28
v_fma_f32 v105, v79, v65, v101
v_fma_f32 v21, v21, v78, v105
mav 8,116,28
v_mul_f32_e64 v20, v20, |v20|
s_waitcnt lgkmcnt(2)
mav 0,120,32
R 48,14336
v_mul_f32_e64 v21, v21, |v21|
mav 4,124,32
v_cvt_pk_bf16_f32 v20, v20, v21
v_pk_max_i16 v72, v20, 0
mav 8,128,32
v_pk_max_i16 v74, v20, 0
v_fma_f32 v104, v79, v66, v102
mav 0,132,36
R 52,15360
v_fma_f32 v22, v22, v78, v104
mav 4,136,36
v_fma_f32 v105, v79, v67, v103
v_fma_f32 v23, v23, v78, v105
mav 8,140,36
v_mul_f32_e64 v22, v22, |v22|
s_waitcnt lgkmcnt(2)
mav 0,144,40
R 24,16384
v_mul_f32_e64 v23, v23, |v23|
mav 4,148,40
v_cvt_pk_bf16_f32 v22, v22, v23
v_pk_max_i16 v73, v22, 0
mav 8,152,40
v_pk_max_i16 v75, v22, 0
v_permlane16_swap_b32 v72, v74
mav 0,156,44
R 28,17408
v_permlane16_swap_b32 v73, v75
mav 4,160,44
buffer_store_dwordx4 v[72:75], v84, s[36:39], s49 offen sc1
v_add_u32 v76, 1, v76
mav 8,164,44
s_waitcnt vmcnt(13)
buffer_store_dwordx2 v[76:77], v81, s[36:39], s52 offen sc1
s_waitcnt lgkmcnt(2)
mav 0,168,48
R 32,18432
mav 4,172,48
mav 8,176,48
mav 0,180,52
R 36,19456
mav 4,184,52
mav 8,188,52
s_waitcnt lgkmcnt(2)
mav 0,192,24
R 40,20480
mav 4,196,24
mav 8,200,24
s_waitcnt vmcnt(11)
v_add_f32 v108, v108, v110
mav 0,204,28
R 44,21504
v_add_f32 v109, v109, v111
mav 4,208,28
mav 8,212,28
v_permlane32_swap_b32 v108, v109
s_waitcnt lgkmcnt(2)
mav 0,216,32
R 48,22528
v_add_f32 v110, v108, v109
mav 4,220,32
v_add_f32 v111, v108, v109
mav 8,224,32
mav 0,228,36
R 52,23552
v_permlane16_swap_b32 v110, v111
mav 4,232,36
v_add_f32 v108, v110, v111
v_add_f32 v109, v110, v111
mav 8,236,36
s_waitcnt lgkmcnt(2)
mav 0,240,40
R 24,24576
v_permlane32_swap_b32 v108, v109
mav 4,244,40
v_mul_f32 v110, 0x3a800000, v108
v_mul_f32 v111, 0x3a800000, v109
mav 8,248,40
v_fma_f32 v111, -v110, v110, v111
v_max_f32 v111, 0, v111
mav 0,252,44
R 28,25600
v_add_f32 v111, 0x3727c5ac, v111
mvv 4,128,44
v_rsq_f32 v108, v111
mvv 8,132,44
v_mul_f32_e64 v109, -v110, v108
s_waitcnt lgkmcnt(2)
mvv 0,136,48
R 32,26624
mvv 4,140,48
ds_write_b64 v89, v[108:109] offset:0
mvv 8,144,48
mvv 0,148,52
R 36,27648
mvv 4,152,52
mvv 8,156,52
s_waitcnt lgkmcnt(3)
mvv 0,160,24
R 40,28672
mvv 4,164,24
mvv 8,168,24
mvv 0,172,28
R 44,29696
mvv 4,176,28
mvv 8,180,28
s_waitcnt lgkmcnt(2)
mvv 0,184,32
R 48,30720
mvv 4,188,32
mvv 8,192,32
mvv 0,196,36
R 52,31744
mvv 4,200,36
s_add_u32 s49, s49, 346112
mvv 8,204,36
s_waitcnt vmcnt(3)
s_barrier
s_waitcnt lgkmcnt(2)
mvv 0,208,40
R 24,32768
buffer_load_dwordx2 v[108:109], v90, s[36:39], s49 offen sc1
mvv 4,212,40
buffer_load_dwordx2 v[110:111], v91, s[36:39], s49 offen sc1
ds_read_b64 v[78:79], v89 offset:0
mvv 8,216,40
mvv 0,220,44
R 28,33792
mvv 4,224,44
mvv 8,228,44
s_waitcnt lgkmcnt(3)
mvv 0,232,48
R 32,34816
mvv 4,236,48
mvv 8,240,48
mvv 0,244,52
R 36,35840
mvv 4,248,52
mvv 8,252,52
s_waitcnt lgkmcnt(2)
v_mfma_f32_16x16x32_bf16 v[12:15], a[0:3], v[24:27], 0
R 40,36864
v_fma_f32 v104, v79, v56, v92
v_mfma_f32_16x16x32_bf16 v[16:19], a[4:7], v[24:27], 0
v_fma_f32 v0, v0, v78, v104
v_fma_f32 v105, v79, v57, v93
v_mfma_f32_16x16x32_bf16 v[20:23], a[8:11], v[24:27], 0
v_fma_f32 v1, v1, v78, v105
v_mul_f32_e64 v0, v0, |v0|
mav 12,12,28
R 44,37888
v_mul_f32_e64 v1, v1, |v1|
mav 16,16,28
v_cvt_pk_bf16_f32 v0, v0, v1
v_pk_max_i16 v68, v0, 0
mav 20,20,28
v_fma_f32 v104, v79, v58, v94
s_waitcnt lgkmcnt(2)
mav 12,24,32
R 48,38912
v_fma_f32 v2, v2, v78, v104
mav 16,28,32
v_fma_f32 v105, v79, v59, v95
v_fma_f32 v3, v3, v78, v105
mav 20,32,32
v_mul_f32_e64 v2, v2, |v2|
v_mul_f32_e64 v3, v3, |v3|
mav 12,36,36
R 52,39936
v_cvt_pk_bf16_f32 v2, v2, v3
mav 16,40,36
v_pk_max_i16 v69, v2, 0
v_fma_f32 v104, v79, v60, v96
mav 20,44,36
v_fma_f32 v4, v4, v78, v104
s_waitcnt lgkmcnt(2)
mav 12,48,40
R 24,40960
v_fma_f32 v105, v79, v61, v97
mav 16,52,40
v_fma_f32 v5, v5, v78, v105
v_mul_f32_e64 v4, v4, |v4|
mav 20,56,40
v_mul_f32_e64 v5, v5, |v5|
v_cvt_pk_bf16_f32 v4, v4, v5
mav 12,60,44
R 28,41984
v_pk_max_i16 v70, v4, 0
mav 16,64,44
v_fma_f32 v104, v79, v62, v98
v_fma_f32 v6, v6, v78, v104
mav 20,68,44
v_fma_f32 v105, v79, v63, v99
s_waitcnt lgkmcnt(2)
mav 12,72,48
R 32,43008
v_fma_f32 v7, v7, v78, v105
mav 16,76,48
v_mul_f32_e64 v6, v6, |v6|
v_mul_f32_e64 v7, v7, |v7|
mav 20,80,48
v_cvt_pk_bf16_f32 v6, v6, v7
v_pk_max_i16 v71, v6, 0
mav 12,84,52
R 36,44032
buffer_store_dwordx4 v[68:71], v83, s[36:39], s49 offen sc1
mav 16,88,52
v_fma_f32 v104, v79, v64, v100
v_fma_f32 v8, v8, v78, v104
mav 20,92,52
v_fma_f32 v105, v79, v65, v101
s_waitcnt lgkmcnt(2)
mav 12,96,24
R 40,45056
v_fma_f32 v9, v9, v78, v105
mav 16,100,24
v_mul_f32_e64 v8, v8, |v8|
v_mul_f32_e64 v9, v9, |v9|
mav 20,104,24
v_cvt_pk_bf16_f32 v8, v8, v9
v_pk_max_i16 v72, v8, 0
mav 12,108,28
R 44,46080
v_pk_max_i16 v74, v8, 0
mav 16,112,28
v_fma_f32 v104, v79, v66, v102
v_fma_f32 v10, v10, v78, v104
mav 20,116,28
v_fma_f32 v105, v79, v67, v103
s_waitcnt lgkmcnt(2)
mav 12,120,32
R 48,47104
v_fma_f32 v11, v11, v78, v105
mav 16,124,32
v_mul_f32_e64 v10, v10, |v10|
v_mul_f32_e64 v11, v11, |v11|
mav 20,128,32
v_cvt_pk_bf16_f32 v10, v10, v11
v_pk_max_i16 v73, v10, 0
mav 12,132,36
R 52,48128
v_pk_max_i16 v75, v10, 0
mav 16,136,36
v_permlane16_swap_b32 v72, v74
v_permlane16_swap_b32 v73, v75
mav 20,140,36
buffer_store_dwordx4 v[72:75], v84, s[36:39], s49 offen sc1
s_waitcnt lgkmcnt(2)
mav 12,144,40
R 24,49152
v_add_u32 v76, 1, v76
mav 16,148,40
s_waitcnt vmcnt(5)
buffer_store_dwordx2 v[76:77], v81, s[36:39], s52 offen sc1
mav 20,152,40
mav 12,156,44
R 28,50176
mav 16,160,44
mav 20,164,44
s_waitcnt lgkmcnt(2)
mav 12,168,48
R 32,51200
mav 16,172,48
mav 20,176,48
mav 12,180,52
R 36,52224
mav 16,184,52
mav 20,188,52
s_waitcnt lgkmcnt(2)
mav 12,192,24
R 40,53248
mav 16,196,24
mav 20,200,24
s_waitcnt vmcnt(3)
v_add_f32 v108, v108, v110
mav 12,204,28
R 44,54272
v_add_f32 v109, v109, v111
mav 16,208,28
mav 20,212,28
v_permlane32_swap_b32 v108, v109
s_waitcnt lgkmcnt(2)
mav 12,216,32
R 48,55296
v_add_f32 v110, v108, v109
mav 16,220,32
v_add_f32 v111, v108, v109
mav 20,224,32
mav 12,228,36
R 52,56320
v_permlane16_swap_b32 v110, v111
mav 16,232,36
v_add_f32 v108, v110, v111
v_add_f32 v109, v110, v111
mav 20,236,36
s_waitcnt lgkmcnt(2)
mav 12,240,40
R 24,57344
v_permlane32_swap_b32 v108, v109
mav 16,244,40
v_mul_f32 v110, 0x3a800000, v108
v_mul_f32 v111, 0x3a800000, v109
mav 20,248,40
v_fma_f32 v111, -v110, v110, v111
v_max_f32 v111, 0, v111
mav 12,252,44
R 28,58368
v_add_f32 v111, 0x3727c5ac, v111
mvv 16,128,44
v_rsq_f32 v108, v111
mvv 20,132,44
v_mul_f32_e64 v109, -v110, v108
s_waitcnt lgkmcnt(2)
mvv 12,136,48
R 32,59392
mvv 16,140,48
ds_write_b64 v89, v[108:109] offset:128
mvv 20,144,48
mvv 12,148,52
R 36,60416
mvv 16,152,52
mvv 20,156,52
s_waitcnt lgkmcnt(3)
mvv 12,160,24
R 40,61440
mvv 16,164,24
mvv 20,168,24
mvv 12,172,28
R 44,62464
mvv 16,176,28
mvv 20,180,28
s_waitcnt lgkmcnt(2)
mvv 12,184,32
R 48,63488
mvv 16,188,32
mvv 20,192,32
mvv 12,196,36
R 52,64512
mvv 16,200,36
s_add_u32 s49, s49, 346112
mvv 20,204,36
s_barrier
s_waitcnt lgkmcnt(2)
mvv 12,208,40
mvv 16,212,40
ds_read_b64 v[78:79], v89 offset:128
mvv 20,216,40
mvv 12,220,44
mvv 16,224,44
mvv 20,228,44
s_waitcnt lgkmcnt(1)
mvv 12,232,48
mvv 16,236,48
mvv 20,240,48
mvv 12,244,52
mvv 16,248,52
mvv 20,252,52
s_waitcnt lgkmcnt(0)
v_fma_f32 v104, v79, v56, v92
s_nop 3
v_fma_f32 v12, v12, v78, v104
v_fma_f32 v105, v79, v57, v93
v_fma_f32 v13, v13, v78, v105
v_mul_f32_e64 v12, v12, |v12|
v_mul_f32_e64 v13, v13, |v13|
v_cvt_pk_bf16_f32 v12, v12, v13
v_pk_max_i16 v68, v12, 0
v_fma_f32 v104, v79, v58, v94
v_fma_f32 v14, v14, v78, v104
v_fma_f32 v105, v79, v59, v95
v_fma_f32 v15, v15, v78, v105
v_mul_f32_e64 v14, v14, |v14|
v_mul_f32_e64 v15, v15, |v15|
v_cvt_pk_bf16_f32 v14, v14, v15
v_pk_max_i16 v69, v14, 0
v_fma_f32 v104, v79, v60, v96
v_fma_f32 v16, v16, v78, v104
v_fma_f32 v105, v79, v61, v97
v_fma_f32 v17, v17, v78, v105
v_mul_f32_e64 v16, v16, |v16|
v_mul_f32_e64 v17, v17, |v17|
v_cvt_pk_bf16_f32 v16, v16, v17
v_pk_max_i16 v70, v16, 0
v_fma_f32 v104, v79, v62, v98
v_fma_f32 v18, v18, v78, v104
v_fma_f32 v105, v79, v63, v99
v_fma_f32 v19, v19, v78, v105
v_mul_f32_e64 v18, v18, |v18|
v_mul_f32_e64 v19, v19, |v19|
v_cvt_pk_bf16_f32 v18, v18, v19
v_pk_max_i16 v71, v18, 0
buffer_store_dwordx4 v[68:71], v83, s[36:39], s49 offen sc1
v_fma_f32 v104, v79, v64, v100
v_fma_f32 v20, v20, v78, v104
v_fma_f32 v105, v79, v65, v101
v_fma_f32 v21, v21, v78, v105
v_mul_f32_e64 v20, v20, |v20|
v_mul_f32_e64 v21, v21, |v21|
v_cvt_pk_bf16_f32 v20, v20, v21
v_pk_max_i16 v72, v20, 0
v_pk_max_i16 v74, v20, 0
v_fma_f32 v104, v79, v66, v102
v_fma_f32 v22, v22, v78, v104
v_fma_f32 v105, v79, v67, v103
v_fma_f32 v23, v23, v78, v105
v_mul_f32_e64 v22, v22, |v22|
v_mul_f32_e64 v23, v23, |v23|
v_cvt_pk_bf16_f32 v22, v22, v23
v_pk_max_i16 v73, v22, 0
v_pk_max_i16 v75, v22, 0
v_permlane16_swap_b32 v72, v74
s_nop 0
v_permlane16_swap_b32 v73, v75
buffer_store_dwordx4 v[72:75], v84, s[36:39], s49 offen sc1
v_add_u32 v76, 1, v76
s_waitcnt vmcnt(3)
buffer_store_dwordx2 v[76:77], v81, s[36:39], s52 offen sc1
v_add_u32 v76, 37, v76
s_waitcnt vmcnt(1)
buffer_store_dwordx2 v[76:77], v81, s[36:39], s52 offen sc1
s_endpgm
LvF2:
s_add_u32 s49, s49, 346112
s_barrier
s_add_u32 m0, s50, 0
s_nop 0
D 85,0
D 85,1024
D 85,2048
D 85,3072
s_add_u32 m0, s50, 4096
s_nop 0
D 86,0
D 86,1024
D 86,2048
D 86,3072
s_add_u32 s49, s49, 346112
s_waitcnt vmcnt(0)
s_barrier
R 24,0
s_add_u32 m0, s50, 32768
R 28,1024
D 85,0
R 32,2048
D 85,1024
R 36,3072
s_waitcnt lgkmcnt(2)
v_mfma_f32_16x16x32_bf16 v[0:3], a[0:3], v[24:27], 0
R 40,4096
D 85,2048
v_mfma_f32_16x16x32_bf16 v[4:7], a[4:7], v[24:27], 0
v_mfma_f32_16x16x32_bf16 v[8:11], a[8:11], v[24:27], 0
mav 0,12,28
R 44,5120
mav 4,16,28
D 85,3072
mav 8,20,28
s_waitcnt lgkmcnt(2)
mav 0,24,32
R 48,6144
mav 4,28,32
s_add_u32 m0, s50, 36864
mav 8,32,32
mav 0,36,36
R 52,7168
D 86,0
mav 4,40,36
mav 8,44,36
s_waitcnt lgkmcnt(2)
mav 0,48,40
R 24,8192
mav 4,52,40
D 86,1024
mav 8,56,40
mav 0,60,44
R 28,9216
mav 4,64,44
mav 8,68,44
s_waitcnt lgkmcnt(2)
mav 0,72,48
R 32,10240
D 86,2048
mav 4,76,48
mav 8,80,48
mav 0,84,52
R 36,11264
mav 4,88,52
D 86,3072
mav 8,92,52
s_waitcnt lgkmcnt(2)
mav 0,96,24
R 40,12288
mav 4,100,24
mav 8,104,24
mav 0,108,28
R 44,13312
mav 4,112,28
mav 8,116,28
s_waitcnt lgkmcnt(2)
mav 0,120,32
R 48,14336
mav 4,124,32
mav 8,128,32
mav 0,132,36
R 52,15360
mav 4,136,36
mav 8,140,36
s_waitcnt lgkmcnt(2)
mav 0,144,40
R 24,16384
mav 4,148,40
mav 8,152,40
mav 0,156,44
R 28,17408
mav 4,160,44
mav 8,164,44
s_waitcnt lgkmcnt(2)
mav 0,168,48
R 32,18432
mav 4,172,48
mav 8,176,48
mav 0,180,52
R 36,19456
mav 4,184,52
mav 8,188,52
s_waitcnt lgkmcnt(2)
mav 0,192,24
R 40,20480
mav 4,196,24
mav 8,200,24
mav 0,204,28
R 44,21504
mav 4,208,28
mav 8,212,28
s_waitcnt lgkmcnt(2)
mav 0,216,32
R 48,22528
mav 4,220,32
mav 8,224,32
mav 0,228,36
R 52,23552
mav 4,232,36
mav 8,236,36
s_waitcnt lgkmcnt(2)
mav 0,240,40
R 24,24576
mav 4,244,40
mav 8,248,40
mav 0,252,44
R 28,25600
mvv 4,128,44
mvv 8,132,44
s_waitcnt lgkmcnt(2)
mvv 0,136,48
R 32,26624
mvv 4,140,48
mvv 8,144,48
mvv 0,148,52
R 36,27648
mvv 4,152,52
mvv 8,156,52
s_waitcnt lgkmcnt(2)
mvv 0,160,24
R 40,28672
mvv 4,164,24
mvv 8,168,24
mvv 0,172,28
R 44,29696
mvv 4,176,28
mvv 8,180,28
s_waitcnt lgkmcnt(2)
mvv 0,184,32
R 48,30720
s_xor_b32 s50, s50, 0x10000
mvv 4,188,32
mvv 8,192,32
mvv 0,196,36
R 52,31744
mvv 4,200,36
s_add_u32 s49, s49, 346112
mvv 8,204,36
s_waitcnt vmcnt(0)
s_barrier
s_waitcnt lgkmcnt(2)
mvv 0,208,40
R 24,32768
mvv 4,212,40
s_add_u32 m0, s50, 0
ds_read_b64 v[78:79], v89 offset:0
mvv 8,216,40
mvv 0,220,44
R 28,33792
D 85,0
mvv 4,224,44
mvv 8,228,44
s_waitcnt lgkmcnt(3)
mvv 0,232,48
R 32,34816
mvv 4,236,48
D 85,1024
mvv 8,240,48
mvv 0,244,52
R 36,35840
mvv 4,248,52
mvv 8,252,52
s_waitcnt lgkmcnt(2)
v_mfma_f32_16x16x32_bf16 v[12:15], a[0:3], v[24:27], 0
R 40,36864
D 85,2048
v_mfma_f32_16x16x32_bf16 v[16:19], a[4:7], v[24:27], 0
v_fma_f32 v104, v79, v56, v92
v_fma_f32 v0, v0, v78, v104
v_mfma_f32_16x16x32_bf16 v[20:23], a[8:11], v[24:27], 0
v_fma_f32 v105, v79, v57, v93
v_fma_f32 v1, v1, v78, v105
mav 12,12,28
R 44,37888
v_mul_f32_e64 v0, v0, |v0|
mav 16,16,28
D 85,3072
v_mul_f32_e64 v1, v1, |v1|
mav 20,20,28
v_cvt_pk_bf16_f32 v0, v0, v1
s_waitcnt lgkmcnt(2)
mav 12,24,32
R 48,38912
v_pk_max_i16 v68, v0, 0
mav 16,28,32
s_add_u32 m0, s50, 4096
v_fma_f32 v104, v79, v58, v94
mav 20,32,32
v_fma_f32 v2, v2, v78, v104
v_fma_f32 v105, v79, v59, v95
mav 12,36,36
R 52,39936
D 86,0
mav 16,40,36
v_fma_f32 v3, v3, v78, v105
v_mul_f32_e64 v2, v2, |v2|
mav 20,44,36
v_mul_f32_e64 v3, v3, |v3|
s_waitcnt lgkmcnt(2)
mav 12,48,40
R 24,40960
v_cvt_pk_bf16_f32 v2, v2, v3
mav 16,52,40
D 86,1024
v_pk_max_i16 v69, v2, 0
mav 20,56,40
v_fma_f32 v104, v79, v60, v96
v_fma_f32 v4, v4, v78, v104
mav 12,60,44
R 28,41984
v_fma_f32 v105, v79, v61, v97
mav 16,64,44
v_fma_f32 v5, v5, v78, v105
v_mul_f32_e64 v4, v4, |v4|
mav 20,68,44
v_mul_f32_e64 v5, v5, |v5|
s_waitcnt lgkmcnt(2)
mav 12,72,48
R 32,43008
D 86,2048
mav 16,76,48
v_cvt_pk_bf16_f32 v4, v4, v5
v_pk_max_i16 v70, v4, 0
mav 20,80,48
v_fma_f32 v104, v79, v62, v98
v_fma_f32 v6, v6, v78, v104
mav 12,84,52
R 36,44032
v_fma_f32 v105, v79, v63, v99
mav 16,88,52
D 86,3072
v_fma_f32 v7, v7, v78, v105
mav 20,92,52
v_mul_f32_e64 v6, v6, |v6|
s_waitcnt lgkmcnt(2)
mav 12,96,24
R 40,45056
v_mul_f32_e64 v7, v7, |v7|
mav 16,100,24
v_cvt_pk_bf16_f32 v6, v6, v7
v_pk_max_i16 v71, v6, 0
mav 20,104,24
buffer_store_dwordx4 v[68:71], v83, s[36:39], s49 offen sc1
v_fma_f32 v104, v79, v64, v100
mav 12,108,28
R 44,46080
v_fma_f32 v8, v8, v78, v104
mav 16,112,28
v_fma_f32 v105, v79, v65, v101
v_fma_f32 v9, v9, v78, v105
mav 20,116,28
v_mul_f32_e64 v8, v8, |v8|
s_waitcnt lgkmcnt(2)
mav 12,120,32
R 48,47104
v_mul_f32_e64 v9, v9, |v9|
mav 16,124,32
v_cvt_pk_bf16_f32 v8, v8, v9
v_pk_max_i16 v72, v8, 0
mav 20,128,32
v_pk_max_i16 v74, v8, 0
v_fma_f32 v104, v79, v66, v102
mav 12,132,36
R 52,48128
v_fma_f32 v10, v10, v78, v104
mav 16,136,36
v_fma_f32 v105, v79, v67, v103
v_fma_f32 v11, v11, v78, v105
mav 20,140,36
v_mul_f32_e64 v10, v10, |v10|
s_waitcnt lgkmcnt(2)
mav 12,144,40
R 24,49152
v_mul_f32_e64 v11, v11, |v11|
mav 16,148,40
v_cvt_pk_bf16_f32 v10, v10, v11
v_pk_max_i16 v73, v10, 0
mav 20,152,40
v_pk_max_i16 v75, v10, 0
v_permlane16_swap_b32 v72, v74
mav 12,156,44
R 28,50176
v_permlane16_swap_b32 v73, v75
mav 16,160,44
buffer_store_dwordx4 v[72:75], v84, s[36:39], s49 offen sc1
mav 20,164,44
s_waitcnt lgkmcnt(2)
mav 12,168,48
R 32,51200
mav 16,172,48
mav 20,176,48
mav 12,180,52
R 36,52224
mav 16,184,52
mav 20,188,52
s_waitcnt lgkmcnt(2)
mav 12,192,24
R 40,53248
mav 16,196,24
mav 20,200,24
mav 12,204,28
R 44,54272
mav 16,208,28
mav 20,212,28
s_waitcnt lgkmcnt(2)
mav 12,216,32
R 48,55296
mav 16,220,32
mav 20,224,32
mav 12,228,36
R 52,56320
mav 16,232,36
)"
R"(
mav 20,236,36
s_waitcnt lgkmcnt(2)
mav 12,240,40
R 24,57344
mav 16,244,40
mav 20,248,40
mav 12,252,44
R 28,58368
mvv 16,128,44
mvv 20,132,44
s_waitcnt lgkmcnt(2)
mvv 12,136,48
R 32,59392
mvv 16,140,48
mvv 20,144,48
mvv 12,148,52
R 36,60416
mvv 16,152,52
mvv 20,156,52
s_waitcnt lgkmcnt(2)
mvv 12,160,24
R 40,61440
mvv 16,164,24
mvv 20,168,24
mvv 12,172,28
R 44,62464
mvv 16,176,28
mvv 20,180,28
s_waitcnt lgkmcnt(2)
mvv 12,184,32
R 48,63488
mvv 16,188,32
mvv 20,192,32
mvv 12,196,36
R 52,64512
v_xor_b32 v80, 0x10000, v80
mvv 16,200,36
s_add_u32 s49, s49, 346112
mvv 20,204,36
s_waitcnt vmcnt(2)
s_barrier
s_waitcnt lgkmcnt(2)
mvv 12,208,40
R 24,0
mvv 16,212,40
s_add_u32 m0, s50, 32768
ds_read_b64 v[78:79], v89 offset:128
mvv 20,216,40
mvv 12,220,44
R 28,1024
D 85,0
mvv 16,224,44
mvv 20,228,44
s_waitcnt lgkmcnt(3)
mvv 12,232,48
R 32,2048
mvv 16,236,48
D 85,1024
mvv 20,240,48
mvv 12,244,52
R 36,3072
mvv 16,248,52
mvv 20,252,52
s_waitcnt lgkmcnt(2)
v_mfma_f32_16x16x32_bf16 v[0:3], a[0:3], v[24:27], 0
R 40,4096
D 85,2048
v_mfma_f32_16x16x32_bf16 v[4:7], a[4:7], v[24:27], 0
v_fma_f32 v104, v79, v56, v92
v_fma_f32 v12, v12, v78, v104
v_mfma_f32_16x16x32_bf16 v[8:11], a[8:11], v[24:27], 0
v_fma_f32 v105, v79, v57, v93
v_fma_f32 v13, v13, v78, v105
mav 0,12,28
R 44,5120
v_mul_f32_e64 v12, v12, |v12|
mav 4,16,28
D 85,3072
v_mul_f32_e64 v13, v13, |v13|
mav 8,20,28
v_cvt_pk_bf16_f32 v12, v12, v13
s_waitcnt lgkmcnt(2)
mav 0,24,32
R 48,6144
v_pk_max_i16 v68, v12, 0
mav 4,28,32
s_add_u32 m0, s50, 36864
v_fma_f32 v104, v79, v58, v94
mav 8,32,32
v_fma_f32 v14, v14, v78, v104
v_fma_f32 v105, v79, v59, v95
mav 0,36,36
R 52,7168
D 86,0
mav 4,40,36
v_fma_f32 v15, v15, v78, v105
v_mul_f32_e64 v14, v14, |v14|
mav 8,44,36
v_mul_f32_e64 v15, v15, |v15|
s_waitcnt lgkmcnt(2)
mav 0,48,40
R 24,8192
v_cvt_pk_bf16_f32 v14, v14, v15
mav 4,52,40
D 86,1024
v_pk_max_i16 v69, v14, 0
mav 8,56,40
v_fma_f32 v104, v79, v60, v96
v_fma_f32 v16, v16, v78, v104
mav 0,60,44
R 28,9216
v_fma_f32 v105, v79, v61, v97
mav 4,64,44
v_fma_f32 v17, v17, v78, v105
v_mul_f32_e64 v16, v16, |v16|
mav 8,68,44
v_mul_f32_e64 v17, v17, |v17|
s_waitcnt lgkmcnt(2)
mav 0,72,48
R 32,10240
D 86,2048
mav 4,76,48
v_cvt_pk_bf16_f32 v16, v16, v17
v_pk_max_i16 v70, v16, 0
mav 8,80,48
v_fma_f32 v104, v79, v62, v98
v_fma_f32 v18, v18, v78, v104
mav 0,84,52
R 36,11264
v_fma_f32 v105, v79, v63, v99
mav 4,88,52
D 86,3072
v_fma_f32 v19, v19, v78, v105
mav 8,92,52
v_mul_f32_e64 v18, v18, |v18|
s_waitcnt lgkmcnt(2)
mav 0,96,24
R 40,12288
v_mul_f32_e64 v19, v19, |v19|
mav 4,100,24
v_cvt_pk_bf16_f32 v18, v18, v19
v_pk_max_i16 v71, v18, 0
mav 8,104,24
buffer_store_dwordx4 v[68:71], v83, s[36:39], s49 offen sc1
v_fma_f32 v104, v79, v64, v100
mav 0,108,28
R 44,13312
v_fma_f32 v20, v20, v78, v104
mav 4,112,28
v_fma_f32 v105, v79, v65, v101
v_fma_f32 v21, v21, v78, v105
mav 8,116,28
v_mul_f32_e64 v20, v20, |v20|
s_waitcnt lgkmcnt(2)
mav 0,120,32
R 48,14336
v_mul_f32_e64 v21, v21, |v21|
mav 4,124,32
v_cvt_pk_bf16_f32 v20, v20, v21
v_pk_max_i16 v72, v20, 0
mav 8,128,32
v_pk_max_i16 v74, v20, 0
v_fma_f32 v104, v79, v66, v102
mav 0,132,36
R 52,15360
v_fma_f32 v22, v22, v78, v104
mav 4,136,36
v_fma_f32 v105, v79, v67, v103
v_fma_f32 v23, v23, v78, v105
mav 8,140,36
v_mul_f32_e64 v22, v22, |v22|
s_waitcnt lgkmcnt(2)
mav 0,144,40
R 24,16384
v_mul_f32_e64 v23, v23, |v23|
mav 4,148,40
v_cvt_pk_bf16_f32 v22, v22, v23
v_pk_max_i16 v73, v22, 0
mav 8,152,40
v_pk_max_i16 v75, v22, 0
v_permlane16_swap_b32 v72, v74
mav 0,156,44
R 28,17408
v_permlane16_swap_b32 v73, v75
mav 4,160,44
buffer_store_dwordx4 v[72:75], v84, s[36:39], s49 offen sc1
v_add_u32 v76, 1, v76
mav 8,164,44
s_waitcnt vmcnt(10)
buffer_store_dwordx2 v[76:77], v81, s[36:39], s52 offen sc1
s_waitcnt lgkmcnt(2)
mav 0,168,48
R 32,18432
mav 4,172,48
mav 8,176,48
mav 0,180,52
R 36,19456
mav 4,184,52
mav 8,188,52
s_waitcnt lgkmcnt(2)
mav 0,192,24
R 40,20480
mav 4,196,24
mav 8,200,24
mav 0,204,28
R 44,21504
mav 4,208,28
mav 8,212,28
s_waitcnt lgkmcnt(2)
mav 0,216,32
R 48,22528
mav 4,220,32
mav 8,224,32
mav 0,228,36
R 52,23552
mav 4,232,36
mav 8,236,36
s_waitcnt lgkmcnt(2)
mav 0,240,40
R 24,24576
mav 4,244,40
mav 8,248,40
mav 0,252,44
R 28,25600
mvv 4,128,44
mvv 8,132,44
s_waitcnt lgkmcnt(2)
mvv 0,136,48
R 32,26624
mvv 4,140,48
mvv 8,144,48
mvv 0,148,52
R 36,27648
mvv 4,152,52
mvv 8,156,52
s_waitcnt lgkmcnt(2)
mvv 0,160,24
R 40,28672
mvv 4,164,24
mvv 8,168,24
mvv 0,172,28
R 44,29696
mvv 4,176,28
mvv 8,180,28
s_waitcnt lgkmcnt(2)
mvv 0,184,32
R 48,30720
s_xor_b32 s50, s50, 0x10000
mvv 4,188,32
mvv 8,192,32
mvv 0,196,36
R 52,31744
mvv 4,200,36
s_add_u32 s49, s49, 346112
mvv 8,204,36
s_waitcnt vmcnt(3)
s_barrier
s_waitcnt lgkmcnt(2)
mvv 0,208,40
R 24,32768
mvv 4,212,40
s_add_u32 m0, s50, 0
ds_read_b64 v[78:79], v89 offset:0
mvv 8,216,40
mvv 0,220,44
R 28,33792
D 85,0
mvv 4,224,44
mvv 8,228,44
s_waitcnt lgkmcnt(3)
mvv 0,232,48
R 32,34816
mvv 4,236,48
D 85,1024
mvv 8,240,48
mvv 0,244,52
R 36,35840
mvv 4,248,52
mvv 8,252,52
s_waitcnt lgkmcnt(2)
v_mfma_f32_16x16x32_bf16 v[12:15], a[0:3], v[24:27], 0
R 40,36864
D 85,2048
v_mfma_f32_16x16x32_bf16 v[16:19], a[4:7], v[24:27], 0
v_fma_f32 v104, v79, v56, v92
v_fma_f32 v0, v0, v78, v104
v_mfma_f32_16x16x32_bf16 v[20:23], a[8:11], v[24:27], 0
v_fma_f32 v105, v79, v57, v93
v_fma_f32 v1, v1, v78, v105
mav 12,12,28
R 44,37888
v_mul_f32_e64 v0, v0, |v0|
mav 16,16,28
D 85,3072
v_mul_f32_e64 v1, v1, |v1|
mav 20,20,28
v_cvt_pk_bf16_f32 v0, v0, v1
s_waitcnt lgkmcnt(2)
mav 12,24,32
R 48,38912
v_pk_max_i16 v68, v0, 0
mav 16,28,32
s_add_u32 m0, s50, 4096
v_fma_f32 v104, v79, v58, v94
mav 20,32,32
v_fma_f32 v2, v2, v78, v104
v_fma_f32 v105, v79, v59, v95
mav 12,36,36
R 52,39936
D 86,0
mav 16,40,36
v_fma_f32 v3, v3, v78, v105
v_mul_f32_e64 v2, v2, |v2|
mav 20,44,36
v_mul_f32_e64 v3, v3, |v3|
s_waitcnt lgkmcnt(2)
mav 12,48,40
R 24,40960
v_cvt_pk_bf16_f32 v2, v2, v3
mav 16,52,40
D 86,1024
v_pk_max_i16 v69, v2, 0
mav 20,56,40
v_fma_f32 v104, v79, v60, v96
v_fma_f32 v4, v4, v78, v104
mav 12,60,44
R 28,41984
v_fma_f32 v105, v79, v61, v97
mav 16,64,44
v_fma_f32 v5, v5, v78, v105
v_mul_f32_e64 v4, v4, |v4|
mav 20,68,44
v_mul_f32_e64 v5, v5, |v5|
s_waitcnt lgkmcnt(2)
mav 12,72,48
R 32,43008
D 86,2048
mav 16,76,48
v_cvt_pk_bf16_f32 v4, v4, v5
v_pk_max_i16 v70, v4, 0
mav 20,80,48
v_fma_f32 v104, v79, v62, v98
v_fma_f32 v6, v6, v78, v104
mav 12,84,52
R 36,44032
v_fma_f32 v105, v79, v63, v99
mav 16,88,52
D 86,3072
v_fma_f32 v7, v7, v78, v105
mav 20,92,52
v_mul_f32_e64 v6, v6, |v6|
s_waitcnt lgkmcnt(2)
mav 12,96,24
R 40,45056
v_mul_f32_e64 v7, v7, |v7|
mav 16,100,24
v_cvt_pk_bf16_f32 v6, v6, v7
v_pk_max_i16 v71, v6, 0
mav 20,104,24
buffer_store_dwordx4 v[68:71], v83, s[36:39], s49 offen sc1
v_fma_f32 v104, v79, v64, v100
mav 12,108,28
R 44,46080
v_fma_f32 v8, v8, v78, v104
mav 16,112,28
v_fma_f32 v105, v79, v65, v101
v_fma_f32 v9, v9, v78, v105
mav 20,116,28
v_mul_f32_e64 v8, v8, |v8|
s_waitcnt lgkmcnt(2)
mav 12,120,32
R 48,47104
v_mul_f32_e64 v9, v9, |v9|
mav 16,124,32
v_cvt_pk_bf16_f32 v8, v8, v9
v_pk_max_i16 v72, v8, 0
mav 20,128,32
v_pk_max_i16 v74, v8, 0
v_fma_f32 v104, v79, v66, v102
mav 12,132,36
R 52,48128
v_fma_f32 v10, v10, v78, v104
mav 16,136,36
v_fma_f32 v105, v79, v67, v103
v_fma_f32 v11, v11, v78, v105
mav 20,140,36
v_mul_f32_e64 v10, v10, |v10|
s_waitcnt lgkmcnt(2)
mav 12,144,40
R 24,49152
v_mul_f32_e64 v11, v11, |v11|
mav 16,148,40
v_cvt_pk_bf16_f32 v10, v10, v11
v_pk_max_i16 v73, v10, 0
mav 20,152,40
v_pk_max_i16 v75, v10, 0
v_permlane16_swap_b32 v72, v74
mav 12,156,44
R 28,50176
v_permlane16_swap_b32 v73, v75
mav 16,160,44
buffer_store_dwordx4 v[72:75], v84, s[36:39], s49 offen sc1
v_add_u32 v76, 1, v76
mav 20,164,44
s_waitcnt vmcnt(11)
buffer_store_dwordx2 v[76:77], v81, s[36:39], s52 offen sc1
s_waitcnt lgkmcnt(2)
mav 12,168,48
R 32,51200
mav 16,172,48
mav 20,176,48
mav 12,180,52
R 36,52224
mav 16,184,52
mav 20,188,52
s_waitcnt lgkmcnt(2)
mav 12,192,24
R 40,53248
mav 16,196,24
mav 20,200,24
mav 12,204,28
R 44,54272
mav 16,208,28
mav 20,212,28
s_waitcnt lgkmcnt(2)
mav 12,216,32
R 48,55296
mav 16,220,32
mav 20,224,32
mav 12,228,36
R 52,56320
mav 16,232,36
mav 20,236,36
s_waitcnt lgkmcnt(2)
mav 12,240,40
R 24,57344
mav 16,244,40
mav 20,248,40
mav 12,252,44
R 28,58368
mvv 16,128,44
mvv 20,132,44
s_waitcnt lgkmcnt(2)
mvv 12,136,48
R 32,59392
mvv 16,140,48
mvv 20,144,48
mvv 12,148,52
R 36,60416
mvv 16,152,52
mvv 20,156,52
s_waitcnt lgkmcnt(2)
mvv 12,160,24
R 40,61440
mvv 16,164,24
mvv 20,168,24
mvv 12,172,28
R 44,62464
mvv 16,176,28
mvv 20,180,28
s_waitcnt lgkmcnt(2)
mvv 12,184,32
R 48,63488
mvv 16,188,32
mvv 20,192,32
mvv 12,196,36
R 52,64512
v_xor_b32 v80, 0x10000, v80
mvv 16,200,36
s_add_u32 s49, s49, 346112
mvv 20,204,36
s_waitcnt vmcnt(3)
s_barrier
s_waitcnt lgkmcnt(2)
mvv 12,208,40
R 24,0
mvv 16,212,40
s_add_u32 m0, s50, 32768
ds_read_b64 v[78:79], v89 offset:128
mvv 20,216,40
mvv 12,220,44
R 28,1024
D 85,0
mvv 16,224,44
mvv 20,228,44
s_waitcnt lgkmcnt(3)
mvv 12,232,48
R 32,2048
mvv 16,236,48
D 85,1024
mvv 20,240,48
mvv 12,244,52
R 36,3072
mvv 16,248,52
mvv 20,252,52
s_mov_b32 s60, -29
.p2align 6
LF2_1:
s_waitcnt lgkmcnt(2)
v_mfma_f32_16x16x32_bf16 v[0:3], a[0:3], v[24:27], 0
R 40,4096
D 85,2048
v_mfma_f32_16x16x32_bf16 v[4:7], a[4:7], v[24:27], 0
v_fma_f32 v104, v79, v56, v92
v_fma_f32 v12, v12, v78, v104
v_mfma_f32_16x16x32_bf16 v[8:11], a[8:11], v[24:27], 0
v_fma_f32 v105, v79, v57, v93
v_fma_f32 v13, v13, v78, v105
mav 0,12,28
R 44,5120
v_mul_f32_e64 v12, v12, |v12|
mav 4,16,28
D 85,3072
v_mul_f32_e64 v13, v13, |v13|
mav 8,20,28
v_cvt_pk_bf16_f32 v12, v12, v13
s_waitcnt lgkmcnt(2)
mav 0,24,32
R 48,6144
v_pk_max_i16 v68, v12, 0
mav 4,28,32
s_add_u32 m0, s50, 36864
v_fma_f32 v104, v79, v58, v94
mav 8,32,32
v_fma_f32 v14, v14, v78, v104
v_fma_f32 v105, v79, v59, v95
mav 0,36,36
R 52,7168
D 86,0
mav 4,40,36
v_fma_f32 v15, v15, v78, v105
v_mul_f32_e64 v14, v14, |v14|
mav 8,44,36
v_mul_f32_e64 v15, v15, |v15|
s_waitcnt lgkmcnt(2)
mav 0,48,40
R 24,8192
v_cvt_pk_bf16_f32 v14, v14, v15
mav 4,52,40
D 86,1024
v_pk_max_i16 v69, v14, 0
mav 8,56,40
v_fma_f32 v104, v79, v60, v96
v_fma_f32 v16, v16, v78, v104
mav 0,60,44
R 28,9216
v_fma_f32 v105, v79, v61, v97
mav 4,64,44
v_fma_f32 v17, v17, v78, v105
v_mul_f32_e64 v16, v16, |v16|
mav 8,68,44
v_mul_f32_e64 v17, v17, |v17|
s_waitcnt lgkmcnt(2)
mav 0,72,48
R 32,10240
D 86,2048
mav 4,76,48
v_cvt_pk_bf16_f32 v16, v16, v17
v_pk_max_i16 v70, v16, 0
mav 8,80,48
v_fma_f32 v104, v79, v62, v98
v_fma_f32 v18, v18, v78, v104
mav 0,84,52
R 36,11264
v_fma_f32 v105, v79, v63, v99
mav 4,88,52
D 86,3072
v_fma_f32 v19, v19, v78, v105
mav 8,92,52
v_mul_f32_e64 v18, v18, |v18|
s_waitcnt lgkmcnt(2)
mav 0,96,24
R 40,12288
v_mul_f32_e64 v19, v19, |v19|
mav 4,100,24
v_cvt_pk_bf16_f32 v18, v18, v19
v_pk_max_i16 v71, v18, 0
mav 8,104,24
buffer_store_dwordx4 v[68:71], v83, s[36:39], s49 offen sc1
v_fma_f32 v104, v79, v64, v100
mav 0,108,28
R 44,13312
v_fma_f32 v20, v20, v78, v104
mav 4,112,28
v_fma_f32 v105, v79, v65, v101
v_fma_f32 v21, v21, v78, v105
mav 8,116,28
v_mul_f32_e64 v20, v20, |v20|
s_waitcnt lgkmcnt(2)
mav 0,120,32
R 48,14336
v_mul_f32_e64 v21, v21, |v21|
mav 4,124,32
v_cvt_pk_bf16_f32 v20, v20, v21
v_pk_max_i16 v72, v20, 0
mav 8,128,32
v_pk_max_i16 v74, v20, 0
v_fma_f32 v104, v79, v66, v102
mav 0,132,36
R 52,15360
v_fma_f32 v22, v22, v78, v104
mav 4,136,36
v_fma_f32 v105, v79, v67, v103
v_fma_f32 v23, v23, v78, v105
mav 8,140,36
v_mul_f32_e64 v22, v22, |v22|
s_waitcnt lgkmcnt(2)
mav 0,144,40
R 24,16384
v_mul_f32_e64 v23, v23, |v23|
mav 4,148,40
v_cvt_pk_bf16_f32 v22, v22, v23
v_pk_max_i16 v73, v22, 0
mav 8,152,40
v_pk_max_i16 v75, v22, 0
v_permlane16_swap_b32 v72, v74
mav 0,156,44
R 28,17408
v_permlane16_swap_b32 v73, v75
mav 4,160,44
buffer_store_dwordx4 v[72:75], v84, s[36:39], s49 offen sc1
v_add_u32 v76, 1, v76
mav 8,164,44
s_waitcnt vmcnt(11)
buffer_store_dwordx2 v[76:77], v81, s[36:39], s52 offen sc1
s_waitcnt lgkmcnt(2)
mav 0,168,48
R 32,18432
mav 4,172,48
mav 8,176,48
mav 0,180,52
R 36,19456
mav 4,184,52
mav 8,188,52
s_waitcnt lgkmcnt(2)
mav 0,192,24
R 40,20480
mav 4,196,24
mav 8,200,24
mav 0,204,28
R 44,21504
mav 4,208,28
mav 8,212,28
s_waitcnt lgkmcnt(2)
mav 0,216,32
R 48,22528
mav 4,220,32
mav 8,224,32
mav 0,228,36
R 52,23552
mav 4,232,36
mav 8,236,36
s_waitcnt lgkmcnt(2)
mav 0,240,40
R 24,24576
mav 4,244,40
mav 8,248,40
mav 0,252,44
R 28,25600
mvv 4,128,44
mvv 8,132,44
s_waitcnt lgkmcnt(2)
mvv 0,136,48
R 32,26624
mvv 4,140,48
mvv 8,144,48
mvv 0,148,52
R 36,27648
mvv 4,152,52
mvv 8,156,52
s_waitcnt lgkmcnt(2)
mvv 0,160,24
R 40,28672
mvv 4,164,24
mvv 8,168,24
mvv 0,172,28
R 44,29696
mvv 4,176,28
mvv 8,180,28
s_waitcnt lgkmcnt(2)
mvv 0,184,32
R 48,30720
s_xor_b32 s50, s50, 0x10000
mvv 4,188,32
mvv 8,192,32
mvv 0,196,36
R 52,31744
mvv 4,200,36
s_add_u32 s49, s49, 346112
mvv 8,204,36
s_waitcnt vmcnt(3)
s_barrier
s_waitcnt lgkmcnt(2)
mvv 0,208,40
R 24,32768
mvv 4,212,40
s_add_u32 m0, s50, 0
ds_read_b64 v[78:79], v89 offset:0
mvv 8,216,40
mvv 0,220,44
R 28,33792
D 85,0
mvv 4,224,44
mvv 8,228,44
s_waitcnt lgkmcnt(3)
mvv 0,232,48
R 32,34816
mvv 4,236,48
D 85,1024
mvv 8,240,48
mvv 0,244,52
R 36,35840
mvv 4,248,52
mvv 8,252,52
s_waitcnt lgkmcnt(2)
v_mfma_f32_16x16x32_bf16 v[12:15], a[0:3], v[24:27], 0
R 40,36864
D 85,2048
v_mfma_f32_16x16x32_bf16 v[16:19], a[4:7], v[24:27], 0
v_fma_f32 v104, v79, v56, v92
v_fma_f32 v0, v0, v78, v104
v_mfma_f32_16x16x32_bf16 v[20:23], a[8:11], v[24:27], 0
v_fma_f32 v105, v79, v57, v93
v_fma_f32 v1, v1, v78, v105
mav 12,12,28
R 44,37888
v_mul_f32_e64 v0, v0, |v0|
mav 16,16,28
D 85,3072
v_mul_f32_e64 v1, v1, |v1|
mav 20,20,28
v_cvt_pk_bf16_f32 v0, v0, v1
s_waitcnt lgkmcnt(2)
mav 12,24,32
R 48,38912
v_pk_max_i16 v68, v0, 0
mav 16,28,32
s_add_u32 m0, s50, 4096
v_fma_f32 v104, v79, v58, v94
mav 20,32,32
v_fma_f32 v2, v2, v78, v104
v_fma_f32 v105, v79, v59, v95
mav 12,36,36
R 52,39936
D 86,0
mav 16,40,36
v_fma_f32 v3, v3, v78, v105
v_mul_f32_e64 v2, v2, |v2|
mav 20,44,36
v_mul_f32_e64 v3, v3, |v3|
s_waitcnt lgkmcnt(2)
mav 12,48,40
R 24,40960
v_cvt_pk_bf16_f32 v2, v2, v3
mav 16,52,40
D 86,1024
v_pk_max_i16 v69, v2, 0
mav 20,56,40
v_fma_f32 v104, v79, v60, v96
v_fma_f32 v4, v4, v78, v104
mav 12,60,44
R 28,41984
v_fma_f32 v105, v79, v61, v97
mav 16,64,44
v_fma_f32 v5, v5, v78, v105
v_mul_f32_e64 v4, v4, |v4|
mav 20,68,44
v_mul_f32_e64 v5, v5, |v5|
s_waitcnt lgkmcnt(2)
mav 12,72,48
R 32,43008
D 86,2048
mav 16,76,48
v_cvt_pk_bf16_f32 v4, v4, v5
v_pk_max_i16 v70, v4, 0
mav 20,80,48
v_fma_f32 v104, v79, v62, v98
v_fma_f32 v6, v6, v78, v104
mav 12,84,52
R 36,44032
v_fma_f32 v105, v79, v63, v99
mav 16,88,52
D 86,3072
v_fma_f32 v7, v7, v78, v105
mav 20,92,52
v_mul_f32_e64 v6, v6, |v6|
s_waitcnt lgkmcnt(2)
mav 12,96,24
R 40,45056
v_mul_f32_e64 v7, v7, |v7|
mav 16,100,24
v_cvt_pk_bf16_f32 v6, v6, v7
v_pk_max_i16 v71, v6, 0
mav 20,104,24
buffer_store_dwordx4 v[68:71], v83, s[36:39], s49 offen sc1
v_fma_f32 v104, v79, v64, v100
mav 12,108,28
R 44,46080
v_fma_f32 v8, v8, v78, v104
mav 16,112,28
v_fma_f32 v105, v79, v65, v101
v_fma_f32 v9, v9, v78, v105
mav 20,116,28
v_mul_f32_e64 v8, v8, |v8|
s_waitcnt lgkmcnt(2)
mav 12,120,32
R 48,47104
v_mul_f32_e64 v9, v9, |v9|
mav 16,124,32
v_cvt_pk_bf16_f32 v8, v8, v9
v_pk_max_i16 v72, v8, 0
mav 20,128,32
v_pk_max_i16 v74, v8, 0
v_fma_f32 v104, v79, v66, v102
mav 12,132,36
R 52,48128
v_fma_f32 v10, v10, v78, v104
mav 16,136,36
v_fma_f32 v105, v79, v67, v103
v_fma_f32 v11, v11, v78, v105
mav 20,140,36
v_mul_f32_e64 v10, v10, |v10|
s_waitcnt lgkmcnt(2)
mav 12,144,40
R 24,49152
v_mul_f32_e64 v11, v11, |v11|
mav 16,148,40
v_cvt_pk_bf16_f32 v10, v10, v11
v_pk_max_i16 v73, v10, 0
mav 20,152,40
v_pk_max_i16 v75, v10, 0
v_permlane16_swap_b32 v72, v74
mav 12,156,44
R 28,50176
v_permlane16_swap_b32 v73, v75
mav 16,160,44
buffer_store_dwordx4 v[72:75], v84, s[36:39], s49 offen sc1
v_add_u32 v76, 1, v76
mav 20,164,44
s_waitcnt vmcnt(11)
buffer_store_dwordx2 v[76:77], v81, s[36:39], s52 offen sc1
s_waitcnt lgkmcnt(2)
mav 12,168,48
R 32,51200
mav 16,172,48
mav 20,176,48
mav 12,180,52
R 36,52224
mav 16,184,52
mav 20,188,52
s_waitcnt lgkmcnt(2)
mav 12,192,24
R 40,53248
mav 16,196,24
mav 20,200,24
mav 12,204,28
R 44,54272
mav 16,208,28
mav 20,212,28
s_waitcnt lgkmcnt(2)
mav 12,216,32
R 48,55296
mav 16,220,32
mav 20,224,32
mav 12,228,36
R 52,56320
mav 16,232,36
mav 20,236,36
s_waitcnt lgkmcnt(2)
mav 12,240,40
R 24,57344
mav 16,244,40
mav 20,248,40
mav 12,252,44
R 28,58368
mvv 16,128,44
mvv 20,132,44
s_waitcnt lgkmcnt(2)
mvv 12,136,48
R 32,59392
mvv 16,140,48
mvv 20,144,48
mvv 12,148,52
R 36,60416
mvv 16,152,52
mvv 20,156,52
s_waitcnt lgkmcnt(2)
mvv 12,160,24
R 40,61440
mvv 16,164,24
mvv 20,168,24
mvv 12,172,28
R 44,62464
mvv 16,176,28
mvv 20,180,28
s_waitcnt lgkmcnt(2)
mvv 12,184,32
R 48,63488
mvv 16,188,32
mvv 20,192,32
mvv 12,196,36
R 52,64512
v_xor_b32 v80, 0x10000, v80
mvv 16,200,36
s_add_u32 s49, s49, 346112
mvv 20,204,36
s_waitcnt vmcnt(3)
s_barrier
s_waitcnt lgkmcnt(2)
mvv 12,208,40
R 24,0
mvv 16,212,40
s_add_u32 m0, s50, 32768
ds_read_b64 v[78:79], v89 offset:128
mvv 20,216,40
mvv 12,220,44
R 28,1024
D 85,0
mvv 16,224,44
mvv 20,228,44
s_waitcnt lgkmcnt(3)
mvv 12,232,48
R 32,2048
mvv 16,236,48
D 85,1024
mvv 20,240,48
mvv 12,244,52
R 36,3072
mvv 16,248,52
mvv 20,252,52
s_add_u32 s60, s60, 1
s_cbranch_scc0 LF2_1
s_waitcnt lgkmcnt(2)
v_mfma_f32_16x16x32_bf16 v[0:3], a[0:3], v[24:27], 0
R 40,4096
D 85,2048
v_mfma_f32_16x16x32_bf16 v[4:7], a[4:7], v[24:27], 0
v_fma_f32 v104, v79, v56, v92
v_fma_f32 v12, v12, v78, v104
v_mfma_f32_16x16x32_bf16 v[8:11], a[8:11], v[24:27], 0
v_fma_f32 v105, v79, v57, v93
v_fma_f32 v13, v13, v78, v105
mav 0,12,28
R 44,5120
v_mul_f32_e64 v12, v12, |v12|
mav 4,16,28
D 85,3072
v_mul_f32_e64 v13, v13, |v13|
mav 8,20,28
v_cvt_pk_bf16_f32 v12, v12, v13
s_waitcnt lgkmcnt(2)
mav 0,24,32
R 48,6144
v_pk_max_i16 v68, v12, 0
mav 4,28,32
s_add_u32 m0, s50, 36864
v_fma_f32 v104, v79, v58, v94
mav 8,32,32
v_fma_f32 v14, v14, v78, v104
v_fma_f32 v105, v79, v59, v95
mav 0,36,36
R 52,7168
D 86,0
mav 4,40,36
v_fma_f32 v15, v15, v78, v105
v_mul_f32_e64 v14, v14, |v14|
mav 8,44,36
v_mul_f32_e64 v15, v15, |v15|
s_waitcnt lgkmcnt(2)
mav 0,48,40
R 24,8192
v_cvt_pk_bf16_f32 v14, v14, v15
mav 4,52,40
D 86,1024
v_pk_max_i16 v69, v14, 0
mav 8,56,40
v_fma_f32 v104, v79, v60, v96
v_fma_f32 v16, v16, v78, v104
mav 0,60,44
R 28,9216
v_fma_f32 v105, v79, v61, v97
mav 4,64,44
v_fma_f32 v17, v17, v78, v105
v_mul_f32_e64 v16, v16, |v16|
mav 8,68,44
v_mul_f32_e64 v17, v17, |v17|
s_waitcnt lgkmcnt(2)
mav 0,72,48
R 32,10240
D 86,2048
mav 4,76,48
v_cvt_pk_bf16_f32 v16, v16, v17
v_pk_max_i16 v70, v16, 0
mav 8,80,48
v_fma_f32 v104, v79, v62, v98
v_fma_f32 v18, v18, v78, v104
mav 0,84,52
R 36,11264
v_fma_f32 v105, v79, v63, v99
mav 4,88,52
D 86,3072
v_fma_f32 v19, v19, v78, v105
mav 8,92,52
v_mul_f32_e64 v18, v18, |v18|
s_waitcnt lgkmcnt(2)
mav 0,96,24
R 40,12288
v_mul_f32_e64 v19, v19, |v19|
mav 4,100,24
v_cvt_pk_bf16_f32 v18, v18, v19
v_pk_max_i16 v71, v18, 0
mav 8,104,24
buffer_store_dwordx4 v[68:71], v83, s[36:39], s49 offen sc1
v_fma_f32 v104, v79, v64, v100
mav 0,108,28
R 44,13312
v_fma_f32 v20, v20, v78, v104
mav 4,112,28
v_fma_f32 v105, v79, v65, v101
v_fma_f32 v21, v21, v78, v105
mav 8,116,28
v_mul_f32_e64 v20, v20, |v20|
s_waitcnt lgkmcnt(2)
mav 0,120,32
R 48,14336
v_mul_f32_e64 v21, v21, |v21|
mav 4,124,32
v_cvt_pk_bf16_f32 v20, v20, v21
v_pk_max_i16 v72, v20, 0
mav 8,128,32
v_pk_max_i16 v74, v20, 0
v_fma_f32 v104, v79, v66, v102
mav 0,132,36
R 52,15360
v_fma_f32 v22, v22, v78, v104
mav 4,136,36
v_fma_f32 v105, v79, v67, v103
v_fma_f32 v23, v23, v78, v105
mav 8,140,36
v_mul_f32_e64 v22, v22, |v22|
s_waitcnt lgkmcnt(2)
mav 0,144,40
R 24,16384
v_mul_f32_e64 v23, v23, |v23|
mav 4,148,40
v_cvt_pk_bf16_f32 v22, v22, v23
v_pk_max_i16 v73, v22, 0
mav 8,152,40
v_pk_max_i16 v75, v22, 0
v_permlane16_swap_b32 v72, v74
mav 0,156,44
R 28,17408
v_permlane16_swap_b32 v73, v75
mav 4,160,44
buffer_store_dwordx4 v[72:75], v84, s[36:39], s49 offen sc1
v_add_u32 v76, 1, v76
mav 8,164,44
s_waitcnt vmcnt(11)
buffer_store_dwordx2 v[76:77], v81, s[36:39], s52 offen sc1
s_waitcnt lgkmcnt(2)
mav 0,168,48
R 32,18432
mav 4,172,48
mav 8,176,48
mav 0,180,52
R 36,19456
mav 4,184,52
mav 8,188,52
s_waitcnt lgkmcnt(2)
mav 0,192,24
R 40,20480
mav 4,196,24
mav 8,200,24
mav 0,204,28
R 44,21504
mav 4,208,28
mav 8,212,28
s_waitcnt lgkmcnt(2)
mav 0,216,32
R 48,22528
mav 4,220,32
mav 8,224,32
mav 0,228,36
R 52,23552
mav 4,232,36
mav 8,236,36
s_waitcnt lgkmcnt(2)
mav 0,240,40
R 24,24576
mav 4,244,40
mav 8,248,40
mav 0,252,44
R 28,25600
mvv 4,128,44
mvv 8,132,44
s_waitcnt lgkmcnt(2)
mvv 0,136,48
R 32,26624
mvv 4,140,48
mvv 8,144,48
mvv 0,148,52
R 36,27648
mvv 4,152,52
mvv 8,156,52
s_waitcnt lgkmcnt(2)
mvv 0,160,24
R 40,28672
mvv 4,164,24
mvv 8,168,24
mvv 0,172,28
R 44,29696
mvv 4,176,28
mvv 8,180,28
s_waitcnt lgkmcnt(2)
mvv 0,184,32
R 48,30720
mvv 4,188,32
mvv 8,192,32
mvv 0,196,36
R 52,31744
mvv 4,200,36
s_add_u32 s49, s49, 346112
mvv 8,204,36
s_waitcnt vmcnt(3)
s_barrier
s_waitcnt lgkmcnt(2)
mvv 0,208,40
R 24,32768
mvv 4,212,40
ds_read_b64 v[78:79], v89 offset:0
mvv 8,216,40
mvv 0,220,44
R 28,33792
mvv 4,224,44
mvv 8,228,44
s_waitcnt lgkmcnt(3)
mvv 0,232,48
R 32,34816
mvv 4,236,48
mvv 8,240,48
mvv 0,244,52
R 36,35840
mvv 4,248,52
mvv 8,252,52
s_waitcnt lgkmcnt(2)
v_mfma_f32_16x16x32_bf16 v[12:15], a[0:3], v[24:27], 0
R 40,36864
v_fma_f32 v104, v79, v56, v92
v_mfma_f32_16x16x32_bf16 v[16:19], a[4:7], v[24:27], 0
v_fma_f32 v0, v0, v78, v104
v_fma_f32 v105, v79, v57, v93
v_mfma_f32_16x16x32_bf16 v[20:23], a[8:11], v[24:27], 0
v_fma_f32 v1, v1, v78, v105
v_mul_f32_e64 v0, v0, |v0|
mav 12,12,28
R 44,37888
v_mul_f32_e64 v1, v1, |v1|
mav 16,16,28
v_cvt_pk_bf16_f32 v0, v0, v1
v_pk_max_i16 v68, v0, 0
mav 20,20,28
v_fma_f32 v104, v79, v58, v94
s_waitcnt lgkmcnt(2)
mav 12,24,32
R 48,38912
v_fma_f32 v2, v2, v78, v104
mav 16,28,32
v_fma_f32 v105, v79, v59, v95
v_fma_f32 v3, v3, v78, v105
mav 20,32,32
v_mul_f32_e64 v2, v2, |v2|
v_mul_f32_e64 v3, v3, |v3|
mav 12,36,36
R 52,39936
v_cvt_pk_bf16_f32 v2, v2, v3
mav 16,40,36
v_pk_max_i16 v69, v2, 0
v_fma_f32 v104, v79, v60, v96
mav 20,44,36
v_fma_f32 v4, v4, v78, v104
s_waitcnt lgkmcnt(2)
mav 12,48,40
R 24,40960
v_fma_f32 v105, v79, v61, v97
mav 16,52,40
v_fma_f32 v5, v5, v78, v105
v_mul_f32_e64 v4, v4, |v4|
mav 20,56,40
v_mul_f32_e64 v5, v5, |v5|
v_cvt_pk_bf16_f32 v4, v4, v5
mav 12,60,44
R 28,41984
v_pk_max_i16 v70, v4, 0
mav 16,64,44
v_fma_f32 v104, v79, v62, v98
v_fma_f32 v6, v6, v78, v104
mav 20,68,44
v_fma_f32 v105, v79, v63, v99
s_waitcnt lgkmcnt(2)
mav 12,72,48
R 32,43008
v_fma_f32 v7, v7, v78, v105
mav 16,76,48
v_mul_f32_e64 v6, v6, |v6|
v_mul_f32_e64 v7, v7, |v7|
mav 20,80,48
v_cvt_pk_bf16_f32 v6, v6, v7
v_pk_max_i16 v71, v6, 0
mav 12,84,52
R 36,44032
buffer_store_dwordx4 v[68:71], v83, s[36:39], s49 offen sc1
mav 16,88,52
v_fma_f32 v104, v79, v64, v100
v_fma_f32 v8, v8, v78, v104
mav 20,92,52
v_fma_f32 v105, v79, v65, v101
s_waitcnt lgkmcnt(2)
mav 12,96,24
R 40,45056
v_fma_f32 v9, v9, v78, v105
mav 16,100,24
v_mul_f32_e64 v8, v8, |v8|
v_mul_f32_e64 v9, v9, |v9|
mav 20,104,24
v_cvt_pk_bf16_f32 v8, v8, v9
v_pk_max_i16 v72, v8, 0
mav 12,108,28
R 44,46080
v_pk_max_i16 v74, v8, 0
mav 16,112,28
v_fma_f32 v104, v79, v66, v102
v_fma_f32 v10, v10, v78, v104
mav 20,116,28
v_fma_f32 v105, v79, v67, v103
s_waitcnt lgkmcnt(2)
mav 12,120,32
R 48,47104
v_fma_f32 v11, v11, v78, v105
mav 16,124,32
v_mul_f32_e64 v10, v10, |v10|
v_mul_f32_e64 v11, v11, |v11|
mav 20,128,32
v_cvt_pk_bf16_f32 v10, v10, v11
v_pk_max_i16 v73, v10, 0
mav 12,132,36
R 52,48128
v_pk_max_i16 v75, v10, 0
mav 16,136,36
v_permlane16_swap_b32 v72, v74
v_permlane16_swap_b32 v73, v75
mav 20,140,36
buffer_store_dwordx4 v[72:75], v84, s[36:39], s49 offen sc1
s_waitcnt lgkmcnt(2)
mav 12,144,40
R 24,49152
v_add_u32 v76, 1, v76
mav 16,148,40
s_waitcnt vmcnt(3)
buffer_store_dwordx2 v[76:77], v81, s[36:39], s52 offen sc1
mav 20,152,40
mav 12,156,44
R 28,50176
mav 16,160,44
mav 20,164,44
s_waitcnt lgkmcnt(2)
mav 12,168,48
R 32,51200
mav 16,172,48
mav 20,176,48
mav 12,180,52
R 36,52224
mav 16,184,52
mav 20,188,52
s_waitcnt lgkmcnt(2)
mav 12,192,24
R 40,53248
mav 16,196,24
mav 20,200,24
mav 12,204,28
R 44,54272
mav 16,208,28
mav 20,212,28
s_waitcnt lgkmcnt(2)
mav 12,216,32
R 48,55296
mav 16,220,32
mav 20,224,32
mav 12,228,36
R 52,56320
mav 16,232,36
mav 20,236,36
s_waitcnt lgkmcnt(2)
mav 12,240,40
R 24,57344
mav 16,244,40
mav 20,248,40
mav 12,252,44
R 28,58368
mvv 16,128,44
mvv 20,132,44
s_waitcnt lgkmcnt(2)
mvv 12,136,48
R 32,59392
mvv 16,140,48
mvv 20,144,48
mvv 12,148,52
R 36,60416
mvv 16,152,52
mvv 20,156,52
s_waitcnt lgkmcnt(2)
mvv 12,160,24
R 40,61440
mvv 16,164,24
mvv 20,168,24
mvv 12,172,28
R 44,62464
mvv 16,176,28
mvv 20,180,28
s_waitcnt lgkmcnt(2)
mvv 12,184,32
R 48,63488
mvv 16,188,32
mvv 20,192,32
mvv 12,196,36
R 52,64512
mvv 16,200,36
s_add_u32 s49, s49, 346112
mvv 20,204,36
s_barrier
s_waitcnt lgkmcnt(2)
mvv 12,208,40
mvv 16,212,40
ds_read_b64 v[78:79], v89 offset:128
mvv 20,216,40
mvv 12,220,44
mvv 16,224,44
mvv 20,228,44
s_waitcnt lgkmcnt(1)
mvv 12,232,48
mvv 16,236,48
mvv 20,240,48
mvv 12,244,52
mvv 16,248,52
mvv 20,252,52
s_waitcnt lgkmcnt(0)
v_fma_f32 v104, v79, v56, v92
s_nop 3
v_fma_f32 v12, v12, v78, v104
v_fma_f32 v105, v79, v57, v93
v_fma_f32 v13, v13, v78, v105
v_mul_f32_e64 v12, v12, |v12|
v_mul_f32_e64 v13, v13, |v13|
v_cvt_pk_bf16_f32 v12, v12, v13
v_pk_max_i16 v68, v12, 0
v_fma_f32 v104, v79, v58, v94
v_fma_f32 v14, v14, v78, v104
v_fma_f32 v105, v79, v59, v95
v_fma_f32 v15, v15, v78, v105
v_mul_f32_e64 v14, v14, |v14|
v_mul_f32_e64 v15, v15, |v15|
v_cvt_pk_bf16_f32 v14, v14, v15
v_pk_max_i16 v69, v14, 0
v_fma_f32 v104, v79, v60, v96
v_fma_f32 v16, v16, v78, v104
v_fma_f32 v105, v79, v61, v97
v_fma_f32 v17, v17, v78, v105
v_mul_f32_e64 v16, v16, |v16|
v_mul_f32_e64 v17, v17, |v17|
v_cvt_pk_bf16_f32 v16, v16, v17
v_pk_max_i16 v70, v16, 0
v_fma_f32 v104, v79, v62, v98
v_fma_f32 v18, v18, v78, v104
v_fma_f32 v105, v79, v63, v99
v_fma_f32 v19, v19, v78, v105
v_mul_f32_e64 v18, v18, |v18|
v_mul_f32_e64 v19, v19, |v19|
v_cvt_pk_bf16_f32 v18, v18, v19
v_pk_max_i16 v71, v18, 0
buffer_store_dwordx4 v[68:71], v83, s[36:39], s49 offen sc1
v_fma_f32 v104, v79, v64, v100
v_fma_f32 v20, v20, v78, v104
v_fma_f32 v105, v79, v65, v101
v_fma_f32 v21, v21, v78, v105
v_mul_f32_e64 v20, v20, |v20|
v_mul_f32_e64 v21, v21, |v21|
v_cvt_pk_bf16_f32 v20, v20, v21
v_pk_max_i16 v72, v20, 0
v_pk_max_i16 v74, v20, 0
v_fma_f32 v104, v79, v66, v102
v_fma_f32 v22, v22, v78, v104
v_fma_f32 v105, v79, v67, v103
v_fma_f32 v23, v23, v78, v105
v_mul_f32_e64 v22, v22, |v22|
v_mul_f32_e64 v23, v23, |v23|
v_cvt_pk_bf16_f32 v22, v22, v23
v_pk_max_i16 v73, v22, 0
v_pk_max_i16 v75, v22, 0
v_permlane16_swap_b32 v72, v74
s_nop 0
v_permlane16_swap_b32 v73, v75
buffer_store_dwordx4 v[72:75], v84, s[36:39], s49 offen sc1
v_add_u32 v76, 1, v76
s_waitcnt vmcnt(3)
buffer_store_dwordx2 v[76:77], v81, s[36:39], s52 offen sc1
v_add_u32 v76, 37, v76
s_waitcnt vmcnt(1)
buffer_store_dwordx2 v[76:77], v81, s[36:39], s52 offen sc1
s_endpgm
Lr4:
s_cmp_eq_u32 s60, 4
s_cbranch_scc0 Lr5
buffer_load_dword v73, off, s[36:39], 0 sc1
s_waitcnt vmcnt(0)
v_mov_b32 v72, 0x100
v_add_u32 v73, 0x9e3779b9, v73
buffer_store_dwordx2 v[72:73], v81, s[36:39], s52 offen sc1
v_add_u32 v86, v86, v72
s_sleep 32
s_sleep 32
s_sleep 32
E 0,65536
E 1,69632
E 2,73728
E 3,77824
s_add_u32 s51, s51, 65536
E 0,81920
E 1,86016
E 2,90112
E 3,94208
s_add_u32 s51, s51, 65536
E 0,98304
E 1,102400
E 2,106496
E 3,110592
s_add_u32 s51, s51, 65536
E 0,114688
E 1,118784
E 2,122880
E 3,126976
s_add_u32 s51, s51, 65536
s_waitcnt vmcnt(8)
s_barrier
v_add_u32 v4, 65536, v4
v_add_u32 v5, 65536, v5
v_add_u32 v6, 65536, v6
v_add_u32 v7, 65536, v7
Ta 0,4,0
Ta 4,5,0
Ta 8,6,0
Ta 12,7,0
Ta 16,4,16384
Ta 20,5,16384
Ta 24,6,16384
Ta 28,7,16384
E 0,131072
E 1,135168
E 2,139264
E 3,143360
s_add_u32 s51, s51, 65536
E 0,147456
E 1,151552
E 2,155648
E 3,159744
s_add_u32 s51, s51, 65536
s_cmp_ge_u32 s51, s57
s_cselect_b32 s60, 1572864, 0
s_sub_u32 s51, s51, s60
s_waitcnt lgkmcnt(0)
s_waitcnt vmcnt(8)
s_barrier
E 0,65536
Ta 32,4,32768
Ta 36,5,32768
E 1,69632
Ta 40,6,32768
Ta 44,7,32768
E 2,73728
Ta 48,4,49152
E 3,77824
s_add_u32 s51, s51, 65536
Ta 52,5,49152
Ta 56,6,49152
E 0,81920
Ta 60,7,49152
E 1,86016
E 2,90112
E 3,94208
s_add_u32 s51, s51, 65536
s_waitcnt lgkmcnt(0)
s_waitcnt vmcnt(8)
s_barrier
v_add_u32 v4, 65536, v4
v_add_u32 v5, 65536, v5
v_add_u32 v6, 65536, v6
v_add_u32 v7, 65536, v7
E 0,98304
Ta 64,4,0
Ta 68,5,0
E 1,102400
Ta 72,6,0
Ta 76,7,0
E 2,106496
Ta 80,4,16384
E 3,110592
s_add_u32 s51, s51, 65536
Ta 84,5,16384
Ta 88,6,16384
E 0,114688
Ta 92,7,16384
E 1,118784
E 2,122880
E 3,126976
s_add_u32 s51, s51, 65536
s_waitcnt lgkmcnt(0)
s_waitcnt vmcnt(8)
s_barrier
v_add_u32 v4, -65536, v4
v_add_u32 v5, -65536, v5
v_add_u32 v6, -65536, v6
v_add_u32 v7, -65536, v7
E 0,131072
Ta 96,4,0
Ta 100,5,0
E 1,135168
Ta 104,6,0
Ta 108,7,0
E 2,139264
Ta 112,4,16384
E 3,143360
s_add_u32 s51, s51, 65536
Ta 116,5,16384
Ta 120,6,16384
E 0,147456
Ta 124,7,16384
E 1,151552
E 2,155648
E 3,159744
s_add_u32 s51, s51, 65536
s_cmp_ge_u32 s51, s57
s_cselect_b32 s60, 1572864, 0
s_sub_u32 s51, s51, s60
s_waitcnt lgkmcnt(0)
s_waitcnt vmcnt(8)
s_barrier
E 0,65536
Ta 128,4,32768
Ta 132,5,32768
E 1,69632
Ta 136,6,32768
Ta 140,7,32768
E 2,73728
Ta 144,4,49152
E 3,77824
s_add_u32 s51, s51, 65536
Ta 148,5,49152
Ta 152,6,49152
E 0,81920
Ta 156,7,49152
E 1,86016
E 2,90112
E 3,94208
s_add_u32 s51, s51, 65536
s_waitcnt lgkmcnt(0)
s_waitcnt vmcnt(8)
s_barrier
v_add_u32 v4, 65536, v4
v_add_u32 v5, 65536, v5
v_add_u32 v6, 65536, v6
v_add_u32 v7, 65536, v7
E 0,98304
Ta 160,4,0
Ta 164,5,0
E 1,102400
Ta 168,6,0
Ta 172,7,0
E 2,106496
Ta 176,4,16384
E 3,110592
s_add_u32 s51, s51, 65536
Ta 180,5,16384
Ta 184,6,16384
E 0,114688
Ta 188,7,16384
E 1,118784
E 2,122880
E 3,126976
s_add_u32 s51, s51, 65536
s_waitcnt lgkmcnt(0)
s_waitcnt vmcnt(8)
s_barrier
v_add_u32 v4, -65536, v4
v_add_u32 v5, -65536, v5
v_add_u32 v6, -65536, v6
v_add_u32 v7, -65536, v7
E 0,131072
Ta 192,4,0
Ta 196,5,0
E 1,135168
Ta 200,6,0
Ta 204,7,0
E 2,139264
Ta 208,4,16384
E 3,143360
s_add_u32 s51, s51, 65536
Ta 212,5,16384
Ta 216,6,16384
E 0,147456
Ta 220,7,16384
E 1,151552
E 2,155648
E 3,159744
s_add_u32 s51, s51, 65536
s_cmp_ge_u32 s51, s57
s_cselect_b32 s60, 1572864, 0
s_sub_u32 s51, s51, s60
s_waitcnt lgkmcnt(0)
s_waitcnt vmcnt(8)
s_barrier
E 0,65536
Ta 224,4,32768
Ta 228,5,32768
E 1,69632
Ta 232,6,32768
Ta 236,7,32768
E 2,73728
Ta 240,4,49152
E 3,77824
s_add_u32 s51, s51, 65536
Ta 244,5,49152
Ta 248,6,49152
E 0,81920
Ta 252,7,49152
E 1,86016
E 2,90112
E 3,94208
s_add_u32 s51, s51, 65536
s_waitcnt lgkmcnt(0)
s_waitcnt vmcnt(8)
s_barrier
v_add_u32 v4, 65536, v4
v_add_u32 v5, 65536, v5
v_add_u32 v6, 65536, v6
v_add_u32 v7, 65536, v7
E 0,98304
Tv 128,4,0
Tv 132,5,0
E 1,102400
Tv 136,6,0
Tv 140,7,0
E 2,106496
Tv 144,4,16384
E 3,110592
s_add_u32 s51, s51, 65536
Tv 148,5,16384
Tv 152,6,16384
E 0,114688
Tv 156,7,16384
E 1,118784
E 2,122880
E 3,126976
s_add_u32 s51, s51, 65536
s_waitcnt lgkmcnt(0)
s_waitcnt vmcnt(8)
s_barrier
v_add_u32 v4, -65536, v4
v_add_u32 v5, -65536, v5
v_add_u32 v6, -65536, v6
v_add_u32 v7, -65536, v7
E 0,131072
Tv 160,4,0
Tv 164,5,0
E 1,135168
Tv 168,6,0
Tv 172,7,0
E 2,139264
Tv 176,4,16384
E 3,143360
s_add_u32 s51, s51, 65536
Tv 180,5,16384
Tv 184,6,16384
E 0,147456
Tv 188,7,16384
E 1,151552
E 2,155648
E 3,159744
s_add_u32 s51, s51, 65536
s_cmp_ge_u32 s51, s57
s_cselect_b32 s60, 1572864, 0
s_sub_u32 s51, s51, s60
s_waitcnt lgkmcnt(0)
s_waitcnt vmcnt(8)
s_barrier
Tv 192,4,32768
Tv 196,5,32768
Tv 200,6,32768
Tv 204,7,32768
Tv 208,4,49152
Tv 212,5,49152
Tv 216,6,49152
Tv 220,7,49152
s_waitcnt lgkmcnt(0)
s_waitcnt vmcnt(0)
s_barrier
v_add_u32 v4, 65536, v4
v_add_u32 v5, 65536, v5
v_add_u32 v6, 65536, v6
v_add_u32 v7, 65536, v7
Tv 224,4,0
Tv 228,5,0
Tv 232,6,0
Tv 236,7,0
Tv 240,4,16384
Tv 244,5,16384
Tv 248,6,16384
Tv 252,7,16384
s_waitcnt lgkmcnt(0)
s_waitcnt vmcnt(0)
s_barrier
s_nop 7
s_and_b32 s60, s48, 3
s_cmp_eq_u32 s60, 1
s_cbranch_scc1 LvG1
LvG0:
buffer_load_dwordx2 v[74:75], v85, s[36:39], 0 offen sc1
s_waitcnt vmcnt(0)
ds_write_b32 v82, v74
v_sub_u32 v74, v74, v86
v_xor_b32 v75, v75, v73
v_cmp_lt_u64 vcc, s[62:63], v[74:75]
s_waitcnt lgkmcnt(0)
s_cbranch_vccnz CG0_1
BG0_1:
s_add_u32 s49, s49, 346112
s_barrier
s_add_u32 m0, s50, 0
s_nop 0
D 83,0
D 83,1024
D 83,2048
D 83,3072
s_add_u32 m0, s50, 4096
s_nop 0
D 84,0
D 84,1024
s_mov_b32 m0, 142336
s_nop 0
buffer_load_dword v85, s[36:39], 0 offen sc1 lds
v_add_u32 v86, 1, v86
buffer_load_dword v85, s[36:39], 0 offen sc1 lds
buffer_load_dword v85, s[36:39], 0 offen sc1 lds
ds_read_b32 v74, v82 offset:0
s_waitcnt lgkmcnt(0)
v_cmp_gt_i32 vcc, v86, v74
s_waitcnt vmcnt(3)
s_cbranch_vccnz CG0_2
BG0_2:
s_add_u32 s49, s49, 346112
s_barrier
s_add_u32 m0, s50, 32768
R 32,0
D 83,0
R 36,1024
D 83,1024
R 40,2048
D 83,2048
s_waitcnt lgkmcnt(1)
v_mfma_f32_16x16x32_bf16 v[0:3], a[0:3], v[32:35], 0
R 44,3072
v_mfma_f32_16x16x32_bf16 v[4:7], a[4:7], v[32:35], 0
v_mfma_f32_16x16x32_bf16 v[8:11], a[8:11], v[32:35], 0
D 83,3072
v_mfma_f32_16x16x32_bf16 v[12:15], a[12:15], v[32:35], 0
mav 0,16,36
R 48,4096
mav 4,20,36
s_add_u32 m0, s50, 36864
mav 8,24,36
mav 12,28,36
D 84,0
s_waitcnt lgkmcnt(1)
mav 0,32,40
R 52,5120
mav 4,36,40
mav 8,40,40
mav 12,44,40
D 84,1024
mav 0,48,44
R 56,6144
mav 4,52,44
mav 8,56,44
mav 12,60,44
s_waitcnt lgkmcnt(1)
mav 0,64,48
R 60,7168
mav 4,68,48
mav 8,72,48
mav 12,76,48
mav 0,80,52
R 32,8192
mav 4,84,52
mav 8,88,52
mav 12,92,52
s_waitcnt lgkmcnt(1)
mav 0,96,56
R 36,9216
mav 4,100,56
mav 8,104,56
mav 12,108,56
mav 0,112,60
R 40,10240
mav 4,116,60
mav 8,120,60
s_mov_b32 m0, 142336
mav 12,124,60
s_waitcnt lgkmcnt(1)
mav 0,128,32
R 44,11264
buffer_load_dword v85, s[36:39], 0 offen sc1 lds
mav 4,132,32
mav 8,136,32
mav 12,140,32
mav 0,144,36
R 48,12288
mav 4,148,36
mav 8,152,36
mav 12,156,36
s_waitcnt lgkmcnt(1)
mav 0,160,40
R 52,13312
v_add_u32 v86, 1, v86
mav 4,164,40
mav 8,168,40
mav 12,172,40
mav 0,176,44
R 56,14336
buffer_load_dword v85, s[36:39], 0 offen sc1 lds
mav 4,180,44
mav 8,184,44
mav 12,188,44
s_waitcnt lgkmcnt(1)
mav 0,192,48
R 60,15360
mav 4,196,48
mav 8,200,48
mav 12,204,48
mav 0,208,52
R 32,16384
mav 4,212,52
mav 8,216,52
mav 12,220,52
s_waitcnt lgkmcnt(1)
mav 0,224,56
R 36,17408
buffer_load_dword v85, s[36:39], 0 offen sc1 lds
mav 4,228,56
mav 8,232,56
mav 12,236,56
mav 0,240,60
R 40,18432
mav 4,244,60
mav 8,248,60
mav 12,252,60
s_waitcnt lgkmcnt(1)
mvv 0,128,32
R 44,19456
mvv 4,132,32
mvv 8,136,32
mvv 12,140,32
mvv 0,144,36
R 48,20480
mvv 4,148,36
mvv 8,152,36
mvv 12,156,36
s_waitcnt lgkmcnt(1)
mvv 0,160,40
R 52,21504
mvv 4,164,40
ds_read_b32 v74, v82 offset:0
mvv 8,168,40
mvv 12,172,40
mvv 0,176,44
R 56,22528
mvv 4,180,44
mvv 8,184,44
s_xor_b32 s50, s50, 0x10000
mvv 12,188,44
s_waitcnt lgkmcnt(1)
mvv 0,192,48
R 60,23552
mvv 4,196,48
v_cmp_gt_i32 vcc, v86, v74
mvv 8,200,48
s_waitcnt vmcnt(3)
s_cbranch_vccnz CG0_3
BG0_3:
mvv 12,204,48
s_barrier
s_add_u32 s49, s49, 346112
mvv 0,208,52
R 32,32768
s_add_u32 m0, s50, 0
mvv 4,212,52
D 83,0
mvv 8,216,52
mvv 12,220,52
s_waitcnt lgkmcnt(1)
mvv 0,224,56
R 36,33792
mvv 4,228,56
D 83,1024
mvv 8,232,56
mvv 12,236,56
mvv 0,240,60
R 40,34816
mvv 4,244,60
mvv 8,248,60
D 83,2048
mvv 12,252,60
s_waitcnt lgkmcnt(1)
v_mfma_f32_16x16x32_bf16 v[16:19], a[0:3], v[32:35], 0
R 44,35840
v_cvt_pk_bf16_f32 v64, v0, v1
v_mfma_f32_16x16x32_bf16 v[20:23], a[4:7], v[32:35], 0
v_cvt_pk_bf16_f32 v65, v2, v3
v_cvt_pk_bf16_f32 v66, v4, v5
v_mfma_f32_16x16x32_bf16 v[24:27], a[8:11], v[32:35], 0
D 83,3072
v_cvt_pk_bf16_f32 v67, v6, v7
v_mfma_f32_16x16x32_bf16 v[28:31], a[12:15], v[32:35], 0
v_cvt_pk_bf16_f32 v68, v8, v9
v_cvt_pk_bf16_f32 v69, v10, v11
mav 16,16,36
R 48,36864
v_cvt_pk_bf16_f32 v70, v12, v13
mav 20,20,36
s_add_u32 m0, s50, 4096
v_cvt_pk_bf16_f32 v71, v14, v15
mav 24,24,36
buffer_store_dwordx4 v[64:67], v87, s[36:39], s49 offen sc1
mav 28,28,36
s_waitcnt lgkmcnt(1)
mav 16,32,40
R 52,37888
mav 20,36,40
mav 24,40,40
buffer_store_dwordx4 v[68:71], v87, s[36:39], s49 offen offset:1024 sc1
mav 28,44,40
mav 16,48,44
R 56,38912
mav 20,52,44
mav 24,56,44
D 84,0
mav 28,60,44
s_waitcnt lgkmcnt(1)
mav 16,64,48
R 60,39936
mav 20,68,48
mav 24,72,48
D 84,1024
mav 28,76,48
mav 16,80,52
R 32,40960
mav 20,84,52
mav 24,88,52
mav 28,92,52
s_waitcnt lgkmcnt(1)
mav 16,96,56
R 36,41984
mav 20,100,56
mav 24,104,56
mav 28,108,56
mav 16,112,60
R 40,43008
mav 20,116,60
mav 24,120,60
s_mov_b32 m0, 142336
mav 28,124,60
s_waitcnt lgkmcnt(1)
mav 16,128,32
R 44,44032
buffer_load_dword v85, s[36:39], 0 offen sc1 lds
mav 20,132,32
mav 24,136,32
mav 28,140,32
mav 16,144,36
R 48,45056
mav 20,148,36
mav 24,152,36
mav 28,156,36
s_waitcnt lgkmcnt(1)
mav 16,160,40
R 52,46080
v_add_u32 v86, 1, v86
mav 20,164,40
mav 24,168,40
mav 28,172,40
mav 16,176,44
R 56,47104
buffer_load_dword v85, s[36:39], 0 offen sc1 lds
mav 20,180,44
)"
R"(
mav 24,184,44
mav 28,188,44
s_waitcnt lgkmcnt(1)
mav 16,192,48
R 60,48128
mav 20,196,48
mav 24,200,48
mav 28,204,48
mav 16,208,52
R 32,49152
mav 20,212,52
mav 24,216,52
v_add_u32 v72, 1, v72
mav 28,220,52
s_waitcnt lgkmcnt(1)
mav 16,224,56
R 36,50176
buffer_load_dword v85, s[36:39], 0 offen sc1 lds
mav 20,228,56
mav 24,232,56
mav 28,236,56
mav 16,240,60
R 40,51200
mav 20,244,60
s_waitcnt vmcnt(5)
buffer_store_dwordx2 v[72:73], v81, s[36:39], s52 offen sc1
mav 24,248,60
mav 28,252,60
s_waitcnt lgkmcnt(1)
mvv 16,128,32
R 44,52224
mvv 20,132,32
mvv 24,136,32
mvv 28,140,32
mvv 16,144,36
R 48,53248
mvv 20,148,36
mvv 24,152,36
mvv 28,156,36
s_waitcnt lgkmcnt(1)
mvv 16,160,40
R 52,54272
mvv 20,164,40
ds_read_b32 v74, v82 offset:0
mvv 24,168,40
mvv 28,172,40
mvv 16,176,44
R 56,55296
mvv 20,180,44
mvv 24,184,44
mvv 28,188,44
s_waitcnt lgkmcnt(1)
mvv 16,192,48
R 60,56320
mvv 20,196,48
v_xor_b32 v80, 0x10000, v80
v_cmp_gt_i32 vcc, v86, v74
mvv 24,200,48
s_waitcnt vmcnt(4)
s_cbranch_vccnz CG0_4
BG0_4:
mvv 28,204,48
s_barrier
s_add_u32 s49, s49, 346112
mvv 16,208,52
R 32,0
s_add_u32 m0, s50, 32768
mvv 20,212,52
D 83,0
mvv 24,216,52
mvv 28,220,52
s_waitcnt lgkmcnt(1)
mvv 16,224,56
R 36,1024
mvv 20,228,56
D 83,1024
mvv 24,232,56
mvv 28,236,56
mvv 16,240,60
R 40,2048
mvv 20,244,60
mvv 24,248,60
D 83,2048
mvv 28,252,60
s_mov_b32 s60, -30
.p2align 6
LG0_5:
s_waitcnt lgkmcnt(1)
v_mfma_f32_16x16x32_bf16 v[0:3], a[0:3], v[32:35], 0
R 44,3072
v_cvt_pk_bf16_f32 v64, v16, v17
v_mfma_f32_16x16x32_bf16 v[4:7], a[4:7], v[32:35], 0
v_cvt_pk_bf16_f32 v65, v18, v19
v_cvt_pk_bf16_f32 v66, v20, v21
v_mfma_f32_16x16x32_bf16 v[8:11], a[8:11], v[32:35], 0
D 83,3072
v_cvt_pk_bf16_f32 v67, v22, v23
v_mfma_f32_16x16x32_bf16 v[12:15], a[12:15], v[32:35], 0
v_cvt_pk_bf16_f32 v68, v24, v25
v_cvt_pk_bf16_f32 v69, v26, v27
mav 0,16,36
R 48,4096
v_cvt_pk_bf16_f32 v70, v28, v29
mav 4,20,36
s_add_u32 m0, s50, 36864
v_cvt_pk_bf16_f32 v71, v30, v31
mav 8,24,36
buffer_store_dwordx4 v[64:67], v87, s[36:39], s49 offen sc1
mav 12,28,36
s_waitcnt lgkmcnt(1)
mav 0,32,40
R 52,5120
mav 4,36,40
mav 8,40,40
buffer_store_dwordx4 v[68:71], v87, s[36:39], s49 offen offset:1024 sc1
mav 12,44,40
mav 0,48,44
R 56,6144
mav 4,52,44
mav 8,56,44
D 84,0
mav 12,60,44
s_waitcnt lgkmcnt(1)
mav 0,64,48
R 60,7168
mav 4,68,48
mav 8,72,48
D 84,1024
mav 12,76,48
mav 0,80,52
R 32,8192
mav 4,84,52
mav 8,88,52
mav 12,92,52
s_waitcnt lgkmcnt(1)
mav 0,96,56
R 36,9216
mav 4,100,56
mav 8,104,56
mav 12,108,56
mav 0,112,60
R 40,10240
mav 4,116,60
mav 8,120,60
s_mov_b32 m0, 142336
mav 12,124,60
s_waitcnt lgkmcnt(1)
mav 0,128,32
R 44,11264
buffer_load_dword v85, s[36:39], 0 offen sc1 lds
mav 4,132,32
mav 8,136,32
mav 12,140,32
mav 0,144,36
R 48,12288
mav 4,148,36
mav 8,152,36
mav 12,156,36
s_waitcnt lgkmcnt(1)
mav 0,160,40
R 52,13312
v_add_u32 v86, 1, v86
mav 4,164,40
mav 8,168,40
mav 12,172,40
mav 0,176,44
R 56,14336
buffer_load_dword v85, s[36:39], 0 offen sc1 lds
mav 4,180,44
mav 8,184,44
mav 12,188,44
s_waitcnt lgkmcnt(1)
mav 0,192,48
R 60,15360
mav 4,196,48
mav 8,200,48
mav 12,204,48
mav 0,208,52
R 32,16384
mav 4,212,52
mav 8,216,52
v_add_u32 v72, 1, v72
mav 12,220,52
s_waitcnt lgkmcnt(1)
mav 0,224,56
R 36,17408
buffer_load_dword v85, s[36:39], 0 offen sc1 lds
mav 4,228,56
mav 8,232,56
mav 12,236,56
mav 0,240,60
R 40,18432
mav 4,244,60
s_waitcnt vmcnt(5)
buffer_store_dwordx2 v[72:73], v81, s[36:39], s52 offen sc1
mav 8,248,60
mav 12,252,60
s_waitcnt lgkmcnt(1)
mvv 0,128,32
R 44,19456
mvv 4,132,32
mvv 8,136,32
mvv 12,140,32
mvv 0,144,36
R 48,20480
mvv 4,148,36
mvv 8,152,36
mvv 12,156,36
s_waitcnt lgkmcnt(1)
mvv 0,160,40
R 52,21504
mvv 4,164,40
ds_read_b32 v74, v82 offset:0
mvv 8,168,40
mvv 12,172,40
mvv 0,176,44
R 56,22528
mvv 4,180,44
mvv 8,184,44
s_xor_b32 s50, s50, 0x10000
mvv 12,188,44
s_waitcnt lgkmcnt(1)
mvv 0,192,48
R 60,23552
mvv 4,196,48
v_cmp_gt_i32 vcc, v86, v74
mvv 8,200,48
s_waitcnt vmcnt(4)
s_cbranch_vccnz CG0_6
BG0_6:
mvv 12,204,48
s_barrier
s_add_u32 s49, s49, 346112
mvv 0,208,52
R 32,32768
s_add_u32 m0, s50, 0
mvv 4,212,52
D 83,0
mvv 8,216,52
mvv 12,220,52
s_waitcnt lgkmcnt(1)
mvv 0,224,56
R 36,33792
mvv 4,228,56
D 83,1024
mvv 8,232,56
mvv 12,236,56
mvv 0,240,60
R 40,34816
mvv 4,244,60
mvv 8,248,60
D 83,2048
mvv 12,252,60
s_waitcnt lgkmcnt(1)
v_mfma_f32_16x16x32_bf16 v[16:19], a[0:3], v[32:35], 0
R 44,35840
v_cvt_pk_bf16_f32 v64, v0, v1
v_mfma_f32_16x16x32_bf16 v[20:23], a[4:7], v[32:35], 0
v_cvt_pk_bf16_f32 v65, v2, v3
v_cvt_pk_bf16_f32 v66, v4, v5
v_mfma_f32_16x16x32_bf16 v[24:27], a[8:11], v[32:35], 0
D 83,3072
v_cvt_pk_bf16_f32 v67, v6, v7
v_mfma_f32_16x16x32_bf16 v[28:31], a[12:15], v[32:35], 0
v_cvt_pk_bf16_f32 v68, v8, v9
v_cvt_pk_bf16_f32 v69, v10, v11
mav 16,16,36
R 48,36864
v_cvt_pk_bf16_f32 v70, v12, v13
mav 20,20,36
s_add_u32 m0, s50, 4096
v_cvt_pk_bf16_f32 v71, v14, v15
mav 24,24,36
buffer_store_dwordx4 v[64:67], v87, s[36:39], s49 offen sc1
mav 28,28,36
s_waitcnt lgkmcnt(1)
mav 16,32,40
R 52,37888
mav 20,36,40
mav 24,40,40
buffer_store_dwordx4 v[68:71], v87, s[36:39], s49 offen offset:1024 sc1
mav 28,44,40
mav 16,48,44
R 56,38912
mav 20,52,44
mav 24,56,44
D 84,0
mav 28,60,44
s_waitcnt lgkmcnt(1)
mav 16,64,48
R 60,39936
mav 20,68,48
mav 24,72,48
D 84,1024
mav 28,76,48
mav 16,80,52
R 32,40960
mav 20,84,52
mav 24,88,52
mav 28,92,52
s_waitcnt lgkmcnt(1)
mav 16,96,56
R 36,41984
mav 20,100,56
mav 24,104,56
mav 28,108,56
mav 16,112,60
R 40,43008
mav 20,116,60
mav 24,120,60
s_mov_b32 m0, 142336
mav 28,124,60
s_waitcnt lgkmcnt(1)
mav 16,128,32
R 44,44032
buffer_load_dword v85, s[36:39], 0 offen sc1 lds
mav 20,132,32
mav 24,136,32
mav 28,140,32
mav 16,144,36
R 48,45056
mav 20,148,36
mav 24,152,36
mav 28,156,36
s_waitcnt lgkmcnt(1)
mav 16,160,40
R 52,46080
v_add_u32 v86, 1, v86
mav 20,164,40
mav 24,168,40
mav 28,172,40
mav 16,176,44
R 56,47104
buffer_load_dword v85, s[36:39], 0 offen sc1 lds
mav 20,180,44
mav 24,184,44
mav 28,188,44
s_waitcnt lgkmcnt(1)
mav 16,192,48
R 60,48128
mav 20,196,48
mav 24,200,48
mav 28,204,48
mav 16,208,52
R 32,49152
mav 20,212,52
mav 24,216,52
v_add_u32 v72, 1, v72
mav 28,220,52
s_waitcnt lgkmcnt(1)
mav 16,224,56
R 36,50176
buffer_load_dword v85, s[36:39], 0 offen sc1 lds
mav 20,228,56
mav 24,232,56
mav 28,236,56
mav 16,240,60
R 40,51200
mav 20,244,60
s_waitcnt vmcnt(5)
buffer_store_dwordx2 v[72:73], v81, s[36:39], s52 offen sc1
mav 24,248,60
mav 28,252,60
s_waitcnt lgkmcnt(1)
mvv 16,128,32
R 44,52224
mvv 20,132,32
mvv 24,136,32
mvv 28,140,32
mvv 16,144,36
R 48,53248
mvv 20,148,36
mvv 24,152,36
mvv 28,156,36
s_waitcnt lgkmcnt(1)
mvv 16,160,40
R 52,54272
mvv 20,164,40
ds_read_b32 v74, v82 offset:0
mvv 24,168,40
mvv 28,172,40
mvv 16,176,44
R 56,55296
mvv 20,180,44
mvv 24,184,44
mvv 28,188,44
s_waitcnt lgkmcnt(1)
mvv 16,192,48
R 60,56320
mvv 20,196,48
v_xor_b32 v80, 0x10000, v80
v_cmp_gt_i32 vcc, v86, v74
mvv 24,200,48
s_waitcnt vmcnt(4)
s_cbranch_vccnz CG0_7
BG0_7:
mvv 28,204,48
s_barrier
s_add_u32 s49, s49, 346112
mvv 16,208,52
R 32,0
s_add_u32 m0, s50, 32768
mvv 20,212,52
D 83,0
mvv 24,216,52
mvv 28,220,52
s_waitcnt lgkmcnt(1)
mvv 16,224,56
R 36,1024
mvv 20,228,56
D 83,1024
mvv 24,232,56
mvv 28,236,56
mvv 16,240,60
R 40,2048
mvv 20,244,60
mvv 24,248,60
D 83,2048
mvv 28,252,60
s_add_u32 s60, s60, 1
s_cbranch_scc0 LG0_5
s_waitcnt lgkmcnt(1)
v_mfma_f32_16x16x32_bf16 v[0:3], a[0:3], v[32:35], 0
R 44,3072
v_cvt_pk_bf16_f32 v64, v16, v17
v_mfma_f32_16x16x32_bf16 v[4:7], a[4:7], v[32:35], 0
v_cvt_pk_bf16_f32 v65, v18, v19
v_cvt_pk_bf16_f32 v66, v20, v21
v_mfma_f32_16x16x32_bf16 v[8:11], a[8:11], v[32:35], 0
D 83,3072
v_cvt_pk_bf16_f32 v67, v22, v23
v_mfma_f32_16x16x32_bf16 v[12:15], a[12:15], v[32:35], 0
v_cvt_pk_bf16_f32 v68, v24, v25
v_cvt_pk_bf16_f32 v69, v26, v27
mav 0,16,36
R 48,4096
v_cvt_pk_bf16_f32 v70, v28, v29
mav 4,20,36
s_add_u32 m0, s50, 36864
v_cvt_pk_bf16_f32 v71, v30, v31
mav 8,24,36
buffer_store_dwordx4 v[64:67], v87, s[36:39], s49 offen sc1
mav 12,28,36
s_waitcnt lgkmcnt(1)
mav 0,32,40
R 52,5120
mav 4,36,40
mav 8,40,40
buffer_store_dwordx4 v[68:71], v87, s[36:39], s49 offen offset:1024 sc1
mav 12,44,40
mav 0,48,44
R 56,6144
mav 4,52,44
mav 8,56,44
D 84,0
mav 12,60,44
s_waitcnt lgkmcnt(1)
mav 0,64,48
R 60,7168
mav 4,68,48
mav 8,72,48
D 84,1024
mav 12,76,48
mav 0,80,52
R 32,8192
mav 4,84,52
mav 8,88,52
mav 12,92,52
s_waitcnt lgkmcnt(1)
mav 0,96,56
R 36,9216
mav 4,100,56
mav 8,104,56
mav 12,108,56
mav 0,112,60
R 40,10240
mav 4,116,60
mav 8,120,60
mav 12,124,60
s_waitcnt lgkmcnt(1)
mav 0,128,32
R 44,11264
mav 4,132,32
mav 8,136,32
mav 12,140,32
mav 0,144,36
R 48,12288
mav 4,148,36
mav 8,152,36
mav 12,156,36
s_waitcnt lgkmcnt(1)
mav 0,160,40
R 52,13312
mav 4,164,40
mav 8,168,40
mav 12,172,40
mav 0,176,44
R 56,14336
mav 4,180,44
mav 8,184,44
mav 12,188,44
s_waitcnt lgkmcnt(1)
mav 0,192,48
R 60,15360
mav 4,196,48
mav 8,200,48
mav 12,204,48
mav 0,208,52
R 32,16384
mav 4,212,52
mav 8,216,52
v_add_u32 v72, 1, v72
mav 12,220,52
s_waitcnt lgkmcnt(1)
mav 0,224,56
R 36,17408
mav 4,228,56
mav 8,232,56
mav 12,236,56
mav 0,240,60
R 40,18432
mav 4,244,60
s_waitcnt vmcnt(2)
buffer_store_dwordx2 v[72:73], v81, s[36:39], s52 offen sc1
mav 8,248,60
mav 12,252,60
s_waitcnt lgkmcnt(1)
mvv 0,128,32
R 44,19456
mvv 4,132,32
mvv 8,136,32
mvv 12,140,32
mvv 0,144,36
R 48,20480
mvv 4,148,36
mvv 8,152,36
mvv 12,156,36
s_waitcnt lgkmcnt(1)
mvv 0,160,40
R 52,21504
mvv 4,164,40
mvv 8,168,40
mvv 12,172,40
mvv 0,176,44
R 56,22528
mvv 4,180,44
mvv 8,184,44
mvv 12,188,44
s_waitcnt lgkmcnt(1)
mvv 0,192,48
R 60,23552
mvv 4,196,48
mvv 8,200,48
s_add_u32 s49, s49, 346112
mvv 12,204,48
s_waitcnt vmcnt(1)
s_barrier
mvv 0,208,52
R 32,32768
mvv 4,212,52
mvv 8,216,52
mvv 12,220,52
s_waitcnt lgkmcnt(1)
mvv 0,224,56
R 36,33792
mvv 4,228,56
mvv 8,232,56
mvv 12,236,56
mvv 0,240,60
R 40,34816
mvv 4,244,60
mvv 8,248,60
mvv 12,252,60
s_waitcnt lgkmcnt(1)
v_mfma_f32_16x16x32_bf16 v[16:19], a[0:3], v[32:35], 0
R 44,35840
v_mfma_f32_16x16x32_bf16 v[20:23], a[4:7], v[32:35], 0
v_cvt_pk_bf16_f32 v64, v0, v1
v_cvt_pk_bf16_f32 v65, v2, v3
v_mfma_f32_16x16x32_bf16 v[24:27], a[8:11], v[32:35], 0
v_cvt_pk_bf16_f32 v66, v4, v5
v_cvt_pk_bf16_f32 v67, v6, v7
v_mfma_f32_16x16x32_bf16 v[28:31], a[12:15], v[32:35], 0
buffer_store_dwordx4 v[64:67], v87, s[36:39], s49 offen sc1
v_cvt_pk_bf16_f32 v68, v8, v9
mav 16,16,36
R 48,36864
v_cvt_pk_bf16_f32 v69, v10, v11
mav 20,20,36
v_cvt_pk_bf16_f32 v70, v12, v13
v_cvt_pk_bf16_f32 v71, v14, v15
mav 24,24,36
mav 28,28,36
buffer_store_dwordx4 v[68:71], v87, s[36:39], s49 offen offset:1024 sc1
s_waitcnt lgkmcnt(1)
mav 16,32,40
R 52,37888
mav 20,36,40
mav 24,40,40
mav 28,44,40
mav 16,48,44
R 56,38912
mav 20,52,44
mav 24,56,44
mav 28,60,44
s_waitcnt lgkmcnt(1)
mav 16,64,48
R 60,39936
mav 20,68,48
mav 24,72,48
mav 28,76,48
mav 16,80,52
R 32,40960
mav 20,84,52
mav 24,88,52
mav 28,92,52
s_waitcnt lgkmcnt(1)
mav 16,96,56
R 36,41984
mav 20,100,56
mav 24,104,56
mav 28,108,56
mav 16,112,60
R 40,43008
mav 20,116,60
mav 24,120,60
mav 28,124,60
s_waitcnt lgkmcnt(1)
mav 16,128,32
R 44,44032
mav 20,132,32
mav 24,136,32
mav 28,140,32
mav 16,144,36
R 48,45056
mav 20,148,36
mav 24,152,36
mav 28,156,36
s_waitcnt lgkmcnt(1)
mav 16,160,40
R 52,46080
mav 20,164,40
mav 24,168,40
mav 28,172,40
mav 16,176,44
R 56,47104
mav 20,180,44
mav 24,184,44
mav 28,188,44
s_waitcnt lgkmcnt(1)
mav 16,192,48
R 60,48128
mav 20,196,48
mav 24,200,48
mav 28,204,48
mav 16,208,52
R 32,49152
mav 20,212,52
mav 24,216,52
v_add_u32 v72, 1, v72
mav 28,220,52
s_waitcnt lgkmcnt(1)
mav 16,224,56
R 36,50176
mav 20,228,56
mav 24,232,56
mav 28,236,56
mav 16,240,60
R 40,51200
mav 20,244,60
s_waitcnt vmcnt(0)
buffer_store_dwordx2 v[72:73], v81, s[36:39], s52 offen sc1
mav 24,248,60
mav 28,252,60
s_waitcnt lgkmcnt(1)
mvv 16,128,32
R 44,52224
mvv 20,132,32
mvv 24,136,32
mvv 28,140,32
mvv 16,144,36
R 48,53248
mvv 20,148,36
mvv 24,152,36
mvv 28,156,36
s_waitcnt lgkmcnt(1)
mvv 16,160,40
R 52,54272
mvv 20,164,40
mvv 24,168,40
mvv 28,172,40
mvv 16,176,44
R 56,55296
mvv 20,180,44
mvv 24,184,44
mvv 28,188,44
s_waitcnt lgkmcnt(1)
mvv 16,192,48
R 60,56320
mvv 20,196,48
mvv 24,200,48
s_add_u32 s49, s49, 346112
mvv 28,204,48
mvv 16,208,52
mvv 20,212,52
mvv 24,216,52
mvv 28,220,52
s_waitcnt lgkmcnt(0)
mvv 16,224,56
mvv 20,228,56
mvv 24,232,56
mvv 28,236,56
mvv 16,240,60
mvv 20,244,60
mvv 24,248,60
mvv 28,252,60
s_nop 4
v_cvt_pk_bf16_f32 v64, v16, v17
v_cvt_pk_bf16_f32 v65, v18, v19
v_cvt_pk_bf16_f32 v66, v20, v21
v_cvt_pk_bf16_f32 v67, v22, v23
buffer_store_dwordx4 v[64:67], v87, s[36:39], s49 offen sc1
v_cvt_pk_bf16_f32 v68, v24, v25
v_cvt_pk_bf16_f32 v69, v26, v27
v_cvt_pk_bf16_f32 v70, v28, v29
v_cvt_pk_bf16_f32 v71, v30, v31
buffer_store_dwordx4 v[68:71], v87, s[36:39], s49 offen offset:1024 sc1
v_add_u32 v72, 37, v72
s_waitcnt vmcnt(0)
buffer_store_dwordx2 v[72:73], v81, s[36:39], s52 offen sc1
s_endpgm
CG0_1:
s_cmp_eq_u32 s61, 0
s_cbranch_scc1 BG0_1
s_sub_u32 s61, s61, 1
s_sleep 8
buffer_load_dwordx2 v[74:75], v85, s[36:39], 0 offen sc1
s_waitcnt vmcnt(0)
ds_write_b32 v82, v74
v_sub_u32 v74, v74, v86
v_xor_b32 v75, v75, v73
v_cmp_lt_u64 vcc, s[62:63], v[74:75]
s_waitcnt lgkmcnt(0)
s_cbranch_vccnz CG0_1
s_branch BG0_1
CG0_2:
s_mov_b32 s58, m0
s_mov_b32 m0, 142336
DG0_2:
s_cmp_eq_u32 s61, 0
s_cbranch_scc1 EG0_2
s_sub_u32 s61, s61, 1
buffer_load_dword v85, s[36:39], 0 offen sc1 lds
s_sleep 1
ds_read_b32 v74, v82
s_waitcnt lgkmcnt(0)
v_cmp_gt_i32 vcc, v86, v74
s_cbranch_vccnz DG0_2
EG0_2:
s_mov_b32 m0, s58
s_branch BG0_2
CG0_3:
s_mov_b32 s58, m0
s_mov_b32 m0, 142336
DG0_3:
s_cmp_eq_u32 s61, 0
s_cbranch_scc1 EG0_3
s_sub_u32 s61, s61, 1
buffer_load_dword v85, s[36:39], 0 offen sc1 lds
s_sleep 1
ds_read_b32 v74, v82
s_waitcnt lgkmcnt(0)
v_cmp_gt_i32 vcc, v86, v74
s_cbranch_vccnz DG0_3
EG0_3:
s_mov_b32 m0, s58
s_branch BG0_3
CG0_4:
s_mov_b32 s58, m0
s_mov_b32 m0, 142336
DG0_4:
s_cmp_eq_u32 s61, 0
s_cbranch_scc1 EG0_4
s_sub_u32 s61, s61, 1
buffer_load_dword v85, s[36:39], 0 offen sc1 lds
s_sleep 1
ds_read_b32 v74, v82
s_waitcnt lgkmcnt(0)
v_cmp_gt_i32 vcc, v86, v74
s_cbranch_vccnz DG0_4
EG0_4:
s_mov_b32 m0, s58
s_branch BG0_4
CG0_6:
s_mov_b32 s58, m0
s_mov_b32 m0, 142336
DG0_6:
s_cmp_eq_u32 s61, 0
s_cbranch_scc1 EG0_6
s_sub_u32 s61, s61, 1
buffer_load_dword v85, s[36:39], 0 offen sc1 lds
s_sleep 1
ds_read_b32 v74, v82
s_waitcnt lgkmcnt(0)
v_cmp_gt_i32 vcc, v86, v74
s_cbranch_vccnz DG0_6
EG0_6:
s_mov_b32 m0, s58
s_branch BG0_6
CG0_7:
s_mov_b32 s58, m0
s_mov_b32 m0, 142336
DG0_7:
s_cmp_eq_u32 s61, 0
s_cbranch_scc1 EG0_7
s_sub_u32 s61, s61, 1
buffer_load_dword v85, s[36:39], 0 offen sc1 lds
s_sleep 1
ds_read_b32 v74, v82
s_waitcnt lgkmcnt(0)
v_cmp_gt_i32 vcc, v86, v74
s_cbranch_vccnz DG0_7
EG0_7:
s_mov_b32 m0, s58
s_branch BG0_7
LvG1:
s_add_u32 s49, s49, 346112
s_barrier
s_add_u32 m0, s50, 0
s_nop 0
D 83,0
D 83,1024
D 83,2048
D 83,3072
s_add_u32 m0, s50, 4096
s_nop 0
D 84,0
D 84,1024
s_add_u32 s49, s49, 346112
s_waitcnt vmcnt(0)
s_barrier
s_add_u32 m0, s50, 32768
R 32,0
D 83,0
R 36,1024
D 83,1024
R 40,2048
D 83,2048
s_waitcnt lgkmcnt(1)
v_mfma_f32_16x16x32_bf16 v[0:3], a[0:3], v[32:35], 0
R 44,3072
v_mfma_f32_16x16x32_bf16 v[4:7], a[4:7], v[32:35], 0
v_mfma_f32_16x16x32_bf16 v[8:11], a[8:11], v[32:35], 0
D 83,3072
v_mfma_f32_16x16x32_bf16 v[12:15], a[12:15], v[32:35], 0
mav 0,16,36
R 48,4096
mav 4,20,36
s_add_u32 m0, s50, 36864
mav 8,24,36
mav 12,28,36
D 84,0
s_waitcnt lgkmcnt(1)
mav 0,32,40
R 52,5120
mav 4,36,40
mav 8,40,40
mav 12,44,40
D 84,1024
mav 0,48,44
R 56,6144
mav 4,52,44
mav 8,56,44
mav 12,60,44
s_waitcnt lgkmcnt(1)
mav 0,64,48
R 60,7168
mav 4,68,48
mav 8,72,48
mav 12,76,48
mav 0,80,52
R 32,8192
mav 4,84,52
mav 8,88,52
mav 12,92,52
s_waitcnt lgkmcnt(1)
mav 0,96,56
R 36,9216
mav 4,100,56
mav 8,104,56
mav 12,108,56
mav 0,112,60
R 40,10240
mav 4,116,60
mav 8,120,60
mav 12,124,60
s_waitcnt lgkmcnt(1)
mav 0,128,32
R 44,11264
mav 4,132,32
mav 8,136,32
mav 12,140,32
mav 0,144,36
R 48,12288
mav 4,148,36
mav 8,152,36
mav 12,156,36
s_waitcnt lgkmcnt(1)
mav 0,160,40
R 52,13312
mav 4,164,40
mav 8,168,40
mav 12,172,40
mav 0,176,44
R 56,14336
mav 4,180,44
mav 8,184,44
mav 12,188,44
s_waitcnt lgkmcnt(1)
mav 0,192,48
R 60,15360
mav 4,196,48
mav 8,200,48
mav 12,204,48
mav 0,208,52
R 32,16384
mav 4,212,52
mav 8,216,52
mav 12,220,52
s_waitcnt lgkmcnt(1)
mav 0,224,56
R 36,17408
mav 4,228,56
mav 8,232,56
mav 12,236,56
mav 0,240,60
R 40,18432
mav 4,244,60
mav 8,248,60
mav 12,252,60
s_waitcnt lgkmcnt(1)
mvv 0,128,32
R 44,19456
mvv 4,132,32
mvv 8,136,32
mvv 12,140,32
mvv 0,144,36
R 48,20480
mvv 4,148,36
mvv 8,152,36
mvv 12,156,36
s_waitcnt lgkmcnt(1)
mvv 0,160,40
R 52,21504
mvv 4,164,40
mvv 8,168,40
mvv 12,172,40
mvv 0,176,44
R 56,22528
mvv 4,180,44
mvv 8,184,44
s_xor_b32 s50, s50, 0x10000
mvv 12,188,44
s_waitcnt lgkmcnt(1)
mvv 0,192,48
R 60,23552
mvv 4,196,48
mvv 8,200,48
s_add_u32 s49, s49, 346112
mvv 12,204,48
s_waitcnt vmcnt(0)
s_barrier
mvv 0,208,52
R 32,32768
s_add_u32 m0, s50, 0
mvv 4,212,52
D 83,0
mvv 8,216,52
mvv 12,220,52
s_waitcnt lgkmcnt(1)
mvv 0,224,56
R 36,33792
mvv 4,228,56
D 83,1024
mvv 8,232,56
mvv 12,236,56
mvv 0,240,60
R 40,34816
mvv 4,244,60
mvv 8,248,60
D 83,2048
mvv 12,252,60
s_waitcnt lgkmcnt(1)
v_mfma_f32_16x16x32_bf16 v[16:19], a[0:3], v[32:35], 0
R 44,35840
v_cvt_pk_bf16_f32 v64, v0, v1
v_mfma_f32_16x16x32_bf16 v[20:23], a[4:7], v[32:35], 0
v_cvt_pk_bf16_f32 v65, v2, v3
v_cvt_pk_bf16_f32 v66, v4, v5
v_mfma_f32_16x16x32_bf16 v[24:27], a[8:11], v[32:35], 0
D 83,3072
v_cvt_pk_bf16_f32 v67, v6, v7
v_mfma_f32_16x16x32_bf16 v[28:31], a[12:15], v[32:35], 0
v_cvt_pk_bf16_f32 v68, v8, v9
v_cvt_pk_bf16_f32 v69, v10, v11
mav 16,16,36
R 48,36864
v_cvt_pk_bf16_f32 v70, v12, v13
mav 20,20,36
s_add_u32 m0, s50, 4096
v_cvt_pk_bf16_f32 v71, v14, v15
mav 24,24,36
buffer_store_dwordx4 v[64:67], v87, s[36:39], s49 offen sc1
mav 28,28,36
s_waitcnt lgkmcnt(1)
mav 16,32,40
R 52,37888
mav 20,36,40
mav 24,40,40
buffer_store_dwordx4 v[68:71], v87, s[36:39], s49 offen offset:1024 sc1
mav 28,44,40
mav 16,48,44
R 56,38912
mav 20,52,44
mav 24,56,44
D 84,0
mav 28,60,44
s_waitcnt lgkmcnt(1)
mav 16,64,48
R 60,39936
mav 20,68,48
mav 24,72,48
D 84,1024
mav 28,76,48
mav 16,80,52
R 32,40960
mav 20,84,52
mav 24,88,52
mav 28,92,52
s_waitcnt lgkmcnt(1)
mav 16,96,56
R 36,41984
mav 20,100,56
mav 24,104,56
mav 28,108,56
mav 16,112,60
R 40,43008
mav 20,116,60
mav 24,120,60
mav 28,124,60
s_waitcnt lgkmcnt(1)
mav 16,128,32
R 44,44032
mav 20,132,32
mav 24,136,32
mav 28,140,32
mav 16,144,36
R 48,45056
mav 20,148,36
mav 24,152,36
mav 28,156,36
s_waitcnt lgkmcnt(1)
mav 16,160,40
R 52,46080
mav 20,164,40
mav 24,168,40
mav 28,172,40
mav 16,176,44
R 56,47104
mav 20,180,44
mav 24,184,44
mav 28,188,44
s_waitcnt lgkmcnt(1)
mav 16,192,48
R 60,48128
mav 20,196,48
mav 24,200,48
mav 28,204,48
mav 16,208,52
R 32,49152
mav 20,212,52
mav 24,216,52
v_add_u32 v72, 1, v72
mav 28,220,52
s_waitcnt lgkmcnt(1)
mav 16,224,56
R 36,50176
mav 20,228,56
mav 24,232,56
mav 28,236,56
mav 16,240,60
R 40,51200
mav 20,244,60
s_waitcnt vmcnt(2)
buffer_store_dwordx2 v[72:73], v81, s[36:39], s52 offen sc1
mav 24,248,60
mav 28,252,60
s_waitcnt lgkmcnt(1)
mvv 16,128,32
R 44,52224
mvv 20,132,32
mvv 24,136,32
mvv 28,140,32
mvv 16,144,36
R 48,53248
mvv 20,148,36
mvv 24,152,36
mvv 28,156,36
s_waitcnt lgkmcnt(1)
mvv 16,160,40
R 52,54272
mvv 20,164,40
mvv 24,168,40
mvv 28,172,40
mvv 16,176,44
R 56,55296
mvv 20,180,44
mvv 24,184,44
mvv 28,188,44
s_waitcnt lgkmcnt(1)
mvv 16,192,48
R 60,56320
mvv 20,196,48
v_xor_b32 v80, 0x10000, v80
mvv 24,200,48
s_add_u32 s49, s49, 346112
mvv 28,204,48
s_waitcnt vmcnt(1)
s_barrier
mvv 16,208,52
R 32,0
s_add_u32 m0, s50, 32768
mvv 20,212,52
D 83,0
mvv 24,216,52
mvv 28,220,52
s_waitcnt lgkmcnt(1)
mvv 16,224,56
R 36,1024
mvv 20,228,56
D 83,1024
mvv 24,232,56
mvv 28,236,56
mvv 16,240,60
R 40,2048
mvv 20,244,60
mvv 24,248,60
D 83,2048
mvv 28,252,60
s_mov_b32 s60, -30
.p2align 6
LG1_1:
s_waitcnt lgkmcnt(1)
v_mfma_f32_16x16x32_bf16 v[0:3], a[0:3], v[32:35], 0
R 44,3072
v_cvt_pk_bf16_f32 v64, v16, v17
v_mfma_f32_16x16x32_bf16 v[4:7], a[4:7], v[32:35], 0
v_cvt_pk_bf16_f32 v65, v18, v19
v_cvt_pk_bf16_f32 v66, v20, v21
v_mfma_f32_16x16x32_bf16 v[8:11], a[8:11], v[32:35], 0
D 83,3072
v_cvt_pk_bf16_f32 v67, v22, v23
v_mfma_f32_16x16x32_bf16 v[12:15], a[12:15], v[32:35], 0
v_cvt_pk_bf16_f32 v68, v24, v25
v_cvt_pk_bf16_f32 v69, v26, v27
mav 0,16,36
R 48,4096
v_cvt_pk_bf16_f32 v70, v28, v29
mav 4,20,36
s_add_u32 m0, s50, 36864
v_cvt_pk_bf16_f32 v71, v30, v31
mav 8,24,36
buffer_store_dwordx4 v[64:67], v87, s[36:39], s49 offen sc1
mav 12,28,36
s_waitcnt lgkmcnt(1)
mav 0,32,40
R 52,5120
mav 4,36,40
mav 8,40,40
buffer_store_dwordx4 v[68:71], v87, s[36:39], s49 offen offset:1024 sc1
mav 12,44,40
mav 0,48,44
R 56,6144
mav 4,52,44
mav 8,56,44
D 84,0
mav 12,60,44
s_waitcnt lgkmcnt(1)
mav 0,64,48
R 60,7168
mav 4,68,48
mav 8,72,48
D 84,1024
mav 12,76,48
mav 0,80,52
R 32,8192
mav 4,84,52
mav 8,88,52
mav 12,92,52
s_waitcnt lgkmcnt(1)
mav 0,96,56
R 36,9216
mav 4,100,56
mav 8,104,56
mav 12,108,56
mav 0,112,60
R 40,10240
mav 4,116,60
mav 8,120,60
mav 12,124,60
s_waitcnt lgkmcnt(1)
mav 0,128,32
R 44,11264
mav 4,132,32
mav 8,136,32
mav 12,140,32
mav 0,144,36
R 48,12288
mav 4,148,36
mav 8,152,36
mav 12,156,36
s_waitcnt lgkmcnt(1)
mav 0,160,40
R 52,13312
mav 4,164,40
mav 8,168,40
mav 12,172,40
mav 0,176,44
R 56,14336
mav 4,180,44
mav 8,184,44
mav 12,188,44
s_waitcnt lgkmcnt(1)
mav 0,192,48
R 60,15360
mav 4,196,48
mav 8,200,48
mav 12,204,48
mav 0,208,52
R 32,16384
mav 4,212,52
mav 8,216,52
v_add_u32 v72, 1, v72
mav 12,220,52
s_waitcnt lgkmcnt(1)
mav 0,224,56
R 36,17408
mav 4,228,56
mav 8,232,56
mav 12,236,56
mav 0,240,60
R 40,18432
mav 4,244,60
s_waitcnt vmcnt(2)
buffer_store_dwordx2 v[72:73], v81, s[36:39], s52 offen sc1
mav 8,248,60
mav 12,252,60
s_waitcnt lgkmcnt(1)
mvv 0,128,32
R 44,19456
mvv 4,132,32
mvv 8,136,32
mvv 12,140,32
mvv 0,144,36
R 48,20480
mvv 4,148,36
mvv 8,152,36
mvv 12,156,36
s_waitcnt lgkmcnt(1)
mvv 0,160,40
R 52,21504
mvv 4,164,40
mvv 8,168,40
mvv 12,172,40
mvv 0,176,44
R 56,22528
mvv 4,180,44
mvv 8,184,44
s_xor_b32 s50, s50, 0x10000
mvv 12,188,44
s_waitcnt lgkmcnt(1)
mvv 0,192,48
R 60,23552
mvv 4,196,48
mvv 8,200,48
s_add_u32 s49, s49, 346112
mvv 12,204,48
s_waitcnt vmcnt(1)
s_barrier
mvv 0,208,52
R 32,32768
s_add_u32 m0, s50, 0
mvv 4,212,52
D 83,0
mvv 8,216,52
mvv 12,220,52
s_waitcnt lgkmcnt(1)
mvv 0,224,56
R 36,33792
mvv 4,228,56
D 83,1024
mvv 8,232,56
mvv 12,236,56
mvv 0,240,60
R 40,34816
mvv 4,244,60
mvv 8,248,60
D 83,2048
mvv 12,252,60
s_waitcnt lgkmcnt(1)
v_mfma_f32_16x16x32_bf16 v[16:19], a[0:3], v[32:35], 0
R 44,35840
v_cvt_pk_bf16_f32 v64, v0, v1
v_mfma_f32_16x16x32_bf16 v[20:23], a[4:7], v[32:35], 0
v_cvt_pk_bf16_f32 v65, v2, v3
v_cvt_pk_bf16_f32 v66, v4, v5
v_mfma_f32_16x16x32_bf16 v[24:27], a[8:11], v[32:35], 0
D 83,3072
v_cvt_pk_bf16_f32 v67, v6, v7
v_mfma_f32_16x16x32_bf16 v[28:31], a[12:15], v[32:35], 0
v_cvt_pk_bf16_f32 v68, v8, v9
v_cvt_pk_bf16_f32 v69, v10, v11
mav 16,16,36
R 48,36864
v_cvt_pk_bf16_f32 v70, v12, v13
mav 20,20,36
s_add_u32 m0, s50, 4096
v_cvt_pk_bf16_f32 v71, v14, v15
mav 24,24,36
buffer_store_dwordx4 v[64:67], v87, s[36:39], s49 offen sc1
mav 28,28,36
s_waitcnt lgkmcnt(1)
mav 16,32,40
R 52,37888
mav 20,36,40
mav 24,40,40
buffer_store_dwordx4 v[68:71], v87, s[36:39], s49 offen offset:1024 sc1
mav 28,44,40
mav 16,48,44
R 56,38912
mav 20,52,44
mav 24,56,44
D 84,0
mav 28,60,44
s_waitcnt lgkmcnt(1)
mav 16,64,48
R 60,39936
mav 20,68,48
mav 24,72,48
D 84,1024
mav 28,76,48
mav 16,80,52
R 32,40960
mav 20,84,52
mav 24,88,52
mav 28,92,52
s_waitcnt lgkmcnt(1)
mav 16,96,56
R 36,41984
mav 20,100,56
mav 24,104,56
mav 28,108,56
mav 16,112,60
R 40,43008
mav 20,116,60
mav 24,120,60
mav 28,124,60
s_waitcnt lgkmcnt(1)
mav 16,128,32
R 44,44032
mav 20,132,32
mav 24,136,32
mav 28,140,32
mav 16,144,36
R 48,45056
mav 20,148,36
mav 24,152,36
mav 28,156,36
s_waitcnt lgkmcnt(1)
mav 16,160,40
R 52,46080
mav 20,164,40
mav 24,168,40
mav 28,172,40
mav 16,176,44
R 56,47104
mav 20,180,44
mav 24,184,44
mav 28,188,44
s_waitcnt lgkmcnt(1)
mav 16,192,48
R 60,48128
mav 20,196,48
mav 24,200,48
mav 28,204,48
mav 16,208,52
R 32,49152
mav 20,212,52
mav 24,216,52
v_add_u32 v72, 1, v72
mav 28,220,52
s_waitcnt lgkmcnt(1)
mav 16,224,56
R 36,50176
mav 20,228,56
mav 24,232,56
mav 28,236,56
mav 16,240,60
R 40,51200
mav 20,244,60
s_waitcnt vmcnt(2)
buffer_store_dwordx2 v[72:73], v81, s[36:39], s52 offen sc1
mav 24,248,60
mav 28,252,60
s_waitcnt lgkmcnt(1)
mvv 16,128,32
R 44,52224
mvv 20,132,32
mvv 24,136,32
mvv 28,140,32
mvv 16,144,36
R 48,53248
mvv 20,148,36
mvv 24,152,36
mvv 28,156,36
s_waitcnt lgkmcnt(1)
mvv 16,160,40
R 52,54272
mvv 20,164,40
mvv 24,168,40
mvv 28,172,40
mvv 16,176,44
R 56,55296
mvv 20,180,44
mvv 24,184,44
mvv 28,188,44
s_waitcnt lgkmcnt(1)
mvv 16,192,48
R 60,56320
mvv 20,196,48
v_xor_b32 v80, 0x10000, v80
mvv 24,200,48
s_add_u32 s49, s49, 346112
mvv 28,204,48
s_waitcnt vmcnt(1)
s_barrier
mvv 16,208,52
R 32,0
s_add_u32 m0, s50, 32768
mvv 20,212,52
D 83,0
mvv 24,216,52
mvv 28,220,52
s_waitcnt lgkmcnt(1)
mvv 16,224,56
R 36,1024
mvv 20,228,56
D 83,1024
mvv 24,232,56
mvv 28,236,56
mvv 16,240,60
R 40,2048
mvv 20,244,60
mvv 24,248,60
D 83,2048
mvv 28,252,60
s_add_u32 s60, s60, 1
s_cbranch_scc0 LG1_1
s_waitcnt lgkmcnt(1)
v_mfma_f32_16x16x32_bf16 v[0:3], a[0:3], v[32:35], 0
R 44,3072
v_cvt_pk_bf16_f32 v64, v16, v17
v_mfma_f32_16x16x32_bf16 v[4:7], a[4:7], v[32:35], 0
v_cvt_pk_bf16_f32 v65, v18, v19
v_cvt_pk_bf16_f32 v66, v20, v21
v_mfma_f32_16x16x32_bf16 v[8:11], a[8:11], v[32:35], 0
D 83,3072
v_cvt_pk_bf16_f32 v67, v22, v23
v_mfma_f32_16x16x32_bf16 v[12:15], a[12:15], v[32:35], 0
v_cvt_pk_bf16_f32 v68, v24, v25
v_cvt_pk_bf16_f32 v69, v26, v27
mav 0,16,36
R 48,4096
v_cvt_pk_bf16_f32 v70, v28, v29
mav 4,20,36
s_add_u32 m0, s50, 36864
v_cvt_pk_bf16_f32 v71, v30, v31
mav 8,24,36
buffer_store_dwordx4 v[64:67], v87, s[36:39], s49 offen sc1
mav 12,28,36
s_waitcnt lgkmcnt(1)
mav 0,32,40
R 52,5120
mav 4,36,40
mav 8,40,40
buffer_store_dwordx4 v[68:71], v87, s[36:39], s49 offen offset:1024 sc1
mav 12,44,40
mav 0,48,44
R 56,6144
mav 4,52,44
mav 8,56,44
D 84,0
mav 12,60,44
s_waitcnt lgkmcnt(1)
mav 0,64,48
R 60,7168
mav 4,68,48
mav 8,72,48
D 84,1024
mav 12,76,48
mav 0,80,52
R 32,8192
mav 4,84,52
mav 8,88,52
mav 12,92,52
s_waitcnt lgkmcnt(1)
mav 0,96,56
R 36,9216
mav 4,100,56
mav 8,104,56
mav 12,108,56
mav 0,112,60
R 40,10240
mav 4,116,60
mav 8,120,60
mav 12,124,60
s_waitcnt lgkmcnt(1)
mav 0,128,32
R 44,11264
mav 4,132,32
mav 8,136,32
mav 12,140,32
mav 0,144,36
R 48,12288
mav 4,148,36
mav 8,152,36
mav 12,156,36
s_waitcnt lgkmcnt(1)
mav 0,160,40
R 52,13312
mav 4,164,40
mav 8,168,40
mav 12,172,40
mav 0,176,44
R 56,14336
mav 4,180,44
mav 8,184,44
mav 12,188,44
s_waitcnt lgkmcnt(1)
mav 0,192,48
R 60,15360
mav 4,196,48
mav 8,200,48
mav 12,204,48
mav 0,208,52
R 32,16384
mav 4,212,52
mav 8,216,52
v_add_u32 v72, 1, v72
mav 12,220,52
s_waitcnt lgkmcnt(1)
mav 0,224,56
R 36,17408
mav 4,228,56
mav 8,232,56
mav 12,236,56
mav 0,240,60
R 40,18432
mav 4,244,60
s_waitcnt vmcnt(2)
buffer_store_dwordx2 v[72:73], v81, s[36:39], s52 offen sc1
mav 8,248,60
mav 12,252,60
s_waitcnt lgkmcnt(1)
mvv 0,128,32
R 44,19456
mvv 4,132,32
mvv 8,136,32
mvv 12,140,32
mvv 0,144,36
R 48,20480
mvv 4,148,36
mvv 8,152,36
mvv 12,156,36
s_waitcnt lgkmcnt(1)
mvv 0,160,40
R 52,21504
mvv 4,164,40
mvv 8,168,40
mvv 12,172,40
mvv 0,176,44
R 56,22528
mvv 4,180,44
mvv 8,184,44
mvv 12,188,44
s_waitcnt lgkmcnt(1)
mvv 0,192,48
R 60,23552
mvv 4,196,48
mvv 8,200,48
s_add_u32 s49, s49, 346112
mvv 12,204,48
s_waitcnt vmcnt(1)
s_barrier
mvv 0,208,52
R 32,32768
mvv 4,212,52
mvv 8,216,52
mvv 12,220,52
s_waitcnt lgkmcnt(1)
mvv 0,224,56
R 36,33792
mvv 4,228,56
mvv 8,232,56
mvv 12,236,56
mvv 0,240,60
R 40,34816
mvv 4,244,60
mvv 8,248,60
mvv 12,252,60
s_waitcnt lgkmcnt(1)
v_mfma_f32_16x16x32_bf16 v[16:19], a[0:3], v[32:35], 0
R 44,35840
v_mfma_f32_16x16x32_bf16 v[20:23], a[4:7], v[32:35], 0
v_cvt_pk_bf16_f32 v64, v0, v1
v_cvt_pk_bf16_f32 v65, v2, v3
v_mfma_f32_16x16x32_bf16 v[24:27], a[8:11], v[32:35], 0
v_cvt_pk_bf16_f32 v66, v4, v5
v_cvt_pk_bf16_f32 v67, v6, v7
v_mfma_f32_16x16x32_bf16 v[28:31], a[12:15], v[32:35], 0
buffer_store_dwordx4 v[64:67], v87, s[36:39], s49 offen sc1
v_cvt_pk_bf16_f32 v68, v8, v9
mav 16,16,36
R 48,36864
v_cvt_pk_bf16_f32 v69, v10, v11
mav 20,20,36
v_cvt_pk_bf16_f32 v70, v12, v13
v_cvt_pk_bf16_f32 v71, v14, v15
mav 24,24,36
mav 28,28,36
buffer_store_dwordx4 v[68:71], v87, s[36:39], s49 offen offset:1024 sc1
s_waitcnt lgkmcnt(1)
mav 16,32,40
R 52,37888
mav 20,36,40
mav 24,40,40
mav 28,44,40
mav 16,48,44
R 56,38912
mav 20,52,44
mav 24,56,44
mav 28,60,44
s_waitcnt lgkmcnt(1)
mav 16,64,48
R 60,39936
mav 20,68,48
mav 24,72,48
mav 28,76,48
mav 16,80,52
R 32,40960
mav 20,84,52
mav 24,88,52
mav 28,92,52
s_waitcnt lgkmcnt(1)
mav 16,96,56
R 36,41984
mav 20,100,56
mav 24,104,56
mav 28,108,56
mav 16,112,60
R 40,43008
mav 20,116,60
mav 24,120,60
mav 28,124,60
s_waitcnt lgkmcnt(1)
mav 16,128,32
R 44,44032
mav 20,132,32
mav 24,136,32
mav 28,140,32
mav 16,144,36
R 48,45056
mav 20,148,36
mav 24,152,36
mav 28,156,36
s_waitcnt lgkmcnt(1)
mav 16,160,40
R 52,46080
mav 20,164,40
mav 24,168,40
mav 28,172,40
mav 16,176,44
R 56,47104
mav 20,180,44
mav 24,184,44
mav 28,188,44
s_waitcnt lgkmcnt(1)
mav 16,192,48
R 60,48128
mav 20,196,48
mav 24,200,48
mav 28,204,48
mav 16,208,52
R 32,49152
mav 20,212,52
mav 24,216,52
v_add_u32 v72, 1, v72
mav 28,220,52
s_waitcnt lgkmcnt(1)
mav 16,224,56
R 36,50176
mav 20,228,56
mav 24,232,56
mav 28,236,56
mav 16,240,60
R 40,51200
mav 20,244,60
s_waitcnt vmcnt(0)
buffer_store_dwordx2 v[72:73], v81, s[36:39], s52 offen sc1
mav 24,248,60
mav 28,252,60
s_waitcnt lgkmcnt(1)
mvv 16,128,32
R 44,52224
mvv 20,132,32
mvv 24,136,32
mvv 28,140,32
mvv 16,144,36
R 48,53248
mvv 20,148,36
mvv 24,152,36
mvv 28,156,36
s_waitcnt lgkmcnt(1)
mvv 16,160,40
R 52,54272
mvv 20,164,40
mvv 24,168,40
mvv 28,172,40
mvv 16,176,44
R 56,55296
mvv 20,180,44
mvv 24,184,44
mvv 28,188,44
s_waitcnt lgkmcnt(1)
mvv 16,192,48
R 60,56320
mvv 20,196,48
mvv 24,200,48
s_add_u32 s49, s49, 346112
mvv 28,204,48
mvv 16,208,52
mvv 20,212,52
mvv 24,216,52
mvv 28,220,52
s_waitcnt lgkmcnt(0)
mvv 16,224,56
mvv 20,228,56
mvv 24,232,56
mvv 28,236,56
mvv 16,240,60
mvv 20,244,60
mvv 24,248,60
mvv 28,252,60
s_nop 4
v_cvt_pk_bf16_f32 v64, v16, v17
v_cvt_pk_bf16_f32 v65, v18, v19
v_cvt_pk_bf16_f32 v66, v20, v21
v_cvt_pk_bf16_f32 v67, v22, v23
buffer_store_dwordx4 v[64:67], v87, s[36:39], s49 offen sc1
v_cvt_pk_bf16_f32 v68, v24, v25
v_cvt_pk_bf16_f32 v69, v26, v27
v_cvt_pk_bf16_f32 v70, v28, v29
v_cvt_pk_bf16_f32 v71, v30, v31
buffer_store_dwordx4 v[68:71], v87, s[36:39], s49 offen offset:1024 sc1
v_add_u32 v72, 37, v72
s_waitcnt vmcnt(0)
buffer_store_dwordx2 v[72:73], v81, s[36:39], s52 offen sc1
s_endpgm
Lr5:
buffer_load_dword v73, off, s[36:39], 0 sc1
s_waitcnt vmcnt(0)
v_mov_b32 v72, 0x100
v_add_u32 v73, 0x9e3779b9, v73
buffer_store_dwordx2 v[72:73], v81, s[36:39], s52 offen sc1
v_add_u32 v85, v85, v72
s_sleep 32
s_sleep 32
s_sleep 32
E 0,65536
E 1,69632
E 2,73728
E 3,77824
s_add_u32 s51, s51, 65536
E 0,81920
E 1,86016
E 2,90112
E 3,94208
s_add_u32 s51, s51, 65536
E 0,98304
E 1,102400
E 2,106496
E 3,110592
s_add_u32 s51, s51, 65536
E 0,114688
E 1,118784
E 2,122880
E 3,126976
s_add_u32 s51, s51, 65536
s_cmp_ge_u32 s51, s57
s_cselect_b32 s60, 1048576, 0
s_sub_u32 s51, s51, s60
s_waitcnt vmcnt(8)
s_barrier
v_add_u32 v4, 65536, v4
v_add_u32 v5, 65536, v5
v_add_u32 v6, 65536, v6
v_add_u32 v7, 65536, v7
Ta 0,4,0
Ta 4,5,0
Ta 8,6,0
Ta 12,7,0
Ta 16,4,16384
Ta 20,5,16384
Ta 24,6,16384
Ta 28,7,16384
E 0,131072
E 1,135168
E 2,139264
E 3,143360
s_add_u32 s51, s51, 65536
E 0,147456
E 1,151552
E 2,155648
E 3,159744
s_add_u32 s51, s51, 65536
s_waitcnt lgkmcnt(0)
s_waitcnt vmcnt(8)
s_barrier
E 0,65536
Ta 32,4,32768
Ta 36,5,32768
E 1,69632
Ta 40,6,32768
Ta 44,7,32768
E 2,73728
Ta 48,4,49152
E 3,77824
s_add_u32 s51, s51, 65536
Ta 52,5,49152
Ta 56,6,49152
E 0,81920
Ta 60,7,49152
E 1,86016
E 2,90112
E 3,94208
s_add_u32 s51, s51, 65536
s_cmp_ge_u32 s51, s57
s_cselect_b32 s60, 1048576, 0
s_sub_u32 s51, s51, s60
s_waitcnt lgkmcnt(0)
s_waitcnt vmcnt(8)
s_barrier
v_add_u32 v4, 65536, v4
v_add_u32 v5, 65536, v5
v_add_u32 v6, 65536, v6
v_add_u32 v7, 65536, v7
E 0,98304
Ta 64,4,0
Ta 68,5,0
E 1,102400
Ta 72,6,0
Ta 76,7,0
E 2,106496
Ta 80,4,16384
E 3,110592
s_add_u32 s51, s51, 65536
Ta 84,5,16384
Ta 88,6,16384
E 0,114688
Ta 92,7,16384
E 1,118784
E 2,122880
E 3,126976
s_add_u32 s51, s51, 65536
s_waitcnt lgkmcnt(0)
s_waitcnt vmcnt(8)
s_barrier
v_add_u32 v4, -65536, v4
v_add_u32 v5, -65536, v5
v_add_u32 v6, -65536, v6
v_add_u32 v7, -65536, v7
E 0,131072
Ta 96,4,0
Ta 100,5,0
E 1,135168
Ta 104,6,0
Ta 108,7,0
E 2,139264
Ta 112,4,16384
E 3,143360
s_add_u32 s51, s51, 65536
Ta 116,5,16384
Ta 120,6,16384
E 0,147456
Ta 124,7,16384
E 1,151552
E 2,155648
E 3,159744
s_add_u32 s51, s51, 65536
s_cmp_ge_u32 s51, s57
s_cselect_b32 s60, 1048576, 0
s_sub_u32 s51, s51, s60
s_waitcnt lgkmcnt(0)
s_waitcnt vmcnt(8)
s_barrier
E 0,65536
Ta 128,4,32768
Ta 132,5,32768
E 1,69632
Ta 136,6,32768
Ta 140,7,32768
E 2,73728
Ta 144,4,49152
E 3,77824
s_add_u32 s51, s51, 65536
Ta 148,5,49152
Ta 152,6,49152
E 0,81920
Ta 156,7,49152
E 1,86016
E 2,90112
E 3,94208
s_add_u32 s51, s51, 65536
s_waitcnt lgkmcnt(0)
s_waitcnt vmcnt(8)
s_barrier
v_add_u32 v4, 65536, v4
v_add_u32 v5, 65536, v5
v_add_u32 v6, 65536, v6
v_add_u32 v7, 65536, v7
E 0,98304
Ta 160,4,0
Ta 164,5,0
E 1,102400
Ta 168,6,0
Ta 172,7,0
E 2,106496
Ta 176,4,16384
E 3,110592
s_add_u32 s51, s51, 65536
Ta 180,5,16384
Ta 184,6,16384
E 0,114688
Ta 188,7,16384
E 1,118784
E 2,122880
E 3,126976
s_add_u32 s51, s51, 65536
s_cmp_ge_u32 s51, s57
s_cselect_b32 s60, 1048576, 0
s_sub_u32 s51, s51, s60
s_waitcnt lgkmcnt(0)
s_waitcnt vmcnt(8)
s_barrier
v_add_u32 v4, -65536, v4
v_add_u32 v5, -65536, v5
v_add_u32 v6, -65536, v6
v_add_u32 v7, -65536, v7
Ta 192,4,0
Ta 196,5,0
Ta 200,6,0
Ta 204,7,0
Ta 208,4,16384
Ta 212,5,16384
Ta 216,6,16384
Ta 220,7,16384
s_waitcnt lgkmcnt(0)
s_waitcnt vmcnt(0)
s_barrier
Ta 224,4,32768
Ta 228,5,32768
Ta 232,6,32768
Ta 236,7,32768
Ta 240,4,49152
Ta 244,5,49152
Ta 248,6,49152
Ta 252,7,49152
s_waitcnt lgkmcnt(0)
s_waitcnt vmcnt(0)
s_barrier
s_nop 7
s_and_b32 s60, s48, 3
s_cmp_eq_u32 s60, 1
s_cbranch_scc1 LvR1
s_cmp_eq_u32 s60, 2
s_cbranch_scc1 LvR2
LvR0:
buffer_load_dwordx2 v[76:77], v84, s[36:39], 0 offen sc1
s_waitcnt vmcnt(0)
ds_write_b32 v82, v76
)"
R"(
v_sub_u32 v76, v76, v85
v_xor_b32 v77, v77, v73
v_cmp_lt_u64 vcc, s[62:63], v[76:77]
s_waitcnt lgkmcnt(0)
s_cbranch_vccnz CR0_1
BR0_1:
s_add_u32 s49, s49, 346112
s_barrier
s_add_u32 m0, s50, 0
s_nop 0
D 83,0
D 83,1024
D 83,2048
D 83,3072
s_mov_b32 m0, 142336
v_add_u32 v85, 1, v85
buffer_load_dword v84, s[36:39], 0 offen sc1 lds
buffer_load_dword v84, s[36:39], 0 offen sc1 lds
ds_read_b32 v76, v82 offset:0
s_waitcnt lgkmcnt(0)
v_cmp_gt_i32 vcc, v85, v76
s_cbranch_vccnz CR0_2
BR0_2:
s_add_u32 s49, s49, 346112
s_barrier
s_add_u32 m0, s50, 32768
s_nop 0
D 83,0
D 83,1024
D 83,2048
D 83,3072
s_mov_b32 m0, 142336
v_add_u32 v85, 1, v85
buffer_load_dword v84, s[36:39], 0 offen sc1 lds
buffer_load_dwordx4 v[124:127], v90, s[36:39], s49 offen sc1
buffer_load_dwordx4 v[128:131], v90, s[36:39], s49 offen offset:1024 sc1
buffer_load_dword v84, s[36:39], 0 offen sc1 lds
ds_read_b32 v76, v82 offset:0
s_xor_b32 s50, s50, 0x10000
s_waitcnt lgkmcnt(0)
v_cmp_gt_i32 vcc, v85, v76
s_waitcnt vmcnt(10)
s_cbranch_vccnz CR0_3
BR0_3:
s_add_u32 s49, s49, 346112
s_barrier
R 32,0
buffer_load_dwordx4 v[132:135], v86, s[36:39], s49 offen sc1
R 36,1024
buffer_load_dwordx4 v[136:139], v86, s[36:39], s49 offen offset:1024 sc1
R 40,2048
buffer_load_dwordx4 v[140:143], v87, s[36:39], s49 offen sc1
s_waitcnt lgkmcnt(1)
v_mfma_f32_16x16x32_bf16 v[0:3], a[0:3], v[32:35], v[100:103]
R 44,3072
v_mfma_f32_16x16x32_bf16 v[4:7], a[4:7], v[32:35], v[104:107]
v_mfma_f32_16x16x32_bf16 v[8:11], a[8:11], v[32:35], v[108:111]
buffer_load_dwordx4 v[144:147], v87, s[36:39], s49 offen offset:1024 sc1
v_mfma_f32_16x16x32_bf16 v[12:15], a[12:15], v[32:35], v[112:115]
mav 0,16,36
R 48,4096
mav 4,20,36
mav 8,24,36
mav 12,28,36
buffer_load_dwordx4 v[148:151], v88, s[36:39], s49 offen sc1
s_waitcnt lgkmcnt(1)
mav 0,32,40
R 52,5120
mav 4,36,40
mav 8,40,40
mav 12,44,40
buffer_load_dwordx4 v[152:155], v88, s[36:39], s49 offen offset:1024 sc1
mav 0,48,44
R 56,6144
mav 4,52,44
mav 8,56,44
mav 12,60,44
s_waitcnt lgkmcnt(1)
mav 0,64,48
R 60,7168
buffer_load_dwordx4 v[156:159], v89, s[36:39], s49 offen sc1
mav 4,68,48
mav 8,72,48
mav 12,76,48
mav 0,80,52
R 32,8192
buffer_load_dwordx4 v[160:163], v89, s[36:39], s49 offen offset:1024 sc1
mav 4,84,52
mav 8,88,52
mav 12,92,52
s_add_u32 m0, s50, 0
s_waitcnt lgkmcnt(1)
mav 0,96,56
R 36,9216
mav 4,100,56
D 83,0
mav 8,104,56
mav 12,108,56
mav 0,112,60
R 40,10240
mav 4,116,60
D 83,1024
mav 8,120,60
mav 12,124,60
s_waitcnt lgkmcnt(1)
mav 0,128,32
R 44,11264
mav 4,132,32
mav 8,136,32
D 83,2048
mav 12,140,32
mav 0,144,36
R 48,12288
mav 4,148,36
mav 8,152,36
D 83,3072
mav 12,156,36
s_waitcnt lgkmcnt(1)
mav 0,160,40
R 52,13312
s_mov_b32 m0, 142336
mav 4,164,40
v_add_u32 v85, 1, v85
mav 8,168,40
buffer_load_dword v84, s[36:39], 0 offen sc1 lds
mav 12,172,40
buffer_load_dwordx4 v[164:167], v90, s[36:39], s49 offen sc1
mav 0,176,44
R 56,14336
mav 4,180,44
mav 8,184,44
mav 12,188,44
buffer_load_dwordx4 v[168:171], v90, s[36:39], s49 offen offset:1024 sc1
s_waitcnt lgkmcnt(1)
mav 0,192,48
R 60,15360
mav 4,196,48
mav 8,200,48
mav 12,204,48
mav 0,208,52
mav 4,212,52
mav 8,216,52
buffer_load_dword v84, s[36:39], 0 offen sc1 lds
mav 12,220,52
s_waitcnt lgkmcnt(0)
mav 0,224,56
mav 4,228,56
mav 8,232,56
mav 12,236,56
mav 0,240,60
mav 4,244,60
ds_read_b32 v76, v82 offset:0
mav 8,248,60
mav 12,252,60
s_waitcnt vmcnt(18)
mvv 0,116,124
mvv 4,120,124
s_waitcnt vmcnt(17)
mvv 8,116,128
mvv 12,120,128
s_waitcnt vmcnt(15)
mvv 0,116,132
mvv 4,120,132
s_waitcnt lgkmcnt(0)
v_cmp_gt_i32 vcc, v85, v76
s_waitcnt vmcnt(14)
mvv 8,116,136
s_cbranch_vccnz CR0_4
BR0_4:
s_add_u32 s49, s49, 346112
mvv 12,120,136
s_barrier
s_waitcnt vmcnt(13)
mvv 0,116,140
R 32,32768
mvv 4,120,140
buffer_load_dwordx4 v[172:175], v86, s[36:39], s49 offen sc1
s_waitcnt vmcnt(13)
mvv 8,116,144
mvv 12,120,144
s_waitcnt vmcnt(12)
mvv 0,116,148
R 36,33792
mvv 4,120,148
buffer_load_dwordx4 v[176:179], v86, s[36:39], s49 offen offset:1024 sc1
s_waitcnt vmcnt(12)
mvv 8,116,152
mvv 12,120,152
s_waitcnt vmcnt(11)
mvv 0,116,156
R 40,34816
mvv 4,120,156
s_waitcnt vmcnt(10)
mvv 8,116,160
buffer_load_dwordx4 v[180:183], v87, s[36:39], s49 offen sc1
mvv 12,120,160
s_waitcnt lgkmcnt(1)
v_mfma_f32_16x16x32_bf16 v[16:19], a[0:3], v[32:35], v[100:103]
R 44,35840
v_cvt_pk_bf16_f32 v64, v0, v1
v_mfma_f32_16x16x32_bf16 v[20:23], a[4:7], v[32:35], v[104:107]
v_cvt_pk_bf16_f32 v65, v2, v3
v_cvt_pk_bf16_f32 v66, v4, v5
v_mfma_f32_16x16x32_bf16 v[24:27], a[8:11], v[32:35], v[108:111]
buffer_load_dwordx4 v[184:187], v87, s[36:39], s49 offen offset:1024 sc1
v_cvt_pk_bf16_f32 v67, v6, v7
v_mfma_f32_16x16x32_bf16 v[28:31], a[12:15], v[32:35], v[112:115]
v_cvt_pk_bf16_f32 v68, v8, v9
v_cvt_pk_bf16_f32 v69, v10, v11
mav 16,16,36
R 48,36864
v_cvt_pk_bf16_f32 v70, v12, v13
mav 20,20,36
v_cvt_pk_bf16_f32 v71, v14, v15
v_mul_f32 v75, v0, v0
mav 24,24,36
buffer_store_dwordx4 v[64:67], v91, s[36:39], s49 offen sc1
v_add_f32 v74, v1, v0
mav 28,28,36
v_fmac_f32 v75, v1, v1
s_waitcnt lgkmcnt(1)
mav 16,32,40
R 52,37888
v_add_f32 v74, v2, v74
mav 20,36,40
v_fmac_f32 v75, v2, v2
v_add_f32 v74, v3, v74
mav 24,40,40
buffer_store_dwordx4 v[68:71], v91, s[36:39], s49 offen offset:1024 sc1
v_fmac_f32 v75, v3, v3
mav 28,44,40
v_add_f32 v74, v4, v74
v_fmac_f32 v75, v4, v4
mav 16,48,44
R 56,38912
v_add_f32 v74, v5, v74
mav 20,52,44
v_fmac_f32 v75, v5, v5
v_add_f32 v74, v6, v74
mav 24,56,44
buffer_load_dwordx4 v[188:191], v88, s[36:39], s49 offen sc1
v_fmac_f32 v75, v6, v6
mav 28,60,44
v_add_f32 v74, v7, v74
s_waitcnt lgkmcnt(1)
mav 16,64,48
R 60,39936
v_fmac_f32 v75, v7, v7
mav 20,68,48
v_add_f32 v74, v8, v74
v_fmac_f32 v75, v8, v8
mav 24,72,48
buffer_load_dwordx4 v[192:195], v88, s[36:39], s49 offen offset:1024 sc1
v_add_f32 v74, v9, v74
mav 28,76,48
v_fmac_f32 v75, v9, v9
v_add_f32 v74, v10, v74
mav 16,80,52
R 32,40960
v_fmac_f32 v75, v10, v10
mav 20,84,52
v_add_f32 v74, v11, v74
v_fmac_f32 v75, v11, v11
mav 24,88,52
buffer_load_dwordx4 v[196:199], v89, s[36:39], s49 offen sc1
v_add_f32 v74, v12, v74
mav 28,92,52
s_add_u32 m0, s50, 32768
s_waitcnt lgkmcnt(1)
mav 16,96,56
R 36,41984
v_fmac_f32 v75, v12, v12
mav 20,100,56
v_add_f32 v74, v13, v74
v_fmac_f32 v75, v13, v13
mav 24,104,56
buffer_load_dwordx4 v[200:203], v89, s[36:39], s49 offen offset:1024 sc1
v_add_f32 v74, v14, v74
mav 28,108,56
v_fmac_f32 v75, v14, v14
v_add_f32 v74, v15, v74
mav 16,112,60
R 40,43008
v_fmac_f32 v75, v15, v15
mav 20,116,60
mav 24,120,60
D 83,0
v_permlane32_swap_b32 v74, v75
mav 28,124,60
v_add_f32 v74, v74, v75
s_waitcnt lgkmcnt(1)
mav 16,128,32
R 44,44032
mav 20,132,32
mav 24,136,32
D 83,1024
mav 28,140,32
mav 16,144,36
R 48,45056
mav 20,148,36
mav 24,152,36
D 83,2048
mav 28,156,36
s_waitcnt lgkmcnt(1)
mav 16,160,40
R 52,46080
v_add_u32 v85, 1, v85
mav 20,164,40
mav 24,168,40
D 83,3072
s_mov_b32 m0, 142336
mav 28,172,40
buffer_load_dword v84, s[36:39], 0 offen sc1 lds
mav 16,176,44
R 56,47104
mav 20,180,44
mav 24,184,44
buffer_load_dwordx4 v[124:127], v90, s[36:39], s49 offen sc1
mav 28,188,44
s_waitcnt lgkmcnt(1)
mav 16,192,48
R 60,48128
mav 20,196,48
mav 24,200,48
buffer_load_dwordx4 v[128:131], v90, s[36:39], s49 offen offset:1024 sc1
mav 28,204,48
mav 16,208,52
mav 20,212,52
mav 24,216,52
buffer_load_dword v84, s[36:39], 0 offen sc1 lds
v_add_u32 v72, 1, v72
mav 28,220,52
s_waitcnt lgkmcnt(0)
mav 16,224,56
mav 20,228,56
mav 24,232,56
mav 28,236,56
mav 16,240,60
s_waitcnt vmcnt(12)
buffer_store_dwordx2 v[72:73], v81, s[36:39], s52 offen sc1
mav 20,244,60
ds_read_b32 v76, v82 offset:0
mav 24,248,60
ds_write_b32 v97, v74 offset:0
mav 28,252,60
mvv 16,116,164
mvv 20,120,164
mvv 24,116,168
s_xor_b32 s50, s50, 0x10000
mvv 28,120,168
mvv 16,116,172
mvv 20,120,172
v_xor_b32 v80, 0x10000, v80
mvv 24,116,176
s_waitcnt lgkmcnt(0)
v_cmp_gt_i32 vcc, v85, v76
mvv 28,120,176
s_cbranch_vccnz CR0_5
BR0_5:
s_barrier
mvv 16,116,180
R 32,0
s_add_u32 s49, s49, 346112
mvv 20,120,180
buffer_load_dwordx4 v[132:135], v86, s[36:39], s49 offen sc1
mvv 24,116,184
mvv 28,120,184
s_waitcnt vmcnt(13)
mvv 16,116,188
R 36,1024
mvv 20,120,188
buffer_load_dwordx4 v[136:139], v86, s[36:39], s49 offen offset:1024 sc1
s_waitcnt vmcnt(13)
mvv 24,116,192
mvv 28,120,192
s_waitcnt vmcnt(12)
mvv 16,116,196
R 40,2048
mvv 20,120,196
s_waitcnt vmcnt(11)
mvv 24,116,200
buffer_load_dwordx4 v[140:143], v87, s[36:39], s49 offen sc1
mvv 28,120,200
s_mov_b32 s60, -15
.p2align 6
LR0_6:
s_waitcnt lgkmcnt(1)
v_mfma_f32_16x16x32_bf16 v[0:3], a[0:3], v[32:35], v[100:103]
R 44,3072
v_cvt_pk_bf16_f32 v64, v16, v17
v_mfma_f32_16x16x32_bf16 v[4:7], a[4:7], v[32:35], v[104:107]
v_cvt_pk_bf16_f32 v65, v18, v19
v_cvt_pk_bf16_f32 v66, v20, v21
v_mfma_f32_16x16x32_bf16 v[8:11], a[8:11], v[32:35], v[108:111]
buffer_load_dwordx4 v[144:147], v87, s[36:39], s49 offen offset:1024 sc1
v_cvt_pk_bf16_f32 v67, v22, v23
v_mfma_f32_16x16x32_bf16 v[12:15], a[12:15], v[32:35], v[112:115]
v_cvt_pk_bf16_f32 v68, v24, v25
v_cvt_pk_bf16_f32 v69, v26, v27
mav 0,16,36
R 48,4096
v_cvt_pk_bf16_f32 v70, v28, v29
mav 4,20,36
v_cvt_pk_bf16_f32 v71, v30, v31
v_mul_f32 v75, v16, v16
mav 8,24,36
buffer_store_dwordx4 v[64:67], v91, s[36:39], s49 offen sc1
v_add_f32 v74, v17, v16
mav 12,28,36
v_fmac_f32 v75, v17, v17
s_waitcnt lgkmcnt(1)
mav 0,32,40
R 52,5120
v_add_f32 v74, v18, v74
mav 4,36,40
v_fmac_f32 v75, v18, v18
v_add_f32 v74, v19, v74
mav 8,40,40
buffer_store_dwordx4 v[68:71], v91, s[36:39], s49 offen offset:1024 sc1
v_fmac_f32 v75, v19, v19
mav 12,44,40
v_add_f32 v74, v20, v74
v_fmac_f32 v75, v20, v20
mav 0,48,44
R 56,6144
v_add_f32 v74, v21, v74
mav 4,52,44
v_fmac_f32 v75, v21, v21
v_add_f32 v74, v22, v74
mav 8,56,44
buffer_load_dwordx4 v[148:151], v88, s[36:39], s49 offen sc1
v_fmac_f32 v75, v22, v22
mav 12,60,44
v_add_f32 v74, v23, v74
s_waitcnt lgkmcnt(1)
mav 0,64,48
R 60,7168
v_fmac_f32 v75, v23, v23
mav 4,68,48
v_add_f32 v74, v24, v74
v_fmac_f32 v75, v24, v24
mav 8,72,48
buffer_load_dwordx4 v[152:155], v88, s[36:39], s49 offen offset:1024 sc1
v_add_f32 v74, v25, v74
mav 12,76,48
v_fmac_f32 v75, v25, v25
v_add_f32 v74, v26, v74
mav 0,80,52
R 32,8192
v_fmac_f32 v75, v26, v26
mav 4,84,52
v_add_f32 v74, v27, v74
v_fmac_f32 v75, v27, v27
mav 8,88,52
buffer_load_dwordx4 v[156:159], v89, s[36:39], s49 offen sc1
v_add_f32 v74, v28, v74
mav 12,92,52
s_add_u32 m0, s50, 0
s_waitcnt lgkmcnt(1)
mav 0,96,56
R 36,9216
v_fmac_f32 v75, v28, v28
mav 4,100,56
v_add_f32 v74, v29, v74
v_fmac_f32 v75, v29, v29
mav 8,104,56
buffer_load_dwordx4 v[160:163], v89, s[36:39], s49 offen offset:1024 sc1
v_add_f32 v74, v30, v74
mav 12,108,56
v_fmac_f32 v75, v30, v30
v_add_f32 v74, v31, v74
mav 0,112,60
R 40,10240
v_fmac_f32 v75, v31, v31
mav 4,116,60
mav 8,120,60
D 83,0
v_permlane32_swap_b32 v74, v75
mav 12,124,60
v_add_f32 v74, v74, v75
s_waitcnt lgkmcnt(1)
mav 0,128,32
R 44,11264
mav 4,132,32
mav 8,136,32
D 83,1024
mav 12,140,32
mav 0,144,36
R 48,12288
mav 4,148,36
mav 8,152,36
D 83,2048
mav 12,156,36
s_waitcnt lgkmcnt(1)
mav 0,160,40
R 52,13312
v_add_u32 v85, 1, v85
mav 4,164,40
mav 8,168,40
D 83,3072
s_mov_b32 m0, 142336
mav 12,172,40
buffer_load_dword v84, s[36:39], 0 offen sc1 lds
mav 0,176,44
R 56,14336
mav 4,180,44
mav 8,184,44
buffer_load_dwordx4 v[164:167], v90, s[36:39], s49 offen sc1
mav 12,188,44
s_waitcnt lgkmcnt(1)
mav 0,192,48
R 60,15360
mav 4,196,48
mav 8,200,48
buffer_load_dwordx4 v[168:171], v90, s[36:39], s49 offen offset:1024 sc1
mav 12,204,48
mav 0,208,52
mav 4,212,52
mav 8,216,52
buffer_load_dword v84, s[36:39], 0 offen sc1 lds
v_add_u32 v72, 1, v72
mav 12,220,52
s_waitcnt lgkmcnt(0)
mav 0,224,56
mav 4,228,56
mav 8,232,56
mav 12,236,56
mav 0,240,60
s_waitcnt vmcnt(12)
buffer_store_dwordx2 v[72:73], v81, s[36:39], s52 offen sc1
mav 4,244,60
ds_read_b32 v76, v82 offset:0
mav 8,248,60
ds_write_b32 v97, v74 offset:1024
mav 12,252,60
mvv 0,116,124
mvv 4,120,124
mvv 8,116,128
mvv 12,120,128
mvv 0,116,132
mvv 4,120,132
s_waitcnt lgkmcnt(0)
v_cmp_gt_i32 vcc, v85, v76
mvv 8,116,136
s_cbranch_vccnz CR0_7
BR0_7:
s_add_u32 s49, s49, 346112
mvv 12,120,136
s_barrier
mvv 0,116,140
R 32,32768
mvv 4,120,140
buffer_load_dwordx4 v[172:175], v86, s[36:39], s49 offen sc1
mvv 8,116,144
mvv 12,120,144
s_waitcnt vmcnt(13)
mvv 0,116,148
R 36,33792
mvv 4,120,148
buffer_load_dwordx4 v[176:179], v86, s[36:39], s49 offen offset:1024 sc1
s_waitcnt vmcnt(13)
mvv 8,116,152
mvv 12,120,152
s_waitcnt vmcnt(12)
mvv 0,116,156
R 40,34816
mvv 4,120,156
s_waitcnt vmcnt(11)
mvv 8,116,160
buffer_load_dwordx4 v[180:183], v87, s[36:39], s49 offen sc1
mvv 12,120,160
s_waitcnt lgkmcnt(1)
v_mfma_f32_16x16x32_bf16 v[16:19], a[0:3], v[32:35], v[100:103]
R 44,35840
v_cvt_pk_bf16_f32 v64, v0, v1
v_mfma_f32_16x16x32_bf16 v[20:23], a[4:7], v[32:35], v[104:107]
v_cvt_pk_bf16_f32 v65, v2, v3
v_cvt_pk_bf16_f32 v66, v4, v5
v_mfma_f32_16x16x32_bf16 v[24:27], a[8:11], v[32:35], v[108:111]
buffer_load_dwordx4 v[184:187], v87, s[36:39], s49 offen offset:1024 sc1
v_cvt_pk_bf16_f32 v67, v6, v7
v_mfma_f32_16x16x32_bf16 v[28:31], a[12:15], v[32:35], v[112:115]
v_cvt_pk_bf16_f32 v68, v8, v9
v_cvt_pk_bf16_f32 v69, v10, v11
mav 16,16,36
R 48,36864
v_cvt_pk_bf16_f32 v70, v12, v13
mav 20,20,36
v_cvt_pk_bf16_f32 v71, v14, v15
v_mul_f32 v75, v0, v0
mav 24,24,36
buffer_store_dwordx4 v[64:67], v91, s[36:39], s49 offen sc1
v_add_f32 v74, v1, v0
mav 28,28,36
v_fmac_f32 v75, v1, v1
s_waitcnt lgkmcnt(1)
mav 16,32,40
R 52,37888
v_add_f32 v74, v2, v74
mav 20,36,40
v_fmac_f32 v75, v2, v2
v_add_f32 v74, v3, v74
mav 24,40,40
buffer_store_dwordx4 v[68:71], v91, s[36:39], s49 offen offset:1024 sc1
v_fmac_f32 v75, v3, v3
mav 28,44,40
v_add_f32 v74, v4, v74
v_fmac_f32 v75, v4, v4
mav 16,48,44
R 56,38912
v_add_f32 v74, v5, v74
mav 20,52,44
v_fmac_f32 v75, v5, v5
v_add_f32 v74, v6, v74
mav 24,56,44
buffer_load_dwordx4 v[188:191], v88, s[36:39], s49 offen sc1
v_fmac_f32 v75, v6, v6
mav 28,60,44
v_add_f32 v74, v7, v74
s_waitcnt lgkmcnt(1)
mav 16,64,48
R 60,39936
v_fmac_f32 v75, v7, v7
mav 20,68,48
v_add_f32 v74, v8, v74
v_fmac_f32 v75, v8, v8
mav 24,72,48
buffer_load_dwordx4 v[192:195], v88, s[36:39], s49 offen offset:1024 sc1
v_add_f32 v74, v9, v74
mav 28,76,48
v_fmac_f32 v75, v9, v9
v_add_f32 v74, v10, v74
mav 16,80,52
R 32,40960
v_fmac_f32 v75, v10, v10
mav 20,84,52
v_add_f32 v74, v11, v74
v_fmac_f32 v75, v11, v11
mav 24,88,52
buffer_load_dwordx4 v[196:199], v89, s[36:39], s49 offen sc1
v_add_f32 v74, v12, v74
mav 28,92,52
s_add_u32 m0, s50, 32768
s_waitcnt lgkmcnt(1)
mav 16,96,56
R 36,41984
v_fmac_f32 v75, v12, v12
mav 20,100,56
v_add_f32 v74, v13, v74
v_fmac_f32 v75, v13, v13
mav 24,104,56
buffer_load_dwordx4 v[200:203], v89, s[36:39], s49 offen offset:1024 sc1
v_add_f32 v74, v14, v74
mav 28,108,56
v_fmac_f32 v75, v14, v14
v_add_f32 v74, v15, v74
mav 16,112,60
R 40,43008
v_fmac_f32 v75, v15, v15
mav 20,116,60
mav 24,120,60
D 83,0
v_permlane32_swap_b32 v74, v75
mav 28,124,60
v_add_f32 v74, v74, v75
s_waitcnt lgkmcnt(1)
mav 16,128,32
R 44,44032
mav 20,132,32
mav 24,136,32
D 83,1024
mav 28,140,32
mav 16,144,36
R 48,45056
mav 20,148,36
mav 24,152,36
D 83,2048
mav 28,156,36
s_waitcnt lgkmcnt(1)
mav 16,160,40
R 52,46080
v_add_u32 v85, 1, v85
mav 20,164,40
mav 24,168,40
D 83,3072
s_mov_b32 m0, 142336
mav 28,172,40
buffer_load_dword v84, s[36:39], 0 offen sc1 lds
mav 16,176,44
R 56,47104
mav 20,180,44
mav 24,184,44
buffer_load_dwordx4 v[124:127], v90, s[36:39], s49 offen sc1
mav 28,188,44
s_waitcnt lgkmcnt(1)
mav 16,192,48
R 60,48128
mav 20,196,48
mav 24,200,48
buffer_load_dwordx4 v[128:131], v90, s[36:39], s49 offen offset:1024 sc1
mav 28,204,48
mav 16,208,52
mav 20,212,52
mav 24,216,52
buffer_load_dword v84, s[36:39], 0 offen sc1 lds
v_add_u32 v72, 1, v72
mav 28,220,52
s_waitcnt lgkmcnt(0)
mav 16,224,56
mav 20,228,56
mav 24,232,56
mav 28,236,56
mav 16,240,60
s_waitcnt vmcnt(12)
buffer_store_dwordx2 v[72:73], v81, s[36:39], s52 offen sc1
mav 20,244,60
ds_read_b32 v76, v82 offset:0
mav 24,248,60
ds_write_b32 v97, v74 offset:0
mav 28,252,60
mvv 16,116,164
mvv 20,120,164
mvv 24,116,168
s_xor_b32 s50, s50, 0x10000
mvv 28,120,168
mvv 16,116,172
mvv 20,120,172
v_xor_b32 v80, 0x10000, v80
mvv 24,116,176
s_waitcnt lgkmcnt(0)
v_cmp_gt_i32 vcc, v85, v76
mvv 28,120,176
s_cbranch_vccnz CR0_8
BR0_8:
s_barrier
mvv 16,116,180
R 32,0
s_add_u32 s49, s49, 346112
mvv 20,120,180
buffer_load_dwordx4 v[132:135], v86, s[36:39], s49 offen sc1
mvv 24,116,184
mvv 28,120,184
s_waitcnt vmcnt(13)
mvv 16,116,188
R 36,1024
mvv 20,120,188
buffer_load_dwordx4 v[136:139], v86, s[36:39], s49 offen offset:1024 sc1
s_waitcnt vmcnt(13)
mvv 24,116,192
mvv 28,120,192
s_waitcnt vmcnt(12)
mvv 16,116,196
R 40,2048
mvv 20,120,196
s_waitcnt vmcnt(11)
mvv 24,116,200
buffer_load_dwordx4 v[140:143], v87, s[36:39], s49 offen sc1
mvv 28,120,200
s_add_u32 s60, s60, 1
s_cbranch_scc0 LR0_6
s_waitcnt lgkmcnt(1)
v_mfma_f32_16x16x32_bf16 v[0:3], a[0:3], v[32:35], v[100:103]
R 44,3072
v_cvt_pk_bf16_f32 v64, v16, v17
v_mfma_f32_16x16x32_bf16 v[4:7], a[4:7], v[32:35], v[104:107]
v_cvt_pk_bf16_f32 v65, v18, v19
v_cvt_pk_bf16_f32 v66, v20, v21
v_mfma_f32_16x16x32_bf16 v[8:11], a[8:11], v[32:35], v[108:111]
buffer_load_dwordx4 v[144:147], v87, s[36:39], s49 offen offset:1024 sc1
v_cvt_pk_bf16_f32 v67, v22, v23
v_mfma_f32_16x16x32_bf16 v[12:15], a[12:15], v[32:35], v[112:115]
v_cvt_pk_bf16_f32 v68, v24, v25
v_cvt_pk_bf16_f32 v69, v26, v27
mav 0,16,36
R 48,4096
v_cvt_pk_bf16_f32 v70, v28, v29
mav 4,20,36
v_cvt_pk_bf16_f32 v71, v30, v31
v_mul_f32 v75, v16, v16
mav 8,24,36
buffer_store_dwordx4 v[64:67], v91, s[36:39], s49 offen sc1
v_add_f32 v74, v17, v16
mav 12,28,36
v_fmac_f32 v75, v17, v17
s_waitcnt lgkmcnt(1)
mav 0,32,40
R 52,5120
v_add_f32 v74, v18, v74
mav 4,36,40
v_fmac_f32 v75, v18, v18
v_add_f32 v74, v19, v74
mav 8,40,40
buffer_store_dwordx4 v[68:71], v91, s[36:39], s49 offen offset:1024 sc1
v_fmac_f32 v75, v19, v19
mav 12,44,40
v_add_f32 v74, v20, v74
v_fmac_f32 v75, v20, v20
mav 0,48,44
R 56,6144
v_add_f32 v74, v21, v74
mav 4,52,44
v_fmac_f32 v75, v21, v21
v_add_f32 v74, v22, v74
mav 8,56,44
buffer_load_dwordx4 v[148:151], v88, s[36:39], s49 offen sc1
v_fmac_f32 v75, v22, v22
mav 12,60,44
v_add_f32 v74, v23, v74
s_waitcnt lgkmcnt(1)
mav 0,64,48
R 60,7168
v_fmac_f32 v75, v23, v23
mav 4,68,48
v_add_f32 v74, v24, v74
v_fmac_f32 v75, v24, v24
mav 8,72,48
buffer_load_dwordx4 v[152:155], v88, s[36:39], s49 offen offset:1024 sc1
v_add_f32 v74, v25, v74
mav 12,76,48
v_fmac_f32 v75, v25, v25
v_add_f32 v74, v26, v74
mav 0,80,52
R 32,8192
v_fmac_f32 v75, v26, v26
mav 4,84,52
v_add_f32 v74, v27, v74
v_fmac_f32 v75, v27, v27
mav 8,88,52
buffer_load_dwordx4 v[156:159], v89, s[36:39], s49 offen sc1
v_add_f32 v74, v28, v74
mav 12,92,52
s_add_u32 m0, s50, 0
s_waitcnt lgkmcnt(1)
mav 0,96,56
R 36,9216
v_fmac_f32 v75, v28, v28
mav 4,100,56
v_add_f32 v74, v29, v74
v_fmac_f32 v75, v29, v29
mav 8,104,56
buffer_load_dwordx4 v[160:163], v89, s[36:39], s49 offen offset:1024 sc1
v_add_f32 v74, v30, v74
mav 12,108,56
v_fmac_f32 v75, v30, v30
v_add_f32 v74, v31, v74
mav 0,112,60
R 40,10240
v_fmac_f32 v75, v31, v31
mav 4,116,60
mav 8,120,60
D 83,0
v_permlane32_swap_b32 v74, v75
mav 12,124,60
v_add_f32 v74, v74, v75
s_waitcnt lgkmcnt(1)
mav 0,128,32
R 44,11264
mav 4,132,32
mav 8,136,32
D 83,1024
mav 12,140,32
mav 0,144,36
R 48,12288
mav 4,148,36
mav 8,152,36
D 83,2048
mav 12,156,36
s_waitcnt lgkmcnt(1)
mav 0,160,40
R 52,13312
v_add_u32 v85, 1, v85
mav 4,164,40
mav 8,168,40
D 83,3072
s_mov_b32 m0, 142336
mav 12,172,40
buffer_load_dword v84, s[36:39], 0 offen sc1 lds
mav 0,176,44
R 56,14336
mav 4,180,44
mav 8,184,44
buffer_load_dwordx4 v[164:167], v90, s[36:39], s49 offen sc1
mav 12,188,44
s_waitcnt lgkmcnt(1)
mav 0,192,48
R 60,15360
mav 4,196,48
mav 8,200,48
buffer_load_dwordx4 v[168:171], v90, s[36:39], s49 offen offset:1024 sc1
mav 12,204,48
mav 0,208,52
mav 4,212,52
mav 8,216,52
buffer_load_dword v84, s[36:39], 0 offen sc1 lds
v_add_u32 v72, 69, v72
mav 12,220,52
s_waitcnt lgkmcnt(0)
mav 0,224,56
mav 4,228,56
mav 8,232,56
mav 12,236,56
mav 0,240,60
s_waitcnt vmcnt(12)
buffer_store_dwordx2 v[72:73], v81, s[36:39], s52 offen sc1
mav 4,244,60
ds_read_b32 v76, v82 offset:0
mav 8,248,60
ds_write_b32 v97, v74 offset:1024
mav 12,252,60
mvv 0,116,124
mvv 4,120,124
mvv 8,116,128
mvv 12,120,128
mvv 0,116,132
mvv 4,120,132
s_waitcnt lgkmcnt(0)
v_cmp_gt_i32 vcc, v85, v76
mvv 8,116,136
s_cbranch_vccnz CR0_9
BR0_9:
s_add_u32 s49, s49, 346112
mvv 12,120,136
s_barrier
mvv 0,116,140
R 32,32768
mvv 4,120,140
buffer_load_dwordx4 v[172:175], v86, s[36:39], s49 offen sc1
mvv 8,116,144
mvv 12,120,144
s_waitcnt vmcnt(13)
mvv 0,116,148
R 36,33792
mvv 4,120,148
buffer_load_dwordx4 v[176:179], v86, s[36:39], s49 offen offset:1024 sc1
s_waitcnt vmcnt(13)
mvv 8,116,152
mvv 12,120,152
s_waitcnt vmcnt(12)
mvv 0,116,156
R 40,34816
mvv 4,120,156
s_waitcnt vmcnt(11)
mvv 8,116,160
buffer_load_dwordx4 v[180:183], v87, s[36:39], s49 offen sc1
mvv 12,120,160
s_waitcnt lgkmcnt(1)
v_mfma_f32_16x16x32_bf16 v[16:19], a[0:3], v[32:35], v[100:103]
R 44,35840
v_cvt_pk_bf16_f32 v64, v0, v1
v_mfma_f32_16x16x32_bf16 v[20:23], a[4:7], v[32:35], v[104:107]
v_cvt_pk_bf16_f32 v65, v2, v3
v_cvt_pk_bf16_f32 v66, v4, v5
v_mfma_f32_16x16x32_bf16 v[24:27], a[8:11], v[32:35], v[108:111]
buffer_load_dwordx4 v[184:187], v87, s[36:39], s49 offen offset:1024 sc1
v_cvt_pk_bf16_f32 v67, v6, v7
v_mfma_f32_16x16x32_bf16 v[28:31], a[12:15], v[32:35], v[112:115]
ds_write_b128 v94, v[64:67] offset:0
mav 16,16,36
R 48,36864
v_cvt_pk_bf16_f32 v68, v8, v9
mav 20,20,36
v_cvt_pk_bf16_f32 v69, v10, v11
v_cvt_pk_bf16_f32 v70, v12, v13
mav 24,24,36
v_cvt_pk_bf16_f32 v71, v14, v15
mav 28,28,36
buffer_load_dwordx4 v[188:191], v88, s[36:39], s49 offen sc1
s_waitcnt lgkmcnt(2)
mav 16,32,40
R 52,37888
mav 20,36,40
ds_write_b128 v95, v[68:71] offset:0
mav 24,40,40
mav 28,44,40
buffer_load_dwordx4 v[192:195], v88, s[36:39], s49 offen offset:1024 sc1
mav 16,48,44
R 56,38912
mav 20,52,44
s_waitcnt lgkmcnt(1)
ds_read_b128 v[64:67], v96 offset:0
mav 24,56,44
ds_read_b128 v[68:71], v96 offset:1024
mav 28,60,44
mav 16,64,48
R 60,39936
buffer_load_dwordx4 v[196:199], v89, s[36:39], s49 offen sc1
mav 20,68,48
mav 24,72,48
mav 28,76,48
mav 16,80,52
R 32,40960
buffer_load_dwordx4 v[200:203], v89, s[36:39], s49 offen offset:1024 sc1
mav 20,84,52
mav 24,88,52
mav 28,92,52
s_add_u32 m0, s50, 32768
s_waitcnt lgkmcnt(1)
mav 16,96,56
R 36,41984
buffer_store_dwordx4 v[64:67], v92, s[44:47], s54 offen
mav 20,100,56
mav 24,104,56
mav 28,108,56
mav 16,112,60
R 40,43008
buffer_store_dwordx4 v[68:71], v93, s[44:47], s54 offen
mav 20,116,60
mav 24,120,60
mav 28,124,60
s_waitcnt lgkmcnt(1)
mav 16,128,32
R 44,44032
D 83,0
mav 20,132,32
mav 24,136,32
mav 28,140,32
mav 16,144,36
R 48,45056
D 83,1024
mav 20,148,36
mav 24,152,36
mav 28,156,36
s_waitcnt lgkmcnt(1)
mav 16,160,40
R 52,46080
D 83,2048
mav 20,164,40
s_sub_u32 s54, s54, 0x8000
v_add_u32 v85, 1, v85
mav 24,168,40
mav 28,172,40
mav 16,176,44
R 56,47104
D 83,3072
mav 20,180,44
s_mov_b32 m0, 142336
mav 24,184,44
buffer_load_dword v84, s[36:39], 0 offen sc1 lds
mav 28,188,44
s_waitcnt lgkmcnt(1)
mav 16,192,48
R 60,48128
buffer_load_dwordx4 v[124:127], v90, s[36:39], s49 offen sc1
mav 20,196,48
mav 24,200,48
mav 28,204,48
mav 16,208,52
buffer_load_dwordx4 v[128:131], v90, s[36:39], s49 offen offset:1024 sc1
mav 20,212,52
mav 24,216,52
buffer_load_dword v84, s[36:39], 0 offen sc1 lds
mav 28,220,52
s_waitcnt lgkmcnt(0)
mav 16,224,56
mav 20,228,56
mav 24,232,56
mav 28,236,56
mav 16,240,60
mav 20,244,60
ds_read_b32 v76, v82 offset:0
mav 24,248,60
mav 28,252,60
s_waitcnt vmcnt(21)
mvv 16,116,164
mvv 20,120,164
s_waitcnt vmcnt(20)
mvv 24,116,168
s_xor_b32 s50, s50, 0x10000
mvv 28,120,168
s_waitcnt vmcnt(17)
mvv 16,116,172
mvv 20,120,172
v_xor_b32 v80, 0x10000, v80
s_waitcnt vmcnt(16)
mvv 24,116,176
s_waitcnt lgkmcnt(0)
v_cmp_gt_i32 vcc, v85, v76
mvv 28,120,176
s_cbranch_vccnz CR0_10
BR0_10:
s_waitcnt vmcnt(15)
mvv 16,116,180
s_barrier
R 32,0
mvv 20,120,180
s_add_u32 s49, s49, 346112
s_waitcnt vmcnt(14)
mvv 24,116,184
buffer_load_dwordx4 v[132:135], v86, s[36:39], s49 offen sc1
mvv 28,120,184
s_waitcnt vmcnt(14)
mvv 16,116,188
R 36,1024
mvv 20,120,188
s_waitcnt vmcnt(13)
mvv 24,116,192
buffer_load_dwordx4 v[136:139], v86, s[36:39], s49 offen offset:1024 sc1
mvv 28,120,192
s_waitcnt vmcnt(13)
mvv 16,116,196
R 40,2048
mvv 20,120,196
s_waitcnt vmcnt(12)
mvv 24,116,200
buffer_load_dwordx4 v[140:143], v87, s[36:39], s49 offen sc1
mvv 28,120,200
s_mov_b32 s60, -13
.p2align 6
LR0_11:
s_waitcnt lgkmcnt(1)
v_mfma_f32_16x16x32_bf16 v[0:3], a[0:3], v[32:35], v[100:103]
R 44,3072
v_cvt_pk_bf16_f32 v64, v16, v17
v_mfma_f32_16x16x32_bf16 v[4:7], a[4:7], v[32:35], v[104:107]
v_cvt_pk_bf16_f32 v65, v18, v19
v_cvt_pk_bf16_f32 v66, v20, v21
v_mfma_f32_16x16x32_bf16 v[8:11], a[8:11], v[32:35], v[108:111]
buffer_load_dwordx4 v[144:147], v87, s[36:39], s49 offen offset:1024 sc1
v_cvt_pk_bf16_f32 v67, v22, v23
v_mfma_f32_16x16x32_bf16 v[12:15], a[12:15], v[32:35], v[112:115]
ds_write_b128 v94, v[64:67] offset:0
mav 0,16,36
R 48,4096
v_cvt_pk_bf16_f32 v68, v24, v25
mav 4,20,36
v_cvt_pk_bf16_f32 v69, v26, v27
v_cvt_pk_bf16_f32 v70, v28, v29
mav 8,24,36
v_cvt_pk_bf16_f32 v71, v30, v31
mav 12,28,36
buffer_load_dwordx4 v[148:151], v88, s[36:39], s49 offen sc1
s_waitcnt lgkmcnt(2)
mav 0,32,40
R 52,5120
mav 4,36,40
ds_write_b128 v95, v[68:71] offset:0
mav 8,40,40
mav 12,44,40
buffer_load_dwordx4 v[152:155], v88, s[36:39], s49 offen offset:1024 sc1
mav 0,48,44
R 56,6144
mav 4,52,44
s_waitcnt lgkmcnt(1)
ds_read_b128 v[64:67], v96 offset:0
mav 8,56,44
ds_read_b128 v[68:71], v96 offset:1024
mav 12,60,44
mav 0,64,48
R 60,7168
buffer_load_dwordx4 v[156:159], v89, s[36:39], s49 offen sc1
mav 4,68,48
mav 8,72,48
mav 12,76,48
mav 0,80,52
R 32,8192
buffer_load_dwordx4 v[160:163], v89, s[36:39], s49 offen offset:1024 sc1
mav 4,84,52
mav 8,88,52
mav 12,92,52
s_add_u32 m0, s50, 0
s_waitcnt lgkmcnt(1)
mav 0,96,56
R 36,9216
buffer_store_dwordx4 v[64:67], v92, s[44:47], s54 offen
mav 4,100,56
mav 8,104,56
mav 12,108,56
mav 0,112,60
R 40,10240
buffer_store_dwordx4 v[68:71], v93, s[44:47], s54 offen
mav 4,116,60
mav 8,120,60
mav 12,124,60
s_waitcnt lgkmcnt(1)
mav 0,128,32
R 44,11264
D 83,0
mav 4,132,32
mav 8,136,32
mav 12,140,32
mav 0,144,36
R 48,12288
D 83,1024
mav 4,148,36
mav 8,152,36
mav 12,156,36
s_waitcnt lgkmcnt(1)
mav 0,160,40
R 52,13312
D 83,2048
mav 4,164,40
s_sub_u32 s54, s54, 0x8000
v_add_u32 v85, 1, v85
mav 8,168,40
mav 12,172,40
mav 0,176,44
R 56,14336
D 83,3072
mav 4,180,44
s_mov_b32 m0, 142336
mav 8,184,44
buffer_load_dword v84, s[36:39], 0 offen sc1 lds
mav 12,188,44
s_waitcnt lgkmcnt(1)
mav 0,192,48
R 60,15360
buffer_load_dwordx4 v[164:167], v90, s[36:39], s49 offen sc1
mav 4,196,48
mav 8,200,48
mav 12,204,48
mav 0,208,52
buffer_load_dwordx4 v[168:171], v90, s[36:39], s49 offen offset:1024 sc1
mav 4,212,52
mav 8,216,52
buffer_load_dword v84, s[36:39], 0 offen sc1 lds
mav 12,220,52
s_waitcnt lgkmcnt(0)
mav 0,224,56
mav 4,228,56
mav 8,232,56
mav 12,236,56
mav 0,240,60
mav 4,244,60
ds_read_b32 v76, v82 offset:0
mav 8,248,60
mav 12,252,60
s_waitcnt vmcnt(20)
mvv 0,116,124
mvv 4,120,124
s_waitcnt vmcnt(19)
mvv 8,116,128
mvv 12,120,128
s_waitcnt vmcnt(17)
mvv 0,116,132
mvv 4,120,132
s_waitcnt lgkmcnt(0)
v_cmp_gt_i32 vcc, v85, v76
s_waitcnt vmcnt(16)
mvv 8,116,136
s_cbranch_vccnz CR0_12
BR0_12:
s_add_u32 s49, s49, 346112
mvv 12,120,136
s_barrier
s_waitcnt vmcnt(15)
mvv 0,116,140
R 32,32768
mvv 4,120,140
buffer_load_dwordx4 v[172:175], v86, s[36:39], s49 offen sc1
s_waitcnt vmcnt(15)
mvv 8,116,144
mvv 12,120,144
s_waitcnt vmcnt(14)
mvv 0,116,148
R 36,33792
mvv 4,120,148
buffer_load_dwordx4 v[176:179], v86, s[36:39], s49 offen offset:1024 sc1
s_waitcnt vmcnt(14)
mvv 8,116,152
mvv 12,120,152
s_waitcnt vmcnt(13)
mvv 0,116,156
R 40,34816
mvv 4,120,156
s_waitcnt vmcnt(12)
mvv 8,116,160
buffer_load_dwordx4 v[180:183], v87, s[36:39], s49 offen sc1
mvv 12,120,160
s_waitcnt lgkmcnt(1)
v_mfma_f32_16x16x32_bf16 v[16:19], a[0:3], v[32:35], v[100:103]
R 44,35840
v_cvt_pk_bf16_f32 v64, v0, v1
v_mfma_f32_16x16x32_bf16 v[20:23], a[4:7], v[32:35], v[104:107]
v_cvt_pk_bf16_f32 v65, v2, v3
v_cvt_pk_bf16_f32 v66, v4, v5
v_mfma_f32_16x16x32_bf16 v[24:27], a[8:11], v[32:35], v[108:111]
buffer_load_dwordx4 v[184:187], v87, s[36:39], s49 offen offset:1024 sc1
v_cvt_pk_bf16_f32 v67, v6, v7
v_mfma_f32_16x16x32_bf16 v[28:31], a[12:15], v[32:35], v[112:115]
ds_write_b128 v94, v[64:67] offset:0
mav 16,16,36
R 48,36864
v_cvt_pk_bf16_f32 v68, v8, v9
mav 20,20,36
v_cvt_pk_bf16_f32 v69, v10, v11
v_cvt_pk_bf16_f32 v70, v12, v13
mav 24,24,36
v_cvt_pk_bf16_f32 v71, v14, v15
mav 28,28,36
buffer_load_dwordx4 v[188:191], v88, s[36:39], s49 offen sc1
s_waitcnt lgkmcnt(2)
mav 16,32,40
R 52,37888
mav 20,36,40
ds_write_b128 v95, v[68:71] offset:0
mav 24,40,40
mav 28,44,40
buffer_load_dwordx4 v[192:195], v88, s[36:39], s49 offen offset:1024 sc1
mav 16,48,44
R 56,38912
mav 20,52,44
s_waitcnt lgkmcnt(1)
ds_read_b128 v[64:67], v96 offset:0
mav 24,56,44
ds_read_b128 v[68:71], v96 offset:1024
mav 28,60,44
mav 16,64,48
R 60,39936
buffer_load_dwordx4 v[196:199], v89, s[36:39], s49 offen sc1
mav 20,68,48
mav 24,72,48
mav 28,76,48
mav 16,80,52
R 32,40960
buffer_load_dwordx4 v[200:203], v89, s[36:39], s49 offen offset:1024 sc1
mav 20,84,52
mav 24,88,52
mav 28,92,52
s_add_u32 m0, s50, 32768
s_waitcnt lgkmcnt(1)
mav 16,96,56
R 36,41984
buffer_store_dwordx4 v[64:67], v92, s[44:47], s54 offen
mav 20,100,56
mav 24,104,56
mav 28,108,56
mav 16,112,60
R 40,43008
buffer_store_dwordx4 v[68:71], v93, s[44:47], s54 offen
mav 20,116,60
mav 24,120,60
mav 28,124,60
s_waitcnt lgkmcnt(1)
mav 16,128,32
R 44,44032
D 83,0
mav 20,132,32
mav 24,136,32
mav 28,140,32
mav 16,144,36
R 48,45056
D 83,1024
mav 20,148,36
mav 24,152,36
mav 28,156,36
s_waitcnt lgkmcnt(1)
mav 16,160,40
R 52,46080
D 83,2048
mav 20,164,40
s_sub_u32 s54, s54, 0x8000
v_add_u32 v85, 1, v85
mav 24,168,40
mav 28,172,40
mav 16,176,44
R 56,47104
D 83,3072
mav 20,180,44
s_mov_b32 m0, 142336
mav 24,184,44
buffer_load_dword v84, s[36:39], 0 offen sc1 lds
mav 28,188,44
s_waitcnt lgkmcnt(1)
mav 16,192,48
R 60,48128
buffer_load_dwordx4 v[124:127], v90, s[36:39], s49 offen sc1
mav 20,196,48
mav 24,200,48
mav 28,204,48
mav 16,208,52
buffer_load_dwordx4 v[128:131], v90, s[36:39], s49 offen offset:1024 sc1
mav 20,212,52
mav 24,216,52
buffer_load_dword v84, s[36:39], 0 offen sc1 lds
mav 28,220,52
s_waitcnt lgkmcnt(0)
mav 16,224,56
mav 20,228,56
mav 24,232,56
mav 28,236,56
mav 16,240,60
mav 20,244,60
ds_read_b32 v76, v82 offset:0
mav 24,248,60
mav 28,252,60
s_waitcnt vmcnt(20)
mvv 16,116,164
mvv 20,120,164
s_waitcnt vmcnt(19)
mvv 24,116,168
s_xor_b32 s50, s50, 0x10000
mvv 28,120,168
s_waitcnt vmcnt(17)
mvv 16,116,172
mvv 20,120,172
v_xor_b32 v80, 0x10000, v80
s_waitcnt vmcnt(16)
mvv 24,116,176
s_waitcnt lgkmcnt(0)
v_cmp_gt_i32 vcc, v85, v76
mvv 28,120,176
s_cbranch_vccnz CR0_13
BR0_13:
s_waitcnt vmcnt(15)
mvv 16,116,180
s_barrier
R 32,0
mvv 20,120,180
s_add_u32 s49, s49, 346112
s_waitcnt vmcnt(14)
mvv 24,116,184
buffer_load_dwordx4 v[132:135], v86, s[36:39], s49 offen sc1
mvv 28,120,184
s_waitcnt vmcnt(14)
mvv 16,116,188
R 36,1024
mvv 20,120,188
s_waitcnt vmcnt(13)
mvv 24,116,192
buffer_load_dwordx4 v[136:139], v86, s[36:39], s49 offen offset:1024 sc1
mvv 28,120,192
s_waitcnt vmcnt(13)
mvv 16,116,196
R 40,2048
mvv 20,120,196
s_waitcnt vmcnt(12)
mvv 24,116,200
buffer_load_dwordx4 v[140:143], v87, s[36:39], s49 offen sc1
mvv 28,120,200
s_add_u32 s60, s60, 1
s_cbranch_scc0 LR0_11
s_waitcnt lgkmcnt(1)
v_mfma_f32_16x16x32_bf16 v[0:3], a[0:3], v[32:35], v[100:103]
R 44,3072
v_cvt_pk_bf16_f32 v64, v16, v17
v_mfma_f32_16x16x32_bf16 v[4:7], a[4:7], v[32:35], v[104:107]
v_cvt_pk_bf16_f32 v65, v18, v19
v_cvt_pk_bf16_f32 v66, v20, v21
v_mfma_f32_16x16x32_bf16 v[8:11], a[8:11], v[32:35], v[108:111]
buffer_load_dwordx4 v[144:147], v87, s[36:39], s49 offen offset:1024 sc1
v_cvt_pk_bf16_f32 v67, v22, v23
v_mfma_f32_16x16x32_bf16 v[12:15], a[12:15], v[32:35], v[112:115]
ds_write_b128 v94, v[64:67] offset:0
mav 0,16,36
R 48,4096
v_cvt_pk_bf16_f32 v68, v24, v25
mav 4,20,36
v_cvt_pk_bf16_f32 v69, v26, v27
v_cvt_pk_bf16_f32 v70, v28, v29
mav 8,24,36
v_cvt_pk_bf16_f32 v71, v30, v31
mav 12,28,36
buffer_load_dwordx4 v[148:151], v88, s[36:39], s49 offen sc1
s_waitcnt lgkmcnt(2)
mav 0,32,40
R 52,5120
mav 4,36,40
ds_write_b128 v95, v[68:71] offset:0
mav 8,40,40
mav 12,44,40
buffer_load_dwordx4 v[152:155], v88, s[36:39], s49 offen offset:1024 sc1
mav 0,48,44
R 56,6144
mav 4,52,44
s_waitcnt lgkmcnt(1)
ds_read_b128 v[64:67], v96 offset:0
mav 8,56,44
ds_read_b128 v[68:71], v96 offset:1024
mav 12,60,44
mav 0,64,48
R 60,7168
buffer_load_dwordx4 v[156:159], v89, s[36:39], s49 offen sc1
mav 4,68,48
mav 8,72,48
mav 12,76,48
mav 0,80,52
R 32,8192
buffer_load_dwordx4 v[160:163], v89, s[36:39], s49 offen offset:1024 sc1
mav 4,84,52
mav 8,88,52
mav 12,92,52
s_add_u32 m0, s50, 0
s_waitcnt lgkmcnt(1)
mav 0,96,56
R 36,9216
buffer_store_dwordx4 v[64:67], v92, s[44:47], s54 offen
mav 4,100,56
mav 8,104,56
mav 12,108,56
mav 0,112,60
R 40,10240
buffer_store_dwordx4 v[68:71], v93, s[44:47], s54 offen
mav 4,116,60
mav 8,120,60
mav 12,124,60
s_waitcnt lgkmcnt(1)
mav 0,128,32
R 44,11264
D 83,0
mav 4,132,32
mav 8,136,32
mav 12,140,32
mav 0,144,36
R 48,12288
D 83,1024
mav 4,148,36
mav 8,152,36
mav 12,156,36
s_waitcnt lgkmcnt(1)
mav 0,160,40
R 52,13312
D 83,2048
mav 4,164,40
s_sub_u32 s54, s54, 0x8000
v_add_u32 v85, 1, v85
mav 8,168,40
mav 12,172,40
mav 0,176,44
R 56,14336
D 83,3072
mav 4,180,44
s_mov_b32 m0, 142336
mav 8,184,44
buffer_load_dword v84, s[36:39], 0 offen sc1 lds
mav 12,188,44
s_waitcnt lgkmcnt(1)
mav 0,192,48
R 60,15360
buffer_load_dwordx4 v[164:167], v90, s[36:39], s49 offen sc1
mav 4,196,48
mav 8,200,48
mav 12,204,48
mav 0,208,52
buffer_load_dwordx4 v[168:171], v90, s[36:39], s49 offen offset:1024 sc1
mav 4,212,52
mav 8,216,52
buffer_load_dword v84, s[36:39], 0 offen sc1 lds
mav 12,220,52
s_waitcnt lgkmcnt(0)
mav 0,224,56
mav 4,228,56
mav 8,232,56
mav 12,236,56
mav 0,240,60
mav 4,244,60
ds_read_b32 v76, v82 offset:0
mav 8,248,60
mav 12,252,60
s_waitcnt vmcnt(20)
mvv 0,116,124
mvv 4,120,124
s_waitcnt vmcnt(19)
mvv 8,116,128
mvv 12,120,128
s_waitcnt vmcnt(17)
mvv 0,116,132
mvv 4,120,132
s_waitcnt lgkmcnt(0)
v_cmp_gt_i32 vcc, v85, v76
s_waitcnt vmcnt(16)
mvv 8,116,136
s_cbranch_vccnz CR0_14
BR0_14:
s_add_u32 s49, s49, 346112
mvv 12,120,136
s_barrier
s_waitcnt vmcnt(15)
mvv 0,116,140
R 32,32768
mvv 4,120,140
buffer_load_dwordx4 v[172:175], v86, s[36:39], s49 offen sc1
s_waitcnt vmcnt(15)
mvv 8,116,144
mvv 12,120,144
s_waitcnt vmcnt(14)
mvv 0,116,148
R 36,33792
mvv 4,120,148
buffer_load_dwordx4 v[176:179], v86, s[36:39], s49 offen offset:1024 sc1
s_waitcnt vmcnt(14)
mvv 8,116,152
mvv 12,120,152
s_waitcnt vmcnt(13)
mvv 0,116,156
R 40,34816
mvv 4,120,156
s_waitcnt vmcnt(12)
mvv 8,116,160
buffer_load_dwordx4 v[180:183], v87, s[36:39], s49 offen sc1
mvv 12,120,160
s_waitcnt lgkmcnt(1)
v_mfma_f32_16x16x32_bf16 v[16:19], a[0:3], v[32:35], v[100:103]
R 44,35840
v_cvt_pk_bf16_f32 v64, v0, v1
v_mfma_f32_16x16x32_bf16 v[20:23], a[4:7], v[32:35], v[104:107]
v_cvt_pk_bf16_f32 v65, v2, v3
v_cvt_pk_bf16_f32 v66, v4, v5
v_mfma_f32_16x16x32_bf16 v[24:27], a[8:11], v[32:35], v[108:111]
buffer_load_dwordx4 v[184:187], v87, s[36:39], s49 offen offset:1024 sc1
v_cvt_pk_bf16_f32 v67, v6, v7
v_mfma_f32_16x16x32_bf16 v[28:31], a[12:15], v[32:35], v[112:115]
ds_write_b128 v94, v[64:67] offset:0
mav 16,16,36
R 48,36864
v_cvt_pk_bf16_f32 v68, v8, v9
mav 20,20,36
v_cvt_pk_bf16_f32 v69, v10, v11
v_cvt_pk_bf16_f32 v70, v12, v13
mav 24,24,36
v_cvt_pk_bf16_f32 v71, v14, v15
mav 28,28,36
buffer_load_dwordx4 v[188:191], v88, s[36:39], s49 offen sc1
s_waitcnt lgkmcnt(2)
mav 16,32,40
R 52,37888
mav 20,36,40
ds_write_b128 v95, v[68:71] offset:0
mav 24,40,40
mav 28,44,40
buffer_load_dwordx4 v[192:195], v88, s[36:39], s49 offen offset:1024 sc1
mav 16,48,44
R 56,38912
mav 20,52,44
s_waitcnt lgkmcnt(1)
ds_read_b128 v[64:67], v96 offset:0
mav 24,56,44
ds_read_b128 v[68:71], v96 offset:1024
mav 28,60,44
mav 16,64,48
R 60,39936
buffer_load_dwordx4 v[196:199], v89, s[36:39], s49 offen sc1
mav 20,68,48
mav 24,72,48
mav 28,76,48
mav 16,80,52
R 32,40960
buffer_load_dwordx4 v[200:203], v89, s[36:39], s49 offen offset:1024 sc1
mav 20,84,52
mav 24,88,52
mav 28,92,52
s_add_u32 m0, s50, 32768
s_waitcnt lgkmcnt(1)
mav 16,96,56
R 36,41984
buffer_store_dwordx4 v[64:67], v92, s[44:47], s54 offen
mav 20,100,56
mav 24,104,56
mav 28,108,56
mav 16,112,60
R 40,43008
buffer_store_dwordx4 v[68:71], v93, s[44:47], s54 offen
mav 20,116,60
mav 24,120,60
mav 28,124,60
s_waitcnt lgkmcnt(1)
mav 16,128,32
R 44,44032
D 83,0
mav 20,132,32
mav 24,136,32
mav 28,140,32
mav 16,144,36
R 48,45056
D 83,1024
mav 20,148,36
mav 24,152,36
mav 28,156,36
s_waitcnt lgkmcnt(1)
mav 16,160,40
R 52,46080
D 83,2048
mav 20,164,40
s_sub_u32 s54, s54, 0x8000
v_add_u32 v85, 1, v85
mav 24,168,40
mav 28,172,40
mav 16,176,44
R 56,47104
D 83,3072
mav 20,180,44
s_mov_b32 m0, 142336
mav 24,184,44
buffer_load_dword v84, s[36:39], 0 offen sc1 lds
mav 28,188,44
s_waitcnt lgkmcnt(1)
mav 16,192,48
R 60,48128
buffer_load_dwordx4 v[124:127], v90, s[36:39], s49 offen sc1
mav 20,196,48
mav 24,200,48
mav 28,204,48
mav 16,208,52
buffer_load_dwordx4 v[128:131], v90, s[36:39], s49 offen offset:1024 sc1
mav 20,212,52
mav 24,216,52
buffer_load_dword v84, s[36:39], 0 offen sc1 lds
mav 28,220,52
s_waitcnt lgkmcnt(0)
mav 16,224,56
mav 20,228,56
mav 24,232,56
mav 28,236,56
mav 16,240,60
mav 20,244,60
ds_read_b32 v76, v82 offset:0
mav 24,248,60
mav 28,252,60
s_waitcnt vmcnt(20)
mvv 16,116,164
mvv 20,120,164
s_waitcnt vmcnt(19)
mvv 24,116,168
mvv 28,120,168
s_waitcnt vmcnt(17)
mvv 16,116,172
mvv 20,120,172
v_xor_b32 v80, 0x10000, v80
s_waitcnt vmcnt(16)
mvv 24,116,176
s_waitcnt lgkmcnt(0)
v_cmp_gt_i32 vcc, v85, v76
mvv 28,120,176
s_cbranch_vccnz CR0_15
BR0_15:
s_waitcnt vmcnt(15)
mvv 16,116,180
s_barrier
R 32,0
mvv 20,120,180
s_add_u32 s49, s49, 346112
s_waitcnt vmcnt(14)
mvv 24,116,184
buffer_load_dwordx4 v[132:135], v86, s[36:39], s49 offen sc1
mvv 28,120,184
s_waitcnt vmcnt(14)
mvv 16,116,188
R 36,1024
mvv 20,120,188
s_waitcnt vmcnt(13)
mvv 24,116,192
buffer_load_dwordx4 v[136:139], v86, s[36:39], s49 offen offset:1024 sc1
mvv 28,120,192
s_waitcnt vmcnt(13)
mvv 16,116,196
R 40,2048
mvv 20,120,196
s_waitcnt vmcnt(12)
mvv 24,116,200
buffer_load_dwordx4 v[140:143], v87, s[36:39], s49 offen sc1
mvv 28,120,200
s_waitcnt lgkmcnt(1)
v_mfma_f32_16x16x32_bf16 v[0:3], a[0:3], v[32:35], v[100:103]
R 44,3072
v_cvt_pk_bf16_f32 v64, v16, v17
v_mfma_f32_16x16x32_bf16 v[4:7], a[4:7], v[32:35], v[104:107]
v_cvt_pk_bf16_f32 v65, v18, v19
v_cvt_pk_bf16_f32 v66, v20, v21
v_mfma_f32_16x16x32_bf16 v[8:11], a[8:11], v[32:35], v[108:111]
buffer_load_dwordx4 v[144:147], v87, s[36:39], s49 offen offset:1024 sc1
v_cvt_pk_bf16_f32 v67, v22, v23
v_mfma_f32_16x16x32_bf16 v[12:15], a[12:15], v[32:35], v[112:115]
ds_write_b128 v94, v[64:67] offset:0
mav 0,16,36
R 48,4096
v_cvt_pk_bf16_f32 v68, v24, v25
mav 4,20,36
v_cvt_pk_bf16_f32 v69, v26, v27
v_cvt_pk_bf16_f32 v70, v28, v29
mav 8,24,36
v_cvt_pk_bf16_f32 v71, v30, v31
mav 12,28,36
buffer_load_dwordx4 v[148:151], v88, s[36:39], s49 offen sc1
s_waitcnt lgkmcnt(2)
mav 0,32,40
R 52,5120
mav 4,36,40
ds_write_b128 v95, v[68:71] offset:0
mav 8,40,40
mav 12,44,40
buffer_load_dwordx4 v[152:155], v88, s[36:39], s49 offen offset:1024 sc1
mav 0,48,44
R 56,6144
mav 4,52,44
s_waitcnt lgkmcnt(1)
ds_read_b128 v[64:67], v96 offset:0
mav 8,56,44
ds_read_b128 v[68:71], v96 offset:1024
mav 12,60,44
mav 0,64,48
R 60,7168
buffer_load_dwordx4 v[156:159], v89, s[36:39], s49 offen sc1
mav 4,68,48
mav 8,72,48
mav 12,76,48
mav 0,80,52
R 32,8192
buffer_load_dwordx4 v[160:163], v89, s[36:39], s49 offen offset:1024 sc1
mav 4,84,52
mav 8,88,52
mav 12,92,52
s_waitcnt lgkmcnt(1)
mav 0,96,56
R 36,9216
buffer_store_dwordx4 v[64:67], v92, s[44:47], s54 offen
mav 4,100,56
mav 8,104,56
mav 12,108,56
mav 0,112,60
R 40,10240
buffer_store_dwordx4 v[68:71], v93, s[44:47], s54 offen
mav 4,116,60
mav 8,120,60
mav 12,124,60
s_waitcnt lgkmcnt(1)
mav 0,128,32
R 44,11264
mav 4,132,32
mav 8,136,32
mav 12,140,32
mav 0,144,36
R 48,12288
mav 4,148,36
mav 8,152,36
mav 12,156,36
s_waitcnt lgkmcnt(1)
mav 0,160,40
R 52,13312
s_mov_b32 m0, 142336
mav 4,164,40
s_sub_u32 s54, s54, 0x8000
v_add_u32 v85, 1, v85
mav 8,168,40
buffer_load_dword v84, s[36:39], 0 offen sc1 lds
mav 12,172,40
buffer_load_dwordx4 v[164:167], v90, s[36:39], s49 offen sc1
mav 0,176,44
R 56,14336
mav 4,180,44
mav 8,184,44
mav 12,188,44
buffer_load_dwordx4 v[168:171], v90, s[36:39], s49 offen offset:1024 sc1
s_waitcnt lgkmcnt(1)
mav 0,192,48
R 60,15360
mav 4,196,48
mav 8,200,48
mav 12,204,48
mav 0,208,52
mav 4,212,52
mav 8,216,52
buffer_load_dword v84, s[36:39], 0 offen sc1 lds
mav 12,220,52
s_waitcnt lgkmcnt(0)
mav 0,224,56
mav 4,228,56
mav 8,232,56
mav 12,236,56
mav 0,240,60
mav 4,244,60
ds_read_b32 v76, v82 offset:0
mav 8,248,60
mav 12,252,60
s_waitcnt vmcnt(16)
mvv 0,116,124
mvv 4,120,124
s_waitcnt vmcnt(15)
mvv 8,116,128
mvv 12,120,128
s_waitcnt vmcnt(13)
mvv 0,116,132
mvv 4,120,132
s_waitcnt lgkmcnt(0)
v_cmp_gt_i32 vcc, v85, v76
s_waitcnt vmcnt(12)
mvv 8,116,136
s_cbranch_vccnz CR0_16
BR0_16:
s_add_u32 s49, s49, 346112
mvv 12,120,136
s_barrier
s_waitcnt vmcnt(11)
mvv 0,116,140
R 32,32768
mvv 4,120,140
buffer_load_dwordx4 v[172:175], v86, s[36:39], s49 offen sc1
s_waitcnt vmcnt(11)
mvv 8,116,144
mvv 12,120,144
s_waitcnt vmcnt(10)
mvv 0,116,148
R 36,33792
mvv 4,120,148
buffer_load_dwordx4 v[176:179], v86, s[36:39], s49 offen offset:1024 sc1
s_waitcnt vmcnt(10)
mvv 8,116,152
mvv 12,120,152
s_waitcnt vmcnt(9)
mvv 0,116,156
R 40,34816
mvv 4,120,156
s_waitcnt vmcnt(8)
mvv 8,116,160
buffer_load_dwordx4 v[180:183], v87, s[36:39], s49 offen sc1
mvv 12,120,160
s_waitcnt lgkmcnt(1)
v_mfma_f32_16x16x32_bf16 v[16:19], a[0:3], v[32:35], v[100:103]
R 44,35840
v_cvt_pk_bf16_f32 v64, v0, v1
v_mfma_f32_16x16x32_bf16 v[20:23], a[4:7], v[32:35], v[104:107]
v_cvt_pk_bf16_f32 v65, v2, v3
v_cvt_pk_bf16_f32 v66, v4, v5
v_mfma_f32_16x16x32_bf16 v[24:27], a[8:11], v[32:35], v[108:111]
buffer_load_dwordx4 v[184:187], v87, s[36:39], s49 offen offset:1024 sc1
v_cvt_pk_bf16_f32 v67, v6, v7
v_mfma_f32_16x16x32_bf16 v[28:31], a[12:15], v[32:35], v[112:115]
ds_write_b128 v94, v[64:67] offset:0
mav 16,16,36
R 48,36864
v_cvt_pk_bf16_f32 v68, v8, v9
mav 20,20,36
v_cvt_pk_bf16_f32 v69, v10, v11
v_cvt_pk_bf16_f32 v70, v12, v13
mav 24,24,36
v_cvt_pk_bf16_f32 v71, v14, v15
mav 28,28,36
buffer_load_dwordx4 v[188:191], v88, s[36:39], s49 offen sc1
s_waitcnt lgkmcnt(2)
mav 16,32,40
R 52,37888
mav 20,36,40
ds_write_b128 v95, v[68:71] offset:0
mav 24,40,40
mav 28,44,40
buffer_load_dwordx4 v[192:195], v88, s[36:39], s49 offen offset:1024 sc1
mav 16,48,44
R 56,38912
mav 20,52,44
s_waitcnt lgkmcnt(1)
ds_read_b128 v[64:67], v96 offset:0
mav 24,56,44
ds_read_b128 v[68:71], v96 offset:1024
mav 28,60,44
mav 16,64,48
R 60,39936
buffer_load_dwordx4 v[196:199], v89, s[36:39], s49 offen sc1
mav 20,68,48
mav 24,72,48
mav 28,76,48
mav 16,80,52
R 32,40960
buffer_load_dwordx4 v[200:203], v89, s[36:39], s49 offen offset:1024 sc1
mav 20,84,52
mav 24,88,52
mav 28,92,52
s_waitcnt lgkmcnt(1)
mav 16,96,56
R 36,41984
buffer_store_dwordx4 v[64:67], v92, s[44:47], s54 offen
mav 20,100,56
mav 24,104,56
mav 28,108,56
mav 16,112,60
R 40,43008
buffer_store_dwordx4 v[68:71], v93, s[44:47], s54 offen
mav 20,116,60
mav 24,120,60
mav 28,124,60
s_waitcnt lgkmcnt(1)
mav 16,128,32
R 44,44032
mav 20,132,32
mav 24,136,32
mav 28,140,32
mav 16,144,36
R 48,45056
mav 20,148,36
mav 24,152,36
mav 28,156,36
s_waitcnt lgkmcnt(1)
mav 16,160,40
R 52,46080
s_sub_u32 s54, s54, 0x8000
mav 20,164,40
mav 24,168,40
mav 28,172,40
mav 16,176,44
R 56,47104
mav 20,180,44
mav 24,184,44
mav 28,188,44
s_waitcnt lgkmcnt(1)
mav 16,192,48
R 60,48128
mav 20,196,48
mav 24,200,48
mav 28,204,48
mav 16,208,52
mav 20,212,52
mav 24,216,52
mav 28,220,52
)"
R"(
s_waitcnt lgkmcnt(0)
mav 16,224,56
mav 20,228,56
mav 24,232,56
mav 28,236,56
mav 16,240,60
mav 20,244,60
mav 24,248,60
mav 28,252,60
s_waitcnt vmcnt(12)
mvv 16,116,164
mvv 20,120,164
s_waitcnt vmcnt(11)
mvv 24,116,168
mvv 28,120,168
s_waitcnt vmcnt(9)
mvv 16,116,172
mvv 20,120,172
s_waitcnt vmcnt(8)
mvv 24,116,176
mvv 28,120,176
s_waitcnt vmcnt(7)
mvv 16,116,180
mvv 20,120,180
s_waitcnt vmcnt(6)
mvv 24,116,184
mvv 28,120,184
s_waitcnt vmcnt(5)
mvv 16,116,188
mvv 20,120,188
s_waitcnt vmcnt(4)
mvv 24,116,192
mvv 28,120,192
s_waitcnt vmcnt(3)
mvv 16,116,196
mvv 20,120,196
s_waitcnt vmcnt(2)
mvv 24,116,200
mvv 28,120,200
s_nop 3
v_cvt_pk_bf16_f32 v64, v16, v17
v_cvt_pk_bf16_f32 v65, v18, v19
v_cvt_pk_bf16_f32 v66, v20, v21
v_cvt_pk_bf16_f32 v67, v22, v23
ds_write_b128 v94, v[64:67] offset:0
v_cvt_pk_bf16_f32 v68, v24, v25
v_cvt_pk_bf16_f32 v69, v26, v27
v_cvt_pk_bf16_f32 v70, v28, v29
v_cvt_pk_bf16_f32 v71, v30, v31
ds_write_b128 v95, v[68:71] offset:0
s_waitcnt lgkmcnt(0)
ds_read_b128 v[64:67], v96 offset:0
ds_read_b128 v[68:71], v96 offset:1024
s_waitcnt lgkmcnt(0)
buffer_store_dwordx4 v[64:67], v92, s[44:47], s54 offen
buffer_store_dwordx4 v[68:71], v93, s[44:47], s54 offen
s_endpgm
CR0_1:
s_cmp_eq_u32 s61, 0
s_cbranch_scc1 BR0_1
s_sub_u32 s61, s61, 1
s_sleep 8
buffer_load_dwordx2 v[76:77], v84, s[36:39], 0 offen sc1
s_waitcnt vmcnt(0)
ds_write_b32 v82, v76
v_sub_u32 v76, v76, v85
v_xor_b32 v77, v77, v73
v_cmp_lt_u64 vcc, s[62:63], v[76:77]
s_waitcnt lgkmcnt(0)
s_cbranch_vccnz CR0_1
s_branch BR0_1
CR0_2:
s_mov_b32 s58, m0
s_mov_b32 m0, 142336
DR0_2:
s_cmp_eq_u32 s61, 0
s_cbranch_scc1 ER0_2
s_sub_u32 s61, s61, 1
buffer_load_dword v84, s[36:39], 0 offen sc1 lds
s_sleep 1
ds_read_b32 v76, v82
s_waitcnt lgkmcnt(0)
v_cmp_gt_i32 vcc, v85, v76
s_cbranch_vccnz DR0_2
ER0_2:
s_mov_b32 m0, s58
s_branch BR0_2
CR0_3:
s_mov_b32 s58, m0
s_mov_b32 m0, 142336
DR0_3:
s_cmp_eq_u32 s61, 0
s_cbranch_scc1 ER0_3
s_sub_u32 s61, s61, 1
buffer_load_dword v84, s[36:39], 0 offen sc1 lds
s_sleep 1
ds_read_b32 v76, v82
s_waitcnt lgkmcnt(0)
v_cmp_gt_i32 vcc, v85, v76
s_cbranch_vccnz DR0_3
ER0_3:
s_mov_b32 m0, s58
s_branch BR0_3
CR0_4:
s_mov_b32 s58, m0
s_mov_b32 m0, 142336
DR0_4:
s_cmp_eq_u32 s61, 0
s_cbranch_scc1 ER0_4
s_sub_u32 s61, s61, 1
buffer_load_dword v84, s[36:39], 0 offen sc1 lds
s_sleep 1
ds_read_b32 v76, v82
s_waitcnt lgkmcnt(0)
v_cmp_gt_i32 vcc, v85, v76
s_cbranch_vccnz DR0_4
ER0_4:
s_mov_b32 m0, s58
s_branch BR0_4
CR0_5:
s_mov_b32 s58, m0
s_mov_b32 m0, 142336
DR0_5:
s_cmp_eq_u32 s61, 0
s_cbranch_scc1 ER0_5
s_sub_u32 s61, s61, 1
buffer_load_dword v84, s[36:39], 0 offen sc1 lds
s_sleep 1
ds_read_b32 v76, v82
s_waitcnt lgkmcnt(0)
v_cmp_gt_i32 vcc, v85, v76
s_cbranch_vccnz DR0_5
ER0_5:
s_mov_b32 m0, s58
s_branch BR0_5
CR0_7:
s_mov_b32 s58, m0
s_mov_b32 m0, 142336
DR0_7:
s_cmp_eq_u32 s61, 0
s_cbranch_scc1 ER0_7
s_sub_u32 s61, s61, 1
buffer_load_dword v84, s[36:39], 0 offen sc1 lds
s_sleep 1
ds_read_b32 v76, v82
s_waitcnt lgkmcnt(0)
v_cmp_gt_i32 vcc, v85, v76
s_cbranch_vccnz DR0_7
ER0_7:
s_mov_b32 m0, s58
s_branch BR0_7
CR0_8:
s_mov_b32 s58, m0
s_mov_b32 m0, 142336
DR0_8:
s_cmp_eq_u32 s61, 0
s_cbranch_scc1 ER0_8
s_sub_u32 s61, s61, 1
buffer_load_dword v84, s[36:39], 0 offen sc1 lds
s_sleep 1
ds_read_b32 v76, v82
s_waitcnt lgkmcnt(0)
v_cmp_gt_i32 vcc, v85, v76
s_cbranch_vccnz DR0_8
ER0_8:
s_mov_b32 m0, s58
s_branch BR0_8
CR0_9:
s_mov_b32 s58, m0
s_mov_b32 m0, 142336
DR0_9:
s_cmp_eq_u32 s61, 0
s_cbranch_scc1 ER0_9
s_sub_u32 s61, s61, 1
buffer_load_dword v84, s[36:39], 0 offen sc1 lds
s_sleep 1
ds_read_b32 v76, v82
s_waitcnt lgkmcnt(0)
v_cmp_gt_i32 vcc, v85, v76
s_cbranch_vccnz DR0_9
ER0_9:
s_mov_b32 m0, s58
s_branch BR0_9
CR0_10:
s_mov_b32 s58, m0
s_mov_b32 m0, 142336
DR0_10:
s_cmp_eq_u32 s61, 0
s_cbranch_scc1 ER0_10
s_sub_u32 s61, s61, 1
buffer_load_dword v84, s[36:39], 0 offen sc1 lds
s_sleep 1
ds_read_b32 v76, v82
s_waitcnt lgkmcnt(0)
v_cmp_gt_i32 vcc, v85, v76
s_cbranch_vccnz DR0_10
ER0_10:
s_mov_b32 m0, s58
s_branch BR0_10
CR0_12:
s_mov_b32 s58, m0
s_mov_b32 m0, 142336
DR0_12:
s_cmp_eq_u32 s61, 0
s_cbranch_scc1 ER0_12
s_sub_u32 s61, s61, 1
buffer_load_dword v84, s[36:39], 0 offen sc1 lds
s_sleep 1
ds_read_b32 v76, v82
s_waitcnt lgkmcnt(0)
v_cmp_gt_i32 vcc, v85, v76
s_cbranch_vccnz DR0_12
ER0_12:
s_mov_b32 m0, s58
s_branch BR0_12
CR0_13:
s_mov_b32 s58, m0
s_mov_b32 m0, 142336
DR0_13:
s_cmp_eq_u32 s61, 0
s_cbranch_scc1 ER0_13
s_sub_u32 s61, s61, 1
buffer_load_dword v84, s[36:39], 0 offen sc1 lds
s_sleep 1
ds_read_b32 v76, v82
s_waitcnt lgkmcnt(0)
v_cmp_gt_i32 vcc, v85, v76
s_cbranch_vccnz DR0_13
ER0_13:
s_mov_b32 m0, s58
s_branch BR0_13
CR0_14:
s_mov_b32 s58, m0
s_mov_b32 m0, 142336
DR0_14:
s_cmp_eq_u32 s61, 0
s_cbranch_scc1 ER0_14
s_sub_u32 s61, s61, 1
buffer_load_dword v84, s[36:39], 0 offen sc1 lds
s_sleep 1
ds_read_b32 v76, v82
s_waitcnt lgkmcnt(0)
v_cmp_gt_i32 vcc, v85, v76
s_cbranch_vccnz DR0_14
ER0_14:
s_mov_b32 m0, s58
s_branch BR0_14
CR0_15:
s_mov_b32 s58, m0
s_mov_b32 m0, 142336
DR0_15:
s_cmp_eq_u32 s61, 0
s_cbranch_scc1 ER0_15
s_sub_u32 s61, s61, 1
buffer_load_dword v84, s[36:39], 0 offen sc1 lds
s_sleep 1
ds_read_b32 v76, v82
s_waitcnt lgkmcnt(0)
v_cmp_gt_i32 vcc, v85, v76
s_cbranch_vccnz DR0_15
ER0_15:
s_mov_b32 m0, s58
s_branch BR0_15
CR0_16:
s_mov_b32 s58, m0
s_mov_b32 m0, 142336
DR0_16:
s_cmp_eq_u32 s61, 0
s_cbranch_scc1 ER0_16
s_sub_u32 s61, s61, 1
buffer_load_dword v84, s[36:39], 0 offen sc1 lds
s_sleep 1
ds_read_b32 v76, v82
s_waitcnt lgkmcnt(0)
v_cmp_gt_i32 vcc, v85, v76
s_cbranch_vccnz DR0_16
ER0_16:
s_mov_b32 m0, s58
s_branch BR0_16
LvR1:
s_add_u32 s49, s49, 346112
s_barrier
s_add_u32 m0, s50, 0
s_nop 0
D 83,0
D 83,1024
D 83,2048
D 83,3072
s_add_u32 s49, s49, 346112
s_barrier
s_add_u32 m0, s50, 32768
s_nop 0
D 83,0
D 83,1024
D 83,2048
D 83,3072
buffer_load_dwordx4 v[124:127], v90, s[36:39], s49 offen sc1
buffer_load_dwordx4 v[128:131], v90, s[36:39], s49 offen offset:1024 sc1
s_xor_b32 s50, s50, 0x10000
s_add_u32 s49, s49, 346112
s_waitcnt vmcnt(6)
s_barrier
R 32,0
buffer_load_dwordx4 v[132:135], v86, s[36:39], s49 offen sc1
R 36,1024
buffer_load_dwordx4 v[136:139], v86, s[36:39], s49 offen offset:1024 sc1
R 40,2048
buffer_load_dwordx4 v[140:143], v87, s[36:39], s49 offen sc1
s_waitcnt lgkmcnt(1)
v_mfma_f32_16x16x32_bf16 v[0:3], a[0:3], v[32:35], v[100:103]
R 44,3072
v_mfma_f32_16x16x32_bf16 v[4:7], a[4:7], v[32:35], v[104:107]
v_mfma_f32_16x16x32_bf16 v[8:11], a[8:11], v[32:35], v[108:111]
buffer_load_dwordx4 v[144:147], v87, s[36:39], s49 offen offset:1024 sc1
v_mfma_f32_16x16x32_bf16 v[12:15], a[12:15], v[32:35], v[112:115]
mav 0,16,36
R 48,4096
mav 4,20,36
mav 8,24,36
mav 12,28,36
buffer_load_dwordx4 v[148:151], v88, s[36:39], s49 offen sc1
s_waitcnt lgkmcnt(1)
mav 0,32,40
R 52,5120
mav 4,36,40
mav 8,40,40
mav 12,44,40
buffer_load_dwordx4 v[152:155], v88, s[36:39], s49 offen offset:1024 sc1
mav 0,48,44
R 56,6144
mav 4,52,44
mav 8,56,44
mav 12,60,44
s_waitcnt lgkmcnt(1)
mav 0,64,48
R 60,7168
buffer_load_dwordx4 v[156:159], v89, s[36:39], s49 offen sc1
mav 4,68,48
mav 8,72,48
mav 12,76,48
mav 0,80,52
R 32,8192
buffer_load_dwordx4 v[160:163], v89, s[36:39], s49 offen offset:1024 sc1
mav 4,84,52
mav 8,88,52
mav 12,92,52
s_add_u32 m0, s50, 0
s_waitcnt lgkmcnt(1)
mav 0,96,56
R 36,9216
mav 4,100,56
D 83,0
mav 8,104,56
mav 12,108,56
mav 0,112,60
R 40,10240
mav 4,116,60
D 83,1024
mav 8,120,60
mav 12,124,60
s_waitcnt lgkmcnt(1)
mav 0,128,32
R 44,11264
mav 4,132,32
mav 8,136,32
D 83,2048
mav 12,140,32
mav 0,144,36
R 48,12288
mav 4,148,36
mav 8,152,36
D 83,3072
mav 12,156,36
s_waitcnt lgkmcnt(1)
mav 0,160,40
R 52,13312
mav 4,164,40
mav 8,168,40
mav 12,172,40
buffer_load_dwordx4 v[164:167], v90, s[36:39], s49 offen sc1
mav 0,176,44
R 56,14336
mav 4,180,44
mav 8,184,44
mav 12,188,44
buffer_load_dwordx4 v[168:171], v90, s[36:39], s49 offen offset:1024 sc1
s_waitcnt lgkmcnt(1)
mav 0,192,48
R 60,15360
mav 4,196,48
mav 8,200,48
mav 12,204,48
mav 0,208,52
mav 4,212,52
mav 8,216,52
mav 12,220,52
s_waitcnt lgkmcnt(0)
mav 0,224,56
mav 4,228,56
mav 8,232,56
mav 12,236,56
mav 0,240,60
mav 4,244,60
mav 8,248,60
mav 12,252,60
s_waitcnt vmcnt(15)
mvv 0,116,124
mvv 4,120,124
s_waitcnt vmcnt(14)
mvv 8,116,128
mvv 12,120,128
s_waitcnt vmcnt(13)
mvv 0,116,132
mvv 4,120,132
s_waitcnt vmcnt(12)
mvv 8,116,136
s_add_u32 s49, s49, 346112
mvv 12,120,136
s_barrier
s_waitcnt vmcnt(11)
mvv 0,116,140
R 32,32768
mvv 4,120,140
buffer_load_dwordx4 v[172:175], v86, s[36:39], s49 offen sc1
s_waitcnt vmcnt(11)
mvv 8,116,144
mvv 12,120,144
s_waitcnt vmcnt(10)
mvv 0,116,148
R 36,33792
mvv 4,120,148
buffer_load_dwordx4 v[176:179], v86, s[36:39], s49 offen offset:1024 sc1
s_waitcnt vmcnt(10)
mvv 8,116,152
mvv 12,120,152
s_waitcnt vmcnt(9)
mvv 0,116,156
R 40,34816
mvv 4,120,156
s_waitcnt vmcnt(8)
mvv 8,116,160
buffer_load_dwordx4 v[180:183], v87, s[36:39], s49 offen sc1
mvv 12,120,160
s_waitcnt lgkmcnt(1)
v_mfma_f32_16x16x32_bf16 v[16:19], a[0:3], v[32:35], v[100:103]
R 44,35840
v_cvt_pk_bf16_f32 v64, v0, v1
v_mfma_f32_16x16x32_bf16 v[20:23], a[4:7], v[32:35], v[104:107]
v_cvt_pk_bf16_f32 v65, v2, v3
v_cvt_pk_bf16_f32 v66, v4, v5
v_mfma_f32_16x16x32_bf16 v[24:27], a[8:11], v[32:35], v[108:111]
buffer_load_dwordx4 v[184:187], v87, s[36:39], s49 offen offset:1024 sc1
v_cvt_pk_bf16_f32 v67, v6, v7
v_mfma_f32_16x16x32_bf16 v[28:31], a[12:15], v[32:35], v[112:115]
v_cvt_pk_bf16_f32 v68, v8, v9
v_cvt_pk_bf16_f32 v69, v10, v11
mav 16,16,36
R 48,36864
v_cvt_pk_bf16_f32 v70, v12, v13
mav 20,20,36
v_cvt_pk_bf16_f32 v71, v14, v15
v_mul_f32 v75, v0, v0
mav 24,24,36
buffer_store_dwordx4 v[64:67], v91, s[36:39], s49 offen sc1
v_add_f32 v74, v1, v0
mav 28,28,36
v_fmac_f32 v75, v1, v1
s_waitcnt lgkmcnt(1)
mav 16,32,40
R 52,37888
v_add_f32 v74, v2, v74
mav 20,36,40
v_fmac_f32 v75, v2, v2
v_add_f32 v74, v3, v74
mav 24,40,40
buffer_store_dwordx4 v[68:71], v91, s[36:39], s49 offen offset:1024 sc1
v_fmac_f32 v75, v3, v3
mav 28,44,40
v_add_f32 v74, v4, v74
v_fmac_f32 v75, v4, v4
mav 16,48,44
R 56,38912
v_add_f32 v74, v5, v74
mav 20,52,44
v_fmac_f32 v75, v5, v5
v_add_f32 v74, v6, v74
mav 24,56,44
buffer_load_dwordx4 v[188:191], v88, s[36:39], s49 offen sc1
v_fmac_f32 v75, v6, v6
mav 28,60,44
v_add_f32 v74, v7, v74
s_waitcnt lgkmcnt(1)
mav 16,64,48
R 60,39936
v_fmac_f32 v75, v7, v7
mav 20,68,48
v_add_f32 v74, v8, v74
v_fmac_f32 v75, v8, v8
mav 24,72,48
buffer_load_dwordx4 v[192:195], v88, s[36:39], s49 offen offset:1024 sc1
v_add_f32 v74, v9, v74
mav 28,76,48
v_fmac_f32 v75, v9, v9
v_add_f32 v74, v10, v74
mav 16,80,52
R 32,40960
v_fmac_f32 v75, v10, v10
mav 20,84,52
v_add_f32 v74, v11, v74
v_fmac_f32 v75, v11, v11
mav 24,88,52
buffer_load_dwordx4 v[196:199], v89, s[36:39], s49 offen sc1
v_add_f32 v74, v12, v74
mav 28,92,52
s_add_u32 m0, s50, 32768
s_waitcnt lgkmcnt(1)
mav 16,96,56
R 36,41984
v_fmac_f32 v75, v12, v12
mav 20,100,56
v_add_f32 v74, v13, v74
v_fmac_f32 v75, v13, v13
mav 24,104,56
buffer_load_dwordx4 v[200:203], v89, s[36:39], s49 offen offset:1024 sc1
v_add_f32 v74, v14, v74
mav 28,108,56
v_fmac_f32 v75, v14, v14
v_add_f32 v74, v15, v74
mav 16,112,60
R 40,43008
v_fmac_f32 v75, v15, v15
mav 20,116,60
mav 24,120,60
D 83,0
v_permlane32_swap_b32 v74, v75
mav 28,124,60
v_add_f32 v74, v74, v75
s_waitcnt lgkmcnt(1)
mav 16,128,32
R 44,44032
mav 20,132,32
mav 24,136,32
D 83,1024
mav 28,140,32
mav 16,144,36
R 48,45056
mav 20,148,36
mav 24,152,36
D 83,2048
mav 28,156,36
s_waitcnt lgkmcnt(1)
mav 16,160,40
R 52,46080
mav 20,164,40
mav 24,168,40
D 83,3072
mav 28,172,40
mav 16,176,44
R 56,47104
mav 20,180,44
mav 24,184,44
buffer_load_dwordx4 v[124:127], v90, s[36:39], s49 offen sc1
mav 28,188,44
s_waitcnt lgkmcnt(1)
mav 16,192,48
R 60,48128
mav 20,196,48
mav 24,200,48
buffer_load_dwordx4 v[128:131], v90, s[36:39], s49 offen offset:1024 sc1
mav 28,204,48
mav 16,208,52
mav 20,212,52
mav 24,216,52
v_add_u32 v72, 1, v72
mav 28,220,52
s_waitcnt lgkmcnt(0)
mav 16,224,56
mav 20,228,56
mav 24,232,56
mav 28,236,56
mav 16,240,60
s_waitcnt vmcnt(10)
buffer_store_dwordx2 v[72:73], v81, s[36:39], s52 offen sc1
mav 20,244,60
mav 24,248,60
ds_write_b32 v97, v74 offset:0
mav 28,252,60
mvv 16,116,164
mvv 20,120,164
mvv 24,116,168
s_xor_b32 s50, s50, 0x10000
mvv 28,120,168
mvv 16,116,172
mvv 20,120,172
v_xor_b32 v80, 0x10000, v80
mvv 24,116,176
s_add_u32 s49, s49, 346112
mvv 28,120,176
s_waitcnt lgkmcnt(0)
s_barrier
mvv 16,116,180
R 32,0
ds_read_b128 v[204:207], v98 offset:0
mvv 20,120,180
buffer_load_dwordx4 v[132:135], v86, s[36:39], s49 offen sc1
mvv 24,116,184
mvv 28,120,184
s_waitcnt vmcnt(11)
mvv 16,116,188
R 36,1024
mvv 20,120,188
buffer_load_dwordx4 v[136:139], v86, s[36:39], s49 offen offset:1024 sc1
s_waitcnt vmcnt(11)
mvv 24,116,192
mvv 28,120,192
s_waitcnt vmcnt(10)
mvv 16,116,196
R 40,2048
mvv 20,120,196
s_waitcnt vmcnt(9)
mvv 24,116,200
buffer_load_dwordx4 v[140:143], v87, s[36:39], s49 offen sc1
mvv 28,120,200
s_mov_b32 s60, -15
.p2align 6
LR1_1:
s_waitcnt lgkmcnt(1)
v_mfma_f32_16x16x32_bf16 v[0:3], a[0:3], v[32:35], v[100:103]
R 44,3072
v_add_f32 v204, v204, v205
v_mfma_f32_16x16x32_bf16 v[4:7], a[4:7], v[32:35], v[104:107]
v_add_f32 v206, v206, v207
v_add_f32 v204, v204, v206
v_mfma_f32_16x16x32_bf16 v[8:11], a[8:11], v[32:35], v[108:111]
buffer_load_dwordx4 v[144:147], v87, s[36:39], s49 offen offset:1024 sc1
v_add_f32_dpp v205, v204, v204 quad_perm:[1,0,3,2] row_mask:0xf bank_mask:0xf
v_mfma_f32_16x16x32_bf16 v[12:15], a[12:15], v[32:35], v[112:115]
buffer_store_dword v205, v99, s[36:39], s49 offen sc1
v_cvt_pk_bf16_f32 v64, v16, v17
mav 0,16,36
R 48,4096
v_cvt_pk_bf16_f32 v65, v18, v19
mav 4,20,36
v_cvt_pk_bf16_f32 v66, v20, v21
v_cvt_pk_bf16_f32 v67, v22, v23
mav 8,24,36
buffer_store_dwordx4 v[64:67], v91, s[36:39], s49 offen sc1
v_cvt_pk_bf16_f32 v68, v24, v25
mav 12,28,36
v_cvt_pk_bf16_f32 v69, v26, v27
s_waitcnt lgkmcnt(1)
mav 0,32,40
R 52,5120
v_cvt_pk_bf16_f32 v70, v28, v29
mav 4,36,40
v_cvt_pk_bf16_f32 v71, v30, v31
v_mul_f32 v75, v16, v16
mav 8,40,40
buffer_store_dwordx4 v[68:71], v91, s[36:39], s49 offen offset:1024 sc1
v_add_f32 v74, v17, v16
mav 12,44,40
v_fmac_f32 v75, v17, v17
v_add_f32 v74, v18, v74
mav 0,48,44
R 56,6144
v_fmac_f32 v75, v18, v18
mav 4,52,44
v_add_f32 v74, v19, v74
v_fmac_f32 v75, v19, v19
mav 8,56,44
buffer_load_dwordx4 v[148:151], v88, s[36:39], s49 offen sc1
v_add_f32 v74, v20, v74
mav 12,60,44
v_fmac_f32 v75, v20, v20
s_waitcnt lgkmcnt(1)
mav 0,64,48
R 60,7168
v_add_f32 v74, v21, v74
mav 4,68,48
v_fmac_f32 v75, v21, v21
v_add_f32 v74, v22, v74
mav 8,72,48
buffer_load_dwordx4 v[152:155], v88, s[36:39], s49 offen offset:1024 sc1
v_fmac_f32 v75, v22, v22
mav 12,76,48
v_add_f32 v74, v23, v74
v_fmac_f32 v75, v23, v23
mav 0,80,52
R 32,8192
v_add_f32 v74, v24, v74
mav 4,84,52
v_fmac_f32 v75, v24, v24
v_add_f32 v74, v25, v74
mav 8,88,52
buffer_load_dwordx4 v[156:159], v89, s[36:39], s49 offen sc1
v_fmac_f32 v75, v25, v25
mav 12,92,52
s_add_u32 m0, s50, 0
s_waitcnt lgkmcnt(1)
mav 0,96,56
R 36,9216
v_add_f32 v74, v26, v74
mav 4,100,56
v_fmac_f32 v75, v26, v26
v_add_f32 v74, v27, v74
mav 8,104,56
buffer_load_dwordx4 v[160:163], v89, s[36:39], s49 offen offset:1024 sc1
v_fmac_f32 v75, v27, v27
mav 12,108,56
v_add_f32 v74, v28, v74
v_fmac_f32 v75, v28, v28
mav 0,112,60
R 40,10240
v_add_f32 v74, v29, v74
mav 4,116,60
v_fmac_f32 v75, v29, v29
v_add_f32 v74, v30, v74
mav 8,120,60
D 83,0
v_fmac_f32 v75, v30, v30
mav 12,124,60
v_add_f32 v74, v31, v74
s_waitcnt lgkmcnt(1)
mav 0,128,32
R 44,11264
v_fmac_f32 v75, v31, v31
mav 4,132,32
mav 8,136,32
D 83,1024
v_permlane32_swap_b32 v74, v75
mav 12,140,32
v_add_f32 v74, v74, v75
mav 0,144,36
R 48,12288
mav 4,148,36
mav 8,152,36
D 83,2048
mav 12,156,36
s_waitcnt lgkmcnt(1)
mav 0,160,40
R 52,13312
mav 4,164,40
mav 8,168,40
D 83,3072
mav 12,172,40
mav 0,176,44
R 56,14336
mav 4,180,44
mav 8,184,44
buffer_load_dwordx4 v[164:167], v90, s[36:39], s49 offen sc1
mav 12,188,44
s_waitcnt lgkmcnt(1)
mav 0,192,48
R 60,15360
mav 4,196,48
mav 8,200,48
buffer_load_dwordx4 v[168:171], v90, s[36:39], s49 offen offset:1024 sc1
mav 12,204,48
mav 0,208,52
mav 4,212,52
mav 8,216,52
v_add_u32 v72, 1, v72
mav 12,220,52
s_waitcnt lgkmcnt(0)
mav 0,224,56
mav 4,228,56
mav 8,232,56
mav 12,236,56
mav 0,240,60
s_waitcnt vmcnt(10)
buffer_store_dwordx2 v[72:73], v81, s[36:39], s52 offen sc1
mav 4,244,60
mav 8,248,60
ds_write_b32 v97, v74 offset:1024
mav 12,252,60
mvv 0,116,124
mvv 4,120,124
mvv 8,116,128
mvv 12,120,128
mvv 0,116,132
mvv 4,120,132
mvv 8,116,136
s_add_u32 s49, s49, 346112
mvv 12,120,136
s_waitcnt lgkmcnt(0)
s_barrier
mvv 0,116,140
R 32,32768
ds_read_b128 v[204:207], v98 offset:1024
mvv 4,120,140
buffer_load_dwordx4 v[172:175], v86, s[36:39], s49 offen sc1
mvv 8,116,144
mvv 12,120,144
s_waitcnt vmcnt(11)
mvv 0,116,148
R 36,33792
mvv 4,120,148
buffer_load_dwordx4 v[176:179], v86, s[36:39], s49 offen offset:1024 sc1
s_waitcnt vmcnt(11)
mvv 8,116,152
mvv 12,120,152
s_waitcnt vmcnt(10)
mvv 0,116,156
R 40,34816
mvv 4,120,156
s_waitcnt vmcnt(9)
mvv 8,116,160
buffer_load_dwordx4 v[180:183], v87, s[36:39], s49 offen sc1
mvv 12,120,160
s_waitcnt lgkmcnt(1)
v_mfma_f32_16x16x32_bf16 v[16:19], a[0:3], v[32:35], v[100:103]
R 44,35840
v_add_f32 v204, v204, v205
v_mfma_f32_16x16x32_bf16 v[20:23], a[4:7], v[32:35], v[104:107]
v_add_f32 v206, v206, v207
v_add_f32 v204, v204, v206
v_mfma_f32_16x16x32_bf16 v[24:27], a[8:11], v[32:35], v[108:111]
buffer_load_dwordx4 v[184:187], v87, s[36:39], s49 offen offset:1024 sc1
v_add_f32_dpp v205, v204, v204 quad_perm:[1,0,3,2] row_mask:0xf bank_mask:0xf
v_mfma_f32_16x16x32_bf16 v[28:31], a[12:15], v[32:35], v[112:115]
buffer_store_dword v205, v99, s[36:39], s49 offen sc1
v_cvt_pk_bf16_f32 v64, v0, v1
mav 16,16,36
R 48,36864
v_cvt_pk_bf16_f32 v65, v2, v3
mav 20,20,36
v_cvt_pk_bf16_f32 v66, v4, v5
v_cvt_pk_bf16_f32 v67, v6, v7
mav 24,24,36
buffer_store_dwordx4 v[64:67], v91, s[36:39], s49 offen sc1
v_cvt_pk_bf16_f32 v68, v8, v9
mav 28,28,36
v_cvt_pk_bf16_f32 v69, v10, v11
s_waitcnt lgkmcnt(1)
mav 16,32,40
R 52,37888
v_cvt_pk_bf16_f32 v70, v12, v13
mav 20,36,40
v_cvt_pk_bf16_f32 v71, v14, v15
v_mul_f32 v75, v0, v0
mav 24,40,40
buffer_store_dwordx4 v[68:71], v91, s[36:39], s49 offen offset:1024 sc1
v_add_f32 v74, v1, v0
mav 28,44,40
v_fmac_f32 v75, v1, v1
v_add_f32 v74, v2, v74
mav 16,48,44
R 56,38912
v_fmac_f32 v75, v2, v2
mav 20,52,44
v_add_f32 v74, v3, v74
v_fmac_f32 v75, v3, v3
mav 24,56,44
buffer_load_dwordx4 v[188:191], v88, s[36:39], s49 offen sc1
v_add_f32 v74, v4, v74
mav 28,60,44
v_fmac_f32 v75, v4, v4
s_waitcnt lgkmcnt(1)
mav 16,64,48
R 60,39936
v_add_f32 v74, v5, v74
mav 20,68,48
v_fmac_f32 v75, v5, v5
v_add_f32 v74, v6, v74
mav 24,72,48
buffer_load_dwordx4 v[192:195], v88, s[36:39], s49 offen offset:1024 sc1
v_fmac_f32 v75, v6, v6
mav 28,76,48
v_add_f32 v74, v7, v74
v_fmac_f32 v75, v7, v7
mav 16,80,52
R 32,40960
v_add_f32 v74, v8, v74
mav 20,84,52
v_fmac_f32 v75, v8, v8
v_add_f32 v74, v9, v74
mav 24,88,52
buffer_load_dwordx4 v[196:199], v89, s[36:39], s49 offen sc1
v_fmac_f32 v75, v9, v9
mav 28,92,52
s_add_u32 m0, s50, 32768
s_waitcnt lgkmcnt(1)
mav 16,96,56
R 36,41984
v_add_f32 v74, v10, v74
mav 20,100,56
v_fmac_f32 v75, v10, v10
v_add_f32 v74, v11, v74
mav 24,104,56
buffer_load_dwordx4 v[200:203], v89, s[36:39], s49 offen offset:1024 sc1
v_fmac_f32 v75, v11, v11
mav 28,108,56
v_add_f32 v74, v12, v74
v_fmac_f32 v75, v12, v12
mav 16,112,60
R 40,43008
v_add_f32 v74, v13, v74
mav 20,116,60
v_fmac_f32 v75, v13, v13
v_add_f32 v74, v14, v74
mav 24,120,60
D 83,0
v_fmac_f32 v75, v14, v14
mav 28,124,60
v_add_f32 v74, v15, v74
s_waitcnt lgkmcnt(1)
mav 16,128,32
R 44,44032
v_fmac_f32 v75, v15, v15
mav 20,132,32
mav 24,136,32
D 83,1024
v_permlane32_swap_b32 v74, v75
mav 28,140,32
v_add_f32 v74, v74, v75
mav 16,144,36
R 48,45056
mav 20,148,36
mav 24,152,36
D 83,2048
mav 28,156,36
s_waitcnt lgkmcnt(1)
mav 16,160,40
R 52,46080
mav 20,164,40
mav 24,168,40
D 83,3072
mav 28,172,40
mav 16,176,44
R 56,47104
mav 20,180,44
mav 24,184,44
buffer_load_dwordx4 v[124:127], v90, s[36:39], s49 offen sc1
mav 28,188,44
s_waitcnt lgkmcnt(1)
mav 16,192,48
R 60,48128
mav 20,196,48
mav 24,200,48
buffer_load_dwordx4 v[128:131], v90, s[36:39], s49 offen offset:1024 sc1
mav 28,204,48
mav 16,208,52
mav 20,212,52
mav 24,216,52
v_add_u32 v72, 1, v72
mav 28,220,52
s_waitcnt lgkmcnt(0)
mav 16,224,56
mav 20,228,56
mav 24,232,56
mav 28,236,56
mav 16,240,60
s_waitcnt vmcnt(10)
buffer_store_dwordx2 v[72:73], v81, s[36:39], s52 offen sc1
mav 20,244,60
mav 24,248,60
ds_write_b32 v97, v74 offset:0
mav 28,252,60
mvv 16,116,164
mvv 20,120,164
mvv 24,116,168
s_xor_b32 s50, s50, 0x10000
mvv 28,120,168
mvv 16,116,172
mvv 20,120,172
v_xor_b32 v80, 0x10000, v80
mvv 24,116,176
s_add_u32 s49, s49, 346112
mvv 28,120,176
s_waitcnt lgkmcnt(0)
s_barrier
mvv 16,116,180
R 32,0
ds_read_b128 v[204:207], v98 offset:0
mvv 20,120,180
buffer_load_dwordx4 v[132:135], v86, s[36:39], s49 offen sc1
mvv 24,116,184
mvv 28,120,184
s_waitcnt vmcnt(11)
mvv 16,116,188
R 36,1024
mvv 20,120,188
buffer_load_dwordx4 v[136:139], v86, s[36:39], s49 offen offset:1024 sc1
s_waitcnt vmcnt(11)
mvv 24,116,192
mvv 28,120,192
s_waitcnt vmcnt(10)
mvv 16,116,196
R 40,2048
mvv 20,120,196
s_waitcnt vmcnt(9)
mvv 24,116,200
buffer_load_dwordx4 v[140:143], v87, s[36:39], s49 offen sc1
mvv 28,120,200
s_add_u32 s60, s60, 1
s_cbranch_scc0 LR1_1
s_waitcnt lgkmcnt(1)
v_mfma_f32_16x16x32_bf16 v[0:3], a[0:3], v[32:35], v[100:103]
R 44,3072
v_add_f32 v204, v204, v205
v_mfma_f32_16x16x32_bf16 v[4:7], a[4:7], v[32:35], v[104:107]
v_add_f32 v206, v206, v207
v_add_f32 v204, v204, v206
v_mfma_f32_16x16x32_bf16 v[8:11], a[8:11], v[32:35], v[108:111]
buffer_load_dwordx4 v[144:147], v87, s[36:39], s49 offen offset:1024 sc1
v_add_f32_dpp v205, v204, v204 quad_perm:[1,0,3,2] row_mask:0xf bank_mask:0xf
v_mfma_f32_16x16x32_bf16 v[12:15], a[12:15], v[32:35], v[112:115]
buffer_store_dword v205, v99, s[36:39], s49 offen sc1
v_cvt_pk_bf16_f32 v64, v16, v17
mav 0,16,36
R 48,4096
v_cvt_pk_bf16_f32 v65, v18, v19
mav 4,20,36
v_cvt_pk_bf16_f32 v66, v20, v21
v_cvt_pk_bf16_f32 v67, v22, v23
mav 8,24,36
buffer_store_dwordx4 v[64:67], v91, s[36:39], s49 offen sc1
v_cvt_pk_bf16_f32 v68, v24, v25
mav 12,28,36
v_cvt_pk_bf16_f32 v69, v26, v27
s_waitcnt lgkmcnt(1)
mav 0,32,40
R 52,5120
v_cvt_pk_bf16_f32 v70, v28, v29
mav 4,36,40
v_cvt_pk_bf16_f32 v71, v30, v31
v_mul_f32 v75, v16, v16
mav 8,40,40
buffer_store_dwordx4 v[68:71], v91, s[36:39], s49 offen offset:1024 sc1
v_add_f32 v74, v17, v16
mav 12,44,40
v_fmac_f32 v75, v17, v17
v_add_f32 v74, v18, v74
mav 0,48,44
R 56,6144
v_fmac_f32 v75, v18, v18
mav 4,52,44
v_add_f32 v74, v19, v74
v_fmac_f32 v75, v19, v19
mav 8,56,44
buffer_load_dwordx4 v[148:151], v88, s[36:39], s49 offen sc1
v_add_f32 v74, v20, v74
mav 12,60,44
v_fmac_f32 v75, v20, v20
s_waitcnt lgkmcnt(1)
mav 0,64,48
R 60,7168
v_add_f32 v74, v21, v74
mav 4,68,48
v_fmac_f32 v75, v21, v21
v_add_f32 v74, v22, v74
mav 8,72,48
buffer_load_dwordx4 v[152:155], v88, s[36:39], s49 offen offset:1024 sc1
v_fmac_f32 v75, v22, v22
mav 12,76,48
v_add_f32 v74, v23, v74
v_fmac_f32 v75, v23, v23
mav 0,80,52
R 32,8192
v_add_f32 v74, v24, v74
mav 4,84,52
v_fmac_f32 v75, v24, v24
v_add_f32 v74, v25, v74
mav 8,88,52
buffer_load_dwordx4 v[156:159], v89, s[36:39], s49 offen sc1
v_fmac_f32 v75, v25, v25
mav 12,92,52
s_add_u32 m0, s50, 0
s_waitcnt lgkmcnt(1)
mav 0,96,56
R 36,9216
v_add_f32 v74, v26, v74
mav 4,100,56
v_fmac_f32 v75, v26, v26
v_add_f32 v74, v27, v74
mav 8,104,56
buffer_load_dwordx4 v[160:163], v89, s[36:39], s49 offen offset:1024 sc1
v_fmac_f32 v75, v27, v27
mav 12,108,56
v_add_f32 v74, v28, v74
v_fmac_f32 v75, v28, v28
mav 0,112,60
R 40,10240
v_add_f32 v74, v29, v74
mav 4,116,60
v_fmac_f32 v75, v29, v29
v_add_f32 v74, v30, v74
mav 8,120,60
D 83,0
v_fmac_f32 v75, v30, v30
mav 12,124,60
v_add_f32 v74, v31, v74
s_waitcnt lgkmcnt(1)
mav 0,128,32
R 44,11264
v_fmac_f32 v75, v31, v31
mav 4,132,32
mav 8,136,32
D 83,1024
v_permlane32_swap_b32 v74, v75
mav 12,140,32
v_add_f32 v74, v74, v75
mav 0,144,36
R 48,12288
mav 4,148,36
mav 8,152,36
D 83,2048
mav 12,156,36
s_waitcnt lgkmcnt(1)
mav 0,160,40
R 52,13312
mav 4,164,40
mav 8,168,40
D 83,3072
mav 12,172,40
mav 0,176,44
R 56,14336
mav 4,180,44
mav 8,184,44
buffer_load_dwordx4 v[164:167], v90, s[36:39], s49 offen sc1
mav 12,188,44
s_waitcnt lgkmcnt(1)
mav 0,192,48
R 60,15360
mav 4,196,48
mav 8,200,48
buffer_load_dwordx4 v[168:171], v90, s[36:39], s49 offen offset:1024 sc1
mav 12,204,48
mav 0,208,52
mav 4,212,52
mav 8,216,52
v_add_u32 v72, 1, v72
mav 12,220,52
s_waitcnt lgkmcnt(0)
mav 0,224,56
mav 4,228,56
mav 8,232,56
mav 12,236,56
mav 0,240,60
s_waitcnt vmcnt(10)
buffer_store_dwordx2 v[72:73], v81, s[36:39], s52 offen sc1
mav 4,244,60
mav 8,248,60
ds_write_b32 v97, v74 offset:1024
mav 12,252,60
mvv 0,116,124
mvv 4,120,124
mvv 8,116,128
mvv 12,120,128
mvv 0,116,132
mvv 4,120,132
mvv 8,116,136
s_add_u32 s49, s49, 346112
mvv 12,120,136
s_waitcnt lgkmcnt(0)
s_barrier
mvv 0,116,140
R 32,32768
ds_read_b128 v[204:207], v98 offset:1024
mvv 4,120,140
buffer_load_dwordx4 v[172:175], v86, s[36:39], s49 offen sc1
mvv 8,116,144
mvv 12,120,144
s_waitcnt vmcnt(11)
mvv 0,116,148
R 36,33792
mvv 4,120,148
buffer_load_dwordx4 v[176:179], v86, s[36:39], s49 offen offset:1024 sc1
s_waitcnt vmcnt(11)
mvv 8,116,152
mvv 12,120,152
s_waitcnt vmcnt(10)
mvv 0,116,156
R 40,34816
mvv 4,120,156
s_waitcnt vmcnt(9)
mvv 8,116,160
buffer_load_dwordx4 v[180:183], v87, s[36:39], s49 offen sc1
mvv 12,120,160
s_waitcnt lgkmcnt(1)
v_mfma_f32_16x16x32_bf16 v[16:19], a[0:3], v[32:35], v[100:103]
R 44,35840
v_add_f32 v204, v204, v205
v_mfma_f32_16x16x32_bf16 v[20:23], a[4:7], v[32:35], v[104:107]
v_add_f32 v206, v206, v207
v_add_f32 v204, v204, v206
v_mfma_f32_16x16x32_bf16 v[24:27], a[8:11], v[32:35], v[108:111]
buffer_load_dwordx4 v[184:187], v87, s[36:39], s49 offen offset:1024 sc1
v_add_f32_dpp v205, v204, v204 quad_perm:[1,0,3,2] row_mask:0xf bank_mask:0xf
v_mfma_f32_16x16x32_bf16 v[28:31], a[12:15], v[32:35], v[112:115]
buffer_store_dword v205, v99, s[36:39], s49 offen sc1
v_cvt_pk_bf16_f32 v64, v0, v1
mav 16,16,36
R 48,36864
v_cvt_pk_bf16_f32 v65, v2, v3
mav 20,20,36
v_cvt_pk_bf16_f32 v66, v4, v5
v_cvt_pk_bf16_f32 v67, v6, v7
mav 24,24,36
ds_write_b128 v94, v[64:67] offset:0
mav 28,28,36
buffer_load_dwordx4 v[188:191], v88, s[36:39], s49 offen sc1
s_waitcnt lgkmcnt(2)
mav 16,32,40
R 52,37888
v_cvt_pk_bf16_f32 v68, v8, v9
mav 20,36,40
v_cvt_pk_bf16_f32 v69, v10, v11
v_cvt_pk_bf16_f32 v70, v12, v13
mav 24,40,40
v_cvt_pk_bf16_f32 v71, v14, v15
mav 28,44,40
ds_write_b128 v95, v[68:71] offset:0
mav 16,48,44
R 56,38912
buffer_load_dwordx4 v[192:195], v88, s[36:39], s49 offen offset:1024 sc1
mav 20,52,44
mav 24,56,44
s_waitcnt lgkmcnt(1)
ds_read_b128 v[64:67], v96 offset:0
mav 28,60,44
ds_read_b128 v[68:71], v96 offset:1024
mav 16,64,48
R 60,39936
buffer_load_dwordx4 v[196:199], v89, s[36:39], s49 offen sc1
mav 20,68,48
mav 24,72,48
mav 28,76,48
mav 16,80,52
R 32,40960
buffer_load_dwordx4 v[200:203], v89, s[36:39], s49 offen offset:1024 sc1
mav 20,84,52
mav 24,88,52
mav 28,92,52
s_add_u32 m0, s50, 32768
s_waitcnt lgkmcnt(1)
mav 16,96,56
R 36,41984
buffer_store_dwordx4 v[64:67], v92, s[44:47], s54 offen
mav 20,100,56
mav 24,104,56
mav 28,108,56
mav 16,112,60
R 40,43008
buffer_store_dwordx4 v[68:71], v93, s[44:47], s54 offen
mav 20,116,60
mav 24,120,60
mav 28,124,60
s_waitcnt lgkmcnt(1)
mav 16,128,32
R 44,44032
D 83,0
mav 20,132,32
mav 24,136,32
mav 28,140,32
mav 16,144,36
R 48,45056
D 83,1024
mav 20,148,36
mav 24,152,36
mav 28,156,36
s_waitcnt lgkmcnt(1)
mav 16,160,40
R 52,46080
D 83,2048
mav 20,164,40
s_sub_u32 s54, s54, 0x8000
mav 24,168,40
mav 28,172,40
mav 16,176,44
R 56,47104
D 83,3072
mav 20,180,44
mav 24,184,44
mav 28,188,44
s_waitcnt lgkmcnt(1)
mav 16,192,48
R 60,48128
buffer_load_dwordx4 v[124:127], v90, s[36:39], s49 offen sc1
mav 20,196,48
mav 24,200,48
mav 28,204,48
mav 16,208,52
buffer_load_dwordx4 v[128:131], v90, s[36:39], s49 offen offset:1024 sc1
mav 20,212,52
mav 24,216,52
v_add_u32 v72, 68, v72
mav 28,220,52
s_waitcnt lgkmcnt(0)
mav 16,224,56
mav 20,228,56
mav 24,232,56
mav 28,236,56
mav 16,240,60
s_waitcnt vmcnt(12)
buffer_store_dwordx2 v[72:73], v81, s[36:39], s52 offen sc1
mav 20,244,60
mav 24,248,60
mav 28,252,60
mvv 16,116,164
mvv 20,120,164
mvv 24,116,168
s_xor_b32 s50, s50, 0x10000
mvv 28,120,168
mvv 16,116,172
mvv 20,120,172
v_xor_b32 v80, 0x10000, v80
mvv 24,116,176
s_add_u32 s49, s49, 346112
mvv 28,120,176
s_barrier
mvv 16,116,180
R 32,0
mvv 20,120,180
buffer_load_dwordx4 v[132:135], v86, s[36:39], s49 offen sc1
mvv 24,116,184
mvv 28,120,184
s_waitcnt vmcnt(13)
mvv 16,116,188
R 36,1024
mvv 20,120,188
buffer_load_dwordx4 v[136:139], v86, s[36:39], s49 offen offset:1024 sc1
s_waitcnt vmcnt(13)
mvv 24,116,192
mvv 28,120,192
s_waitcnt vmcnt(12)
mvv 16,116,196
R 40,2048
mvv 20,120,196
s_waitcnt vmcnt(11)
mvv 24,116,200
buffer_load_dwordx4 v[140:143], v87, s[36:39], s49 offen sc1
mvv 28,120,200
s_waitcnt lgkmcnt(1)
v_mfma_f32_16x16x32_bf16 v[0:3], a[0:3], v[32:35], v[100:103]
R 44,3072
v_cvt_pk_bf16_f32 v64, v16, v17
v_mfma_f32_16x16x32_bf16 v[4:7], a[4:7], v[32:35], v[104:107]
v_cvt_pk_bf16_f32 v65, v18, v19
v_cvt_pk_bf16_f32 v66, v20, v21
v_mfma_f32_16x16x32_bf16 v[8:11], a[8:11], v[32:35], v[108:111]
buffer_load_dwordx4 v[144:147], v87, s[36:39], s49 offen offset:1024 sc1
v_cvt_pk_bf16_f32 v67, v22, v23
v_mfma_f32_16x16x32_bf16 v[12:15], a[12:15], v[32:35], v[112:115]
ds_write_b128 v94, v[64:67] offset:0
mav 0,16,36
R 48,4096
v_cvt_pk_bf16_f32 v68, v24, v25
mav 4,20,36
v_cvt_pk_bf16_f32 v69, v26, v27
v_cvt_pk_bf16_f32 v70, v28, v29
mav 8,24,36
v_cvt_pk_bf16_f32 v71, v30, v31
mav 12,28,36
buffer_load_dwordx4 v[148:151], v88, s[36:39], s49 offen sc1
s_waitcnt lgkmcnt(2)
mav 0,32,40
R 52,5120
mav 4,36,40
ds_write_b128 v95, v[68:71] offset:0
mav 8,40,40
mav 12,44,40
buffer_load_dwordx4 v[152:155], v88, s[36:39], s49 offen offset:1024 sc1
mav 0,48,44
R 56,6144
mav 4,52,44
s_waitcnt lgkmcnt(1)
ds_read_b128 v[64:67], v96 offset:0
mav 8,56,44
ds_read_b128 v[68:71], v96 offset:1024
mav 12,60,44
mav 0,64,48
R 60,7168
buffer_load_dwordx4 v[156:159], v89, s[36:39], s49 offen sc1
mav 4,68,48
mav 8,72,48
mav 12,76,48
mav 0,80,52
R 32,8192
buffer_load_dwordx4 v[160:163], v89, s[36:39], s49 offen offset:1024 sc1
mav 4,84,52
mav 8,88,52
mav 12,92,52
s_add_u32 m0, s50, 0
s_waitcnt lgkmcnt(1)
mav 0,96,56
R 36,9216
buffer_store_dwordx4 v[64:67], v92, s[44:47], s54 offen
mav 4,100,56
mav 8,104,56
mav 12,108,56
mav 0,112,60
R 40,10240
buffer_store_dwordx4 v[68:71], v93, s[44:47], s54 offen
mav 4,116,60
mav 8,120,60
mav 12,124,60
s_waitcnt lgkmcnt(1)
mav 0,128,32
R 44,11264
D 83,0
mav 4,132,32
mav 8,136,32
mav 12,140,32
mav 0,144,36
R 48,12288
D 83,1024
mav 4,148,36
mav 8,152,36
mav 12,156,36
s_waitcnt lgkmcnt(1)
mav 0,160,40
R 52,13312
D 83,2048
mav 4,164,40
s_sub_u32 s54, s54, 0x8000
mav 8,168,40
mav 12,172,40
mav 0,176,44
R 56,14336
D 83,3072
mav 4,180,44
mav 8,184,44
mav 12,188,44
s_waitcnt lgkmcnt(1)
mav 0,192,48
R 60,15360
buffer_load_dwordx4 v[164:167], v90, s[36:39], s49 offen sc1
mav 4,196,48
mav 8,200,48
mav 12,204,48
mav 0,208,52
buffer_load_dwordx4 v[168:171], v90, s[36:39], s49 offen offset:1024 sc1
mav 4,212,52
mav 8,216,52
mav 12,220,52
s_waitcnt lgkmcnt(0)
mav 0,224,56
mav 4,228,56
mav 8,232,56
mav 12,236,56
mav 0,240,60
mav 4,244,60
mav 8,248,60
mav 12,252,60
s_waitcnt vmcnt(18)
mvv 0,116,124
mvv 4,120,124
s_waitcnt vmcnt(17)
mvv 8,116,128
mvv 12,120,128
s_waitcnt vmcnt(15)
mvv 0,116,132
mvv 4,120,132
s_waitcnt vmcnt(14)
mvv 8,116,136
s_add_u32 s49, s49, 346112
mvv 12,120,136
s_barrier
s_waitcnt vmcnt(13)
mvv 0,116,140
R 32,32768
mvv 4,120,140
buffer_load_dwordx4 v[172:175], v86, s[36:39], s49 offen sc1
s_waitcnt vmcnt(13)
mvv 8,116,144
mvv 12,120,144
s_waitcnt vmcnt(12)
mvv 0,116,148
R 36,33792
mvv 4,120,148
buffer_load_dwordx4 v[176:179], v86, s[36:39], s49 offen offset:1024 sc1
s_waitcnt vmcnt(12)
mvv 8,116,152
mvv 12,120,152
s_waitcnt vmcnt(11)
mvv 0,116,156
R 40,34816
mvv 4,120,156
s_waitcnt vmcnt(10)
mvv 8,116,160
buffer_load_dwordx4 v[180:183], v87, s[36:39], s49 offen sc1
mvv 12,120,160
s_waitcnt lgkmcnt(1)
v_mfma_f32_16x16x32_bf16 v[16:19], a[0:3], v[32:35], v[100:103]
R 44,35840
v_cvt_pk_bf16_f32 v64, v0, v1
v_mfma_f32_16x16x32_bf16 v[20:23], a[4:7], v[32:35], v[104:107]
v_cvt_pk_bf16_f32 v65, v2, v3
v_cvt_pk_bf16_f32 v66, v4, v5
v_mfma_f32_16x16x32_bf16 v[24:27], a[8:11], v[32:35], v[108:111]
buffer_load_dwordx4 v[184:187], v87, s[36:39], s49 offen offset:1024 sc1
v_cvt_pk_bf16_f32 v67, v6, v7
v_mfma_f32_16x16x32_bf16 v[28:31], a[12:15], v[32:35], v[112:115]
ds_write_b128 v94, v[64:67] offset:0
mav 16,16,36
R 48,36864
v_cvt_pk_bf16_f32 v68, v8, v9
mav 20,20,36
v_cvt_pk_bf16_f32 v69, v10, v11
v_cvt_pk_bf16_f32 v70, v12, v13
mav 24,24,36
v_cvt_pk_bf16_f32 v71, v14, v15
mav 28,28,36
buffer_load_dwordx4 v[188:191], v88, s[36:39], s49 offen sc1
s_waitcnt lgkmcnt(2)
mav 16,32,40
R 52,37888
mav 20,36,40
ds_write_b128 v95, v[68:71] offset:0
mav 24,40,40
mav 28,44,40
buffer_load_dwordx4 v[192:195], v88, s[36:39], s49 offen offset:1024 sc1
mav 16,48,44
R 56,38912
mav 20,52,44
s_waitcnt lgkmcnt(1)
ds_read_b128 v[64:67], v96 offset:0
mav 24,56,44
ds_read_b128 v[68:71], v96 offset:1024
mav 28,60,44
mav 16,64,48
R 60,39936
buffer_load_dwordx4 v[196:199], v89, s[36:39], s49 offen sc1
mav 20,68,48
mav 24,72,48
mav 28,76,48
mav 16,80,52
R 32,40960
buffer_load_dwordx4 v[200:203], v89, s[36:39], s49 offen offset:1024 sc1
mav 20,84,52
mav 24,88,52
mav 28,92,52
s_add_u32 m0, s50, 32768
s_waitcnt lgkmcnt(1)
mav 16,96,56
R 36,41984
buffer_store_dwordx4 v[64:67], v92, s[44:47], s54 offen
mav 20,100,56
mav 24,104,56
mav 28,108,56
mav 16,112,60
R 40,43008
buffer_store_dwordx4 v[68:71], v93, s[44:47], s54 offen
mav 20,116,60
mav 24,120,60
mav 28,124,60
s_waitcnt lgkmcnt(1)
mav 16,128,32
R 44,44032
D 83,0
mav 20,132,32
mav 24,136,32
mav 28,140,32
mav 16,144,36
R 48,45056
D 83,1024
mav 20,148,36
mav 24,152,36
mav 28,156,36
s_waitcnt lgkmcnt(1)
mav 16,160,40
R 52,46080
D 83,2048
mav 20,164,40
s_sub_u32 s54, s54, 0x8000
mav 24,168,40
mav 28,172,40
mav 16,176,44
R 56,47104
D 83,3072
mav 20,180,44
mav 24,184,44
mav 28,188,44
s_waitcnt lgkmcnt(1)
mav 16,192,48
R 60,48128
buffer_load_dwordx4 v[124:127], v90, s[36:39], s49 offen sc1
mav 20,196,48
mav 24,200,48
mav 28,204,48
mav 16,208,52
buffer_load_dwordx4 v[128:131], v90, s[36:39], s49 offen offset:1024 sc1
mav 20,212,52
mav 24,216,52
mav 28,220,52
s_waitcnt lgkmcnt(0)
mav 16,224,56
mav 20,228,56
mav 24,232,56
mav 28,236,56
mav 16,240,60
mav 20,244,60
mav 24,248,60
mav 28,252,60
s_waitcnt vmcnt(17)
mvv 16,116,164
mvv 20,120,164
s_waitcnt vmcnt(16)
mvv 24,116,168
s_xor_b32 s50, s50, 0x10000
mvv 28,120,168
s_waitcnt vmcnt(15)
mvv 16,116,172
mvv 20,120,172
v_xor_b32 v80, 0x10000, v80
s_waitcnt vmcnt(14)
mvv 24,116,176
s_add_u32 s49, s49, 346112
mvv 28,120,176
s_barrier
s_waitcnt vmcnt(13)
mvv 16,116,180
R 32,0
mvv 20,120,180
buffer_load_dwordx4 v[132:135], v86, s[36:39], s49 offen sc1
s_waitcnt vmcnt(13)
mvv 24,116,184
mvv 28,120,184
s_waitcnt vmcnt(12)
mvv 16,116,188
R 36,1024
mvv 20,120,188
buffer_load_dwordx4 v[136:139], v86, s[36:39], s49 offen offset:1024 sc1
s_waitcnt vmcnt(12)
mvv 24,116,192
mvv 28,120,192
s_waitcnt vmcnt(11)
mvv 16,116,196
R 40,2048
mvv 20,120,196
s_waitcnt vmcnt(10)
mvv 24,116,200
buffer_load_dwordx4 v[140:143], v87, s[36:39], s49 offen sc1
mvv 28,120,200
s_mov_b32 s60, -12
.p2align 6
LR1_2:
s_waitcnt lgkmcnt(1)
v_mfma_f32_16x16x32_bf16 v[0:3], a[0:3], v[32:35], v[100:103]
R 44,3072
v_cvt_pk_bf16_f32 v64, v16, v17
v_mfma_f32_16x16x32_bf16 v[4:7], a[4:7], v[32:35], v[104:107]
v_cvt_pk_bf16_f32 v65, v18, v19
v_cvt_pk_bf16_f32 v66, v20, v21
v_mfma_f32_16x16x32_bf16 v[8:11], a[8:11], v[32:35], v[108:111]
buffer_load_dwordx4 v[144:147], v87, s[36:39], s49 offen offset:1024 sc1
v_cvt_pk_bf16_f32 v67, v22, v23
v_mfma_f32_16x16x32_bf16 v[12:15], a[12:15], v[32:35], v[112:115]
ds_write_b128 v94, v[64:67] offset:0
mav 0,16,36
R 48,4096
v_cvt_pk_bf16_f32 v68, v24, v25
mav 4,20,36
v_cvt_pk_bf16_f32 v69, v26, v27
v_cvt_pk_bf16_f32 v70, v28, v29
mav 8,24,36
v_cvt_pk_bf16_f32 v71, v30, v31
mav 12,28,36
buffer_load_dwordx4 v[148:151], v88, s[36:39], s49 offen sc1
s_waitcnt lgkmcnt(2)
mav 0,32,40
R 52,5120
mav 4,36,40
ds_write_b128 v95, v[68:71] offset:0
mav 8,40,40
mav 12,44,40
buffer_load_dwordx4 v[152:155], v88, s[36:39], s49 offen offset:1024 sc1
mav 0,48,44
R 56,6144
mav 4,52,44
s_waitcnt lgkmcnt(1)
ds_read_b128 v[64:67], v96 offset:0
mav 8,56,44
ds_read_b128 v[68:71], v96 offset:1024
mav 12,60,44
mav 0,64,48
R 60,7168
buffer_load_dwordx4 v[156:159], v89, s[36:39], s49 offen sc1
mav 4,68,48
mav 8,72,48
mav 12,76,48
mav 0,80,52
R 32,8192
buffer_load_dwordx4 v[160:163], v89, s[36:39], s49 offen offset:1024 sc1
mav 4,84,52
mav 8,88,52
mav 12,92,52
s_add_u32 m0, s50, 0
s_waitcnt lgkmcnt(1)
mav 0,96,56
R 36,9216
buffer_store_dwordx4 v[64:67], v92, s[44:47], s54 offen
mav 4,100,56
mav 8,104,56
mav 12,108,56
mav 0,112,60
R 40,10240
buffer_store_dwordx4 v[68:71], v93, s[44:47], s54 offen
mav 4,116,60
mav 8,120,60
mav 12,124,60
s_waitcnt lgkmcnt(1)
mav 0,128,32
R 44,11264
D 83,0
mav 4,132,32
mav 8,136,32
mav 12,140,32
mav 0,144,36
R 48,12288
D 83,1024
mav 4,148,36
mav 8,152,36
mav 12,156,36
s_waitcnt lgkmcnt(1)
mav 0,160,40
R 52,13312
D 83,2048
mav 4,164,40
s_sub_u32 s54, s54, 0x8000
mav 8,168,40
mav 12,172,40
mav 0,176,44
R 56,14336
D 83,3072
mav 4,180,44
mav 8,184,44
mav 12,188,44
s_waitcnt lgkmcnt(1)
mav 0,192,48
R 60,15360
buffer_load_dwordx4 v[164:167], v90, s[36:39], s49 offen sc1
mav 4,196,48
mav 8,200,48
mav 12,204,48
mav 0,208,52
buffer_load_dwordx4 v[168:171], v90, s[36:39], s49 offen offset:1024 sc1
mav 4,212,52
mav 8,216,52
mav 12,220,52
s_waitcnt lgkmcnt(0)
mav 0,224,56
mav 4,228,56
mav 8,232,56
mav 12,236,56
mav 0,240,60
mav 4,244,60
mav 8,248,60
mav 12,252,60
s_waitcnt vmcnt(17)
mvv 0,116,124
mvv 4,120,124
s_waitcnt vmcnt(16)
mvv 8,116,128
mvv 12,120,128
s_waitcnt vmcnt(15)
mvv 0,116,132
mvv 4,120,132
s_waitcnt vmcnt(14)
mvv 8,116,136
s_add_u32 s49, s49, 346112
mvv 12,120,136
s_barrier
s_waitcnt vmcnt(13)
mvv 0,116,140
R 32,32768
mvv 4,120,140
buffer_load_dwordx4 v[172:175], v86, s[36:39], s49 offen sc1
s_waitcnt vmcnt(13)
mvv 8,116,144
mvv 12,120,144
s_waitcnt vmcnt(12)
mvv 0,116,148
R 36,33792
mvv 4,120,148
buffer_load_dwordx4 v[176:179], v86, s[36:39], s49 offen offset:1024 sc1
s_waitcnt vmcnt(12)
mvv 8,116,152
mvv 12,120,152
s_waitcnt vmcnt(11)
mvv 0,116,156
R 40,34816
mvv 4,120,156
s_waitcnt vmcnt(10)
mvv 8,116,160
buffer_load_dwordx4 v[180:183], v87, s[36:39], s49 offen sc1
mvv 12,120,160
s_waitcnt lgkmcnt(1)
v_mfma_f32_16x16x32_bf16 v[16:19], a[0:3], v[32:35], v[100:103]
R 44,35840
v_cvt_pk_bf16_f32 v64, v0, v1
v_mfma_f32_16x16x32_bf16 v[20:23], a[4:7], v[32:35], v[104:107]
v_cvt_pk_bf16_f32 v65, v2, v3
v_cvt_pk_bf16_f32 v66, v4, v5
v_mfma_f32_16x16x32_bf16 v[24:27], a[8:11], v[32:35], v[108:111]
buffer_load_dwordx4 v[184:187], v87, s[36:39], s49 offen offset:1024 sc1
v_cvt_pk_bf16_f32 v67, v6, v7
v_mfma_f32_16x16x32_bf16 v[28:31], a[12:15], v[32:35], v[112:115]
ds_write_b128 v94, v[64:67] offset:0
mav 16,16,36
R 48,36864
v_cvt_pk_bf16_f32 v68, v8, v9
mav 20,20,36
v_cvt_pk_bf16_f32 v69, v10, v11
v_cvt_pk_bf16_f32 v70, v12, v13
mav 24,24,36
v_cvt_pk_bf16_f32 v71, v14, v15
mav 28,28,36
buffer_load_dwordx4 v[188:191], v88, s[36:39], s49 offen sc1
s_waitcnt lgkmcnt(2)
mav 16,32,40
R 52,37888
mav 20,36,40
ds_write_b128 v95, v[68:71] offset:0
mav 24,40,40
mav 28,44,40
buffer_load_dwordx4 v[192:195], v88, s[36:39], s49 offen offset:1024 sc1
mav 16,48,44
R 56,38912
mav 20,52,44
s_waitcnt lgkmcnt(1)
ds_read_b128 v[64:67], v96 offset:0
mav 24,56,44
ds_read_b128 v[68:71], v96 offset:1024
mav 28,60,44
mav 16,64,48
R 60,39936
buffer_load_dwordx4 v[196:199], v89, s[36:39], s49 offen sc1
mav 20,68,48
mav 24,72,48
mav 28,76,48
mav 16,80,52
R 32,40960
buffer_load_dwordx4 v[200:203], v89, s[36:39], s49 offen offset:1024 sc1
mav 20,84,52
mav 24,88,52
mav 28,92,52
s_add_u32 m0, s50, 32768
s_waitcnt lgkmcnt(1)
mav 16,96,56
R 36,41984
buffer_store_dwordx4 v[64:67], v92, s[44:47], s54 offen
mav 20,100,56
mav 24,104,56
mav 28,108,56
mav 16,112,60
R 40,43008
buffer_store_dwordx4 v[68:71], v93, s[44:47], s54 offen
mav 20,116,60
mav 24,120,60
mav 28,124,60
s_waitcnt lgkmcnt(1)
mav 16,128,32
R 44,44032
D 83,0
mav 20,132,32
mav 24,136,32
mav 28,140,32
mav 16,144,36
R 48,45056
D 83,1024
mav 20,148,36
mav 24,152,36
mav 28,156,36
s_waitcnt lgkmcnt(1)
mav 16,160,40
R 52,46080
D 83,2048
mav 20,164,40
s_sub_u32 s54, s54, 0x8000
mav 24,168,40
mav 28,172,40
mav 16,176,44
R 56,47104
D 83,3072
mav 20,180,44
mav 24,184,44
mav 28,188,44
s_waitcnt lgkmcnt(1)
mav 16,192,48
R 60,48128
buffer_load_dwordx4 v[124:127], v90, s[36:39], s49 offen sc1
mav 20,196,48
mav 24,200,48
mav 28,204,48
mav 16,208,52
buffer_load_dwordx4 v[128:131], v90, s[36:39], s49 offen offset:1024 sc1
mav 20,212,52
mav 24,216,52
mav 28,220,52
s_waitcnt lgkmcnt(0)
mav 16,224,56
mav 20,228,56
mav 24,232,56
mav 28,236,56
mav 16,240,60
mav 20,244,60
mav 24,248,60
mav 28,252,60
s_waitcnt vmcnt(17)
mvv 16,116,164
mvv 20,120,164
s_waitcnt vmcnt(16)
mvv 24,116,168
s_xor_b32 s50, s50, 0x10000
mvv 28,120,168
s_waitcnt vmcnt(15)
mvv 16,116,172
mvv 20,120,172
v_xor_b32 v80, 0x10000, v80
s_waitcnt vmcnt(14)
mvv 24,116,176
s_add_u32 s49, s49, 346112
mvv 28,120,176
s_barrier
s_waitcnt vmcnt(13)
mvv 16,116,180
R 32,0
mvv 20,120,180
buffer_load_dwordx4 v[132:135], v86, s[36:39], s49 offen sc1
s_waitcnt vmcnt(13)
mvv 24,116,184
mvv 28,120,184
s_waitcnt vmcnt(12)
mvv 16,116,188
R 36,1024
mvv 20,120,188
buffer_load_dwordx4 v[136:139], v86, s[36:39], s49 offen offset:1024 sc1
s_waitcnt vmcnt(12)
mvv 24,116,192
mvv 28,120,192
s_waitcnt vmcnt(11)
mvv 16,116,196
R 40,2048
mvv 20,120,196
s_waitcnt vmcnt(10)
mvv 24,116,200
buffer_load_dwordx4 v[140:143], v87, s[36:39], s49 offen sc1
mvv 28,120,200
s_add_u32 s60, s60, 1
s_cbranch_scc0 LR1_2
s_waitcnt lgkmcnt(1)
v_mfma_f32_16x16x32_bf16 v[0:3], a[0:3], v[32:35], v[100:103]
R 44,3072
v_cvt_pk_bf16_f32 v64, v16, v17
v_mfma_f32_16x16x32_bf16 v[4:7], a[4:7], v[32:35], v[104:107]
v_cvt_pk_bf16_f32 v65, v18, v19
v_cvt_pk_bf16_f32 v66, v20, v21
v_mfma_f32_16x16x32_bf16 v[8:11], a[8:11], v[32:35], v[108:111]
buffer_load_dwordx4 v[144:147], v87, s[36:39], s49 offen offset:1024 sc1
v_cvt_pk_bf16_f32 v67, v22, v23
v_mfma_f32_16x16x32_bf16 v[12:15], a[12:15], v[32:35], v[112:115]
ds_write_b128 v94, v[64:67] offset:0
mav 0,16,36
R 48,4096
v_cvt_pk_bf16_f32 v68, v24, v25
mav 4,20,36
v_cvt_pk_bf16_f32 v69, v26, v27
v_cvt_pk_bf16_f32 v70, v28, v29
mav 8,24,36
v_cvt_pk_bf16_f32 v71, v30, v31
mav 12,28,36
buffer_load_dwordx4 v[148:151], v88, s[36:39], s49 offen sc1
s_waitcnt lgkmcnt(2)
mav 0,32,40
R 52,5120
mav 4,36,40
ds_write_b128 v95, v[68:71] offset:0
mav 8,40,40
mav 12,44,40
buffer_load_dwordx4 v[152:155], v88, s[36:39], s49 offen offset:1024 sc1
mav 0,48,44
R 56,6144
mav 4,52,44
s_waitcnt lgkmcnt(1)
ds_read_b128 v[64:67], v96 offset:0
mav 8,56,44
ds_read_b128 v[68:71], v96 offset:1024
mav 12,60,44
mav 0,64,48
R 60,7168
buffer_load_dwordx4 v[156:159], v89, s[36:39], s49 offen sc1
mav 4,68,48
mav 8,72,48
mav 12,76,48
mav 0,80,52
R 32,8192
buffer_load_dwordx4 v[160:163], v89, s[36:39], s49 offen offset:1024 sc1
mav 4,84,52
mav 8,88,52
mav 12,92,52
s_add_u32 m0, s50, 0
s_waitcnt lgkmcnt(1)
mav 0,96,56
R 36,9216
buffer_store_dwordx4 v[64:67], v92, s[44:47], s54 offen
mav 4,100,56
mav 8,104,56
mav 12,108,56
)"
R"(
mav 0,112,60
R 40,10240
buffer_store_dwordx4 v[68:71], v93, s[44:47], s54 offen
mav 4,116,60
mav 8,120,60
mav 12,124,60
s_waitcnt lgkmcnt(1)
mav 0,128,32
R 44,11264
D 83,0
mav 4,132,32
mav 8,136,32
mav 12,140,32
mav 0,144,36
R 48,12288
D 83,1024
mav 4,148,36
mav 8,152,36
mav 12,156,36
s_waitcnt lgkmcnt(1)
mav 0,160,40
R 52,13312
D 83,2048
mav 4,164,40
s_sub_u32 s54, s54, 0x8000
mav 8,168,40
mav 12,172,40
mav 0,176,44
R 56,14336
D 83,3072
mav 4,180,44
mav 8,184,44
mav 12,188,44
s_waitcnt lgkmcnt(1)
mav 0,192,48
R 60,15360
buffer_load_dwordx4 v[164:167], v90, s[36:39], s49 offen sc1
mav 4,196,48
mav 8,200,48
mav 12,204,48
mav 0,208,52
buffer_load_dwordx4 v[168:171], v90, s[36:39], s49 offen offset:1024 sc1
mav 4,212,52
mav 8,216,52
mav 12,220,52
s_waitcnt lgkmcnt(0)
mav 0,224,56
mav 4,228,56
mav 8,232,56
mav 12,236,56
mav 0,240,60
mav 4,244,60
mav 8,248,60
mav 12,252,60
s_waitcnt vmcnt(17)
mvv 0,116,124
mvv 4,120,124
s_waitcnt vmcnt(16)
mvv 8,116,128
mvv 12,120,128
s_waitcnt vmcnt(15)
mvv 0,116,132
mvv 4,120,132
s_waitcnt vmcnt(14)
mvv 8,116,136
s_add_u32 s49, s49, 346112
mvv 12,120,136
s_barrier
s_waitcnt vmcnt(13)
mvv 0,116,140
R 32,32768
mvv 4,120,140
buffer_load_dwordx4 v[172:175], v86, s[36:39], s49 offen sc1
s_waitcnt vmcnt(13)
mvv 8,116,144
mvv 12,120,144
s_waitcnt vmcnt(12)
mvv 0,116,148
R 36,33792
mvv 4,120,148
buffer_load_dwordx4 v[176:179], v86, s[36:39], s49 offen offset:1024 sc1
s_waitcnt vmcnt(12)
mvv 8,116,152
mvv 12,120,152
s_waitcnt vmcnt(11)
mvv 0,116,156
R 40,34816
mvv 4,120,156
s_waitcnt vmcnt(10)
mvv 8,116,160
buffer_load_dwordx4 v[180:183], v87, s[36:39], s49 offen sc1
mvv 12,120,160
s_waitcnt lgkmcnt(1)
v_mfma_f32_16x16x32_bf16 v[16:19], a[0:3], v[32:35], v[100:103]
R 44,35840
v_cvt_pk_bf16_f32 v64, v0, v1
v_mfma_f32_16x16x32_bf16 v[20:23], a[4:7], v[32:35], v[104:107]
v_cvt_pk_bf16_f32 v65, v2, v3
v_cvt_pk_bf16_f32 v66, v4, v5
v_mfma_f32_16x16x32_bf16 v[24:27], a[8:11], v[32:35], v[108:111]
buffer_load_dwordx4 v[184:187], v87, s[36:39], s49 offen offset:1024 sc1
v_cvt_pk_bf16_f32 v67, v6, v7
v_mfma_f32_16x16x32_bf16 v[28:31], a[12:15], v[32:35], v[112:115]
ds_write_b128 v94, v[64:67] offset:0
mav 16,16,36
R 48,36864
v_cvt_pk_bf16_f32 v68, v8, v9
mav 20,20,36
v_cvt_pk_bf16_f32 v69, v10, v11
v_cvt_pk_bf16_f32 v70, v12, v13
mav 24,24,36
v_cvt_pk_bf16_f32 v71, v14, v15
mav 28,28,36
buffer_load_dwordx4 v[188:191], v88, s[36:39], s49 offen sc1
s_waitcnt lgkmcnt(2)
mav 16,32,40
R 52,37888
mav 20,36,40
ds_write_b128 v95, v[68:71] offset:0
mav 24,40,40
mav 28,44,40
buffer_load_dwordx4 v[192:195], v88, s[36:39], s49 offen offset:1024 sc1
mav 16,48,44
R 56,38912
mav 20,52,44
s_waitcnt lgkmcnt(1)
ds_read_b128 v[64:67], v96 offset:0
mav 24,56,44
ds_read_b128 v[68:71], v96 offset:1024
mav 28,60,44
mav 16,64,48
R 60,39936
buffer_load_dwordx4 v[196:199], v89, s[36:39], s49 offen sc1
mav 20,68,48
mav 24,72,48
mav 28,76,48
mav 16,80,52
R 32,40960
buffer_load_dwordx4 v[200:203], v89, s[36:39], s49 offen offset:1024 sc1
mav 20,84,52
mav 24,88,52
mav 28,92,52
s_add_u32 m0, s50, 32768
s_waitcnt lgkmcnt(1)
mav 16,96,56
R 36,41984
buffer_store_dwordx4 v[64:67], v92, s[44:47], s54 offen
mav 20,100,56
mav 24,104,56
mav 28,108,56
mav 16,112,60
R 40,43008
buffer_store_dwordx4 v[68:71], v93, s[44:47], s54 offen
mav 20,116,60
mav 24,120,60
mav 28,124,60
s_waitcnt lgkmcnt(1)
mav 16,128,32
R 44,44032
D 83,0
mav 20,132,32
mav 24,136,32
mav 28,140,32
mav 16,144,36
R 48,45056
D 83,1024
mav 20,148,36
mav 24,152,36
mav 28,156,36
s_waitcnt lgkmcnt(1)
mav 16,160,40
R 52,46080
D 83,2048
mav 20,164,40
s_sub_u32 s54, s54, 0x8000
mav 24,168,40
mav 28,172,40
mav 16,176,44
R 56,47104
D 83,3072
mav 20,180,44
mav 24,184,44
mav 28,188,44
s_waitcnt lgkmcnt(1)
mav 16,192,48
R 60,48128
buffer_load_dwordx4 v[124:127], v90, s[36:39], s49 offen sc1
mav 20,196,48
mav 24,200,48
mav 28,204,48
mav 16,208,52
buffer_load_dwordx4 v[128:131], v90, s[36:39], s49 offen offset:1024 sc1
mav 20,212,52
mav 24,216,52
mav 28,220,52
s_waitcnt lgkmcnt(0)
mav 16,224,56
mav 20,228,56
mav 24,232,56
mav 28,236,56
mav 16,240,60
mav 20,244,60
mav 24,248,60
mav 28,252,60
s_waitcnt vmcnt(17)
mvv 16,116,164
mvv 20,120,164
s_waitcnt vmcnt(16)
mvv 24,116,168
mvv 28,120,168
s_waitcnt vmcnt(15)
mvv 16,116,172
mvv 20,120,172
v_xor_b32 v80, 0x10000, v80
s_waitcnt vmcnt(14)
mvv 24,116,176
s_add_u32 s49, s49, 346112
mvv 28,120,176
s_barrier
s_waitcnt vmcnt(13)
mvv 16,116,180
R 32,0
mvv 20,120,180
buffer_load_dwordx4 v[132:135], v86, s[36:39], s49 offen sc1
s_waitcnt vmcnt(13)
mvv 24,116,184
mvv 28,120,184
s_waitcnt vmcnt(12)
mvv 16,116,188
R 36,1024
mvv 20,120,188
buffer_load_dwordx4 v[136:139], v86, s[36:39], s49 offen offset:1024 sc1
s_waitcnt vmcnt(12)
mvv 24,116,192
mvv 28,120,192
s_waitcnt vmcnt(11)
mvv 16,116,196
R 40,2048
mvv 20,120,196
s_waitcnt vmcnt(10)
mvv 24,116,200
buffer_load_dwordx4 v[140:143], v87, s[36:39], s49 offen sc1
mvv 28,120,200
s_waitcnt lgkmcnt(1)
v_mfma_f32_16x16x32_bf16 v[0:3], a[0:3], v[32:35], v[100:103]
R 44,3072
v_cvt_pk_bf16_f32 v64, v16, v17
v_mfma_f32_16x16x32_bf16 v[4:7], a[4:7], v[32:35], v[104:107]
v_cvt_pk_bf16_f32 v65, v18, v19
v_cvt_pk_bf16_f32 v66, v20, v21
v_mfma_f32_16x16x32_bf16 v[8:11], a[8:11], v[32:35], v[108:111]
buffer_load_dwordx4 v[144:147], v87, s[36:39], s49 offen offset:1024 sc1
v_cvt_pk_bf16_f32 v67, v22, v23
v_mfma_f32_16x16x32_bf16 v[12:15], a[12:15], v[32:35], v[112:115]
ds_write_b128 v94, v[64:67] offset:0
mav 0,16,36
R 48,4096
v_cvt_pk_bf16_f32 v68, v24, v25
mav 4,20,36
v_cvt_pk_bf16_f32 v69, v26, v27
v_cvt_pk_bf16_f32 v70, v28, v29
mav 8,24,36
v_cvt_pk_bf16_f32 v71, v30, v31
mav 12,28,36
buffer_load_dwordx4 v[148:151], v88, s[36:39], s49 offen sc1
s_waitcnt lgkmcnt(2)
mav 0,32,40
R 52,5120
mav 4,36,40
ds_write_b128 v95, v[68:71] offset:0
mav 8,40,40
mav 12,44,40
buffer_load_dwordx4 v[152:155], v88, s[36:39], s49 offen offset:1024 sc1
mav 0,48,44
R 56,6144
mav 4,52,44
s_waitcnt lgkmcnt(1)
ds_read_b128 v[64:67], v96 offset:0
mav 8,56,44
ds_read_b128 v[68:71], v96 offset:1024
mav 12,60,44
mav 0,64,48
R 60,7168
buffer_load_dwordx4 v[156:159], v89, s[36:39], s49 offen sc1
mav 4,68,48
mav 8,72,48
mav 12,76,48
mav 0,80,52
R 32,8192
buffer_load_dwordx4 v[160:163], v89, s[36:39], s49 offen offset:1024 sc1
mav 4,84,52
mav 8,88,52
mav 12,92,52
s_waitcnt lgkmcnt(1)
mav 0,96,56
R 36,9216
buffer_store_dwordx4 v[64:67], v92, s[44:47], s54 offen
mav 4,100,56
mav 8,104,56
mav 12,108,56
mav 0,112,60
R 40,10240
buffer_store_dwordx4 v[68:71], v93, s[44:47], s54 offen
mav 4,116,60
mav 8,120,60
mav 12,124,60
s_waitcnt lgkmcnt(1)
mav 0,128,32
R 44,11264
mav 4,132,32
mav 8,136,32
mav 12,140,32
mav 0,144,36
R 48,12288
mav 4,148,36
mav 8,152,36
mav 12,156,36
s_waitcnt lgkmcnt(1)
mav 0,160,40
R 52,13312
s_sub_u32 s54, s54, 0x8000
mav 4,164,40
mav 8,168,40
mav 12,172,40
buffer_load_dwordx4 v[164:167], v90, s[36:39], s49 offen sc1
mav 0,176,44
R 56,14336
mav 4,180,44
mav 8,184,44
mav 12,188,44
buffer_load_dwordx4 v[168:171], v90, s[36:39], s49 offen offset:1024 sc1
s_waitcnt lgkmcnt(1)
mav 0,192,48
R 60,15360
mav 4,196,48
mav 8,200,48
mav 12,204,48
mav 0,208,52
mav 4,212,52
mav 8,216,52
mav 12,220,52
s_waitcnt lgkmcnt(0)
mav 0,224,56
mav 4,228,56
mav 8,232,56
mav 12,236,56
mav 0,240,60
mav 4,244,60
mav 8,248,60
mav 12,252,60
s_waitcnt vmcnt(13)
mvv 0,116,124
mvv 4,120,124
s_waitcnt vmcnt(12)
mvv 8,116,128
mvv 12,120,128
s_waitcnt vmcnt(11)
mvv 0,116,132
mvv 4,120,132
s_waitcnt vmcnt(10)
mvv 8,116,136
s_add_u32 s49, s49, 346112
mvv 12,120,136
s_barrier
s_waitcnt vmcnt(9)
mvv 0,116,140
R 32,32768
mvv 4,120,140
buffer_load_dwordx4 v[172:175], v86, s[36:39], s49 offen sc1
s_waitcnt vmcnt(9)
mvv 8,116,144
mvv 12,120,144
s_waitcnt vmcnt(8)
mvv 0,116,148
R 36,33792
mvv 4,120,148
buffer_load_dwordx4 v[176:179], v86, s[36:39], s49 offen offset:1024 sc1
s_waitcnt vmcnt(8)
mvv 8,116,152
mvv 12,120,152
s_waitcnt vmcnt(7)
mvv 0,116,156
R 40,34816
mvv 4,120,156
s_waitcnt vmcnt(6)
mvv 8,116,160
buffer_load_dwordx4 v[180:183], v87, s[36:39], s49 offen sc1
mvv 12,120,160
s_waitcnt lgkmcnt(1)
v_mfma_f32_16x16x32_bf16 v[16:19], a[0:3], v[32:35], v[100:103]
R 44,35840
v_cvt_pk_bf16_f32 v64, v0, v1
v_mfma_f32_16x16x32_bf16 v[20:23], a[4:7], v[32:35], v[104:107]
v_cvt_pk_bf16_f32 v65, v2, v3
v_cvt_pk_bf16_f32 v66, v4, v5
v_mfma_f32_16x16x32_bf16 v[24:27], a[8:11], v[32:35], v[108:111]
buffer_load_dwordx4 v[184:187], v87, s[36:39], s49 offen offset:1024 sc1
v_cvt_pk_bf16_f32 v67, v6, v7
v_mfma_f32_16x16x32_bf16 v[28:31], a[12:15], v[32:35], v[112:115]
ds_write_b128 v94, v[64:67] offset:0
mav 16,16,36
R 48,36864
v_cvt_pk_bf16_f32 v68, v8, v9
mav 20,20,36
v_cvt_pk_bf16_f32 v69, v10, v11
v_cvt_pk_bf16_f32 v70, v12, v13
mav 24,24,36
v_cvt_pk_bf16_f32 v71, v14, v15
mav 28,28,36
buffer_load_dwordx4 v[188:191], v88, s[36:39], s49 offen sc1
s_waitcnt lgkmcnt(2)
mav 16,32,40
R 52,37888
mav 20,36,40
ds_write_b128 v95, v[68:71] offset:0
mav 24,40,40
mav 28,44,40
buffer_load_dwordx4 v[192:195], v88, s[36:39], s49 offen offset:1024 sc1
mav 16,48,44
R 56,38912
mav 20,52,44
s_waitcnt lgkmcnt(1)
ds_read_b128 v[64:67], v96 offset:0
mav 24,56,44
ds_read_b128 v[68:71], v96 offset:1024
mav 28,60,44
mav 16,64,48
R 60,39936
buffer_load_dwordx4 v[196:199], v89, s[36:39], s49 offen sc1
mav 20,68,48
mav 24,72,48
mav 28,76,48
mav 16,80,52
R 32,40960
buffer_load_dwordx4 v[200:203], v89, s[36:39], s49 offen offset:1024 sc1
mav 20,84,52
mav 24,88,52
mav 28,92,52
s_waitcnt lgkmcnt(1)
mav 16,96,56
R 36,41984
buffer_store_dwordx4 v[64:67], v92, s[44:47], s54 offen
mav 20,100,56
mav 24,104,56
mav 28,108,56
mav 16,112,60
R 40,43008
buffer_store_dwordx4 v[68:71], v93, s[44:47], s54 offen
mav 20,116,60
mav 24,120,60
mav 28,124,60
s_waitcnt lgkmcnt(1)
mav 16,128,32
R 44,44032
mav 20,132,32
mav 24,136,32
mav 28,140,32
mav 16,144,36
R 48,45056
mav 20,148,36
mav 24,152,36
mav 28,156,36
s_waitcnt lgkmcnt(1)
mav 16,160,40
R 52,46080
s_sub_u32 s54, s54, 0x8000
mav 20,164,40
mav 24,168,40
mav 28,172,40
mav 16,176,44
R 56,47104
mav 20,180,44
mav 24,184,44
mav 28,188,44
s_waitcnt lgkmcnt(1)
mav 16,192,48
R 60,48128
mav 20,196,48
mav 24,200,48
mav 28,204,48
mav 16,208,52
mav 20,212,52
mav 24,216,52
mav 28,220,52
s_waitcnt lgkmcnt(0)
mav 16,224,56
mav 20,228,56
mav 24,232,56
mav 28,236,56
mav 16,240,60
mav 20,244,60
mav 24,248,60
mav 28,252,60
s_waitcnt vmcnt(11)
mvv 16,116,164
mvv 20,120,164
s_waitcnt vmcnt(10)
mvv 24,116,168
mvv 28,120,168
s_waitcnt vmcnt(9)
mvv 16,116,172
mvv 20,120,172
s_waitcnt vmcnt(8)
mvv 24,116,176
mvv 28,120,176
s_waitcnt vmcnt(7)
mvv 16,116,180
mvv 20,120,180
s_waitcnt vmcnt(6)
mvv 24,116,184
mvv 28,120,184
s_waitcnt vmcnt(5)
mvv 16,116,188
mvv 20,120,188
s_waitcnt vmcnt(4)
mvv 24,116,192
mvv 28,120,192
s_waitcnt vmcnt(3)
mvv 16,116,196
mvv 20,120,196
s_waitcnt vmcnt(2)
mvv 24,116,200
mvv 28,120,200
s_nop 3
v_cvt_pk_bf16_f32 v64, v16, v17
v_cvt_pk_bf16_f32 v65, v18, v19
v_cvt_pk_bf16_f32 v66, v20, v21
v_cvt_pk_bf16_f32 v67, v22, v23
ds_write_b128 v94, v[64:67] offset:0
v_cvt_pk_bf16_f32 v68, v24, v25
v_cvt_pk_bf16_f32 v69, v26, v27
v_cvt_pk_bf16_f32 v70, v28, v29
v_cvt_pk_bf16_f32 v71, v30, v31
ds_write_b128 v95, v[68:71] offset:0
s_waitcnt lgkmcnt(0)
ds_read_b128 v[64:67], v96 offset:0
ds_read_b128 v[68:71], v96 offset:1024
s_waitcnt lgkmcnt(0)
buffer_store_dwordx4 v[64:67], v92, s[44:47], s54 offen
buffer_store_dwordx4 v[68:71], v93, s[44:47], s54 offen
s_endpgm
LvR2:
s_add_u32 s49, s49, 346112
s_barrier
s_add_u32 m0, s50, 0
s_nop 0
D 83,0
D 83,1024
D 83,2048
D 83,3072
s_add_u32 s49, s49, 346112
s_barrier
s_add_u32 m0, s50, 32768
s_nop 0
D 83,0
D 83,1024
D 83,2048
D 83,3072
buffer_load_dwordx4 v[124:127], v90, s[36:39], s49 offen sc1
buffer_load_dwordx4 v[128:131], v90, s[36:39], s49 offen offset:1024 sc1
s_xor_b32 s50, s50, 0x10000
s_add_u32 s49, s49, 346112
s_waitcnt vmcnt(6)
s_barrier
R 32,0
buffer_load_dwordx4 v[132:135], v86, s[36:39], s49 offen sc1
R 36,1024
buffer_load_dwordx4 v[136:139], v86, s[36:39], s49 offen offset:1024 sc1
R 40,2048
buffer_load_dwordx4 v[140:143], v87, s[36:39], s49 offen sc1
s_waitcnt lgkmcnt(1)
v_mfma_f32_16x16x32_bf16 v[0:3], a[0:3], v[32:35], v[100:103]
R 44,3072
v_mfma_f32_16x16x32_bf16 v[4:7], a[4:7], v[32:35], v[104:107]
v_mfma_f32_16x16x32_bf16 v[8:11], a[8:11], v[32:35], v[108:111]
buffer_load_dwordx4 v[144:147], v87, s[36:39], s49 offen offset:1024 sc1
v_mfma_f32_16x16x32_bf16 v[12:15], a[12:15], v[32:35], v[112:115]
mav 0,16,36
R 48,4096
mav 4,20,36
mav 8,24,36
mav 12,28,36
buffer_load_dwordx4 v[148:151], v88, s[36:39], s49 offen sc1
s_waitcnt lgkmcnt(1)
mav 0,32,40
R 52,5120
mav 4,36,40
mav 8,40,40
mav 12,44,40
buffer_load_dwordx4 v[152:155], v88, s[36:39], s49 offen offset:1024 sc1
mav 0,48,44
R 56,6144
mav 4,52,44
mav 8,56,44
mav 12,60,44
s_waitcnt lgkmcnt(1)
mav 0,64,48
R 60,7168
buffer_load_dwordx4 v[156:159], v89, s[36:39], s49 offen sc1
mav 4,68,48
mav 8,72,48
mav 12,76,48
mav 0,80,52
R 32,8192
buffer_load_dwordx4 v[160:163], v89, s[36:39], s49 offen offset:1024 sc1
mav 4,84,52
mav 8,88,52
mav 12,92,52
s_add_u32 m0, s50, 0
s_waitcnt lgkmcnt(1)
mav 0,96,56
R 36,9216
mav 4,100,56
D 83,0
mav 8,104,56
mav 12,108,56
mav 0,112,60
R 40,10240
mav 4,116,60
D 83,1024
mav 8,120,60
mav 12,124,60
s_waitcnt lgkmcnt(1)
mav 0,128,32
R 44,11264
mav 4,132,32
mav 8,136,32
D 83,2048
mav 12,140,32
mav 0,144,36
R 48,12288
mav 4,148,36
mav 8,152,36
D 83,3072
mav 12,156,36
s_waitcnt lgkmcnt(1)
mav 0,160,40
R 52,13312
mav 4,164,40
mav 8,168,40
mav 12,172,40
buffer_load_dwordx4 v[164:167], v90, s[36:39], s49 offen sc1
mav 0,176,44
R 56,14336
mav 4,180,44
mav 8,184,44
mav 12,188,44
buffer_load_dwordx4 v[168:171], v90, s[36:39], s49 offen offset:1024 sc1
s_waitcnt lgkmcnt(1)
mav 0,192,48
R 60,15360
mav 4,196,48
mav 8,200,48
mav 12,204,48
mav 0,208,52
mav 4,212,52
mav 8,216,52
mav 12,220,52
s_waitcnt lgkmcnt(0)
mav 0,224,56
mav 4,228,56
mav 8,232,56
mav 12,236,56
mav 0,240,60
mav 4,244,60
mav 8,248,60
mav 12,252,60
s_waitcnt vmcnt(15)
mvv 0,116,124
mvv 4,120,124
s_waitcnt vmcnt(14)
mvv 8,116,128
mvv 12,120,128
s_waitcnt vmcnt(13)
mvv 0,116,132
mvv 4,120,132
s_waitcnt vmcnt(12)
mvv 8,116,136
s_add_u32 s49, s49, 346112
mvv 12,120,136
s_barrier
s_waitcnt vmcnt(11)
mvv 0,116,140
R 32,32768
mvv 4,120,140
buffer_load_dwordx4 v[172:175], v86, s[36:39], s49 offen sc1
s_waitcnt vmcnt(11)
mvv 8,116,144
mvv 12,120,144
s_waitcnt vmcnt(10)
mvv 0,116,148
R 36,33792
mvv 4,120,148
buffer_load_dwordx4 v[176:179], v86, s[36:39], s49 offen offset:1024 sc1
s_waitcnt vmcnt(10)
mvv 8,116,152
mvv 12,120,152
s_waitcnt vmcnt(9)
mvv 0,116,156
R 40,34816
mvv 4,120,156
s_waitcnt vmcnt(8)
mvv 8,116,160
buffer_load_dwordx4 v[180:183], v87, s[36:39], s49 offen sc1
mvv 12,120,160
s_waitcnt lgkmcnt(1)
v_mfma_f32_16x16x32_bf16 v[16:19], a[0:3], v[32:35], v[100:103]
R 44,35840
v_cvt_pk_bf16_f32 v64, v0, v1
v_mfma_f32_16x16x32_bf16 v[20:23], a[4:7], v[32:35], v[104:107]
v_cvt_pk_bf16_f32 v65, v2, v3
v_cvt_pk_bf16_f32 v66, v4, v5
v_mfma_f32_16x16x32_bf16 v[24:27], a[8:11], v[32:35], v[108:111]
buffer_load_dwordx4 v[184:187], v87, s[36:39], s49 offen offset:1024 sc1
v_cvt_pk_bf16_f32 v67, v6, v7
v_mfma_f32_16x16x32_bf16 v[28:31], a[12:15], v[32:35], v[112:115]
v_cvt_pk_bf16_f32 v68, v8, v9
v_cvt_pk_bf16_f32 v69, v10, v11
mav 16,16,36
R 48,36864
v_cvt_pk_bf16_f32 v70, v12, v13
mav 20,20,36
v_cvt_pk_bf16_f32 v71, v14, v15
v_mul_f32 v75, v0, v0
mav 24,24,36
buffer_store_dwordx4 v[64:67], v91, s[36:39], s49 offen sc1
v_add_f32 v74, v1, v0
mav 28,28,36
v_fmac_f32 v75, v1, v1
s_waitcnt lgkmcnt(1)
mav 16,32,40
R 52,37888
v_add_f32 v74, v2, v74
mav 20,36,40
v_fmac_f32 v75, v2, v2
v_add_f32 v74, v3, v74
mav 24,40,40
buffer_store_dwordx4 v[68:71], v91, s[36:39], s49 offen offset:1024 sc1
v_fmac_f32 v75, v3, v3
mav 28,44,40
v_add_f32 v74, v4, v74
v_fmac_f32 v75, v4, v4
mav 16,48,44
R 56,38912
v_add_f32 v74, v5, v74
mav 20,52,44
v_fmac_f32 v75, v5, v5
v_add_f32 v74, v6, v74
mav 24,56,44
buffer_load_dwordx4 v[188:191], v88, s[36:39], s49 offen sc1
v_fmac_f32 v75, v6, v6
mav 28,60,44
v_add_f32 v74, v7, v74
s_waitcnt lgkmcnt(1)
mav 16,64,48
R 60,39936
v_fmac_f32 v75, v7, v7
mav 20,68,48
v_add_f32 v74, v8, v74
v_fmac_f32 v75, v8, v8
mav 24,72,48
buffer_load_dwordx4 v[192:195], v88, s[36:39], s49 offen offset:1024 sc1
v_add_f32 v74, v9, v74
mav 28,76,48
v_fmac_f32 v75, v9, v9
v_add_f32 v74, v10, v74
mav 16,80,52
R 32,40960
v_fmac_f32 v75, v10, v10
mav 20,84,52
v_add_f32 v74, v11, v74
v_fmac_f32 v75, v11, v11
mav 24,88,52
buffer_load_dwordx4 v[196:199], v89, s[36:39], s49 offen sc1
v_add_f32 v74, v12, v74
mav 28,92,52
s_add_u32 m0, s50, 32768
s_waitcnt lgkmcnt(1)
mav 16,96,56
R 36,41984
v_fmac_f32 v75, v12, v12
mav 20,100,56
v_add_f32 v74, v13, v74
v_fmac_f32 v75, v13, v13
mav 24,104,56
buffer_load_dwordx4 v[200:203], v89, s[36:39], s49 offen offset:1024 sc1
v_add_f32 v74, v14, v74
mav 28,108,56
v_fmac_f32 v75, v14, v14
v_add_f32 v74, v15, v74
mav 16,112,60
R 40,43008
v_fmac_f32 v75, v15, v15
mav 20,116,60
mav 24,120,60
D 83,0
v_permlane32_swap_b32 v74, v75
mav 28,124,60
v_add_f32 v74, v74, v75
s_waitcnt lgkmcnt(1)
mav 16,128,32
R 44,44032
mav 20,132,32
mav 24,136,32
D 83,1024
mav 28,140,32
mav 16,144,36
R 48,45056
mav 20,148,36
mav 24,152,36
D 83,2048
mav 28,156,36
s_waitcnt lgkmcnt(1)
mav 16,160,40
R 52,46080
mav 20,164,40
mav 24,168,40
D 83,3072
mav 28,172,40
mav 16,176,44
R 56,47104
mav 20,180,44
mav 24,184,44
buffer_load_dwordx4 v[124:127], v90, s[36:39], s49 offen sc1
mav 28,188,44
s_waitcnt lgkmcnt(1)
mav 16,192,48
R 60,48128
mav 20,196,48
mav 24,200,48
buffer_load_dwordx4 v[128:131], v90, s[36:39], s49 offen offset:1024 sc1
mav 28,204,48
mav 16,208,52
mav 20,212,52
mav 24,216,52
v_add_u32 v72, 1, v72
mav 28,220,52
s_waitcnt lgkmcnt(0)
mav 16,224,56
mav 20,228,56
mav 24,232,56
mav 28,236,56
mav 16,240,60
s_waitcnt vmcnt(10)
buffer_store_dwordx2 v[72:73], v81, s[36:39], s52 offen sc1
mav 20,244,60
mav 24,248,60
ds_write_b32 v97, v74 offset:0
mav 28,252,60
mvv 16,116,164
mvv 20,120,164
mvv 24,116,168
s_xor_b32 s50, s50, 0x10000
mvv 28,120,168
mvv 16,116,172
mvv 20,120,172
v_xor_b32 v80, 0x10000, v80
mvv 24,116,176
s_add_u32 s49, s49, 346112
mvv 28,120,176
s_waitcnt lgkmcnt(0)
s_barrier
mvv 16,116,180
R 32,0
mvv 20,120,180
buffer_load_dwordx4 v[132:135], v86, s[36:39], s49 offen sc1
mvv 24,116,184
mvv 28,120,184
s_waitcnt vmcnt(11)
mvv 16,116,188
R 36,1024
mvv 20,120,188
buffer_load_dwordx4 v[136:139], v86, s[36:39], s49 offen offset:1024 sc1
s_waitcnt vmcnt(11)
mvv 24,116,192
mvv 28,120,192
s_waitcnt vmcnt(10)
mvv 16,116,196
R 40,2048
mvv 20,120,196
s_waitcnt vmcnt(9)
mvv 24,116,200
buffer_load_dwordx4 v[140:143], v87, s[36:39], s49 offen sc1
mvv 28,120,200
s_mov_b32 s60, -15
.p2align 6
LR2_1:
s_waitcnt lgkmcnt(1)
v_mfma_f32_16x16x32_bf16 v[0:3], a[0:3], v[32:35], v[100:103]
R 44,3072
v_cvt_pk_bf16_f32 v64, v16, v17
v_mfma_f32_16x16x32_bf16 v[4:7], a[4:7], v[32:35], v[104:107]
v_cvt_pk_bf16_f32 v65, v18, v19
v_cvt_pk_bf16_f32 v66, v20, v21
v_mfma_f32_16x16x32_bf16 v[8:11], a[8:11], v[32:35], v[108:111]
buffer_load_dwordx4 v[144:147], v87, s[36:39], s49 offen offset:1024 sc1
v_cvt_pk_bf16_f32 v67, v22, v23
v_mfma_f32_16x16x32_bf16 v[12:15], a[12:15], v[32:35], v[112:115]
v_cvt_pk_bf16_f32 v68, v24, v25
v_cvt_pk_bf16_f32 v69, v26, v27
mav 0,16,36
R 48,4096
v_cvt_pk_bf16_f32 v70, v28, v29
mav 4,20,36
v_cvt_pk_bf16_f32 v71, v30, v31
v_mul_f32 v75, v16, v16
mav 8,24,36
buffer_store_dwordx4 v[64:67], v91, s[36:39], s49 offen sc1
v_add_f32 v74, v17, v16
mav 12,28,36
v_fmac_f32 v75, v17, v17
s_waitcnt lgkmcnt(1)
mav 0,32,40
R 52,5120
v_add_f32 v74, v18, v74
mav 4,36,40
v_fmac_f32 v75, v18, v18
v_add_f32 v74, v19, v74
mav 8,40,40
buffer_store_dwordx4 v[68:71], v91, s[36:39], s49 offen offset:1024 sc1
v_fmac_f32 v75, v19, v19
mav 12,44,40
v_add_f32 v74, v20, v74
v_fmac_f32 v75, v20, v20
mav 0,48,44
R 56,6144
v_add_f32 v74, v21, v74
mav 4,52,44
v_fmac_f32 v75, v21, v21
v_add_f32 v74, v22, v74
mav 8,56,44
buffer_load_dwordx4 v[148:151], v88, s[36:39], s49 offen sc1
v_fmac_f32 v75, v22, v22
mav 12,60,44
v_add_f32 v74, v23, v74
s_waitcnt lgkmcnt(1)
mav 0,64,48
R 60,7168
v_fmac_f32 v75, v23, v23
mav 4,68,48
v_add_f32 v74, v24, v74
v_fmac_f32 v75, v24, v24
mav 8,72,48
buffer_load_dwordx4 v[152:155], v88, s[36:39], s49 offen offset:1024 sc1
v_add_f32 v74, v25, v74
mav 12,76,48
v_fmac_f32 v75, v25, v25
v_add_f32 v74, v26, v74
mav 0,80,52
R 32,8192
v_fmac_f32 v75, v26, v26
mav 4,84,52
v_add_f32 v74, v27, v74
v_fmac_f32 v75, v27, v27
mav 8,88,52
buffer_load_dwordx4 v[156:159], v89, s[36:39], s49 offen sc1
v_add_f32 v74, v28, v74
mav 12,92,52
s_add_u32 m0, s50, 0
s_waitcnt lgkmcnt(1)
mav 0,96,56
R 36,9216
v_fmac_f32 v75, v28, v28
mav 4,100,56
v_add_f32 v74, v29, v74
v_fmac_f32 v75, v29, v29
mav 8,104,56
buffer_load_dwordx4 v[160:163], v89, s[36:39], s49 offen offset:1024 sc1
v_add_f32 v74, v30, v74
mav 12,108,56
v_fmac_f32 v75, v30, v30
v_add_f32 v74, v31, v74
mav 0,112,60
R 40,10240
v_fmac_f32 v75, v31, v31
mav 4,116,60
mav 8,120,60
D 83,0
v_permlane32_swap_b32 v74, v75
mav 12,124,60
v_add_f32 v74, v74, v75
s_waitcnt lgkmcnt(1)
mav 0,128,32
R 44,11264
mav 4,132,32
mav 8,136,32
D 83,1024
mav 12,140,32
mav 0,144,36
R 48,12288
mav 4,148,36
mav 8,152,36
D 83,2048
mav 12,156,36
s_waitcnt lgkmcnt(1)
mav 0,160,40
R 52,13312
mav 4,164,40
mav 8,168,40
D 83,3072
mav 12,172,40
mav 0,176,44
R 56,14336
mav 4,180,44
mav 8,184,44
buffer_load_dwordx4 v[164:167], v90, s[36:39], s49 offen sc1
mav 12,188,44
s_waitcnt lgkmcnt(1)
mav 0,192,48
R 60,15360
mav 4,196,48
mav 8,200,48
buffer_load_dwordx4 v[168:171], v90, s[36:39], s49 offen offset:1024 sc1
mav 12,204,48
mav 0,208,52
mav 4,212,52
mav 8,216,52
v_add_u32 v72, 1, v72
mav 12,220,52
s_waitcnt lgkmcnt(0)
mav 0,224,56
mav 4,228,56
mav 8,232,56
mav 12,236,56
mav 0,240,60
s_waitcnt vmcnt(10)
buffer_store_dwordx2 v[72:73], v81, s[36:39], s52 offen sc1
mav 4,244,60
mav 8,248,60
ds_write_b32 v97, v74 offset:1024
mav 12,252,60
mvv 0,116,124
mvv 4,120,124
mvv 8,116,128
mvv 12,120,128
mvv 0,116,132
mvv 4,120,132
mvv 8,116,136
s_add_u32 s49, s49, 346112
mvv 12,120,136
s_waitcnt lgkmcnt(0)
s_barrier
mvv 0,116,140
R 32,32768
mvv 4,120,140
buffer_load_dwordx4 v[172:175], v86, s[36:39], s49 offen sc1
mvv 8,116,144
mvv 12,120,144
s_waitcnt vmcnt(11)
mvv 0,116,148
R 36,33792
mvv 4,120,148
buffer_load_dwordx4 v[176:179], v86, s[36:39], s49 offen offset:1024 sc1
s_waitcnt vmcnt(11)
mvv 8,116,152
mvv 12,120,152
s_waitcnt vmcnt(10)
mvv 0,116,156
R 40,34816
mvv 4,120,156
s_waitcnt vmcnt(9)
mvv 8,116,160
buffer_load_dwordx4 v[180:183], v87, s[36:39], s49 offen sc1
mvv 12,120,160
s_waitcnt lgkmcnt(1)
v_mfma_f32_16x16x32_bf16 v[16:19], a[0:3], v[32:35], v[100:103]
R 44,35840
v_cvt_pk_bf16_f32 v64, v0, v1
v_mfma_f32_16x16x32_bf16 v[20:23], a[4:7], v[32:35], v[104:107]
v_cvt_pk_bf16_f32 v65, v2, v3
v_cvt_pk_bf16_f32 v66, v4, v5
v_mfma_f32_16x16x32_bf16 v[24:27], a[8:11], v[32:35], v[108:111]
buffer_load_dwordx4 v[184:187], v87, s[36:39], s49 offen offset:1024 sc1
v_cvt_pk_bf16_f32 v67, v6, v7
v_mfma_f32_16x16x32_bf16 v[28:31], a[12:15], v[32:35], v[112:115]
v_cvt_pk_bf16_f32 v68, v8, v9
v_cvt_pk_bf16_f32 v69, v10, v11
mav 16,16,36
R 48,36864
v_cvt_pk_bf16_f32 v70, v12, v13
mav 20,20,36
v_cvt_pk_bf16_f32 v71, v14, v15
v_mul_f32 v75, v0, v0
mav 24,24,36
buffer_store_dwordx4 v[64:67], v91, s[36:39], s49 offen sc1
v_add_f32 v74, v1, v0
mav 28,28,36
v_fmac_f32 v75, v1, v1
s_waitcnt lgkmcnt(1)
mav 16,32,40
R 52,37888
v_add_f32 v74, v2, v74
mav 20,36,40
v_fmac_f32 v75, v2, v2
v_add_f32 v74, v3, v74
mav 24,40,40
buffer_store_dwordx4 v[68:71], v91, s[36:39], s49 offen offset:1024 sc1
v_fmac_f32 v75, v3, v3
mav 28,44,40
v_add_f32 v74, v4, v74
v_fmac_f32 v75, v4, v4
mav 16,48,44
R 56,38912
v_add_f32 v74, v5, v74
mav 20,52,44
v_fmac_f32 v75, v5, v5
v_add_f32 v74, v6, v74
mav 24,56,44
buffer_load_dwordx4 v[188:191], v88, s[36:39], s49 offen sc1
v_fmac_f32 v75, v6, v6
mav 28,60,44
v_add_f32 v74, v7, v74
s_waitcnt lgkmcnt(1)
mav 16,64,48
R 60,39936
v_fmac_f32 v75, v7, v7
mav 20,68,48
v_add_f32 v74, v8, v74
v_fmac_f32 v75, v8, v8
mav 24,72,48
buffer_load_dwordx4 v[192:195], v88, s[36:39], s49 offen offset:1024 sc1
v_add_f32 v74, v9, v74
mav 28,76,48
v_fmac_f32 v75, v9, v9
v_add_f32 v74, v10, v74
mav 16,80,52
R 32,40960
v_fmac_f32 v75, v10, v10
mav 20,84,52
v_add_f32 v74, v11, v74
v_fmac_f32 v75, v11, v11
mav 24,88,52
buffer_load_dwordx4 v[196:199], v89, s[36:39], s49 offen sc1
v_add_f32 v74, v12, v74
mav 28,92,52
s_add_u32 m0, s50, 32768
s_waitcnt lgkmcnt(1)
mav 16,96,56
R 36,41984
v_fmac_f32 v75, v12, v12
mav 20,100,56
v_add_f32 v74, v13, v74
v_fmac_f32 v75, v13, v13
mav 24,104,56
buffer_load_dwordx4 v[200:203], v89, s[36:39], s49 offen offset:1024 sc1
v_add_f32 v74, v14, v74
mav 28,108,56
v_fmac_f32 v75, v14, v14
v_add_f32 v74, v15, v74
mav 16,112,60
R 40,43008
v_fmac_f32 v75, v15, v15
mav 20,116,60
mav 24,120,60
D 83,0
v_permlane32_swap_b32 v74, v75
mav 28,124,60
v_add_f32 v74, v74, v75
s_waitcnt lgkmcnt(1)
mav 16,128,32
R 44,44032
mav 20,132,32
mav 24,136,32
D 83,1024
mav 28,140,32
mav 16,144,36
R 48,45056
mav 20,148,36
mav 24,152,36
D 83,2048
mav 28,156,36
s_waitcnt lgkmcnt(1)
mav 16,160,40
R 52,46080
mav 20,164,40
mav 24,168,40
D 83,3072
mav 28,172,40
mav 16,176,44
R 56,47104
mav 20,180,44
mav 24,184,44
buffer_load_dwordx4 v[124:127], v90, s[36:39], s49 offen sc1
mav 28,188,44
s_waitcnt lgkmcnt(1)
mav 16,192,48
R 60,48128
mav 20,196,48
mav 24,200,48
buffer_load_dwordx4 v[128:131], v90, s[36:39], s49 offen offset:1024 sc1
mav 28,204,48
mav 16,208,52
mav 20,212,52
mav 24,216,52
v_add_u32 v72, 1, v72
mav 28,220,52
s_waitcnt lgkmcnt(0)
mav 16,224,56
mav 20,228,56
mav 24,232,56
mav 28,236,56
mav 16,240,60
s_waitcnt vmcnt(10)
buffer_store_dwordx2 v[72:73], v81, s[36:39], s52 offen sc1
mav 20,244,60
mav 24,248,60
ds_write_b32 v97, v74 offset:0
mav 28,252,60
mvv 16,116,164
mvv 20,120,164
mvv 24,116,168
s_xor_b32 s50, s50, 0x10000
mvv 28,120,168
mvv 16,116,172
mvv 20,120,172
v_xor_b32 v80, 0x10000, v80
mvv 24,116,176
s_add_u32 s49, s49, 346112
mvv 28,120,176
s_waitcnt lgkmcnt(0)
s_barrier
mvv 16,116,180
R 32,0
mvv 20,120,180
buffer_load_dwordx4 v[132:135], v86, s[36:39], s49 offen sc1
mvv 24,116,184
mvv 28,120,184
s_waitcnt vmcnt(11)
mvv 16,116,188
R 36,1024
mvv 20,120,188
buffer_load_dwordx4 v[136:139], v86, s[36:39], s49 offen offset:1024 sc1
s_waitcnt vmcnt(11)
mvv 24,116,192
mvv 28,120,192
s_waitcnt vmcnt(10)
mvv 16,116,196
R 40,2048
mvv 20,120,196
s_waitcnt vmcnt(9)
mvv 24,116,200
buffer_load_dwordx4 v[140:143], v87, s[36:39], s49 offen sc1
mvv 28,120,200
s_add_u32 s60, s60, 1
s_cbranch_scc0 LR2_1
s_waitcnt lgkmcnt(1)
v_mfma_f32_16x16x32_bf16 v[0:3], a[0:3], v[32:35], v[100:103]
R 44,3072
v_cvt_pk_bf16_f32 v64, v16, v17
v_mfma_f32_16x16x32_bf16 v[4:7], a[4:7], v[32:35], v[104:107]
v_cvt_pk_bf16_f32 v65, v18, v19
v_cvt_pk_bf16_f32 v66, v20, v21
v_mfma_f32_16x16x32_bf16 v[8:11], a[8:11], v[32:35], v[108:111]
buffer_load_dwordx4 v[144:147], v87, s[36:39], s49 offen offset:1024 sc1
v_cvt_pk_bf16_f32 v67, v22, v23
v_mfma_f32_16x16x32_bf16 v[12:15], a[12:15], v[32:35], v[112:115]
v_cvt_pk_bf16_f32 v68, v24, v25
v_cvt_pk_bf16_f32 v69, v26, v27
mav 0,16,36
R 48,4096
v_cvt_pk_bf16_f32 v70, v28, v29
mav 4,20,36
v_cvt_pk_bf16_f32 v71, v30, v31
v_mul_f32 v75, v16, v16
mav 8,24,36
buffer_store_dwordx4 v[64:67], v91, s[36:39], s49 offen sc1
v_add_f32 v74, v17, v16
mav 12,28,36
v_fmac_f32 v75, v17, v17
s_waitcnt lgkmcnt(1)
mav 0,32,40
R 52,5120
v_add_f32 v74, v18, v74
mav 4,36,40
v_fmac_f32 v75, v18, v18
v_add_f32 v74, v19, v74
mav 8,40,40
buffer_store_dwordx4 v[68:71], v91, s[36:39], s49 offen offset:1024 sc1
v_fmac_f32 v75, v19, v19
mav 12,44,40
v_add_f32 v74, v20, v74
v_fmac_f32 v75, v20, v20
mav 0,48,44
R 56,6144
v_add_f32 v74, v21, v74
mav 4,52,44
v_fmac_f32 v75, v21, v21
v_add_f32 v74, v22, v74
mav 8,56,44
buffer_load_dwordx4 v[148:151], v88, s[36:39], s49 offen sc1
v_fmac_f32 v75, v22, v22
mav 12,60,44
v_add_f32 v74, v23, v74
s_waitcnt lgkmcnt(1)
mav 0,64,48
R 60,7168
v_fmac_f32 v75, v23, v23
mav 4,68,48
v_add_f32 v74, v24, v74
v_fmac_f32 v75, v24, v24
mav 8,72,48
buffer_load_dwordx4 v[152:155], v88, s[36:39], s49 offen offset:1024 sc1
v_add_f32 v74, v25, v74
mav 12,76,48
v_fmac_f32 v75, v25, v25
v_add_f32 v74, v26, v74
mav 0,80,52
R 32,8192
v_fmac_f32 v75, v26, v26
mav 4,84,52
v_add_f32 v74, v27, v74
v_fmac_f32 v75, v27, v27
mav 8,88,52
buffer_load_dwordx4 v[156:159], v89, s[36:39], s49 offen sc1
v_add_f32 v74, v28, v74
mav 12,92,52
s_add_u32 m0, s50, 0
s_waitcnt lgkmcnt(1)
mav 0,96,56
R 36,9216
v_fmac_f32 v75, v28, v28
mav 4,100,56
v_add_f32 v74, v29, v74
v_fmac_f32 v75, v29, v29
mav 8,104,56
buffer_load_dwordx4 v[160:163], v89, s[36:39], s49 offen offset:1024 sc1
v_add_f32 v74, v30, v74
mav 12,108,56
v_fmac_f32 v75, v30, v30
v_add_f32 v74, v31, v74
mav 0,112,60
R 40,10240
v_fmac_f32 v75, v31, v31
mav 4,116,60
mav 8,120,60
D 83,0
v_permlane32_swap_b32 v74, v75
mav 12,124,60
v_add_f32 v74, v74, v75
s_waitcnt lgkmcnt(1)
mav 0,128,32
R 44,11264
mav 4,132,32
mav 8,136,32
D 83,1024
mav 12,140,32
mav 0,144,36
R 48,12288
mav 4,148,36
mav 8,152,36
D 83,2048
mav 12,156,36
s_waitcnt lgkmcnt(1)
mav 0,160,40
R 52,13312
mav 4,164,40
mav 8,168,40
D 83,3072
mav 12,172,40
mav 0,176,44
R 56,14336
mav 4,180,44
mav 8,184,44
buffer_load_dwordx4 v[164:167], v90, s[36:39], s49 offen sc1
mav 12,188,44
s_waitcnt lgkmcnt(1)
mav 0,192,48
R 60,15360
mav 4,196,48
mav 8,200,48
buffer_load_dwordx4 v[168:171], v90, s[36:39], s49 offen offset:1024 sc1
mav 12,204,48
mav 0,208,52
mav 4,212,52
mav 8,216,52
v_add_u32 v72, 69, v72
mav 12,220,52
s_waitcnt lgkmcnt(0)
mav 0,224,56
mav 4,228,56
mav 8,232,56
mav 12,236,56
mav 0,240,60
s_waitcnt vmcnt(10)
buffer_store_dwordx2 v[72:73], v81, s[36:39], s52 offen sc1
mav 4,244,60
mav 8,248,60
ds_write_b32 v97, v74 offset:1024
mav 12,252,60
mvv 0,116,124
mvv 4,120,124
mvv 8,116,128
mvv 12,120,128
mvv 0,116,132
mvv 4,120,132
mvv 8,116,136
s_add_u32 s49, s49, 346112
mvv 12,120,136
s_waitcnt lgkmcnt(0)
s_barrier
mvv 0,116,140
R 32,32768
mvv 4,120,140
buffer_load_dwordx4 v[172:175], v86, s[36:39], s49 offen sc1
mvv 8,116,144
mvv 12,120,144
s_waitcnt vmcnt(11)
mvv 0,116,148
R 36,33792
mvv 4,120,148
buffer_load_dwordx4 v[176:179], v86, s[36:39], s49 offen offset:1024 sc1
s_waitcnt vmcnt(11)
mvv 8,116,152
mvv 12,120,152
s_waitcnt vmcnt(10)
mvv 0,116,156
R 40,34816
mvv 4,120,156
s_waitcnt vmcnt(9)
mvv 8,116,160
buffer_load_dwordx4 v[180:183], v87, s[36:39], s49 offen sc1
mvv 12,120,160
s_waitcnt lgkmcnt(1)
v_mfma_f32_16x16x32_bf16 v[16:19], a[0:3], v[32:35], v[100:103]
R 44,35840
v_cvt_pk_bf16_f32 v64, v0, v1
v_mfma_f32_16x16x32_bf16 v[20:23], a[4:7], v[32:35], v[104:107]
v_cvt_pk_bf16_f32 v65, v2, v3
v_cvt_pk_bf16_f32 v66, v4, v5
v_mfma_f32_16x16x32_bf16 v[24:27], a[8:11], v[32:35], v[108:111]
buffer_load_dwordx4 v[184:187], v87, s[36:39], s49 offen offset:1024 sc1
v_cvt_pk_bf16_f32 v67, v6, v7
v_mfma_f32_16x16x32_bf16 v[28:31], a[12:15], v[32:35], v[112:115]
ds_write_b128 v94, v[64:67] offset:0
mav 16,16,36
R 48,36864
v_cvt_pk_bf16_f32 v68, v8, v9
mav 20,20,36
v_cvt_pk_bf16_f32 v69, v10, v11
v_cvt_pk_bf16_f32 v70, v12, v13
mav 24,24,36
v_cvt_pk_bf16_f32 v71, v14, v15
mav 28,28,36
buffer_load_dwordx4 v[188:191], v88, s[36:39], s49 offen sc1
s_waitcnt lgkmcnt(2)
mav 16,32,40
R 52,37888
mav 20,36,40
ds_write_b128 v95, v[68:71] offset:0
mav 24,40,40
mav 28,44,40
buffer_load_dwordx4 v[192:195], v88, s[36:39], s49 offen offset:1024 sc1
mav 16,48,44
R 56,38912
mav 20,52,44
s_waitcnt lgkmcnt(1)
ds_read_b128 v[64:67], v96 offset:0
mav 24,56,44
ds_read_b128 v[68:71], v96 offset:1024
mav 28,60,44
mav 16,64,48
R 60,39936
buffer_load_dwordx4 v[196:199], v89, s[36:39], s49 offen sc1
mav 20,68,48
mav 24,72,48
mav 28,76,48
mav 16,80,52
R 32,40960
buffer_load_dwordx4 v[200:203], v89, s[36:39], s49 offen offset:1024 sc1
mav 20,84,52
mav 24,88,52
mav 28,92,52
s_add_u32 m0, s50, 32768
s_waitcnt lgkmcnt(1)
mav 16,96,56
R 36,41984
buffer_store_dwordx4 v[64:67], v92, s[44:47], s54 offen
mav 20,100,56
mav 24,104,56
mav 28,108,56
mav 16,112,60
R 40,43008
buffer_store_dwordx4 v[68:71], v93, s[44:47], s54 offen
mav 20,116,60
mav 24,120,60
mav 28,124,60
s_waitcnt lgkmcnt(1)
mav 16,128,32
R 44,44032
D 83,0
mav 20,132,32
mav 24,136,32
mav 28,140,32
mav 16,144,36
R 48,45056
D 83,1024
mav 20,148,36
mav 24,152,36
mav 28,156,36
s_waitcnt lgkmcnt(1)
mav 16,160,40
R 52,46080
D 83,2048
mav 20,164,40
s_sub_u32 s54, s54, 0x8000
mav 24,168,40
mav 28,172,40
mav 16,176,44
R 56,47104
D 83,3072
mav 20,180,44
mav 24,184,44
mav 28,188,44
s_waitcnt lgkmcnt(1)
mav 16,192,48
R 60,48128
buffer_load_dwordx4 v[124:127], v90, s[36:39], s49 offen sc1
mav 20,196,48
mav 24,200,48
mav 28,204,48
mav 16,208,52
buffer_load_dwordx4 v[128:131], v90, s[36:39], s49 offen offset:1024 sc1
mav 20,212,52
mav 24,216,52
mav 28,220,52
s_waitcnt lgkmcnt(0)
mav 16,224,56
mav 20,228,56
mav 24,232,56
mav 28,236,56
mav 16,240,60
mav 20,244,60
mav 24,248,60
mav 28,252,60
s_waitcnt vmcnt(18)
mvv 16,116,164
mvv 20,120,164
s_waitcnt vmcnt(17)
mvv 24,116,168
s_xor_b32 s50, s50, 0x10000
mvv 28,120,168
s_waitcnt vmcnt(15)
mvv 16,116,172
mvv 20,120,172
v_xor_b32 v80, 0x10000, v80
s_waitcnt vmcnt(14)
mvv 24,116,176
s_add_u32 s49, s49, 346112
mvv 28,120,176
s_barrier
s_waitcnt vmcnt(13)
mvv 16,116,180
R 32,0
mvv 20,120,180
buffer_load_dwordx4 v[132:135], v86, s[36:39], s49 offen sc1
s_waitcnt vmcnt(13)
mvv 24,116,184
mvv 28,120,184
s_waitcnt vmcnt(12)
mvv 16,116,188
R 36,1024
mvv 20,120,188
buffer_load_dwordx4 v[136:139], v86, s[36:39], s49 offen offset:1024 sc1
s_waitcnt vmcnt(12)
mvv 24,116,192
mvv 28,120,192
s_waitcnt vmcnt(11)
mvv 16,116,196
R 40,2048
mvv 20,120,196
s_waitcnt vmcnt(10)
mvv 24,116,200
buffer_load_dwordx4 v[140:143], v87, s[36:39], s49 offen sc1
mvv 28,120,200
s_mov_b32 s60, -13
.p2align 6
LR2_2:
s_waitcnt lgkmcnt(1)
v_mfma_f32_16x16x32_bf16 v[0:3], a[0:3], v[32:35], v[100:103]
R 44,3072
v_cvt_pk_bf16_f32 v64, v16, v17
v_mfma_f32_16x16x32_bf16 v[4:7], a[4:7], v[32:35], v[104:107]
v_cvt_pk_bf16_f32 v65, v18, v19
v_cvt_pk_bf16_f32 v66, v20, v21
v_mfma_f32_16x16x32_bf16 v[8:11], a[8:11], v[32:35], v[108:111]
buffer_load_dwordx4 v[144:147], v87, s[36:39], s49 offen offset:1024 sc1
v_cvt_pk_bf16_f32 v67, v22, v23
v_mfma_f32_16x16x32_bf16 v[12:15], a[12:15], v[32:35], v[112:115]
ds_write_b128 v94, v[64:67] offset:0
mav 0,16,36
R 48,4096
v_cvt_pk_bf16_f32 v68, v24, v25
mav 4,20,36
v_cvt_pk_bf16_f32 v69, v26, v27
v_cvt_pk_bf16_f32 v70, v28, v29
mav 8,24,36
v_cvt_pk_bf16_f32 v71, v30, v31
mav 12,28,36
buffer_load_dwordx4 v[148:151], v88, s[36:39], s49 offen sc1
s_waitcnt lgkmcnt(2)
mav 0,32,40
R 52,5120
mav 4,36,40
ds_write_b128 v95, v[68:71] offset:0
mav 8,40,40
mav 12,44,40
buffer_load_dwordx4 v[152:155], v88, s[36:39], s49 offen offset:1024 sc1
mav 0,48,44
R 56,6144
mav 4,52,44
s_waitcnt lgkmcnt(1)
ds_read_b128 v[64:67], v96 offset:0
mav 8,56,44
ds_read_b128 v[68:71], v96 offset:1024
mav 12,60,44
mav 0,64,48
R 60,7168
buffer_load_dwordx4 v[156:159], v89, s[36:39], s49 offen sc1
mav 4,68,48
mav 8,72,48
mav 12,76,48
mav 0,80,52
R 32,8192
buffer_load_dwordx4 v[160:163], v89, s[36:39], s49 offen offset:1024 sc1
mav 4,84,52
mav 8,88,52
mav 12,92,52
s_add_u32 m0, s50, 0
s_waitcnt lgkmcnt(1)
mav 0,96,56
R 36,9216
buffer_store_dwordx4 v[64:67], v92, s[44:47], s54 offen
mav 4,100,56
mav 8,104,56
mav 12,108,56
mav 0,112,60
R 40,10240
buffer_store_dwordx4 v[68:71], v93, s[44:47], s54 offen
mav 4,116,60
mav 8,120,60
mav 12,124,60
s_waitcnt lgkmcnt(1)
mav 0,128,32
R 44,11264
D 83,0
mav 4,132,32
mav 8,136,32
mav 12,140,32
mav 0,144,36
R 48,12288
D 83,1024
mav 4,148,36
mav 8,152,36
mav 12,156,36
s_waitcnt lgkmcnt(1)
mav 0,160,40
R 52,13312
D 83,2048
mav 4,164,40
s_sub_u32 s54, s54, 0x8000
mav 8,168,40
mav 12,172,40
mav 0,176,44
R 56,14336
D 83,3072
mav 4,180,44
mav 8,184,44
mav 12,188,44
s_waitcnt lgkmcnt(1)
mav 0,192,48
R 60,15360
buffer_load_dwordx4 v[164:167], v90, s[36:39], s49 offen sc1
mav 4,196,48
mav 8,200,48
mav 12,204,48
mav 0,208,52
buffer_load_dwordx4 v[168:171], v90, s[36:39], s49 offen offset:1024 sc1
mav 4,212,52
mav 8,216,52
mav 12,220,52
s_waitcnt lgkmcnt(0)
mav 0,224,56
mav 4,228,56
mav 8,232,56
mav 12,236,56
mav 0,240,60
mav 4,244,60
mav 8,248,60
mav 12,252,60
s_waitcnt vmcnt(17)
mvv 0,116,124
mvv 4,120,124
s_waitcnt vmcnt(16)
mvv 8,116,128
mvv 12,120,128
s_waitcnt vmcnt(15)
mvv 0,116,132
mvv 4,120,132
s_waitcnt vmcnt(14)
mvv 8,116,136
s_add_u32 s49, s49, 346112
mvv 12,120,136
s_barrier
s_waitcnt vmcnt(13)
mvv 0,116,140
R 32,32768
mvv 4,120,140
buffer_load_dwordx4 v[172:175], v86, s[36:39], s49 offen sc1
s_waitcnt vmcnt(13)
mvv 8,116,144
mvv 12,120,144
s_waitcnt vmcnt(12)
mvv 0,116,148
R 36,33792
mvv 4,120,148
buffer_load_dwordx4 v[176:179], v86, s[36:39], s49 offen offset:1024 sc1
s_waitcnt vmcnt(12)
mvv 8,116,152
mvv 12,120,152
s_waitcnt vmcnt(11)
mvv 0,116,156
R 40,34816
mvv 4,120,156
s_waitcnt vmcnt(10)
mvv 8,116,160
buffer_load_dwordx4 v[180:183], v87, s[36:39], s49 offen sc1
mvv 12,120,160
s_waitcnt lgkmcnt(1)
v_mfma_f32_16x16x32_bf16 v[16:19], a[0:3], v[32:35], v[100:103]
R 44,35840
v_cvt_pk_bf16_f32 v64, v0, v1
v_mfma_f32_16x16x32_bf16 v[20:23], a[4:7], v[32:35], v[104:107]
v_cvt_pk_bf16_f32 v65, v2, v3
v_cvt_pk_bf16_f32 v66, v4, v5
v_mfma_f32_16x16x32_bf16 v[24:27], a[8:11], v[32:35], v[108:111]
buffer_load_dwordx4 v[184:187], v87, s[36:39], s49 offen offset:1024 sc1
v_cvt_pk_bf16_f32 v67, v6, v7
v_mfma_f32_16x16x32_bf16 v[28:31], a[12:15], v[32:35], v[112:115]
ds_write_b128 v94, v[64:67] offset:0
mav 16,16,36
R 48,36864
v_cvt_pk_bf16_f32 v68, v8, v9
mav 20,20,36
v_cvt_pk_bf16_f32 v69, v10, v11
v_cvt_pk_bf16_f32 v70, v12, v13
mav 24,24,36
v_cvt_pk_bf16_f32 v71, v14, v15
mav 28,28,36
buffer_load_dwordx4 v[188:191], v88, s[36:39], s49 offen sc1
s_waitcnt lgkmcnt(2)
mav 16,32,40
R 52,37888
mav 20,36,40
ds_write_b128 v95, v[68:71] offset:0
mav 24,40,40
mav 28,44,40
buffer_load_dwordx4 v[192:195], v88, s[36:39], s49 offen offset:1024 sc1
mav 16,48,44
R 56,38912
mav 20,52,44
s_waitcnt lgkmcnt(1)
ds_read_b128 v[64:67], v96 offset:0
mav 24,56,44
ds_read_b128 v[68:71], v96 offset:1024
mav 28,60,44
mav 16,64,48
R 60,39936
buffer_load_dwordx4 v[196:199], v89, s[36:39], s49 offen sc1
mav 20,68,48
mav 24,72,48
mav 28,76,48
mav 16,80,52
R 32,40960
buffer_load_dwordx4 v[200:203], v89, s[36:39], s49 offen offset:1024 sc1
mav 20,84,52
mav 24,88,52
mav 28,92,52
s_add_u32 m0, s50, 32768
s_waitcnt lgkmcnt(1)
mav 16,96,56
R 36,41984
buffer_store_dwordx4 v[64:67], v92, s[44:47], s54 offen
mav 20,100,56
mav 24,104,56
mav 28,108,56
mav 16,112,60
R 40,43008
buffer_store_dwordx4 v[68:71], v93, s[44:47], s54 offen
mav 20,116,60
mav 24,120,60
mav 28,124,60
s_waitcnt lgkmcnt(1)
mav 16,128,32
R 44,44032
D 83,0
mav 20,132,32
mav 24,136,32
mav 28,140,32
mav 16,144,36
R 48,45056
D 83,1024
mav 20,148,36
mav 24,152,36
mav 28,156,36
s_waitcnt lgkmcnt(1)
mav 16,160,40
R 52,46080
D 83,2048
mav 20,164,40
s_sub_u32 s54, s54, 0x8000
mav 24,168,40
mav 28,172,40
mav 16,176,44
R 56,47104
D 83,3072
mav 20,180,44
mav 24,184,44
mav 28,188,44
s_waitcnt lgkmcnt(1)
mav 16,192,48
R 60,48128
buffer_load_dwordx4 v[124:127], v90, s[36:39], s49 offen sc1
mav 20,196,48
mav 24,200,48
mav 28,204,48
mav 16,208,52
buffer_load_dwordx4 v[128:131], v90, s[36:39], s49 offen offset:1024 sc1
mav 20,212,52
mav 24,216,52
mav 28,220,52
s_waitcnt lgkmcnt(0)
mav 16,224,56
mav 20,228,56
mav 24,232,56
mav 28,236,56
mav 16,240,60
mav 20,244,60
mav 24,248,60
mav 28,252,60
s_waitcnt vmcnt(17)
mvv 16,116,164
mvv 20,120,164
s_waitcnt vmcnt(16)
mvv 24,116,168
s_xor_b32 s50, s50, 0x10000
mvv 28,120,168
s_waitcnt vmcnt(15)
mvv 16,116,172
mvv 20,120,172
v_xor_b32 v80, 0x10000, v80
s_waitcnt vmcnt(14)
mvv 24,116,176
s_add_u32 s49, s49, 346112
mvv 28,120,176
s_barrier
s_waitcnt vmcnt(13)
mvv 16,116,180
R 32,0
mvv 20,120,180
buffer_load_dwordx4 v[132:135], v86, s[36:39], s49 offen sc1
s_waitcnt vmcnt(13)
mvv 24,116,184
mvv 28,120,184
s_waitcnt vmcnt(12)
mvv 16,116,188
R 36,1024
mvv 20,120,188
buffer_load_dwordx4 v[136:139], v86, s[36:39], s49 offen offset:1024 sc1
s_waitcnt vmcnt(12)
mvv 24,116,192
mvv 28,120,192
s_waitcnt vmcnt(11)
mvv 16,116,196
R 40,2048
mvv 20,120,196
s_waitcnt vmcnt(10)
mvv 24,116,200
buffer_load_dwordx4 v[140:143], v87, s[36:39], s49 offen sc1
mvv 28,120,200
s_add_u32 s60, s60, 1
s_cbranch_scc0 LR2_2
s_waitcnt lgkmcnt(1)
v_mfma_f32_16x16x32_bf16 v[0:3], a[0:3], v[32:35], v[100:103]
R 44,3072
v_cvt_pk_bf16_f32 v64, v16, v17
v_mfma_f32_16x16x32_bf16 v[4:7], a[4:7], v[32:35], v[104:107]
v_cvt_pk_bf16_f32 v65, v18, v19
v_cvt_pk_bf16_f32 v66, v20, v21
v_mfma_f32_16x16x32_bf16 v[8:11], a[8:11], v[32:35], v[108:111]
buffer_load_dwordx4 v[144:147], v87, s[36:39], s49 offen offset:1024 sc1
v_cvt_pk_bf16_f32 v67, v22, v23
v_mfma_f32_16x16x32_bf16 v[12:15], a[12:15], v[32:35], v[112:115]
ds_write_b128 v94, v[64:67] offset:0
mav 0,16,36
R 48,4096
v_cvt_pk_bf16_f32 v68, v24, v25
mav 4,20,36
v_cvt_pk_bf16_f32 v69, v26, v27
v_cvt_pk_bf16_f32 v70, v28, v29
mav 8,24,36
v_cvt_pk_bf16_f32 v71, v30, v31
mav 12,28,36
buffer_load_dwordx4 v[148:151], v88, s[36:39], s49 offen sc1
s_waitcnt lgkmcnt(2)
mav 0,32,40
R 52,5120
mav 4,36,40
ds_write_b128 v95, v[68:71] offset:0
mav 8,40,40
mav 12,44,40
buffer_load_dwordx4 v[152:155], v88, s[36:39], s49 offen offset:1024 sc1
mav 0,48,44
R 56,6144
mav 4,52,44
s_waitcnt lgkmcnt(1)
ds_read_b128 v[64:67], v96 offset:0
mav 8,56,44
ds_read_b128 v[68:71], v96 offset:1024
mav 12,60,44
mav 0,64,48
R 60,7168
buffer_load_dwordx4 v[156:159], v89, s[36:39], s49 offen sc1
mav 4,68,48
mav 8,72,48
mav 12,76,48
mav 0,80,52
R 32,8192
buffer_load_dwordx4 v[160:163], v89, s[36:39], s49 offen offset:1024 sc1
mav 4,84,52
mav 8,88,52
mav 12,92,52
s_add_u32 m0, s50, 0
s_waitcnt lgkmcnt(1)
mav 0,96,56
R 36,9216
buffer_store_dwordx4 v[64:67], v92, s[44:47], s54 offen
mav 4,100,56
mav 8,104,56
mav 12,108,56
mav 0,112,60
R 40,10240
buffer_store_dwordx4 v[68:71], v93, s[44:47], s54 offen
mav 4,116,60
mav 8,120,60
mav 12,124,60
s_waitcnt lgkmcnt(1)
mav 0,128,32
R 44,11264
D 83,0
mav 4,132,32
mav 8,136,32
mav 12,140,32
mav 0,144,36
R 48,12288
D 83,1024
mav 4,148,36
mav 8,152,36
mav 12,156,36
s_waitcnt lgkmcnt(1)
mav 0,160,40
R 52,13312
D 83,2048
mav 4,164,40
s_sub_u32 s54, s54, 0x8000
mav 8,168,40
mav 12,172,40
mav 0,176,44
R 56,14336
D 83,3072
mav 4,180,44
mav 8,184,44
mav 12,188,44
s_waitcnt lgkmcnt(1)
mav 0,192,48
R 60,15360
buffer_load_dwordx4 v[164:167], v90, s[36:39], s49 offen sc1
mav 4,196,48
mav 8,200,48
mav 12,204,48
mav 0,208,52
buffer_load_dwordx4 v[168:171], v90, s[36:39], s49 offen offset:1024 sc1
mav 4,212,52
mav 8,216,52
mav 12,220,52
s_waitcnt lgkmcnt(0)
mav 0,224,56
mav 4,228,56
mav 8,232,56
mav 12,236,56
mav 0,240,60
mav 4,244,60
mav 8,248,60
mav 12,252,60
s_waitcnt vmcnt(17)
mvv 0,116,124
mvv 4,120,124
s_waitcnt vmcnt(16)
mvv 8,116,128
mvv 12,120,128
s_waitcnt vmcnt(15)
)"
R"(
mvv 0,116,132
mvv 4,120,132
s_waitcnt vmcnt(14)
mvv 8,116,136
s_add_u32 s49, s49, 346112
mvv 12,120,136
s_barrier
s_waitcnt vmcnt(13)
mvv 0,116,140
R 32,32768
mvv 4,120,140
buffer_load_dwordx4 v[172:175], v86, s[36:39], s49 offen sc1
s_waitcnt vmcnt(13)
mvv 8,116,144
mvv 12,120,144
s_waitcnt vmcnt(12)
mvv 0,116,148
R 36,33792
mvv 4,120,148
buffer_load_dwordx4 v[176:179], v86, s[36:39], s49 offen offset:1024 sc1
s_waitcnt vmcnt(12)
mvv 8,116,152
mvv 12,120,152
s_waitcnt vmcnt(11)
mvv 0,116,156
R 40,34816
mvv 4,120,156
s_waitcnt vmcnt(10)
mvv 8,116,160
buffer_load_dwordx4 v[180:183], v87, s[36:39], s49 offen sc1
mvv 12,120,160
s_waitcnt lgkmcnt(1)
v_mfma_f32_16x16x32_bf16 v[16:19], a[0:3], v[32:35], v[100:103]
R 44,35840
v_cvt_pk_bf16_f32 v64, v0, v1
v_mfma_f32_16x16x32_bf16 v[20:23], a[4:7], v[32:35], v[104:107]
v_cvt_pk_bf16_f32 v65, v2, v3
v_cvt_pk_bf16_f32 v66, v4, v5
v_mfma_f32_16x16x32_bf16 v[24:27], a[8:11], v[32:35], v[108:111]
buffer_load_dwordx4 v[184:187], v87, s[36:39], s49 offen offset:1024 sc1
v_cvt_pk_bf16_f32 v67, v6, v7
v_mfma_f32_16x16x32_bf16 v[28:31], a[12:15], v[32:35], v[112:115]
ds_write_b128 v94, v[64:67] offset:0
mav 16,16,36
R 48,36864
v_cvt_pk_bf16_f32 v68, v8, v9
mav 20,20,36
v_cvt_pk_bf16_f32 v69, v10, v11
v_cvt_pk_bf16_f32 v70, v12, v13
mav 24,24,36
v_cvt_pk_bf16_f32 v71, v14, v15
mav 28,28,36
buffer_load_dwordx4 v[188:191], v88, s[36:39], s49 offen sc1
s_waitcnt lgkmcnt(2)
mav 16,32,40
R 52,37888
mav 20,36,40
ds_write_b128 v95, v[68:71] offset:0
mav 24,40,40
mav 28,44,40
buffer_load_dwordx4 v[192:195], v88, s[36:39], s49 offen offset:1024 sc1
mav 16,48,44
R 56,38912
mav 20,52,44
s_waitcnt lgkmcnt(1)
ds_read_b128 v[64:67], v96 offset:0
mav 24,56,44
ds_read_b128 v[68:71], v96 offset:1024
mav 28,60,44
mav 16,64,48
R 60,39936
buffer_load_dwordx4 v[196:199], v89, s[36:39], s49 offen sc1
mav 20,68,48
mav 24,72,48
mav 28,76,48
mav 16,80,52
R 32,40960
buffer_load_dwordx4 v[200:203], v89, s[36:39], s49 offen offset:1024 sc1
mav 20,84,52
mav 24,88,52
mav 28,92,52
s_add_u32 m0, s50, 32768
s_waitcnt lgkmcnt(1)
mav 16,96,56
R 36,41984
buffer_store_dwordx4 v[64:67], v92, s[44:47], s54 offen
mav 20,100,56
mav 24,104,56
mav 28,108,56
mav 16,112,60
R 40,43008
buffer_store_dwordx4 v[68:71], v93, s[44:47], s54 offen
mav 20,116,60
mav 24,120,60
mav 28,124,60
s_waitcnt lgkmcnt(1)
mav 16,128,32
R 44,44032
D 83,0
mav 20,132,32
mav 24,136,32
mav 28,140,32
mav 16,144,36
R 48,45056
D 83,1024
mav 20,148,36
mav 24,152,36
mav 28,156,36
s_waitcnt lgkmcnt(1)
mav 16,160,40
R 52,46080
D 83,2048
mav 20,164,40
s_sub_u32 s54, s54, 0x8000
mav 24,168,40
mav 28,172,40
mav 16,176,44
R 56,47104
D 83,3072
mav 20,180,44
mav 24,184,44
mav 28,188,44
s_waitcnt lgkmcnt(1)
mav 16,192,48
R 60,48128
buffer_load_dwordx4 v[124:127], v90, s[36:39], s49 offen sc1
mav 20,196,48
mav 24,200,48
mav 28,204,48
mav 16,208,52
buffer_load_dwordx4 v[128:131], v90, s[36:39], s49 offen offset:1024 sc1
mav 20,212,52
mav 24,216,52
mav 28,220,52
s_waitcnt lgkmcnt(0)
mav 16,224,56
mav 20,228,56
mav 24,232,56
mav 28,236,56
mav 16,240,60
mav 20,244,60
mav 24,248,60
mav 28,252,60
s_waitcnt vmcnt(17)
mvv 16,116,164
mvv 20,120,164
s_waitcnt vmcnt(16)
mvv 24,116,168
mvv 28,120,168
s_waitcnt vmcnt(15)
mvv 16,116,172
mvv 20,120,172
v_xor_b32 v80, 0x10000, v80
s_waitcnt vmcnt(14)
mvv 24,116,176
s_add_u32 s49, s49, 346112
mvv 28,120,176
s_barrier
s_waitcnt vmcnt(13)
mvv 16,116,180
R 32,0
mvv 20,120,180
buffer_load_dwordx4 v[132:135], v86, s[36:39], s49 offen sc1
s_waitcnt vmcnt(13)
mvv 24,116,184
mvv 28,120,184
s_waitcnt vmcnt(12)
mvv 16,116,188
R 36,1024
mvv 20,120,188
buffer_load_dwordx4 v[136:139], v86, s[36:39], s49 offen offset:1024 sc1
s_waitcnt vmcnt(12)
mvv 24,116,192
mvv 28,120,192
s_waitcnt vmcnt(11)
mvv 16,116,196
R 40,2048
mvv 20,120,196
s_waitcnt vmcnt(10)
mvv 24,116,200
buffer_load_dwordx4 v[140:143], v87, s[36:39], s49 offen sc1
mvv 28,120,200
s_waitcnt lgkmcnt(1)
v_mfma_f32_16x16x32_bf16 v[0:3], a[0:3], v[32:35], v[100:103]
R 44,3072
v_cvt_pk_bf16_f32 v64, v16, v17
v_mfma_f32_16x16x32_bf16 v[4:7], a[4:7], v[32:35], v[104:107]
v_cvt_pk_bf16_f32 v65, v18, v19
v_cvt_pk_bf16_f32 v66, v20, v21
v_mfma_f32_16x16x32_bf16 v[8:11], a[8:11], v[32:35], v[108:111]
buffer_load_dwordx4 v[144:147], v87, s[36:39], s49 offen offset:1024 sc1
v_cvt_pk_bf16_f32 v67, v22, v23
v_mfma_f32_16x16x32_bf16 v[12:15], a[12:15], v[32:35], v[112:115]
ds_write_b128 v94, v[64:67] offset:0
mav 0,16,36
R 48,4096
v_cvt_pk_bf16_f32 v68, v24, v25
mav 4,20,36
v_cvt_pk_bf16_f32 v69, v26, v27
v_cvt_pk_bf16_f32 v70, v28, v29
mav 8,24,36
v_cvt_pk_bf16_f32 v71, v30, v31
mav 12,28,36
buffer_load_dwordx4 v[148:151], v88, s[36:39], s49 offen sc1
s_waitcnt lgkmcnt(2)
mav 0,32,40
R 52,5120
mav 4,36,40
ds_write_b128 v95, v[68:71] offset:0
mav 8,40,40
mav 12,44,40
buffer_load_dwordx4 v[152:155], v88, s[36:39], s49 offen offset:1024 sc1
mav 0,48,44
R 56,6144
mav 4,52,44
s_waitcnt lgkmcnt(1)
ds_read_b128 v[64:67], v96 offset:0
mav 8,56,44
ds_read_b128 v[68:71], v96 offset:1024
mav 12,60,44
mav 0,64,48
R 60,7168
buffer_load_dwordx4 v[156:159], v89, s[36:39], s49 offen sc1
mav 4,68,48
mav 8,72,48
mav 12,76,48
mav 0,80,52
R 32,8192
buffer_load_dwordx4 v[160:163], v89, s[36:39], s49 offen offset:1024 sc1
mav 4,84,52
mav 8,88,52
mav 12,92,52
s_waitcnt lgkmcnt(1)
mav 0,96,56
R 36,9216
buffer_store_dwordx4 v[64:67], v92, s[44:47], s54 offen
mav 4,100,56
mav 8,104,56
mav 12,108,56
mav 0,112,60
R 40,10240
buffer_store_dwordx4 v[68:71], v93, s[44:47], s54 offen
mav 4,116,60
mav 8,120,60
mav 12,124,60
s_waitcnt lgkmcnt(1)
mav 0,128,32
R 44,11264
mav 4,132,32
mav 8,136,32
mav 12,140,32
mav 0,144,36
R 48,12288
mav 4,148,36
mav 8,152,36
mav 12,156,36
s_waitcnt lgkmcnt(1)
mav 0,160,40
R 52,13312
s_sub_u32 s54, s54, 0x8000
mav 4,164,40
mav 8,168,40
mav 12,172,40
buffer_load_dwordx4 v[164:167], v90, s[36:39], s49 offen sc1
mav 0,176,44
R 56,14336
mav 4,180,44
mav 8,184,44
mav 12,188,44
buffer_load_dwordx4 v[168:171], v90, s[36:39], s49 offen offset:1024 sc1
s_waitcnt lgkmcnt(1)
mav 0,192,48
R 60,15360
mav 4,196,48
mav 8,200,48
mav 12,204,48
mav 0,208,52
mav 4,212,52
mav 8,216,52
mav 12,220,52
s_waitcnt lgkmcnt(0)
mav 0,224,56
mav 4,228,56
mav 8,232,56
mav 12,236,56
mav 0,240,60
mav 4,244,60
mav 8,248,60
mav 12,252,60
s_waitcnt vmcnt(13)
mvv 0,116,124
mvv 4,120,124
s_waitcnt vmcnt(12)
mvv 8,116,128
mvv 12,120,128
s_waitcnt vmcnt(11)
mvv 0,116,132
mvv 4,120,132
s_waitcnt vmcnt(10)
mvv 8,116,136
s_add_u32 s49, s49, 346112
mvv 12,120,136
s_barrier
s_waitcnt vmcnt(9)
mvv 0,116,140
R 32,32768
mvv 4,120,140
buffer_load_dwordx4 v[172:175], v86, s[36:39], s49 offen sc1
s_waitcnt vmcnt(9)
mvv 8,116,144
mvv 12,120,144
s_waitcnt vmcnt(8)
mvv 0,116,148
R 36,33792
mvv 4,120,148
buffer_load_dwordx4 v[176:179], v86, s[36:39], s49 offen offset:1024 sc1
s_waitcnt vmcnt(8)
mvv 8,116,152
mvv 12,120,152
s_waitcnt vmcnt(7)
mvv 0,116,156
R 40,34816
mvv 4,120,156
s_waitcnt vmcnt(6)
mvv 8,116,160
buffer_load_dwordx4 v[180:183], v87, s[36:39], s49 offen sc1
mvv 12,120,160
s_waitcnt lgkmcnt(1)
v_mfma_f32_16x16x32_bf16 v[16:19], a[0:3], v[32:35], v[100:103]
R 44,35840
v_cvt_pk_bf16_f32 v64, v0, v1
v_mfma_f32_16x16x32_bf16 v[20:23], a[4:7], v[32:35], v[104:107]
v_cvt_pk_bf16_f32 v65, v2, v3
v_cvt_pk_bf16_f32 v66, v4, v5
v_mfma_f32_16x16x32_bf16 v[24:27], a[8:11], v[32:35], v[108:111]
buffer_load_dwordx4 v[184:187], v87, s[36:39], s49 offen offset:1024 sc1
v_cvt_pk_bf16_f32 v67, v6, v7
v_mfma_f32_16x16x32_bf16 v[28:31], a[12:15], v[32:35], v[112:115]
ds_write_b128 v94, v[64:67] offset:0
mav 16,16,36
R 48,36864
v_cvt_pk_bf16_f32 v68, v8, v9
mav 20,20,36
v_cvt_pk_bf16_f32 v69, v10, v11
v_cvt_pk_bf16_f32 v70, v12, v13
mav 24,24,36
v_cvt_pk_bf16_f32 v71, v14, v15
mav 28,28,36
buffer_load_dwordx4 v[188:191], v88, s[36:39], s49 offen sc1
s_waitcnt lgkmcnt(2)
mav 16,32,40
R 52,37888
mav 20,36,40
ds_write_b128 v95, v[68:71] offset:0
mav 24,40,40
mav 28,44,40
buffer_load_dwordx4 v[192:195], v88, s[36:39], s49 offen offset:1024 sc1
mav 16,48,44
R 56,38912
mav 20,52,44
s_waitcnt lgkmcnt(1)
ds_read_b128 v[64:67], v96 offset:0
mav 24,56,44
ds_read_b128 v[68:71], v96 offset:1024
mav 28,60,44
mav 16,64,48
R 60,39936
buffer_load_dwordx4 v[196:199], v89, s[36:39], s49 offen sc1
mav 20,68,48
mav 24,72,48
mav 28,76,48
mav 16,80,52
R 32,40960
buffer_load_dwordx4 v[200:203], v89, s[36:39], s49 offen offset:1024 sc1
mav 20,84,52
mav 24,88,52
mav 28,92,52
s_waitcnt lgkmcnt(1)
mav 16,96,56
R 36,41984
buffer_store_dwordx4 v[64:67], v92, s[44:47], s54 offen
mav 20,100,56
mav 24,104,56
mav 28,108,56
mav 16,112,60
R 40,43008
buffer_store_dwordx4 v[68:71], v93, s[44:47], s54 offen
mav 20,116,60
mav 24,120,60
mav 28,124,60
s_waitcnt lgkmcnt(1)
mav 16,128,32
R 44,44032
mav 20,132,32
mav 24,136,32
mav 28,140,32
mav 16,144,36
R 48,45056
mav 20,148,36
mav 24,152,36
mav 28,156,36
s_waitcnt lgkmcnt(1)
mav 16,160,40
R 52,46080
s_sub_u32 s54, s54, 0x8000
mav 20,164,40
mav 24,168,40
mav 28,172,40
mav 16,176,44
R 56,47104
mav 20,180,44
mav 24,184,44
mav 28,188,44
s_waitcnt lgkmcnt(1)
mav 16,192,48
R 60,48128
mav 20,196,48
mav 24,200,48
mav 28,204,48
mav 16,208,52
mav 20,212,52
mav 24,216,52
mav 28,220,52
s_waitcnt lgkmcnt(0)
mav 16,224,56
mav 20,228,56
mav 24,232,56
mav 28,236,56
mav 16,240,60
mav 20,244,60
mav 24,248,60
mav 28,252,60
s_waitcnt vmcnt(11)
mvv 16,116,164
mvv 20,120,164
s_waitcnt vmcnt(10)
mvv 24,116,168
mvv 28,120,168
s_waitcnt vmcnt(9)
mvv 16,116,172
mvv 20,120,172
s_waitcnt vmcnt(8)
mvv 24,116,176
mvv 28,120,176
s_waitcnt vmcnt(7)
mvv 16,116,180
mvv 20,120,180
s_waitcnt vmcnt(6)
mvv 24,116,184
mvv 28,120,184
s_waitcnt vmcnt(5)
mvv 16,116,188
mvv 20,120,188
s_waitcnt vmcnt(4)
mvv 24,116,192
mvv 28,120,192
s_waitcnt vmcnt(3)
mvv 16,116,196
mvv 20,120,196
s_waitcnt vmcnt(2)
mvv 24,116,200
mvv 28,120,200
s_nop 3
v_cvt_pk_bf16_f32 v64, v16, v17
v_cvt_pk_bf16_f32 v65, v18, v19
v_cvt_pk_bf16_f32 v66, v20, v21
v_cvt_pk_bf16_f32 v67, v22, v23
ds_write_b128 v94, v[64:67] offset:0
v_cvt_pk_bf16_f32 v68, v24, v25
v_cvt_pk_bf16_f32 v69, v26, v27
v_cvt_pk_bf16_f32 v70, v28, v29
v_cvt_pk_bf16_f32 v71, v30, v31
ds_write_b128 v95, v[68:71] offset:0
s_waitcnt lgkmcnt(0)
ds_read_b128 v[64:67], v96 offset:0
ds_read_b128 v[68:71], v96 offset:1024
s_waitcnt lgkmcnt(0)
buffer_store_dwordx4 v[64:67], v92, s[44:47], s54 offen
buffer_store_dwordx4 v[68:71], v93, s[44:47], s54 offen
s_endpgm
.purgem mav
.purgem Ta
.purgem mva
.purgem mvv
.purgem Tv
.purgem R
.purgem D
.purgem E
)"
    : "+{v[0:7]}"(v0), "+{v[80:95]}"(c0), "+{v[96:111]}"(c1), "+{v[112:127]}"(c2), "+{s[36:51]}"(s0), "+{s[52:59]}"(s1)
    :
    : "v8", "v9", "v10", "v11", "v12", "v13", "v14", "v15", "v16", "v17", "v18", "v19", "v20", "v21", "v22", "v23", "v24", "v25", "v26", "v27", "v28", "v29", "v30", "v31", "v32", "v33", "v34", "v35", "v36", "v37", "v38", "v39", "v40", "v41", "v42", "v43", "v44", "v45", "v46", "v47", "v48", "v49", "v50", "v51", "v52", "v53", "v54", "v55", "v56", "v57", "v58", "v59", "v60", "v61", "v62", "v63", "v64", "v65", "v66", "v67", "v68", "v69", "v70", "v71", "v72", "v73", "v74", "v75", "v76", "v77", "v78", "v79", "v128", "v129", "v130", "v131", "v132", "v133", "v134", "v135", "v136", "v137", "v138", "v139", "v140", "v141", "v142", "v143", "v144", "v145", "v146", "v147", "v148", "v149", "v150", "v151", "v152", "v153", "v154", "v155", "v156", "v157", "v158", "v159", "v160", "v161", "v162", "v163", "v164", "v165", "v166", "v167", "v168", "v169", "v170", "v171", "v172", "v173", "v174", "v175", "v176", "v177", "v178", "v179", "v180", "v181", "v182", "v183", "v184", "v185", "v186", "v187", "v188", "v189", "v190", "v191", "v192", "v193", "v194", "v195", "v196", "v197", "v198", "v199", "v200", "v201", "v202", "v203", "v204", "v205", "v206", "v207", "v208", "v209", "v210", "v211", "v212", "v213", "v214", "v215", "v216", "v217", "v218", "v219", "v220", "v221", "v222", "v223", "v224", "v225", "v226", "v227", "v228", "v229", "v230", "v231", "v232", "v233", "v234", "v235", "v236", "v237", "v238", "v239", "v240", "v241", "v242", "v243", "v244", "v245", "v246", "v247", "v248", "v249", "v250", "v251", "v252", "v253", "v254", "v255", "a0", "a1", "a2", "a3", "a4", "a5", "a6", "a7", "a8", "a9", "a10", "a11", "a12", "a13", "a14", "a15", "a16", "a17", "a18", "a19", "a20", "a21", "a22", "a23", "a24", "a25", "a26", "a27", "a28", "a29", "a30", "a31", "a32", "a33", "a34", "a35", "a36", "a37", "a38", "a39", "a40", "a41", "a42", "a43", "a44", "a45", "a46", "a47", "a48", "a49", "a50", "a51", "a52", "a53", "a54", "a55", "a56", "a57", "a58", "a59", "a60", "a61", "a62", "a63", "a64", "a65", "a66", "a67", "a68", "a69", "a70", "a71", "a72", "a73", "a74", "a75", "a76", "a77", "a78", "a79", "a80", "a81", "a82", "a83", "a84", "a85", "a86", "a87", "a88", "a89", "a90", "a91", "a92", "a93", "a94", "a95", "a96", "a97", "a98", "a99", "a100", "a101", "a102", "a103", "a104", "a105", "a106", "a107", "a108", "a109", "a110", "a111", "a112", "a113", "a114", "a115", "a116", "a117", "a118", "a119", "a120", "a121", "a122", "a123", "a124", "a125", "a126", "a127", "a128", "a129", "a130", "a131", "a132", "a133", "a134", "a135", "a136", "a137", "a138", "a139", "a140", "a141", "a142", "a143", "a144", "a145", "a146", "a147", "a148", "a149", "a150", "a151", "a152", "a153", "a154", "a155", "a156", "a157", "a158", "a159", "a160", "a161", "a162", "a163", "a164", "a165", "a166", "a167", "a168", "a169", "a170", "a171", "a172", "a173", "a174", "a175", "a176", "a177", "a178", "a179", "a180", "a181", "a182", "a183", "a184", "a185", "a186", "a187", "a188", "a189", "a190", "a191", "a192", "a193", "a194", "a195", "a196", "a197", "a198", "a199", "a200", "a201", "a202", "a203", "a204", "a205", "a206", "a207", "a208", "a209", "a210", "a211", "a212", "a213", "a214", "a215", "a216", "a217", "a218", "a219", "a220", "a221", "a222", "a223", "a224", "a225", "a226", "a227", "a228", "a229", "a230", "a231", "a232", "a233", "a234", "a235", "a236", "a237", "a238", "a239", "a240", "a241", "a242", "a243", "a244", "a245", "a246", "a247", "a248", "a249", "a250", "a251", "a252", "a253", "a254", "a255", "s60", "s61", "s62", "s63", "s64", "s65", "s66", "s67", "s68", "s69", "s70", "s71", "s72", "s73", "s74", "s75", "s76", "s77", "s78", "s79", "s80", "s81", "s82", "s83", "s84", "s85", "s86", "s87", "s88", "s89", "s90", "s91", "s92", "s93", "s94", "s95", "s96", "s97", "s98", "s99", "vcc", "scc", "memory");
}

extern "C" void kernel_launch(void* const* d_in, const int* in_sizes, int n_in, void* d_out, int out_size,
                              void* d_ws, size_t ws_size, hipStream_t stream) {
  Args m{};
  m.x = (const u16*)d_in[0]; m.wqkv = (const u16*)d_in[1]; m.wo = (const u16*)d_in[2]; m.w1 = (const u16*)d_in[4];
  m.b1 = (const u16*)d_in[5]; m.w2 = (const u16*)d_in[6]; m.b2 = (const u16*)d_in[7];
  m.out = (u16*)d_out; m.ws = (char*)d_ws;
  (void)hipFuncSetAttribute((const void*)df_kernel, hipFuncAttributeMaxDynamicSharedMemorySize, LDS_BYTES);
  hipLaunchKernelGGL(df_kernel, dim3(256), dim3(256), LDS_BYTES, stream, m);
}
```

```cpp
#include <hip/hip_runtime.h>
#include <stdint.h>

typedef __attribute__((ext_vector_type(16))) unsigned u32x16;
typedef __attribute__((ext_vector_type(8))) unsigned u32x8;
typedef __attribute__((ext_vector_type(4))) unsigned u32x4;
typedef __attribute__((ext_vector_type(2))) unsigned u32x2;
typedef __attribute__((ext_vector_type(2))) __bf16 bf2_t;
typedef unsigned short u16;
typedef __amdgpu_buffer_rsrc_t rsrc_t;
typedef __attribute__((address_space(3))) void lds_t;

constexpr unsigned FLAGS = 4096, RB = 4194304, REC = 346112, SX = 344064, WS_END = 92798976, DBG = 1048576;
constexpr int LDS_BYTES = 163840;
#define NEGB 0xff61b1e6u
#define FW(p, s, w) (FLAGS + (unsigned)(((p) * 64 + (s)) * 128 + (w) * 8))
#define blk0(s) (RB + (unsigned)(p * 64 + (s) - 2) * REC)
#define TA(seg, hf, sub) (unsigned)((seg) * 4096 + (8 * lg + (li >> 2)) * 128 + ((((hf) * 4 + (li & 3)) ^ ((li >> 1) & 4)) << 4) + (sub))
#define TO(seg, hf, half) (unsigned)((seg) * 4096 + (8 * lg + (li >> 2)) * 128 + ((((hf) * 4 + (half) * 2 + ((li & 3) >> 1)) ^ ((li >> 1) & 4)) << 4) + (li & 1) * 8)
#define BIAS(ptr, n, i) ((unsigned)__builtin_amdgcn_raw_buffer_load_b16(__builtin_amdgcn_make_buffer_rsrc((void*)(ptr), 0, (n) * 2, 0x00020000), (i) * 2, 0, 0) << 16)
#define SPIN_CAP 200000

struct Args {
  const u16 *x, *wqkv, *wo, *w1, *w2;
  char* ws;
  u16* out;
  const u16 *b1, *b2;
};

__device__ __forceinline__ float rows_sum(float v) {
  const auto a = __builtin_amdgcn_permlane16_swap(__float_as_uint(v), __float_as_uint(v), false, false);
  const float m = __uint_as_float(a[0]) + __uint_as_float(a[1]);
  const auto b = __builtin_amdgcn_permlane32_swap(__float_as_uint(m), __float_as_uint(m), false, false);
  return __uint_as_float(b[0]) + __uint_as_float(b[1]);
}

__device__ __forceinline__ const u16* arg_b2() {
  auto ka = (const __attribute__((address_space(4))) Args*)__builtin_amdgcn_kernarg_segment_ptr();
  asm volatile("" : "+s"(ka));
  return ka->b2;
}

__global__ __launch_bounds__(256, 1) void df_kernel(Args m) {
  const int w = __builtin_amdgcn_readfirstlane(threadIdx.x >> 6);
  const int bid = blockIdx.x, xc = bid & 7, j_ = bid >> 3;
  {
    const unsigned tid = threadIdx.x;
    lds_t* const sink = (lds_t*)(size_t)(65536 + w * 1024);
    if (w & 1) {
      unsigned long long pc; unsigned o = w == 1 ? (tid & 63) * 128 : (tid & 63) * 4096 + j_ * 128;
      asm volatile("s_mov_b32 m0, %2\n\ts_getpc_b64 %0\nLbeg:\n\tv_min_u32 %1, Lfin - Lbeg + 60, %1\n\tglobal_load_lds_dword %1, %0" : "=&s"(pc), "+v"(o) : "s"(65536 + w * 1024) : "memory");
    }
#define QLINE(l) (((l) >> 2) * 2048 + ((l) & 3) * 128 + (xc & 3) * 512)
    if (!(j_ & 2)) {
      const rsrc_t rT = __builtin_amdgcn_make_buffer_rsrc((void*)(w == 3 ? m.x : m.wqkv), 0, w == 3 ? 2048 * 1024 * 2 : 1024 * 3072 * 2, 0x00020000);
      const unsigned i16 = (j_ >> 2) * 2 + (j_ & 1), lx = i16 * 64 + (tid & 63), lh = (j_ >> 2) * 192 + tid;
      __builtin_amdgcn_raw_ptr_buffer_load_lds(rT, sink, 4, w == 3 ? ((lx >> 9) + ((xc >> 1) & 2)) * 1048576 + (lx & 511) * 128 + 30 * 32768 : QLINE(i16 * 192 + tid), 0, 0, 0);
      if ((j_ & 1) && w < 3) {
#pragma unroll
        for (int i = 2; i < 8; ++i) __builtin_amdgcn_raw_ptr_buffer_load_lds(rT, sink, 4, QLINE(lh), i * 1536 * 512, 0, 0);
      }
      if (1 && (j_ & 1) && !(xc & 3)) {
        const rsrc_t rU = __builtin_amdgcn_make_buffer_rsrc((void*)m.wo, 0, 1024 * 1024 * 2, 0x00020000);
#pragma unroll
        for (int i = 0; i < 8; ++i) __builtin_amdgcn_raw_ptr_buffer_load_lds(rU, sink, 4, ((j_ >> 2) * 256 + tid) * 128, i * 262144, 0, 0);
      }
    }
  }
  const int ps = (j_ & 1) * 4 + (j_ >> 3), p = (((xc >> 1) ^ j_) & 2) + ((j_ >> 2) & 1);
  const unsigned long long tb = ((j_ & 2) ? ((xc & 2) ? ((xc & 1) ? 0x2c2a292827262524ull : 0x172e232221201f1eull) : ((xc & 1) ? 0x162d1d1c1b1a1918ull : 0x2f2b151413121110ull)) : ((xc & 2) ? ((xc & 1) ? 0x3f3e3d3c0f0e0d0cull : 0x3b3a39380b0a0908ull) : ((xc & 1) ? 0x3736353407060504ull : 0x3332313003020100ull)));
  const int slot = (int)(tb >> (ps * 8)) & 63;
  if (slot >= 16 && slot < 48) {
    int lane;
    asm volatile("v_mbcnt_lo_u32_b32 %0, -1, 0\n\tv_mbcnt_hi_u32_b32 %0, -1, %0" : "=v"(lane));
    const int lg = lane >> 4, li = lane & 15;
    const rsrc_t rS = __builtin_amdgcn_make_buffer_rsrc((void*)m.ws, 0, WS_END, 0x00020000);
    const rsrc_t rX = __builtin_amdgcn_make_buffer_rsrc((void*)m.x, 0, 2048 * 1024 * 2, 0x00020000);
    const bf2_t one2 = {(__bf16)1.0f, (__bf16)1.0f};
    const unsigned go = lg * 2048 + w * 512 + ((li ^ (lg * 4)) << 4), lb = 73728 + w * 10240, lo = lb + (li >> 2) * 2064 + (li & 3) * 256 + lg * 16;
#pragma unroll
    for (int i = 0; i < 8; ++i)
      __builtin_amdgcn_raw_ptr_buffer_load_lds(rX, (lds_t*)(size_t)(lb + (i >> 1) * 2064 + (i & 1) * 1024), 16, go + ((i >> 1) * 8192 + (i & 1) * 256), (p * 32 + 47 - slot) * 32768, 0, 0);
    asm volatile("s_waitcnt vmcnt(0)" ::: "memory");
    u32x4 v[8];
#pragma unroll
    for (int k = 0; k < 8; ++k) v[k] = *(const __attribute__((address_space(3))) u32x4*)(size_t)(lo + (k >> 2) * 1024 + (((k ^ li) & 3) << 6));
    const unsigned rb = RB + (unsigned)(p * 64 + slot - 16) * REC;
#pragma unroll
    for (int k = 0; k < 8; ++k) __builtin_amdgcn_raw_buffer_store_b128(v[k], rS, lane * 16 + w * 8192 + k * 1024, rb, 16);
    float s = 0.f, q = 0.f;
#pragma unroll
    for (int k = 0; k < 8; ++k) {
#pragma unroll
      for (int e = 0; e < 4; ++e) {
        const unsigned xe = v[k][e];
        const bf2_t x = __builtin_bit_cast(bf2_t, xe);
        s = __builtin_amdgcn_fdot2_f32_bf16(x, one2, s, false);
        q = __builtin_amdgcn_fdot2_f32_bf16(x, x, q, false);
      }
    }
    s = rows_sum(s); q = rows_sum(q);
    __builtin_amdgcn_raw_buffer_store_b64((u32x2){__float_as_uint(s), __float_as_uint(q)}, rS, lg ? 0x80000000u : w * 128 + li * 8 + SX, rb, 16);
    if (slot == 22 || slot == 23 || slot == 47) {
      asm volatile("v_mbcnt_lo_u32_b32 %0, -1, 0\n\tv_mbcnt_hi_u32_b32 %0, -1, %0" : "=v"(lane));
      const u32x2 tag = {0x100u, __builtin_amdgcn_raw_buffer_load_b32(rS, 0, 0, 16) + 0x9e3779b9u};
      asm volatile("s_waitcnt vmcnt(0)\n\ts_barrier\nLfin:" ::: "memory");
      __builtin_amdgcn_raw_buffer_store_b64(tag, rS, lane ? 0x80000000u : FW(p, slot, w), 0, 16);
      if (slot == 47 && p == 3 && w == 3) {
        for (int n = 0; n < SPIN_CAP; ++n) {
          __builtin_amdgcn_s_sleep(127);
          bool bad = false;
#pragma unroll
          for (int i = 0; i < 8; ++i) {
            const u32x4 f = __builtin_amdgcn_raw_buffer_load_b128(rS, FW(i >> 1, (i & 1) * 32 + (lane >> 1), (lane & 1) * 2), 0, 16);
            bad = bad || f[1] != tag[1] || f[3] != tag[1] || f[0] - tag[0] > 255u || f[2] - tag[0] > 255u;
          }
          if (__builtin_amdgcn_ballot_w64(bad) == 0ull) break;
        }
        __builtin_amdgcn_raw_buffer_store_b32(tag[1], rS, lane ? 0x80000000u : 0u, 0, 16);
      }
      return;
    }
  }
  const int lane = threadIdx.x & 63, lg = lane >> 4, li = lane & 15;
  const unsigned l16 = lane * 16;
  u32x16 c0 = 0, c1 = 0, c2 = 0, s0 = 0;
  u32x8 v0 = 0, s1 = 0;
  const u16 *wp, *op = m.out; unsigned wn;
  const u16 *rQp = m.wqkv, *rOp = m.wo, *r1p = m.w1, *r2p = m.w2;
  constexpr unsigned rQn = 1024 * 3072 * 2, rOn = 1024 * 1024 * 2, r1n = 1024 * 3584 * 2, r2n = r1n;
  const int rot = p * 1, wr = (w + rot) & 3;
  if (slot < 16 && !(w & 1)) {
    s0[12] = 0 + (0); wp = rQp; wn = rQn;
    v0[3] = (p * 32 + 30) * 32768 + (w * 4 + lg) * 2048 + ((li ^ (lg * 4)) << 4); v0[7] = 2048 + (li >> 2) * 8208 + (li & 3) * 256 + lg * 16; op = m.x;
    s0[13] = blk0(0) + 32768u + (slot * 2 + (w >> 1)) * 1024 - (131072u + (w >> 1) * 2048);
    s0[15] = w * 8 * 6144 + slot * 128;
    s1[2] = 262144u + (p * 32 + slot * 2 + (w >> 1)) * 4096 - (131072u + (w >> 1) * 2048);
    s1[3] = p ? 262144u + ((p - 1) * 32 + slot * 2 + (w >> 1)) * 4096 - (131072u + (w >> 1) * 2048) : 0x80000000u;
    s1[4] = p ? 0u : NEGB;
    v0[0] = (lane >> 3) * 6144 + (((lane & 7) ^ ((lane >> 2) & 4)) << 4) + 0u;
    v0[1] = (lane >> 3) * 6144 + (((lane & 7) ^ ((lane >> 2) & 4)) << 4) + 2048u;
    v0[2] = (lane >> 3) * 6144 + (((lane & 7) ^ ((lane >> 2) & 4)) << 4) + 4096u;
    v0[4] = TA(0, w >> 1, 0);
    v0[5] = TA(0, w >> 1, 8);
    v0[6] = TA(2, w >> 1, 0);
    c0[0] = l16;
    c0[1] = 131072u + (w >> 1) * 2048 + l16;
    c0[2] = 139264u + li * 8;
    c0[3] = 139264u + lg * 32;
    c0[4] = (((4 * lg + 0) ^ li) & 1) == 0 && (4 * lg + 0 <= li) ? 0u : NEGB;
    c0[5] = (((4 * lg + 1) ^ li) & 1) == 0 && (4 * lg + 1 <= li) ? 0u : NEGB;
    c0[6] = (((4 * lg + 2) ^ li) & 1) == 0 && (4 * lg + 2 <= li) ? 0u : NEGB;
    c0[7] = (((4 * lg + 3) ^ li) & 1) == 0 && (4 * lg + 3 <= li) ? 0u : NEGB;
    c0[8] = (((4 * lg + 0) ^ li) & 1) == 0 && (4 * lg + 0 > li) ? 0u : NEGB;
    c0[9] = (((4 * lg + 1) ^ li) & 1) == 0 && (4 * lg + 1 > li) ? 0u : NEGB;
    c0[10] = (((4 * lg + 2) ^ li) & 1) == 0 && (4 * lg + 2 > li) ? 0u : NEGB;
    c0[11] = (((4 * lg + 3) ^ li) & 1) == 0 && (4 * lg + 3 > li) ? 0u : NEGB;
    c0[12] = (4 * lg + 0 + (li & 1) <= li) ? ((li & 1) ? 0xffff0000u : 0xffffu) : 0u;
    c0[13] = (4 * lg + 2 + (li & 1) <= li) ? ((li & 1) ? 0xffff0000u : 0xffffu) : 0u;
    c0[14] = (4 * lg + 0 + (li & 1) > li) ? ((li & 1) ? 0xffff0000u : 0xffffu) : 0u;
    c0[15] = (4 * lg + 2 + (li & 1) > li) ? ((li & 1) ? 0xffff0000u : 0xffffu) : 0u;
  } else if (slot < 16) {
    s0[12] = 4 + (w >> 1); wp = rQp; wn = rQn;
    v0[3] = (p * 32 + 30) * 32768 + (w * 4 + lg) * 2048 + ((li ^ (lg * 4)) << 4); v0[7] = 2048 + (li >> 2) * 8208 + (li & 3) * 256 + lg * 16; op = m.x;
    s0[13] = blk0(0);
    s0[14] = 65536 + (w >> 1) * 4096;
    s0[15] = w * 8 * 6144 + slot * 128;
    s1[2] = blk0(0) + (w >> 1) * 346112u;
    s1[3] = (w >> 1) * 128;
    v0[0] = (lane >> 3) * 6144 + (((lane & 7) ^ ((lane >> 2) & 4)) << 4) + 0u;
    v0[1] = (lane >> 3) * 6144 + (((lane & 7) ^ ((lane >> 2) & 4)) << 4) + 2048u;
    v0[2] = (lane >> 3) * 6144 + (((lane & 7) ^ ((lane >> 2) & 4)) << 4) + 4096u;
    v0[4] = TA(1, w >> 1, 0);
    v0[5] = TA(1, w >> 1, 8);
    v0[6] = TA(2, w >> 1, 8);
    c0[0] = l16;
    c0[1] = lane >= 1 ? 0x80000000u : lane * 33024u;
    c0[2] = 142336u + lane * 4;
    c0[3] = 131072u + (w >> 1) * 2048 + l16;
    c0[4] = 139264u + li * 8;
    c0[5] = 139264u + lg * 32;
    c0[6] = 1384448u + 0 + (w >> 1) * 4096 + l16;
    c0[7] = 1384448u + 8192 + (w >> 1) * 4096 + l16;
    c0[8] = 1384448u + 16384 + (w >> 1) * 4096 + l16;
    c0[9] = 1384448u + 24576 + (w >> 1) * 4096 + l16;
    c0[10] = (lane >> 1) == 24 && p ? FW(p - 1, slot, (lane & 1) * 2) : FW(p, 43 + ((lane & 15) >> 2), lane & 3) + (slot >> 2) % 1 * 33024u;
    c0[11] = lane >= 16 && lane < 48 ? FW(p, lane, 0) : (lane >> 1) == 24 && p ? FW(p - 1, slot, (lane & 1) * 2) : FW(p, 43 + ((lane & 15) >> 2), lane & 3) + (slot >> 2) % 1 * 33024u;
    c0[12] = (unsigned)-26;
    c0[13] = 1036288u + lane * 8;
  } else if (slot < 22) {
    s0[12] = 8 + (w < 2 ? w : 2); wp = rOp; wn = rOn;
    s0[13] = blk0(-2);
    s0[14] = w * 8192;
    s0[15] = w * 8 * 2048 + (slot - 16) * 384 + rot * 524288;
    s1[5] = w * 8 * 2048 + (slot - 16) * 384 + 2097152u;
    v0[0] = (lane >> 3) * 2048 + (((lane & 7) ^ ((lane >> 2) & 4)) << 4) + ((slot - 16) * 6 + 0 < 32 ? 0u : 0x80000000u);
    v0[1] = (lane >> 3) * 2048 + (((lane & 7) ^ ((lane >> 2) & 4)) << 4) + ((slot - 16) * 6 + 2 < 32 ? 128u : 0x80000000u);
    v0[2] = (lane >> 3) * 2048 + (((lane & 7) ^ ((lane >> 2) & 4)) << 4) + ((slot - 16) * 6 + 4 < 32 ? 256u : 0x80000000u);
    v0[4] = TA(((w >> 1) * 3 + (w & 1)) >> 1, ((w >> 1) * 3 + (w & 1)) & 1, 0);
    v0[5] = TA(((w >> 1) * 3 + (w & 1)) >> 1, ((w >> 1) * 3 + (w & 1)) & 1, 8);
    v0[6] = TO(((w >> 1) * 3 + 2) >> 1, ((w >> 1) * 3 + 2) & 1, w & 1);
    c0[0] = l16;
    c0[1] = lane >= 1 ? 0x80000000u : lane * 33024u;
    c0[2] = 142336u + lane * 4;
    c0[3] = ((slot - 16) * 6 + (w >> 1) * 3 + (w & 1)) < 32 ? 411648u + ((slot - 16) * 6 + (w >> 1) * 3 + (w & 1)) * 1024 + l16 : 0x80000000u;
    c0[4] = ((slot - 16) * 6 + (w >> 1) * 3 + 2) < 32 && !(lg & 1) ? 411648u + ((slot - 16) * 6 + (w >> 1) * 3 + 2) * 1024 + ((w & 1) * 2 + (lg >> 1)) * 256 + li * 16 : 0x80000000u;
    c0[5] = 1071104u + wr * 8192 + l16;
    c0[6] = 1075200u + wr * 8192 + l16;
    c0[7] = FW(p, (lane & 31) >> 1, (lane & 1) * 2) + (slot) % 1 * 33024u;
    c0[8] = 1u;
    c0[9] = ((slot - 16) * 6 + (w >> 1) * 3 + (w & 1)) < 32 ? 1384448u + ((slot - 16) * 6 + (w >> 1) * 3 + (w & 1)) * 1024 + l16 : 0x80000000u;
    c0[10] = ((slot - 16) * 6 + (w >> 1) * 3 + 2) < 32 ? 1384448u + ((slot - 16) * 6 + (w >> 1) * 3 + 2) * 1024 + ((w & 1) * 2 + (lg >> 1)) * 256 + li * 16 + (lg & 1) * 8 : 0x80000000u;
    c0[11] = 140288u + ((li * 2 + (lg >> 1)) * 8 + w * 2 + (lg & 1)) * 4;
    c0[12] = 140288u + l16;
    c0[13] = (lane & 1) ? 0x80000000u : 345088u + (slot - 16) * 128 + (lane >> 1) * 4;
  } else if (slot < 43) {
    s0[12] = 12 + (w < 2 ? w : 2); wp = r1p; wn = r1n;
    s0[13] = blk0(-2);
    s0[14] = w * 8192;
    s0[15] = w * 8 * 7168 + (slot - 24) * 384 + rot * 1835008;
    s1[5] = w * 8 * 7168 + (slot - 24) * 384 + 7340032u;
    v0[0] = (lane >> 3) * 7168 + (((lane & 7) ^ ((lane >> 2) & 4)) << 4) + ((slot - 24) * 6 + 0 < 112 ? 0u : 0x80000000u);
    v0[1] = (lane >> 3) * 7168 + (((lane & 7) ^ ((lane >> 2) & 4)) << 4) + ((slot - 24) * 6 + 2 < 112 ? 128u : 0x80000000u);
    v0[2] = (lane >> 3) * 7168 + (((lane & 7) ^ ((lane >> 2) & 4)) << 4) + ((slot - 24) * 6 + 4 < 112 ? 256u : 0x80000000u);
    v0[4] = TA(((w >> 1) * 3 + (w & 1)) >> 1, ((w >> 1) * 3 + (w & 1)) & 1, 0);
    v0[5] = TA(((w >> 1) * 3 + (w & 1)) >> 1, ((w >> 1) * 3 + (w & 1)) & 1, 8);
    v0[6] = TO(((w >> 1) * 3 + 2) >> 1, ((w >> 1) * 3 + 2) & 1, w & 1);
    c0[0] = l16;
    c0[1] = lane >= 1 ? 0x80000000u : lane * 33024u;
    c0[2] = 142336u + lane * 4;
    c0[3] = ((slot - 24) * 6 + (w >> 1) * 3 + (w & 1)) < 112 ? 444416u + ((slot - 24) * 6 + (w >> 1) * 3 + (w & 1)) * 1024 + l16 : 0x80000000u;
    c0[4] = ((slot - 24) * 6 + (w >> 1) * 3 + 2) < 112 && !(lg & 1) ? 444416u + ((slot - 24) * 6 + (w >> 1) * 3 + 2) * 1024 + ((w & 1) * 2 + (lg >> 1)) * 256 + li * 16 : 0x80000000u;
    c0[5] = 1103872u + wr * 8192 + l16;
    c0[6] = 1107968u + wr * 8192 + l16;
    c0[7] = (lane < 24 ? FW(p, 16 + (lane >> 2), lane & 3) : FW(p, 16, 0)) + (slot) % 1 * 33024u;
    c0[8] = 1u;
    c0[9] = 139264u + li * 8;
    c0[10] = 1037312u + lane * 8;
    c0[11] = lane < 32 ? 1037824u + lane * 8 : 0x80000000u;
    c0[12] = BIAS(m.b1, 3584, ((slot - 24) * 6 + (w >> 1) * 3 + (w & 1)) * 32 + lg * 8 + 0);
    c0[13] = BIAS(m.b1, 3584, ((slot - 24) * 6 + (w >> 1) * 3 + (w & 1)) * 32 + lg * 8 + 1);
    c0[14] = BIAS(m.b1, 3584, ((slot - 24) * 6 + (w >> 1) * 3 + (w & 1)) * 32 + lg * 8 + 2);
    c0[15] = BIAS(m.b1, 3584, ((slot - 24) * 6 + (w >> 1) * 3 + (w & 1)) * 32 + lg * 8 + 3);
    c1[0] = BIAS(m.b1, 3584, ((slot - 24) * 6 + (w >> 1) * 3 + (w & 1)) * 32 + lg * 8 + 4);
    c1[1] = BIAS(m.b1, 3584, ((slot - 24) * 6 + (w >> 1) * 3 + (w & 1)) * 32 + lg * 8 + 5);
    c1[2] = BIAS(m.b1, 3584, ((slot - 24) * 6 + (w >> 1) * 3 + (w & 1)) * 32 + lg * 8 + 6);
    c1[3] = BIAS(m.b1, 3584, ((slot - 24) * 6 + (w >> 1) * 3 + (w & 1)) * 32 + lg * 8 + 7);
    c1[4] = BIAS(m.b1, 3584, ((slot - 24) * 6 + (w >> 1) * 3 + 2) * 32 + (w & 1) * 16 + lg * 4 + 0);
    c1[5] = BIAS(m.b1, 3584, ((slot - 24) * 6 + (w >> 1) * 3 + 2) * 32 + (w & 1) * 16 + lg * 4 + 1);
    c1[6] = BIAS(m.b1, 3584, ((slot - 24) * 6 + (w >> 1) * 3 + 2) * 32 + (w & 1) * 16 + lg * 4 + 2);
    c1[7] = BIAS(m.b1, 3584, ((slot - 24) * 6 + (w >> 1) * 3 + 2) * 32 + (w & 1) * 16 + lg * 4 + 3);
  } else if (slot >= 48) {
    s0[12] = 16 + (w ? 1 : 0); wp = r2p; wn = r2n;
    s0[13] = blk0(-2);
    s0[14] = w * 6144;
    s0[15] = (((slot - 48) >> 2) * 768 + w * 8) * 2048 + (slot & 3) * 512 + rot * 393216;
    s1[5] = (((slot - 48) >> 2) * 768 + w * 8) * 2048 + (slot & 3) * 512 + 1572864u;
    v0[0] = (lane >> 3) * 2048 + (((lane & 7) ^ ((lane >> 2) & 4)) << 4) + 0u;
    v0[1] = (lane >> 3) * 2048 + (((lane & 7) ^ ((lane >> 2) & 4)) << 4) + 128u;
    v0[2] = (lane >> 3) * 2048 + (((lane & 7) ^ ((lane >> 2) & 4)) << 4) + 256u;
    v0[3] = (lane >> 3) * 2048 + (((lane & 7) ^ ((lane >> 2) & 4)) << 4) + 384u;
    v0[4] = TA(w, 0, 0);
    v0[5] = TA(w, 0, 8);
    v0[6] = TA(w, 1, 0);
    v0[7] = TA(w, 1, 8);
    c0[0] = l16;
    c0[1] = lane >= 1 ? 0x80000000u : lane * 33024u;
    c0[2] = 142336u + lane * 4;
    c0[3] = 1136640u + ((slot - 48) >> 2) * 24576 + wr * 6144 + l16;
    c0[4] = 1140736u + ((slot - 48) >> 2) * 24576 + wr * 6144 + l16;
    c0[5] = lane >= 52 && slot >= 60 ? FW(p, 40 + (lane - 52) / 4, lane & 3) : FW(p, 24 + ((slot - 48) & ~3) + ((lane & 15) >> 2), lane & 3) + (slot) % 1 * 33024u;
    c0[6] = 1u;
    c0[7] = 559104u + (slot - 48) * 8192 + w * 2048 + l16;
  } else if (true) {
    s0[12] = 20 + (w < 2 ? w : 2); wp = r2p; wn = r2n;
    const u16* const b2p = arg_b2();
    s0[13] = blk0(-3);
    s0[14] = w * 4096;
    s0[15] = (3072 + w * 8) * 2048 + (slot - 43) * 512 + rot * 262144;
    s1[2] = (p * 32 + 31) * 32768;
    s1[5] = (3072 + w * 8) * 2048 + (slot - 43) * 512 + 1048576u;
    v0[0] = (lane >> 3) * 2048 + (((lane & 7) ^ ((lane >> 2) & 4)) << 4) + 0u;
    v0[1] = (lane >> 3) * 2048 + (((lane & 7) ^ ((lane >> 2) & 4)) << 4) + 128u;
    v0[2] = (lane >> 3) * 2048 + (((lane & 7) ^ ((lane >> 2) & 4)) << 4) + 256u;
    v0[3] = (lane >> 3) * 2048 + (((lane & 7) ^ ((lane >> 2) & 4)) << 4) + 384u;
    v0[4] = TA(w, 0, 0);
    v0[5] = TA(w, 0, 8);
    v0[6] = TA(w, 1, 0);
    v0[7] = TA(w, 1, 8);
    c0[0] = l16;
    c0[1] = lane >= 1 ? 0x80000000u : lane * 33024u;
    c0[2] = 142336u + lane * 4;
    c0[3] = 1581056u + wr * 4096 + l16;
    c0[4] = FW(p, 48 + (lane & 12) + slot - 43, lane & 3);
    c0[5] = 1u;
    c0[6] = 905216u + (slot - 43) * 8192 + w * 2048 + l16;
    c0[7] = 937984u + (slot - 43) * 8192 + w * 2048 + l16;
    c0[8] = 970752u + (slot - 43) * 8192 + w * 2048 + l16;
    c0[9] = 1003520u + (slot - 43) * 8192 + w * 2048 + l16;
    c0[10] = 1103872u + (slot - 43) * 8192 + w * 2048 + l16;
    c0[11] = 11421696u + (slot - 43) * 8192 + w * 2048 + l16;
    c0[12] = (lane >> 3) * 2048 + (lane & 7) * 16 + (slot - 43) * 512 + w * 128;
    c0[13] = ((lane >> 3) + 8) * 2048 + (lane & 7) * 16 + (slot - 43) * 512 + w * 128;
    c0[14] = 131072u + w * 2048 + li * 128 + (((0 + lg) ^ (li & 7)) << 4);
    c0[15] = 131072u + w * 2048 + li * 128 + (((4 + lg) ^ (li & 7)) << 4);
    c1[0] = 131072u + w * 2048 + (lane >> 3) * 128 + (((lane ^ (lane >> 3)) & 7) << 4);
    c1[1] = 140288u + ((li * 2 + (lg >> 1)) * 8 + w * 2 + (lg & 1)) * 4;
    c1[2] = 140288u + l16;
    c1[3] = (lane & 1) ? 0x80000000u : 11419648u + (slot - 43) * 128 + (lane >> 1) * 4;
    c1[4] = BIAS(b2p, 1024, (slot - 43) * 256 + w * 64 + 0 + lg * 8);
    c1[5] = BIAS(b2p, 1024, (slot - 43) * 256 + w * 64 + 1 + lg * 8);
    c1[6] = BIAS(b2p, 1024, (slot - 43) * 256 + w * 64 + 2 + lg * 8);
    c1[7] = BIAS(b2p, 1024, (slot - 43) * 256 + w * 64 + 3 + lg * 8);
    c1[8] = BIAS(b2p, 1024, (slot - 43) * 256 + w * 64 + 4 + lg * 8);
    c1[9] = BIAS(b2p, 1024, (slot - 43) * 256 + w * 64 + 5 + lg * 8);
    c1[10] = BIAS(b2p, 1024, (slot - 43) * 256 + w * 64 + 6 + lg * 8);
    c1[11] = BIAS(b2p, 1024, (slot - 43) * 256 + w * 64 + 7 + lg * 8);
    c1[12] = BIAS(b2p, 1024, (slot - 43) * 256 + w * 64 + 32 + lg * 8);
    c1[13] = BIAS(b2p, 1024, (slot - 43) * 256 + w * 64 + 33 + lg * 8);
    c1[14] = BIAS(b2p, 1024, (slot - 43) * 256 + w * 64 + 34 + lg * 8);
    c1[15] = BIAS(b2p, 1024, (slot - 43) * 256 + w * 64 + 35 + lg * 8);
    c2[0] = BIAS(b2p, 1024, (slot - 43) * 256 + w * 64 + 36 + lg * 8);
    c2[1] = BIAS(b2p, 1024, (slot - 43) * 256 + w * 64 + 37 + lg * 8);
    c2[2] = BIAS(b2p, 1024, (slot - 43) * 256 + w * 64 + 38 + lg * 8);
    c2[3] = BIAS(b2p, 1024, (slot - 43) * 256 + w * 64 + 39 + lg * 8);
    c2[4] = (lg == (li >> 2) && ((li & 3) >> 1) + 0 == 0) ? ((li & 1) ? 0x3f800000u : 0x3f80u) : 0u;
    c2[5] = (lg == (li >> 2) && ((li & 3) >> 1) + 0 == 1) ? ((li & 1) ? 0x3f800000u : 0x3f80u) : 0u;
    c2[6] = (lg == (li >> 2) && ((li & 3) >> 1) + 0 == 2) ? ((li & 1) ? 0x3f800000u : 0x3f80u) : 0u;
    c2[7] = (lg == (li >> 2) && ((li & 3) >> 1) + 0 == 3) ? ((li & 1) ? 0x3f800000u : 0x3f80u) : 0u;
    c2[8] = (lg == (li >> 2) && ((li & 3) >> 1) + 2 == 0) ? ((li & 1) ? 0x3f800000u : 0x3f80u) : 0u;
    c2[9] = (lg == (li >> 2) && ((li & 3) >> 1) + 2 == 1) ? ((li & 1) ? 0x3f800000u : 0x3f80u) : 0u;
    c2[10] = (lg == (li >> 2) && ((li & 3) >> 1) + 2 == 2) ? ((li & 1) ? 0x3f800000u : 0x3f80u) : 0u;
    c2[11] = (lg == (li >> 2) && ((li & 3) >> 1) + 2 == 3) ? ((li & 1) ? 0x3f800000u : 0x3f80u) : 0u;
  }
  s0[0] = (unsigned)(size_t)m.ws; s0[1] = (unsigned)((size_t)m.ws >> 32) & 0xffffu; s0[2] = WS_END; s0[3] = 0x00020000;
  s0[4] = (unsigned)(size_t)wp; s0[5] = (unsigned)((size_t)wp >> 32) & 0xffffu; s0[6] = wn; s0[7] = 0x00020000;
  s0[8] = (unsigned)(size_t)op; s0[9] = (unsigned)((size_t)op >> 32) & 0xffffu; s0[10] = 2048 * 1024 * 2; s0[11] = 0x00020000;
  s1[0] = FW(p, slot, w); s1[1] = w * 1024; s1[7] = DBG + ((p * 64 + slot) * 4 + w) * 1024;
#pragma unroll
  for (int i = 0; i < 16; ++i) s0[i] = __builtin_amdgcn_readfirstlane(s0[i]);
#pragma unroll
  for (int i = 0; i < 8; ++i) s1[i] = __builtin_amdgcn_readfirstlane(s1[i]);
  asm volatile(
R"(
.macro mav d,a,b
v_mfma_f32_16x16x32_bf16 v[\d:\d+3], a[\a:\a+3], v[\b:\b+3], v[\d:\d+3]
.endm
.macro Ta d,a,o
ds_read_b64_tr_b16 a[\d:\d+1], v\a offset:\o
ds_read_b64_tr_b16 a[\d+2:\d+3], v\a offset:\o+512
.endm
.macro mva d,a,b
v_mfma_f32_16x16x32_bf16 v[\d:\d+3], v[\a:\a+3], a[\b:\b+3], v[\d:\d+3]
.endm
.macro mvv d,a,b
v_mfma_f32_16x16x32_bf16 v[\d:\d+3], v[\a:\a+3], v[\b:\b+3], v[\d:\d+3]
.endm
.macro Tv d,a,o
ds_read_b64_tr_b16 v[\d:\d+1], v\a offset:\o
ds_read_b64_tr_b16 v[\d+2:\d+3], v\a offset:\o+512
.endm
.macro R d,o
ds_read_b128 v[\d:\d+3], v80 offset:\o
.endm
.macro D v,o
buffer_load_dwordx4 v\v, s[36:39], s49 offen offset:\o sc1 lds
.endm
.macro E v,o
s_add_u32 m0, s53, \o
s_nop 0
buffer_load_dwordx4 v\v, s[40:43], s51 offen lds
.endm
s_nop 4
s_mov_b32 s61, 200000
s_mov_b64 s[62:63], 255
s_mov_b32 s64, 0x3f803f80
s_mov_b32 s65, 0x3e8293ee
s_lshr_b32 s60, s48, 2
Lr0:
s_cmp_eq_u32 s60, 0
s_cbranch_scc0 Lr1
buffer_load_dword v79, off, s[36:39], 0 sc1
s_lshr_b32 s66, s53, 10
s_mul_i32 s66, s66, 8208
s_mov_b32 s67, 0x8000
E 0,110592
E 1,114688
s_add_u32 m0, s66, 2048
s_nop 0
buffer_load_dwordx4 v3, s[44:47], s67 offen offset:0 lds
E 2,118784
s_add_u32 s51, s51, 196608
E 0,122880
s_add_u32 m0, s66, 6144
s_nop 0
buffer_load_dwordx4 v3, s[44:47], 0 offen offset:0 lds
E 1,126976
E 2,131072
s_add_u32 s51, s51, 196608
E 0,86016
E 1,90112
E 2,94208
s_add_u32 s51, s51, 196608
E 0,98304
E 1,102400
E 2,106496
s_add_u32 s51, s51, 196608
v_lshlrev_b32 v12, 2, v80
v_and_b32 v12, 0xc0, v12
v_xor_b32 v13, 0, v12
v_add_u32 v8, v7, v13
v_xor_b32 v13, 64, v12
v_add_u32 v9, v7, v13
v_xor_b32 v13, 128, v12
v_add_u32 v10, v7, v13
v_xor_b32 v13, 192, v12
v_add_u32 v11, v7, v13
v_mov_b32 v110, s64
v_mov_b32 v111, s64
v_mov_b32 v112, s64
v_mov_b32 v113, s64
s_waitcnt vmcnt(6)
s_barrier
v_add_u32 v4, 69632, v4
v_add_u32 v5, 69632, v5
v_add_u32 v6, 69632, v6
Ta 0,4,40960
Ta 4,5,40960
Ta 8,6,40960
Ta 12,4,53248
Ta 16,5,53248
Ta 20,6,53248
ds_read_b128 v[38:41], v8 offset:0
ds_read_b128 v[42:45], v9 offset:0
ds_read_b128 v[46:49], v8 offset:4096
ds_read_b128 v[50:53], v9 offset:4096
v_mov_b32 v78, 0x100
v_add_u32 v79, 0x9e3779b9, v79
buffer_store_dwordx2 v[78:79], off, s[36:39], s52 sc1
E 0,61440
E 1,65536
s_add_u32 m0, s66, 2816
s_nop 0
buffer_load_dwordx4 v3, s[44:47], s67 offen offset:256 lds
E 2,69632
s_add_u32 s51, s51, 196608
E 0,73728
s_add_u32 m0, s66, 6912
s_nop 0
buffer_load_dwordx4 v3, s[44:47], 0 offen offset:256 lds
E 1,77824
E 2,81920
s_add_u32 s51, s51, 196608
E 0,36864
E 1,40960
E 2,45056
s_add_u32 s51, s51, 196608
E 0,49152
E 1,53248
E 2,57344
s_add_u32 s51, s51, 196608
s_waitcnt lgkmcnt(0)
s_waitcnt vmcnt(15)
s_barrier
E 0,110592
Ta 24,4,16384
v_mfma_f32_16x16x32_bf16 v[62:65], a[0:3], v[110:113], 0
v_mfma_f32_16x16x32_bf16 v[66:69], a[4:7], v[110:113], 0
Ta 28,5,16384
v_mfma_f32_16x16x32_bf16 v[70:73], v[110:113], a[8:11], 0
Ta 32,6,16384
E 1,114688
v_mfma_f32_16x16x32_bf16 v[14:17], a[0:3], v[38:41], 0
Ta 36,4,28672
v_mfma_f32_16x16x32_bf16 v[18:21], a[4:7], v[38:41], 0
Ta 40,5,28672
s_add_u32 m0, s66, 3584
s_nop 0
buffer_load_dwordx4 v3, s[44:47], s67 offen offset:512 lds
v_mfma_f32_16x16x32_bf16 v[22:25], v[38:41], a[8:11], 0
Ta 44,6,28672
v_mfma_f32_16x16x32_bf16 v[26:29], a[0:3], v[46:49], 0
ds_read_b128 v[54:57], v10 offset:0
E 2,118784
s_add_u32 s51, s51, 196608
v_mfma_f32_16x16x32_bf16 v[30:33], a[4:7], v[46:49], 0
ds_read_b128 v[58:61], v11 offset:0
v_mfma_f32_16x16x32_bf16 v[34:37], v[46:49], a[8:11], 0
ds_read_b128 v[96:99], v10 offset:4096
E 0,122880
mav 62,12,110
ds_read_b128 v[100:103], v11 offset:4096
mav 66,16,110
mva 70,110,20
s_add_u32 m0, s66, 7680
s_nop 0
buffer_load_dwordx4 v3, s[44:47], 0 offen offset:512 lds
mav 14,12,42
mav 18,16,42
E 1,126976
mva 22,42,20
mav 26,12,50
E 2,131072
s_add_u32 s51, s51, 196608
mav 30,16,50
mva 34,50,20
s_waitcnt lgkmcnt(0)
s_waitcnt vmcnt(14)
s_barrier
v_add_u32 v4, -49152, v4
v_add_u32 v5, -49152, v5
v_add_u32 v6, -49152, v6
E 0,86016
Ta 48,4,40960
mav 62,24,110
mav 66,28,110
Ta 52,5,40960
mva 70,110,32
Ta 56,6,40960
E 1,90112
mav 14,24,54
Ta 60,4,53248
mav 18,28,54
Ta 64,5,53248
mva 22,54,32
Ta 68,6,53248
E 2,94208
s_add_u32 s51, s51, 196608
mav 26,24,96
ds_read_b128 v[38:41], v8 offset:1024
mav 30,28,96
ds_read_b128 v[42:45], v9 offset:1024
mva 34,96,32
ds_read_b128 v[46:49], v8 offset:5120
E 0,98304
mav 62,36,110
ds_read_b128 v[50:53], v9 offset:5120
mav 66,40,110
mva 70,110,44
E 1,102400
mav 14,36,58
mav 18,40,58
mva 22,58,44
E 2,106496
s_add_u32 s51, s51, 196608
mav 26,36,100
mav 30,40,100
mva 34,100,44
s_waitcnt lgkmcnt(0)
s_waitcnt vmcnt(14)
s_barrier
E 0,61440
Ta 72,4,16384
mav 62,48,110
mav 66,52,110
Ta 76,5,16384
mva 70,110,56
Ta 80,6,16384
E 1,65536
mav 14,48,38
Ta 84,4,28672
mav 18,52,38
Ta 88,5,28672
s_add_u32 m0, s66, 4352
s_nop 0
buffer_load_dwordx4 v3, s[44:47], s67 offen offset:768 lds
mva 22,38,56
Ta 92,6,28672
mav 26,48,46
ds_read_b128 v[54:57], v10 offset:1024
E 2,69632
s_add_u32 s51, s51, 196608
mav 30,52,46
ds_read_b128 v[58:61], v11 offset:1024
mva 34,46,56
ds_read_b128 v[96:99], v10 offset:5120
E 0,73728
mav 62,60,110
ds_read_b128 v[100:103], v11 offset:5120
mav 66,64,110
mva 70,110,68
s_add_u32 m0, s66, 8448
s_nop 0
buffer_load_dwordx4 v3, s[44:47], 0 offen offset:768 lds
mav 14,60,42
mav 18,64,42
E 1,77824
mva 22,42,68
mav 26,60,50
E 2,81920
s_add_u32 s51, s51, 196608
mav 30,64,50
mva 34,50,68
s_waitcnt lgkmcnt(0)
s_waitcnt vmcnt(14)
s_barrier
v_add_u32 v4, 49152, v4
v_add_u32 v5, 49152, v5
v_add_u32 v6, 49152, v6
E 0,36864
Ta 96,4,40960
mav 62,72,110
mav 66,76,110
Ta 100,5,40960
mva 70,110,80
Ta 104,6,40960
E 1,40960
mav 14,72,54
Ta 108,4,53248
mav 18,76,54
Ta 112,5,53248
mva 22,54,80
Ta 116,6,53248
E 2,45056
s_add_u32 s51, s51, 196608
mav 26,72,96
ds_read_b128 v[38:41], v8 offset:2048
mav 30,76,96
ds_read_b128 v[42:45], v9 offset:2048
mva 34,96,80
ds_read_b128 v[46:49], v8 offset:6144
E 0,49152
mav 62,84,110
ds_read_b128 v[50:53], v9 offset:6144
mav 66,88,110
mva 70,110,92
E 1,53248
mav 14,84,58
mav 18,88,58
mva 22,58,92
E 2,57344
s_add_u32 s51, s51, 196608
mav 26,84,100
mav 30,88,100
mva 34,100,92
s_waitcnt lgkmcnt(0)
s_waitcnt vmcnt(14)
s_barrier
E 0,110592
Ta 120,4,16384
mav 62,96,110
mav 66,100,110
Ta 124,5,16384
mva 70,110,104
Ta 128,6,16384
E 1,114688
mav 14,96,38
Ta 132,4,28672
mav 18,100,38
Ta 136,5,28672
s_add_u32 m0, s66, 1024
s_nop 0
buffer_load_dwordx4 v3, s[44:47], s67 offen offset:1024 lds
mva 22,38,104
Ta 140,6,28672
mav 26,96,46
ds_read_b128 v[54:57], v10 offset:2048
E 2,118784
s_add_u32 s51, s51, 196608
mav 30,100,46
ds_read_b128 v[58:61], v11 offset:2048
mva 34,46,104
ds_read_b128 v[96:99], v10 offset:6144
E 0,122880
mav 62,108,110
ds_read_b128 v[100:103], v11 offset:6144
mav 66,112,110
mva 70,110,116
s_add_u32 m0, s66, 5120
s_nop 0
buffer_load_dwordx4 v3, s[44:47], 0 offen offset:1024 lds
mav 14,108,42
mav 18,112,42
E 1,126976
mva 22,42,116
mav 26,108,50
E 2,131072
s_add_u32 s51, s51, 196608
mav 30,112,50
mva 34,50,116
s_waitcnt lgkmcnt(0)
s_waitcnt vmcnt(14)
s_barrier
v_add_u32 v4, -49152, v4
v_add_u32 v5, -49152, v5
v_add_u32 v6, -49152, v6
E 0,86016
Ta 144,4,40960
mav 62,120,110
mav 66,124,110
Ta 148,5,40960
mva 70,110,128
Ta 152,6,40960
E 1,90112
mav 14,120,54
Ta 156,4,53248
mav 18,124,54
Ta 160,5,53248
mva 22,54,128
Ta 164,6,53248
E 2,94208
s_add_u32 s51, s51, 196608
mav 26,120,96
ds_read_b128 v[38:41], v8 offset:3072
mav 30,124,96
ds_read_b128 v[42:45], v9 offset:3072
mva 34,96,128
ds_read_b128 v[46:49], v8 offset:7168
E 0,98304
mav 62,132,110
ds_read_b128 v[50:53], v9 offset:7168
mav 66,136,110
mva 70,110,140
E 1,102400
mav 14,132,58
mav 18,136,58
mva 22,58,140
E 2,106496
s_add_u32 s51, s51, 196608
mav 26,132,100
mav 30,136,100
mva 34,100,140
s_waitcnt lgkmcnt(0)
s_waitcnt vmcnt(14)
s_barrier
E 0,61440
Ta 168,4,16384
mav 62,144,110
mav 66,148,110
Ta 172,5,16384
mva 70,110,152
Ta 176,6,16384
E 1,65536
mav 14,144,38
Ta 180,4,28672
mav 18,148,38
Ta 184,5,28672
s_add_u32 m0, s66, 1792
s_nop 0
buffer_load_dwordx4 v3, s[44:47], s67 offen offset:1280 lds
mva 22,38,152
Ta 188,6,28672
mav 26,144,46
ds_read_b128 v[54:57], v10 offset:3072
E 2,69632
s_add_u32 s51, s51, 196608
mav 30,148,46
ds_read_b128 v[58:61], v11 offset:3072
mva 34,46,152
ds_read_b128 v[96:99], v10 offset:7168
E 0,73728
mav 62,156,110
ds_read_b128 v[100:103], v11 offset:7168
mav 66,160,110
mva 70,110,164
s_add_u32 m0, s66, 5888
s_nop 0
buffer_load_dwordx4 v3, s[44:47], 0 offen offset:1280 lds
mav 14,156,42
mav 18,160,42
E 1,77824
mva 22,42,164
mav 26,156,50
E 2,81920
s_add_u32 s51, s51, 196608
mav 30,160,50
mva 34,50,164
s_waitcnt lgkmcnt(0)
s_waitcnt vmcnt(14)
s_barrier
v_add_u32 v4, 49152, v4
v_add_u32 v5, 49152, v5
v_add_u32 v6, 49152, v6
E 0,36864
Ta 192,4,40960
mav 62,168,110
mav 66,172,110
Ta 196,5,40960
mva 70,110,176
Ta 200,6,40960
E 1,40960
mav 14,168,54
Ta 204,4,53248
mav 18,172,54
Ta 208,5,53248
mva 22,54,176
Ta 212,6,53248
E 2,45056
s_add_u32 s51, s51, 196608
mav 26,168,96
ds_read_b128 v[38:41], v8 offset:0
mav 30,172,96
ds_read_b128 v[42:45], v9 offset:0
mva 34,96,176
ds_read_b128 v[46:49], v8 offset:4096
E 0,49152
mav 62,180,110
ds_read_b128 v[50:53], v9 offset:4096
mav 66,184,110
mva 70,110,188
E 1,53248
mav 14,180,58
mav 18,184,58
mva 22,58,188
E 2,57344
s_add_u32 s51, s51, 196608
mav 26,180,100
mav 30,184,100
mva 34,100,188
s_waitcnt lgkmcnt(0)
s_waitcnt vmcnt(14)
s_barrier
E 0,110592
Ta 216,4,16384
mav 62,192,110
mav 66,196,110
Ta 220,5,16384
mva 70,110,200
Ta 224,6,16384
E 1,114688
mav 14,192,38
Ta 228,4,28672
mav 18,196,38
Ta 232,5,28672
s_add_u32 m0, s66, 2560
s_nop 0
buffer_load_dwordx4 v3, s[44:47], s67 offen offset:1536 lds
mva 22,38,200
Ta 236,6,28672
mav 26,192,46
ds_read_b128 v[54:57], v10 offset:0
E 2,118784
s_add_u32 s51, s51, 196608
mav 30,196,46
ds_read_b128 v[58:61], v11 offset:0
mva 34,46,200
ds_read_b128 v[96:99], v10 offset:4096
E 0,122880
mav 62,204,110
ds_read_b128 v[100:103], v11 offset:4096
mav 66,208,110
mva 70,110,212
s_add_u32 m0, s66, 6656
s_nop 0
buffer_load_dwordx4 v3, s[44:47], 0 offen offset:1536 lds
mav 14,204,42
mav 18,208,42
E 1,126976
mva 22,42,212
mav 26,204,50
E 2,131072
s_add_u32 s51, s51, 196608
mav 30,208,50
mva 34,50,212
s_waitcnt lgkmcnt(0)
s_waitcnt vmcnt(14)
s_barrier
v_add_u32 v4, -49152, v4
v_add_u32 v5, -49152, v5
v_add_u32 v6, -49152, v6
E 0,86016
Ta 240,4,40960
mav 62,216,110
mav 66,220,110
Ta 244,5,40960
mva 70,110,224
Ta 248,6,40960
E 1,90112
mav 14,216,54
Ta 252,4,53248
mav 18,220,54
Tv 128,5,53248
mva 22,54,224
Tv 132,6,53248
E 2,94208
s_add_u32 s51, s51, 196608
mav 26,216,96
ds_read_b128 v[38:41], v8 offset:1024
mav 30,220,96
ds_read_b128 v[42:45], v9 offset:1024
mva 34,96,224
ds_read_b128 v[46:49], v8 offset:5120
E 0,98304
mav 62,228,110
ds_read_b128 v[50:53], v9 offset:5120
mav 66,232,110
mva 70,110,236
E 1,102400
mav 14,228,58
mav 18,232,58
mva 22,58,236
E 2,106496
s_add_u32 s51, s51, 196608
mav 26,228,100
mav 30,232,100
mva 34,100,236
s_waitcnt lgkmcnt(0)
s_waitcnt vmcnt(14)
s_barrier
E 0,61440
Tv 136,4,16384
mav 62,240,110
mav 66,244,110
Tv 140,5,16384
mva 70,110,248
Tv 144,6,16384
E 1,65536
mav 14,240,38
Tv 148,4,28672
mav 18,244,38
Tv 152,5,28672
s_add_u32 m0, s66, 3328
s_nop 0
buffer_load_dwordx4 v3, s[44:47], s67 offen offset:1792 lds
mva 22,38,248
Tv 156,6,28672
mav 26,240,46
ds_read_b128 v[54:57], v10 offset:1024
E 2,69632
s_add_u32 s51, s51, 196608
mav 30,244,46
ds_read_b128 v[58:61], v11 offset:1024
mva 34,46,248
ds_read_b128 v[96:99], v10 offset:5120
E 0,73728
mav 62,252,110
ds_read_b128 v[100:103], v11 offset:5120
mvv 66,128,110
mvv 70,110,132
s_add_u32 m0, s66, 7424
s_nop 0
buffer_load_dwordx4 v3, s[44:47], 0 offen offset:1792 lds
mav 14,252,42
mvv 18,128,42
E 1,77824
mvv 22,42,132
mav 26,252,50
E 2,81920
s_add_u32 s51, s51, 196608
mvv 30,128,50
mvv 34,50,132
s_waitcnt lgkmcnt(0)
s_waitcnt vmcnt(14)
s_barrier
v_add_u32 v4, 49152, v4
v_add_u32 v5, 49152, v5
v_add_u32 v6, 49152, v6
E 0,36864
Tv 160,4,40960
mvv 62,136,110
mvv 66,140,110
Tv 164,5,40960
mvv 70,110,144
Tv 168,6,40960
E 1,40960
mvv 14,136,54
Tv 172,4,53248
mvv 18,140,54
Tv 176,5,53248
mvv 22,54,144
Tv 180,6,53248
E 2,45056
s_add_u32 s51, s51, 196608
mvv 26,136,96
ds_read_b128 v[38:41], v8 offset:2048
mvv 30,140,96
ds_read_b128 v[42:45], v9 offset:2048
mvv 34,96,144
ds_read_b128 v[46:49], v8 offset:6144
E 0,49152
mvv 62,148,110
ds_read_b128 v[50:53], v9 offset:6144
mvv 66,152,110
mvv 70,110,156
E 1,53248
mvv 14,148,58
mvv 18,152,58
mvv 22,58,156
E 2,57344
s_add_u32 s51, s51, 196608
mvv 26,148,100
mvv 30,152,100
mvv 34,100,156
s_waitcnt lgkmcnt(0)
s_waitcnt vmcnt(14)
s_barrier
Tv 184,4,16384
mvv 62,160,110
mvv 66,164,110
Tv 188,5,16384
mvv 70,110,168
Tv 192,6,16384
mvv 14,160,38
Tv 196,4,28672
mvv 18,164,38
Tv 200,5,28672
mvv 22,38,168
Tv 204,6,28672
mvv 26,160,46
ds_read_b128 v[54:57], v10 offset:2048
mvv 30,164,46
ds_read_b128 v[58:61], v11 offset:2048
mvv 34,46,168
ds_read_b128 v[96:99], v10 offset:6144
mvv 62,172,110
ds_read_b128 v[100:103], v11 offset:6144
mvv 66,176,110
mvv 70,110,180
mvv 14,172,42
mvv 18,176,42
mvv 22,42,180
mvv 26,172,50
mvv 30,176,50
mvv 34,50,180
s_waitcnt lgkmcnt(0)
s_waitcnt vmcnt(6)
s_barrier
v_add_u32 v4, -49152, v4
v_add_u32 v5, -49152, v5
v_add_u32 v6, -49152, v6
Tv 208,4,40960
mvv 62,184,110
mvv 66,188,110
Tv 212,5,40960
mvv 70,110,192
Tv 216,6,40960
mvv 14,184,54
Tv 220,4,53248
mvv 18,188,54
Tv 224,5,53248
mvv 22,54,192
Tv 228,6,53248
mvv 26,184,96
ds_read_b128 v[38:41], v8 offset:3072
mvv 30,188,96
ds_read_b128 v[42:45], v9 offset:3072
mvv 34,96,192
ds_read_b128 v[46:49], v8 offset:7168
mvv 62,196,110
ds_read_b128 v[50:53], v9 offset:7168
mvv 66,200,110
mvv 70,110,204
mvv 14,196,58
mvv 18,200,58
mvv 22,58,204
mvv 26,196,100
mvv 30,200,100
mvv 34,100,204
s_waitcnt lgkmcnt(0)
s_waitcnt vmcnt(0)
s_barrier
Tv 232,4,16384
mvv 62,208,110
mvv 66,212,110
Tv 236,5,16384
mvv 70,110,216
Tv 240,6,16384
mvv 14,208,38
Tv 244,4,28672
mvv 18,212,38
Tv 248,5,28672
mvv 22,38,216
Tv 252,6,28672
mvv 26,208,46
ds_read_b128 v[54:57], v10 offset:3072
mvv 30,212,46
ds_read_b128 v[58:61], v11 offset:3072
mvv 34,46,216
ds_read_b128 v[96:99], v10 offset:7168
mvv 62,220,110
ds_read_b128 v[100:103], v11 offset:7168
mvv 66,224,110
mvv 70,110,228
mvv 14,220,42
mvv 18,224,42
mvv 22,42,228
mvv 26,220,50
mvv 30,224,50
mvv 34,50,228
s_waitcnt lgkmcnt(0)
mvv 62,232,110
mvv 66,236,110
mvv 70,110,240
mvv 14,232,54
mvv 18,236,54
mvv 22,54,240
mvv 26,232,96
mvv 30,236,96
mvv 34,96,240
mvv 62,244,110
mvv 66,248,110
mvv 70,110,252
mvv 14,244,58
mvv 18,248,58
mvv 22,58,252
mvv 26,244,100
mvv 30,248,100
mvv 34,100,252
s_barrier
s_nop 7
v_mov_b32 v118, v70
s_and_b32 s60, s48, 3
LvQA:
v_xor_b32 v80, 0x10000, v80
ds_read_b64 v[120:121], v82 offset:0
ds_read_b128 v[70:73], v83 offset:0
ds_read_b128 v[74:77], v83 offset:16
s_add_u32 s49, s49, 346112
R 38,32768
R 42,33792
R 46,34816
R 50,35840
s_waitcnt lgkmcnt(2)
v_mul_f32 v71, v71, v118
v_fma_f32 v22, v22, v70, v71
v_mul_f32 v73, v73, v118
v_fma_f32 v23, v23, v72, v73
v_mul_f32 v75, v75, v118
v_fma_f32 v24, v24, v74, v75
v_mul_f32 v77, v77, v118
v_fma_f32 v25, v25, v76, v77
v_cvt_pk_bf16_f32 v8, v22, v23
v_cvt_pk_bf16_f32 v9, v24, v25
v_mul_f32 v122, v121, v62
v_fma_f32 v14, v14, v120, v122
v_mul_f32 v123, v121, v63
v_fma_f32 v15, v15, v120, v123
v_mul_f32 v122, v121, v64
v_fma_f32 v16, v16, v120, v122
v_mul_f32 v123, v121, v65
v_fma_f32 v17, v17, v120, v123
v_mul_f32 v122, v121, v66
v_fma_f32 v18, v18, v120, v122
v_mul_f32 v123, v121, v67
v_fma_f32 v19, v19, v120, v123
v_mul_f32 v122, v121, v68
v_fma_f32 v20, v20, v120, v122
v_mul_f32 v123, v121, v69
v_fma_f32 v21, v21, v120, v123
v_cvt_pk_bf16_f32 v0, v14, v15
v_cvt_pk_bf16_f32 v1, v16, v17
v_cvt_pk_bf16_f32 v2, v18, v19
v_cvt_pk_bf16_f32 v3, v20, v21
s_add_u32 s49, s49, 346112
s_barrier
ds_read_b128 v[96:99], v81 offset:0
ds_read_b64 v[104:105], v81 offset:1024
ds_read_b64 v[120:121], v82 offset:128
ds_read_b128 v[70:73], v83 offset:128
ds_read_b128 v[74:77], v83 offset:144
v_mfma_f32_16x16x32_bf16 v[14:17], a[0:3], v[38:41], 0
R 54,36864
v_mfma_f32_16x16x32_bf16 v[18:21], a[4:7], v[38:41], 0
s_waitcnt lgkmcnt(1)
v_mul_f32 v71, v71, v118
v_mfma_f32_16x16x32_bf16 v[22:25], v[38:41], a[8:11], 0
v_fma_f32 v34, v34, v70, v71
v_mul_f32 v73, v73, v118
mav 14,12,42
R 58,37888
v_fma_f32 v35, v35, v72, v73
mav 18,16,42
v_mul_f32 v75, v75, v118
v_fma_f32 v36, v36, v74, v75
mva 22,42,20
v_mul_f32 v77, v77, v118
v_fma_f32 v37, v37, v76, v77
mav 14,24,46
R 38,38912
v_cvt_pk_bf16_f32 v10, v34, v35
mav 18,28,46
v_cvt_pk_bf16_f32 v11, v36, v37
v_mul_f32 v122, v121, v62
mva 22,46,32
v_mfma_f32_16x16x32_bf16 v[70:73], v[96:99], v[0:3], v[84:87]
v_fma_f32 v26, v26, v120, v122
v_mul_f32 v123, v121, v63
mav 14,36,50
R 42,39936
v_fma_f32 v27, v27, v120, v123
mav 18,40,50
v_mul_f32 v122, v121, v64
v_fma_f32 v28, v28, v120, v122
mva 22,50,44
v_mul_f32 v123, v121, v65
s_waitcnt lgkmcnt(2)
mav 14,48,54
R 46,40960
v_fma_f32 v29, v29, v120, v123
mav 18,52,54
v_mul_f32 v122, v121, v66
v_fma_f32 v30, v30, v120, v122
mva 22,54,56
v_mul_f32 v123, v121, v67
v_fma_f32 v31, v31, v120, v123
mav 14,60,58
R 50,41984
v_mul_f32 v122, v121, v68
mav 18,64,58
v_fma_f32 v32, v32, v120, v122
v_mul_f32 v123, v121, v69
mva 22,58,68
v_fma_f32 v33, v33, v120, v123
s_waitcnt lgkmcnt(2)
mav 14,72,38
R 54,43008
mav 18,76,38
buffer_store_dwordx4 v[96:99], v81, s[36:39], s54 offen sc1
buffer_store_dwordx2 v[8:9], v81, s[36:39], s54 offen offset:1024 sc1
mva 22,38,80
buffer_store_dwordx2 v[104:105], v81, s[36:39], s54 offen offset:1032 sc1
mav 14,84,42
R 58,44032
mav 18,88,42
mva 22,42,92
s_waitcnt lgkmcnt(2)
mav 14,96,46
R 38,45056
mav 18,100,46
s_barrier
ds_read_b128 v[100:103], v81 offset:4096
mva 22,46,104
ds_read_b64 v[106:107], v81 offset:5120
mav 14,108,50
R 42,46080
mav 18,112,50
mva 22,50,116
s_waitcnt lgkmcnt(4)
mav 14,120,54
R 46,47104
mav 18,124,54
mva 22,54,128
s_waitcnt lgkmcnt(3)
v_mfma_f32_16x16x32_bf16 v[74:77], v[100:103], v[0:3], v[88:91]
v_cvt_pk_bf16_f32 v0, v26, v27
v_cvt_pk_bf16_f32 v1, v28, v29
mav 14,132,58
R 50,48128
v_cvt_pk_bf16_f32 v2, v30, v31
mav 18,136,58
v_cvt_pk_bf16_f32 v3, v32, v33
mva 22,58,140
v_max3_f32 v114, v70, v71, v74
v_max3_f32 v115, v72, v73, v76
mav 14,144,38
R 54,49152
v_max_f32 v114, v114, v75
mav 18,148,38
v_max_f32 v115, v115, v77
v_max_f32 v116, v114, v115
mva 22,38,152
v_max_f32 v117, v114, v115
s_waitcnt lgkmcnt(2)
mav 14,156,42
R 38,50176
v_permlane32_swap_b32 v116, v117
mav 18,160,42
v_max_f32 v110, v116, v117
v_max_f32 v111, v116, v117
mva 22,42,164
mav 14,168,46
R 42,51200
v_permlane16_swap_b32 v110, v111
mav 18,172,46
v_max_f32 v116, v110, v111
v_mul_f32 v116, s65, v116
mva 22,46,176
v_fma_f32 v114, v114, s65, -v116
s_waitcnt lgkmcnt(2)
mav 14,180,50
R 46,52224
v_fma_f32 v115, v115, s65, -v116
mav 18,184,50
v_exp_f32 v114, v114
mva 22,50,188
v_exp_f32 v115, v115
mav 14,192,54
R 50,53248
v_add_f32 v116, v114, v115
mav 18,196,54
v_add_f32 v117, v114, v115
mva 22,54,200
s_waitcnt lgkmcnt(2)
mav 14,204,38
R 54,54272
v_permlane32_swap_b32 v116, v117
mav 18,208,38
v_add_f32 v110, v116, v117
v_add_f32 v111, v116, v117
mva 22,38,212
mav 14,216,42
R 38,55296
v_permlane16_swap_b32 v110, v111
mav 18,220,42
v_add_f32 v116, v110, v111
mva 22,42,224
s_waitcnt lgkmcnt(2)
mav 14,228,46
R 42,56320
mav 18,232,46
v_rcp_f32 v116, v116
mva 22,46,236
v_mul_f32 v114, v114, v116
v_mul_f32 v115, v115, v116
mav 14,240,50
R 46,57344
v_cvt_pk_bf16_f32 v114, v114, v114
mav 18,244,50
v_cvt_pk_bf16_f32 v115, v115, v115
v_and_b32 v110, v114, v92
mva 22,50,248
v_and_b32 v111, v115, v93
s_waitcnt lgkmcnt(2)
mav 14,252,54
R 50,58368
v_and_b32 v112, v114, v94
mvv 18,128,54
v_and_b32 v113, v115, v95
mvv 22,54,132
mvv 14,136,38
R 54,59392
v_mfma_f32_16x16x32_bf16 v[70:73], v[8:11], v[110:113], 0
v_mfma_f32_16x16x32_bf16 v[74:77], v[104:107], v[110:113], 0
mvv 18,140,38
mvv 22,38,144
s_waitcnt lgkmcnt(2)
mvv 14,148,42
R 38,60416
mvv 18,152,42
mvv 22,42,156
v_cvt_pk_bf16_f32 v110, v70, v71
v_cvt_pk_bf16_f32 v111, v72, v73
mvv 14,160,46
R 42,61440
v_cvt_pk_bf16_f32 v112, v74, v75
mvv 18,164,46
v_cvt_pk_bf16_f32 v113, v76, v77
buffer_store_dwordx4 v[110:113], v81, s[36:39], s49 offen sc1
mvv 22,46,168
s_add_u32 s49, s49, 346112
s_waitcnt lgkmcnt(2)
mvv 14,172,50
R 46,62464
mvv 18,176,50
mvv 22,50,180
mvv 14,184,54
R 50,63488
mvv 18,188,54
mvv 22,54,192
s_waitcnt lgkmcnt(2)
mvv 14,196,38
R 54,64512
mvv 18,200,38
mvv 22,38,204
mvv 14,208,42
R 38,0
mvv 18,212,42
ds_read_b64 v[120:121], v82 offset:0
ds_read_b128 v[70:73], v83 offset:0
mvv 22,42,216
ds_read_b128 v[74:77], v83 offset:16
s_waitcnt lgkmcnt(5)
mvv 14,220,46
R 42,1024
mvv 18,224,46
mvv 22,46,228
mvv 14,232,50
R 46,2048
mvv 18,236,50
mvv 22,50,240
s_waitcnt lgkmcnt(2)
mvv 14,244,54
R 50,3072
mvv 18,248,54
mvv 22,54,252
v_mfma_f32_16x16x32_bf16 v[26:29], a[0:3], v[38:41], 0
R 54,4096
v_mul_f32 v71, v71, v118
v_mfma_f32_16x16x32_bf16 v[30:33], a[4:7], v[38:41], 0
v_mul_f32 v73, v73, v118
v_mul_f32 v75, v75, v118
v_mfma_f32_16x16x32_bf16 v[34:37], v[38:41], a[8:11], 0
v_mul_f32 v77, v77, v118
s_waitcnt lgkmcnt(2)
mav 26,12,42
R 58,5120
v_fma_f32 v22, v22, v70, v71
mav 30,16,42
v_fma_f32 v23, v23, v72, v73
v_fma_f32 v24, v24, v74, v75
mva 34,42,20
v_fma_f32 v25, v25, v76, v77
v_cvt_pk_bf16_f32 v8, v22, v23
mav 26,24,46
R 38,6144
v_cvt_pk_bf16_f32 v9, v24, v25
mav 30,28,46
v_mul_f32 v122, v121, v62
v_mfma_f32_16x16x32_bf16 v[70:73], v[100:103], v[0:3], v[84:87]
v_fma_f32 v14, v14, v120, v122
v_mul_f32 v123, v121, v63
mva 34,46,32
v_fma_f32 v15, v15, v120, v123
s_waitcnt lgkmcnt(2)
mav 26,36,50
R 42,7168
v_mul_f32 v122, v121, v64
mav 30,40,50
v_fma_f32 v16, v16, v120, v122
v_mul_f32 v123, v121, v65
mva 34,50,44
v_fma_f32 v17, v17, v120, v123
v_mul_f32 v122, v121, v66
mav 26,48,54
R 46,8192
v_fma_f32 v18, v18, v120, v122
mav 30,52,54
v_mul_f32 v123, v121, v67
v_fma_f32 v19, v19, v120, v123
mva 34,54,56
v_mul_f32 v122, v121, v68
s_waitcnt lgkmcnt(2)
mav 26,60,58
R 50,9216
v_fma_f32 v20, v20, v120, v122
mav 30,64,58
v_mul_f32 v123, v121, v69
v_fma_f32 v21, v21, v120, v123
mva 34,58,68
mav 26,72,38
R 54,10240
mav 30,76,38
mva 34,38,80
s_waitcnt lgkmcnt(2)
mav 26,84,42
R 58,11264
mav 30,88,42
mva 34,42,92
mav 26,96,46
R 38,12288
v_add_u32 v78, 1, v78
mav 30,100,46
s_barrier
ds_read_b128 v[96:99], v81 offset:0
mva 34,46,104
ds_read_b64 v[104:105], v81 offset:1024
s_waitcnt lgkmcnt(4)
mav 26,108,50
R 42,13312
mav 30,112,50
mva 34,50,116
mav 26,120,54
R 46,14336
mav 30,124,54
s_waitcnt vmcnt(0)
buffer_store_dwordx2 v[78:79], off, s[36:39], s52 sc1
mva 34,54,128
s_waitcnt lgkmcnt(3)
v_mfma_f32_16x16x32_bf16 v[74:77], v[96:99], v[0:3], v[88:91]
v_cvt_pk_bf16_f32 v0, v14, v15
v_cvt_pk_bf16_f32 v1, v16, v17
mav 26,132,58
R 50,15360
v_cvt_pk_bf16_f32 v2, v18, v19
mav 30,136,58
v_cvt_pk_bf16_f32 v3, v20, v21
mva 34,58,140
v_max3_f32 v114, v70, v71, v74
v_max3_f32 v115, v72, v73, v76
mav 26,144,38
R 54,16384
v_max_f32 v114, v114, v75
mav 30,148,38
v_max_f32 v115, v115, v77
v_max_f32 v116, v114, v115
mva 34,38,152
v_max_f32 v117, v114, v115
s_waitcnt lgkmcnt(2)
mav 26,156,42
R 38,17408
v_permlane32_swap_b32 v116, v117
mav 30,160,42
v_max_f32 v110, v116, v117
v_max_f32 v111, v116, v117
mva 34,42,164
mav 26,168,46
R 42,18432
v_permlane16_swap_b32 v110, v111
mav 30,172,46
v_max_f32 v116, v110, v111
v_mul_f32 v116, s65, v116
mva 34,46,176
v_fma_f32 v114, v114, s65, -v116
s_waitcnt lgkmcnt(2)
mav 26,180,50
R 46,19456
v_fma_f32 v115, v115, s65, -v116
mav 30,184,50
v_exp_f32 v114, v114
mva 34,50,188
v_exp_f32 v115, v115
mav 26,192,54
R 50,20480
v_add_f32 v116, v114, v115
mav 30,196,54
v_add_f32 v117, v114, v115
mva 34,54,200
s_waitcnt lgkmcnt(2)
mav 26,204,38
R 54,21504
v_permlane32_swap_b32 v116, v117
mav 30,208,38
v_add_f32 v110, v116, v117
v_add_f32 v111, v116, v117
mva 34,38,212
mav 26,216,42
R 38,22528
v_permlane16_swap_b32 v110, v111
mav 30,220,42
v_add_f32 v116, v110, v111
mva 34,42,224
s_waitcnt lgkmcnt(2)
mav 26,228,46
R 42,23552
mav 30,232,46
v_rcp_f32 v116, v116
mva 34,46,236
v_mul_f32 v114, v114, v116
v_mul_f32 v115, v115, v116
mav 26,240,50
R 46,24576
v_cvt_pk_bf16_f32 v114, v114, v114
mav 30,244,50
v_cvt_pk_bf16_f32 v115, v115, v115
v_and_b32 v110, v114, v94
mva 34,50,248
v_and_b32 v111, v115, v95
s_waitcnt lgkmcnt(2)
mav 26,252,54
R 50,25600
v_and_b32 v112, v114, v92
mvv 30,128,54
v_and_b32 v113, v115, v93
mvv 34,54,132
mvv 26,136,38
R 54,26624
v_mfma_f32_16x16x32_bf16 v[70:73], v[8:11], v[110:113], 0
v_mfma_f32_16x16x32_bf16 v[74:77], v[104:107], v[110:113], 0
mvv 30,140,38
mvv 34,38,144
s_waitcnt lgkmcnt(2)
mvv 26,148,42
R 38,27648
mvv 30,152,42
mvv 34,42,156
v_cvt_pk_bf16_f32 v110, v70, v71
v_cvt_pk_bf16_f32 v111, v72, v73
mvv 26,160,46
R 42,28672
v_cvt_pk_bf16_f32 v112, v74, v75
mvv 30,164,46
v_cvt_pk_bf16_f32 v113, v76, v77
buffer_store_dwordx4 v[110:113], v81, s[36:39], s49 offen sc1
mvv 34,46,168
s_add_u32 s49, s49, 346112
s_waitcnt lgkmcnt(2)
mvv 26,172,50
R 46,29696
mvv 30,176,50
mvv 34,50,180
mvv 26,184,54
R 50,30720
mvv 30,188,54
mvv 34,54,192
s_waitcnt lgkmcnt(2)
mvv 26,196,38
R 54,31744
v_xor_b32 v80, 0x10000, v80
mvv 30,200,38
mvv 34,38,204
mvv 26,208,42
R 38,32768
mvv 30,212,42
ds_read_b64 v[120:121], v82 offset:128
ds_read_b128 v[70:73], v83 offset:128
mvv 34,42,216
ds_read_b128 v[74:77], v83 offset:144
s_waitcnt lgkmcnt(5)
mvv 26,220,46
R 42,33792
mvv 30,224,46
mvv 34,46,228
mvv 26,232,50
R 46,34816
mvv 30,236,50
mvv 34,50,240
s_waitcnt lgkmcnt(2)
mvv 26,244,54
R 50,35840
mvv 30,248,54
mvv 34,54,252
s_mov_b32 s60, -13
.p2align 6
LQA_1:
v_mfma_f32_16x16x32_bf16 v[14:17], a[0:3], v[38:41], 0
R 54,36864
v_mul_f32 v71, v71, v118
v_mfma_f32_16x16x32_bf16 v[18:21], a[4:7], v[38:41], 0
v_mul_f32 v73, v73, v118
v_mul_f32 v75, v75, v118
v_mfma_f32_16x16x32_bf16 v[22:25], v[38:41], a[8:11], 0
v_mul_f32 v77, v77, v118
s_waitcnt lgkmcnt(2)
mav 14,12,42
R 58,37888
v_fma_f32 v34, v34, v70, v71
mav 18,16,42
v_fma_f32 v35, v35, v72, v73
v_fma_f32 v36, v36, v74, v75
mva 22,42,20
v_fma_f32 v37, v37, v76, v77
v_cvt_pk_bf16_f32 v10, v34, v35
mav 14,24,46
R 38,38912
v_cvt_pk_bf16_f32 v11, v36, v37
mav 18,28,46
v_mul_f32 v122, v121, v62
v_mfma_f32_16x16x32_bf16 v[70:73], v[96:99], v[0:3], v[84:87]
v_fma_f32 v26, v26, v120, v122
v_mul_f32 v123, v121, v63
mva 22,46,32
v_fma_f32 v27, v27, v120, v123
s_waitcnt lgkmcnt(2)
mav 14,36,50
R 42,39936
v_mul_f32 v122, v121, v64
mav 18,40,50
v_fma_f32 v28, v28, v120, v122
v_mul_f32 v123, v121, v65
mva 22,50,44
v_fma_f32 v29, v29, v120, v123
v_mul_f32 v122, v121, v66
mav 14,48,54
R 46,40960
v_fma_f32 v30, v30, v120, v122
mav 18,52,54
v_mul_f32 v123, v121, v67
v_fma_f32 v31, v31, v120, v123
mva 22,54,56
v_mul_f32 v122, v121, v68
s_waitcnt lgkmcnt(2)
mav 14,60,58
R 50,41984
v_fma_f32 v32, v32, v120, v122
mav 18,64,58
v_mul_f32 v123, v121, v69
v_fma_f32 v33, v33, v120, v123
mva 22,58,68
mav 14,72,38
R 54,43008
mav 18,76,38
mva 22,38,80
s_waitcnt lgkmcnt(2)
mav 14,84,42
R 58,44032
mav 18,88,42
mva 22,42,92
mav 14,96,46
R 38,45056
v_add_u32 v78, 1, v78
mav 18,100,46
s_barrier
ds_read_b128 v[100:103], v81 offset:4096
mva 22,46,104
ds_read_b64 v[106:107], v81 offset:5120
s_waitcnt lgkmcnt(4)
mav 14,108,50
R 42,46080
mav 18,112,50
mva 22,50,116
mav 14,120,54
R 46,47104
mav 18,124,54
s_waitcnt vmcnt(0)
buffer_store_dwordx2 v[78:79], off, s[36:39], s52 sc1
mva 22,54,128
s_waitcnt lgkmcnt(3)
v_mfma_f32_16x16x32_bf16 v[74:77], v[100:103], v[0:3], v[88:91]
v_cvt_pk_bf16_f32 v0, v26, v27
v_cvt_pk_bf16_f32 v1, v28, v29
mav 14,132,58
R 50,48128
v_cvt_pk_bf16_f32 v2, v30, v31
mav 18,136,58
v_cvt_pk_bf16_f32 v3, v32, v33
mva 22,58,140
v_max3_f32 v114, v70, v71, v74
v_max3_f32 v115, v72, v73, v76
mav 14,144,38
R 54,49152
v_max_f32 v114, v114, v75
mav 18,148,38
v_max_f32 v115, v115, v77
v_max_f32 v116, v114, v115
mva 22,38,152
v_max_f32 v117, v114, v115
s_waitcnt lgkmcnt(2)
mav 14,156,42
R 38,50176
v_permlane32_swap_b32 v116, v117
mav 18,160,42
v_max_f32 v110, v116, v117
v_max_f32 v111, v116, v117
mva 22,42,164
mav 14,168,46
R 42,51200
v_permlane16_swap_b32 v110, v111
mav 18,172,46
v_max_f32 v116, v110, v111
v_mul_f32 v116, s65, v116
mva 22,46,176
v_fma_f32 v114, v114, s65, -v116
s_waitcnt lgkmcnt(2)
mav 14,180,50
R 46,52224
v_fma_f32 v115, v115, s65, -v116
mav 18,184,50
v_exp_f32 v114, v114
mva 22,50,188
v_exp_f32 v115, v115
mav 14,192,54
R 50,53248
v_add_f32 v116, v114, v115
mav 18,196,54
v_add_f32 v117, v114, v115
mva 22,54,200
s_waitcnt lgkmcnt(2)
mav 14,204,38
R 54,54272
v_permlane32_swap_b32 v116, v117
mav 18,208,38
v_add_f32 v110, v116, v117
v_add_f32 v111, v116, v117
mva 22,38,212
mav 14,216,42
R 38,55296
v_permlane16_swap_b32 v110, v111
mav 18,220,42
v_add_f32 v116, v110, v111
mva 22,42,224
s_waitcnt lgkmcnt(2)
mav 14,228,46
R 42,56320
mav 18,232,46
v_rcp_f32 v116, v116
mva 22,46,236
v_mul_f32 v114, v114, v116
v_mul_f32 v115, v115, v116
mav 14,240,50
R 46,57344
v_cvt_pk_bf16_f32 v114, v114, v114
mav 18,244,50
v_cvt_pk_bf16_f32 v115, v115, v115
v_and_b32 v110, v114, v92
mva 22,50,248
v_and_b32 v111, v115, v93
s_waitcnt lgkmcnt(2)
mav 14,252,54
R 50,58368
v_and_b32 v112, v114, v94
mvv 18,128,54
v_and_b32 v113, v115, v95
mvv 22,54,132
mvv 14,136,38
R 54,59392
v_mfma_f32_16x16x32_bf16 v[70:73], v[8:11], v[110:113], 0
v_mfma_f32_16x16x32_bf16 v[74:77], v[104:107], v[110:113], 0
mvv 18,140,38
mvv 22,38,144
s_waitcnt lgkmcnt(2)
mvv 14,148,42
R 38,60416
mvv 18,152,42
mvv 22,42,156
v_cvt_pk_bf16_f32 v110, v70, v71
v_cvt_pk_bf16_f32 v111, v72, v73
mvv 14,160,46
R 42,61440
v_cvt_pk_bf16_f32 v112, v74, v75
mvv 18,164,46
v_cvt_pk_bf16_f32 v113, v76, v77
buffer_store_dwordx4 v[110:113], v81, s[36:39], s49 offen sc1
mvv 22,46,168
s_add_u32 s49, s49, 346112
s_waitcnt lgkmcnt(2)
mvv 14,172,50
R 46,62464
mvv 18,176,50
mvv 22,50,180
mvv 14,184,54
R 50,63488
mvv 18,188,54
mvv 22,54,192
s_waitcnt lgkmcnt(2)
mvv 14,196,38
R 54,64512
mvv 18,200,38
mvv 22,38,204
mvv 14,208,42
R 38,0
mvv 18,212,42
ds_read_b64 v[120:121], v82 offset:0
ds_read_b128 v[70:73], v83 offset:0
mvv 22,42,216
ds_read_b128 v[74:77], v83 offset:16
s_waitcnt lgkmcnt(5)
mvv 14,220,46
R 42,1024
mvv 18,224,46
mvv 22,46,228
mvv 14,232,50
R 46,2048
mvv 18,236,50
mvv 22,50,240
s_waitcnt lgkmcnt(2)
mvv 14,244,54
R 50,3072
mvv 18,248,54
mvv 22,54,252
v_mfma_f32_16x16x32_bf16 v[26:29], a[0:3], v[38:41], 0
R 54,4096
v_mul_f32 v71, v71, v118
v_mfma_f32_16x16x32_bf16 v[30:33], a[4:7], v[38:41], 0
v_mul_f32 v73, v73, v118
v_mul_f32 v75, v75, v118
v_mfma_f32_16x16x32_bf16 v[34:37], v[38:41], a[8:11], 0
v_mul_f32 v77, v77, v118
s_waitcnt lgkmcnt(2)
mav 26,12,42
R 58,5120
v_fma_f32 v22, v22, v70, v71
mav 30,16,42
v_fma_f32 v23, v23, v72, v73
v_fma_f32 v24, v24, v74, v75
mva 34,42,20
v_fma_f32 v25, v25, v76, v77
v_cvt_pk_bf16_f32 v8, v22, v23
mav 26,24,46
R 38,6144
v_cvt_pk_bf16_f32 v9, v24, v25
mav 30,28,46
v_mul_f32 v122, v121, v62
v_mfma_f32_16x16x32_bf16 v[70:73], v[100:103], v[0:3], v[84:87]
v_fma_f32 v14, v14, v120, v122
v_mul_f32 v123, v121, v63
mva 34,46,32
v_fma_f32 v15, v15, v120, v123
s_waitcnt lgkmcnt(2)
mav 26,36,50
R 42,7168
v_mul_f32 v122, v121, v64
mav 30,40,50
v_fma_f32 v16, v16, v120, v122
v_mul_f32 v123, v121, v65
mva 34,50,44
v_fma_f32 v17, v17, v120, v123
v_mul_f32 v122, v121, v66
mav 26,48,54
R 46,8192
v_fma_f32 v18, v18, v120, v122
mav 30,52,54
v_mul_f32 v123, v121, v67
v_fma_f32 v19, v19, v120, v123
mva 34,54,56
v_mul_f32 v122, v121, v68
s_waitcnt lgkmcnt(2)
mav 26,60,58
R 50,9216
v_fma_f32 v20, v20, v120, v122
mav 30,64,58
v_mul_f32 v123, v121, v69
v_fma_f32 v21, v21, v120, v123
mva 34,58,68
mav 26,72,38
R 54,10240
mav 30,76,38
mva 34,38,80
s_waitcnt lgkmcnt(2)
mav 26,84,42
R 58,11264
mav 30,88,42
mva 34,42,92
mav 26,96,46
R 38,12288
v_add_u32 v78, 1, v78
mav 30,100,46
s_barrier
ds_read_b128 v[96:99], v81 offset:0
mva 34,46,104
ds_read_b64 v[104:105], v81 offset:1024
s_waitcnt lgkmcnt(4)
mav 26,108,50
R 42,13312
mav 30,112,50
mva 34,50,116
mav 26,120,54
R 46,14336
mav 30,124,54
s_waitcnt vmcnt(0)
buffer_store_dwordx2 v[78:79], off, s[36:39], s52 sc1
mva 34,54,128
s_waitcnt lgkmcnt(3)
v_mfma_f32_16x16x32_bf16 v[74:77], v[96:99], v[0:3], v[88:91]
v_cvt_pk_bf16_f32 v0, v14, v15
v_cvt_pk_bf16_f32 v1, v16, v17
mav 26,132,58
R 50,15360
v_cvt_pk_bf16_f32 v2, v18, v19
mav 30,136,58
v_cvt_pk_bf16_f32 v3, v20, v21
mva 34,58,140
v_max3_f32 v114, v70, v71, v74
v_max3_f32 v115, v72, v73, v76
mav 26,144,38
R 54,16384
v_max_f32 v114, v114, v75
mav 30,148,38
v_max_f32 v115, v115, v77
v_max_f32 v116, v114, v115
mva 34,38,152
v_max_f32 v117, v114, v115
s_waitcnt lgkmcnt(2)
mav 26,156,42
R 38,17408
v_permlane32_swap_b32 v116, v117
mav 30,160,42
v_max_f32 v110, v116, v117
v_max_f32 v111, v116, v117
mva 34,42,164
mav 26,168,46
R 42,18432
v_permlane16_swap_b32 v110, v111
mav 30,172,46
v_max_f32 v116, v110, v111
v_mul_f32 v116, s65, v116
mva 34,46,176
v_fma_f32 v114, v114, s65, -v116
s_waitcnt lgkmcnt(2)
mav 26,180,50
R 46,19456
v_fma_f32 v115, v115, s65, -v116
mav 30,184,50
v_exp_f32 v114, v114
mva 34,50,188
v_exp_f32 v115, v115
mav 26,192,54
R 50,20480
v_add_f32 v116, v114, v115
mav 30,196,54
v_add_f32 v117, v114, v115
mva 34,54,200
s_waitcnt lgkmcnt(2)
mav 26,204,38
R 54,21504
v_permlane32_swap_b32 v116, v117
mav 30,208,38
v_add_f32 v110, v116, v117
v_add_f32 v111, v116, v117
mva 34,38,212
mav 26,216,42
R 38,22528
v_permlane16_swap_b32 v110, v111
mav 30,220,42
v_add_f32 v116, v110, v111
mva 34,42,224
s_waitcnt lgkmcnt(2)
mav 26,228,46
R 42,23552
mav 30,232,46
v_rcp_f32 v116, v116
mva 34,46,236
v_mul_f32 v114, v114, v116
v_mul_f32 v115, v115, v116
mav 26,240,50
R 46,24576
v_cvt_pk_bf16_f32 v114, v114, v114
mav 30,244,50
v_cvt_pk_bf16_f32 v115, v115, v115
v_and_b32 v110, v114, v94
mva 34,50,248
v_and_b32 v111, v115, v95
s_waitcnt lgkmcnt(2)
mav 26,252,54
R 50,25600
v_and_b32 v112, v114, v92
mvv 30,128,54
v_and_b32 v113, v115, v93
mvv 34,54,132
mvv 26,136,38
R 54,26624
v_mfma_f32_16x16x32_bf16 v[70:73], v[8:11], v[110:113], 0
v_mfma_f32_16x16x32_bf16 v[74:77], v[104:107], v[110:113], 0
mvv 30,140,38
mvv 34,38,144
s_waitcnt lgkmcnt(2)
mvv 26,148,42
R 38,27648
mvv 30,152,42
mvv 34,42,156
v_cvt_pk_bf16_f32 v110, v70, v71
v_cvt_pk_bf16_f32 v111, v72, v73
mvv 26,160,46
R 42,28672
v_cvt_pk_bf16_f32 v112, v74, v75
mvv 30,164,46
v_cvt_pk_bf16_f32 v113, v76, v77
buffer_store_dwordx4 v[110:113], v81, s[36:39], s49 offen sc1
mvv 34,46,168
s_add_u32 s49, s49, 346112
s_waitcnt lgkmcnt(2)
mvv 26,172,50
R 46,29696
mvv 30,176,50
mvv 34,50,180
mvv 26,184,54
R 50,30720
mvv 30,188,54
mvv 34,54,192
s_waitcnt lgkmcnt(2)
mvv 26,196,38
R 54,31744
v_xor_b32 v80, 0x10000, v80
mvv 30,200,38
mvv 34,38,204
mvv 26,208,42
R 38,32768
mvv 30,212,42
ds_read_b64 v[120:121], v82 offset:128
ds_read_b128 v[70:73], v83 offset:128
mvv 34,42,216
ds_read_b128 v[74:77], v83 offset:144
s_waitcnt lgkmcnt(5)
mvv 26,220,46
R 42,33792
mvv 30,224,46
mvv 34,46,228
mvv 26,232,50
R 46,34816
mvv 30,236,50
mvv 34,50,240
s_waitcnt lgkmcnt(2)
mvv 26,244,54
R 50,35840
mvv 30,248,54
mvv 34,54,252
s_add_u32 s60, s60, 1
s_cbranch_scc0 LQA_1
v_mfma_f32_16x16x32_bf16 v[14:17], a[0:3], v[38:41], 0
R 54,36864
v_mul_f32 v71, v71, v118
v_mfma_f32_16x16x32_bf16 v[18:21], a[4:7], v[38:41], 0
v_mul_f32 v73, v73, v118
v_mul_f32 v75, v75, v118
v_mfma_f32_16x16x32_bf16 v[22:25], v[38:41], a[8:11], 0
v_mul_f32 v77, v77, v118
s_waitcnt lgkmcnt(2)
mav 14,12,42
R 58,37888
v_fma_f32 v34, v34, v70, v71
mav 18,16,42
v_fma_f32 v35, v35, v72, v73
v_fma_f32 v36, v36, v74, v75
mva 22,42,20
v_fma_f32 v37, v37, v76, v77
v_cvt_pk_bf16_f32 v10, v34, v35
mav 14,24,46
R 38,38912
v_cvt_pk_bf16_f32 v11, v36, v37
mav 18,28,46
v_mul_f32 v122, v121, v62
v_mfma_f32_16x16x32_bf16 v[70:73], v[96:99], v[0:3], v[84:87]
v_fma_f32 v26, v26, v120, v122
v_mul_f32 v123, v121, v63
mva 22,46,32
v_fma_f32 v27, v27, v120, v123
s_waitcnt lgkmcnt(2)
mav 14,36,50
R 42,39936
v_mul_f32 v122, v121, v64
mav 18,40,50
v_fma_f32 v28, v28, v120, v122
v_mul_f32 v123, v121, v65
mva 22,50,44
v_fma_f32 v29, v29, v120, v123
v_mul_f32 v122, v121, v66
mav 14,48,54
R 46,40960
v_fma_f32 v30, v30, v120, v122
mav 18,52,54
v_mul_f32 v123, v121, v67
v_fma_f32 v31, v31, v120, v123
mva 22,54,56
v_mul_f32 v122, v121, v68
s_waitcnt lgkmcnt(2)
mav 14,60,58
R 50,41984
v_fma_f32 v32, v32, v120, v122
mav 18,64,58
v_mul_f32 v123, v121, v69
v_fma_f32 v33, v33, v120, v123
mva 22,58,68
mav 14,72,38
R 54,43008
mav 18,76,38
mva 22,38,80
s_waitcnt lgkmcnt(2)
mav 14,84,42
R 58,44032
mav 18,88,42
mva 22,42,92
mav 14,96,46
R 38,45056
v_add_u32 v78, 1, v78
mav 18,100,46
s_barrier
ds_read_b128 v[100:103], v81 offset:4096
mva 22,46,104
ds_read_b64 v[106:107], v81 offset:5120
s_waitcnt lgkmcnt(4)
mav 14,108,50
R 42,46080
mav 18,112,50
mva 22,50,116
mav 14,120,54
R 46,47104
mav 18,124,54
s_waitcnt vmcnt(0)
buffer_store_dwordx2 v[78:79], off, s[36:39], s52 sc1
mva 22,54,128
s_waitcnt lgkmcnt(3)
v_mfma_f32_16x16x32_bf16 v[74:77], v[100:103], v[0:3], v[88:91]
v_cvt_pk_bf16_f32 v0, v26, v27
v_cvt_pk_bf16_f32 v1, v28, v29
mav 14,132,58
R 50,48128
v_cvt_pk_bf16_f32 v2, v30, v31
mav 18,136,58
v_cvt_pk_bf16_f32 v3, v32, v33
mva 22,58,140
v_max3_f32 v114, v70, v71, v74
v_max3_f32 v115, v72, v73, v76
mav 14,144,38
R 54,49152
v_max_f32 v114, v114, v75
mav 18,148,38
v_max_f32 v115, v115, v77
v_max_f32 v116, v114, v115
mva 22,38,152
v_max_f32 v117, v114, v115
s_waitcnt lgkmcnt(2)
mav 14,156,42
R 38,50176
v_permlane32_swap_b32 v116, v117
mav 18,160,42
v_max_f32 v110, v116, v117
v_max_f32 v111, v116, v117
mva 22,42,164
mav 14,168,46
R 42,51200
v_permlane16_swap_b32 v110, v111
mav 18,172,46
v_max_f32 v116, v110, v111
v_mul_f32 v116, s65, v116
mva 22,46,176
v_fma_f32 v114, v114, s65, -v116
s_waitcnt lgkmcnt(2)
mav 14,180,50
R 46,52224
v_fma_f32 v115, v115, s65, -v116
mav 18,184,50
v_exp_f32 v114, v114
mva 22,50,188
v_exp_f32 v115, v115
mav 14,192,54
R 50,53248
v_add_f32 v116, v114, v115
mav 18,196,54
v_add_f32 v117, v114, v115
mva 22,54,200
s_waitcnt lgkmcnt(2)
mav 14,204,38
R 54,54272
v_permlane32_swap_b32 v116, v117
mav 18,208,38
v_add_f32 v110, v116, v117
v_add_f32 v111, v116, v117
mva 22,38,212
mav 14,216,42
R 38,55296
v_permlane16_swap_b32 v110, v111
mav 18,220,42
v_add_f32 v116, v110, v111
mva 22,42,224
s_waitcnt lgkmcnt(2)
mav 14,228,46
R 42,56320
mav 18,232,46
v_rcp_f32 v116, v116
mva 22,46,236
v_mul_f32 v114, v114, v116
v_mul_f32 v115, v115, v116
mav 14,240,50
R 46,57344
v_cvt_pk_bf16_f32 v114, v114, v114
mav 18,244,50
v_cvt_pk_bf16_f32 v115, v115, v115
v_and_b32 v110, v114, v92
mva 22,50,248
v_and_b32 v111, v115, v93
s_waitcnt lgkmcnt(2)
mav 14,252,54
R 50,58368
v_and_b32 v112, v114, v94
mvv 18,128,54
v_and_b32 v113, v115, v95
mvv 22,54,132
mvv 14,136,38
R 54,59392
v_mfma_f32_16x16x32_bf16 v[70:73], v[8:11], v[110:113], 0
v_mfma_f32_16x16x32_bf16 v[74:77], v[104:107], v[110:113], 0
mvv 18,140,38
mvv 22,38,144
s_waitcnt lgkmcnt(2)
mvv 14,148,42
R 38,60416
mvv 18,152,42
mvv 22,42,156
v_cvt_pk_bf16_f32 v110, v70, v71
v_cvt_pk_bf16_f32 v111, v72, v73
mvv 14,160,46
R 42,61440
v_cvt_pk_bf16_f32 v112, v74, v75
mvv 18,164,46
v_cvt_pk_bf16_f32 v113, v76, v77
buffer_store_dwordx4 v[110:113], v81, s[36:39], s49 offen sc1
mvv 22,46,168
s_add_u32 s49, s49, 346112
s_waitcnt lgkmcnt(2)
mvv 14,172,50
R 46,62464
mvv 18,176,50
mvv 22,50,180
mvv 14,184,54
R 50,63488
mvv 18,188,54
mvv 22,54,192
s_waitcnt lgkmcnt(2)
mvv 14,196,38
R 54,64512
mvv 18,200,38
mvv 22,38,204
mvv 14,208,42
R 38,0
mvv 18,212,42
ds_read_b64 v[120:121], v82 offset:0
ds_read_b128 v[70:73], v83 offset:0
mvv 22,42,216
ds_read_b128 v[74:77], v83 offset:16
s_waitcnt lgkmcnt(5)
mvv 14,220,46
R 42,1024
mvv 18,224,46
mvv 22,46,228
mvv 14,232,50
R 46,2048
mvv 18,236,50
mvv 22,50,240
s_waitcnt lgkmcnt(2)
mvv 14,244,54
R 50,3072
mvv 18,248,54
mvv 22,54,252
v_mfma_f32_16x16x32_bf16 v[26:29], a[0:3], v[38:41], 0
R 54,4096
v_mul_f32 v71, v71, v118
v_mfma_f32_16x16x32_bf16 v[30:33], a[4:7], v[38:41], 0
v_mul_f32 v73, v73, v118
v_mul_f32 v75, v75, v118
v_mfma_f32_16x16x32_bf16 v[34:37], v[38:41], a[8:11], 0
v_mul_f32 v77, v77, v118
s_waitcnt lgkmcnt(2)
mav 26,12,42
R 58,5120
v_fma_f32 v22, v22, v70, v71
mav 30,16,42
v_fma_f32 v23, v23, v72, v73
v_fma_f32 v24, v24, v74, v75
mva 34,42,20
v_fma_f32 v25, v25, v76, v77
v_cvt_pk_bf16_f32 v8, v22, v23
mav 26,24,46
R 38,6144
v_cvt_pk_bf16_f32 v9, v24, v25
mav 30,28,46
v_mul_f32 v122, v121, v62
v_mfma_f32_16x16x32_bf16 v[70:73], v[100:103], v[0:3], v[84:87]
v_fma_f32 v14, v14, v120, v122
v_mul_f32 v123, v121, v63
mva 34,46,32
v_fma_f32 v15, v15, v120, v123
s_waitcnt lgkmcnt(2)
mav 26,36,50
R 42,7168
v_mul_f32 v122, v121, v64
mav 30,40,50
buffer_load_dwordx4 v[4:7], v81, s[36:39], s55 offen sc1
buffer_load_dwordx2 v[12:13], v81, s[36:39], s55 offen offset:1024 sc1
mva 34,50,44
buffer_load_dwordx2 v[108:109], v81, s[36:39], s55 offen offset:1032 sc1
v_fma_f32 v16, v16, v120, v122
mav 26,48,54
R 46,8192
v_mul_f32 v123, v121, v65
mav 30,52,54
v_fma_f32 v17, v17, v120, v123
v_mul_f32 v122, v121, v66
mva 34,54,56
v_fma_f32 v18, v18, v120, v122
s_waitcnt lgkmcnt(2)
mav 26,60,58
R 50,9216
v_mul_f32 v123, v121, v67
mav 30,64,58
v_fma_f32 v19, v19, v120, v123
v_mul_f32 v122, v121, v68
mva 34,58,68
v_fma_f32 v20, v20, v120, v122
v_mul_f32 v123, v121, v69
mav 26,72,38
R 54,10240
v_fma_f32 v21, v21, v120, v123
mav 30,76,38
mva 34,38,80
s_waitcnt lgkmcnt(2)
)"
R"(
mav 26,84,42
R 58,11264
mav 30,88,42
mva 34,42,92
mav 26,96,46
R 38,12288
v_add_u32 v78, 1, v78
mav 30,100,46
s_barrier
ds_read_b128 v[96:99], v81 offset:0
mva 34,46,104
ds_read_b64 v[104:105], v81 offset:1024
s_waitcnt lgkmcnt(4)
mav 26,108,50
R 42,13312
mav 30,112,50
mva 34,50,116
mav 26,120,54
R 46,14336
mav 30,124,54
s_waitcnt vmcnt(3)
buffer_store_dwordx2 v[78:79], off, s[36:39], s52 sc1
mva 34,54,128
s_waitcnt lgkmcnt(3)
v_mfma_f32_16x16x32_bf16 v[74:77], v[96:99], v[0:3], v[88:91]
v_cvt_pk_bf16_f32 v0, v14, v15
v_cvt_pk_bf16_f32 v1, v16, v17
mav 26,132,58
R 50,15360
v_cvt_pk_bf16_f32 v2, v18, v19
mav 30,136,58
v_cvt_pk_bf16_f32 v3, v20, v21
mva 34,58,140
v_max3_f32 v114, v70, v71, v74
v_max3_f32 v115, v72, v73, v76
mav 26,144,38
R 54,16384
v_max_f32 v114, v114, v75
mav 30,148,38
v_max_f32 v115, v115, v77
v_max_f32 v116, v114, v115
mva 34,38,152
v_max_f32 v117, v114, v115
s_waitcnt lgkmcnt(2)
mav 26,156,42
R 38,17408
v_permlane32_swap_b32 v116, v117
mav 30,160,42
v_max_f32 v110, v116, v117
v_max_f32 v111, v116, v117
mva 34,42,164
mav 26,168,46
R 42,18432
v_permlane16_swap_b32 v110, v111
mav 30,172,46
v_max_f32 v116, v110, v111
v_mul_f32 v116, s65, v116
mva 34,46,176
v_fma_f32 v114, v114, s65, -v116
s_waitcnt lgkmcnt(2)
mav 26,180,50
R 46,19456
v_fma_f32 v115, v115, s65, -v116
mav 30,184,50
v_exp_f32 v114, v114
mva 34,50,188
v_exp_f32 v115, v115
mav 26,192,54
R 50,20480
v_add_f32 v116, v114, v115
mav 30,196,54
v_add_f32 v117, v114, v115
mva 34,54,200
s_waitcnt lgkmcnt(2)
mav 26,204,38
R 54,21504
v_permlane32_swap_b32 v116, v117
mav 30,208,38
v_add_f32 v110, v116, v117
v_add_f32 v111, v116, v117
mva 34,38,212
mav 26,216,42
R 38,22528
v_permlane16_swap_b32 v110, v111
mav 30,220,42
v_add_f32 v116, v110, v111
mva 34,42,224
s_waitcnt lgkmcnt(2)
mav 26,228,46
R 42,23552
mav 30,232,46
v_rcp_f32 v116, v116
mva 34,46,236
v_mul_f32 v114, v114, v116
v_mul_f32 v115, v115, v116
mav 26,240,50
R 46,24576
v_cvt_pk_bf16_f32 v114, v114, v114
mav 30,244,50
v_cvt_pk_bf16_f32 v115, v115, v115
v_and_b32 v110, v114, v94
mva 34,50,248
v_and_b32 v111, v115, v95
s_waitcnt lgkmcnt(2)
mav 26,252,54
R 50,25600
v_and_b32 v112, v114, v92
mvv 30,128,54
v_and_b32 v113, v115, v93
mvv 34,54,132
mvv 26,136,38
R 54,26624
v_mfma_f32_16x16x32_bf16 v[70:73], v[8:11], v[110:113], 0
v_mfma_f32_16x16x32_bf16 v[74:77], v[104:107], v[110:113], 0
mvv 30,140,38
mvv 34,38,144
s_waitcnt lgkmcnt(2)
mvv 26,148,42
R 38,27648
mvv 30,152,42
mvv 34,42,156
v_cvt_pk_bf16_f32 v110, v70, v71
v_cvt_pk_bf16_f32 v111, v72, v73
mvv 26,160,46
R 42,28672
v_cvt_pk_bf16_f32 v112, v74, v75
mvv 30,164,46
v_cvt_pk_bf16_f32 v113, v76, v77
buffer_store_dwordx4 v[110:113], v81, s[36:39], s49 offen sc1
mvv 34,46,168
s_add_u32 s49, s49, 346112
s_waitcnt lgkmcnt(2)
mvv 26,172,50
R 46,29696
mvv 30,176,50
mvv 34,50,180
mvv 26,184,54
R 50,30720
mvv 30,188,54
mvv 34,54,192
s_waitcnt lgkmcnt(2)
mvv 26,196,38
R 54,31744
v_xor_b32 v80, 0x10000, v80
mvv 30,200,38
mvv 34,38,204
mvv 26,208,42
R 38,32768
mvv 30,212,42
ds_read_b64 v[120:121], v82 offset:128
ds_read_b128 v[70:73], v83 offset:128
mvv 34,42,216
ds_read_b128 v[74:77], v83 offset:144
s_waitcnt lgkmcnt(5)
mvv 26,220,46
R 42,33792
mvv 30,224,46
mvv 34,46,228
mvv 26,232,50
R 46,34816
mvv 30,236,50
mvv 34,50,240
s_waitcnt lgkmcnt(2)
mvv 26,244,54
R 50,35840
mvv 30,248,54
mvv 34,54,252
v_mfma_f32_16x16x32_bf16 v[14:17], a[0:3], v[38:41], 0
R 54,36864
v_mul_f32 v71, v71, v118
v_mfma_f32_16x16x32_bf16 v[18:21], a[4:7], v[38:41], 0
v_mul_f32 v73, v73, v118
v_mul_f32 v75, v75, v118
v_mfma_f32_16x16x32_bf16 v[22:25], v[38:41], a[8:11], 0
v_mul_f32 v77, v77, v118
s_waitcnt lgkmcnt(2)
mav 14,12,42
R 58,37888
v_fma_f32 v34, v34, v70, v71
mav 18,16,42
v_fma_f32 v35, v35, v72, v73
v_fma_f32 v36, v36, v74, v75
mva 22,42,20
v_fma_f32 v37, v37, v76, v77
v_cvt_pk_bf16_f32 v10, v34, v35
mav 14,24,46
R 38,38912
v_cvt_pk_bf16_f32 v11, v36, v37
mav 18,28,46
v_mul_f32 v122, v121, v62
v_mfma_f32_16x16x32_bf16 v[70:73], v[96:99], v[0:3], v[84:87]
v_fma_f32 v26, v26, v120, v122
v_mul_f32 v123, v121, v63
mva 22,46,32
v_fma_f32 v27, v27, v120, v123
s_waitcnt lgkmcnt(2)
mav 14,36,50
R 42,39936
v_mul_f32 v122, v121, v64
mav 18,40,50
v_fma_f32 v28, v28, v120, v122
v_mul_f32 v123, v121, v65
mva 22,50,44
v_fma_f32 v29, v29, v120, v123
v_mul_f32 v122, v121, v66
mav 14,48,54
R 46,40960
v_fma_f32 v30, v30, v120, v122
mav 18,52,54
v_mul_f32 v123, v121, v67
v_fma_f32 v31, v31, v120, v123
mva 22,54,56
v_mul_f32 v122, v121, v68
s_waitcnt lgkmcnt(2)
mav 14,60,58
R 50,41984
v_fma_f32 v32, v32, v120, v122
mav 18,64,58
v_mul_f32 v123, v121, v69
v_fma_f32 v33, v33, v120, v123
mva 22,58,68
mav 14,72,38
R 54,43008
mav 18,76,38
mva 22,38,80
s_waitcnt lgkmcnt(2)
mav 14,84,42
R 58,44032
mav 18,88,42
mva 22,42,92
mav 14,96,46
R 38,45056
v_add_u32 v78, 1, v78
mav 18,100,46
s_barrier
ds_read_b128 v[100:103], v81 offset:4096
mva 22,46,104
ds_read_b64 v[106:107], v81 offset:5120
s_waitcnt lgkmcnt(4)
mav 14,108,50
R 42,46080
mav 18,112,50
mva 22,50,116
mav 14,120,54
R 46,47104
mav 18,124,54
s_waitcnt vmcnt(0)
buffer_store_dwordx2 v[78:79], off, s[36:39], s52 sc1
mva 22,54,128
s_waitcnt lgkmcnt(3)
v_mfma_f32_16x16x32_bf16 v[74:77], v[100:103], v[0:3], v[88:91]
v_cvt_pk_bf16_f32 v0, v26, v27
v_cvt_pk_bf16_f32 v1, v28, v29
mav 14,132,58
R 50,48128
v_cvt_pk_bf16_f32 v2, v30, v31
mav 18,136,58
v_cvt_pk_bf16_f32 v3, v32, v33
mva 22,58,140
v_max3_f32 v114, v70, v71, v74
v_max3_f32 v115, v72, v73, v76
mav 14,144,38
R 54,49152
v_max_f32 v114, v114, v75
mav 18,148,38
v_max_f32 v115, v115, v77
v_max_f32 v116, v114, v115
mva 22,38,152
v_max_f32 v117, v114, v115
s_waitcnt lgkmcnt(2)
mav 14,156,42
R 38,50176
v_permlane32_swap_b32 v116, v117
mav 18,160,42
v_max_f32 v110, v116, v117
v_max_f32 v111, v116, v117
mva 22,42,164
mav 14,168,46
R 42,51200
v_permlane16_swap_b32 v110, v111
mav 18,172,46
v_max_f32 v116, v110, v111
v_mul_f32 v116, s65, v116
mva 22,46,176
v_fma_f32 v114, v114, s65, -v116
s_waitcnt lgkmcnt(2)
mav 14,180,50
R 46,52224
v_fma_f32 v115, v115, s65, -v116
mav 18,184,50
v_exp_f32 v114, v114
mva 22,50,188
v_exp_f32 v115, v115
mav 14,192,54
R 50,53248
v_add_f32 v116, v114, v115
mav 18,196,54
v_add_f32 v117, v114, v115
mva 22,54,200
s_waitcnt lgkmcnt(2)
mav 14,204,38
R 54,54272
v_permlane32_swap_b32 v116, v117
mav 18,208,38
v_add_f32 v110, v116, v117
v_add_f32 v111, v116, v117
mva 22,38,212
mav 14,216,42
R 38,55296
v_permlane16_swap_b32 v110, v111
mav 18,220,42
v_add_f32 v116, v110, v111
mva 22,42,224
s_waitcnt lgkmcnt(2)
mav 14,228,46
R 42,56320
mav 18,232,46
v_rcp_f32 v116, v116
mva 22,46,236
v_mul_f32 v114, v114, v116
v_mul_f32 v115, v115, v116
mav 14,240,50
R 46,57344
v_cvt_pk_bf16_f32 v114, v114, v114
mav 18,244,50
v_cvt_pk_bf16_f32 v115, v115, v115
v_and_b32 v110, v114, v92
mva 22,50,248
v_and_b32 v111, v115, v93
s_waitcnt lgkmcnt(2)
mav 14,252,54
R 50,58368
v_and_b32 v112, v114, v94
mvv 18,128,54
v_and_b32 v113, v115, v95
mvv 22,54,132
mvv 14,136,38
R 54,59392
v_mfma_f32_16x16x32_bf16 v[70:73], v[8:11], v[110:113], 0
v_mfma_f32_16x16x32_bf16 v[74:77], v[104:107], v[110:113], 0
mvv 18,140,38
mvv 22,38,144
s_waitcnt lgkmcnt(2)
mvv 14,148,42
R 38,60416
mvv 18,152,42
mvv 22,42,156
v_cvt_pk_bf16_f32 v110, v70, v71
v_cvt_pk_bf16_f32 v111, v72, v73
mvv 14,160,46
R 42,61440
v_cvt_pk_bf16_f32 v112, v74, v75
mvv 18,164,46
v_cvt_pk_bf16_f32 v113, v76, v77
buffer_store_dwordx4 v[110:113], v81, s[36:39], s49 offen sc1
mvv 22,46,168
s_add_u32 s49, s49, 346112
s_waitcnt lgkmcnt(2)
mvv 14,172,50
R 46,62464
mvv 18,176,50
mvv 22,50,180
mvv 14,184,54
R 50,63488
mvv 18,188,54
mvv 22,54,192
s_waitcnt lgkmcnt(2)
mvv 14,196,38
R 54,64512
mvv 18,200,38
mvv 22,38,204
mvv 14,208,42
R 38,0
mvv 18,212,42
ds_read_b64 v[120:121], v82 offset:0
ds_read_b128 v[70:73], v83 offset:0
mvv 22,42,216
ds_read_b128 v[74:77], v83 offset:16
s_waitcnt lgkmcnt(5)
mvv 14,220,46
R 42,1024
mvv 18,224,46
mvv 22,46,228
mvv 14,232,50
R 46,2048
mvv 18,236,50
mvv 22,50,240
s_waitcnt lgkmcnt(2)
mvv 14,244,54
R 50,3072
mvv 18,248,54
mvv 22,54,252
v_mfma_f32_16x16x32_bf16 v[26:29], a[0:3], v[38:41], 0
R 54,4096
v_mul_f32 v71, v71, v118
v_mfma_f32_16x16x32_bf16 v[30:33], a[4:7], v[38:41], 0
v_mul_f32 v73, v73, v118
v_mul_f32 v75, v75, v118
v_mfma_f32_16x16x32_bf16 v[34:37], v[38:41], a[8:11], 0
v_mul_f32 v77, v77, v118
s_waitcnt lgkmcnt(2)
mav 26,12,42
R 58,5120
v_fma_f32 v22, v22, v70, v71
mav 30,16,42
v_fma_f32 v23, v23, v72, v73
v_fma_f32 v24, v24, v74, v75
mva 34,42,20
v_fma_f32 v25, v25, v76, v77
v_cvt_pk_bf16_f32 v8, v22, v23
mav 26,24,46
R 38,6144
v_cvt_pk_bf16_f32 v9, v24, v25
mav 30,28,46
v_min_f32 v74, s56, v88
v_mfma_f32_16x16x32_bf16 v[70:73], v[100:103], v[0:3], v[84:87]
v_min_f32 v75, s56, v89
v_min_f32 v76, s56, v90
mva 34,46,32
v_min_f32 v77, s56, v91
s_waitcnt lgkmcnt(2)
mav 26,36,50
R 42,7168
mvv 74,4,0
v_mul_f32 v122, v121, v62
v_fma_f32 v14, v14, v120, v122
mav 30,40,50
v_mul_f32 v123, v121, v63
v_fma_f32 v15, v15, v120, v123
mva 34,50,44
v_mul_f32 v122, v121, v64
v_fma_f32 v16, v16, v120, v122
mav 26,48,54
R 46,8192
v_max3_f32 v114, v70, v71, v74
mav 30,52,54
v_max3_f32 v115, v72, v73, v76
v_max_f32 v114, v114, v75
mva 34,54,56
v_max_f32 v115, v115, v77
s_waitcnt lgkmcnt(2)
mav 26,60,58
R 50,9216
v_max_f32 v116, v114, v115
mav 30,64,58
v_max_f32 v117, v114, v115
v_mul_f32 v123, v121, v65
mva 34,58,68
v_permlane32_swap_b32 v116, v117
v_max_f32 v110, v116, v117
mav 26,72,38
R 54,10240
v_max_f32 v111, v116, v117
mav 30,76,38
v_fma_f32 v17, v17, v120, v123
v_permlane16_swap_b32 v110, v111
mva 34,38,80
v_max_f32 v116, v110, v111
s_waitcnt lgkmcnt(2)
mav 26,84,42
R 58,11264
v_mul_f32 v116, s65, v116
mav 30,88,42
v_fma_f32 v114, v114, s65, -v116
v_fma_f32 v115, v115, s65, -v116
mva 34,42,92
v_exp_f32 v114, v114
mav 26,96,46
R 38,12288
v_mul_f32 v122, v121, v66
mav 30,100,46
s_barrier
ds_read_b128 v[96:99], v81 offset:0
mva 34,46,104
ds_read_b64 v[104:105], v81 offset:1024
s_waitcnt lgkmcnt(4)
mav 26,108,50
R 42,13312
v_fma_f32 v18, v18, v120, v122
mav 30,112,50
v_exp_f32 v115, v115
mva 34,50,116
v_add_f32 v116, v114, v115
v_add_f32 v117, v114, v115
mav 26,120,54
R 46,14336
v_permlane32_swap_b32 v116, v117
mav 30,124,54
v_add_f32 v110, v116, v117
v_add_f32 v111, v116, v117
mva 34,54,128
v_mul_f32 v123, v121, v67
s_waitcnt lgkmcnt(2)
mav 26,132,58
R 50,15360
v_permlane16_swap_b32 v110, v111
mav 30,136,58
v_add_f32 v116, v110, v111
v_fma_f32 v19, v19, v120, v123
mva 34,58,140
v_rcp_f32 v116, v116
mav 26,144,38
R 54,16384
v_mul_f32 v114, v114, v116
mav 30,148,38
v_mul_f32 v115, v115, v116
v_cvt_pk_bf16_f32 v114, v114, v114
mva 34,38,152
v_cvt_pk_bf16_f32 v115, v115, v115
s_waitcnt lgkmcnt(2)
mav 26,156,42
R 38,17408
v_and_b32 v110, v114, v92
mav 30,160,42
v_and_b32 v111, v115, v93
v_and_b32 v112, v114, v94
mva 34,42,164
v_and_b32 v113, v115, v95
buffer_store_dwordx4 v[96:99], v81, s[36:39], s54 offen offset:2048 sc1
mav 26,168,46
R 42,18432
v_mfma_f32_16x16x32_bf16 v[70:73], v[10:13], v[110:113], 0
v_mfma_f32_16x16x32_bf16 v[74:77], v[106:109], v[110:113], 0
buffer_store_dwordx2 v[8:9], v81, s[36:39], s54 offen offset:3072 sc1
buffer_store_dwordx2 v[104:105], v81, s[36:39], s54 offen offset:3080 sc1
mav 30,172,46
v_mul_f32 v122, v121, v68
v_fma_f32 v20, v20, v120, v122
mva 34,46,176
v_mul_f32 v123, v121, v69
s_waitcnt lgkmcnt(2)
mav 26,180,50
R 46,19456
v_cvt_pk_bf16_f32 v110, v70, v71
mav 30,184,50
v_cvt_pk_bf16_f32 v111, v72, v73
v_cvt_pk_bf16_f32 v112, v74, v75
mva 34,50,188
v_cvt_pk_bf16_f32 v113, v76, v77
buffer_store_dwordx4 v[110:113], v81, s[36:39], s49 offen sc1
mav 26,192,54
R 50,20480
s_add_u32 s49, s49, 346112
mav 30,196,54
v_fma_f32 v21, v21, v120, v123
v_cvt_pk_bf16_f32 v0, v14, v15
mva 34,54,200
v_cvt_pk_bf16_f32 v1, v16, v17
s_waitcnt lgkmcnt(2)
mav 26,204,38
R 54,21504
v_cvt_pk_bf16_f32 v2, v18, v19
mav 30,208,38
v_cvt_pk_bf16_f32 v3, v20, v21
v_add_u32 v78, 1, v78
mva 34,38,212
s_waitcnt vmcnt(4)
buffer_store_dwordx2 v[78:79], off, s[36:39], s52 sc1
mav 26,216,42
R 38,22528
mav 30,220,42
mva 34,42,224
s_waitcnt lgkmcnt(2)
mav 26,228,46
R 42,23552
mav 30,232,46
mva 34,46,236
mav 26,240,50
R 46,24576
mav 30,244,50
mva 34,50,248
s_waitcnt lgkmcnt(2)
mav 26,252,54
R 50,25600
mvv 30,128,54
mvv 34,54,132
mvv 26,136,38
R 54,26624
mvv 30,140,38
mvv 34,38,144
s_waitcnt lgkmcnt(2)
mvv 26,148,42
R 38,27648
mvv 30,152,42
mvv 34,42,156
mvv 26,160,46
R 42,28672
mvv 30,164,46
mvv 34,46,168
s_waitcnt lgkmcnt(2)
mvv 26,172,50
R 46,29696
mvv 30,176,50
mvv 34,50,180
mvv 26,184,54
R 50,30720
mvv 30,188,54
mvv 34,54,192
s_waitcnt lgkmcnt(2)
mvv 26,196,38
R 54,31744
v_xor_b32 v80, 0x10000, v80
mvv 30,200,38
mvv 34,38,204
mvv 26,208,42
R 38,32768
mvv 30,212,42
ds_read_b64 v[120:121], v82 offset:128
ds_read_b128 v[70:73], v83 offset:128
mvv 34,42,216
ds_read_b128 v[74:77], v83 offset:144
s_waitcnt lgkmcnt(5)
mvv 26,220,46
R 42,33792
mvv 30,224,46
mvv 34,46,228
mvv 26,232,50
R 46,34816
mvv 30,236,50
mvv 34,50,240
s_waitcnt lgkmcnt(2)
mvv 26,244,54
R 50,35840
mvv 30,248,54
mvv 34,54,252
v_mfma_f32_16x16x32_bf16 v[14:17], a[0:3], v[38:41], 0
R 54,36864
v_mul_f32 v71, v71, v118
v_mfma_f32_16x16x32_bf16 v[18:21], a[4:7], v[38:41], 0
v_mul_f32 v73, v73, v118
v_mul_f32 v75, v75, v118
v_mfma_f32_16x16x32_bf16 v[22:25], v[38:41], a[8:11], 0
v_mul_f32 v77, v77, v118
s_waitcnt lgkmcnt(2)
mav 14,12,42
R 58,37888
v_fma_f32 v34, v34, v70, v71
mav 18,16,42
v_fma_f32 v35, v35, v72, v73
v_fma_f32 v36, v36, v74, v75
mva 22,42,20
v_fma_f32 v37, v37, v76, v77
v_cvt_pk_bf16_f32 v10, v34, v35
mav 14,24,46
R 38,38912
v_cvt_pk_bf16_f32 v11, v36, v37
mav 18,28,46
v_mul_f32 v122, v121, v62
v_mfma_f32_16x16x32_bf16 v[70:73], v[96:99], v[0:3], v[84:87]
v_fma_f32 v26, v26, v120, v122
v_mul_f32 v123, v121, v63
mva 22,46,32
v_fma_f32 v27, v27, v120, v123
s_waitcnt lgkmcnt(2)
mav 14,36,50
R 42,39936
v_mul_f32 v122, v121, v64
mav 18,40,50
v_fma_f32 v28, v28, v120, v122
v_mul_f32 v123, v121, v65
mva 22,50,44
v_fma_f32 v29, v29, v120, v123
v_mul_f32 v122, v121, v66
mav 14,48,54
R 46,40960
v_fma_f32 v30, v30, v120, v122
mav 18,52,54
v_mul_f32 v123, v121, v67
v_fma_f32 v31, v31, v120, v123
mva 22,54,56
v_mul_f32 v122, v121, v68
s_waitcnt lgkmcnt(2)
mav 14,60,58
R 50,41984
v_fma_f32 v32, v32, v120, v122
mav 18,64,58
v_mul_f32 v123, v121, v69
v_fma_f32 v33, v33, v120, v123
mva 22,58,68
mav 14,72,38
R 54,43008
mav 18,76,38
mva 22,38,80
s_waitcnt lgkmcnt(2)
mav 14,84,42
R 58,44032
mav 18,88,42
mva 22,42,92
mav 14,96,46
R 38,45056
v_add_u32 v78, 1, v78
mav 18,100,46
s_barrier
ds_read_b128 v[100:103], v81 offset:4096
mva 22,46,104
ds_read_b64 v[106:107], v81 offset:5120
s_waitcnt lgkmcnt(4)
mav 14,108,50
R 42,46080
mav 18,112,50
mva 22,50,116
mav 14,120,54
R 46,47104
mav 18,124,54
s_waitcnt vmcnt(1)
buffer_store_dwordx2 v[78:79], off, s[36:39], s52 sc1
mva 22,54,128
s_waitcnt lgkmcnt(3)
v_mfma_f32_16x16x32_bf16 v[74:77], v[100:103], v[0:3], v[88:91]
v_cvt_pk_bf16_f32 v0, v26, v27
v_cvt_pk_bf16_f32 v1, v28, v29
mav 14,132,58
R 50,48128
v_cvt_pk_bf16_f32 v2, v30, v31
mav 18,136,58
v_cvt_pk_bf16_f32 v3, v32, v33
mva 22,58,140
v_max3_f32 v114, v70, v71, v74
v_max3_f32 v115, v72, v73, v76
mav 14,144,38
R 54,49152
v_max_f32 v114, v114, v75
mav 18,148,38
v_max_f32 v115, v115, v77
v_max_f32 v116, v114, v115
mva 22,38,152
v_max_f32 v117, v114, v115
s_waitcnt lgkmcnt(2)
mav 14,156,42
R 38,50176
v_permlane32_swap_b32 v116, v117
mav 18,160,42
v_max_f32 v110, v116, v117
v_max_f32 v111, v116, v117
mva 22,42,164
mav 14,168,46
R 42,51200
v_permlane16_swap_b32 v110, v111
mav 18,172,46
v_max_f32 v116, v110, v111
v_mul_f32 v116, s65, v116
mva 22,46,176
v_fma_f32 v114, v114, s65, -v116
s_waitcnt lgkmcnt(2)
mav 14,180,50
R 46,52224
v_fma_f32 v115, v115, s65, -v116
mav 18,184,50
v_exp_f32 v114, v114
mva 22,50,188
v_exp_f32 v115, v115
mav 14,192,54
R 50,53248
v_add_f32 v116, v114, v115
mav 18,196,54
v_add_f32 v117, v114, v115
mva 22,54,200
s_waitcnt lgkmcnt(2)
mav 14,204,38
R 54,54272
v_permlane32_swap_b32 v116, v117
mav 18,208,38
v_add_f32 v110, v116, v117
v_add_f32 v111, v116, v117
mva 22,38,212
mav 14,216,42
R 38,55296
v_permlane16_swap_b32 v110, v111
mav 18,220,42
v_add_f32 v116, v110, v111
mva 22,42,224
s_waitcnt lgkmcnt(2)
mav 14,228,46
R 42,56320
mav 18,232,46
v_rcp_f32 v116, v116
mva 22,46,236
v_mul_f32 v114, v114, v116
v_mul_f32 v115, v115, v116
mav 14,240,50
R 46,57344
v_cvt_pk_bf16_f32 v114, v114, v114
mav 18,244,50
v_cvt_pk_bf16_f32 v115, v115, v115
v_and_b32 v110, v114, v92
mva 22,50,248
v_and_b32 v111, v115, v93
s_waitcnt lgkmcnt(2)
mav 14,252,54
R 50,58368
v_and_b32 v112, v114, v94
mvv 18,128,54
v_and_b32 v113, v115, v95
mvv 22,54,132
mvv 14,136,38
R 54,59392
v_mfma_f32_16x16x32_bf16 v[70:73], v[8:11], v[110:113], 0
v_mfma_f32_16x16x32_bf16 v[74:77], v[104:107], v[110:113], 0
mvv 18,140,38
mvv 22,38,144
s_waitcnt lgkmcnt(2)
mvv 14,148,42
R 38,60416
mvv 18,152,42
mvv 22,42,156
v_cvt_pk_bf16_f32 v110, v70, v71
v_cvt_pk_bf16_f32 v111, v72, v73
mvv 14,160,46
R 42,61440
v_cvt_pk_bf16_f32 v112, v74, v75
mvv 18,164,46
v_cvt_pk_bf16_f32 v113, v76, v77
buffer_store_dwordx4 v[110:113], v81, s[36:39], s49 offen sc1
mvv 22,46,168
s_add_u32 s49, s49, 346112
s_waitcnt lgkmcnt(2)
mvv 14,172,50
R 46,62464
mvv 18,176,50
mvv 22,50,180
mvv 14,184,54
R 50,63488
mvv 18,188,54
mvv 22,54,192
s_waitcnt lgkmcnt(2)
mvv 14,196,38
R 54,64512
mvv 18,200,38
mvv 22,38,204
mvv 14,208,42
R 38,0
mvv 18,212,42
ds_read_b64 v[120:121], v82 offset:0
ds_read_b128 v[70:73], v83 offset:0
mvv 22,42,216
ds_read_b128 v[74:77], v83 offset:16
s_waitcnt lgkmcnt(5)
mvv 14,220,46
R 42,1024
mvv 18,224,46
mvv 22,46,228
mvv 14,232,50
R 46,2048
mvv 18,236,50
mvv 22,50,240
s_waitcnt lgkmcnt(2)
mvv 14,244,54
R 50,3072
mvv 18,248,54
mvv 22,54,252
v_mfma_f32_16x16x32_bf16 v[26:29], a[0:3], v[38:41], 0
R 54,4096
v_mul_f32 v71, v71, v118
v_mfma_f32_16x16x32_bf16 v[30:33], a[4:7], v[38:41], 0
v_mul_f32 v73, v73, v118
v_mul_f32 v75, v75, v118
v_mfma_f32_16x16x32_bf16 v[34:37], v[38:41], a[8:11], 0
v_mul_f32 v77, v77, v118
s_waitcnt lgkmcnt(2)
mav 26,12,42
R 58,5120
v_fma_f32 v22, v22, v70, v71
mav 30,16,42
v_fma_f32 v23, v23, v72, v73
v_fma_f32 v24, v24, v74, v75
mva 34,42,20
v_fma_f32 v25, v25, v76, v77
v_cvt_pk_bf16_f32 v8, v22, v23
mav 26,24,46
R 38,6144
v_cvt_pk_bf16_f32 v9, v24, v25
mav 30,28,46
v_mul_f32 v122, v121, v62
v_mfma_f32_16x16x32_bf16 v[70:73], v[100:103], v[0:3], v[84:87]
v_fma_f32 v14, v14, v120, v122
v_mul_f32 v123, v121, v63
mva 34,46,32
v_fma_f32 v15, v15, v120, v123
s_waitcnt lgkmcnt(2)
mav 26,36,50
R 42,7168
v_mul_f32 v122, v121, v64
mav 30,40,50
v_fma_f32 v16, v16, v120, v122
v_mul_f32 v123, v121, v65
mva 34,50,44
v_fma_f32 v17, v17, v120, v123
v_mul_f32 v122, v121, v66
mav 26,48,54
R 46,8192
v_fma_f32 v18, v18, v120, v122
mav 30,52,54
v_mul_f32 v123, v121, v67
v_fma_f32 v19, v19, v120, v123
mva 34,54,56
v_mul_f32 v122, v121, v68
s_waitcnt lgkmcnt(2)
mav 26,60,58
R 50,9216
v_fma_f32 v20, v20, v120, v122
mav 30,64,58
v_mul_f32 v123, v121, v69
v_fma_f32 v21, v21, v120, v123
mva 34,58,68
mav 26,72,38
R 54,10240
mav 30,76,38
mva 34,38,80
s_waitcnt lgkmcnt(2)
mav 26,84,42
R 58,11264
mav 30,88,42
mva 34,42,92
mav 26,96,46
R 38,12288
v_add_u32 v78, 1, v78
mav 30,100,46
s_barrier
ds_read_b128 v[96:99], v81 offset:0
mva 34,46,104
ds_read_b64 v[104:105], v81 offset:1024
s_waitcnt lgkmcnt(4)
mav 26,108,50
R 42,13312
mav 30,112,50
mva 34,50,116
mav 26,120,54
R 46,14336
mav 30,124,54
s_waitcnt vmcnt(0)
buffer_store_dwordx2 v[78:79], off, s[36:39], s52 sc1
mva 34,54,128
s_waitcnt lgkmcnt(3)
v_mfma_f32_16x16x32_bf16 v[74:77], v[96:99], v[0:3], v[88:91]
v_cvt_pk_bf16_f32 v0, v14, v15
v_cvt_pk_bf16_f32 v1, v16, v17
mav 26,132,58
R 50,15360
v_cvt_pk_bf16_f32 v2, v18, v19
mav 30,136,58
v_cvt_pk_bf16_f32 v3, v20, v21
mva 34,58,140
v_max3_f32 v114, v70, v71, v74
v_max3_f32 v115, v72, v73, v76
mav 26,144,38
R 54,16384
v_max_f32 v114, v114, v75
mav 30,148,38
v_max_f32 v115, v115, v77
v_max_f32 v116, v114, v115
mva 34,38,152
v_max_f32 v117, v114, v115
s_waitcnt lgkmcnt(2)
mav 26,156,42
R 38,17408
v_permlane32_swap_b32 v116, v117
mav 30,160,42
v_max_f32 v110, v116, v117
v_max_f32 v111, v116, v117
mva 34,42,164
mav 26,168,46
R 42,18432
v_permlane16_swap_b32 v110, v111
mav 30,172,46
v_max_f32 v116, v110, v111
v_mul_f32 v116, s65, v116
mva 34,46,176
v_fma_f32 v114, v114, s65, -v116
s_waitcnt lgkmcnt(2)
mav 26,180,50
R 46,19456
v_fma_f32 v115, v115, s65, -v116
mav 30,184,50
v_exp_f32 v114, v114
mva 34,50,188
v_exp_f32 v115, v115
mav 26,192,54
R 50,20480
v_add_f32 v116, v114, v115
mav 30,196,54
v_add_f32 v117, v114, v115
mva 34,54,200
s_waitcnt lgkmcnt(2)
mav 26,204,38
R 54,21504
v_permlane32_swap_b32 v116, v117
mav 30,208,38
v_add_f32 v110, v116, v117
v_add_f32 v111, v116, v117
mva 34,38,212
mav 26,216,42
R 38,22528
v_permlane16_swap_b32 v110, v111
mav 30,220,42
v_add_f32 v116, v110, v111
mva 34,42,224
s_waitcnt lgkmcnt(2)
mav 26,228,46
R 42,23552
mav 30,232,46
v_rcp_f32 v116, v116
mva 34,46,236
v_mul_f32 v114, v114, v116
v_mul_f32 v115, v115, v116
mav 26,240,50
R 46,24576
v_cvt_pk_bf16_f32 v114, v114, v114
mav 30,244,50
v_cvt_pk_bf16_f32 v115, v115, v115
v_and_b32 v110, v114, v94
mva 34,50,248
v_and_b32 v111, v115, v95
s_waitcnt lgkmcnt(2)
mav 26,252,54
R 50,25600
v_and_b32 v112, v114, v92
mvv 30,128,54
v_and_b32 v113, v115, v93
mvv 34,54,132
mvv 26,136,38
R 54,26624
v_mfma_f32_16x16x32_bf16 v[70:73], v[8:11], v[110:113], 0
v_mfma_f32_16x16x32_bf16 v[74:77], v[104:107], v[110:113], 0
mvv 30,140,38
mvv 34,38,144
s_waitcnt lgkmcnt(2)
mvv 26,148,42
R 38,27648
mvv 30,152,42
mvv 34,42,156
v_cvt_pk_bf16_f32 v110, v70, v71
v_cvt_pk_bf16_f32 v111, v72, v73
mvv 26,160,46
R 42,28672
v_cvt_pk_bf16_f32 v112, v74, v75
mvv 30,164,46
v_cvt_pk_bf16_f32 v113, v76, v77
buffer_store_dwordx4 v[110:113], v81, s[36:39], s49 offen sc1
mvv 34,46,168
s_add_u32 s49, s49, 346112
s_waitcnt lgkmcnt(2)
mvv 26,172,50
R 46,29696
mvv 30,176,50
mvv 34,50,180
mvv 26,184,54
R 50,30720
mvv 30,188,54
mvv 34,54,192
s_waitcnt lgkmcnt(2)
mvv 26,196,38
R 54,31744
v_xor_b32 v80, 0x10000, v80
mvv 30,200,38
mvv 34,38,204
mvv 26,208,42
R 38,32768
mvv 30,212,42
ds_read_b64 v[120:121], v82 offset:128
ds_read_b128 v[70:73], v83 offset:128
mvv 34,42,216
ds_read_b128 v[74:77], v83 offset:144
s_waitcnt lgkmcnt(5)
mvv 26,220,46
R 42,33792
mvv 30,224,46
mvv 34,46,228
mvv 26,232,50
R 46,34816
mvv 30,236,50
mvv 34,50,240
s_waitcnt lgkmcnt(2)
mvv 26,244,54
R 50,35840
mvv 30,248,54
mvv 34,54,252
s_mov_b32 s60, -13
.p2align 6
LQA_2:
v_mfma_f32_16x16x32_bf16 v[14:17], a[0:3], v[38:41], 0
R 54,36864
v_mul_f32 v71, v71, v118
v_mfma_f32_16x16x32_bf16 v[18:21], a[4:7], v[38:41], 0
v_mul_f32 v73, v73, v118
v_mul_f32 v75, v75, v118
v_mfma_f32_16x16x32_bf16 v[22:25], v[38:41], a[8:11], 0
v_mul_f32 v77, v77, v118
s_waitcnt lgkmcnt(2)
mav 14,12,42
R 58,37888
v_fma_f32 v34, v34, v70, v71
mav 18,16,42
v_fma_f32 v35, v35, v72, v73
v_fma_f32 v36, v36, v74, v75
mva 22,42,20
v_fma_f32 v37, v37, v76, v77
v_cvt_pk_bf16_f32 v10, v34, v35
mav 14,24,46
R 38,38912
v_cvt_pk_bf16_f32 v11, v36, v37
mav 18,28,46
v_mul_f32 v122, v121, v62
v_mfma_f32_16x16x32_bf16 v[70:73], v[96:99], v[0:3], v[84:87]
v_fma_f32 v26, v26, v120, v122
v_mul_f32 v123, v121, v63
mva 22,46,32
v_fma_f32 v27, v27, v120, v123
s_waitcnt lgkmcnt(2)
mav 14,36,50
R 42,39936
v_mul_f32 v122, v121, v64
mav 18,40,50
v_fma_f32 v28, v28, v120, v122
v_mul_f32 v123, v121, v65
mva 22,50,44
v_fma_f32 v29, v29, v120, v123
v_mul_f32 v122, v121, v66
mav 14,48,54
R 46,40960
v_fma_f32 v30, v30, v120, v122
mav 18,52,54
v_mul_f32 v123, v121, v67
v_fma_f32 v31, v31, v120, v123
mva 22,54,56
v_mul_f32 v122, v121, v68
s_waitcnt lgkmcnt(2)
mav 14,60,58
R 50,41984
v_fma_f32 v32, v32, v120, v122
mav 18,64,58
v_mul_f32 v123, v121, v69
v_fma_f32 v33, v33, v120, v123
mva 22,58,68
mav 14,72,38
R 54,43008
mav 18,76,38
mva 22,38,80
s_waitcnt lgkmcnt(2)
mav 14,84,42
R 58,44032
mav 18,88,42
mva 22,42,92
mav 14,96,46
R 38,45056
v_add_u32 v78, 1, v78
mav 18,100,46
s_barrier
ds_read_b128 v[100:103], v81 offset:4096
mva 22,46,104
ds_read_b64 v[106:107], v81 offset:5120
s_waitcnt lgkmcnt(4)
mav 14,108,50
R 42,46080
mav 18,112,50
mva 22,50,116
mav 14,120,54
R 46,47104
mav 18,124,54
s_waitcnt vmcnt(0)
buffer_store_dwordx2 v[78:79], off, s[36:39], s52 sc1
mva 22,54,128
s_waitcnt lgkmcnt(3)
v_mfma_f32_16x16x32_bf16 v[74:77], v[100:103], v[0:3], v[88:91]
v_cvt_pk_bf16_f32 v0, v26, v27
v_cvt_pk_bf16_f32 v1, v28, v29
mav 14,132,58
R 50,48128
v_cvt_pk_bf16_f32 v2, v30, v31
mav 18,136,58
v_cvt_pk_bf16_f32 v3, v32, v33
mva 22,58,140
v_max3_f32 v114, v70, v71, v74
v_max3_f32 v115, v72, v73, v76
mav 14,144,38
R 54,49152
v_max_f32 v114, v114, v75
mav 18,148,38
v_max_f32 v115, v115, v77
v_max_f32 v116, v114, v115
mva 22,38,152
v_max_f32 v117, v114, v115
s_waitcnt lgkmcnt(2)
mav 14,156,42
R 38,50176
v_permlane32_swap_b32 v116, v117
mav 18,160,42
v_max_f32 v110, v116, v117
v_max_f32 v111, v116, v117
mva 22,42,164
mav 14,168,46
R 42,51200
v_permlane16_swap_b32 v110, v111
mav 18,172,46
v_max_f32 v116, v110, v111
v_mul_f32 v116, s65, v116
mva 22,46,176
v_fma_f32 v114, v114, s65, -v116
s_waitcnt lgkmcnt(2)
mav 14,180,50
R 46,52224
v_fma_f32 v115, v115, s65, -v116
mav 18,184,50
v_exp_f32 v114, v114
mva 22,50,188
v_exp_f32 v115, v115
mav 14,192,54
R 50,53248
v_add_f32 v116, v114, v115
mav 18,196,54
v_add_f32 v117, v114, v115
mva 22,54,200
s_waitcnt lgkmcnt(2)
mav 14,204,38
R 54,54272
v_permlane32_swap_b32 v116, v117
mav 18,208,38
v_add_f32 v110, v116, v117
v_add_f32 v111, v116, v117
mva 22,38,212
mav 14,216,42
R 38,55296
v_permlane16_swap_b32 v110, v111
mav 18,220,42
v_add_f32 v116, v110, v111
mva 22,42,224
s_waitcnt lgkmcnt(2)
mav 14,228,46
R 42,56320
mav 18,232,46
v_rcp_f32 v116, v116
mva 22,46,236
v_mul_f32 v114, v114, v116
v_mul_f32 v115, v115, v116
mav 14,240,50
R 46,57344
v_cvt_pk_bf16_f32 v114, v114, v114
mav 18,244,50
v_cvt_pk_bf16_f32 v115, v115, v115
v_and_b32 v110, v114, v92
mva 22,50,248
v_and_b32 v111, v115, v93
s_waitcnt lgkmcnt(2)
mav 14,252,54
R 50,58368
v_and_b32 v112, v114, v94
mvv 18,128,54
v_and_b32 v113, v115, v95
mvv 22,54,132
mvv 14,136,38
R 54,59392
v_mfma_f32_16x16x32_bf16 v[70:73], v[8:11], v[110:113], 0
v_mfma_f32_16x16x32_bf16 v[74:77], v[104:107], v[110:113], 0
mvv 18,140,38
mvv 22,38,144
s_waitcnt lgkmcnt(2)
mvv 14,148,42
R 38,60416
mvv 18,152,42
mvv 22,42,156
v_cvt_pk_bf16_f32 v110, v70, v71
v_cvt_pk_bf16_f32 v111, v72, v73
mvv 14,160,46
R 42,61440
v_cvt_pk_bf16_f32 v112, v74, v75
mvv 18,164,46
v_cvt_pk_bf16_f32 v113, v76, v77
buffer_store_dwordx4 v[110:113], v81, s[36:39], s49 offen sc1
mvv 22,46,168
s_add_u32 s49, s49, 346112
s_waitcnt lgkmcnt(2)
mvv 14,172,50
R 46,62464
mvv 18,176,50
mvv 22,50,180
mvv 14,184,54
R 50,63488
mvv 18,188,54
mvv 22,54,192
s_waitcnt lgkmcnt(2)
mvv 14,196,38
R 54,64512
mvv 18,200,38
mvv 22,38,204
mvv 14,208,42
R 38,0
mvv 18,212,42
ds_read_b64 v[120:121], v82 offset:0
ds_read_b128 v[70:73], v83 offset:0
mvv 22,42,216
ds_read_b128 v[74:77], v83 offset:16
s_waitcnt lgkmcnt(5)
mvv 14,220,46
R 42,1024
mvv 18,224,46
mvv 22,46,228
mvv 14,232,50
R 46,2048
mvv 18,236,50
mvv 22,50,240
s_waitcnt lgkmcnt(2)
mvv 14,244,54
R 50,3072
mvv 18,248,54
mvv 22,54,252
v_mfma_f32_16x16x32_bf16 v[26:29], a[0:3], v[38:41], 0
R 54,4096
v_mul_f32 v71, v71, v118
v_mfma_f32_16x16x32_bf16 v[30:33], a[4:7], v[38:41], 0
v_mul_f32 v73, v73, v118
v_mul_f32 v75, v75, v118
v_mfma_f32_16x16x32_bf16 v[34:37], v[38:41], a[8:11], 0
v_mul_f32 v77, v77, v118
s_waitcnt lgkmcnt(2)
mav 26,12,42
R 58,5120
v_fma_f32 v22, v22, v70, v71
mav 30,16,42
v_fma_f32 v23, v23, v72, v73
v_fma_f32 v24, v24, v74, v75
mva 34,42,20
v_fma_f32 v25, v25, v76, v77
v_cvt_pk_bf16_f32 v8, v22, v23
mav 26,24,46
R 38,6144
v_cvt_pk_bf16_f32 v9, v24, v25
mav 30,28,46
v_mul_f32 v122, v121, v62
v_mfma_f32_16x16x32_bf16 v[70:73], v[100:103], v[0:3], v[84:87]
v_fma_f32 v14, v14, v120, v122
v_mul_f32 v123, v121, v63
mva 34,46,32
v_fma_f32 v15, v15, v120, v123
s_waitcnt lgkmcnt(2)
mav 26,36,50
R 42,7168
v_mul_f32 v122, v121, v64
mav 30,40,50
v_fma_f32 v16, v16, v120, v122
v_mul_f32 v123, v121, v65
mva 34,50,44
v_fma_f32 v17, v17, v120, v123
v_mul_f32 v122, v121, v66
mav 26,48,54
R 46,8192
v_fma_f32 v18, v18, v120, v122
mav 30,52,54
v_mul_f32 v123, v121, v67
v_fma_f32 v19, v19, v120, v123
mva 34,54,56
v_mul_f32 v122, v121, v68
s_waitcnt lgkmcnt(2)
mav 26,60,58
R 50,9216
v_fma_f32 v20, v20, v120, v122
mav 30,64,58
v_mul_f32 v123, v121, v69
v_fma_f32 v21, v21, v120, v123
mva 34,58,68
mav 26,72,38
R 54,10240
mav 30,76,38
mva 34,38,80
s_waitcnt lgkmcnt(2)
mav 26,84,42
R 58,11264
mav 30,88,42
mva 34,42,92
mav 26,96,46
R 38,12288
v_add_u32 v78, 1, v78
mav 30,100,46
s_barrier
ds_read_b128 v[96:99], v81 offset:0
mva 34,46,104
ds_read_b64 v[104:105], v81 offset:1024
s_waitcnt lgkmcnt(4)
mav 26,108,50
R 42,13312
mav 30,112,50
mva 34,50,116
mav 26,120,54
R 46,14336
mav 30,124,54
s_waitcnt vmcnt(0)
buffer_store_dwordx2 v[78:79], off, s[36:39], s52 sc1
mva 34,54,128
s_waitcnt lgkmcnt(3)
v_mfma_f32_16x16x32_bf16 v[74:77], v[96:99], v[0:3], v[88:91]
v_cvt_pk_bf16_f32 v0, v14, v15
v_cvt_pk_bf16_f32 v1, v16, v17
mav 26,132,58
R 50,15360
v_cvt_pk_bf16_f32 v2, v18, v19
mav 30,136,58
v_cvt_pk_bf16_f32 v3, v20, v21
mva 34,58,140
v_max3_f32 v114, v70, v71, v74
v_max3_f32 v115, v72, v73, v76
mav 26,144,38
R 54,16384
v_max_f32 v114, v114, v75
mav 30,148,38
v_max_f32 v115, v115, v77
v_max_f32 v116, v114, v115
mva 34,38,152
v_max_f32 v117, v114, v115
s_waitcnt lgkmcnt(2)
mav 26,156,42
R 38,17408
v_permlane32_swap_b32 v116, v117
mav 30,160,42
v_max_f32 v110, v116, v117
v_max_f32 v111, v116, v117
mva 34,42,164
mav 26,168,46
R 42,18432
v_permlane16_swap_b32 v110, v111
mav 30,172,46
v_max_f32 v116, v110, v111
v_mul_f32 v116, s65, v116
mva 34,46,176
v_fma_f32 v114, v114, s65, -v116
s_waitcnt lgkmcnt(2)
mav 26,180,50
R 46,19456
v_fma_f32 v115, v115, s65, -v116
mav 30,184,50
v_exp_f32 v114, v114
mva 34,50,188
v_exp_f32 v115, v115
mav 26,192,54
R 50,20480
v_add_f32 v116, v114, v115
mav 30,196,54
v_add_f32 v117, v114, v115
mva 34,54,200
s_waitcnt lgkmcnt(2)
mav 26,204,38
R 54,21504
v_permlane32_swap_b32 v116, v117
mav 30,208,38
v_add_f32 v110, v116, v117
v_add_f32 v111, v116, v117
mva 34,38,212
mav 26,216,42
R 38,22528
v_permlane16_swap_b32 v110, v111
mav 30,220,42
v_add_f32 v116, v110, v111
mva 34,42,224
s_waitcnt lgkmcnt(2)
mav 26,228,46
R 42,23552
mav 30,232,46
v_rcp_f32 v116, v116
mva 34,46,236
v_mul_f32 v114, v114, v116
v_mul_f32 v115, v115, v116
mav 26,240,50
R 46,24576
v_cvt_pk_bf16_f32 v114, v114, v114
mav 30,244,50
v_cvt_pk_bf16_f32 v115, v115, v115
v_and_b32 v110, v114, v94
mva 34,50,248
v_and_b32 v111, v115, v95
s_waitcnt lgkmcnt(2)
mav 26,252,54
R 50,25600
v_and_b32 v112, v114, v92
mvv 30,128,54
v_and_b32 v113, v115, v93
mvv 34,54,132
mvv 26,136,38
R 54,26624
v_mfma_f32_16x16x32_bf16 v[70:73], v[8:11], v[110:113], 0
v_mfma_f32_16x16x32_bf16 v[74:77], v[104:107], v[110:113], 0
mvv 30,140,38
mvv 34,38,144
s_waitcnt lgkmcnt(2)
mvv 26,148,42
R 38,27648
mvv 30,152,42
mvv 34,42,156
v_cvt_pk_bf16_f32 v110, v70, v71
v_cvt_pk_bf16_f32 v111, v72, v73
mvv 26,160,46
R 42,28672
v_cvt_pk_bf16_f32 v112, v74, v75
mvv 30,164,46
v_cvt_pk_bf16_f32 v113, v76, v77
buffer_store_dwordx4 v[110:113], v81, s[36:39], s49 offen sc1
mvv 34,46,168
s_add_u32 s49, s49, 346112
s_waitcnt lgkmcnt(2)
mvv 26,172,50
R 46,29696
mvv 30,176,50
mvv 34,50,180
mvv 26,184,54
R 50,30720
mvv 30,188,54
mvv 34,54,192
s_waitcnt lgkmcnt(2)
mvv 26,196,38
R 54,31744
v_xor_b32 v80, 0x10000, v80
mvv 30,200,38
mvv 34,38,204
mvv 26,208,42
R 38,32768
mvv 30,212,42
ds_read_b64 v[120:121], v82 offset:128
ds_read_b128 v[70:73], v83 offset:128
mvv 34,42,216
ds_read_b128 v[74:77], v83 offset:144
s_waitcnt lgkmcnt(5)
mvv 26,220,46
R 42,33792
mvv 30,224,46
mvv 34,46,228
mvv 26,232,50
R 46,34816
mvv 30,236,50
mvv 34,50,240
s_waitcnt lgkmcnt(2)
mvv 26,244,54
R 50,35840
mvv 30,248,54
mvv 34,54,252
s_add_u32 s60, s60, 1
s_cbranch_scc0 LQA_2
v_mfma_f32_16x16x32_bf16 v[14:17], a[0:3], v[38:41], 0
R 54,36864
v_mul_f32 v71, v71, v118
v_mfma_f32_16x16x32_bf16 v[18:21], a[4:7], v[38:41], 0
v_mul_f32 v73, v73, v118
v_mul_f32 v75, v75, v118
v_mfma_f32_16x16x32_bf16 v[22:25], v[38:41], a[8:11], 0
v_mul_f32 v77, v77, v118
s_waitcnt lgkmcnt(2)
mav 14,12,42
R 58,37888
v_fma_f32 v34, v34, v70, v71
mav 18,16,42
v_fma_f32 v35, v35, v72, v73
v_fma_f32 v36, v36, v74, v75
mva 22,42,20
v_fma_f32 v37, v37, v76, v77
v_cvt_pk_bf16_f32 v10, v34, v35
mav 14,24,46
R 38,38912
v_cvt_pk_bf16_f32 v11, v36, v37
mav 18,28,46
v_mul_f32 v122, v121, v62
v_mfma_f32_16x16x32_bf16 v[70:73], v[96:99], v[0:3], v[84:87]
v_fma_f32 v26, v26, v120, v122
v_mul_f32 v123, v121, v63
mva 22,46,32
v_fma_f32 v27, v27, v120, v123
s_waitcnt lgkmcnt(2)
mav 14,36,50
R 42,39936
v_mul_f32 v122, v121, v64
mav 18,40,50
v_fma_f32 v28, v28, v120, v122
v_mul_f32 v123, v121, v65
mva 22,50,44
v_fma_f32 v29, v29, v120, v123
v_mul_f32 v122, v121, v66
mav 14,48,54
R 46,40960
v_fma_f32 v30, v30, v120, v122
mav 18,52,54
v_mul_f32 v123, v121, v67
v_fma_f32 v31, v31, v120, v123
mva 22,54,56
v_mul_f32 v122, v121, v68
s_waitcnt lgkmcnt(2)
mav 14,60,58
R 50,41984
v_fma_f32 v32, v32, v120, v122
mav 18,64,58
v_mul_f32 v123, v121, v69
v_fma_f32 v33, v33, v120, v123
mva 22,58,68
mav 14,72,38
R 54,43008
mav 18,76,38
mva 22,38,80
s_waitcnt lgkmcnt(2)
mav 14,84,42
R 58,44032
mav 18,88,42
mva 22,42,92
mav 14,96,46
R 38,45056
v_add_u32 v78, 1, v78
mav 18,100,46
s_barrier
ds_read_b128 v[100:103], v81 offset:4096
mva 22,46,104
ds_read_b64 v[106:107], v81 offset:5120
s_waitcnt lgkmcnt(4)
mav 14,108,50
R 42,46080
mav 18,112,50
mva 22,50,116
mav 14,120,54
R 46,47104
mav 18,124,54
s_waitcnt vmcnt(0)
buffer_store_dwordx2 v[78:79], off, s[36:39], s52 sc1
mva 22,54,128
s_waitcnt lgkmcnt(3)
v_mfma_f32_16x16x32_bf16 v[74:77], v[100:103], v[0:3], v[88:91]
v_cvt_pk_bf16_f32 v0, v26, v27
v_cvt_pk_bf16_f32 v1, v28, v29
mav 14,132,58
R 50,48128
v_cvt_pk_bf16_f32 v2, v30, v31
mav 18,136,58
v_cvt_pk_bf16_f32 v3, v32, v33
mva 22,58,140
v_max3_f32 v114, v70, v71, v74
v_max3_f32 v115, v72, v73, v76
mav 14,144,38
R 54,49152
v_max_f32 v114, v114, v75
mav 18,148,38
v_max_f32 v115, v115, v77
v_max_f32 v116, v114, v115
mva 22,38,152
v_max_f32 v117, v114, v115
s_waitcnt lgkmcnt(2)
mav 14,156,42
R 38,50176
v_permlane32_swap_b32 v116, v117
mav 18,160,42
v_max_f32 v110, v116, v117
v_max_f32 v111, v116, v117
mva 22,42,164
mav 14,168,46
R 42,51200
v_permlane16_swap_b32 v110, v111
mav 18,172,46
v_max_f32 v116, v110, v111
v_mul_f32 v116, s65, v116
mva 22,46,176
v_fma_f32 v114, v114, s65, -v116
s_waitcnt lgkmcnt(2)
mav 14,180,50
R 46,52224
v_fma_f32 v115, v115, s65, -v116
mav 18,184,50
v_exp_f32 v114, v114
mva 22,50,188
v_exp_f32 v115, v115
mav 14,192,54
R 50,53248
v_add_f32 v116, v114, v115
mav 18,196,54
v_add_f32 v117, v114, v115
mva 22,54,200
s_waitcnt lgkmcnt(2)
mav 14,204,38
R 54,54272
v_permlane32_swap_b32 v116, v117
mav 18,208,38
v_add_f32 v110, v116, v117
v_add_f32 v111, v116, v117
mva 22,38,212
mav 14,216,42
R 38,55296
v_permlane16_swap_b32 v110, v111
mav 18,220,42
v_add_f32 v116, v110, v111
mva 22,42,224
s_waitcnt lgkmcnt(2)
mav 14,228,46
R 42,56320
mav 18,232,46
v_rcp_f32 v116, v116
mva 22,46,236
v_mul_f32 v114, v114, v116
v_mul_f32 v115, v115, v116
mav 14,240,50
R 46,57344
v_cvt_pk_bf16_f32 v114, v114, v114
mav 18,244,50
v_cvt_pk_bf16_f32 v115, v115, v115
v_and_b32 v110, v114, v92
mva 22,50,248
v_and_b32 v111, v115, v93
s_waitcnt lgkmcnt(2)
mav 14,252,54
R 50,58368
v_and_b32 v112, v114, v94
mvv 18,128,54
v_and_b32 v113, v115, v95
mvv 22,54,132
mvv 14,136,38
R 54,59392
v_mfma_f32_16x16x32_bf16 v[70:73], v[8:11], v[110:113], 0
v_mfma_f32_16x16x32_bf16 v[74:77], v[104:107], v[110:113], 0
mvv 18,140,38
mvv 22,38,144
s_waitcnt lgkmcnt(2)
mvv 14,148,42
R 38,60416
mvv 18,152,42
mvv 22,42,156
v_cvt_pk_bf16_f32 v110, v70, v71
v_cvt_pk_bf16_f32 v111, v72, v73
mvv 14,160,46
R 42,61440
v_cvt_pk_bf16_f32 v112, v74, v75
mvv 18,164,46
v_cvt_pk_bf16_f32 v113, v76, v77
buffer_store_dwordx4 v[110:113], v81, s[36:39], s49 offen sc1
mvv 22,46,168
s_add_u32 s49, s49, 346112
s_waitcnt lgkmcnt(2)
mvv 14,172,50
R 46,62464
mvv 18,176,50
mvv 22,50,180
mvv 14,184,54
R 50,63488
mvv 18,188,54
mvv 22,54,192
s_waitcnt lgkmcnt(2)
mvv 14,196,38
R 54,64512
mvv 18,200,38
mvv 22,38,204
mvv 14,208,42
R 38,0
mvv 18,212,42
ds_read_b64 v[120:121], v82 offset:0
ds_read_b128 v[70:73], v83 offset:0
mvv 22,42,216
ds_read_b128 v[74:77], v83 offset:16
s_waitcnt lgkmcnt(5)
mvv 14,220,46
R 42,1024
mvv 18,224,46
mvv 22,46,228
mvv 14,232,50
R 46,2048
mvv 18,236,50
mvv 22,50,240
s_waitcnt lgkmcnt(2)
mvv 14,244,54
R 50,3072
mvv 18,248,54
mvv 22,54,252
v_mfma_f32_16x16x32_bf16 v[26:29], a[0:3], v[38:41], 0
R 54,4096
v_mul_f32 v71, v71, v118
v_mfma_f32_16x16x32_bf16 v[30:33], a[4:7], v[38:41], 0
v_mul_f32 v73, v73, v118
v_mul_f32 v75, v75, v118
v_mfma_f32_16x16x32_bf16 v[34:37], v[38:41], a[8:11], 0
v_mul_f32 v77, v77, v118
s_waitcnt lgkmcnt(2)
mav 26,12,42
R 58,5120
v_fma_f32 v22, v22, v70, v71
mav 30,16,42
v_fma_f32 v23, v23, v72, v73
v_fma_f32 v24, v24, v74, v75
mva 34,42,20
v_fma_f32 v25, v25, v76, v77
v_cvt_pk_bf16_f32 v8, v22, v23
mav 26,24,46
R 38,6144
v_cvt_pk_bf16_f32 v9, v24, v25
mav 30,28,46
v_mul_f32 v122, v121, v62
v_mfma_f32_16x16x32_bf16 v[70:73], v[100:103], v[0:3], v[84:87]
v_fma_f32 v14, v14, v120, v122
v_mul_f32 v123, v121, v63
mva 34,46,32
v_fma_f32 v15, v15, v120, v123
s_waitcnt lgkmcnt(2)
mav 26,36,50
R 42,7168
v_mul_f32 v122, v121, v64
mav 30,40,50
buffer_load_dwordx4 v[4:7], v81, s[36:39], s55 offen offset:2048 sc1
buffer_load_dwordx2 v[12:13], v81, s[36:39], s55 offen offset:3072 sc1
mva 34,50,44
buffer_load_dwordx2 v[108:109], v81, s[36:39], s55 offen offset:3080 sc1
v_fma_f32 v16, v16, v120, v122
mav 26,48,54
R 46,8192
v_mul_f32 v123, v121, v65
mav 30,52,54
v_fma_f32 v17, v17, v120, v123
v_mul_f32 v122, v121, v66
mva 34,54,56
v_fma_f32 v18, v18, v120, v122
s_waitcnt lgkmcnt(2)
mav 26,60,58
R 50,9216
v_mul_f32 v123, v121, v67
mav 30,64,58
v_fma_f32 v19, v19, v120, v123
v_mul_f32 v122, v121, v68
mva 34,58,68
v_fma_f32 v20, v20, v120, v122
v_mul_f32 v123, v121, v69
mav 26,72,38
R 54,10240
v_fma_f32 v21, v21, v120, v123
mav 30,76,38
mva 34,38,80
s_waitcnt lgkmcnt(2)
mav 26,84,42
R 58,11264
mav 30,88,42
mva 34,42,92
mav 26,96,46
R 38,12288
v_add_u32 v78, 1, v78
mav 30,100,46
s_barrier
ds_read_b128 v[96:99], v81 offset:0
mva 34,46,104
ds_read_b64 v[104:105], v81 offset:1024
s_waitcnt lgkmcnt(4)
mav 26,108,50
R 42,13312
mav 30,112,50
mva 34,50,116
mav 26,120,54
R 46,14336
mav 30,124,54
s_waitcnt vmcnt(3)
buffer_store_dwordx2 v[78:79], off, s[36:39], s52 sc1
mva 34,54,128
s_waitcnt lgkmcnt(3)
v_mfma_f32_16x16x32_bf16 v[74:77], v[96:99], v[0:3], v[88:91]
v_cvt_pk_bf16_f32 v0, v14, v15
v_cvt_pk_bf16_f32 v1, v16, v17
mav 26,132,58
R 50,15360
v_cvt_pk_bf16_f32 v2, v18, v19
mav 30,136,58
v_cvt_pk_bf16_f32 v3, v20, v21
mva 34,58,140
v_max3_f32 v114, v70, v71, v74
v_max3_f32 v115, v72, v73, v76
mav 26,144,38
R 54,16384
v_max_f32 v114, v114, v75
mav 30,148,38
v_max_f32 v115, v115, v77
v_max_f32 v116, v114, v115
mva 34,38,152
v_max_f32 v117, v114, v115
s_waitcnt lgkmcnt(2)
mav 26,156,42
R 38,17408
v_permlane32_swap_b32 v116, v117
mav 30,160,42
v_max_f32 v110, v116, v117
v_max_f32 v111, v116, v117
mva 34,42,164
mav 26,168,46
R 42,18432
v_permlane16_swap_b32 v110, v111
mav 30,172,46
v_max_f32 v116, v110, v111
v_mul_f32 v116, s65, v116
mva 34,46,176
v_fma_f32 v114, v114, s65, -v116
s_waitcnt lgkmcnt(2)
mav 26,180,50
R 46,19456
v_fma_f32 v115, v115, s65, -v116
mav 30,184,50
v_exp_f32 v114, v114
mva 34,50,188
v_exp_f32 v115, v115
mav 26,192,54
R 50,20480
v_add_f32 v116, v114, v115
mav 30,196,54
v_add_f32 v117, v114, v115
mva 34,54,200
s_waitcnt lgkmcnt(2)
mav 26,204,38
R 54,21504
v_permlane32_swap_b32 v116, v117
mav 30,208,38
v_add_f32 v110, v116, v117
v_add_f32 v111, v116, v117
mva 34,38,212
mav 26,216,42
R 38,22528
v_permlane16_swap_b32 v110, v111
mav 30,220,42
v_add_f32 v116, v110, v111
mva 34,42,224
s_waitcnt lgkmcnt(2)
mav 26,228,46
R 42,23552
mav 30,232,46
v_rcp_f32 v116, v116
mva 34,46,236
v_mul_f32 v114, v114, v116
v_mul_f32 v115, v115, v116
mav 26,240,50
R 46,24576
v_cvt_pk_bf16_f32 v114, v114, v114
mav 30,244,50
v_cvt_pk_bf16_f32 v115, v115, v115
v_and_b32 v110, v114, v94
mva 34,50,248
v_and_b32 v111, v115, v95
s_waitcnt lgkmcnt(2)
mav 26,252,54
R 50,25600
v_and_b32 v112, v114, v92
mvv 30,128,54
v_and_b32 v113, v115, v93
mvv 34,54,132
mvv 26,136,38
R 54,26624
v_mfma_f32_16x16x32_bf16 v[70:73], v[8:11], v[110:113], 0
v_mfma_f32_16x16x32_bf16 v[74:77], v[104:107], v[110:113], 0
mvv 30,140,38
mvv 34,38,144
s_waitcnt lgkmcnt(2)
mvv 26,148,42
R 38,27648
mvv 30,152,42
mvv 34,42,156
v_cvt_pk_bf16_f32 v110, v70, v71
v_cvt_pk_bf16_f32 v111, v72, v73
mvv 26,160,46
R 42,28672
v_cvt_pk_bf16_f32 v112, v74, v75
mvv 30,164,46
v_cvt_pk_bf16_f32 v113, v76, v77
buffer_store_dwordx4 v[110:113], v81, s[36:39], s49 offen sc1
mvv 34,46,168
s_add_u32 s49, s49, 346112
s_waitcnt lgkmcnt(2)
mvv 26,172,50
R 46,29696
mvv 30,176,50
mvv 34,50,180
mvv 26,184,54
R 50,30720
mvv 30,188,54
mvv 34,54,192
s_waitcnt lgkmcnt(2)
mvv 26,196,38
R 54,31744
mvv 30,200,38
mvv 34,38,204
mvv 26,208,42
mvv 30,212,42
ds_read_b64 v[120:121], v82 offset:128
ds_read_b128 v[70:73], v83 offset:128
mvv 34,42,216
ds_read_b128 v[74:77], v83 offset:144
s_waitcnt lgkmcnt(4)
mvv 26,220,46
mvv 30,224,46
mvv 34,46,228
mvv 26,232,50
mvv 30,236,50
mvv 34,50,240
s_waitcnt lgkmcnt(0)
mvv 26,244,54
mvv 30,248,54
mvv 34,54,252
v_mul_f32 v71, v71, v118
s_nop 6
v_fma_f32 v34, v34, v70, v71
v_mul_f32 v73, v73, v118
v_fma_f32 v35, v35, v72, v73
v_mul_f32 v75, v75, v118
v_fma_f32 v36, v36, v74, v75
v_mul_f32 v77, v77, v118
v_fma_f32 v37, v37, v76, v77
v_cvt_pk_bf16_f32 v10, v34, v35
v_cvt_pk_bf16_f32 v11, v36, v37
s_nop 1
v_mfma_f32_16x16x32_bf16 v[70:73], v[96:99], v[0:3], v[84:87]
v_mul_f32 v122, v121, v62
v_fma_f32 v26, v26, v120, v122
v_mul_f32 v123, v121, v63
v_fma_f32 v27, v27, v120, v123
v_mul_f32 v122, v121, v64
v_fma_f32 v28, v28, v120, v122
)"
R"(
v_mul_f32 v123, v121, v65
v_fma_f32 v29, v29, v120, v123
v_mul_f32 v122, v121, v66
v_fma_f32 v30, v30, v120, v122
v_mul_f32 v123, v121, v67
v_fma_f32 v31, v31, v120, v123
v_mul_f32 v122, v121, v68
v_fma_f32 v32, v32, v120, v122
v_mul_f32 v123, v121, v69
v_fma_f32 v33, v33, v120, v123
s_barrier
ds_read_b128 v[100:103], v81 offset:4096
ds_read_b64 v[106:107], v81 offset:5120
s_waitcnt lgkmcnt(0)
v_mfma_f32_16x16x32_bf16 v[74:77], v[100:103], v[0:3], v[88:91]
s_nop 7
v_max3_f32 v114, v70, v71, v74
v_max3_f32 v115, v72, v73, v76
v_max_f32 v114, v114, v75
v_max_f32 v115, v115, v77
v_max_f32 v116, v114, v115
v_max_f32 v117, v114, v115
s_nop 1
v_permlane32_swap_b32 v116, v117
v_max_f32 v110, v116, v117
v_max_f32 v111, v116, v117
s_nop 1
v_permlane16_swap_b32 v110, v111
v_max_f32 v116, v110, v111
v_mul_f32 v116, s65, v116
v_fma_f32 v114, v114, s65, -v116
v_fma_f32 v115, v115, s65, -v116
v_exp_f32 v114, v114
v_exp_f32 v115, v115
s_nop 0
v_add_f32 v116, v114, v115
v_add_f32 v117, v114, v115
s_nop 1
v_permlane32_swap_b32 v116, v117
v_add_f32 v110, v116, v117
v_add_f32 v111, v116, v117
s_nop 1
v_permlane16_swap_b32 v110, v111
v_add_f32 v116, v110, v111
v_rcp_f32 v116, v116
s_nop 0
v_mul_f32 v114, v114, v116
v_mul_f32 v115, v115, v116
v_cvt_pk_bf16_f32 v114, v114, v114
v_cvt_pk_bf16_f32 v115, v115, v115
v_and_b32 v110, v114, v92
v_and_b32 v111, v115, v93
v_and_b32 v112, v114, v94
v_and_b32 v113, v115, v95
s_nop 1
v_mfma_f32_16x16x32_bf16 v[70:73], v[8:11], v[110:113], 0
v_mfma_f32_16x16x32_bf16 v[74:77], v[104:107], v[110:113], 0
s_nop 6
v_cvt_pk_bf16_f32 v110, v70, v71
v_cvt_pk_bf16_f32 v111, v72, v73
v_cvt_pk_bf16_f32 v112, v74, v75
v_cvt_pk_bf16_f32 v113, v76, v77
buffer_store_dwordx4 v[110:113], v81, s[36:39], s49 offen sc1
s_add_u32 s49, s49, 346112
v_cvt_pk_bf16_f32 v0, v26, v27
v_cvt_pk_bf16_f32 v1, v28, v29
v_cvt_pk_bf16_f32 v2, v30, v31
v_cvt_pk_bf16_f32 v3, v32, v33
v_add_u32 v78, 1, v78
s_waitcnt vmcnt(1)
buffer_store_dwordx2 v[78:79], off, s[36:39], s52 sc1
v_mfma_f32_16x16x32_bf16 v[70:73], v[100:103], v[0:3], v[84:87]
v_min_f32 v74, s56, v88
v_min_f32 v75, s56, v89
v_min_f32 v76, s56, v90
v_min_f32 v77, s56, v91
s_nop 1
mvv 74,4,0
s_nop 7
v_max3_f32 v114, v70, v71, v74
v_max3_f32 v115, v72, v73, v76
v_max_f32 v114, v114, v75
v_max_f32 v115, v115, v77
v_max_f32 v116, v114, v115
v_max_f32 v117, v114, v115
s_nop 1
v_permlane32_swap_b32 v116, v117
v_max_f32 v110, v116, v117
v_max_f32 v111, v116, v117
s_nop 1
v_permlane16_swap_b32 v110, v111
v_max_f32 v116, v110, v111
v_mul_f32 v116, s65, v116
v_fma_f32 v114, v114, s65, -v116
v_fma_f32 v115, v115, s65, -v116
v_exp_f32 v114, v114
v_exp_f32 v115, v115
s_nop 0
v_add_f32 v116, v114, v115
v_add_f32 v117, v114, v115
s_nop 1
v_permlane32_swap_b32 v116, v117
v_add_f32 v110, v116, v117
v_add_f32 v111, v116, v117
s_nop 1
v_permlane16_swap_b32 v110, v111
v_add_f32 v116, v110, v111
v_rcp_f32 v116, v116
s_nop 0
v_mul_f32 v114, v114, v116
v_mul_f32 v115, v115, v116
v_cvt_pk_bf16_f32 v114, v114, v114
v_cvt_pk_bf16_f32 v115, v115, v115
v_and_b32 v110, v114, v92
v_and_b32 v111, v115, v93
v_and_b32 v112, v114, v94
v_and_b32 v113, v115, v95
s_nop 1
v_mfma_f32_16x16x32_bf16 v[70:73], v[10:13], v[110:113], 0
v_mfma_f32_16x16x32_bf16 v[74:77], v[106:109], v[110:113], 0
s_nop 6
v_cvt_pk_bf16_f32 v110, v70, v71
v_cvt_pk_bf16_f32 v111, v72, v73
v_cvt_pk_bf16_f32 v112, v74, v75
v_cvt_pk_bf16_f32 v113, v76, v77
buffer_store_dwordx4 v[110:113], v81, s[36:39], s49 offen sc1
v_add_u32 v78, 1, v78
s_waitcnt vmcnt(2)
buffer_store_dwordx2 v[78:79], off, s[36:39], s52 sc1
v_add_u32 v78, 37, v78
s_waitcnt vmcnt(1)
buffer_store_dwordx2 v[78:79], off, s[36:39], s52 sc1
s_endpgm
Lr1:
s_cmp_eq_u32 s60, 1
s_cbranch_scc0 Lr2
buffer_load_dword v115, off, s[36:39], 0 sc1
s_lshr_b32 s66, s53, 10
s_mul_i32 s66, s66, 8208
s_mov_b32 s67, 0x8000
E 0,110592
E 1,114688
s_add_u32 m0, s66, 2048
s_nop 0
buffer_load_dwordx4 v3, s[44:47], s67 offen offset:0 lds
E 2,118784
s_add_u32 s51, s51, 196608
E 0,122880
s_add_u32 m0, s66, 6144
s_nop 0
buffer_load_dwordx4 v3, s[44:47], 0 offen offset:0 lds
E 1,126976
E 2,131072
s_add_u32 s51, s51, 196608
E 0,86016
E 1,90112
E 2,94208
s_add_u32 s51, s51, 196608
E 0,98304
E 1,102400
E 2,106496
s_add_u32 s51, s51, 196608
v_lshlrev_b32 v12, 2, v80
v_and_b32 v12, 0xc0, v12
v_xor_b32 v13, 0, v12
v_add_u32 v8, v7, v13
v_xor_b32 v13, 64, v12
v_add_u32 v9, v7, v13
v_xor_b32 v13, 128, v12
v_add_u32 v10, v7, v13
v_xor_b32 v13, 192, v12
v_add_u32 v11, v7, v13
v_mov_b32 v110, s64
v_mov_b32 v111, s64
v_mov_b32 v112, s64
v_mov_b32 v113, s64
s_waitcnt vmcnt(6)
s_barrier
v_add_u32 v4, 69632, v4
v_add_u32 v5, 69632, v5
v_add_u32 v6, 69632, v6
Ta 0,4,40960
Ta 4,5,40960
Ta 8,6,40960
Ta 12,4,53248
Ta 16,5,53248
Ta 20,6,53248
ds_read_b128 v[46:49], v8 offset:0
ds_read_b128 v[50:53], v9 offset:0
ds_read_b128 v[54:57], v8 offset:4096
ds_read_b128 v[58:61], v9 offset:4096
v_mov_b32 v114, 0x100
v_add_u32 v115, 0x9e3779b9, v115
buffer_store_dwordx2 v[114:115], v81, s[36:39], s52 offen sc1
v_add_u32 v92, v92, v114
E 0,61440
E 1,65536
s_add_u32 m0, s66, 2816
s_nop 0
buffer_load_dwordx4 v3, s[44:47], s67 offen offset:256 lds
E 2,69632
s_add_u32 s51, s51, 196608
E 0,73728
s_add_u32 m0, s66, 6912
s_nop 0
buffer_load_dwordx4 v3, s[44:47], 0 offen offset:256 lds
E 1,77824
E 2,81920
s_add_u32 s51, s51, 196608
E 0,36864
E 1,40960
E 2,45056
s_add_u32 s51, s51, 196608
E 0,49152
E 1,53248
E 2,57344
s_add_u32 s51, s51, 196608
s_waitcnt lgkmcnt(0)
s_waitcnt vmcnt(15)
s_barrier
E 0,110592
Ta 24,4,16384
v_mfma_f32_16x16x32_bf16 v[94:97], a[0:3], v[110:113], 0
v_mfma_f32_16x16x32_bf16 v[98:101], a[4:7], v[110:113], 0
Ta 28,5,16384
v_mfma_f32_16x16x32_bf16 v[102:105], v[110:113], a[8:11], 0
Ta 32,6,16384
E 1,114688
v_mfma_f32_16x16x32_bf16 v[22:25], a[0:3], v[46:49], 0
Ta 36,4,28672
v_mfma_f32_16x16x32_bf16 v[26:29], a[4:7], v[46:49], 0
Ta 40,5,28672
s_add_u32 m0, s66, 3584
s_nop 0
buffer_load_dwordx4 v3, s[44:47], s67 offen offset:512 lds
v_mfma_f32_16x16x32_bf16 v[30:33], v[46:49], a[8:11], 0
Ta 44,6,28672
v_mfma_f32_16x16x32_bf16 v[34:37], a[0:3], v[54:57], 0
ds_read_b128 v[62:65], v10 offset:0
E 2,118784
s_add_u32 s51, s51, 196608
v_mfma_f32_16x16x32_bf16 v[38:41], a[4:7], v[54:57], 0
ds_read_b128 v[66:69], v11 offset:0
v_mfma_f32_16x16x32_bf16 v[42:45], v[54:57], a[8:11], 0
ds_read_b128 v[70:73], v10 offset:4096
E 0,122880
mav 94,12,110
ds_read_b128 v[74:77], v11 offset:4096
mav 98,16,110
mva 102,110,20
s_add_u32 m0, s66, 7680
s_nop 0
buffer_load_dwordx4 v3, s[44:47], 0 offen offset:512 lds
mav 22,12,50
mav 26,16,50
E 1,126976
mva 30,50,20
mav 34,12,58
E 2,131072
s_add_u32 s51, s51, 196608
mav 38,16,58
mva 42,58,20
s_waitcnt lgkmcnt(0)
s_waitcnt vmcnt(14)
s_barrier
v_add_u32 v4, -49152, v4
v_add_u32 v5, -49152, v5
v_add_u32 v6, -49152, v6
E 0,86016
Ta 48,4,40960
mav 94,24,110
mav 98,28,110
Ta 52,5,40960
mva 102,110,32
Ta 56,6,40960
E 1,90112
mav 22,24,62
Ta 60,4,53248
mav 26,28,62
Ta 64,5,53248
mva 30,62,32
Ta 68,6,53248
E 2,94208
s_add_u32 s51, s51, 196608
mav 34,24,70
ds_read_b128 v[46:49], v8 offset:1024
mav 38,28,70
ds_read_b128 v[50:53], v9 offset:1024
mva 42,70,32
ds_read_b128 v[54:57], v8 offset:5120
E 0,98304
mav 94,36,110
ds_read_b128 v[58:61], v9 offset:5120
mav 98,40,110
mva 102,110,44
E 1,102400
mav 22,36,66
mav 26,40,66
mva 30,66,44
E 2,106496
s_add_u32 s51, s51, 196608
mav 34,36,74
mav 38,40,74
mva 42,74,44
s_waitcnt lgkmcnt(0)
s_waitcnt vmcnt(14)
s_barrier
E 0,61440
Ta 72,4,16384
mav 94,48,110
mav 98,52,110
Ta 76,5,16384
mva 102,110,56
Ta 80,6,16384
E 1,65536
mav 22,48,46
Ta 84,4,28672
mav 26,52,46
Ta 88,5,28672
s_add_u32 m0, s66, 4352
s_nop 0
buffer_load_dwordx4 v3, s[44:47], s67 offen offset:768 lds
mva 30,46,56
Ta 92,6,28672
mav 34,48,54
ds_read_b128 v[62:65], v10 offset:1024
E 2,69632
s_add_u32 s51, s51, 196608
mav 38,52,54
ds_read_b128 v[66:69], v11 offset:1024
mva 42,54,56
ds_read_b128 v[70:73], v10 offset:5120
E 0,73728
mav 94,60,110
ds_read_b128 v[74:77], v11 offset:5120
mav 98,64,110
mva 102,110,68
s_add_u32 m0, s66, 8448
s_nop 0
buffer_load_dwordx4 v3, s[44:47], 0 offen offset:768 lds
mav 22,60,50
mav 26,64,50
E 1,77824
mva 30,50,68
mav 34,60,58
E 2,81920
s_add_u32 s51, s51, 196608
mav 38,64,58
mva 42,58,68
s_waitcnt lgkmcnt(0)
s_waitcnt vmcnt(14)
s_barrier
v_add_u32 v4, 49152, v4
v_add_u32 v5, 49152, v5
v_add_u32 v6, 49152, v6
E 0,36864
Ta 96,4,40960
mav 94,72,110
mav 98,76,110
Ta 100,5,40960
mva 102,110,80
Ta 104,6,40960
E 1,40960
mav 22,72,62
Ta 108,4,53248
mav 26,76,62
Ta 112,5,53248
mva 30,62,80
Ta 116,6,53248
E 2,45056
s_add_u32 s51, s51, 196608
mav 34,72,70
ds_read_b128 v[46:49], v8 offset:2048
mav 38,76,70
ds_read_b128 v[50:53], v9 offset:2048
mva 42,70,80
ds_read_b128 v[54:57], v8 offset:6144
E 0,49152
mav 94,84,110
ds_read_b128 v[58:61], v9 offset:6144
mav 98,88,110
mva 102,110,92
E 1,53248
mav 22,84,66
mav 26,88,66
mva 30,66,92
E 2,57344
s_add_u32 s51, s51, 196608
mav 34,84,74
mav 38,88,74
mva 42,74,92
s_waitcnt lgkmcnt(0)
s_waitcnt vmcnt(14)
s_barrier
E 0,110592
Ta 120,4,16384
mav 94,96,110
mav 98,100,110
Ta 124,5,16384
mva 102,110,104
Ta 128,6,16384
E 1,114688
mav 22,96,46
Ta 132,4,28672
mav 26,100,46
Ta 136,5,28672
s_add_u32 m0, s66, 1024
s_nop 0
buffer_load_dwordx4 v3, s[44:47], s67 offen offset:1024 lds
mva 30,46,104
Ta 140,6,28672
mav 34,96,54
ds_read_b128 v[62:65], v10 offset:2048
E 2,118784
s_add_u32 s51, s51, 196608
mav 38,100,54
ds_read_b128 v[66:69], v11 offset:2048
mva 42,54,104
ds_read_b128 v[70:73], v10 offset:6144
E 0,122880
mav 94,108,110
ds_read_b128 v[74:77], v11 offset:6144
mav 98,112,110
mva 102,110,116
s_add_u32 m0, s66, 5120
s_nop 0
buffer_load_dwordx4 v3, s[44:47], 0 offen offset:1024 lds
mav 22,108,50
mav 26,112,50
E 1,126976
mva 30,50,116
mav 34,108,58
E 2,131072
s_add_u32 s51, s51, 196608
mav 38,112,58
mva 42,58,116
s_waitcnt lgkmcnt(0)
s_waitcnt vmcnt(14)
s_barrier
v_add_u32 v4, -49152, v4
v_add_u32 v5, -49152, v5
v_add_u32 v6, -49152, v6
E 0,86016
Ta 144,4,40960
mav 94,120,110
mav 98,124,110
Ta 148,5,40960
mva 102,110,128
Ta 152,6,40960
E 1,90112
mav 22,120,62
Ta 156,4,53248
mav 26,124,62
Ta 160,5,53248
mva 30,62,128
Ta 164,6,53248
E 2,94208
s_add_u32 s51, s51, 196608
mav 34,120,70
ds_read_b128 v[46:49], v8 offset:3072
mav 38,124,70
ds_read_b128 v[50:53], v9 offset:3072
mva 42,70,128
ds_read_b128 v[54:57], v8 offset:7168
E 0,98304
mav 94,132,110
ds_read_b128 v[58:61], v9 offset:7168
mav 98,136,110
mva 102,110,140
E 1,102400
mav 22,132,66
mav 26,136,66
mva 30,66,140
E 2,106496
s_add_u32 s51, s51, 196608
mav 34,132,74
mav 38,136,74
mva 42,74,140
s_waitcnt lgkmcnt(0)
s_waitcnt vmcnt(14)
s_barrier
E 0,61440
Ta 168,4,16384
mav 94,144,110
mav 98,148,110
Ta 172,5,16384
mva 102,110,152
Ta 176,6,16384
E 1,65536
mav 22,144,46
Ta 180,4,28672
mav 26,148,46
Ta 184,5,28672
s_add_u32 m0, s66, 1792
s_nop 0
buffer_load_dwordx4 v3, s[44:47], s67 offen offset:1280 lds
mva 30,46,152
Ta 188,6,28672
mav 34,144,54
ds_read_b128 v[62:65], v10 offset:3072
E 2,69632
s_add_u32 s51, s51, 196608
mav 38,148,54
ds_read_b128 v[66:69], v11 offset:3072
mva 42,54,152
ds_read_b128 v[70:73], v10 offset:7168
E 0,73728
mav 94,156,110
ds_read_b128 v[74:77], v11 offset:7168
mav 98,160,110
mva 102,110,164
s_add_u32 m0, s66, 5888
s_nop 0
buffer_load_dwordx4 v3, s[44:47], 0 offen offset:1280 lds
mav 22,156,50
mav 26,160,50
E 1,77824
mva 30,50,164
mav 34,156,58
E 2,81920
s_add_u32 s51, s51, 196608
mav 38,160,58
mva 42,58,164
s_waitcnt lgkmcnt(0)
s_waitcnt vmcnt(14)
s_barrier
v_add_u32 v4, 49152, v4
v_add_u32 v5, 49152, v5
v_add_u32 v6, 49152, v6
E 0,36864
Ta 192,4,40960
mav 94,168,110
mav 98,172,110
Ta 196,5,40960
mva 102,110,176
Ta 200,6,40960
E 1,40960
mav 22,168,62
Ta 204,4,53248
mav 26,172,62
Ta 208,5,53248
mva 30,62,176
Ta 212,6,53248
E 2,45056
s_add_u32 s51, s51, 196608
mav 34,168,70
ds_read_b128 v[46:49], v8 offset:0
mav 38,172,70
ds_read_b128 v[50:53], v9 offset:0
mva 42,70,176
ds_read_b128 v[54:57], v8 offset:4096
E 0,49152
mav 94,180,110
ds_read_b128 v[58:61], v9 offset:4096
mav 98,184,110
mva 102,110,188
E 1,53248
mav 22,180,66
mav 26,184,66
mva 30,66,188
E 2,57344
s_add_u32 s51, s51, 196608
mav 34,180,74
mav 38,184,74
mva 42,74,188
s_waitcnt lgkmcnt(0)
s_waitcnt vmcnt(14)
s_barrier
E 0,110592
Ta 216,4,16384
mav 94,192,110
mav 98,196,110
Ta 220,5,16384
mva 102,110,200
Ta 224,6,16384
E 1,114688
mav 22,192,46
Ta 228,4,28672
mav 26,196,46
Ta 232,5,28672
s_add_u32 m0, s66, 2560
s_nop 0
buffer_load_dwordx4 v3, s[44:47], s67 offen offset:1536 lds
mva 30,46,200
Ta 236,6,28672
mav 34,192,54
ds_read_b128 v[62:65], v10 offset:0
E 2,118784
s_add_u32 s51, s51, 196608
mav 38,196,54
ds_read_b128 v[66:69], v11 offset:0
mva 42,54,200
ds_read_b128 v[70:73], v10 offset:4096
E 0,122880
mav 94,204,110
ds_read_b128 v[74:77], v11 offset:4096
mav 98,208,110
mva 102,110,212
s_add_u32 m0, s66, 6656
s_nop 0
buffer_load_dwordx4 v3, s[44:47], 0 offen offset:1536 lds
mav 22,204,50
mav 26,208,50
E 1,126976
mva 30,50,212
mav 34,204,58
E 2,131072
s_add_u32 s51, s51, 196608
mav 38,208,58
mva 42,58,212
s_waitcnt lgkmcnt(0)
s_waitcnt vmcnt(14)
s_barrier
v_add_u32 v4, -49152, v4
v_add_u32 v5, -49152, v5
v_add_u32 v6, -49152, v6
E 0,86016
Ta 240,4,40960
mav 94,216,110
mav 98,220,110
Ta 244,5,40960
mva 102,110,224
Ta 248,6,40960
E 1,90112
mav 22,216,62
Ta 252,4,53248
mav 26,220,62
Tv 128,5,53248
mva 30,62,224
E 2,94208
s_add_u32 s51, s51, 196608
Tv 132,6,53248
mav 34,216,70
ds_read_b128 v[46:49], v8 offset:1024
mav 38,220,70
ds_read_b128 v[50:53], v9 offset:1024
E 0,98304
mva 42,70,224
ds_read_b128 v[54:57], v8 offset:5120
mav 94,228,110
ds_read_b128 v[58:61], v9 offset:5120
mav 98,232,110
E 1,102400
mva 102,110,236
mav 22,228,66
E 2,106496
s_add_u32 s51, s51, 196608
mav 26,232,66
mva 30,66,236
mav 34,228,74
buffer_load_dwordx2 v[122:123], v91, s[36:39], 0 offen sc1
mav 38,232,74
mva 42,74,236
s_waitcnt lgkmcnt(0)
s_waitcnt vmcnt(15)
s_barrier
E 0,61440
Tv 136,4,16384
mav 94,240,110
mav 98,244,110
Tv 140,5,16384
mva 102,110,248
Tv 144,6,16384
E 1,65536
mav 22,240,46
Tv 148,4,28672
mav 26,244,46
Tv 152,5,28672
s_add_u32 m0, s66, 3328
s_nop 0
buffer_load_dwordx4 v3, s[44:47], s67 offen offset:1792 lds
mva 30,46,248
Tv 156,6,28672
mav 34,240,54
ds_read_b128 v[62:65], v10 offset:1024
E 2,69632
s_add_u32 s51, s51, 196608
mav 38,244,54
ds_read_b128 v[66:69], v11 offset:1024
mva 42,54,248
ds_read_b128 v[70:73], v10 offset:5120
E 0,73728
mav 94,252,110
ds_read_b128 v[74:77], v11 offset:5120
mvv 98,128,110
mvv 102,110,132
s_add_u32 m0, s66, 7424
s_nop 0
buffer_load_dwordx4 v3, s[44:47], 0 offen offset:1792 lds
mav 22,252,50
mvv 26,128,50
E 1,77824
mvv 30,50,132
mav 34,252,58
E 2,81920
s_add_u32 s51, s51, 196608
mvv 38,128,58
mvv 42,58,132
s_waitcnt lgkmcnt(0)
s_waitcnt vmcnt(15)
s_barrier
v_add_u32 v4, 49152, v4
v_add_u32 v5, 49152, v5
v_add_u32 v6, 49152, v6
E 0,36864
Tv 160,4,40960
mvv 94,136,110
mvv 98,140,110
Tv 164,5,40960
mvv 102,110,144
Tv 168,6,40960
E 1,40960
mvv 22,136,62
Tv 172,4,53248
mvv 26,140,62
Tv 176,5,53248
mvv 30,62,144
Tv 180,6,53248
E 2,45056
s_add_u32 s51, s51, 196608
mvv 34,136,70
ds_read_b128 v[46:49], v8 offset:2048
mvv 38,140,70
ds_read_b128 v[50:53], v9 offset:2048
mvv 42,70,144
ds_read_b128 v[54:57], v8 offset:6144
E 0,49152
mvv 94,148,110
ds_read_b128 v[58:61], v9 offset:6144
mvv 98,152,110
mvv 102,110,156
E 1,53248
mvv 22,148,66
mvv 26,152,66
mvv 30,66,156
E 2,57344
s_add_u32 s51, s51, 196608
mvv 34,148,74
mvv 38,152,74
mvv 42,74,156
s_waitcnt lgkmcnt(0)
s_waitcnt vmcnt(15)
s_barrier
Tv 184,4,16384
Tv 188,5,16384
Tv 192,6,16384
Tv 196,4,28672
Tv 200,5,28672
Tv 204,6,28672
ds_read_b128 v[62:65], v10 offset:2048
ds_read_b128 v[66:69], v11 offset:2048
ds_read_b128 v[70:73], v10 offset:6144
ds_read_b128 v[74:77], v11 offset:6144
s_waitcnt vmcnt(14)
ds_write_b32 v82, v122
v_sub_u32 v122, v122, v92
v_xor_b32 v123, v123, v115
v_cmp_lt_u64 vcc, s[62:63], v[122:123]
s_cbranch_vccnz Cq
Bq:
v_add_u32 v92, 1, v92
s_add_u32 s60, s49, 346112
s_add_u32 s56, s54, 692224
v_add_u32 v12, s55, v84
buffer_load_dwordx2 v[14:15], v93, s[36:39], s54 offen sc1
mvv 94,160,110
mvv 98,164,110
buffer_load_dwordx2 v[18:19], v93, s[36:39], s56 offen sc1
mvv 102,110,168
mvv 22,160,46
s_add_u32 m0, s50, 49152
s_nop 0
buffer_load_dwordx4 v88, s[36:39], s49 offen offset:0 sc1 lds
mvv 26,164,46
mvv 30,46,168
buffer_load_dwordx4 v88, s[36:39], s49 offen offset:1024 sc1 lds
mvv 34,160,54
mvv 38,164,54
buffer_load_dwordx4 v88, s[36:39], s49 offen offset:2048 sc1 lds
mvv 42,54,168
buffer_load_dwordx4 v88, s[36:39], s49 offen offset:3072 sc1 lds
mvv 94,172,110
mvv 98,176,110
s_add_u32 m0, s50, 57344
s_nop 0
buffer_load_dwordx4 v89, s[36:39], s49 offen offset:0 sc1 lds
mvv 102,110,180
mvv 22,172,50
buffer_load_dwordx4 v89, s[36:39], s49 offen offset:1024 sc1 lds
mvv 26,176,50
mvv 30,50,180
buffer_load_dwordx4 v89, s[36:39], s49 offen offset:2048 sc1 lds
mvv 34,172,58
mvv 38,176,58
buffer_load_dwordx4 v89, s[36:39], s49 offen offset:3072 sc1 lds
mvv 42,58,180
s_waitcnt lgkmcnt(0)
s_waitcnt vmcnt(16)
s_barrier
v_add_u32 v4, -49152, v4
v_add_u32 v5, -49152, v5
v_add_u32 v6, -49152, v6
s_add_u32 m0, s50, 32768
s_nop 0
buffer_load_dwordx4 v86, s[36:39], s49 offen offset:0 sc1 lds
Tv 208,4,40960
mvv 94,184,110
mvv 98,188,110
Tv 212,5,40960
mvv 102,110,192
Tv 216,6,40960
buffer_load_dwordx4 v86, s[36:39], s49 offen offset:1024 sc1 lds
mvv 22,184,62
Tv 220,4,53248
mvv 26,188,62
Tv 224,5,53248
buffer_load_dwordx4 v86, s[36:39], s49 offen offset:2048 sc1 lds
mvv 30,62,192
Tv 228,6,53248
mvv 34,184,70
ds_read_b128 v[46:49], v8 offset:3072
buffer_load_dwordx4 v86, s[36:39], s49 offen offset:3072 sc1 lds
mvv 38,188,70
ds_read_b128 v[50:53], v9 offset:3072
mvv 42,70,192
ds_read_b128 v[54:57], v8 offset:7168
s_add_u32 m0, s50, 40960
s_nop 0
buffer_load_dwordx4 v87, s[36:39], s49 offen offset:0 sc1 lds
mvv 94,196,110
ds_read_b128 v[58:61], v9 offset:7168
mvv 98,200,110
mvv 102,110,204
buffer_load_dwordx4 v87, s[36:39], s49 offen offset:1024 sc1 lds
mvv 22,196,66
mvv 26,200,66
buffer_load_dwordx4 v87, s[36:39], s49 offen offset:2048 sc1 lds
mvv 30,66,204
mvv 34,196,74
buffer_load_dwordx4 v87, s[36:39], s49 offen offset:3072 sc1 lds
mvv 38,200,74
mvv 42,74,204
s_waitcnt lgkmcnt(0)
s_waitcnt vmcnt(18)
s_barrier
s_waitcnt vmcnt(16)
s_add_u32 m0, s50, 0
s_nop 0
buffer_load_dwordx4 v86, s[36:39], s60 offen offset:0 sc1 lds
Tv 232,4,16384
mvv 94,208,110
v_permlane32_swap_b32 v14, v15
mvv 98,212,110
Tv 236,5,16384
v_add_f32 v16, v14, v15
mvv 102,110,216
Tv 240,6,16384
v_add_f32 v17, v14, v15
buffer_load_dwordx4 v86, s[36:39], s60 offen offset:1024 sc1 lds
mvv 22,208,46
Tv 244,4,28672
s_nop 1
v_permlane16_swap_b32 v16, v17
mvv 26,212,46
Tv 248,5,28672
v_add_f32 v14, v16, v17
buffer_load_dwordx4 v86, s[36:39], s60 offen offset:2048 sc1 lds
mvv 30,46,216
Tv 252,6,28672
v_add_f32 v15, v16, v17
mvv 34,208,54
ds_read_b128 v[62:65], v10 offset:3072
s_nop 1
v_permlane32_swap_b32 v14, v15
buffer_load_dwordx4 v86, s[36:39], s60 offen offset:3072 sc1 lds
mvv 38,212,54
v_mul_f32 v16, 0x3a800000, v14
ds_read_b128 v[66:69], v11 offset:3072
mvv 42,54,216
ds_read_b128 v[70:73], v10 offset:7168
v_mul_f32 v17, 0x3a800000, v15
s_add_u32 m0, s50, 8192
s_nop 0
buffer_load_dwordx4 v87, s[36:39], s60 offen offset:0 sc1 lds
mvv 94,220,110
ds_read_b128 v[74:77], v11 offset:7168
v_fma_f32 v17, -v16, v16, v17
mvv 98,224,110
v_max_f32 v17, 0, v17
mvv 102,110,228
buffer_load_dwordx4 v87, s[36:39], s60 offen offset:1024 sc1 lds
v_add_f32 v17, 0x3727c5ac, v17
mvv 22,220,50
v_rsq_f32 v14, v17
mvv 26,224,50
buffer_load_dwordx4 v87, s[36:39], s60 offen offset:2048 sc1 lds
mvv 30,50,228
s_nop 0
v_mul_f32_e64 v15, -v16, v14
mvv 34,220,58
ds_write_b64 v12, v[14:15]
buffer_load_dwordx4 v87, s[36:39], s60 offen offset:3072 sc1 lds
mvv 38,224,58
mvv 42,58,228
s_waitcnt lgkmcnt(0)
s_add_u32 m0, s50, 16384
s_nop 0
buffer_load_dwordx4 v88, s[36:39], s60 offen offset:0 sc1 lds
mvv 94,232,110
v_permlane32_swap_b32 v18, v19
mvv 98,236,110
v_add_f32 v20, v18, v19
mvv 102,110,240
buffer_load_dwordx4 v88, s[36:39], s60 offen offset:1024 sc1 lds
v_add_f32 v21, v18, v19
mvv 22,232,62
s_nop 1
v_permlane16_swap_b32 v20, v21
mvv 26,236,62
buffer_load_dwordx4 v88, s[36:39], s60 offen offset:2048 sc1 lds
mvv 30,62,240
v_add_f32 v18, v20, v21
mvv 34,232,70
v_add_f32 v19, v20, v21
buffer_load_dwordx4 v88, s[36:39], s60 offen offset:3072 sc1 lds
mvv 38,236,70
s_nop 1
v_permlane32_swap_b32 v18, v19
mvv 42,70,240
s_add_u32 m0, s50, 24576
s_nop 0
buffer_load_dwordx4 v89, s[36:39], s60 offen offset:0 sc1 lds
mvv 94,244,110
v_mul_f32 v20, 0x3a800000, v18
mvv 98,248,110
v_mul_f32 v21, 0x3a800000, v19
mvv 102,110,252
buffer_load_dwordx4 v89, s[36:39], s60 offen offset:1024 sc1 lds
v_fma_f32 v21, -v20, v20, v21
mvv 22,244,66
v_max_f32 v21, 0, v21
mvv 26,248,66
buffer_load_dwordx4 v89, s[36:39], s60 offen offset:2048 sc1 lds
mvv 30,66,252
v_add_f32 v21, 0x3727c5ac, v21
mvv 34,244,74
v_rsq_f32 v18, v21
buffer_load_dwordx4 v89, s[36:39], s60 offen offset:3072 sc1 lds
mvv 38,248,74
s_nop 0
v_mul_f32_e64 v19, -v20, v18
mvv 42,74,252
s_waitcnt vmcnt(16)
s_barrier
s_nop 7
v_mov_b32 v116, v102
s_and_b32 s60, s48, 3
s_cmp_eq_u32 s60, 1
s_cbranch_scc1 LvQB1
LvQB0:
v_xor_b32 v80, 0x10000, v80
ds_read_b64 v[118:119], v84 offset:0
ds_read_b128 v[102:105], v85 offset:0
ds_read_b128 v[106:109], v85 offset:16
s_add_u32 s49, s49, 346112
R 46,32768
R 50,33792
R 54,34816
R 58,35840
s_waitcnt lgkmcnt(2)
v_mul_f32 v120, v119, v94
v_fma_f32 v22, v22, v118, v120
v_mul_f32 v121, v119, v95
v_fma_f32 v23, v23, v118, v121
v_mul_f32 v120, v119, v96
v_fma_f32 v24, v24, v118, v120
v_mul_f32 v121, v119, v97
v_fma_f32 v25, v25, v118, v121
v_mul_f32 v120, v119, v98
v_fma_f32 v26, v26, v118, v120
v_mul_f32 v121, v119, v99
v_fma_f32 v27, v27, v118, v121
v_mul_f32 v120, v119, v100
v_fma_f32 v28, v28, v118, v120
v_mul_f32 v121, v119, v101
v_fma_f32 v29, v29, v118, v121
v_cvt_pk_bf16_f32 v110, v22, v23
v_cvt_pk_bf16_f32 v111, v24, v25
v_cvt_pk_bf16_f32 v112, v26, v27
v_cvt_pk_bf16_f32 v113, v28, v29
ds_write_b128 v83, v[110:113] offset:0
v_mul_f32 v103, v103, v116
v_fma_f32 v30, v30, v102, v103
v_mul_f32 v105, v105, v116
v_fma_f32 v31, v31, v104, v105
v_mul_f32 v107, v107, v116
v_fma_f32 v32, v32, v106, v107
v_mul_f32 v109, v109, v116
v_fma_f32 v33, v33, v108, v109
v_cvt_pk_bf16_f32 v78, v30, v31
v_cvt_pk_bf16_f32 v79, v32, v33
ds_write_b64 v83, v[78:79] offset:1024
s_xor_b32 s50, s50, 0x10000
s_add_u32 s49, s49, 346112
s_waitcnt lgkmcnt(0)
s_barrier
ds_write_b64 v84, v[18:19] offset:0
ds_read_b64 v[118:119], v84 offset:128
ds_read_b128 v[102:105], v85 offset:128
ds_read_b128 v[106:109], v85 offset:144
v_mfma_f32_16x16x32_bf16 v[22:25], a[0:3], v[46:49], 0
R 62,36864
v_mfma_f32_16x16x32_bf16 v[26:29], a[4:7], v[46:49], 0
s_waitcnt lgkmcnt(1)
v_mul_f32 v120, v119, v94
v_mfma_f32_16x16x32_bf16 v[30:33], v[46:49], a[8:11], 0
v_fma_f32 v34, v34, v118, v120
v_mul_f32 v121, v119, v95
mav 22,12,50
R 66,37888
v_fma_f32 v35, v35, v118, v121
mav 26,16,50
v_mul_f32 v120, v119, v96
v_fma_f32 v36, v36, v118, v120
mva 30,50,20
v_mul_f32 v121, v119, v97
v_fma_f32 v37, v37, v118, v121
mav 22,24,54
R 70,38912
v_mul_f32 v120, v119, v98
mav 26,28,54
v_fma_f32 v38, v38, v118, v120
v_mul_f32 v121, v119, v99
mva 30,54,32
v_fma_f32 v39, v39, v118, v121
v_mul_f32 v120, v119, v100
mav 22,36,58
R 74,39936
v_fma_f32 v40, v40, v118, v120
mav 26,40,58
v_mul_f32 v121, v119, v101
v_fma_f32 v41, v41, v118, v121
mva 30,58,44
v_cvt_pk_bf16_f32 v110, v34, v35
s_waitcnt lgkmcnt(2)
mav 22,48,62
R 46,40960
v_cvt_pk_bf16_f32 v111, v36, v37
mav 26,52,62
v_cvt_pk_bf16_f32 v112, v38, v39
v_cvt_pk_bf16_f32 v113, v40, v41
mva 30,62,56
ds_write_b128 v83, v[110:113] offset:4096
mav 22,60,66
R 50,41984
v_mul_f32 v103, v103, v116
mav 26,64,66
v_fma_f32 v42, v42, v102, v103
v_mul_f32 v105, v105, v116
mva 30,66,68
v_fma_f32 v43, v43, v104, v105
s_waitcnt lgkmcnt(3)
mav 22,72,70
R 54,43008
v_mul_f32 v107, v107, v116
mav 26,76,70
v_fma_f32 v44, v44, v106, v107
v_mul_f32 v109, v109, v116
mva 30,70,80
v_fma_f32 v45, v45, v108, v109
v_cvt_pk_bf16_f32 v78, v42, v43
mav 22,84,74
R 58,44032
v_cvt_pk_bf16_f32 v79, v44, v45
mav 26,88,74
ds_write_b64 v83, v[78:79] offset:5120
mva 30,74,92
s_waitcnt lgkmcnt(3)
mav 22,96,46
R 62,45056
s_mov_b32 m0, 142336
mav 26,100,46
s_waitcnt vmcnt(0) lgkmcnt(1)
s_barrier
mva 30,46,104
buffer_load_dword v90, s[36:39], 0 offen sc1 lds
mav 22,108,50
R 66,46080
s_add_u32 m0, s50, 32768
mav 26,112,50
mva 30,50,116
D 86,0
mav 22,120,54
R 70,47104
mav 26,124,54
D 86,1024
mva 30,54,128
mav 22,132,58
R 74,48128
D 86,2048
mav 26,136,58
mva 30,58,140
D 86,3072
s_waitcnt lgkmcnt(2)
mav 22,144,62
R 46,49152
s_add_u32 m0, s50, 40960
mav 26,148,62
D 87,0
mva 30,62,152
mav 22,156,66
R 50,50176
D 87,1024
mav 26,160,66
mva 30,66,164
D 87,2048
s_waitcnt lgkmcnt(2)
mav 22,168,70
R 54,51200
mav 26,172,70
D 87,3072
s_add_u32 m0, s50, 49152
mva 30,70,176
mav 22,180,74
R 58,52224
D 88,0
mav 26,184,74
mva 30,74,188
D 88,1024
s_waitcnt lgkmcnt(2)
mav 22,192,46
R 62,53248
mav 26,196,46
D 88,2048
mva 30,46,200
mav 22,204,50
R 66,54272
D 88,3072
mav 26,208,50
s_add_u32 m0, s50, 57344
mva 30,50,212
D 89,0
s_waitcnt lgkmcnt(2)
mav 22,216,54
R 70,55296
mav 26,220,54
D 89,1024
mva 30,54,224
mav 22,228,58
R 74,56320
D 89,2048
mav 26,232,58
mva 30,58,236
D 89,3072
s_waitcnt lgkmcnt(2)
mav 22,240,62
R 46,57344
s_add_u32 s49, s49, 346112
mav 26,244,62
s_add_u32 m0, s50, 0
mva 30,62,248
D 86,0
mav 22,252,66
R 50,58368
mvv 26,128,66
D 86,1024
mvv 30,66,132
s_waitcnt lgkmcnt(2)
mvv 22,136,70
R 54,59392
D 86,2048
mvv 26,140,70
mvv 30,70,144
D 86,3072
s_add_u32 m0, s50, 8192
mvv 22,148,74
R 58,60416
mvv 26,152,74
D 87,0
mvv 30,74,156
s_waitcnt lgkmcnt(2)
mvv 22,160,46
R 62,61440
D 87,1024
mvv 26,164,46
mvv 30,46,168
D 87,2048
mvv 22,172,50
R 66,62464
mvv 26,176,50
D 87,3072
s_add_u32 m0, s50, 16384
mvv 30,50,180
s_waitcnt lgkmcnt(2)
mvv 22,184,54
R 70,63488
D 88,0
mvv 26,188,54
mvv 30,54,192
D 88,1024
mvv 22,196,58
R 74,64512
mvv 26,200,58
D 88,2048
mvv 30,58,204
s_waitcnt lgkmcnt(2)
mvv 22,208,62
R 46,0
D 88,3072
mvv 26,212,62
ds_read_b32 v122, v82 offset:0
s_add_u32 m0, s50, 24576
mvv 30,62,216
ds_read_b64 v[118:119], v84 offset:0
ds_read_b128 v[102:105], v85 offset:0
mvv 22,220,66
R 50,1024
ds_read_b128 v[106:109], v85 offset:16
mvv 26,224,66
D 89,0
mvv 30,66,228
s_waitcnt lgkmcnt(6)
mvv 22,232,70
R 54,2048
D 89,1024
mvv 26,236,70
mvv 30,70,240
D 89,2048
mvv 22,244,74
R 58,3072
mvv 26,248,74
s_waitcnt lgkmcnt(2)
v_cmp_gt_i32 vcc, v92, v122
mvv 30,74,252
s_cbranch_vccnz CQB0_1
BQB0_1:
D 89,3072
v_mfma_f32_16x16x32_bf16 v[34:37], a[0:3], v[46:49], 0
R 62,4096
v_mul_f32 v120, v119, v94
v_mfma_f32_16x16x32_bf16 v[38:41], a[4:7], v[46:49], 0
v_fma_f32 v22, v22, v118, v120
v_mul_f32 v121, v119, v95
v_mfma_f32_16x16x32_bf16 v[42:45], v[46:49], a[8:11], 0
v_fma_f32 v23, v23, v118, v121
v_mul_f32 v120, v119, v96
mav 34,12,50
R 66,5120
v_fma_f32 v24, v24, v118, v120
mav 38,16,50
v_mul_f32 v121, v119, v97
v_fma_f32 v25, v25, v118, v121
mva 42,50,20
v_mul_f32 v120, v119, v98
s_waitcnt lgkmcnt(2)
mav 34,24,54
R 70,6144
v_fma_f32 v26, v26, v118, v120
mav 38,28,54
v_mul_f32 v121, v119, v99
v_fma_f32 v27, v27, v118, v121
mva 42,54,32
v_mul_f32 v120, v119, v100
v_fma_f32 v28, v28, v118, v120
mav 34,36,58
R 74,7168
v_mul_f32 v121, v119, v101
mav 38,40,58
v_fma_f32 v29, v29, v118, v121
v_cvt_pk_bf16_f32 v110, v22, v23
mva 42,58,44
v_cvt_pk_bf16_f32 v111, v24, v25
s_waitcnt lgkmcnt(2)
mav 34,48,62
R 46,8192
v_cvt_pk_bf16_f32 v112, v26, v27
mav 38,52,62
v_cvt_pk_bf16_f32 v113, v28, v29
v_mul_f32 v103, v103, v116
mva 42,62,56
ds_write_b128 v83, v[110:113] offset:0
mav 34,60,66
R 50,9216
v_fma_f32 v30, v30, v102, v103
mav 38,64,66
v_mul_f32 v105, v105, v116
v_fma_f32 v31, v31, v104, v105
mva 42,66,68
v_mul_f32 v107, v107, v116
s_waitcnt lgkmcnt(3)
mav 34,72,70
R 54,10240
v_fma_f32 v32, v32, v106, v107
mav 38,76,70
v_mul_f32 v109, v109, v116
v_fma_f32 v33, v33, v108, v109
mva 42,70,80
s_xor_b32 s50, s50, 0x10000
v_cvt_pk_bf16_f32 v78, v30, v31
mav 34,84,74
R 58,11264
v_cvt_pk_bf16_f32 v79, v32, v33
mav 38,88,74
ds_write_b64 v83, v[78:79] offset:1024
mva 42,74,92
s_waitcnt lgkmcnt(3)
mav 34,96,46
R 62,12288
s_mov_b32 m0, 142336
mav 38,100,46
s_waitcnt vmcnt(16) lgkmcnt(1)
s_barrier
mva 42,46,104
s_add_u32 s49, s49, 346112
buffer_load_dword v90, s[36:39], 0 offen sc1 lds
mav 34,108,50
R 66,13312
s_add_u32 m0, s50, 32768
mav 38,112,50
mva 42,50,116
D 86,0
mav 34,120,54
R 70,14336
mav 38,124,54
D 86,1024
mva 42,54,128
mav 34,132,58
R 74,15360
mav 38,136,58
D 86,2048
mva 42,58,140
s_waitcnt lgkmcnt(2)
mav 34,144,62
R 46,16384
D 86,3072
mav 38,148,62
s_add_u32 m0, s50, 40960
mva 42,62,152
mav 34,156,66
R 50,17408
D 87,0
mav 38,160,66
v_add_u32 v92, 1, v92
mva 42,66,164
D 87,1024
s_waitcnt lgkmcnt(2)
mav 34,168,70
R 54,18432
mav 38,172,70
mva 42,70,176
D 87,2048
mav 34,180,74
R 58,19456
mav 38,184,74
D 87,3072
mva 42,74,188
s_add_u32 m0, s50, 49152
s_waitcnt lgkmcnt(2)
mav 34,192,46
R 62,20480
mav 38,196,46
D 88,0
mva 42,46,200
mav 34,204,50
R 66,21504
D 88,1024
mav 38,208,50
mva 42,50,212
s_waitcnt lgkmcnt(2)
mav 34,216,54
R 70,22528
D 88,2048
mav 38,220,54
mva 42,54,224
D 88,3072
mav 34,228,58
R 74,23552
s_add_u32 m0, s50, 57344
mav 38,232,58
mva 42,58,236
D 89,0
s_waitcnt lgkmcnt(2)
mav 34,240,62
R 46,24576
mav 38,244,62
D 89,1024
mva 42,62,248
mav 34,252,66
R 50,25600
mvv 38,128,66
D 89,2048
mvv 42,66,132
s_waitcnt lgkmcnt(2)
mvv 34,136,70
R 54,26624
D 89,3072
mvv 38,140,70
mvv 42,70,144
mvv 34,148,74
R 58,27648
mvv 38,152,74
mvv 42,74,156
s_waitcnt lgkmcnt(2)
mvv 34,160,46
R 62,28672
mvv 38,164,46
mvv 42,46,168
mvv 34,172,50
R 66,29696
mvv 38,176,50
mvv 42,50,180
s_waitcnt lgkmcnt(2)
mvv 34,184,54
R 70,30720
mvv 38,188,54
mvv 42,54,192
mvv 34,196,58
R 74,31744
v_xor_b32 v80, 0x10000, v80
mvv 38,200,58
mvv 42,58,204
s_waitcnt lgkmcnt(2)
mvv 34,208,62
R 46,32768
mvv 38,212,62
ds_read_b32 v122, v82 offset:0
ds_read_b64 v[118:119], v84 offset:128
mvv 42,62,216
ds_read_b128 v[102:105], v85 offset:128
ds_read_b128 v[106:109], v85 offset:144
mvv 34,220,66
R 50,33792
mvv 38,224,66
mvv 42,66,228
s_waitcnt lgkmcnt(6)
mvv 34,232,70
R 54,34816
mvv 38,236,70
mvv 42,70,240
mvv 34,244,74
R 58,35840
mvv 38,248,74
s_waitcnt lgkmcnt(2)
v_cmp_gt_i32 vcc, v92, v122
mvv 42,74,252
s_mov_b32 s60, -28
.p2align 6
LQB0_2:
v_mfma_f32_16x16x32_bf16 v[22:25], a[0:3], v[46:49], 0
s_cbranch_vccnz CQB0_3
BQB0_3:
R 62,36864
v_mfma_f32_16x16x32_bf16 v[26:29], a[4:7], v[46:49], 0
v_mul_f32 v120, v119, v94
v_fma_f32 v34, v34, v118, v120
v_mfma_f32_16x16x32_bf16 v[30:33], v[46:49], a[8:11], 0
v_mul_f32 v121, v119, v95
v_fma_f32 v35, v35, v118, v121
mav 22,12,50
R 66,37888
v_mul_f32 v120, v119, v96
mav 26,16,50
v_fma_f32 v36, v36, v118, v120
v_mul_f32 v121, v119, v97
mva 30,50,20
v_fma_f32 v37, v37, v118, v121
s_waitcnt lgkmcnt(2)
mav 22,24,54
R 70,38912
v_mul_f32 v120, v119, v98
mav 26,28,54
v_fma_f32 v38, v38, v118, v120
v_mul_f32 v121, v119, v99
mva 30,54,32
v_fma_f32 v39, v39, v118, v121
v_mul_f32 v120, v119, v100
mav 22,36,58
R 74,39936
v_fma_f32 v40, v40, v118, v120
mav 26,40,58
v_mul_f32 v121, v119, v101
v_fma_f32 v41, v41, v118, v121
mva 30,58,44
v_cvt_pk_bf16_f32 v110, v34, v35
s_waitcnt lgkmcnt(2)
mav 22,48,62
R 46,40960
v_cvt_pk_bf16_f32 v111, v36, v37
mav 26,52,62
v_cvt_pk_bf16_f32 v112, v38, v39
v_cvt_pk_bf16_f32 v113, v40, v41
mva 30,62,56
ds_write_b128 v83, v[110:113] offset:4096
mav 22,60,66
R 50,41984
v_mul_f32 v103, v103, v116
mav 26,64,66
v_fma_f32 v42, v42, v102, v103
v_mul_f32 v105, v105, v116
mva 30,66,68
v_fma_f32 v43, v43, v104, v105
s_waitcnt lgkmcnt(3)
mav 22,72,70
R 54,43008
v_mul_f32 v107, v107, v116
mav 26,76,70
v_fma_f32 v44, v44, v106, v107
v_mul_f32 v109, v109, v116
mva 30,70,80
v_fma_f32 v45, v45, v108, v109
v_cvt_pk_bf16_f32 v78, v42, v43
mav 22,84,74
R 58,44032
v_cvt_pk_bf16_f32 v79, v44, v45
mav 26,88,74
ds_write_b64 v83, v[78:79] offset:5120
mva 30,74,92
s_waitcnt lgkmcnt(3)
mav 22,96,46
R 62,45056
s_mov_b32 m0, 142336
mav 26,100,46
s_waitcnt vmcnt(17) lgkmcnt(1)
s_barrier
mva 30,46,104
s_add_u32 s49, s49, 346112
buffer_load_dword v90, s[36:39], 0 offen sc1 lds
mav 22,108,50
R 66,46080
s_add_u32 m0, s50, 0
mav 26,112,50
mva 30,50,116
D 86,0
mav 22,120,54
R 70,47104
mav 26,124,54
D 86,1024
mva 30,54,128
mav 22,132,58
R 74,48128
mav 26,136,58
D 86,2048
mva 30,58,140
s_waitcnt lgkmcnt(2)
mav 22,144,62
R 46,49152
D 86,3072
mav 26,148,62
s_add_u32 m0, s50, 8192
mva 30,62,152
mav 22,156,66
R 50,50176
D 87,0
mav 26,160,66
v_add_u32 v92, 1, v92
mva 30,66,164
D 87,1024
s_waitcnt lgkmcnt(2)
mav 22,168,70
R 54,51200
mav 26,172,70
mva 30,70,176
D 87,2048
mav 22,180,74
R 58,52224
mav 26,184,74
D 87,3072
mva 30,74,188
s_add_u32 m0, s50, 16384
s_waitcnt lgkmcnt(2)
mav 22,192,46
R 62,53248
mav 26,196,46
D 88,0
mva 30,46,200
mav 22,204,50
R 66,54272
D 88,1024
mav 26,208,50
mva 30,50,212
s_waitcnt lgkmcnt(2)
mav 22,216,54
R 70,55296
D 88,2048
mav 26,220,54
mva 30,54,224
D 88,3072
mav 22,228,58
R 74,56320
s_add_u32 m0, s50, 24576
mav 26,232,58
mva 30,58,236
D 89,0
s_waitcnt lgkmcnt(2)
mav 22,240,62
R 46,57344
mav 26,244,62
D 89,1024
mva 30,62,248
mav 22,252,66
R 50,58368
mvv 26,128,66
D 89,2048
mvv 30,66,132
s_waitcnt lgkmcnt(2)
mvv 22,136,70
R 54,59392
D 89,3072
mvv 26,140,70
mvv 30,70,144
mvv 22,148,74
R 58,60416
mvv 26,152,74
mvv 30,74,156
s_waitcnt lgkmcnt(2)
mvv 22,160,46
R 62,61440
mvv 26,164,46
mvv 30,46,168
mvv 22,172,50
R 66,62464
mvv 26,176,50
mvv 30,50,180
s_waitcnt lgkmcnt(2)
mvv 22,184,54
R 70,63488
mvv 26,188,54
mvv 30,54,192
mvv 22,196,58
R 74,64512
mvv 26,200,58
mvv 30,58,204
s_waitcnt lgkmcnt(2)
mvv 22,208,62
R 46,0
mvv 26,212,62
ds_read_b32 v122, v82 offset:0
ds_read_b64 v[118:119], v84 offset:0
mvv 30,62,216
ds_read_b128 v[102:105], v85 offset:0
ds_read_b128 v[106:109], v85 offset:16
mvv 22,220,66
R 50,1024
mvv 26,224,66
mvv 30,66,228
s_waitcnt lgkmcnt(6)
mvv 22,232,70
R 54,2048
mvv 26,236,70
mvv 30,70,240
mvv 22,244,74
R 58,3072
mvv 26,248,74
s_waitcnt lgkmcnt(2)
v_cmp_gt_i32 vcc, v92, v122
mvv 30,74,252
s_cbranch_vccnz CQB0_4
BQB0_4:
v_mfma_f32_16x16x32_bf16 v[34:37], a[0:3], v[46:49], 0
R 62,4096
v_mul_f32 v120, v119, v94
v_mfma_f32_16x16x32_bf16 v[38:41], a[4:7], v[46:49], 0
v_fma_f32 v22, v22, v118, v120
v_mul_f32 v121, v119, v95
v_mfma_f32_16x16x32_bf16 v[42:45], v[46:49], a[8:11], 0
v_fma_f32 v23, v23, v118, v121
v_mul_f32 v120, v119, v96
mav 34,12,50
R 66,5120
v_fma_f32 v24, v24, v118, v120
mav 38,16,50
v_mul_f32 v121, v119, v97
v_fma_f32 v25, v25, v118, v121
mva 42,50,20
v_mul_f32 v120, v119, v98
s_waitcnt lgkmcnt(2)
mav 34,24,54
R 70,6144
v_fma_f32 v26, v26, v118, v120
mav 38,28,54
v_mul_f32 v121, v119, v99
v_fma_f32 v27, v27, v118, v121
mva 42,54,32
v_mul_f32 v120, v119, v100
v_fma_f32 v28, v28, v118, v120
mav 34,36,58
R 74,7168
v_mul_f32 v121, v119, v101
mav 38,40,58
v_fma_f32 v29, v29, v118, v121
v_cvt_pk_bf16_f32 v110, v22, v23
mva 42,58,44
v_cvt_pk_bf16_f32 v111, v24, v25
s_waitcnt lgkmcnt(2)
mav 34,48,62
R 46,8192
v_cvt_pk_bf16_f32 v112, v26, v27
mav 38,52,62
v_cvt_pk_bf16_f32 v113, v28, v29
v_mul_f32 v103, v103, v116
mva 42,62,56
ds_write_b128 v83, v[110:113] offset:0
mav 34,60,66
R 50,9216
v_fma_f32 v30, v30, v102, v103
mav 38,64,66
v_mul_f32 v105, v105, v116
v_fma_f32 v31, v31, v104, v105
mva 42,66,68
v_mul_f32 v107, v107, v116
s_waitcnt lgkmcnt(3)
mav 34,72,70
R 54,10240
v_fma_f32 v32, v32, v106, v107
mav 38,76,70
v_mul_f32 v109, v109, v116
v_fma_f32 v33, v33, v108, v109
mva 42,70,80
s_xor_b32 s50, s50, 0x10000
v_cvt_pk_bf16_f32 v78, v30, v31
mav 34,84,74
R 58,11264
v_cvt_pk_bf16_f32 v79, v32, v33
mav 38,88,74
ds_write_b64 v83, v[78:79] offset:1024
mva 42,74,92
s_waitcnt lgkmcnt(3)
mav 34,96,46
R 62,12288
s_mov_b32 m0, 142336
mav 38,100,46
s_waitcnt vmcnt(17) lgkmcnt(1)
s_barrier
mva 42,46,104
s_add_u32 s49, s49, 346112
buffer_load_dword v90, s[36:39], 0 offen sc1 lds
mav 34,108,50
R 66,13312
s_add_u32 m0, s50, 32768
mav 38,112,50
mva 42,50,116
D 86,0
mav 34,120,54
R 70,14336
mav 38,124,54
D 86,1024
mva 42,54,128
mav 34,132,58
R 74,15360
mav 38,136,58
D 86,2048
mva 42,58,140
s_waitcnt lgkmcnt(2)
mav 34,144,62
R 46,16384
D 86,3072
mav 38,148,62
s_add_u32 m0, s50, 40960
mva 42,62,152
mav 34,156,66
R 50,17408
D 87,0
mav 38,160,66
v_add_u32 v92, 1, v92
mva 42,66,164
D 87,1024
s_waitcnt lgkmcnt(2)
mav 34,168,70
R 54,18432
mav 38,172,70
mva 42,70,176
D 87,2048
mav 34,180,74
R 58,19456
mav 38,184,74
D 87,3072
mva 42,74,188
s_add_u32 m0, s50, 49152
s_waitcnt lgkmcnt(2)
mav 34,192,46
R 62,20480
mav 38,196,46
D 88,0
mva 42,46,200
mav 34,204,50
R 66,21504
D 88,1024
mav 38,208,50
mva 42,50,212
s_waitcnt lgkmcnt(2)
mav 34,216,54
R 70,22528
D 88,2048
mav 38,220,54
mva 42,54,224
D 88,3072
mav 34,228,58
R 74,23552
s_add_u32 m0, s50, 57344
mav 38,232,58
mva 42,58,236
D 89,0
s_waitcnt lgkmcnt(2)
mav 34,240,62
R 46,24576
mav 38,244,62
D 89,1024
mva 42,62,248
mav 34,252,66
R 50,25600
mvv 38,128,66
D 89,2048
mvv 42,66,132
s_waitcnt lgkmcnt(2)
mvv 34,136,70
R 54,26624
D 89,3072
mvv 38,140,70
mvv 42,70,144
mvv 34,148,74
R 58,27648
mvv 38,152,74
mvv 42,74,156
s_waitcnt lgkmcnt(2)
mvv 34,160,46
R 62,28672
mvv 38,164,46
mvv 42,46,168
mvv 34,172,50
R 66,29696
mvv 38,176,50
mvv 42,50,180
s_waitcnt lgkmcnt(2)
mvv 34,184,54
R 70,30720
mvv 38,188,54
mvv 42,54,192
mvv 34,196,58
R 74,31744
v_xor_b32 v80, 0x10000, v80
mvv 38,200,58
mvv 42,58,204
s_waitcnt lgkmcnt(2)
mvv 34,208,62
R 46,32768
mvv 38,212,62
ds_read_b32 v122, v82 offset:0
ds_read_b64 v[118:119], v84 offset:128
mvv 42,62,216
ds_read_b128 v[102:105], v85 offset:128
ds_read_b128 v[106:109], v85 offset:144
mvv 34,220,66
R 50,33792
mvv 38,224,66
mvv 42,66,228
s_waitcnt lgkmcnt(6)
mvv 34,232,70
R 54,34816
mvv 38,236,70
mvv 42,70,240
mvv 34,244,74
R 58,35840
mvv 38,248,74
s_waitcnt lgkmcnt(2)
v_cmp_gt_i32 vcc, v92, v122
mvv 42,74,252
s_add_u32 s60, s60, 1
s_cbranch_scc0 LQB0_2
v_mfma_f32_16x16x32_bf16 v[22:25], a[0:3], v[46:49], 0
s_cbranch_vccnz CQB0_5
BQB0_5:
R 62,36864
v_mfma_f32_16x16x32_bf16 v[26:29], a[4:7], v[46:49], 0
v_mul_f32 v120, v119, v94
v_fma_f32 v34, v34, v118, v120
v_mfma_f32_16x16x32_bf16 v[30:33], v[46:49], a[8:11], 0
v_mul_f32 v121, v119, v95
v_fma_f32 v35, v35, v118, v121
mav 22,12,50
R 66,37888
v_mul_f32 v120, v119, v96
mav 26,16,50
v_fma_f32 v36, v36, v118, v120
v_mul_f32 v121, v119, v97
mva 30,50,20
v_fma_f32 v37, v37, v118, v121
s_waitcnt lgkmcnt(2)
mav 22,24,54
R 70,38912
v_mul_f32 v120, v119, v98
mav 26,28,54
v_fma_f32 v38, v38, v118, v120
v_mul_f32 v121, v119, v99
mva 30,54,32
v_fma_f32 v39, v39, v118, v121
v_mul_f32 v120, v119, v100
mav 22,36,58
R 74,39936
v_fma_f32 v40, v40, v118, v120
mav 26,40,58
v_mul_f32 v121, v119, v101
v_fma_f32 v41, v41, v118, v121
mva 30,58,44
v_cvt_pk_bf16_f32 v110, v34, v35
s_waitcnt lgkmcnt(2)
mav 22,48,62
R 46,40960
v_cvt_pk_bf16_f32 v111, v36, v37
mav 26,52,62
v_cvt_pk_bf16_f32 v112, v38, v39
v_cvt_pk_bf16_f32 v113, v40, v41
mva 30,62,56
ds_write_b128 v83, v[110:113] offset:4096
mav 22,60,66
R 50,41984
v_mul_f32 v103, v103, v116
mav 26,64,66
v_fma_f32 v42, v42, v102, v103
v_mul_f32 v105, v105, v116
mva 30,66,68
v_fma_f32 v43, v43, v104, v105
s_waitcnt lgkmcnt(3)
mav 22,72,70
R 54,43008
v_mul_f32 v107, v107, v116
mav 26,76,70
v_fma_f32 v44, v44, v106, v107
v_mul_f32 v109, v109, v116
mva 30,70,80
v_fma_f32 v45, v45, v108, v109
v_cvt_pk_bf16_f32 v78, v42, v43
mav 22,84,74
R 58,44032
v_cvt_pk_bf16_f32 v79, v44, v45
mav 26,88,74
ds_write_b64 v83, v[78:79] offset:5120
mva 30,74,92
s_waitcnt lgkmcnt(3)
mav 22,96,46
R 62,45056
s_mov_b32 m0, 142336
mav 26,100,46
s_waitcnt vmcnt(17) lgkmcnt(1)
s_barrier
mva 30,46,104
s_add_u32 s49, s49, 346112
buffer_load_dword v90, s[36:39], 0 offen sc1 lds
mav 22,108,50
R 66,46080
s_add_u32 m0, s50, 0
mav 26,112,50
mva 30,50,116
D 86,0
mav 22,120,54
R 70,47104
mav 26,124,54
D 86,1024
mva 30,54,128
mav 22,132,58
R 74,48128
mav 26,136,58
D 86,2048
mva 30,58,140
s_waitcnt lgkmcnt(2)
mav 22,144,62
R 46,49152
D 86,3072
mav 26,148,62
s_add_u32 m0, s50, 8192
mva 30,62,152
mav 22,156,66
R 50,50176
D 87,0
mav 26,160,66
v_add_u32 v92, 1, v92
mva 30,66,164
D 87,1024
s_waitcnt lgkmcnt(2)
mav 22,168,70
R 54,51200
mav 26,172,70
mva 30,70,176
D 87,2048
mav 22,180,74
R 58,52224
mav 26,184,74
D 87,3072
mva 30,74,188
s_add_u32 m0, s50, 16384
s_waitcnt lgkmcnt(2)
mav 22,192,46
R 62,53248
mav 26,196,46
D 88,0
mva 30,46,200
mav 22,204,50
R 66,54272
D 88,1024
mav 26,208,50
mva 30,50,212
)"
R"(
s_waitcnt lgkmcnt(2)
mav 22,216,54
R 70,55296
D 88,2048
mav 26,220,54
mva 30,54,224
D 88,3072
mav 22,228,58
R 74,56320
s_add_u32 m0, s50, 24576
mav 26,232,58
mva 30,58,236
D 89,0
s_waitcnt lgkmcnt(2)
mav 22,240,62
R 46,57344
mav 26,244,62
D 89,1024
mva 30,62,248
mav 22,252,66
R 50,58368
mvv 26,128,66
D 89,2048
mvv 30,66,132
s_waitcnt lgkmcnt(2)
mvv 22,136,70
R 54,59392
D 89,3072
mvv 26,140,70
mvv 30,70,144
mvv 22,148,74
R 58,60416
mvv 26,152,74
mvv 30,74,156
s_waitcnt lgkmcnt(2)
mvv 22,160,46
R 62,61440
mvv 26,164,46
mvv 30,46,168
mvv 22,172,50
R 66,62464
mvv 26,176,50
mvv 30,50,180
s_waitcnt lgkmcnt(2)
mvv 22,184,54
R 70,63488
mvv 26,188,54
mvv 30,54,192
mvv 22,196,58
R 74,64512
mvv 26,200,58
mvv 30,58,204
s_waitcnt lgkmcnt(2)
mvv 22,208,62
R 46,0
mvv 26,212,62
ds_read_b32 v122, v82 offset:0
ds_read_b64 v[118:119], v84 offset:0
mvv 30,62,216
ds_read_b128 v[102:105], v85 offset:0
ds_read_b128 v[106:109], v85 offset:16
mvv 22,220,66
R 50,1024
mvv 26,224,66
mvv 30,66,228
s_waitcnt lgkmcnt(6)
mvv 22,232,70
R 54,2048
mvv 26,236,70
mvv 30,70,240
mvv 22,244,74
R 58,3072
mvv 26,248,74
s_waitcnt lgkmcnt(2)
v_cmp_gt_i32 vcc, v92, v122
mvv 30,74,252
s_cbranch_vccnz CQB0_6
BQB0_6:
v_mfma_f32_16x16x32_bf16 v[34:37], a[0:3], v[46:49], 0
R 62,4096
v_mul_f32 v120, v119, v94
v_mfma_f32_16x16x32_bf16 v[38:41], a[4:7], v[46:49], 0
v_fma_f32 v22, v22, v118, v120
v_mul_f32 v121, v119, v95
v_mfma_f32_16x16x32_bf16 v[42:45], v[46:49], a[8:11], 0
v_fma_f32 v23, v23, v118, v121
v_mul_f32 v120, v119, v96
mav 34,12,50
R 66,5120
v_fma_f32 v24, v24, v118, v120
mav 38,16,50
v_mul_f32 v121, v119, v97
v_fma_f32 v25, v25, v118, v121
mva 42,50,20
v_mul_f32 v120, v119, v98
s_waitcnt lgkmcnt(2)
mav 34,24,54
R 70,6144
v_fma_f32 v26, v26, v118, v120
mav 38,28,54
v_mul_f32 v121, v119, v99
v_fma_f32 v27, v27, v118, v121
mva 42,54,32
v_mul_f32 v120, v119, v100
v_fma_f32 v28, v28, v118, v120
mav 34,36,58
R 74,7168
v_mul_f32 v121, v119, v101
mav 38,40,58
v_fma_f32 v29, v29, v118, v121
v_cvt_pk_bf16_f32 v110, v22, v23
mva 42,58,44
v_cvt_pk_bf16_f32 v111, v24, v25
s_waitcnt lgkmcnt(2)
mav 34,48,62
R 46,8192
v_cvt_pk_bf16_f32 v112, v26, v27
mav 38,52,62
v_cvt_pk_bf16_f32 v113, v28, v29
v_mul_f32 v103, v103, v116
mva 42,62,56
ds_write_b128 v83, v[110:113] offset:0
mav 34,60,66
R 50,9216
v_fma_f32 v30, v30, v102, v103
mav 38,64,66
v_mul_f32 v105, v105, v116
v_fma_f32 v31, v31, v104, v105
mva 42,66,68
v_mul_f32 v107, v107, v116
s_waitcnt lgkmcnt(3)
mav 34,72,70
R 54,10240
v_fma_f32 v32, v32, v106, v107
mav 38,76,70
v_mul_f32 v109, v109, v116
v_fma_f32 v33, v33, v108, v109
mva 42,70,80
v_cvt_pk_bf16_f32 v78, v30, v31
v_cvt_pk_bf16_f32 v79, v32, v33
mav 34,84,74
R 58,11264
mav 38,88,74
ds_write_b64 v83, v[78:79] offset:1024
mva 42,74,92
s_waitcnt lgkmcnt(3)
mav 34,96,46
R 62,12288
s_add_u32 s49, s49, 346112
mav 38,100,46
s_waitcnt vmcnt(17) lgkmcnt(1)
s_barrier
mva 42,46,104
mav 34,108,50
R 66,13312
mav 38,112,50
mva 42,50,116
mav 34,120,54
R 70,14336
mav 38,124,54
mva 42,54,128
mav 34,132,58
R 74,15360
mav 38,136,58
mva 42,58,140
s_waitcnt lgkmcnt(2)
mav 34,144,62
R 46,16384
mav 38,148,62
mva 42,62,152
mav 34,156,66
R 50,17408
mav 38,160,66
mva 42,66,164
s_waitcnt lgkmcnt(2)
mav 34,168,70
R 54,18432
mav 38,172,70
mva 42,70,176
mav 34,180,74
R 58,19456
mav 38,184,74
mva 42,74,188
s_waitcnt lgkmcnt(2)
mav 34,192,46
R 62,20480
mav 38,196,46
mva 42,46,200
mav 34,204,50
R 66,21504
mav 38,208,50
mva 42,50,212
s_waitcnt lgkmcnt(2)
mav 34,216,54
R 70,22528
mav 38,220,54
mva 42,54,224
mav 34,228,58
R 74,23552
mav 38,232,58
mva 42,58,236
s_waitcnt lgkmcnt(2)
mav 34,240,62
R 46,24576
mav 38,244,62
mva 42,62,248
mav 34,252,66
R 50,25600
mvv 38,128,66
mvv 42,66,132
s_waitcnt lgkmcnt(2)
mvv 34,136,70
R 54,26624
mvv 38,140,70
mvv 42,70,144
mvv 34,148,74
R 58,27648
mvv 38,152,74
mvv 42,74,156
s_waitcnt lgkmcnt(2)
mvv 34,160,46
R 62,28672
mvv 38,164,46
mvv 42,46,168
mvv 34,172,50
R 66,29696
mvv 38,176,50
mvv 42,50,180
s_waitcnt lgkmcnt(2)
mvv 34,184,54
R 70,30720
mvv 38,188,54
mvv 42,54,192
mvv 34,196,58
R 74,31744
v_xor_b32 v80, 0x10000, v80
mvv 38,200,58
mvv 42,58,204
s_waitcnt lgkmcnt(2)
mvv 34,208,62
R 46,32768
mvv 38,212,62
ds_read_b64 v[118:119], v84 offset:128
ds_read_b128 v[102:105], v85 offset:128
mvv 42,62,216
ds_read_b128 v[106:109], v85 offset:144
mvv 34,220,66
R 50,33792
mvv 38,224,66
mvv 42,66,228
s_waitcnt lgkmcnt(5)
mvv 34,232,70
R 54,34816
mvv 38,236,70
mvv 42,70,240
mvv 34,244,74
R 58,35840
mvv 38,248,74
mvv 42,74,252
s_waitcnt lgkmcnt(2)
v_mfma_f32_16x16x32_bf16 v[22:25], a[0:3], v[46:49], 0
R 62,36864
v_mul_f32 v120, v119, v94
v_mfma_f32_16x16x32_bf16 v[26:29], a[4:7], v[46:49], 0
v_fma_f32 v34, v34, v118, v120
v_mul_f32 v121, v119, v95
v_mfma_f32_16x16x32_bf16 v[30:33], v[46:49], a[8:11], 0
v_fma_f32 v35, v35, v118, v121
v_mul_f32 v120, v119, v96
mav 22,12,50
R 66,37888
v_fma_f32 v36, v36, v118, v120
mav 26,16,50
v_mul_f32 v121, v119, v97
v_fma_f32 v37, v37, v118, v121
mva 30,50,20
v_mul_f32 v120, v119, v98
s_waitcnt lgkmcnt(2)
mav 22,24,54
R 70,38912
v_fma_f32 v38, v38, v118, v120
mav 26,28,54
v_mul_f32 v121, v119, v99
v_fma_f32 v39, v39, v118, v121
mva 30,54,32
v_mul_f32 v120, v119, v100
v_fma_f32 v40, v40, v118, v120
mav 22,36,58
R 74,39936
v_mul_f32 v121, v119, v101
mav 26,40,58
v_fma_f32 v41, v41, v118, v121
v_cvt_pk_bf16_f32 v110, v34, v35
mva 30,58,44
v_cvt_pk_bf16_f32 v111, v36, v37
s_waitcnt lgkmcnt(2)
mav 22,48,62
R 46,40960
v_cvt_pk_bf16_f32 v112, v38, v39
mav 26,52,62
v_cvt_pk_bf16_f32 v113, v40, v41
v_mul_f32 v103, v103, v116
mva 30,62,56
ds_write_b128 v83, v[110:113] offset:4096
mav 22,60,66
R 50,41984
v_fma_f32 v42, v42, v102, v103
mav 26,64,66
v_mul_f32 v105, v105, v116
v_fma_f32 v43, v43, v104, v105
mva 30,66,68
v_mul_f32 v107, v107, v116
s_waitcnt lgkmcnt(3)
mav 22,72,70
R 54,43008
v_fma_f32 v44, v44, v106, v107
mav 26,76,70
v_mul_f32 v109, v109, v116
v_fma_f32 v45, v45, v108, v109
mva 30,70,80
v_cvt_pk_bf16_f32 v78, v42, v43
v_cvt_pk_bf16_f32 v79, v44, v45
mav 22,84,74
R 58,44032
mav 26,88,74
ds_write_b64 v83, v[78:79] offset:5120
mva 30,74,92
s_waitcnt lgkmcnt(3)
mav 22,96,46
R 62,45056
s_add_u32 s49, s49, 346112
mav 26,100,46
s_waitcnt vmcnt(0) lgkmcnt(1)
s_barrier
mva 30,46,104
mav 22,108,50
R 66,46080
mav 26,112,50
mva 30,50,116
mav 22,120,54
R 70,47104
mav 26,124,54
mva 30,54,128
mav 22,132,58
R 74,48128
mav 26,136,58
mva 30,58,140
s_waitcnt lgkmcnt(2)
mav 22,144,62
R 46,49152
mav 26,148,62
mva 30,62,152
mav 22,156,66
R 50,50176
mav 26,160,66
mva 30,66,164
s_waitcnt lgkmcnt(2)
mav 22,168,70
R 54,51200
mav 26,172,70
mva 30,70,176
mav 22,180,74
R 58,52224
mav 26,184,74
mva 30,74,188
s_waitcnt lgkmcnt(2)
mav 22,192,46
R 62,53248
mav 26,196,46
mva 30,46,200
mav 22,204,50
R 66,54272
mav 26,208,50
mva 30,50,212
s_waitcnt lgkmcnt(2)
mav 22,216,54
R 70,55296
mav 26,220,54
mva 30,54,224
mav 22,228,58
R 74,56320
mav 26,232,58
mva 30,58,236
s_waitcnt lgkmcnt(2)
mav 22,240,62
R 46,57344
mav 26,244,62
mva 30,62,248
mav 22,252,66
R 50,58368
mvv 26,128,66
mvv 30,66,132
s_waitcnt lgkmcnt(2)
mvv 22,136,70
R 54,59392
mvv 26,140,70
mvv 30,70,144
mvv 22,148,74
R 58,60416
mvv 26,152,74
mvv 30,74,156
s_waitcnt lgkmcnt(2)
mvv 22,160,46
R 62,61440
mvv 26,164,46
mvv 30,46,168
mvv 22,172,50
R 66,62464
mvv 26,176,50
mvv 30,50,180
s_waitcnt lgkmcnt(2)
mvv 22,184,54
R 70,63488
mvv 26,188,54
mvv 30,54,192
mvv 22,196,58
R 74,64512
mvv 26,200,58
mvv 30,58,204
s_waitcnt lgkmcnt(2)
mvv 22,208,62
R 46,0
mvv 26,212,62
ds_read_b64 v[118:119], v84 offset:0
ds_read_b128 v[102:105], v85 offset:0
mvv 30,62,216
ds_read_b128 v[106:109], v85 offset:16
mvv 22,220,66
R 50,1024
mvv 26,224,66
mvv 30,66,228
s_waitcnt lgkmcnt(5)
mvv 22,232,70
R 54,2048
mvv 26,236,70
mvv 30,70,240
mvv 22,244,74
R 58,3072
mvv 26,248,74
mvv 30,74,252
s_waitcnt lgkmcnt(2)
v_mfma_f32_16x16x32_bf16 v[34:37], a[0:3], v[46:49], 0
R 62,4096
v_mul_f32 v120, v119, v94
v_mfma_f32_16x16x32_bf16 v[38:41], a[4:7], v[46:49], 0
v_fma_f32 v22, v22, v118, v120
v_mul_f32 v121, v119, v95
v_mfma_f32_16x16x32_bf16 v[42:45], v[46:49], a[8:11], 0
v_fma_f32 v23, v23, v118, v121
v_mul_f32 v120, v119, v96
mav 34,12,50
R 66,5120
v_fma_f32 v24, v24, v118, v120
mav 38,16,50
v_mul_f32 v121, v119, v97
v_fma_f32 v25, v25, v118, v121
mva 42,50,20
v_mul_f32 v120, v119, v98
s_waitcnt lgkmcnt(2)
mav 34,24,54
R 70,6144
v_fma_f32 v26, v26, v118, v120
mav 38,28,54
v_mul_f32 v121, v119, v99
v_fma_f32 v27, v27, v118, v121
mva 42,54,32
v_mul_f32 v120, v119, v100
v_fma_f32 v28, v28, v118, v120
mav 34,36,58
R 74,7168
v_mul_f32 v121, v119, v101
mav 38,40,58
v_fma_f32 v29, v29, v118, v121
v_cvt_pk_bf16_f32 v110, v22, v23
mva 42,58,44
v_cvt_pk_bf16_f32 v111, v24, v25
s_waitcnt lgkmcnt(2)
mav 34,48,62
R 46,8192
v_cvt_pk_bf16_f32 v112, v26, v27
mav 38,52,62
v_cvt_pk_bf16_f32 v113, v28, v29
v_mul_f32 v103, v103, v116
mva 42,62,56
ds_write_b128 v83, v[110:113] offset:0
mav 34,60,66
R 50,9216
v_fma_f32 v30, v30, v102, v103
mav 38,64,66
v_mul_f32 v105, v105, v116
v_fma_f32 v31, v31, v104, v105
mva 42,66,68
v_mul_f32 v107, v107, v116
s_waitcnt lgkmcnt(3)
mav 34,72,70
R 54,10240
v_fma_f32 v32, v32, v106, v107
mav 38,76,70
v_mul_f32 v109, v109, v116
v_fma_f32 v33, v33, v108, v109
mva 42,70,80
v_cvt_pk_bf16_f32 v78, v30, v31
v_cvt_pk_bf16_f32 v79, v32, v33
mav 34,84,74
R 58,11264
mav 38,88,74
ds_write_b64 v83, v[78:79] offset:1024
mva 42,74,92
s_waitcnt lgkmcnt(3)
mav 34,96,46
R 62,12288
mav 38,100,46
s_waitcnt lgkmcnt(1)
s_barrier
mva 42,46,104
mav 34,108,50
R 66,13312
mav 38,112,50
mva 42,50,116
mav 34,120,54
R 70,14336
mav 38,124,54
mva 42,54,128
mav 34,132,58
R 74,15360
mav 38,136,58
mva 42,58,140
s_waitcnt lgkmcnt(2)
mav 34,144,62
R 46,16384
mav 38,148,62
mva 42,62,152
mav 34,156,66
R 50,17408
mav 38,160,66
mva 42,66,164
s_waitcnt lgkmcnt(2)
mav 34,168,70
R 54,18432
mav 38,172,70
mva 42,70,176
mav 34,180,74
R 58,19456
mav 38,184,74
mva 42,74,188
s_waitcnt lgkmcnt(2)
mav 34,192,46
R 62,20480
mav 38,196,46
mva 42,46,200
mav 34,204,50
R 66,21504
mav 38,208,50
mva 42,50,212
s_waitcnt lgkmcnt(2)
mav 34,216,54
R 70,22528
mav 38,220,54
mva 42,54,224
mav 34,228,58
R 74,23552
mav 38,232,58
mva 42,58,236
s_waitcnt lgkmcnt(2)
mav 34,240,62
R 46,24576
mav 38,244,62
mva 42,62,248
mav 34,252,66
R 50,25600
mvv 38,128,66
mvv 42,66,132
s_waitcnt lgkmcnt(2)
mvv 34,136,70
R 54,26624
mvv 38,140,70
mvv 42,70,144
mvv 34,148,74
R 58,27648
mvv 38,152,74
mvv 42,74,156
s_waitcnt lgkmcnt(2)
mvv 34,160,46
R 62,28672
mvv 38,164,46
mvv 42,46,168
mvv 34,172,50
R 66,29696
mvv 38,176,50
mvv 42,50,180
s_waitcnt lgkmcnt(2)
mvv 34,184,54
R 70,30720
mvv 38,188,54
mvv 42,54,192
mvv 34,196,58
R 74,31744
mvv 38,200,58
mvv 42,58,204
s_waitcnt lgkmcnt(2)
mvv 34,208,62
mvv 38,212,62
ds_read_b64 v[118:119], v84 offset:128
ds_read_b128 v[102:105], v85 offset:128
mvv 42,62,216
ds_read_b128 v[106:109], v85 offset:144
mvv 34,220,66
mvv 38,224,66
mvv 42,66,228
s_waitcnt lgkmcnt(3)
mvv 34,232,70
mvv 38,236,70
mvv 42,70,240
mvv 34,244,74
mvv 38,248,74
mvv 42,74,252
s_waitcnt lgkmcnt(0)
v_mul_f32 v120, v119, v94
s_nop 3
v_fma_f32 v34, v34, v118, v120
v_mul_f32 v121, v119, v95
v_fma_f32 v35, v35, v118, v121
v_mul_f32 v120, v119, v96
v_fma_f32 v36, v36, v118, v120
v_mul_f32 v121, v119, v97
v_fma_f32 v37, v37, v118, v121
v_mul_f32 v120, v119, v98
v_fma_f32 v38, v38, v118, v120
v_mul_f32 v121, v119, v99
v_fma_f32 v39, v39, v118, v121
v_mul_f32 v120, v119, v100
v_fma_f32 v40, v40, v118, v120
v_mul_f32 v121, v119, v101
v_fma_f32 v41, v41, v118, v121
v_cvt_pk_bf16_f32 v110, v34, v35
v_cvt_pk_bf16_f32 v111, v36, v37
v_cvt_pk_bf16_f32 v112, v38, v39
v_cvt_pk_bf16_f32 v113, v40, v41
ds_write_b128 v83, v[110:113] offset:4096
v_mul_f32 v103, v103, v116
v_fma_f32 v42, v42, v102, v103
v_mul_f32 v105, v105, v116
v_fma_f32 v43, v43, v104, v105
v_mul_f32 v107, v107, v116
v_fma_f32 v44, v44, v106, v107
v_mul_f32 v109, v109, v116
v_fma_f32 v45, v45, v108, v109
v_cvt_pk_bf16_f32 v78, v42, v43
v_cvt_pk_bf16_f32 v79, v44, v45
ds_write_b64 v83, v[78:79] offset:5120
s_waitcnt lgkmcnt(0)
s_barrier
s_endpgm
CQB0_1:
s_mov_b32 s58, m0
s_mov_b32 m0, 142336
DQB0_1:
s_cmp_eq_u32 s61, 0
s_cbranch_scc1 EQB0_1
s_sub_u32 s61, s61, 1
buffer_load_dword v90, s[36:39], 0 offen sc1 lds
s_sleep 1
ds_read_b32 v122, v82
s_waitcnt lgkmcnt(0)
v_cmp_gt_i32 vcc, v92, v122
s_cbranch_vccnz DQB0_1
EQB0_1:
s_mov_b32 m0, s58
s_branch BQB0_1
CQB0_3:
s_mov_b32 s58, m0
s_mov_b32 m0, 142336
DQB0_3:
s_cmp_eq_u32 s61, 0
s_cbranch_scc1 EQB0_3
s_sub_u32 s61, s61, 1
buffer_load_dword v90, s[36:39], 0 offen sc1 lds
s_sleep 1
ds_read_b32 v122, v82
s_waitcnt lgkmcnt(0)
v_cmp_gt_i32 vcc, v92, v122
s_cbranch_vccnz DQB0_3
EQB0_3:
s_mov_b32 m0, s58
s_branch BQB0_3
CQB0_4:
s_mov_b32 s58, m0
s_mov_b32 m0, 142336
DQB0_4:
s_cmp_eq_u32 s61, 0
s_cbranch_scc1 EQB0_4
s_sub_u32 s61, s61, 1
buffer_load_dword v90, s[36:39], 0 offen sc1 lds
s_sleep 1
ds_read_b32 v122, v82
s_waitcnt lgkmcnt(0)
v_cmp_gt_i32 vcc, v92, v122
s_cbranch_vccnz DQB0_4
EQB0_4:
s_mov_b32 m0, s58
s_branch BQB0_4
CQB0_5:
s_mov_b32 s58, m0
s_mov_b32 m0, 142336
DQB0_5:
s_cmp_eq_u32 s61, 0
s_cbranch_scc1 EQB0_5
s_sub_u32 s61, s61, 1
buffer_load_dword v90, s[36:39], 0 offen sc1 lds
s_sleep 1
ds_read_b32 v122, v82
s_waitcnt lgkmcnt(0)
v_cmp_gt_i32 vcc, v92, v122
s_cbranch_vccnz DQB0_5
EQB0_5:
s_mov_b32 m0, s58
s_branch BQB0_5
CQB0_6:
s_mov_b32 s58, m0
s_mov_b32 m0, 142336
DQB0_6:
s_cmp_eq_u32 s61, 0
s_cbranch_scc1 EQB0_6
s_sub_u32 s61, s61, 1
buffer_load_dword v90, s[36:39], 0 offen sc1 lds
s_sleep 1
ds_read_b32 v122, v82
s_waitcnt lgkmcnt(0)
v_cmp_gt_i32 vcc, v92, v122
s_cbranch_vccnz DQB0_6
EQB0_6:
s_mov_b32 m0, s58
s_branch BQB0_6
LvQB1:
v_xor_b32 v80, 0x10000, v80
ds_read_b64 v[118:119], v84 offset:0
ds_read_b128 v[102:105], v85 offset:0
ds_read_b128 v[106:109], v85 offset:16
s_add_u32 s49, s49, 346112
R 46,32768
R 50,33792
R 54,34816
R 58,35840
s_waitcnt lgkmcnt(2)
v_mul_f32 v120, v119, v94
v_fma_f32 v22, v22, v118, v120
v_mul_f32 v121, v119, v95
v_fma_f32 v23, v23, v118, v121
v_mul_f32 v120, v119, v96
v_fma_f32 v24, v24, v118, v120
v_mul_f32 v121, v119, v97
v_fma_f32 v25, v25, v118, v121
v_mul_f32 v120, v119, v98
v_fma_f32 v26, v26, v118, v120
v_mul_f32 v121, v119, v99
v_fma_f32 v27, v27, v118, v121
v_mul_f32 v120, v119, v100
v_fma_f32 v28, v28, v118, v120
v_mul_f32 v121, v119, v101
v_fma_f32 v29, v29, v118, v121
v_cvt_pk_bf16_f32 v110, v22, v23
v_cvt_pk_bf16_f32 v111, v24, v25
v_cvt_pk_bf16_f32 v112, v26, v27
v_cvt_pk_bf16_f32 v113, v28, v29
ds_write_b128 v83, v[110:113] offset:0
v_mul_f32 v103, v103, v116
v_fma_f32 v30, v30, v102, v103
v_mul_f32 v105, v105, v116
v_fma_f32 v31, v31, v104, v105
v_mul_f32 v107, v107, v116
v_fma_f32 v32, v32, v106, v107
v_mul_f32 v109, v109, v116
v_fma_f32 v33, v33, v108, v109
v_cvt_pk_bf16_f32 v78, v30, v31
v_cvt_pk_bf16_f32 v79, v32, v33
ds_write_b64 v83, v[78:79] offset:1024
s_xor_b32 s50, s50, 0x10000
s_add_u32 s49, s49, 346112
s_waitcnt lgkmcnt(0)
s_barrier
ds_read_b64 v[118:119], v84 offset:128
ds_read_b128 v[102:105], v85 offset:128
ds_read_b128 v[106:109], v85 offset:144
v_mfma_f32_16x16x32_bf16 v[22:25], a[0:3], v[46:49], 0
R 62,36864
v_mfma_f32_16x16x32_bf16 v[26:29], a[4:7], v[46:49], 0
s_waitcnt lgkmcnt(1)
v_mul_f32 v120, v119, v94
v_mfma_f32_16x16x32_bf16 v[30:33], v[46:49], a[8:11], 0
v_fma_f32 v34, v34, v118, v120
v_mul_f32 v121, v119, v95
mav 22,12,50
R 66,37888
v_fma_f32 v35, v35, v118, v121
mav 26,16,50
v_mul_f32 v120, v119, v96
v_fma_f32 v36, v36, v118, v120
mva 30,50,20
v_mul_f32 v121, v119, v97
v_fma_f32 v37, v37, v118, v121
mav 22,24,54
R 70,38912
v_mul_f32 v120, v119, v98
mav 26,28,54
v_fma_f32 v38, v38, v118, v120
v_mul_f32 v121, v119, v99
mva 30,54,32
v_fma_f32 v39, v39, v118, v121
v_mul_f32 v120, v119, v100
mav 22,36,58
R 74,39936
v_fma_f32 v40, v40, v118, v120
mav 26,40,58
v_mul_f32 v121, v119, v101
v_fma_f32 v41, v41, v118, v121
mva 30,58,44
v_cvt_pk_bf16_f32 v110, v34, v35
s_waitcnt lgkmcnt(2)
mav 22,48,62
R 46,40960
v_cvt_pk_bf16_f32 v111, v36, v37
mav 26,52,62
v_cvt_pk_bf16_f32 v112, v38, v39
v_cvt_pk_bf16_f32 v113, v40, v41
mva 30,62,56
ds_write_b128 v83, v[110:113] offset:4096
mav 22,60,66
R 50,41984
v_mul_f32 v103, v103, v116
mav 26,64,66
v_fma_f32 v42, v42, v102, v103
v_mul_f32 v105, v105, v116
mva 30,66,68
v_fma_f32 v43, v43, v104, v105
s_waitcnt lgkmcnt(3)
mav 22,72,70
R 54,43008
v_mul_f32 v107, v107, v116
mav 26,76,70
v_fma_f32 v44, v44, v106, v107
v_mul_f32 v109, v109, v116
mva 30,70,80
v_fma_f32 v45, v45, v108, v109
v_cvt_pk_bf16_f32 v78, v42, v43
mav 22,84,74
R 58,44032
v_cvt_pk_bf16_f32 v79, v44, v45
mav 26,88,74
ds_write_b64 v83, v[78:79] offset:5120
mva 30,74,92
s_waitcnt lgkmcnt(3)
mav 22,96,46
R 62,45056
mav 26,100,46
s_waitcnt vmcnt(0) lgkmcnt(1)
s_barrier
mva 30,46,104
ds_write_b64 v84, v[18:19] offset:128
mav 22,108,50
R 66,46080
s_add_u32 m0, s50, 32768
mav 26,112,50
mva 30,50,116
D 86,0
mav 22,120,54
R 70,47104
mav 26,124,54
D 86,1024
mva 30,54,128
mav 22,132,58
R 74,48128
D 86,2048
mav 26,136,58
mva 30,58,140
D 86,3072
s_waitcnt lgkmcnt(2)
mav 22,144,62
R 46,49152
s_add_u32 m0, s50, 40960
mav 26,148,62
D 87,0
mva 30,62,152
mav 22,156,66
R 50,50176
D 87,1024
mav 26,160,66
mva 30,66,164
D 87,2048
s_waitcnt lgkmcnt(2)
mav 22,168,70
R 54,51200
mav 26,172,70
D 87,3072
s_add_u32 m0, s50, 49152
mva 30,70,176
mav 22,180,74
R 58,52224
D 88,0
mav 26,184,74
mva 30,74,188
D 88,1024
s_waitcnt lgkmcnt(2)
mav 22,192,46
R 62,53248
mav 26,196,46
D 88,2048
mva 30,46,200
mav 22,204,50
R 66,54272
D 88,3072
mav 26,208,50
s_add_u32 m0, s50, 57344
mva 30,50,212
D 89,0
s_waitcnt lgkmcnt(2)
mav 22,216,54
R 70,55296
mav 26,220,54
D 89,1024
mva 30,54,224
mav 22,228,58
R 74,56320
D 89,2048
mav 26,232,58
mva 30,58,236
D 89,3072
s_waitcnt lgkmcnt(2)
mav 22,240,62
R 46,57344
s_add_u32 s49, s49, 346112
mav 26,244,62
s_add_u32 m0, s50, 0
mva 30,62,248
D 86,0
mav 22,252,66
R 50,58368
mvv 26,128,66
D 86,1024
mvv 30,66,132
s_waitcnt lgkmcnt(2)
mvv 22,136,70
R 54,59392
D 86,2048
mvv 26,140,70
mvv 30,70,144
D 86,3072
s_add_u32 m0, s50, 8192
mvv 22,148,74
R 58,60416
mvv 26,152,74
D 87,0
mvv 30,74,156
s_waitcnt lgkmcnt(2)
mvv 22,160,46
R 62,61440
D 87,1024
mvv 26,164,46
mvv 30,46,168
D 87,2048
mvv 22,172,50
R 66,62464
mvv 26,176,50
D 87,3072
s_add_u32 m0, s50, 16384
mvv 30,50,180
s_waitcnt lgkmcnt(2)
mvv 22,184,54
R 70,63488
D 88,0
mvv 26,188,54
mvv 30,54,192
D 88,1024
mvv 22,196,58
R 74,64512
mvv 26,200,58
D 88,2048
mvv 30,58,204
s_waitcnt lgkmcnt(2)
mvv 22,208,62
R 46,0
D 88,3072
mvv 26,212,62
s_add_u32 m0, s50, 24576
ds_read_b64 v[118:119], v84 offset:0
mvv 30,62,216
ds_read_b128 v[102:105], v85 offset:0
ds_read_b128 v[106:109], v85 offset:16
mvv 22,220,66
R 50,1024
D 89,0
mvv 26,224,66
mvv 30,66,228
D 89,1024
s_waitcnt lgkmcnt(5)
mvv 22,232,70
R 54,2048
mvv 26,236,70
D 89,2048
mvv 30,70,240
mvv 22,244,74
R 58,3072
D 89,3072
mvv 26,248,74
mvv 30,74,252
s_waitcnt lgkmcnt(2)
v_mfma_f32_16x16x32_bf16 v[34:37], a[0:3], v[46:49], 0
R 62,4096
v_mul_f32 v120, v119, v94
v_mfma_f32_16x16x32_bf16 v[38:41], a[4:7], v[46:49], 0
v_fma_f32 v22, v22, v118, v120
v_mul_f32 v121, v119, v95
v_mfma_f32_16x16x32_bf16 v[42:45], v[46:49], a[8:11], 0
v_fma_f32 v23, v23, v118, v121
v_mul_f32 v120, v119, v96
mav 34,12,50
R 66,5120
v_fma_f32 v24, v24, v118, v120
mav 38,16,50
v_mul_f32 v121, v119, v97
v_fma_f32 v25, v25, v118, v121
mva 42,50,20
v_mul_f32 v120, v119, v98
s_waitcnt lgkmcnt(2)
mav 34,24,54
R 70,6144
v_fma_f32 v26, v26, v118, v120
mav 38,28,54
v_mul_f32 v121, v119, v99
v_fma_f32 v27, v27, v118, v121
mva 42,54,32
v_mul_f32 v120, v119, v100
v_fma_f32 v28, v28, v118, v120
mav 34,36,58
R 74,7168
v_mul_f32 v121, v119, v101
mav 38,40,58
v_fma_f32 v29, v29, v118, v121
v_cvt_pk_bf16_f32 v110, v22, v23
mva 42,58,44
v_cvt_pk_bf16_f32 v111, v24, v25
s_waitcnt lgkmcnt(2)
mav 34,48,62
R 46,8192
v_cvt_pk_bf16_f32 v112, v26, v27
mav 38,52,62
v_cvt_pk_bf16_f32 v113, v28, v29
v_mul_f32 v103, v103, v116
mva 42,62,56
ds_write_b128 v83, v[110:113] offset:0
mav 34,60,66
R 50,9216
v_fma_f32 v30, v30, v102, v103
mav 38,64,66
v_mul_f32 v105, v105, v116
v_fma_f32 v31, v31, v104, v105
mva 42,66,68
v_mul_f32 v107, v107, v116
s_waitcnt lgkmcnt(3)
mav 34,72,70
R 54,10240
v_fma_f32 v32, v32, v106, v107
mav 38,76,70
v_mul_f32 v109, v109, v116
v_fma_f32 v33, v33, v108, v109
mva 42,70,80
s_xor_b32 s50, s50, 0x10000
v_cvt_pk_bf16_f32 v78, v30, v31
mav 34,84,74
R 58,11264
v_cvt_pk_bf16_f32 v79, v32, v33
mav 38,88,74
ds_write_b64 v83, v[78:79] offset:1024
mva 42,74,92
s_waitcnt lgkmcnt(3)
mav 34,96,46
R 62,12288
s_add_u32 s49, s49, 346112
mav 38,100,46
s_waitcnt vmcnt(16) lgkmcnt(1)
s_barrier
mva 42,46,104
buffer_load_dwordx2 v[14:15], v93, s[36:39], s49 offen sc1
mav 34,108,50
R 66,13312
s_add_u32 m0, s50, 32768
mav 38,112,50
mva 42,50,116
D 86,0
mav 34,120,54
R 70,14336
mav 38,124,54
D 86,1024
mva 42,54,128
mav 34,132,58
R 74,15360
mav 38,136,58
D 86,2048
mva 42,58,140
s_waitcnt lgkmcnt(2)
mav 34,144,62
R 46,16384
D 86,3072
mav 38,148,62
s_add_u32 m0, s50, 40960
mva 42,62,152
mav 34,156,66
R 50,17408
D 87,0
mav 38,160,66
mva 42,66,164
D 87,1024
s_waitcnt lgkmcnt(2)
mav 34,168,70
R 54,18432
mav 38,172,70
mva 42,70,176
D 87,2048
mav 34,180,74
R 58,19456
mav 38,184,74
D 87,3072
mva 42,74,188
s_add_u32 m0, s50, 49152
s_waitcnt lgkmcnt(2)
mav 34,192,46
R 62,20480
mav 38,196,46
D 88,0
mva 42,46,200
mav 34,204,50
R 66,21504
D 88,1024
mav 38,208,50
mva 42,50,212
s_waitcnt lgkmcnt(2)
mav 34,216,54
R 70,22528
D 88,2048
mav 38,220,54
mva 42,54,224
D 88,3072
mav 34,228,58
R 74,23552
s_add_u32 m0, s50, 57344
mav 38,232,58
mva 42,58,236
D 89,0
s_waitcnt lgkmcnt(2)
mav 34,240,62
R 46,24576
mav 38,244,62
D 89,1024
mva 42,62,248
mav 34,252,66
R 50,25600
mvv 38,128,66
D 89,2048
mvv 42,66,132
s_waitcnt lgkmcnt(2)
mvv 34,136,70
R 54,26624
D 89,3072
mvv 38,140,70
mvv 42,70,144
mvv 34,148,74
R 58,27648
mvv 38,152,74
mvv 42,74,156
s_waitcnt lgkmcnt(2)
mvv 34,160,46
R 62,28672
mvv 38,164,46
mvv 42,46,168
s_waitcnt vmcnt(16)
v_permlane32_swap_b32 v14, v15
mvv 34,172,50
R 66,29696
v_add_f32 v16, v14, v15
mvv 38,176,50
v_add_f32 v17, v14, v15
mvv 42,50,180
s_waitcnt lgkmcnt(2)
mvv 34,184,54
R 70,30720
v_permlane16_swap_b32 v16, v17
mvv 38,188,54
v_add_f32 v14, v16, v17
v_add_f32 v15, v16, v17
mvv 42,54,192
mvv 34,196,58
R 74,31744
v_xor_b32 v80, 0x10000, v80
mvv 38,200,58
v_permlane32_swap_b32 v14, v15
v_mul_f32 v16, 0x3a800000, v14
mvv 42,58,204
v_mul_f32 v17, 0x3a800000, v15
s_waitcnt lgkmcnt(2)
mvv 34,208,62
R 46,32768
v_fma_f32 v17, -v16, v16, v17
mvv 38,212,62
ds_read_b64 v[118:119], v84 offset:128
ds_read_b128 v[102:105], v85 offset:128
mvv 42,62,216
ds_read_b128 v[106:109], v85 offset:144
v_max_f32 v17, 0, v17
mvv 34,220,66
R 50,33792
v_add_f32 v17, 0x3727c5ac, v17
mvv 38,224,66
v_rsq_f32 v14, v17
mvv 42,66,228
v_mul_f32_e64 v15, -v16, v14
s_waitcnt lgkmcnt(5)
mvv 34,232,70
R 54,34816
mvv 38,236,70
mvv 42,70,240
mvv 34,244,74
R 58,35840
mvv 38,248,74
mvv 42,74,252
s_waitcnt lgkmcnt(2)
v_mfma_f32_16x16x32_bf16 v[22:25], a[0:3], v[46:49], 0
R 62,36864
v_mul_f32 v120, v119, v94
v_mfma_f32_16x16x32_bf16 v[26:29], a[4:7], v[46:49], 0
v_fma_f32 v34, v34, v118, v120
v_mul_f32 v121, v119, v95
v_mfma_f32_16x16x32_bf16 v[30:33], v[46:49], a[8:11], 0
v_fma_f32 v35, v35, v118, v121
v_mul_f32 v120, v119, v96
mav 22,12,50
R 66,37888
v_fma_f32 v36, v36, v118, v120
mav 26,16,50
v_mul_f32 v121, v119, v97
v_fma_f32 v37, v37, v118, v121
mva 30,50,20
v_mul_f32 v120, v119, v98
s_waitcnt lgkmcnt(2)
mav 22,24,54
R 70,38912
v_fma_f32 v38, v38, v118, v120
mav 26,28,54
v_mul_f32 v121, v119, v99
v_fma_f32 v39, v39, v118, v121
mva 30,54,32
v_mul_f32 v120, v119, v100
v_fma_f32 v40, v40, v118, v120
mav 22,36,58
R 74,39936
v_mul_f32 v121, v119, v101
mav 26,40,58
v_fma_f32 v41, v41, v118, v121
v_cvt_pk_bf16_f32 v110, v34, v35
mva 30,58,44
v_cvt_pk_bf16_f32 v111, v36, v37
s_waitcnt lgkmcnt(2)
mav 22,48,62
R 46,40960
v_cvt_pk_bf16_f32 v112, v38, v39
mav 26,52,62
v_cvt_pk_bf16_f32 v113, v40, v41
v_mul_f32 v103, v103, v116
mva 30,62,56
ds_write_b128 v83, v[110:113] offset:4096
mav 22,60,66
R 50,41984
v_fma_f32 v42, v42, v102, v103
mav 26,64,66
v_mul_f32 v105, v105, v116
v_fma_f32 v43, v43, v104, v105
mva 30,66,68
v_mul_f32 v107, v107, v116
s_waitcnt lgkmcnt(3)
mav 22,72,70
R 54,43008
v_fma_f32 v44, v44, v106, v107
mav 26,76,70
v_mul_f32 v109, v109, v116
v_fma_f32 v45, v45, v108, v109
mva 30,70,80
v_cvt_pk_bf16_f32 v78, v42, v43
v_cvt_pk_bf16_f32 v79, v44, v45
mav 22,84,74
R 58,44032
mav 26,88,74
ds_write_b64 v83, v[78:79] offset:5120
mva 30,74,92
s_waitcnt lgkmcnt(3)
mav 22,96,46
R 62,45056
s_add_u32 s49, s49, 346112
mav 26,100,46
ds_write_b64 v84, v[14:15] offset:0
mva 30,46,104
mav 22,108,50
R 66,46080
s_add_u32 m0, s50, 0
mav 26,112,50
s_waitcnt lgkmcnt(1)
s_barrier
mva 30,50,116
buffer_load_dwordx2 v[14:15], v93, s[36:39], s49 offen sc1
D 86,0
mav 22,120,54
R 70,47104
mav 26,124,54
D 86,1024
mva 30,54,128
mav 22,132,58
R 74,48128
mav 26,136,58
D 86,2048
mva 30,58,140
mav 22,144,62
R 46,49152
D 86,3072
mav 26,148,62
s_add_u32 m0, s50, 8192
mva 30,62,152
s_waitcnt lgkmcnt(2)
mav 22,156,66
R 50,50176
D 87,0
mav 26,160,66
mva 30,66,164
D 87,1024
mav 22,168,70
R 54,51200
mav 26,172,70
mva 30,70,176
D 87,2048
s_waitcnt lgkmcnt(2)
mav 22,180,74
R 58,52224
mav 26,184,74
D 87,3072
mva 30,74,188
s_add_u32 m0, s50, 16384
mav 22,192,46
R 62,53248
mav 26,196,46
D 88,0
mva 30,46,200
s_waitcnt lgkmcnt(2)
mav 22,204,50
R 66,54272
D 88,1024
mav 26,208,50
mva 30,50,212
mav 22,216,54
R 70,55296
D 88,2048
mav 26,220,54
mva 30,54,224
D 88,3072
s_waitcnt lgkmcnt(2)
mav 22,228,58
R 74,56320
s_add_u32 m0, s50, 24576
mav 26,232,58
mva 30,58,236
D 89,0
mav 22,240,62
R 46,57344
mav 26,244,62
D 89,1024
mva 30,62,248
s_waitcnt lgkmcnt(2)
mav 22,252,66
R 50,58368
mvv 26,128,66
D 89,2048
mvv 30,66,132
mvv 22,136,70
R 54,59392
D 89,3072
mvv 26,140,70
mvv 30,70,144
s_waitcnt lgkmcnt(2)
mvv 22,148,74
R 58,60416
mvv 26,152,74
mvv 30,74,156
mvv 22,160,46
R 62,61440
mvv 26,164,46
mvv 30,46,168
s_waitcnt lgkmcnt(2)
mvv 22,172,50
R 66,62464
mvv 26,176,50
s_waitcnt vmcnt(16)
v_permlane32_swap_b32 v14, v15
mvv 30,50,180
v_add_f32 v16, v14, v15
v_add_f32 v17, v14, v15
mvv 22,184,54
R 70,63488
v_permlane16_swap_b32 v16, v17
mvv 26,188,54
v_add_f32 v14, v16, v17
v_add_f32 v15, v16, v17
mvv 30,54,192
s_waitcnt lgkmcnt(2)
mvv 22,196,58
R 74,64512
v_permlane32_swap_b32 v14, v15
mvv 26,200,58
v_mul_f32 v16, 0x3a800000, v14
v_mul_f32 v17, 0x3a800000, v15
mvv 30,58,204
v_fma_f32 v17, -v16, v16, v17
v_max_f32 v17, 0, v17
mvv 22,208,62
R 46,0
v_add_f32 v17, 0x3727c5ac, v17
mvv 26,212,62
ds_read_b64 v[118:119], v84 offset:0
ds_read_b128 v[102:105], v85 offset:0
mvv 30,62,216
ds_read_b128 v[106:109], v85 offset:16
s_waitcnt lgkmcnt(5)
mvv 22,220,66
R 50,1024
mvv 26,224,66
v_rsq_f32 v14, v17
mvv 30,66,228
v_mul_f32_e64 v15, -v16, v14
mvv 22,232,70
R 54,2048
mvv 26,236,70
mvv 30,70,240
s_waitcnt lgkmcnt(2)
mvv 22,244,74
R 58,3072
mvv 26,248,74
mvv 30,74,252
v_mfma_f32_16x16x32_bf16 v[34:37], a[0:3], v[46:49], 0
R 62,4096
v_mul_f32 v120, v119, v94
v_mfma_f32_16x16x32_bf16 v[38:41], a[4:7], v[46:49], 0
v_mul_f32 v121, v119, v95
v_fma_f32 v22, v22, v118, v120
v_mfma_f32_16x16x32_bf16 v[42:45], v[46:49], a[8:11], 0
v_fma_f32 v23, v23, v118, v121
s_waitcnt lgkmcnt(2)
mav 34,12,50
R 66,5120
v_mul_f32 v120, v119, v96
mav 38,16,50
v_fma_f32 v24, v24, v118, v120
v_mul_f32 v121, v119, v97
mva 42,50,20
v_fma_f32 v25, v25, v118, v121
v_mul_f32 v120, v119, v98
mav 34,24,54
R 70,6144
v_fma_f32 v26, v26, v118, v120
mav 38,28,54
v_mul_f32 v121, v119, v99
v_fma_f32 v27, v27, v118, v121
mva 42,54,32
v_mul_f32 v120, v119, v100
s_waitcnt lgkmcnt(2)
mav 34,36,58
R 74,7168
v_fma_f32 v28, v28, v118, v120
mav 38,40,58
v_mul_f32 v121, v119, v101
v_fma_f32 v29, v29, v118, v121
mva 42,58,44
v_cvt_pk_bf16_f32 v110, v22, v23
v_cvt_pk_bf16_f32 v111, v24, v25
mav 34,48,62
R 46,8192
v_cvt_pk_bf16_f32 v112, v26, v27
mav 38,52,62
v_cvt_pk_bf16_f32 v113, v28, v29
v_mul_f32 v103, v103, v116
mva 42,62,56
v_fma_f32 v30, v30, v102, v103
s_waitcnt lgkmcnt(2)
mav 34,60,66
R 50,9216
v_mul_f32 v105, v105, v116
mav 38,64,66
ds_write_b128 v83, v[110:113] offset:0
mva 42,66,68
v_fma_f32 v31, v31, v104, v105
v_mul_f32 v107, v107, v116
mav 34,72,70
R 54,10240
v_fma_f32 v32, v32, v106, v107
mav 38,76,70
v_mul_f32 v109, v109, v116
v_fma_f32 v33, v33, v108, v109
mva 42,70,80
s_xor_b32 s50, s50, 0x10000
s_waitcnt lgkmcnt(3)
mav 34,84,74
R 58,11264
v_cvt_pk_bf16_f32 v78, v30, v31
mav 38,88,74
ds_write_b64 v84, v[14:15] offset:128
mva 42,74,92
v_cvt_pk_bf16_f32 v79, v32, v33
mav 34,96,46
R 62,12288
s_add_u32 s49, s49, 346112
mav 38,100,46
ds_write_b64 v83, v[78:79] offset:1024
mva 42,46,104
s_waitcnt lgkmcnt(4)
mav 34,108,50
R 66,13312
s_add_u32 m0, s50, 32768
mav 38,112,50
s_waitcnt lgkmcnt(1)
s_barrier
mva 42,50,116
buffer_load_dwordx2 v[14:15], v93, s[36:39], s49 offen sc1
D 86,0
mav 34,120,54
R 70,14336
mav 38,124,54
D 86,1024
mva 42,54,128
mav 34,132,58
R 74,15360
mav 38,136,58
D 86,2048
mva 42,58,140
mav 34,144,62
R 46,16384
D 86,3072
mav 38,148,62
s_add_u32 m0, s50, 40960
mva 42,62,152
s_waitcnt lgkmcnt(2)
mav 34,156,66
R 50,17408
D 87,0
mav 38,160,66
mva 42,66,164
D 87,1024
mav 34,168,70
R 54,18432
mav 38,172,70
mva 42,70,176
D 87,2048
s_waitcnt lgkmcnt(2)
mav 34,180,74
R 58,19456
mav 38,184,74
D 87,3072
mva 42,74,188
s_add_u32 m0, s50, 49152
mav 34,192,46
R 62,20480
mav 38,196,46
D 88,0
mva 42,46,200
s_waitcnt lgkmcnt(2)
mav 34,204,50
R 66,21504
D 88,1024
mav 38,208,50
mva 42,50,212
mav 34,216,54
R 70,22528
D 88,2048
mav 38,220,54
mva 42,54,224
D 88,3072
s_waitcnt lgkmcnt(2)
mav 34,228,58
R 74,23552
s_add_u32 m0, s50, 57344
mav 38,232,58
mva 42,58,236
D 89,0
mav 34,240,62
R 46,24576
mav 38,244,62
D 89,1024
mva 42,62,248
s_waitcnt lgkmcnt(2)
mav 34,252,66
R 50,25600
mvv 38,128,66
D 89,2048
mvv 42,66,132
mvv 34,136,70
R 54,26624
D 89,3072
mvv 38,140,70
mvv 42,70,144
s_waitcnt lgkmcnt(2)
mvv 34,148,74
R 58,27648
mvv 38,152,74
mvv 42,74,156
mvv 34,160,46
R 62,28672
mvv 38,164,46
mvv 42,46,168
s_waitcnt lgkmcnt(2)
mvv 34,172,50
R 66,29696
mvv 38,176,50
s_waitcnt vmcnt(16)
v_permlane32_swap_b32 v14, v15
mvv 42,50,180
v_add_f32 v16, v14, v15
v_add_f32 v17, v14, v15
mvv 34,184,54
R 70,30720
v_permlane16_swap_b32 v16, v17
mvv 38,188,54
v_add_f32 v14, v16, v17
v_add_f32 v15, v16, v17
mvv 42,54,192
s_waitcnt lgkmcnt(2)
mvv 34,196,58
R 74,31744
v_xor_b32 v80, 0x10000, v80
mvv 38,200,58
v_permlane32_swap_b32 v14, v15
v_mul_f32 v16, 0x3a800000, v14
mvv 42,58,204
v_mul_f32 v17, 0x3a800000, v15
v_fma_f32 v17, -v16, v16, v17
mvv 34,208,62
R 46,32768
v_max_f32 v17, 0, v17
mvv 38,212,62
ds_read_b64 v[118:119], v84 offset:128
ds_read_b128 v[102:105], v85 offset:128
mvv 42,62,216
ds_read_b128 v[106:109], v85 offset:144
s_waitcnt lgkmcnt(5)
mvv 34,220,66
R 50,33792
v_add_f32 v17, 0x3727c5ac, v17
mvv 38,224,66
v_rsq_f32 v14, v17
mvv 42,66,228
v_mul_f32_e64 v15, -v16, v14
mvv 34,232,70
R 54,34816
mvv 38,236,70
mvv 42,70,240
s_waitcnt lgkmcnt(2)
mvv 34,244,74
R 58,35840
mvv 38,248,74
mvv 42,74,252
s_mov_b32 s60, -27
.p2align 6
LQB1_1:
v_mfma_f32_16x16x32_bf16 v[22:25], a[0:3], v[46:49], 0
R 62,36864
v_mul_f32 v120, v119, v94
v_mfma_f32_16x16x32_bf16 v[26:29], a[4:7], v[46:49], 0
v_mul_f32 v121, v119, v95
v_fma_f32 v34, v34, v118, v120
v_mfma_f32_16x16x32_bf16 v[30:33], v[46:49], a[8:11], 0
v_fma_f32 v35, v35, v118, v121
s_waitcnt lgkmcnt(2)
mav 22,12,50
R 66,37888
v_mul_f32 v120, v119, v96
mav 26,16,50
v_fma_f32 v36, v36, v118, v120
v_mul_f32 v121, v119, v97
mva 30,50,20
v_fma_f32 v37, v37, v118, v121
v_mul_f32 v120, v119, v98
mav 22,24,54
R 70,38912
v_fma_f32 v38, v38, v118, v120
mav 26,28,54
v_mul_f32 v121, v119, v99
v_fma_f32 v39, v39, v118, v121
mva 30,54,32
v_mul_f32 v120, v119, v100
s_waitcnt lgkmcnt(2)
mav 22,36,58
R 74,39936
v_fma_f32 v40, v40, v118, v120
mav 26,40,58
v_mul_f32 v121, v119, v101
v_fma_f32 v41, v41, v118, v121
mva 30,58,44
v_cvt_pk_bf16_f32 v110, v34, v35
v_cvt_pk_bf16_f32 v111, v36, v37
mav 22,48,62
R 46,40960
v_cvt_pk_bf16_f32 v112, v38, v39
mav 26,52,62
v_cvt_pk_bf16_f32 v113, v40, v41
v_mul_f32 v103, v103, v116
mva 30,62,56
v_fma_f32 v42, v42, v102, v103
s_waitcnt lgkmcnt(2)
mav 22,60,66
R 50,41984
v_mul_f32 v105, v105, v116
mav 26,64,66
ds_write_b128 v83, v[110:113] offset:4096
mva 30,66,68
v_fma_f32 v43, v43, v104, v105
v_mul_f32 v107, v107, v116
mav 22,72,70
R 54,43008
v_fma_f32 v44, v44, v106, v107
mav 26,76,70
v_mul_f32 v109, v109, v116
v_fma_f32 v45, v45, v108, v109
mva 30,70,80
v_cvt_pk_bf16_f32 v78, v42, v43
s_waitcnt lgkmcnt(3)
mav 22,84,74
R 58,44032
v_cvt_pk_bf16_f32 v79, v44, v45
mav 26,88,74
ds_write_b64 v83, v[78:79] offset:5120
mva 30,74,92
ds_write_b64 v84, v[14:15] offset:0
mav 22,96,46
R 62,45056
s_add_u32 s49, s49, 346112
mav 26,100,46
mva 30,46,104
s_waitcnt lgkmcnt(1)
s_barrier
mav 22,108,50
buffer_load_dwordx2 v[14:15], v93, s[36:39], s49 offen sc1
R 66,46080
mav 26,112,50
s_add_u32 m0, s50, 0
mva 30,50,116
D 86,0
mav 22,120,54
R 70,47104
mav 26,124,54
D 86,1024
mva 30,54,128
mav 22,132,58
R 74,48128
mav 26,136,58
D 86,2048
mva 30,58,140
s_waitcnt lgkmcnt(2)
mav 22,144,62
R 46,49152
D 86,3072
mav 26,148,62
s_add_u32 m0, s50, 8192
mva 30,62,152
mav 22,156,66
R 50,50176
D 87,0
mav 26,160,66
mva 30,66,164
D 87,1024
s_waitcnt lgkmcnt(2)
mav 22,168,70
R 54,51200
mav 26,172,70
mva 30,70,176
D 87,2048
mav 22,180,74
R 58,52224
mav 26,184,74
D 87,3072
mva 30,74,188
s_add_u32 m0, s50, 16384
s_waitcnt lgkmcnt(2)
mav 22,192,46
R 62,53248
mav 26,196,46
D 88,0
mva 30,46,200
mav 22,204,50
R 66,54272
D 88,1024
mav 26,208,50
mva 30,50,212
s_waitcnt lgkmcnt(2)
mav 22,216,54
R 70,55296
D 88,2048
mav 26,220,54
mva 30,54,224
D 88,3072
mav 22,228,58
R 74,56320
s_add_u32 m0, s50, 24576
mav 26,232,58
mva 30,58,236
D 89,0
s_waitcnt lgkmcnt(2)
mav 22,240,62
R 46,57344
mav 26,244,62
D 89,1024
mva 30,62,248
mav 22,252,66
R 50,58368
mvv 26,128,66
D 89,2048
mvv 30,66,132
s_waitcnt lgkmcnt(2)
mvv 22,136,70
R 54,59392
D 89,3072
mvv 26,140,70
mvv 30,70,144
mvv 22,148,74
R 58,60416
mvv 26,152,74
mvv 30,74,156
s_waitcnt lgkmcnt(2)
mvv 22,160,46
R 62,61440
mvv 26,164,46
mvv 30,46,168
s_waitcnt vmcnt(16)
v_permlane32_swap_b32 v14, v15
mvv 22,172,50
R 66,62464
v_add_f32 v16, v14, v15
mvv 26,176,50
v_add_f32 v17, v14, v15
mvv 30,50,180
s_waitcnt lgkmcnt(2)
mvv 22,184,54
R 70,63488
v_permlane16_swap_b32 v16, v17
mvv 26,188,54
v_add_f32 v14, v16, v17
v_add_f32 v15, v16, v17
mvv 30,54,192
mvv 22,196,58
R 74,64512
v_permlane32_swap_b32 v14, v15
mvv 26,200,58
v_mul_f32 v16, 0x3a800000, v14
v_mul_f32 v17, 0x3a800000, v15
mvv 30,58,204
v_fma_f32 v17, -v16, v16, v17
s_waitcnt lgkmcnt(2)
mvv 22,208,62
R 46,0
v_max_f32 v17, 0, v17
mvv 26,212,62
ds_read_b64 v[118:119], v84 offset:0
ds_read_b128 v[102:105], v85 offset:0
mvv 30,62,216
ds_read_b128 v[106:109], v85 offset:16
v_add_f32 v17, 0x3727c5ac, v17
mvv 22,220,66
R 50,1024
mvv 26,224,66
v_rsq_f32 v14, v17
mvv 30,66,228
v_mul_f32_e64 v15, -v16, v14
s_waitcnt lgkmcnt(5)
mvv 22,232,70
R 54,2048
mvv 26,236,70
mvv 30,70,240
mvv 22,244,74
R 58,3072
mvv 26,248,74
mvv 30,74,252
s_waitcnt lgkmcnt(2)
v_mfma_f32_16x16x32_bf16 v[34:37], a[0:3], v[46:49], 0
R 62,4096
v_mul_f32 v120, v119, v94
v_mfma_f32_16x16x32_bf16 v[38:41], a[4:7], v[46:49], 0
v_fma_f32 v22, v22, v118, v120
v_mul_f32 v121, v119, v95
v_mfma_f32_16x16x32_bf16 v[42:45], v[46:49], a[8:11], 0
v_fma_f32 v23, v23, v118, v121
v_mul_f32 v120, v119, v96
mav 34,12,50
R 66,5120
v_fma_f32 v24, v24, v118, v120
mav 38,16,50
v_mul_f32 v121, v119, v97
v_fma_f32 v25, v25, v118, v121
mva 42,50,20
v_mul_f32 v120, v119, v98
s_waitcnt lgkmcnt(2)
mav 34,24,54
R 70,6144
v_fma_f32 v26, v26, v118, v120
mav 38,28,54
v_mul_f32 v121, v119, v99
v_fma_f32 v27, v27, v118, v121
mva 42,54,32
v_mul_f32 v120, v119, v100
v_fma_f32 v28, v28, v118, v120
mav 34,36,58
R 74,7168
v_mul_f32 v121, v119, v101
mav 38,40,58
v_fma_f32 v29, v29, v118, v121
v_cvt_pk_bf16_f32 v110, v22, v23
mva 42,58,44
v_cvt_pk_bf16_f32 v111, v24, v25
s_waitcnt lgkmcnt(2)
mav 34,48,62
R 46,8192
v_cvt_pk_bf16_f32 v112, v26, v27
mav 38,52,62
v_cvt_pk_bf16_f32 v113, v28, v29
v_mul_f32 v103, v103, v116
mva 42,62,56
ds_write_b128 v83, v[110:113] offset:0
mav 34,60,66
R 50,9216
v_fma_f32 v30, v30, v102, v103
mav 38,64,66
v_mul_f32 v105, v105, v116
v_fma_f32 v31, v31, v104, v105
mva 42,66,68
v_mul_f32 v107, v107, v116
s_waitcnt lgkmcnt(3)
mav 34,72,70
R 54,10240
v_fma_f32 v32, v32, v106, v107
mav 38,76,70
v_mul_f32 v109, v109, v116
v_fma_f32 v33, v33, v108, v109
mva 42,70,80
s_xor_b32 s50, s50, 0x10000
v_cvt_pk_bf16_f32 v78, v30, v31
mav 34,84,74
R 58,11264
v_cvt_pk_bf16_f32 v79, v32, v33
mav 38,88,74
ds_write_b64 v83, v[78:79] offset:1024
mva 42,74,92
s_waitcnt lgkmcnt(3)
mav 34,96,46
R 62,12288
s_add_u32 s49, s49, 346112
mav 38,100,46
ds_write_b64 v84, v[14:15] offset:128
mva 42,46,104
mav 34,108,50
R 66,13312
s_add_u32 m0, s50, 32768
mav 38,112,50
s_waitcnt lgkmcnt(1)
s_barrier
mva 42,50,116
buffer_load_dwordx2 v[14:15], v93, s[36:39], s49 offen sc1
D 86,0
mav 34,120,54
R 70,14336
mav 38,124,54
D 86,1024
mva 42,54,128
mav 34,132,58
R 74,15360
mav 38,136,58
D 86,2048
mva 42,58,140
mav 34,144,62
R 46,16384
D 86,3072
mav 38,148,62
s_add_u32 m0, s50, 40960
mva 42,62,152
s_waitcnt lgkmcnt(2)
mav 34,156,66
R 50,17408
D 87,0
mav 38,160,66
mva 42,66,164
D 87,1024
mav 34,168,70
R 54,18432
mav 38,172,70
mva 42,70,176
D 87,2048
s_waitcnt lgkmcnt(2)
mav 34,180,74
R 58,19456
mav 38,184,74
D 87,3072
mva 42,74,188
)"
R"(
s_add_u32 m0, s50, 49152
mav 34,192,46
R 62,20480
mav 38,196,46
D 88,0
mva 42,46,200
s_waitcnt lgkmcnt(2)
mav 34,204,50
R 66,21504
D 88,1024
mav 38,208,50
mva 42,50,212
mav 34,216,54
R 70,22528
D 88,2048
mav 38,220,54
mva 42,54,224
D 88,3072
s_waitcnt lgkmcnt(2)
mav 34,228,58
R 74,23552
s_add_u32 m0, s50, 57344
mav 38,232,58
mva 42,58,236
D 89,0
mav 34,240,62
R 46,24576
mav 38,244,62
D 89,1024
mva 42,62,248
s_waitcnt lgkmcnt(2)
mav 34,252,66
R 50,25600
mvv 38,128,66
D 89,2048
mvv 42,66,132
mvv 34,136,70
R 54,26624
D 89,3072
mvv 38,140,70
mvv 42,70,144
s_waitcnt lgkmcnt(2)
mvv 34,148,74
R 58,27648
mvv 38,152,74
mvv 42,74,156
mvv 34,160,46
R 62,28672
mvv 38,164,46
mvv 42,46,168
s_waitcnt lgkmcnt(2)
mvv 34,172,50
R 66,29696
mvv 38,176,50
s_waitcnt vmcnt(16)
v_permlane32_swap_b32 v14, v15
mvv 42,50,180
v_add_f32 v16, v14, v15
v_add_f32 v17, v14, v15
mvv 34,184,54
R 70,30720
v_permlane16_swap_b32 v16, v17
mvv 38,188,54
v_add_f32 v14, v16, v17
v_add_f32 v15, v16, v17
mvv 42,54,192
s_waitcnt lgkmcnt(2)
mvv 34,196,58
R 74,31744
v_xor_b32 v80, 0x10000, v80
mvv 38,200,58
v_permlane32_swap_b32 v14, v15
v_mul_f32 v16, 0x3a800000, v14
mvv 42,58,204
v_mul_f32 v17, 0x3a800000, v15
v_fma_f32 v17, -v16, v16, v17
mvv 34,208,62
R 46,32768
v_max_f32 v17, 0, v17
mvv 38,212,62
ds_read_b64 v[118:119], v84 offset:128
ds_read_b128 v[102:105], v85 offset:128
mvv 42,62,216
ds_read_b128 v[106:109], v85 offset:144
s_waitcnt lgkmcnt(5)
mvv 34,220,66
R 50,33792
v_add_f32 v17, 0x3727c5ac, v17
mvv 38,224,66
v_rsq_f32 v14, v17
mvv 42,66,228
v_mul_f32_e64 v15, -v16, v14
mvv 34,232,70
R 54,34816
mvv 38,236,70
mvv 42,70,240
s_waitcnt lgkmcnt(2)
mvv 34,244,74
R 58,35840
mvv 38,248,74
mvv 42,74,252
s_add_u32 s60, s60, 1
s_cbranch_scc0 LQB1_1
v_mfma_f32_16x16x32_bf16 v[22:25], a[0:3], v[46:49], 0
R 62,36864
v_mul_f32 v120, v119, v94
v_mfma_f32_16x16x32_bf16 v[26:29], a[4:7], v[46:49], 0
v_mul_f32 v121, v119, v95
v_fma_f32 v34, v34, v118, v120
v_mfma_f32_16x16x32_bf16 v[30:33], v[46:49], a[8:11], 0
v_fma_f32 v35, v35, v118, v121
s_waitcnt lgkmcnt(2)
mav 22,12,50
R 66,37888
v_mul_f32 v120, v119, v96
mav 26,16,50
v_fma_f32 v36, v36, v118, v120
v_mul_f32 v121, v119, v97
mva 30,50,20
v_fma_f32 v37, v37, v118, v121
v_mul_f32 v120, v119, v98
mav 22,24,54
R 70,38912
v_fma_f32 v38, v38, v118, v120
mav 26,28,54
v_mul_f32 v121, v119, v99
v_fma_f32 v39, v39, v118, v121
mva 30,54,32
v_mul_f32 v120, v119, v100
s_waitcnt lgkmcnt(2)
mav 22,36,58
R 74,39936
v_fma_f32 v40, v40, v118, v120
mav 26,40,58
v_mul_f32 v121, v119, v101
v_fma_f32 v41, v41, v118, v121
mva 30,58,44
v_cvt_pk_bf16_f32 v110, v34, v35
v_cvt_pk_bf16_f32 v111, v36, v37
mav 22,48,62
R 46,40960
v_cvt_pk_bf16_f32 v112, v38, v39
mav 26,52,62
v_cvt_pk_bf16_f32 v113, v40, v41
v_mul_f32 v103, v103, v116
mva 30,62,56
v_fma_f32 v42, v42, v102, v103
s_waitcnt lgkmcnt(2)
mav 22,60,66
R 50,41984
v_mul_f32 v105, v105, v116
mav 26,64,66
ds_write_b128 v83, v[110:113] offset:4096
mva 30,66,68
v_fma_f32 v43, v43, v104, v105
v_mul_f32 v107, v107, v116
mav 22,72,70
R 54,43008
v_fma_f32 v44, v44, v106, v107
mav 26,76,70
v_mul_f32 v109, v109, v116
v_fma_f32 v45, v45, v108, v109
mva 30,70,80
v_cvt_pk_bf16_f32 v78, v42, v43
s_waitcnt lgkmcnt(3)
mav 22,84,74
R 58,44032
v_cvt_pk_bf16_f32 v79, v44, v45
mav 26,88,74
ds_write_b64 v83, v[78:79] offset:5120
mva 30,74,92
ds_write_b64 v84, v[14:15] offset:0
mav 22,96,46
R 62,45056
s_add_u32 s49, s49, 346112
mav 26,100,46
mva 30,46,104
s_waitcnt lgkmcnt(1)
s_barrier
mav 22,108,50
buffer_load_dwordx2 v[14:15], v93, s[36:39], s49 offen sc1
R 66,46080
mav 26,112,50
s_add_u32 m0, s50, 0
mva 30,50,116
D 86,0
mav 22,120,54
R 70,47104
mav 26,124,54
D 86,1024
mva 30,54,128
mav 22,132,58
R 74,48128
mav 26,136,58
D 86,2048
mva 30,58,140
s_waitcnt lgkmcnt(2)
mav 22,144,62
R 46,49152
D 86,3072
mav 26,148,62
s_add_u32 m0, s50, 8192
mva 30,62,152
mav 22,156,66
R 50,50176
D 87,0
mav 26,160,66
mva 30,66,164
D 87,1024
s_waitcnt lgkmcnt(2)
mav 22,168,70
R 54,51200
mav 26,172,70
mva 30,70,176
D 87,2048
mav 22,180,74
R 58,52224
mav 26,184,74
D 87,3072
mva 30,74,188
s_add_u32 m0, s50, 16384
s_waitcnt lgkmcnt(2)
mav 22,192,46
R 62,53248
mav 26,196,46
D 88,0
mva 30,46,200
mav 22,204,50
R 66,54272
D 88,1024
mav 26,208,50
mva 30,50,212
s_waitcnt lgkmcnt(2)
mav 22,216,54
R 70,55296
D 88,2048
mav 26,220,54
mva 30,54,224
D 88,3072
mav 22,228,58
R 74,56320
s_add_u32 m0, s50, 24576
mav 26,232,58
mva 30,58,236
D 89,0
s_waitcnt lgkmcnt(2)
mav 22,240,62
R 46,57344
mav 26,244,62
D 89,1024
mva 30,62,248
mav 22,252,66
R 50,58368
mvv 26,128,66
D 89,2048
mvv 30,66,132
s_waitcnt lgkmcnt(2)
mvv 22,136,70
R 54,59392
D 89,3072
mvv 26,140,70
mvv 30,70,144
mvv 22,148,74
R 58,60416
mvv 26,152,74
mvv 30,74,156
s_waitcnt lgkmcnt(2)
mvv 22,160,46
R 62,61440
mvv 26,164,46
mvv 30,46,168
s_waitcnt vmcnt(16)
v_permlane32_swap_b32 v14, v15
mvv 22,172,50
R 66,62464
v_add_f32 v16, v14, v15
mvv 26,176,50
v_add_f32 v17, v14, v15
mvv 30,50,180
s_waitcnt lgkmcnt(2)
mvv 22,184,54
R 70,63488
v_permlane16_swap_b32 v16, v17
mvv 26,188,54
v_add_f32 v14, v16, v17
v_add_f32 v15, v16, v17
mvv 30,54,192
mvv 22,196,58
R 74,64512
v_permlane32_swap_b32 v14, v15
mvv 26,200,58
v_mul_f32 v16, 0x3a800000, v14
v_mul_f32 v17, 0x3a800000, v15
mvv 30,58,204
v_fma_f32 v17, -v16, v16, v17
s_waitcnt lgkmcnt(2)
mvv 22,208,62
R 46,0
v_max_f32 v17, 0, v17
mvv 26,212,62
ds_read_b64 v[118:119], v84 offset:0
ds_read_b128 v[102:105], v85 offset:0
mvv 30,62,216
ds_read_b128 v[106:109], v85 offset:16
v_add_f32 v17, 0x3727c5ac, v17
mvv 22,220,66
R 50,1024
mvv 26,224,66
v_rsq_f32 v14, v17
mvv 30,66,228
v_mul_f32_e64 v15, -v16, v14
s_waitcnt lgkmcnt(5)
mvv 22,232,70
R 54,2048
mvv 26,236,70
mvv 30,70,240
mvv 22,244,74
R 58,3072
mvv 26,248,74
mvv 30,74,252
s_waitcnt lgkmcnt(2)
v_mfma_f32_16x16x32_bf16 v[34:37], a[0:3], v[46:49], 0
R 62,4096
v_mul_f32 v120, v119, v94
v_mfma_f32_16x16x32_bf16 v[38:41], a[4:7], v[46:49], 0
v_fma_f32 v22, v22, v118, v120
v_mul_f32 v121, v119, v95
v_mfma_f32_16x16x32_bf16 v[42:45], v[46:49], a[8:11], 0
v_fma_f32 v23, v23, v118, v121
v_mul_f32 v120, v119, v96
mav 34,12,50
R 66,5120
v_fma_f32 v24, v24, v118, v120
mav 38,16,50
v_mul_f32 v121, v119, v97
v_fma_f32 v25, v25, v118, v121
mva 42,50,20
v_mul_f32 v120, v119, v98
s_waitcnt lgkmcnt(2)
mav 34,24,54
R 70,6144
v_fma_f32 v26, v26, v118, v120
mav 38,28,54
v_mul_f32 v121, v119, v99
v_fma_f32 v27, v27, v118, v121
mva 42,54,32
v_mul_f32 v120, v119, v100
v_fma_f32 v28, v28, v118, v120
mav 34,36,58
R 74,7168
v_mul_f32 v121, v119, v101
mav 38,40,58
v_fma_f32 v29, v29, v118, v121
v_cvt_pk_bf16_f32 v110, v22, v23
mva 42,58,44
v_cvt_pk_bf16_f32 v111, v24, v25
s_waitcnt lgkmcnt(2)
mav 34,48,62
R 46,8192
v_cvt_pk_bf16_f32 v112, v26, v27
mav 38,52,62
v_cvt_pk_bf16_f32 v113, v28, v29
v_mul_f32 v103, v103, v116
mva 42,62,56
ds_write_b128 v83, v[110:113] offset:0
mav 34,60,66
R 50,9216
v_fma_f32 v30, v30, v102, v103
mav 38,64,66
v_mul_f32 v105, v105, v116
v_fma_f32 v31, v31, v104, v105
mva 42,66,68
v_mul_f32 v107, v107, v116
s_waitcnt lgkmcnt(3)
mav 34,72,70
R 54,10240
v_fma_f32 v32, v32, v106, v107
mav 38,76,70
v_mul_f32 v109, v109, v116
v_fma_f32 v33, v33, v108, v109
mva 42,70,80
v_cvt_pk_bf16_f32 v78, v30, v31
v_cvt_pk_bf16_f32 v79, v32, v33
mav 34,84,74
R 58,11264
mav 38,88,74
ds_write_b64 v83, v[78:79] offset:1024
mva 42,74,92
s_waitcnt lgkmcnt(3)
mav 34,96,46
R 62,12288
s_add_u32 s49, s49, 346112
mav 38,100,46
ds_write_b64 v84, v[14:15] offset:128
mva 42,46,104
mav 34,108,50
R 66,13312
mav 38,112,50
s_waitcnt lgkmcnt(1)
s_barrier
mva 42,50,116
buffer_load_dwordx2 v[14:15], v93, s[36:39], s49 offen sc1
mav 34,120,54
R 70,14336
mav 38,124,54
mva 42,54,128
mav 34,132,58
R 74,15360
mav 38,136,58
mva 42,58,140
mav 34,144,62
R 46,16384
mav 38,148,62
mva 42,62,152
s_waitcnt lgkmcnt(2)
mav 34,156,66
R 50,17408
mav 38,160,66
mva 42,66,164
mav 34,168,70
R 54,18432
mav 38,172,70
mva 42,70,176
s_waitcnt lgkmcnt(2)
mav 34,180,74
R 58,19456
mav 38,184,74
mva 42,74,188
mav 34,192,46
R 62,20480
mav 38,196,46
mva 42,46,200
s_waitcnt lgkmcnt(2)
mav 34,204,50
R 66,21504
mav 38,208,50
mva 42,50,212
mav 34,216,54
R 70,22528
mav 38,220,54
mva 42,54,224
s_waitcnt lgkmcnt(2)
mav 34,228,58
R 74,23552
mav 38,232,58
mva 42,58,236
mav 34,240,62
R 46,24576
mav 38,244,62
mva 42,62,248
s_waitcnt lgkmcnt(2)
mav 34,252,66
R 50,25600
mvv 38,128,66
mvv 42,66,132
mvv 34,136,70
R 54,26624
mvv 38,140,70
mvv 42,70,144
s_waitcnt lgkmcnt(2)
mvv 34,148,74
R 58,27648
mvv 38,152,74
mvv 42,74,156
mvv 34,160,46
R 62,28672
mvv 38,164,46
mvv 42,46,168
s_waitcnt lgkmcnt(2)
mvv 34,172,50
R 66,29696
mvv 38,176,50
s_waitcnt vmcnt(0)
v_permlane32_swap_b32 v14, v15
mvv 42,50,180
v_add_f32 v16, v14, v15
v_add_f32 v17, v14, v15
mvv 34,184,54
R 70,30720
v_permlane16_swap_b32 v16, v17
mvv 38,188,54
v_add_f32 v14, v16, v17
v_add_f32 v15, v16, v17
mvv 42,54,192
s_waitcnt lgkmcnt(2)
mvv 34,196,58
R 74,31744
v_xor_b32 v80, 0x10000, v80
mvv 38,200,58
v_permlane32_swap_b32 v14, v15
v_mul_f32 v16, 0x3a800000, v14
mvv 42,58,204
v_mul_f32 v17, 0x3a800000, v15
v_fma_f32 v17, -v16, v16, v17
mvv 34,208,62
R 46,32768
v_max_f32 v17, 0, v17
mvv 38,212,62
ds_read_b64 v[118:119], v84 offset:128
ds_read_b128 v[102:105], v85 offset:128
mvv 42,62,216
ds_read_b128 v[106:109], v85 offset:144
s_waitcnt lgkmcnt(5)
mvv 34,220,66
R 50,33792
v_add_f32 v17, 0x3727c5ac, v17
mvv 38,224,66
v_rsq_f32 v14, v17
mvv 42,66,228
v_mul_f32_e64 v15, -v16, v14
mvv 34,232,70
R 54,34816
mvv 38,236,70
mvv 42,70,240
s_waitcnt lgkmcnt(2)
mvv 34,244,74
R 58,35840
mvv 38,248,74
mvv 42,74,252
v_mfma_f32_16x16x32_bf16 v[22:25], a[0:3], v[46:49], 0
R 62,36864
v_mul_f32 v120, v119, v94
v_mfma_f32_16x16x32_bf16 v[26:29], a[4:7], v[46:49], 0
v_mul_f32 v121, v119, v95
v_fma_f32 v34, v34, v118, v120
v_mfma_f32_16x16x32_bf16 v[30:33], v[46:49], a[8:11], 0
v_fma_f32 v35, v35, v118, v121
s_waitcnt lgkmcnt(2)
mav 22,12,50
R 66,37888
v_mul_f32 v120, v119, v96
mav 26,16,50
v_fma_f32 v36, v36, v118, v120
v_mul_f32 v121, v119, v97
mva 30,50,20
v_fma_f32 v37, v37, v118, v121
v_mul_f32 v120, v119, v98
mav 22,24,54
R 70,38912
v_fma_f32 v38, v38, v118, v120
mav 26,28,54
v_mul_f32 v121, v119, v99
v_fma_f32 v39, v39, v118, v121
mva 30,54,32
v_mul_f32 v120, v119, v100
s_waitcnt lgkmcnt(2)
mav 22,36,58
R 74,39936
v_fma_f32 v40, v40, v118, v120
mav 26,40,58
v_mul_f32 v121, v119, v101
v_fma_f32 v41, v41, v118, v121
mva 30,58,44
v_cvt_pk_bf16_f32 v110, v34, v35
v_cvt_pk_bf16_f32 v111, v36, v37
mav 22,48,62
R 46,40960
v_cvt_pk_bf16_f32 v112, v38, v39
mav 26,52,62
v_cvt_pk_bf16_f32 v113, v40, v41
v_mul_f32 v103, v103, v116
mva 30,62,56
v_fma_f32 v42, v42, v102, v103
s_waitcnt lgkmcnt(2)
mav 22,60,66
R 50,41984
v_mul_f32 v105, v105, v116
mav 26,64,66
ds_write_b128 v83, v[110:113] offset:4096
mva 30,66,68
v_fma_f32 v43, v43, v104, v105
v_mul_f32 v107, v107, v116
mav 22,72,70
R 54,43008
v_fma_f32 v44, v44, v106, v107
mav 26,76,70
v_mul_f32 v109, v109, v116
v_fma_f32 v45, v45, v108, v109
mva 30,70,80
v_cvt_pk_bf16_f32 v78, v42, v43
s_waitcnt lgkmcnt(3)
mav 22,84,74
R 58,44032
v_cvt_pk_bf16_f32 v79, v44, v45
mav 26,88,74
ds_write_b64 v83, v[78:79] offset:5120
mva 30,74,92
ds_write_b64 v84, v[14:15] offset:0
mav 22,96,46
R 62,45056
s_add_u32 s49, s49, 346112
mav 26,100,46
mva 30,46,104
s_waitcnt lgkmcnt(1)
s_barrier
mav 22,108,50
buffer_load_dwordx2 v[14:15], v93, s[36:39], s49 offen sc1
R 66,46080
mav 26,112,50
mva 30,50,116
mav 22,120,54
R 70,47104
mav 26,124,54
mva 30,54,128
mav 22,132,58
R 74,48128
mav 26,136,58
mva 30,58,140
s_waitcnt lgkmcnt(2)
mav 22,144,62
R 46,49152
mav 26,148,62
mva 30,62,152
mav 22,156,66
R 50,50176
mav 26,160,66
mva 30,66,164
s_waitcnt lgkmcnt(2)
mav 22,168,70
R 54,51200
mav 26,172,70
mva 30,70,176
mav 22,180,74
R 58,52224
mav 26,184,74
mva 30,74,188
s_waitcnt lgkmcnt(2)
mav 22,192,46
R 62,53248
mav 26,196,46
mva 30,46,200
mav 22,204,50
R 66,54272
mav 26,208,50
mva 30,50,212
s_waitcnt lgkmcnt(2)
mav 22,216,54
R 70,55296
mav 26,220,54
mva 30,54,224
mav 22,228,58
R 74,56320
mav 26,232,58
mva 30,58,236
s_waitcnt lgkmcnt(2)
mav 22,240,62
R 46,57344
mav 26,244,62
mva 30,62,248
mav 22,252,66
R 50,58368
mvv 26,128,66
mvv 30,66,132
s_waitcnt lgkmcnt(2)
mvv 22,136,70
R 54,59392
mvv 26,140,70
mvv 30,70,144
mvv 22,148,74
R 58,60416
mvv 26,152,74
mvv 30,74,156
s_waitcnt lgkmcnt(2)
mvv 22,160,46
R 62,61440
mvv 26,164,46
mvv 30,46,168
s_waitcnt vmcnt(0)
v_permlane32_swap_b32 v14, v15
mvv 22,172,50
R 66,62464
v_add_f32 v16, v14, v15
mvv 26,176,50
v_add_f32 v17, v14, v15
mvv 30,50,180
s_waitcnt lgkmcnt(2)
mvv 22,184,54
R 70,63488
v_permlane16_swap_b32 v16, v17
mvv 26,188,54
v_add_f32 v14, v16, v17
v_add_f32 v15, v16, v17
mvv 30,54,192
mvv 22,196,58
R 74,64512
v_permlane32_swap_b32 v14, v15
mvv 26,200,58
v_mul_f32 v16, 0x3a800000, v14
v_mul_f32 v17, 0x3a800000, v15
mvv 30,58,204
v_fma_f32 v17, -v16, v16, v17
s_waitcnt lgkmcnt(2)
mvv 22,208,62
R 46,0
v_max_f32 v17, 0, v17
mvv 26,212,62
ds_read_b64 v[118:119], v84 offset:0
ds_read_b128 v[102:105], v85 offset:0
mvv 30,62,216
ds_read_b128 v[106:109], v85 offset:16
v_add_f32 v17, 0x3727c5ac, v17
mvv 22,220,66
R 50,1024
mvv 26,224,66
v_rsq_f32 v14, v17
mvv 30,66,228
v_mul_f32_e64 v15, -v16, v14
s_waitcnt lgkmcnt(5)
mvv 22,232,70
R 54,2048
mvv 26,236,70
mvv 30,70,240
mvv 22,244,74
R 58,3072
mvv 26,248,74
mvv 30,74,252
s_waitcnt lgkmcnt(2)
v_mfma_f32_16x16x32_bf16 v[34:37], a[0:3], v[46:49], 0
R 62,4096
v_mul_f32 v120, v119, v94
v_mfma_f32_16x16x32_bf16 v[38:41], a[4:7], v[46:49], 0
v_fma_f32 v22, v22, v118, v120
v_mul_f32 v121, v119, v95
v_mfma_f32_16x16x32_bf16 v[42:45], v[46:49], a[8:11], 0
v_fma_f32 v23, v23, v118, v121
v_mul_f32 v120, v119, v96
mav 34,12,50
R 66,5120
v_fma_f32 v24, v24, v118, v120
mav 38,16,50
v_mul_f32 v121, v119, v97
v_fma_f32 v25, v25, v118, v121
mva 42,50,20
v_mul_f32 v120, v119, v98
s_waitcnt lgkmcnt(2)
mav 34,24,54
R 70,6144
v_fma_f32 v26, v26, v118, v120
mav 38,28,54
v_mul_f32 v121, v119, v99
v_fma_f32 v27, v27, v118, v121
mva 42,54,32
v_mul_f32 v120, v119, v100
v_fma_f32 v28, v28, v118, v120
mav 34,36,58
R 74,7168
v_mul_f32 v121, v119, v101
mav 38,40,58
v_fma_f32 v29, v29, v118, v121
v_cvt_pk_bf16_f32 v110, v22, v23
mva 42,58,44
v_cvt_pk_bf16_f32 v111, v24, v25
s_waitcnt lgkmcnt(2)
mav 34,48,62
R 46,8192
v_cvt_pk_bf16_f32 v112, v26, v27
mav 38,52,62
v_cvt_pk_bf16_f32 v113, v28, v29
v_mul_f32 v103, v103, v116
mva 42,62,56
ds_write_b128 v83, v[110:113] offset:0
mav 34,60,66
R 50,9216
v_fma_f32 v30, v30, v102, v103
mav 38,64,66
v_mul_f32 v105, v105, v116
v_fma_f32 v31, v31, v104, v105
mva 42,66,68
v_mul_f32 v107, v107, v116
s_waitcnt lgkmcnt(3)
mav 34,72,70
R 54,10240
v_fma_f32 v32, v32, v106, v107
mav 38,76,70
v_mul_f32 v109, v109, v116
v_fma_f32 v33, v33, v108, v109
mva 42,70,80
v_cvt_pk_bf16_f32 v78, v30, v31
v_cvt_pk_bf16_f32 v79, v32, v33
mav 34,84,74
R 58,11264
mav 38,88,74
ds_write_b64 v83, v[78:79] offset:1024
mva 42,74,92
s_waitcnt lgkmcnt(3)
mav 34,96,46
R 62,12288
mav 38,100,46
ds_write_b64 v84, v[14:15] offset:128
mva 42,46,104
mav 34,108,50
R 66,13312
mav 38,112,50
s_waitcnt lgkmcnt(1)
s_barrier
mva 42,50,116
mav 34,120,54
R 70,14336
mav 38,124,54
mva 42,54,128
mav 34,132,58
R 74,15360
mav 38,136,58
mva 42,58,140
mav 34,144,62
R 46,16384
mav 38,148,62
mva 42,62,152
s_waitcnt lgkmcnt(2)
mav 34,156,66
R 50,17408
mav 38,160,66
mva 42,66,164
mav 34,168,70
R 54,18432
mav 38,172,70
mva 42,70,176
s_waitcnt lgkmcnt(2)
mav 34,180,74
R 58,19456
mav 38,184,74
mva 42,74,188
mav 34,192,46
R 62,20480
mav 38,196,46
mva 42,46,200
s_waitcnt lgkmcnt(2)
mav 34,204,50
R 66,21504
mav 38,208,50
mva 42,50,212
mav 34,216,54
R 70,22528
mav 38,220,54
mva 42,54,224
s_waitcnt lgkmcnt(2)
mav 34,228,58
R 74,23552
mav 38,232,58
mva 42,58,236
mav 34,240,62
R 46,24576
mav 38,244,62
mva 42,62,248
s_waitcnt lgkmcnt(2)
mav 34,252,66
R 50,25600
mvv 38,128,66
mvv 42,66,132
mvv 34,136,70
R 54,26624
mvv 38,140,70
mvv 42,70,144
s_waitcnt lgkmcnt(2)
mvv 34,148,74
R 58,27648
mvv 38,152,74
mvv 42,74,156
mvv 34,160,46
R 62,28672
mvv 38,164,46
mvv 42,46,168
s_waitcnt lgkmcnt(2)
mvv 34,172,50
R 66,29696
mvv 38,176,50
mvv 42,50,180
mvv 34,184,54
R 70,30720
mvv 38,188,54
mvv 42,54,192
s_waitcnt lgkmcnt(2)
mvv 34,196,58
R 74,31744
mvv 38,200,58
mvv 42,58,204
mvv 34,208,62
mvv 38,212,62
ds_read_b64 v[118:119], v84 offset:128
ds_read_b128 v[102:105], v85 offset:128
mvv 42,62,216
ds_read_b128 v[106:109], v85 offset:144
s_waitcnt lgkmcnt(4)
mvv 34,220,66
mvv 38,224,66
mvv 42,66,228
mvv 34,232,70
mvv 38,236,70
mvv 42,70,240
s_waitcnt lgkmcnt(0)
mvv 34,244,74
mvv 38,248,74
mvv 42,74,252
v_mul_f32 v120, v119, v94
s_nop 4
v_fma_f32 v34, v34, v118, v120
v_mul_f32 v121, v119, v95
v_fma_f32 v35, v35, v118, v121
v_mul_f32 v120, v119, v96
v_fma_f32 v36, v36, v118, v120
v_mul_f32 v121, v119, v97
v_fma_f32 v37, v37, v118, v121
v_mul_f32 v120, v119, v98
v_fma_f32 v38, v38, v118, v120
v_mul_f32 v121, v119, v99
v_fma_f32 v39, v39, v118, v121
v_mul_f32 v120, v119, v100
v_fma_f32 v40, v40, v118, v120
v_mul_f32 v121, v119, v101
v_fma_f32 v41, v41, v118, v121
v_cvt_pk_bf16_f32 v110, v34, v35
v_cvt_pk_bf16_f32 v111, v36, v37
v_cvt_pk_bf16_f32 v112, v38, v39
v_cvt_pk_bf16_f32 v113, v40, v41
ds_write_b128 v83, v[110:113] offset:4096
v_mul_f32 v103, v103, v116
v_fma_f32 v42, v42, v102, v103
v_mul_f32 v105, v105, v116
v_fma_f32 v43, v43, v104, v105
v_mul_f32 v107, v107, v116
v_fma_f32 v44, v44, v106, v107
v_mul_f32 v109, v109, v116
v_fma_f32 v45, v45, v108, v109
v_cvt_pk_bf16_f32 v78, v42, v43
v_cvt_pk_bf16_f32 v79, v44, v45
ds_write_b64 v83, v[78:79] offset:5120
s_waitcnt lgkmcnt(0)
s_barrier
s_endpgm
Cq:
s_cmp_eq_u32 s61, 0
s_cbranch_scc1 Bq
s_sub_u32 s61, s61, 1
s_sleep 2
buffer_load_dwordx2 v[122:123], v91, s[36:39], 0 offen sc1
s_waitcnt vmcnt(0)
ds_write_b32 v82, v122
v_sub_u32 v122, v122, v92
v_xor_b32 v123, v123, v115
v_cmp_lt_u64 vcc, s[62:63], v[122:123]
s_cbranch_vccnz Cq
s_branch Bq
Lr2:
s_cmp_eq_u32 s60, 2
s_cbranch_scc0 Lr3
buffer_load_dword v71, off, s[36:39], 0 sc1
s_waitcnt vmcnt(0)
s_barrier
v_mov_b32 v70, 0x100
v_add_u32 v71, 0x9e3779b9, v71
buffer_store_dwordx2 v[70:71], v81, s[36:39], s52 offen sc1
v_add_u32 v88, v88, v70
E 0,65536
E 1,69632
E 2,73728
s_add_u32 s51, s51, 65536
E 0,77824
E 1,81920
E 2,86016
s_add_u32 s51, s51, 65536
E 0,90112
E 1,94208
E 2,98304
s_add_u32 s51, s51, 65536
E 0,102400
E 1,106496
E 2,110592
s_add_u32 s51, s51, 65536
s_waitcnt vmcnt(6)
s_barrier
v_add_u32 v4, 65536, v4
v_add_u32 v5, 65536, v5
v_add_u32 v6, 65536, v6
Ta 0,4,0
Ta 4,5,0
Ta 8,6,0
Ta 12,4,12288
Ta 16,5,12288
Ta 20,6,12288
E 0,114688
E 1,118784
E 2,122880
s_add_u32 s51, s51, 65536
E 0,126976
E 1,131072
E 2,135168
s_add_u32 s51, s51, 65536
E 0,139264
E 1,143360
E 2,147456
s_add_u32 s51, s51, 65536
E 0,151552
E 1,155648
E 2,159744
s_add_u32 s51, s51, 65536
s_cmp_ge_u32 s51, s57
s_cselect_b32 s60, 2097152, 0
s_sub_u32 s51, s51, s60
s_waitcnt lgkmcnt(0)
s_waitcnt vmcnt(12)
s_barrier
E 0,65536
Ta 24,4,24576
Ta 28,5,24576
E 1,69632
Ta 32,6,24576
Ta 36,4,36864
E 2,73728
s_add_u32 s51, s51, 65536
Ta 40,5,36864
E 0,77824
Ta 44,6,36864
E 1,81920
E 2,86016
s_add_u32 s51, s51, 65536
s_waitcnt lgkmcnt(0)
s_waitcnt vmcnt(12)
s_barrier
v_add_u32 v4, 49152, v4
v_add_u32 v5, 49152, v5
v_add_u32 v6, 49152, v6
E 0,90112
Ta 48,4,0
Ta 52,5,0
E 1,94208
Ta 56,6,0
Ta 60,4,12288
E 2,98304
s_add_u32 s51, s51, 65536
Ta 64,5,12288
E 0,102400
Ta 68,6,12288
E 1,106496
E 2,110592
s_add_u32 s51, s51, 65536
s_waitcnt lgkmcnt(0)
s_waitcnt vmcnt(12)
s_barrier
E 0,114688
Ta 72,4,24576
Ta 76,5,24576
E 1,118784
Ta 80,6,24576
Ta 84,4,36864
E 2,122880
s_add_u32 s51, s51, 65536
Ta 88,5,36864
E 0,126976
Ta 92,6,36864
E 1,131072
E 2,135168
s_add_u32 s51, s51, 65536
s_waitcnt lgkmcnt(0)
s_waitcnt vmcnt(12)
s_barrier
v_add_u32 v4, -49152, v4
v_add_u32 v5, -49152, v5
v_add_u32 v6, -49152, v6
E 0,139264
Ta 96,4,0
Ta 100,5,0
E 1,143360
Ta 104,6,0
Ta 108,4,12288
E 2,147456
s_add_u32 s51, s51, 65536
Ta 112,5,12288
E 0,151552
Ta 116,6,12288
E 1,155648
E 2,159744
s_add_u32 s51, s51, 65536
s_cmp_ge_u32 s51, s57
s_cselect_b32 s60, 2097152, 0
s_sub_u32 s51, s51, s60
s_waitcnt lgkmcnt(0)
s_waitcnt vmcnt(12)
s_barrier
E 0,65536
Ta 120,4,24576
Ta 124,5,24576
E 1,69632
Ta 128,6,24576
Ta 132,4,36864
E 2,73728
s_add_u32 s51, s51, 65536
Ta 136,5,36864
E 0,77824
Ta 140,6,36864
E 1,81920
E 2,86016
s_add_u32 s51, s51, 65536
s_waitcnt lgkmcnt(0)
s_waitcnt vmcnt(12)
s_barrier
v_add_u32 v4, 49152, v4
v_add_u32 v5, 49152, v5
v_add_u32 v6, 49152, v6
E 0,90112
Ta 144,4,0
Ta 148,5,0
E 1,94208
Ta 152,6,0
Ta 156,4,12288
E 2,98304
s_add_u32 s51, s51, 65536
Ta 160,5,12288
E 0,102400
Ta 164,6,12288
E 1,106496
E 2,110592
s_add_u32 s51, s51, 65536
s_waitcnt lgkmcnt(0)
s_waitcnt vmcnt(12)
s_barrier
E 0,114688
Ta 168,4,24576
Ta 172,5,24576
E 1,118784
Ta 176,6,24576
Ta 180,4,36864
E 2,122880
s_add_u32 s51, s51, 65536
Ta 184,5,36864
E 0,126976
Ta 188,6,36864
E 1,131072
E 2,135168
s_add_u32 s51, s51, 65536
s_waitcnt lgkmcnt(0)
s_waitcnt vmcnt(12)
s_barrier
v_add_u32 v4, -49152, v4
v_add_u32 v5, -49152, v5
v_add_u32 v6, -49152, v6
E 0,139264
Ta 192,4,0
Ta 196,5,0
E 1,143360
Ta 200,6,0
Ta 204,4,12288
E 2,147456
s_add_u32 s51, s51, 65536
Ta 208,5,12288
E 0,151552
Ta 212,6,12288
E 1,155648
E 2,159744
s_add_u32 s51, s51, 65536
s_cmp_ge_u32 s51, s57
s_cselect_b32 s60, 2097152, 0
s_sub_u32 s51, s51, s60
s_waitcnt lgkmcnt(0)
s_waitcnt vmcnt(12)
s_barrier
E 0,65536
Ta 216,4,24576
Ta 220,5,24576
E 1,69632
Ta 224,6,24576
Ta 228,4,36864
E 2,73728
s_add_u32 s51, s51, 65536
Ta 232,5,36864
E 0,77824
Ta 236,6,36864
E 1,81920
E 2,86016
s_add_u32 s51, s51, 65536
s_waitcnt lgkmcnt(0)
s_waitcnt vmcnt(12)
s_barrier
v_add_u32 v4, 49152, v4
v_add_u32 v5, 49152, v5
v_add_u32 v6, 49152, v6
E 0,90112
Ta 240,4,0
Ta 244,5,0
E 1,94208
Ta 248,6,0
Ta 252,4,12288
E 2,98304
s_add_u32 s51, s51, 65536
Tv 128,5,12288
E 0,102400
Tv 132,6,12288
E 1,106496
E 2,110592
s_add_u32 s51, s51, 65536
s_waitcnt lgkmcnt(0)
s_waitcnt vmcnt(12)
s_barrier
E 0,114688
Tv 136,4,24576
Tv 140,5,24576
E 1,118784
Tv 144,6,24576
Tv 148,4,36864
E 2,122880
s_add_u32 s51, s51, 65536
Tv 152,5,36864
E 0,126976
Tv 156,6,36864
E 1,131072
E 2,135168
s_add_u32 s51, s51, 65536
s_waitcnt lgkmcnt(0)
s_waitcnt vmcnt(12)
s_barrier
v_add_u32 v4, -49152, v4
v_add_u32 v5, -49152, v5
v_add_u32 v6, -49152, v6
E 0,139264
Tv 160,4,0
Tv 164,5,0
E 1,143360
Tv 168,6,0
Tv 172,4,12288
E 2,147456
s_add_u32 s51, s51, 65536
Tv 176,5,12288
E 0,151552
Tv 180,6,12288
E 1,155648
E 2,159744
s_add_u32 s51, s51, 65536
s_cmp_ge_u32 s51, s57
s_cselect_b32 s60, 2097152, 0
s_sub_u32 s51, s51, s60
s_waitcnt lgkmcnt(0)
s_waitcnt vmcnt(12)
s_barrier
Tv 184,4,24576
Tv 188,5,24576
Tv 192,6,24576
Tv 196,4,36864
Tv 200,5,36864
Tv 204,6,36864
s_waitcnt lgkmcnt(0)
s_waitcnt vmcnt(6)
s_barrier
v_add_u32 v4, 49152, v4
v_add_u32 v5, 49152, v5
v_add_u32 v6, 49152, v6
Tv 208,4,0
Tv 212,5,0
Tv 216,6,0
Tv 220,4,12288
Tv 224,5,12288
Tv 228,6,12288
s_waitcnt lgkmcnt(0)
s_waitcnt vmcnt(0)
s_barrier
Tv 232,4,24576
Tv 236,5,24576
Tv 240,6,24576
Tv 244,4,36864
Tv 248,5,36864
Tv 252,6,36864
s_waitcnt lgkmcnt(0)
s_waitcnt vmcnt(0)
s_barrier
s_nop 7
s_and_b32 s60, s48, 3
s_cmp_eq_u32 s60, 1
s_cbranch_scc1 LvO1
s_cmp_eq_u32 s60, 2
s_cbranch_scc1 LvO2
LvO0:
buffer_load_dwordx2 v[74:75], v87, s[36:39], 0 offen sc1
s_waitcnt vmcnt(0)
ds_write_b32 v82, v74
v_sub_u32 v74, v74, v88
v_xor_b32 v75, v75, v71
v_cmp_lt_u64 vcc, s[62:63], v[74:75]
s_waitcnt lgkmcnt(0)
s_cbranch_vccnz CO0_1
BO0_1:
s_barrier
buffer_load_dwordx4 v[56:59], v89, s[36:39], s49 offen sc1
buffer_load_dwordx2 v[60:61], v90, s[36:39], s49 offen sc1
s_add_u32 s49, s49, 346112
s_add_u32 m0, s50, 0
s_nop 0
D 85,0
D 85,1024
D 85,2048
D 85,3072
s_add_u32 m0, s50, 4096
s_nop 0
D 86,0
D 86,1024
D 86,2048
D 86,3072
s_mov_b32 m0, 142336
s_nop 0
buffer_load_dword v87, s[36:39], 0 offen sc1 lds
v_add_u32 v88, 1, v88
s_waitcnt vmcnt(10)
v_lshlrev_b32 v0, 16, v56
v_and_b32 v1, 0xffff0000, v56
v_lshlrev_b32 v2, 16, v57
v_and_b32 v3, 0xffff0000, v57
v_lshlrev_b32 v4, 16, v58
v_and_b32 v5, 0xffff0000, v58
v_lshlrev_b32 v6, 16, v59
v_and_b32 v7, 0xffff0000, v59
s_waitcnt vmcnt(9)
v_lshlrev_b32 v8, 16, v60
v_and_b32 v9, 0xffff0000, v60
v_lshlrev_b32 v10, 16, v61
v_and_b32 v11, 0xffff0000, v61
buffer_load_dwordx4 v[56:59], v89, s[36:39], s49 offen sc1
buffer_load_dwordx2 v[60:61], v90, s[36:39], s49 offen sc1
ds_read_b32 v74, v82 offset:0
s_waitcnt lgkmcnt(0)
v_cmp_gt_i32 vcc, v88, v74
s_waitcnt vmcnt(3)
s_cbranch_vccnz CO0_2
BO0_2:
s_add_u32 s49, s49, 346112
s_barrier
s_add_u32 m0, s50, 32768
R 24,0
D 85,0
R 28,1024
D 85,1024
R 32,2048
R 36,3072
D 85,2048
s_waitcnt lgkmcnt(2)
mav 0,0,24
R 40,4096
mav 4,4,24
mav 8,8,24
D 85,3072
mav 0,12,28
R 44,5120
mav 4,16,28
mav 8,20,28
s_add_u32 m0, s50, 36864
s_waitcnt lgkmcnt(2)
mav 0,24,32
R 48,6144
mav 4,28,32
D 86,0
mav 8,32,32
mav 0,36,36
R 52,7168
mav 4,40,36
mav 8,44,36
D 86,1024
s_waitcnt lgkmcnt(2)
mav 0,48,40
R 24,8192
mav 4,52,40
mav 8,56,40
mav 0,60,44
R 28,9216
mav 4,64,44
D 86,2048
mav 8,68,44
s_waitcnt lgkmcnt(2)
mav 0,72,48
R 32,10240
mav 4,76,48
mav 8,80,48
D 86,3072
mav 0,84,52
R 36,11264
mav 4,88,52
mav 8,92,52
s_waitcnt lgkmcnt(2)
mav 0,96,24
R 40,12288
mav 4,100,24
mav 8,104,24
mav 0,108,28
R 44,13312
mav 4,112,28
mav 8,116,28
s_waitcnt lgkmcnt(2)
mav 0,120,32
R 48,14336
mav 4,124,32
mav 8,128,32
mav 0,132,36
R 52,15360
mav 4,136,36
s_mov_b32 m0, 142336
mav 8,140,36
s_waitcnt lgkmcnt(2)
mav 0,144,40
R 24,16384
buffer_load_dword v87, s[36:39], 0 offen sc1 lds
mav 4,148,40
mav 8,152,40
mav 0,156,44
R 28,17408
mav 4,160,44
v_add_u32 v88, 1, v88
mav 8,164,44
s_waitcnt lgkmcnt(2)
mav 0,168,48
R 32,18432
mav 4,172,48
mav 8,176,48
mav 0,180,52
R 36,19456
mav 4,184,52
mav 8,188,52
s_waitcnt lgkmcnt(2)
mav 0,192,24
R 40,20480
mav 4,196,24
mav 8,200,24
s_waitcnt vmcnt(10)
v_lshlrev_b32 v12, 16, v56
mav 0,204,28
R 44,21504
v_and_b32 v13, 0xffff0000, v56
mav 4,208,28
v_lshlrev_b32 v14, 16, v57
v_and_b32 v15, 0xffff0000, v57
mav 8,212,28
v_lshlrev_b32 v16, 16, v58
s_waitcnt lgkmcnt(2)
mav 0,216,32
R 48,22528
v_and_b32 v17, 0xffff0000, v58
mav 4,220,32
v_lshlrev_b32 v18, 16, v59
v_and_b32 v19, 0xffff0000, v59
mav 8,224,32
s_waitcnt vmcnt(9)
v_lshlrev_b32 v20, 16, v60
mav 0,228,36
R 52,23552
v_and_b32 v21, 0xffff0000, v60
mav 4,232,36
v_lshlrev_b32 v22, 16, v61
v_and_b32 v23, 0xffff0000, v61
mav 8,236,36
s_waitcnt lgkmcnt(2)
mav 0,240,40
R 24,24576
mav 4,244,40
mav 8,248,40
mav 0,252,44
R 28,25600
mvv 4,128,44
buffer_load_dwordx4 v[56:59], v89, s[36:39], s49 offen sc1
mvv 8,132,44
s_waitcnt lgkmcnt(2)
mvv 0,136,48
R 32,26624
mvv 4,140,48
mvv 8,144,48
buffer_load_dwordx2 v[60:61], v90, s[36:39], s49 offen sc1
mvv 0,148,52
R 36,27648
mvv 4,152,52
mvv 8,156,52
s_waitcnt lgkmcnt(2)
mvv 0,160,24
R 40,28672
mvv 4,164,24
ds_read_b32 v74, v82 offset:0
mvv 8,168,24
mvv 0,172,28
R 44,29696
mvv 4,176,28
mvv 8,180,28
s_waitcnt lgkmcnt(3)
mvv 0,184,32
R 48,30720
s_xor_b32 s50, s50, 0x10000
mvv 4,188,32
mvv 8,192,32
mvv 0,196,36
R 52,31744
mvv 4,200,36
s_waitcnt lgkmcnt(2)
v_cmp_gt_i32 vcc, v88, v74
mvv 8,204,36
s_waitcnt vmcnt(3)
s_cbranch_vccnz CO0_3
BO0_3:
mvv 0,208,40
s_barrier
R 24,32768
mvv 4,212,40
s_add_u32 s49, s49, 346112
s_add_u32 m0, s50, 0
mvv 8,216,40
D 85,0
mvv 0,220,44
R 28,33792
mvv 4,224,44
mvv 8,228,44
s_waitcnt lgkmcnt(2)
mvv 0,232,48
R 32,34816
D 85,1024
mvv 4,236,48
mvv 8,240,48
mvv 0,244,52
R 36,35840
mvv 4,248,52
D 85,2048
mvv 8,252,52
s_waitcnt lgkmcnt(2)
mav 12,0,24
R 40,36864
mav 16,4,24
v_cvt_pk_bf16_f32 v62, v0, v1
v_cvt_pk_bf16_f32 v63, v2, v3
mav 20,8,24
D 85,3072
v_cvt_pk_bf16_f32 v64, v4, v5
mav 12,12,28
R 44,37888
v_cvt_pk_bf16_f32 v65, v6, v7
mav 16,16,28
v_cvt_pk_bf16_f32 v66, v8, v9
v_cvt_pk_bf16_f32 v67, v10, v11
mav 20,20,28
s_add_u32 m0, s50, 4096
s_waitcnt lgkmcnt(2)
mav 12,24,32
R 48,38912
buffer_store_dwordx4 v[62:65], v83, s[36:39], s49 offen sc1
mav 16,28,32
v_cvt_pk_bf16_f32 v68, v8, v9
v_cvt_pk_bf16_f32 v69, v10, v11
mav 20,32,32
v_permlane16_swap_b32 v66, v68
v_permlane16_swap_b32 v67, v69
mav 12,36,36
R 52,39936
v_mul_f32 v73, v0, v0
mav 16,40,36
buffer_store_dwordx4 v[66:69], v84, s[36:39], s49 offen sc1
v_add_f32 v72, v1, v0
mav 20,44,36
v_fmac_f32 v73, v1, v1
s_waitcnt lgkmcnt(2)
mav 12,48,40
R 24,40960
v_add_f32 v72, v2, v72
mav 16,52,40
v_fmac_f32 v73, v2, v2
v_add_f32 v72, v3, v72
mav 20,56,40
D 86,0
v_fmac_f32 v73, v3, v3
mav 12,60,44
R 28,41984
v_add_f32 v72, v4, v72
mav 16,64,44
v_fmac_f32 v73, v4, v4
v_add_f32 v72, v5, v72
mav 20,68,44
v_fmac_f32 v73, v5, v5
s_waitcnt lgkmcnt(2)
mav 12,72,48
R 32,43008
D 86,1024
mav 16,76,48
v_add_f32 v72, v6, v72
v_fmac_f32 v73, v6, v6
mav 20,80,48
v_add_f32 v72, v7, v72
v_fmac_f32 v73, v7, v7
mav 12,84,52
R 36,44032
v_add_f32 v72, v8, v72
mav 16,88,52
D 86,2048
v_fmac_f32 v73, v8, v8
mav 20,92,52
v_add_f32 v72, v9, v72
s_waitcnt lgkmcnt(2)
mav 12,96,24
R 40,45056
v_fmac_f32 v73, v9, v9
mav 16,100,24
v_add_f32 v72, v10, v72
v_fmac_f32 v73, v10, v10
mav 20,104,24
D 86,3072
v_add_f32 v72, v11, v72
mav 12,108,28
R 44,46080
v_fmac_f32 v73, v11, v11
mav 16,112,28
mav 20,116,28
v_permlane32_swap_b32 v72, v73
s_waitcnt lgkmcnt(2)
mav 12,120,32
R 48,47104
v_add_f32 v72, v72, v73
mav 16,124,32
mav 20,128,32
mav 12,132,36
R 52,48128
mav 16,136,36
s_mov_b32 m0, 142336
mav 20,140,36
s_waitcnt lgkmcnt(2)
mav 12,144,40
R 24,49152
buffer_load_dword v87, s[36:39], 0 offen sc1 lds
mav 16,148,40
mav 20,152,40
mav 12,156,44
R 28,50176
mav 16,160,44
v_add_u32 v88, 1, v88
mav 20,164,44
s_waitcnt lgkmcnt(2)
mav 12,168,48
R 32,51200
mav 16,172,48
mav 20,176,48
mav 12,180,52
R 36,52224
mav 16,184,52
mav 20,188,52
s_waitcnt lgkmcnt(2)
mav 12,192,24
R 40,53248
mav 16,196,24
mav 20,200,24
s_waitcnt vmcnt(12)
v_lshlrev_b32 v0, 16, v56
mav 12,204,28
R 44,54272
v_and_b32 v1, 0xffff0000, v56
mav 16,208,28
v_lshlrev_b32 v2, 16, v57
v_and_b32 v3, 0xffff0000, v57
mav 20,212,28
v_lshlrev_b32 v4, 16, v58
s_waitcnt lgkmcnt(2)
mav 12,216,32
R 48,55296
v_and_b32 v5, 0xffff0000, v58
mav 16,220,32
v_lshlrev_b32 v6, 16, v59
v_and_b32 v7, 0xffff0000, v59
mav 20,224,32
s_waitcnt vmcnt(11)
v_lshlrev_b32 v8, 16, v60
mav 12,228,36
R 52,56320
v_and_b32 v9, 0xffff0000, v60
mav 16,232,36
v_lshlrev_b32 v10, 16, v61
v_and_b32 v11, 0xffff0000, v61
mav 20,236,36
s_waitcnt lgkmcnt(2)
mav 12,240,40
R 24,57344
mav 16,244,40
mav 20,248,40
mav 12,252,44
R 28,58368
mvv 16,128,44
buffer_load_dwordx4 v[56:59], v89, s[36:39], s49 offen sc1
v_add_u32 v70, 1, v70
mvv 20,132,44
s_waitcnt lgkmcnt(2)
mvv 12,136,48
R 32,59392
mvv 16,140,48
mvv 20,144,48
buffer_load_dwordx2 v[60:61], v90, s[36:39], s49 offen sc1
mvv 12,148,52
R 36,60416
mvv 16,152,52
s_waitcnt vmcnt(7)
buffer_store_dwordx2 v[70:71], v81, s[36:39], s52 offen sc1
mvv 20,156,52
s_waitcnt lgkmcnt(2)
mvv 12,160,24
R 40,61440
mvv 16,164,24
ds_read_b32 v74, v82 offset:0
mvv 20,168,24
ds_write_b32 v91, v72 offset:0
mvv 12,172,28
R 44,62464
mvv 16,176,28
mvv 20,180,28
s_waitcnt lgkmcnt(4)
mvv 12,184,32
R 48,63488
mvv 16,188,32
mvv 20,192,32
mvv 12,196,36
R 52,64512
v_xor_b32 v80, 0x10000, v80
mvv 16,200,36
s_waitcnt lgkmcnt(2)
v_cmp_gt_i32 vcc, v88, v74
mvv 20,204,36
s_waitcnt vmcnt(4)
s_cbranch_vccnz CO0_4
BO0_4:
mvv 12,208,40
s_barrier
R 24,0
mvv 16,212,40
s_add_u32 s49, s49, 346112
s_add_u32 m0, s50, 32768
mvv 20,216,40
D 85,0
mvv 12,220,44
R 28,1024
mvv 16,224,44
mvv 20,228,44
s_waitcnt lgkmcnt(2)
mvv 12,232,48
R 32,2048
D 85,1024
mvv 16,236,48
mvv 20,240,48
mvv 12,244,52
R 36,3072
mvv 16,248,52
D 85,2048
mvv 20,252,52
s_mov_b32 s60, -30
.p2align 6
LO0_5:
s_waitcnt lgkmcnt(2)
mav 0,0,24
R 40,4096
mav 4,4,24
v_cvt_pk_bf16_f32 v62, v12, v13
v_cvt_pk_bf16_f32 v63, v14, v15
mav 8,8,24
D 85,3072
v_cvt_pk_bf16_f32 v64, v16, v17
mav 0,12,28
R 44,5120
v_cvt_pk_bf16_f32 v65, v18, v19
mav 4,16,28
v_cvt_pk_bf16_f32 v66, v20, v21
v_cvt_pk_bf16_f32 v67, v22, v23
mav 8,20,28
s_add_u32 m0, s50, 36864
s_waitcnt lgkmcnt(2)
mav 0,24,32
R 48,6144
buffer_store_dwordx4 v[62:65], v83, s[36:39], s49 offen sc1
mav 4,28,32
v_cvt_pk_bf16_f32 v68, v20, v21
v_cvt_pk_bf16_f32 v69, v22, v23
mav 8,32,32
v_permlane16_swap_b32 v66, v68
v_permlane16_swap_b32 v67, v69
mav 0,36,36
R 52,7168
v_mul_f32 v73, v12, v12
mav 4,40,36
buffer_store_dwordx4 v[66:69], v84, s[36:39], s49 offen sc1
v_add_f32 v72, v13, v12
mav 8,44,36
v_fmac_f32 v73, v13, v13
s_waitcnt lgkmcnt(2)
mav 0,48,40
R 24,8192
v_add_f32 v72, v14, v72
mav 4,52,40
v_fmac_f32 v73, v14, v14
v_add_f32 v72, v15, v72
mav 8,56,40
D 86,0
v_fmac_f32 v73, v15, v15
mav 0,60,44
R 28,9216
v_add_f32 v72, v16, v72
mav 4,64,44
v_fmac_f32 v73, v16, v16
v_add_f32 v72, v17, v72
mav 8,68,44
v_fmac_f32 v73, v17, v17
s_waitcnt lgkmcnt(2)
mav 0,72,48
R 32,10240
D 86,1024
mav 4,76,48
v_add_f32 v72, v18, v72
v_fmac_f32 v73, v18, v18
mav 8,80,48
v_add_f32 v72, v19, v72
v_fmac_f32 v73, v19, v19
mav 0,84,52
R 36,11264
v_add_f32 v72, v20, v72
mav 4,88,52
D 86,2048
v_fmac_f32 v73, v20, v20
mav 8,92,52
v_add_f32 v72, v21, v72
s_waitcnt lgkmcnt(2)
mav 0,96,24
R 40,12288
v_fmac_f32 v73, v21, v21
mav 4,100,24
v_add_f32 v72, v22, v72
v_fmac_f32 v73, v22, v22
mav 8,104,24
D 86,3072
v_add_f32 v72, v23, v72
mav 0,108,28
R 44,13312
v_fmac_f32 v73, v23, v23
mav 4,112,28
mav 8,116,28
v_permlane32_swap_b32 v72, v73
s_waitcnt lgkmcnt(2)
mav 0,120,32
R 48,14336
v_add_f32 v72, v72, v73
mav 4,124,32
mav 8,128,32
mav 0,132,36
R 52,15360
mav 4,136,36
s_mov_b32 m0, 142336
mav 8,140,36
s_waitcnt lgkmcnt(2)
mav 0,144,40
R 24,16384
buffer_load_dword v87, s[36:39], 0 offen sc1 lds
mav 4,148,40
mav 8,152,40
mav 0,156,44
R 28,17408
mav 4,160,44
v_add_u32 v88, 1, v88
mav 8,164,44
s_waitcnt lgkmcnt(2)
mav 0,168,48
R 32,18432
mav 4,172,48
mav 8,176,48
mav 0,180,52
R 36,19456
mav 4,184,52
mav 8,188,52
s_waitcnt lgkmcnt(2)
mav 0,192,24
R 40,20480
mav 4,196,24
mav 8,200,24
s_waitcnt vmcnt(13)
v_lshlrev_b32 v12, 16, v56
mav 0,204,28
R 44,21504
v_and_b32 v13, 0xffff0000, v56
mav 4,208,28
v_lshlrev_b32 v14, 16, v57
v_and_b32 v15, 0xffff0000, v57
mav 8,212,28
v_lshlrev_b32 v16, 16, v58
s_waitcnt lgkmcnt(2)
mav 0,216,32
R 48,22528
v_and_b32 v17, 0xffff0000, v58
mav 4,220,32
v_lshlrev_b32 v18, 16, v59
v_and_b32 v19, 0xffff0000, v59
mav 8,224,32
s_waitcnt vmcnt(12)
v_lshlrev_b32 v20, 16, v60
mav 0,228,36
R 52,23552
v_and_b32 v21, 0xffff0000, v60
mav 4,232,36
v_lshlrev_b32 v22, 16, v61
v_and_b32 v23, 0xffff0000, v61
mav 8,236,36
s_waitcnt lgkmcnt(2)
mav 0,240,40
R 24,24576
mav 4,244,40
mav 8,248,40
mav 0,252,44
R 28,25600
mvv 4,128,44
buffer_load_dwordx4 v[56:59], v89, s[36:39], s49 offen sc1
v_add_u32 v70, 1, v70
mvv 8,132,44
s_waitcnt lgkmcnt(2)
mvv 0,136,48
R 32,26624
mvv 4,140,48
mvv 8,144,48
buffer_load_dwordx2 v[60:61], v90, s[36:39], s49 offen sc1
mvv 0,148,52
R 36,27648
mvv 4,152,52
s_waitcnt vmcnt(7)
buffer_store_dwordx2 v[70:71], v81, s[36:39], s52 offen sc1
mvv 8,156,52
s_waitcnt lgkmcnt(2)
mvv 0,160,24
R 40,28672
mvv 4,164,24
ds_read_b32 v74, v82 offset:0
mvv 8,168,24
ds_write_b32 v91, v72 offset:1024
mvv 0,172,28
R 44,29696
mvv 4,176,28
mvv 8,180,28
s_waitcnt lgkmcnt(4)
mvv 0,184,32
R 48,30720
s_xor_b32 s50, s50, 0x10000
mvv 4,188,32
mvv 8,192,32
mvv 0,196,36
R 52,31744
mvv 4,200,36
s_waitcnt lgkmcnt(2)
v_cmp_gt_i32 vcc, v88, v74
mvv 8,204,36
s_waitcnt vmcnt(4)
s_cbranch_vccnz CO0_6
BO0_6:
mvv 0,208,40
s_barrier
R 24,32768
mvv 4,212,40
s_add_u32 s49, s49, 346112
s_add_u32 m0, s50, 0
mvv 8,216,40
D 85,0
mvv 0,220,44
R 28,33792
mvv 4,224,44
mvv 8,228,44
s_waitcnt lgkmcnt(2)
mvv 0,232,48
R 32,34816
D 85,1024
mvv 4,236,48
mvv 8,240,48
mvv 0,244,52
R 36,35840
mvv 4,248,52
D 85,2048
mvv 8,252,52
s_waitcnt lgkmcnt(2)
mav 12,0,24
R 40,36864
mav 16,4,24
v_cvt_pk_bf16_f32 v62, v0, v1
v_cvt_pk_bf16_f32 v63, v2, v3
mav 20,8,24
D 85,3072
v_cvt_pk_bf16_f32 v64, v4, v5
mav 12,12,28
R 44,37888
v_cvt_pk_bf16_f32 v65, v6, v7
mav 16,16,28
v_cvt_pk_bf16_f32 v66, v8, v9
v_cvt_pk_bf16_f32 v67, v10, v11
mav 20,20,28
s_add_u32 m0, s50, 4096
s_waitcnt lgkmcnt(2)
mav 12,24,32
R 48,38912
buffer_store_dwordx4 v[62:65], v83, s[36:39], s49 offen sc1
mav 16,28,32
v_cvt_pk_bf16_f32 v68, v8, v9
v_cvt_pk_bf16_f32 v69, v10, v11
mav 20,32,32
v_permlane16_swap_b32 v66, v68
v_permlane16_swap_b32 v67, v69
mav 12,36,36
R 52,39936
v_mul_f32 v73, v0, v0
mav 16,40,36
buffer_store_dwordx4 v[66:69], v84, s[36:39], s49 offen sc1
v_add_f32 v72, v1, v0
mav 20,44,36
v_fmac_f32 v73, v1, v1
s_waitcnt lgkmcnt(2)
mav 12,48,40
R 24,40960
v_add_f32 v72, v2, v72
mav 16,52,40
v_fmac_f32 v73, v2, v2
v_add_f32 v72, v3, v72
)"
R"(
mav 20,56,40
D 86,0
v_fmac_f32 v73, v3, v3
mav 12,60,44
R 28,41984
v_add_f32 v72, v4, v72
mav 16,64,44
v_fmac_f32 v73, v4, v4
v_add_f32 v72, v5, v72
mav 20,68,44
v_fmac_f32 v73, v5, v5
s_waitcnt lgkmcnt(2)
mav 12,72,48
R 32,43008
D 86,1024
mav 16,76,48
v_add_f32 v72, v6, v72
v_fmac_f32 v73, v6, v6
mav 20,80,48
v_add_f32 v72, v7, v72
v_fmac_f32 v73, v7, v7
mav 12,84,52
R 36,44032
v_add_f32 v72, v8, v72
mav 16,88,52
D 86,2048
v_fmac_f32 v73, v8, v8
mav 20,92,52
v_add_f32 v72, v9, v72
s_waitcnt lgkmcnt(2)
mav 12,96,24
R 40,45056
v_fmac_f32 v73, v9, v9
mav 16,100,24
v_add_f32 v72, v10, v72
v_fmac_f32 v73, v10, v10
mav 20,104,24
D 86,3072
v_add_f32 v72, v11, v72
mav 12,108,28
R 44,46080
v_fmac_f32 v73, v11, v11
mav 16,112,28
mav 20,116,28
v_permlane32_swap_b32 v72, v73
s_waitcnt lgkmcnt(2)
mav 12,120,32
R 48,47104
v_add_f32 v72, v72, v73
mav 16,124,32
mav 20,128,32
mav 12,132,36
R 52,48128
mav 16,136,36
s_mov_b32 m0, 142336
mav 20,140,36
s_waitcnt lgkmcnt(2)
mav 12,144,40
R 24,49152
buffer_load_dword v87, s[36:39], 0 offen sc1 lds
mav 16,148,40
mav 20,152,40
mav 12,156,44
R 28,50176
mav 16,160,44
v_add_u32 v88, 1, v88
mav 20,164,44
s_waitcnt lgkmcnt(2)
mav 12,168,48
R 32,51200
mav 16,172,48
mav 20,176,48
mav 12,180,52
R 36,52224
mav 16,184,52
mav 20,188,52
s_waitcnt lgkmcnt(2)
mav 12,192,24
R 40,53248
mav 16,196,24
mav 20,200,24
s_waitcnt vmcnt(13)
v_lshlrev_b32 v0, 16, v56
mav 12,204,28
R 44,54272
v_and_b32 v1, 0xffff0000, v56
mav 16,208,28
v_lshlrev_b32 v2, 16, v57
v_and_b32 v3, 0xffff0000, v57
mav 20,212,28
v_lshlrev_b32 v4, 16, v58
s_waitcnt lgkmcnt(2)
mav 12,216,32
R 48,55296
v_and_b32 v5, 0xffff0000, v58
mav 16,220,32
v_lshlrev_b32 v6, 16, v59
v_and_b32 v7, 0xffff0000, v59
mav 20,224,32
s_waitcnt vmcnt(12)
v_lshlrev_b32 v8, 16, v60
mav 12,228,36
R 52,56320
v_and_b32 v9, 0xffff0000, v60
mav 16,232,36
v_lshlrev_b32 v10, 16, v61
v_and_b32 v11, 0xffff0000, v61
mav 20,236,36
s_waitcnt lgkmcnt(2)
mav 12,240,40
R 24,57344
mav 16,244,40
mav 20,248,40
mav 12,252,44
R 28,58368
mvv 16,128,44
buffer_load_dwordx4 v[56:59], v89, s[36:39], s49 offen sc1
v_add_u32 v70, 1, v70
mvv 20,132,44
s_waitcnt lgkmcnt(2)
mvv 12,136,48
R 32,59392
mvv 16,140,48
mvv 20,144,48
buffer_load_dwordx2 v[60:61], v90, s[36:39], s49 offen sc1
mvv 12,148,52
R 36,60416
mvv 16,152,52
s_waitcnt vmcnt(7)
buffer_store_dwordx2 v[70:71], v81, s[36:39], s52 offen sc1
mvv 20,156,52
s_waitcnt lgkmcnt(2)
mvv 12,160,24
R 40,61440
mvv 16,164,24
ds_read_b32 v74, v82 offset:0
mvv 20,168,24
ds_write_b32 v91, v72 offset:0
mvv 12,172,28
R 44,62464
mvv 16,176,28
mvv 20,180,28
s_waitcnt lgkmcnt(4)
mvv 12,184,32
R 48,63488
mvv 16,188,32
mvv 20,192,32
mvv 12,196,36
R 52,64512
v_xor_b32 v80, 0x10000, v80
mvv 16,200,36
s_waitcnt lgkmcnt(2)
v_cmp_gt_i32 vcc, v88, v74
mvv 20,204,36
s_waitcnt vmcnt(4)
s_cbranch_vccnz CO0_7
BO0_7:
mvv 12,208,40
s_barrier
R 24,0
mvv 16,212,40
s_add_u32 s49, s49, 346112
s_add_u32 m0, s50, 32768
mvv 20,216,40
D 85,0
mvv 12,220,44
R 28,1024
mvv 16,224,44
mvv 20,228,44
s_waitcnt lgkmcnt(2)
mvv 12,232,48
R 32,2048
D 85,1024
mvv 16,236,48
mvv 20,240,48
mvv 12,244,52
R 36,3072
mvv 16,248,52
D 85,2048
mvv 20,252,52
s_add_u32 s60, s60, 1
s_cbranch_scc0 LO0_5
s_waitcnt lgkmcnt(2)
mav 0,0,24
R 40,4096
mav 4,4,24
v_cvt_pk_bf16_f32 v62, v12, v13
v_cvt_pk_bf16_f32 v63, v14, v15
mav 8,8,24
D 85,3072
v_cvt_pk_bf16_f32 v64, v16, v17
mav 0,12,28
R 44,5120
v_cvt_pk_bf16_f32 v65, v18, v19
mav 4,16,28
v_cvt_pk_bf16_f32 v66, v20, v21
v_cvt_pk_bf16_f32 v67, v22, v23
mav 8,20,28
s_add_u32 m0, s50, 36864
s_waitcnt lgkmcnt(2)
mav 0,24,32
R 48,6144
buffer_store_dwordx4 v[62:65], v83, s[36:39], s49 offen sc1
mav 4,28,32
v_cvt_pk_bf16_f32 v68, v20, v21
v_cvt_pk_bf16_f32 v69, v22, v23
mav 8,32,32
v_permlane16_swap_b32 v66, v68
v_permlane16_swap_b32 v67, v69
mav 0,36,36
R 52,7168
v_mul_f32 v73, v12, v12
mav 4,40,36
buffer_store_dwordx4 v[66:69], v84, s[36:39], s49 offen sc1
v_add_f32 v72, v13, v12
mav 8,44,36
v_fmac_f32 v73, v13, v13
s_waitcnt lgkmcnt(2)
mav 0,48,40
R 24,8192
v_add_f32 v72, v14, v72
mav 4,52,40
v_fmac_f32 v73, v14, v14
v_add_f32 v72, v15, v72
mav 8,56,40
D 86,0
v_fmac_f32 v73, v15, v15
mav 0,60,44
R 28,9216
v_add_f32 v72, v16, v72
mav 4,64,44
v_fmac_f32 v73, v16, v16
v_add_f32 v72, v17, v72
mav 8,68,44
v_fmac_f32 v73, v17, v17
s_waitcnt lgkmcnt(2)
mav 0,72,48
R 32,10240
D 86,1024
mav 4,76,48
v_add_f32 v72, v18, v72
v_fmac_f32 v73, v18, v18
mav 8,80,48
v_add_f32 v72, v19, v72
v_fmac_f32 v73, v19, v19
mav 0,84,52
R 36,11264
v_add_f32 v72, v20, v72
mav 4,88,52
D 86,2048
v_fmac_f32 v73, v20, v20
mav 8,92,52
v_add_f32 v72, v21, v72
s_waitcnt lgkmcnt(2)
mav 0,96,24
R 40,12288
v_fmac_f32 v73, v21, v21
mav 4,100,24
v_add_f32 v72, v22, v72
v_fmac_f32 v73, v22, v22
mav 8,104,24
D 86,3072
v_add_f32 v72, v23, v72
mav 0,108,28
R 44,13312
v_fmac_f32 v73, v23, v23
mav 4,112,28
mav 8,116,28
v_permlane32_swap_b32 v72, v73
s_waitcnt lgkmcnt(2)
mav 0,120,32
R 48,14336
v_add_f32 v72, v72, v73
mav 4,124,32
mav 8,128,32
mav 0,132,36
R 52,15360
mav 4,136,36
mav 8,140,36
s_waitcnt lgkmcnt(2)
mav 0,144,40
R 24,16384
mav 4,148,40
mav 8,152,40
mav 0,156,44
R 28,17408
mav 4,160,44
mav 8,164,44
s_waitcnt lgkmcnt(2)
mav 0,168,48
R 32,18432
mav 4,172,48
mav 8,176,48
mav 0,180,52
R 36,19456
mav 4,184,52
mav 8,188,52
s_waitcnt lgkmcnt(2)
mav 0,192,24
R 40,20480
mav 4,196,24
mav 8,200,24
s_waitcnt vmcnt(12)
v_lshlrev_b32 v12, 16, v56
mav 0,204,28
R 44,21504
v_and_b32 v13, 0xffff0000, v56
mav 4,208,28
v_lshlrev_b32 v14, 16, v57
v_and_b32 v15, 0xffff0000, v57
mav 8,212,28
v_lshlrev_b32 v16, 16, v58
s_waitcnt lgkmcnt(2)
mav 0,216,32
R 48,22528
v_and_b32 v17, 0xffff0000, v58
mav 4,220,32
v_lshlrev_b32 v18, 16, v59
v_and_b32 v19, 0xffff0000, v59
mav 8,224,32
s_waitcnt vmcnt(11)
v_lshlrev_b32 v20, 16, v60
mav 0,228,36
R 52,23552
v_and_b32 v21, 0xffff0000, v60
mav 4,232,36
v_lshlrev_b32 v22, 16, v61
v_and_b32 v23, 0xffff0000, v61
mav 8,236,36
s_waitcnt lgkmcnt(2)
mav 0,240,40
R 24,24576
mav 4,244,40
mav 8,248,40
mav 0,252,44
R 28,25600
mvv 4,128,44
v_add_u32 v70, 1, v70
mvv 8,132,44
s_waitcnt lgkmcnt(2)
mvv 0,136,48
R 32,26624
mvv 4,140,48
mvv 8,144,48
mvv 0,148,52
R 36,27648
mvv 4,152,52
s_waitcnt vmcnt(4)
buffer_store_dwordx2 v[70:71], v81, s[36:39], s52 offen sc1
mvv 8,156,52
s_waitcnt lgkmcnt(2)
mvv 0,160,24
R 40,28672
mvv 4,164,24
mvv 8,168,24
ds_write_b32 v91, v72 offset:1024
mvv 0,172,28
R 44,29696
mvv 4,176,28
mvv 8,180,28
s_waitcnt lgkmcnt(3)
mvv 0,184,32
R 48,30720
mvv 4,188,32
mvv 8,192,32
mvv 0,196,36
R 52,31744
mvv 4,200,36
s_add_u32 s49, s49, 346112
mvv 8,204,36
s_waitcnt vmcnt(1) lgkmcnt(2)
s_barrier
mvv 0,208,40
R 24,32768
mvv 4,212,40
mvv 8,216,40
mvv 0,220,44
R 28,33792
mvv 4,224,44
mvv 8,228,44
s_waitcnt lgkmcnt(2)
mvv 0,232,48
R 32,34816
mvv 4,236,48
mvv 8,240,48
mvv 0,244,52
R 36,35840
mvv 4,248,52
mvv 8,252,52
s_waitcnt lgkmcnt(2)
mav 12,0,24
R 40,36864
mav 16,4,24
mav 20,8,24
v_cvt_pk_bf16_f32 v62, v0, v1
v_cvt_pk_bf16_f32 v63, v2, v3
mav 12,12,28
R 44,37888
v_cvt_pk_bf16_f32 v64, v4, v5
mav 16,16,28
v_cvt_pk_bf16_f32 v65, v6, v7
buffer_store_dwordx4 v[62:65], v83, s[36:39], s49 offen sc1
mav 20,20,28
v_cvt_pk_bf16_f32 v66, v8, v9
s_waitcnt lgkmcnt(2)
mav 12,24,32
R 48,38912
v_cvt_pk_bf16_f32 v67, v10, v11
mav 16,28,32
v_cvt_pk_bf16_f32 v68, v8, v9
v_cvt_pk_bf16_f32 v69, v10, v11
mav 20,32,32
v_permlane16_swap_b32 v66, v68
v_permlane16_swap_b32 v67, v69
mav 12,36,36
R 52,39936
buffer_store_dwordx4 v[66:69], v84, s[36:39], s49 offen sc1
mav 16,40,36
v_mul_f32 v73, v0, v0
v_add_f32 v72, v1, v0
mav 20,44,36
v_fmac_f32 v73, v1, v1
s_waitcnt lgkmcnt(2)
mav 12,48,40
R 24,40960
v_add_f32 v72, v2, v72
mav 16,52,40
v_fmac_f32 v73, v2, v2
v_add_f32 v72, v3, v72
mav 20,56,40
v_fmac_f32 v73, v3, v3
v_add_f32 v72, v4, v72
mav 12,60,44
R 28,41984
v_fmac_f32 v73, v4, v4
mav 16,64,44
v_add_f32 v72, v5, v72
v_fmac_f32 v73, v5, v5
mav 20,68,44
v_add_f32 v72, v6, v72
s_waitcnt lgkmcnt(2)
mav 12,72,48
R 32,43008
v_fmac_f32 v73, v6, v6
mav 16,76,48
v_add_f32 v72, v7, v72
v_fmac_f32 v73, v7, v7
mav 20,80,48
v_add_f32 v72, v8, v72
v_fmac_f32 v73, v8, v8
mav 12,84,52
R 36,44032
v_add_f32 v72, v9, v72
mav 16,88,52
v_fmac_f32 v73, v9, v9
v_add_f32 v72, v10, v72
mav 20,92,52
v_fmac_f32 v73, v10, v10
s_waitcnt lgkmcnt(2)
mav 12,96,24
R 40,45056
v_add_f32 v72, v11, v72
mav 16,100,24
v_fmac_f32 v73, v11, v11
mav 20,104,24
mav 12,108,28
R 44,46080
v_permlane32_swap_b32 v72, v73
mav 16,112,28
v_add_f32 v72, v72, v73
mav 20,116,28
s_waitcnt lgkmcnt(2)
mav 12,120,32
R 48,47104
mav 16,124,32
mav 20,128,32
mav 12,132,36
R 52,48128
mav 16,136,36
mav 20,140,36
s_waitcnt lgkmcnt(2)
mav 12,144,40
R 24,49152
mav 16,148,40
mav 20,152,40
mav 12,156,44
R 28,50176
mav 16,160,44
mav 20,164,44
s_waitcnt lgkmcnt(2)
mav 12,168,48
R 32,51200
mav 16,172,48
mav 20,176,48
mav 12,180,52
R 36,52224
mav 16,184,52
mav 20,188,52
s_waitcnt lgkmcnt(2)
mav 12,192,24
R 40,53248
mav 16,196,24
mav 20,200,24
mav 12,204,28
R 44,54272
mav 16,208,28
mav 20,212,28
s_waitcnt lgkmcnt(2)
mav 12,216,32
R 48,55296
mav 16,220,32
mav 20,224,32
mav 12,228,36
R 52,56320
mav 16,232,36
mav 20,236,36
s_waitcnt lgkmcnt(2)
mav 12,240,40
R 24,57344
mav 16,244,40
mav 20,248,40
mav 12,252,44
R 28,58368
mvv 16,128,44
v_add_u32 v70, 1, v70
mvv 20,132,44
s_waitcnt lgkmcnt(2)
mvv 12,136,48
R 32,59392
mvv 16,140,48
mvv 20,144,48
mvv 12,148,52
R 36,60416
mvv 16,152,52
s_waitcnt vmcnt(0)
buffer_store_dwordx2 v[70:71], v81, s[36:39], s52 offen sc1
mvv 20,156,52
s_waitcnt lgkmcnt(2)
mvv 12,160,24
R 40,61440
mvv 16,164,24
mvv 20,168,24
ds_write_b32 v91, v72 offset:0
mvv 12,172,28
R 44,62464
mvv 16,176,28
mvv 20,180,28
s_waitcnt lgkmcnt(3)
mvv 12,184,32
R 48,63488
mvv 16,188,32
mvv 20,192,32
mvv 12,196,36
R 52,64512
mvv 16,200,36
s_add_u32 s49, s49, 346112
mvv 20,204,36
s_waitcnt lgkmcnt(2)
s_barrier
mvv 12,208,40
mvv 16,212,40
mvv 20,216,40
mvv 12,220,44
mvv 16,224,44
mvv 20,228,44
s_waitcnt lgkmcnt(0)
mvv 12,232,48
mvv 16,236,48
mvv 20,240,48
mvv 12,244,52
mvv 16,248,52
mvv 20,252,52
s_nop 5
v_cvt_pk_bf16_f32 v62, v12, v13
v_cvt_pk_bf16_f32 v63, v14, v15
v_cvt_pk_bf16_f32 v64, v16, v17
v_cvt_pk_bf16_f32 v65, v18, v19
buffer_store_dwordx4 v[62:65], v83, s[36:39], s49 offen sc1
v_cvt_pk_bf16_f32 v66, v20, v21
v_cvt_pk_bf16_f32 v67, v22, v23
v_cvt_pk_bf16_f32 v68, v20, v21
v_cvt_pk_bf16_f32 v69, v22, v23
s_nop 0
v_permlane16_swap_b32 v66, v68
v_permlane16_swap_b32 v67, v69
buffer_store_dwordx4 v[66:69], v84, s[36:39], s49 offen sc1
v_mul_f32 v73, v12, v12
v_add_f32 v72, v13, v12
v_fmac_f32 v73, v13, v13
v_add_f32 v72, v14, v72
v_fmac_f32 v73, v14, v14
v_add_f32 v72, v15, v72
v_fmac_f32 v73, v15, v15
v_add_f32 v72, v16, v72
v_fmac_f32 v73, v16, v16
v_add_f32 v72, v17, v72
v_fmac_f32 v73, v17, v17
v_add_f32 v72, v18, v72
v_fmac_f32 v73, v18, v18
v_add_f32 v72, v19, v72
v_fmac_f32 v73, v19, v19
v_add_f32 v72, v20, v72
v_fmac_f32 v73, v20, v20
v_add_f32 v72, v21, v72
v_fmac_f32 v73, v21, v21
v_add_f32 v72, v22, v72
v_fmac_f32 v73, v22, v22
v_add_f32 v72, v23, v72
v_fmac_f32 v73, v23, v23
s_nop 1
v_permlane32_swap_b32 v72, v73
v_add_f32 v72, v72, v73
v_add_u32 v70, 37, v70
s_waitcnt vmcnt(0)
buffer_store_dwordx2 v[70:71], v81, s[36:39], s52 offen sc1
ds_write_b32 v91, v72 offset:1024
s_add_u32 s49, s49, 346112
s_waitcnt lgkmcnt(0)
s_barrier
s_endpgm
CO0_1:
s_cmp_eq_u32 s61, 0
s_cbranch_scc1 BO0_1
s_sub_u32 s61, s61, 1
s_sleep 8
buffer_load_dwordx2 v[74:75], v87, s[36:39], 0 offen sc1
s_waitcnt vmcnt(0)
ds_write_b32 v82, v74
v_sub_u32 v74, v74, v88
v_xor_b32 v75, v75, v71
v_cmp_lt_u64 vcc, s[62:63], v[74:75]
s_waitcnt lgkmcnt(0)
s_cbranch_vccnz CO0_1
s_branch BO0_1
CO0_2:
s_mov_b32 s58, m0
s_mov_b32 m0, 142336
DO0_2:
s_cmp_eq_u32 s61, 0
s_cbranch_scc1 EO0_2
s_sub_u32 s61, s61, 1
buffer_load_dword v87, s[36:39], 0 offen sc1 lds
s_sleep 1
ds_read_b32 v74, v82
s_waitcnt lgkmcnt(0)
v_cmp_gt_i32 vcc, v88, v74
s_cbranch_vccnz DO0_2
EO0_2:
s_mov_b32 m0, s58
s_branch BO0_2
CO0_3:
s_mov_b32 s58, m0
s_mov_b32 m0, 142336
DO0_3:
s_cmp_eq_u32 s61, 0
s_cbranch_scc1 EO0_3
s_sub_u32 s61, s61, 1
buffer_load_dword v87, s[36:39], 0 offen sc1 lds
s_sleep 1
ds_read_b32 v74, v82
s_waitcnt lgkmcnt(0)
v_cmp_gt_i32 vcc, v88, v74
s_cbranch_vccnz DO0_3
EO0_3:
s_mov_b32 m0, s58
s_branch BO0_3
CO0_4:
s_mov_b32 s58, m0
s_mov_b32 m0, 142336
DO0_4:
s_cmp_eq_u32 s61, 0
s_cbranch_scc1 EO0_4
s_sub_u32 s61, s61, 1
buffer_load_dword v87, s[36:39], 0 offen sc1 lds
s_sleep 1
ds_read_b32 v74, v82
s_waitcnt lgkmcnt(0)
v_cmp_gt_i32 vcc, v88, v74
s_cbranch_vccnz DO0_4
EO0_4:
s_mov_b32 m0, s58
s_branch BO0_4
CO0_6:
s_mov_b32 s58, m0
s_mov_b32 m0, 142336
DO0_6:
s_cmp_eq_u32 s61, 0
s_cbranch_scc1 EO0_6
s_sub_u32 s61, s61, 1
buffer_load_dword v87, s[36:39], 0 offen sc1 lds
s_sleep 1
ds_read_b32 v74, v82
s_waitcnt lgkmcnt(0)
v_cmp_gt_i32 vcc, v88, v74
s_cbranch_vccnz DO0_6
EO0_6:
s_mov_b32 m0, s58
s_branch BO0_6
CO0_7:
s_mov_b32 s58, m0
s_mov_b32 m0, 142336
DO0_7:
s_cmp_eq_u32 s61, 0
s_cbranch_scc1 EO0_7
s_sub_u32 s61, s61, 1
buffer_load_dword v87, s[36:39], 0 offen sc1 lds
s_sleep 1
ds_read_b32 v74, v82
s_waitcnt lgkmcnt(0)
v_cmp_gt_i32 vcc, v88, v74
s_cbranch_vccnz DO0_7
EO0_7:
s_mov_b32 m0, s58
s_branch BO0_7
LvO1:
s_barrier
buffer_load_dwordx4 v[56:59], v89, s[36:39], s49 offen sc1
buffer_load_dwordx2 v[60:61], v90, s[36:39], s49 offen sc1
s_add_u32 s49, s49, 346112
s_add_u32 m0, s50, 0
s_nop 0
D 85,0
D 85,1024
D 85,2048
D 85,3072
s_add_u32 m0, s50, 4096
s_nop 0
D 86,0
D 86,1024
D 86,2048
D 86,3072
s_waitcnt vmcnt(9)
v_lshlrev_b32 v0, 16, v56
v_and_b32 v1, 0xffff0000, v56
v_lshlrev_b32 v2, 16, v57
v_and_b32 v3, 0xffff0000, v57
v_lshlrev_b32 v4, 16, v58
v_and_b32 v5, 0xffff0000, v58
v_lshlrev_b32 v6, 16, v59
v_and_b32 v7, 0xffff0000, v59
s_waitcnt vmcnt(8)
v_lshlrev_b32 v8, 16, v60
v_and_b32 v9, 0xffff0000, v60
v_lshlrev_b32 v10, 16, v61
v_and_b32 v11, 0xffff0000, v61
buffer_load_dwordx4 v[56:59], v89, s[36:39], s49 offen sc1
buffer_load_dwordx2 v[60:61], v90, s[36:39], s49 offen sc1
s_add_u32 s49, s49, 346112
s_waitcnt vmcnt(2)
s_barrier
s_add_u32 m0, s50, 32768
R 24,0
D 85,0
R 28,1024
D 85,1024
R 32,2048
R 36,3072
D 85,2048
s_waitcnt lgkmcnt(2)
mav 0,0,24
R 40,4096
mav 4,4,24
mav 8,8,24
D 85,3072
mav 0,12,28
R 44,5120
mav 4,16,28
mav 8,20,28
s_add_u32 m0, s50, 36864
s_waitcnt lgkmcnt(2)
mav 0,24,32
R 48,6144
mav 4,28,32
D 86,0
mav 8,32,32
mav 0,36,36
R 52,7168
mav 4,40,36
mav 8,44,36
D 86,1024
s_waitcnt lgkmcnt(2)
mav 0,48,40
R 24,8192
mav 4,52,40
mav 8,56,40
mav 0,60,44
R 28,9216
mav 4,64,44
D 86,2048
mav 8,68,44
s_waitcnt lgkmcnt(2)
mav 0,72,48
R 32,10240
mav 4,76,48
mav 8,80,48
D 86,3072
mav 0,84,52
R 36,11264
mav 4,88,52
mav 8,92,52
s_waitcnt lgkmcnt(2)
mav 0,96,24
R 40,12288
mav 4,100,24
mav 8,104,24
mav 0,108,28
R 44,13312
mav 4,112,28
mav 8,116,28
s_waitcnt lgkmcnt(2)
mav 0,120,32
R 48,14336
mav 4,124,32
mav 8,128,32
mav 0,132,36
R 52,15360
mav 4,136,36
mav 8,140,36
s_waitcnt lgkmcnt(2)
mav 0,144,40
R 24,16384
mav 4,148,40
mav 8,152,40
mav 0,156,44
R 28,17408
mav 4,160,44
mav 8,164,44
s_waitcnt lgkmcnt(2)
mav 0,168,48
R 32,18432
mav 4,172,48
mav 8,176,48
mav 0,180,52
R 36,19456
mav 4,184,52
mav 8,188,52
s_waitcnt lgkmcnt(2)
mav 0,192,24
R 40,20480
mav 4,196,24
mav 8,200,24
s_waitcnt vmcnt(9)
v_lshlrev_b32 v12, 16, v56
mav 0,204,28
R 44,21504
v_and_b32 v13, 0xffff0000, v56
mav 4,208,28
v_lshlrev_b32 v14, 16, v57
v_and_b32 v15, 0xffff0000, v57
mav 8,212,28
v_lshlrev_b32 v16, 16, v58
s_waitcnt lgkmcnt(2)
mav 0,216,32
R 48,22528
v_and_b32 v17, 0xffff0000, v58
mav 4,220,32
v_lshlrev_b32 v18, 16, v59
v_and_b32 v19, 0xffff0000, v59
mav 8,224,32
s_waitcnt vmcnt(8)
v_lshlrev_b32 v20, 16, v60
mav 0,228,36
R 52,23552
v_and_b32 v21, 0xffff0000, v60
mav 4,232,36
v_lshlrev_b32 v22, 16, v61
v_and_b32 v23, 0xffff0000, v61
mav 8,236,36
s_waitcnt lgkmcnt(2)
mav 0,240,40
R 24,24576
mav 4,244,40
mav 8,248,40
mav 0,252,44
R 28,25600
mvv 4,128,44
buffer_load_dwordx4 v[56:59], v89, s[36:39], s49 offen sc1
mvv 8,132,44
s_waitcnt lgkmcnt(2)
mvv 0,136,48
R 32,26624
mvv 4,140,48
mvv 8,144,48
buffer_load_dwordx2 v[60:61], v90, s[36:39], s49 offen sc1
mvv 0,148,52
R 36,27648
mvv 4,152,52
mvv 8,156,52
s_waitcnt lgkmcnt(2)
mvv 0,160,24
R 40,28672
mvv 4,164,24
mvv 8,168,24
mvv 0,172,28
R 44,29696
mvv 4,176,28
mvv 8,180,28
s_waitcnt lgkmcnt(2)
mvv 0,184,32
R 48,30720
s_xor_b32 s50, s50, 0x10000
mvv 4,188,32
mvv 8,192,32
mvv 0,196,36
R 52,31744
mvv 4,200,36
s_add_u32 s49, s49, 346112
mvv 8,204,36
s_waitcnt vmcnt(2)
s_barrier
s_waitcnt lgkmcnt(2)
mvv 0,208,40
R 24,32768
s_add_u32 m0, s50, 0
mvv 4,212,40
D 85,0
mvv 8,216,40
mvv 0,220,44
R 28,33792
mvv 4,224,44
mvv 8,228,44
D 85,1024
s_waitcnt lgkmcnt(2)
mvv 0,232,48
R 32,34816
mvv 4,236,48
mvv 8,240,48
mvv 0,244,52
R 36,35840
mvv 4,248,52
D 85,2048
mvv 8,252,52
s_waitcnt lgkmcnt(2)
mav 12,0,24
R 40,36864
mav 16,4,24
v_cvt_pk_bf16_f32 v62, v0, v1
v_cvt_pk_bf16_f32 v63, v2, v3
mav 20,8,24
D 85,3072
v_cvt_pk_bf16_f32 v64, v4, v5
mav 12,12,28
R 44,37888
v_cvt_pk_bf16_f32 v65, v6, v7
mav 16,16,28
v_cvt_pk_bf16_f32 v66, v8, v9
v_cvt_pk_bf16_f32 v67, v10, v11
mav 20,20,28
s_add_u32 m0, s50, 4096
s_waitcnt lgkmcnt(2)
mav 12,24,32
R 48,38912
buffer_store_dwordx4 v[62:65], v83, s[36:39], s49 offen sc1
mav 16,28,32
v_cvt_pk_bf16_f32 v68, v8, v9
v_cvt_pk_bf16_f32 v69, v10, v11
mav 20,32,32
v_permlane16_swap_b32 v66, v68
v_permlane16_swap_b32 v67, v69
mav 12,36,36
R 52,39936
v_mul_f32 v73, v0, v0
mav 16,40,36
buffer_store_dwordx4 v[66:69], v84, s[36:39], s49 offen sc1
v_add_f32 v72, v1, v0
mav 20,44,36
v_fmac_f32 v73, v1, v1
s_waitcnt lgkmcnt(2)
mav 12,48,40
R 24,40960
v_add_f32 v72, v2, v72
mav 16,52,40
v_fmac_f32 v73, v2, v2
v_add_f32 v72, v3, v72
mav 20,56,40
D 86,0
v_fmac_f32 v73, v3, v3
mav 12,60,44
R 28,41984
v_add_f32 v72, v4, v72
mav 16,64,44
v_fmac_f32 v73, v4, v4
v_add_f32 v72, v5, v72
mav 20,68,44
v_fmac_f32 v73, v5, v5
s_waitcnt lgkmcnt(2)
mav 12,72,48
R 32,43008
D 86,1024
mav 16,76,48
v_add_f32 v72, v6, v72
v_fmac_f32 v73, v6, v6
mav 20,80,48
v_add_f32 v72, v7, v72
v_fmac_f32 v73, v7, v7
mav 12,84,52
R 36,44032
v_add_f32 v72, v8, v72
mav 16,88,52
D 86,2048
v_fmac_f32 v73, v8, v8
mav 20,92,52
v_add_f32 v72, v9, v72
s_waitcnt lgkmcnt(2)
mav 12,96,24
R 40,45056
v_fmac_f32 v73, v9, v9
mav 16,100,24
v_add_f32 v72, v10, v72
v_fmac_f32 v73, v10, v10
mav 20,104,24
D 86,3072
v_add_f32 v72, v11, v72
mav 12,108,28
R 44,46080
v_fmac_f32 v73, v11, v11
mav 16,112,28
mav 20,116,28
v_permlane32_swap_b32 v72, v73
s_waitcnt lgkmcnt(2)
mav 12,120,32
R 48,47104
v_add_f32 v72, v72, v73
mav 16,124,32
mav 20,128,32
mav 12,132,36
R 52,48128
mav 16,136,36
mav 20,140,36
s_waitcnt lgkmcnt(2)
mav 12,144,40
R 24,49152
mav 16,148,40
mav 20,152,40
mav 12,156,44
R 28,50176
mav 16,160,44
mav 20,164,44
s_waitcnt lgkmcnt(2)
mav 12,168,48
R 32,51200
mav 16,172,48
mav 20,176,48
mav 12,180,52
R 36,52224
mav 16,184,52
mav 20,188,52
s_waitcnt lgkmcnt(2)
mav 12,192,24
R 40,53248
mav 16,196,24
mav 20,200,24
s_waitcnt vmcnt(11)
v_lshlrev_b32 v0, 16, v56
mav 12,204,28
R 44,54272
v_and_b32 v1, 0xffff0000, v56
mav 16,208,28
v_lshlrev_b32 v2, 16, v57
v_and_b32 v3, 0xffff0000, v57
mav 20,212,28
v_lshlrev_b32 v4, 16, v58
s_waitcnt lgkmcnt(2)
mav 12,216,32
R 48,55296
v_and_b32 v5, 0xffff0000, v58
mav 16,220,32
v_lshlrev_b32 v6, 16, v59
v_and_b32 v7, 0xffff0000, v59
mav 20,224,32
s_waitcnt vmcnt(10)
v_lshlrev_b32 v8, 16, v60
mav 12,228,36
R 52,56320
v_and_b32 v9, 0xffff0000, v60
mav 16,232,36
v_lshlrev_b32 v10, 16, v61
v_and_b32 v11, 0xffff0000, v61
mav 20,236,36
s_waitcnt lgkmcnt(2)
mav 12,240,40
R 24,57344
mav 16,244,40
mav 20,248,40
mav 12,252,44
R 28,58368
mvv 16,128,44
buffer_load_dwordx4 v[56:59], v89, s[36:39], s49 offen sc1
v_add_u32 v70, 1, v70
mvv 20,132,44
s_waitcnt lgkmcnt(2)
mvv 12,136,48
R 32,59392
mvv 16,140,48
mvv 20,144,48
buffer_load_dwordx2 v[60:61], v90, s[36:39], s49 offen sc1
mvv 12,148,52
R 36,60416
mvv 16,152,52
s_waitcnt vmcnt(6)
buffer_store_dwordx2 v[70:71], v81, s[36:39], s52 offen sc1
mvv 20,156,52
s_waitcnt lgkmcnt(2)
mvv 12,160,24
R 40,61440
mvv 16,164,24
mvv 20,168,24
ds_write_b32 v91, v72 offset:0
mvv 12,172,28
R 44,62464
mvv 16,176,28
mvv 20,180,28
s_waitcnt lgkmcnt(3)
mvv 12,184,32
R 48,63488
mvv 16,188,32
mvv 20,192,32
mvv 12,196,36
R 52,64512
v_xor_b32 v80, 0x10000, v80
mvv 16,200,36
s_add_u32 s49, s49, 346112
mvv 20,204,36
s_waitcnt vmcnt(3) lgkmcnt(2)
s_barrier
mvv 12,208,40
R 24,0
s_add_u32 m0, s50, 32768
mvv 16,212,40
ds_read_b128 v[76:79], v92 offset:0
D 85,0
mvv 20,216,40
mvv 12,220,44
R 28,1024
mvv 16,224,44
mvv 20,228,44
D 85,1024
s_waitcnt lgkmcnt(3)
mvv 12,232,48
R 32,2048
mvv 16,236,48
mvv 20,240,48
s_waitcnt lgkmcnt(2)
v_add_f32 v76, v76, v77
mvv 12,244,52
R 36,3072
v_add_f32 v78, v78, v79
mvv 16,248,52
D 85,2048
v_add_f32 v76, v76, v78
mvv 20,252,52
s_mov_b32 s60, -30
.p2align 6
LO1_1:
mav 0,0,24
R 40,4096
v_add_f32_dpp v77, v76, v76 quad_perm:[1,0,3,2] row_mask:0xf bank_mask:0xf
mav 4,4,24
buffer_store_dword v77, v93, s[36:39], s49 offen sc1
v_cvt_pk_bf16_f32 v62, v12, v13
mav 8,8,24
D 85,3072
s_waitcnt lgkmcnt(2)
mav 0,12,28
R 44,5120
v_cvt_pk_bf16_f32 v63, v14, v15
mav 4,16,28
v_cvt_pk_bf16_f32 v64, v16, v17
v_cvt_pk_bf16_f32 v65, v18, v19
mav 8,20,28
s_add_u32 m0, s50, 36864
v_cvt_pk_bf16_f32 v66, v20, v21
mav 0,24,32
R 48,6144
buffer_store_dwordx4 v[62:65], v83, s[36:39], s49 offen sc1
mav 4,28,32
v_cvt_pk_bf16_f32 v67, v22, v23
v_cvt_pk_bf16_f32 v68, v20, v21
mav 8,32,32
v_cvt_pk_bf16_f32 v69, v22, v23
s_waitcnt lgkmcnt(2)
mav 0,36,36
R 52,7168
v_permlane16_swap_b32 v66, v68
mav 4,40,36
D 86,0
v_permlane16_swap_b32 v67, v69
mav 8,44,36
v_mul_f32 v73, v12, v12
v_add_f32 v72, v13, v12
mav 0,48,40
R 24,8192
v_fmac_f32 v73, v13, v13
mav 4,52,40
v_add_f32 v72, v14, v72
v_fmac_f32 v73, v14, v14
mav 8,56,40
buffer_store_dwordx4 v[66:69], v84, s[36:39], s49 offen sc1
s_waitcnt lgkmcnt(2)
mav 0,60,44
R 28,9216
v_add_f32 v72, v15, v72
mav 4,64,44
v_fmac_f32 v73, v15, v15
v_add_f32 v72, v16, v72
mav 8,68,44
v_fmac_f32 v73, v16, v16
v_add_f32 v72, v17, v72
mav 0,72,48
R 32,10240
D 86,1024
mav 4,76,48
v_fmac_f32 v73, v17, v17
v_add_f32 v72, v18, v72
mav 8,80,48
v_fmac_f32 v73, v18, v18
s_waitcnt lgkmcnt(2)
mav 0,84,52
R 36,11264
v_add_f32 v72, v19, v72
mav 4,88,52
D 86,2048
v_fmac_f32 v73, v19, v19
mav 8,92,52
v_add_f32 v72, v20, v72
v_fmac_f32 v73, v20, v20
mav 0,96,24
R 40,12288
v_add_f32 v72, v21, v72
mav 4,100,24
v_fmac_f32 v73, v21, v21
v_add_f32 v72, v22, v72
mav 8,104,24
D 86,3072
s_waitcnt lgkmcnt(2)
mav 0,108,28
R 44,13312
v_fmac_f32 v73, v22, v22
mav 4,112,28
v_add_f32 v72, v23, v72
v_fmac_f32 v73, v23, v23
mav 8,116,28
mav 0,120,32
R 48,14336
v_permlane32_swap_b32 v72, v73
mav 4,124,32
v_add_f32 v72, v72, v73
mav 8,128,32
s_waitcnt lgkmcnt(2)
mav 0,132,36
R 52,15360
mav 4,136,36
mav 8,140,36
mav 0,144,40
R 24,16384
mav 4,148,40
mav 8,152,40
s_waitcnt lgkmcnt(2)
mav 0,156,44
R 28,17408
mav 4,160,44
mav 8,164,44
mav 0,168,48
R 32,18432
mav 4,172,48
mav 8,176,48
s_waitcnt lgkmcnt(2)
mav 0,180,52
R 36,19456
mav 4,184,52
mav 8,188,52
mav 0,192,24
R 40,20480
mav 4,196,24
mav 8,200,24
s_waitcnt lgkmcnt(2)
mav 0,204,28
R 44,21504
mav 4,208,28
s_waitcnt vmcnt(13)
v_lshlrev_b32 v12, 16, v56
mav 8,212,28
v_and_b32 v13, 0xffff0000, v56
v_lshlrev_b32 v14, 16, v57
mav 0,216,32
R 48,22528
v_and_b32 v15, 0xffff0000, v57
mav 4,220,32
v_lshlrev_b32 v16, 16, v58
v_and_b32 v17, 0xffff0000, v58
mav 8,224,32
v_lshlrev_b32 v18, 16, v59
s_waitcnt lgkmcnt(2)
mav 0,228,36
R 52,23552
v_and_b32 v19, 0xffff0000, v59
mav 4,232,36
s_waitcnt vmcnt(12)
v_lshlrev_b32 v20, 16, v60
mav 8,236,36
v_and_b32 v21, 0xffff0000, v60
v_lshlrev_b32 v22, 16, v61
mav 0,240,40
R 24,24576
v_and_b32 v23, 0xffff0000, v61
mav 4,244,40
mav 8,248,40
s_waitcnt lgkmcnt(2)
mav 0,252,44
R 28,25600
mvv 4,128,44
buffer_load_dwordx4 v[56:59], v89, s[36:39], s49 offen sc1
v_add_u32 v70, 1, v70
mvv 8,132,44
mvv 0,136,48
R 32,26624
mvv 4,140,48
mvv 8,144,48
buffer_load_dwordx2 v[60:61], v90, s[36:39], s49 offen sc1
s_waitcnt lgkmcnt(2)
mvv 0,148,52
R 36,27648
mvv 4,152,52
s_waitcnt vmcnt(5)
buffer_store_dwordx2 v[70:71], v81, s[36:39], s52 offen sc1
mvv 8,156,52
mvv 0,160,24
R 40,28672
mvv 4,164,24
mvv 8,168,24
s_waitcnt lgkmcnt(2)
mvv 0,172,28
R 44,29696
mvv 4,176,28
ds_write_b32 v91, v72 offset:1024
mvv 8,180,28
mvv 0,184,32
R 48,30720
s_xor_b32 s50, s50, 0x10000
mvv 4,188,32
mvv 8,192,32
s_waitcnt lgkmcnt(3)
mvv 0,196,36
R 52,31744
mvv 4,200,36
s_add_u32 s49, s49, 346112
mvv 8,204,36
s_waitcnt vmcnt(3) lgkmcnt(2)
s_barrier
mvv 0,208,40
R 24,32768
s_add_u32 m0, s50, 0
mvv 4,212,40
ds_read_b128 v[76:79], v92 offset:1024
D 85,0
mvv 8,216,40
mvv 0,220,44
R 28,33792
mvv 4,224,44
mvv 8,228,44
D 85,1024
s_waitcnt lgkmcnt(3)
mvv 0,232,48
R 32,34816
mvv 4,236,48
mvv 8,240,48
s_waitcnt lgkmcnt(2)
v_add_f32 v76, v76, v77
mvv 0,244,52
R 36,35840
v_add_f32 v78, v78, v79
mvv 4,248,52
D 85,2048
v_add_f32 v76, v76, v78
mvv 8,252,52
mav 12,0,24
R 40,36864
v_add_f32_dpp v77, v76, v76 quad_perm:[1,0,3,2] row_mask:0xf bank_mask:0xf
mav 16,4,24
buffer_store_dword v77, v93, s[36:39], s49 offen sc1
v_cvt_pk_bf16_f32 v62, v0, v1
mav 20,8,24
D 85,3072
s_waitcnt lgkmcnt(2)
mav 12,12,28
R 44,37888
v_cvt_pk_bf16_f32 v63, v2, v3
mav 16,16,28
v_cvt_pk_bf16_f32 v64, v4, v5
v_cvt_pk_bf16_f32 v65, v6, v7
mav 20,20,28
s_add_u32 m0, s50, 4096
v_cvt_pk_bf16_f32 v66, v8, v9
mav 12,24,32
R 48,38912
buffer_store_dwordx4 v[62:65], v83, s[36:39], s49 offen sc1
mav 16,28,32
v_cvt_pk_bf16_f32 v67, v10, v11
v_cvt_pk_bf16_f32 v68, v8, v9
mav 20,32,32
v_cvt_pk_bf16_f32 v69, v10, v11
s_waitcnt lgkmcnt(2)
mav 12,36,36
R 52,39936
v_permlane16_swap_b32 v66, v68
mav 16,40,36
D 86,0
v_permlane16_swap_b32 v67, v69
mav 20,44,36
v_mul_f32 v73, v0, v0
v_add_f32 v72, v1, v0
mav 12,48,40
R 24,40960
v_fmac_f32 v73, v1, v1
mav 16,52,40
v_add_f32 v72, v2, v72
v_fmac_f32 v73, v2, v2
mav 20,56,40
buffer_store_dwordx4 v[66:69], v84, s[36:39], s49 offen sc1
s_waitcnt lgkmcnt(2)
mav 12,60,44
R 28,41984
v_add_f32 v72, v3, v72
mav 16,64,44
v_fmac_f32 v73, v3, v3
v_add_f32 v72, v4, v72
mav 20,68,44
v_fmac_f32 v73, v4, v4
v_add_f32 v72, v5, v72
mav 12,72,48
R 32,43008
D 86,1024
mav 16,76,48
v_fmac_f32 v73, v5, v5
v_add_f32 v72, v6, v72
mav 20,80,48
v_fmac_f32 v73, v6, v6
s_waitcnt lgkmcnt(2)
mav 12,84,52
R 36,44032
v_add_f32 v72, v7, v72
mav 16,88,52
D 86,2048
v_fmac_f32 v73, v7, v7
mav 20,92,52
v_add_f32 v72, v8, v72
v_fmac_f32 v73, v8, v8
mav 12,96,24
R 40,45056
v_add_f32 v72, v9, v72
mav 16,100,24
v_fmac_f32 v73, v9, v9
v_add_f32 v72, v10, v72
mav 20,104,24
D 86,3072
s_waitcnt lgkmcnt(2)
mav 12,108,28
R 44,46080
v_fmac_f32 v73, v10, v10
mav 16,112,28
v_add_f32 v72, v11, v72
v_fmac_f32 v73, v11, v11
mav 20,116,28
mav 12,120,32
R 48,47104
v_permlane32_swap_b32 v72, v73
mav 16,124,32
v_add_f32 v72, v72, v73
mav 20,128,32
s_waitcnt lgkmcnt(2)
mav 12,132,36
R 52,48128
mav 16,136,36
mav 20,140,36
mav 12,144,40
R 24,49152
mav 16,148,40
mav 20,152,40
s_waitcnt lgkmcnt(2)
mav 12,156,44
R 28,50176
mav 16,160,44
mav 20,164,44
mav 12,168,48
R 32,51200
mav 16,172,48
mav 20,176,48
s_waitcnt lgkmcnt(2)
mav 12,180,52
R 36,52224
mav 16,184,52
mav 20,188,52
mav 12,192,24
R 40,53248
mav 16,196,24
mav 20,200,24
s_waitcnt lgkmcnt(2)
mav 12,204,28
R 44,54272
mav 16,208,28
s_waitcnt vmcnt(13)
v_lshlrev_b32 v0, 16, v56
mav 20,212,28
v_and_b32 v1, 0xffff0000, v56
v_lshlrev_b32 v2, 16, v57
mav 12,216,32
R 48,55296
v_and_b32 v3, 0xffff0000, v57
mav 16,220,32
v_lshlrev_b32 v4, 16, v58
v_and_b32 v5, 0xffff0000, v58
mav 20,224,32
v_lshlrev_b32 v6, 16, v59
s_waitcnt lgkmcnt(2)
mav 12,228,36
R 52,56320
v_and_b32 v7, 0xffff0000, v59
mav 16,232,36
s_waitcnt vmcnt(12)
v_lshlrev_b32 v8, 16, v60
mav 20,236,36
v_and_b32 v9, 0xffff0000, v60
v_lshlrev_b32 v10, 16, v61
mav 12,240,40
R 24,57344
v_and_b32 v11, 0xffff0000, v61
mav 16,244,40
mav 20,248,40
s_waitcnt lgkmcnt(2)
mav 12,252,44
R 28,58368
mvv 16,128,44
buffer_load_dwordx4 v[56:59], v89, s[36:39], s49 offen sc1
v_add_u32 v70, 1, v70
mvv 20,132,44
mvv 12,136,48
R 32,59392
mvv 16,140,48
mvv 20,144,48
buffer_load_dwordx2 v[60:61], v90, s[36:39], s49 offen sc1
s_waitcnt lgkmcnt(2)
mvv 12,148,52
R 36,60416
mvv 16,152,52
s_waitcnt vmcnt(5)
buffer_store_dwordx2 v[70:71], v81, s[36:39], s52 offen sc1
mvv 20,156,52
mvv 12,160,24
R 40,61440
mvv 16,164,24
mvv 20,168,24
s_waitcnt lgkmcnt(2)
mvv 12,172,28
R 44,62464
mvv 16,176,28
ds_write_b32 v91, v72 offset:0
mvv 20,180,28
mvv 12,184,32
R 48,63488
mvv 16,188,32
mvv 20,192,32
s_waitcnt lgkmcnt(3)
mvv 12,196,36
R 52,64512
v_xor_b32 v80, 0x10000, v80
mvv 16,200,36
s_add_u32 s49, s49, 346112
mvv 20,204,36
s_waitcnt vmcnt(3) lgkmcnt(2)
s_barrier
mvv 12,208,40
R 24,0
s_add_u32 m0, s50, 32768
mvv 16,212,40
ds_read_b128 v[76:79], v92 offset:0
D 85,0
mvv 20,216,40
mvv 12,220,44
R 28,1024
mvv 16,224,44
mvv 20,228,44
D 85,1024
s_waitcnt lgkmcnt(3)
mvv 12,232,48
R 32,2048
mvv 16,236,48
mvv 20,240,48
s_waitcnt lgkmcnt(2)
v_add_f32 v76, v76, v77
mvv 12,244,52
R 36,3072
v_add_f32 v78, v78, v79
mvv 16,248,52
D 85,2048
v_add_f32 v76, v76, v78
mvv 20,252,52
s_add_u32 s60, s60, 1
s_cbranch_scc0 LO1_1
mav 0,0,24
R 40,4096
v_add_f32_dpp v77, v76, v76 quad_perm:[1,0,3,2] row_mask:0xf bank_mask:0xf
mav 4,4,24
buffer_store_dword v77, v93, s[36:39], s49 offen sc1
v_cvt_pk_bf16_f32 v62, v12, v13
mav 8,8,24
D 85,3072
s_waitcnt lgkmcnt(2)
mav 0,12,28
R 44,5120
v_cvt_pk_bf16_f32 v63, v14, v15
mav 4,16,28
v_cvt_pk_bf16_f32 v64, v16, v17
v_cvt_pk_bf16_f32 v65, v18, v19
mav 8,20,28
s_add_u32 m0, s50, 36864
v_cvt_pk_bf16_f32 v66, v20, v21
mav 0,24,32
R 48,6144
buffer_store_dwordx4 v[62:65], v83, s[36:39], s49 offen sc1
mav 4,28,32
v_cvt_pk_bf16_f32 v67, v22, v23
v_cvt_pk_bf16_f32 v68, v20, v21
mav 8,32,32
v_cvt_pk_bf16_f32 v69, v22, v23
s_waitcnt lgkmcnt(2)
mav 0,36,36
R 52,7168
v_permlane16_swap_b32 v66, v68
mav 4,40,36
D 86,0
v_permlane16_swap_b32 v67, v69
mav 8,44,36
v_mul_f32 v73, v12, v12
v_add_f32 v72, v13, v12
mav 0,48,40
R 24,8192
v_fmac_f32 v73, v13, v13
mav 4,52,40
v_add_f32 v72, v14, v72
v_fmac_f32 v73, v14, v14
mav 8,56,40
buffer_store_dwordx4 v[66:69], v84, s[36:39], s49 offen sc1
s_waitcnt lgkmcnt(2)
mav 0,60,44
R 28,9216
v_add_f32 v72, v15, v72
mav 4,64,44
v_fmac_f32 v73, v15, v15
v_add_f32 v72, v16, v72
mav 8,68,44
v_fmac_f32 v73, v16, v16
v_add_f32 v72, v17, v72
mav 0,72,48
R 32,10240
D 86,1024
mav 4,76,48
v_fmac_f32 v73, v17, v17
v_add_f32 v72, v18, v72
mav 8,80,48
v_fmac_f32 v73, v18, v18
s_waitcnt lgkmcnt(2)
mav 0,84,52
R 36,11264
v_add_f32 v72, v19, v72
mav 4,88,52
D 86,2048
v_fmac_f32 v73, v19, v19
mav 8,92,52
v_add_f32 v72, v20, v72
v_fmac_f32 v73, v20, v20
mav 0,96,24
R 40,12288
v_add_f32 v72, v21, v72
mav 4,100,24
v_fmac_f32 v73, v21, v21
v_add_f32 v72, v22, v72
mav 8,104,24
D 86,3072
s_waitcnt lgkmcnt(2)
mav 0,108,28
R 44,13312
v_fmac_f32 v73, v22, v22
mav 4,112,28
v_add_f32 v72, v23, v72
v_fmac_f32 v73, v23, v23
mav 8,116,28
mav 0,120,32
R 48,14336
v_permlane32_swap_b32 v72, v73
mav 4,124,32
v_add_f32 v72, v72, v73
mav 8,128,32
s_waitcnt lgkmcnt(2)
mav 0,132,36
R 52,15360
mav 4,136,36
mav 8,140,36
mav 0,144,40
R 24,16384
mav 4,148,40
mav 8,152,40
s_waitcnt lgkmcnt(2)
mav 0,156,44
R 28,17408
mav 4,160,44
mav 8,164,44
mav 0,168,48
R 32,18432
mav 4,172,48
mav 8,176,48
s_waitcnt lgkmcnt(2)
mav 0,180,52
R 36,19456
mav 4,184,52
mav 8,188,52
mav 0,192,24
R 40,20480
mav 4,196,24
mav 8,200,24
s_waitcnt lgkmcnt(2)
mav 0,204,28
R 44,21504
mav 4,208,28
s_waitcnt vmcnt(13)
v_lshlrev_b32 v12, 16, v56
mav 8,212,28
v_and_b32 v13, 0xffff0000, v56
v_lshlrev_b32 v14, 16, v57
mav 0,216,32
R 48,22528
v_and_b32 v15, 0xffff0000, v57
mav 4,220,32
v_lshlrev_b32 v16, 16, v58
v_and_b32 v17, 0xffff0000, v58
mav 8,224,32
v_lshlrev_b32 v18, 16, v59
s_waitcnt lgkmcnt(2)
mav 0,228,36
R 52,23552
v_and_b32 v19, 0xffff0000, v59
mav 4,232,36
s_waitcnt vmcnt(12)
v_lshlrev_b32 v20, 16, v60
mav 8,236,36
v_and_b32 v21, 0xffff0000, v60
v_lshlrev_b32 v22, 16, v61
mav 0,240,40
R 24,24576
v_and_b32 v23, 0xffff0000, v61
mav 4,244,40
mav 8,248,40
s_waitcnt lgkmcnt(2)
mav 0,252,44
R 28,25600
mvv 4,128,44
v_add_u32 v70, 1, v70
mvv 8,132,44
mvv 0,136,48
R 32,26624
mvv 4,140,48
mvv 8,144,48
s_waitcnt lgkmcnt(2)
mvv 0,148,52
R 36,27648
mvv 4,152,52
s_waitcnt vmcnt(3)
buffer_store_dwordx2 v[70:71], v81, s[36:39], s52 offen sc1
mvv 8,156,52
mvv 0,160,24
R 40,28672
mvv 4,164,24
mvv 8,168,24
s_waitcnt lgkmcnt(2)
mvv 0,172,28
R 44,29696
mvv 4,176,28
ds_write_b32 v91, v72 offset:1024
mvv 8,180,28
mvv 0,184,32
R 48,30720
mvv 4,188,32
mvv 8,192,32
s_waitcnt lgkmcnt(3)
mvv 0,196,36
R 52,31744
mvv 4,200,36
s_add_u32 s49, s49, 346112
mvv 8,204,36
s_waitcnt vmcnt(1) lgkmcnt(2)
s_barrier
mvv 0,208,40
R 24,32768
ds_read_b128 v[76:79], v92 offset:1024
mvv 4,212,40
mvv 8,216,40
mvv 0,220,44
R 28,33792
mvv 4,224,44
mvv 8,228,44
s_waitcnt lgkmcnt(3)
mvv 0,232,48
R 32,34816
mvv 4,236,48
s_waitcnt lgkmcnt(2)
v_add_f32 v76, v76, v77
mvv 8,240,48
v_add_f32 v78, v78, v79
v_add_f32 v76, v76, v78
mvv 0,244,52
R 36,35840
v_add_f32_dpp v77, v76, v76 quad_perm:[1,0,3,2] row_mask:0xf bank_mask:0xf
mvv 4,248,52
buffer_store_dword v77, v93, s[36:39], s49 offen sc1
mvv 8,252,52
mav 12,0,24
R 40,36864
mav 16,4,24
v_cvt_pk_bf16_f32 v62, v0, v1
v_cvt_pk_bf16_f32 v63, v2, v3
mav 20,8,24
v_cvt_pk_bf16_f32 v64, v4, v5
s_waitcnt lgkmcnt(2)
mav 12,12,28
R 44,37888
v_cvt_pk_bf16_f32 v65, v6, v7
mav 16,16,28
buffer_store_dwordx4 v[62:65], v83, s[36:39], s49 offen sc1
v_cvt_pk_bf16_f32 v66, v8, v9
mav 20,20,28
v_cvt_pk_bf16_f32 v67, v10, v11
v_cvt_pk_bf16_f32 v68, v8, v9
mav 12,24,32
R 48,38912
v_cvt_pk_bf16_f32 v69, v10, v11
mav 16,28,32
v_permlane16_swap_b32 v66, v68
v_permlane16_swap_b32 v67, v69
mav 20,32,32
buffer_store_dwordx4 v[66:69], v84, s[36:39], s49 offen sc1
s_waitcnt lgkmcnt(2)
mav 12,36,36
R 52,39936
v_mul_f32 v73, v0, v0
mav 16,40,36
v_add_f32 v72, v1, v0
v_fmac_f32 v73, v1, v1
mav 20,44,36
v_add_f32 v72, v2, v72
v_fmac_f32 v73, v2, v2
mav 12,48,40
R 24,40960
v_add_f32 v72, v3, v72
mav 16,52,40
v_fmac_f32 v73, v3, v3
v_add_f32 v72, v4, v72
mav 20,56,40
v_fmac_f32 v73, v4, v4
s_waitcnt lgkmcnt(2)
mav 12,60,44
R 28,41984
v_add_f32 v72, v5, v72
mav 16,64,44
v_fmac_f32 v73, v5, v5
v_add_f32 v72, v6, v72
mav 20,68,44
v_fmac_f32 v73, v6, v6
v_add_f32 v72, v7, v72
mav 12,72,48
R 32,43008
v_fmac_f32 v73, v7, v7
mav 16,76,48
v_add_f32 v72, v8, v72
v_fmac_f32 v73, v8, v8
mav 20,80,48
v_add_f32 v72, v9, v72
s_waitcnt lgkmcnt(2)
mav 12,84,52
R 36,44032
v_fmac_f32 v73, v9, v9
mav 16,88,52
v_add_f32 v72, v10, v72
v_fmac_f32 v73, v10, v10
mav 20,92,52
v_add_f32 v72, v11, v72
v_fmac_f32 v73, v11, v11
mav 12,96,24
R 40,45056
v_permlane32_swap_b32 v72, v73
mav 16,100,24
v_add_f32 v72, v72, v73
mav 20,104,24
s_waitcnt lgkmcnt(2)
mav 12,108,28
R 44,46080
mav 16,112,28
mav 20,116,28
mav 12,120,32
R 48,47104
mav 16,124,32
mav 20,128,32
s_waitcnt lgkmcnt(2)
mav 12,132,36
R 52,48128
mav 16,136,36
mav 20,140,36
mav 12,144,40
R 24,49152
mav 16,148,40
mav 20,152,40
s_waitcnt lgkmcnt(2)
mav 12,156,44
R 28,50176
mav 16,160,44
mav 20,164,44
mav 12,168,48
R 32,51200
mav 16,172,48
mav 20,176,48
s_waitcnt lgkmcnt(2)
mav 12,180,52
R 36,52224
mav 16,184,52
mav 20,188,52
mav 12,192,24
R 40,53248
mav 16,196,24
mav 20,200,24
s_waitcnt lgkmcnt(2)
mav 12,204,28
R 44,54272
mav 16,208,28
mav 20,212,28
mav 12,216,32
R 48,55296
mav 16,220,32
mav 20,224,32
s_waitcnt lgkmcnt(2)
mav 12,228,36
R 52,56320
mav 16,232,36
mav 20,236,36
mav 12,240,40
R 24,57344
mav 16,244,40
mav 20,248,40
s_waitcnt lgkmcnt(2)
mav 12,252,44
R 28,58368
mvv 16,128,44
v_add_u32 v70, 1, v70
mvv 20,132,44
mvv 12,136,48
R 32,59392
mvv 16,140,48
mvv 20,144,48
s_waitcnt lgkmcnt(2)
mvv 12,148,52
R 36,60416
mvv 16,152,52
s_waitcnt vmcnt(0)
buffer_store_dwordx2 v[70:71], v81, s[36:39], s52 offen sc1
mvv 20,156,52
mvv 12,160,24
R 40,61440
mvv 16,164,24
mvv 20,168,24
s_waitcnt lgkmcnt(2)
mvv 12,172,28
R 44,62464
mvv 16,176,28
ds_write_b32 v91, v72 offset:0
mvv 20,180,28
mvv 12,184,32
R 48,63488
mvv 16,188,32
mvv 20,192,32
s_waitcnt lgkmcnt(3)
mvv 12,196,36
R 52,64512
mvv 16,200,36
s_add_u32 s49, s49, 346112
mvv 20,204,36
s_waitcnt lgkmcnt(2)
s_barrier
mvv 12,208,40
ds_read_b128 v[76:79], v92 offset:0
mvv 16,212,40
mvv 20,216,40
mvv 12,220,44
mvv 16,224,44
mvv 20,228,44
s_waitcnt lgkmcnt(1)
mvv 12,232,48
mvv 16,236,48
s_waitcnt lgkmcnt(0)
v_add_f32 v76, v76, v77
mvv 20,240,48
v_add_f32 v78, v78, v79
v_add_f32 v76, v76, v78
mvv 12,244,52
)"
R"(
mvv 16,248,52
v_add_f32_dpp v77, v76, v76 quad_perm:[1,0,3,2] row_mask:0xf bank_mask:0xf
buffer_store_dword v77, v93, s[36:39], s49 offen sc1
mvv 20,252,52
s_nop 3
v_cvt_pk_bf16_f32 v62, v12, v13
v_cvt_pk_bf16_f32 v63, v14, v15
v_cvt_pk_bf16_f32 v64, v16, v17
v_cvt_pk_bf16_f32 v65, v18, v19
buffer_store_dwordx4 v[62:65], v83, s[36:39], s49 offen sc1
v_cvt_pk_bf16_f32 v66, v20, v21
v_cvt_pk_bf16_f32 v67, v22, v23
v_cvt_pk_bf16_f32 v68, v20, v21
v_cvt_pk_bf16_f32 v69, v22, v23
s_nop 0
v_permlane16_swap_b32 v66, v68
v_permlane16_swap_b32 v67, v69
buffer_store_dwordx4 v[66:69], v84, s[36:39], s49 offen sc1
v_mul_f32 v73, v12, v12
v_add_f32 v72, v13, v12
v_fmac_f32 v73, v13, v13
v_add_f32 v72, v14, v72
v_fmac_f32 v73, v14, v14
v_add_f32 v72, v15, v72
v_fmac_f32 v73, v15, v15
v_add_f32 v72, v16, v72
v_fmac_f32 v73, v16, v16
v_add_f32 v72, v17, v72
v_fmac_f32 v73, v17, v17
v_add_f32 v72, v18, v72
v_fmac_f32 v73, v18, v18
v_add_f32 v72, v19, v72
v_fmac_f32 v73, v19, v19
v_add_f32 v72, v20, v72
v_fmac_f32 v73, v20, v20
v_add_f32 v72, v21, v72
v_fmac_f32 v73, v21, v21
v_add_f32 v72, v22, v72
v_fmac_f32 v73, v22, v22
v_add_f32 v72, v23, v72
v_fmac_f32 v73, v23, v23
s_nop 1
v_permlane32_swap_b32 v72, v73
v_add_f32 v72, v72, v73
ds_write_b32 v91, v72 offset:1024
s_add_u32 s49, s49, 346112
s_waitcnt lgkmcnt(0)
s_barrier
ds_read_b128 v[76:79], v92 offset:1024
s_waitcnt lgkmcnt(0)
v_add_f32 v76, v76, v77
v_add_f32 v78, v78, v79
v_add_f32 v76, v76, v78
s_nop 1
v_add_f32_dpp v77, v76, v76 quad_perm:[1,0,3,2] row_mask:0xf bank_mask:0xf
buffer_store_dword v77, v93, s[36:39], s49 offen sc1
v_add_u32 v70, 37, v70
s_waitcnt vmcnt(0)
buffer_store_dwordx2 v[70:71], v81, s[36:39], s52 offen sc1
s_endpgm
LvO2:
s_barrier
buffer_load_dwordx4 v[56:59], v89, s[36:39], s49 offen sc1
buffer_load_dwordx2 v[60:61], v90, s[36:39], s49 offen sc1
s_add_u32 s49, s49, 346112
s_add_u32 m0, s50, 0
s_nop 0
D 85,0
D 85,1024
D 85,2048
D 85,3072
s_add_u32 m0, s50, 4096
s_nop 0
D 86,0
D 86,1024
D 86,2048
D 86,3072
s_waitcnt vmcnt(9)
v_lshlrev_b32 v0, 16, v56
v_and_b32 v1, 0xffff0000, v56
v_lshlrev_b32 v2, 16, v57
v_and_b32 v3, 0xffff0000, v57
v_lshlrev_b32 v4, 16, v58
v_and_b32 v5, 0xffff0000, v58
v_lshlrev_b32 v6, 16, v59
v_and_b32 v7, 0xffff0000, v59
s_waitcnt vmcnt(8)
v_lshlrev_b32 v8, 16, v60
v_and_b32 v9, 0xffff0000, v60
v_lshlrev_b32 v10, 16, v61
v_and_b32 v11, 0xffff0000, v61
buffer_load_dwordx4 v[56:59], v89, s[36:39], s49 offen sc1
buffer_load_dwordx2 v[60:61], v90, s[36:39], s49 offen sc1
s_add_u32 s49, s49, 346112
s_waitcnt vmcnt(2)
s_barrier
s_add_u32 m0, s50, 32768
R 24,0
D 85,0
R 28,1024
D 85,1024
R 32,2048
R 36,3072
D 85,2048
s_waitcnt lgkmcnt(2)
mav 0,0,24
R 40,4096
mav 4,4,24
mav 8,8,24
D 85,3072
mav 0,12,28
R 44,5120
mav 4,16,28
mav 8,20,28
s_add_u32 m0, s50, 36864
s_waitcnt lgkmcnt(2)
mav 0,24,32
R 48,6144
mav 4,28,32
D 86,0
mav 8,32,32
mav 0,36,36
R 52,7168
mav 4,40,36
mav 8,44,36
D 86,1024
s_waitcnt lgkmcnt(2)
mav 0,48,40
R 24,8192
mav 4,52,40
mav 8,56,40
mav 0,60,44
R 28,9216
mav 4,64,44
D 86,2048
mav 8,68,44
s_waitcnt lgkmcnt(2)
mav 0,72,48
R 32,10240
mav 4,76,48
mav 8,80,48
D 86,3072
mav 0,84,52
R 36,11264
mav 4,88,52
mav 8,92,52
s_waitcnt lgkmcnt(2)
mav 0,96,24
R 40,12288
mav 4,100,24
mav 8,104,24
mav 0,108,28
R 44,13312
mav 4,112,28
mav 8,116,28
s_waitcnt lgkmcnt(2)
mav 0,120,32
R 48,14336
mav 4,124,32
mav 8,128,32
mav 0,132,36
R 52,15360
mav 4,136,36
mav 8,140,36
s_waitcnt lgkmcnt(2)
mav 0,144,40
R 24,16384
mav 4,148,40
mav 8,152,40
mav 0,156,44
R 28,17408
mav 4,160,44
mav 8,164,44
s_waitcnt lgkmcnt(2)
mav 0,168,48
R 32,18432
mav 4,172,48
mav 8,176,48
mav 0,180,52
R 36,19456
mav 4,184,52
mav 8,188,52
s_waitcnt lgkmcnt(2)
mav 0,192,24
R 40,20480
mav 4,196,24
mav 8,200,24
s_waitcnt vmcnt(9)
v_lshlrev_b32 v12, 16, v56
mav 0,204,28
R 44,21504
v_and_b32 v13, 0xffff0000, v56
mav 4,208,28
v_lshlrev_b32 v14, 16, v57
v_and_b32 v15, 0xffff0000, v57
mav 8,212,28
v_lshlrev_b32 v16, 16, v58
s_waitcnt lgkmcnt(2)
mav 0,216,32
R 48,22528
v_and_b32 v17, 0xffff0000, v58
mav 4,220,32
v_lshlrev_b32 v18, 16, v59
v_and_b32 v19, 0xffff0000, v59
mav 8,224,32
s_waitcnt vmcnt(8)
v_lshlrev_b32 v20, 16, v60
mav 0,228,36
R 52,23552
v_and_b32 v21, 0xffff0000, v60
mav 4,232,36
v_lshlrev_b32 v22, 16, v61
v_and_b32 v23, 0xffff0000, v61
mav 8,236,36
s_waitcnt lgkmcnt(2)
mav 0,240,40
R 24,24576
mav 4,244,40
mav 8,248,40
mav 0,252,44
R 28,25600
mvv 4,128,44
buffer_load_dwordx4 v[56:59], v89, s[36:39], s49 offen sc1
mvv 8,132,44
s_waitcnt lgkmcnt(2)
mvv 0,136,48
R 32,26624
mvv 4,140,48
mvv 8,144,48
buffer_load_dwordx2 v[60:61], v90, s[36:39], s49 offen sc1
mvv 0,148,52
R 36,27648
mvv 4,152,52
mvv 8,156,52
s_waitcnt lgkmcnt(2)
mvv 0,160,24
R 40,28672
mvv 4,164,24
mvv 8,168,24
mvv 0,172,28
R 44,29696
mvv 4,176,28
mvv 8,180,28
s_waitcnt lgkmcnt(2)
mvv 0,184,32
R 48,30720
s_xor_b32 s50, s50, 0x10000
mvv 4,188,32
mvv 8,192,32
mvv 0,196,36
R 52,31744
mvv 4,200,36
s_add_u32 s49, s49, 346112
mvv 8,204,36
s_waitcnt vmcnt(2)
s_barrier
s_waitcnt lgkmcnt(2)
mvv 0,208,40
R 24,32768
s_add_u32 m0, s50, 0
mvv 4,212,40
D 85,0
mvv 8,216,40
mvv 0,220,44
R 28,33792
mvv 4,224,44
mvv 8,228,44
D 85,1024
s_waitcnt lgkmcnt(2)
mvv 0,232,48
R 32,34816
mvv 4,236,48
mvv 8,240,48
mvv 0,244,52
R 36,35840
mvv 4,248,52
D 85,2048
mvv 8,252,52
s_waitcnt lgkmcnt(2)
mav 12,0,24
R 40,36864
mav 16,4,24
v_cvt_pk_bf16_f32 v62, v0, v1
v_cvt_pk_bf16_f32 v63, v2, v3
mav 20,8,24
D 85,3072
v_cvt_pk_bf16_f32 v64, v4, v5
mav 12,12,28
R 44,37888
v_cvt_pk_bf16_f32 v65, v6, v7
mav 16,16,28
v_cvt_pk_bf16_f32 v66, v8, v9
v_cvt_pk_bf16_f32 v67, v10, v11
mav 20,20,28
s_add_u32 m0, s50, 4096
s_waitcnt lgkmcnt(2)
mav 12,24,32
R 48,38912
buffer_store_dwordx4 v[62:65], v83, s[36:39], s49 offen sc1
mav 16,28,32
v_cvt_pk_bf16_f32 v68, v8, v9
v_cvt_pk_bf16_f32 v69, v10, v11
mav 20,32,32
v_permlane16_swap_b32 v66, v68
v_permlane16_swap_b32 v67, v69
mav 12,36,36
R 52,39936
v_mul_f32 v73, v0, v0
mav 16,40,36
buffer_store_dwordx4 v[66:69], v84, s[36:39], s49 offen sc1
v_add_f32 v72, v1, v0
mav 20,44,36
v_fmac_f32 v73, v1, v1
s_waitcnt lgkmcnt(2)
mav 12,48,40
R 24,40960
v_add_f32 v72, v2, v72
mav 16,52,40
v_fmac_f32 v73, v2, v2
v_add_f32 v72, v3, v72
mav 20,56,40
D 86,0
v_fmac_f32 v73, v3, v3
mav 12,60,44
R 28,41984
v_add_f32 v72, v4, v72
mav 16,64,44
v_fmac_f32 v73, v4, v4
v_add_f32 v72, v5, v72
mav 20,68,44
v_fmac_f32 v73, v5, v5
s_waitcnt lgkmcnt(2)
mav 12,72,48
R 32,43008
D 86,1024
mav 16,76,48
v_add_f32 v72, v6, v72
v_fmac_f32 v73, v6, v6
mav 20,80,48
v_add_f32 v72, v7, v72
v_fmac_f32 v73, v7, v7
mav 12,84,52
R 36,44032
v_add_f32 v72, v8, v72
mav 16,88,52
D 86,2048
v_fmac_f32 v73, v8, v8
mav 20,92,52
v_add_f32 v72, v9, v72
s_waitcnt lgkmcnt(2)
mav 12,96,24
R 40,45056
v_fmac_f32 v73, v9, v9
mav 16,100,24
v_add_f32 v72, v10, v72
v_fmac_f32 v73, v10, v10
mav 20,104,24
D 86,3072
v_add_f32 v72, v11, v72
mav 12,108,28
R 44,46080
v_fmac_f32 v73, v11, v11
mav 16,112,28
mav 20,116,28
v_permlane32_swap_b32 v72, v73
s_waitcnt lgkmcnt(2)
mav 12,120,32
R 48,47104
v_add_f32 v72, v72, v73
mav 16,124,32
mav 20,128,32
mav 12,132,36
R 52,48128
mav 16,136,36
mav 20,140,36
s_waitcnt lgkmcnt(2)
mav 12,144,40
R 24,49152
mav 16,148,40
mav 20,152,40
mav 12,156,44
R 28,50176
mav 16,160,44
mav 20,164,44
s_waitcnt lgkmcnt(2)
mav 12,168,48
R 32,51200
mav 16,172,48
mav 20,176,48
mav 12,180,52
R 36,52224
mav 16,184,52
mav 20,188,52
s_waitcnt lgkmcnt(2)
mav 12,192,24
R 40,53248
mav 16,196,24
mav 20,200,24
s_waitcnt vmcnt(11)
v_lshlrev_b32 v0, 16, v56
mav 12,204,28
R 44,54272
v_and_b32 v1, 0xffff0000, v56
mav 16,208,28
v_lshlrev_b32 v2, 16, v57
v_and_b32 v3, 0xffff0000, v57
mav 20,212,28
v_lshlrev_b32 v4, 16, v58
s_waitcnt lgkmcnt(2)
mav 12,216,32
R 48,55296
v_and_b32 v5, 0xffff0000, v58
mav 16,220,32
v_lshlrev_b32 v6, 16, v59
v_and_b32 v7, 0xffff0000, v59
mav 20,224,32
s_waitcnt vmcnt(10)
v_lshlrev_b32 v8, 16, v60
mav 12,228,36
R 52,56320
v_and_b32 v9, 0xffff0000, v60
mav 16,232,36
v_lshlrev_b32 v10, 16, v61
v_and_b32 v11, 0xffff0000, v61
mav 20,236,36
s_waitcnt lgkmcnt(2)
mav 12,240,40
R 24,57344
mav 16,244,40
mav 20,248,40
mav 12,252,44
R 28,58368
mvv 16,128,44
buffer_load_dwordx4 v[56:59], v89, s[36:39], s49 offen sc1
v_add_u32 v70, 1, v70
mvv 20,132,44
s_waitcnt lgkmcnt(2)
mvv 12,136,48
R 32,59392
mvv 16,140,48
mvv 20,144,48
buffer_load_dwordx2 v[60:61], v90, s[36:39], s49 offen sc1
mvv 12,148,52
R 36,60416
mvv 16,152,52
s_waitcnt vmcnt(6)
buffer_store_dwordx2 v[70:71], v81, s[36:39], s52 offen sc1
mvv 20,156,52
s_waitcnt lgkmcnt(2)
mvv 12,160,24
R 40,61440
mvv 16,164,24
mvv 20,168,24
ds_write_b32 v91, v72 offset:0
mvv 12,172,28
R 44,62464
mvv 16,176,28
mvv 20,180,28
s_waitcnt lgkmcnt(3)
mvv 12,184,32
R 48,63488
mvv 16,188,32
mvv 20,192,32
mvv 12,196,36
R 52,64512
v_xor_b32 v80, 0x10000, v80
mvv 16,200,36
s_add_u32 s49, s49, 346112
mvv 20,204,36
s_waitcnt vmcnt(3) lgkmcnt(2)
s_barrier
mvv 12,208,40
R 24,0
s_add_u32 m0, s50, 32768
mvv 16,212,40
D 85,0
mvv 20,216,40
mvv 12,220,44
R 28,1024
mvv 16,224,44
mvv 20,228,44
D 85,1024
s_waitcnt lgkmcnt(2)
mvv 12,232,48
R 32,2048
mvv 16,236,48
mvv 20,240,48
mvv 12,244,52
R 36,3072
mvv 16,248,52
D 85,2048
mvv 20,252,52
s_mov_b32 s60, -30
.p2align 6
LO2_1:
s_waitcnt lgkmcnt(2)
mav 0,0,24
R 40,4096
mav 4,4,24
v_cvt_pk_bf16_f32 v62, v12, v13
v_cvt_pk_bf16_f32 v63, v14, v15
mav 8,8,24
D 85,3072
v_cvt_pk_bf16_f32 v64, v16, v17
mav 0,12,28
R 44,5120
v_cvt_pk_bf16_f32 v65, v18, v19
mav 4,16,28
v_cvt_pk_bf16_f32 v66, v20, v21
v_cvt_pk_bf16_f32 v67, v22, v23
mav 8,20,28
s_add_u32 m0, s50, 36864
s_waitcnt lgkmcnt(2)
mav 0,24,32
R 48,6144
buffer_store_dwordx4 v[62:65], v83, s[36:39], s49 offen sc1
mav 4,28,32
v_cvt_pk_bf16_f32 v68, v20, v21
v_cvt_pk_bf16_f32 v69, v22, v23
mav 8,32,32
v_permlane16_swap_b32 v66, v68
v_permlane16_swap_b32 v67, v69
mav 0,36,36
R 52,7168
v_mul_f32 v73, v12, v12
mav 4,40,36
buffer_store_dwordx4 v[66:69], v84, s[36:39], s49 offen sc1
v_add_f32 v72, v13, v12
mav 8,44,36
v_fmac_f32 v73, v13, v13
s_waitcnt lgkmcnt(2)
mav 0,48,40
R 24,8192
v_add_f32 v72, v14, v72
mav 4,52,40
v_fmac_f32 v73, v14, v14
v_add_f32 v72, v15, v72
mav 8,56,40
D 86,0
v_fmac_f32 v73, v15, v15
mav 0,60,44
R 28,9216
v_add_f32 v72, v16, v72
mav 4,64,44
v_fmac_f32 v73, v16, v16
v_add_f32 v72, v17, v72
mav 8,68,44
v_fmac_f32 v73, v17, v17
s_waitcnt lgkmcnt(2)
mav 0,72,48
R 32,10240
D 86,1024
mav 4,76,48
v_add_f32 v72, v18, v72
v_fmac_f32 v73, v18, v18
mav 8,80,48
v_add_f32 v72, v19, v72
v_fmac_f32 v73, v19, v19
mav 0,84,52
R 36,11264
v_add_f32 v72, v20, v72
mav 4,88,52
D 86,2048
v_fmac_f32 v73, v20, v20
mav 8,92,52
v_add_f32 v72, v21, v72
s_waitcnt lgkmcnt(2)
mav 0,96,24
R 40,12288
v_fmac_f32 v73, v21, v21
mav 4,100,24
v_add_f32 v72, v22, v72
v_fmac_f32 v73, v22, v22
mav 8,104,24
D 86,3072
v_add_f32 v72, v23, v72
mav 0,108,28
R 44,13312
v_fmac_f32 v73, v23, v23
mav 4,112,28
mav 8,116,28
v_permlane32_swap_b32 v72, v73
s_waitcnt lgkmcnt(2)
mav 0,120,32
R 48,14336
v_add_f32 v72, v72, v73
mav 4,124,32
mav 8,128,32
mav 0,132,36
R 52,15360
mav 4,136,36
mav 8,140,36
s_waitcnt lgkmcnt(2)
mav 0,144,40
R 24,16384
mav 4,148,40
mav 8,152,40
mav 0,156,44
R 28,17408
mav 4,160,44
mav 8,164,44
s_waitcnt lgkmcnt(2)
mav 0,168,48
R 32,18432
mav 4,172,48
mav 8,176,48
mav 0,180,52
R 36,19456
mav 4,184,52
mav 8,188,52
s_waitcnt lgkmcnt(2)
mav 0,192,24
R 40,20480
mav 4,196,24
mav 8,200,24
s_waitcnt vmcnt(12)
v_lshlrev_b32 v12, 16, v56
mav 0,204,28
R 44,21504
v_and_b32 v13, 0xffff0000, v56
mav 4,208,28
v_lshlrev_b32 v14, 16, v57
v_and_b32 v15, 0xffff0000, v57
mav 8,212,28
v_lshlrev_b32 v16, 16, v58
s_waitcnt lgkmcnt(2)
mav 0,216,32
R 48,22528
v_and_b32 v17, 0xffff0000, v58
mav 4,220,32
v_lshlrev_b32 v18, 16, v59
v_and_b32 v19, 0xffff0000, v59
mav 8,224,32
s_waitcnt vmcnt(11)
v_lshlrev_b32 v20, 16, v60
mav 0,228,36
R 52,23552
v_and_b32 v21, 0xffff0000, v60
mav 4,232,36
v_lshlrev_b32 v22, 16, v61
v_and_b32 v23, 0xffff0000, v61
mav 8,236,36
s_waitcnt lgkmcnt(2)
mav 0,240,40
R 24,24576
mav 4,244,40
mav 8,248,40
mav 0,252,44
R 28,25600
mvv 4,128,44
buffer_load_dwordx4 v[56:59], v89, s[36:39], s49 offen sc1
v_add_u32 v70, 1, v70
mvv 8,132,44
s_waitcnt lgkmcnt(2)
mvv 0,136,48
R 32,26624
mvv 4,140,48
mvv 8,144,48
buffer_load_dwordx2 v[60:61], v90, s[36:39], s49 offen sc1
mvv 0,148,52
R 36,27648
mvv 4,152,52
s_waitcnt vmcnt(6)
buffer_store_dwordx2 v[70:71], v81, s[36:39], s52 offen sc1
mvv 8,156,52
s_waitcnt lgkmcnt(2)
mvv 0,160,24
R 40,28672
mvv 4,164,24
mvv 8,168,24
ds_write_b32 v91, v72 offset:1024
mvv 0,172,28
R 44,29696
mvv 4,176,28
mvv 8,180,28
s_waitcnt lgkmcnt(3)
mvv 0,184,32
R 48,30720
s_xor_b32 s50, s50, 0x10000
mvv 4,188,32
mvv 8,192,32
mvv 0,196,36
R 52,31744
mvv 4,200,36
s_add_u32 s49, s49, 346112
mvv 8,204,36
s_waitcnt vmcnt(3) lgkmcnt(2)
s_barrier
mvv 0,208,40
R 24,32768
s_add_u32 m0, s50, 0
mvv 4,212,40
D 85,0
mvv 8,216,40
mvv 0,220,44
R 28,33792
mvv 4,224,44
mvv 8,228,44
D 85,1024
s_waitcnt lgkmcnt(2)
mvv 0,232,48
R 32,34816
mvv 4,236,48
mvv 8,240,48
mvv 0,244,52
R 36,35840
mvv 4,248,52
D 85,2048
mvv 8,252,52
s_waitcnt lgkmcnt(2)
mav 12,0,24
R 40,36864
mav 16,4,24
v_cvt_pk_bf16_f32 v62, v0, v1
v_cvt_pk_bf16_f32 v63, v2, v3
mav 20,8,24
D 85,3072
v_cvt_pk_bf16_f32 v64, v4, v5
mav 12,12,28
R 44,37888
v_cvt_pk_bf16_f32 v65, v6, v7
mav 16,16,28
v_cvt_pk_bf16_f32 v66, v8, v9
v_cvt_pk_bf16_f32 v67, v10, v11
mav 20,20,28
s_add_u32 m0, s50, 4096
s_waitcnt lgkmcnt(2)
mav 12,24,32
R 48,38912
buffer_store_dwordx4 v[62:65], v83, s[36:39], s49 offen sc1
mav 16,28,32
v_cvt_pk_bf16_f32 v68, v8, v9
v_cvt_pk_bf16_f32 v69, v10, v11
mav 20,32,32
v_permlane16_swap_b32 v66, v68
v_permlane16_swap_b32 v67, v69
mav 12,36,36
R 52,39936
v_mul_f32 v73, v0, v0
mav 16,40,36
buffer_store_dwordx4 v[66:69], v84, s[36:39], s49 offen sc1
v_add_f32 v72, v1, v0
mav 20,44,36
v_fmac_f32 v73, v1, v1
s_waitcnt lgkmcnt(2)
mav 12,48,40
R 24,40960
v_add_f32 v72, v2, v72
mav 16,52,40
v_fmac_f32 v73, v2, v2
v_add_f32 v72, v3, v72
mav 20,56,40
D 86,0
v_fmac_f32 v73, v3, v3
mav 12,60,44
R 28,41984
v_add_f32 v72, v4, v72
mav 16,64,44
v_fmac_f32 v73, v4, v4
v_add_f32 v72, v5, v72
mav 20,68,44
v_fmac_f32 v73, v5, v5
s_waitcnt lgkmcnt(2)
mav 12,72,48
R 32,43008
D 86,1024
mav 16,76,48
v_add_f32 v72, v6, v72
v_fmac_f32 v73, v6, v6
mav 20,80,48
v_add_f32 v72, v7, v72
v_fmac_f32 v73, v7, v7
mav 12,84,52
R 36,44032
v_add_f32 v72, v8, v72
mav 16,88,52
D 86,2048
v_fmac_f32 v73, v8, v8
mav 20,92,52
v_add_f32 v72, v9, v72
s_waitcnt lgkmcnt(2)
mav 12,96,24
R 40,45056
v_fmac_f32 v73, v9, v9
mav 16,100,24
v_add_f32 v72, v10, v72
v_fmac_f32 v73, v10, v10
mav 20,104,24
D 86,3072
v_add_f32 v72, v11, v72
mav 12,108,28
R 44,46080
v_fmac_f32 v73, v11, v11
mav 16,112,28
mav 20,116,28
v_permlane32_swap_b32 v72, v73
s_waitcnt lgkmcnt(2)
mav 12,120,32
R 48,47104
v_add_f32 v72, v72, v73
mav 16,124,32
mav 20,128,32
mav 12,132,36
R 52,48128
mav 16,136,36
mav 20,140,36
s_waitcnt lgkmcnt(2)
mav 12,144,40
R 24,49152
mav 16,148,40
mav 20,152,40
mav 12,156,44
R 28,50176
mav 16,160,44
mav 20,164,44
s_waitcnt lgkmcnt(2)
mav 12,168,48
R 32,51200
mav 16,172,48
mav 20,176,48
mav 12,180,52
R 36,52224
mav 16,184,52
mav 20,188,52
s_waitcnt lgkmcnt(2)
mav 12,192,24
R 40,53248
mav 16,196,24
mav 20,200,24
s_waitcnt vmcnt(12)
v_lshlrev_b32 v0, 16, v56
mav 12,204,28
R 44,54272
v_and_b32 v1, 0xffff0000, v56
mav 16,208,28
v_lshlrev_b32 v2, 16, v57
v_and_b32 v3, 0xffff0000, v57
mav 20,212,28
v_lshlrev_b32 v4, 16, v58
s_waitcnt lgkmcnt(2)
mav 12,216,32
R 48,55296
v_and_b32 v5, 0xffff0000, v58
mav 16,220,32
v_lshlrev_b32 v6, 16, v59
v_and_b32 v7, 0xffff0000, v59
mav 20,224,32
s_waitcnt vmcnt(11)
v_lshlrev_b32 v8, 16, v60
mav 12,228,36
R 52,56320
v_and_b32 v9, 0xffff0000, v60
mav 16,232,36
v_lshlrev_b32 v10, 16, v61
v_and_b32 v11, 0xffff0000, v61
mav 20,236,36
s_waitcnt lgkmcnt(2)
mav 12,240,40
R 24,57344
mav 16,244,40
mav 20,248,40
mav 12,252,44
R 28,58368
mvv 16,128,44
buffer_load_dwordx4 v[56:59], v89, s[36:39], s49 offen sc1
v_add_u32 v70, 1, v70
mvv 20,132,44
s_waitcnt lgkmcnt(2)
mvv 12,136,48
R 32,59392
mvv 16,140,48
mvv 20,144,48
buffer_load_dwordx2 v[60:61], v90, s[36:39], s49 offen sc1
mvv 12,148,52
R 36,60416
mvv 16,152,52
s_waitcnt vmcnt(6)
buffer_store_dwordx2 v[70:71], v81, s[36:39], s52 offen sc1
mvv 20,156,52
s_waitcnt lgkmcnt(2)
mvv 12,160,24
R 40,61440
mvv 16,164,24
mvv 20,168,24
ds_write_b32 v91, v72 offset:0
mvv 12,172,28
R 44,62464
mvv 16,176,28
mvv 20,180,28
s_waitcnt lgkmcnt(3)
mvv 12,184,32
R 48,63488
mvv 16,188,32
mvv 20,192,32
mvv 12,196,36
R 52,64512
v_xor_b32 v80, 0x10000, v80
mvv 16,200,36
s_add_u32 s49, s49, 346112
mvv 20,204,36
s_waitcnt vmcnt(3) lgkmcnt(2)
s_barrier
mvv 12,208,40
R 24,0
s_add_u32 m0, s50, 32768
mvv 16,212,40
D 85,0
mvv 20,216,40
mvv 12,220,44
R 28,1024
mvv 16,224,44
mvv 20,228,44
D 85,1024
s_waitcnt lgkmcnt(2)
mvv 12,232,48
R 32,2048
mvv 16,236,48
mvv 20,240,48
mvv 12,244,52
R 36,3072
mvv 16,248,52
D 85,2048
mvv 20,252,52
s_add_u32 s60, s60, 1
s_cbranch_scc0 LO2_1
s_waitcnt lgkmcnt(2)
mav 0,0,24
R 40,4096
mav 4,4,24
v_cvt_pk_bf16_f32 v62, v12, v13
v_cvt_pk_bf16_f32 v63, v14, v15
mav 8,8,24
D 85,3072
v_cvt_pk_bf16_f32 v64, v16, v17
mav 0,12,28
R 44,5120
v_cvt_pk_bf16_f32 v65, v18, v19
mav 4,16,28
v_cvt_pk_bf16_f32 v66, v20, v21
v_cvt_pk_bf16_f32 v67, v22, v23
mav 8,20,28
s_add_u32 m0, s50, 36864
s_waitcnt lgkmcnt(2)
mav 0,24,32
R 48,6144
buffer_store_dwordx4 v[62:65], v83, s[36:39], s49 offen sc1
mav 4,28,32
v_cvt_pk_bf16_f32 v68, v20, v21
v_cvt_pk_bf16_f32 v69, v22, v23
mav 8,32,32
v_permlane16_swap_b32 v66, v68
v_permlane16_swap_b32 v67, v69
mav 0,36,36
R 52,7168
v_mul_f32 v73, v12, v12
mav 4,40,36
buffer_store_dwordx4 v[66:69], v84, s[36:39], s49 offen sc1
v_add_f32 v72, v13, v12
mav 8,44,36
v_fmac_f32 v73, v13, v13
s_waitcnt lgkmcnt(2)
mav 0,48,40
R 24,8192
v_add_f32 v72, v14, v72
mav 4,52,40
v_fmac_f32 v73, v14, v14
v_add_f32 v72, v15, v72
mav 8,56,40
D 86,0
v_fmac_f32 v73, v15, v15
mav 0,60,44
R 28,9216
v_add_f32 v72, v16, v72
mav 4,64,44
v_fmac_f32 v73, v16, v16
v_add_f32 v72, v17, v72
mav 8,68,44
v_fmac_f32 v73, v17, v17
s_waitcnt lgkmcnt(2)
mav 0,72,48
R 32,10240
D 86,1024
mav 4,76,48
v_add_f32 v72, v18, v72
v_fmac_f32 v73, v18, v18
mav 8,80,48
v_add_f32 v72, v19, v72
v_fmac_f32 v73, v19, v19
mav 0,84,52
R 36,11264
v_add_f32 v72, v20, v72
mav 4,88,52
D 86,2048
v_fmac_f32 v73, v20, v20
mav 8,92,52
v_add_f32 v72, v21, v72
s_waitcnt lgkmcnt(2)
mav 0,96,24
R 40,12288
v_fmac_f32 v73, v21, v21
mav 4,100,24
v_add_f32 v72, v22, v72
v_fmac_f32 v73, v22, v22
mav 8,104,24
D 86,3072
v_add_f32 v72, v23, v72
mav 0,108,28
R 44,13312
v_fmac_f32 v73, v23, v23
mav 4,112,28
mav 8,116,28
v_permlane32_swap_b32 v72, v73
s_waitcnt lgkmcnt(2)
mav 0,120,32
R 48,14336
v_add_f32 v72, v72, v73
mav 4,124,32
mav 8,128,32
mav 0,132,36
R 52,15360
mav 4,136,36
mav 8,140,36
s_waitcnt lgkmcnt(2)
mav 0,144,40
R 24,16384
mav 4,148,40
mav 8,152,40
mav 0,156,44
R 28,17408
mav 4,160,44
mav 8,164,44
s_waitcnt lgkmcnt(2)
mav 0,168,48
R 32,18432
mav 4,172,48
mav 8,176,48
mav 0,180,52
R 36,19456
mav 4,184,52
mav 8,188,52
s_waitcnt lgkmcnt(2)
mav 0,192,24
R 40,20480
mav 4,196,24
mav 8,200,24
s_waitcnt vmcnt(12)
v_lshlrev_b32 v12, 16, v56
mav 0,204,28
R 44,21504
v_and_b32 v13, 0xffff0000, v56
mav 4,208,28
v_lshlrev_b32 v14, 16, v57
v_and_b32 v15, 0xffff0000, v57
mav 8,212,28
v_lshlrev_b32 v16, 16, v58
s_waitcnt lgkmcnt(2)
mav 0,216,32
R 48,22528
v_and_b32 v17, 0xffff0000, v58
mav 4,220,32
v_lshlrev_b32 v18, 16, v59
v_and_b32 v19, 0xffff0000, v59
mav 8,224,32
s_waitcnt vmcnt(11)
v_lshlrev_b32 v20, 16, v60
mav 0,228,36
R 52,23552
v_and_b32 v21, 0xffff0000, v60
mav 4,232,36
v_lshlrev_b32 v22, 16, v61
v_and_b32 v23, 0xffff0000, v61
mav 8,236,36
s_waitcnt lgkmcnt(2)
mav 0,240,40
R 24,24576
mav 4,244,40
mav 8,248,40
mav 0,252,44
R 28,25600
mvv 4,128,44
v_add_u32 v70, 1, v70
mvv 8,132,44
s_waitcnt lgkmcnt(2)
mvv 0,136,48
R 32,26624
mvv 4,140,48
mvv 8,144,48
mvv 0,148,52
R 36,27648
mvv 4,152,52
s_waitcnt vmcnt(4)
buffer_store_dwordx2 v[70:71], v81, s[36:39], s52 offen sc1
mvv 8,156,52
s_waitcnt lgkmcnt(2)
mvv 0,160,24
R 40,28672
mvv 4,164,24
mvv 8,168,24
ds_write_b32 v91, v72 offset:1024
mvv 0,172,28
R 44,29696
mvv 4,176,28
mvv 8,180,28
s_waitcnt lgkmcnt(3)
mvv 0,184,32
R 48,30720
mvv 4,188,32
mvv 8,192,32
mvv 0,196,36
R 52,31744
mvv 4,200,36
s_add_u32 s49, s49, 346112
mvv 8,204,36
s_waitcnt vmcnt(1) lgkmcnt(2)
s_barrier
mvv 0,208,40
R 24,32768
mvv 4,212,40
mvv 8,216,40
mvv 0,220,44
R 28,33792
mvv 4,224,44
mvv 8,228,44
s_waitcnt lgkmcnt(2)
mvv 0,232,48
R 32,34816
mvv 4,236,48
mvv 8,240,48
mvv 0,244,52
R 36,35840
mvv 4,248,52
mvv 8,252,52
s_waitcnt lgkmcnt(2)
mav 12,0,24
R 40,36864
mav 16,4,24
mav 20,8,24
v_cvt_pk_bf16_f32 v62, v0, v1
v_cvt_pk_bf16_f32 v63, v2, v3
mav 12,12,28
R 44,37888
v_cvt_pk_bf16_f32 v64, v4, v5
mav 16,16,28
v_cvt_pk_bf16_f32 v65, v6, v7
buffer_store_dwordx4 v[62:65], v83, s[36:39], s49 offen sc1
mav 20,20,28
v_cvt_pk_bf16_f32 v66, v8, v9
s_waitcnt lgkmcnt(2)
mav 12,24,32
R 48,38912
v_cvt_pk_bf16_f32 v67, v10, v11
mav 16,28,32
v_cvt_pk_bf16_f32 v68, v8, v9
v_cvt_pk_bf16_f32 v69, v10, v11
mav 20,32,32
v_permlane16_swap_b32 v66, v68
v_permlane16_swap_b32 v67, v69
mav 12,36,36
R 52,39936
buffer_store_dwordx4 v[66:69], v84, s[36:39], s49 offen sc1
mav 16,40,36
v_mul_f32 v73, v0, v0
v_add_f32 v72, v1, v0
mav 20,44,36
v_fmac_f32 v73, v1, v1
s_waitcnt lgkmcnt(2)
mav 12,48,40
R 24,40960
v_add_f32 v72, v2, v72
mav 16,52,40
v_fmac_f32 v73, v2, v2
v_add_f32 v72, v3, v72
mav 20,56,40
v_fmac_f32 v73, v3, v3
v_add_f32 v72, v4, v72
mav 12,60,44
R 28,41984
v_fmac_f32 v73, v4, v4
mav 16,64,44
v_add_f32 v72, v5, v72
v_fmac_f32 v73, v5, v5
mav 20,68,44
v_add_f32 v72, v6, v72
s_waitcnt lgkmcnt(2)
mav 12,72,48
R 32,43008
v_fmac_f32 v73, v6, v6
mav 16,76,48
v_add_f32 v72, v7, v72
v_fmac_f32 v73, v7, v7
mav 20,80,48
v_add_f32 v72, v8, v72
v_fmac_f32 v73, v8, v8
mav 12,84,52
R 36,44032
v_add_f32 v72, v9, v72
mav 16,88,52
v_fmac_f32 v73, v9, v9
v_add_f32 v72, v10, v72
mav 20,92,52
v_fmac_f32 v73, v10, v10
s_waitcnt lgkmcnt(2)
mav 12,96,24
R 40,45056
v_add_f32 v72, v11, v72
mav 16,100,24
v_fmac_f32 v73, v11, v11
mav 20,104,24
mav 12,108,28
R 44,46080
v_permlane32_swap_b32 v72, v73
mav 16,112,28
v_add_f32 v72, v72, v73
mav 20,116,28
s_waitcnt lgkmcnt(2)
mav 12,120,32
R 48,47104
mav 16,124,32
mav 20,128,32
mav 12,132,36
R 52,48128
mav 16,136,36
mav 20,140,36
s_waitcnt lgkmcnt(2)
mav 12,144,40
R 24,49152
mav 16,148,40
mav 20,152,40
mav 12,156,44
R 28,50176
mav 16,160,44
mav 20,164,44
s_waitcnt lgkmcnt(2)
mav 12,168,48
R 32,51200
mav 16,172,48
mav 20,176,48
mav 12,180,52
R 36,52224
mav 16,184,52
mav 20,188,52
s_waitcnt lgkmcnt(2)
mav 12,192,24
R 40,53248
mav 16,196,24
mav 20,200,24
mav 12,204,28
R 44,54272
mav 16,208,28
mav 20,212,28
s_waitcnt lgkmcnt(2)
mav 12,216,32
R 48,55296
mav 16,220,32
mav 20,224,32
mav 12,228,36
R 52,56320
mav 16,232,36
mav 20,236,36
s_waitcnt lgkmcnt(2)
mav 12,240,40
R 24,57344
mav 16,244,40
mav 20,248,40
mav 12,252,44
R 28,58368
mvv 16,128,44
v_add_u32 v70, 1, v70
mvv 20,132,44
s_waitcnt lgkmcnt(2)
mvv 12,136,48
R 32,59392
mvv 16,140,48
mvv 20,144,48
mvv 12,148,52
R 36,60416
mvv 16,152,52
s_waitcnt vmcnt(0)
buffer_store_dwordx2 v[70:71], v81, s[36:39], s52 offen sc1
mvv 20,156,52
s_waitcnt lgkmcnt(2)
mvv 12,160,24
R 40,61440
mvv 16,164,24
mvv 20,168,24
ds_write_b32 v91, v72 offset:0
mvv 12,172,28
R 44,62464
mvv 16,176,28
mvv 20,180,28
s_waitcnt lgkmcnt(3)
mvv 12,184,32
R 48,63488
mvv 16,188,32
mvv 20,192,32
mvv 12,196,36
R 52,64512
mvv 16,200,36
s_add_u32 s49, s49, 346112
mvv 20,204,36
s_waitcnt lgkmcnt(2)
s_barrier
mvv 12,208,40
mvv 16,212,40
mvv 20,216,40
mvv 12,220,44
mvv 16,224,44
mvv 20,228,44
s_waitcnt lgkmcnt(0)
mvv 12,232,48
mvv 16,236,48
mvv 20,240,48
mvv 12,244,52
mvv 16,248,52
mvv 20,252,52
s_nop 5
v_cvt_pk_bf16_f32 v62, v12, v13
v_cvt_pk_bf16_f32 v63, v14, v15
v_cvt_pk_bf16_f32 v64, v16, v17
v_cvt_pk_bf16_f32 v65, v18, v19
buffer_store_dwordx4 v[62:65], v83, s[36:39], s49 offen sc1
v_cvt_pk_bf16_f32 v66, v20, v21
v_cvt_pk_bf16_f32 v67, v22, v23
v_cvt_pk_bf16_f32 v68, v20, v21
v_cvt_pk_bf16_f32 v69, v22, v23
s_nop 0
v_permlane16_swap_b32 v66, v68
v_permlane16_swap_b32 v67, v69
buffer_store_dwordx4 v[66:69], v84, s[36:39], s49 offen sc1
v_mul_f32 v73, v12, v12
v_add_f32 v72, v13, v12
v_fmac_f32 v73, v13, v13
v_add_f32 v72, v14, v72
v_fmac_f32 v73, v14, v14
v_add_f32 v72, v15, v72
v_fmac_f32 v73, v15, v15
v_add_f32 v72, v16, v72
v_fmac_f32 v73, v16, v16
v_add_f32 v72, v17, v72
v_fmac_f32 v73, v17, v17
v_add_f32 v72, v18, v72
v_fmac_f32 v73, v18, v18
v_add_f32 v72, v19, v72
v_fmac_f32 v73, v19, v19
v_add_f32 v72, v20, v72
v_fmac_f32 v73, v20, v20
v_add_f32 v72, v21, v72
v_fmac_f32 v73, v21, v21
v_add_f32 v72, v22, v72
v_fmac_f32 v73, v22, v22
v_add_f32 v72, v23, v72
v_fmac_f32 v73, v23, v23
s_nop 1
v_permlane32_swap_b32 v72, v73
v_add_f32 v72, v72, v73
v_add_u32 v70, 37, v70
s_waitcnt vmcnt(0)
buffer_store_dwordx2 v[70:71], v81, s[36:39], s52 offen sc1
ds_write_b32 v91, v72 offset:1024
s_add_u32 s49, s49, 346112
s_waitcnt lgkmcnt(0)
s_barrier
s_endpgm
Lr3:
s_cmp_eq_u32 s60, 3
s_cbranch_scc0 Lr4
buffer_load_dword v77, off, s[36:39], 0 sc1
s_waitcnt vmcnt(0)
s_barrier
v_mov_b32 v76, 0x100
v_add_u32 v77, 0x9e3779b9, v77
buffer_store_dwordx2 v[76:77], v81, s[36:39], s52 offen sc1
v_add_u32 v88, v88, v76
s_sleep 32
E 0,65536
E 1,69632
E 2,73728
s_add_u32 s51, s51, 229376
E 0,77824
E 1,81920
E 2,86016
s_add_u32 s51, s51, 229376
E 0,90112
E 1,94208
E 2,98304
s_add_u32 s51, s51, 229376
E 0,102400
E 1,106496
E 2,110592
s_add_u32 s51, s51, 229376
v_mov_b32 v24, s64
v_mov_b32 v25, s64
v_mov_b32 v26, s64
v_mov_b32 v27, s64
s_waitcnt vmcnt(6)
s_barrier
v_add_u32 v4, 65536, v4
v_add_u32 v5, 65536, v5
v_add_u32 v6, 65536, v6
Ta 0,4,0
Ta 4,5,0
Ta 8,6,0
Ta 12,4,12288
Ta 16,5,12288
Ta 20,6,12288
E 0,114688
E 1,118784
E 2,122880
s_add_u32 s51, s51, 229376
E 0,126976
E 1,131072
E 2,135168
s_add_u32 s51, s51, 229376
E 0,139264
E 1,143360
E 2,147456
s_add_u32 s51, s51, 229376
E 0,151552
E 1,155648
E 2,159744
s_add_u32 s51, s51, 229376
s_cmp_ge_u32 s51, s57
s_cselect_b32 s60, 7340032, 0
s_sub_u32 s51, s51, s60
s_waitcnt lgkmcnt(0)
s_waitcnt vmcnt(12)
s_barrier
E 0,65536
Ta 24,4,24576
v_mfma_f32_16x16x32_bf16 v[56:59], a[0:3], v[24:27], 0
Ta 28,5,24576
E 1,69632
v_mfma_f32_16x16x32_bf16 v[60:63], a[4:7], v[24:27], 0
Ta 32,6,24576
Ta 36,4,36864
E 2,73728
s_add_u32 s51, s51, 229376
v_mfma_f32_16x16x32_bf16 v[64:67], a[8:11], v[24:27], 0
Ta 40,5,36864
E 0,77824
Ta 44,6,36864
mav 56,12,24
E 1,81920
mav 60,16,24
E 2,86016
s_add_u32 s51, s51, 229376
mav 64,20,24
s_waitcnt lgkmcnt(0)
s_waitcnt vmcnt(12)
s_barrier
v_add_u32 v4, 49152, v4
v_add_u32 v5, 49152, v5
v_add_u32 v6, 49152, v6
E 0,90112
Ta 48,4,0
mav 56,24,24
Ta 52,5,0
E 1,94208
mav 60,28,24
Ta 56,6,0
Ta 60,4,12288
E 2,98304
s_add_u32 s51, s51, 229376
mav 64,32,24
Ta 64,5,12288
E 0,102400
Ta 68,6,12288
mav 56,36,24
E 1,106496
mav 60,40,24
E 2,110592
s_add_u32 s51, s51, 229376
mav 64,44,24
s_waitcnt lgkmcnt(0)
s_waitcnt vmcnt(12)
s_barrier
E 0,114688
Ta 72,4,24576
mav 56,48,24
Ta 76,5,24576
E 1,118784
mav 60,52,24
Ta 80,6,24576
Ta 84,4,36864
E 2,122880
s_add_u32 s51, s51, 229376
mav 64,56,24
Ta 88,5,36864
E 0,126976
Ta 92,6,36864
mav 56,60,24
E 1,131072
mav 60,64,24
E 2,135168
s_add_u32 s51, s51, 229376
mav 64,68,24
s_waitcnt lgkmcnt(0)
s_waitcnt vmcnt(12)
s_barrier
v_add_u32 v4, -49152, v4
v_add_u32 v5, -49152, v5
v_add_u32 v6, -49152, v6
E 0,139264
Ta 96,4,0
mav 56,72,24
Ta 100,5,0
E 1,143360
mav 60,76,24
Ta 104,6,0
Ta 108,4,12288
E 2,147456
s_add_u32 s51, s51, 229376
mav 64,80,24
Ta 112,5,12288
E 0,151552
Ta 116,6,12288
mav 56,84,24
E 1,155648
mav 60,88,24
E 2,159744
s_add_u32 s51, s51, 229376
s_cmp_ge_u32 s51, s57
s_cselect_b32 s60, 7340032, 0
s_sub_u32 s51, s51, s60
mav 64,92,24
s_waitcnt lgkmcnt(0)
s_waitcnt vmcnt(12)
s_barrier
E 0,65536
Ta 120,4,24576
mav 56,96,24
Ta 124,5,24576
E 1,69632
mav 60,100,24
Ta 128,6,24576
Ta 132,4,36864
E 2,73728
s_add_u32 s51, s51, 229376
mav 64,104,24
Ta 136,5,36864
E 0,77824
Ta 140,6,36864
mav 56,108,24
E 1,81920
mav 60,112,24
E 2,86016
s_add_u32 s51, s51, 229376
mav 64,116,24
s_waitcnt lgkmcnt(0)
s_waitcnt vmcnt(12)
s_barrier
v_add_u32 v4, 49152, v4
v_add_u32 v5, 49152, v5
v_add_u32 v6, 49152, v6
E 0,90112
Ta 144,4,0
mav 56,120,24
Ta 148,5,0
E 1,94208
mav 60,124,24
Ta 152,6,0
Ta 156,4,12288
E 2,98304
s_add_u32 s51, s51, 229376
mav 64,128,24
Ta 160,5,12288
E 0,102400
Ta 164,6,12288
mav 56,132,24
E 1,106496
mav 60,136,24
E 2,110592
s_add_u32 s51, s51, 229376
mav 64,140,24
s_waitcnt lgkmcnt(0)
s_waitcnt vmcnt(12)
s_barrier
E 0,114688
Ta 168,4,24576
mav 56,144,24
Ta 172,5,24576
E 1,118784
mav 60,148,24
Ta 176,6,24576
Ta 180,4,36864
E 2,122880
s_add_u32 s51, s51, 229376
mav 64,152,24
Ta 184,5,36864
E 0,126976
Ta 188,6,36864
mav 56,156,24
E 1,131072
mav 60,160,24
E 2,135168
s_add_u32 s51, s51, 229376
mav 64,164,24
s_waitcnt lgkmcnt(0)
s_waitcnt vmcnt(12)
s_barrier
v_add_u32 v4, -49152, v4
v_add_u32 v5, -49152, v5
v_add_u32 v6, -49152, v6
E 0,139264
Ta 192,4,0
mav 56,168,24
Ta 196,5,0
E 1,143360
mav 60,172,24
Ta 200,6,0
Ta 204,4,12288
E 2,147456
s_add_u32 s51, s51, 229376
mav 64,176,24
Ta 208,5,12288
E 0,151552
Ta 212,6,12288
mav 56,180,24
E 1,155648
mav 60,184,24
E 2,159744
s_add_u32 s51, s51, 229376
s_cmp_ge_u32 s51, s57
s_cselect_b32 s60, 7340032, 0
s_sub_u32 s51, s51, s60
mav 64,188,24
s_waitcnt lgkmcnt(0)
s_waitcnt vmcnt(12)
s_barrier
E 0,65536
Ta 216,4,24576
mav 56,192,24
Ta 220,5,24576
E 1,69632
mav 60,196,24
Ta 224,6,24576
Ta 228,4,36864
E 2,73728
s_add_u32 s51, s51, 229376
mav 64,200,24
Ta 232,5,36864
E 0,77824
Ta 236,6,36864
mav 56,204,24
E 1,81920
mav 60,208,24
E 2,86016
s_add_u32 s51, s51, 229376
mav 64,212,24
s_waitcnt lgkmcnt(0)
s_waitcnt vmcnt(12)
s_barrier
v_add_u32 v4, 49152, v4
v_add_u32 v5, 49152, v5
v_add_u32 v6, 49152, v6
E 0,90112
Ta 240,4,0
mav 56,216,24
Ta 244,5,0
E 1,94208
mav 60,220,24
Ta 248,6,0
Ta 252,4,12288
E 2,98304
s_add_u32 s51, s51, 229376
mav 64,224,24
Tv 128,5,12288
E 0,102400
Tv 132,6,12288
mav 56,228,24
E 1,106496
mav 60,232,24
E 2,110592
s_add_u32 s51, s51, 229376
mav 64,236,24
s_waitcnt lgkmcnt(0)
s_waitcnt vmcnt(12)
s_barrier
E 0,114688
Tv 136,4,24576
mav 56,240,24
Tv 140,5,24576
E 1,118784
mav 60,244,24
Tv 144,6,24576
Tv 148,4,36864
E 2,122880
s_add_u32 s51, s51, 229376
mav 64,248,24
Tv 152,5,36864
E 0,126976
Tv 156,6,36864
mav 56,252,24
E 1,131072
mvv 60,128,24
E 2,135168
s_add_u32 s51, s51, 229376
mvv 64,132,24
s_waitcnt lgkmcnt(0)
s_waitcnt vmcnt(12)
s_barrier
v_add_u32 v4, -49152, v4
v_add_u32 v5, -49152, v5
v_add_u32 v6, -49152, v6
E 0,139264
Tv 160,4,0
mvv 56,136,24
Tv 164,5,0
E 1,143360
mvv 60,140,24
Tv 168,6,0
Tv 172,4,12288
E 2,147456
s_add_u32 s51, s51, 229376
mvv 64,144,24
Tv 176,5,12288
E 0,151552
Tv 180,6,12288
mvv 56,148,24
E 1,155648
mvv 60,152,24
E 2,159744
s_add_u32 s51, s51, 229376
s_cmp_ge_u32 s51, s57
s_cselect_b32 s60, 7340032, 0
s_sub_u32 s51, s51, s60
mvv 64,156,24
s_waitcnt lgkmcnt(0)
s_waitcnt vmcnt(12)
s_barrier
Tv 184,4,24576
mvv 56,160,24
Tv 188,5,24576
mvv 60,164,24
Tv 192,6,24576
Tv 196,4,36864
mvv 64,168,24
Tv 200,5,36864
Tv 204,6,36864
mvv 56,172,24
mvv 60,176,24
mvv 64,180,24
s_waitcnt lgkmcnt(0)
s_waitcnt vmcnt(6)
s_barrier
v_add_u32 v4, 49152, v4
v_add_u32 v5, 49152, v5
v_add_u32 v6, 49152, v6
Tv 208,4,0
mvv 56,184,24
Tv 212,5,0
mvv 60,188,24
Tv 216,6,0
Tv 220,4,12288
mvv 64,192,24
Tv 224,5,12288
Tv 228,6,12288
mvv 56,196,24
mvv 60,200,24
mvv 64,204,24
s_waitcnt lgkmcnt(0)
s_waitcnt vmcnt(0)
s_barrier
Tv 232,4,24576
mvv 56,208,24
Tv 236,5,24576
mvv 60,212,24
Tv 240,6,24576
Tv 244,4,36864
mvv 64,216,24
Tv 248,5,36864
Tv 252,6,36864
mvv 56,220,24
mvv 60,224,24
mvv 64,228,24
s_waitcnt lgkmcnt(0)
mvv 56,232,24
mvv 60,236,24
mvv 64,240,24
mvv 56,244,24
mvv 60,248,24
mvv 64,252,24
s_waitcnt vmcnt(0)
s_barrier
s_nop 7
s_and_b32 s60, s48, 3
s_cmp_eq_u32 s60, 1
s_cbranch_scc1 LvF1
s_cmp_eq_u32 s60, 2
s_cbranch_scc1 LvF2
LvF0:
buffer_load_dwordx2 v[106:107], v87, s[36:39], 0 offen sc1
s_waitcnt vmcnt(0)
ds_write_b32 v82, v106
v_sub_u32 v106, v106, v88
v_xor_b32 v107, v107, v77
v_cmp_lt_u64 vcc, s[62:63], v[106:107]
s_waitcnt lgkmcnt(0)
s_cbranch_vccnz CF0_1
BF0_1:
s_add_u32 s49, s49, 346112
s_barrier
s_add_u32 m0, s50, 0
s_nop 0
D 85,0
D 85,1024
D 85,2048
D 85,3072
s_add_u32 m0, s50, 4096
s_nop 0
D 86,0
D 86,1024
D 86,2048
D 86,3072
s_mov_b32 m0, 142336
s_nop 0
buffer_load_dword v87, s[36:39], 0 offen sc1 lds
v_add_u32 v88, 1, v88
ds_read_b32 v106, v82 offset:0
s_waitcnt lgkmcnt(0)
v_cmp_gt_i32 vcc, v88, v106
s_waitcnt vmcnt(1)
s_cbranch_vccnz CF0_2
BF0_2:
s_add_u32 s49, s49, 346112
s_barrier
R 24,0
s_add_u32 m0, s50, 32768
R 28,1024
D 85,0
R 32,2048
D 85,1024
R 36,3072
s_waitcnt lgkmcnt(2)
v_mfma_f32_16x16x32_bf16 v[0:3], a[0:3], v[24:27], 0
R 40,4096
D 85,2048
v_mfma_f32_16x16x32_bf16 v[4:7], a[4:7], v[24:27], 0
v_mfma_f32_16x16x32_bf16 v[8:11], a[8:11], v[24:27], 0
mav 0,12,28
R 44,5120
mav 4,16,28
D 85,3072
mav 8,20,28
s_waitcnt lgkmcnt(2)
mav 0,24,32
R 48,6144
mav 4,28,32
s_add_u32 m0, s50, 36864
mav 8,32,32
mav 0,36,36
R 52,7168
D 86,0
mav 4,40,36
mav 8,44,36
s_waitcnt lgkmcnt(2)
mav 0,48,40
R 24,8192
mav 4,52,40
D 86,1024
mav 8,56,40
mav 0,60,44
R 28,9216
mav 4,64,44
mav 8,68,44
s_waitcnt lgkmcnt(2)
mav 0,72,48
R 32,10240
D 86,2048
mav 4,76,48
mav 8,80,48
mav 0,84,52
R 36,11264
mav 4,88,52
D 86,3072
mav 8,92,52
s_waitcnt lgkmcnt(2)
mav 0,96,24
R 40,12288
mav 4,100,24
mav 8,104,24
mav 0,108,28
R 44,13312
mav 4,112,28
mav 8,116,28
s_waitcnt lgkmcnt(2)
mav 0,120,32
R 48,14336
mav 4,124,32
mav 8,128,32
mav 0,132,36
R 52,15360
mav 4,136,36
s_mov_b32 m0, 142336
mav 8,140,36
s_waitcnt lgkmcnt(2)
mav 0,144,40
R 24,16384
buffer_load_dword v87, s[36:39], 0 offen sc1 lds
mav 4,148,40
mav 8,152,40
mav 0,156,44
R 28,17408
mav 4,160,44
v_add_u32 v88, 1, v88
mav 8,164,44
s_waitcnt lgkmcnt(2)
mav 0,168,48
R 32,18432
mav 4,172,48
mav 8,176,48
mav 0,180,52
R 36,19456
mav 4,184,52
mav 8,188,52
s_waitcnt lgkmcnt(2)
mav 0,192,24
R 40,20480
mav 4,196,24
mav 8,200,24
mav 0,204,28
R 44,21504
mav 4,208,28
mav 8,212,28
s_waitcnt lgkmcnt(2)
mav 0,216,32
R 48,22528
mav 4,220,32
mav 8,224,32
mav 0,228,36
R 52,23552
mav 4,232,36
mav 8,236,36
s_waitcnt lgkmcnt(2)
mav 0,240,40
R 24,24576
mav 4,244,40
mav 8,248,40
mav 0,252,44
R 28,25600
mvv 4,128,44
mvv 8,132,44
s_waitcnt lgkmcnt(2)
mvv 0,136,48
R 32,26624
mvv 4,140,48
mvv 8,144,48
mvv 0,148,52
R 36,27648
mvv 4,152,52
mvv 8,156,52
s_waitcnt lgkmcnt(2)
mvv 0,160,24
R 40,28672
mvv 4,164,24
ds_read_b32 v106, v82 offset:0
mvv 8,168,24
mvv 0,172,28
R 44,29696
mvv 4,176,28
mvv 8,180,28
s_waitcnt lgkmcnt(3)
mvv 0,184,32
R 48,30720
s_xor_b32 s50, s50, 0x10000
mvv 4,188,32
mvv 8,192,32
mvv 0,196,36
R 52,31744
mvv 4,200,36
s_waitcnt lgkmcnt(2)
v_cmp_gt_i32 vcc, v88, v106
mvv 8,204,36
s_waitcnt vmcnt(1)
s_cbranch_vccnz CF0_3
BF0_3:
mvv 0,208,40
s_barrier
R 24,32768
mvv 4,212,40
s_add_u32 s49, s49, 346112
s_add_u32 m0, s50, 0
mvv 8,216,40
ds_read_b64 v[78:79], v89 offset:0
mvv 0,220,44
R 28,33792
D 85,0
mvv 4,224,44
mvv 8,228,44
s_waitcnt lgkmcnt(3)
mvv 0,232,48
R 32,34816
mvv 4,236,48
D 85,1024
mvv 8,240,48
mvv 0,244,52
R 36,35840
mvv 4,248,52
mvv 8,252,52
s_waitcnt lgkmcnt(2)
)"
R"(
v_mfma_f32_16x16x32_bf16 v[12:15], a[0:3], v[24:27], 0
R 40,36864
D 85,2048
v_mfma_f32_16x16x32_bf16 v[16:19], a[4:7], v[24:27], 0
v_fma_f32 v104, v79, v56, v92
v_fma_f32 v0, v0, v78, v104
v_mfma_f32_16x16x32_bf16 v[20:23], a[8:11], v[24:27], 0
v_fma_f32 v105, v79, v57, v93
v_fma_f32 v1, v1, v78, v105
mav 12,12,28
R 44,37888
v_mul_f32_e64 v0, v0, |v0|
mav 16,16,28
D 85,3072
v_mul_f32_e64 v1, v1, |v1|
mav 20,20,28
v_cvt_pk_bf16_f32 v0, v0, v1
s_waitcnt lgkmcnt(2)
mav 12,24,32
R 48,38912
v_pk_max_i16 v68, v0, 0
mav 16,28,32
s_add_u32 m0, s50, 4096
v_fma_f32 v104, v79, v58, v94
mav 20,32,32
v_fma_f32 v2, v2, v78, v104
v_fma_f32 v105, v79, v59, v95
mav 12,36,36
R 52,39936
D 86,0
mav 16,40,36
v_fma_f32 v3, v3, v78, v105
v_mul_f32_e64 v2, v2, |v2|
mav 20,44,36
v_mul_f32_e64 v3, v3, |v3|
s_waitcnt lgkmcnt(2)
mav 12,48,40
R 24,40960
v_cvt_pk_bf16_f32 v2, v2, v3
mav 16,52,40
D 86,1024
v_pk_max_i16 v69, v2, 0
mav 20,56,40
v_fma_f32 v104, v79, v60, v96
v_fma_f32 v4, v4, v78, v104
mav 12,60,44
R 28,41984
v_fma_f32 v105, v79, v61, v97
mav 16,64,44
v_fma_f32 v5, v5, v78, v105
v_mul_f32_e64 v4, v4, |v4|
mav 20,68,44
v_mul_f32_e64 v5, v5, |v5|
s_waitcnt lgkmcnt(2)
mav 12,72,48
R 32,43008
D 86,2048
mav 16,76,48
v_cvt_pk_bf16_f32 v4, v4, v5
v_pk_max_i16 v70, v4, 0
mav 20,80,48
v_fma_f32 v104, v79, v62, v98
v_fma_f32 v6, v6, v78, v104
mav 12,84,52
R 36,44032
v_fma_f32 v105, v79, v63, v99
mav 16,88,52
D 86,3072
v_fma_f32 v7, v7, v78, v105
mav 20,92,52
v_mul_f32_e64 v6, v6, |v6|
s_waitcnt lgkmcnt(2)
mav 12,96,24
R 40,45056
v_mul_f32_e64 v7, v7, |v7|
mav 16,100,24
v_cvt_pk_bf16_f32 v6, v6, v7
v_pk_max_i16 v71, v6, 0
mav 20,104,24
buffer_store_dwordx4 v[68:71], v83, s[36:39], s49 offen sc1
v_fma_f32 v104, v79, v64, v100
mav 12,108,28
R 44,46080
v_fma_f32 v8, v8, v78, v104
mav 16,112,28
v_fma_f32 v105, v79, v65, v101
v_fma_f32 v9, v9, v78, v105
mav 20,116,28
v_mul_f32_e64 v8, v8, |v8|
s_waitcnt lgkmcnt(2)
mav 12,120,32
R 48,47104
v_mul_f32_e64 v9, v9, |v9|
mav 16,124,32
v_cvt_pk_bf16_f32 v8, v8, v9
v_pk_max_i16 v72, v8, 0
mav 20,128,32
v_pk_max_i16 v74, v8, 0
v_fma_f32 v104, v79, v66, v102
mav 12,132,36
R 52,48128
v_fma_f32 v10, v10, v78, v104
mav 16,136,36
s_mov_b32 m0, 142336
v_fma_f32 v105, v79, v67, v103
mav 20,140,36
v_fma_f32 v11, v11, v78, v105
s_waitcnt lgkmcnt(2)
mav 12,144,40
R 24,49152
buffer_load_dword v87, s[36:39], 0 offen sc1 lds
mav 16,148,40
v_mul_f32_e64 v10, v10, |v10|
v_mul_f32_e64 v11, v11, |v11|
mav 20,152,40
v_cvt_pk_bf16_f32 v10, v10, v11
v_pk_max_i16 v73, v10, 0
mav 12,156,44
R 28,50176
v_pk_max_i16 v75, v10, 0
mav 16,160,44
v_permlane16_swap_b32 v72, v74
v_permlane16_swap_b32 v73, v75
mav 20,164,44
buffer_store_dwordx4 v[72:75], v84, s[36:39], s49 offen sc1
s_waitcnt lgkmcnt(2)
mav 12,168,48
R 32,51200
v_add_u32 v88, 1, v88
mav 16,172,48
mav 20,176,48
mav 12,180,52
R 36,52224
mav 16,184,52
mav 20,188,52
s_waitcnt lgkmcnt(2)
mav 12,192,24
R 40,53248
mav 16,196,24
mav 20,200,24
mav 12,204,28
R 44,54272
mav 16,208,28
mav 20,212,28
s_waitcnt lgkmcnt(2)
mav 12,216,32
R 48,55296
mav 16,220,32
mav 20,224,32
mav 12,228,36
R 52,56320
mav 16,232,36
mav 20,236,36
s_waitcnt lgkmcnt(2)
mav 12,240,40
R 24,57344
mav 16,244,40
mav 20,248,40
mav 12,252,44
R 28,58368
mvv 16,128,44
mvv 20,132,44
s_waitcnt lgkmcnt(2)
mvv 12,136,48
R 32,59392
mvv 16,140,48
mvv 20,144,48
mvv 12,148,52
R 36,60416
mvv 16,152,52
mvv 20,156,52
s_waitcnt lgkmcnt(2)
mvv 12,160,24
R 40,61440
mvv 16,164,24
ds_read_b32 v106, v82 offset:0
mvv 20,168,24
mvv 12,172,28
R 44,62464
mvv 16,176,28
mvv 20,180,28
s_waitcnt lgkmcnt(3)
mvv 12,184,32
R 48,63488
mvv 16,188,32
mvv 20,192,32
mvv 12,196,36
R 52,64512
v_xor_b32 v80, 0x10000, v80
mvv 16,200,36
s_waitcnt lgkmcnt(2)
v_cmp_gt_i32 vcc, v88, v106
mvv 20,204,36
s_waitcnt vmcnt(3)
s_cbranch_vccnz CF0_4
BF0_4:
mvv 12,208,40
s_barrier
R 24,0
mvv 16,212,40
s_add_u32 s49, s49, 346112
s_add_u32 m0, s50, 32768
mvv 20,216,40
ds_read_b64 v[78:79], v89 offset:128
mvv 12,220,44
R 28,1024
D 85,0
mvv 16,224,44
mvv 20,228,44
s_waitcnt lgkmcnt(3)
mvv 12,232,48
R 32,2048
mvv 16,236,48
D 85,1024
mvv 20,240,48
mvv 12,244,52
R 36,3072
mvv 16,248,52
mvv 20,252,52
s_waitcnt lgkmcnt(2)
v_mfma_f32_16x16x32_bf16 v[0:3], a[0:3], v[24:27], 0
R 40,4096
D 85,2048
v_mfma_f32_16x16x32_bf16 v[4:7], a[4:7], v[24:27], 0
v_fma_f32 v104, v79, v56, v92
v_fma_f32 v12, v12, v78, v104
v_mfma_f32_16x16x32_bf16 v[8:11], a[8:11], v[24:27], 0
v_fma_f32 v105, v79, v57, v93
v_fma_f32 v13, v13, v78, v105
mav 0,12,28
R 44,5120
v_mul_f32_e64 v12, v12, |v12|
mav 4,16,28
D 85,3072
v_mul_f32_e64 v13, v13, |v13|
mav 8,20,28
v_cvt_pk_bf16_f32 v12, v12, v13
s_waitcnt lgkmcnt(2)
mav 0,24,32
R 48,6144
v_pk_max_i16 v68, v12, 0
mav 4,28,32
s_add_u32 m0, s50, 36864
v_fma_f32 v104, v79, v58, v94
mav 8,32,32
v_fma_f32 v14, v14, v78, v104
v_fma_f32 v105, v79, v59, v95
mav 0,36,36
R 52,7168
D 86,0
mav 4,40,36
v_fma_f32 v15, v15, v78, v105
v_mul_f32_e64 v14, v14, |v14|
mav 8,44,36
v_mul_f32_e64 v15, v15, |v15|
s_waitcnt lgkmcnt(2)
mav 0,48,40
R 24,8192
v_cvt_pk_bf16_f32 v14, v14, v15
mav 4,52,40
D 86,1024
v_pk_max_i16 v69, v14, 0
mav 8,56,40
v_fma_f32 v104, v79, v60, v96
v_fma_f32 v16, v16, v78, v104
mav 0,60,44
R 28,9216
v_fma_f32 v105, v79, v61, v97
mav 4,64,44
v_fma_f32 v17, v17, v78, v105
v_mul_f32_e64 v16, v16, |v16|
mav 8,68,44
v_mul_f32_e64 v17, v17, |v17|
s_waitcnt lgkmcnt(2)
mav 0,72,48
R 32,10240
D 86,2048
mav 4,76,48
v_cvt_pk_bf16_f32 v16, v16, v17
v_pk_max_i16 v70, v16, 0
mav 8,80,48
v_fma_f32 v104, v79, v62, v98
v_fma_f32 v18, v18, v78, v104
mav 0,84,52
R 36,11264
v_fma_f32 v105, v79, v63, v99
mav 4,88,52
D 86,3072
v_fma_f32 v19, v19, v78, v105
mav 8,92,52
v_mul_f32_e64 v18, v18, |v18|
s_waitcnt lgkmcnt(2)
mav 0,96,24
R 40,12288
v_mul_f32_e64 v19, v19, |v19|
mav 4,100,24
v_cvt_pk_bf16_f32 v18, v18, v19
v_pk_max_i16 v71, v18, 0
mav 8,104,24
buffer_store_dwordx4 v[68:71], v83, s[36:39], s49 offen sc1
v_fma_f32 v104, v79, v64, v100
mav 0,108,28
R 44,13312
v_fma_f32 v20, v20, v78, v104
mav 4,112,28
v_fma_f32 v105, v79, v65, v101
v_fma_f32 v21, v21, v78, v105
mav 8,116,28
v_mul_f32_e64 v20, v20, |v20|
s_waitcnt lgkmcnt(2)
mav 0,120,32
R 48,14336
v_mul_f32_e64 v21, v21, |v21|
mav 4,124,32
v_cvt_pk_bf16_f32 v20, v20, v21
v_pk_max_i16 v72, v20, 0
mav 8,128,32
v_pk_max_i16 v74, v20, 0
v_fma_f32 v104, v79, v66, v102
mav 0,132,36
R 52,15360
v_fma_f32 v22, v22, v78, v104
mav 4,136,36
s_mov_b32 m0, 142336
v_fma_f32 v105, v79, v67, v103
mav 8,140,36
v_fma_f32 v23, v23, v78, v105
s_waitcnt lgkmcnt(2)
mav 0,144,40
R 24,16384
buffer_load_dword v87, s[36:39], 0 offen sc1 lds
mav 4,148,40
v_mul_f32_e64 v22, v22, |v22|
v_mul_f32_e64 v23, v23, |v23|
mav 8,152,40
v_cvt_pk_bf16_f32 v22, v22, v23
v_pk_max_i16 v73, v22, 0
mav 0,156,44
R 28,17408
v_pk_max_i16 v75, v22, 0
mav 4,160,44
v_permlane16_swap_b32 v72, v74
v_permlane16_swap_b32 v73, v75
mav 8,164,44
buffer_store_dwordx4 v[72:75], v84, s[36:39], s49 offen sc1
s_waitcnt lgkmcnt(2)
mav 0,168,48
R 32,18432
v_add_u32 v76, 1, v76
mav 4,172,48
s_waitcnt vmcnt(11)
buffer_store_dwordx2 v[76:77], v81, s[36:39], s52 offen sc1
mav 8,176,48
v_add_u32 v88, 1, v88
mav 0,180,52
R 36,19456
mav 4,184,52
mav 8,188,52
s_waitcnt lgkmcnt(2)
mav 0,192,24
R 40,20480
mav 4,196,24
mav 8,200,24
mav 0,204,28
R 44,21504
mav 4,208,28
mav 8,212,28
s_waitcnt lgkmcnt(2)
mav 0,216,32
R 48,22528
mav 4,220,32
mav 8,224,32
mav 0,228,36
R 52,23552
mav 4,232,36
mav 8,236,36
s_waitcnt lgkmcnt(2)
mav 0,240,40
R 24,24576
mav 4,244,40
mav 8,248,40
mav 0,252,44
R 28,25600
mvv 4,128,44
mvv 8,132,44
s_waitcnt lgkmcnt(2)
mvv 0,136,48
R 32,26624
mvv 4,140,48
mvv 8,144,48
mvv 0,148,52
R 36,27648
mvv 4,152,52
mvv 8,156,52
s_waitcnt lgkmcnt(2)
mvv 0,160,24
R 40,28672
mvv 4,164,24
ds_read_b32 v106, v82 offset:0
mvv 8,168,24
mvv 0,172,28
R 44,29696
mvv 4,176,28
mvv 8,180,28
s_waitcnt lgkmcnt(3)
mvv 0,184,32
R 48,30720
s_xor_b32 s50, s50, 0x10000
mvv 4,188,32
mvv 8,192,32
mvv 0,196,36
R 52,31744
mvv 4,200,36
s_waitcnt lgkmcnt(2)
v_cmp_gt_i32 vcc, v88, v106
mvv 8,204,36
s_waitcnt vmcnt(4)
s_cbranch_vccnz CF0_5
BF0_5:
mvv 0,208,40
s_barrier
R 24,32768
mvv 4,212,40
s_add_u32 s49, s49, 346112
s_add_u32 m0, s50, 0
mvv 8,216,40
ds_read_b64 v[78:79], v89 offset:0
mvv 0,220,44
R 28,33792
D 85,0
mvv 4,224,44
mvv 8,228,44
s_waitcnt lgkmcnt(3)
mvv 0,232,48
R 32,34816
mvv 4,236,48
D 85,1024
mvv 8,240,48
mvv 0,244,52
R 36,35840
mvv 4,248,52
mvv 8,252,52
s_waitcnt lgkmcnt(2)
v_mfma_f32_16x16x32_bf16 v[12:15], a[0:3], v[24:27], 0
R 40,36864
D 85,2048
v_mfma_f32_16x16x32_bf16 v[16:19], a[4:7], v[24:27], 0
v_fma_f32 v104, v79, v56, v92
v_fma_f32 v0, v0, v78, v104
v_mfma_f32_16x16x32_bf16 v[20:23], a[8:11], v[24:27], 0
v_fma_f32 v105, v79, v57, v93
v_fma_f32 v1, v1, v78, v105
mav 12,12,28
R 44,37888
v_mul_f32_e64 v0, v0, |v0|
mav 16,16,28
D 85,3072
v_mul_f32_e64 v1, v1, |v1|
mav 20,20,28
v_cvt_pk_bf16_f32 v0, v0, v1
s_waitcnt lgkmcnt(2)
mav 12,24,32
R 48,38912
v_pk_max_i16 v68, v0, 0
mav 16,28,32
s_add_u32 m0, s50, 4096
v_fma_f32 v104, v79, v58, v94
mav 20,32,32
v_fma_f32 v2, v2, v78, v104
v_fma_f32 v105, v79, v59, v95
mav 12,36,36
R 52,39936
D 86,0
mav 16,40,36
v_fma_f32 v3, v3, v78, v105
v_mul_f32_e64 v2, v2, |v2|
mav 20,44,36
v_mul_f32_e64 v3, v3, |v3|
s_waitcnt lgkmcnt(2)
mav 12,48,40
R 24,40960
v_cvt_pk_bf16_f32 v2, v2, v3
mav 16,52,40
D 86,1024
v_pk_max_i16 v69, v2, 0
mav 20,56,40
v_fma_f32 v104, v79, v60, v96
v_fma_f32 v4, v4, v78, v104
mav 12,60,44
R 28,41984
v_fma_f32 v105, v79, v61, v97
mav 16,64,44
v_fma_f32 v5, v5, v78, v105
v_mul_f32_e64 v4, v4, |v4|
mav 20,68,44
v_mul_f32_e64 v5, v5, |v5|
s_waitcnt lgkmcnt(2)
mav 12,72,48
R 32,43008
D 86,2048
mav 16,76,48
v_cvt_pk_bf16_f32 v4, v4, v5
v_pk_max_i16 v70, v4, 0
mav 20,80,48
v_fma_f32 v104, v79, v62, v98
v_fma_f32 v6, v6, v78, v104
mav 12,84,52
R 36,44032
v_fma_f32 v105, v79, v63, v99
mav 16,88,52
D 86,3072
v_fma_f32 v7, v7, v78, v105
mav 20,92,52
v_mul_f32_e64 v6, v6, |v6|
s_waitcnt lgkmcnt(2)
mav 12,96,24
R 40,45056
v_mul_f32_e64 v7, v7, |v7|
mav 16,100,24
v_cvt_pk_bf16_f32 v6, v6, v7
v_pk_max_i16 v71, v6, 0
mav 20,104,24
buffer_store_dwordx4 v[68:71], v83, s[36:39], s49 offen sc1
v_fma_f32 v104, v79, v64, v100
mav 12,108,28
R 44,46080
v_fma_f32 v8, v8, v78, v104
mav 16,112,28
v_fma_f32 v105, v79, v65, v101
v_fma_f32 v9, v9, v78, v105
mav 20,116,28
v_mul_f32_e64 v8, v8, |v8|
s_waitcnt lgkmcnt(2)
mav 12,120,32
R 48,47104
v_mul_f32_e64 v9, v9, |v9|
mav 16,124,32
v_cvt_pk_bf16_f32 v8, v8, v9
v_pk_max_i16 v72, v8, 0
mav 20,128,32
v_pk_max_i16 v74, v8, 0
v_fma_f32 v104, v79, v66, v102
mav 12,132,36
R 52,48128
v_fma_f32 v10, v10, v78, v104
mav 16,136,36
s_mov_b32 m0, 142336
v_fma_f32 v105, v79, v67, v103
mav 20,140,36
v_fma_f32 v11, v11, v78, v105
s_waitcnt lgkmcnt(2)
mav 12,144,40
R 24,49152
buffer_load_dword v87, s[36:39], 0 offen sc1 lds
mav 16,148,40
v_mul_f32_e64 v10, v10, |v10|
v_mul_f32_e64 v11, v11, |v11|
mav 20,152,40
v_cvt_pk_bf16_f32 v10, v10, v11
v_pk_max_i16 v73, v10, 0
mav 12,156,44
R 28,50176
v_pk_max_i16 v75, v10, 0
mav 16,160,44
v_permlane16_swap_b32 v72, v74
v_permlane16_swap_b32 v73, v75
mav 20,164,44
buffer_store_dwordx4 v[72:75], v84, s[36:39], s49 offen sc1
s_waitcnt lgkmcnt(2)
mav 12,168,48
R 32,51200
v_add_u32 v76, 1, v76
mav 16,172,48
s_waitcnt vmcnt(12)
buffer_store_dwordx2 v[76:77], v81, s[36:39], s52 offen sc1
mav 20,176,48
v_add_u32 v88, 1, v88
mav 12,180,52
R 36,52224
mav 16,184,52
mav 20,188,52
s_waitcnt lgkmcnt(2)
mav 12,192,24
R 40,53248
mav 16,196,24
mav 20,200,24
mav 12,204,28
R 44,54272
mav 16,208,28
mav 20,212,28
s_waitcnt lgkmcnt(2)
mav 12,216,32
R 48,55296
mav 16,220,32
mav 20,224,32
mav 12,228,36
R 52,56320
mav 16,232,36
mav 20,236,36
s_waitcnt lgkmcnt(2)
mav 12,240,40
R 24,57344
mav 16,244,40
mav 20,248,40
mav 12,252,44
R 28,58368
mvv 16,128,44
mvv 20,132,44
s_waitcnt lgkmcnt(2)
mvv 12,136,48
R 32,59392
mvv 16,140,48
mvv 20,144,48
mvv 12,148,52
R 36,60416
mvv 16,152,52
mvv 20,156,52
s_waitcnt lgkmcnt(2)
mvv 12,160,24
R 40,61440
mvv 16,164,24
ds_read_b32 v106, v82 offset:0
mvv 20,168,24
mvv 12,172,28
R 44,62464
mvv 16,176,28
mvv 20,180,28
s_waitcnt lgkmcnt(3)
mvv 12,184,32
R 48,63488
mvv 16,188,32
mvv 20,192,32
mvv 12,196,36
R 52,64512
v_xor_b32 v80, 0x10000, v80
mvv 16,200,36
s_waitcnt lgkmcnt(2)
v_cmp_gt_i32 vcc, v88, v106
mvv 20,204,36
s_waitcnt vmcnt(4)
s_cbranch_vccnz CF0_6
BF0_6:
mvv 12,208,40
s_barrier
R 24,0
mvv 16,212,40
s_add_u32 s49, s49, 346112
s_add_u32 m0, s50, 32768
mvv 20,216,40
ds_read_b64 v[78:79], v89 offset:128
mvv 12,220,44
R 28,1024
D 85,0
mvv 16,224,44
mvv 20,228,44
s_waitcnt lgkmcnt(3)
mvv 12,232,48
R 32,2048
mvv 16,236,48
D 85,1024
mvv 20,240,48
mvv 12,244,52
R 36,3072
mvv 16,248,52
mvv 20,252,52
s_mov_b32 s60, -29
.p2align 6
LF0_7:
s_waitcnt lgkmcnt(2)
v_mfma_f32_16x16x32_bf16 v[0:3], a[0:3], v[24:27], 0
R 40,4096
D 85,2048
v_mfma_f32_16x16x32_bf16 v[4:7], a[4:7], v[24:27], 0
v_fma_f32 v104, v79, v56, v92
v_fma_f32 v12, v12, v78, v104
v_mfma_f32_16x16x32_bf16 v[8:11], a[8:11], v[24:27], 0
v_fma_f32 v105, v79, v57, v93
v_fma_f32 v13, v13, v78, v105
mav 0,12,28
R 44,5120
v_mul_f32_e64 v12, v12, |v12|
mav 4,16,28
D 85,3072
v_mul_f32_e64 v13, v13, |v13|
mav 8,20,28
v_cvt_pk_bf16_f32 v12, v12, v13
s_waitcnt lgkmcnt(2)
mav 0,24,32
R 48,6144
v_pk_max_i16 v68, v12, 0
mav 4,28,32
s_add_u32 m0, s50, 36864
v_fma_f32 v104, v79, v58, v94
mav 8,32,32
v_fma_f32 v14, v14, v78, v104
v_fma_f32 v105, v79, v59, v95
mav 0,36,36
R 52,7168
D 86,0
mav 4,40,36
v_fma_f32 v15, v15, v78, v105
v_mul_f32_e64 v14, v14, |v14|
mav 8,44,36
v_mul_f32_e64 v15, v15, |v15|
s_waitcnt lgkmcnt(2)
mav 0,48,40
R 24,8192
v_cvt_pk_bf16_f32 v14, v14, v15
mav 4,52,40
D 86,1024
v_pk_max_i16 v69, v14, 0
mav 8,56,40
v_fma_f32 v104, v79, v60, v96
v_fma_f32 v16, v16, v78, v104
mav 0,60,44
R 28,9216
v_fma_f32 v105, v79, v61, v97
mav 4,64,44
v_fma_f32 v17, v17, v78, v105
v_mul_f32_e64 v16, v16, |v16|
mav 8,68,44
v_mul_f32_e64 v17, v17, |v17|
s_waitcnt lgkmcnt(2)
mav 0,72,48
R 32,10240
D 86,2048
mav 4,76,48
v_cvt_pk_bf16_f32 v16, v16, v17
v_pk_max_i16 v70, v16, 0
mav 8,80,48
v_fma_f32 v104, v79, v62, v98
v_fma_f32 v18, v18, v78, v104
mav 0,84,52
R 36,11264
v_fma_f32 v105, v79, v63, v99
mav 4,88,52
D 86,3072
v_fma_f32 v19, v19, v78, v105
mav 8,92,52
v_mul_f32_e64 v18, v18, |v18|
s_waitcnt lgkmcnt(2)
mav 0,96,24
R 40,12288
v_mul_f32_e64 v19, v19, |v19|
mav 4,100,24
v_cvt_pk_bf16_f32 v18, v18, v19
v_pk_max_i16 v71, v18, 0
mav 8,104,24
buffer_store_dwordx4 v[68:71], v83, s[36:39], s49 offen sc1
v_fma_f32 v104, v79, v64, v100
mav 0,108,28
R 44,13312
v_fma_f32 v20, v20, v78, v104
mav 4,112,28
v_fma_f32 v105, v79, v65, v101
v_fma_f32 v21, v21, v78, v105
mav 8,116,28
v_mul_f32_e64 v20, v20, |v20|
s_waitcnt lgkmcnt(2)
mav 0,120,32
R 48,14336
v_mul_f32_e64 v21, v21, |v21|
mav 4,124,32
v_cvt_pk_bf16_f32 v20, v20, v21
v_pk_max_i16 v72, v20, 0
mav 8,128,32
v_pk_max_i16 v74, v20, 0
v_fma_f32 v104, v79, v66, v102
mav 0,132,36
R 52,15360
v_fma_f32 v22, v22, v78, v104
mav 4,136,36
s_mov_b32 m0, 142336
v_fma_f32 v105, v79, v67, v103
mav 8,140,36
v_fma_f32 v23, v23, v78, v105
s_waitcnt lgkmcnt(2)
mav 0,144,40
R 24,16384
buffer_load_dword v87, s[36:39], 0 offen sc1 lds
mav 4,148,40
v_mul_f32_e64 v22, v22, |v22|
v_mul_f32_e64 v23, v23, |v23|
mav 8,152,40
v_cvt_pk_bf16_f32 v22, v22, v23
v_pk_max_i16 v73, v22, 0
mav 0,156,44
R 28,17408
v_pk_max_i16 v75, v22, 0
mav 4,160,44
v_permlane16_swap_b32 v72, v74
v_permlane16_swap_b32 v73, v75
mav 8,164,44
buffer_store_dwordx4 v[72:75], v84, s[36:39], s49 offen sc1
s_waitcnt lgkmcnt(2)
mav 0,168,48
R 32,18432
v_add_u32 v76, 1, v76
mav 4,172,48
s_waitcnt vmcnt(12)
buffer_store_dwordx2 v[76:77], v81, s[36:39], s52 offen sc1
mav 8,176,48
v_add_u32 v88, 1, v88
mav 0,180,52
R 36,19456
mav 4,184,52
mav 8,188,52
s_waitcnt lgkmcnt(2)
mav 0,192,24
R 40,20480
mav 4,196,24
mav 8,200,24
mav 0,204,28
R 44,21504
mav 4,208,28
mav 8,212,28
s_waitcnt lgkmcnt(2)
mav 0,216,32
R 48,22528
mav 4,220,32
mav 8,224,32
mav 0,228,36
R 52,23552
mav 4,232,36
mav 8,236,36
s_waitcnt lgkmcnt(2)
mav 0,240,40
R 24,24576
mav 4,244,40
mav 8,248,40
mav 0,252,44
R 28,25600
mvv 4,128,44
mvv 8,132,44
s_waitcnt lgkmcnt(2)
mvv 0,136,48
R 32,26624
mvv 4,140,48
mvv 8,144,48
mvv 0,148,52
R 36,27648
mvv 4,152,52
mvv 8,156,52
s_waitcnt lgkmcnt(2)
mvv 0,160,24
R 40,28672
mvv 4,164,24
ds_read_b32 v106, v82 offset:0
mvv 8,168,24
mvv 0,172,28
R 44,29696
mvv 4,176,28
mvv 8,180,28
s_waitcnt lgkmcnt(3)
mvv 0,184,32
R 48,30720
s_xor_b32 s50, s50, 0x10000
mvv 4,188,32
mvv 8,192,32
mvv 0,196,36
R 52,31744
mvv 4,200,36
s_waitcnt lgkmcnt(2)
v_cmp_gt_i32 vcc, v88, v106
mvv 8,204,36
s_waitcnt vmcnt(4)
s_cbranch_vccnz CF0_8
BF0_8:
mvv 0,208,40
s_barrier
R 24,32768
mvv 4,212,40
s_add_u32 s49, s49, 346112
s_add_u32 m0, s50, 0
mvv 8,216,40
ds_read_b64 v[78:79], v89 offset:0
mvv 0,220,44
R 28,33792
D 85,0
mvv 4,224,44
mvv 8,228,44
s_waitcnt lgkmcnt(3)
mvv 0,232,48
R 32,34816
mvv 4,236,48
D 85,1024
mvv 8,240,48
mvv 0,244,52
R 36,35840
mvv 4,248,52
mvv 8,252,52
s_waitcnt lgkmcnt(2)
v_mfma_f32_16x16x32_bf16 v[12:15], a[0:3], v[24:27], 0
R 40,36864
D 85,2048
v_mfma_f32_16x16x32_bf16 v[16:19], a[4:7], v[24:27], 0
v_fma_f32 v104, v79, v56, v92
v_fma_f32 v0, v0, v78, v104
v_mfma_f32_16x16x32_bf16 v[20:23], a[8:11], v[24:27], 0
v_fma_f32 v105, v79, v57, v93
v_fma_f32 v1, v1, v78, v105
mav 12,12,28
R 44,37888
v_mul_f32_e64 v0, v0, |v0|
mav 16,16,28
D 85,3072
v_mul_f32_e64 v1, v1, |v1|
mav 20,20,28
v_cvt_pk_bf16_f32 v0, v0, v1
s_waitcnt lgkmcnt(2)
mav 12,24,32
R 48,38912
v_pk_max_i16 v68, v0, 0
mav 16,28,32
s_add_u32 m0, s50, 4096
v_fma_f32 v104, v79, v58, v94
mav 20,32,32
v_fma_f32 v2, v2, v78, v104
v_fma_f32 v105, v79, v59, v95
mav 12,36,36
R 52,39936
D 86,0
mav 16,40,36
v_fma_f32 v3, v3, v78, v105
v_mul_f32_e64 v2, v2, |v2|
mav 20,44,36
v_mul_f32_e64 v3, v3, |v3|
s_waitcnt lgkmcnt(2)
mav 12,48,40
R 24,40960
v_cvt_pk_bf16_f32 v2, v2, v3
mav 16,52,40
D 86,1024
v_pk_max_i16 v69, v2, 0
mav 20,56,40
v_fma_f32 v104, v79, v60, v96
v_fma_f32 v4, v4, v78, v104
mav 12,60,44
R 28,41984
v_fma_f32 v105, v79, v61, v97
mav 16,64,44
v_fma_f32 v5, v5, v78, v105
v_mul_f32_e64 v4, v4, |v4|
mav 20,68,44
v_mul_f32_e64 v5, v5, |v5|
s_waitcnt lgkmcnt(2)
mav 12,72,48
R 32,43008
D 86,2048
mav 16,76,48
v_cvt_pk_bf16_f32 v4, v4, v5
v_pk_max_i16 v70, v4, 0
mav 20,80,48
v_fma_f32 v104, v79, v62, v98
v_fma_f32 v6, v6, v78, v104
mav 12,84,52
R 36,44032
v_fma_f32 v105, v79, v63, v99
mav 16,88,52
D 86,3072
v_fma_f32 v7, v7, v78, v105
mav 20,92,52
v_mul_f32_e64 v6, v6, |v6|
s_waitcnt lgkmcnt(2)
mav 12,96,24
R 40,45056
v_mul_f32_e64 v7, v7, |v7|
mav 16,100,24
v_cvt_pk_bf16_f32 v6, v6, v7
v_pk_max_i16 v71, v6, 0
mav 20,104,24
buffer_store_dwordx4 v[68:71], v83, s[36:39], s49 offen sc1
v_fma_f32 v104, v79, v64, v100
mav 12,108,28
R 44,46080
v_fma_f32 v8, v8, v78, v104
mav 16,112,28
v_fma_f32 v105, v79, v65, v101
v_fma_f32 v9, v9, v78, v105
mav 20,116,28
v_mul_f32_e64 v8, v8, |v8|
s_waitcnt lgkmcnt(2)
mav 12,120,32
R 48,47104
v_mul_f32_e64 v9, v9, |v9|
mav 16,124,32
v_cvt_pk_bf16_f32 v8, v8, v9
v_pk_max_i16 v72, v8, 0
mav 20,128,32
v_pk_max_i16 v74, v8, 0
v_fma_f32 v104, v79, v66, v102
mav 12,132,36
R 52,48128
v_fma_f32 v10, v10, v78, v104
mav 16,136,36
s_mov_b32 m0, 142336
v_fma_f32 v105, v79, v67, v103
mav 20,140,36
v_fma_f32 v11, v11, v78, v105
s_waitcnt lgkmcnt(2)
mav 12,144,40
R 24,49152
buffer_load_dword v87, s[36:39], 0 offen sc1 lds
mav 16,148,40
v_mul_f32_e64 v10, v10, |v10|
v_mul_f32_e64 v11, v11, |v11|
mav 20,152,40
v_cvt_pk_bf16_f32 v10, v10, v11
v_pk_max_i16 v73, v10, 0
mav 12,156,44
R 28,50176
v_pk_max_i16 v75, v10, 0
mav 16,160,44
v_permlane16_swap_b32 v72, v74
v_permlane16_swap_b32 v73, v75
mav 20,164,44
buffer_store_dwordx4 v[72:75], v84, s[36:39], s49 offen sc1
s_waitcnt lgkmcnt(2)
mav 12,168,48
R 32,51200
v_add_u32 v76, 1, v76
mav 16,172,48
s_waitcnt vmcnt(12)
buffer_store_dwordx2 v[76:77], v81, s[36:39], s52 offen sc1
mav 20,176,48
v_add_u32 v88, 1, v88
mav 12,180,52
R 36,52224
mav 16,184,52
mav 20,188,52
s_waitcnt lgkmcnt(2)
mav 12,192,24
R 40,53248
mav 16,196,24
mav 20,200,24
mav 12,204,28
R 44,54272
mav 16,208,28
mav 20,212,28
s_waitcnt lgkmcnt(2)
mav 12,216,32
R 48,55296
mav 16,220,32
mav 20,224,32
mav 12,228,36
R 52,56320
mav 16,232,36
mav 20,236,36
s_waitcnt lgkmcnt(2)
mav 12,240,40
R 24,57344
mav 16,244,40
mav 20,248,40
mav 12,252,44
R 28,58368
mvv 16,128,44
mvv 20,132,44
s_waitcnt lgkmcnt(2)
mvv 12,136,48
R 32,59392
mvv 16,140,48
mvv 20,144,48
mvv 12,148,52
R 36,60416
mvv 16,152,52
mvv 20,156,52
s_waitcnt lgkmcnt(2)
mvv 12,160,24
R 40,61440
mvv 16,164,24
ds_read_b32 v106, v82 offset:0
mvv 20,168,24
mvv 12,172,28
R 44,62464
mvv 16,176,28
mvv 20,180,28
s_waitcnt lgkmcnt(3)
mvv 12,184,32
R 48,63488
mvv 16,188,32
mvv 20,192,32
mvv 12,196,36
R 52,64512
v_xor_b32 v80, 0x10000, v80
mvv 16,200,36
s_waitcnt lgkmcnt(2)
v_cmp_gt_i32 vcc, v88, v106
mvv 20,204,36
s_waitcnt vmcnt(4)
s_cbranch_vccnz CF0_9
BF0_9:
mvv 12,208,40
s_barrier
R 24,0
mvv 16,212,40
s_add_u32 s49, s49, 346112
s_add_u32 m0, s50, 32768
mvv 20,216,40
ds_read_b64 v[78:79], v89 offset:128
mvv 12,220,44
R 28,1024
D 85,0
mvv 16,224,44
mvv 20,228,44
s_waitcnt lgkmcnt(3)
mvv 12,232,48
R 32,2048
mvv 16,236,48
D 85,1024
mvv 20,240,48
mvv 12,244,52
R 36,3072
mvv 16,248,52
mvv 20,252,52
s_add_u32 s60, s60, 1
s_cbranch_scc0 LF0_7
s_waitcnt lgkmcnt(2)
v_mfma_f32_16x16x32_bf16 v[0:3], a[0:3], v[24:27], 0
R 40,4096
D 85,2048
v_mfma_f32_16x16x32_bf16 v[4:7], a[4:7], v[24:27], 0
v_fma_f32 v104, v79, v56, v92
v_fma_f32 v12, v12, v78, v104
v_mfma_f32_16x16x32_bf16 v[8:11], a[8:11], v[24:27], 0
v_fma_f32 v105, v79, v57, v93
v_fma_f32 v13, v13, v78, v105
mav 0,12,28
R 44,5120
v_mul_f32_e64 v12, v12, |v12|
mav 4,16,28
D 85,3072
v_mul_f32_e64 v13, v13, |v13|
mav 8,20,28
v_cvt_pk_bf16_f32 v12, v12, v13
s_waitcnt lgkmcnt(2)
mav 0,24,32
R 48,6144
v_pk_max_i16 v68, v12, 0
mav 4,28,32
s_add_u32 m0, s50, 36864
v_fma_f32 v104, v79, v58, v94
mav 8,32,32
v_fma_f32 v14, v14, v78, v104
v_fma_f32 v105, v79, v59, v95
mav 0,36,36
R 52,7168
D 86,0
mav 4,40,36
v_fma_f32 v15, v15, v78, v105
v_mul_f32_e64 v14, v14, |v14|
mav 8,44,36
v_mul_f32_e64 v15, v15, |v15|
s_waitcnt lgkmcnt(2)
mav 0,48,40
R 24,8192
v_cvt_pk_bf16_f32 v14, v14, v15
mav 4,52,40
D 86,1024
v_pk_max_i16 v69, v14, 0
mav 8,56,40
v_fma_f32 v104, v79, v60, v96
v_fma_f32 v16, v16, v78, v104
mav 0,60,44
R 28,9216
v_fma_f32 v105, v79, v61, v97
mav 4,64,44
v_fma_f32 v17, v17, v78, v105
v_mul_f32_e64 v16, v16, |v16|
mav 8,68,44
v_mul_f32_e64 v17, v17, |v17|
s_waitcnt lgkmcnt(2)
mav 0,72,48
R 32,10240
D 86,2048
mav 4,76,48
v_cvt_pk_bf16_f32 v16, v16, v17
v_pk_max_i16 v70, v16, 0
mav 8,80,48
v_fma_f32 v104, v79, v62, v98
v_fma_f32 v18, v18, v78, v104
mav 0,84,52
R 36,11264
v_fma_f32 v105, v79, v63, v99
mav 4,88,52
D 86,3072
v_fma_f32 v19, v19, v78, v105
mav 8,92,52
v_mul_f32_e64 v18, v18, |v18|
s_waitcnt lgkmcnt(2)
mav 0,96,24
R 40,12288
v_mul_f32_e64 v19, v19, |v19|
mav 4,100,24
v_cvt_pk_bf16_f32 v18, v18, v19
v_pk_max_i16 v71, v18, 0
mav 8,104,24
buffer_store_dwordx4 v[68:71], v83, s[36:39], s49 offen sc1
v_fma_f32 v104, v79, v64, v100
mav 0,108,28
R 44,13312
v_fma_f32 v20, v20, v78, v104
mav 4,112,28
v_fma_f32 v105, v79, v65, v101
v_fma_f32 v21, v21, v78, v105
mav 8,116,28
v_mul_f32_e64 v20, v20, |v20|
s_waitcnt lgkmcnt(2)
mav 0,120,32
R 48,14336
v_mul_f32_e64 v21, v21, |v21|
mav 4,124,32
v_cvt_pk_bf16_f32 v20, v20, v21
v_pk_max_i16 v72, v20, 0
mav 8,128,32
v_pk_max_i16 v74, v20, 0
v_fma_f32 v104, v79, v66, v102
mav 0,132,36
R 52,15360
v_fma_f32 v22, v22, v78, v104
mav 4,136,36
s_mov_b32 m0, 142336
v_fma_f32 v105, v79, v67, v103
mav 8,140,36
v_fma_f32 v23, v23, v78, v105
s_waitcnt lgkmcnt(2)
mav 0,144,40
R 24,16384
buffer_load_dword v87, s[36:39], 0 offen sc1 lds
mav 4,148,40
v_mul_f32_e64 v22, v22, |v22|
v_mul_f32_e64 v23, v23, |v23|
mav 8,152,40
v_cvt_pk_bf16_f32 v22, v22, v23
v_pk_max_i16 v73, v22, 0
mav 0,156,44
R 28,17408
v_pk_max_i16 v75, v22, 0
mav 4,160,44
v_permlane16_swap_b32 v72, v74
v_permlane16_swap_b32 v73, v75
mav 8,164,44
buffer_store_dwordx4 v[72:75], v84, s[36:39], s49 offen sc1
s_waitcnt lgkmcnt(2)
mav 0,168,48
R 32,18432
v_add_u32 v76, 1, v76
mav 4,172,48
s_waitcnt vmcnt(12)
buffer_store_dwordx2 v[76:77], v81, s[36:39], s52 offen sc1
mav 8,176,48
v_add_u32 v88, 1, v88
mav 0,180,52
R 36,19456
mav 4,184,52
mav 8,188,52
s_waitcnt lgkmcnt(2)
mav 0,192,24
R 40,20480
mav 4,196,24
mav 8,200,24
mav 0,204,28
R 44,21504
mav 4,208,28
mav 8,212,28
s_waitcnt lgkmcnt(2)
mav 0,216,32
R 48,22528
mav 4,220,32
mav 8,224,32
mav 0,228,36
R 52,23552
mav 4,232,36
mav 8,236,36
s_waitcnt lgkmcnt(2)
mav 0,240,40
R 24,24576
mav 4,244,40
mav 8,248,40
mav 0,252,44
R 28,25600
mvv 4,128,44
mvv 8,132,44
s_waitcnt lgkmcnt(2)
mvv 0,136,48
R 32,26624
mvv 4,140,48
mvv 8,144,48
mvv 0,148,52
R 36,27648
mvv 4,152,52
mvv 8,156,52
s_waitcnt lgkmcnt(2)
mvv 0,160,24
R 40,28672
mvv 4,164,24
ds_read_b32 v106, v82 offset:0
mvv 8,168,24
mvv 0,172,28
R 44,29696
mvv 4,176,28
mvv 8,180,28
s_waitcnt lgkmcnt(3)
mvv 0,184,32
R 48,30720
mvv 4,188,32
mvv 8,192,32
mvv 0,196,36
R 52,31744
mvv 4,200,36
s_waitcnt lgkmcnt(2)
v_cmp_gt_i32 vcc, v88, v106
mvv 8,204,36
s_waitcnt vmcnt(4)
s_cbranch_vccnz CF0_10
BF0_10:
mvv 0,208,40
s_barrier
R 24,32768
mvv 4,212,40
s_add_u32 s49, s49, 346112
ds_read_b64 v[78:79], v89 offset:0
mvv 8,216,40
mvv 0,220,44
R 28,33792
mvv 4,224,44
mvv 8,228,44
s_waitcnt lgkmcnt(3)
mvv 0,232,48
R 32,34816
mvv 4,236,48
mvv 8,240,48
mvv 0,244,52
R 36,35840
mvv 4,248,52
mvv 8,252,52
s_waitcnt lgkmcnt(2)
v_mfma_f32_16x16x32_bf16 v[12:15], a[0:3], v[24:27], 0
R 40,36864
v_fma_f32 v104, v79, v56, v92
v_mfma_f32_16x16x32_bf16 v[16:19], a[4:7], v[24:27], 0
v_fma_f32 v0, v0, v78, v104
v_fma_f32 v105, v79, v57, v93
v_mfma_f32_16x16x32_bf16 v[20:23], a[8:11], v[24:27], 0
v_fma_f32 v1, v1, v78, v105
v_mul_f32_e64 v0, v0, |v0|
mav 12,12,28
R 44,37888
v_mul_f32_e64 v1, v1, |v1|
mav 16,16,28
v_cvt_pk_bf16_f32 v0, v0, v1
v_pk_max_i16 v68, v0, 0
mav 20,20,28
v_fma_f32 v104, v79, v58, v94
s_waitcnt lgkmcnt(2)
mav 12,24,32
R 48,38912
v_fma_f32 v2, v2, v78, v104
mav 16,28,32
v_fma_f32 v105, v79, v59, v95
v_fma_f32 v3, v3, v78, v105
mav 20,32,32
v_mul_f32_e64 v2, v2, |v2|
v_mul_f32_e64 v3, v3, |v3|
mav 12,36,36
R 52,39936
v_cvt_pk_bf16_f32 v2, v2, v3
mav 16,40,36
v_pk_max_i16 v69, v2, 0
v_fma_f32 v104, v79, v60, v96
mav 20,44,36
v_fma_f32 v4, v4, v78, v104
s_waitcnt lgkmcnt(2)
mav 12,48,40
R 24,40960
v_fma_f32 v105, v79, v61, v97
mav 16,52,40
v_fma_f32 v5, v5, v78, v105
v_mul_f32_e64 v4, v4, |v4|
mav 20,56,40
v_mul_f32_e64 v5, v5, |v5|
v_cvt_pk_bf16_f32 v4, v4, v5
mav 12,60,44
R 28,41984
v_pk_max_i16 v70, v4, 0
mav 16,64,44
v_fma_f32 v104, v79, v62, v98
v_fma_f32 v6, v6, v78, v104
mav 20,68,44
v_fma_f32 v105, v79, v63, v99
s_waitcnt lgkmcnt(2)
mav 12,72,48
R 32,43008
v_fma_f32 v7, v7, v78, v105
mav 16,76,48
v_mul_f32_e64 v6, v6, |v6|
v_mul_f32_e64 v7, v7, |v7|
mav 20,80,48
v_cvt_pk_bf16_f32 v6, v6, v7
v_pk_max_i16 v71, v6, 0
mav 12,84,52
R 36,44032
buffer_store_dwordx4 v[68:71], v83, s[36:39], s49 offen sc1
mav 16,88,52
v_fma_f32 v104, v79, v64, v100
v_fma_f32 v8, v8, v78, v104
mav 20,92,52
v_fma_f32 v105, v79, v65, v101
s_waitcnt lgkmcnt(2)
mav 12,96,24
R 40,45056
v_fma_f32 v9, v9, v78, v105
mav 16,100,24
v_mul_f32_e64 v8, v8, |v8|
v_mul_f32_e64 v9, v9, |v9|
mav 20,104,24
v_cvt_pk_bf16_f32 v8, v8, v9
v_pk_max_i16 v72, v8, 0
mav 12,108,28
R 44,46080
v_pk_max_i16 v74, v8, 0
mav 16,112,28
v_fma_f32 v104, v79, v66, v102
v_fma_f32 v10, v10, v78, v104
mav 20,116,28
v_fma_f32 v105, v79, v67, v103
s_waitcnt lgkmcnt(2)
mav 12,120,32
R 48,47104
v_fma_f32 v11, v11, v78, v105
mav 16,124,32
v_mul_f32_e64 v10, v10, |v10|
v_mul_f32_e64 v11, v11, |v11|
mav 20,128,32
v_cvt_pk_bf16_f32 v10, v10, v11
v_pk_max_i16 v73, v10, 0
mav 12,132,36
R 52,48128
v_pk_max_i16 v75, v10, 0
mav 16,136,36
v_permlane16_swap_b32 v72, v74
v_permlane16_swap_b32 v73, v75
mav 20,140,36
buffer_store_dwordx4 v[72:75], v84, s[36:39], s49 offen sc1
s_waitcnt lgkmcnt(2)
mav 12,144,40
R 24,49152
v_add_u32 v76, 1, v76
mav 16,148,40
s_waitcnt vmcnt(3)
buffer_store_dwordx2 v[76:77], v81, s[36:39], s52 offen sc1
mav 20,152,40
mav 12,156,44
R 28,50176
mav 16,160,44
mav 20,164,44
s_waitcnt lgkmcnt(2)
mav 12,168,48
R 32,51200
mav 16,172,48
mav 20,176,48
mav 12,180,52
R 36,52224
mav 16,184,52
mav 20,188,52
s_waitcnt lgkmcnt(2)
mav 12,192,24
R 40,53248
mav 16,196,24
mav 20,200,24
mav 12,204,28
R 44,54272
mav 16,208,28
mav 20,212,28
s_waitcnt lgkmcnt(2)
mav 12,216,32
R 48,55296
mav 16,220,32
mav 20,224,32
mav 12,228,36
R 52,56320
mav 16,232,36
mav 20,236,36
s_waitcnt lgkmcnt(2)
mav 12,240,40
R 24,57344
mav 16,244,40
mav 20,248,40
mav 12,252,44
R 28,58368
mvv 16,128,44
mvv 20,132,44
s_waitcnt lgkmcnt(2)
mvv 12,136,48
R 32,59392
mvv 16,140,48
mvv 20,144,48
mvv 12,148,52
R 36,60416
mvv 16,152,52
mvv 20,156,52
s_waitcnt lgkmcnt(2)
mvv 12,160,24
R 40,61440
mvv 16,164,24
mvv 20,168,24
mvv 12,172,28
R 44,62464
mvv 16,176,28
mvv 20,180,28
s_waitcnt lgkmcnt(2)
mvv 12,184,32
R 48,63488
mvv 16,188,32
mvv 20,192,32
mvv 12,196,36
R 52,64512
mvv 16,200,36
s_add_u32 s49, s49, 346112
mvv 20,204,36
s_barrier
s_waitcnt lgkmcnt(2)
mvv 12,208,40
mvv 16,212,40
ds_read_b64 v[78:79], v89 offset:128
mvv 20,216,40
mvv 12,220,44
mvv 16,224,44
mvv 20,228,44
s_waitcnt lgkmcnt(1)
mvv 12,232,48
mvv 16,236,48
mvv 20,240,48
mvv 12,244,52
mvv 16,248,52
mvv 20,252,52
s_waitcnt lgkmcnt(0)
v_fma_f32 v104, v79, v56, v92
s_nop 3
v_fma_f32 v12, v12, v78, v104
v_fma_f32 v105, v79, v57, v93
v_fma_f32 v13, v13, v78, v105
v_mul_f32_e64 v12, v12, |v12|
v_mul_f32_e64 v13, v13, |v13|
v_cvt_pk_bf16_f32 v12, v12, v13
v_pk_max_i16 v68, v12, 0
v_fma_f32 v104, v79, v58, v94
v_fma_f32 v14, v14, v78, v104
v_fma_f32 v105, v79, v59, v95
v_fma_f32 v15, v15, v78, v105
v_mul_f32_e64 v14, v14, |v14|
v_mul_f32_e64 v15, v15, |v15|
v_cvt_pk_bf16_f32 v14, v14, v15
v_pk_max_i16 v69, v14, 0
v_fma_f32 v104, v79, v60, v96
v_fma_f32 v16, v16, v78, v104
v_fma_f32 v105, v79, v61, v97
v_fma_f32 v17, v17, v78, v105
v_mul_f32_e64 v16, v16, |v16|
v_mul_f32_e64 v17, v17, |v17|
v_cvt_pk_bf16_f32 v16, v16, v17
v_pk_max_i16 v70, v16, 0
v_fma_f32 v104, v79, v62, v98
v_fma_f32 v18, v18, v78, v104
v_fma_f32 v105, v79, v63, v99
v_fma_f32 v19, v19, v78, v105
v_mul_f32_e64 v18, v18, |v18|
v_mul_f32_e64 v19, v19, |v19|
v_cvt_pk_bf16_f32 v18, v18, v19
v_pk_max_i16 v71, v18, 0
buffer_store_dwordx4 v[68:71], v83, s[36:39], s49 offen sc1
v_fma_f32 v104, v79, v64, v100
v_fma_f32 v20, v20, v78, v104
v_fma_f32 v105, v79, v65, v101
v_fma_f32 v21, v21, v78, v105
v_mul_f32_e64 v20, v20, |v20|
v_mul_f32_e64 v21, v21, |v21|
v_cvt_pk_bf16_f32 v20, v20, v21
v_pk_max_i16 v72, v20, 0
v_pk_max_i16 v74, v20, 0
v_fma_f32 v104, v79, v66, v102
v_fma_f32 v22, v22, v78, v104
v_fma_f32 v105, v79, v67, v103
v_fma_f32 v23, v23, v78, v105
v_mul_f32_e64 v22, v22, |v22|
v_mul_f32_e64 v23, v23, |v23|
v_cvt_pk_bf16_f32 v22, v22, v23
v_pk_max_i16 v73, v22, 0
v_pk_max_i16 v75, v22, 0
v_permlane16_swap_b32 v72, v74
s_nop 0
v_permlane16_swap_b32 v73, v75
buffer_store_dwordx4 v[72:75], v84, s[36:39], s49 offen sc1
v_add_u32 v76, 1, v76
s_waitcnt vmcnt(3)
buffer_store_dwordx2 v[76:77], v81, s[36:39], s52 offen sc1
v_add_u32 v76, 37, v76
s_waitcnt vmcnt(1)
buffer_store_dwordx2 v[76:77], v81, s[36:39], s52 offen sc1
s_endpgm
CF0_1:
s_cmp_eq_u32 s61, 0
s_cbranch_scc1 BF0_1
s_sub_u32 s61, s61, 1
s_sleep 8
buffer_load_dwordx2 v[106:107], v87, s[36:39], 0 offen sc1
s_waitcnt vmcnt(0)
ds_write_b32 v82, v106
v_sub_u32 v106, v106, v88
v_xor_b32 v107, v107, v77
v_cmp_lt_u64 vcc, s[62:63], v[106:107]
s_waitcnt lgkmcnt(0)
s_cbranch_vccnz CF0_1
s_branch BF0_1
CF0_2:
s_mov_b32 s58, m0
s_mov_b32 m0, 142336
DF0_2:
s_cmp_eq_u32 s61, 0
s_cbranch_scc1 EF0_2
s_sub_u32 s61, s61, 1
buffer_load_dword v87, s[36:39], 0 offen sc1 lds
s_sleep 1
ds_read_b32 v106, v82
s_waitcnt lgkmcnt(0)
v_cmp_gt_i32 vcc, v88, v106
s_cbranch_vccnz DF0_2
EF0_2:
s_mov_b32 m0, s58
s_branch BF0_2
CF0_3:
s_mov_b32 s58, m0
s_mov_b32 m0, 142336
DF0_3:
s_cmp_eq_u32 s61, 0
s_cbranch_scc1 EF0_3
s_sub_u32 s61, s61, 1
buffer_load_dword v87, s[36:39], 0 offen sc1 lds
s_sleep 1
ds_read_b32 v106, v82
s_waitcnt lgkmcnt(0)
v_cmp_gt_i32 vcc, v88, v106
s_cbranch_vccnz DF0_3
EF0_3:
s_mov_b32 m0, s58
s_branch BF0_3
CF0_4:
s_mov_b32 s58, m0
s_mov_b32 m0, 142336
DF0_4:
s_cmp_eq_u32 s61, 0
s_cbranch_scc1 EF0_4
s_sub_u32 s61, s61, 1
buffer_load_dword v87, s[36:39], 0 offen sc1 lds
s_sleep 1
ds_read_b32 v106, v82
s_waitcnt lgkmcnt(0)
v_cmp_gt_i32 vcc, v88, v106
s_cbranch_vccnz DF0_4
EF0_4:
s_mov_b32 m0, s58
s_branch BF0_4
CF0_5:
s_mov_b32 s58, m0
s_mov_b32 m0, 142336
DF0_5:
s_cmp_eq_u32 s61, 0
s_cbranch_scc1 EF0_5
s_sub_u32 s61, s61, 1
buffer_load_dword v87, s[36:39], 0 offen sc1 lds
s_sleep 1
ds_read_b32 v106, v82
s_waitcnt lgkmcnt(0)
v_cmp_gt_i32 vcc, v88, v106
s_cbranch_vccnz DF0_5
EF0_5:
s_mov_b32 m0, s58
s_branch BF0_5
CF0_6:
s_mov_b32 s58, m0
s_mov_b32 m0, 142336
DF0_6:
s_cmp_eq_u32 s61, 0
s_cbranch_scc1 EF0_6
s_sub_u32 s61, s61, 1
buffer_load_dword v87, s[36:39], 0 offen sc1 lds
s_sleep 1
ds_read_b32 v106, v82
s_waitcnt lgkmcnt(0)
v_cmp_gt_i32 vcc, v88, v106
s_cbranch_vccnz DF0_6
EF0_6:
s_mov_b32 m0, s58
s_branch BF0_6
CF0_8:
s_mov_b32 s58, m0
s_mov_b32 m0, 142336
DF0_8:
s_cmp_eq_u32 s61, 0
s_cbranch_scc1 EF0_8
s_sub_u32 s61, s61, 1
buffer_load_dword v87, s[36:39], 0 offen sc1 lds
s_sleep 1
ds_read_b32 v106, v82
s_waitcnt lgkmcnt(0)
v_cmp_gt_i32 vcc, v88, v106
s_cbranch_vccnz DF0_8
EF0_8:
s_mov_b32 m0, s58
s_branch BF0_8
CF0_9:
s_mov_b32 s58, m0
s_mov_b32 m0, 142336
DF0_9:
s_cmp_eq_u32 s61, 0
s_cbranch_scc1 EF0_9
s_sub_u32 s61, s61, 1
buffer_load_dword v87, s[36:39], 0 offen sc1 lds
s_sleep 1
ds_read_b32 v106, v82
s_waitcnt lgkmcnt(0)
v_cmp_gt_i32 vcc, v88, v106
s_cbranch_vccnz DF0_9
EF0_9:
s_mov_b32 m0, s58
s_branch BF0_9
CF0_10:
s_mov_b32 s58, m0
s_mov_b32 m0, 142336
DF0_10:
s_cmp_eq_u32 s61, 0
s_cbranch_scc1 EF0_10
s_sub_u32 s61, s61, 1
buffer_load_dword v87, s[36:39], 0 offen sc1 lds
s_sleep 1
ds_read_b32 v106, v82
s_waitcnt lgkmcnt(0)
v_cmp_gt_i32 vcc, v88, v106
s_cbranch_vccnz DF0_10
EF0_10:
s_mov_b32 m0, s58
s_branch BF0_10
LvF1:
s_add_u32 s49, s49, 346112
s_barrier
s_add_u32 m0, s50, 0
s_nop 0
D 85,0
D 85,1024
D 85,2048
D 85,3072
s_add_u32 m0, s50, 4096
s_nop 0
D 86,0
D 86,1024
D 86,2048
D 86,3072
s_add_u32 s49, s49, 346112
s_waitcnt vmcnt(0)
s_barrier
R 24,0
buffer_load_dwordx2 v[108:109], v90, s[36:39], s49 offen sc1
buffer_load_dwordx2 v[110:111], v91, s[36:39], s49 offen sc1
s_add_u32 m0, s50, 32768
R 28,1024
D 85,0
R 32,2048
D 85,1024
R 36,3072
s_waitcnt lgkmcnt(2)
v_mfma_f32_16x16x32_bf16 v[0:3], a[0:3], v[24:27], 0
R 40,4096
D 85,2048
v_mfma_f32_16x16x32_bf16 v[4:7], a[4:7], v[24:27], 0
v_mfma_f32_16x16x32_bf16 v[8:11], a[8:11], v[24:27], 0
mav 0,12,28
R 44,5120
mav 4,16,28
D 85,3072
mav 8,20,28
s_waitcnt lgkmcnt(2)
mav 0,24,32
R 48,6144
mav 4,28,32
s_add_u32 m0, s50, 36864
mav 8,32,32
mav 0,36,36
R 52,7168
D 86,0
mav 4,40,36
mav 8,44,36
s_waitcnt lgkmcnt(2)
mav 0,48,40
R 24,8192
mav 4,52,40
D 86,1024
mav 8,56,40
mav 0,60,44
R 28,9216
mav 4,64,44
mav 8,68,44
s_waitcnt lgkmcnt(2)
mav 0,72,48
R 32,10240
D 86,2048
mav 4,76,48
mav 8,80,48
mav 0,84,52
R 36,11264
mav 4,88,52
D 86,3072
mav 8,92,52
s_waitcnt lgkmcnt(2)
mav 0,96,24
R 40,12288
mav 4,100,24
mav 8,104,24
mav 0,108,28
R 44,13312
mav 4,112,28
mav 8,116,28
s_waitcnt lgkmcnt(2)
mav 0,120,32
R 48,14336
mav 4,124,32
mav 8,128,32
mav 0,132,36
R 52,15360
mav 4,136,36
mav 8,140,36
s_waitcnt lgkmcnt(2)
mav 0,144,40
R 24,16384
mav 4,148,40
mav 8,152,40
mav 0,156,44
R 28,17408
mav 4,160,44
mav 8,164,44
s_waitcnt lgkmcnt(2)
mav 0,168,48
R 32,18432
mav 4,172,48
mav 8,176,48
mav 0,180,52
R 36,19456
mav 4,184,52
mav 8,188,52
s_waitcnt lgkmcnt(2)
mav 0,192,24
R 40,20480
mav 4,196,24
mav 8,200,24
s_waitcnt vmcnt(8)
v_add_f32 v108, v108, v110
mav 0,204,28
R 44,21504
v_add_f32 v109, v109, v111
mav 4,208,28
mav 8,212,28
v_permlane32_swap_b32 v108, v109
s_waitcnt lgkmcnt(2)
mav 0,216,32
R 48,22528
v_add_f32 v110, v108, v109
mav 4,220,32
v_add_f32 v111, v108, v109
mav 8,224,32
mav 0,228,36
R 52,23552
v_permlane16_swap_b32 v110, v111
mav 4,232,36
v_add_f32 v108, v110, v111
v_add_f32 v109, v110, v111
mav 8,236,36
s_waitcnt lgkmcnt(2)
mav 0,240,40
R 24,24576
v_permlane32_swap_b32 v108, v109
mav 4,244,40
v_mul_f32 v110, 0x3a800000, v108
v_mul_f32 v111, 0x3a800000, v109
mav 8,248,40
v_fma_f32 v111, -v110, v110, v111
v_max_f32 v111, 0, v111
mav 0,252,44
R 28,25600
v_add_f32 v111, 0x3727c5ac, v111
mvv 4,128,44
v_rsq_f32 v108, v111
mvv 8,132,44
v_mul_f32_e64 v109, -v110, v108
s_waitcnt lgkmcnt(2)
mvv 0,136,48
R 32,26624
mvv 4,140,48
ds_write_b64 v89, v[108:109] offset:0
mvv 8,144,48
mvv 0,148,52
R 36,27648
mvv 4,152,52
mvv 8,156,52
s_waitcnt lgkmcnt(3)
mvv 0,160,24
R 40,28672
mvv 4,164,24
mvv 8,168,24
mvv 0,172,28
R 44,29696
mvv 4,176,28
mvv 8,180,28
s_waitcnt lgkmcnt(2)
mvv 0,184,32
R 48,30720
s_xor_b32 s50, s50, 0x10000
mvv 4,188,32
mvv 8,192,32
mvv 0,196,36
R 52,31744
mvv 4,200,36
s_add_u32 s49, s49, 346112
mvv 8,204,36
s_waitcnt vmcnt(0)
s_barrier
s_waitcnt lgkmcnt(2)
mvv 0,208,40
R 24,32768
buffer_load_dwordx2 v[108:109], v90, s[36:39], s49 offen sc1
mvv 4,212,40
buffer_load_dwordx2 v[110:111], v91, s[36:39], s49 offen sc1
s_add_u32 m0, s50, 0
mvv 8,216,40
ds_read_b64 v[78:79], v89 offset:0
mvv 0,220,44
R 28,33792
D 85,0
mvv 4,224,44
mvv 8,228,44
s_waitcnt lgkmcnt(3)
mvv 0,232,48
R 32,34816
mvv 4,236,48
D 85,1024
mvv 8,240,48
mvv 0,244,52
R 36,35840
mvv 4,248,52
mvv 8,252,52
s_waitcnt lgkmcnt(2)
v_mfma_f32_16x16x32_bf16 v[12:15], a[0:3], v[24:27], 0
R 40,36864
D 85,2048
v_mfma_f32_16x16x32_bf16 v[16:19], a[4:7], v[24:27], 0
v_fma_f32 v104, v79, v56, v92
v_fma_f32 v0, v0, v78, v104
v_mfma_f32_16x16x32_bf16 v[20:23], a[8:11], v[24:27], 0
v_fma_f32 v105, v79, v57, v93
v_fma_f32 v1, v1, v78, v105
mav 12,12,28
R 44,37888
v_mul_f32_e64 v0, v0, |v0|
mav 16,16,28
D 85,3072
v_mul_f32_e64 v1, v1, |v1|
mav 20,20,28
v_cvt_pk_bf16_f32 v0, v0, v1
s_waitcnt lgkmcnt(2)
mav 12,24,32
R 48,38912
v_pk_max_i16 v68, v0, 0
mav 16,28,32
s_add_u32 m0, s50, 4096
v_fma_f32 v104, v79, v58, v94
mav 20,32,32
v_fma_f32 v2, v2, v78, v104
v_fma_f32 v105, v79, v59, v95
mav 12,36,36
R 52,39936
D 86,0
mav 16,40,36
v_fma_f32 v3, v3, v78, v105
v_mul_f32_e64 v2, v2, |v2|
mav 20,44,36
v_mul_f32_e64 v3, v3, |v3|
s_waitcnt lgkmcnt(2)
mav 12,48,40
R 24,40960
v_cvt_pk_bf16_f32 v2, v2, v3
mav 16,52,40
D 86,1024
v_pk_max_i16 v69, v2, 0
mav 20,56,40
v_fma_f32 v104, v79, v60, v96
v_fma_f32 v4, v4, v78, v104
mav 12,60,44
R 28,41984
v_fma_f32 v105, v79, v61, v97
mav 16,64,44
v_fma_f32 v5, v5, v78, v105
v_mul_f32_e64 v4, v4, |v4|
mav 20,68,44
)"
R"(
v_mul_f32_e64 v5, v5, |v5|
s_waitcnt lgkmcnt(2)
mav 12,72,48
R 32,43008
D 86,2048
mav 16,76,48
v_cvt_pk_bf16_f32 v4, v4, v5
v_pk_max_i16 v70, v4, 0
mav 20,80,48
v_fma_f32 v104, v79, v62, v98
v_fma_f32 v6, v6, v78, v104
mav 12,84,52
R 36,44032
v_fma_f32 v105, v79, v63, v99
mav 16,88,52
D 86,3072
v_fma_f32 v7, v7, v78, v105
mav 20,92,52
v_mul_f32_e64 v6, v6, |v6|
s_waitcnt lgkmcnt(2)
mav 12,96,24
R 40,45056
v_mul_f32_e64 v7, v7, |v7|
mav 16,100,24
v_cvt_pk_bf16_f32 v6, v6, v7
v_pk_max_i16 v71, v6, 0
mav 20,104,24
buffer_store_dwordx4 v[68:71], v83, s[36:39], s49 offen sc1
v_fma_f32 v104, v79, v64, v100
mav 12,108,28
R 44,46080
v_fma_f32 v8, v8, v78, v104
mav 16,112,28
v_fma_f32 v105, v79, v65, v101
v_fma_f32 v9, v9, v78, v105
mav 20,116,28
v_mul_f32_e64 v8, v8, |v8|
s_waitcnt lgkmcnt(2)
mav 12,120,32
R 48,47104
v_mul_f32_e64 v9, v9, |v9|
mav 16,124,32
v_cvt_pk_bf16_f32 v8, v8, v9
v_pk_max_i16 v72, v8, 0
mav 20,128,32
v_pk_max_i16 v74, v8, 0
v_fma_f32 v104, v79, v66, v102
mav 12,132,36
R 52,48128
v_fma_f32 v10, v10, v78, v104
mav 16,136,36
v_fma_f32 v105, v79, v67, v103
v_fma_f32 v11, v11, v78, v105
mav 20,140,36
v_mul_f32_e64 v10, v10, |v10|
s_waitcnt lgkmcnt(2)
mav 12,144,40
R 24,49152
v_mul_f32_e64 v11, v11, |v11|
mav 16,148,40
v_cvt_pk_bf16_f32 v10, v10, v11
v_pk_max_i16 v73, v10, 0
mav 20,152,40
v_pk_max_i16 v75, v10, 0
v_permlane16_swap_b32 v72, v74
mav 12,156,44
R 28,50176
v_permlane16_swap_b32 v73, v75
mav 16,160,44
buffer_store_dwordx4 v[72:75], v84, s[36:39], s49 offen sc1
mav 20,164,44
s_waitcnt lgkmcnt(2)
mav 12,168,48
R 32,51200
mav 16,172,48
mav 20,176,48
mav 12,180,52
R 36,52224
mav 16,184,52
mav 20,188,52
s_waitcnt lgkmcnt(2)
mav 12,192,24
R 40,53248
mav 16,196,24
mav 20,200,24
s_waitcnt vmcnt(10)
v_add_f32 v108, v108, v110
mav 12,204,28
R 44,54272
v_add_f32 v109, v109, v111
mav 16,208,28
mav 20,212,28
v_permlane32_swap_b32 v108, v109
s_waitcnt lgkmcnt(2)
mav 12,216,32
R 48,55296
v_add_f32 v110, v108, v109
mav 16,220,32
v_add_f32 v111, v108, v109
mav 20,224,32
mav 12,228,36
R 52,56320
v_permlane16_swap_b32 v110, v111
mav 16,232,36
v_add_f32 v108, v110, v111
v_add_f32 v109, v110, v111
mav 20,236,36
s_waitcnt lgkmcnt(2)
mav 12,240,40
R 24,57344
v_permlane32_swap_b32 v108, v109
mav 16,244,40
v_mul_f32 v110, 0x3a800000, v108
v_mul_f32 v111, 0x3a800000, v109
mav 20,248,40
v_fma_f32 v111, -v110, v110, v111
v_max_f32 v111, 0, v111
mav 12,252,44
R 28,58368
v_add_f32 v111, 0x3727c5ac, v111
mvv 16,128,44
v_rsq_f32 v108, v111
mvv 20,132,44
v_mul_f32_e64 v109, -v110, v108
s_waitcnt lgkmcnt(2)
mvv 12,136,48
R 32,59392
mvv 16,140,48
ds_write_b64 v89, v[108:109] offset:128
mvv 20,144,48
mvv 12,148,52
R 36,60416
mvv 16,152,52
mvv 20,156,52
s_waitcnt lgkmcnt(3)
mvv 12,160,24
R 40,61440
mvv 16,164,24
mvv 20,168,24
mvv 12,172,28
R 44,62464
mvv 16,176,28
mvv 20,180,28
s_waitcnt lgkmcnt(2)
mvv 12,184,32
R 48,63488
mvv 16,188,32
mvv 20,192,32
mvv 12,196,36
R 52,64512
v_xor_b32 v80, 0x10000, v80
mvv 16,200,36
s_add_u32 s49, s49, 346112
mvv 20,204,36
s_waitcnt vmcnt(2)
s_barrier
s_waitcnt lgkmcnt(2)
mvv 12,208,40
R 24,0
buffer_load_dwordx2 v[108:109], v90, s[36:39], s49 offen sc1
mvv 16,212,40
buffer_load_dwordx2 v[110:111], v91, s[36:39], s49 offen sc1
s_add_u32 m0, s50, 32768
mvv 20,216,40
ds_read_b64 v[78:79], v89 offset:128
mvv 12,220,44
R 28,1024
D 85,0
mvv 16,224,44
mvv 20,228,44
s_waitcnt lgkmcnt(3)
mvv 12,232,48
R 32,2048
mvv 16,236,48
D 85,1024
mvv 20,240,48
mvv 12,244,52
R 36,3072
mvv 16,248,52
mvv 20,252,52
s_waitcnt lgkmcnt(2)
v_mfma_f32_16x16x32_bf16 v[0:3], a[0:3], v[24:27], 0
R 40,4096
D 85,2048
v_mfma_f32_16x16x32_bf16 v[4:7], a[4:7], v[24:27], 0
v_fma_f32 v104, v79, v56, v92
v_fma_f32 v12, v12, v78, v104
v_mfma_f32_16x16x32_bf16 v[8:11], a[8:11], v[24:27], 0
v_fma_f32 v105, v79, v57, v93
v_fma_f32 v13, v13, v78, v105
mav 0,12,28
R 44,5120
v_mul_f32_e64 v12, v12, |v12|
mav 4,16,28
D 85,3072
v_mul_f32_e64 v13, v13, |v13|
mav 8,20,28
v_cvt_pk_bf16_f32 v12, v12, v13
s_waitcnt lgkmcnt(2)
mav 0,24,32
R 48,6144
v_pk_max_i16 v68, v12, 0
mav 4,28,32
s_add_u32 m0, s50, 36864
v_fma_f32 v104, v79, v58, v94
mav 8,32,32
v_fma_f32 v14, v14, v78, v104
v_fma_f32 v105, v79, v59, v95
mav 0,36,36
R 52,7168
D 86,0
mav 4,40,36
v_fma_f32 v15, v15, v78, v105
v_mul_f32_e64 v14, v14, |v14|
mav 8,44,36
v_mul_f32_e64 v15, v15, |v15|
s_waitcnt lgkmcnt(2)
mav 0,48,40
R 24,8192
v_cvt_pk_bf16_f32 v14, v14, v15
mav 4,52,40
D 86,1024
v_pk_max_i16 v69, v14, 0
mav 8,56,40
v_fma_f32 v104, v79, v60, v96
v_fma_f32 v16, v16, v78, v104
mav 0,60,44
R 28,9216
v_fma_f32 v105, v79, v61, v97
mav 4,64,44
v_fma_f32 v17, v17, v78, v105
v_mul_f32_e64 v16, v16, |v16|
mav 8,68,44
v_mul_f32_e64 v17, v17, |v17|
s_waitcnt lgkmcnt(2)
mav 0,72,48
R 32,10240
D 86,2048
mav 4,76,48
v_cvt_pk_bf16_f32 v16, v16, v17
v_pk_max_i16 v70, v16, 0
mav 8,80,48
v_fma_f32 v104, v79, v62, v98
v_fma_f32 v18, v18, v78, v104
mav 0,84,52
R 36,11264
v_fma_f32 v105, v79, v63, v99
mav 4,88,52
D 86,3072
v_fma_f32 v19, v19, v78, v105
mav 8,92,52
v_mul_f32_e64 v18, v18, |v18|
s_waitcnt lgkmcnt(2)
mav 0,96,24
R 40,12288
v_mul_f32_e64 v19, v19, |v19|
mav 4,100,24
v_cvt_pk_bf16_f32 v18, v18, v19
v_pk_max_i16 v71, v18, 0
mav 8,104,24
buffer_store_dwordx4 v[68:71], v83, s[36:39], s49 offen sc1
v_fma_f32 v104, v79, v64, v100
mav 0,108,28
R 44,13312
v_fma_f32 v20, v20, v78, v104
mav 4,112,28
v_fma_f32 v105, v79, v65, v101
v_fma_f32 v21, v21, v78, v105
mav 8,116,28
v_mul_f32_e64 v20, v20, |v20|
s_waitcnt lgkmcnt(2)
mav 0,120,32
R 48,14336
v_mul_f32_e64 v21, v21, |v21|
mav 4,124,32
v_cvt_pk_bf16_f32 v20, v20, v21
v_pk_max_i16 v72, v20, 0
mav 8,128,32
v_pk_max_i16 v74, v20, 0
v_fma_f32 v104, v79, v66, v102
mav 0,132,36
R 52,15360
v_fma_f32 v22, v22, v78, v104
mav 4,136,36
v_fma_f32 v105, v79, v67, v103
v_fma_f32 v23, v23, v78, v105
mav 8,140,36
v_mul_f32_e64 v22, v22, |v22|
s_waitcnt lgkmcnt(2)
mav 0,144,40
R 24,16384
v_mul_f32_e64 v23, v23, |v23|
mav 4,148,40
v_cvt_pk_bf16_f32 v22, v22, v23
v_pk_max_i16 v73, v22, 0
mav 8,152,40
v_pk_max_i16 v75, v22, 0
v_permlane16_swap_b32 v72, v74
mav 0,156,44
R 28,17408
v_permlane16_swap_b32 v73, v75
mav 4,160,44
buffer_store_dwordx4 v[72:75], v84, s[36:39], s49 offen sc1
v_add_u32 v76, 1, v76
mav 8,164,44
s_waitcnt vmcnt(12)
buffer_store_dwordx2 v[76:77], v81, s[36:39], s52 offen sc1
s_waitcnt lgkmcnt(2)
mav 0,168,48
R 32,18432
mav 4,172,48
mav 8,176,48
mav 0,180,52
R 36,19456
mav 4,184,52
mav 8,188,52
s_waitcnt lgkmcnt(2)
mav 0,192,24
R 40,20480
mav 4,196,24
mav 8,200,24
s_waitcnt vmcnt(11)
v_add_f32 v108, v108, v110
mav 0,204,28
R 44,21504
v_add_f32 v109, v109, v111
mav 4,208,28
mav 8,212,28
v_permlane32_swap_b32 v108, v109
s_waitcnt lgkmcnt(2)
mav 0,216,32
R 48,22528
v_add_f32 v110, v108, v109
mav 4,220,32
v_add_f32 v111, v108, v109
mav 8,224,32
mav 0,228,36
R 52,23552
v_permlane16_swap_b32 v110, v111
mav 4,232,36
v_add_f32 v108, v110, v111
v_add_f32 v109, v110, v111
mav 8,236,36
s_waitcnt lgkmcnt(2)
mav 0,240,40
R 24,24576
v_permlane32_swap_b32 v108, v109
mav 4,244,40
v_mul_f32 v110, 0x3a800000, v108
v_mul_f32 v111, 0x3a800000, v109
mav 8,248,40
v_fma_f32 v111, -v110, v110, v111
v_max_f32 v111, 0, v111
mav 0,252,44
R 28,25600
v_add_f32 v111, 0x3727c5ac, v111
mvv 4,128,44
v_rsq_f32 v108, v111
mvv 8,132,44
v_mul_f32_e64 v109, -v110, v108
s_waitcnt lgkmcnt(2)
mvv 0,136,48
R 32,26624
mvv 4,140,48
ds_write_b64 v89, v[108:109] offset:0
mvv 8,144,48
mvv 0,148,52
R 36,27648
mvv 4,152,52
mvv 8,156,52
s_waitcnt lgkmcnt(3)
mvv 0,160,24
R 40,28672
mvv 4,164,24
mvv 8,168,24
mvv 0,172,28
R 44,29696
mvv 4,176,28
mvv 8,180,28
s_waitcnt lgkmcnt(2)
mvv 0,184,32
R 48,30720
s_xor_b32 s50, s50, 0x10000
mvv 4,188,32
mvv 8,192,32
mvv 0,196,36
R 52,31744
mvv 4,200,36
s_add_u32 s49, s49, 346112
mvv 8,204,36
s_waitcnt vmcnt(3)
s_barrier
s_waitcnt lgkmcnt(2)
mvv 0,208,40
R 24,32768
buffer_load_dwordx2 v[108:109], v90, s[36:39], s49 offen sc1
mvv 4,212,40
buffer_load_dwordx2 v[110:111], v91, s[36:39], s49 offen sc1
s_add_u32 m0, s50, 0
mvv 8,216,40
ds_read_b64 v[78:79], v89 offset:0
mvv 0,220,44
R 28,33792
D 85,0
mvv 4,224,44
mvv 8,228,44
s_waitcnt lgkmcnt(3)
mvv 0,232,48
R 32,34816
mvv 4,236,48
D 85,1024
mvv 8,240,48
mvv 0,244,52
R 36,35840
mvv 4,248,52
mvv 8,252,52
s_waitcnt lgkmcnt(2)
v_mfma_f32_16x16x32_bf16 v[12:15], a[0:3], v[24:27], 0
R 40,36864
D 85,2048
v_mfma_f32_16x16x32_bf16 v[16:19], a[4:7], v[24:27], 0
v_fma_f32 v104, v79, v56, v92
v_fma_f32 v0, v0, v78, v104
v_mfma_f32_16x16x32_bf16 v[20:23], a[8:11], v[24:27], 0
v_fma_f32 v105, v79, v57, v93
v_fma_f32 v1, v1, v78, v105
mav 12,12,28
R 44,37888
v_mul_f32_e64 v0, v0, |v0|
mav 16,16,28
D 85,3072
v_mul_f32_e64 v1, v1, |v1|
mav 20,20,28
v_cvt_pk_bf16_f32 v0, v0, v1
s_waitcnt lgkmcnt(2)
mav 12,24,32
R 48,38912
v_pk_max_i16 v68, v0, 0
mav 16,28,32
s_add_u32 m0, s50, 4096
v_fma_f32 v104, v79, v58, v94
mav 20,32,32
v_fma_f32 v2, v2, v78, v104
v_fma_f32 v105, v79, v59, v95
mav 12,36,36
R 52,39936
D 86,0
mav 16,40,36
v_fma_f32 v3, v3, v78, v105
v_mul_f32_e64 v2, v2, |v2|
mav 20,44,36
v_mul_f32_e64 v3, v3, |v3|
s_waitcnt lgkmcnt(2)
mav 12,48,40
R 24,40960
v_cvt_pk_bf16_f32 v2, v2, v3
mav 16,52,40
D 86,1024
v_pk_max_i16 v69, v2, 0
mav 20,56,40
v_fma_f32 v104, v79, v60, v96
v_fma_f32 v4, v4, v78, v104
mav 12,60,44
R 28,41984
v_fma_f32 v105, v79, v61, v97
mav 16,64,44
v_fma_f32 v5, v5, v78, v105
v_mul_f32_e64 v4, v4, |v4|
mav 20,68,44
v_mul_f32_e64 v5, v5, |v5|
s_waitcnt lgkmcnt(2)
mav 12,72,48
R 32,43008
D 86,2048
mav 16,76,48
v_cvt_pk_bf16_f32 v4, v4, v5
v_pk_max_i16 v70, v4, 0
mav 20,80,48
v_fma_f32 v104, v79, v62, v98
v_fma_f32 v6, v6, v78, v104
mav 12,84,52
R 36,44032
v_fma_f32 v105, v79, v63, v99
mav 16,88,52
D 86,3072
v_fma_f32 v7, v7, v78, v105
mav 20,92,52
v_mul_f32_e64 v6, v6, |v6|
s_waitcnt lgkmcnt(2)
mav 12,96,24
R 40,45056
v_mul_f32_e64 v7, v7, |v7|
mav 16,100,24
v_cvt_pk_bf16_f32 v6, v6, v7
v_pk_max_i16 v71, v6, 0
mav 20,104,24
buffer_store_dwordx4 v[68:71], v83, s[36:39], s49 offen sc1
v_fma_f32 v104, v79, v64, v100
mav 12,108,28
R 44,46080
v_fma_f32 v8, v8, v78, v104
mav 16,112,28
v_fma_f32 v105, v79, v65, v101
v_fma_f32 v9, v9, v78, v105
mav 20,116,28
v_mul_f32_e64 v8, v8, |v8|
s_waitcnt lgkmcnt(2)
mav 12,120,32
R 48,47104
v_mul_f32_e64 v9, v9, |v9|
mav 16,124,32
v_cvt_pk_bf16_f32 v8, v8, v9
v_pk_max_i16 v72, v8, 0
mav 20,128,32
v_pk_max_i16 v74, v8, 0
v_fma_f32 v104, v79, v66, v102
mav 12,132,36
R 52,48128
v_fma_f32 v10, v10, v78, v104
mav 16,136,36
v_fma_f32 v105, v79, v67, v103
v_fma_f32 v11, v11, v78, v105
mav 20,140,36
v_mul_f32_e64 v10, v10, |v10|
s_waitcnt lgkmcnt(2)
mav 12,144,40
R 24,49152
v_mul_f32_e64 v11, v11, |v11|
mav 16,148,40
v_cvt_pk_bf16_f32 v10, v10, v11
v_pk_max_i16 v73, v10, 0
mav 20,152,40
v_pk_max_i16 v75, v10, 0
v_permlane16_swap_b32 v72, v74
mav 12,156,44
R 28,50176
v_permlane16_swap_b32 v73, v75
mav 16,160,44
buffer_store_dwordx4 v[72:75], v84, s[36:39], s49 offen sc1
v_add_u32 v76, 1, v76
mav 20,164,44
s_waitcnt vmcnt(13)
buffer_store_dwordx2 v[76:77], v81, s[36:39], s52 offen sc1
s_waitcnt lgkmcnt(2)
mav 12,168,48
R 32,51200
mav 16,172,48
mav 20,176,48
mav 12,180,52
R 36,52224
mav 16,184,52
mav 20,188,52
s_waitcnt lgkmcnt(2)
mav 12,192,24
R 40,53248
mav 16,196,24
mav 20,200,24
s_waitcnt vmcnt(11)
v_add_f32 v108, v108, v110
mav 12,204,28
R 44,54272
v_add_f32 v109, v109, v111
mav 16,208,28
mav 20,212,28
v_permlane32_swap_b32 v108, v109
s_waitcnt lgkmcnt(2)
mav 12,216,32
R 48,55296
v_add_f32 v110, v108, v109
mav 16,220,32
v_add_f32 v111, v108, v109
mav 20,224,32
mav 12,228,36
R 52,56320
v_permlane16_swap_b32 v110, v111
mav 16,232,36
v_add_f32 v108, v110, v111
v_add_f32 v109, v110, v111
mav 20,236,36
s_waitcnt lgkmcnt(2)
mav 12,240,40
R 24,57344
v_permlane32_swap_b32 v108, v109
mav 16,244,40
v_mul_f32 v110, 0x3a800000, v108
v_mul_f32 v111, 0x3a800000, v109
mav 20,248,40
v_fma_f32 v111, -v110, v110, v111
v_max_f32 v111, 0, v111
mav 12,252,44
R 28,58368
v_add_f32 v111, 0x3727c5ac, v111
mvv 16,128,44
v_rsq_f32 v108, v111
mvv 20,132,44
v_mul_f32_e64 v109, -v110, v108
s_waitcnt lgkmcnt(2)
mvv 12,136,48
R 32,59392
mvv 16,140,48
ds_write_b64 v89, v[108:109] offset:128
mvv 20,144,48
mvv 12,148,52
R 36,60416
mvv 16,152,52
mvv 20,156,52
s_waitcnt lgkmcnt(3)
mvv 12,160,24
R 40,61440
mvv 16,164,24
mvv 20,168,24
mvv 12,172,28
R 44,62464
mvv 16,176,28
mvv 20,180,28
s_waitcnt lgkmcnt(2)
mvv 12,184,32
R 48,63488
mvv 16,188,32
mvv 20,192,32
mvv 12,196,36
R 52,64512
v_xor_b32 v80, 0x10000, v80
mvv 16,200,36
s_add_u32 s49, s49, 346112
mvv 20,204,36
s_waitcnt vmcnt(3)
s_barrier
s_waitcnt lgkmcnt(2)
mvv 12,208,40
R 24,0
buffer_load_dwordx2 v[108:109], v90, s[36:39], s49 offen sc1
mvv 16,212,40
buffer_load_dwordx2 v[110:111], v91, s[36:39], s49 offen sc1
s_add_u32 m0, s50, 32768
mvv 20,216,40
ds_read_b64 v[78:79], v89 offset:128
mvv 12,220,44
R 28,1024
D 85,0
mvv 16,224,44
mvv 20,228,44
s_waitcnt lgkmcnt(3)
mvv 12,232,48
R 32,2048
mvv 16,236,48
D 85,1024
mvv 20,240,48
mvv 12,244,52
R 36,3072
mvv 16,248,52
mvv 20,252,52
s_mov_b32 s60, -29
.p2align 6
LF1_1:
s_waitcnt lgkmcnt(2)
v_mfma_f32_16x16x32_bf16 v[0:3], a[0:3], v[24:27], 0
R 40,4096
D 85,2048
v_mfma_f32_16x16x32_bf16 v[4:7], a[4:7], v[24:27], 0
v_fma_f32 v104, v79, v56, v92
v_fma_f32 v12, v12, v78, v104
v_mfma_f32_16x16x32_bf16 v[8:11], a[8:11], v[24:27], 0
v_fma_f32 v105, v79, v57, v93
v_fma_f32 v13, v13, v78, v105
mav 0,12,28
R 44,5120
v_mul_f32_e64 v12, v12, |v12|
mav 4,16,28
D 85,3072
v_mul_f32_e64 v13, v13, |v13|
mav 8,20,28
v_cvt_pk_bf16_f32 v12, v12, v13
s_waitcnt lgkmcnt(2)
mav 0,24,32
R 48,6144
v_pk_max_i16 v68, v12, 0
mav 4,28,32
s_add_u32 m0, s50, 36864
v_fma_f32 v104, v79, v58, v94
mav 8,32,32
v_fma_f32 v14, v14, v78, v104
v_fma_f32 v105, v79, v59, v95
mav 0,36,36
R 52,7168
D 86,0
mav 4,40,36
v_fma_f32 v15, v15, v78, v105
v_mul_f32_e64 v14, v14, |v14|
mav 8,44,36
v_mul_f32_e64 v15, v15, |v15|
s_waitcnt lgkmcnt(2)
mav 0,48,40
R 24,8192
v_cvt_pk_bf16_f32 v14, v14, v15
mav 4,52,40
D 86,1024
v_pk_max_i16 v69, v14, 0
mav 8,56,40
v_fma_f32 v104, v79, v60, v96
v_fma_f32 v16, v16, v78, v104
mav 0,60,44
R 28,9216
v_fma_f32 v105, v79, v61, v97
mav 4,64,44
v_fma_f32 v17, v17, v78, v105
v_mul_f32_e64 v16, v16, |v16|
mav 8,68,44
v_mul_f32_e64 v17, v17, |v17|
s_waitcnt lgkmcnt(2)
mav 0,72,48
R 32,10240
D 86,2048
mav 4,76,48
v_cvt_pk_bf16_f32 v16, v16, v17
v_pk_max_i16 v70, v16, 0
mav 8,80,48
v_fma_f32 v104, v79, v62, v98
v_fma_f32 v18, v18, v78, v104
mav 0,84,52
R 36,11264
v_fma_f32 v105, v79, v63, v99
mav 4,88,52
D 86,3072
v_fma_f32 v19, v19, v78, v105
mav 8,92,52
v_mul_f32_e64 v18, v18, |v18|
s_waitcnt lgkmcnt(2)
mav 0,96,24
R 40,12288
v_mul_f32_e64 v19, v19, |v19|
mav 4,100,24
v_cvt_pk_bf16_f32 v18, v18, v19
v_pk_max_i16 v71, v18, 0
mav 8,104,24
buffer_store_dwordx4 v[68:71], v83, s[36:39], s49 offen sc1
v_fma_f32 v104, v79, v64, v100
mav 0,108,28
R 44,13312
v_fma_f32 v20, v20, v78, v104
mav 4,112,28
v_fma_f32 v105, v79, v65, v101
v_fma_f32 v21, v21, v78, v105
mav 8,116,28
v_mul_f32_e64 v20, v20, |v20|
s_waitcnt lgkmcnt(2)
mav 0,120,32
R 48,14336
v_mul_f32_e64 v21, v21, |v21|
mav 4,124,32
v_cvt_pk_bf16_f32 v20, v20, v21
v_pk_max_i16 v72, v20, 0
mav 8,128,32
v_pk_max_i16 v74, v20, 0
v_fma_f32 v104, v79, v66, v102
mav 0,132,36
R 52,15360
v_fma_f32 v22, v22, v78, v104
mav 4,136,36
v_fma_f32 v105, v79, v67, v103
v_fma_f32 v23, v23, v78, v105
mav 8,140,36
v_mul_f32_e64 v22, v22, |v22|
s_waitcnt lgkmcnt(2)
mav 0,144,40
R 24,16384
v_mul_f32_e64 v23, v23, |v23|
mav 4,148,40
v_cvt_pk_bf16_f32 v22, v22, v23
v_pk_max_i16 v73, v22, 0
mav 8,152,40
v_pk_max_i16 v75, v22, 0
v_permlane16_swap_b32 v72, v74
mav 0,156,44
R 28,17408
v_permlane16_swap_b32 v73, v75
mav 4,160,44
buffer_store_dwordx4 v[72:75], v84, s[36:39], s49 offen sc1
v_add_u32 v76, 1, v76
mav 8,164,44
s_waitcnt vmcnt(13)
buffer_store_dwordx2 v[76:77], v81, s[36:39], s52 offen sc1
s_waitcnt lgkmcnt(2)
mav 0,168,48
R 32,18432
mav 4,172,48
mav 8,176,48
mav 0,180,52
R 36,19456
mav 4,184,52
mav 8,188,52
s_waitcnt lgkmcnt(2)
mav 0,192,24
R 40,20480
mav 4,196,24
mav 8,200,24
s_waitcnt vmcnt(11)
v_add_f32 v108, v108, v110
mav 0,204,28
R 44,21504
v_add_f32 v109, v109, v111
mav 4,208,28
mav 8,212,28
v_permlane32_swap_b32 v108, v109
s_waitcnt lgkmcnt(2)
mav 0,216,32
R 48,22528
v_add_f32 v110, v108, v109
mav 4,220,32
v_add_f32 v111, v108, v109
mav 8,224,32
mav 0,228,36
R 52,23552
v_permlane16_swap_b32 v110, v111
mav 4,232,36
v_add_f32 v108, v110, v111
v_add_f32 v109, v110, v111
mav 8,236,36
s_waitcnt lgkmcnt(2)
mav 0,240,40
R 24,24576
v_permlane32_swap_b32 v108, v109
mav 4,244,40
v_mul_f32 v110, 0x3a800000, v108
v_mul_f32 v111, 0x3a800000, v109
mav 8,248,40
v_fma_f32 v111, -v110, v110, v111
v_max_f32 v111, 0, v111
mav 0,252,44
R 28,25600
v_add_f32 v111, 0x3727c5ac, v111
mvv 4,128,44
v_rsq_f32 v108, v111
mvv 8,132,44
v_mul_f32_e64 v109, -v110, v108
s_waitcnt lgkmcnt(2)
mvv 0,136,48
R 32,26624
mvv 4,140,48
ds_write_b64 v89, v[108:109] offset:0
mvv 8,144,48
mvv 0,148,52
R 36,27648
mvv 4,152,52
mvv 8,156,52
s_waitcnt lgkmcnt(3)
mvv 0,160,24
R 40,28672
mvv 4,164,24
mvv 8,168,24
mvv 0,172,28
R 44,29696
mvv 4,176,28
mvv 8,180,28
s_waitcnt lgkmcnt(2)
mvv 0,184,32
R 48,30720
s_xor_b32 s50, s50, 0x10000
mvv 4,188,32
mvv 8,192,32
mvv 0,196,36
R 52,31744
mvv 4,200,36
s_add_u32 s49, s49, 346112
mvv 8,204,36
s_waitcnt vmcnt(3)
s_barrier
s_waitcnt lgkmcnt(2)
mvv 0,208,40
R 24,32768
buffer_load_dwordx2 v[108:109], v90, s[36:39], s49 offen sc1
mvv 4,212,40
buffer_load_dwordx2 v[110:111], v91, s[36:39], s49 offen sc1
s_add_u32 m0, s50, 0
mvv 8,216,40
ds_read_b64 v[78:79], v89 offset:0
mvv 0,220,44
R 28,33792
D 85,0
mvv 4,224,44
mvv 8,228,44
s_waitcnt lgkmcnt(3)
mvv 0,232,48
R 32,34816
mvv 4,236,48
D 85,1024
mvv 8,240,48
mvv 0,244,52
R 36,35840
mvv 4,248,52
mvv 8,252,52
s_waitcnt lgkmcnt(2)
v_mfma_f32_16x16x32_bf16 v[12:15], a[0:3], v[24:27], 0
R 40,36864
D 85,2048
v_mfma_f32_16x16x32_bf16 v[16:19], a[4:7], v[24:27], 0
v_fma_f32 v104, v79, v56, v92
v_fma_f32 v0, v0, v78, v104
v_mfma_f32_16x16x32_bf16 v[20:23], a[8:11], v[24:27], 0
v_fma_f32 v105, v79, v57, v93
v_fma_f32 v1, v1, v78, v105
mav 12,12,28
R 44,37888
v_mul_f32_e64 v0, v0, |v0|
mav 16,16,28
D 85,3072
v_mul_f32_e64 v1, v1, |v1|
mav 20,20,28
v_cvt_pk_bf16_f32 v0, v0, v1
s_waitcnt lgkmcnt(2)
mav 12,24,32
R 48,38912
v_pk_max_i16 v68, v0, 0
mav 16,28,32
s_add_u32 m0, s50, 4096
v_fma_f32 v104, v79, v58, v94
mav 20,32,32
v_fma_f32 v2, v2, v78, v104
v_fma_f32 v105, v79, v59, v95
mav 12,36,36
R 52,39936
D 86,0
mav 16,40,36
v_fma_f32 v3, v3, v78, v105
v_mul_f32_e64 v2, v2, |v2|
mav 20,44,36
v_mul_f32_e64 v3, v3, |v3|
s_waitcnt lgkmcnt(2)
mav 12,48,40
R 24,40960
v_cvt_pk_bf16_f32 v2, v2, v3
mav 16,52,40
D 86,1024
v_pk_max_i16 v69, v2, 0
mav 20,56,40
v_fma_f32 v104, v79, v60, v96
v_fma_f32 v4, v4, v78, v104
mav 12,60,44
R 28,41984
v_fma_f32 v105, v79, v61, v97
mav 16,64,44
v_fma_f32 v5, v5, v78, v105
v_mul_f32_e64 v4, v4, |v4|
mav 20,68,44
v_mul_f32_e64 v5, v5, |v5|
s_waitcnt lgkmcnt(2)
mav 12,72,48
R 32,43008
D 86,2048
mav 16,76,48
v_cvt_pk_bf16_f32 v4, v4, v5
v_pk_max_i16 v70, v4, 0
mav 20,80,48
v_fma_f32 v104, v79, v62, v98
v_fma_f32 v6, v6, v78, v104
mav 12,84,52
R 36,44032
v_fma_f32 v105, v79, v63, v99
mav 16,88,52
D 86,3072
v_fma_f32 v7, v7, v78, v105
mav 20,92,52
v_mul_f32_e64 v6, v6, |v6|
s_waitcnt lgkmcnt(2)
mav 12,96,24
R 40,45056
v_mul_f32_e64 v7, v7, |v7|
mav 16,100,24
v_cvt_pk_bf16_f32 v6, v6, v7
v_pk_max_i16 v71, v6, 0
mav 20,104,24
buffer_store_dwordx4 v[68:71], v83, s[36:39], s49 offen sc1
v_fma_f32 v104, v79, v64, v100
mav 12,108,28
R 44,46080
v_fma_f32 v8, v8, v78, v104
mav 16,112,28
v_fma_f32 v105, v79, v65, v101
v_fma_f32 v9, v9, v78, v105
mav 20,116,28
v_mul_f32_e64 v8, v8, |v8|
s_waitcnt lgkmcnt(2)
mav 12,120,32
R 48,47104
v_mul_f32_e64 v9, v9, |v9|
mav 16,124,32
v_cvt_pk_bf16_f32 v8, v8, v9
v_pk_max_i16 v72, v8, 0
mav 20,128,32
v_pk_max_i16 v74, v8, 0
v_fma_f32 v104, v79, v66, v102
mav 12,132,36
R 52,48128
v_fma_f32 v10, v10, v78, v104
mav 16,136,36
v_fma_f32 v105, v79, v67, v103
v_fma_f32 v11, v11, v78, v105
mav 20,140,36
v_mul_f32_e64 v10, v10, |v10|
s_waitcnt lgkmcnt(2)
mav 12,144,40
R 24,49152
v_mul_f32_e64 v11, v11, |v11|
mav 16,148,40
v_cvt_pk_bf16_f32 v10, v10, v11
v_pk_max_i16 v73, v10, 0
mav 20,152,40
v_pk_max_i16 v75, v10, 0
v_permlane16_swap_b32 v72, v74
mav 12,156,44
R 28,50176
v_permlane16_swap_b32 v73, v75
mav 16,160,44
buffer_store_dwordx4 v[72:75], v84, s[36:39], s49 offen sc1
v_add_u32 v76, 1, v76
mav 20,164,44
s_waitcnt vmcnt(13)
buffer_store_dwordx2 v[76:77], v81, s[36:39], s52 offen sc1
s_waitcnt lgkmcnt(2)
mav 12,168,48
R 32,51200
mav 16,172,48
mav 20,176,48
mav 12,180,52
R 36,52224
mav 16,184,52
mav 20,188,52
s_waitcnt lgkmcnt(2)
mav 12,192,24
R 40,53248
mav 16,196,24
mav 20,200,24
s_waitcnt vmcnt(11)
v_add_f32 v108, v108, v110
mav 12,204,28
R 44,54272
v_add_f32 v109, v109, v111
mav 16,208,28
mav 20,212,28
v_permlane32_swap_b32 v108, v109
s_waitcnt lgkmcnt(2)
mav 12,216,32
R 48,55296
v_add_f32 v110, v108, v109
mav 16,220,32
v_add_f32 v111, v108, v109
mav 20,224,32
mav 12,228,36
R 52,56320
v_permlane16_swap_b32 v110, v111
mav 16,232,36
v_add_f32 v108, v110, v111
v_add_f32 v109, v110, v111
mav 20,236,36
s_waitcnt lgkmcnt(2)
mav 12,240,40
R 24,57344
v_permlane32_swap_b32 v108, v109
mav 16,244,40
v_mul_f32 v110, 0x3a800000, v108
v_mul_f32 v111, 0x3a800000, v109
mav 20,248,40
v_fma_f32 v111, -v110, v110, v111
v_max_f32 v111, 0, v111
mav 12,252,44
R 28,58368
v_add_f32 v111, 0x3727c5ac, v111
mvv 16,128,44
v_rsq_f32 v108, v111
mvv 20,132,44
v_mul_f32_e64 v109, -v110, v108
s_waitcnt lgkmcnt(2)
mvv 12,136,48
R 32,59392
mvv 16,140,48
ds_write_b64 v89, v[108:109] offset:128
mvv 20,144,48
mvv 12,148,52
R 36,60416
mvv 16,152,52
mvv 20,156,52
s_waitcnt lgkmcnt(3)
mvv 12,160,24
R 40,61440
mvv 16,164,24
mvv 20,168,24
mvv 12,172,28
R 44,62464
mvv 16,176,28
mvv 20,180,28
s_waitcnt lgkmcnt(2)
mvv 12,184,32
R 48,63488
mvv 16,188,32
mvv 20,192,32
mvv 12,196,36
R 52,64512
v_xor_b32 v80, 0x10000, v80
mvv 16,200,36
s_add_u32 s49, s49, 346112
mvv 20,204,36
s_waitcnt vmcnt(3)
s_barrier
s_waitcnt lgkmcnt(2)
mvv 12,208,40
R 24,0
buffer_load_dwordx2 v[108:109], v90, s[36:39], s49 offen sc1
mvv 16,212,40
buffer_load_dwordx2 v[110:111], v91, s[36:39], s49 offen sc1
s_add_u32 m0, s50, 32768
mvv 20,216,40
ds_read_b64 v[78:79], v89 offset:128
mvv 12,220,44
R 28,1024
D 85,0
mvv 16,224,44
mvv 20,228,44
s_waitcnt lgkmcnt(3)
mvv 12,232,48
R 32,2048
mvv 16,236,48
D 85,1024
mvv 20,240,48
mvv 12,244,52
R 36,3072
mvv 16,248,52
mvv 20,252,52
s_add_u32 s60, s60, 1
s_cbranch_scc0 LF1_1
s_waitcnt lgkmcnt(2)
v_mfma_f32_16x16x32_bf16 v[0:3], a[0:3], v[24:27], 0
R 40,4096
D 85,2048
v_mfma_f32_16x16x32_bf16 v[4:7], a[4:7], v[24:27], 0
v_fma_f32 v104, v79, v56, v92
v_fma_f32 v12, v12, v78, v104
v_mfma_f32_16x16x32_bf16 v[8:11], a[8:11], v[24:27], 0
v_fma_f32 v105, v79, v57, v93
v_fma_f32 v13, v13, v78, v105
mav 0,12,28
R 44,5120
v_mul_f32_e64 v12, v12, |v12|
mav 4,16,28
D 85,3072
v_mul_f32_e64 v13, v13, |v13|
mav 8,20,28
v_cvt_pk_bf16_f32 v12, v12, v13
s_waitcnt lgkmcnt(2)
mav 0,24,32
R 48,6144
v_pk_max_i16 v68, v12, 0
mav 4,28,32
s_add_u32 m0, s50, 36864
v_fma_f32 v104, v79, v58, v94
mav 8,32,32
v_fma_f32 v14, v14, v78, v104
v_fma_f32 v105, v79, v59, v95
mav 0,36,36
R 52,7168
D 86,0
mav 4,40,36
v_fma_f32 v15, v15, v78, v105
v_mul_f32_e64 v14, v14, |v14|
mav 8,44,36
v_mul_f32_e64 v15, v15, |v15|
s_waitcnt lgkmcnt(2)
mav 0,48,40
R 24,8192
v_cvt_pk_bf16_f32 v14, v14, v15
mav 4,52,40
D 86,1024
v_pk_max_i16 v69, v14, 0
mav 8,56,40
v_fma_f32 v104, v79, v60, v96
v_fma_f32 v16, v16, v78, v104
mav 0,60,44
R 28,9216
v_fma_f32 v105, v79, v61, v97
mav 4,64,44
v_fma_f32 v17, v17, v78, v105
v_mul_f32_e64 v16, v16, |v16|
mav 8,68,44
v_mul_f32_e64 v17, v17, |v17|
s_waitcnt lgkmcnt(2)
mav 0,72,48
R 32,10240
D 86,2048
mav 4,76,48
v_cvt_pk_bf16_f32 v16, v16, v17
v_pk_max_i16 v70, v16, 0
mav 8,80,48
v_fma_f32 v104, v79, v62, v98
v_fma_f32 v18, v18, v78, v104
mav 0,84,52
R 36,11264
v_fma_f32 v105, v79, v63, v99
mav 4,88,52
D 86,3072
v_fma_f32 v19, v19, v78, v105
mav 8,92,52
v_mul_f32_e64 v18, v18, |v18|
s_waitcnt lgkmcnt(2)
mav 0,96,24
R 40,12288
v_mul_f32_e64 v19, v19, |v19|
mav 4,100,24
v_cvt_pk_bf16_f32 v18, v18, v19
v_pk_max_i16 v71, v18, 0
mav 8,104,24
buffer_store_dwordx4 v[68:71], v83, s[36:39], s49 offen sc1
v_fma_f32 v104, v79, v64, v100
mav 0,108,28
R 44,13312
v_fma_f32 v20, v20, v78, v104
mav 4,112,28
v_fma_f32 v105, v79, v65, v101
v_fma_f32 v21, v21, v78, v105
mav 8,116,28
v_mul_f32_e64 v20, v20, |v20|
s_waitcnt lgkmcnt(2)
mav 0,120,32
R 48,14336
v_mul_f32_e64 v21, v21, |v21|
mav 4,124,32
v_cvt_pk_bf16_f32 v20, v20, v21
v_pk_max_i16 v72, v20, 0
mav 8,128,32
v_pk_max_i16 v74, v20, 0
v_fma_f32 v104, v79, v66, v102
mav 0,132,36
R 52,15360
v_fma_f32 v22, v22, v78, v104
mav 4,136,36
v_fma_f32 v105, v79, v67, v103
v_fma_f32 v23, v23, v78, v105
mav 8,140,36
v_mul_f32_e64 v22, v22, |v22|
s_waitcnt lgkmcnt(2)
mav 0,144,40
R 24,16384
v_mul_f32_e64 v23, v23, |v23|
mav 4,148,40
v_cvt_pk_bf16_f32 v22, v22, v23
v_pk_max_i16 v73, v22, 0
mav 8,152,40
v_pk_max_i16 v75, v22, 0
v_permlane16_swap_b32 v72, v74
mav 0,156,44
R 28,17408
v_permlane16_swap_b32 v73, v75
mav 4,160,44
buffer_store_dwordx4 v[72:75], v84, s[36:39], s49 offen sc1
v_add_u32 v76, 1, v76
mav 8,164,44
s_waitcnt vmcnt(13)
buffer_store_dwordx2 v[76:77], v81, s[36:39], s52 offen sc1
s_waitcnt lgkmcnt(2)
mav 0,168,48
R 32,18432
mav 4,172,48
mav 8,176,48
mav 0,180,52
R 36,19456
mav 4,184,52
mav 8,188,52
s_waitcnt lgkmcnt(2)
mav 0,192,24
R 40,20480
mav 4,196,24
mav 8,200,24
s_waitcnt vmcnt(11)
v_add_f32 v108, v108, v110
mav 0,204,28
R 44,21504
v_add_f32 v109, v109, v111
mav 4,208,28
mav 8,212,28
v_permlane32_swap_b32 v108, v109
s_waitcnt lgkmcnt(2)
mav 0,216,32
R 48,22528
v_add_f32 v110, v108, v109
mav 4,220,32
v_add_f32 v111, v108, v109
mav 8,224,32
mav 0,228,36
R 52,23552
v_permlane16_swap_b32 v110, v111
mav 4,232,36
v_add_f32 v108, v110, v111
v_add_f32 v109, v110, v111
mav 8,236,36
s_waitcnt lgkmcnt(2)
mav 0,240,40
R 24,24576
v_permlane32_swap_b32 v108, v109
mav 4,244,40
v_mul_f32 v110, 0x3a800000, v108
v_mul_f32 v111, 0x3a800000, v109
mav 8,248,40
v_fma_f32 v111, -v110, v110, v111
v_max_f32 v111, 0, v111
mav 0,252,44
R 28,25600
v_add_f32 v111, 0x3727c5ac, v111
mvv 4,128,44
v_rsq_f32 v108, v111
mvv 8,132,44
v_mul_f32_e64 v109, -v110, v108
s_waitcnt lgkmcnt(2)
mvv 0,136,48
R 32,26624
mvv 4,140,48
ds_write_b64 v89, v[108:109] offset:0
mvv 8,144,48
mvv 0,148,52
R 36,27648
mvv 4,152,52
mvv 8,156,52
s_waitcnt lgkmcnt(3)
mvv 0,160,24
R 40,28672
mvv 4,164,24
mvv 8,168,24
mvv 0,172,28
R 44,29696
mvv 4,176,28
mvv 8,180,28
s_waitcnt lgkmcnt(2)
mvv 0,184,32
R 48,30720
mvv 4,188,32
mvv 8,192,32
mvv 0,196,36
R 52,31744
mvv 4,200,36
s_add_u32 s49, s49, 346112
mvv 8,204,36
s_waitcnt vmcnt(3)
s_barrier
s_waitcnt lgkmcnt(2)
mvv 0,208,40
R 24,32768
buffer_load_dwordx2 v[108:109], v90, s[36:39], s49 offen sc1
mvv 4,212,40
buffer_load_dwordx2 v[110:111], v91, s[36:39], s49 offen sc1
ds_read_b64 v[78:79], v89 offset:0
mvv 8,216,40
mvv 0,220,44
R 28,33792
mvv 4,224,44
mvv 8,228,44
s_waitcnt lgkmcnt(3)
mvv 0,232,48
R 32,34816
mvv 4,236,48
mvv 8,240,48
mvv 0,244,52
R 36,35840
mvv 4,248,52
mvv 8,252,52
s_waitcnt lgkmcnt(2)
v_mfma_f32_16x16x32_bf16 v[12:15], a[0:3], v[24:27], 0
R 40,36864
v_fma_f32 v104, v79, v56, v92
v_mfma_f32_16x16x32_bf16 v[16:19], a[4:7], v[24:27], 0
v_fma_f32 v0, v0, v78, v104
v_fma_f32 v105, v79, v57, v93
v_mfma_f32_16x16x32_bf16 v[20:23], a[8:11], v[24:27], 0
v_fma_f32 v1, v1, v78, v105
v_mul_f32_e64 v0, v0, |v0|
mav 12,12,28
R 44,37888
v_mul_f32_e64 v1, v1, |v1|
mav 16,16,28
v_cvt_pk_bf16_f32 v0, v0, v1
v_pk_max_i16 v68, v0, 0
mav 20,20,28
v_fma_f32 v104, v79, v58, v94
s_waitcnt lgkmcnt(2)
mav 12,24,32
R 48,38912
v_fma_f32 v2, v2, v78, v104
mav 16,28,32
v_fma_f32 v105, v79, v59, v95
v_fma_f32 v3, v3, v78, v105
mav 20,32,32
v_mul_f32_e64 v2, v2, |v2|
v_mul_f32_e64 v3, v3, |v3|
mav 12,36,36
R 52,39936
v_cvt_pk_bf16_f32 v2, v2, v3
mav 16,40,36
v_pk_max_i16 v69, v2, 0
v_fma_f32 v104, v79, v60, v96
mav 20,44,36
v_fma_f32 v4, v4, v78, v104
s_waitcnt lgkmcnt(2)
mav 12,48,40
R 24,40960
v_fma_f32 v105, v79, v61, v97
mav 16,52,40
v_fma_f32 v5, v5, v78, v105
v_mul_f32_e64 v4, v4, |v4|
mav 20,56,40
v_mul_f32_e64 v5, v5, |v5|
v_cvt_pk_bf16_f32 v4, v4, v5
mav 12,60,44
R 28,41984
v_pk_max_i16 v70, v4, 0
mav 16,64,44
v_fma_f32 v104, v79, v62, v98
v_fma_f32 v6, v6, v78, v104
mav 20,68,44
v_fma_f32 v105, v79, v63, v99
s_waitcnt lgkmcnt(2)
mav 12,72,48
R 32,43008
v_fma_f32 v7, v7, v78, v105
mav 16,76,48
v_mul_f32_e64 v6, v6, |v6|
v_mul_f32_e64 v7, v7, |v7|
mav 20,80,48
v_cvt_pk_bf16_f32 v6, v6, v7
v_pk_max_i16 v71, v6, 0
mav 12,84,52
R 36,44032
buffer_store_dwordx4 v[68:71], v83, s[36:39], s49 offen sc1
mav 16,88,52
v_fma_f32 v104, v79, v64, v100
v_fma_f32 v8, v8, v78, v104
mav 20,92,52
v_fma_f32 v105, v79, v65, v101
s_waitcnt lgkmcnt(2)
mav 12,96,24
R 40,45056
v_fma_f32 v9, v9, v78, v105
mav 16,100,24
v_mul_f32_e64 v8, v8, |v8|
v_mul_f32_e64 v9, v9, |v9|
mav 20,104,24
v_cvt_pk_bf16_f32 v8, v8, v9
v_pk_max_i16 v72, v8, 0
mav 12,108,28
R 44,46080
v_pk_max_i16 v74, v8, 0
mav 16,112,28
v_fma_f32 v104, v79, v66, v102
v_fma_f32 v10, v10, v78, v104
mav 20,116,28
v_fma_f32 v105, v79, v67, v103
s_waitcnt lgkmcnt(2)
mav 12,120,32
R 48,47104
v_fma_f32 v11, v11, v78, v105
mav 16,124,32
v_mul_f32_e64 v10, v10, |v10|
v_mul_f32_e64 v11, v11, |v11|
mav 20,128,32
v_cvt_pk_bf16_f32 v10, v10, v11
v_pk_max_i16 v73, v10, 0
mav 12,132,36
R 52,48128
v_pk_max_i16 v75, v10, 0
mav 16,136,36
v_permlane16_swap_b32 v72, v74
v_permlane16_swap_b32 v73, v75
mav 20,140,36
buffer_store_dwordx4 v[72:75], v84, s[36:39], s49 offen sc1
s_waitcnt lgkmcnt(2)
mav 12,144,40
R 24,49152
v_add_u32 v76, 1, v76
mav 16,148,40
s_waitcnt vmcnt(5)
buffer_store_dwordx2 v[76:77], v81, s[36:39], s52 offen sc1
mav 20,152,40
mav 12,156,44
R 28,50176
mav 16,160,44
mav 20,164,44
s_waitcnt lgkmcnt(2)
mav 12,168,48
R 32,51200
mav 16,172,48
mav 20,176,48
mav 12,180,52
R 36,52224
mav 16,184,52
mav 20,188,52
s_waitcnt lgkmcnt(2)
mav 12,192,24
R 40,53248
mav 16,196,24
mav 20,200,24
s_waitcnt vmcnt(3)
v_add_f32 v108, v108, v110
mav 12,204,28
R 44,54272
v_add_f32 v109, v109, v111
mav 16,208,28
mav 20,212,28
v_permlane32_swap_b32 v108, v109
s_waitcnt lgkmcnt(2)
mav 12,216,32
R 48,55296
v_add_f32 v110, v108, v109
mav 16,220,32
v_add_f32 v111, v108, v109
mav 20,224,32
mav 12,228,36
R 52,56320
v_permlane16_swap_b32 v110, v111
mav 16,232,36
v_add_f32 v108, v110, v111
v_add_f32 v109, v110, v111
mav 20,236,36
s_waitcnt lgkmcnt(2)
mav 12,240,40
R 24,57344
v_permlane32_swap_b32 v108, v109
mav 16,244,40
v_mul_f32 v110, 0x3a800000, v108
v_mul_f32 v111, 0x3a800000, v109
mav 20,248,40
v_fma_f32 v111, -v110, v110, v111
v_max_f32 v111, 0, v111
mav 12,252,44
R 28,58368
v_add_f32 v111, 0x3727c5ac, v111
mvv 16,128,44
v_rsq_f32 v108, v111
mvv 20,132,44
v_mul_f32_e64 v109, -v110, v108
s_waitcnt lgkmcnt(2)
mvv 12,136,48
R 32,59392
mvv 16,140,48
ds_write_b64 v89, v[108:109] offset:128
mvv 20,144,48
mvv 12,148,52
R 36,60416
mvv 16,152,52
mvv 20,156,52
s_waitcnt lgkmcnt(3)
mvv 12,160,24
R 40,61440
mvv 16,164,24
mvv 20,168,24
mvv 12,172,28
R 44,62464
mvv 16,176,28
mvv 20,180,28
s_waitcnt lgkmcnt(2)
mvv 12,184,32
R 48,63488
mvv 16,188,32
mvv 20,192,32
mvv 12,196,36
R 52,64512
mvv 16,200,36
s_add_u32 s49, s49, 346112
mvv 20,204,36
s_barrier
s_waitcnt lgkmcnt(2)
mvv 12,208,40
mvv 16,212,40
ds_read_b64 v[78:79], v89 offset:128
mvv 20,216,40
mvv 12,220,44
mvv 16,224,44
mvv 20,228,44
s_waitcnt lgkmcnt(1)
mvv 12,232,48
mvv 16,236,48
mvv 20,240,48
mvv 12,244,52
mvv 16,248,52
mvv 20,252,52
s_waitcnt lgkmcnt(0)
v_fma_f32 v104, v79, v56, v92
s_nop 3
v_fma_f32 v12, v12, v78, v104
v_fma_f32 v105, v79, v57, v93
v_fma_f32 v13, v13, v78, v105
v_mul_f32_e64 v12, v12, |v12|
v_mul_f32_e64 v13, v13, |v13|
v_cvt_pk_bf16_f32 v12, v12, v13
v_pk_max_i16 v68, v12, 0
v_fma_f32 v104, v79, v58, v94
v_fma_f32 v14, v14, v78, v104
v_fma_f32 v105, v79, v59, v95
v_fma_f32 v15, v15, v78, v105
v_mul_f32_e64 v14, v14, |v14|
v_mul_f32_e64 v15, v15, |v15|
v_cvt_pk_bf16_f32 v14, v14, v15
v_pk_max_i16 v69, v14, 0
v_fma_f32 v104, v79, v60, v96
v_fma_f32 v16, v16, v78, v104
v_fma_f32 v105, v79, v61, v97
v_fma_f32 v17, v17, v78, v105
v_mul_f32_e64 v16, v16, |v16|
v_mul_f32_e64 v17, v17, |v17|
v_cvt_pk_bf16_f32 v16, v16, v17
v_pk_max_i16 v70, v16, 0
v_fma_f32 v104, v79, v62, v98
v_fma_f32 v18, v18, v78, v104
v_fma_f32 v105, v79, v63, v99
v_fma_f32 v19, v19, v78, v105
v_mul_f32_e64 v18, v18, |v18|
v_mul_f32_e64 v19, v19, |v19|
v_cvt_pk_bf16_f32 v18, v18, v19
v_pk_max_i16 v71, v18, 0
buffer_store_dwordx4 v[68:71], v83, s[36:39], s49 offen sc1
v_fma_f32 v104, v79, v64, v100
v_fma_f32 v20, v20, v78, v104
v_fma_f32 v105, v79, v65, v101
v_fma_f32 v21, v21, v78, v105
v_mul_f32_e64 v20, v20, |v20|
v_mul_f32_e64 v21, v21, |v21|
v_cvt_pk_bf16_f32 v20, v20, v21
v_pk_max_i16 v72, v20, 0
v_pk_max_i16 v74, v20, 0
v_fma_f32 v104, v79, v66, v102
v_fma_f32 v22, v22, v78, v104
v_fma_f32 v105, v79, v67, v103
v_fma_f32 v23, v23, v78, v105
v_mul_f32_e64 v22, v22, |v22|
v_mul_f32_e64 v23, v23, |v23|
v_cvt_pk_bf16_f32 v22, v22, v23
v_pk_max_i16 v73, v22, 0
v_pk_max_i16 v75, v22, 0
v_permlane16_swap_b32 v72, v74
s_nop 0
v_permlane16_swap_b32 v73, v75
buffer_store_dwordx4 v[72:75], v84, s[36:39], s49 offen sc1
v_add_u32 v76, 1, v76
s_waitcnt vmcnt(3)
buffer_store_dwordx2 v[76:77], v81, s[36:39], s52 offen sc1
v_add_u32 v76, 37, v76
s_waitcnt vmcnt(1)
buffer_store_dwordx2 v[76:77], v81, s[36:39], s52 offen sc1
s_endpgm
LvF2:
s_add_u32 s49, s49, 346112
s_barrier
s_add_u32 m0, s50, 0
s_nop 0
D 85,0
D 85,1024
D 85,2048
D 85,3072
s_add_u32 m0, s50, 4096
s_nop 0
D 86,0
D 86,1024
D 86,2048
D 86,3072
s_add_u32 s49, s49, 346112
s_waitcnt vmcnt(0)
s_barrier
R 24,0
s_add_u32 m0, s50, 32768
R 28,1024
D 85,0
R 32,2048
D 85,1024
R 36,3072
s_waitcnt lgkmcnt(2)
v_mfma_f32_16x16x32_bf16 v[0:3], a[0:3], v[24:27], 0
R 40,4096
D 85,2048
v_mfma_f32_16x16x32_bf16 v[4:7], a[4:7], v[24:27], 0
v_mfma_f32_16x16x32_bf16 v[8:11], a[8:11], v[24:27], 0
mav 0,12,28
R 44,5120
mav 4,16,28
D 85,3072
mav 8,20,28
s_waitcnt lgkmcnt(2)
mav 0,24,32
R 48,6144
mav 4,28,32
s_add_u32 m0, s50, 36864
mav 8,32,32
mav 0,36,36
R 52,7168
D 86,0
mav 4,40,36
mav 8,44,36
s_waitcnt lgkmcnt(2)
mav 0,48,40
R 24,8192
mav 4,52,40
D 86,1024
mav 8,56,40
mav 0,60,44
R 28,9216
mav 4,64,44
mav 8,68,44
s_waitcnt lgkmcnt(2)
mav 0,72,48
R 32,10240
D 86,2048
mav 4,76,48
mav 8,80,48
mav 0,84,52
R 36,11264
mav 4,88,52
D 86,3072
mav 8,92,52
s_waitcnt lgkmcnt(2)
mav 0,96,24
R 40,12288
mav 4,100,24
mav 8,104,24
mav 0,108,28
R 44,13312
mav 4,112,28
mav 8,116,28
s_waitcnt lgkmcnt(2)
mav 0,120,32
R 48,14336
mav 4,124,32
mav 8,128,32
mav 0,132,36
R 52,15360
mav 4,136,36
mav 8,140,36
s_waitcnt lgkmcnt(2)
mav 0,144,40
R 24,16384
mav 4,148,40
mav 8,152,40
mav 0,156,44
R 28,17408
mav 4,160,44
mav 8,164,44
s_waitcnt lgkmcnt(2)
mav 0,168,48
R 32,18432
mav 4,172,48
mav 8,176,48
mav 0,180,52
R 36,19456
mav 4,184,52
mav 8,188,52
s_waitcnt lgkmcnt(2)
mav 0,192,24
R 40,20480
mav 4,196,24
mav 8,200,24
mav 0,204,28
R 44,21504
mav 4,208,28
mav 8,212,28
s_waitcnt lgkmcnt(2)
mav 0,216,32
R 48,22528
mav 4,220,32
mav 8,224,32
mav 0,228,36
R 52,23552
mav 4,232,36
mav 8,236,36
s_waitcnt lgkmcnt(2)
mav 0,240,40
R 24,24576
mav 4,244,40
mav 8,248,40
mav 0,252,44
R 28,25600
mvv 4,128,44
mvv 8,132,44
s_waitcnt lgkmcnt(2)
mvv 0,136,48
R 32,26624
mvv 4,140,48
mvv 8,144,48
mvv 0,148,52
R 36,27648
mvv 4,152,52
mvv 8,156,52
s_waitcnt lgkmcnt(2)
mvv 0,160,24
R 40,28672
mvv 4,164,24
mvv 8,168,24
mvv 0,172,28
R 44,29696
mvv 4,176,28
mvv 8,180,28
s_waitcnt lgkmcnt(2)
mvv 0,184,32
R 48,30720
s_xor_b32 s50, s50, 0x10000
mvv 4,188,32
mvv 8,192,32
mvv 0,196,36
R 52,31744
mvv 4,200,36
s_add_u32 s49, s49, 346112
mvv 8,204,36
s_waitcnt vmcnt(0)
s_barrier
s_waitcnt lgkmcnt(2)
mvv 0,208,40
R 24,32768
mvv 4,212,40
s_add_u32 m0, s50, 0
ds_read_b64 v[78:79], v89 offset:0
mvv 8,216,40
mvv 0,220,44
R 28,33792
D 85,0
mvv 4,224,44
mvv 8,228,44
s_waitcnt lgkmcnt(3)
mvv 0,232,48
R 32,34816
mvv 4,236,48
D 85,1024
mvv 8,240,48
mvv 0,244,52
R 36,35840
mvv 4,248,52
mvv 8,252,52
s_waitcnt lgkmcnt(2)
v_mfma_f32_16x16x32_bf16 v[12:15], a[0:3], v[24:27], 0
R 40,36864
D 85,2048
v_mfma_f32_16x16x32_bf16 v[16:19], a[4:7], v[24:27], 0
v_fma_f32 v104, v79, v56, v92
v_fma_f32 v0, v0, v78, v104
v_mfma_f32_16x16x32_bf16 v[20:23], a[8:11], v[24:27], 0
v_fma_f32 v105, v79, v57, v93
v_fma_f32 v1, v1, v78, v105
mav 12,12,28
R 44,37888
v_mul_f32_e64 v0, v0, |v0|
mav 16,16,28
D 85,3072
v_mul_f32_e64 v1, v1, |v1|
mav 20,20,28
v_cvt_pk_bf16_f32 v0, v0, v1
s_waitcnt lgkmcnt(2)
mav 12,24,32
R 48,38912
v_pk_max_i16 v68, v0, 0
mav 16,28,32
s_add_u32 m0, s50, 4096
v_fma_f32 v104, v79, v58, v94
mav 20,32,32
v_fma_f32 v2, v2, v78, v104
v_fma_f32 v105, v79, v59, v95
mav 12,36,36
R 52,39936
D 86,0
mav 16,40,36
v_fma_f32 v3, v3, v78, v105
v_mul_f32_e64 v2, v2, |v2|
mav 20,44,36
v_mul_f32_e64 v3, v3, |v3|
s_waitcnt lgkmcnt(2)
mav 12,48,40
R 24,40960
v_cvt_pk_bf16_f32 v2, v2, v3
mav 16,52,40
D 86,1024
v_pk_max_i16 v69, v2, 0
mav 20,56,40
v_fma_f32 v104, v79, v60, v96
v_fma_f32 v4, v4, v78, v104
mav 12,60,44
R 28,41984
v_fma_f32 v105, v79, v61, v97
mav 16,64,44
v_fma_f32 v5, v5, v78, v105
v_mul_f32_e64 v4, v4, |v4|
mav 20,68,44
v_mul_f32_e64 v5, v5, |v5|
s_waitcnt lgkmcnt(2)
mav 12,72,48
R 32,43008
D 86,2048
mav 16,76,48
v_cvt_pk_bf16_f32 v4, v4, v5
v_pk_max_i16 v70, v4, 0
mav 20,80,48
v_fma_f32 v104, v79, v62, v98
v_fma_f32 v6, v6, v78, v104
mav 12,84,52
R 36,44032
v_fma_f32 v105, v79, v63, v99
mav 16,88,52
D 86,3072
v_fma_f32 v7, v7, v78, v105
mav 20,92,52
v_mul_f32_e64 v6, v6, |v6|
s_waitcnt lgkmcnt(2)
mav 12,96,24
R 40,45056
v_mul_f32_e64 v7, v7, |v7|
mav 16,100,24
v_cvt_pk_bf16_f32 v6, v6, v7
v_pk_max_i16 v71, v6, 0
mav 20,104,24
buffer_store_dwordx4 v[68:71], v83, s[36:39], s49 offen sc1
v_fma_f32 v104, v79, v64, v100
mav 12,108,28
R 44,46080
v_fma_f32 v8, v8, v78, v104
mav 16,112,28
v_fma_f32 v105, v79, v65, v101
v_fma_f32 v9, v9, v78, v105
mav 20,116,28
v_mul_f32_e64 v8, v8, |v8|
s_waitcnt lgkmcnt(2)
mav 12,120,32
R 48,47104
v_mul_f32_e64 v9, v9, |v9|
mav 16,124,32
v_cvt_pk_bf16_f32 v8, v8, v9
v_pk_max_i16 v72, v8, 0
mav 20,128,32
v_pk_max_i16 v74, v8, 0
v_fma_f32 v104, v79, v66, v102
mav 12,132,36
R 52,48128
v_fma_f32 v10, v10, v78, v104
mav 16,136,36
v_fma_f32 v105, v79, v67, v103
v_fma_f32 v11, v11, v78, v105
mav 20,140,36
v_mul_f32_e64 v10, v10, |v10|
s_waitcnt lgkmcnt(2)
mav 12,144,40
R 24,49152
v_mul_f32_e64 v11, v11, |v11|
mav 16,148,40
v_cvt_pk_bf16_f32 v10, v10, v11
v_pk_max_i16 v73, v10, 0
mav 20,152,40
v_pk_max_i16 v75, v10, 0
v_permlane16_swap_b32 v72, v74
mav 12,156,44
R 28,50176
v_permlane16_swap_b32 v73, v75
mav 16,160,44
buffer_store_dwordx4 v[72:75], v84, s[36:39], s49 offen sc1
mav 20,164,44
s_waitcnt lgkmcnt(2)
mav 12,168,48
R 32,51200
mav 16,172,48
mav 20,176,48
mav 12,180,52
R 36,52224
mav 16,184,52
mav 20,188,52
s_waitcnt lgkmcnt(2)
mav 12,192,24
R 40,53248
mav 16,196,24
mav 20,200,24
mav 12,204,28
R 44,54272
mav 16,208,28
mav 20,212,28
s_waitcnt lgkmcnt(2)
mav 12,216,32
R 48,55296
mav 16,220,32
mav 20,224,32
mav 12,228,36
R 52,56320
mav 16,232,36
mav 20,236,36
s_waitcnt lgkmcnt(2)
mav 12,240,40
R 24,57344
mav 16,244,40
mav 20,248,40
mav 12,252,44
R 28,58368
mvv 16,128,44
mvv 20,132,44
s_waitcnt lgkmcnt(2)
mvv 12,136,48
R 32,59392
mvv 16,140,48
mvv 20,144,48
mvv 12,148,52
R 36,60416
mvv 16,152,52
mvv 20,156,52
s_waitcnt lgkmcnt(2)
mvv 12,160,24
R 40,61440
mvv 16,164,24
)"
R"(
mvv 20,168,24
mvv 12,172,28
R 44,62464
mvv 16,176,28
mvv 20,180,28
s_waitcnt lgkmcnt(2)
mvv 12,184,32
R 48,63488
mvv 16,188,32
mvv 20,192,32
mvv 12,196,36
R 52,64512
v_xor_b32 v80, 0x10000, v80
mvv 16,200,36
s_add_u32 s49, s49, 346112
mvv 20,204,36
s_waitcnt vmcnt(2)
s_barrier
s_waitcnt lgkmcnt(2)
mvv 12,208,40
R 24,0
mvv 16,212,40
s_add_u32 m0, s50, 32768
ds_read_b64 v[78:79], v89 offset:128
mvv 20,216,40
mvv 12,220,44
R 28,1024
D 85,0
mvv 16,224,44
mvv 20,228,44
s_waitcnt lgkmcnt(3)
mvv 12,232,48
R 32,2048
mvv 16,236,48
D 85,1024
mvv 20,240,48
mvv 12,244,52
R 36,3072
mvv 16,248,52
mvv 20,252,52
s_waitcnt lgkmcnt(2)
v_mfma_f32_16x16x32_bf16 v[0:3], a[0:3], v[24:27], 0
R 40,4096
D 85,2048
v_mfma_f32_16x16x32_bf16 v[4:7], a[4:7], v[24:27], 0
v_fma_f32 v104, v79, v56, v92
v_fma_f32 v12, v12, v78, v104
v_mfma_f32_16x16x32_bf16 v[8:11], a[8:11], v[24:27], 0
v_fma_f32 v105, v79, v57, v93
v_fma_f32 v13, v13, v78, v105
mav 0,12,28
R 44,5120
v_mul_f32_e64 v12, v12, |v12|
mav 4,16,28
D 85,3072
v_mul_f32_e64 v13, v13, |v13|
mav 8,20,28
v_cvt_pk_bf16_f32 v12, v12, v13
s_waitcnt lgkmcnt(2)
mav 0,24,32
R 48,6144
v_pk_max_i16 v68, v12, 0
mav 4,28,32
s_add_u32 m0, s50, 36864
v_fma_f32 v104, v79, v58, v94
mav 8,32,32
v_fma_f32 v14, v14, v78, v104
v_fma_f32 v105, v79, v59, v95
mav 0,36,36
R 52,7168
D 86,0
mav 4,40,36
v_fma_f32 v15, v15, v78, v105
v_mul_f32_e64 v14, v14, |v14|
mav 8,44,36
v_mul_f32_e64 v15, v15, |v15|
s_waitcnt lgkmcnt(2)
mav 0,48,40
R 24,8192
v_cvt_pk_bf16_f32 v14, v14, v15
mav 4,52,40
D 86,1024
v_pk_max_i16 v69, v14, 0
mav 8,56,40
v_fma_f32 v104, v79, v60, v96
v_fma_f32 v16, v16, v78, v104
mav 0,60,44
R 28,9216
v_fma_f32 v105, v79, v61, v97
mav 4,64,44
v_fma_f32 v17, v17, v78, v105
v_mul_f32_e64 v16, v16, |v16|
mav 8,68,44
v_mul_f32_e64 v17, v17, |v17|
s_waitcnt lgkmcnt(2)
mav 0,72,48
R 32,10240
D 86,2048
mav 4,76,48
v_cvt_pk_bf16_f32 v16, v16, v17
v_pk_max_i16 v70, v16, 0
mav 8,80,48
v_fma_f32 v104, v79, v62, v98
v_fma_f32 v18, v18, v78, v104
mav 0,84,52
R 36,11264
v_fma_f32 v105, v79, v63, v99
mav 4,88,52
D 86,3072
v_fma_f32 v19, v19, v78, v105
mav 8,92,52
v_mul_f32_e64 v18, v18, |v18|
s_waitcnt lgkmcnt(2)
mav 0,96,24
R 40,12288
v_mul_f32_e64 v19, v19, |v19|
mav 4,100,24
v_cvt_pk_bf16_f32 v18, v18, v19
v_pk_max_i16 v71, v18, 0
mav 8,104,24
buffer_store_dwordx4 v[68:71], v83, s[36:39], s49 offen sc1
v_fma_f32 v104, v79, v64, v100
mav 0,108,28
R 44,13312
v_fma_f32 v20, v20, v78, v104
mav 4,112,28
v_fma_f32 v105, v79, v65, v101
v_fma_f32 v21, v21, v78, v105
mav 8,116,28
v_mul_f32_e64 v20, v20, |v20|
s_waitcnt lgkmcnt(2)
mav 0,120,32
R 48,14336
v_mul_f32_e64 v21, v21, |v21|
mav 4,124,32
v_cvt_pk_bf16_f32 v20, v20, v21
v_pk_max_i16 v72, v20, 0
mav 8,128,32
v_pk_max_i16 v74, v20, 0
v_fma_f32 v104, v79, v66, v102
mav 0,132,36
R 52,15360
v_fma_f32 v22, v22, v78, v104
mav 4,136,36
v_fma_f32 v105, v79, v67, v103
v_fma_f32 v23, v23, v78, v105
mav 8,140,36
v_mul_f32_e64 v22, v22, |v22|
s_waitcnt lgkmcnt(2)
mav 0,144,40
R 24,16384
v_mul_f32_e64 v23, v23, |v23|
mav 4,148,40
v_cvt_pk_bf16_f32 v22, v22, v23
v_pk_max_i16 v73, v22, 0
mav 8,152,40
v_pk_max_i16 v75, v22, 0
v_permlane16_swap_b32 v72, v74
mav 0,156,44
R 28,17408
v_permlane16_swap_b32 v73, v75
mav 4,160,44
buffer_store_dwordx4 v[72:75], v84, s[36:39], s49 offen sc1
v_add_u32 v76, 1, v76
mav 8,164,44
s_waitcnt vmcnt(10)
buffer_store_dwordx2 v[76:77], v81, s[36:39], s52 offen sc1
s_waitcnt lgkmcnt(2)
mav 0,168,48
R 32,18432
mav 4,172,48
mav 8,176,48
mav 0,180,52
R 36,19456
mav 4,184,52
mav 8,188,52
s_waitcnt lgkmcnt(2)
mav 0,192,24
R 40,20480
mav 4,196,24
mav 8,200,24
mav 0,204,28
R 44,21504
mav 4,208,28
mav 8,212,28
s_waitcnt lgkmcnt(2)
mav 0,216,32
R 48,22528
mav 4,220,32
mav 8,224,32
mav 0,228,36
R 52,23552
mav 4,232,36
mav 8,236,36
s_waitcnt lgkmcnt(2)
mav 0,240,40
R 24,24576
mav 4,244,40
mav 8,248,40
mav 0,252,44
R 28,25600
mvv 4,128,44
mvv 8,132,44
s_waitcnt lgkmcnt(2)
mvv 0,136,48
R 32,26624
mvv 4,140,48
mvv 8,144,48
mvv 0,148,52
R 36,27648
mvv 4,152,52
mvv 8,156,52
s_waitcnt lgkmcnt(2)
mvv 0,160,24
R 40,28672
mvv 4,164,24
mvv 8,168,24
mvv 0,172,28
R 44,29696
mvv 4,176,28
mvv 8,180,28
s_waitcnt lgkmcnt(2)
mvv 0,184,32
R 48,30720
s_xor_b32 s50, s50, 0x10000
mvv 4,188,32
mvv 8,192,32
mvv 0,196,36
R 52,31744
mvv 4,200,36
s_add_u32 s49, s49, 346112
mvv 8,204,36
s_waitcnt vmcnt(3)
s_barrier
s_waitcnt lgkmcnt(2)
mvv 0,208,40
R 24,32768
mvv 4,212,40
s_add_u32 m0, s50, 0
ds_read_b64 v[78:79], v89 offset:0
mvv 8,216,40
mvv 0,220,44
R 28,33792
D 85,0
mvv 4,224,44
mvv 8,228,44
s_waitcnt lgkmcnt(3)
mvv 0,232,48
R 32,34816
mvv 4,236,48
D 85,1024
mvv 8,240,48
mvv 0,244,52
R 36,35840
mvv 4,248,52
mvv 8,252,52
s_waitcnt lgkmcnt(2)
v_mfma_f32_16x16x32_bf16 v[12:15], a[0:3], v[24:27], 0
R 40,36864
D 85,2048
v_mfma_f32_16x16x32_bf16 v[16:19], a[4:7], v[24:27], 0
v_fma_f32 v104, v79, v56, v92
v_fma_f32 v0, v0, v78, v104
v_mfma_f32_16x16x32_bf16 v[20:23], a[8:11], v[24:27], 0
v_fma_f32 v105, v79, v57, v93
v_fma_f32 v1, v1, v78, v105
mav 12,12,28
R 44,37888
v_mul_f32_e64 v0, v0, |v0|
mav 16,16,28
D 85,3072
v_mul_f32_e64 v1, v1, |v1|
mav 20,20,28
v_cvt_pk_bf16_f32 v0, v0, v1
s_waitcnt lgkmcnt(2)
mav 12,24,32
R 48,38912
v_pk_max_i16 v68, v0, 0
mav 16,28,32
s_add_u32 m0, s50, 4096
v_fma_f32 v104, v79, v58, v94
mav 20,32,32
v_fma_f32 v2, v2, v78, v104
v_fma_f32 v105, v79, v59, v95
mav 12,36,36
R 52,39936
D 86,0
mav 16,40,36
v_fma_f32 v3, v3, v78, v105
v_mul_f32_e64 v2, v2, |v2|
mav 20,44,36
v_mul_f32_e64 v3, v3, |v3|
s_waitcnt lgkmcnt(2)
mav 12,48,40
R 24,40960
v_cvt_pk_bf16_f32 v2, v2, v3
mav 16,52,40
D 86,1024
v_pk_max_i16 v69, v2, 0
mav 20,56,40
v_fma_f32 v104, v79, v60, v96
v_fma_f32 v4, v4, v78, v104
mav 12,60,44
R 28,41984
v_fma_f32 v105, v79, v61, v97
mav 16,64,44
v_fma_f32 v5, v5, v78, v105
v_mul_f32_e64 v4, v4, |v4|
mav 20,68,44
v_mul_f32_e64 v5, v5, |v5|
s_waitcnt lgkmcnt(2)
mav 12,72,48
R 32,43008
D 86,2048
mav 16,76,48
v_cvt_pk_bf16_f32 v4, v4, v5
v_pk_max_i16 v70, v4, 0
mav 20,80,48
v_fma_f32 v104, v79, v62, v98
v_fma_f32 v6, v6, v78, v104
mav 12,84,52
R 36,44032
v_fma_f32 v105, v79, v63, v99
mav 16,88,52
D 86,3072
v_fma_f32 v7, v7, v78, v105
mav 20,92,52
v_mul_f32_e64 v6, v6, |v6|
s_waitcnt lgkmcnt(2)
mav 12,96,24
R 40,45056
v_mul_f32_e64 v7, v7, |v7|
mav 16,100,24
v_cvt_pk_bf16_f32 v6, v6, v7
v_pk_max_i16 v71, v6, 0
mav 20,104,24
buffer_store_dwordx4 v[68:71], v83, s[36:39], s49 offen sc1
v_fma_f32 v104, v79, v64, v100
mav 12,108,28
R 44,46080
v_fma_f32 v8, v8, v78, v104
mav 16,112,28
v_fma_f32 v105, v79, v65, v101
v_fma_f32 v9, v9, v78, v105
mav 20,116,28
v_mul_f32_e64 v8, v8, |v8|
s_waitcnt lgkmcnt(2)
mav 12,120,32
R 48,47104
v_mul_f32_e64 v9, v9, |v9|
mav 16,124,32
v_cvt_pk_bf16_f32 v8, v8, v9
v_pk_max_i16 v72, v8, 0
mav 20,128,32
v_pk_max_i16 v74, v8, 0
v_fma_f32 v104, v79, v66, v102
mav 12,132,36
R 52,48128
v_fma_f32 v10, v10, v78, v104
mav 16,136,36
v_fma_f32 v105, v79, v67, v103
v_fma_f32 v11, v11, v78, v105
mav 20,140,36
v_mul_f32_e64 v10, v10, |v10|
s_waitcnt lgkmcnt(2)
mav 12,144,40
R 24,49152
v_mul_f32_e64 v11, v11, |v11|
mav 16,148,40
v_cvt_pk_bf16_f32 v10, v10, v11
v_pk_max_i16 v73, v10, 0
mav 20,152,40
v_pk_max_i16 v75, v10, 0
v_permlane16_swap_b32 v72, v74
mav 12,156,44
R 28,50176
v_permlane16_swap_b32 v73, v75
mav 16,160,44
buffer_store_dwordx4 v[72:75], v84, s[36:39], s49 offen sc1
v_add_u32 v76, 1, v76
mav 20,164,44
s_waitcnt vmcnt(11)
buffer_store_dwordx2 v[76:77], v81, s[36:39], s52 offen sc1
s_waitcnt lgkmcnt(2)
mav 12,168,48
R 32,51200
mav 16,172,48
mav 20,176,48
mav 12,180,52
R 36,52224
mav 16,184,52
mav 20,188,52
s_waitcnt lgkmcnt(2)
mav 12,192,24
R 40,53248
mav 16,196,24
mav 20,200,24
mav 12,204,28
R 44,54272
mav 16,208,28
mav 20,212,28
s_waitcnt lgkmcnt(2)
mav 12,216,32
R 48,55296
mav 16,220,32
mav 20,224,32
mav 12,228,36
R 52,56320
mav 16,232,36
mav 20,236,36
s_waitcnt lgkmcnt(2)
mav 12,240,40
R 24,57344
mav 16,244,40
mav 20,248,40
mav 12,252,44
R 28,58368
mvv 16,128,44
mvv 20,132,44
s_waitcnt lgkmcnt(2)
mvv 12,136,48
R 32,59392
mvv 16,140,48
mvv 20,144,48
mvv 12,148,52
R 36,60416
mvv 16,152,52
mvv 20,156,52
s_waitcnt lgkmcnt(2)
mvv 12,160,24
R 40,61440
mvv 16,164,24
mvv 20,168,24
mvv 12,172,28
R 44,62464
mvv 16,176,28
mvv 20,180,28
s_waitcnt lgkmcnt(2)
mvv 12,184,32
R 48,63488
mvv 16,188,32
mvv 20,192,32
mvv 12,196,36
R 52,64512
v_xor_b32 v80, 0x10000, v80
mvv 16,200,36
s_add_u32 s49, s49, 346112
mvv 20,204,36
s_waitcnt vmcnt(3)
s_barrier
s_waitcnt lgkmcnt(2)
mvv 12,208,40
R 24,0
mvv 16,212,40
s_add_u32 m0, s50, 32768
ds_read_b64 v[78:79], v89 offset:128
mvv 20,216,40
mvv 12,220,44
R 28,1024
D 85,0
mvv 16,224,44
mvv 20,228,44
s_waitcnt lgkmcnt(3)
mvv 12,232,48
R 32,2048
mvv 16,236,48
D 85,1024
mvv 20,240,48
mvv 12,244,52
R 36,3072
mvv 16,248,52
mvv 20,252,52
s_mov_b32 s60, -29
.p2align 6
LF2_1:
s_waitcnt lgkmcnt(2)
v_mfma_f32_16x16x32_bf16 v[0:3], a[0:3], v[24:27], 0
R 40,4096
D 85,2048
v_mfma_f32_16x16x32_bf16 v[4:7], a[4:7], v[24:27], 0
v_fma_f32 v104, v79, v56, v92
v_fma_f32 v12, v12, v78, v104
v_mfma_f32_16x16x32_bf16 v[8:11], a[8:11], v[24:27], 0
v_fma_f32 v105, v79, v57, v93
v_fma_f32 v13, v13, v78, v105
mav 0,12,28
R 44,5120
v_mul_f32_e64 v12, v12, |v12|
mav 4,16,28
D 85,3072
v_mul_f32_e64 v13, v13, |v13|
mav 8,20,28
v_cvt_pk_bf16_f32 v12, v12, v13
s_waitcnt lgkmcnt(2)
mav 0,24,32
R 48,6144
v_pk_max_i16 v68, v12, 0
mav 4,28,32
s_add_u32 m0, s50, 36864
v_fma_f32 v104, v79, v58, v94
mav 8,32,32
v_fma_f32 v14, v14, v78, v104
v_fma_f32 v105, v79, v59, v95
mav 0,36,36
R 52,7168
D 86,0
mav 4,40,36
v_fma_f32 v15, v15, v78, v105
v_mul_f32_e64 v14, v14, |v14|
mav 8,44,36
v_mul_f32_e64 v15, v15, |v15|
s_waitcnt lgkmcnt(2)
mav 0,48,40
R 24,8192
v_cvt_pk_bf16_f32 v14, v14, v15
mav 4,52,40
D 86,1024
v_pk_max_i16 v69, v14, 0
mav 8,56,40
v_fma_f32 v104, v79, v60, v96
v_fma_f32 v16, v16, v78, v104
mav 0,60,44
R 28,9216
v_fma_f32 v105, v79, v61, v97
mav 4,64,44
v_fma_f32 v17, v17, v78, v105
v_mul_f32_e64 v16, v16, |v16|
mav 8,68,44
v_mul_f32_e64 v17, v17, |v17|
s_waitcnt lgkmcnt(2)
mav 0,72,48
R 32,10240
D 86,2048
mav 4,76,48
v_cvt_pk_bf16_f32 v16, v16, v17
v_pk_max_i16 v70, v16, 0
mav 8,80,48
v_fma_f32 v104, v79, v62, v98
v_fma_f32 v18, v18, v78, v104
mav 0,84,52
R 36,11264
v_fma_f32 v105, v79, v63, v99
mav 4,88,52
D 86,3072
v_fma_f32 v19, v19, v78, v105
mav 8,92,52
v_mul_f32_e64 v18, v18, |v18|
s_waitcnt lgkmcnt(2)
mav 0,96,24
R 40,12288
v_mul_f32_e64 v19, v19, |v19|
mav 4,100,24
v_cvt_pk_bf16_f32 v18, v18, v19
v_pk_max_i16 v71, v18, 0
mav 8,104,24
buffer_store_dwordx4 v[68:71], v83, s[36:39], s49 offen sc1
v_fma_f32 v104, v79, v64, v100
mav 0,108,28
R 44,13312
v_fma_f32 v20, v20, v78, v104
mav 4,112,28
v_fma_f32 v105, v79, v65, v101
v_fma_f32 v21, v21, v78, v105
mav 8,116,28
v_mul_f32_e64 v20, v20, |v20|
s_waitcnt lgkmcnt(2)
mav 0,120,32
R 48,14336
v_mul_f32_e64 v21, v21, |v21|
mav 4,124,32
v_cvt_pk_bf16_f32 v20, v20, v21
v_pk_max_i16 v72, v20, 0
mav 8,128,32
v_pk_max_i16 v74, v20, 0
v_fma_f32 v104, v79, v66, v102
mav 0,132,36
R 52,15360
v_fma_f32 v22, v22, v78, v104
mav 4,136,36
v_fma_f32 v105, v79, v67, v103
v_fma_f32 v23, v23, v78, v105
mav 8,140,36
v_mul_f32_e64 v22, v22, |v22|
s_waitcnt lgkmcnt(2)
mav 0,144,40
R 24,16384
v_mul_f32_e64 v23, v23, |v23|
mav 4,148,40
v_cvt_pk_bf16_f32 v22, v22, v23
v_pk_max_i16 v73, v22, 0
mav 8,152,40
v_pk_max_i16 v75, v22, 0
v_permlane16_swap_b32 v72, v74
mav 0,156,44
R 28,17408
v_permlane16_swap_b32 v73, v75
mav 4,160,44
buffer_store_dwordx4 v[72:75], v84, s[36:39], s49 offen sc1
v_add_u32 v76, 1, v76
mav 8,164,44
s_waitcnt vmcnt(11)
buffer_store_dwordx2 v[76:77], v81, s[36:39], s52 offen sc1
s_waitcnt lgkmcnt(2)
mav 0,168,48
R 32,18432
mav 4,172,48
mav 8,176,48
mav 0,180,52
R 36,19456
mav 4,184,52
mav 8,188,52
s_waitcnt lgkmcnt(2)
mav 0,192,24
R 40,20480
mav 4,196,24
mav 8,200,24
mav 0,204,28
R 44,21504
mav 4,208,28
mav 8,212,28
s_waitcnt lgkmcnt(2)
mav 0,216,32
R 48,22528
mav 4,220,32
mav 8,224,32
mav 0,228,36
R 52,23552
mav 4,232,36
mav 8,236,36
s_waitcnt lgkmcnt(2)
mav 0,240,40
R 24,24576
mav 4,244,40
mav 8,248,40
mav 0,252,44
R 28,25600
mvv 4,128,44
mvv 8,132,44
s_waitcnt lgkmcnt(2)
mvv 0,136,48
R 32,26624
mvv 4,140,48
mvv 8,144,48
mvv 0,148,52
R 36,27648
mvv 4,152,52
mvv 8,156,52
s_waitcnt lgkmcnt(2)
mvv 0,160,24
R 40,28672
mvv 4,164,24
mvv 8,168,24
mvv 0,172,28
R 44,29696
mvv 4,176,28
mvv 8,180,28
s_waitcnt lgkmcnt(2)
mvv 0,184,32
R 48,30720
s_xor_b32 s50, s50, 0x10000
mvv 4,188,32
mvv 8,192,32
mvv 0,196,36
R 52,31744
mvv 4,200,36
s_add_u32 s49, s49, 346112
mvv 8,204,36
s_waitcnt vmcnt(3)
s_barrier
s_waitcnt lgkmcnt(2)
mvv 0,208,40
R 24,32768
mvv 4,212,40
s_add_u32 m0, s50, 0
ds_read_b64 v[78:79], v89 offset:0
mvv 8,216,40
mvv 0,220,44
R 28,33792
D 85,0
mvv 4,224,44
mvv 8,228,44
s_waitcnt lgkmcnt(3)
mvv 0,232,48
R 32,34816
mvv 4,236,48
D 85,1024
mvv 8,240,48
mvv 0,244,52
R 36,35840
mvv 4,248,52
mvv 8,252,52
s_waitcnt lgkmcnt(2)
v_mfma_f32_16x16x32_bf16 v[12:15], a[0:3], v[24:27], 0
R 40,36864
D 85,2048
v_mfma_f32_16x16x32_bf16 v[16:19], a[4:7], v[24:27], 0
v_fma_f32 v104, v79, v56, v92
v_fma_f32 v0, v0, v78, v104
v_mfma_f32_16x16x32_bf16 v[20:23], a[8:11], v[24:27], 0
v_fma_f32 v105, v79, v57, v93
v_fma_f32 v1, v1, v78, v105
mav 12,12,28
R 44,37888
v_mul_f32_e64 v0, v0, |v0|
mav 16,16,28
D 85,3072
v_mul_f32_e64 v1, v1, |v1|
mav 20,20,28
v_cvt_pk_bf16_f32 v0, v0, v1
s_waitcnt lgkmcnt(2)
mav 12,24,32
R 48,38912
v_pk_max_i16 v68, v0, 0
mav 16,28,32
s_add_u32 m0, s50, 4096
v_fma_f32 v104, v79, v58, v94
mav 20,32,32
v_fma_f32 v2, v2, v78, v104
v_fma_f32 v105, v79, v59, v95
mav 12,36,36
R 52,39936
D 86,0
mav 16,40,36
v_fma_f32 v3, v3, v78, v105
v_mul_f32_e64 v2, v2, |v2|
mav 20,44,36
v_mul_f32_e64 v3, v3, |v3|
s_waitcnt lgkmcnt(2)
mav 12,48,40
R 24,40960
v_cvt_pk_bf16_f32 v2, v2, v3
mav 16,52,40
D 86,1024
v_pk_max_i16 v69, v2, 0
mav 20,56,40
v_fma_f32 v104, v79, v60, v96
v_fma_f32 v4, v4, v78, v104
mav 12,60,44
R 28,41984
v_fma_f32 v105, v79, v61, v97
mav 16,64,44
v_fma_f32 v5, v5, v78, v105
v_mul_f32_e64 v4, v4, |v4|
mav 20,68,44
v_mul_f32_e64 v5, v5, |v5|
s_waitcnt lgkmcnt(2)
mav 12,72,48
R 32,43008
D 86,2048
mav 16,76,48
v_cvt_pk_bf16_f32 v4, v4, v5
v_pk_max_i16 v70, v4, 0
mav 20,80,48
v_fma_f32 v104, v79, v62, v98
v_fma_f32 v6, v6, v78, v104
mav 12,84,52
R 36,44032
v_fma_f32 v105, v79, v63, v99
mav 16,88,52
D 86,3072
v_fma_f32 v7, v7, v78, v105
mav 20,92,52
v_mul_f32_e64 v6, v6, |v6|
s_waitcnt lgkmcnt(2)
mav 12,96,24
R 40,45056
v_mul_f32_e64 v7, v7, |v7|
mav 16,100,24
v_cvt_pk_bf16_f32 v6, v6, v7
v_pk_max_i16 v71, v6, 0
mav 20,104,24
buffer_store_dwordx4 v[68:71], v83, s[36:39], s49 offen sc1
v_fma_f32 v104, v79, v64, v100
mav 12,108,28
R 44,46080
v_fma_f32 v8, v8, v78, v104
mav 16,112,28
v_fma_f32 v105, v79, v65, v101
v_fma_f32 v9, v9, v78, v105
mav 20,116,28
v_mul_f32_e64 v8, v8, |v8|
s_waitcnt lgkmcnt(2)
mav 12,120,32
R 48,47104
v_mul_f32_e64 v9, v9, |v9|
mav 16,124,32
v_cvt_pk_bf16_f32 v8, v8, v9
v_pk_max_i16 v72, v8, 0
mav 20,128,32
v_pk_max_i16 v74, v8, 0
v_fma_f32 v104, v79, v66, v102
mav 12,132,36
R 52,48128
v_fma_f32 v10, v10, v78, v104
mav 16,136,36
v_fma_f32 v105, v79, v67, v103
v_fma_f32 v11, v11, v78, v105
mav 20,140,36
v_mul_f32_e64 v10, v10, |v10|
s_waitcnt lgkmcnt(2)
mav 12,144,40
R 24,49152
v_mul_f32_e64 v11, v11, |v11|
mav 16,148,40
v_cvt_pk_bf16_f32 v10, v10, v11
v_pk_max_i16 v73, v10, 0
mav 20,152,40
v_pk_max_i16 v75, v10, 0
v_permlane16_swap_b32 v72, v74
mav 12,156,44
R 28,50176
v_permlane16_swap_b32 v73, v75
mav 16,160,44
buffer_store_dwordx4 v[72:75], v84, s[36:39], s49 offen sc1
v_add_u32 v76, 1, v76
mav 20,164,44
s_waitcnt vmcnt(11)
buffer_store_dwordx2 v[76:77], v81, s[36:39], s52 offen sc1
s_waitcnt lgkmcnt(2)
mav 12,168,48
R 32,51200
mav 16,172,48
mav 20,176,48
mav 12,180,52
R 36,52224
mav 16,184,52
mav 20,188,52
s_waitcnt lgkmcnt(2)
mav 12,192,24
R 40,53248
mav 16,196,24
mav 20,200,24
mav 12,204,28
R 44,54272
mav 16,208,28
mav 20,212,28
s_waitcnt lgkmcnt(2)
mav 12,216,32
R 48,55296
mav 16,220,32
mav 20,224,32
mav 12,228,36
R 52,56320
mav 16,232,36
mav 20,236,36
s_waitcnt lgkmcnt(2)
mav 12,240,40
R 24,57344
mav 16,244,40
mav 20,248,40
mav 12,252,44
R 28,58368
mvv 16,128,44
mvv 20,132,44
s_waitcnt lgkmcnt(2)
mvv 12,136,48
R 32,59392
mvv 16,140,48
mvv 20,144,48
mvv 12,148,52
R 36,60416
mvv 16,152,52
mvv 20,156,52
s_waitcnt lgkmcnt(2)
mvv 12,160,24
R 40,61440
mvv 16,164,24
mvv 20,168,24
mvv 12,172,28
R 44,62464
mvv 16,176,28
mvv 20,180,28
s_waitcnt lgkmcnt(2)
mvv 12,184,32
R 48,63488
mvv 16,188,32
mvv 20,192,32
mvv 12,196,36
R 52,64512
v_xor_b32 v80, 0x10000, v80
mvv 16,200,36
s_add_u32 s49, s49, 346112
mvv 20,204,36
s_waitcnt vmcnt(3)
s_barrier
s_waitcnt lgkmcnt(2)
mvv 12,208,40
R 24,0
mvv 16,212,40
s_add_u32 m0, s50, 32768
ds_read_b64 v[78:79], v89 offset:128
mvv 20,216,40
mvv 12,220,44
R 28,1024
D 85,0
mvv 16,224,44
mvv 20,228,44
s_waitcnt lgkmcnt(3)
mvv 12,232,48
R 32,2048
mvv 16,236,48
D 85,1024
mvv 20,240,48
mvv 12,244,52
R 36,3072
mvv 16,248,52
mvv 20,252,52
s_add_u32 s60, s60, 1
s_cbranch_scc0 LF2_1
s_waitcnt lgkmcnt(2)
v_mfma_f32_16x16x32_bf16 v[0:3], a[0:3], v[24:27], 0
R 40,4096
D 85,2048
v_mfma_f32_16x16x32_bf16 v[4:7], a[4:7], v[24:27], 0
v_fma_f32 v104, v79, v56, v92
v_fma_f32 v12, v12, v78, v104
v_mfma_f32_16x16x32_bf16 v[8:11], a[8:11], v[24:27], 0
v_fma_f32 v105, v79, v57, v93
v_fma_f32 v13, v13, v78, v105
mav 0,12,28
R 44,5120
v_mul_f32_e64 v12, v12, |v12|
mav 4,16,28
D 85,3072
v_mul_f32_e64 v13, v13, |v13|
mav 8,20,28
v_cvt_pk_bf16_f32 v12, v12, v13
s_waitcnt lgkmcnt(2)
mav 0,24,32
R 48,6144
v_pk_max_i16 v68, v12, 0
mav 4,28,32
s_add_u32 m0, s50, 36864
v_fma_f32 v104, v79, v58, v94
mav 8,32,32
v_fma_f32 v14, v14, v78, v104
v_fma_f32 v105, v79, v59, v95
mav 0,36,36
R 52,7168
D 86,0
mav 4,40,36
v_fma_f32 v15, v15, v78, v105
v_mul_f32_e64 v14, v14, |v14|
mav 8,44,36
v_mul_f32_e64 v15, v15, |v15|
s_waitcnt lgkmcnt(2)
mav 0,48,40
R 24,8192
v_cvt_pk_bf16_f32 v14, v14, v15
mav 4,52,40
D 86,1024
v_pk_max_i16 v69, v14, 0
mav 8,56,40
v_fma_f32 v104, v79, v60, v96
v_fma_f32 v16, v16, v78, v104
mav 0,60,44
R 28,9216
v_fma_f32 v105, v79, v61, v97
mav 4,64,44
v_fma_f32 v17, v17, v78, v105
v_mul_f32_e64 v16, v16, |v16|
mav 8,68,44
v_mul_f32_e64 v17, v17, |v17|
s_waitcnt lgkmcnt(2)
mav 0,72,48
R 32,10240
D 86,2048
mav 4,76,48
v_cvt_pk_bf16_f32 v16, v16, v17
v_pk_max_i16 v70, v16, 0
mav 8,80,48
v_fma_f32 v104, v79, v62, v98
v_fma_f32 v18, v18, v78, v104
mav 0,84,52
R 36,11264
v_fma_f32 v105, v79, v63, v99
mav 4,88,52
D 86,3072
v_fma_f32 v19, v19, v78, v105
mav 8,92,52
v_mul_f32_e64 v18, v18, |v18|
s_waitcnt lgkmcnt(2)
mav 0,96,24
R 40,12288
v_mul_f32_e64 v19, v19, |v19|
mav 4,100,24
v_cvt_pk_bf16_f32 v18, v18, v19
v_pk_max_i16 v71, v18, 0
mav 8,104,24
buffer_store_dwordx4 v[68:71], v83, s[36:39], s49 offen sc1
v_fma_f32 v104, v79, v64, v100
mav 0,108,28
R 44,13312
v_fma_f32 v20, v20, v78, v104
mav 4,112,28
v_fma_f32 v105, v79, v65, v101
v_fma_f32 v21, v21, v78, v105
mav 8,116,28
v_mul_f32_e64 v20, v20, |v20|
s_waitcnt lgkmcnt(2)
mav 0,120,32
R 48,14336
v_mul_f32_e64 v21, v21, |v21|
mav 4,124,32
v_cvt_pk_bf16_f32 v20, v20, v21
v_pk_max_i16 v72, v20, 0
mav 8,128,32
v_pk_max_i16 v74, v20, 0
v_fma_f32 v104, v79, v66, v102
mav 0,132,36
R 52,15360
v_fma_f32 v22, v22, v78, v104
mav 4,136,36
v_fma_f32 v105, v79, v67, v103
v_fma_f32 v23, v23, v78, v105
mav 8,140,36
v_mul_f32_e64 v22, v22, |v22|
s_waitcnt lgkmcnt(2)
mav 0,144,40
R 24,16384
v_mul_f32_e64 v23, v23, |v23|
mav 4,148,40
v_cvt_pk_bf16_f32 v22, v22, v23
v_pk_max_i16 v73, v22, 0
mav 8,152,40
v_pk_max_i16 v75, v22, 0
v_permlane16_swap_b32 v72, v74
mav 0,156,44
R 28,17408
v_permlane16_swap_b32 v73, v75
mav 4,160,44
buffer_store_dwordx4 v[72:75], v84, s[36:39], s49 offen sc1
v_add_u32 v76, 1, v76
mav 8,164,44
s_waitcnt vmcnt(11)
buffer_store_dwordx2 v[76:77], v81, s[36:39], s52 offen sc1
s_waitcnt lgkmcnt(2)
mav 0,168,48
R 32,18432
mav 4,172,48
mav 8,176,48
mav 0,180,52
R 36,19456
mav 4,184,52
mav 8,188,52
s_waitcnt lgkmcnt(2)
mav 0,192,24
R 40,20480
mav 4,196,24
mav 8,200,24
mav 0,204,28
R 44,21504
mav 4,208,28
mav 8,212,28
s_waitcnt lgkmcnt(2)
mav 0,216,32
R 48,22528
mav 4,220,32
mav 8,224,32
mav 0,228,36
R 52,23552
mav 4,232,36
mav 8,236,36
s_waitcnt lgkmcnt(2)
mav 0,240,40
R 24,24576
mav 4,244,40
mav 8,248,40
mav 0,252,44
R 28,25600
mvv 4,128,44
mvv 8,132,44
s_waitcnt lgkmcnt(2)
mvv 0,136,48
R 32,26624
mvv 4,140,48
mvv 8,144,48
mvv 0,148,52
R 36,27648
mvv 4,152,52
mvv 8,156,52
s_waitcnt lgkmcnt(2)
mvv 0,160,24
R 40,28672
mvv 4,164,24
mvv 8,168,24
mvv 0,172,28
R 44,29696
mvv 4,176,28
mvv 8,180,28
s_waitcnt lgkmcnt(2)
mvv 0,184,32
R 48,30720
mvv 4,188,32
mvv 8,192,32
mvv 0,196,36
R 52,31744
mvv 4,200,36
s_add_u32 s49, s49, 346112
mvv 8,204,36
s_waitcnt vmcnt(3)
s_barrier
s_waitcnt lgkmcnt(2)
mvv 0,208,40
R 24,32768
mvv 4,212,40
ds_read_b64 v[78:79], v89 offset:0
mvv 8,216,40
mvv 0,220,44
R 28,33792
mvv 4,224,44
mvv 8,228,44
s_waitcnt lgkmcnt(3)
mvv 0,232,48
R 32,34816
mvv 4,236,48
mvv 8,240,48
mvv 0,244,52
R 36,35840
mvv 4,248,52
mvv 8,252,52
s_waitcnt lgkmcnt(2)
v_mfma_f32_16x16x32_bf16 v[12:15], a[0:3], v[24:27], 0
R 40,36864
v_fma_f32 v104, v79, v56, v92
v_mfma_f32_16x16x32_bf16 v[16:19], a[4:7], v[24:27], 0
v_fma_f32 v0, v0, v78, v104
v_fma_f32 v105, v79, v57, v93
v_mfma_f32_16x16x32_bf16 v[20:23], a[8:11], v[24:27], 0
v_fma_f32 v1, v1, v78, v105
v_mul_f32_e64 v0, v0, |v0|
mav 12,12,28
R 44,37888
v_mul_f32_e64 v1, v1, |v1|
mav 16,16,28
v_cvt_pk_bf16_f32 v0, v0, v1
v_pk_max_i16 v68, v0, 0
mav 20,20,28
v_fma_f32 v104, v79, v58, v94
s_waitcnt lgkmcnt(2)
mav 12,24,32
R 48,38912
v_fma_f32 v2, v2, v78, v104
mav 16,28,32
v_fma_f32 v105, v79, v59, v95
v_fma_f32 v3, v3, v78, v105
mav 20,32,32
v_mul_f32_e64 v2, v2, |v2|
v_mul_f32_e64 v3, v3, |v3|
mav 12,36,36
R 52,39936
v_cvt_pk_bf16_f32 v2, v2, v3
mav 16,40,36
v_pk_max_i16 v69, v2, 0
v_fma_f32 v104, v79, v60, v96
mav 20,44,36
v_fma_f32 v4, v4, v78, v104
s_waitcnt lgkmcnt(2)
mav 12,48,40
R 24,40960
v_fma_f32 v105, v79, v61, v97
mav 16,52,40
v_fma_f32 v5, v5, v78, v105
v_mul_f32_e64 v4, v4, |v4|
mav 20,56,40
v_mul_f32_e64 v5, v5, |v5|
v_cvt_pk_bf16_f32 v4, v4, v5
mav 12,60,44
R 28,41984
v_pk_max_i16 v70, v4, 0
mav 16,64,44
v_fma_f32 v104, v79, v62, v98
v_fma_f32 v6, v6, v78, v104
mav 20,68,44
v_fma_f32 v105, v79, v63, v99
s_waitcnt lgkmcnt(2)
mav 12,72,48
R 32,43008
v_fma_f32 v7, v7, v78, v105
mav 16,76,48
v_mul_f32_e64 v6, v6, |v6|
v_mul_f32_e64 v7, v7, |v7|
mav 20,80,48
v_cvt_pk_bf16_f32 v6, v6, v7
v_pk_max_i16 v71, v6, 0
mav 12,84,52
R 36,44032
buffer_store_dwordx4 v[68:71], v83, s[36:39], s49 offen sc1
mav 16,88,52
v_fma_f32 v104, v79, v64, v100
v_fma_f32 v8, v8, v78, v104
mav 20,92,52
v_fma_f32 v105, v79, v65, v101
s_waitcnt lgkmcnt(2)
mav 12,96,24
R 40,45056
v_fma_f32 v9, v9, v78, v105
mav 16,100,24
v_mul_f32_e64 v8, v8, |v8|
v_mul_f32_e64 v9, v9, |v9|
mav 20,104,24
v_cvt_pk_bf16_f32 v8, v8, v9
v_pk_max_i16 v72, v8, 0
mav 12,108,28
R 44,46080
v_pk_max_i16 v74, v8, 0
mav 16,112,28
v_fma_f32 v104, v79, v66, v102
v_fma_f32 v10, v10, v78, v104
mav 20,116,28
v_fma_f32 v105, v79, v67, v103
s_waitcnt lgkmcnt(2)
mav 12,120,32
R 48,47104
v_fma_f32 v11, v11, v78, v105
mav 16,124,32
v_mul_f32_e64 v10, v10, |v10|
v_mul_f32_e64 v11, v11, |v11|
mav 20,128,32
v_cvt_pk_bf16_f32 v10, v10, v11
v_pk_max_i16 v73, v10, 0
mav 12,132,36
R 52,48128
v_pk_max_i16 v75, v10, 0
mav 16,136,36
v_permlane16_swap_b32 v72, v74
v_permlane16_swap_b32 v73, v75
mav 20,140,36
buffer_store_dwordx4 v[72:75], v84, s[36:39], s49 offen sc1
s_waitcnt lgkmcnt(2)
mav 12,144,40
R 24,49152
v_add_u32 v76, 1, v76
mav 16,148,40
s_waitcnt vmcnt(3)
buffer_store_dwordx2 v[76:77], v81, s[36:39], s52 offen sc1
mav 20,152,40
mav 12,156,44
R 28,50176
mav 16,160,44
mav 20,164,44
s_waitcnt lgkmcnt(2)
mav 12,168,48
R 32,51200
mav 16,172,48
mav 20,176,48
mav 12,180,52
R 36,52224
mav 16,184,52
mav 20,188,52
s_waitcnt lgkmcnt(2)
mav 12,192,24
R 40,53248
mav 16,196,24
mav 20,200,24
mav 12,204,28
R 44,54272
mav 16,208,28
mav 20,212,28
s_waitcnt lgkmcnt(2)
mav 12,216,32
R 48,55296
mav 16,220,32
mav 20,224,32
mav 12,228,36
R 52,56320
mav 16,232,36
mav 20,236,36
s_waitcnt lgkmcnt(2)
mav 12,240,40
R 24,57344
mav 16,244,40
mav 20,248,40
mav 12,252,44
R 28,58368
mvv 16,128,44
mvv 20,132,44
s_waitcnt lgkmcnt(2)
mvv 12,136,48
R 32,59392
mvv 16,140,48
mvv 20,144,48
mvv 12,148,52
R 36,60416
mvv 16,152,52
mvv 20,156,52
s_waitcnt lgkmcnt(2)
mvv 12,160,24
R 40,61440
mvv 16,164,24
mvv 20,168,24
mvv 12,172,28
R 44,62464
mvv 16,176,28
mvv 20,180,28
s_waitcnt lgkmcnt(2)
mvv 12,184,32
R 48,63488
mvv 16,188,32
mvv 20,192,32
mvv 12,196,36
R 52,64512
mvv 16,200,36
s_add_u32 s49, s49, 346112
mvv 20,204,36
s_barrier
s_waitcnt lgkmcnt(2)
mvv 12,208,40
mvv 16,212,40
ds_read_b64 v[78:79], v89 offset:128
mvv 20,216,40
mvv 12,220,44
mvv 16,224,44
mvv 20,228,44
s_waitcnt lgkmcnt(1)
mvv 12,232,48
mvv 16,236,48
mvv 20,240,48
mvv 12,244,52
mvv 16,248,52
mvv 20,252,52
s_waitcnt lgkmcnt(0)
v_fma_f32 v104, v79, v56, v92
s_nop 3
v_fma_f32 v12, v12, v78, v104
v_fma_f32 v105, v79, v57, v93
v_fma_f32 v13, v13, v78, v105
v_mul_f32_e64 v12, v12, |v12|
v_mul_f32_e64 v13, v13, |v13|
v_cvt_pk_bf16_f32 v12, v12, v13
v_pk_max_i16 v68, v12, 0
v_fma_f32 v104, v79, v58, v94
v_fma_f32 v14, v14, v78, v104
v_fma_f32 v105, v79, v59, v95
v_fma_f32 v15, v15, v78, v105
v_mul_f32_e64 v14, v14, |v14|
v_mul_f32_e64 v15, v15, |v15|
v_cvt_pk_bf16_f32 v14, v14, v15
v_pk_max_i16 v69, v14, 0
v_fma_f32 v104, v79, v60, v96
v_fma_f32 v16, v16, v78, v104
v_fma_f32 v105, v79, v61, v97
v_fma_f32 v17, v17, v78, v105
v_mul_f32_e64 v16, v16, |v16|
v_mul_f32_e64 v17, v17, |v17|
v_cvt_pk_bf16_f32 v16, v16, v17
v_pk_max_i16 v70, v16, 0
v_fma_f32 v104, v79, v62, v98
v_fma_f32 v18, v18, v78, v104
v_fma_f32 v105, v79, v63, v99
v_fma_f32 v19, v19, v78, v105
v_mul_f32_e64 v18, v18, |v18|
v_mul_f32_e64 v19, v19, |v19|
v_cvt_pk_bf16_f32 v18, v18, v19
v_pk_max_i16 v71, v18, 0
buffer_store_dwordx4 v[68:71], v83, s[36:39], s49 offen sc1
v_fma_f32 v104, v79, v64, v100
v_fma_f32 v20, v20, v78, v104
v_fma_f32 v105, v79, v65, v101
v_fma_f32 v21, v21, v78, v105
v_mul_f32_e64 v20, v20, |v20|
v_mul_f32_e64 v21, v21, |v21|
v_cvt_pk_bf16_f32 v20, v20, v21
v_pk_max_i16 v72, v20, 0
v_pk_max_i16 v74, v20, 0
v_fma_f32 v104, v79, v66, v102
v_fma_f32 v22, v22, v78, v104
v_fma_f32 v105, v79, v67, v103
v_fma_f32 v23, v23, v78, v105
v_mul_f32_e64 v22, v22, |v22|
v_mul_f32_e64 v23, v23, |v23|
v_cvt_pk_bf16_f32 v22, v22, v23
v_pk_max_i16 v73, v22, 0
v_pk_max_i16 v75, v22, 0
v_permlane16_swap_b32 v72, v74
s_nop 0
v_permlane16_swap_b32 v73, v75
buffer_store_dwordx4 v[72:75], v84, s[36:39], s49 offen sc1
v_add_u32 v76, 1, v76
s_waitcnt vmcnt(3)
buffer_store_dwordx2 v[76:77], v81, s[36:39], s52 offen sc1
v_add_u32 v76, 37, v76
s_waitcnt vmcnt(1)
buffer_store_dwordx2 v[76:77], v81, s[36:39], s52 offen sc1
s_endpgm
Lr4:
s_cmp_eq_u32 s60, 4
s_cbranch_scc0 Lr5
buffer_load_dword v73, off, s[36:39], 0 sc1
s_waitcnt vmcnt(0)
v_mov_b32 v72, 0x100
v_add_u32 v73, 0x9e3779b9, v73
buffer_store_dwordx2 v[72:73], v81, s[36:39], s52 offen sc1
v_add_u32 v86, v86, v72
s_sleep 32
s_sleep 32
s_sleep 32
E 0,65536
E 1,69632
E 2,73728
E 3,77824
s_add_u32 s51, s51, 65536
E 0,81920
E 1,86016
E 2,90112
E 3,94208
s_add_u32 s51, s51, 65536
E 0,98304
E 1,102400
E 2,106496
E 3,110592
s_add_u32 s51, s51, 65536
E 0,114688
E 1,118784
E 2,122880
E 3,126976
s_add_u32 s51, s51, 65536
s_waitcnt vmcnt(8)
s_barrier
v_add_u32 v4, 65536, v4
v_add_u32 v5, 65536, v5
v_add_u32 v6, 65536, v6
v_add_u32 v7, 65536, v7
Ta 0,4,0
Ta 4,5,0
Ta 8,6,0
Ta 12,7,0
Ta 16,4,16384
Ta 20,5,16384
Ta 24,6,16384
Ta 28,7,16384
E 0,131072
E 1,135168
E 2,139264
E 3,143360
s_add_u32 s51, s51, 65536
E 0,147456
E 1,151552
E 2,155648
E 3,159744
s_add_u32 s51, s51, 65536
s_cmp_ge_u32 s51, s57
s_cselect_b32 s60, 1572864, 0
s_sub_u32 s51, s51, s60
s_waitcnt lgkmcnt(0)
s_waitcnt vmcnt(8)
s_barrier
E 0,65536
Ta 32,4,32768
Ta 36,5,32768
E 1,69632
Ta 40,6,32768
Ta 44,7,32768
E 2,73728
Ta 48,4,49152
E 3,77824
s_add_u32 s51, s51, 65536
Ta 52,5,49152
Ta 56,6,49152
E 0,81920
Ta 60,7,49152
E 1,86016
E 2,90112
E 3,94208
s_add_u32 s51, s51, 65536
s_waitcnt lgkmcnt(0)
s_waitcnt vmcnt(8)
s_barrier
v_add_u32 v4, 65536, v4
v_add_u32 v5, 65536, v5
v_add_u32 v6, 65536, v6
v_add_u32 v7, 65536, v7
E 0,98304
Ta 64,4,0
Ta 68,5,0
E 1,102400
Ta 72,6,0
Ta 76,7,0
E 2,106496
Ta 80,4,16384
E 3,110592
s_add_u32 s51, s51, 65536
Ta 84,5,16384
Ta 88,6,16384
E 0,114688
Ta 92,7,16384
E 1,118784
E 2,122880
E 3,126976
s_add_u32 s51, s51, 65536
s_waitcnt lgkmcnt(0)
s_waitcnt vmcnt(8)
s_barrier
v_add_u32 v4, -65536, v4
v_add_u32 v5, -65536, v5
v_add_u32 v6, -65536, v6
v_add_u32 v7, -65536, v7
E 0,131072
Ta 96,4,0
Ta 100,5,0
E 1,135168
Ta 104,6,0
Ta 108,7,0
E 2,139264
Ta 112,4,16384
E 3,143360
s_add_u32 s51, s51, 65536
Ta 116,5,16384
Ta 120,6,16384
E 0,147456
Ta 124,7,16384
E 1,151552
E 2,155648
E 3,159744
s_add_u32 s51, s51, 65536
s_cmp_ge_u32 s51, s57
s_cselect_b32 s60, 1572864, 0
s_sub_u32 s51, s51, s60
s_waitcnt lgkmcnt(0)
s_waitcnt vmcnt(8)
s_barrier
E 0,65536
Ta 128,4,32768
Ta 132,5,32768
E 1,69632
Ta 136,6,32768
Ta 140,7,32768
E 2,73728
Ta 144,4,49152
E 3,77824
s_add_u32 s51, s51, 65536
Ta 148,5,49152
Ta 152,6,49152
E 0,81920
Ta 156,7,49152
E 1,86016
E 2,90112
E 3,94208
s_add_u32 s51, s51, 65536
s_waitcnt lgkmcnt(0)
s_waitcnt vmcnt(8)
s_barrier
v_add_u32 v4, 65536, v4
v_add_u32 v5, 65536, v5
v_add_u32 v6, 65536, v6
v_add_u32 v7, 65536, v7
E 0,98304
Ta 160,4,0
Ta 164,5,0
E 1,102400
Ta 168,6,0
Ta 172,7,0
E 2,106496
Ta 176,4,16384
E 3,110592
s_add_u32 s51, s51, 65536
Ta 180,5,16384
Ta 184,6,16384
E 0,114688
Ta 188,7,16384
E 1,118784
E 2,122880
E 3,126976
s_add_u32 s51, s51, 65536
s_waitcnt lgkmcnt(0)
s_waitcnt vmcnt(8)
s_barrier
v_add_u32 v4, -65536, v4
v_add_u32 v5, -65536, v5
v_add_u32 v6, -65536, v6
v_add_u32 v7, -65536, v7
E 0,131072
Ta 192,4,0
Ta 196,5,0
E 1,135168
Ta 200,6,0
Ta 204,7,0
E 2,139264
Ta 208,4,16384
E 3,143360
s_add_u32 s51, s51, 65536
Ta 212,5,16384
Ta 216,6,16384
E 0,147456
Ta 220,7,16384
E 1,151552
E 2,155648
E 3,159744
s_add_u32 s51, s51, 65536
s_cmp_ge_u32 s51, s57
s_cselect_b32 s60, 1572864, 0
s_sub_u32 s51, s51, s60
s_waitcnt lgkmcnt(0)
s_waitcnt vmcnt(8)
s_barrier
E 0,65536
Ta 224,4,32768
Ta 228,5,32768
E 1,69632
Ta 232,6,32768
Ta 236,7,32768
E 2,73728
Ta 240,4,49152
E 3,77824
s_add_u32 s51, s51, 65536
Ta 244,5,49152
Ta 248,6,49152
E 0,81920
Ta 252,7,49152
E 1,86016
E 2,90112
E 3,94208
s_add_u32 s51, s51, 65536
s_waitcnt lgkmcnt(0)
s_waitcnt vmcnt(8)
s_barrier
v_add_u32 v4, 65536, v4
v_add_u32 v5, 65536, v5
v_add_u32 v6, 65536, v6
v_add_u32 v7, 65536, v7
E 0,98304
Tv 128,4,0
Tv 132,5,0
E 1,102400
Tv 136,6,0
Tv 140,7,0
E 2,106496
Tv 144,4,16384
E 3,110592
s_add_u32 s51, s51, 65536
Tv 148,5,16384
Tv 152,6,16384
E 0,114688
Tv 156,7,16384
E 1,118784
E 2,122880
E 3,126976
s_add_u32 s51, s51, 65536
s_waitcnt lgkmcnt(0)
s_waitcnt vmcnt(8)
s_barrier
v_add_u32 v4, -65536, v4
v_add_u32 v5, -65536, v5
v_add_u32 v6, -65536, v6
v_add_u32 v7, -65536, v7
E 0,131072
Tv 160,4,0
Tv 164,5,0
E 1,135168
Tv 168,6,0
Tv 172,7,0
E 2,139264
Tv 176,4,16384
E 3,143360
s_add_u32 s51, s51, 65536
Tv 180,5,16384
Tv 184,6,16384
E 0,147456
Tv 188,7,16384
E 1,151552
E 2,155648
E 3,159744
s_add_u32 s51, s51, 65536
s_cmp_ge_u32 s51, s57
s_cselect_b32 s60, 1572864, 0
s_sub_u32 s51, s51, s60
s_waitcnt lgkmcnt(0)
s_waitcnt vmcnt(8)
s_barrier
Tv 192,4,32768
Tv 196,5,32768
Tv 200,6,32768
Tv 204,7,32768
Tv 208,4,49152
Tv 212,5,49152
Tv 216,6,49152
Tv 220,7,49152
s_waitcnt lgkmcnt(0)
s_waitcnt vmcnt(0)
s_barrier
v_add_u32 v4, 65536, v4
v_add_u32 v5, 65536, v5
v_add_u32 v6, 65536, v6
v_add_u32 v7, 65536, v7
Tv 224,4,0
Tv 228,5,0
Tv 232,6,0
Tv 236,7,0
Tv 240,4,16384
Tv 244,5,16384
Tv 248,6,16384
Tv 252,7,16384
s_waitcnt lgkmcnt(0)
s_waitcnt vmcnt(0)
s_barrier
s_nop 7
s_and_b32 s60, s48, 3
s_cmp_eq_u32 s60, 1
s_cbranch_scc1 LvG1
LvG0:
v_add_u32 v72, 1, v72
buffer_load_dwordx2 v[74:75], v85, s[36:39], 0 offen sc1
s_waitcnt vmcnt(0)
ds_write_b32 v82, v74
v_sub_u32 v74, v74, v86
v_xor_b32 v75, v75, v73
v_cmp_lt_u64 vcc, s[62:63], v[74:75]
s_waitcnt lgkmcnt(0)
s_cbranch_vccnz CG0_1
BG0_1:
s_add_u32 s49, s49, 346112
s_barrier
s_add_u32 m0, s50, 0
buffer_store_dwordx2 v[72:73], v81, s[36:39], s52 offen sc1
D 83,0
D 83,1024
D 83,2048
D 83,3072
s_add_u32 m0, s50, 4096
s_nop 0
D 84,0
D 84,1024
s_mov_b32 m0, 142336
s_nop 0
buffer_load_dword v85, s[36:39], 0 offen sc1 lds
v_add_u32 v86, 1, v86
ds_read_b32 v74, v82 offset:0
v_add_u32 v72, 1, v72
s_waitcnt lgkmcnt(0)
v_cmp_gt_i32 vcc, v86, v74
s_waitcnt vmcnt(1)
s_cbranch_vccnz CG0_2
BG0_2:
s_add_u32 s49, s49, 346112
s_barrier
s_add_u32 m0, s50, 32768
R 32,0
buffer_store_dwordx2 v[72:73], v81, s[36:39], s52 offen sc1
D 83,0
R 36,1024
D 83,1024
R 40,2048
D 83,2048
s_waitcnt lgkmcnt(1)
v_mfma_f32_16x16x32_bf16 v[0:3], a[0:3], v[32:35], 0
R 44,3072
v_mfma_f32_16x16x32_bf16 v[4:7], a[4:7], v[32:35], 0
v_mfma_f32_16x16x32_bf16 v[8:11], a[8:11], v[32:35], 0
D 83,3072
v_mfma_f32_16x16x32_bf16 v[12:15], a[12:15], v[32:35], 0
mav 0,16,36
R 48,4096
mav 4,20,36
s_add_u32 m0, s50, 36864
mav 8,24,36
mav 12,28,36
D 84,0
s_waitcnt lgkmcnt(1)
mav 0,32,40
R 52,5120
mav 4,36,40
mav 8,40,40
mav 12,44,40
D 84,1024
mav 0,48,44
R 56,6144
mav 4,52,44
mav 8,56,44
mav 12,60,44
s_waitcnt lgkmcnt(1)
mav 0,64,48
R 60,7168
mav 4,68,48
mav 8,72,48
mav 12,76,48
mav 0,80,52
R 32,8192
mav 4,84,52
mav 8,88,52
mav 12,92,52
s_waitcnt lgkmcnt(1)
mav 0,96,56
R 36,9216
mav 4,100,56
mav 8,104,56
mav 12,108,56
mav 0,112,60
R 40,10240
mav 4,116,60
mav 8,120,60
mav 12,124,60
s_waitcnt lgkmcnt(1)
mav 0,128,32
R 44,11264
mav 4,132,32
mav 8,136,32
s_mov_b32 m0, 142336
mav 12,140,32
mav 0,144,36
R 48,12288
buffer_load_dword v85, s[36:39], 0 offen sc1 lds
mav 4,148,36
mav 8,152,36
mav 12,156,36
s_waitcnt lgkmcnt(1)
mav 0,160,40
R 52,13312
v_add_u32 v86, 1, v86
mav 4,164,40
mav 8,168,40
mav 12,172,40
mav 0,176,44
R 56,14336
mav 4,180,44
mav 8,184,44
mav 12,188,44
s_waitcnt lgkmcnt(1)
mav 0,192,48
R 60,15360
mav 4,196,48
mav 8,200,48
mav 12,204,48
mav 0,208,52
R 32,16384
mav 4,212,52
mav 8,216,52
mav 12,220,52
s_waitcnt lgkmcnt(1)
mav 0,224,56
R 36,17408
mav 4,228,56
mav 8,232,56
mav 12,236,56
mav 0,240,60
R 40,18432
mav 4,244,60
mav 8,248,60
mav 12,252,60
s_waitcnt lgkmcnt(1)
mvv 0,128,32
R 44,19456
mvv 4,132,32
mvv 8,136,32
mvv 12,140,32
mvv 0,144,36
R 48,20480
mvv 4,148,36
mvv 8,152,36
mvv 12,156,36
s_waitcnt lgkmcnt(1)
mvv 0,160,40
R 52,21504
mvv 4,164,40
ds_read_b32 v74, v82 offset:0
mvv 8,168,40
mvv 12,172,40
mvv 0,176,44
R 56,22528
mvv 4,180,44
mvv 8,184,44
s_xor_b32 s50, s50, 0x10000
mvv 12,188,44
s_waitcnt lgkmcnt(1)
mvv 0,192,48
R 60,23552
mvv 4,196,48
v_cmp_gt_i32 vcc, v86, v74
mvv 8,200,48
s_waitcnt vmcnt(1)
s_cbranch_vccnz CG0_3
BG0_3:
mvv 12,204,48
s_barrier
s_add_u32 s49, s49, 346112
mvv 0,208,52
R 32,32768
s_add_u32 m0, s50, 0
mvv 4,212,52
D 83,0
mvv 8,216,52
mvv 12,220,52
s_waitcnt lgkmcnt(1)
mvv 0,224,56
R 36,33792
mvv 4,228,56
D 83,1024
mvv 8,232,56
mvv 12,236,56
mvv 0,240,60
R 40,34816
mvv 4,244,60
mvv 8,248,60
D 83,2048
mvv 12,252,60
s_waitcnt lgkmcnt(1)
v_mfma_f32_16x16x32_bf16 v[16:19], a[0:3], v[32:35], 0
R 44,35840
v_cvt_pk_bf16_f32 v64, v0, v1
v_mfma_f32_16x16x32_bf16 v[20:23], a[4:7], v[32:35], 0
v_cvt_pk_bf16_f32 v65, v2, v3
v_cvt_pk_bf16_f32 v66, v4, v5
v_mfma_f32_16x16x32_bf16 v[24:27], a[8:11], v[32:35], 0
D 83,3072
v_cvt_pk_bf16_f32 v67, v6, v7
v_mfma_f32_16x16x32_bf16 v[28:31], a[12:15], v[32:35], 0
v_cvt_pk_bf16_f32 v68, v8, v9
v_cvt_pk_bf16_f32 v69, v10, v11
mav 16,16,36
R 48,36864
v_cvt_pk_bf16_f32 v70, v12, v13
mav 20,20,36
s_add_u32 m0, s50, 4096
v_cvt_pk_bf16_f32 v71, v14, v15
mav 24,24,36
buffer_store_dwordx4 v[64:67], v87, s[36:39], s49 offen sc1
mav 28,28,36
s_waitcnt lgkmcnt(1)
mav 16,32,40
R 52,37888
mav 20,36,40
mav 24,40,40
buffer_store_dwordx4 v[68:71], v87, s[36:39], s49 offen offset:1024 sc1
mav 28,44,40
mav 16,48,44
R 56,38912
mav 20,52,44
mav 24,56,44
D 84,0
mav 28,60,44
s_waitcnt lgkmcnt(1)
mav 16,64,48
R 60,39936
mav 20,68,48
mav 24,72,48
D 84,1024
mav 28,76,48
mav 16,80,52
R 32,40960
mav 20,84,52
mav 24,88,52
mav 28,92,52
s_waitcnt lgkmcnt(1)
mav 16,96,56
R 36,41984
mav 20,100,56
mav 24,104,56
mav 28,108,56
mav 16,112,60
R 40,43008
mav 20,116,60
mav 24,120,60
mav 28,124,60
s_waitcnt lgkmcnt(1)
mav 16,128,32
R 44,44032
mav 20,132,32
mav 24,136,32
s_mov_b32 m0, 142336
mav 28,140,32
mav 16,144,36
R 48,45056
buffer_load_dword v85, s[36:39], 0 offen sc1 lds
mav 20,148,36
mav 24,152,36
mav 28,156,36
s_waitcnt lgkmcnt(1)
mav 16,160,40
R 52,46080
v_add_u32 v86, 1, v86
mav 20,164,40
mav 24,168,40
mav 28,172,40
mav 16,176,44
R 56,47104
mav 20,180,44
mav 24,184,44
mav 28,188,44
s_waitcnt lgkmcnt(1)
mav 16,192,48
R 60,48128
mav 20,196,48
mav 24,200,48
mav 28,204,48
mav 16,208,52
R 32,49152
mav 20,212,52
mav 24,216,52
v_add_u32 v72, 1, v72
mav 28,220,52
s_waitcnt lgkmcnt(1)
mav 16,224,56
R 36,50176
mav 20,228,56
mav 24,232,56
mav 28,236,56
mav 16,240,60
R 40,51200
mav 20,244,60
s_waitcnt vmcnt(3)
buffer_store_dwordx2 v[72:73], v81, s[36:39], s52 offen sc1
)"
R"(
mav 24,248,60
mav 28,252,60
s_waitcnt lgkmcnt(1)
mvv 16,128,32
R 44,52224
mvv 20,132,32
mvv 24,136,32
mvv 28,140,32
mvv 16,144,36
R 48,53248
mvv 20,148,36
mvv 24,152,36
mvv 28,156,36
s_waitcnt lgkmcnt(1)
mvv 16,160,40
R 52,54272
mvv 20,164,40
ds_read_b32 v74, v82 offset:0
mvv 24,168,40
mvv 28,172,40
mvv 16,176,44
R 56,55296
mvv 20,180,44
mvv 24,184,44
mvv 28,188,44
s_waitcnt lgkmcnt(1)
mvv 16,192,48
R 60,56320
mvv 20,196,48
v_xor_b32 v80, 0x10000, v80
v_cmp_gt_i32 vcc, v86, v74
mvv 24,200,48
s_waitcnt vmcnt(2)
s_cbranch_vccnz CG0_4
BG0_4:
mvv 28,204,48
s_barrier
s_add_u32 s49, s49, 346112
mvv 16,208,52
R 32,0
s_add_u32 m0, s50, 32768
mvv 20,212,52
D 83,0
mvv 24,216,52
mvv 28,220,52
s_waitcnt lgkmcnt(1)
mvv 16,224,56
R 36,1024
mvv 20,228,56
D 83,1024
mvv 24,232,56
mvv 28,236,56
mvv 16,240,60
R 40,2048
mvv 20,244,60
mvv 24,248,60
D 83,2048
mvv 28,252,60
s_mov_b32 s60, -30
.p2align 6
LG0_5:
s_waitcnt lgkmcnt(1)
v_mfma_f32_16x16x32_bf16 v[0:3], a[0:3], v[32:35], 0
R 44,3072
v_cvt_pk_bf16_f32 v64, v16, v17
v_mfma_f32_16x16x32_bf16 v[4:7], a[4:7], v[32:35], 0
v_cvt_pk_bf16_f32 v65, v18, v19
v_cvt_pk_bf16_f32 v66, v20, v21
v_mfma_f32_16x16x32_bf16 v[8:11], a[8:11], v[32:35], 0
D 83,3072
v_cvt_pk_bf16_f32 v67, v22, v23
v_mfma_f32_16x16x32_bf16 v[12:15], a[12:15], v[32:35], 0
v_cvt_pk_bf16_f32 v68, v24, v25
v_cvt_pk_bf16_f32 v69, v26, v27
mav 0,16,36
R 48,4096
v_cvt_pk_bf16_f32 v70, v28, v29
mav 4,20,36
s_add_u32 m0, s50, 36864
v_cvt_pk_bf16_f32 v71, v30, v31
mav 8,24,36
buffer_store_dwordx4 v[64:67], v87, s[36:39], s49 offen sc1
mav 12,28,36
s_waitcnt lgkmcnt(1)
mav 0,32,40
R 52,5120
mav 4,36,40
mav 8,40,40
buffer_store_dwordx4 v[68:71], v87, s[36:39], s49 offen offset:1024 sc1
mav 12,44,40
mav 0,48,44
R 56,6144
mav 4,52,44
mav 8,56,44
D 84,0
mav 12,60,44
s_waitcnt lgkmcnt(1)
mav 0,64,48
R 60,7168
mav 4,68,48
mav 8,72,48
D 84,1024
mav 12,76,48
mav 0,80,52
R 32,8192
mav 4,84,52
mav 8,88,52
mav 12,92,52
s_waitcnt lgkmcnt(1)
mav 0,96,56
R 36,9216
mav 4,100,56
mav 8,104,56
mav 12,108,56
mav 0,112,60
R 40,10240
mav 4,116,60
mav 8,120,60
mav 12,124,60
s_waitcnt lgkmcnt(1)
mav 0,128,32
R 44,11264
mav 4,132,32
mav 8,136,32
s_mov_b32 m0, 142336
mav 12,140,32
mav 0,144,36
R 48,12288
buffer_load_dword v85, s[36:39], 0 offen sc1 lds
mav 4,148,36
mav 8,152,36
mav 12,156,36
s_waitcnt lgkmcnt(1)
mav 0,160,40
R 52,13312
v_add_u32 v86, 1, v86
mav 4,164,40
mav 8,168,40
mav 12,172,40
mav 0,176,44
R 56,14336
mav 4,180,44
mav 8,184,44
mav 12,188,44
s_waitcnt lgkmcnt(1)
mav 0,192,48
R 60,15360
mav 4,196,48
mav 8,200,48
mav 12,204,48
mav 0,208,52
R 32,16384
mav 4,212,52
mav 8,216,52
v_add_u32 v72, 1, v72
mav 12,220,52
s_waitcnt lgkmcnt(1)
mav 0,224,56
R 36,17408
mav 4,228,56
mav 8,232,56
mav 12,236,56
mav 0,240,60
R 40,18432
mav 4,244,60
s_waitcnt vmcnt(3)
buffer_store_dwordx2 v[72:73], v81, s[36:39], s52 offen sc1
mav 8,248,60
mav 12,252,60
s_waitcnt lgkmcnt(1)
mvv 0,128,32
R 44,19456
mvv 4,132,32
mvv 8,136,32
mvv 12,140,32
mvv 0,144,36
R 48,20480
mvv 4,148,36
mvv 8,152,36
mvv 12,156,36
s_waitcnt lgkmcnt(1)
mvv 0,160,40
R 52,21504
mvv 4,164,40
ds_read_b32 v74, v82 offset:0
mvv 8,168,40
mvv 12,172,40
mvv 0,176,44
R 56,22528
mvv 4,180,44
mvv 8,184,44
s_xor_b32 s50, s50, 0x10000
mvv 12,188,44
s_waitcnt lgkmcnt(1)
mvv 0,192,48
R 60,23552
mvv 4,196,48
v_cmp_gt_i32 vcc, v86, v74
mvv 8,200,48
s_waitcnt vmcnt(2)
s_cbranch_vccnz CG0_6
BG0_6:
mvv 12,204,48
s_barrier
s_add_u32 s49, s49, 346112
mvv 0,208,52
R 32,32768
s_add_u32 m0, s50, 0
mvv 4,212,52
D 83,0
mvv 8,216,52
mvv 12,220,52
s_waitcnt lgkmcnt(1)
mvv 0,224,56
R 36,33792
mvv 4,228,56
D 83,1024
mvv 8,232,56
mvv 12,236,56
mvv 0,240,60
R 40,34816
mvv 4,244,60
mvv 8,248,60
D 83,2048
mvv 12,252,60
s_waitcnt lgkmcnt(1)
v_mfma_f32_16x16x32_bf16 v[16:19], a[0:3], v[32:35], 0
R 44,35840
v_cvt_pk_bf16_f32 v64, v0, v1
v_mfma_f32_16x16x32_bf16 v[20:23], a[4:7], v[32:35], 0
v_cvt_pk_bf16_f32 v65, v2, v3
v_cvt_pk_bf16_f32 v66, v4, v5
v_mfma_f32_16x16x32_bf16 v[24:27], a[8:11], v[32:35], 0
D 83,3072
v_cvt_pk_bf16_f32 v67, v6, v7
v_mfma_f32_16x16x32_bf16 v[28:31], a[12:15], v[32:35], 0
v_cvt_pk_bf16_f32 v68, v8, v9
v_cvt_pk_bf16_f32 v69, v10, v11
mav 16,16,36
R 48,36864
v_cvt_pk_bf16_f32 v70, v12, v13
mav 20,20,36
s_add_u32 m0, s50, 4096
v_cvt_pk_bf16_f32 v71, v14, v15
mav 24,24,36
buffer_store_dwordx4 v[64:67], v87, s[36:39], s49 offen sc1
mav 28,28,36
s_waitcnt lgkmcnt(1)
mav 16,32,40
R 52,37888
mav 20,36,40
mav 24,40,40
buffer_store_dwordx4 v[68:71], v87, s[36:39], s49 offen offset:1024 sc1
mav 28,44,40
mav 16,48,44
R 56,38912
mav 20,52,44
mav 24,56,44
D 84,0
mav 28,60,44
s_waitcnt lgkmcnt(1)
mav 16,64,48
R 60,39936
mav 20,68,48
mav 24,72,48
D 84,1024
mav 28,76,48
mav 16,80,52
R 32,40960
mav 20,84,52
mav 24,88,52
mav 28,92,52
s_waitcnt lgkmcnt(1)
mav 16,96,56
R 36,41984
mav 20,100,56
mav 24,104,56
mav 28,108,56
mav 16,112,60
R 40,43008
mav 20,116,60
mav 24,120,60
mav 28,124,60
s_waitcnt lgkmcnt(1)
mav 16,128,32
R 44,44032
mav 20,132,32
mav 24,136,32
s_mov_b32 m0, 142336
mav 28,140,32
mav 16,144,36
R 48,45056
buffer_load_dword v85, s[36:39], 0 offen sc1 lds
mav 20,148,36
mav 24,152,36
mav 28,156,36
s_waitcnt lgkmcnt(1)
mav 16,160,40
R 52,46080
v_add_u32 v86, 1, v86
mav 20,164,40
mav 24,168,40
mav 28,172,40
mav 16,176,44
R 56,47104
mav 20,180,44
mav 24,184,44
mav 28,188,44
s_waitcnt lgkmcnt(1)
mav 16,192,48
R 60,48128
mav 20,196,48
mav 24,200,48
mav 28,204,48
mav 16,208,52
R 32,49152
mav 20,212,52
mav 24,216,52
v_add_u32 v72, 1, v72
mav 28,220,52
s_waitcnt lgkmcnt(1)
mav 16,224,56
R 36,50176
mav 20,228,56
mav 24,232,56
mav 28,236,56
mav 16,240,60
R 40,51200
mav 20,244,60
s_waitcnt vmcnt(3)
buffer_store_dwordx2 v[72:73], v81, s[36:39], s52 offen sc1
mav 24,248,60
mav 28,252,60
s_waitcnt lgkmcnt(1)
mvv 16,128,32
R 44,52224
mvv 20,132,32
mvv 24,136,32
mvv 28,140,32
mvv 16,144,36
R 48,53248
mvv 20,148,36
mvv 24,152,36
mvv 28,156,36
s_waitcnt lgkmcnt(1)
mvv 16,160,40
R 52,54272
mvv 20,164,40
ds_read_b32 v74, v82 offset:0
mvv 24,168,40
mvv 28,172,40
mvv 16,176,44
R 56,55296
mvv 20,180,44
mvv 24,184,44
mvv 28,188,44
s_waitcnt lgkmcnt(1)
mvv 16,192,48
R 60,56320
mvv 20,196,48
v_xor_b32 v80, 0x10000, v80
v_cmp_gt_i32 vcc, v86, v74
mvv 24,200,48
s_waitcnt vmcnt(2)
s_cbranch_vccnz CG0_7
BG0_7:
mvv 28,204,48
s_barrier
s_add_u32 s49, s49, 346112
mvv 16,208,52
R 32,0
s_add_u32 m0, s50, 32768
mvv 20,212,52
D 83,0
mvv 24,216,52
mvv 28,220,52
s_waitcnt lgkmcnt(1)
mvv 16,224,56
R 36,1024
mvv 20,228,56
D 83,1024
mvv 24,232,56
mvv 28,236,56
mvv 16,240,60
R 40,2048
mvv 20,244,60
mvv 24,248,60
D 83,2048
mvv 28,252,60
s_add_u32 s60, s60, 1
s_cbranch_scc0 LG0_5
s_waitcnt lgkmcnt(1)
v_mfma_f32_16x16x32_bf16 v[0:3], a[0:3], v[32:35], 0
R 44,3072
v_cvt_pk_bf16_f32 v64, v16, v17
v_mfma_f32_16x16x32_bf16 v[4:7], a[4:7], v[32:35], 0
v_cvt_pk_bf16_f32 v65, v18, v19
v_cvt_pk_bf16_f32 v66, v20, v21
v_mfma_f32_16x16x32_bf16 v[8:11], a[8:11], v[32:35], 0
D 83,3072
v_cvt_pk_bf16_f32 v67, v22, v23
v_mfma_f32_16x16x32_bf16 v[12:15], a[12:15], v[32:35], 0
v_cvt_pk_bf16_f32 v68, v24, v25
v_cvt_pk_bf16_f32 v69, v26, v27
mav 0,16,36
R 48,4096
v_cvt_pk_bf16_f32 v70, v28, v29
mav 4,20,36
s_add_u32 m0, s50, 36864
v_cvt_pk_bf16_f32 v71, v30, v31
mav 8,24,36
buffer_store_dwordx4 v[64:67], v87, s[36:39], s49 offen sc1
mav 12,28,36
s_waitcnt lgkmcnt(1)
mav 0,32,40
R 52,5120
mav 4,36,40
mav 8,40,40
buffer_store_dwordx4 v[68:71], v87, s[36:39], s49 offen offset:1024 sc1
mav 12,44,40
mav 0,48,44
R 56,6144
mav 4,52,44
mav 8,56,44
D 84,0
mav 12,60,44
s_waitcnt lgkmcnt(1)
mav 0,64,48
R 60,7168
mav 4,68,48
mav 8,72,48
D 84,1024
mav 12,76,48
mav 0,80,52
R 32,8192
mav 4,84,52
mav 8,88,52
mav 12,92,52
s_waitcnt lgkmcnt(1)
mav 0,96,56
R 36,9216
mav 4,100,56
mav 8,104,56
mav 12,108,56
mav 0,112,60
R 40,10240
mav 4,116,60
mav 8,120,60
mav 12,124,60
s_waitcnt lgkmcnt(1)
mav 0,128,32
R 44,11264
mav 4,132,32
mav 8,136,32
mav 12,140,32
mav 0,144,36
R 48,12288
mav 4,148,36
mav 8,152,36
mav 12,156,36
s_waitcnt lgkmcnt(1)
mav 0,160,40
R 52,13312
mav 4,164,40
mav 8,168,40
mav 12,172,40
mav 0,176,44
R 56,14336
mav 4,180,44
mav 8,184,44
mav 12,188,44
s_waitcnt lgkmcnt(1)
mav 0,192,48
R 60,15360
mav 4,196,48
mav 8,200,48
mav 12,204,48
mav 0,208,52
R 32,16384
mav 4,212,52
mav 8,216,52
v_add_u32 v72, 1, v72
mav 12,220,52
s_waitcnt lgkmcnt(1)
mav 0,224,56
R 36,17408
mav 4,228,56
mav 8,232,56
mav 12,236,56
mav 0,240,60
R 40,18432
mav 4,244,60
s_waitcnt vmcnt(2)
buffer_store_dwordx2 v[72:73], v81, s[36:39], s52 offen sc1
mav 8,248,60
mav 12,252,60
s_waitcnt lgkmcnt(1)
mvv 0,128,32
R 44,19456
mvv 4,132,32
mvv 8,136,32
mvv 12,140,32
mvv 0,144,36
R 48,20480
mvv 4,148,36
mvv 8,152,36
mvv 12,156,36
s_waitcnt lgkmcnt(1)
mvv 0,160,40
R 52,21504
mvv 4,164,40
mvv 8,168,40
mvv 12,172,40
mvv 0,176,44
R 56,22528
mvv 4,180,44
mvv 8,184,44
mvv 12,188,44
s_waitcnt lgkmcnt(1)
mvv 0,192,48
R 60,23552
mvv 4,196,48
mvv 8,200,48
s_add_u32 s49, s49, 346112
mvv 12,204,48
s_waitcnt vmcnt(1)
s_barrier
mvv 0,208,52
R 32,32768
mvv 4,212,52
mvv 8,216,52
mvv 12,220,52
s_waitcnt lgkmcnt(1)
mvv 0,224,56
R 36,33792
mvv 4,228,56
mvv 8,232,56
mvv 12,236,56
mvv 0,240,60
R 40,34816
mvv 4,244,60
mvv 8,248,60
mvv 12,252,60
s_waitcnt lgkmcnt(1)
v_mfma_f32_16x16x32_bf16 v[16:19], a[0:3], v[32:35], 0
R 44,35840
v_mfma_f32_16x16x32_bf16 v[20:23], a[4:7], v[32:35], 0
v_cvt_pk_bf16_f32 v64, v0, v1
v_cvt_pk_bf16_f32 v65, v2, v3
v_mfma_f32_16x16x32_bf16 v[24:27], a[8:11], v[32:35], 0
v_cvt_pk_bf16_f32 v66, v4, v5
v_cvt_pk_bf16_f32 v67, v6, v7
v_mfma_f32_16x16x32_bf16 v[28:31], a[12:15], v[32:35], 0
buffer_store_dwordx4 v[64:67], v87, s[36:39], s49 offen sc1
v_cvt_pk_bf16_f32 v68, v8, v9
mav 16,16,36
R 48,36864
v_cvt_pk_bf16_f32 v69, v10, v11
mav 20,20,36
v_cvt_pk_bf16_f32 v70, v12, v13
v_cvt_pk_bf16_f32 v71, v14, v15
mav 24,24,36
mav 28,28,36
buffer_store_dwordx4 v[68:71], v87, s[36:39], s49 offen offset:1024 sc1
s_waitcnt lgkmcnt(1)
mav 16,32,40
R 52,37888
mav 20,36,40
mav 24,40,40
mav 28,44,40
mav 16,48,44
R 56,38912
mav 20,52,44
mav 24,56,44
mav 28,60,44
s_waitcnt lgkmcnt(1)
mav 16,64,48
R 60,39936
mav 20,68,48
mav 24,72,48
mav 28,76,48
mav 16,80,52
R 32,40960
mav 20,84,52
mav 24,88,52
mav 28,92,52
s_waitcnt lgkmcnt(1)
mav 16,96,56
R 36,41984
mav 20,100,56
mav 24,104,56
mav 28,108,56
mav 16,112,60
R 40,43008
mav 20,116,60
mav 24,120,60
mav 28,124,60
s_waitcnt lgkmcnt(1)
mav 16,128,32
R 44,44032
mav 20,132,32
mav 24,136,32
mav 28,140,32
mav 16,144,36
R 48,45056
mav 20,148,36
mav 24,152,36
mav 28,156,36
s_waitcnt lgkmcnt(1)
mav 16,160,40
R 52,46080
mav 20,164,40
mav 24,168,40
mav 28,172,40
mav 16,176,44
R 56,47104
mav 20,180,44
mav 24,184,44
mav 28,188,44
s_waitcnt lgkmcnt(1)
mav 16,192,48
R 60,48128
mav 20,196,48
mav 24,200,48
mav 28,204,48
mav 16,208,52
R 32,49152
mav 20,212,52
mav 24,216,52
v_add_u32 v72, 1, v72
mav 28,220,52
s_waitcnt lgkmcnt(1)
mav 16,224,56
R 36,50176
mav 20,228,56
mav 24,232,56
mav 28,236,56
mav 16,240,60
R 40,51200
mav 20,244,60
s_waitcnt vmcnt(0)
buffer_store_dwordx2 v[72:73], v81, s[36:39], s52 offen sc1
mav 24,248,60
mav 28,252,60
s_waitcnt lgkmcnt(1)
mvv 16,128,32
R 44,52224
mvv 20,132,32
mvv 24,136,32
mvv 28,140,32
mvv 16,144,36
R 48,53248
mvv 20,148,36
mvv 24,152,36
mvv 28,156,36
s_waitcnt lgkmcnt(1)
mvv 16,160,40
R 52,54272
mvv 20,164,40
mvv 24,168,40
mvv 28,172,40
mvv 16,176,44
R 56,55296
mvv 20,180,44
mvv 24,184,44
mvv 28,188,44
s_waitcnt lgkmcnt(1)
mvv 16,192,48
R 60,56320
mvv 20,196,48
mvv 24,200,48
s_add_u32 s49, s49, 346112
mvv 28,204,48
mvv 16,208,52
mvv 20,212,52
mvv 24,216,52
mvv 28,220,52
s_waitcnt lgkmcnt(0)
mvv 16,224,56
mvv 20,228,56
mvv 24,232,56
mvv 28,236,56
mvv 16,240,60
mvv 20,244,60
mvv 24,248,60
mvv 28,252,60
s_nop 4
v_cvt_pk_bf16_f32 v64, v16, v17
v_cvt_pk_bf16_f32 v65, v18, v19
v_cvt_pk_bf16_f32 v66, v20, v21
v_cvt_pk_bf16_f32 v67, v22, v23
buffer_store_dwordx4 v[64:67], v87, s[36:39], s49 offen sc1
v_cvt_pk_bf16_f32 v68, v24, v25
v_cvt_pk_bf16_f32 v69, v26, v27
v_cvt_pk_bf16_f32 v70, v28, v29
v_cvt_pk_bf16_f32 v71, v30, v31
buffer_store_dwordx4 v[68:71], v87, s[36:39], s49 offen offset:1024 sc1
v_add_u32 v72, 35, v72
s_waitcnt vmcnt(0)
buffer_store_dwordx2 v[72:73], v81, s[36:39], s52 offen sc1
s_endpgm
CG0_1:
s_cmp_eq_u32 s61, 0
s_cbranch_scc1 BG0_1
s_sub_u32 s61, s61, 1
s_sleep 8
buffer_load_dwordx2 v[74:75], v85, s[36:39], 0 offen sc1
s_waitcnt vmcnt(0)
ds_write_b32 v82, v74
v_sub_u32 v74, v74, v86
v_xor_b32 v75, v75, v73
v_cmp_lt_u64 vcc, s[62:63], v[74:75]
s_waitcnt lgkmcnt(0)
s_cbranch_vccnz CG0_1
s_branch BG0_1
CG0_2:
s_mov_b32 s58, m0
s_mov_b32 m0, 142336
DG0_2:
s_cmp_eq_u32 s61, 0
s_cbranch_scc1 EG0_2
s_sub_u32 s61, s61, 1
buffer_load_dword v85, s[36:39], 0 offen sc1 lds
s_sleep 1
ds_read_b32 v74, v82
s_waitcnt lgkmcnt(0)
v_cmp_gt_i32 vcc, v86, v74
s_cbranch_vccnz DG0_2
EG0_2:
s_mov_b32 m0, s58
s_branch BG0_2
CG0_3:
s_mov_b32 s58, m0
s_mov_b32 m0, 142336
DG0_3:
s_cmp_eq_u32 s61, 0
s_cbranch_scc1 EG0_3
s_sub_u32 s61, s61, 1
buffer_load_dword v85, s[36:39], 0 offen sc1 lds
s_sleep 1
ds_read_b32 v74, v82
s_waitcnt lgkmcnt(0)
v_cmp_gt_i32 vcc, v86, v74
s_cbranch_vccnz DG0_3
EG0_3:
s_mov_b32 m0, s58
s_branch BG0_3
CG0_4:
s_mov_b32 s58, m0
s_mov_b32 m0, 142336
DG0_4:
s_cmp_eq_u32 s61, 0
s_cbranch_scc1 EG0_4
s_sub_u32 s61, s61, 1
buffer_load_dword v85, s[36:39], 0 offen sc1 lds
s_sleep 1
ds_read_b32 v74, v82
s_waitcnt lgkmcnt(0)
v_cmp_gt_i32 vcc, v86, v74
s_cbranch_vccnz DG0_4
EG0_4:
s_mov_b32 m0, s58
s_branch BG0_4
CG0_6:
s_mov_b32 s58, m0
s_mov_b32 m0, 142336
DG0_6:
s_cmp_eq_u32 s61, 0
s_cbranch_scc1 EG0_6
s_sub_u32 s61, s61, 1
buffer_load_dword v85, s[36:39], 0 offen sc1 lds
s_sleep 1
ds_read_b32 v74, v82
s_waitcnt lgkmcnt(0)
v_cmp_gt_i32 vcc, v86, v74
s_cbranch_vccnz DG0_6
EG0_6:
s_mov_b32 m0, s58
s_branch BG0_6
CG0_7:
s_mov_b32 s58, m0
s_mov_b32 m0, 142336
DG0_7:
s_cmp_eq_u32 s61, 0
s_cbranch_scc1 EG0_7
s_sub_u32 s61, s61, 1
buffer_load_dword v85, s[36:39], 0 offen sc1 lds
s_sleep 1
ds_read_b32 v74, v82
s_waitcnt lgkmcnt(0)
v_cmp_gt_i32 vcc, v86, v74
s_cbranch_vccnz DG0_7
EG0_7:
s_mov_b32 m0, s58
s_branch BG0_7
LvG1:
v_add_u32 v72, 1, v72
s_add_u32 s49, s49, 346112
s_barrier
s_add_u32 m0, s50, 0
buffer_store_dwordx2 v[72:73], v81, s[36:39], s52 offen sc1
D 83,0
D 83,1024
D 83,2048
D 83,3072
s_add_u32 m0, s50, 4096
s_nop 0
D 84,0
D 84,1024
v_add_u32 v72, 1, v72
s_add_u32 s49, s49, 346112
s_waitcnt vmcnt(0)
s_barrier
s_add_u32 m0, s50, 32768
R 32,0
buffer_store_dwordx2 v[72:73], v81, s[36:39], s52 offen sc1
D 83,0
R 36,1024
D 83,1024
R 40,2048
D 83,2048
s_waitcnt lgkmcnt(1)
v_mfma_f32_16x16x32_bf16 v[0:3], a[0:3], v[32:35], 0
R 44,3072
v_mfma_f32_16x16x32_bf16 v[4:7], a[4:7], v[32:35], 0
v_mfma_f32_16x16x32_bf16 v[8:11], a[8:11], v[32:35], 0
D 83,3072
v_mfma_f32_16x16x32_bf16 v[12:15], a[12:15], v[32:35], 0
mav 0,16,36
R 48,4096
mav 4,20,36
s_add_u32 m0, s50, 36864
mav 8,24,36
mav 12,28,36
D 84,0
s_waitcnt lgkmcnt(1)
mav 0,32,40
R 52,5120
mav 4,36,40
mav 8,40,40
mav 12,44,40
D 84,1024
mav 0,48,44
R 56,6144
mav 4,52,44
mav 8,56,44
mav 12,60,44
s_waitcnt lgkmcnt(1)
mav 0,64,48
R 60,7168
mav 4,68,48
mav 8,72,48
mav 12,76,48
mav 0,80,52
R 32,8192
mav 4,84,52
mav 8,88,52
mav 12,92,52
s_waitcnt lgkmcnt(1)
mav 0,96,56
R 36,9216
mav 4,100,56
mav 8,104,56
mav 12,108,56
mav 0,112,60
R 40,10240
mav 4,116,60
mav 8,120,60
mav 12,124,60
s_waitcnt lgkmcnt(1)
mav 0,128,32
R 44,11264
mav 4,132,32
mav 8,136,32
mav 12,140,32
mav 0,144,36
R 48,12288
mav 4,148,36
mav 8,152,36
mav 12,156,36
s_waitcnt lgkmcnt(1)
mav 0,160,40
R 52,13312
mav 4,164,40
mav 8,168,40
mav 12,172,40
mav 0,176,44
R 56,14336
mav 4,180,44
mav 8,184,44
mav 12,188,44
s_waitcnt lgkmcnt(1)
mav 0,192,48
R 60,15360
mav 4,196,48
mav 8,200,48
mav 12,204,48
mav 0,208,52
R 32,16384
mav 4,212,52
mav 8,216,52
mav 12,220,52
s_waitcnt lgkmcnt(1)
mav 0,224,56
R 36,17408
mav 4,228,56
mav 8,232,56
mav 12,236,56
mav 0,240,60
R 40,18432
mav 4,244,60
mav 8,248,60
mav 12,252,60
s_waitcnt lgkmcnt(1)
mvv 0,128,32
R 44,19456
mvv 4,132,32
mvv 8,136,32
mvv 12,140,32
mvv 0,144,36
R 48,20480
mvv 4,148,36
mvv 8,152,36
mvv 12,156,36
s_waitcnt lgkmcnt(1)
mvv 0,160,40
R 52,21504
mvv 4,164,40
mvv 8,168,40
mvv 12,172,40
mvv 0,176,44
R 56,22528
mvv 4,180,44
mvv 8,184,44
s_xor_b32 s50, s50, 0x10000
mvv 12,188,44
s_waitcnt lgkmcnt(1)
mvv 0,192,48
R 60,23552
mvv 4,196,48
mvv 8,200,48
s_add_u32 s49, s49, 346112
mvv 12,204,48
s_waitcnt vmcnt(0)
s_barrier
mvv 0,208,52
R 32,32768
s_add_u32 m0, s50, 0
mvv 4,212,52
D 83,0
mvv 8,216,52
mvv 12,220,52
s_waitcnt lgkmcnt(1)
mvv 0,224,56
R 36,33792
mvv 4,228,56
D 83,1024
mvv 8,232,56
mvv 12,236,56
mvv 0,240,60
R 40,34816
mvv 4,244,60
mvv 8,248,60
D 83,2048
mvv 12,252,60
s_waitcnt lgkmcnt(1)
v_mfma_f32_16x16x32_bf16 v[16:19], a[0:3], v[32:35], 0
R 44,35840
v_cvt_pk_bf16_f32 v64, v0, v1
v_mfma_f32_16x16x32_bf16 v[20:23], a[4:7], v[32:35], 0
v_cvt_pk_bf16_f32 v65, v2, v3
v_cvt_pk_bf16_f32 v66, v4, v5
v_mfma_f32_16x16x32_bf16 v[24:27], a[8:11], v[32:35], 0
D 83,3072
v_cvt_pk_bf16_f32 v67, v6, v7
v_mfma_f32_16x16x32_bf16 v[28:31], a[12:15], v[32:35], 0
v_cvt_pk_bf16_f32 v68, v8, v9
v_cvt_pk_bf16_f32 v69, v10, v11
mav 16,16,36
R 48,36864
v_cvt_pk_bf16_f32 v70, v12, v13
mav 20,20,36
s_add_u32 m0, s50, 4096
v_cvt_pk_bf16_f32 v71, v14, v15
mav 24,24,36
buffer_store_dwordx4 v[64:67], v87, s[36:39], s49 offen sc1
mav 28,28,36
s_waitcnt lgkmcnt(1)
mav 16,32,40
R 52,37888
mav 20,36,40
mav 24,40,40
buffer_store_dwordx4 v[68:71], v87, s[36:39], s49 offen offset:1024 sc1
mav 28,44,40
mav 16,48,44
R 56,38912
mav 20,52,44
mav 24,56,44
D 84,0
mav 28,60,44
s_waitcnt lgkmcnt(1)
mav 16,64,48
R 60,39936
mav 20,68,48
mav 24,72,48
D 84,1024
mav 28,76,48
mav 16,80,52
R 32,40960
mav 20,84,52
mav 24,88,52
mav 28,92,52
s_waitcnt lgkmcnt(1)
mav 16,96,56
R 36,41984
mav 20,100,56
mav 24,104,56
mav 28,108,56
mav 16,112,60
R 40,43008
mav 20,116,60
mav 24,120,60
mav 28,124,60
s_waitcnt lgkmcnt(1)
mav 16,128,32
R 44,44032
mav 20,132,32
mav 24,136,32
mav 28,140,32
mav 16,144,36
R 48,45056
mav 20,148,36
mav 24,152,36
mav 28,156,36
s_waitcnt lgkmcnt(1)
mav 16,160,40
R 52,46080
mav 20,164,40
mav 24,168,40
mav 28,172,40
mav 16,176,44
R 56,47104
mav 20,180,44
mav 24,184,44
mav 28,188,44
s_waitcnt lgkmcnt(1)
mav 16,192,48
R 60,48128
mav 20,196,48
mav 24,200,48
mav 28,204,48
mav 16,208,52
R 32,49152
mav 20,212,52
mav 24,216,52
v_add_u32 v72, 1, v72
mav 28,220,52
s_waitcnt lgkmcnt(1)
mav 16,224,56
R 36,50176
mav 20,228,56
mav 24,232,56
mav 28,236,56
mav 16,240,60
R 40,51200
mav 20,244,60
s_waitcnt vmcnt(2)
buffer_store_dwordx2 v[72:73], v81, s[36:39], s52 offen sc1
mav 24,248,60
mav 28,252,60
s_waitcnt lgkmcnt(1)
mvv 16,128,32
R 44,52224
mvv 20,132,32
mvv 24,136,32
mvv 28,140,32
mvv 16,144,36
R 48,53248
mvv 20,148,36
mvv 24,152,36
mvv 28,156,36
s_waitcnt lgkmcnt(1)
mvv 16,160,40
R 52,54272
mvv 20,164,40
mvv 24,168,40
mvv 28,172,40
mvv 16,176,44
R 56,55296
mvv 20,180,44
mvv 24,184,44
mvv 28,188,44
s_waitcnt lgkmcnt(1)
mvv 16,192,48
R 60,56320
mvv 20,196,48
v_xor_b32 v80, 0x10000, v80
mvv 24,200,48
s_add_u32 s49, s49, 346112
mvv 28,204,48
s_waitcnt vmcnt(1)
s_barrier
mvv 16,208,52
R 32,0
s_add_u32 m0, s50, 32768
mvv 20,212,52
D 83,0
mvv 24,216,52
mvv 28,220,52
s_waitcnt lgkmcnt(1)
mvv 16,224,56
R 36,1024
mvv 20,228,56
D 83,1024
mvv 24,232,56
mvv 28,236,56
mvv 16,240,60
R 40,2048
mvv 20,244,60
mvv 24,248,60
D 83,2048
mvv 28,252,60
s_mov_b32 s60, -30
.p2align 6
LG1_1:
s_waitcnt lgkmcnt(1)
v_mfma_f32_16x16x32_bf16 v[0:3], a[0:3], v[32:35], 0
R 44,3072
v_cvt_pk_bf16_f32 v64, v16, v17
v_mfma_f32_16x16x32_bf16 v[4:7], a[4:7], v[32:35], 0
v_cvt_pk_bf16_f32 v65, v18, v19
v_cvt_pk_bf16_f32 v66, v20, v21
v_mfma_f32_16x16x32_bf16 v[8:11], a[8:11], v[32:35], 0
D 83,3072
v_cvt_pk_bf16_f32 v67, v22, v23
v_mfma_f32_16x16x32_bf16 v[12:15], a[12:15], v[32:35], 0
v_cvt_pk_bf16_f32 v68, v24, v25
v_cvt_pk_bf16_f32 v69, v26, v27
mav 0,16,36
R 48,4096
v_cvt_pk_bf16_f32 v70, v28, v29
mav 4,20,36
s_add_u32 m0, s50, 36864
v_cvt_pk_bf16_f32 v71, v30, v31
mav 8,24,36
buffer_store_dwordx4 v[64:67], v87, s[36:39], s49 offen sc1
mav 12,28,36
s_waitcnt lgkmcnt(1)
mav 0,32,40
R 52,5120
mav 4,36,40
mav 8,40,40
buffer_store_dwordx4 v[68:71], v87, s[36:39], s49 offen offset:1024 sc1
mav 12,44,40
mav 0,48,44
R 56,6144
mav 4,52,44
mav 8,56,44
D 84,0
mav 12,60,44
s_waitcnt lgkmcnt(1)
mav 0,64,48
R 60,7168
mav 4,68,48
mav 8,72,48
D 84,1024
mav 12,76,48
mav 0,80,52
R 32,8192
mav 4,84,52
mav 8,88,52
mav 12,92,52
s_waitcnt lgkmcnt(1)
mav 0,96,56
R 36,9216
mav 4,100,56
mav 8,104,56
mav 12,108,56
mav 0,112,60
R 40,10240
mav 4,116,60
mav 8,120,60
mav 12,124,60
s_waitcnt lgkmcnt(1)
mav 0,128,32
R 44,11264
mav 4,132,32
mav 8,136,32
mav 12,140,32
mav 0,144,36
R 48,12288
mav 4,148,36
mav 8,152,36
mav 12,156,36
s_waitcnt lgkmcnt(1)
mav 0,160,40
R 52,13312
mav 4,164,40
mav 8,168,40
mav 12,172,40
mav 0,176,44
R 56,14336
mav 4,180,44
mav 8,184,44
mav 12,188,44
s_waitcnt lgkmcnt(1)
mav 0,192,48
R 60,15360
mav 4,196,48
mav 8,200,48
mav 12,204,48
mav 0,208,52
R 32,16384
mav 4,212,52
mav 8,216,52
v_add_u32 v72, 1, v72
mav 12,220,52
s_waitcnt lgkmcnt(1)
mav 0,224,56
R 36,17408
mav 4,228,56
mav 8,232,56
mav 12,236,56
mav 0,240,60
R 40,18432
mav 4,244,60
s_waitcnt vmcnt(2)
buffer_store_dwordx2 v[72:73], v81, s[36:39], s52 offen sc1
mav 8,248,60
mav 12,252,60
s_waitcnt lgkmcnt(1)
mvv 0,128,32
R 44,19456
mvv 4,132,32
mvv 8,136,32
mvv 12,140,32
mvv 0,144,36
R 48,20480
mvv 4,148,36
mvv 8,152,36
mvv 12,156,36
s_waitcnt lgkmcnt(1)
mvv 0,160,40
R 52,21504
mvv 4,164,40
mvv 8,168,40
mvv 12,172,40
mvv 0,176,44
R 56,22528
mvv 4,180,44
mvv 8,184,44
s_xor_b32 s50, s50, 0x10000
mvv 12,188,44
s_waitcnt lgkmcnt(1)
mvv 0,192,48
R 60,23552
mvv 4,196,48
mvv 8,200,48
s_add_u32 s49, s49, 346112
mvv 12,204,48
s_waitcnt vmcnt(1)
s_barrier
mvv 0,208,52
R 32,32768
s_add_u32 m0, s50, 0
mvv 4,212,52
D 83,0
mvv 8,216,52
mvv 12,220,52
s_waitcnt lgkmcnt(1)
mvv 0,224,56
R 36,33792
mvv 4,228,56
D 83,1024
mvv 8,232,56
mvv 12,236,56
mvv 0,240,60
R 40,34816
mvv 4,244,60
mvv 8,248,60
D 83,2048
mvv 12,252,60
s_waitcnt lgkmcnt(1)
v_mfma_f32_16x16x32_bf16 v[16:19], a[0:3], v[32:35], 0
R 44,35840
v_cvt_pk_bf16_f32 v64, v0, v1
v_mfma_f32_16x16x32_bf16 v[20:23], a[4:7], v[32:35], 0
v_cvt_pk_bf16_f32 v65, v2, v3
v_cvt_pk_bf16_f32 v66, v4, v5
v_mfma_f32_16x16x32_bf16 v[24:27], a[8:11], v[32:35], 0
D 83,3072
v_cvt_pk_bf16_f32 v67, v6, v7
v_mfma_f32_16x16x32_bf16 v[28:31], a[12:15], v[32:35], 0
v_cvt_pk_bf16_f32 v68, v8, v9
v_cvt_pk_bf16_f32 v69, v10, v11
mav 16,16,36
R 48,36864
v_cvt_pk_bf16_f32 v70, v12, v13
mav 20,20,36
s_add_u32 m0, s50, 4096
v_cvt_pk_bf16_f32 v71, v14, v15
mav 24,24,36
buffer_store_dwordx4 v[64:67], v87, s[36:39], s49 offen sc1
mav 28,28,36
s_waitcnt lgkmcnt(1)
mav 16,32,40
R 52,37888
mav 20,36,40
mav 24,40,40
buffer_store_dwordx4 v[68:71], v87, s[36:39], s49 offen offset:1024 sc1
mav 28,44,40
mav 16,48,44
R 56,38912
mav 20,52,44
mav 24,56,44
D 84,0
mav 28,60,44
s_waitcnt lgkmcnt(1)
mav 16,64,48
R 60,39936
mav 20,68,48
mav 24,72,48
D 84,1024
mav 28,76,48
mav 16,80,52
R 32,40960
mav 20,84,52
mav 24,88,52
mav 28,92,52
s_waitcnt lgkmcnt(1)
mav 16,96,56
R 36,41984
mav 20,100,56
mav 24,104,56
mav 28,108,56
mav 16,112,60
R 40,43008
mav 20,116,60
mav 24,120,60
mav 28,124,60
s_waitcnt lgkmcnt(1)
mav 16,128,32
R 44,44032
mav 20,132,32
mav 24,136,32
mav 28,140,32
mav 16,144,36
R 48,45056
mav 20,148,36
mav 24,152,36
mav 28,156,36
s_waitcnt lgkmcnt(1)
mav 16,160,40
R 52,46080
mav 20,164,40
mav 24,168,40
mav 28,172,40
mav 16,176,44
R 56,47104
mav 20,180,44
mav 24,184,44
mav 28,188,44
s_waitcnt lgkmcnt(1)
mav 16,192,48
R 60,48128
mav 20,196,48
mav 24,200,48
mav 28,204,48
mav 16,208,52
R 32,49152
mav 20,212,52
mav 24,216,52
v_add_u32 v72, 1, v72
mav 28,220,52
s_waitcnt lgkmcnt(1)
mav 16,224,56
R 36,50176
mav 20,228,56
mav 24,232,56
mav 28,236,56
mav 16,240,60
R 40,51200
mav 20,244,60
s_waitcnt vmcnt(2)
buffer_store_dwordx2 v[72:73], v81, s[36:39], s52 offen sc1
mav 24,248,60
mav 28,252,60
s_waitcnt lgkmcnt(1)
mvv 16,128,32
R 44,52224
mvv 20,132,32
mvv 24,136,32
mvv 28,140,32
mvv 16,144,36
R 48,53248
mvv 20,148,36
mvv 24,152,36
mvv 28,156,36
s_waitcnt lgkmcnt(1)
mvv 16,160,40
R 52,54272
mvv 20,164,40
mvv 24,168,40
mvv 28,172,40
mvv 16,176,44
R 56,55296
mvv 20,180,44
mvv 24,184,44
mvv 28,188,44
s_waitcnt lgkmcnt(1)
mvv 16,192,48
R 60,56320
mvv 20,196,48
v_xor_b32 v80, 0x10000, v80
mvv 24,200,48
s_add_u32 s49, s49, 346112
mvv 28,204,48
s_waitcnt vmcnt(1)
s_barrier
mvv 16,208,52
R 32,0
s_add_u32 m0, s50, 32768
mvv 20,212,52
D 83,0
mvv 24,216,52
mvv 28,220,52
s_waitcnt lgkmcnt(1)
mvv 16,224,56
R 36,1024
mvv 20,228,56
D 83,1024
mvv 24,232,56
mvv 28,236,56
mvv 16,240,60
R 40,2048
mvv 20,244,60
mvv 24,248,60
D 83,2048
mvv 28,252,60
s_add_u32 s60, s60, 1
s_cbranch_scc0 LG1_1
s_waitcnt lgkmcnt(1)
v_mfma_f32_16x16x32_bf16 v[0:3], a[0:3], v[32:35], 0
R 44,3072
v_cvt_pk_bf16_f32 v64, v16, v17
v_mfma_f32_16x16x32_bf16 v[4:7], a[4:7], v[32:35], 0
v_cvt_pk_bf16_f32 v65, v18, v19
v_cvt_pk_bf16_f32 v66, v20, v21
v_mfma_f32_16x16x32_bf16 v[8:11], a[8:11], v[32:35], 0
D 83,3072
v_cvt_pk_bf16_f32 v67, v22, v23
v_mfma_f32_16x16x32_bf16 v[12:15], a[12:15], v[32:35], 0
v_cvt_pk_bf16_f32 v68, v24, v25
v_cvt_pk_bf16_f32 v69, v26, v27
mav 0,16,36
R 48,4096
v_cvt_pk_bf16_f32 v70, v28, v29
mav 4,20,36
s_add_u32 m0, s50, 36864
v_cvt_pk_bf16_f32 v71, v30, v31
mav 8,24,36
buffer_store_dwordx4 v[64:67], v87, s[36:39], s49 offen sc1
mav 12,28,36
s_waitcnt lgkmcnt(1)
mav 0,32,40
R 52,5120
mav 4,36,40
mav 8,40,40
buffer_store_dwordx4 v[68:71], v87, s[36:39], s49 offen offset:1024 sc1
mav 12,44,40
mav 0,48,44
R 56,6144
mav 4,52,44
mav 8,56,44
D 84,0
mav 12,60,44
s_waitcnt lgkmcnt(1)
mav 0,64,48
R 60,7168
mav 4,68,48
mav 8,72,48
D 84,1024
mav 12,76,48
mav 0,80,52
R 32,8192
mav 4,84,52
mav 8,88,52
mav 12,92,52
s_waitcnt lgkmcnt(1)
mav 0,96,56
R 36,9216
mav 4,100,56
mav 8,104,56
mav 12,108,56
mav 0,112,60
R 40,10240
mav 4,116,60
mav 8,120,60
mav 12,124,60
s_waitcnt lgkmcnt(1)
mav 0,128,32
R 44,11264
mav 4,132,32
mav 8,136,32
mav 12,140,32
mav 0,144,36
R 48,12288
mav 4,148,36
mav 8,152,36
mav 12,156,36
s_waitcnt lgkmcnt(1)
mav 0,160,40
R 52,13312
mav 4,164,40
mav 8,168,40
mav 12,172,40
mav 0,176,44
R 56,14336
mav 4,180,44
mav 8,184,44
mav 12,188,44
s_waitcnt lgkmcnt(1)
mav 0,192,48
R 60,15360
mav 4,196,48
mav 8,200,48
mav 12,204,48
mav 0,208,52
R 32,16384
mav 4,212,52
mav 8,216,52
v_add_u32 v72, 1, v72
mav 12,220,52
s_waitcnt lgkmcnt(1)
mav 0,224,56
R 36,17408
mav 4,228,56
mav 8,232,56
mav 12,236,56
mav 0,240,60
R 40,18432
mav 4,244,60
s_waitcnt vmcnt(2)
buffer_store_dwordx2 v[72:73], v81, s[36:39], s52 offen sc1
mav 8,248,60
mav 12,252,60
s_waitcnt lgkmcnt(1)
mvv 0,128,32
R 44,19456
mvv 4,132,32
mvv 8,136,32
mvv 12,140,32
mvv 0,144,36
R 48,20480
mvv 4,148,36
mvv 8,152,36
mvv 12,156,36
s_waitcnt lgkmcnt(1)
mvv 0,160,40
R 52,21504
mvv 4,164,40
mvv 8,168,40
mvv 12,172,40
mvv 0,176,44
R 56,22528
mvv 4,180,44
mvv 8,184,44
mvv 12,188,44
s_waitcnt lgkmcnt(1)
mvv 0,192,48
R 60,23552
mvv 4,196,48
mvv 8,200,48
s_add_u32 s49, s49, 346112
mvv 12,204,48
s_waitcnt vmcnt(1)
s_barrier
mvv 0,208,52
R 32,32768
mvv 4,212,52
mvv 8,216,52
mvv 12,220,52
s_waitcnt lgkmcnt(1)
mvv 0,224,56
R 36,33792
mvv 4,228,56
mvv 8,232,56
mvv 12,236,56
mvv 0,240,60
R 40,34816
mvv 4,244,60
mvv 8,248,60
mvv 12,252,60
s_waitcnt lgkmcnt(1)
v_mfma_f32_16x16x32_bf16 v[16:19], a[0:3], v[32:35], 0
R 44,35840
v_mfma_f32_16x16x32_bf16 v[20:23], a[4:7], v[32:35], 0
v_cvt_pk_bf16_f32 v64, v0, v1
v_cvt_pk_bf16_f32 v65, v2, v3
v_mfma_f32_16x16x32_bf16 v[24:27], a[8:11], v[32:35], 0
v_cvt_pk_bf16_f32 v66, v4, v5
v_cvt_pk_bf16_f32 v67, v6, v7
v_mfma_f32_16x16x32_bf16 v[28:31], a[12:15], v[32:35], 0
buffer_store_dwordx4 v[64:67], v87, s[36:39], s49 offen sc1
v_cvt_pk_bf16_f32 v68, v8, v9
mav 16,16,36
R 48,36864
v_cvt_pk_bf16_f32 v69, v10, v11
mav 20,20,36
v_cvt_pk_bf16_f32 v70, v12, v13
v_cvt_pk_bf16_f32 v71, v14, v15
mav 24,24,36
mav 28,28,36
buffer_store_dwordx4 v[68:71], v87, s[36:39], s49 offen offset:1024 sc1
s_waitcnt lgkmcnt(1)
mav 16,32,40
R 52,37888
mav 20,36,40
mav 24,40,40
mav 28,44,40
mav 16,48,44
R 56,38912
mav 20,52,44
mav 24,56,44
mav 28,60,44
s_waitcnt lgkmcnt(1)
mav 16,64,48
R 60,39936
mav 20,68,48
mav 24,72,48
mav 28,76,48
mav 16,80,52
R 32,40960
mav 20,84,52
mav 24,88,52
mav 28,92,52
s_waitcnt lgkmcnt(1)
mav 16,96,56
R 36,41984
mav 20,100,56
mav 24,104,56
mav 28,108,56
mav 16,112,60
R 40,43008
mav 20,116,60
mav 24,120,60
mav 28,124,60
s_waitcnt lgkmcnt(1)
mav 16,128,32
R 44,44032
mav 20,132,32
mav 24,136,32
mav 28,140,32
mav 16,144,36
R 48,45056
mav 20,148,36
mav 24,152,36
mav 28,156,36
s_waitcnt lgkmcnt(1)
mav 16,160,40
R 52,46080
mav 20,164,40
mav 24,168,40
mav 28,172,40
mav 16,176,44
R 56,47104
mav 20,180,44
mav 24,184,44
mav 28,188,44
s_waitcnt lgkmcnt(1)
mav 16,192,48
R 60,48128
mav 20,196,48
mav 24,200,48
mav 28,204,48
mav 16,208,52
R 32,49152
mav 20,212,52
mav 24,216,52
v_add_u32 v72, 1, v72
mav 28,220,52
s_waitcnt lgkmcnt(1)
mav 16,224,56
R 36,50176
mav 20,228,56
mav 24,232,56
mav 28,236,56
mav 16,240,60
R 40,51200
mav 20,244,60
s_waitcnt vmcnt(0)
buffer_store_dwordx2 v[72:73], v81, s[36:39], s52 offen sc1
mav 24,248,60
mav 28,252,60
s_waitcnt lgkmcnt(1)
mvv 16,128,32
R 44,52224
mvv 20,132,32
mvv 24,136,32
mvv 28,140,32
mvv 16,144,36
R 48,53248
mvv 20,148,36
mvv 24,152,36
mvv 28,156,36
s_waitcnt lgkmcnt(1)
mvv 16,160,40
R 52,54272
mvv 20,164,40
mvv 24,168,40
mvv 28,172,40
mvv 16,176,44
R 56,55296
mvv 20,180,44
mvv 24,184,44
mvv 28,188,44
s_waitcnt lgkmcnt(1)
mvv 16,192,48
R 60,56320
mvv 20,196,48
mvv 24,200,48
s_add_u32 s49, s49, 346112
mvv 28,204,48
mvv 16,208,52
mvv 20,212,52
mvv 24,216,52
mvv 28,220,52
s_waitcnt lgkmcnt(0)
mvv 16,224,56
mvv 20,228,56
mvv 24,232,56
mvv 28,236,56
mvv 16,240,60
mvv 20,244,60
mvv 24,248,60
mvv 28,252,60
s_nop 4
v_cvt_pk_bf16_f32 v64, v16, v17
v_cvt_pk_bf16_f32 v65, v18, v19
v_cvt_pk_bf16_f32 v66, v20, v21
v_cvt_pk_bf16_f32 v67, v22, v23
buffer_store_dwordx4 v[64:67], v87, s[36:39], s49 offen sc1
v_cvt_pk_bf16_f32 v68, v24, v25
v_cvt_pk_bf16_f32 v69, v26, v27
v_cvt_pk_bf16_f32 v70, v28, v29
v_cvt_pk_bf16_f32 v71, v30, v31
buffer_store_dwordx4 v[68:71], v87, s[36:39], s49 offen offset:1024 sc1
v_add_u32 v72, 35, v72
s_waitcnt vmcnt(0)
buffer_store_dwordx2 v[72:73], v81, s[36:39], s52 offen sc1
s_endpgm
Lr5:
buffer_load_dword v73, off, s[36:39], 0 sc1
s_waitcnt vmcnt(0)
s_barrier
v_mov_b32 v72, 0x100
v_add_u32 v73, 0x9e3779b9, v73
buffer_store_dwordx2 v[72:73], v81, s[36:39], s52 offen sc1
v_add_u32 v85, v85, v72
s_sleep 32
s_sleep 32
s_sleep 32
E 0,65536
E 1,69632
E 2,73728
E 3,77824
s_add_u32 s51, s51, 65536
E 0,81920
E 1,86016
E 2,90112
E 3,94208
s_add_u32 s51, s51, 65536
E 0,98304
E 1,102400
E 2,106496
E 3,110592
s_add_u32 s51, s51, 65536
E 0,114688
E 1,118784
E 2,122880
E 3,126976
s_add_u32 s51, s51, 65536
s_cmp_ge_u32 s51, s57
s_cselect_b32 s60, 1048576, 0
s_sub_u32 s51, s51, s60
s_waitcnt vmcnt(8)
s_barrier
v_add_u32 v4, 65536, v4
v_add_u32 v5, 65536, v5
v_add_u32 v6, 65536, v6
v_add_u32 v7, 65536, v7
Ta 0,4,0
Ta 4,5,0
Ta 8,6,0
Ta 12,7,0
Ta 16,4,16384
Ta 20,5,16384
Ta 24,6,16384
Ta 28,7,16384
E 0,131072
E 1,135168
E 2,139264
E 3,143360
s_add_u32 s51, s51, 65536
E 0,147456
E 1,151552
E 2,155648
E 3,159744
s_add_u32 s51, s51, 65536
s_waitcnt lgkmcnt(0)
s_waitcnt vmcnt(8)
s_barrier
E 0,65536
Ta 32,4,32768
Ta 36,5,32768
E 1,69632
Ta 40,6,32768
Ta 44,7,32768
E 2,73728
Ta 48,4,49152
E 3,77824
s_add_u32 s51, s51, 65536
Ta 52,5,49152
Ta 56,6,49152
E 0,81920
Ta 60,7,49152
E 1,86016
E 2,90112
E 3,94208
s_add_u32 s51, s51, 65536
s_cmp_ge_u32 s51, s57
s_cselect_b32 s60, 1048576, 0
s_sub_u32 s51, s51, s60
s_waitcnt lgkmcnt(0)
s_waitcnt vmcnt(8)
s_barrier
v_add_u32 v4, 65536, v4
v_add_u32 v5, 65536, v5
v_add_u32 v6, 65536, v6
v_add_u32 v7, 65536, v7
E 0,98304
Ta 64,4,0
Ta 68,5,0
E 1,102400
Ta 72,6,0
Ta 76,7,0
E 2,106496
Ta 80,4,16384
E 3,110592
s_add_u32 s51, s51, 65536
Ta 84,5,16384
Ta 88,6,16384
E 0,114688
Ta 92,7,16384
E 1,118784
E 2,122880
E 3,126976
s_add_u32 s51, s51, 65536
s_waitcnt lgkmcnt(0)
s_waitcnt vmcnt(8)
s_barrier
v_add_u32 v4, -65536, v4
v_add_u32 v5, -65536, v5
v_add_u32 v6, -65536, v6
v_add_u32 v7, -65536, v7
E 0,131072
Ta 96,4,0
Ta 100,5,0
E 1,135168
Ta 104,6,0
Ta 108,7,0
E 2,139264
Ta 112,4,16384
E 3,143360
s_add_u32 s51, s51, 65536
Ta 116,5,16384
Ta 120,6,16384
E 0,147456
Ta 124,7,16384
E 1,151552
E 2,155648
E 3,159744
s_add_u32 s51, s51, 65536
s_cmp_ge_u32 s51, s57
s_cselect_b32 s60, 1048576, 0
s_sub_u32 s51, s51, s60
s_waitcnt lgkmcnt(0)
s_waitcnt vmcnt(8)
s_barrier
E 0,65536
Ta 128,4,32768
Ta 132,5,32768
E 1,69632
Ta 136,6,32768
Ta 140,7,32768
E 2,73728
Ta 144,4,49152
E 3,77824
s_add_u32 s51, s51, 65536
Ta 148,5,49152
Ta 152,6,49152
E 0,81920
Ta 156,7,49152
E 1,86016
E 2,90112
E 3,94208
s_add_u32 s51, s51, 65536
s_waitcnt lgkmcnt(0)
s_waitcnt vmcnt(8)
s_barrier
v_add_u32 v4, 65536, v4
v_add_u32 v5, 65536, v5
v_add_u32 v6, 65536, v6
v_add_u32 v7, 65536, v7
E 0,98304
Ta 160,4,0
Ta 164,5,0
E 1,102400
Ta 168,6,0
Ta 172,7,0
E 2,106496
Ta 176,4,16384
E 3,110592
s_add_u32 s51, s51, 65536
Ta 180,5,16384
Ta 184,6,16384
E 0,114688
Ta 188,7,16384
E 1,118784
E 2,122880
E 3,126976
s_add_u32 s51, s51, 65536
s_cmp_ge_u32 s51, s57
s_cselect_b32 s60, 1048576, 0
s_sub_u32 s51, s51, s60
s_waitcnt lgkmcnt(0)
s_waitcnt vmcnt(8)
s_barrier
v_add_u32 v4, -65536, v4
v_add_u32 v5, -65536, v5
v_add_u32 v6, -65536, v6
v_add_u32 v7, -65536, v7
Ta 192,4,0
Ta 196,5,0
Ta 200,6,0
Ta 204,7,0
Ta 208,4,16384
Ta 212,5,16384
Ta 216,6,16384
Ta 220,7,16384
s_waitcnt lgkmcnt(0)
s_waitcnt vmcnt(0)
s_barrier
Ta 224,4,32768
Ta 228,5,32768
Ta 232,6,32768
Ta 236,7,32768
Ta 240,4,49152
Ta 244,5,49152
Ta 248,6,49152
Ta 252,7,49152
s_waitcnt lgkmcnt(0)
s_waitcnt vmcnt(0)
s_barrier
s_nop 7
s_and_b32 s60, s48, 3
s_cmp_eq_u32 s60, 1
s_cbranch_scc1 LvR1
s_cmp_eq_u32 s60, 2
s_cbranch_scc1 LvR2
LvR0:
buffer_load_dwordx2 v[76:77], v84, s[36:39], 0 offen sc1
s_waitcnt vmcnt(0)
ds_write_b32 v82, v76
v_sub_u32 v76, v76, v85
v_xor_b32 v77, v77, v73
v_cmp_lt_u64 vcc, s[62:63], v[76:77]
s_waitcnt lgkmcnt(0)
s_cbranch_vccnz CR0_1
BR0_1:
s_add_u32 s49, s49, 346112
s_barrier
s_add_u32 m0, s50, 0
s_nop 0
D 83,0
D 83,1024
D 83,2048
D 83,3072
s_mov_b32 m0, 142336
s_nop 0
buffer_load_dword v84, s[36:39], 0 offen sc1 lds
v_add_u32 v85, 1, v85
ds_read_b32 v76, v82 offset:0
s_waitcnt lgkmcnt(0)
v_cmp_gt_i32 vcc, v85, v76
s_cbranch_vccnz CR0_2
BR0_2:
s_add_u32 s49, s49, 346112
s_barrier
s_add_u32 m0, s50, 32768
)"
R"(
s_nop 0
D 83,0
D 83,1024
D 83,2048
D 83,3072
s_mov_b32 m0, 142336
s_nop 0
buffer_load_dword v84, s[36:39], 0 offen sc1 lds
v_add_u32 v85, 1, v85
buffer_load_dwordx4 v[124:127], v90, s[36:39], s49 offen sc1
buffer_load_dwordx4 v[128:131], v90, s[36:39], s49 offen offset:1024 sc1
ds_read_b32 v76, v82 offset:0
s_xor_b32 s50, s50, 0x10000
s_waitcnt lgkmcnt(0)
v_cmp_gt_i32 vcc, v85, v76
s_waitcnt vmcnt(8)
s_cbranch_vccnz CR0_3
BR0_3:
s_add_u32 s49, s49, 346112
s_barrier
R 32,0
buffer_load_dwordx4 v[132:135], v86, s[36:39], s49 offen sc1
R 36,1024
buffer_load_dwordx4 v[136:139], v86, s[36:39], s49 offen offset:1024 sc1
R 40,2048
buffer_load_dwordx4 v[140:143], v87, s[36:39], s49 offen sc1
s_waitcnt lgkmcnt(1)
v_mfma_f32_16x16x32_bf16 v[0:3], a[0:3], v[32:35], v[100:103]
R 44,3072
v_mfma_f32_16x16x32_bf16 v[4:7], a[4:7], v[32:35], v[104:107]
v_mfma_f32_16x16x32_bf16 v[8:11], a[8:11], v[32:35], v[108:111]
buffer_load_dwordx4 v[144:147], v87, s[36:39], s49 offen offset:1024 sc1
v_mfma_f32_16x16x32_bf16 v[12:15], a[12:15], v[32:35], v[112:115]
mav 0,16,36
R 48,4096
mav 4,20,36
mav 8,24,36
mav 12,28,36
buffer_load_dwordx4 v[148:151], v88, s[36:39], s49 offen sc1
s_waitcnt lgkmcnt(1)
mav 0,32,40
R 52,5120
mav 4,36,40
mav 8,40,40
mav 12,44,40
buffer_load_dwordx4 v[152:155], v88, s[36:39], s49 offen offset:1024 sc1
mav 0,48,44
R 56,6144
mav 4,52,44
mav 8,56,44
mav 12,60,44
s_waitcnt lgkmcnt(1)
mav 0,64,48
R 60,7168
buffer_load_dwordx4 v[156:159], v89, s[36:39], s49 offen sc1
mav 4,68,48
mav 8,72,48
mav 12,76,48
mav 0,80,52
R 32,8192
buffer_load_dwordx4 v[160:163], v89, s[36:39], s49 offen offset:1024 sc1
mav 4,84,52
mav 8,88,52
mav 12,92,52
s_add_u32 m0, s50, 0
s_waitcnt lgkmcnt(1)
mav 0,96,56
R 36,9216
mav 4,100,56
D 83,0
mav 8,104,56
mav 12,108,56
mav 0,112,60
R 40,10240
mav 4,116,60
D 83,1024
mav 8,120,60
mav 12,124,60
s_waitcnt lgkmcnt(1)
mav 0,128,32
R 44,11264
mav 4,132,32
mav 8,136,32
D 83,2048
mav 12,140,32
mav 0,144,36
R 48,12288
mav 4,148,36
mav 8,152,36
D 83,3072
s_mov_b32 m0, 142336
mav 12,156,36
buffer_load_dword v84, s[36:39], 0 offen sc1 lds
s_waitcnt lgkmcnt(1)
mav 0,160,40
R 52,13312
v_add_u32 v85, 1, v85
mav 4,164,40
mav 8,168,40
mav 12,172,40
buffer_load_dwordx4 v[164:167], v90, s[36:39], s49 offen sc1
mav 0,176,44
R 56,14336
mav 4,180,44
mav 8,184,44
mav 12,188,44
buffer_load_dwordx4 v[168:171], v90, s[36:39], s49 offen offset:1024 sc1
s_waitcnt lgkmcnt(1)
mav 0,192,48
R 60,15360
mav 4,196,48
mav 8,200,48
mav 12,204,48
mav 0,208,52
mav 4,212,52
mav 8,216,52
mav 12,220,52
s_waitcnt lgkmcnt(0)
mav 0,224,56
mav 4,228,56
mav 8,232,56
mav 12,236,56
mav 0,240,60
mav 4,244,60
ds_read_b32 v76, v82 offset:0
mav 8,248,60
mav 12,252,60
s_waitcnt vmcnt(16)
mvv 0,116,124
mvv 4,120,124
s_waitcnt vmcnt(15)
mvv 8,116,128
mvv 12,120,128
s_waitcnt vmcnt(14)
mvv 0,116,132
mvv 4,120,132
s_waitcnt lgkmcnt(0)
v_cmp_gt_i32 vcc, v85, v76
s_waitcnt vmcnt(13)
mvv 8,116,136
s_cbranch_vccnz CR0_4
BR0_4:
s_add_u32 s49, s49, 346112
mvv 12,120,136
s_barrier
s_waitcnt vmcnt(12)
mvv 0,116,140
R 32,32768
mvv 4,120,140
buffer_load_dwordx4 v[172:175], v86, s[36:39], s49 offen sc1
s_waitcnt vmcnt(12)
mvv 8,116,144
mvv 12,120,144
s_waitcnt vmcnt(11)
mvv 0,116,148
R 36,33792
mvv 4,120,148
buffer_load_dwordx4 v[176:179], v86, s[36:39], s49 offen offset:1024 sc1
s_waitcnt vmcnt(11)
mvv 8,116,152
mvv 12,120,152
s_waitcnt vmcnt(10)
mvv 0,116,156
R 40,34816
mvv 4,120,156
s_waitcnt vmcnt(9)
mvv 8,116,160
buffer_load_dwordx4 v[180:183], v87, s[36:39], s49 offen sc1
mvv 12,120,160
s_waitcnt lgkmcnt(1)
v_mfma_f32_16x16x32_bf16 v[16:19], a[0:3], v[32:35], v[100:103]
R 44,35840
v_cvt_pk_bf16_f32 v64, v0, v1
v_mfma_f32_16x16x32_bf16 v[20:23], a[4:7], v[32:35], v[104:107]
v_cvt_pk_bf16_f32 v65, v2, v3
v_cvt_pk_bf16_f32 v66, v4, v5
v_mfma_f32_16x16x32_bf16 v[24:27], a[8:11], v[32:35], v[108:111]
buffer_load_dwordx4 v[184:187], v87, s[36:39], s49 offen offset:1024 sc1
v_cvt_pk_bf16_f32 v67, v6, v7
v_mfma_f32_16x16x32_bf16 v[28:31], a[12:15], v[32:35], v[112:115]
v_cvt_pk_bf16_f32 v68, v8, v9
v_cvt_pk_bf16_f32 v69, v10, v11
mav 16,16,36
R 48,36864
v_cvt_pk_bf16_f32 v70, v12, v13
mav 20,20,36
v_cvt_pk_bf16_f32 v71, v14, v15
v_mul_f32 v75, v0, v0
mav 24,24,36
buffer_store_dwordx4 v[64:67], v91, s[36:39], s49 offen sc1
v_add_f32 v74, v1, v0
mav 28,28,36
v_fmac_f32 v75, v1, v1
s_waitcnt lgkmcnt(1)
mav 16,32,40
R 52,37888
v_add_f32 v74, v2, v74
mav 20,36,40
v_fmac_f32 v75, v2, v2
v_add_f32 v74, v3, v74
mav 24,40,40
buffer_store_dwordx4 v[68:71], v91, s[36:39], s49 offen offset:1024 sc1
v_fmac_f32 v75, v3, v3
mav 28,44,40
v_add_f32 v74, v4, v74
v_fmac_f32 v75, v4, v4
mav 16,48,44
R 56,38912
v_add_f32 v74, v5, v74
mav 20,52,44
v_fmac_f32 v75, v5, v5
v_add_f32 v74, v6, v74
mav 24,56,44
buffer_load_dwordx4 v[188:191], v88, s[36:39], s49 offen sc1
v_fmac_f32 v75, v6, v6
mav 28,60,44
v_add_f32 v74, v7, v74
s_waitcnt lgkmcnt(1)
mav 16,64,48
R 60,39936
v_fmac_f32 v75, v7, v7
mav 20,68,48
v_add_f32 v74, v8, v74
v_fmac_f32 v75, v8, v8
mav 24,72,48
buffer_load_dwordx4 v[192:195], v88, s[36:39], s49 offen offset:1024 sc1
v_add_f32 v74, v9, v74
mav 28,76,48
v_fmac_f32 v75, v9, v9
v_add_f32 v74, v10, v74
mav 16,80,52
R 32,40960
v_fmac_f32 v75, v10, v10
mav 20,84,52
v_add_f32 v74, v11, v74
v_fmac_f32 v75, v11, v11
mav 24,88,52
buffer_load_dwordx4 v[196:199], v89, s[36:39], s49 offen sc1
v_add_f32 v74, v12, v74
mav 28,92,52
s_add_u32 m0, s50, 32768
s_waitcnt lgkmcnt(1)
mav 16,96,56
R 36,41984
v_fmac_f32 v75, v12, v12
mav 20,100,56
v_add_f32 v74, v13, v74
v_fmac_f32 v75, v13, v13
mav 24,104,56
buffer_load_dwordx4 v[200:203], v89, s[36:39], s49 offen offset:1024 sc1
v_add_f32 v74, v14, v74
mav 28,108,56
v_fmac_f32 v75, v14, v14
v_add_f32 v74, v15, v74
mav 16,112,60
R 40,43008
v_fmac_f32 v75, v15, v15
mav 20,116,60
mav 24,120,60
D 83,0
v_permlane32_swap_b32 v74, v75
mav 28,124,60
v_add_f32 v74, v74, v75
s_waitcnt lgkmcnt(1)
mav 16,128,32
R 44,44032
mav 20,132,32
mav 24,136,32
D 83,1024
mav 28,140,32
mav 16,144,36
R 48,45056
mav 20,148,36
mav 24,152,36
D 83,2048
mav 28,156,36
s_waitcnt lgkmcnt(1)
mav 16,160,40
R 52,46080
v_add_u32 v85, 1, v85
mav 20,164,40
mav 24,168,40
D 83,3072
s_mov_b32 m0, 142336
mav 28,172,40
buffer_load_dword v84, s[36:39], 0 offen sc1 lds
mav 16,176,44
R 56,47104
mav 20,180,44
mav 24,184,44
buffer_load_dwordx4 v[124:127], v90, s[36:39], s49 offen sc1
mav 28,188,44
s_waitcnt lgkmcnt(1)
mav 16,192,48
R 60,48128
mav 20,196,48
mav 24,200,48
buffer_load_dwordx4 v[128:131], v90, s[36:39], s49 offen offset:1024 sc1
mav 28,204,48
mav 16,208,52
mav 20,212,52
mav 24,216,52
v_add_u32 v72, 1, v72
mav 28,220,52
s_waitcnt lgkmcnt(0)
mav 16,224,56
mav 20,228,56
mav 24,232,56
mav 28,236,56
mav 16,240,60
s_waitcnt vmcnt(11)
buffer_store_dwordx2 v[72:73], v81, s[36:39], s52 offen sc1
mav 20,244,60
ds_read_b32 v76, v82 offset:0
mav 24,248,60
ds_write_b32 v97, v74 offset:0
mav 28,252,60
mvv 16,116,164
mvv 20,120,164
mvv 24,116,168
s_xor_b32 s50, s50, 0x10000
mvv 28,120,168
mvv 16,116,172
mvv 20,120,172
v_xor_b32 v80, 0x10000, v80
mvv 24,116,176
s_waitcnt lgkmcnt(0)
v_cmp_gt_i32 vcc, v85, v76
mvv 28,120,176
s_cbranch_vccnz CR0_5
BR0_5:
s_barrier
mvv 16,116,180
R 32,0
s_add_u32 s49, s49, 346112
mvv 20,120,180
buffer_load_dwordx4 v[132:135], v86, s[36:39], s49 offen sc1
mvv 24,116,184
mvv 28,120,184
s_waitcnt vmcnt(12)
mvv 16,116,188
R 36,1024
mvv 20,120,188
buffer_load_dwordx4 v[136:139], v86, s[36:39], s49 offen offset:1024 sc1
s_waitcnt vmcnt(12)
mvv 24,116,192
mvv 28,120,192
s_waitcnt vmcnt(11)
mvv 16,116,196
R 40,2048
mvv 20,120,196
s_waitcnt vmcnt(10)
mvv 24,116,200
buffer_load_dwordx4 v[140:143], v87, s[36:39], s49 offen sc1
mvv 28,120,200
s_mov_b32 s60, -15
.p2align 6
LR0_6:
s_waitcnt lgkmcnt(1)
v_mfma_f32_16x16x32_bf16 v[0:3], a[0:3], v[32:35], v[100:103]
R 44,3072
v_cvt_pk_bf16_f32 v64, v16, v17
v_mfma_f32_16x16x32_bf16 v[4:7], a[4:7], v[32:35], v[104:107]
v_cvt_pk_bf16_f32 v65, v18, v19
v_cvt_pk_bf16_f32 v66, v20, v21
v_mfma_f32_16x16x32_bf16 v[8:11], a[8:11], v[32:35], v[108:111]
buffer_load_dwordx4 v[144:147], v87, s[36:39], s49 offen offset:1024 sc1
v_cvt_pk_bf16_f32 v67, v22, v23
v_mfma_f32_16x16x32_bf16 v[12:15], a[12:15], v[32:35], v[112:115]
v_cvt_pk_bf16_f32 v68, v24, v25
v_cvt_pk_bf16_f32 v69, v26, v27
mav 0,16,36
R 48,4096
v_cvt_pk_bf16_f32 v70, v28, v29
mav 4,20,36
v_cvt_pk_bf16_f32 v71, v30, v31
v_mul_f32 v75, v16, v16
mav 8,24,36
buffer_store_dwordx4 v[64:67], v91, s[36:39], s49 offen sc1
v_add_f32 v74, v17, v16
mav 12,28,36
v_fmac_f32 v75, v17, v17
s_waitcnt lgkmcnt(1)
mav 0,32,40
R 52,5120
v_add_f32 v74, v18, v74
mav 4,36,40
v_fmac_f32 v75, v18, v18
v_add_f32 v74, v19, v74
mav 8,40,40
buffer_store_dwordx4 v[68:71], v91, s[36:39], s49 offen offset:1024 sc1
v_fmac_f32 v75, v19, v19
mav 12,44,40
v_add_f32 v74, v20, v74
v_fmac_f32 v75, v20, v20
mav 0,48,44
R 56,6144
v_add_f32 v74, v21, v74
mav 4,52,44
v_fmac_f32 v75, v21, v21
v_add_f32 v74, v22, v74
mav 8,56,44
buffer_load_dwordx4 v[148:151], v88, s[36:39], s49 offen sc1
v_fmac_f32 v75, v22, v22
mav 12,60,44
v_add_f32 v74, v23, v74
s_waitcnt lgkmcnt(1)
mav 0,64,48
R 60,7168
v_fmac_f32 v75, v23, v23
mav 4,68,48
v_add_f32 v74, v24, v74
v_fmac_f32 v75, v24, v24
mav 8,72,48
buffer_load_dwordx4 v[152:155], v88, s[36:39], s49 offen offset:1024 sc1
v_add_f32 v74, v25, v74
mav 12,76,48
v_fmac_f32 v75, v25, v25
v_add_f32 v74, v26, v74
mav 0,80,52
R 32,8192
v_fmac_f32 v75, v26, v26
mav 4,84,52
v_add_f32 v74, v27, v74
v_fmac_f32 v75, v27, v27
mav 8,88,52
buffer_load_dwordx4 v[156:159], v89, s[36:39], s49 offen sc1
v_add_f32 v74, v28, v74
mav 12,92,52
s_add_u32 m0, s50, 0
s_waitcnt lgkmcnt(1)
mav 0,96,56
R 36,9216
v_fmac_f32 v75, v28, v28
mav 4,100,56
v_add_f32 v74, v29, v74
v_fmac_f32 v75, v29, v29
mav 8,104,56
buffer_load_dwordx4 v[160:163], v89, s[36:39], s49 offen offset:1024 sc1
v_add_f32 v74, v30, v74
mav 12,108,56
v_fmac_f32 v75, v30, v30
v_add_f32 v74, v31, v74
mav 0,112,60
R 40,10240
v_fmac_f32 v75, v31, v31
mav 4,116,60
mav 8,120,60
D 83,0
v_permlane32_swap_b32 v74, v75
mav 12,124,60
v_add_f32 v74, v74, v75
s_waitcnt lgkmcnt(1)
mav 0,128,32
R 44,11264
mav 4,132,32
mav 8,136,32
D 83,1024
mav 12,140,32
mav 0,144,36
R 48,12288
mav 4,148,36
mav 8,152,36
D 83,2048
mav 12,156,36
s_waitcnt lgkmcnt(1)
mav 0,160,40
R 52,13312
v_add_u32 v85, 1, v85
mav 4,164,40
mav 8,168,40
D 83,3072
s_mov_b32 m0, 142336
mav 12,172,40
buffer_load_dword v84, s[36:39], 0 offen sc1 lds
mav 0,176,44
R 56,14336
mav 4,180,44
mav 8,184,44
buffer_load_dwordx4 v[164:167], v90, s[36:39], s49 offen sc1
mav 12,188,44
s_waitcnt lgkmcnt(1)
mav 0,192,48
R 60,15360
mav 4,196,48
mav 8,200,48
buffer_load_dwordx4 v[168:171], v90, s[36:39], s49 offen offset:1024 sc1
mav 12,204,48
mav 0,208,52
mav 4,212,52
mav 8,216,52
v_add_u32 v72, 1, v72
mav 12,220,52
s_waitcnt lgkmcnt(0)
mav 0,224,56
mav 4,228,56
mav 8,232,56
mav 12,236,56
mav 0,240,60
s_waitcnt vmcnt(11)
buffer_store_dwordx2 v[72:73], v81, s[36:39], s52 offen sc1
mav 4,244,60
ds_read_b32 v76, v82 offset:0
mav 8,248,60
ds_write_b32 v97, v74 offset:1024
mav 12,252,60
mvv 0,116,124
mvv 4,120,124
mvv 8,116,128
mvv 12,120,128
mvv 0,116,132
mvv 4,120,132
s_waitcnt lgkmcnt(0)
v_cmp_gt_i32 vcc, v85, v76
mvv 8,116,136
s_cbranch_vccnz CR0_7
BR0_7:
s_add_u32 s49, s49, 346112
mvv 12,120,136
s_barrier
mvv 0,116,140
R 32,32768
mvv 4,120,140
buffer_load_dwordx4 v[172:175], v86, s[36:39], s49 offen sc1
mvv 8,116,144
mvv 12,120,144
s_waitcnt vmcnt(12)
mvv 0,116,148
R 36,33792
mvv 4,120,148
buffer_load_dwordx4 v[176:179], v86, s[36:39], s49 offen offset:1024 sc1
s_waitcnt vmcnt(12)
mvv 8,116,152
mvv 12,120,152
s_waitcnt vmcnt(11)
mvv 0,116,156
R 40,34816
mvv 4,120,156
s_waitcnt vmcnt(10)
mvv 8,116,160
buffer_load_dwordx4 v[180:183], v87, s[36:39], s49 offen sc1
mvv 12,120,160
s_waitcnt lgkmcnt(1)
v_mfma_f32_16x16x32_bf16 v[16:19], a[0:3], v[32:35], v[100:103]
R 44,35840
v_cvt_pk_bf16_f32 v64, v0, v1
v_mfma_f32_16x16x32_bf16 v[20:23], a[4:7], v[32:35], v[104:107]
v_cvt_pk_bf16_f32 v65, v2, v3
v_cvt_pk_bf16_f32 v66, v4, v5
v_mfma_f32_16x16x32_bf16 v[24:27], a[8:11], v[32:35], v[108:111]
buffer_load_dwordx4 v[184:187], v87, s[36:39], s49 offen offset:1024 sc1
v_cvt_pk_bf16_f32 v67, v6, v7
v_mfma_f32_16x16x32_bf16 v[28:31], a[12:15], v[32:35], v[112:115]
v_cvt_pk_bf16_f32 v68, v8, v9
v_cvt_pk_bf16_f32 v69, v10, v11
mav 16,16,36
R 48,36864
v_cvt_pk_bf16_f32 v70, v12, v13
mav 20,20,36
v_cvt_pk_bf16_f32 v71, v14, v15
v_mul_f32 v75, v0, v0
mav 24,24,36
buffer_store_dwordx4 v[64:67], v91, s[36:39], s49 offen sc1
v_add_f32 v74, v1, v0
mav 28,28,36
v_fmac_f32 v75, v1, v1
s_waitcnt lgkmcnt(1)
mav 16,32,40
R 52,37888
v_add_f32 v74, v2, v74
mav 20,36,40
v_fmac_f32 v75, v2, v2
v_add_f32 v74, v3, v74
mav 24,40,40
buffer_store_dwordx4 v[68:71], v91, s[36:39], s49 offen offset:1024 sc1
v_fmac_f32 v75, v3, v3
mav 28,44,40
v_add_f32 v74, v4, v74
v_fmac_f32 v75, v4, v4
mav 16,48,44
R 56,38912
v_add_f32 v74, v5, v74
mav 20,52,44
v_fmac_f32 v75, v5, v5
v_add_f32 v74, v6, v74
mav 24,56,44
buffer_load_dwordx4 v[188:191], v88, s[36:39], s49 offen sc1
v_fmac_f32 v75, v6, v6
mav 28,60,44
v_add_f32 v74, v7, v74
s_waitcnt lgkmcnt(1)
mav 16,64,48
R 60,39936
v_fmac_f32 v75, v7, v7
mav 20,68,48
v_add_f32 v74, v8, v74
v_fmac_f32 v75, v8, v8
mav 24,72,48
buffer_load_dwordx4 v[192:195], v88, s[36:39], s49 offen offset:1024 sc1
v_add_f32 v74, v9, v74
mav 28,76,48
v_fmac_f32 v75, v9, v9
v_add_f32 v74, v10, v74
mav 16,80,52
R 32,40960
v_fmac_f32 v75, v10, v10
mav 20,84,52
v_add_f32 v74, v11, v74
v_fmac_f32 v75, v11, v11
mav 24,88,52
buffer_load_dwordx4 v[196:199], v89, s[36:39], s49 offen sc1
v_add_f32 v74, v12, v74
mav 28,92,52
s_add_u32 m0, s50, 32768
s_waitcnt lgkmcnt(1)
mav 16,96,56
R 36,41984
v_fmac_f32 v75, v12, v12
mav 20,100,56
v_add_f32 v74, v13, v74
v_fmac_f32 v75, v13, v13
mav 24,104,56
buffer_load_dwordx4 v[200:203], v89, s[36:39], s49 offen offset:1024 sc1
v_add_f32 v74, v14, v74
mav 28,108,56
v_fmac_f32 v75, v14, v14
v_add_f32 v74, v15, v74
mav 16,112,60
R 40,43008
v_fmac_f32 v75, v15, v15
mav 20,116,60
mav 24,120,60
D 83,0
v_permlane32_swap_b32 v74, v75
mav 28,124,60
v_add_f32 v74, v74, v75
s_waitcnt lgkmcnt(1)
mav 16,128,32
R 44,44032
mav 20,132,32
mav 24,136,32
D 83,1024
mav 28,140,32
mav 16,144,36
R 48,45056
mav 20,148,36
mav 24,152,36
D 83,2048
mav 28,156,36
s_waitcnt lgkmcnt(1)
mav 16,160,40
R 52,46080
v_add_u32 v85, 1, v85
mav 20,164,40
mav 24,168,40
D 83,3072
s_mov_b32 m0, 142336
mav 28,172,40
buffer_load_dword v84, s[36:39], 0 offen sc1 lds
mav 16,176,44
R 56,47104
mav 20,180,44
mav 24,184,44
buffer_load_dwordx4 v[124:127], v90, s[36:39], s49 offen sc1
mav 28,188,44
s_waitcnt lgkmcnt(1)
mav 16,192,48
R 60,48128
mav 20,196,48
mav 24,200,48
buffer_load_dwordx4 v[128:131], v90, s[36:39], s49 offen offset:1024 sc1
mav 28,204,48
mav 16,208,52
mav 20,212,52
mav 24,216,52
v_add_u32 v72, 1, v72
mav 28,220,52
s_waitcnt lgkmcnt(0)
mav 16,224,56
mav 20,228,56
mav 24,232,56
mav 28,236,56
mav 16,240,60
s_waitcnt vmcnt(11)
buffer_store_dwordx2 v[72:73], v81, s[36:39], s52 offen sc1
mav 20,244,60
ds_read_b32 v76, v82 offset:0
mav 24,248,60
ds_write_b32 v97, v74 offset:0
mav 28,252,60
mvv 16,116,164
mvv 20,120,164
mvv 24,116,168
s_xor_b32 s50, s50, 0x10000
mvv 28,120,168
mvv 16,116,172
mvv 20,120,172
v_xor_b32 v80, 0x10000, v80
mvv 24,116,176
s_waitcnt lgkmcnt(0)
v_cmp_gt_i32 vcc, v85, v76
mvv 28,120,176
s_cbranch_vccnz CR0_8
BR0_8:
s_barrier
mvv 16,116,180
R 32,0
s_add_u32 s49, s49, 346112
mvv 20,120,180
buffer_load_dwordx4 v[132:135], v86, s[36:39], s49 offen sc1
mvv 24,116,184
mvv 28,120,184
s_waitcnt vmcnt(12)
mvv 16,116,188
R 36,1024
mvv 20,120,188
buffer_load_dwordx4 v[136:139], v86, s[36:39], s49 offen offset:1024 sc1
s_waitcnt vmcnt(12)
mvv 24,116,192
mvv 28,120,192
s_waitcnt vmcnt(11)
mvv 16,116,196
R 40,2048
mvv 20,120,196
s_waitcnt vmcnt(10)
mvv 24,116,200
buffer_load_dwordx4 v[140:143], v87, s[36:39], s49 offen sc1
mvv 28,120,200
s_add_u32 s60, s60, 1
s_cbranch_scc0 LR0_6
s_waitcnt lgkmcnt(1)
v_mfma_f32_16x16x32_bf16 v[0:3], a[0:3], v[32:35], v[100:103]
R 44,3072
v_cvt_pk_bf16_f32 v64, v16, v17
v_mfma_f32_16x16x32_bf16 v[4:7], a[4:7], v[32:35], v[104:107]
v_cvt_pk_bf16_f32 v65, v18, v19
v_cvt_pk_bf16_f32 v66, v20, v21
v_mfma_f32_16x16x32_bf16 v[8:11], a[8:11], v[32:35], v[108:111]
buffer_load_dwordx4 v[144:147], v87, s[36:39], s49 offen offset:1024 sc1
v_cvt_pk_bf16_f32 v67, v22, v23
v_mfma_f32_16x16x32_bf16 v[12:15], a[12:15], v[32:35], v[112:115]
v_cvt_pk_bf16_f32 v68, v24, v25
v_cvt_pk_bf16_f32 v69, v26, v27
mav 0,16,36
R 48,4096
v_cvt_pk_bf16_f32 v70, v28, v29
mav 4,20,36
v_cvt_pk_bf16_f32 v71, v30, v31
v_mul_f32 v75, v16, v16
mav 8,24,36
buffer_store_dwordx4 v[64:67], v91, s[36:39], s49 offen sc1
v_add_f32 v74, v17, v16
mav 12,28,36
v_fmac_f32 v75, v17, v17
s_waitcnt lgkmcnt(1)
mav 0,32,40
R 52,5120
v_add_f32 v74, v18, v74
mav 4,36,40
v_fmac_f32 v75, v18, v18
v_add_f32 v74, v19, v74
mav 8,40,40
buffer_store_dwordx4 v[68:71], v91, s[36:39], s49 offen offset:1024 sc1
v_fmac_f32 v75, v19, v19
mav 12,44,40
v_add_f32 v74, v20, v74
v_fmac_f32 v75, v20, v20
mav 0,48,44
R 56,6144
v_add_f32 v74, v21, v74
mav 4,52,44
v_fmac_f32 v75, v21, v21
v_add_f32 v74, v22, v74
mav 8,56,44
buffer_load_dwordx4 v[148:151], v88, s[36:39], s49 offen sc1
v_fmac_f32 v75, v22, v22
mav 12,60,44
v_add_f32 v74, v23, v74
s_waitcnt lgkmcnt(1)
mav 0,64,48
R 60,7168
v_fmac_f32 v75, v23, v23
mav 4,68,48
v_add_f32 v74, v24, v74
v_fmac_f32 v75, v24, v24
mav 8,72,48
buffer_load_dwordx4 v[152:155], v88, s[36:39], s49 offen offset:1024 sc1
v_add_f32 v74, v25, v74
mav 12,76,48
v_fmac_f32 v75, v25, v25
v_add_f32 v74, v26, v74
mav 0,80,52
R 32,8192
v_fmac_f32 v75, v26, v26
mav 4,84,52
v_add_f32 v74, v27, v74
v_fmac_f32 v75, v27, v27
mav 8,88,52
buffer_load_dwordx4 v[156:159], v89, s[36:39], s49 offen sc1
v_add_f32 v74, v28, v74
mav 12,92,52
s_add_u32 m0, s50, 0
s_waitcnt lgkmcnt(1)
mav 0,96,56
R 36,9216
v_fmac_f32 v75, v28, v28
mav 4,100,56
v_add_f32 v74, v29, v74
v_fmac_f32 v75, v29, v29
mav 8,104,56
buffer_load_dwordx4 v[160:163], v89, s[36:39], s49 offen offset:1024 sc1
v_add_f32 v74, v30, v74
mav 12,108,56
v_fmac_f32 v75, v30, v30
v_add_f32 v74, v31, v74
mav 0,112,60
R 40,10240
v_fmac_f32 v75, v31, v31
mav 4,116,60
mav 8,120,60
D 83,0
v_permlane32_swap_b32 v74, v75
mav 12,124,60
v_add_f32 v74, v74, v75
s_waitcnt lgkmcnt(1)
mav 0,128,32
R 44,11264
mav 4,132,32
mav 8,136,32
D 83,1024
mav 12,140,32
mav 0,144,36
R 48,12288
mav 4,148,36
mav 8,152,36
D 83,2048
mav 12,156,36
s_waitcnt lgkmcnt(1)
mav 0,160,40
R 52,13312
v_add_u32 v85, 1, v85
mav 4,164,40
mav 8,168,40
D 83,3072
s_mov_b32 m0, 142336
mav 12,172,40
buffer_load_dword v84, s[36:39], 0 offen sc1 lds
mav 0,176,44
R 56,14336
mav 4,180,44
mav 8,184,44
buffer_load_dwordx4 v[164:167], v90, s[36:39], s49 offen sc1
mav 12,188,44
s_waitcnt lgkmcnt(1)
mav 0,192,48
R 60,15360
mav 4,196,48
mav 8,200,48
buffer_load_dwordx4 v[168:171], v90, s[36:39], s49 offen offset:1024 sc1
mav 12,204,48
mav 0,208,52
mav 4,212,52
mav 8,216,52
v_add_u32 v72, 69, v72
mav 12,220,52
s_waitcnt lgkmcnt(0)
mav 0,224,56
mav 4,228,56
mav 8,232,56
mav 12,236,56
mav 0,240,60
s_waitcnt vmcnt(11)
buffer_store_dwordx2 v[72:73], v81, s[36:39], s52 offen sc1
mav 4,244,60
ds_read_b32 v76, v82 offset:0
mav 8,248,60
ds_write_b32 v97, v74 offset:1024
mav 12,252,60
mvv 0,116,124
mvv 4,120,124
mvv 8,116,128
mvv 12,120,128
mvv 0,116,132
mvv 4,120,132
s_waitcnt lgkmcnt(0)
v_cmp_gt_i32 vcc, v85, v76
mvv 8,116,136
s_cbranch_vccnz CR0_9
BR0_9:
s_add_u32 s49, s49, 346112
mvv 12,120,136
s_barrier
mvv 0,116,140
R 32,32768
mvv 4,120,140
buffer_load_dwordx4 v[172:175], v86, s[36:39], s49 offen sc1
mvv 8,116,144
mvv 12,120,144
s_waitcnt vmcnt(12)
mvv 0,116,148
R 36,33792
mvv 4,120,148
buffer_load_dwordx4 v[176:179], v86, s[36:39], s49 offen offset:1024 sc1
s_waitcnt vmcnt(12)
mvv 8,116,152
mvv 12,120,152
s_waitcnt vmcnt(11)
mvv 0,116,156
R 40,34816
mvv 4,120,156
s_waitcnt vmcnt(10)
mvv 8,116,160
buffer_load_dwordx4 v[180:183], v87, s[36:39], s49 offen sc1
mvv 12,120,160
s_waitcnt lgkmcnt(1)
v_mfma_f32_16x16x32_bf16 v[16:19], a[0:3], v[32:35], v[100:103]
R 44,35840
v_cvt_pk_bf16_f32 v64, v0, v1
v_mfma_f32_16x16x32_bf16 v[20:23], a[4:7], v[32:35], v[104:107]
v_cvt_pk_bf16_f32 v65, v2, v3
v_cvt_pk_bf16_f32 v66, v4, v5
v_mfma_f32_16x16x32_bf16 v[24:27], a[8:11], v[32:35], v[108:111]
buffer_load_dwordx4 v[184:187], v87, s[36:39], s49 offen offset:1024 sc1
v_cvt_pk_bf16_f32 v67, v6, v7
v_mfma_f32_16x16x32_bf16 v[28:31], a[12:15], v[32:35], v[112:115]
ds_write_b128 v94, v[64:67] offset:0
mav 16,16,36
R 48,36864
v_cvt_pk_bf16_f32 v68, v8, v9
mav 20,20,36
v_cvt_pk_bf16_f32 v69, v10, v11
v_cvt_pk_bf16_f32 v70, v12, v13
mav 24,24,36
v_cvt_pk_bf16_f32 v71, v14, v15
mav 28,28,36
buffer_load_dwordx4 v[188:191], v88, s[36:39], s49 offen sc1
s_waitcnt lgkmcnt(2)
mav 16,32,40
R 52,37888
mav 20,36,40
ds_write_b128 v95, v[68:71] offset:0
mav 24,40,40
mav 28,44,40
buffer_load_dwordx4 v[192:195], v88, s[36:39], s49 offen offset:1024 sc1
mav 16,48,44
R 56,38912
mav 20,52,44
s_waitcnt lgkmcnt(1)
ds_read_b128 v[64:67], v96 offset:0
mav 24,56,44
ds_read_b128 v[68:71], v96 offset:1024
mav 28,60,44
mav 16,64,48
R 60,39936
buffer_load_dwordx4 v[196:199], v89, s[36:39], s49 offen sc1
mav 20,68,48
mav 24,72,48
mav 28,76,48
mav 16,80,52
R 32,40960
buffer_load_dwordx4 v[200:203], v89, s[36:39], s49 offen offset:1024 sc1
mav 20,84,52
mav 24,88,52
mav 28,92,52
s_add_u32 m0, s50, 32768
s_waitcnt lgkmcnt(1)
mav 16,96,56
R 36,41984
buffer_store_dwordx4 v[64:67], v92, s[44:47], s54 offen
mav 20,100,56
mav 24,104,56
mav 28,108,56
mav 16,112,60
R 40,43008
buffer_store_dwordx4 v[68:71], v93, s[44:47], s54 offen
mav 20,116,60
mav 24,120,60
mav 28,124,60
s_waitcnt lgkmcnt(1)
mav 16,128,32
R 44,44032
D 83,0
mav 20,132,32
mav 24,136,32
mav 28,140,32
mav 16,144,36
R 48,45056
D 83,1024
mav 20,148,36
mav 24,152,36
mav 28,156,36
s_waitcnt lgkmcnt(1)
mav 16,160,40
R 52,46080
D 83,2048
mav 20,164,40
s_sub_u32 s54, s54, 0x8000
v_add_u32 v85, 1, v85
mav 24,168,40
mav 28,172,40
mav 16,176,44
R 56,47104
D 83,3072
mav 20,180,44
s_mov_b32 m0, 142336
mav 24,184,44
buffer_load_dword v84, s[36:39], 0 offen sc1 lds
mav 28,188,44
s_waitcnt lgkmcnt(1)
mav 16,192,48
R 60,48128
buffer_load_dwordx4 v[124:127], v90, s[36:39], s49 offen sc1
mav 20,196,48
mav 24,200,48
mav 28,204,48
mav 16,208,52
buffer_load_dwordx4 v[128:131], v90, s[36:39], s49 offen offset:1024 sc1
mav 20,212,52
mav 24,216,52
mav 28,220,52
s_waitcnt lgkmcnt(0)
mav 16,224,56
mav 20,228,56
mav 24,232,56
mav 28,236,56
mav 16,240,60
mav 20,244,60
ds_read_b32 v76, v82 offset:0
mav 24,248,60
mav 28,252,60
s_waitcnt vmcnt(19)
mvv 16,116,164
mvv 20,120,164
s_waitcnt vmcnt(18)
mvv 24,116,168
s_xor_b32 s50, s50, 0x10000
mvv 28,120,168
s_waitcnt vmcnt(16)
mvv 16,116,172
mvv 20,120,172
v_xor_b32 v80, 0x10000, v80
s_waitcnt vmcnt(15)
mvv 24,116,176
s_waitcnt lgkmcnt(0)
v_cmp_gt_i32 vcc, v85, v76
mvv 28,120,176
s_cbranch_vccnz CR0_10
BR0_10:
s_waitcnt vmcnt(14)
mvv 16,116,180
s_barrier
R 32,0
mvv 20,120,180
s_add_u32 s49, s49, 346112
s_waitcnt vmcnt(13)
mvv 24,116,184
buffer_load_dwordx4 v[132:135], v86, s[36:39], s49 offen sc1
mvv 28,120,184
s_waitcnt vmcnt(13)
mvv 16,116,188
R 36,1024
mvv 20,120,188
s_waitcnt vmcnt(12)
mvv 24,116,192
buffer_load_dwordx4 v[136:139], v86, s[36:39], s49 offen offset:1024 sc1
mvv 28,120,192
s_waitcnt vmcnt(12)
mvv 16,116,196
R 40,2048
mvv 20,120,196
s_waitcnt vmcnt(11)
mvv 24,116,200
buffer_load_dwordx4 v[140:143], v87, s[36:39], s49 offen sc1
mvv 28,120,200
s_mov_b32 s60, -13
.p2align 6
LR0_11:
s_waitcnt lgkmcnt(1)
v_mfma_f32_16x16x32_bf16 v[0:3], a[0:3], v[32:35], v[100:103]
R 44,3072
v_cvt_pk_bf16_f32 v64, v16, v17
v_mfma_f32_16x16x32_bf16 v[4:7], a[4:7], v[32:35], v[104:107]
v_cvt_pk_bf16_f32 v65, v18, v19
v_cvt_pk_bf16_f32 v66, v20, v21
v_mfma_f32_16x16x32_bf16 v[8:11], a[8:11], v[32:35], v[108:111]
buffer_load_dwordx4 v[144:147], v87, s[36:39], s49 offen offset:1024 sc1
v_cvt_pk_bf16_f32 v67, v22, v23
v_mfma_f32_16x16x32_bf16 v[12:15], a[12:15], v[32:35], v[112:115]
ds_write_b128 v94, v[64:67] offset:0
mav 0,16,36
R 48,4096
v_cvt_pk_bf16_f32 v68, v24, v25
mav 4,20,36
v_cvt_pk_bf16_f32 v69, v26, v27
v_cvt_pk_bf16_f32 v70, v28, v29
mav 8,24,36
v_cvt_pk_bf16_f32 v71, v30, v31
mav 12,28,36
buffer_load_dwordx4 v[148:151], v88, s[36:39], s49 offen sc1
s_waitcnt lgkmcnt(2)
mav 0,32,40
R 52,5120
mav 4,36,40
ds_write_b128 v95, v[68:71] offset:0
mav 8,40,40
mav 12,44,40
buffer_load_dwordx4 v[152:155], v88, s[36:39], s49 offen offset:1024 sc1
mav 0,48,44
R 56,6144
mav 4,52,44
s_waitcnt lgkmcnt(1)
ds_read_b128 v[64:67], v96 offset:0
mav 8,56,44
ds_read_b128 v[68:71], v96 offset:1024
mav 12,60,44
mav 0,64,48
R 60,7168
buffer_load_dwordx4 v[156:159], v89, s[36:39], s49 offen sc1
mav 4,68,48
mav 8,72,48
mav 12,76,48
mav 0,80,52
R 32,8192
buffer_load_dwordx4 v[160:163], v89, s[36:39], s49 offen offset:1024 sc1
mav 4,84,52
mav 8,88,52
mav 12,92,52
s_add_u32 m0, s50, 0
s_waitcnt lgkmcnt(1)
mav 0,96,56
R 36,9216
buffer_store_dwordx4 v[64:67], v92, s[44:47], s54 offen
mav 4,100,56
mav 8,104,56
mav 12,108,56
mav 0,112,60
R 40,10240
buffer_store_dwordx4 v[68:71], v93, s[44:47], s54 offen
mav 4,116,60
mav 8,120,60
mav 12,124,60
s_waitcnt lgkmcnt(1)
mav 0,128,32
R 44,11264
D 83,0
mav 4,132,32
mav 8,136,32
mav 12,140,32
mav 0,144,36
R 48,12288
D 83,1024
mav 4,148,36
mav 8,152,36
mav 12,156,36
s_waitcnt lgkmcnt(1)
mav 0,160,40
R 52,13312
D 83,2048
mav 4,164,40
s_sub_u32 s54, s54, 0x8000
v_add_u32 v85, 1, v85
mav 8,168,40
mav 12,172,40
mav 0,176,44
R 56,14336
D 83,3072
mav 4,180,44
s_mov_b32 m0, 142336
mav 8,184,44
buffer_load_dword v84, s[36:39], 0 offen sc1 lds
mav 12,188,44
s_waitcnt lgkmcnt(1)
mav 0,192,48
R 60,15360
buffer_load_dwordx4 v[164:167], v90, s[36:39], s49 offen sc1
mav 4,196,48
mav 8,200,48
mav 12,204,48
mav 0,208,52
buffer_load_dwordx4 v[168:171], v90, s[36:39], s49 offen offset:1024 sc1
mav 4,212,52
mav 8,216,52
mav 12,220,52
s_waitcnt lgkmcnt(0)
mav 0,224,56
mav 4,228,56
mav 8,232,56
mav 12,236,56
mav 0,240,60
mav 4,244,60
ds_read_b32 v76, v82 offset:0
mav 8,248,60
mav 12,252,60
s_waitcnt vmcnt(18)
mvv 0,116,124
mvv 4,120,124
s_waitcnt vmcnt(17)
mvv 8,116,128
mvv 12,120,128
s_waitcnt vmcnt(16)
mvv 0,116,132
mvv 4,120,132
s_waitcnt lgkmcnt(0)
v_cmp_gt_i32 vcc, v85, v76
s_waitcnt vmcnt(15)
mvv 8,116,136
s_cbranch_vccnz CR0_12
BR0_12:
s_add_u32 s49, s49, 346112
mvv 12,120,136
s_barrier
s_waitcnt vmcnt(14)
mvv 0,116,140
R 32,32768
mvv 4,120,140
buffer_load_dwordx4 v[172:175], v86, s[36:39], s49 offen sc1
s_waitcnt vmcnt(14)
mvv 8,116,144
mvv 12,120,144
s_waitcnt vmcnt(13)
mvv 0,116,148
R 36,33792
mvv 4,120,148
buffer_load_dwordx4 v[176:179], v86, s[36:39], s49 offen offset:1024 sc1
s_waitcnt vmcnt(13)
mvv 8,116,152
mvv 12,120,152
s_waitcnt vmcnt(12)
mvv 0,116,156
R 40,34816
mvv 4,120,156
s_waitcnt vmcnt(11)
mvv 8,116,160
buffer_load_dwordx4 v[180:183], v87, s[36:39], s49 offen sc1
mvv 12,120,160
s_waitcnt lgkmcnt(1)
v_mfma_f32_16x16x32_bf16 v[16:19], a[0:3], v[32:35], v[100:103]
R 44,35840
v_cvt_pk_bf16_f32 v64, v0, v1
v_mfma_f32_16x16x32_bf16 v[20:23], a[4:7], v[32:35], v[104:107]
v_cvt_pk_bf16_f32 v65, v2, v3
v_cvt_pk_bf16_f32 v66, v4, v5
v_mfma_f32_16x16x32_bf16 v[24:27], a[8:11], v[32:35], v[108:111]
buffer_load_dwordx4 v[184:187], v87, s[36:39], s49 offen offset:1024 sc1
v_cvt_pk_bf16_f32 v67, v6, v7
v_mfma_f32_16x16x32_bf16 v[28:31], a[12:15], v[32:35], v[112:115]
ds_write_b128 v94, v[64:67] offset:0
mav 16,16,36
R 48,36864
v_cvt_pk_bf16_f32 v68, v8, v9
mav 20,20,36
v_cvt_pk_bf16_f32 v69, v10, v11
v_cvt_pk_bf16_f32 v70, v12, v13
mav 24,24,36
v_cvt_pk_bf16_f32 v71, v14, v15
mav 28,28,36
buffer_load_dwordx4 v[188:191], v88, s[36:39], s49 offen sc1
s_waitcnt lgkmcnt(2)
mav 16,32,40
R 52,37888
mav 20,36,40
ds_write_b128 v95, v[68:71] offset:0
mav 24,40,40
mav 28,44,40
buffer_load_dwordx4 v[192:195], v88, s[36:39], s49 offen offset:1024 sc1
mav 16,48,44
R 56,38912
mav 20,52,44
s_waitcnt lgkmcnt(1)
ds_read_b128 v[64:67], v96 offset:0
mav 24,56,44
ds_read_b128 v[68:71], v96 offset:1024
mav 28,60,44
mav 16,64,48
R 60,39936
buffer_load_dwordx4 v[196:199], v89, s[36:39], s49 offen sc1
mav 20,68,48
mav 24,72,48
mav 28,76,48
mav 16,80,52
R 32,40960
buffer_load_dwordx4 v[200:203], v89, s[36:39], s49 offen offset:1024 sc1
mav 20,84,52
mav 24,88,52
mav 28,92,52
s_add_u32 m0, s50, 32768
s_waitcnt lgkmcnt(1)
mav 16,96,56
R 36,41984
buffer_store_dwordx4 v[64:67], v92, s[44:47], s54 offen
mav 20,100,56
mav 24,104,56
mav 28,108,56
mav 16,112,60
R 40,43008
buffer_store_dwordx4 v[68:71], v93, s[44:47], s54 offen
mav 20,116,60
mav 24,120,60
mav 28,124,60
s_waitcnt lgkmcnt(1)
mav 16,128,32
R 44,44032
D 83,0
mav 20,132,32
mav 24,136,32
mav 28,140,32
mav 16,144,36
R 48,45056
D 83,1024
mav 20,148,36
mav 24,152,36
mav 28,156,36
s_waitcnt lgkmcnt(1)
mav 16,160,40
R 52,46080
D 83,2048
mav 20,164,40
s_sub_u32 s54, s54, 0x8000
v_add_u32 v85, 1, v85
mav 24,168,40
mav 28,172,40
mav 16,176,44
R 56,47104
D 83,3072
mav 20,180,44
s_mov_b32 m0, 142336
mav 24,184,44
buffer_load_dword v84, s[36:39], 0 offen sc1 lds
mav 28,188,44
s_waitcnt lgkmcnt(1)
mav 16,192,48
R 60,48128
buffer_load_dwordx4 v[124:127], v90, s[36:39], s49 offen sc1
mav 20,196,48
mav 24,200,48
mav 28,204,48
mav 16,208,52
buffer_load_dwordx4 v[128:131], v90, s[36:39], s49 offen offset:1024 sc1
mav 20,212,52
mav 24,216,52
mav 28,220,52
s_waitcnt lgkmcnt(0)
mav 16,224,56
mav 20,228,56
mav 24,232,56
mav 28,236,56
mav 16,240,60
mav 20,244,60
ds_read_b32 v76, v82 offset:0
mav 24,248,60
mav 28,252,60
s_waitcnt vmcnt(18)
mvv 16,116,164
mvv 20,120,164
s_waitcnt vmcnt(17)
mvv 24,116,168
s_xor_b32 s50, s50, 0x10000
mvv 28,120,168
s_waitcnt vmcnt(16)
mvv 16,116,172
mvv 20,120,172
v_xor_b32 v80, 0x10000, v80
s_waitcnt vmcnt(15)
mvv 24,116,176
s_waitcnt lgkmcnt(0)
v_cmp_gt_i32 vcc, v85, v76
mvv 28,120,176
s_cbranch_vccnz CR0_13
BR0_13:
s_waitcnt vmcnt(14)
mvv 16,116,180
s_barrier
R 32,0
mvv 20,120,180
s_add_u32 s49, s49, 346112
s_waitcnt vmcnt(13)
mvv 24,116,184
buffer_load_dwordx4 v[132:135], v86, s[36:39], s49 offen sc1
mvv 28,120,184
s_waitcnt vmcnt(13)
mvv 16,116,188
R 36,1024
mvv 20,120,188
s_waitcnt vmcnt(12)
mvv 24,116,192
buffer_load_dwordx4 v[136:139], v86, s[36:39], s49 offen offset:1024 sc1
mvv 28,120,192
s_waitcnt vmcnt(12)
mvv 16,116,196
R 40,2048
mvv 20,120,196
s_waitcnt vmcnt(11)
mvv 24,116,200
buffer_load_dwordx4 v[140:143], v87, s[36:39], s49 offen sc1
mvv 28,120,200
s_add_u32 s60, s60, 1
s_cbranch_scc0 LR0_11
s_waitcnt lgkmcnt(1)
v_mfma_f32_16x16x32_bf16 v[0:3], a[0:3], v[32:35], v[100:103]
R 44,3072
v_cvt_pk_bf16_f32 v64, v16, v17
v_mfma_f32_16x16x32_bf16 v[4:7], a[4:7], v[32:35], v[104:107]
v_cvt_pk_bf16_f32 v65, v18, v19
v_cvt_pk_bf16_f32 v66, v20, v21
v_mfma_f32_16x16x32_bf16 v[8:11], a[8:11], v[32:35], v[108:111]
buffer_load_dwordx4 v[144:147], v87, s[36:39], s49 offen offset:1024 sc1
v_cvt_pk_bf16_f32 v67, v22, v23
v_mfma_f32_16x16x32_bf16 v[12:15], a[12:15], v[32:35], v[112:115]
ds_write_b128 v94, v[64:67] offset:0
mav 0,16,36
R 48,4096
v_cvt_pk_bf16_f32 v68, v24, v25
mav 4,20,36
v_cvt_pk_bf16_f32 v69, v26, v27
v_cvt_pk_bf16_f32 v70, v28, v29
mav 8,24,36
v_cvt_pk_bf16_f32 v71, v30, v31
mav 12,28,36
buffer_load_dwordx4 v[148:151], v88, s[36:39], s49 offen sc1
s_waitcnt lgkmcnt(2)
mav 0,32,40
R 52,5120
mav 4,36,40
ds_write_b128 v95, v[68:71] offset:0
mav 8,40,40
mav 12,44,40
buffer_load_dwordx4 v[152:155], v88, s[36:39], s49 offen offset:1024 sc1
mav 0,48,44
R 56,6144
mav 4,52,44
s_waitcnt lgkmcnt(1)
ds_read_b128 v[64:67], v96 offset:0
mav 8,56,44
ds_read_b128 v[68:71], v96 offset:1024
mav 12,60,44
mav 0,64,48
R 60,7168
buffer_load_dwordx4 v[156:159], v89, s[36:39], s49 offen sc1
mav 4,68,48
mav 8,72,48
mav 12,76,48
mav 0,80,52
R 32,8192
buffer_load_dwordx4 v[160:163], v89, s[36:39], s49 offen offset:1024 sc1
mav 4,84,52
mav 8,88,52
mav 12,92,52
s_add_u32 m0, s50, 0
s_waitcnt lgkmcnt(1)
mav 0,96,56
R 36,9216
buffer_store_dwordx4 v[64:67], v92, s[44:47], s54 offen
mav 4,100,56
mav 8,104,56
mav 12,108,56
mav 0,112,60
R 40,10240
buffer_store_dwordx4 v[68:71], v93, s[44:47], s54 offen
mav 4,116,60
mav 8,120,60
mav 12,124,60
s_waitcnt lgkmcnt(1)
mav 0,128,32
R 44,11264
D 83,0
mav 4,132,32
mav 8,136,32
mav 12,140,32
mav 0,144,36
R 48,12288
D 83,1024
mav 4,148,36
mav 8,152,36
mav 12,156,36
s_waitcnt lgkmcnt(1)
mav 0,160,40
R 52,13312
D 83,2048
mav 4,164,40
s_sub_u32 s54, s54, 0x8000
v_add_u32 v85, 1, v85
mav 8,168,40
mav 12,172,40
mav 0,176,44
R 56,14336
D 83,3072
mav 4,180,44
s_mov_b32 m0, 142336
mav 8,184,44
buffer_load_dword v84, s[36:39], 0 offen sc1 lds
mav 12,188,44
s_waitcnt lgkmcnt(1)
mav 0,192,48
R 60,15360
buffer_load_dwordx4 v[164:167], v90, s[36:39], s49 offen sc1
mav 4,196,48
mav 8,200,48
mav 12,204,48
mav 0,208,52
buffer_load_dwordx4 v[168:171], v90, s[36:39], s49 offen offset:1024 sc1
mav 4,212,52
mav 8,216,52
mav 12,220,52
s_waitcnt lgkmcnt(0)
mav 0,224,56
mav 4,228,56
mav 8,232,56
mav 12,236,56
mav 0,240,60
mav 4,244,60
ds_read_b32 v76, v82 offset:0
mav 8,248,60
mav 12,252,60
s_waitcnt vmcnt(18)
mvv 0,116,124
mvv 4,120,124
s_waitcnt vmcnt(17)
mvv 8,116,128
mvv 12,120,128
s_waitcnt vmcnt(16)
mvv 0,116,132
mvv 4,120,132
s_waitcnt lgkmcnt(0)
v_cmp_gt_i32 vcc, v85, v76
s_waitcnt vmcnt(15)
mvv 8,116,136
s_cbranch_vccnz CR0_14
BR0_14:
s_add_u32 s49, s49, 346112
mvv 12,120,136
s_barrier
s_waitcnt vmcnt(14)
mvv 0,116,140
R 32,32768
mvv 4,120,140
buffer_load_dwordx4 v[172:175], v86, s[36:39], s49 offen sc1
s_waitcnt vmcnt(14)
mvv 8,116,144
mvv 12,120,144
s_waitcnt vmcnt(13)
mvv 0,116,148
R 36,33792
mvv 4,120,148
buffer_load_dwordx4 v[176:179], v86, s[36:39], s49 offen offset:1024 sc1
s_waitcnt vmcnt(13)
mvv 8,116,152
mvv 12,120,152
s_waitcnt vmcnt(12)
mvv 0,116,156
R 40,34816
mvv 4,120,156
s_waitcnt vmcnt(11)
mvv 8,116,160
buffer_load_dwordx4 v[180:183], v87, s[36:39], s49 offen sc1
mvv 12,120,160
s_waitcnt lgkmcnt(1)
v_mfma_f32_16x16x32_bf16 v[16:19], a[0:3], v[32:35], v[100:103]
R 44,35840
v_cvt_pk_bf16_f32 v64, v0, v1
v_mfma_f32_16x16x32_bf16 v[20:23], a[4:7], v[32:35], v[104:107]
v_cvt_pk_bf16_f32 v65, v2, v3
v_cvt_pk_bf16_f32 v66, v4, v5
v_mfma_f32_16x16x32_bf16 v[24:27], a[8:11], v[32:35], v[108:111]
buffer_load_dwordx4 v[184:187], v87, s[36:39], s49 offen offset:1024 sc1
v_cvt_pk_bf16_f32 v67, v6, v7
v_mfma_f32_16x16x32_bf16 v[28:31], a[12:15], v[32:35], v[112:115]
ds_write_b128 v94, v[64:67] offset:0
mav 16,16,36
R 48,36864
v_cvt_pk_bf16_f32 v68, v8, v9
mav 20,20,36
v_cvt_pk_bf16_f32 v69, v10, v11
v_cvt_pk_bf16_f32 v70, v12, v13
mav 24,24,36
v_cvt_pk_bf16_f32 v71, v14, v15
mav 28,28,36
buffer_load_dwordx4 v[188:191], v88, s[36:39], s49 offen sc1
s_waitcnt lgkmcnt(2)
mav 16,32,40
R 52,37888
mav 20,36,40
ds_write_b128 v95, v[68:71] offset:0
mav 24,40,40
mav 28,44,40
buffer_load_dwordx4 v[192:195], v88, s[36:39], s49 offen offset:1024 sc1
mav 16,48,44
R 56,38912
mav 20,52,44
s_waitcnt lgkmcnt(1)
ds_read_b128 v[64:67], v96 offset:0
mav 24,56,44
ds_read_b128 v[68:71], v96 offset:1024
mav 28,60,44
mav 16,64,48
R 60,39936
buffer_load_dwordx4 v[196:199], v89, s[36:39], s49 offen sc1
mav 20,68,48
mav 24,72,48
mav 28,76,48
mav 16,80,52
R 32,40960
buffer_load_dwordx4 v[200:203], v89, s[36:39], s49 offen offset:1024 sc1
mav 20,84,52
mav 24,88,52
mav 28,92,52
s_add_u32 m0, s50, 32768
s_waitcnt lgkmcnt(1)
mav 16,96,56
R 36,41984
buffer_store_dwordx4 v[64:67], v92, s[44:47], s54 offen
mav 20,100,56
mav 24,104,56
mav 28,108,56
mav 16,112,60
R 40,43008
buffer_store_dwordx4 v[68:71], v93, s[44:47], s54 offen
mav 20,116,60
mav 24,120,60
mav 28,124,60
s_waitcnt lgkmcnt(1)
mav 16,128,32
R 44,44032
D 83,0
mav 20,132,32
mav 24,136,32
mav 28,140,32
mav 16,144,36
R 48,45056
D 83,1024
mav 20,148,36
mav 24,152,36
mav 28,156,36
s_waitcnt lgkmcnt(1)
mav 16,160,40
R 52,46080
D 83,2048
mav 20,164,40
s_sub_u32 s54, s54, 0x8000
v_add_u32 v85, 1, v85
mav 24,168,40
mav 28,172,40
mav 16,176,44
R 56,47104
D 83,3072
mav 20,180,44
s_mov_b32 m0, 142336
mav 24,184,44
buffer_load_dword v84, s[36:39], 0 offen sc1 lds
mav 28,188,44
s_waitcnt lgkmcnt(1)
mav 16,192,48
R 60,48128
buffer_load_dwordx4 v[124:127], v90, s[36:39], s49 offen sc1
mav 20,196,48
mav 24,200,48
mav 28,204,48
mav 16,208,52
buffer_load_dwordx4 v[128:131], v90, s[36:39], s49 offen offset:1024 sc1
mav 20,212,52
mav 24,216,52
mav 28,220,52
s_waitcnt lgkmcnt(0)
mav 16,224,56
mav 20,228,56
mav 24,232,56
mav 28,236,56
mav 16,240,60
mav 20,244,60
ds_read_b32 v76, v82 offset:0
mav 24,248,60
mav 28,252,60
s_waitcnt vmcnt(18)
mvv 16,116,164
mvv 20,120,164
s_waitcnt vmcnt(17)
mvv 24,116,168
mvv 28,120,168
s_waitcnt vmcnt(16)
mvv 16,116,172
mvv 20,120,172
v_xor_b32 v80, 0x10000, v80
s_waitcnt vmcnt(15)
mvv 24,116,176
s_waitcnt lgkmcnt(0)
v_cmp_gt_i32 vcc, v85, v76
mvv 28,120,176
s_cbranch_vccnz CR0_15
BR0_15:
s_waitcnt vmcnt(14)
mvv 16,116,180
s_barrier
R 32,0
mvv 20,120,180
s_add_u32 s49, s49, 346112
s_waitcnt vmcnt(13)
mvv 24,116,184
buffer_load_dwordx4 v[132:135], v86, s[36:39], s49 offen sc1
mvv 28,120,184
s_waitcnt vmcnt(13)
mvv 16,116,188
R 36,1024
mvv 20,120,188
s_waitcnt vmcnt(12)
mvv 24,116,192
buffer_load_dwordx4 v[136:139], v86, s[36:39], s49 offen offset:1024 sc1
mvv 28,120,192
s_waitcnt vmcnt(12)
mvv 16,116,196
R 40,2048
mvv 20,120,196
s_waitcnt vmcnt(11)
mvv 24,116,200
buffer_load_dwordx4 v[140:143], v87, s[36:39], s49 offen sc1
mvv 28,120,200
s_waitcnt lgkmcnt(1)
v_mfma_f32_16x16x32_bf16 v[0:3], a[0:3], v[32:35], v[100:103]
R 44,3072
v_cvt_pk_bf16_f32 v64, v16, v17
v_mfma_f32_16x16x32_bf16 v[4:7], a[4:7], v[32:35], v[104:107]
v_cvt_pk_bf16_f32 v65, v18, v19
v_cvt_pk_bf16_f32 v66, v20, v21
v_mfma_f32_16x16x32_bf16 v[8:11], a[8:11], v[32:35], v[108:111]
buffer_load_dwordx4 v[144:147], v87, s[36:39], s49 offen offset:1024 sc1
v_cvt_pk_bf16_f32 v67, v22, v23
v_mfma_f32_16x16x32_bf16 v[12:15], a[12:15], v[32:35], v[112:115]
ds_write_b128 v94, v[64:67] offset:0
mav 0,16,36
R 48,4096
v_cvt_pk_bf16_f32 v68, v24, v25
mav 4,20,36
v_cvt_pk_bf16_f32 v69, v26, v27
v_cvt_pk_bf16_f32 v70, v28, v29
mav 8,24,36
v_cvt_pk_bf16_f32 v71, v30, v31
mav 12,28,36
buffer_load_dwordx4 v[148:151], v88, s[36:39], s49 offen sc1
s_waitcnt lgkmcnt(2)
mav 0,32,40
R 52,5120
mav 4,36,40
ds_write_b128 v95, v[68:71] offset:0
mav 8,40,40
mav 12,44,40
buffer_load_dwordx4 v[152:155], v88, s[36:39], s49 offen offset:1024 sc1
mav 0,48,44
R 56,6144
mav 4,52,44
s_waitcnt lgkmcnt(1)
ds_read_b128 v[64:67], v96 offset:0
mav 8,56,44
ds_read_b128 v[68:71], v96 offset:1024
mav 12,60,44
mav 0,64,48
R 60,7168
buffer_load_dwordx4 v[156:159], v89, s[36:39], s49 offen sc1
mav 4,68,48
mav 8,72,48
mav 12,76,48
mav 0,80,52
R 32,8192
buffer_load_dwordx4 v[160:163], v89, s[36:39], s49 offen offset:1024 sc1
mav 4,84,52
mav 8,88,52
mav 12,92,52
s_waitcnt lgkmcnt(1)
mav 0,96,56
R 36,9216
buffer_store_dwordx4 v[64:67], v92, s[44:47], s54 offen
mav 4,100,56
mav 8,104,56
mav 12,108,56
mav 0,112,60
R 40,10240
buffer_store_dwordx4 v[68:71], v93, s[44:47], s54 offen
mav 4,116,60
s_mov_b32 m0, 142336
mav 8,120,60
buffer_load_dword v84, s[36:39], 0 offen sc1 lds
mav 12,124,60
s_waitcnt lgkmcnt(1)
mav 0,128,32
R 44,11264
mav 4,132,32
mav 8,136,32
mav 12,140,32
mav 0,144,36
R 48,12288
mav 4,148,36
mav 8,152,36
mav 12,156,36
s_waitcnt lgkmcnt(1)
mav 0,160,40
R 52,13312
s_sub_u32 s54, s54, 0x8000
mav 4,164,40
v_add_u32 v85, 1, v85
mav 8,168,40
mav 12,172,40
buffer_load_dwordx4 v[164:167], v90, s[36:39], s49 offen sc1
mav 0,176,44
R 56,14336
mav 4,180,44
mav 8,184,44
mav 12,188,44
buffer_load_dwordx4 v[168:171], v90, s[36:39], s49 offen offset:1024 sc1
s_waitcnt lgkmcnt(1)
mav 0,192,48
R 60,15360
mav 4,196,48
mav 8,200,48
mav 12,204,48
mav 0,208,52
mav 4,212,52
mav 8,216,52
mav 12,220,52
s_waitcnt lgkmcnt(0)
mav 0,224,56
mav 4,228,56
mav 8,232,56
mav 12,236,56
mav 0,240,60
mav 4,244,60
ds_read_b32 v76, v82 offset:0
mav 8,248,60
mav 12,252,60
s_waitcnt vmcnt(14)
mvv 0,116,124
mvv 4,120,124
s_waitcnt vmcnt(13)
mvv 8,116,128
mvv 12,120,128
s_waitcnt vmcnt(12)
mvv 0,116,132
mvv 4,120,132
s_waitcnt lgkmcnt(0)
v_cmp_gt_i32 vcc, v85, v76
s_waitcnt vmcnt(11)
mvv 8,116,136
s_cbranch_vccnz CR0_16
BR0_16:
s_add_u32 s49, s49, 346112
mvv 12,120,136
s_barrier
s_waitcnt vmcnt(10)
mvv 0,116,140
R 32,32768
mvv 4,120,140
buffer_load_dwordx4 v[172:175], v86, s[36:39], s49 offen sc1
s_waitcnt vmcnt(10)
mvv 8,116,144
mvv 12,120,144
s_waitcnt vmcnt(9)
mvv 0,116,148
R 36,33792
mvv 4,120,148
buffer_load_dwordx4 v[176:179], v86, s[36:39], s49 offen offset:1024 sc1
s_waitcnt vmcnt(9)
mvv 8,116,152
mvv 12,120,152
s_waitcnt vmcnt(8)
mvv 0,116,156
R 40,34816
mvv 4,120,156
s_waitcnt vmcnt(7)
mvv 8,116,160
buffer_load_dwordx4 v[180:183], v87, s[36:39], s49 offen sc1
mvv 12,120,160
s_waitcnt lgkmcnt(1)
v_mfma_f32_16x16x32_bf16 v[16:19], a[0:3], v[32:35], v[100:103]
R 44,35840
v_cvt_pk_bf16_f32 v64, v0, v1
v_mfma_f32_16x16x32_bf16 v[20:23], a[4:7], v[32:35], v[104:107]
v_cvt_pk_bf16_f32 v65, v2, v3
v_cvt_pk_bf16_f32 v66, v4, v5
v_mfma_f32_16x16x32_bf16 v[24:27], a[8:11], v[32:35], v[108:111]
buffer_load_dwordx4 v[184:187], v87, s[36:39], s49 offen offset:1024 sc1
v_cvt_pk_bf16_f32 v67, v6, v7
v_mfma_f32_16x16x32_bf16 v[28:31], a[12:15], v[32:35], v[112:115]
ds_write_b128 v94, v[64:67] offset:0
mav 16,16,36
R 48,36864
v_cvt_pk_bf16_f32 v68, v8, v9
mav 20,20,36
v_cvt_pk_bf16_f32 v69, v10, v11
v_cvt_pk_bf16_f32 v70, v12, v13
mav 24,24,36
v_cvt_pk_bf16_f32 v71, v14, v15
mav 28,28,36
buffer_load_dwordx4 v[188:191], v88, s[36:39], s49 offen sc1
s_waitcnt lgkmcnt(2)
mav 16,32,40
R 52,37888
mav 20,36,40
ds_write_b128 v95, v[68:71] offset:0
mav 24,40,40
mav 28,44,40
buffer_load_dwordx4 v[192:195], v88, s[36:39], s49 offen offset:1024 sc1
mav 16,48,44
R 56,38912
mav 20,52,44
s_waitcnt lgkmcnt(1)
ds_read_b128 v[64:67], v96 offset:0
mav 24,56,44
ds_read_b128 v[68:71], v96 offset:1024
mav 28,60,44
mav 16,64,48
R 60,39936
buffer_load_dwordx4 v[196:199], v89, s[36:39], s49 offen sc1
mav 20,68,48
mav 24,72,48
mav 28,76,48
mav 16,80,52
R 32,40960
buffer_load_dwordx4 v[200:203], v89, s[36:39], s49 offen offset:1024 sc1
mav 20,84,52
mav 24,88,52
mav 28,92,52
s_waitcnt lgkmcnt(1)
mav 16,96,56
R 36,41984
buffer_store_dwordx4 v[64:67], v92, s[44:47], s54 offen
mav 20,100,56
mav 24,104,56
mav 28,108,56
mav 16,112,60
R 40,43008
buffer_store_dwordx4 v[68:71], v93, s[44:47], s54 offen
mav 20,116,60
mav 24,120,60
mav 28,124,60
s_waitcnt lgkmcnt(1)
mav 16,128,32
R 44,44032
mav 20,132,32
mav 24,136,32
mav 28,140,32
mav 16,144,36
R 48,45056
mav 20,148,36
mav 24,152,36
mav 28,156,36
s_waitcnt lgkmcnt(1)
mav 16,160,40
R 52,46080
s_sub_u32 s54, s54, 0x8000
mav 20,164,40
mav 24,168,40
mav 28,172,40
mav 16,176,44
R 56,47104
mav 20,180,44
mav 24,184,44
mav 28,188,44
s_waitcnt lgkmcnt(1)
mav 16,192,48
R 60,48128
mav 20,196,48
mav 24,200,48
mav 28,204,48
mav 16,208,52
mav 20,212,52
mav 24,216,52
mav 28,220,52
s_waitcnt lgkmcnt(0)
mav 16,224,56
mav 20,228,56
mav 24,232,56
mav 28,236,56
mav 16,240,60
mav 20,244,60
mav 24,248,60
mav 28,252,60
s_waitcnt vmcnt(11)
mvv 16,116,164
mvv 20,120,164
s_waitcnt vmcnt(10)
mvv 24,116,168
mvv 28,120,168
s_waitcnt vmcnt(9)
mvv 16,116,172
mvv 20,120,172
s_waitcnt vmcnt(8)
mvv 24,116,176
mvv 28,120,176
s_waitcnt vmcnt(7)
mvv 16,116,180
mvv 20,120,180
s_waitcnt vmcnt(6)
mvv 24,116,184
mvv 28,120,184
s_waitcnt vmcnt(5)
mvv 16,116,188
mvv 20,120,188
s_waitcnt vmcnt(4)
mvv 24,116,192
mvv 28,120,192
s_waitcnt vmcnt(3)
mvv 16,116,196
mvv 20,120,196
s_waitcnt vmcnt(2)
)"
R"(
mvv 24,116,200
mvv 28,120,200
s_nop 3
v_cvt_pk_bf16_f32 v64, v16, v17
v_cvt_pk_bf16_f32 v65, v18, v19
v_cvt_pk_bf16_f32 v66, v20, v21
v_cvt_pk_bf16_f32 v67, v22, v23
ds_write_b128 v94, v[64:67] offset:0
v_cvt_pk_bf16_f32 v68, v24, v25
v_cvt_pk_bf16_f32 v69, v26, v27
v_cvt_pk_bf16_f32 v70, v28, v29
v_cvt_pk_bf16_f32 v71, v30, v31
ds_write_b128 v95, v[68:71] offset:0
s_waitcnt lgkmcnt(0)
ds_read_b128 v[64:67], v96 offset:0
ds_read_b128 v[68:71], v96 offset:1024
s_waitcnt lgkmcnt(0)
buffer_store_dwordx4 v[64:67], v92, s[44:47], s54 offen
buffer_store_dwordx4 v[68:71], v93, s[44:47], s54 offen
s_endpgm
CR0_1:
s_cmp_eq_u32 s61, 0
s_cbranch_scc1 BR0_1
s_sub_u32 s61, s61, 1
s_sleep 8
buffer_load_dwordx2 v[76:77], v84, s[36:39], 0 offen sc1
s_waitcnt vmcnt(0)
ds_write_b32 v82, v76
v_sub_u32 v76, v76, v85
v_xor_b32 v77, v77, v73
v_cmp_lt_u64 vcc, s[62:63], v[76:77]
s_waitcnt lgkmcnt(0)
s_cbranch_vccnz CR0_1
s_branch BR0_1
CR0_2:
s_mov_b32 s58, m0
s_mov_b32 m0, 142336
DR0_2:
s_cmp_eq_u32 s61, 0
s_cbranch_scc1 ER0_2
s_sub_u32 s61, s61, 1
buffer_load_dword v84, s[36:39], 0 offen sc1 lds
s_sleep 1
ds_read_b32 v76, v82
s_waitcnt lgkmcnt(0)
v_cmp_gt_i32 vcc, v85, v76
s_cbranch_vccnz DR0_2
ER0_2:
s_mov_b32 m0, s58
s_branch BR0_2
CR0_3:
s_mov_b32 s58, m0
s_mov_b32 m0, 142336
DR0_3:
s_cmp_eq_u32 s61, 0
s_cbranch_scc1 ER0_3
s_sub_u32 s61, s61, 1
buffer_load_dword v84, s[36:39], 0 offen sc1 lds
s_sleep 1
ds_read_b32 v76, v82
s_waitcnt lgkmcnt(0)
v_cmp_gt_i32 vcc, v85, v76
s_cbranch_vccnz DR0_3
ER0_3:
s_mov_b32 m0, s58
s_branch BR0_3
CR0_4:
s_mov_b32 s58, m0
s_mov_b32 m0, 142336
DR0_4:
s_cmp_eq_u32 s61, 0
s_cbranch_scc1 ER0_4
s_sub_u32 s61, s61, 1
buffer_load_dword v84, s[36:39], 0 offen sc1 lds
s_sleep 1
ds_read_b32 v76, v82
s_waitcnt lgkmcnt(0)
v_cmp_gt_i32 vcc, v85, v76
s_cbranch_vccnz DR0_4
ER0_4:
s_mov_b32 m0, s58
s_branch BR0_4
CR0_5:
s_mov_b32 s58, m0
s_mov_b32 m0, 142336
DR0_5:
s_cmp_eq_u32 s61, 0
s_cbranch_scc1 ER0_5
s_sub_u32 s61, s61, 1
buffer_load_dword v84, s[36:39], 0 offen sc1 lds
s_sleep 1
ds_read_b32 v76, v82
s_waitcnt lgkmcnt(0)
v_cmp_gt_i32 vcc, v85, v76
s_cbranch_vccnz DR0_5
ER0_5:
s_mov_b32 m0, s58
s_branch BR0_5
CR0_7:
s_mov_b32 s58, m0
s_mov_b32 m0, 142336
DR0_7:
s_cmp_eq_u32 s61, 0
s_cbranch_scc1 ER0_7
s_sub_u32 s61, s61, 1
buffer_load_dword v84, s[36:39], 0 offen sc1 lds
s_sleep 1
ds_read_b32 v76, v82
s_waitcnt lgkmcnt(0)
v_cmp_gt_i32 vcc, v85, v76
s_cbranch_vccnz DR0_7
ER0_7:
s_mov_b32 m0, s58
s_branch BR0_7
CR0_8:
s_mov_b32 s58, m0
s_mov_b32 m0, 142336
DR0_8:
s_cmp_eq_u32 s61, 0
s_cbranch_scc1 ER0_8
s_sub_u32 s61, s61, 1
buffer_load_dword v84, s[36:39], 0 offen sc1 lds
s_sleep 1
ds_read_b32 v76, v82
s_waitcnt lgkmcnt(0)
v_cmp_gt_i32 vcc, v85, v76
s_cbranch_vccnz DR0_8
ER0_8:
s_mov_b32 m0, s58
s_branch BR0_8
CR0_9:
s_mov_b32 s58, m0
s_mov_b32 m0, 142336
DR0_9:
s_cmp_eq_u32 s61, 0
s_cbranch_scc1 ER0_9
s_sub_u32 s61, s61, 1
buffer_load_dword v84, s[36:39], 0 offen sc1 lds
s_sleep 1
ds_read_b32 v76, v82
s_waitcnt lgkmcnt(0)
v_cmp_gt_i32 vcc, v85, v76
s_cbranch_vccnz DR0_9
ER0_9:
s_mov_b32 m0, s58
s_branch BR0_9
CR0_10:
s_mov_b32 s58, m0
s_mov_b32 m0, 142336
DR0_10:
s_cmp_eq_u32 s61, 0
s_cbranch_scc1 ER0_10
s_sub_u32 s61, s61, 1
buffer_load_dword v84, s[36:39], 0 offen sc1 lds
s_sleep 1
ds_read_b32 v76, v82
s_waitcnt lgkmcnt(0)
v_cmp_gt_i32 vcc, v85, v76
s_cbranch_vccnz DR0_10
ER0_10:
s_mov_b32 m0, s58
s_branch BR0_10
CR0_12:
s_mov_b32 s58, m0
s_mov_b32 m0, 142336
DR0_12:
s_cmp_eq_u32 s61, 0
s_cbranch_scc1 ER0_12
s_sub_u32 s61, s61, 1
buffer_load_dword v84, s[36:39], 0 offen sc1 lds
s_sleep 1
ds_read_b32 v76, v82
s_waitcnt lgkmcnt(0)
v_cmp_gt_i32 vcc, v85, v76
s_cbranch_vccnz DR0_12
ER0_12:
s_mov_b32 m0, s58
s_branch BR0_12
CR0_13:
s_mov_b32 s58, m0
s_mov_b32 m0, 142336
DR0_13:
s_cmp_eq_u32 s61, 0
s_cbranch_scc1 ER0_13
s_sub_u32 s61, s61, 1
buffer_load_dword v84, s[36:39], 0 offen sc1 lds
s_sleep 1
ds_read_b32 v76, v82
s_waitcnt lgkmcnt(0)
v_cmp_gt_i32 vcc, v85, v76
s_cbranch_vccnz DR0_13
ER0_13:
s_mov_b32 m0, s58
s_branch BR0_13
CR0_14:
s_mov_b32 s58, m0
s_mov_b32 m0, 142336
DR0_14:
s_cmp_eq_u32 s61, 0
s_cbranch_scc1 ER0_14
s_sub_u32 s61, s61, 1
buffer_load_dword v84, s[36:39], 0 offen sc1 lds
s_sleep 1
ds_read_b32 v76, v82
s_waitcnt lgkmcnt(0)
v_cmp_gt_i32 vcc, v85, v76
s_cbranch_vccnz DR0_14
ER0_14:
s_mov_b32 m0, s58
s_branch BR0_14
CR0_15:
s_mov_b32 s58, m0
s_mov_b32 m0, 142336
DR0_15:
s_cmp_eq_u32 s61, 0
s_cbranch_scc1 ER0_15
s_sub_u32 s61, s61, 1
buffer_load_dword v84, s[36:39], 0 offen sc1 lds
s_sleep 1
ds_read_b32 v76, v82
s_waitcnt lgkmcnt(0)
v_cmp_gt_i32 vcc, v85, v76
s_cbranch_vccnz DR0_15
ER0_15:
s_mov_b32 m0, s58
s_branch BR0_15
CR0_16:
s_mov_b32 s58, m0
s_mov_b32 m0, 142336
DR0_16:
s_cmp_eq_u32 s61, 0
s_cbranch_scc1 ER0_16
s_sub_u32 s61, s61, 1
buffer_load_dword v84, s[36:39], 0 offen sc1 lds
s_sleep 1
ds_read_b32 v76, v82
s_waitcnt lgkmcnt(0)
v_cmp_gt_i32 vcc, v85, v76
s_cbranch_vccnz DR0_16
ER0_16:
s_mov_b32 m0, s58
s_branch BR0_16
LvR1:
s_add_u32 s49, s49, 346112
s_barrier
s_add_u32 m0, s50, 0
s_nop 0
D 83,0
D 83,1024
D 83,2048
D 83,3072
s_add_u32 s49, s49, 346112
s_barrier
s_add_u32 m0, s50, 32768
s_nop 0
D 83,0
D 83,1024
D 83,2048
D 83,3072
buffer_load_dwordx4 v[124:127], v90, s[36:39], s49 offen sc1
buffer_load_dwordx4 v[128:131], v90, s[36:39], s49 offen offset:1024 sc1
s_xor_b32 s50, s50, 0x10000
s_add_u32 s49, s49, 346112
s_waitcnt vmcnt(6)
s_barrier
R 32,0
buffer_load_dwordx4 v[132:135], v86, s[36:39], s49 offen sc1
R 36,1024
buffer_load_dwordx4 v[136:139], v86, s[36:39], s49 offen offset:1024 sc1
R 40,2048
buffer_load_dwordx4 v[140:143], v87, s[36:39], s49 offen sc1
s_waitcnt lgkmcnt(1)
v_mfma_f32_16x16x32_bf16 v[0:3], a[0:3], v[32:35], v[100:103]
R 44,3072
v_mfma_f32_16x16x32_bf16 v[4:7], a[4:7], v[32:35], v[104:107]
v_mfma_f32_16x16x32_bf16 v[8:11], a[8:11], v[32:35], v[108:111]
buffer_load_dwordx4 v[144:147], v87, s[36:39], s49 offen offset:1024 sc1
v_mfma_f32_16x16x32_bf16 v[12:15], a[12:15], v[32:35], v[112:115]
mav 0,16,36
R 48,4096
mav 4,20,36
mav 8,24,36
mav 12,28,36
buffer_load_dwordx4 v[148:151], v88, s[36:39], s49 offen sc1
s_waitcnt lgkmcnt(1)
mav 0,32,40
R 52,5120
mav 4,36,40
mav 8,40,40
mav 12,44,40
buffer_load_dwordx4 v[152:155], v88, s[36:39], s49 offen offset:1024 sc1
mav 0,48,44
R 56,6144
mav 4,52,44
mav 8,56,44
mav 12,60,44
s_waitcnt lgkmcnt(1)
mav 0,64,48
R 60,7168
buffer_load_dwordx4 v[156:159], v89, s[36:39], s49 offen sc1
mav 4,68,48
mav 8,72,48
mav 12,76,48
mav 0,80,52
R 32,8192
buffer_load_dwordx4 v[160:163], v89, s[36:39], s49 offen offset:1024 sc1
mav 4,84,52
mav 8,88,52
mav 12,92,52
s_add_u32 m0, s50, 0
s_waitcnt lgkmcnt(1)
mav 0,96,56
R 36,9216
mav 4,100,56
D 83,0
mav 8,104,56
mav 12,108,56
mav 0,112,60
R 40,10240
mav 4,116,60
D 83,1024
mav 8,120,60
mav 12,124,60
s_waitcnt lgkmcnt(1)
mav 0,128,32
R 44,11264
mav 4,132,32
mav 8,136,32
D 83,2048
mav 12,140,32
mav 0,144,36
R 48,12288
mav 4,148,36
mav 8,152,36
D 83,3072
mav 12,156,36
s_waitcnt lgkmcnt(1)
mav 0,160,40
R 52,13312
mav 4,164,40
mav 8,168,40
mav 12,172,40
buffer_load_dwordx4 v[164:167], v90, s[36:39], s49 offen sc1
mav 0,176,44
R 56,14336
mav 4,180,44
mav 8,184,44
mav 12,188,44
buffer_load_dwordx4 v[168:171], v90, s[36:39], s49 offen offset:1024 sc1
s_waitcnt lgkmcnt(1)
mav 0,192,48
R 60,15360
mav 4,196,48
mav 8,200,48
mav 12,204,48
mav 0,208,52
mav 4,212,52
mav 8,216,52
mav 12,220,52
s_waitcnt lgkmcnt(0)
mav 0,224,56
mav 4,228,56
mav 8,232,56
mav 12,236,56
mav 0,240,60
mav 4,244,60
mav 8,248,60
mav 12,252,60
s_waitcnt vmcnt(15)
mvv 0,116,124
mvv 4,120,124
s_waitcnt vmcnt(14)
mvv 8,116,128
mvv 12,120,128
s_waitcnt vmcnt(13)
mvv 0,116,132
mvv 4,120,132
s_waitcnt vmcnt(12)
mvv 8,116,136
s_add_u32 s49, s49, 346112
mvv 12,120,136
s_barrier
s_waitcnt vmcnt(11)
mvv 0,116,140
R 32,32768
mvv 4,120,140
buffer_load_dwordx4 v[172:175], v86, s[36:39], s49 offen sc1
s_waitcnt vmcnt(11)
mvv 8,116,144
mvv 12,120,144
s_waitcnt vmcnt(10)
mvv 0,116,148
R 36,33792
mvv 4,120,148
buffer_load_dwordx4 v[176:179], v86, s[36:39], s49 offen offset:1024 sc1
s_waitcnt vmcnt(10)
mvv 8,116,152
mvv 12,120,152
s_waitcnt vmcnt(9)
mvv 0,116,156
R 40,34816
mvv 4,120,156
s_waitcnt vmcnt(8)
mvv 8,116,160
buffer_load_dwordx4 v[180:183], v87, s[36:39], s49 offen sc1
mvv 12,120,160
s_waitcnt lgkmcnt(1)
v_mfma_f32_16x16x32_bf16 v[16:19], a[0:3], v[32:35], v[100:103]
R 44,35840
v_cvt_pk_bf16_f32 v64, v0, v1
v_mfma_f32_16x16x32_bf16 v[20:23], a[4:7], v[32:35], v[104:107]
v_cvt_pk_bf16_f32 v65, v2, v3
v_cvt_pk_bf16_f32 v66, v4, v5
v_mfma_f32_16x16x32_bf16 v[24:27], a[8:11], v[32:35], v[108:111]
buffer_load_dwordx4 v[184:187], v87, s[36:39], s49 offen offset:1024 sc1
v_cvt_pk_bf16_f32 v67, v6, v7
v_mfma_f32_16x16x32_bf16 v[28:31], a[12:15], v[32:35], v[112:115]
v_cvt_pk_bf16_f32 v68, v8, v9
v_cvt_pk_bf16_f32 v69, v10, v11
mav 16,16,36
R 48,36864
v_cvt_pk_bf16_f32 v70, v12, v13
mav 20,20,36
v_cvt_pk_bf16_f32 v71, v14, v15
v_mul_f32 v75, v0, v0
mav 24,24,36
buffer_store_dwordx4 v[64:67], v91, s[36:39], s49 offen sc1
v_add_f32 v74, v1, v0
mav 28,28,36
v_fmac_f32 v75, v1, v1
s_waitcnt lgkmcnt(1)
mav 16,32,40
R 52,37888
v_add_f32 v74, v2, v74
mav 20,36,40
v_fmac_f32 v75, v2, v2
v_add_f32 v74, v3, v74
mav 24,40,40
buffer_store_dwordx4 v[68:71], v91, s[36:39], s49 offen offset:1024 sc1
v_fmac_f32 v75, v3, v3
mav 28,44,40
v_add_f32 v74, v4, v74
v_fmac_f32 v75, v4, v4
mav 16,48,44
R 56,38912
v_add_f32 v74, v5, v74
mav 20,52,44
v_fmac_f32 v75, v5, v5
v_add_f32 v74, v6, v74
mav 24,56,44
buffer_load_dwordx4 v[188:191], v88, s[36:39], s49 offen sc1
v_fmac_f32 v75, v6, v6
mav 28,60,44
v_add_f32 v74, v7, v74
s_waitcnt lgkmcnt(1)
mav 16,64,48
R 60,39936
v_fmac_f32 v75, v7, v7
mav 20,68,48
v_add_f32 v74, v8, v74
v_fmac_f32 v75, v8, v8
mav 24,72,48
buffer_load_dwordx4 v[192:195], v88, s[36:39], s49 offen offset:1024 sc1
v_add_f32 v74, v9, v74
mav 28,76,48
v_fmac_f32 v75, v9, v9
v_add_f32 v74, v10, v74
mav 16,80,52
R 32,40960
v_fmac_f32 v75, v10, v10
mav 20,84,52
v_add_f32 v74, v11, v74
v_fmac_f32 v75, v11, v11
mav 24,88,52
buffer_load_dwordx4 v[196:199], v89, s[36:39], s49 offen sc1
v_add_f32 v74, v12, v74
mav 28,92,52
s_add_u32 m0, s50, 32768
s_waitcnt lgkmcnt(1)
mav 16,96,56
R 36,41984
v_fmac_f32 v75, v12, v12
mav 20,100,56
v_add_f32 v74, v13, v74
v_fmac_f32 v75, v13, v13
mav 24,104,56
buffer_load_dwordx4 v[200:203], v89, s[36:39], s49 offen offset:1024 sc1
v_add_f32 v74, v14, v74
mav 28,108,56
v_fmac_f32 v75, v14, v14
v_add_f32 v74, v15, v74
mav 16,112,60
R 40,43008
v_fmac_f32 v75, v15, v15
mav 20,116,60
mav 24,120,60
D 83,0
v_permlane32_swap_b32 v74, v75
mav 28,124,60
v_add_f32 v74, v74, v75
s_waitcnt lgkmcnt(1)
mav 16,128,32
R 44,44032
mav 20,132,32
mav 24,136,32
D 83,1024
mav 28,140,32
mav 16,144,36
R 48,45056
mav 20,148,36
mav 24,152,36
D 83,2048
mav 28,156,36
s_waitcnt lgkmcnt(1)
mav 16,160,40
R 52,46080
mav 20,164,40
mav 24,168,40
D 83,3072
mav 28,172,40
mav 16,176,44
R 56,47104
mav 20,180,44
mav 24,184,44
buffer_load_dwordx4 v[124:127], v90, s[36:39], s49 offen sc1
mav 28,188,44
s_waitcnt lgkmcnt(1)
mav 16,192,48
R 60,48128
mav 20,196,48
mav 24,200,48
buffer_load_dwordx4 v[128:131], v90, s[36:39], s49 offen offset:1024 sc1
mav 28,204,48
mav 16,208,52
mav 20,212,52
mav 24,216,52
v_add_u32 v72, 1, v72
mav 28,220,52
s_waitcnt lgkmcnt(0)
mav 16,224,56
mav 20,228,56
mav 24,232,56
mav 28,236,56
mav 16,240,60
s_waitcnt vmcnt(10)
buffer_store_dwordx2 v[72:73], v81, s[36:39], s52 offen sc1
mav 20,244,60
mav 24,248,60
ds_write_b32 v97, v74 offset:0
mav 28,252,60
mvv 16,116,164
mvv 20,120,164
mvv 24,116,168
s_xor_b32 s50, s50, 0x10000
mvv 28,120,168
mvv 16,116,172
mvv 20,120,172
v_xor_b32 v80, 0x10000, v80
mvv 24,116,176
s_add_u32 s49, s49, 346112
mvv 28,120,176
s_waitcnt lgkmcnt(0)
s_barrier
mvv 16,116,180
R 32,0
ds_read_b128 v[204:207], v98 offset:0
mvv 20,120,180
buffer_load_dwordx4 v[132:135], v86, s[36:39], s49 offen sc1
mvv 24,116,184
mvv 28,120,184
s_waitcnt vmcnt(11)
mvv 16,116,188
R 36,1024
mvv 20,120,188
buffer_load_dwordx4 v[136:139], v86, s[36:39], s49 offen offset:1024 sc1
s_waitcnt vmcnt(11)
mvv 24,116,192
mvv 28,120,192
s_waitcnt vmcnt(10)
mvv 16,116,196
R 40,2048
mvv 20,120,196
s_waitcnt vmcnt(9)
mvv 24,116,200
buffer_load_dwordx4 v[140:143], v87, s[36:39], s49 offen sc1
mvv 28,120,200
s_mov_b32 s60, -15
.p2align 6
LR1_1:
s_waitcnt lgkmcnt(1)
v_mfma_f32_16x16x32_bf16 v[0:3], a[0:3], v[32:35], v[100:103]
R 44,3072
v_add_f32 v204, v204, v205
v_mfma_f32_16x16x32_bf16 v[4:7], a[4:7], v[32:35], v[104:107]
v_add_f32 v206, v206, v207
v_add_f32 v204, v204, v206
v_mfma_f32_16x16x32_bf16 v[8:11], a[8:11], v[32:35], v[108:111]
buffer_load_dwordx4 v[144:147], v87, s[36:39], s49 offen offset:1024 sc1
v_add_f32_dpp v205, v204, v204 quad_perm:[1,0,3,2] row_mask:0xf bank_mask:0xf
v_mfma_f32_16x16x32_bf16 v[12:15], a[12:15], v[32:35], v[112:115]
buffer_store_dword v205, v99, s[36:39], s49 offen sc1
v_cvt_pk_bf16_f32 v64, v16, v17
mav 0,16,36
R 48,4096
v_cvt_pk_bf16_f32 v65, v18, v19
mav 4,20,36
v_cvt_pk_bf16_f32 v66, v20, v21
v_cvt_pk_bf16_f32 v67, v22, v23
mav 8,24,36
buffer_store_dwordx4 v[64:67], v91, s[36:39], s49 offen sc1
v_cvt_pk_bf16_f32 v68, v24, v25
mav 12,28,36
v_cvt_pk_bf16_f32 v69, v26, v27
s_waitcnt lgkmcnt(1)
mav 0,32,40
R 52,5120
v_cvt_pk_bf16_f32 v70, v28, v29
mav 4,36,40
v_cvt_pk_bf16_f32 v71, v30, v31
v_mul_f32 v75, v16, v16
mav 8,40,40
buffer_store_dwordx4 v[68:71], v91, s[36:39], s49 offen offset:1024 sc1
v_add_f32 v74, v17, v16
mav 12,44,40
v_fmac_f32 v75, v17, v17
v_add_f32 v74, v18, v74
mav 0,48,44
R 56,6144
v_fmac_f32 v75, v18, v18
mav 4,52,44
v_add_f32 v74, v19, v74
v_fmac_f32 v75, v19, v19
mav 8,56,44
buffer_load_dwordx4 v[148:151], v88, s[36:39], s49 offen sc1
v_add_f32 v74, v20, v74
mav 12,60,44
v_fmac_f32 v75, v20, v20
s_waitcnt lgkmcnt(1)
mav 0,64,48
R 60,7168
v_add_f32 v74, v21, v74
mav 4,68,48
v_fmac_f32 v75, v21, v21
v_add_f32 v74, v22, v74
mav 8,72,48
buffer_load_dwordx4 v[152:155], v88, s[36:39], s49 offen offset:1024 sc1
v_fmac_f32 v75, v22, v22
mav 12,76,48
v_add_f32 v74, v23, v74
v_fmac_f32 v75, v23, v23
mav 0,80,52
R 32,8192
v_add_f32 v74, v24, v74
mav 4,84,52
v_fmac_f32 v75, v24, v24
v_add_f32 v74, v25, v74
mav 8,88,52
buffer_load_dwordx4 v[156:159], v89, s[36:39], s49 offen sc1
v_fmac_f32 v75, v25, v25
mav 12,92,52
s_add_u32 m0, s50, 0
s_waitcnt lgkmcnt(1)
mav 0,96,56
R 36,9216
v_add_f32 v74, v26, v74
mav 4,100,56
v_fmac_f32 v75, v26, v26
v_add_f32 v74, v27, v74
mav 8,104,56
buffer_load_dwordx4 v[160:163], v89, s[36:39], s49 offen offset:1024 sc1
v_fmac_f32 v75, v27, v27
mav 12,108,56
v_add_f32 v74, v28, v74
v_fmac_f32 v75, v28, v28
mav 0,112,60
R 40,10240
v_add_f32 v74, v29, v74
mav 4,116,60
v_fmac_f32 v75, v29, v29
v_add_f32 v74, v30, v74
mav 8,120,60
D 83,0
v_fmac_f32 v75, v30, v30
mav 12,124,60
v_add_f32 v74, v31, v74
s_waitcnt lgkmcnt(1)
mav 0,128,32
R 44,11264
v_fmac_f32 v75, v31, v31
mav 4,132,32
mav 8,136,32
D 83,1024
v_permlane32_swap_b32 v74, v75
mav 12,140,32
v_add_f32 v74, v74, v75
mav 0,144,36
R 48,12288
mav 4,148,36
mav 8,152,36
D 83,2048
mav 12,156,36
s_waitcnt lgkmcnt(1)
mav 0,160,40
R 52,13312
mav 4,164,40
mav 8,168,40
D 83,3072
mav 12,172,40
mav 0,176,44
R 56,14336
mav 4,180,44
mav 8,184,44
buffer_load_dwordx4 v[164:167], v90, s[36:39], s49 offen sc1
mav 12,188,44
s_waitcnt lgkmcnt(1)
mav 0,192,48
R 60,15360
mav 4,196,48
mav 8,200,48
buffer_load_dwordx4 v[168:171], v90, s[36:39], s49 offen offset:1024 sc1
mav 12,204,48
mav 0,208,52
mav 4,212,52
mav 8,216,52
v_add_u32 v72, 1, v72
mav 12,220,52
s_waitcnt lgkmcnt(0)
mav 0,224,56
mav 4,228,56
mav 8,232,56
mav 12,236,56
mav 0,240,60
s_waitcnt vmcnt(10)
buffer_store_dwordx2 v[72:73], v81, s[36:39], s52 offen sc1
mav 4,244,60
mav 8,248,60
ds_write_b32 v97, v74 offset:1024
mav 12,252,60
mvv 0,116,124
mvv 4,120,124
mvv 8,116,128
mvv 12,120,128
mvv 0,116,132
mvv 4,120,132
mvv 8,116,136
s_add_u32 s49, s49, 346112
mvv 12,120,136
s_waitcnt lgkmcnt(0)
s_barrier
mvv 0,116,140
R 32,32768
ds_read_b128 v[204:207], v98 offset:1024
mvv 4,120,140
buffer_load_dwordx4 v[172:175], v86, s[36:39], s49 offen sc1
mvv 8,116,144
mvv 12,120,144
s_waitcnt vmcnt(11)
mvv 0,116,148
R 36,33792
mvv 4,120,148
buffer_load_dwordx4 v[176:179], v86, s[36:39], s49 offen offset:1024 sc1
s_waitcnt vmcnt(11)
mvv 8,116,152
mvv 12,120,152
s_waitcnt vmcnt(10)
mvv 0,116,156
R 40,34816
mvv 4,120,156
s_waitcnt vmcnt(9)
mvv 8,116,160
buffer_load_dwordx4 v[180:183], v87, s[36:39], s49 offen sc1
mvv 12,120,160
s_waitcnt lgkmcnt(1)
v_mfma_f32_16x16x32_bf16 v[16:19], a[0:3], v[32:35], v[100:103]
R 44,35840
v_add_f32 v204, v204, v205
v_mfma_f32_16x16x32_bf16 v[20:23], a[4:7], v[32:35], v[104:107]
v_add_f32 v206, v206, v207
v_add_f32 v204, v204, v206
v_mfma_f32_16x16x32_bf16 v[24:27], a[8:11], v[32:35], v[108:111]
buffer_load_dwordx4 v[184:187], v87, s[36:39], s49 offen offset:1024 sc1
v_add_f32_dpp v205, v204, v204 quad_perm:[1,0,3,2] row_mask:0xf bank_mask:0xf
v_mfma_f32_16x16x32_bf16 v[28:31], a[12:15], v[32:35], v[112:115]
buffer_store_dword v205, v99, s[36:39], s49 offen sc1
v_cvt_pk_bf16_f32 v64, v0, v1
mav 16,16,36
R 48,36864
v_cvt_pk_bf16_f32 v65, v2, v3
mav 20,20,36
v_cvt_pk_bf16_f32 v66, v4, v5
v_cvt_pk_bf16_f32 v67, v6, v7
mav 24,24,36
buffer_store_dwordx4 v[64:67], v91, s[36:39], s49 offen sc1
v_cvt_pk_bf16_f32 v68, v8, v9
mav 28,28,36
v_cvt_pk_bf16_f32 v69, v10, v11
s_waitcnt lgkmcnt(1)
mav 16,32,40
R 52,37888
v_cvt_pk_bf16_f32 v70, v12, v13
mav 20,36,40
v_cvt_pk_bf16_f32 v71, v14, v15
v_mul_f32 v75, v0, v0
mav 24,40,40
buffer_store_dwordx4 v[68:71], v91, s[36:39], s49 offen offset:1024 sc1
v_add_f32 v74, v1, v0
mav 28,44,40
v_fmac_f32 v75, v1, v1
v_add_f32 v74, v2, v74
mav 16,48,44
R 56,38912
v_fmac_f32 v75, v2, v2
mav 20,52,44
v_add_f32 v74, v3, v74
v_fmac_f32 v75, v3, v3
mav 24,56,44
buffer_load_dwordx4 v[188:191], v88, s[36:39], s49 offen sc1
v_add_f32 v74, v4, v74
mav 28,60,44
v_fmac_f32 v75, v4, v4
s_waitcnt lgkmcnt(1)
mav 16,64,48
R 60,39936
v_add_f32 v74, v5, v74
mav 20,68,48
v_fmac_f32 v75, v5, v5
v_add_f32 v74, v6, v74
mav 24,72,48
buffer_load_dwordx4 v[192:195], v88, s[36:39], s49 offen offset:1024 sc1
v_fmac_f32 v75, v6, v6
mav 28,76,48
v_add_f32 v74, v7, v74
v_fmac_f32 v75, v7, v7
mav 16,80,52
R 32,40960
v_add_f32 v74, v8, v74
mav 20,84,52
v_fmac_f32 v75, v8, v8
v_add_f32 v74, v9, v74
mav 24,88,52
buffer_load_dwordx4 v[196:199], v89, s[36:39], s49 offen sc1
v_fmac_f32 v75, v9, v9
mav 28,92,52
s_add_u32 m0, s50, 32768
s_waitcnt lgkmcnt(1)
mav 16,96,56
R 36,41984
v_add_f32 v74, v10, v74
mav 20,100,56
v_fmac_f32 v75, v10, v10
v_add_f32 v74, v11, v74
mav 24,104,56
buffer_load_dwordx4 v[200:203], v89, s[36:39], s49 offen offset:1024 sc1
v_fmac_f32 v75, v11, v11
mav 28,108,56
v_add_f32 v74, v12, v74
v_fmac_f32 v75, v12, v12
mav 16,112,60
R 40,43008
v_add_f32 v74, v13, v74
mav 20,116,60
v_fmac_f32 v75, v13, v13
v_add_f32 v74, v14, v74
mav 24,120,60
D 83,0
v_fmac_f32 v75, v14, v14
mav 28,124,60
v_add_f32 v74, v15, v74
s_waitcnt lgkmcnt(1)
mav 16,128,32
R 44,44032
v_fmac_f32 v75, v15, v15
mav 20,132,32
mav 24,136,32
D 83,1024
v_permlane32_swap_b32 v74, v75
mav 28,140,32
v_add_f32 v74, v74, v75
mav 16,144,36
R 48,45056
mav 20,148,36
mav 24,152,36
D 83,2048
mav 28,156,36
s_waitcnt lgkmcnt(1)
mav 16,160,40
R 52,46080
mav 20,164,40
mav 24,168,40
D 83,3072
mav 28,172,40
mav 16,176,44
R 56,47104
mav 20,180,44
mav 24,184,44
buffer_load_dwordx4 v[124:127], v90, s[36:39], s49 offen sc1
mav 28,188,44
s_waitcnt lgkmcnt(1)
mav 16,192,48
R 60,48128
mav 20,196,48
mav 24,200,48
buffer_load_dwordx4 v[128:131], v90, s[36:39], s49 offen offset:1024 sc1
mav 28,204,48
mav 16,208,52
mav 20,212,52
mav 24,216,52
v_add_u32 v72, 1, v72
mav 28,220,52
s_waitcnt lgkmcnt(0)
mav 16,224,56
mav 20,228,56
mav 24,232,56
mav 28,236,56
mav 16,240,60
s_waitcnt vmcnt(10)
buffer_store_dwordx2 v[72:73], v81, s[36:39], s52 offen sc1
mav 20,244,60
mav 24,248,60
ds_write_b32 v97, v74 offset:0
mav 28,252,60
mvv 16,116,164
mvv 20,120,164
mvv 24,116,168
s_xor_b32 s50, s50, 0x10000
mvv 28,120,168
mvv 16,116,172
mvv 20,120,172
v_xor_b32 v80, 0x10000, v80
mvv 24,116,176
s_add_u32 s49, s49, 346112
mvv 28,120,176
s_waitcnt lgkmcnt(0)
s_barrier
mvv 16,116,180
R 32,0
ds_read_b128 v[204:207], v98 offset:0
mvv 20,120,180
buffer_load_dwordx4 v[132:135], v86, s[36:39], s49 offen sc1
mvv 24,116,184
mvv 28,120,184
s_waitcnt vmcnt(11)
mvv 16,116,188
R 36,1024
mvv 20,120,188
buffer_load_dwordx4 v[136:139], v86, s[36:39], s49 offen offset:1024 sc1
s_waitcnt vmcnt(11)
mvv 24,116,192
mvv 28,120,192
s_waitcnt vmcnt(10)
mvv 16,116,196
R 40,2048
mvv 20,120,196
s_waitcnt vmcnt(9)
mvv 24,116,200
buffer_load_dwordx4 v[140:143], v87, s[36:39], s49 offen sc1
mvv 28,120,200
s_add_u32 s60, s60, 1
s_cbranch_scc0 LR1_1
s_waitcnt lgkmcnt(1)
v_mfma_f32_16x16x32_bf16 v[0:3], a[0:3], v[32:35], v[100:103]
R 44,3072
v_add_f32 v204, v204, v205
v_mfma_f32_16x16x32_bf16 v[4:7], a[4:7], v[32:35], v[104:107]
v_add_f32 v206, v206, v207
v_add_f32 v204, v204, v206
v_mfma_f32_16x16x32_bf16 v[8:11], a[8:11], v[32:35], v[108:111]
buffer_load_dwordx4 v[144:147], v87, s[36:39], s49 offen offset:1024 sc1
v_add_f32_dpp v205, v204, v204 quad_perm:[1,0,3,2] row_mask:0xf bank_mask:0xf
v_mfma_f32_16x16x32_bf16 v[12:15], a[12:15], v[32:35], v[112:115]
buffer_store_dword v205, v99, s[36:39], s49 offen sc1
v_cvt_pk_bf16_f32 v64, v16, v17
mav 0,16,36
R 48,4096
v_cvt_pk_bf16_f32 v65, v18, v19
mav 4,20,36
v_cvt_pk_bf16_f32 v66, v20, v21
v_cvt_pk_bf16_f32 v67, v22, v23
mav 8,24,36
buffer_store_dwordx4 v[64:67], v91, s[36:39], s49 offen sc1
v_cvt_pk_bf16_f32 v68, v24, v25
mav 12,28,36
v_cvt_pk_bf16_f32 v69, v26, v27
s_waitcnt lgkmcnt(1)
mav 0,32,40
R 52,5120
v_cvt_pk_bf16_f32 v70, v28, v29
mav 4,36,40
v_cvt_pk_bf16_f32 v71, v30, v31
v_mul_f32 v75, v16, v16
mav 8,40,40
buffer_store_dwordx4 v[68:71], v91, s[36:39], s49 offen offset:1024 sc1
v_add_f32 v74, v17, v16
mav 12,44,40
v_fmac_f32 v75, v17, v17
v_add_f32 v74, v18, v74
mav 0,48,44
R 56,6144
v_fmac_f32 v75, v18, v18
mav 4,52,44
v_add_f32 v74, v19, v74
v_fmac_f32 v75, v19, v19
mav 8,56,44
buffer_load_dwordx4 v[148:151], v88, s[36:39], s49 offen sc1
v_add_f32 v74, v20, v74
mav 12,60,44
v_fmac_f32 v75, v20, v20
s_waitcnt lgkmcnt(1)
mav 0,64,48
R 60,7168
v_add_f32 v74, v21, v74
mav 4,68,48
v_fmac_f32 v75, v21, v21
v_add_f32 v74, v22, v74
mav 8,72,48
buffer_load_dwordx4 v[152:155], v88, s[36:39], s49 offen offset:1024 sc1
v_fmac_f32 v75, v22, v22
mav 12,76,48
v_add_f32 v74, v23, v74
v_fmac_f32 v75, v23, v23
mav 0,80,52
R 32,8192
v_add_f32 v74, v24, v74
mav 4,84,52
v_fmac_f32 v75, v24, v24
v_add_f32 v74, v25, v74
mav 8,88,52
buffer_load_dwordx4 v[156:159], v89, s[36:39], s49 offen sc1
v_fmac_f32 v75, v25, v25
mav 12,92,52
s_add_u32 m0, s50, 0
s_waitcnt lgkmcnt(1)
mav 0,96,56
R 36,9216
v_add_f32 v74, v26, v74
mav 4,100,56
v_fmac_f32 v75, v26, v26
v_add_f32 v74, v27, v74
mav 8,104,56
buffer_load_dwordx4 v[160:163], v89, s[36:39], s49 offen offset:1024 sc1
v_fmac_f32 v75, v27, v27
mav 12,108,56
v_add_f32 v74, v28, v74
v_fmac_f32 v75, v28, v28
mav 0,112,60
R 40,10240
v_add_f32 v74, v29, v74
mav 4,116,60
v_fmac_f32 v75, v29, v29
v_add_f32 v74, v30, v74
mav 8,120,60
D 83,0
v_fmac_f32 v75, v30, v30
mav 12,124,60
v_add_f32 v74, v31, v74
s_waitcnt lgkmcnt(1)
mav 0,128,32
R 44,11264
v_fmac_f32 v75, v31, v31
mav 4,132,32
mav 8,136,32
D 83,1024
v_permlane32_swap_b32 v74, v75
mav 12,140,32
v_add_f32 v74, v74, v75
mav 0,144,36
R 48,12288
mav 4,148,36
mav 8,152,36
D 83,2048
mav 12,156,36
s_waitcnt lgkmcnt(1)
mav 0,160,40
R 52,13312
mav 4,164,40
mav 8,168,40
D 83,3072
mav 12,172,40
mav 0,176,44
R 56,14336
mav 4,180,44
mav 8,184,44
buffer_load_dwordx4 v[164:167], v90, s[36:39], s49 offen sc1
mav 12,188,44
s_waitcnt lgkmcnt(1)
mav 0,192,48
R 60,15360
mav 4,196,48
mav 8,200,48
buffer_load_dwordx4 v[168:171], v90, s[36:39], s49 offen offset:1024 sc1
mav 12,204,48
mav 0,208,52
mav 4,212,52
mav 8,216,52
v_add_u32 v72, 1, v72
mav 12,220,52
s_waitcnt lgkmcnt(0)
mav 0,224,56
mav 4,228,56
mav 8,232,56
mav 12,236,56
mav 0,240,60
s_waitcnt vmcnt(10)
buffer_store_dwordx2 v[72:73], v81, s[36:39], s52 offen sc1
mav 4,244,60
mav 8,248,60
ds_write_b32 v97, v74 offset:1024
mav 12,252,60
mvv 0,116,124
mvv 4,120,124
mvv 8,116,128
mvv 12,120,128
mvv 0,116,132
mvv 4,120,132
mvv 8,116,136
s_add_u32 s49, s49, 346112
mvv 12,120,136
s_waitcnt lgkmcnt(0)
s_barrier
mvv 0,116,140
R 32,32768
ds_read_b128 v[204:207], v98 offset:1024
mvv 4,120,140
buffer_load_dwordx4 v[172:175], v86, s[36:39], s49 offen sc1
mvv 8,116,144
mvv 12,120,144
s_waitcnt vmcnt(11)
mvv 0,116,148
R 36,33792
mvv 4,120,148
buffer_load_dwordx4 v[176:179], v86, s[36:39], s49 offen offset:1024 sc1
s_waitcnt vmcnt(11)
mvv 8,116,152
mvv 12,120,152
s_waitcnt vmcnt(10)
mvv 0,116,156
R 40,34816
mvv 4,120,156
s_waitcnt vmcnt(9)
mvv 8,116,160
buffer_load_dwordx4 v[180:183], v87, s[36:39], s49 offen sc1
mvv 12,120,160
s_waitcnt lgkmcnt(1)
v_mfma_f32_16x16x32_bf16 v[16:19], a[0:3], v[32:35], v[100:103]
R 44,35840
v_add_f32 v204, v204, v205
v_mfma_f32_16x16x32_bf16 v[20:23], a[4:7], v[32:35], v[104:107]
v_add_f32 v206, v206, v207
v_add_f32 v204, v204, v206
v_mfma_f32_16x16x32_bf16 v[24:27], a[8:11], v[32:35], v[108:111]
buffer_load_dwordx4 v[184:187], v87, s[36:39], s49 offen offset:1024 sc1
v_add_f32_dpp v205, v204, v204 quad_perm:[1,0,3,2] row_mask:0xf bank_mask:0xf
v_mfma_f32_16x16x32_bf16 v[28:31], a[12:15], v[32:35], v[112:115]
buffer_store_dword v205, v99, s[36:39], s49 offen sc1
v_cvt_pk_bf16_f32 v64, v0, v1
mav 16,16,36
R 48,36864
v_cvt_pk_bf16_f32 v65, v2, v3
mav 20,20,36
v_cvt_pk_bf16_f32 v66, v4, v5
v_cvt_pk_bf16_f32 v67, v6, v7
mav 24,24,36
ds_write_b128 v94, v[64:67] offset:0
mav 28,28,36
buffer_load_dwordx4 v[188:191], v88, s[36:39], s49 offen sc1
s_waitcnt lgkmcnt(2)
mav 16,32,40
R 52,37888
v_cvt_pk_bf16_f32 v68, v8, v9
mav 20,36,40
v_cvt_pk_bf16_f32 v69, v10, v11
v_cvt_pk_bf16_f32 v70, v12, v13
mav 24,40,40
v_cvt_pk_bf16_f32 v71, v14, v15
mav 28,44,40
ds_write_b128 v95, v[68:71] offset:0
mav 16,48,44
R 56,38912
buffer_load_dwordx4 v[192:195], v88, s[36:39], s49 offen offset:1024 sc1
mav 20,52,44
mav 24,56,44
s_waitcnt lgkmcnt(1)
ds_read_b128 v[64:67], v96 offset:0
mav 28,60,44
ds_read_b128 v[68:71], v96 offset:1024
mav 16,64,48
R 60,39936
buffer_load_dwordx4 v[196:199], v89, s[36:39], s49 offen sc1
mav 20,68,48
mav 24,72,48
mav 28,76,48
mav 16,80,52
R 32,40960
buffer_load_dwordx4 v[200:203], v89, s[36:39], s49 offen offset:1024 sc1
mav 20,84,52
mav 24,88,52
mav 28,92,52
s_add_u32 m0, s50, 32768
s_waitcnt lgkmcnt(1)
mav 16,96,56
R 36,41984
buffer_store_dwordx4 v[64:67], v92, s[44:47], s54 offen
mav 20,100,56
mav 24,104,56
mav 28,108,56
mav 16,112,60
R 40,43008
buffer_store_dwordx4 v[68:71], v93, s[44:47], s54 offen
mav 20,116,60
mav 24,120,60
mav 28,124,60
s_waitcnt lgkmcnt(1)
mav 16,128,32
R 44,44032
D 83,0
mav 20,132,32
mav 24,136,32
mav 28,140,32
mav 16,144,36
R 48,45056
D 83,1024
mav 20,148,36
mav 24,152,36
mav 28,156,36
s_waitcnt lgkmcnt(1)
mav 16,160,40
R 52,46080
D 83,2048
mav 20,164,40
s_sub_u32 s54, s54, 0x8000
mav 24,168,40
mav 28,172,40
mav 16,176,44
R 56,47104
D 83,3072
mav 20,180,44
mav 24,184,44
mav 28,188,44
s_waitcnt lgkmcnt(1)
mav 16,192,48
R 60,48128
buffer_load_dwordx4 v[124:127], v90, s[36:39], s49 offen sc1
mav 20,196,48
mav 24,200,48
mav 28,204,48
mav 16,208,52
buffer_load_dwordx4 v[128:131], v90, s[36:39], s49 offen offset:1024 sc1
mav 20,212,52
mav 24,216,52
v_add_u32 v72, 68, v72
mav 28,220,52
s_waitcnt lgkmcnt(0)
mav 16,224,56
mav 20,228,56
mav 24,232,56
mav 28,236,56
mav 16,240,60
s_waitcnt vmcnt(12)
buffer_store_dwordx2 v[72:73], v81, s[36:39], s52 offen sc1
mav 20,244,60
mav 24,248,60
mav 28,252,60
mvv 16,116,164
mvv 20,120,164
mvv 24,116,168
s_xor_b32 s50, s50, 0x10000
mvv 28,120,168
mvv 16,116,172
mvv 20,120,172
v_xor_b32 v80, 0x10000, v80
mvv 24,116,176
s_add_u32 s49, s49, 346112
mvv 28,120,176
s_barrier
mvv 16,116,180
R 32,0
mvv 20,120,180
buffer_load_dwordx4 v[132:135], v86, s[36:39], s49 offen sc1
mvv 24,116,184
mvv 28,120,184
s_waitcnt vmcnt(13)
mvv 16,116,188
R 36,1024
mvv 20,120,188
buffer_load_dwordx4 v[136:139], v86, s[36:39], s49 offen offset:1024 sc1
s_waitcnt vmcnt(13)
mvv 24,116,192
mvv 28,120,192
s_waitcnt vmcnt(12)
mvv 16,116,196
R 40,2048
mvv 20,120,196
s_waitcnt vmcnt(11)
mvv 24,116,200
buffer_load_dwordx4 v[140:143], v87, s[36:39], s49 offen sc1
mvv 28,120,200
s_waitcnt lgkmcnt(1)
v_mfma_f32_16x16x32_bf16 v[0:3], a[0:3], v[32:35], v[100:103]
R 44,3072
v_cvt_pk_bf16_f32 v64, v16, v17
v_mfma_f32_16x16x32_bf16 v[4:7], a[4:7], v[32:35], v[104:107]
v_cvt_pk_bf16_f32 v65, v18, v19
v_cvt_pk_bf16_f32 v66, v20, v21
v_mfma_f32_16x16x32_bf16 v[8:11], a[8:11], v[32:35], v[108:111]
buffer_load_dwordx4 v[144:147], v87, s[36:39], s49 offen offset:1024 sc1
v_cvt_pk_bf16_f32 v67, v22, v23
v_mfma_f32_16x16x32_bf16 v[12:15], a[12:15], v[32:35], v[112:115]
ds_write_b128 v94, v[64:67] offset:0
mav 0,16,36
R 48,4096
v_cvt_pk_bf16_f32 v68, v24, v25
mav 4,20,36
v_cvt_pk_bf16_f32 v69, v26, v27
v_cvt_pk_bf16_f32 v70, v28, v29
mav 8,24,36
v_cvt_pk_bf16_f32 v71, v30, v31
mav 12,28,36
buffer_load_dwordx4 v[148:151], v88, s[36:39], s49 offen sc1
s_waitcnt lgkmcnt(2)
mav 0,32,40
R 52,5120
mav 4,36,40
ds_write_b128 v95, v[68:71] offset:0
mav 8,40,40
mav 12,44,40
buffer_load_dwordx4 v[152:155], v88, s[36:39], s49 offen offset:1024 sc1
mav 0,48,44
R 56,6144
mav 4,52,44
s_waitcnt lgkmcnt(1)
ds_read_b128 v[64:67], v96 offset:0
mav 8,56,44
ds_read_b128 v[68:71], v96 offset:1024
mav 12,60,44
mav 0,64,48
R 60,7168
buffer_load_dwordx4 v[156:159], v89, s[36:39], s49 offen sc1
mav 4,68,48
mav 8,72,48
mav 12,76,48
mav 0,80,52
R 32,8192
buffer_load_dwordx4 v[160:163], v89, s[36:39], s49 offen offset:1024 sc1
mav 4,84,52
mav 8,88,52
mav 12,92,52
s_add_u32 m0, s50, 0
s_waitcnt lgkmcnt(1)
mav 0,96,56
R 36,9216
buffer_store_dwordx4 v[64:67], v92, s[44:47], s54 offen
mav 4,100,56
mav 8,104,56
mav 12,108,56
mav 0,112,60
R 40,10240
buffer_store_dwordx4 v[68:71], v93, s[44:47], s54 offen
mav 4,116,60
mav 8,120,60
mav 12,124,60
s_waitcnt lgkmcnt(1)
mav 0,128,32
R 44,11264
D 83,0
mav 4,132,32
mav 8,136,32
mav 12,140,32
mav 0,144,36
R 48,12288
D 83,1024
mav 4,148,36
mav 8,152,36
mav 12,156,36
s_waitcnt lgkmcnt(1)
mav 0,160,40
R 52,13312
D 83,2048
mav 4,164,40
s_sub_u32 s54, s54, 0x8000
mav 8,168,40
mav 12,172,40
mav 0,176,44
R 56,14336
D 83,3072
mav 4,180,44
mav 8,184,44
mav 12,188,44
s_waitcnt lgkmcnt(1)
mav 0,192,48
R 60,15360
buffer_load_dwordx4 v[164:167], v90, s[36:39], s49 offen sc1
mav 4,196,48
mav 8,200,48
mav 12,204,48
mav 0,208,52
buffer_load_dwordx4 v[168:171], v90, s[36:39], s49 offen offset:1024 sc1
mav 4,212,52
mav 8,216,52
mav 12,220,52
s_waitcnt lgkmcnt(0)
mav 0,224,56
mav 4,228,56
mav 8,232,56
mav 12,236,56
mav 0,240,60
mav 4,244,60
mav 8,248,60
mav 12,252,60
s_waitcnt vmcnt(18)
mvv 0,116,124
mvv 4,120,124
s_waitcnt vmcnt(17)
mvv 8,116,128
mvv 12,120,128
s_waitcnt vmcnt(15)
mvv 0,116,132
mvv 4,120,132
s_waitcnt vmcnt(14)
mvv 8,116,136
s_add_u32 s49, s49, 346112
mvv 12,120,136
s_barrier
s_waitcnt vmcnt(13)
mvv 0,116,140
R 32,32768
mvv 4,120,140
buffer_load_dwordx4 v[172:175], v86, s[36:39], s49 offen sc1
s_waitcnt vmcnt(13)
mvv 8,116,144
mvv 12,120,144
s_waitcnt vmcnt(12)
mvv 0,116,148
R 36,33792
mvv 4,120,148
buffer_load_dwordx4 v[176:179], v86, s[36:39], s49 offen offset:1024 sc1
s_waitcnt vmcnt(12)
mvv 8,116,152
mvv 12,120,152
s_waitcnt vmcnt(11)
mvv 0,116,156
R 40,34816
mvv 4,120,156
s_waitcnt vmcnt(10)
mvv 8,116,160
buffer_load_dwordx4 v[180:183], v87, s[36:39], s49 offen sc1
mvv 12,120,160
s_waitcnt lgkmcnt(1)
v_mfma_f32_16x16x32_bf16 v[16:19], a[0:3], v[32:35], v[100:103]
R 44,35840
v_cvt_pk_bf16_f32 v64, v0, v1
v_mfma_f32_16x16x32_bf16 v[20:23], a[4:7], v[32:35], v[104:107]
v_cvt_pk_bf16_f32 v65, v2, v3
v_cvt_pk_bf16_f32 v66, v4, v5
v_mfma_f32_16x16x32_bf16 v[24:27], a[8:11], v[32:35], v[108:111]
buffer_load_dwordx4 v[184:187], v87, s[36:39], s49 offen offset:1024 sc1
v_cvt_pk_bf16_f32 v67, v6, v7
v_mfma_f32_16x16x32_bf16 v[28:31], a[12:15], v[32:35], v[112:115]
ds_write_b128 v94, v[64:67] offset:0
mav 16,16,36
R 48,36864
v_cvt_pk_bf16_f32 v68, v8, v9
mav 20,20,36
v_cvt_pk_bf16_f32 v69, v10, v11
v_cvt_pk_bf16_f32 v70, v12, v13
mav 24,24,36
v_cvt_pk_bf16_f32 v71, v14, v15
mav 28,28,36
buffer_load_dwordx4 v[188:191], v88, s[36:39], s49 offen sc1
s_waitcnt lgkmcnt(2)
mav 16,32,40
R 52,37888
mav 20,36,40
ds_write_b128 v95, v[68:71] offset:0
mav 24,40,40
mav 28,44,40
buffer_load_dwordx4 v[192:195], v88, s[36:39], s49 offen offset:1024 sc1
mav 16,48,44
R 56,38912
mav 20,52,44
s_waitcnt lgkmcnt(1)
ds_read_b128 v[64:67], v96 offset:0
mav 24,56,44
ds_read_b128 v[68:71], v96 offset:1024
mav 28,60,44
mav 16,64,48
R 60,39936
buffer_load_dwordx4 v[196:199], v89, s[36:39], s49 offen sc1
mav 20,68,48
mav 24,72,48
mav 28,76,48
mav 16,80,52
R 32,40960
buffer_load_dwordx4 v[200:203], v89, s[36:39], s49 offen offset:1024 sc1
mav 20,84,52
mav 24,88,52
mav 28,92,52
s_add_u32 m0, s50, 32768
s_waitcnt lgkmcnt(1)
mav 16,96,56
R 36,41984
buffer_store_dwordx4 v[64:67], v92, s[44:47], s54 offen
mav 20,100,56
mav 24,104,56
mav 28,108,56
mav 16,112,60
R 40,43008
buffer_store_dwordx4 v[68:71], v93, s[44:47], s54 offen
mav 20,116,60
mav 24,120,60
mav 28,124,60
s_waitcnt lgkmcnt(1)
mav 16,128,32
R 44,44032
D 83,0
mav 20,132,32
mav 24,136,32
mav 28,140,32
mav 16,144,36
R 48,45056
D 83,1024
mav 20,148,36
mav 24,152,36
mav 28,156,36
s_waitcnt lgkmcnt(1)
mav 16,160,40
R 52,46080
D 83,2048
mav 20,164,40
s_sub_u32 s54, s54, 0x8000
mav 24,168,40
mav 28,172,40
mav 16,176,44
R 56,47104
D 83,3072
mav 20,180,44
mav 24,184,44
mav 28,188,44
s_waitcnt lgkmcnt(1)
mav 16,192,48
R 60,48128
buffer_load_dwordx4 v[124:127], v90, s[36:39], s49 offen sc1
mav 20,196,48
mav 24,200,48
mav 28,204,48
mav 16,208,52
buffer_load_dwordx4 v[128:131], v90, s[36:39], s49 offen offset:1024 sc1
mav 20,212,52
mav 24,216,52
mav 28,220,52
s_waitcnt lgkmcnt(0)
mav 16,224,56
mav 20,228,56
mav 24,232,56
mav 28,236,56
mav 16,240,60
mav 20,244,60
mav 24,248,60
mav 28,252,60
s_waitcnt vmcnt(17)
mvv 16,116,164
mvv 20,120,164
s_waitcnt vmcnt(16)
mvv 24,116,168
s_xor_b32 s50, s50, 0x10000
mvv 28,120,168
s_waitcnt vmcnt(15)
mvv 16,116,172
mvv 20,120,172
v_xor_b32 v80, 0x10000, v80
s_waitcnt vmcnt(14)
mvv 24,116,176
s_add_u32 s49, s49, 346112
mvv 28,120,176
s_barrier
s_waitcnt vmcnt(13)
mvv 16,116,180
R 32,0
mvv 20,120,180
buffer_load_dwordx4 v[132:135], v86, s[36:39], s49 offen sc1
s_waitcnt vmcnt(13)
mvv 24,116,184
mvv 28,120,184
s_waitcnt vmcnt(12)
mvv 16,116,188
R 36,1024
mvv 20,120,188
buffer_load_dwordx4 v[136:139], v86, s[36:39], s49 offen offset:1024 sc1
s_waitcnt vmcnt(12)
mvv 24,116,192
mvv 28,120,192
s_waitcnt vmcnt(11)
mvv 16,116,196
R 40,2048
mvv 20,120,196
s_waitcnt vmcnt(10)
mvv 24,116,200
buffer_load_dwordx4 v[140:143], v87, s[36:39], s49 offen sc1
mvv 28,120,200
s_mov_b32 s60, -12
.p2align 6
LR1_2:
s_waitcnt lgkmcnt(1)
v_mfma_f32_16x16x32_bf16 v[0:3], a[0:3], v[32:35], v[100:103]
R 44,3072
v_cvt_pk_bf16_f32 v64, v16, v17
v_mfma_f32_16x16x32_bf16 v[4:7], a[4:7], v[32:35], v[104:107]
v_cvt_pk_bf16_f32 v65, v18, v19
v_cvt_pk_bf16_f32 v66, v20, v21
v_mfma_f32_16x16x32_bf16 v[8:11], a[8:11], v[32:35], v[108:111]
buffer_load_dwordx4 v[144:147], v87, s[36:39], s49 offen offset:1024 sc1
v_cvt_pk_bf16_f32 v67, v22, v23
v_mfma_f32_16x16x32_bf16 v[12:15], a[12:15], v[32:35], v[112:115]
ds_write_b128 v94, v[64:67] offset:0
mav 0,16,36
R 48,4096
v_cvt_pk_bf16_f32 v68, v24, v25
mav 4,20,36
v_cvt_pk_bf16_f32 v69, v26, v27
v_cvt_pk_bf16_f32 v70, v28, v29
mav 8,24,36
v_cvt_pk_bf16_f32 v71, v30, v31
mav 12,28,36
buffer_load_dwordx4 v[148:151], v88, s[36:39], s49 offen sc1
s_waitcnt lgkmcnt(2)
mav 0,32,40
R 52,5120
mav 4,36,40
ds_write_b128 v95, v[68:71] offset:0
mav 8,40,40
mav 12,44,40
buffer_load_dwordx4 v[152:155], v88, s[36:39], s49 offen offset:1024 sc1
mav 0,48,44
R 56,6144
mav 4,52,44
s_waitcnt lgkmcnt(1)
ds_read_b128 v[64:67], v96 offset:0
mav 8,56,44
ds_read_b128 v[68:71], v96 offset:1024
mav 12,60,44
mav 0,64,48
R 60,7168
buffer_load_dwordx4 v[156:159], v89, s[36:39], s49 offen sc1
mav 4,68,48
mav 8,72,48
mav 12,76,48
mav 0,80,52
R 32,8192
buffer_load_dwordx4 v[160:163], v89, s[36:39], s49 offen offset:1024 sc1
mav 4,84,52
mav 8,88,52
mav 12,92,52
s_add_u32 m0, s50, 0
s_waitcnt lgkmcnt(1)
mav 0,96,56
R 36,9216
buffer_store_dwordx4 v[64:67], v92, s[44:47], s54 offen
mav 4,100,56
mav 8,104,56
mav 12,108,56
mav 0,112,60
R 40,10240
buffer_store_dwordx4 v[68:71], v93, s[44:47], s54 offen
mav 4,116,60
mav 8,120,60
mav 12,124,60
s_waitcnt lgkmcnt(1)
mav 0,128,32
R 44,11264
D 83,0
mav 4,132,32
mav 8,136,32
mav 12,140,32
mav 0,144,36
R 48,12288
D 83,1024
mav 4,148,36
mav 8,152,36
mav 12,156,36
s_waitcnt lgkmcnt(1)
mav 0,160,40
R 52,13312
D 83,2048
mav 4,164,40
s_sub_u32 s54, s54, 0x8000
mav 8,168,40
mav 12,172,40
mav 0,176,44
R 56,14336
D 83,3072
mav 4,180,44
mav 8,184,44
mav 12,188,44
s_waitcnt lgkmcnt(1)
mav 0,192,48
R 60,15360
buffer_load_dwordx4 v[164:167], v90, s[36:39], s49 offen sc1
mav 4,196,48
mav 8,200,48
mav 12,204,48
mav 0,208,52
buffer_load_dwordx4 v[168:171], v90, s[36:39], s49 offen offset:1024 sc1
mav 4,212,52
mav 8,216,52
mav 12,220,52
s_waitcnt lgkmcnt(0)
mav 0,224,56
mav 4,228,56
mav 8,232,56
mav 12,236,56
mav 0,240,60
mav 4,244,60
mav 8,248,60
mav 12,252,60
s_waitcnt vmcnt(17)
mvv 0,116,124
mvv 4,120,124
s_waitcnt vmcnt(16)
mvv 8,116,128
mvv 12,120,128
s_waitcnt vmcnt(15)
mvv 0,116,132
mvv 4,120,132
s_waitcnt vmcnt(14)
mvv 8,116,136
s_add_u32 s49, s49, 346112
mvv 12,120,136
s_barrier
s_waitcnt vmcnt(13)
mvv 0,116,140
R 32,32768
mvv 4,120,140
buffer_load_dwordx4 v[172:175], v86, s[36:39], s49 offen sc1
s_waitcnt vmcnt(13)
mvv 8,116,144
mvv 12,120,144
s_waitcnt vmcnt(12)
mvv 0,116,148
R 36,33792
mvv 4,120,148
buffer_load_dwordx4 v[176:179], v86, s[36:39], s49 offen offset:1024 sc1
s_waitcnt vmcnt(12)
mvv 8,116,152
mvv 12,120,152
s_waitcnt vmcnt(11)
mvv 0,116,156
R 40,34816
mvv 4,120,156
s_waitcnt vmcnt(10)
mvv 8,116,160
buffer_load_dwordx4 v[180:183], v87, s[36:39], s49 offen sc1
mvv 12,120,160
s_waitcnt lgkmcnt(1)
v_mfma_f32_16x16x32_bf16 v[16:19], a[0:3], v[32:35], v[100:103]
R 44,35840
v_cvt_pk_bf16_f32 v64, v0, v1
v_mfma_f32_16x16x32_bf16 v[20:23], a[4:7], v[32:35], v[104:107]
v_cvt_pk_bf16_f32 v65, v2, v3
v_cvt_pk_bf16_f32 v66, v4, v5
v_mfma_f32_16x16x32_bf16 v[24:27], a[8:11], v[32:35], v[108:111]
buffer_load_dwordx4 v[184:187], v87, s[36:39], s49 offen offset:1024 sc1
v_cvt_pk_bf16_f32 v67, v6, v7
v_mfma_f32_16x16x32_bf16 v[28:31], a[12:15], v[32:35], v[112:115]
ds_write_b128 v94, v[64:67] offset:0
mav 16,16,36
R 48,36864
v_cvt_pk_bf16_f32 v68, v8, v9
mav 20,20,36
v_cvt_pk_bf16_f32 v69, v10, v11
v_cvt_pk_bf16_f32 v70, v12, v13
mav 24,24,36
v_cvt_pk_bf16_f32 v71, v14, v15
mav 28,28,36
buffer_load_dwordx4 v[188:191], v88, s[36:39], s49 offen sc1
s_waitcnt lgkmcnt(2)
mav 16,32,40
R 52,37888
mav 20,36,40
ds_write_b128 v95, v[68:71] offset:0
mav 24,40,40
mav 28,44,40
buffer_load_dwordx4 v[192:195], v88, s[36:39], s49 offen offset:1024 sc1
mav 16,48,44
R 56,38912
mav 20,52,44
s_waitcnt lgkmcnt(1)
ds_read_b128 v[64:67], v96 offset:0
mav 24,56,44
ds_read_b128 v[68:71], v96 offset:1024
mav 28,60,44
mav 16,64,48
R 60,39936
buffer_load_dwordx4 v[196:199], v89, s[36:39], s49 offen sc1
mav 20,68,48
mav 24,72,48
mav 28,76,48
mav 16,80,52
R 32,40960
buffer_load_dwordx4 v[200:203], v89, s[36:39], s49 offen offset:1024 sc1
mav 20,84,52
mav 24,88,52
mav 28,92,52
s_add_u32 m0, s50, 32768
s_waitcnt lgkmcnt(1)
mav 16,96,56
R 36,41984
buffer_store_dwordx4 v[64:67], v92, s[44:47], s54 offen
mav 20,100,56
mav 24,104,56
mav 28,108,56
mav 16,112,60
R 40,43008
buffer_store_dwordx4 v[68:71], v93, s[44:47], s54 offen
mav 20,116,60
mav 24,120,60
mav 28,124,60
s_waitcnt lgkmcnt(1)
mav 16,128,32
R 44,44032
D 83,0
mav 20,132,32
mav 24,136,32
mav 28,140,32
mav 16,144,36
R 48,45056
D 83,1024
mav 20,148,36
mav 24,152,36
mav 28,156,36
s_waitcnt lgkmcnt(1)
mav 16,160,40
R 52,46080
D 83,2048
mav 20,164,40
s_sub_u32 s54, s54, 0x8000
mav 24,168,40
mav 28,172,40
mav 16,176,44
R 56,47104
D 83,3072
mav 20,180,44
mav 24,184,44
mav 28,188,44
s_waitcnt lgkmcnt(1)
mav 16,192,48
R 60,48128
buffer_load_dwordx4 v[124:127], v90, s[36:39], s49 offen sc1
mav 20,196,48
mav 24,200,48
mav 28,204,48
mav 16,208,52
buffer_load_dwordx4 v[128:131], v90, s[36:39], s49 offen offset:1024 sc1
mav 20,212,52
mav 24,216,52
mav 28,220,52
s_waitcnt lgkmcnt(0)
mav 16,224,56
mav 20,228,56
mav 24,232,56
mav 28,236,56
mav 16,240,60
mav 20,244,60
mav 24,248,60
mav 28,252,60
s_waitcnt vmcnt(17)
mvv 16,116,164
mvv 20,120,164
s_waitcnt vmcnt(16)
mvv 24,116,168
s_xor_b32 s50, s50, 0x10000
mvv 28,120,168
s_waitcnt vmcnt(15)
mvv 16,116,172
mvv 20,120,172
v_xor_b32 v80, 0x10000, v80
s_waitcnt vmcnt(14)
mvv 24,116,176
s_add_u32 s49, s49, 346112
mvv 28,120,176
s_barrier
s_waitcnt vmcnt(13)
mvv 16,116,180
R 32,0
mvv 20,120,180
buffer_load_dwordx4 v[132:135], v86, s[36:39], s49 offen sc1
s_waitcnt vmcnt(13)
mvv 24,116,184
mvv 28,120,184
s_waitcnt vmcnt(12)
mvv 16,116,188
R 36,1024
mvv 20,120,188
buffer_load_dwordx4 v[136:139], v86, s[36:39], s49 offen offset:1024 sc1
s_waitcnt vmcnt(12)
mvv 24,116,192
mvv 28,120,192
s_waitcnt vmcnt(11)
mvv 16,116,196
R 40,2048
mvv 20,120,196
s_waitcnt vmcnt(10)
mvv 24,116,200
buffer_load_dwordx4 v[140:143], v87, s[36:39], s49 offen sc1
mvv 28,120,200
s_add_u32 s60, s60, 1
s_cbranch_scc0 LR1_2
s_waitcnt lgkmcnt(1)
v_mfma_f32_16x16x32_bf16 v[0:3], a[0:3], v[32:35], v[100:103]
R 44,3072
v_cvt_pk_bf16_f32 v64, v16, v17
v_mfma_f32_16x16x32_bf16 v[4:7], a[4:7], v[32:35], v[104:107]
v_cvt_pk_bf16_f32 v65, v18, v19
v_cvt_pk_bf16_f32 v66, v20, v21
v_mfma_f32_16x16x32_bf16 v[8:11], a[8:11], v[32:35], v[108:111]
buffer_load_dwordx4 v[144:147], v87, s[36:39], s49 offen offset:1024 sc1
v_cvt_pk_bf16_f32 v67, v22, v23
v_mfma_f32_16x16x32_bf16 v[12:15], a[12:15], v[32:35], v[112:115]
ds_write_b128 v94, v[64:67] offset:0
mav 0,16,36
R 48,4096
v_cvt_pk_bf16_f32 v68, v24, v25
mav 4,20,36
v_cvt_pk_bf16_f32 v69, v26, v27
v_cvt_pk_bf16_f32 v70, v28, v29
mav 8,24,36
v_cvt_pk_bf16_f32 v71, v30, v31
mav 12,28,36
buffer_load_dwordx4 v[148:151], v88, s[36:39], s49 offen sc1
s_waitcnt lgkmcnt(2)
mav 0,32,40
R 52,5120
mav 4,36,40
ds_write_b128 v95, v[68:71] offset:0
mav 8,40,40
mav 12,44,40
buffer_load_dwordx4 v[152:155], v88, s[36:39], s49 offen offset:1024 sc1
mav 0,48,44
R 56,6144
mav 4,52,44
s_waitcnt lgkmcnt(1)
ds_read_b128 v[64:67], v96 offset:0
mav 8,56,44
ds_read_b128 v[68:71], v96 offset:1024
mav 12,60,44
mav 0,64,48
R 60,7168
buffer_load_dwordx4 v[156:159], v89, s[36:39], s49 offen sc1
mav 4,68,48
mav 8,72,48
mav 12,76,48
mav 0,80,52
R 32,8192
buffer_load_dwordx4 v[160:163], v89, s[36:39], s49 offen offset:1024 sc1
mav 4,84,52
mav 8,88,52
mav 12,92,52
s_add_u32 m0, s50, 0
s_waitcnt lgkmcnt(1)
mav 0,96,56
R 36,9216
buffer_store_dwordx4 v[64:67], v92, s[44:47], s54 offen
mav 4,100,56
mav 8,104,56
mav 12,108,56
mav 0,112,60
R 40,10240
buffer_store_dwordx4 v[68:71], v93, s[44:47], s54 offen
mav 4,116,60
mav 8,120,60
mav 12,124,60
s_waitcnt lgkmcnt(1)
mav 0,128,32
R 44,11264
D 83,0
mav 4,132,32
mav 8,136,32
mav 12,140,32
mav 0,144,36
R 48,12288
D 83,1024
mav 4,148,36
mav 8,152,36
mav 12,156,36
s_waitcnt lgkmcnt(1)
mav 0,160,40
R 52,13312
D 83,2048
mav 4,164,40
s_sub_u32 s54, s54, 0x8000
mav 8,168,40
mav 12,172,40
mav 0,176,44
R 56,14336
D 83,3072
mav 4,180,44
mav 8,184,44
mav 12,188,44
s_waitcnt lgkmcnt(1)
mav 0,192,48
R 60,15360
buffer_load_dwordx4 v[164:167], v90, s[36:39], s49 offen sc1
)"
R"(
mav 4,196,48
mav 8,200,48
mav 12,204,48
mav 0,208,52
buffer_load_dwordx4 v[168:171], v90, s[36:39], s49 offen offset:1024 sc1
mav 4,212,52
mav 8,216,52
mav 12,220,52
s_waitcnt lgkmcnt(0)
mav 0,224,56
mav 4,228,56
mav 8,232,56
mav 12,236,56
mav 0,240,60
mav 4,244,60
mav 8,248,60
mav 12,252,60
s_waitcnt vmcnt(17)
mvv 0,116,124
mvv 4,120,124
s_waitcnt vmcnt(16)
mvv 8,116,128
mvv 12,120,128
s_waitcnt vmcnt(15)
mvv 0,116,132
mvv 4,120,132
s_waitcnt vmcnt(14)
mvv 8,116,136
s_add_u32 s49, s49, 346112
mvv 12,120,136
s_barrier
s_waitcnt vmcnt(13)
mvv 0,116,140
R 32,32768
mvv 4,120,140
buffer_load_dwordx4 v[172:175], v86, s[36:39], s49 offen sc1
s_waitcnt vmcnt(13)
mvv 8,116,144
mvv 12,120,144
s_waitcnt vmcnt(12)
mvv 0,116,148
R 36,33792
mvv 4,120,148
buffer_load_dwordx4 v[176:179], v86, s[36:39], s49 offen offset:1024 sc1
s_waitcnt vmcnt(12)
mvv 8,116,152
mvv 12,120,152
s_waitcnt vmcnt(11)
mvv 0,116,156
R 40,34816
mvv 4,120,156
s_waitcnt vmcnt(10)
mvv 8,116,160
buffer_load_dwordx4 v[180:183], v87, s[36:39], s49 offen sc1
mvv 12,120,160
s_waitcnt lgkmcnt(1)
v_mfma_f32_16x16x32_bf16 v[16:19], a[0:3], v[32:35], v[100:103]
R 44,35840
v_cvt_pk_bf16_f32 v64, v0, v1
v_mfma_f32_16x16x32_bf16 v[20:23], a[4:7], v[32:35], v[104:107]
v_cvt_pk_bf16_f32 v65, v2, v3
v_cvt_pk_bf16_f32 v66, v4, v5
v_mfma_f32_16x16x32_bf16 v[24:27], a[8:11], v[32:35], v[108:111]
buffer_load_dwordx4 v[184:187], v87, s[36:39], s49 offen offset:1024 sc1
v_cvt_pk_bf16_f32 v67, v6, v7
v_mfma_f32_16x16x32_bf16 v[28:31], a[12:15], v[32:35], v[112:115]
ds_write_b128 v94, v[64:67] offset:0
mav 16,16,36
R 48,36864
v_cvt_pk_bf16_f32 v68, v8, v9
mav 20,20,36
v_cvt_pk_bf16_f32 v69, v10, v11
v_cvt_pk_bf16_f32 v70, v12, v13
mav 24,24,36
v_cvt_pk_bf16_f32 v71, v14, v15
mav 28,28,36
buffer_load_dwordx4 v[188:191], v88, s[36:39], s49 offen sc1
s_waitcnt lgkmcnt(2)
mav 16,32,40
R 52,37888
mav 20,36,40
ds_write_b128 v95, v[68:71] offset:0
mav 24,40,40
mav 28,44,40
buffer_load_dwordx4 v[192:195], v88, s[36:39], s49 offen offset:1024 sc1
mav 16,48,44
R 56,38912
mav 20,52,44
s_waitcnt lgkmcnt(1)
ds_read_b128 v[64:67], v96 offset:0
mav 24,56,44
ds_read_b128 v[68:71], v96 offset:1024
mav 28,60,44
mav 16,64,48
R 60,39936
buffer_load_dwordx4 v[196:199], v89, s[36:39], s49 offen sc1
mav 20,68,48
mav 24,72,48
mav 28,76,48
mav 16,80,52
R 32,40960
buffer_load_dwordx4 v[200:203], v89, s[36:39], s49 offen offset:1024 sc1
mav 20,84,52
mav 24,88,52
mav 28,92,52
s_add_u32 m0, s50, 32768
s_waitcnt lgkmcnt(1)
mav 16,96,56
R 36,41984
buffer_store_dwordx4 v[64:67], v92, s[44:47], s54 offen
mav 20,100,56
mav 24,104,56
mav 28,108,56
mav 16,112,60
R 40,43008
buffer_store_dwordx4 v[68:71], v93, s[44:47], s54 offen
mav 20,116,60
mav 24,120,60
mav 28,124,60
s_waitcnt lgkmcnt(1)
mav 16,128,32
R 44,44032
D 83,0
mav 20,132,32
mav 24,136,32
mav 28,140,32
mav 16,144,36
R 48,45056
D 83,1024
mav 20,148,36
mav 24,152,36
mav 28,156,36
s_waitcnt lgkmcnt(1)
mav 16,160,40
R 52,46080
D 83,2048
mav 20,164,40
s_sub_u32 s54, s54, 0x8000
mav 24,168,40
mav 28,172,40
mav 16,176,44
R 56,47104
D 83,3072
mav 20,180,44
mav 24,184,44
mav 28,188,44
s_waitcnt lgkmcnt(1)
mav 16,192,48
R 60,48128
buffer_load_dwordx4 v[124:127], v90, s[36:39], s49 offen sc1
mav 20,196,48
mav 24,200,48
mav 28,204,48
mav 16,208,52
buffer_load_dwordx4 v[128:131], v90, s[36:39], s49 offen offset:1024 sc1
mav 20,212,52
mav 24,216,52
mav 28,220,52
s_waitcnt lgkmcnt(0)
mav 16,224,56
mav 20,228,56
mav 24,232,56
mav 28,236,56
mav 16,240,60
mav 20,244,60
mav 24,248,60
mav 28,252,60
s_waitcnt vmcnt(17)
mvv 16,116,164
mvv 20,120,164
s_waitcnt vmcnt(16)
mvv 24,116,168
mvv 28,120,168
s_waitcnt vmcnt(15)
mvv 16,116,172
mvv 20,120,172
v_xor_b32 v80, 0x10000, v80
s_waitcnt vmcnt(14)
mvv 24,116,176
s_add_u32 s49, s49, 346112
mvv 28,120,176
s_barrier
s_waitcnt vmcnt(13)
mvv 16,116,180
R 32,0
mvv 20,120,180
buffer_load_dwordx4 v[132:135], v86, s[36:39], s49 offen sc1
s_waitcnt vmcnt(13)
mvv 24,116,184
mvv 28,120,184
s_waitcnt vmcnt(12)
mvv 16,116,188
R 36,1024
mvv 20,120,188
buffer_load_dwordx4 v[136:139], v86, s[36:39], s49 offen offset:1024 sc1
s_waitcnt vmcnt(12)
mvv 24,116,192
mvv 28,120,192
s_waitcnt vmcnt(11)
mvv 16,116,196
R 40,2048
mvv 20,120,196
s_waitcnt vmcnt(10)
mvv 24,116,200
buffer_load_dwordx4 v[140:143], v87, s[36:39], s49 offen sc1
mvv 28,120,200
s_waitcnt lgkmcnt(1)
v_mfma_f32_16x16x32_bf16 v[0:3], a[0:3], v[32:35], v[100:103]
R 44,3072
v_cvt_pk_bf16_f32 v64, v16, v17
v_mfma_f32_16x16x32_bf16 v[4:7], a[4:7], v[32:35], v[104:107]
v_cvt_pk_bf16_f32 v65, v18, v19
v_cvt_pk_bf16_f32 v66, v20, v21
v_mfma_f32_16x16x32_bf16 v[8:11], a[8:11], v[32:35], v[108:111]
buffer_load_dwordx4 v[144:147], v87, s[36:39], s49 offen offset:1024 sc1
v_cvt_pk_bf16_f32 v67, v22, v23
v_mfma_f32_16x16x32_bf16 v[12:15], a[12:15], v[32:35], v[112:115]
ds_write_b128 v94, v[64:67] offset:0
mav 0,16,36
R 48,4096
v_cvt_pk_bf16_f32 v68, v24, v25
mav 4,20,36
v_cvt_pk_bf16_f32 v69, v26, v27
v_cvt_pk_bf16_f32 v70, v28, v29
mav 8,24,36
v_cvt_pk_bf16_f32 v71, v30, v31
mav 12,28,36
buffer_load_dwordx4 v[148:151], v88, s[36:39], s49 offen sc1
s_waitcnt lgkmcnt(2)
mav 0,32,40
R 52,5120
mav 4,36,40
ds_write_b128 v95, v[68:71] offset:0
mav 8,40,40
mav 12,44,40
buffer_load_dwordx4 v[152:155], v88, s[36:39], s49 offen offset:1024 sc1
mav 0,48,44
R 56,6144
mav 4,52,44
s_waitcnt lgkmcnt(1)
ds_read_b128 v[64:67], v96 offset:0
mav 8,56,44
ds_read_b128 v[68:71], v96 offset:1024
mav 12,60,44
mav 0,64,48
R 60,7168
buffer_load_dwordx4 v[156:159], v89, s[36:39], s49 offen sc1
mav 4,68,48
mav 8,72,48
mav 12,76,48
mav 0,80,52
R 32,8192
buffer_load_dwordx4 v[160:163], v89, s[36:39], s49 offen offset:1024 sc1
mav 4,84,52
mav 8,88,52
mav 12,92,52
s_waitcnt lgkmcnt(1)
mav 0,96,56
R 36,9216
buffer_store_dwordx4 v[64:67], v92, s[44:47], s54 offen
mav 4,100,56
mav 8,104,56
mav 12,108,56
mav 0,112,60
R 40,10240
buffer_store_dwordx4 v[68:71], v93, s[44:47], s54 offen
mav 4,116,60
mav 8,120,60
mav 12,124,60
s_waitcnt lgkmcnt(1)
mav 0,128,32
R 44,11264
mav 4,132,32
mav 8,136,32
mav 12,140,32
mav 0,144,36
R 48,12288
mav 4,148,36
mav 8,152,36
mav 12,156,36
s_waitcnt lgkmcnt(1)
mav 0,160,40
R 52,13312
s_sub_u32 s54, s54, 0x8000
mav 4,164,40
mav 8,168,40
mav 12,172,40
buffer_load_dwordx4 v[164:167], v90, s[36:39], s49 offen sc1
mav 0,176,44
R 56,14336
mav 4,180,44
mav 8,184,44
mav 12,188,44
buffer_load_dwordx4 v[168:171], v90, s[36:39], s49 offen offset:1024 sc1
s_waitcnt lgkmcnt(1)
mav 0,192,48
R 60,15360
mav 4,196,48
mav 8,200,48
mav 12,204,48
mav 0,208,52
mav 4,212,52
mav 8,216,52
mav 12,220,52
s_waitcnt lgkmcnt(0)
mav 0,224,56
mav 4,228,56
mav 8,232,56
mav 12,236,56
mav 0,240,60
mav 4,244,60
mav 8,248,60
mav 12,252,60
s_waitcnt vmcnt(13)
mvv 0,116,124
mvv 4,120,124
s_waitcnt vmcnt(12)
mvv 8,116,128
mvv 12,120,128
s_waitcnt vmcnt(11)
mvv 0,116,132
mvv 4,120,132
s_waitcnt vmcnt(10)
mvv 8,116,136
s_add_u32 s49, s49, 346112
mvv 12,120,136
s_barrier
s_waitcnt vmcnt(9)
mvv 0,116,140
R 32,32768
mvv 4,120,140
buffer_load_dwordx4 v[172:175], v86, s[36:39], s49 offen sc1
s_waitcnt vmcnt(9)
mvv 8,116,144
mvv 12,120,144
s_waitcnt vmcnt(8)
mvv 0,116,148
R 36,33792
mvv 4,120,148
buffer_load_dwordx4 v[176:179], v86, s[36:39], s49 offen offset:1024 sc1
s_waitcnt vmcnt(8)
mvv 8,116,152
mvv 12,120,152
s_waitcnt vmcnt(7)
mvv 0,116,156
R 40,34816
mvv 4,120,156
s_waitcnt vmcnt(6)
mvv 8,116,160
buffer_load_dwordx4 v[180:183], v87, s[36:39], s49 offen sc1
mvv 12,120,160
s_waitcnt lgkmcnt(1)
v_mfma_f32_16x16x32_bf16 v[16:19], a[0:3], v[32:35], v[100:103]
R 44,35840
v_cvt_pk_bf16_f32 v64, v0, v1
v_mfma_f32_16x16x32_bf16 v[20:23], a[4:7], v[32:35], v[104:107]
v_cvt_pk_bf16_f32 v65, v2, v3
v_cvt_pk_bf16_f32 v66, v4, v5
v_mfma_f32_16x16x32_bf16 v[24:27], a[8:11], v[32:35], v[108:111]
buffer_load_dwordx4 v[184:187], v87, s[36:39], s49 offen offset:1024 sc1
v_cvt_pk_bf16_f32 v67, v6, v7
v_mfma_f32_16x16x32_bf16 v[28:31], a[12:15], v[32:35], v[112:115]
ds_write_b128 v94, v[64:67] offset:0
mav 16,16,36
R 48,36864
v_cvt_pk_bf16_f32 v68, v8, v9
mav 20,20,36
v_cvt_pk_bf16_f32 v69, v10, v11
v_cvt_pk_bf16_f32 v70, v12, v13
mav 24,24,36
v_cvt_pk_bf16_f32 v71, v14, v15
mav 28,28,36
buffer_load_dwordx4 v[188:191], v88, s[36:39], s49 offen sc1
s_waitcnt lgkmcnt(2)
mav 16,32,40
R 52,37888
mav 20,36,40
ds_write_b128 v95, v[68:71] offset:0
mav 24,40,40
mav 28,44,40
buffer_load_dwordx4 v[192:195], v88, s[36:39], s49 offen offset:1024 sc1
mav 16,48,44
R 56,38912
mav 20,52,44
s_waitcnt lgkmcnt(1)
ds_read_b128 v[64:67], v96 offset:0
mav 24,56,44
ds_read_b128 v[68:71], v96 offset:1024
mav 28,60,44
mav 16,64,48
R 60,39936
buffer_load_dwordx4 v[196:199], v89, s[36:39], s49 offen sc1
mav 20,68,48
mav 24,72,48
mav 28,76,48
mav 16,80,52
R 32,40960
buffer_load_dwordx4 v[200:203], v89, s[36:39], s49 offen offset:1024 sc1
mav 20,84,52
mav 24,88,52
mav 28,92,52
s_waitcnt lgkmcnt(1)
mav 16,96,56
R 36,41984
buffer_store_dwordx4 v[64:67], v92, s[44:47], s54 offen
mav 20,100,56
mav 24,104,56
mav 28,108,56
mav 16,112,60
R 40,43008
buffer_store_dwordx4 v[68:71], v93, s[44:47], s54 offen
mav 20,116,60
mav 24,120,60
mav 28,124,60
s_waitcnt lgkmcnt(1)
mav 16,128,32
R 44,44032
mav 20,132,32
mav 24,136,32
mav 28,140,32
mav 16,144,36
R 48,45056
mav 20,148,36
mav 24,152,36
mav 28,156,36
s_waitcnt lgkmcnt(1)
mav 16,160,40
R 52,46080
s_sub_u32 s54, s54, 0x8000
mav 20,164,40
mav 24,168,40
mav 28,172,40
mav 16,176,44
R 56,47104
mav 20,180,44
mav 24,184,44
mav 28,188,44
s_waitcnt lgkmcnt(1)
mav 16,192,48
R 60,48128
mav 20,196,48
mav 24,200,48
mav 28,204,48
mav 16,208,52
mav 20,212,52
mav 24,216,52
mav 28,220,52
s_waitcnt lgkmcnt(0)
mav 16,224,56
mav 20,228,56
mav 24,232,56
mav 28,236,56
mav 16,240,60
mav 20,244,60
mav 24,248,60
mav 28,252,60
s_waitcnt vmcnt(11)
mvv 16,116,164
mvv 20,120,164
s_waitcnt vmcnt(10)
mvv 24,116,168
mvv 28,120,168
s_waitcnt vmcnt(9)
mvv 16,116,172
mvv 20,120,172
s_waitcnt vmcnt(8)
mvv 24,116,176
mvv 28,120,176
s_waitcnt vmcnt(7)
mvv 16,116,180
mvv 20,120,180
s_waitcnt vmcnt(6)
mvv 24,116,184
mvv 28,120,184
s_waitcnt vmcnt(5)
mvv 16,116,188
mvv 20,120,188
s_waitcnt vmcnt(4)
mvv 24,116,192
mvv 28,120,192
s_waitcnt vmcnt(3)
mvv 16,116,196
mvv 20,120,196
s_waitcnt vmcnt(2)
mvv 24,116,200
mvv 28,120,200
s_nop 3
v_cvt_pk_bf16_f32 v64, v16, v17
v_cvt_pk_bf16_f32 v65, v18, v19
v_cvt_pk_bf16_f32 v66, v20, v21
v_cvt_pk_bf16_f32 v67, v22, v23
ds_write_b128 v94, v[64:67] offset:0
v_cvt_pk_bf16_f32 v68, v24, v25
v_cvt_pk_bf16_f32 v69, v26, v27
v_cvt_pk_bf16_f32 v70, v28, v29
v_cvt_pk_bf16_f32 v71, v30, v31
ds_write_b128 v95, v[68:71] offset:0
s_waitcnt lgkmcnt(0)
ds_read_b128 v[64:67], v96 offset:0
ds_read_b128 v[68:71], v96 offset:1024
s_waitcnt lgkmcnt(0)
buffer_store_dwordx4 v[64:67], v92, s[44:47], s54 offen
buffer_store_dwordx4 v[68:71], v93, s[44:47], s54 offen
s_endpgm
LvR2:
s_add_u32 s49, s49, 346112
s_barrier
s_add_u32 m0, s50, 0
s_nop 0
D 83,0
D 83,1024
D 83,2048
D 83,3072
s_add_u32 s49, s49, 346112
s_barrier
s_add_u32 m0, s50, 32768
s_nop 0
D 83,0
D 83,1024
D 83,2048
D 83,3072
buffer_load_dwordx4 v[124:127], v90, s[36:39], s49 offen sc1
buffer_load_dwordx4 v[128:131], v90, s[36:39], s49 offen offset:1024 sc1
s_xor_b32 s50, s50, 0x10000
s_add_u32 s49, s49, 346112
s_waitcnt vmcnt(6)
s_barrier
R 32,0
buffer_load_dwordx4 v[132:135], v86, s[36:39], s49 offen sc1
R 36,1024
buffer_load_dwordx4 v[136:139], v86, s[36:39], s49 offen offset:1024 sc1
R 40,2048
buffer_load_dwordx4 v[140:143], v87, s[36:39], s49 offen sc1
s_waitcnt lgkmcnt(1)
v_mfma_f32_16x16x32_bf16 v[0:3], a[0:3], v[32:35], v[100:103]
R 44,3072
v_mfma_f32_16x16x32_bf16 v[4:7], a[4:7], v[32:35], v[104:107]
v_mfma_f32_16x16x32_bf16 v[8:11], a[8:11], v[32:35], v[108:111]
buffer_load_dwordx4 v[144:147], v87, s[36:39], s49 offen offset:1024 sc1
v_mfma_f32_16x16x32_bf16 v[12:15], a[12:15], v[32:35], v[112:115]
mav 0,16,36
R 48,4096
mav 4,20,36
mav 8,24,36
mav 12,28,36
buffer_load_dwordx4 v[148:151], v88, s[36:39], s49 offen sc1
s_waitcnt lgkmcnt(1)
mav 0,32,40
R 52,5120
mav 4,36,40
mav 8,40,40
mav 12,44,40
buffer_load_dwordx4 v[152:155], v88, s[36:39], s49 offen offset:1024 sc1
mav 0,48,44
R 56,6144
mav 4,52,44
mav 8,56,44
mav 12,60,44
s_waitcnt lgkmcnt(1)
mav 0,64,48
R 60,7168
buffer_load_dwordx4 v[156:159], v89, s[36:39], s49 offen sc1
mav 4,68,48
mav 8,72,48
mav 12,76,48
mav 0,80,52
R 32,8192
buffer_load_dwordx4 v[160:163], v89, s[36:39], s49 offen offset:1024 sc1
mav 4,84,52
mav 8,88,52
mav 12,92,52
s_add_u32 m0, s50, 0
s_waitcnt lgkmcnt(1)
mav 0,96,56
R 36,9216
mav 4,100,56
D 83,0
mav 8,104,56
mav 12,108,56
mav 0,112,60
R 40,10240
mav 4,116,60
D 83,1024
mav 8,120,60
mav 12,124,60
s_waitcnt lgkmcnt(1)
mav 0,128,32
R 44,11264
mav 4,132,32
mav 8,136,32
D 83,2048
mav 12,140,32
mav 0,144,36
R 48,12288
mav 4,148,36
mav 8,152,36
D 83,3072
mav 12,156,36
s_waitcnt lgkmcnt(1)
mav 0,160,40
R 52,13312
mav 4,164,40
mav 8,168,40
mav 12,172,40
buffer_load_dwordx4 v[164:167], v90, s[36:39], s49 offen sc1
mav 0,176,44
R 56,14336
mav 4,180,44
mav 8,184,44
mav 12,188,44
buffer_load_dwordx4 v[168:171], v90, s[36:39], s49 offen offset:1024 sc1
s_waitcnt lgkmcnt(1)
mav 0,192,48
R 60,15360
mav 4,196,48
mav 8,200,48
mav 12,204,48
mav 0,208,52
mav 4,212,52
mav 8,216,52
mav 12,220,52
s_waitcnt lgkmcnt(0)
mav 0,224,56
mav 4,228,56
mav 8,232,56
mav 12,236,56
mav 0,240,60
mav 4,244,60
mav 8,248,60
mav 12,252,60
s_waitcnt vmcnt(15)
mvv 0,116,124
mvv 4,120,124
s_waitcnt vmcnt(14)
mvv 8,116,128
mvv 12,120,128
s_waitcnt vmcnt(13)
mvv 0,116,132
mvv 4,120,132
s_waitcnt vmcnt(12)
mvv 8,116,136
s_add_u32 s49, s49, 346112
mvv 12,120,136
s_barrier
s_waitcnt vmcnt(11)
mvv 0,116,140
R 32,32768
mvv 4,120,140
buffer_load_dwordx4 v[172:175], v86, s[36:39], s49 offen sc1
s_waitcnt vmcnt(11)
mvv 8,116,144
mvv 12,120,144
s_waitcnt vmcnt(10)
mvv 0,116,148
R 36,33792
mvv 4,120,148
buffer_load_dwordx4 v[176:179], v86, s[36:39], s49 offen offset:1024 sc1
s_waitcnt vmcnt(10)
mvv 8,116,152
mvv 12,120,152
s_waitcnt vmcnt(9)
mvv 0,116,156
R 40,34816
mvv 4,120,156
s_waitcnt vmcnt(8)
mvv 8,116,160
buffer_load_dwordx4 v[180:183], v87, s[36:39], s49 offen sc1
mvv 12,120,160
s_waitcnt lgkmcnt(1)
v_mfma_f32_16x16x32_bf16 v[16:19], a[0:3], v[32:35], v[100:103]
R 44,35840
v_cvt_pk_bf16_f32 v64, v0, v1
v_mfma_f32_16x16x32_bf16 v[20:23], a[4:7], v[32:35], v[104:107]
v_cvt_pk_bf16_f32 v65, v2, v3
v_cvt_pk_bf16_f32 v66, v4, v5
v_mfma_f32_16x16x32_bf16 v[24:27], a[8:11], v[32:35], v[108:111]
buffer_load_dwordx4 v[184:187], v87, s[36:39], s49 offen offset:1024 sc1
v_cvt_pk_bf16_f32 v67, v6, v7
v_mfma_f32_16x16x32_bf16 v[28:31], a[12:15], v[32:35], v[112:115]
v_cvt_pk_bf16_f32 v68, v8, v9
v_cvt_pk_bf16_f32 v69, v10, v11
mav 16,16,36
R 48,36864
v_cvt_pk_bf16_f32 v70, v12, v13
mav 20,20,36
v_cvt_pk_bf16_f32 v71, v14, v15
v_mul_f32 v75, v0, v0
mav 24,24,36
buffer_store_dwordx4 v[64:67], v91, s[36:39], s49 offen sc1
v_add_f32 v74, v1, v0
mav 28,28,36
v_fmac_f32 v75, v1, v1
s_waitcnt lgkmcnt(1)
mav 16,32,40
R 52,37888
v_add_f32 v74, v2, v74
mav 20,36,40
v_fmac_f32 v75, v2, v2
v_add_f32 v74, v3, v74
mav 24,40,40
buffer_store_dwordx4 v[68:71], v91, s[36:39], s49 offen offset:1024 sc1
v_fmac_f32 v75, v3, v3
mav 28,44,40
v_add_f32 v74, v4, v74
v_fmac_f32 v75, v4, v4
mav 16,48,44
R 56,38912
v_add_f32 v74, v5, v74
mav 20,52,44
v_fmac_f32 v75, v5, v5
v_add_f32 v74, v6, v74
mav 24,56,44
buffer_load_dwordx4 v[188:191], v88, s[36:39], s49 offen sc1
v_fmac_f32 v75, v6, v6
mav 28,60,44
v_add_f32 v74, v7, v74
s_waitcnt lgkmcnt(1)
mav 16,64,48
R 60,39936
v_fmac_f32 v75, v7, v7
mav 20,68,48
v_add_f32 v74, v8, v74
v_fmac_f32 v75, v8, v8
mav 24,72,48
buffer_load_dwordx4 v[192:195], v88, s[36:39], s49 offen offset:1024 sc1
v_add_f32 v74, v9, v74
mav 28,76,48
v_fmac_f32 v75, v9, v9
v_add_f32 v74, v10, v74
mav 16,80,52
R 32,40960
v_fmac_f32 v75, v10, v10
mav 20,84,52
v_add_f32 v74, v11, v74
v_fmac_f32 v75, v11, v11
mav 24,88,52
buffer_load_dwordx4 v[196:199], v89, s[36:39], s49 offen sc1
v_add_f32 v74, v12, v74
mav 28,92,52
s_add_u32 m0, s50, 32768
s_waitcnt lgkmcnt(1)
mav 16,96,56
R 36,41984
v_fmac_f32 v75, v12, v12
mav 20,100,56
v_add_f32 v74, v13, v74
v_fmac_f32 v75, v13, v13
mav 24,104,56
buffer_load_dwordx4 v[200:203], v89, s[36:39], s49 offen offset:1024 sc1
v_add_f32 v74, v14, v74
mav 28,108,56
v_fmac_f32 v75, v14, v14
v_add_f32 v74, v15, v74
mav 16,112,60
R 40,43008
v_fmac_f32 v75, v15, v15
mav 20,116,60
mav 24,120,60
D 83,0
v_permlane32_swap_b32 v74, v75
mav 28,124,60
v_add_f32 v74, v74, v75
s_waitcnt lgkmcnt(1)
mav 16,128,32
R 44,44032
mav 20,132,32
mav 24,136,32
D 83,1024
mav 28,140,32
mav 16,144,36
R 48,45056
mav 20,148,36
mav 24,152,36
D 83,2048
mav 28,156,36
s_waitcnt lgkmcnt(1)
mav 16,160,40
R 52,46080
mav 20,164,40
mav 24,168,40
D 83,3072
mav 28,172,40
mav 16,176,44
R 56,47104
mav 20,180,44
mav 24,184,44
buffer_load_dwordx4 v[124:127], v90, s[36:39], s49 offen sc1
mav 28,188,44
s_waitcnt lgkmcnt(1)
mav 16,192,48
R 60,48128
mav 20,196,48
mav 24,200,48
buffer_load_dwordx4 v[128:131], v90, s[36:39], s49 offen offset:1024 sc1
mav 28,204,48
mav 16,208,52
mav 20,212,52
mav 24,216,52
v_add_u32 v72, 1, v72
mav 28,220,52
s_waitcnt lgkmcnt(0)
mav 16,224,56
mav 20,228,56
mav 24,232,56
mav 28,236,56
mav 16,240,60
s_waitcnt vmcnt(10)
buffer_store_dwordx2 v[72:73], v81, s[36:39], s52 offen sc1
mav 20,244,60
mav 24,248,60
ds_write_b32 v97, v74 offset:0
mav 28,252,60
mvv 16,116,164
mvv 20,120,164
mvv 24,116,168
s_xor_b32 s50, s50, 0x10000
mvv 28,120,168
mvv 16,116,172
mvv 20,120,172
v_xor_b32 v80, 0x10000, v80
mvv 24,116,176
s_add_u32 s49, s49, 346112
mvv 28,120,176
s_waitcnt lgkmcnt(0)
s_barrier
mvv 16,116,180
R 32,0
mvv 20,120,180
buffer_load_dwordx4 v[132:135], v86, s[36:39], s49 offen sc1
mvv 24,116,184
mvv 28,120,184
s_waitcnt vmcnt(11)
mvv 16,116,188
R 36,1024
mvv 20,120,188
buffer_load_dwordx4 v[136:139], v86, s[36:39], s49 offen offset:1024 sc1
s_waitcnt vmcnt(11)
mvv 24,116,192
mvv 28,120,192
s_waitcnt vmcnt(10)
mvv 16,116,196
R 40,2048
mvv 20,120,196
s_waitcnt vmcnt(9)
mvv 24,116,200
buffer_load_dwordx4 v[140:143], v87, s[36:39], s49 offen sc1
mvv 28,120,200
s_mov_b32 s60, -15
.p2align 6
LR2_1:
s_waitcnt lgkmcnt(1)
v_mfma_f32_16x16x32_bf16 v[0:3], a[0:3], v[32:35], v[100:103]
R 44,3072
v_cvt_pk_bf16_f32 v64, v16, v17
v_mfma_f32_16x16x32_bf16 v[4:7], a[4:7], v[32:35], v[104:107]
v_cvt_pk_bf16_f32 v65, v18, v19
v_cvt_pk_bf16_f32 v66, v20, v21
v_mfma_f32_16x16x32_bf16 v[8:11], a[8:11], v[32:35], v[108:111]
buffer_load_dwordx4 v[144:147], v87, s[36:39], s49 offen offset:1024 sc1
v_cvt_pk_bf16_f32 v67, v22, v23
v_mfma_f32_16x16x32_bf16 v[12:15], a[12:15], v[32:35], v[112:115]
v_cvt_pk_bf16_f32 v68, v24, v25
v_cvt_pk_bf16_f32 v69, v26, v27
mav 0,16,36
R 48,4096
v_cvt_pk_bf16_f32 v70, v28, v29
mav 4,20,36
v_cvt_pk_bf16_f32 v71, v30, v31
v_mul_f32 v75, v16, v16
mav 8,24,36
buffer_store_dwordx4 v[64:67], v91, s[36:39], s49 offen sc1
v_add_f32 v74, v17, v16
mav 12,28,36
v_fmac_f32 v75, v17, v17
s_waitcnt lgkmcnt(1)
mav 0,32,40
R 52,5120
v_add_f32 v74, v18, v74
mav 4,36,40
v_fmac_f32 v75, v18, v18
v_add_f32 v74, v19, v74
mav 8,40,40
buffer_store_dwordx4 v[68:71], v91, s[36:39], s49 offen offset:1024 sc1
v_fmac_f32 v75, v19, v19
mav 12,44,40
v_add_f32 v74, v20, v74
v_fmac_f32 v75, v20, v20
mav 0,48,44
R 56,6144
v_add_f32 v74, v21, v74
mav 4,52,44
v_fmac_f32 v75, v21, v21
v_add_f32 v74, v22, v74
mav 8,56,44
buffer_load_dwordx4 v[148:151], v88, s[36:39], s49 offen sc1
v_fmac_f32 v75, v22, v22
mav 12,60,44
v_add_f32 v74, v23, v74
s_waitcnt lgkmcnt(1)
mav 0,64,48
R 60,7168
v_fmac_f32 v75, v23, v23
mav 4,68,48
v_add_f32 v74, v24, v74
v_fmac_f32 v75, v24, v24
mav 8,72,48
buffer_load_dwordx4 v[152:155], v88, s[36:39], s49 offen offset:1024 sc1
v_add_f32 v74, v25, v74
mav 12,76,48
v_fmac_f32 v75, v25, v25
v_add_f32 v74, v26, v74
mav 0,80,52
R 32,8192
v_fmac_f32 v75, v26, v26
mav 4,84,52
v_add_f32 v74, v27, v74
v_fmac_f32 v75, v27, v27
mav 8,88,52
buffer_load_dwordx4 v[156:159], v89, s[36:39], s49 offen sc1
v_add_f32 v74, v28, v74
mav 12,92,52
s_add_u32 m0, s50, 0
s_waitcnt lgkmcnt(1)
mav 0,96,56
R 36,9216
v_fmac_f32 v75, v28, v28
mav 4,100,56
v_add_f32 v74, v29, v74
v_fmac_f32 v75, v29, v29
mav 8,104,56
buffer_load_dwordx4 v[160:163], v89, s[36:39], s49 offen offset:1024 sc1
v_add_f32 v74, v30, v74
mav 12,108,56
v_fmac_f32 v75, v30, v30
v_add_f32 v74, v31, v74
mav 0,112,60
R 40,10240
v_fmac_f32 v75, v31, v31
mav 4,116,60
mav 8,120,60
D 83,0
v_permlane32_swap_b32 v74, v75
mav 12,124,60
v_add_f32 v74, v74, v75
s_waitcnt lgkmcnt(1)
mav 0,128,32
R 44,11264
mav 4,132,32
mav 8,136,32
D 83,1024
mav 12,140,32
mav 0,144,36
R 48,12288
mav 4,148,36
mav 8,152,36
D 83,2048
mav 12,156,36
s_waitcnt lgkmcnt(1)
mav 0,160,40
R 52,13312
mav 4,164,40
mav 8,168,40
D 83,3072
mav 12,172,40
mav 0,176,44
R 56,14336
mav 4,180,44
mav 8,184,44
buffer_load_dwordx4 v[164:167], v90, s[36:39], s49 offen sc1
mav 12,188,44
s_waitcnt lgkmcnt(1)
mav 0,192,48
R 60,15360
mav 4,196,48
mav 8,200,48
buffer_load_dwordx4 v[168:171], v90, s[36:39], s49 offen offset:1024 sc1
mav 12,204,48
mav 0,208,52
mav 4,212,52
mav 8,216,52
v_add_u32 v72, 1, v72
mav 12,220,52
s_waitcnt lgkmcnt(0)
mav 0,224,56
mav 4,228,56
mav 8,232,56
mav 12,236,56
mav 0,240,60
s_waitcnt vmcnt(10)
buffer_store_dwordx2 v[72:73], v81, s[36:39], s52 offen sc1
mav 4,244,60
mav 8,248,60
ds_write_b32 v97, v74 offset:1024
mav 12,252,60
mvv 0,116,124
mvv 4,120,124
mvv 8,116,128
mvv 12,120,128
mvv 0,116,132
mvv 4,120,132
mvv 8,116,136
s_add_u32 s49, s49, 346112
mvv 12,120,136
s_waitcnt lgkmcnt(0)
s_barrier
mvv 0,116,140
R 32,32768
mvv 4,120,140
buffer_load_dwordx4 v[172:175], v86, s[36:39], s49 offen sc1
mvv 8,116,144
mvv 12,120,144
s_waitcnt vmcnt(11)
mvv 0,116,148
R 36,33792
mvv 4,120,148
buffer_load_dwordx4 v[176:179], v86, s[36:39], s49 offen offset:1024 sc1
s_waitcnt vmcnt(11)
mvv 8,116,152
mvv 12,120,152
s_waitcnt vmcnt(10)
mvv 0,116,156
R 40,34816
mvv 4,120,156
s_waitcnt vmcnt(9)
mvv 8,116,160
buffer_load_dwordx4 v[180:183], v87, s[36:39], s49 offen sc1
mvv 12,120,160
s_waitcnt lgkmcnt(1)
v_mfma_f32_16x16x32_bf16 v[16:19], a[0:3], v[32:35], v[100:103]
R 44,35840
v_cvt_pk_bf16_f32 v64, v0, v1
v_mfma_f32_16x16x32_bf16 v[20:23], a[4:7], v[32:35], v[104:107]
v_cvt_pk_bf16_f32 v65, v2, v3
v_cvt_pk_bf16_f32 v66, v4, v5
v_mfma_f32_16x16x32_bf16 v[24:27], a[8:11], v[32:35], v[108:111]
buffer_load_dwordx4 v[184:187], v87, s[36:39], s49 offen offset:1024 sc1
v_cvt_pk_bf16_f32 v67, v6, v7
v_mfma_f32_16x16x32_bf16 v[28:31], a[12:15], v[32:35], v[112:115]
v_cvt_pk_bf16_f32 v68, v8, v9
v_cvt_pk_bf16_f32 v69, v10, v11
mav 16,16,36
R 48,36864
v_cvt_pk_bf16_f32 v70, v12, v13
mav 20,20,36
v_cvt_pk_bf16_f32 v71, v14, v15
v_mul_f32 v75, v0, v0
mav 24,24,36
buffer_store_dwordx4 v[64:67], v91, s[36:39], s49 offen sc1
v_add_f32 v74, v1, v0
mav 28,28,36
v_fmac_f32 v75, v1, v1
s_waitcnt lgkmcnt(1)
mav 16,32,40
R 52,37888
v_add_f32 v74, v2, v74
mav 20,36,40
v_fmac_f32 v75, v2, v2
v_add_f32 v74, v3, v74
mav 24,40,40
buffer_store_dwordx4 v[68:71], v91, s[36:39], s49 offen offset:1024 sc1
v_fmac_f32 v75, v3, v3
mav 28,44,40
v_add_f32 v74, v4, v74
v_fmac_f32 v75, v4, v4
mav 16,48,44
R 56,38912
v_add_f32 v74, v5, v74
mav 20,52,44
v_fmac_f32 v75, v5, v5
v_add_f32 v74, v6, v74
mav 24,56,44
buffer_load_dwordx4 v[188:191], v88, s[36:39], s49 offen sc1
v_fmac_f32 v75, v6, v6
mav 28,60,44
v_add_f32 v74, v7, v74
s_waitcnt lgkmcnt(1)
mav 16,64,48
R 60,39936
v_fmac_f32 v75, v7, v7
mav 20,68,48
v_add_f32 v74, v8, v74
v_fmac_f32 v75, v8, v8
mav 24,72,48
buffer_load_dwordx4 v[192:195], v88, s[36:39], s49 offen offset:1024 sc1
v_add_f32 v74, v9, v74
mav 28,76,48
v_fmac_f32 v75, v9, v9
v_add_f32 v74, v10, v74
mav 16,80,52
R 32,40960
v_fmac_f32 v75, v10, v10
mav 20,84,52
v_add_f32 v74, v11, v74
v_fmac_f32 v75, v11, v11
mav 24,88,52
buffer_load_dwordx4 v[196:199], v89, s[36:39], s49 offen sc1
v_add_f32 v74, v12, v74
mav 28,92,52
s_add_u32 m0, s50, 32768
s_waitcnt lgkmcnt(1)
mav 16,96,56
R 36,41984
v_fmac_f32 v75, v12, v12
mav 20,100,56
v_add_f32 v74, v13, v74
v_fmac_f32 v75, v13, v13
mav 24,104,56
buffer_load_dwordx4 v[200:203], v89, s[36:39], s49 offen offset:1024 sc1
v_add_f32 v74, v14, v74
mav 28,108,56
v_fmac_f32 v75, v14, v14
v_add_f32 v74, v15, v74
mav 16,112,60
R 40,43008
v_fmac_f32 v75, v15, v15
mav 20,116,60
mav 24,120,60
D 83,0
v_permlane32_swap_b32 v74, v75
mav 28,124,60
v_add_f32 v74, v74, v75
s_waitcnt lgkmcnt(1)
mav 16,128,32
R 44,44032
mav 20,132,32
mav 24,136,32
D 83,1024
mav 28,140,32
mav 16,144,36
R 48,45056
mav 20,148,36
mav 24,152,36
D 83,2048
mav 28,156,36
s_waitcnt lgkmcnt(1)
mav 16,160,40
R 52,46080
mav 20,164,40
mav 24,168,40
D 83,3072
mav 28,172,40
mav 16,176,44
R 56,47104
mav 20,180,44
mav 24,184,44
buffer_load_dwordx4 v[124:127], v90, s[36:39], s49 offen sc1
mav 28,188,44
s_waitcnt lgkmcnt(1)
mav 16,192,48
R 60,48128
mav 20,196,48
mav 24,200,48
buffer_load_dwordx4 v[128:131], v90, s[36:39], s49 offen offset:1024 sc1
mav 28,204,48
mav 16,208,52
mav 20,212,52
mav 24,216,52
v_add_u32 v72, 1, v72
mav 28,220,52
s_waitcnt lgkmcnt(0)
mav 16,224,56
mav 20,228,56
mav 24,232,56
mav 28,236,56
mav 16,240,60
s_waitcnt vmcnt(10)
buffer_store_dwordx2 v[72:73], v81, s[36:39], s52 offen sc1
mav 20,244,60
mav 24,248,60
ds_write_b32 v97, v74 offset:0
mav 28,252,60
mvv 16,116,164
mvv 20,120,164
mvv 24,116,168
s_xor_b32 s50, s50, 0x10000
mvv 28,120,168
mvv 16,116,172
mvv 20,120,172
v_xor_b32 v80, 0x10000, v80
mvv 24,116,176
s_add_u32 s49, s49, 346112
mvv 28,120,176
s_waitcnt lgkmcnt(0)
s_barrier
mvv 16,116,180
R 32,0
mvv 20,120,180
buffer_load_dwordx4 v[132:135], v86, s[36:39], s49 offen sc1
mvv 24,116,184
mvv 28,120,184
s_waitcnt vmcnt(11)
mvv 16,116,188
R 36,1024
mvv 20,120,188
buffer_load_dwordx4 v[136:139], v86, s[36:39], s49 offen offset:1024 sc1
s_waitcnt vmcnt(11)
mvv 24,116,192
mvv 28,120,192
s_waitcnt vmcnt(10)
mvv 16,116,196
R 40,2048
mvv 20,120,196
s_waitcnt vmcnt(9)
mvv 24,116,200
buffer_load_dwordx4 v[140:143], v87, s[36:39], s49 offen sc1
mvv 28,120,200
s_add_u32 s60, s60, 1
s_cbranch_scc0 LR2_1
s_waitcnt lgkmcnt(1)
v_mfma_f32_16x16x32_bf16 v[0:3], a[0:3], v[32:35], v[100:103]
R 44,3072
v_cvt_pk_bf16_f32 v64, v16, v17
v_mfma_f32_16x16x32_bf16 v[4:7], a[4:7], v[32:35], v[104:107]
v_cvt_pk_bf16_f32 v65, v18, v19
v_cvt_pk_bf16_f32 v66, v20, v21
v_mfma_f32_16x16x32_bf16 v[8:11], a[8:11], v[32:35], v[108:111]
buffer_load_dwordx4 v[144:147], v87, s[36:39], s49 offen offset:1024 sc1
v_cvt_pk_bf16_f32 v67, v22, v23
v_mfma_f32_16x16x32_bf16 v[12:15], a[12:15], v[32:35], v[112:115]
v_cvt_pk_bf16_f32 v68, v24, v25
v_cvt_pk_bf16_f32 v69, v26, v27
mav 0,16,36
R 48,4096
v_cvt_pk_bf16_f32 v70, v28, v29
mav 4,20,36
v_cvt_pk_bf16_f32 v71, v30, v31
v_mul_f32 v75, v16, v16
mav 8,24,36
buffer_store_dwordx4 v[64:67], v91, s[36:39], s49 offen sc1
v_add_f32 v74, v17, v16
mav 12,28,36
v_fmac_f32 v75, v17, v17
s_waitcnt lgkmcnt(1)
mav 0,32,40
R 52,5120
v_add_f32 v74, v18, v74
mav 4,36,40
v_fmac_f32 v75, v18, v18
v_add_f32 v74, v19, v74
mav 8,40,40
buffer_store_dwordx4 v[68:71], v91, s[36:39], s49 offen offset:1024 sc1
v_fmac_f32 v75, v19, v19
mav 12,44,40
v_add_f32 v74, v20, v74
v_fmac_f32 v75, v20, v20
mav 0,48,44
R 56,6144
v_add_f32 v74, v21, v74
mav 4,52,44
v_fmac_f32 v75, v21, v21
v_add_f32 v74, v22, v74
mav 8,56,44
buffer_load_dwordx4 v[148:151], v88, s[36:39], s49 offen sc1
v_fmac_f32 v75, v22, v22
mav 12,60,44
v_add_f32 v74, v23, v74
s_waitcnt lgkmcnt(1)
mav 0,64,48
R 60,7168
v_fmac_f32 v75, v23, v23
mav 4,68,48
v_add_f32 v74, v24, v74
v_fmac_f32 v75, v24, v24
mav 8,72,48
buffer_load_dwordx4 v[152:155], v88, s[36:39], s49 offen offset:1024 sc1
v_add_f32 v74, v25, v74
mav 12,76,48
v_fmac_f32 v75, v25, v25
v_add_f32 v74, v26, v74
mav 0,80,52
R 32,8192
v_fmac_f32 v75, v26, v26
mav 4,84,52
v_add_f32 v74, v27, v74
v_fmac_f32 v75, v27, v27
mav 8,88,52
buffer_load_dwordx4 v[156:159], v89, s[36:39], s49 offen sc1
v_add_f32 v74, v28, v74
mav 12,92,52
s_add_u32 m0, s50, 0
s_waitcnt lgkmcnt(1)
mav 0,96,56
R 36,9216
v_fmac_f32 v75, v28, v28
mav 4,100,56
v_add_f32 v74, v29, v74
v_fmac_f32 v75, v29, v29
mav 8,104,56
buffer_load_dwordx4 v[160:163], v89, s[36:39], s49 offen offset:1024 sc1
v_add_f32 v74, v30, v74
mav 12,108,56
v_fmac_f32 v75, v30, v30
v_add_f32 v74, v31, v74
mav 0,112,60
R 40,10240
v_fmac_f32 v75, v31, v31
mav 4,116,60
mav 8,120,60
D 83,0
v_permlane32_swap_b32 v74, v75
mav 12,124,60
v_add_f32 v74, v74, v75
s_waitcnt lgkmcnt(1)
mav 0,128,32
R 44,11264
mav 4,132,32
mav 8,136,32
D 83,1024
mav 12,140,32
mav 0,144,36
R 48,12288
mav 4,148,36
mav 8,152,36
D 83,2048
mav 12,156,36
s_waitcnt lgkmcnt(1)
mav 0,160,40
R 52,13312
mav 4,164,40
mav 8,168,40
D 83,3072
mav 12,172,40
mav 0,176,44
R 56,14336
mav 4,180,44
mav 8,184,44
buffer_load_dwordx4 v[164:167], v90, s[36:39], s49 offen sc1
mav 12,188,44
s_waitcnt lgkmcnt(1)
mav 0,192,48
R 60,15360
mav 4,196,48
mav 8,200,48
buffer_load_dwordx4 v[168:171], v90, s[36:39], s49 offen offset:1024 sc1
mav 12,204,48
mav 0,208,52
mav 4,212,52
mav 8,216,52
v_add_u32 v72, 69, v72
mav 12,220,52
s_waitcnt lgkmcnt(0)
mav 0,224,56
mav 4,228,56
mav 8,232,56
mav 12,236,56
mav 0,240,60
s_waitcnt vmcnt(10)
buffer_store_dwordx2 v[72:73], v81, s[36:39], s52 offen sc1
mav 4,244,60
mav 8,248,60
ds_write_b32 v97, v74 offset:1024
mav 12,252,60
mvv 0,116,124
mvv 4,120,124
mvv 8,116,128
mvv 12,120,128
mvv 0,116,132
mvv 4,120,132
mvv 8,116,136
s_add_u32 s49, s49, 346112
mvv 12,120,136
s_waitcnt lgkmcnt(0)
s_barrier
mvv 0,116,140
R 32,32768
mvv 4,120,140
buffer_load_dwordx4 v[172:175], v86, s[36:39], s49 offen sc1
mvv 8,116,144
mvv 12,120,144
s_waitcnt vmcnt(11)
mvv 0,116,148
R 36,33792
mvv 4,120,148
buffer_load_dwordx4 v[176:179], v86, s[36:39], s49 offen offset:1024 sc1
s_waitcnt vmcnt(11)
mvv 8,116,152
mvv 12,120,152
s_waitcnt vmcnt(10)
mvv 0,116,156
R 40,34816
mvv 4,120,156
s_waitcnt vmcnt(9)
mvv 8,116,160
buffer_load_dwordx4 v[180:183], v87, s[36:39], s49 offen sc1
mvv 12,120,160
s_waitcnt lgkmcnt(1)
v_mfma_f32_16x16x32_bf16 v[16:19], a[0:3], v[32:35], v[100:103]
R 44,35840
v_cvt_pk_bf16_f32 v64, v0, v1
v_mfma_f32_16x16x32_bf16 v[20:23], a[4:7], v[32:35], v[104:107]
v_cvt_pk_bf16_f32 v65, v2, v3
v_cvt_pk_bf16_f32 v66, v4, v5
v_mfma_f32_16x16x32_bf16 v[24:27], a[8:11], v[32:35], v[108:111]
buffer_load_dwordx4 v[184:187], v87, s[36:39], s49 offen offset:1024 sc1
v_cvt_pk_bf16_f32 v67, v6, v7
v_mfma_f32_16x16x32_bf16 v[28:31], a[12:15], v[32:35], v[112:115]
ds_write_b128 v94, v[64:67] offset:0
mav 16,16,36
R 48,36864
v_cvt_pk_bf16_f32 v68, v8, v9
mav 20,20,36
v_cvt_pk_bf16_f32 v69, v10, v11
v_cvt_pk_bf16_f32 v70, v12, v13
mav 24,24,36
v_cvt_pk_bf16_f32 v71, v14, v15
mav 28,28,36
buffer_load_dwordx4 v[188:191], v88, s[36:39], s49 offen sc1
s_waitcnt lgkmcnt(2)
mav 16,32,40
R 52,37888
mav 20,36,40
ds_write_b128 v95, v[68:71] offset:0
mav 24,40,40
mav 28,44,40
buffer_load_dwordx4 v[192:195], v88, s[36:39], s49 offen offset:1024 sc1
mav 16,48,44
R 56,38912
mav 20,52,44
s_waitcnt lgkmcnt(1)
ds_read_b128 v[64:67], v96 offset:0
mav 24,56,44
ds_read_b128 v[68:71], v96 offset:1024
mav 28,60,44
mav 16,64,48
R 60,39936
buffer_load_dwordx4 v[196:199], v89, s[36:39], s49 offen sc1
mav 20,68,48
mav 24,72,48
mav 28,76,48
mav 16,80,52
R 32,40960
buffer_load_dwordx4 v[200:203], v89, s[36:39], s49 offen offset:1024 sc1
mav 20,84,52
mav 24,88,52
mav 28,92,52
s_add_u32 m0, s50, 32768
s_waitcnt lgkmcnt(1)
mav 16,96,56
R 36,41984
buffer_store_dwordx4 v[64:67], v92, s[44:47], s54 offen
mav 20,100,56
mav 24,104,56
mav 28,108,56
mav 16,112,60
R 40,43008
buffer_store_dwordx4 v[68:71], v93, s[44:47], s54 offen
mav 20,116,60
mav 24,120,60
mav 28,124,60
s_waitcnt lgkmcnt(1)
mav 16,128,32
R 44,44032
D 83,0
mav 20,132,32
mav 24,136,32
mav 28,140,32
mav 16,144,36
R 48,45056
D 83,1024
mav 20,148,36
mav 24,152,36
mav 28,156,36
s_waitcnt lgkmcnt(1)
mav 16,160,40
R 52,46080
D 83,2048
mav 20,164,40
s_sub_u32 s54, s54, 0x8000
mav 24,168,40
mav 28,172,40
mav 16,176,44
R 56,47104
D 83,3072
mav 20,180,44
mav 24,184,44
mav 28,188,44
s_waitcnt lgkmcnt(1)
mav 16,192,48
R 60,48128
buffer_load_dwordx4 v[124:127], v90, s[36:39], s49 offen sc1
mav 20,196,48
mav 24,200,48
mav 28,204,48
mav 16,208,52
buffer_load_dwordx4 v[128:131], v90, s[36:39], s49 offen offset:1024 sc1
mav 20,212,52
mav 24,216,52
mav 28,220,52
s_waitcnt lgkmcnt(0)
mav 16,224,56
mav 20,228,56
mav 24,232,56
mav 28,236,56
mav 16,240,60
mav 20,244,60
mav 24,248,60
mav 28,252,60
s_waitcnt vmcnt(18)
mvv 16,116,164
mvv 20,120,164
s_waitcnt vmcnt(17)
mvv 24,116,168
s_xor_b32 s50, s50, 0x10000
mvv 28,120,168
s_waitcnt vmcnt(15)
mvv 16,116,172
mvv 20,120,172
v_xor_b32 v80, 0x10000, v80
s_waitcnt vmcnt(14)
mvv 24,116,176
s_add_u32 s49, s49, 346112
mvv 28,120,176
s_barrier
s_waitcnt vmcnt(13)
mvv 16,116,180
R 32,0
mvv 20,120,180
buffer_load_dwordx4 v[132:135], v86, s[36:39], s49 offen sc1
s_waitcnt vmcnt(13)
mvv 24,116,184
mvv 28,120,184
s_waitcnt vmcnt(12)
mvv 16,116,188
R 36,1024
mvv 20,120,188
buffer_load_dwordx4 v[136:139], v86, s[36:39], s49 offen offset:1024 sc1
s_waitcnt vmcnt(12)
mvv 24,116,192
mvv 28,120,192
s_waitcnt vmcnt(11)
mvv 16,116,196
R 40,2048
mvv 20,120,196
s_waitcnt vmcnt(10)
mvv 24,116,200
buffer_load_dwordx4 v[140:143], v87, s[36:39], s49 offen sc1
mvv 28,120,200
s_mov_b32 s60, -13
.p2align 6
LR2_2:
s_waitcnt lgkmcnt(1)
v_mfma_f32_16x16x32_bf16 v[0:3], a[0:3], v[32:35], v[100:103]
R 44,3072
v_cvt_pk_bf16_f32 v64, v16, v17
v_mfma_f32_16x16x32_bf16 v[4:7], a[4:7], v[32:35], v[104:107]
v_cvt_pk_bf16_f32 v65, v18, v19
v_cvt_pk_bf16_f32 v66, v20, v21
v_mfma_f32_16x16x32_bf16 v[8:11], a[8:11], v[32:35], v[108:111]
buffer_load_dwordx4 v[144:147], v87, s[36:39], s49 offen offset:1024 sc1
v_cvt_pk_bf16_f32 v67, v22, v23
v_mfma_f32_16x16x32_bf16 v[12:15], a[12:15], v[32:35], v[112:115]
ds_write_b128 v94, v[64:67] offset:0
mav 0,16,36
R 48,4096
v_cvt_pk_bf16_f32 v68, v24, v25
mav 4,20,36
v_cvt_pk_bf16_f32 v69, v26, v27
v_cvt_pk_bf16_f32 v70, v28, v29
mav 8,24,36
v_cvt_pk_bf16_f32 v71, v30, v31
mav 12,28,36
buffer_load_dwordx4 v[148:151], v88, s[36:39], s49 offen sc1
s_waitcnt lgkmcnt(2)
mav 0,32,40
R 52,5120
mav 4,36,40
ds_write_b128 v95, v[68:71] offset:0
mav 8,40,40
mav 12,44,40
buffer_load_dwordx4 v[152:155], v88, s[36:39], s49 offen offset:1024 sc1
mav 0,48,44
R 56,6144
mav 4,52,44
s_waitcnt lgkmcnt(1)
ds_read_b128 v[64:67], v96 offset:0
mav 8,56,44
ds_read_b128 v[68:71], v96 offset:1024
mav 12,60,44
mav 0,64,48
R 60,7168
buffer_load_dwordx4 v[156:159], v89, s[36:39], s49 offen sc1
mav 4,68,48
mav 8,72,48
mav 12,76,48
mav 0,80,52
R 32,8192
buffer_load_dwordx4 v[160:163], v89, s[36:39], s49 offen offset:1024 sc1
mav 4,84,52
mav 8,88,52
mav 12,92,52
s_add_u32 m0, s50, 0
s_waitcnt lgkmcnt(1)
mav 0,96,56
R 36,9216
buffer_store_dwordx4 v[64:67], v92, s[44:47], s54 offen
mav 4,100,56
mav 8,104,56
mav 12,108,56
mav 0,112,60
R 40,10240
buffer_store_dwordx4 v[68:71], v93, s[44:47], s54 offen
mav 4,116,60
mav 8,120,60
mav 12,124,60
s_waitcnt lgkmcnt(1)
mav 0,128,32
R 44,11264
D 83,0
mav 4,132,32
mav 8,136,32
mav 12,140,32
mav 0,144,36
R 48,12288
D 83,1024
mav 4,148,36
mav 8,152,36
mav 12,156,36
s_waitcnt lgkmcnt(1)
mav 0,160,40
R 52,13312
D 83,2048
mav 4,164,40
s_sub_u32 s54, s54, 0x8000
mav 8,168,40
mav 12,172,40
mav 0,176,44
R 56,14336
D 83,3072
mav 4,180,44
mav 8,184,44
mav 12,188,44
s_waitcnt lgkmcnt(1)
mav 0,192,48
R 60,15360
buffer_load_dwordx4 v[164:167], v90, s[36:39], s49 offen sc1
mav 4,196,48
mav 8,200,48
mav 12,204,48
mav 0,208,52
buffer_load_dwordx4 v[168:171], v90, s[36:39], s49 offen offset:1024 sc1
mav 4,212,52
mav 8,216,52
mav 12,220,52
s_waitcnt lgkmcnt(0)
mav 0,224,56
mav 4,228,56
mav 8,232,56
mav 12,236,56
mav 0,240,60
mav 4,244,60
mav 8,248,60
mav 12,252,60
s_waitcnt vmcnt(17)
mvv 0,116,124
mvv 4,120,124
s_waitcnt vmcnt(16)
mvv 8,116,128
mvv 12,120,128
s_waitcnt vmcnt(15)
mvv 0,116,132
mvv 4,120,132
s_waitcnt vmcnt(14)
mvv 8,116,136
s_add_u32 s49, s49, 346112
mvv 12,120,136
s_barrier
s_waitcnt vmcnt(13)
mvv 0,116,140
R 32,32768
mvv 4,120,140
buffer_load_dwordx4 v[172:175], v86, s[36:39], s49 offen sc1
s_waitcnt vmcnt(13)
mvv 8,116,144
mvv 12,120,144
s_waitcnt vmcnt(12)
mvv 0,116,148
R 36,33792
mvv 4,120,148
buffer_load_dwordx4 v[176:179], v86, s[36:39], s49 offen offset:1024 sc1
s_waitcnt vmcnt(12)
mvv 8,116,152
mvv 12,120,152
s_waitcnt vmcnt(11)
mvv 0,116,156
R 40,34816
mvv 4,120,156
s_waitcnt vmcnt(10)
mvv 8,116,160
buffer_load_dwordx4 v[180:183], v87, s[36:39], s49 offen sc1
mvv 12,120,160
s_waitcnt lgkmcnt(1)
v_mfma_f32_16x16x32_bf16 v[16:19], a[0:3], v[32:35], v[100:103]
R 44,35840
v_cvt_pk_bf16_f32 v64, v0, v1
v_mfma_f32_16x16x32_bf16 v[20:23], a[4:7], v[32:35], v[104:107]
v_cvt_pk_bf16_f32 v65, v2, v3
v_cvt_pk_bf16_f32 v66, v4, v5
v_mfma_f32_16x16x32_bf16 v[24:27], a[8:11], v[32:35], v[108:111]
buffer_load_dwordx4 v[184:187], v87, s[36:39], s49 offen offset:1024 sc1
v_cvt_pk_bf16_f32 v67, v6, v7
v_mfma_f32_16x16x32_bf16 v[28:31], a[12:15], v[32:35], v[112:115]
ds_write_b128 v94, v[64:67] offset:0
mav 16,16,36
R 48,36864
v_cvt_pk_bf16_f32 v68, v8, v9
mav 20,20,36
v_cvt_pk_bf16_f32 v69, v10, v11
v_cvt_pk_bf16_f32 v70, v12, v13
mav 24,24,36
v_cvt_pk_bf16_f32 v71, v14, v15
mav 28,28,36
buffer_load_dwordx4 v[188:191], v88, s[36:39], s49 offen sc1
s_waitcnt lgkmcnt(2)
mav 16,32,40
R 52,37888
mav 20,36,40
ds_write_b128 v95, v[68:71] offset:0
mav 24,40,40
mav 28,44,40
buffer_load_dwordx4 v[192:195], v88, s[36:39], s49 offen offset:1024 sc1
mav 16,48,44
R 56,38912
mav 20,52,44
s_waitcnt lgkmcnt(1)
ds_read_b128 v[64:67], v96 offset:0
mav 24,56,44
ds_read_b128 v[68:71], v96 offset:1024
mav 28,60,44
mav 16,64,48
R 60,39936
buffer_load_dwordx4 v[196:199], v89, s[36:39], s49 offen sc1
mav 20,68,48
mav 24,72,48
mav 28,76,48
mav 16,80,52
R 32,40960
buffer_load_dwordx4 v[200:203], v89, s[36:39], s49 offen offset:1024 sc1
mav 20,84,52
mav 24,88,52
mav 28,92,52
s_add_u32 m0, s50, 32768
s_waitcnt lgkmcnt(1)
mav 16,96,56
R 36,41984
buffer_store_dwordx4 v[64:67], v92, s[44:47], s54 offen
mav 20,100,56
mav 24,104,56
mav 28,108,56
mav 16,112,60
R 40,43008
buffer_store_dwordx4 v[68:71], v93, s[44:47], s54 offen
mav 20,116,60
mav 24,120,60
mav 28,124,60
s_waitcnt lgkmcnt(1)
mav 16,128,32
R 44,44032
D 83,0
mav 20,132,32
mav 24,136,32
mav 28,140,32
mav 16,144,36
R 48,45056
D 83,1024
mav 20,148,36
mav 24,152,36
mav 28,156,36
s_waitcnt lgkmcnt(1)
mav 16,160,40
R 52,46080
D 83,2048
mav 20,164,40
s_sub_u32 s54, s54, 0x8000
mav 24,168,40
mav 28,172,40
mav 16,176,44
R 56,47104
D 83,3072
mav 20,180,44
mav 24,184,44
mav 28,188,44
s_waitcnt lgkmcnt(1)
mav 16,192,48
R 60,48128
buffer_load_dwordx4 v[124:127], v90, s[36:39], s49 offen sc1
mav 20,196,48
mav 24,200,48
mav 28,204,48
mav 16,208,52
buffer_load_dwordx4 v[128:131], v90, s[36:39], s49 offen offset:1024 sc1
mav 20,212,52
mav 24,216,52
mav 28,220,52
s_waitcnt lgkmcnt(0)
mav 16,224,56
mav 20,228,56
mav 24,232,56
mav 28,236,56
mav 16,240,60
mav 20,244,60
mav 24,248,60
mav 28,252,60
s_waitcnt vmcnt(17)
mvv 16,116,164
mvv 20,120,164
s_waitcnt vmcnt(16)
mvv 24,116,168
s_xor_b32 s50, s50, 0x10000
mvv 28,120,168
s_waitcnt vmcnt(15)
mvv 16,116,172
mvv 20,120,172
v_xor_b32 v80, 0x10000, v80
s_waitcnt vmcnt(14)
mvv 24,116,176
s_add_u32 s49, s49, 346112
mvv 28,120,176
s_barrier
s_waitcnt vmcnt(13)
mvv 16,116,180
R 32,0
mvv 20,120,180
buffer_load_dwordx4 v[132:135], v86, s[36:39], s49 offen sc1
s_waitcnt vmcnt(13)
mvv 24,116,184
mvv 28,120,184
s_waitcnt vmcnt(12)
mvv 16,116,188
R 36,1024
mvv 20,120,188
buffer_load_dwordx4 v[136:139], v86, s[36:39], s49 offen offset:1024 sc1
s_waitcnt vmcnt(12)
mvv 24,116,192
mvv 28,120,192
s_waitcnt vmcnt(11)
mvv 16,116,196
R 40,2048
mvv 20,120,196
s_waitcnt vmcnt(10)
mvv 24,116,200
buffer_load_dwordx4 v[140:143], v87, s[36:39], s49 offen sc1
mvv 28,120,200
s_add_u32 s60, s60, 1
s_cbranch_scc0 LR2_2
s_waitcnt lgkmcnt(1)
v_mfma_f32_16x16x32_bf16 v[0:3], a[0:3], v[32:35], v[100:103]
R 44,3072
v_cvt_pk_bf16_f32 v64, v16, v17
v_mfma_f32_16x16x32_bf16 v[4:7], a[4:7], v[32:35], v[104:107]
v_cvt_pk_bf16_f32 v65, v18, v19
v_cvt_pk_bf16_f32 v66, v20, v21
v_mfma_f32_16x16x32_bf16 v[8:11], a[8:11], v[32:35], v[108:111]
buffer_load_dwordx4 v[144:147], v87, s[36:39], s49 offen offset:1024 sc1
v_cvt_pk_bf16_f32 v67, v22, v23
v_mfma_f32_16x16x32_bf16 v[12:15], a[12:15], v[32:35], v[112:115]
ds_write_b128 v94, v[64:67] offset:0
mav 0,16,36
R 48,4096
v_cvt_pk_bf16_f32 v68, v24, v25
mav 4,20,36
v_cvt_pk_bf16_f32 v69, v26, v27
v_cvt_pk_bf16_f32 v70, v28, v29
mav 8,24,36
v_cvt_pk_bf16_f32 v71, v30, v31
mav 12,28,36
buffer_load_dwordx4 v[148:151], v88, s[36:39], s49 offen sc1
s_waitcnt lgkmcnt(2)
mav 0,32,40
R 52,5120
mav 4,36,40
ds_write_b128 v95, v[68:71] offset:0
mav 8,40,40
mav 12,44,40
buffer_load_dwordx4 v[152:155], v88, s[36:39], s49 offen offset:1024 sc1
mav 0,48,44
R 56,6144
mav 4,52,44
s_waitcnt lgkmcnt(1)
ds_read_b128 v[64:67], v96 offset:0
mav 8,56,44
ds_read_b128 v[68:71], v96 offset:1024
mav 12,60,44
mav 0,64,48
R 60,7168
buffer_load_dwordx4 v[156:159], v89, s[36:39], s49 offen sc1
mav 4,68,48
mav 8,72,48
mav 12,76,48
mav 0,80,52
R 32,8192
buffer_load_dwordx4 v[160:163], v89, s[36:39], s49 offen offset:1024 sc1
mav 4,84,52
mav 8,88,52
mav 12,92,52
s_add_u32 m0, s50, 0
s_waitcnt lgkmcnt(1)
mav 0,96,56
R 36,9216
buffer_store_dwordx4 v[64:67], v92, s[44:47], s54 offen
mav 4,100,56
mav 8,104,56
mav 12,108,56
mav 0,112,60
R 40,10240
buffer_store_dwordx4 v[68:71], v93, s[44:47], s54 offen
mav 4,116,60
mav 8,120,60
mav 12,124,60
s_waitcnt lgkmcnt(1)
mav 0,128,32
R 44,11264
D 83,0
mav 4,132,32
mav 8,136,32
mav 12,140,32
mav 0,144,36
R 48,12288
D 83,1024
mav 4,148,36
mav 8,152,36
mav 12,156,36
s_waitcnt lgkmcnt(1)
mav 0,160,40
R 52,13312
D 83,2048
mav 4,164,40
s_sub_u32 s54, s54, 0x8000
mav 8,168,40
mav 12,172,40
mav 0,176,44
R 56,14336
D 83,3072
mav 4,180,44
mav 8,184,44
mav 12,188,44
s_waitcnt lgkmcnt(1)
mav 0,192,48
R 60,15360
buffer_load_dwordx4 v[164:167], v90, s[36:39], s49 offen sc1
mav 4,196,48
mav 8,200,48
mav 12,204,48
mav 0,208,52
buffer_load_dwordx4 v[168:171], v90, s[36:39], s49 offen offset:1024 sc1
mav 4,212,52
mav 8,216,52
mav 12,220,52
s_waitcnt lgkmcnt(0)
mav 0,224,56
mav 4,228,56
mav 8,232,56
mav 12,236,56
mav 0,240,60
mav 4,244,60
mav 8,248,60
mav 12,252,60
s_waitcnt vmcnt(17)
mvv 0,116,124
mvv 4,120,124
s_waitcnt vmcnt(16)
mvv 8,116,128
mvv 12,120,128
s_waitcnt vmcnt(15)
mvv 0,116,132
mvv 4,120,132
s_waitcnt vmcnt(14)
mvv 8,116,136
s_add_u32 s49, s49, 346112
mvv 12,120,136
s_barrier
s_waitcnt vmcnt(13)
mvv 0,116,140
R 32,32768
mvv 4,120,140
buffer_load_dwordx4 v[172:175], v86, s[36:39], s49 offen sc1
s_waitcnt vmcnt(13)
mvv 8,116,144
mvv 12,120,144
s_waitcnt vmcnt(12)
mvv 0,116,148
R 36,33792
mvv 4,120,148
buffer_load_dwordx4 v[176:179], v86, s[36:39], s49 offen offset:1024 sc1
s_waitcnt vmcnt(12)
mvv 8,116,152
mvv 12,120,152
s_waitcnt vmcnt(11)
mvv 0,116,156
R 40,34816
mvv 4,120,156
s_waitcnt vmcnt(10)
mvv 8,116,160
buffer_load_dwordx4 v[180:183], v87, s[36:39], s49 offen sc1
mvv 12,120,160
s_waitcnt lgkmcnt(1)
v_mfma_f32_16x16x32_bf16 v[16:19], a[0:3], v[32:35], v[100:103]
R 44,35840
v_cvt_pk_bf16_f32 v64, v0, v1
v_mfma_f32_16x16x32_bf16 v[20:23], a[4:7], v[32:35], v[104:107]
v_cvt_pk_bf16_f32 v65, v2, v3
)"
R"(
v_cvt_pk_bf16_f32 v66, v4, v5
v_mfma_f32_16x16x32_bf16 v[24:27], a[8:11], v[32:35], v[108:111]
buffer_load_dwordx4 v[184:187], v87, s[36:39], s49 offen offset:1024 sc1
v_cvt_pk_bf16_f32 v67, v6, v7
v_mfma_f32_16x16x32_bf16 v[28:31], a[12:15], v[32:35], v[112:115]
ds_write_b128 v94, v[64:67] offset:0
mav 16,16,36
R 48,36864
v_cvt_pk_bf16_f32 v68, v8, v9
mav 20,20,36
v_cvt_pk_bf16_f32 v69, v10, v11
v_cvt_pk_bf16_f32 v70, v12, v13
mav 24,24,36
v_cvt_pk_bf16_f32 v71, v14, v15
mav 28,28,36
buffer_load_dwordx4 v[188:191], v88, s[36:39], s49 offen sc1
s_waitcnt lgkmcnt(2)
mav 16,32,40
R 52,37888
mav 20,36,40
ds_write_b128 v95, v[68:71] offset:0
mav 24,40,40
mav 28,44,40
buffer_load_dwordx4 v[192:195], v88, s[36:39], s49 offen offset:1024 sc1
mav 16,48,44
R 56,38912
mav 20,52,44
s_waitcnt lgkmcnt(1)
ds_read_b128 v[64:67], v96 offset:0
mav 24,56,44
ds_read_b128 v[68:71], v96 offset:1024
mav 28,60,44
mav 16,64,48
R 60,39936
buffer_load_dwordx4 v[196:199], v89, s[36:39], s49 offen sc1
mav 20,68,48
mav 24,72,48
mav 28,76,48
mav 16,80,52
R 32,40960
buffer_load_dwordx4 v[200:203], v89, s[36:39], s49 offen offset:1024 sc1
mav 20,84,52
mav 24,88,52
mav 28,92,52
s_add_u32 m0, s50, 32768
s_waitcnt lgkmcnt(1)
mav 16,96,56
R 36,41984
buffer_store_dwordx4 v[64:67], v92, s[44:47], s54 offen
mav 20,100,56
mav 24,104,56
mav 28,108,56
mav 16,112,60
R 40,43008
buffer_store_dwordx4 v[68:71], v93, s[44:47], s54 offen
mav 20,116,60
mav 24,120,60
mav 28,124,60
s_waitcnt lgkmcnt(1)
mav 16,128,32
R 44,44032
D 83,0
mav 20,132,32
mav 24,136,32
mav 28,140,32
mav 16,144,36
R 48,45056
D 83,1024
mav 20,148,36
mav 24,152,36
mav 28,156,36
s_waitcnt lgkmcnt(1)
mav 16,160,40
R 52,46080
D 83,2048
mav 20,164,40
s_sub_u32 s54, s54, 0x8000
mav 24,168,40
mav 28,172,40
mav 16,176,44
R 56,47104
D 83,3072
mav 20,180,44
mav 24,184,44
mav 28,188,44
s_waitcnt lgkmcnt(1)
mav 16,192,48
R 60,48128
buffer_load_dwordx4 v[124:127], v90, s[36:39], s49 offen sc1
mav 20,196,48
mav 24,200,48
mav 28,204,48
mav 16,208,52
buffer_load_dwordx4 v[128:131], v90, s[36:39], s49 offen offset:1024 sc1
mav 20,212,52
mav 24,216,52
mav 28,220,52
s_waitcnt lgkmcnt(0)
mav 16,224,56
mav 20,228,56
mav 24,232,56
mav 28,236,56
mav 16,240,60
mav 20,244,60
mav 24,248,60
mav 28,252,60
s_waitcnt vmcnt(17)
mvv 16,116,164
mvv 20,120,164
s_waitcnt vmcnt(16)
mvv 24,116,168
mvv 28,120,168
s_waitcnt vmcnt(15)
mvv 16,116,172
mvv 20,120,172
v_xor_b32 v80, 0x10000, v80
s_waitcnt vmcnt(14)
mvv 24,116,176
s_add_u32 s49, s49, 346112
mvv 28,120,176
s_barrier
s_waitcnt vmcnt(13)
mvv 16,116,180
R 32,0
mvv 20,120,180
buffer_load_dwordx4 v[132:135], v86, s[36:39], s49 offen sc1
s_waitcnt vmcnt(13)
mvv 24,116,184
mvv 28,120,184
s_waitcnt vmcnt(12)
mvv 16,116,188
R 36,1024
mvv 20,120,188
buffer_load_dwordx4 v[136:139], v86, s[36:39], s49 offen offset:1024 sc1
s_waitcnt vmcnt(12)
mvv 24,116,192
mvv 28,120,192
s_waitcnt vmcnt(11)
mvv 16,116,196
R 40,2048
mvv 20,120,196
s_waitcnt vmcnt(10)
mvv 24,116,200
buffer_load_dwordx4 v[140:143], v87, s[36:39], s49 offen sc1
mvv 28,120,200
s_waitcnt lgkmcnt(1)
v_mfma_f32_16x16x32_bf16 v[0:3], a[0:3], v[32:35], v[100:103]
R 44,3072
v_cvt_pk_bf16_f32 v64, v16, v17
v_mfma_f32_16x16x32_bf16 v[4:7], a[4:7], v[32:35], v[104:107]
v_cvt_pk_bf16_f32 v65, v18, v19
v_cvt_pk_bf16_f32 v66, v20, v21
v_mfma_f32_16x16x32_bf16 v[8:11], a[8:11], v[32:35], v[108:111]
buffer_load_dwordx4 v[144:147], v87, s[36:39], s49 offen offset:1024 sc1
v_cvt_pk_bf16_f32 v67, v22, v23
v_mfma_f32_16x16x32_bf16 v[12:15], a[12:15], v[32:35], v[112:115]
ds_write_b128 v94, v[64:67] offset:0
mav 0,16,36
R 48,4096
v_cvt_pk_bf16_f32 v68, v24, v25
mav 4,20,36
v_cvt_pk_bf16_f32 v69, v26, v27
v_cvt_pk_bf16_f32 v70, v28, v29
mav 8,24,36
v_cvt_pk_bf16_f32 v71, v30, v31
mav 12,28,36
buffer_load_dwordx4 v[148:151], v88, s[36:39], s49 offen sc1
s_waitcnt lgkmcnt(2)
mav 0,32,40
R 52,5120
mav 4,36,40
ds_write_b128 v95, v[68:71] offset:0
mav 8,40,40
mav 12,44,40
buffer_load_dwordx4 v[152:155], v88, s[36:39], s49 offen offset:1024 sc1
mav 0,48,44
R 56,6144
mav 4,52,44
s_waitcnt lgkmcnt(1)
ds_read_b128 v[64:67], v96 offset:0
mav 8,56,44
ds_read_b128 v[68:71], v96 offset:1024
mav 12,60,44
mav 0,64,48
R 60,7168
buffer_load_dwordx4 v[156:159], v89, s[36:39], s49 offen sc1
mav 4,68,48
mav 8,72,48
mav 12,76,48
mav 0,80,52
R 32,8192
buffer_load_dwordx4 v[160:163], v89, s[36:39], s49 offen offset:1024 sc1
mav 4,84,52
mav 8,88,52
mav 12,92,52
s_waitcnt lgkmcnt(1)
mav 0,96,56
R 36,9216
buffer_store_dwordx4 v[64:67], v92, s[44:47], s54 offen
mav 4,100,56
mav 8,104,56
mav 12,108,56
mav 0,112,60
R 40,10240
buffer_store_dwordx4 v[68:71], v93, s[44:47], s54 offen
mav 4,116,60
mav 8,120,60
mav 12,124,60
s_waitcnt lgkmcnt(1)
mav 0,128,32
R 44,11264
mav 4,132,32
mav 8,136,32
mav 12,140,32
mav 0,144,36
R 48,12288
mav 4,148,36
mav 8,152,36
mav 12,156,36
s_waitcnt lgkmcnt(1)
mav 0,160,40
R 52,13312
s_sub_u32 s54, s54, 0x8000
mav 4,164,40
mav 8,168,40
mav 12,172,40
buffer_load_dwordx4 v[164:167], v90, s[36:39], s49 offen sc1
mav 0,176,44
R 56,14336
mav 4,180,44
mav 8,184,44
mav 12,188,44
buffer_load_dwordx4 v[168:171], v90, s[36:39], s49 offen offset:1024 sc1
s_waitcnt lgkmcnt(1)
mav 0,192,48
R 60,15360
mav 4,196,48
mav 8,200,48
mav 12,204,48
mav 0,208,52
mav 4,212,52
mav 8,216,52
mav 12,220,52
s_waitcnt lgkmcnt(0)
mav 0,224,56
mav 4,228,56
mav 8,232,56
mav 12,236,56
mav 0,240,60
mav 4,244,60
mav 8,248,60
mav 12,252,60
s_waitcnt vmcnt(13)
mvv 0,116,124
mvv 4,120,124
s_waitcnt vmcnt(12)
mvv 8,116,128
mvv 12,120,128
s_waitcnt vmcnt(11)
mvv 0,116,132
mvv 4,120,132
s_waitcnt vmcnt(10)
mvv 8,116,136
s_add_u32 s49, s49, 346112
mvv 12,120,136
s_barrier
s_waitcnt vmcnt(9)
mvv 0,116,140
R 32,32768
mvv 4,120,140
buffer_load_dwordx4 v[172:175], v86, s[36:39], s49 offen sc1
s_waitcnt vmcnt(9)
mvv 8,116,144
mvv 12,120,144
s_waitcnt vmcnt(8)
mvv 0,116,148
R 36,33792
mvv 4,120,148
buffer_load_dwordx4 v[176:179], v86, s[36:39], s49 offen offset:1024 sc1
s_waitcnt vmcnt(8)
mvv 8,116,152
mvv 12,120,152
s_waitcnt vmcnt(7)
mvv 0,116,156
R 40,34816
mvv 4,120,156
s_waitcnt vmcnt(6)
mvv 8,116,160
buffer_load_dwordx4 v[180:183], v87, s[36:39], s49 offen sc1
mvv 12,120,160
s_waitcnt lgkmcnt(1)
v_mfma_f32_16x16x32_bf16 v[16:19], a[0:3], v[32:35], v[100:103]
R 44,35840
v_cvt_pk_bf16_f32 v64, v0, v1
v_mfma_f32_16x16x32_bf16 v[20:23], a[4:7], v[32:35], v[104:107]
v_cvt_pk_bf16_f32 v65, v2, v3
v_cvt_pk_bf16_f32 v66, v4, v5
v_mfma_f32_16x16x32_bf16 v[24:27], a[8:11], v[32:35], v[108:111]
buffer_load_dwordx4 v[184:187], v87, s[36:39], s49 offen offset:1024 sc1
v_cvt_pk_bf16_f32 v67, v6, v7
v_mfma_f32_16x16x32_bf16 v[28:31], a[12:15], v[32:35], v[112:115]
ds_write_b128 v94, v[64:67] offset:0
mav 16,16,36
R 48,36864
v_cvt_pk_bf16_f32 v68, v8, v9
mav 20,20,36
v_cvt_pk_bf16_f32 v69, v10, v11
v_cvt_pk_bf16_f32 v70, v12, v13
mav 24,24,36
v_cvt_pk_bf16_f32 v71, v14, v15
mav 28,28,36
buffer_load_dwordx4 v[188:191], v88, s[36:39], s49 offen sc1
s_waitcnt lgkmcnt(2)
mav 16,32,40
R 52,37888
mav 20,36,40
ds_write_b128 v95, v[68:71] offset:0
mav 24,40,40
mav 28,44,40
buffer_load_dwordx4 v[192:195], v88, s[36:39], s49 offen offset:1024 sc1
mav 16,48,44
R 56,38912
mav 20,52,44
s_waitcnt lgkmcnt(1)
ds_read_b128 v[64:67], v96 offset:0
mav 24,56,44
ds_read_b128 v[68:71], v96 offset:1024
mav 28,60,44
mav 16,64,48
R 60,39936
buffer_load_dwordx4 v[196:199], v89, s[36:39], s49 offen sc1
mav 20,68,48
mav 24,72,48
mav 28,76,48
mav 16,80,52
R 32,40960
buffer_load_dwordx4 v[200:203], v89, s[36:39], s49 offen offset:1024 sc1
mav 20,84,52
mav 24,88,52
mav 28,92,52
s_waitcnt lgkmcnt(1)
mav 16,96,56
R 36,41984
buffer_store_dwordx4 v[64:67], v92, s[44:47], s54 offen
mav 20,100,56
mav 24,104,56
mav 28,108,56
mav 16,112,60
R 40,43008
buffer_store_dwordx4 v[68:71], v93, s[44:47], s54 offen
mav 20,116,60
mav 24,120,60
mav 28,124,60
s_waitcnt lgkmcnt(1)
mav 16,128,32
R 44,44032
mav 20,132,32
mav 24,136,32
mav 28,140,32
mav 16,144,36
R 48,45056
mav 20,148,36
mav 24,152,36
mav 28,156,36
s_waitcnt lgkmcnt(1)
mav 16,160,40
R 52,46080
s_sub_u32 s54, s54, 0x8000
mav 20,164,40
mav 24,168,40
mav 28,172,40
mav 16,176,44
R 56,47104
mav 20,180,44
mav 24,184,44
mav 28,188,44
s_waitcnt lgkmcnt(1)
mav 16,192,48
R 60,48128
mav 20,196,48
mav 24,200,48
mav 28,204,48
mav 16,208,52
mav 20,212,52
mav 24,216,52
mav 28,220,52
s_waitcnt lgkmcnt(0)
mav 16,224,56
mav 20,228,56
mav 24,232,56
mav 28,236,56
mav 16,240,60
mav 20,244,60
mav 24,248,60
mav 28,252,60
s_waitcnt vmcnt(11)
mvv 16,116,164
mvv 20,120,164
s_waitcnt vmcnt(10)
mvv 24,116,168
mvv 28,120,168
s_waitcnt vmcnt(9)
mvv 16,116,172
mvv 20,120,172
s_waitcnt vmcnt(8)
mvv 24,116,176
mvv 28,120,176
s_waitcnt vmcnt(7)
mvv 16,116,180
mvv 20,120,180
s_waitcnt vmcnt(6)
mvv 24,116,184
mvv 28,120,184
s_waitcnt vmcnt(5)
mvv 16,116,188
mvv 20,120,188
s_waitcnt vmcnt(4)
mvv 24,116,192
mvv 28,120,192
s_waitcnt vmcnt(3)
mvv 16,116,196
mvv 20,120,196
s_waitcnt vmcnt(2)
mvv 24,116,200
mvv 28,120,200
s_nop 3
v_cvt_pk_bf16_f32 v64, v16, v17
v_cvt_pk_bf16_f32 v65, v18, v19
v_cvt_pk_bf16_f32 v66, v20, v21
v_cvt_pk_bf16_f32 v67, v22, v23
ds_write_b128 v94, v[64:67] offset:0
v_cvt_pk_bf16_f32 v68, v24, v25
v_cvt_pk_bf16_f32 v69, v26, v27
v_cvt_pk_bf16_f32 v70, v28, v29
v_cvt_pk_bf16_f32 v71, v30, v31
ds_write_b128 v95, v[68:71] offset:0
s_waitcnt lgkmcnt(0)
ds_read_b128 v[64:67], v96 offset:0
ds_read_b128 v[68:71], v96 offset:1024
s_waitcnt lgkmcnt(0)
buffer_store_dwordx4 v[64:67], v92, s[44:47], s54 offen
buffer_store_dwordx4 v[68:71], v93, s[44:47], s54 offen
s_endpgm
.purgem mav
.purgem Ta
.purgem mva
.purgem mvv
.purgem Tv
.purgem R
.purgem D
.purgem E
)"
    : "+{v[0:7]}"(v0), "+{v[80:95]}"(c0), "+{v[96:111]}"(c1), "+{v[112:127]}"(c2), "+{s[36:51]}"(s0), "+{s[52:59]}"(s1)
    :
    : "v8", "v9", "v10", "v11", "v12", "v13", "v14", "v15", "v16", "v17", "v18", "v19", "v20", "v21", "v22", "v23", "v24", "v25", "v26", "v27", "v28", "v29", "v30", "v31", "v32", "v33", "v34", "v35", "v36", "v37", "v38", "v39", "v40", "v41", "v42", "v43", "v44", "v45", "v46", "v47", "v48", "v49", "v50", "v51", "v52", "v53", "v54", "v55", "v56", "v57", "v58", "v59", "v60", "v61", "v62", "v63", "v64", "v65", "v66", "v67", "v68", "v69", "v70", "v71", "v72", "v73", "v74", "v75", "v76", "v77", "v78", "v79", "v128", "v129", "v130", "v131", "v132", "v133", "v134", "v135", "v136", "v137", "v138", "v139", "v140", "v141", "v142", "v143", "v144", "v145", "v146", "v147", "v148", "v149", "v150", "v151", "v152", "v153", "v154", "v155", "v156", "v157", "v158", "v159", "v160", "v161", "v162", "v163", "v164", "v165", "v166", "v167", "v168", "v169", "v170", "v171", "v172", "v173", "v174", "v175", "v176", "v177", "v178", "v179", "v180", "v181", "v182", "v183", "v184", "v185", "v186", "v187", "v188", "v189", "v190", "v191", "v192", "v193", "v194", "v195", "v196", "v197", "v198", "v199", "v200", "v201", "v202", "v203", "v204", "v205", "v206", "v207", "v208", "v209", "v210", "v211", "v212", "v213", "v214", "v215", "v216", "v217", "v218", "v219", "v220", "v221", "v222", "v223", "v224", "v225", "v226", "v227", "v228", "v229", "v230", "v231", "v232", "v233", "v234", "v235", "v236", "v237", "v238", "v239", "v240", "v241", "v242", "v243", "v244", "v245", "v246", "v247", "v248", "v249", "v250", "v251", "v252", "v253", "v254", "v255", "a0", "a1", "a2", "a3", "a4", "a5", "a6", "a7", "a8", "a9", "a10", "a11", "a12", "a13", "a14", "a15", "a16", "a17", "a18", "a19", "a20", "a21", "a22", "a23", "a24", "a25", "a26", "a27", "a28", "a29", "a30", "a31", "a32", "a33", "a34", "a35", "a36", "a37", "a38", "a39", "a40", "a41", "a42", "a43", "a44", "a45", "a46", "a47", "a48", "a49", "a50", "a51", "a52", "a53", "a54", "a55", "a56", "a57", "a58", "a59", "a60", "a61", "a62", "a63", "a64", "a65", "a66", "a67", "a68", "a69", "a70", "a71", "a72", "a73", "a74", "a75", "a76", "a77", "a78", "a79", "a80", "a81", "a82", "a83", "a84", "a85", "a86", "a87", "a88", "a89", "a90", "a91", "a92", "a93", "a94", "a95", "a96", "a97", "a98", "a99", "a100", "a101", "a102", "a103", "a104", "a105", "a106", "a107", "a108", "a109", "a110", "a111", "a112", "a113", "a114", "a115", "a116", "a117", "a118", "a119", "a120", "a121", "a122", "a123", "a124", "a125", "a126", "a127", "a128", "a129", "a130", "a131", "a132", "a133", "a134", "a135", "a136", "a137", "a138", "a139", "a140", "a141", "a142", "a143", "a144", "a145", "a146", "a147", "a148", "a149", "a150", "a151", "a152", "a153", "a154", "a155", "a156", "a157", "a158", "a159", "a160", "a161", "a162", "a163", "a164", "a165", "a166", "a167", "a168", "a169", "a170", "a171", "a172", "a173", "a174", "a175", "a176", "a177", "a178", "a179", "a180", "a181", "a182", "a183", "a184", "a185", "a186", "a187", "a188", "a189", "a190", "a191", "a192", "a193", "a194", "a195", "a196", "a197", "a198", "a199", "a200", "a201", "a202", "a203", "a204", "a205", "a206", "a207", "a208", "a209", "a210", "a211", "a212", "a213", "a214", "a215", "a216", "a217", "a218", "a219", "a220", "a221", "a222", "a223", "a224", "a225", "a226", "a227", "a228", "a229", "a230", "a231", "a232", "a233", "a234", "a235", "a236", "a237", "a238", "a239", "a240", "a241", "a242", "a243", "a244", "a245", "a246", "a247", "a248", "a249", "a250", "a251", "a252", "a253", "a254", "a255", "s60", "s61", "s62", "s63", "s64", "s65", "s66", "s67", "s68", "s69", "s70", "s71", "s72", "s73", "s74", "s75", "s76", "s77", "s78", "s79", "s80", "s81", "s82", "s83", "s84", "s85", "s86", "s87", "s88", "s89", "s90", "s91", "s92", "s93", "s94", "s95", "s96", "s97", "s98", "s99", "vcc", "scc", "memory");
}

extern "C" void kernel_launch(void* const* d_in, const int* in_sizes, int n_in, void* d_out, int out_size,
                              void* d_ws, size_t ws_size, hipStream_t stream) {
  Args m{};
  m.x = (const u16*)d_in[0]; m.wqkv = (const u16*)d_in[1]; m.wo = (const u16*)d_in[2]; m.w1 = (const u16*)d_in[4];
  m.b1 = (const u16*)d_in[5]; m.w2 = (const u16*)d_in[6]; m.b2 = (const u16*)d_in[7];
  m.out = (u16*)d_out; m.ws = (char*)d_ws;
  (void)hipFuncSetAttribute((const void*)df_kernel, hipFuncAttributeMaxDynamicSharedMemorySize, LDS_BYTES);
  hipLaunchKernelGGL(df_kernel, dim3(256), dim3(256), LDS_BYTES, stream, m);
}
```

```cpp
#include <hip/hip_runtime.h>
#include <stdint.h>

typedef __attribute__((ext_vector_type(16))) unsigned u32x16;
typedef __attribute__((ext_vector_type(8))) unsigned u32x8;
typedef __attribute__((ext_vector_type(4))) unsigned u32x4;
typedef __attribute__((ext_vector_type(2))) unsigned u32x2;
typedef __attribute__((ext_vector_type(2))) __bf16 bf2_t;
typedef unsigned short u16;
typedef __amdgpu_buffer_rsrc_t rsrc_t;
typedef __attribute__((address_space(3))) void lds_t;

constexpr unsigned FLAGS = 4096, RB = 4194304, REC = 346112, SX = 344064, WS_END = 92798976, DBG = 1048576;
constexpr int LDS_BYTES = 163840;
#define NEGB 0xff61b1e6u
#define FW(p, s, w) (FLAGS + (unsigned)(((p) * 64 + (s)) * 128 + (w) * 8))
#define blk0(s) (RB + (unsigned)(p * 64 + (s) - 2) * REC)
#define TA(seg, hf, sub) (unsigned)((seg) * 4096 + (8 * lg + (li >> 2)) * 128 + ((((hf) * 4 + (li & 3)) ^ ((li >> 1) & 4)) << 4) + (sub))
#define TO(seg, hf, half) (unsigned)((seg) * 4096 + (8 * lg + (li >> 2)) * 128 + ((((hf) * 4 + (half) * 2 + ((li & 3) >> 1)) ^ ((li >> 1) & 4)) << 4) + (li & 1) * 8)
#define BIAS(ptr, n, i) ((unsigned)__builtin_amdgcn_raw_buffer_load_b16(__builtin_amdgcn_make_buffer_rsrc((void*)(ptr), 0, (n) * 2, 0x00020000), (i) * 2, 0, 0) << 16)
#define SPIN_CAP 200000

struct Args {
  const u16 *x, *wqkv, *wo, *w1, *w2;
  char* ws;
  u16* out;
  const u16 *b1, *b2;
  unsigned tab[32];
};

__device__ __forceinline__ float rows_sum(float v) {
  const auto a = __builtin_amdgcn_permlane16_swap(__float_as_uint(v), __float_as_uint(v), false, false);
  const float m = __uint_as_float(a[0]) + __uint_as_float(a[1]);
  const auto b = __builtin_amdgcn_permlane32_swap(__float_as_uint(m), __float_as_uint(m), false, false);
  return __uint_as_float(b[0]) + __uint_as_float(b[1]);
}

__device__ __forceinline__ const u16* arg_b2() {
  auto ka = (const __attribute__((address_space(4))) Args*)__builtin_amdgcn_kernarg_segment_ptr();
  asm volatile("" : "+s"(ka));
  return ka->b2;
}

__global__ __launch_bounds__(256, 1) void df_kernel(Args m) {
  const int w = __builtin_amdgcn_readfirstlane(threadIdx.x >> 6);
  const int bid = blockIdx.x, xc = bid & 7, j_ = bid >> 3;
  const int te_ = ((const __attribute__((address_space(4))) Args*)__builtin_amdgcn_kernarg_segment_ptr())->tab[(j_ & 28) + (xc & 3)] >> ((j_ & 3) * 8), slot = te_ & 63, p = ((te_ >> 6) ^ (xc >> 1 & 2)) & 3;
  {
    const unsigned tid = threadIdx.x;
    lds_t* const sink = (lds_t*)(size_t)(65536 + w * 1024);
    if (w & 1) {
      unsigned long long pc; unsigned o = w == 1 ? (tid & 63) * 128 : (tid & 63) * 4096 + j_ * 128;
      asm volatile("s_mov_b32 m0, %2\n\ts_getpc_b64 %0\nLbeg:\n\tv_min_u32 %1, Lfin - Lbeg + 60, %1\n\tglobal_load_lds_dword %1, %0" : "=&s"(pc), "+v"(o) : "s"(65536 + w * 1024) : "memory");
    }
    {
      const u32x4 rs = {(unsigned)(size_t)m.ws, (unsigned)((size_t)m.ws >> 32) & 0xffffu, WS_END, 0x00020000};
      asm volatile("s_nop 4\n\tbuffer_load_dword a255, off, %0, 0 sc1" :: "s"(rs) : "a255", "memory");
    }
#define QLINE(l) (((l) >> 2) * 2048 + ((l) & 3) * 128 + (xc & 3) * 512)
    if (((slot + 16) & 63) < 32) {
      const rsrc_t rT = __builtin_amdgcn_make_buffer_rsrc((void*)(w == 3 ? m.x : m.wqkv), 0, w == 3 ? 2048 * 1024 * 2 : 1024 * 3072 * 2, 0x00020000);
      const unsigned i8 = (slot & 3) * 2 + (p & 1), i16 = i8 * 2 + (slot >> 5), lx = i16 * 64 + (tid & 63), lh = i8 * 192 + tid;
      __builtin_amdgcn_raw_ptr_buffer_load_lds(rT, sink, 4, w == 3 ? ((lx >> 9) + ((xc >> 1) & 2)) * 1048576 + (lx & 511) * 128 + 30 * 32768 : QLINE(i16 * 192 + tid), 0, 0, 0);
      if (slot >= 48 && w < 3) {
#pragma unroll
        for (int i = 2; i < 8; ++i) __builtin_amdgcn_raw_ptr_buffer_load_lds(rT, sink, 4, QLINE(lh), i * 1536 * 512, 0, 0);
      }
      if (1 && slot >= 48 && (xc & 3) == 1) {
        const rsrc_t rU = __builtin_amdgcn_make_buffer_rsrc((void*)m.wo, 0, 1024 * 1024 * 2, 0x00020000);
#pragma unroll
        for (int i = 0; i < 8; ++i) __builtin_amdgcn_raw_ptr_buffer_load_lds(rU, sink, 4, (i8 * 256 + tid) * 128, i * 262144, 0, 0);
      }
    }
    if (slot < 16) {
      const rsrc_t rW = __builtin_amdgcn_make_buffer_rsrc((void*)m.wqkv, 0, 1024 * 3072 * 2, 0x00020000);
      const rsrc_t rX = __builtin_amdgcn_make_buffer_rsrc((void*)m.x, 0, 2048 * 1024 * 2, 0x00020000);
      const unsigned ln = tid & 63, vw = (ln >> 3) * 6144 + (((ln & 7) ^ ((ln >> 2) & 4)) << 4), vx = (ln >> 4) * 2048 + (((ln & 15) ^ ((ln >> 4) * 4)) << 4);
#pragma unroll
      for (int g = 0; g < 1; ++g) {
#pragma unroll
        for (int i = 0; i < 6; ++i) {
          __builtin_amdgcn_raw_ptr_buffer_load_lds(rW, (lds_t*)(size_t)(36864 + (3 - g) * 24576 + i * 4096 + w * 1024), 16, vw, w * 8 * 6144 + slot * 128 + (2 * g + i / 3) * 32 * 6144 + i % 3 * 2048, 0, 0);
          if (!(g & 1) && (i == 1 || i == 3))
            __builtin_amdgcn_raw_ptr_buffer_load_lds(rX, (lds_t*)(size_t)(w * 8208 + 2048 + (i >> 1) * 4096 + g * 512), 16, vx, (p * 32 + 31 - (i >> 1)) * 32768 + w * 8192 + g * 128, 0, 0);
        }
      }
    }
  }
  if (slot >= 16 && slot < 48) {
    int lane;
    asm volatile("v_mbcnt_lo_u32_b32 %0, -1, 0\n\tv_mbcnt_hi_u32_b32 %0, -1, %0" : "=v"(lane));
    const int lg = lane >> 4, li = lane & 15;
    const rsrc_t rS = __builtin_amdgcn_make_buffer_rsrc((void*)m.ws, 0, WS_END, 0x00020000);
    const rsrc_t rX = __builtin_amdgcn_make_buffer_rsrc((void*)m.x, 0, 2048 * 1024 * 2, 0x00020000);
    const bf2_t one2 = {(__bf16)1.0f, (__bf16)1.0f};
    const unsigned go = lg * 2048 + w * 512 + ((li ^ (lg * 4)) << 4), lb = 73728 + w * 10240, lo = lb + (li >> 2) * 2064 + (li & 3) * 256 + lg * 16;
#pragma unroll
    for (int i = 0; i < 8; ++i)
      __builtin_amdgcn_raw_ptr_buffer_load_lds(rX, (lds_t*)(size_t)(lb + (i >> 1) * 2064 + (i & 1) * 1024), 16, go + ((i >> 1) * 8192 + (i & 1) * 256), (p * 32 + 47 - slot) * 32768, 0, 0);
    asm volatile("s_waitcnt vmcnt(0)" ::: "memory");
    u32x4 v[8];
#pragma unroll
    for (int k = 0; k < 8; ++k) v[k] = *(const __attribute__((address_space(3))) u32x4*)(size_t)(lo + (k >> 2) * 1024 + (((k ^ li) & 3) << 6));
    const unsigned rb = RB + (unsigned)(p * 64 + slot - 16) * REC;
#pragma unroll
    for (int k = 0; k < 8; ++k) __builtin_amdgcn_raw_buffer_store_b128(v[k], rS, lane * 16 + w * 8192 + k * 1024, rb, 16);
    float s = 0.f, q = 0.f;
#pragma unroll
    for (int k = 0; k < 8; ++k) {
#pragma unroll
      for (int e = 0; e < 4; ++e) {
        const unsigned xe = v[k][e];
        const bf2_t x = __builtin_bit_cast(bf2_t, xe);
        s = __builtin_amdgcn_fdot2_f32_bf16(x, one2, s, false);
        q = __builtin_amdgcn_fdot2_f32_bf16(x, x, q, false);
      }
    }
    s = rows_sum(s); q = rows_sum(q);
    __builtin_amdgcn_raw_buffer_store_b64((u32x2){__float_as_uint(s), __float_as_uint(q)}, rS, lg ? 0x80000000u : w * 128 + li * 8 + SX, rb, 16);
    if (slot == 22 || slot == 23 || slot == 47) {
      asm volatile("v_mbcnt_lo_u32_b32 %0, -1, 0\n\tv_mbcnt_hi_u32_b32 %0, -1, %0" : "=v"(lane));
      u32x2 tag = {0x100u, 0u};
      asm volatile("s_waitcnt vmcnt(0)\n\ts_barrier\nLfin:\n\tv_accvgpr_read_b32 %0, a255" : "=v"(tag[1]) :: "memory");
      tag[1] += 0x9e3779b9u;
      __builtin_amdgcn_raw_buffer_store_b64(tag, rS, lane ? 0x80000000u : FW(p, slot, w), 0, 16);
      if (slot == 47 && p == 3 && w == 3) {
        for (int n = 0; n < SPIN_CAP; ++n) {
          __builtin_amdgcn_s_sleep(127);
          bool bad = false;
#pragma unroll
          for (int i = 0; i < 8; ++i) {
            const u32x4 f = __builtin_amdgcn_raw_buffer_load_b128(rS, FW(i >> 1, (i & 1) * 32 + (lane >> 1), (lane & 1) * 2), 0, 16);
            bad = bad || f[1] != tag[1] || f[3] != tag[1] || f[0] - tag[0] > 255u || f[2] - tag[0] > 255u;
          }
          if (__builtin_amdgcn_ballot_w64(bad) == 0ull) break;
        }
        __builtin_amdgcn_raw_buffer_store_b32(tag[1], rS, lane ? 0x80000000u : 0u, 0, 16);
      }
      return;
    }
  }
  const int lane = threadIdx.x & 63, lg = lane >> 4, li = lane & 15;
  const unsigned l16 = lane * 16;
  u32x16 c0 = 0, c1 = 0, c2 = 0, s0 = 0;
  u32x8 v0 = 0, s1 = 0;
  const u16 *wp, *op = m.out; unsigned wn;
  const u16 *rQp = m.wqkv, *rOp = m.wo, *r1p = m.w1, *r2p = m.w2;
  constexpr unsigned rQn = 1024 * 3072 * 2, rOn = 1024 * 1024 * 2, r1n = 1024 * 3584 * 2, r2n = r1n;
  const int rot = p * 1, wr = (w + rot) & 3;
  if (slot < 16 && !(w & 1)) {
    s0[12] = 0 + (0); wp = rQp; wn = rQn;
    v0[3] = (p * 32 + 30) * 32768 + (w * 4 + lg) * 2048 + ((li ^ (lg * 4)) << 4); v0[7] = 2048 + (li >> 2) * 8208 + (li & 3) * 256 + lg * 16; op = m.x;
    s0[13] = blk0(0) + 32768u + (slot * 2 + (w >> 1)) * 1024 - (131072u + (w >> 1) * 2048);
    s0[15] = w * 8 * 6144 + slot * 128 + 393216;
    s1[2] = 262144u + (p * 32 + slot * 2 + (w >> 1)) * 4096 - (131072u + (w >> 1) * 2048);
    s1[3] = p ? 262144u + ((p - 1) * 32 + slot * 2 + (w >> 1)) * 4096 - (131072u + (w >> 1) * 2048) : 0x80000000u;
    s1[4] = p ? 0u : NEGB;
    v0[0] = (lane >> 3) * 6144 + (((lane & 7) ^ ((lane >> 2) & 4)) << 4) + 0u;
    v0[1] = (lane >> 3) * 6144 + (((lane & 7) ^ ((lane >> 2) & 4)) << 4) + 2048u;
    v0[2] = (lane >> 3) * 6144 + (((lane & 7) ^ ((lane >> 2) & 4)) << 4) + 4096u;
    v0[4] = TA(0, w >> 1, 0);
    v0[5] = TA(0, w >> 1, 8);
    v0[6] = TA(2, w >> 1, 0);
    c0[0] = l16;
    c0[1] = 131072u + (w >> 1) * 2048 + l16;
    c0[2] = 139264u + li * 8;
    c0[3] = 139264u + lg * 32;
    c0[4] = (((4 * lg + 0) ^ li) & 1) == 0 && (4 * lg + 0 <= li) ? 0u : NEGB;
    c0[5] = (((4 * lg + 1) ^ li) & 1) == 0 && (4 * lg + 1 <= li) ? 0u : NEGB;
    c0[6] = (((4 * lg + 2) ^ li) & 1) == 0 && (4 * lg + 2 <= li) ? 0u : NEGB;
    c0[7] = (((4 * lg + 3) ^ li) & 1) == 0 && (4 * lg + 3 <= li) ? 0u : NEGB;
    c0[8] = (((4 * lg + 0) ^ li) & 1) == 0 && (4 * lg + 0 > li) ? 0u : NEGB;
    c0[9] = (((4 * lg + 1) ^ li) & 1) == 0 && (4 * lg + 1 > li) ? 0u : NEGB;
    c0[10] = (((4 * lg + 2) ^ li) & 1) == 0 && (4 * lg + 2 > li) ? 0u : NEGB;
    c0[11] = (((4 * lg + 3) ^ li) & 1) == 0 && (4 * lg + 3 > li) ? 0u : NEGB;
    c0[12] = (4 * lg + 0 + (li & 1) <= li) ? ((li & 1) ? 0xffff0000u : 0xffffu) : 0u;
    c0[13] = (4 * lg + 2 + (li & 1) <= li) ? ((li & 1) ? 0xffff0000u : 0xffffu) : 0u;
    c0[14] = (4 * lg + 0 + (li & 1) > li) ? ((li & 1) ? 0xffff0000u : 0xffffu) : 0u;
    c0[15] = (4 * lg + 2 + (li & 1) > li) ? ((li & 1) ? 0xffff0000u : 0xffffu) : 0u;
    c1[0] = 131072u + (w >> 1) * 2048 + lane * 8;
  } else if (slot < 16) {
    s0[12] = 4 + (w >> 1); wp = rQp; wn = rQn;
    v0[3] = (p * 32 + 30) * 32768 + (w * 4 + lg) * 2048 + ((li ^ (lg * 4)) << 4); v0[7] = 2048 + (li >> 2) * 8208 + (li & 3) * 256 + lg * 16; op = m.x;
    s0[13] = blk0(0);
    s0[14] = 65536 + (w >> 1) * 4096;
    s0[15] = w * 8 * 6144 + slot * 128 + 393216;
    s1[2] = blk0(0) + (w >> 1) * 346112u;
    s1[3] = (w >> 1) * 128;
    v0[0] = (lane >> 3) * 6144 + (((lane & 7) ^ ((lane >> 2) & 4)) << 4) + 0u;
    v0[1] = (lane >> 3) * 6144 + (((lane & 7) ^ ((lane >> 2) & 4)) << 4) + 2048u;
    v0[2] = (lane >> 3) * 6144 + (((lane & 7) ^ ((lane >> 2) & 4)) << 4) + 4096u;
    v0[4] = TA(1, w >> 1, 0);
    v0[5] = TA(1, w >> 1, 8);
    v0[6] = TA(2, w >> 1, 8);
    c0[0] = l16;
    c0[1] = lane >= 1 ? 0x80000000u : lane * 33024u;
    c0[2] = 142336u + lane * 4;
    c0[3] = 131072u + (w >> 1) * 2048 + l16;
    c0[4] = 139264u + li * 8;
    c0[5] = 139264u + lg * 32;
    c0[6] = 1384448u + 0 + (w >> 1) * 4096 + l16;
    c0[7] = 1384448u + 8192 + (w >> 1) * 4096 + l16;
    c0[8] = 1384448u + 16384 + (w >> 1) * 4096 + l16;
    c0[9] = 1384448u + 24576 + (w >> 1) * 4096 + l16;
    c0[10] = (lane >> 1) == 24 && p ? FW(p - 1, slot, (lane & 1) * 2) : FW(p, 43 + ((lane & 15) >> 2), lane & 3) + (slot >> 2) % 1 * 33024u;
    c0[11] = lane >= 16 && lane < 48 ? FW(p, lane, 0) : (lane >> 1) == 24 && p ? FW(p - 1, slot, (lane & 1) * 2) : FW(p, 43 + ((lane & 15) >> 2), lane & 3) + (slot >> 2) % 1 * 33024u;
    c0[12] = (unsigned)-26;
    c0[13] = 1036288u + lane * 8;
  } else if (slot < 22) {
    s0[12] = 8 + (w < 2 ? w : 2); wp = rOp; wn = rOn;
    s0[13] = blk0(-2);
    s0[14] = w * 8192;
    s0[15] = w * 8 * 2048 + (slot - 16) * 384 + rot * 524288;
    s1[5] = w * 8 * 2048 + (slot - 16) * 384 + 2097152u;
    v0[0] = (lane >> 3) * 2048 + (((lane & 7) ^ ((lane >> 2) & 4)) << 4) + ((slot - 16) * 6 + 0 < 32 ? 0u : 0x80000000u);
    v0[1] = (lane >> 3) * 2048 + (((lane & 7) ^ ((lane >> 2) & 4)) << 4) + ((slot - 16) * 6 + 2 < 32 ? 128u : 0x80000000u);
    v0[2] = (lane >> 3) * 2048 + (((lane & 7) ^ ((lane >> 2) & 4)) << 4) + ((slot - 16) * 6 + 4 < 32 ? 256u : 0x80000000u);
    v0[4] = TA(((w >> 1) * 3 + (w & 1)) >> 1, ((w >> 1) * 3 + (w & 1)) & 1, 0);
    v0[5] = TA(((w >> 1) * 3 + (w & 1)) >> 1, ((w >> 1) * 3 + (w & 1)) & 1, 8);
    v0[6] = TO(((w >> 1) * 3 + 2) >> 1, ((w >> 1) * 3 + 2) & 1, w & 1);
    c0[0] = l16;
    c0[1] = lane >= 1 ? 0x80000000u : lane * 33024u;
    c0[2] = 142336u + lane * 4;
    c0[3] = ((slot - 16) * 6 + (w >> 1) * 3 + (w & 1)) < 32 ? 411648u + ((slot - 16) * 6 + (w >> 1) * 3 + (w & 1)) * 1024 + l16 : 0x80000000u;
    c0[4] = ((slot - 16) * 6 + (w >> 1) * 3 + 2) < 32 && !(lg & 1) ? 411648u + ((slot - 16) * 6 + (w >> 1) * 3 + 2) * 1024 + ((w & 1) * 2 + (lg >> 1)) * 256 + li * 16 : 0x80000000u;
    c0[5] = 1071104u + wr * 8192 + l16;
    c0[6] = 1075200u + wr * 8192 + l16;
    c0[7] = FW(p, (lane & 31) >> 1, (lane & 1) * 2) + (slot) % 1 * 33024u;
    c0[8] = 1u;
    c0[9] = ((slot - 16) * 6 + (w >> 1) * 3 + (w & 1)) < 32 ? 1384448u + ((slot - 16) * 6 + (w >> 1) * 3 + (w & 1)) * 1024 + l16 : 0x80000000u;
    c0[10] = ((slot - 16) * 6 + (w >> 1) * 3 + 2) < 32 ? 1384448u + ((slot - 16) * 6 + (w >> 1) * 3 + 2) * 1024 + ((w & 1) * 2 + (lg >> 1)) * 256 + li * 16 + (lg & 1) * 8 : 0x80000000u;
    c0[11] = 140288u + ((li * 2 + (lg >> 1)) * 8 + w * 2 + (lg & 1)) * 4;
    c0[12] = 140288u + l16;
    c0[13] = (lane & 1) ? 0x80000000u : 345088u + (slot - 16) * 128 + (lane >> 1) * 4;
  } else if (slot < 43) {
    s0[12] = 12 + (w < 2 ? w : 2); wp = r1p; wn = r1n;
    s0[13] = blk0(-2);
    s0[14] = w * 8192;
    s0[15] = w * 8 * 7168 + (slot - 24) * 384 + rot * 1835008;
    s1[5] = w * 8 * 7168 + (slot - 24) * 384 + 7340032u;
    v0[0] = (lane >> 3) * 7168 + (((lane & 7) ^ ((lane >> 2) & 4)) << 4) + ((slot - 24) * 6 + 0 < 112 ? 0u : 0x80000000u);
    v0[1] = (lane >> 3) * 7168 + (((lane & 7) ^ ((lane >> 2) & 4)) << 4) + ((slot - 24) * 6 + 2 < 112 ? 128u : 0x80000000u);
    v0[2] = (lane >> 3) * 7168 + (((lane & 7) ^ ((lane >> 2) & 4)) << 4) + ((slot - 24) * 6 + 4 < 112 ? 256u : 0x80000000u);
    v0[4] = TA(((w >> 1) * 3 + (w & 1)) >> 1, ((w >> 1) * 3 + (w & 1)) & 1, 0);
    v0[5] = TA(((w >> 1) * 3 + (w & 1)) >> 1, ((w >> 1) * 3 + (w & 1)) & 1, 8);
    v0[6] = TO(((w >> 1) * 3 + 2) >> 1, ((w >> 1) * 3 + 2) & 1, w & 1);
    c0[0] = l16;
    c0[1] = lane >= 1 ? 0x80000000u : lane * 33024u;
    c0[2] = 142336u + lane * 4;
    c0[3] = ((slot - 24) * 6 + (w >> 1) * 3 + (w & 1)) < 112 ? 444416u + ((slot - 24) * 6 + (w >> 1) * 3 + (w & 1)) * 1024 + l16 : 0x80000000u;
    c0[4] = ((slot - 24) * 6 + (w >> 1) * 3 + 2) < 112 && !(lg & 1) ? 444416u + ((slot - 24) * 6 + (w >> 1) * 3 + 2) * 1024 + ((w & 1) * 2 + (lg >> 1)) * 256 + li * 16 : 0x80000000u;
    c0[5] = 1103872u + wr * 8192 + l16;
    c0[6] = 1107968u + wr * 8192 + l16;
    c0[7] = (lane < 24 ? FW(p, 16 + (lane >> 2), lane & 3) : FW(p, 16, 0)) + (slot) % 1 * 33024u;
    c0[8] = 1u;
    c0[9] = 139264u + li * 8;
    c0[10] = 1037312u + lane * 8;
    c0[11] = lane < 32 ? 1037824u + lane * 8 : 0x80000000u;
    c0[12] = BIAS(m.b1, 3584, ((slot - 24) * 6 + (w >> 1) * 3 + (w & 1)) * 32 + lg * 8 + 0);
    c0[13] = BIAS(m.b1, 3584, ((slot - 24) * 6 + (w >> 1) * 3 + (w & 1)) * 32 + lg * 8 + 1);
    c0[14] = BIAS(m.b1, 3584, ((slot - 24) * 6 + (w >> 1) * 3 + (w & 1)) * 32 + lg * 8 + 2);
    c0[15] = BIAS(m.b1, 3584, ((slot - 24) * 6 + (w >> 1) * 3 + (w & 1)) * 32 + lg * 8 + 3);
    c1[0] = BIAS(m.b1, 3584, ((slot - 24) * 6 + (w >> 1) * 3 + (w & 1)) * 32 + lg * 8 + 4);
    c1[1] = BIAS(m.b1, 3584, ((slot - 24) * 6 + (w >> 1) * 3 + (w & 1)) * 32 + lg * 8 + 5);
    c1[2] = BIAS(m.b1, 3584, ((slot - 24) * 6 + (w >> 1) * 3 + (w & 1)) * 32 + lg * 8 + 6);
    c1[3] = BIAS(m.b1, 3584, ((slot - 24) * 6 + (w >> 1) * 3 + (w & 1)) * 32 + lg * 8 + 7);
    c1[4] = BIAS(m.b1, 3584, ((slot - 24) * 6 + (w >> 1) * 3 + 2) * 32 + (w & 1) * 16 + lg * 4 + 0);
    c1[5] = BIAS(m.b1, 3584, ((slot - 24) * 6 + (w >> 1) * 3 + 2) * 32 + (w & 1) * 16 + lg * 4 + 1);
    c1[6] = BIAS(m.b1, 3584, ((slot - 24) * 6 + (w >> 1) * 3 + 2) * 32 + (w & 1) * 16 + lg * 4 + 2);
    c1[7] = BIAS(m.b1, 3584, ((slot - 24) * 6 + (w >> 1) * 3 + 2) * 32 + (w & 1) * 16 + lg * 4 + 3);
  } else if (slot >= 48) {
    s0[12] = 16 + (w ? 1 : 0); wp = r2p; wn = r2n;
    s0[13] = blk0(-2);
    s0[14] = w * 6144;
    s0[15] = (((slot - 48) >> 2) * 768 + w * 8) * 2048 + (slot & 3) * 512 + rot * 393216;
    s1[5] = (((slot - 48) >> 2) * 768 + w * 8) * 2048 + (slot & 3) * 512 + 1572864u;
    v0[0] = (lane >> 3) * 2048 + (((lane & 7) ^ ((lane >> 2) & 4)) << 4) + 0u;
    v0[1] = (lane >> 3) * 2048 + (((lane & 7) ^ ((lane >> 2) & 4)) << 4) + 128u;
    v0[2] = (lane >> 3) * 2048 + (((lane & 7) ^ ((lane >> 2) & 4)) << 4) + 256u;
    v0[3] = (lane >> 3) * 2048 + (((lane & 7) ^ ((lane >> 2) & 4)) << 4) + 384u;
    v0[4] = TA(w, 0, 0);
    v0[5] = TA(w, 0, 8);
    v0[6] = TA(w, 1, 0);
    v0[7] = TA(w, 1, 8);
    c0[0] = l16;
    c0[1] = lane >= 1 ? 0x80000000u : lane * 33024u;
    c0[2] = 142336u + lane * 4;
    c0[3] = 1136640u + ((slot - 48) >> 2) * 24576 + wr * 6144 + l16;
    c0[4] = 1140736u + ((slot - 48) >> 2) * 24576 + wr * 6144 + l16;
    c0[5] = lane >= 52 && slot >= 60 ? FW(p, 40 + (lane - 52) / 4, lane & 3) : FW(p, 24 + ((slot - 48) & ~3) + ((lane & 15) >> 2), lane & 3) + (slot) % 1 * 33024u;
    c0[6] = 1u;
    c0[7] = 559104u + (slot - 48) * 8192 + w * 2048 + l16;
  } else if (true) {
    s0[12] = 20 + (w < 2 ? w : 2); wp = r2p; wn = r2n;
    const u16* const b2p = arg_b2();
    s0[13] = blk0(-3);
    s0[14] = w * 4096;
    s0[15] = (3072 + w * 8) * 2048 + (slot - 43) * 512 + rot * 262144;
    s1[2] = (p * 32 + 31) * 32768;
    s1[5] = (3072 + w * 8) * 2048 + (slot - 43) * 512 + 1048576u;
    v0[0] = (lane >> 3) * 2048 + (((lane & 7) ^ ((lane >> 2) & 4)) << 4) + 0u;
    v0[1] = (lane >> 3) * 2048 + (((lane & 7) ^ ((lane >> 2) & 4)) << 4) + 128u;
    v0[2] = (lane >> 3) * 2048 + (((lane & 7) ^ ((lane >> 2) & 4)) << 4) + 256u;
    v0[3] = (lane >> 3) * 2048 + (((lane & 7) ^ ((lane >> 2) & 4)) << 4) + 384u;
    v0[4] = TA(w, 0, 0);
    v0[5] = TA(w, 0, 8);
    v0[6] = TA(w, 1, 0);
    v0[7] = TA(w, 1, 8);
    c0[0] = l16;
    c0[1] = lane >= 1 ? 0x80000000u : lane * 33024u;
    c0[2] = 142336u + lane * 4;
    c0[3] = 1581056u + wr * 4096 + l16;
    c0[4] = FW(p, 48 + (lane & 12) + slot - 43, lane & 3);
    c0[5] = 1u;
    c0[6] = 905216u + (slot - 43) * 8192 + w * 2048 + l16;
    c0[7] = 937984u + (slot - 43) * 8192 + w * 2048 + l16;
    c0[8] = 970752u + (slot - 43) * 8192 + w * 2048 + l16;
    c0[9] = 1003520u + (slot - 43) * 8192 + w * 2048 + l16;
    c0[10] = 1103872u + (slot - 43) * 8192 + w * 2048 + l16;
    c0[11] = 11421696u + (slot - 43) * 8192 + w * 2048 + l16;
    c0[12] = (lane >> 3) * 2048 + (lane & 7) * 16 + (slot - 43) * 512 + w * 128;
    c0[13] = ((lane >> 3) + 8) * 2048 + (lane & 7) * 16 + (slot - 43) * 512 + w * 128;
    c0[14] = 131072u + w * 2048 + li * 128 + (((0 + lg) ^ (li & 7)) << 4);
    c0[15] = 131072u + w * 2048 + li * 128 + (((4 + lg) ^ (li & 7)) << 4);
    c1[0] = 131072u + w * 2048 + (lane >> 3) * 128 + (((lane ^ (lane >> 3)) & 7) << 4);
    c1[1] = 140288u + ((li * 2 + (lg >> 1)) * 8 + w * 2 + (lg & 1)) * 4;
    c1[2] = 140288u + l16;
    c1[3] = (lane & 1) ? 0x80000000u : 11419648u + (slot - 43) * 128 + (lane >> 1) * 4;
    c1[4] = BIAS(b2p, 1024, (slot - 43) * 256 + w * 64 + 0 + lg * 8);
    c1[5] = BIAS(b2p, 1024, (slot - 43) * 256 + w * 64 + 1 + lg * 8);
    c1[6] = BIAS(b2p, 1024, (slot - 43) * 256 + w * 64 + 2 + lg * 8);
    c1[7] = BIAS(b2p, 1024, (slot - 43) * 256 + w * 64 + 3 + lg * 8);
    c1[8] = BIAS(b2p, 1024, (slot - 43) * 256 + w * 64 + 4 + lg * 8);
    c1[9] = BIAS(b2p, 1024, (slot - 43) * 256 + w * 64 + 5 + lg * 8);
    c1[10] = BIAS(b2p, 1024, (slot - 43) * 256 + w * 64 + 6 + lg * 8);
    c1[11] = BIAS(b2p, 1024, (slot - 43) * 256 + w * 64 + 7 + lg * 8);
    c1[12] = BIAS(b2p, 1024, (slot - 43) * 256 + w * 64 + 32 + lg * 8);
    c1[13] = BIAS(b2p, 1024, (slot - 43) * 256 + w * 64 + 33 + lg * 8);
    c1[14] = BIAS(b2p, 1024, (slot - 43) * 256 + w * 64 + 34 + lg * 8);
    c1[15] = BIAS(b2p, 1024, (slot - 43) * 256 + w * 64 + 35 + lg * 8);
    c2[0] = BIAS(b2p, 1024, (slot - 43) * 256 + w * 64 + 36 + lg * 8);
    c2[1] = BIAS(b2p, 1024, (slot - 43) * 256 + w * 64 + 37 + lg * 8);
    c2[2] = BIAS(b2p, 1024, (slot - 43) * 256 + w * 64 + 38 + lg * 8);
    c2[3] = BIAS(b2p, 1024, (slot - 43) * 256 + w * 64 + 39 + lg * 8);
    c2[4] = (lg == (li >> 2) && ((li & 3) >> 1) + 0 == 0) ? ((li & 1) ? 0x3f800000u : 0x3f80u) : 0u;
    c2[5] = (lg == (li >> 2) && ((li & 3) >> 1) + 0 == 1) ? ((li & 1) ? 0x3f800000u : 0x3f80u) : 0u;
    c2[6] = (lg == (li >> 2) && ((li & 3) >> 1) + 0 == 2) ? ((li & 1) ? 0x3f800000u : 0x3f80u) : 0u;
    c2[7] = (lg == (li >> 2) && ((li & 3) >> 1) + 0 == 3) ? ((li & 1) ? 0x3f800000u : 0x3f80u) : 0u;
    c2[8] = (lg == (li >> 2) && ((li & 3) >> 1) + 2 == 0) ? ((li & 1) ? 0x3f800000u : 0x3f80u) : 0u;
    c2[9] = (lg == (li >> 2) && ((li & 3) >> 1) + 2 == 1) ? ((li & 1) ? 0x3f800000u : 0x3f80u) : 0u;
    c2[10] = (lg == (li >> 2) && ((li & 3) >> 1) + 2 == 2) ? ((li & 1) ? 0x3f800000u : 0x3f80u) : 0u;
    c2[11] = (lg == (li >> 2) && ((li & 3) >> 1) + 2 == 3) ? ((li & 1) ? 0x3f800000u : 0x3f80u) : 0u;
  }
  s0[0] = (unsigned)(size_t)m.ws; s0[1] = (unsigned)((size_t)m.ws >> 32) & 0xffffu; s0[2] = WS_END; s0[3] = 0x00020000;
  s0[4] = (unsigned)(size_t)wp; s0[5] = (unsigned)((size_t)wp >> 32) & 0xffffu; s0[6] = wn; s0[7] = 0x00020000;
  s0[8] = (unsigned)(size_t)op; s0[9] = (unsigned)((size_t)op >> 32) & 0xffffu; s0[10] = 2048 * 1024 * 2; s0[11] = 0x00020000;
  s1[0] = FW(p, slot, w); s1[1] = w * 1024; s1[7] = DBG + ((p * 64 + slot) * 4 + w) * 1024;
#pragma unroll
  for (int i = 0; i < 16; ++i) s0[i] = __builtin_amdgcn_readfirstlane(s0[i]);
#pragma unroll
  for (int i = 0; i < 8; ++i) s1[i] = __builtin_amdgcn_readfirstlane(s1[i]);
  asm volatile(
R"(
.macro mav d,a,b
v_mfma_f32_16x16x32_bf16 v[\d:\d+3], a[\a:\a+3], v[\b:\b+3], v[\d:\d+3]
.endm
.macro Ta d,a,o
ds_read_b64_tr_b16 a[\d:\d+1], v\a offset:\o
ds_read_b64_tr_b16 a[\d+2:\d+3], v\a offset:\o+512
.endm
.macro mva d,a,b
v_mfma_f32_16x16x32_bf16 v[\d:\d+3], v[\a:\a+3], a[\b:\b+3], v[\d:\d+3]
.endm
.macro mvv d,a,b
v_mfma_f32_16x16x32_bf16 v[\d:\d+3], v[\a:\a+3], v[\b:\b+3], v[\d:\d+3]
.endm
.macro Tv d,a,o
ds_read_b64_tr_b16 v[\d:\d+1], v\a offset:\o
ds_read_b64_tr_b16 v[\d+2:\d+3], v\a offset:\o+512
.endm
.macro R d,o
ds_read_b128 v[\d:\d+3], v80 offset:\o
.endm
.macro D v,o
buffer_load_dwordx4 v\v, s[36:39], s49 offen offset:\o sc1 lds
.endm
.macro E v,o
s_add_u32 m0, s53, \o
s_nop 0
buffer_load_dwordx4 v\v, s[40:43], s51 offen lds
.endm
s_branch La
.p2align 12
La:
s_nop 4
s_mov_b32 s61, 200000
s_mov_b64 s[62:63], 255
s_mov_b32 s64, 0x3f803f80
s_mov_b32 s65, 0x3e8293ee
s_lshr_b32 s60, s48, 2
Lr0:
s_cmp_eq_u32 s60, 0
s_cbranch_scc0 Lr1
s_lshr_b32 s66, s53, 10
s_mul_i32 s66, s66, 8208
s_mov_b32 s67, 0x8000
s_mov_b64 exec, 0
s_cmp_eq_u32 s53, 0
s_cbranch_scc1 LsQ0
s_branch LsQ2
LwQA:
s_mov_b64 exec, -1
E 0,86016
E 1,90112
E 2,94208
s_add_u32 s51, s51, 196608
E 0,98304
E 1,102400
E 2,106496
s_add_u32 s51, s51, 196608
E 0,61440
E 1,65536
s_add_u32 m0, s66, 2816
s_nop 0
buffer_load_dwordx4 v3, s[44:47], s67 offen offset:256 lds
E 2,69632
s_add_u32 s51, s51, 196608
E 0,73728
s_add_u32 m0, s66, 6912
s_nop 0
buffer_load_dwordx4 v3, s[44:47], 0 offen offset:256 lds
E 1,77824
E 2,81920
s_add_u32 s51, s51, 196608
v_lshlrev_b32 v12, 2, v80
v_and_b32 v12, 0xc0, v12
v_xor_b32 v13, 0, v12
v_add_u32 v8, v7, v13
v_xor_b32 v13, 64, v12
v_add_u32 v9, v7, v13
v_xor_b32 v13, 128, v12
v_add_u32 v10, v7, v13
v_xor_b32 v13, 192, v12
v_add_u32 v11, v7, v13
v_mov_b32 v112, s64
v_mov_b32 v113, s64
v_mov_b32 v114, s64
v_mov_b32 v115, s64
s_waitcnt vmcnt(14)
s_barrier
v_add_u32 v4, 69632, v4
v_add_u32 v5, 69632, v5
v_add_u32 v6, 69632, v6
Ta 0,4,40960
Ta 4,5,40960
Ta 8,6,40960
Ta 12,4,53248
Ta 16,5,53248
Ta 20,6,53248
ds_read_b128 v[38:41], v8 offset:0
ds_read_b128 v[42:45], v9 offset:0
ds_read_b128 v[46:49], v8 offset:4096
ds_read_b128 v[50:53], v9 offset:4096
v_accvgpr_read_b32 v79, a255
v_mov_b32 v78, 0x100
v_add_u32 v79, 0x9e3779b9, v79
buffer_store_dwordx2 v[78:79], off, s[36:39], s52 sc1
E 0,36864
E 1,40960
E 2,45056
s_add_u32 s51, s51, 196608
E 0,49152
E 1,53248
E 2,57344
s_add_u32 s51, s51, 196608
s_waitcnt lgkmcnt(0)
s_waitcnt vmcnt(15)
s_barrier
E 0,110592
Ta 24,4,16384
v_mfma_f32_16x16x32_bf16 v[62:65], a[0:3], v[112:115], 0
v_mfma_f32_16x16x32_bf16 v[66:69], a[4:7], v[112:115], 0
Ta 28,5,16384
v_mfma_f32_16x16x32_bf16 v[70:73], v[112:115], a[8:11], 0
Ta 32,6,16384
E 1,114688
v_mfma_f32_16x16x32_bf16 v[14:17], a[0:3], v[38:41], 0
Ta 36,4,28672
v_mfma_f32_16x16x32_bf16 v[18:21], a[4:7], v[38:41], 0
Ta 40,5,28672
s_add_u32 m0, s66, 3584
s_nop 0
buffer_load_dwordx4 v3, s[44:47], s67 offen offset:512 lds
v_mfma_f32_16x16x32_bf16 v[22:25], v[38:41], a[8:11], 0
Ta 44,6,28672
v_mfma_f32_16x16x32_bf16 v[26:29], a[0:3], v[46:49], 0
ds_read_b128 v[54:57], v10 offset:0
E 2,118784
s_add_u32 s51, s51, 196608
v_mfma_f32_16x16x32_bf16 v[30:33], a[4:7], v[46:49], 0
ds_read_b128 v[58:61], v11 offset:0
v_mfma_f32_16x16x32_bf16 v[34:37], v[46:49], a[8:11], 0
ds_read_b128 v[98:101], v10 offset:4096
E 0,122880
mav 62,12,112
ds_read_b128 v[102:105], v11 offset:4096
mav 66,16,112
mva 70,112,20
s_add_u32 m0, s66, 7680
s_nop 0
buffer_load_dwordx4 v3, s[44:47], 0 offen offset:512 lds
mav 14,12,42
mav 18,16,42
E 1,126976
mva 22,42,20
mav 26,12,50
E 2,131072
s_add_u32 s51, s51, 196608
mav 30,16,50
mva 34,50,20
s_waitcnt lgkmcnt(0)
s_waitcnt vmcnt(15)
s_barrier
v_add_u32 v4, -49152, v4
v_add_u32 v5, -49152, v5
v_add_u32 v6, -49152, v6
E 0,86016
Ta 48,4,40960
mav 62,24,112
mav 66,28,112
Ta 52,5,40960
mva 70,112,32
Ta 56,6,40960
E 1,90112
mav 14,24,54
Ta 60,4,53248
mav 18,28,54
Ta 64,5,53248
mva 22,54,32
Ta 68,6,53248
E 2,94208
s_add_u32 s51, s51, 196608
mav 26,24,98
ds_read_b128 v[38:41], v8 offset:1024
mav 30,28,98
ds_read_b128 v[42:45], v9 offset:1024
mva 34,98,32
ds_read_b128 v[46:49], v8 offset:5120
E 0,98304
mav 62,36,112
ds_read_b128 v[50:53], v9 offset:5120
mav 66,40,112
mva 70,112,44
E 1,102400
mav 14,36,58
mav 18,40,58
mva 22,58,44
E 2,106496
s_add_u32 s51, s51, 196608
mav 26,36,102
mav 30,40,102
mva 34,102,44
s_waitcnt lgkmcnt(0)
s_waitcnt vmcnt(14)
s_barrier
E 0,61440
Ta 72,4,16384
mav 62,48,112
mav 66,52,112
Ta 76,5,16384
mva 70,112,56
Ta 80,6,16384
E 1,65536
mav 14,48,38
Ta 84,4,28672
mav 18,52,38
Ta 88,5,28672
s_add_u32 m0, s66, 4352
s_nop 0
buffer_load_dwordx4 v3, s[44:47], s67 offen offset:768 lds
mva 22,38,56
Ta 92,6,28672
mav 26,48,46
ds_read_b128 v[54:57], v10 offset:1024
E 2,69632
s_add_u32 s51, s51, 196608
mav 30,52,46
ds_read_b128 v[58:61], v11 offset:1024
mva 34,46,56
ds_read_b128 v[98:101], v10 offset:5120
E 0,73728
mav 62,60,112
ds_read_b128 v[102:105], v11 offset:5120
mav 66,64,112
mva 70,112,68
s_add_u32 m0, s66, 8448
s_nop 0
buffer_load_dwordx4 v3, s[44:47], 0 offen offset:768 lds
mav 14,60,42
mav 18,64,42
E 1,77824
mva 22,42,68
mav 26,60,50
E 2,81920
s_add_u32 s51, s51, 196608
mav 30,64,50
mva 34,50,68
s_waitcnt lgkmcnt(0)
s_waitcnt vmcnt(14)
s_barrier
v_add_u32 v4, 49152, v4
v_add_u32 v5, 49152, v5
v_add_u32 v6, 49152, v6
E 0,36864
Ta 96,4,40960
mav 62,72,112
mav 66,76,112
Ta 100,5,40960
mva 70,112,80
Ta 104,6,40960
E 1,40960
mav 14,72,54
Ta 108,4,53248
mav 18,76,54
Ta 112,5,53248
mva 22,54,80
Ta 116,6,53248
E 2,45056
s_add_u32 s51, s51, 196608
mav 26,72,98
ds_read_b128 v[38:41], v8 offset:2048
mav 30,76,98
ds_read_b128 v[42:45], v9 offset:2048
mva 34,98,80
ds_read_b128 v[46:49], v8 offset:6144
E 0,49152
mav 62,84,112
ds_read_b128 v[50:53], v9 offset:6144
mav 66,88,112
mva 70,112,92
E 1,53248
mav 14,84,58
mav 18,88,58
mva 22,58,92
E 2,57344
s_add_u32 s51, s51, 196608
mav 26,84,102
mav 30,88,102
mva 34,102,92
s_waitcnt lgkmcnt(0)
s_waitcnt vmcnt(14)
s_barrier
E 0,110592
Ta 120,4,16384
mav 62,96,112
mav 66,100,112
Ta 124,5,16384
mva 70,112,104
Ta 128,6,16384
E 1,114688
mav 14,96,38
Ta 132,4,28672
mav 18,100,38
Ta 136,5,28672
s_add_u32 m0, s66, 1024
s_nop 0
buffer_load_dwordx4 v3, s[44:47], s67 offen offset:1024 lds
mva 22,38,104
Ta 140,6,28672
mav 26,96,46
ds_read_b128 v[54:57], v10 offset:2048
E 2,118784
s_add_u32 s51, s51, 196608
mav 30,100,46
ds_read_b128 v[58:61], v11 offset:2048
mva 34,46,104
ds_read_b128 v[98:101], v10 offset:6144
E 0,122880
mav 62,108,112
ds_read_b128 v[102:105], v11 offset:6144
mav 66,112,112
mva 70,112,116
s_add_u32 m0, s66, 5120
s_nop 0
buffer_load_dwordx4 v3, s[44:47], 0 offen offset:1024 lds
mav 14,108,42
mav 18,112,42
E 1,126976
mva 22,42,116
mav 26,108,50
E 2,131072
s_add_u32 s51, s51, 196608
mav 30,112,50
mva 34,50,116
s_waitcnt lgkmcnt(0)
s_waitcnt vmcnt(14)
s_barrier
v_add_u32 v4, -49152, v4
v_add_u32 v5, -49152, v5
v_add_u32 v6, -49152, v6
E 0,86016
Ta 144,4,40960
mav 62,120,112
mav 66,124,112
Ta 148,5,40960
mva 70,112,128
Ta 152,6,40960
E 1,90112
mav 14,120,54
Ta 156,4,53248
mav 18,124,54
Ta 160,5,53248
mva 22,54,128
Ta 164,6,53248
E 2,94208
s_add_u32 s51, s51, 196608
mav 26,120,98
ds_read_b128 v[38:41], v8 offset:3072
mav 30,124,98
ds_read_b128 v[42:45], v9 offset:3072
mva 34,98,128
ds_read_b128 v[46:49], v8 offset:7168
E 0,98304
mav 62,132,112
ds_read_b128 v[50:53], v9 offset:7168
mav 66,136,112
mva 70,112,140
E 1,102400
mav 14,132,58
mav 18,136,58
mva 22,58,140
E 2,106496
s_add_u32 s51, s51, 196608
mav 26,132,102
mav 30,136,102
mva 34,102,140
s_waitcnt lgkmcnt(0)
s_waitcnt vmcnt(14)
s_barrier
E 0,61440
Ta 168,4,16384
mav 62,144,112
mav 66,148,112
Ta 172,5,16384
mva 70,112,152
Ta 176,6,16384
E 1,65536
mav 14,144,38
Ta 180,4,28672
mav 18,148,38
Ta 184,5,28672
s_add_u32 m0, s66, 1792
s_nop 0
buffer_load_dwordx4 v3, s[44:47], s67 offen offset:1280 lds
mva 22,38,152
Ta 188,6,28672
mav 26,144,46
ds_read_b128 v[54:57], v10 offset:3072
E 2,69632
s_add_u32 s51, s51, 196608
mav 30,148,46
ds_read_b128 v[58:61], v11 offset:3072
mva 34,46,152
ds_read_b128 v[98:101], v10 offset:7168
E 0,73728
mav 62,156,112
ds_read_b128 v[102:105], v11 offset:7168
mav 66,160,112
mva 70,112,164
s_add_u32 m0, s66, 5888
s_nop 0
buffer_load_dwordx4 v3, s[44:47], 0 offen offset:1280 lds
mav 14,156,42
mav 18,160,42
E 1,77824
mva 22,42,164
mav 26,156,50
E 2,81920
s_add_u32 s51, s51, 196608
mav 30,160,50
mva 34,50,164
s_waitcnt lgkmcnt(0)
s_waitcnt vmcnt(14)
s_barrier
v_add_u32 v4, 49152, v4
v_add_u32 v5, 49152, v5
v_add_u32 v6, 49152, v6
E 0,36864
Ta 192,4,40960
mav 62,168,112
mav 66,172,112
Ta 196,5,40960
mva 70,112,176
Ta 200,6,40960
LsQ0:
s_cbranch_execz LsQ4
E 1,40960
mav 14,168,54
Ta 204,4,53248
mav 18,172,54
Ta 208,5,53248
mva 22,54,176
Ta 212,6,53248
E 2,45056
s_add_u32 s51, s51, 196608
mav 26,168,98
ds_read_b128 v[38:41], v8 offset:0
mav 30,172,98
ds_read_b128 v[42:45], v9 offset:0
mva 34,98,176
ds_read_b128 v[46:49], v8 offset:4096
E 0,49152
mav 62,180,112
ds_read_b128 v[50:53], v9 offset:4096
mav 66,184,112
mva 70,112,188
E 1,53248
mav 14,180,58
mav 18,184,58
mva 22,58,188
E 2,57344
s_add_u32 s51, s51, 196608
mav 26,180,102
mav 30,184,102
mva 34,102,188
s_waitcnt lgkmcnt(0)
s_waitcnt vmcnt(14)
s_barrier
E 0,110592
Ta 216,4,16384
mav 62,192,112
mav 66,196,112
Ta 220,5,16384
mva 70,112,200
Ta 224,6,16384
E 1,114688
mav 14,192,38
Ta 228,4,28672
mav 18,196,38
Ta 232,5,28672
s_add_u32 m0, s66, 2560
s_nop 0
buffer_load_dwordx4 v3, s[44:47], s67 offen offset:1536 lds
mva 22,38,200
Ta 236,6,28672
mav 26,192,46
ds_read_b128 v[54:57], v10 offset:0
E 2,118784
s_add_u32 s51, s51, 196608
mav 30,196,46
ds_read_b128 v[58:61], v11 offset:0
mva 34,46,200
ds_read_b128 v[98:101], v10 offset:4096
E 0,122880
mav 62,204,112
ds_read_b128 v[102:105], v11 offset:4096
mav 66,208,112
mva 70,112,212
s_add_u32 m0, s66, 6656
s_nop 0
buffer_load_dwordx4 v3, s[44:47], 0 offen offset:1536 lds
mav 14,204,42
mav 18,208,42
E 1,126976
mva 22,42,212
mav 26,204,50
E 2,131072
s_add_u32 s51, s51, 196608
mav 30,208,50
mva 34,50,212
s_waitcnt lgkmcnt(0)
s_waitcnt vmcnt(14)
s_barrier
v_add_u32 v4, -49152, v4
v_add_u32 v5, -49152, v5
v_add_u32 v6, -49152, v6
E 0,86016
Ta 240,4,40960
mav 62,216,112
mav 66,220,112
Ta 244,5,40960
mva 70,112,224
Ta 248,6,40960
E 1,90112
mav 14,216,54
Ta 252,4,53248
mav 18,220,54
Tv 128,5,53248
mva 22,54,224
Tv 132,6,53248
E 2,94208
s_add_u32 s51, s51, 196608
mav 26,216,98
ds_read_b128 v[38:41], v8 offset:1024
mav 30,220,98
ds_read_b128 v[42:45], v9 offset:1024
mva 34,98,224
ds_read_b128 v[46:49], v8 offset:5120
E 0,98304
mav 62,228,112
ds_read_b128 v[50:53], v9 offset:5120
mav 66,232,112
mva 70,112,236
E 1,102400
mav 14,228,58
mav 18,232,58
mva 22,58,236
E 2,106496
s_add_u32 s51, s51, 196608
mav 26,228,102
mav 30,232,102
mva 34,102,236
s_waitcnt lgkmcnt(0)
s_waitcnt vmcnt(14)
s_barrier
E 0,61440
Tv 136,4,16384
mav 62,240,112
mav 66,244,112
Tv 140,5,16384
mva 70,112,248
Tv 144,6,16384
E 1,65536
mav 14,240,38
Tv 148,4,28672
mav 18,244,38
Tv 152,5,28672
s_add_u32 m0, s66, 3328
s_nop 0
buffer_load_dwordx4 v3, s[44:47], s67 offen offset:1792 lds
mva 22,38,248
Tv 156,6,28672
mav 26,240,46
ds_read_b128 v[54:57], v10 offset:1024
E 2,69632
s_add_u32 s51, s51, 196608
mav 30,244,46
ds_read_b128 v[58:61], v11 offset:1024
mva 34,46,248
ds_read_b128 v[98:101], v10 offset:5120
E 0,73728
mav 62,252,112
ds_read_b128 v[102:105], v11 offset:5120
mvv 66,128,112
mvv 70,112,132
s_add_u32 m0, s66, 7424
s_nop 0
buffer_load_dwordx4 v3, s[44:47], 0 offen offset:1792 lds
mav 14,252,42
mvv 18,128,42
E 1,77824
mvv 22,42,132
mav 26,252,50
E 2,81920
s_add_u32 s51, s51, 196608
mvv 30,128,50
mvv 34,50,132
s_waitcnt lgkmcnt(0)
s_waitcnt vmcnt(14)
s_barrier
v_add_u32 v4, 49152, v4
v_add_u32 v5, 49152, v5
v_add_u32 v6, 49152, v6
E 0,36864
Tv 160,4,40960
mvv 62,136,112
mvv 66,140,112
Tv 164,5,40960
mvv 70,112,144
Tv 168,6,40960
E 1,40960
mvv 14,136,54
Tv 172,4,53248
mvv 18,140,54
Tv 176,5,53248
mvv 22,54,144
Tv 180,6,53248
E 2,45056
s_add_u32 s51, s51, 196608
mvv 26,136,98
ds_read_b128 v[38:41], v8 offset:2048
mvv 30,140,98
ds_read_b128 v[42:45], v9 offset:2048
mvv 34,98,144
ds_read_b128 v[46:49], v8 offset:6144
E 0,49152
mvv 62,148,112
ds_read_b128 v[50:53], v9 offset:6144
mvv 66,152,112
mvv 70,112,156
E 1,53248
mvv 14,148,58
mvv 18,152,58
mvv 22,58,156
E 2,57344
s_add_u32 s51, s51, 196608
mvv 26,148,102
mvv 30,152,102
mvv 34,102,156
s_waitcnt lgkmcnt(0)
s_waitcnt vmcnt(14)
s_barrier
Tv 184,4,16384
mvv 62,160,112
mvv 66,164,112
Tv 188,5,16384
mvv 70,112,168
Tv 192,6,16384
mvv 14,160,38
Tv 196,4,28672
mvv 18,164,38
Tv 200,5,28672
mvv 22,38,168
Tv 204,6,28672
mvv 26,160,46
ds_read_b128 v[54:57], v10 offset:2048
mvv 30,164,46
ds_read_b128 v[58:61], v11 offset:2048
mvv 34,46,168
ds_read_b128 v[98:101], v10 offset:6144
mvv 62,172,112
ds_read_b128 v[102:105], v11 offset:6144
mvv 66,176,112
mvv 70,112,180
mvv 14,172,42
mvv 18,176,42
mvv 22,42,180
mvv 26,172,50
mvv 30,176,50
mvv 34,50,180
s_waitcnt lgkmcnt(0)
s_waitcnt vmcnt(6)
s_barrier
v_add_u32 v4, -49152, v4
v_add_u32 v5, -49152, v5
v_add_u32 v6, -49152, v6
Tv 208,4,40960
mvv 62,184,112
mvv 66,188,112
Tv 212,5,40960
mvv 70,112,192
Tv 216,6,40960
mvv 14,184,54
Tv 220,4,53248
mvv 18,188,54
Tv 224,5,53248
mvv 22,54,192
Tv 228,6,53248
mvv 26,184,98
ds_read_b128 v[38:41], v8 offset:3072
mvv 30,188,98
ds_read_b128 v[42:45], v9 offset:3072
mvv 34,98,192
ds_read_b128 v[46:49], v8 offset:7168
mvv 62,196,112
ds_read_b128 v[50:53], v9 offset:7168
mvv 66,200,112
mvv 70,112,204
mvv 14,196,58
mvv 18,200,58
mvv 22,58,204
mvv 26,196,102
mvv 30,200,102
mvv 34,102,204
s_waitcnt lgkmcnt(0)
s_waitcnt vmcnt(0)
s_barrier
Tv 232,4,16384
mvv 62,208,112
mvv 66,212,112
Tv 236,5,16384
mvv 70,112,216
Tv 240,6,16384
mvv 14,208,38
Tv 244,4,28672
mvv 18,212,38
Tv 248,5,28672
mvv 22,38,216
Tv 252,6,28672
mvv 26,208,46
ds_read_b128 v[54:57], v10 offset:3072
mvv 30,212,46
ds_read_b128 v[58:61], v11 offset:3072
mvv 34,46,216
ds_read_b128 v[98:101], v10 offset:7168
mvv 62,220,112
ds_read_b128 v[102:105], v11 offset:7168
mvv 66,224,112
mvv 70,112,228
mvv 14,220,42
mvv 18,224,42
mvv 22,42,228
mvv 26,220,50
mvv 30,224,50
mvv 34,50,228
s_waitcnt lgkmcnt(0)
mvv 62,232,112
mvv 66,236,112
mvv 70,112,240
mvv 14,232,54
mvv 18,236,54
mvv 22,54,240
mvv 26,232,98
mvv 30,236,98
mvv 34,98,240
mvv 62,244,112
mvv 66,248,112
mvv 70,112,252
mvv 14,244,58
mvv 18,248,58
mvv 22,58,252
mvv 26,244,102
mvv 30,248,102
mvv 34,102,252
s_barrier
s_nop 7
v_mov_b32 v97, v70
s_and_b32 s60, s48, 3
LvQA:
LsQ1:
s_cbranch_execz LsQ5
v_xor_b32 v80, 0x10000, v80
ds_read_b64 v[120:121], v82 offset:0
ds_read_b128 v[70:73], v83 offset:0
ds_read_b128 v[74:77], v83 offset:16
s_add_u32 s49, s49, 346112
R 38,32768
R 42,33792
R 46,34816
R 50,35840
s_waitcnt lgkmcnt(2)
v_mul_f32 v71, v71, v97
v_fma_f32 v22, v22, v70, v71
v_mul_f32 v73, v73, v97
v_fma_f32 v23, v23, v72, v73
v_mul_f32 v75, v75, v97
v_fma_f32 v24, v24, v74, v75
v_mul_f32 v77, v77, v97
v_fma_f32 v25, v25, v76, v77
v_cvt_pk_bf16_f32 v8, v22, v23
v_cvt_pk_bf16_f32 v9, v24, v25
v_mul_f32 v122, v121, v62
v_fma_f32 v14, v14, v120, v122
v_mul_f32 v123, v121, v63
v_fma_f32 v15, v15, v120, v123
v_mul_f32 v122, v121, v64
v_fma_f32 v16, v16, v120, v122
v_mul_f32 v123, v121, v65
v_fma_f32 v17, v17, v120, v123
v_mul_f32 v122, v121, v66
v_fma_f32 v18, v18, v120, v122
v_mul_f32 v123, v121, v67
v_fma_f32 v19, v19, v120, v123
v_mul_f32 v122, v121, v68
v_fma_f32 v20, v20, v120, v122
v_mul_f32 v123, v121, v69
v_fma_f32 v21, v21, v120, v123
v_cvt_pk_bf16_f32 v0, v14, v15
v_cvt_pk_bf16_f32 v1, v16, v17
v_cvt_pk_bf16_f32 v2, v18, v19
v_cvt_pk_bf16_f32 v3, v20, v21
s_add_u32 s49, s49, 346112
s_barrier
ds_read_b128 v[98:101], v81 offset:0
ds_read_b64 v[106:107], v81 offset:1024
ds_read_b64 v[120:121], v82 offset:128
ds_read_b128 v[70:73], v83 offset:128
ds_read_b128 v[74:77], v83 offset:144
v_mfma_f32_16x16x32_bf16 v[14:17], a[0:3], v[38:41], 0
R 54,36864
v_mfma_f32_16x16x32_bf16 v[18:21], a[4:7], v[38:41], 0
s_waitcnt lgkmcnt(1)
v_mul_f32 v71, v71, v97
v_mfma_f32_16x16x32_bf16 v[22:25], v[38:41], a[8:11], 0
v_fma_f32 v34, v34, v70, v71
v_mul_f32 v73, v73, v97
mav 14,12,42
R 58,37888
v_fma_f32 v35, v35, v72, v73
mav 18,16,42
v_mul_f32 v75, v75, v97
v_fma_f32 v36, v36, v74, v75
mva 22,42,20
v_mul_f32 v77, v77, v97
v_fma_f32 v37, v37, v76, v77
mav 14,24,46
R 38,38912
v_cvt_pk_bf16_f32 v10, v34, v35
mav 18,28,46
v_cvt_pk_bf16_f32 v11, v36, v37
v_mul_f32 v122, v121, v62
mva 22,46,32
v_mfma_f32_16x16x32_bf16 v[70:73], v[98:101], v[0:3], v[84:87]
v_fma_f32 v26, v26, v120, v122
v_mul_f32 v123, v121, v63
mav 14,36,50
R 42,39936
v_fma_f32 v27, v27, v120, v123
mav 18,40,50
v_mul_f32 v122, v121, v64
v_fma_f32 v28, v28, v120, v122
mva 22,50,44
v_mul_f32 v123, v121, v65
s_waitcnt lgkmcnt(2)
mav 14,48,54
R 46,40960
v_fma_f32 v29, v29, v120, v123
mav 18,52,54
v_mul_f32 v122, v121, v66
v_fma_f32 v30, v30, v120, v122
mva 22,54,56
v_mul_f32 v123, v121, v67
v_fma_f32 v31, v31, v120, v123
mav 14,60,58
R 50,41984
v_mul_f32 v122, v121, v68
mav 18,64,58
v_fma_f32 v32, v32, v120, v122
v_mul_f32 v123, v121, v69
mva 22,58,68
v_fma_f32 v33, v33, v120, v123
s_waitcnt lgkmcnt(2)
mav 14,72,38
R 54,43008
mav 18,76,38
buffer_store_dwordx4 v[98:101], v81, s[36:39], s54 offen sc1
buffer_store_dwordx2 v[8:9], v96, s[36:39], s54 offen offset:1024 sc1
mva 22,38,80
buffer_store_dwordx2 v[106:107], v96, s[36:39], s54 offen offset:1536 sc1
mav 14,84,42
R 58,44032
mav 18,88,42
mva 22,42,92
s_waitcnt lgkmcnt(2)
mav 14,96,46
R 38,45056
mav 18,100,46
s_barrier
ds_read_b128 v[102:105], v81 offset:4096
mva 22,46,104
ds_read_b64 v[108:109], v81 offset:5120
mav 14,108,50
R 42,46080
mav 18,112,50
mva 22,50,116
s_waitcnt lgkmcnt(4)
mav 14,120,54
R 46,47104
mav 18,124,54
mva 22,54,128
s_waitcnt lgkmcnt(3)
v_mfma_f32_16x16x32_bf16 v[74:77], v[102:105], v[0:3], v[88:91]
v_cvt_pk_bf16_f32 v0, v26, v27
LsQ2:
s_cbranch_execz LsQ6
v_cvt_pk_bf16_f32 v1, v28, v29
mav 14,132,58
R 50,48128
v_cvt_pk_bf16_f32 v2, v30, v31
mav 18,136,58
v_cvt_pk_bf16_f32 v3, v32, v33
mva 22,58,140
v_max3_f32 v116, v70, v71, v74
v_max3_f32 v117, v72, v73, v76
mav 14,144,38
R 54,49152
v_max_f32 v116, v116, v75
mav 18,148,38
v_max_f32 v117, v117, v77
v_max_f32 v118, v116, v117
mva 22,38,152
v_max_f32 v119, v116, v117
s_waitcnt lgkmcnt(2)
mav 14,156,42
R 38,50176
v_permlane32_swap_b32 v118, v119
mav 18,160,42
v_max_f32 v112, v118, v119
v_max_f32 v113, v118, v119
mva 22,42,164
mav 14,168,46
R 42,51200
v_permlane16_swap_b32 v112, v113
mav 18,172,46
v_max_f32 v118, v112, v113
v_mul_f32 v118, s65, v118
mva 22,46,176
v_fma_f32 v116, v116, s65, -v118
s_waitcnt lgkmcnt(2)
mav 14,180,50
R 46,52224
v_fma_f32 v117, v117, s65, -v118
mav 18,184,50
v_exp_f32 v116, v116
mva 22,50,188
v_exp_f32 v117, v117
mav 14,192,54
R 50,53248
v_add_f32 v118, v116, v117
mav 18,196,54
v_add_f32 v119, v116, v117
mva 22,54,200
s_waitcnt lgkmcnt(2)
mav 14,204,38
R 54,54272
v_permlane32_swap_b32 v118, v119
mav 18,208,38
v_add_f32 v112, v118, v119
v_add_f32 v113, v118, v119
mva 22,38,212
mav 14,216,42
R 38,55296
v_permlane16_swap_b32 v112, v113
mav 18,220,42
v_add_f32 v118, v112, v113
mva 22,42,224
s_waitcnt lgkmcnt(2)
mav 14,228,46
R 42,56320
mav 18,232,46
v_rcp_f32 v118, v118
mva 22,46,236
v_mul_f32 v116, v116, v118
v_mul_f32 v117, v117, v118
mav 14,240,50
R 46,57344
v_cvt_pk_bf16_f32 v116, v116, v116
mav 18,244,50
v_cvt_pk_bf16_f32 v117, v117, v117
v_and_b32 v112, v116, v92
mva 22,50,248
v_and_b32 v113, v117, v93
s_waitcnt lgkmcnt(2)
mav 14,252,54
R 50,58368
v_and_b32 v114, v116, v94
mvv 18,128,54
v_and_b32 v115, v117, v95
mvv 22,54,132
mvv 14,136,38
R 54,59392
v_mfma_f32_16x16x32_bf16 v[70:73], v[8:11], v[112:115], 0
v_mfma_f32_16x16x32_bf16 v[74:77], v[106:109], v[112:115], 0
mvv 18,140,38
mvv 22,38,144
s_waitcnt lgkmcnt(2)
mvv 14,148,42
R 38,60416
mvv 18,152,42
mvv 22,42,156
v_cvt_pk_bf16_f32 v112, v70, v71
v_cvt_pk_bf16_f32 v113, v72, v73
mvv 14,160,46
R 42,61440
v_cvt_pk_bf16_f32 v114, v74, v75
mvv 18,164,46
v_cvt_pk_bf16_f32 v115, v76, v77
buffer_store_dwordx4 v[112:115], v81, s[36:39], s49 offen sc1
mvv 22,46,168
s_add_u32 s49, s49, 346112
s_waitcnt lgkmcnt(2)
mvv 14,172,50
R 46,62464
mvv 18,176,50
mvv 22,50,180
mvv 14,184,54
R 50,63488
mvv 18,188,54
mvv 22,54,192
s_waitcnt lgkmcnt(2)
mvv 14,196,38
R 54,64512
mvv 18,200,38
mvv 22,38,204
mvv 14,208,42
R 38,0
mvv 18,212,42
ds_read_b64 v[120:121], v82 offset:0
ds_read_b128 v[70:73], v83 offset:0
mvv 22,42,216
ds_read_b128 v[74:77], v83 offset:16
s_waitcnt lgkmcnt(5)
mvv 14,220,46
R 42,1024
mvv 18,224,46
mvv 22,46,228
mvv 14,232,50
R 46,2048
mvv 18,236,50
mvv 22,50,240
s_waitcnt lgkmcnt(2)
mvv 14,244,54
R 50,3072
mvv 18,248,54
mvv 22,54,252
v_mfma_f32_16x16x32_bf16 v[26:29], a[0:3], v[38:41], 0
R 54,4096
v_mul_f32 v71, v71, v97
v_mfma_f32_16x16x32_bf16 v[30:33], a[4:7], v[38:41], 0
v_mul_f32 v73, v73, v97
v_mul_f32 v75, v75, v97
v_mfma_f32_16x16x32_bf16 v[34:37], v[38:41], a[8:11], 0
v_mul_f32 v77, v77, v97
s_waitcnt lgkmcnt(2)
mav 26,12,42
R 58,5120
v_fma_f32 v22, v22, v70, v71
mav 30,16,42
v_fma_f32 v23, v23, v72, v73
v_fma_f32 v24, v24, v74, v75
mva 34,42,20
v_fma_f32 v25, v25, v76, v77
v_cvt_pk_bf16_f32 v8, v22, v23
mav 26,24,46
R 38,6144
v_cvt_pk_bf16_f32 v9, v24, v25
mav 30,28,46
v_mul_f32 v122, v121, v62
v_mfma_f32_16x16x32_bf16 v[70:73], v[102:105], v[0:3], v[84:87]
v_fma_f32 v14, v14, v120, v122
v_mul_f32 v123, v121, v63
mva 34,46,32
v_fma_f32 v15, v15, v120, v123
s_waitcnt lgkmcnt(2)
mav 26,36,50
R 42,7168
v_mul_f32 v122, v121, v64
mav 30,40,50
v_fma_f32 v16, v16, v120, v122
v_mul_f32 v123, v121, v65
mva 34,50,44
v_fma_f32 v17, v17, v120, v123
v_mul_f32 v122, v121, v66
mav 26,48,54
R 46,8192
v_fma_f32 v18, v18, v120, v122
mav 30,52,54
v_mul_f32 v123, v121, v67
v_fma_f32 v19, v19, v120, v123
mva 34,54,56
v_mul_f32 v122, v121, v68
s_waitcnt lgkmcnt(2)
mav 26,60,58
R 50,9216
v_fma_f32 v20, v20, v120, v122
mav 30,64,58
v_mul_f32 v123, v121, v69
v_fma_f32 v21, v21, v120, v123
mva 34,58,68
v_add_u32 v78, 1, v78
mav 26,72,38
R 54,10240
mav 30,76,38
mva 34,38,80
s_waitcnt lgkmcnt(2)
mav 26,84,42
R 58,11264
mav 30,88,42
mva 34,42,92
mav 26,96,46
R 38,12288
mav 30,100,46
s_barrier
ds_read_b128 v[98:101], v81 offset:0
mva 34,46,104
ds_read_b64 v[106:107], v81 offset:1024
s_waitcnt lgkmcnt(4)
mav 26,108,50
R 42,13312
mav 30,112,50
mva 34,50,116
mav 26,120,54
R 46,14336
mav 30,124,54
s_waitcnt vmcnt(0)
buffer_store_dwordx2 v[78:79], off, s[36:39], s52 sc1
mva 34,54,128
s_waitcnt lgkmcnt(3)
v_mfma_f32_16x16x32_bf16 v[74:77], v[98:101], v[0:3], v[88:91]
v_cvt_pk_bf16_f32 v0, v14, v15
v_cvt_pk_bf16_f32 v1, v16, v17
mav 26,132,58
R 50,15360
v_cvt_pk_bf16_f32 v2, v18, v19
mav 30,136,58
v_cvt_pk_bf16_f32 v3, v20, v21
mva 34,58,140
v_max3_f32 v116, v70, v71, v74
v_max3_f32 v117, v72, v73, v76
mav 26,144,38
R 54,16384
v_max_f32 v116, v116, v75
mav 30,148,38
v_max_f32 v117, v117, v77
v_max_f32 v118, v116, v117
mva 34,38,152
v_max_f32 v119, v116, v117
s_waitcnt lgkmcnt(2)
mav 26,156,42
R 38,17408
v_permlane32_swap_b32 v118, v119
mav 30,160,42
v_max_f32 v112, v118, v119
v_max_f32 v113, v118, v119
mva 34,42,164
mav 26,168,46
R 42,18432
v_permlane16_swap_b32 v112, v113
mav 30,172,46
v_max_f32 v118, v112, v113
v_mul_f32 v118, s65, v118
mva 34,46,176
v_fma_f32 v116, v116, s65, -v118
s_waitcnt lgkmcnt(2)
mav 26,180,50
R 46,19456
v_fma_f32 v117, v117, s65, -v118
mav 30,184,50
v_exp_f32 v116, v116
mva 34,50,188
v_exp_f32 v117, v117
mav 26,192,54
R 50,20480
v_add_f32 v118, v116, v117
mav 30,196,54
v_add_f32 v119, v116, v117
mva 34,54,200
s_waitcnt lgkmcnt(2)
mav 26,204,38
R 54,21504
v_permlane32_swap_b32 v118, v119
mav 30,208,38
v_add_f32 v112, v118, v119
v_add_f32 v113, v118, v119
mva 34,38,212
mav 26,216,42
R 38,22528
v_permlane16_swap_b32 v112, v113
mav 30,220,42
v_add_f32 v118, v112, v113
mva 34,42,224
s_waitcnt lgkmcnt(2)
mav 26,228,46
R 42,23552
mav 30,232,46
v_rcp_f32 v118, v118
mva 34,46,236
v_mul_f32 v116, v116, v118
v_mul_f32 v117, v117, v118
mav 26,240,50
R 46,24576
v_cvt_pk_bf16_f32 v116, v116, v116
mav 30,244,50
v_cvt_pk_bf16_f32 v117, v117, v117
v_and_b32 v112, v116, v94
mva 34,50,248
v_and_b32 v113, v117, v95
s_waitcnt lgkmcnt(2)
mav 26,252,54
R 50,25600
v_and_b32 v114, v116, v92
mvv 30,128,54
v_and_b32 v115, v117, v93
mvv 34,54,132
mvv 26,136,38
R 54,26624
v_mfma_f32_16x16x32_bf16 v[70:73], v[8:11], v[112:115], 0
v_mfma_f32_16x16x32_bf16 v[74:77], v[106:109], v[112:115], 0
mvv 30,140,38
mvv 34,38,144
s_waitcnt lgkmcnt(2)
mvv 26,148,42
R 38,27648
mvv 30,152,42
mvv 34,42,156
v_cvt_pk_bf16_f32 v112, v70, v71
v_cvt_pk_bf16_f32 v113, v72, v73
mvv 26,160,46
R 42,28672
v_cvt_pk_bf16_f32 v114, v74, v75
mvv 30,164,46
v_cvt_pk_bf16_f32 v115, v76, v77
buffer_store_dwordx4 v[112:115], v81, s[36:39], s49 offen sc1
mvv 34,46,168
s_add_u32 s49, s49, 346112
s_waitcnt lgkmcnt(2)
mvv 26,172,50
R 46,29696
mvv 30,176,50
mvv 34,50,180
mvv 26,184,54
R 50,30720
mvv 30,188,54
mvv 34,54,192
s_waitcnt lgkmcnt(2)
mvv 26,196,38
R 54,31744
v_xor_b32 v80, 0x10000, v80
mvv 30,200,38
mvv 34,38,204
mvv 26,208,42
R 38,32768
mvv 30,212,42
ds_read_b64 v[120:121], v82 offset:128
ds_read_b128 v[70:73], v83 offset:128
mvv 34,42,216
ds_read_b128 v[74:77], v83 offset:144
s_waitcnt lgkmcnt(5)
mvv 26,220,46
R 42,33792
mvv 30,224,46
mvv 34,46,228
mvv 26,232,50
R 46,34816
mvv 30,236,50
mvv 34,50,240
s_waitcnt lgkmcnt(2)
mvv 26,244,54
R 50,35840
mvv 30,248,54
mvv 34,54,252
s_mov_b32 s60, -13
.p2align 6
LQA_1:
v_mfma_f32_16x16x32_bf16 v[14:17], a[0:3], v[38:41], 0
R 54,36864
v_mul_f32 v71, v71, v97
v_mfma_f32_16x16x32_bf16 v[18:21], a[4:7], v[38:41], 0
v_mul_f32 v73, v73, v97
v_mul_f32 v75, v75, v97
v_mfma_f32_16x16x32_bf16 v[22:25], v[38:41], a[8:11], 0
v_mul_f32 v77, v77, v97
s_waitcnt lgkmcnt(2)
mav 14,12,42
R 58,37888
v_fma_f32 v34, v34, v70, v71
mav 18,16,42
v_fma_f32 v35, v35, v72, v73
v_fma_f32 v36, v36, v74, v75
mva 22,42,20
v_fma_f32 v37, v37, v76, v77
v_cvt_pk_bf16_f32 v10, v34, v35
mav 14,24,46
R 38,38912
v_cvt_pk_bf16_f32 v11, v36, v37
mav 18,28,46
v_mul_f32 v122, v121, v62
v_mfma_f32_16x16x32_bf16 v[70:73], v[98:101], v[0:3], v[84:87]
v_fma_f32 v26, v26, v120, v122
v_mul_f32 v123, v121, v63
mva 22,46,32
v_fma_f32 v27, v27, v120, v123
s_waitcnt lgkmcnt(2)
mav 14,36,50
R 42,39936
v_mul_f32 v122, v121, v64
mav 18,40,50
v_fma_f32 v28, v28, v120, v122
v_mul_f32 v123, v121, v65
mva 22,50,44
v_fma_f32 v29, v29, v120, v123
v_mul_f32 v122, v121, v66
mav 14,48,54
R 46,40960
v_fma_f32 v30, v30, v120, v122
mav 18,52,54
v_mul_f32 v123, v121, v67
v_fma_f32 v31, v31, v120, v123
mva 22,54,56
v_mul_f32 v122, v121, v68
s_waitcnt lgkmcnt(2)
mav 14,60,58
R 50,41984
v_fma_f32 v32, v32, v120, v122
mav 18,64,58
v_mul_f32 v123, v121, v69
v_fma_f32 v33, v33, v120, v123
mva 22,58,68
v_add_u32 v78, 1, v78
mav 14,72,38
R 54,43008
mav 18,76,38
mva 22,38,80
s_waitcnt lgkmcnt(2)
mav 14,84,42
R 58,44032
mav 18,88,42
mva 22,42,92
mav 14,96,46
R 38,45056
mav 18,100,46
s_barrier
ds_read_b128 v[102:105], v81 offset:4096
mva 22,46,104
ds_read_b64 v[108:109], v81 offset:5120
s_waitcnt lgkmcnt(4)
mav 14,108,50
R 42,46080
mav 18,112,50
mva 22,50,116
mav 14,120,54
R 46,47104
mav 18,124,54
s_waitcnt vmcnt(0)
buffer_store_dwordx2 v[78:79], off, s[36:39], s52 sc1
mva 22,54,128
s_waitcnt lgkmcnt(3)
v_mfma_f32_16x16x32_bf16 v[74:77], v[102:105], v[0:3], v[88:91]
v_cvt_pk_bf16_f32 v0, v26, v27
v_cvt_pk_bf16_f32 v1, v28, v29
mav 14,132,58
R 50,48128
v_cvt_pk_bf16_f32 v2, v30, v31
mav 18,136,58
v_cvt_pk_bf16_f32 v3, v32, v33
mva 22,58,140
v_max3_f32 v116, v70, v71, v74
v_max3_f32 v117, v72, v73, v76
mav 14,144,38
R 54,49152
v_max_f32 v116, v116, v75
mav 18,148,38
v_max_f32 v117, v117, v77
v_max_f32 v118, v116, v117
mva 22,38,152
v_max_f32 v119, v116, v117
s_waitcnt lgkmcnt(2)
mav 14,156,42
R 38,50176
v_permlane32_swap_b32 v118, v119
mav 18,160,42
v_max_f32 v112, v118, v119
v_max_f32 v113, v118, v119
mva 22,42,164
mav 14,168,46
R 42,51200
v_permlane16_swap_b32 v112, v113
mav 18,172,46
v_max_f32 v118, v112, v113
v_mul_f32 v118, s65, v118
mva 22,46,176
v_fma_f32 v116, v116, s65, -v118
s_waitcnt lgkmcnt(2)
mav 14,180,50
R 46,52224
v_fma_f32 v117, v117, s65, -v118
mav 18,184,50
v_exp_f32 v116, v116
mva 22,50,188
v_exp_f32 v117, v117
mav 14,192,54
R 50,53248
v_add_f32 v118, v116, v117
mav 18,196,54
v_add_f32 v119, v116, v117
mva 22,54,200
s_waitcnt lgkmcnt(2)
mav 14,204,38
R 54,54272
v_permlane32_swap_b32 v118, v119
mav 18,208,38
v_add_f32 v112, v118, v119
v_add_f32 v113, v118, v119
mva 22,38,212
mav 14,216,42
R 38,55296
v_permlane16_swap_b32 v112, v113
mav 18,220,42
v_add_f32 v118, v112, v113
mva 22,42,224
s_waitcnt lgkmcnt(2)
mav 14,228,46
R 42,56320
mav 18,232,46
v_rcp_f32 v118, v118
mva 22,46,236
v_mul_f32 v116, v116, v118
v_mul_f32 v117, v117, v118
mav 14,240,50
R 46,57344
v_cvt_pk_bf16_f32 v116, v116, v116
mav 18,244,50
v_cvt_pk_bf16_f32 v117, v117, v117
v_and_b32 v112, v116, v92
mva 22,50,248
v_and_b32 v113, v117, v93
s_waitcnt lgkmcnt(2)
mav 14,252,54
R 50,58368
v_and_b32 v114, v116, v94
mvv 18,128,54
v_and_b32 v115, v117, v95
mvv 22,54,132
mvv 14,136,38
R 54,59392
v_mfma_f32_16x16x32_bf16 v[70:73], v[8:11], v[112:115], 0
v_mfma_f32_16x16x32_bf16 v[74:77], v[106:109], v[112:115], 0
mvv 18,140,38
mvv 22,38,144
s_waitcnt lgkmcnt(2)
mvv 14,148,42
R 38,60416
mvv 18,152,42
mvv 22,42,156
v_cvt_pk_bf16_f32 v112, v70, v71
v_cvt_pk_bf16_f32 v113, v72, v73
mvv 14,160,46
R 42,61440
v_cvt_pk_bf16_f32 v114, v74, v75
mvv 18,164,46
v_cvt_pk_bf16_f32 v115, v76, v77
buffer_store_dwordx4 v[112:115], v81, s[36:39], s49 offen sc1
mvv 22,46,168
s_add_u32 s49, s49, 346112
s_waitcnt lgkmcnt(2)
mvv 14,172,50
R 46,62464
mvv 18,176,50
mvv 22,50,180
mvv 14,184,54
R 50,63488
mvv 18,188,54
mvv 22,54,192
s_waitcnt lgkmcnt(2)
mvv 14,196,38
R 54,64512
mvv 18,200,38
mvv 22,38,204
mvv 14,208,42
R 38,0
mvv 18,212,42
ds_read_b64 v[120:121], v82 offset:0
ds_read_b128 v[70:73], v83 offset:0
mvv 22,42,216
ds_read_b128 v[74:77], v83 offset:16
s_waitcnt lgkmcnt(5)
LsQ3:
s_cbranch_execz LsQ7
mvv 14,220,46
R 42,1024
mvv 18,224,46
mvv 22,46,228
mvv 14,232,50
R 46,2048
mvv 18,236,50
mvv 22,50,240
s_waitcnt lgkmcnt(2)
mvv 14,244,54
R 50,3072
mvv 18,248,54
mvv 22,54,252
v_mfma_f32_16x16x32_bf16 v[26:29], a[0:3], v[38:41], 0
R 54,4096
v_mul_f32 v71, v71, v97
v_mfma_f32_16x16x32_bf16 v[30:33], a[4:7], v[38:41], 0
v_mul_f32 v73, v73, v97
v_mul_f32 v75, v75, v97
v_mfma_f32_16x16x32_bf16 v[34:37], v[38:41], a[8:11], 0
v_mul_f32 v77, v77, v97
s_waitcnt lgkmcnt(2)
mav 26,12,42
R 58,5120
v_fma_f32 v22, v22, v70, v71
mav 30,16,42
v_fma_f32 v23, v23, v72, v73
v_fma_f32 v24, v24, v74, v75
mva 34,42,20
v_fma_f32 v25, v25, v76, v77
v_cvt_pk_bf16_f32 v8, v22, v23
mav 26,24,46
R 38,6144
v_cvt_pk_bf16_f32 v9, v24, v25
mav 30,28,46
v_mul_f32 v122, v121, v62
v_mfma_f32_16x16x32_bf16 v[70:73], v[102:105], v[0:3], v[84:87]
v_fma_f32 v14, v14, v120, v122
v_mul_f32 v123, v121, v63
mva 34,46,32
v_fma_f32 v15, v15, v120, v123
s_waitcnt lgkmcnt(2)
mav 26,36,50
R 42,7168
v_mul_f32 v122, v121, v64
mav 30,40,50
v_fma_f32 v16, v16, v120, v122
v_mul_f32 v123, v121, v65
mva 34,50,44
v_fma_f32 v17, v17, v120, v123
v_mul_f32 v122, v121, v66
mav 26,48,54
R 46,8192
v_fma_f32 v18, v18, v120, v122
mav 30,52,54
v_mul_f32 v123, v121, v67
v_fma_f32 v19, v19, v120, v123
mva 34,54,56
v_mul_f32 v122, v121, v68
s_waitcnt lgkmcnt(2)
mav 26,60,58
R 50,9216
v_fma_f32 v20, v20, v120, v122
mav 30,64,58
v_mul_f32 v123, v121, v69
v_fma_f32 v21, v21, v120, v123
mva 34,58,68
v_add_u32 v78, 1, v78
mav 26,72,38
R 54,10240
mav 30,76,38
mva 34,38,80
s_waitcnt lgkmcnt(2)
mav 26,84,42
R 58,11264
mav 30,88,42
mva 34,42,92
mav 26,96,46
R 38,12288
mav 30,100,46
s_barrier
ds_read_b128 v[98:101], v81 offset:0
mva 34,46,104
ds_read_b64 v[106:107], v81 offset:1024
s_waitcnt lgkmcnt(4)
mav 26,108,50
R 42,13312
mav 30,112,50
mva 34,50,116
mav 26,120,54
R 46,14336
mav 30,124,54
s_waitcnt vmcnt(0)
buffer_store_dwordx2 v[78:79], off, s[36:39], s52 sc1
mva 34,54,128
s_waitcnt lgkmcnt(3)
v_mfma_f32_16x16x32_bf16 v[74:77], v[98:101], v[0:3], v[88:91]
v_cvt_pk_bf16_f32 v0, v14, v15
v_cvt_pk_bf16_f32 v1, v16, v17
mav 26,132,58
R 50,15360
v_cvt_pk_bf16_f32 v2, v18, v19
mav 30,136,58
v_cvt_pk_bf16_f32 v3, v20, v21
mva 34,58,140
v_max3_f32 v116, v70, v71, v74
v_max3_f32 v117, v72, v73, v76
mav 26,144,38
R 54,16384
v_max_f32 v116, v116, v75
mav 30,148,38
v_max_f32 v117, v117, v77
v_max_f32 v118, v116, v117
mva 34,38,152
v_max_f32 v119, v116, v117
s_waitcnt lgkmcnt(2)
mav 26,156,42
R 38,17408
v_permlane32_swap_b32 v118, v119
mav 30,160,42
v_max_f32 v112, v118, v119
v_max_f32 v113, v118, v119
mva 34,42,164
mav 26,168,46
R 42,18432
v_permlane16_swap_b32 v112, v113
mav 30,172,46
v_max_f32 v118, v112, v113
v_mul_f32 v118, s65, v118
mva 34,46,176
v_fma_f32 v116, v116, s65, -v118
s_waitcnt lgkmcnt(2)
mav 26,180,50
R 46,19456
v_fma_f32 v117, v117, s65, -v118
mav 30,184,50
v_exp_f32 v116, v116
mva 34,50,188
v_exp_f32 v117, v117
mav 26,192,54
R 50,20480
v_add_f32 v118, v116, v117
mav 30,196,54
v_add_f32 v119, v116, v117
mva 34,54,200
s_waitcnt lgkmcnt(2)
mav 26,204,38
R 54,21504
v_permlane32_swap_b32 v118, v119
mav 30,208,38
v_add_f32 v112, v118, v119
v_add_f32 v113, v118, v119
mva 34,38,212
mav 26,216,42
R 38,22528
v_permlane16_swap_b32 v112, v113
mav 30,220,42
v_add_f32 v118, v112, v113
mva 34,42,224
s_waitcnt lgkmcnt(2)
mav 26,228,46
R 42,23552
mav 30,232,46
v_rcp_f32 v118, v118
mva 34,46,236
v_mul_f32 v116, v116, v118
v_mul_f32 v117, v117, v118
mav 26,240,50
R 46,24576
v_cvt_pk_bf16_f32 v116, v116, v116
mav 30,244,50
v_cvt_pk_bf16_f32 v117, v117, v117
v_and_b32 v112, v116, v94
mva 34,50,248
v_and_b32 v113, v117, v95
s_waitcnt lgkmcnt(2)
mav 26,252,54
R 50,25600
v_and_b32 v114, v116, v92
mvv 30,128,54
v_and_b32 v115, v117, v93
mvv 34,54,132
mvv 26,136,38
R 54,26624
v_mfma_f32_16x16x32_bf16 v[70:73], v[8:11], v[112:115], 0
v_mfma_f32_16x16x32_bf16 v[74:77], v[106:109], v[112:115], 0
mvv 30,140,38
mvv 34,38,144
s_waitcnt lgkmcnt(2)
mvv 26,148,42
R 38,27648
mvv 30,152,42
mvv 34,42,156
v_cvt_pk_bf16_f32 v112, v70, v71
v_cvt_pk_bf16_f32 v113, v72, v73
mvv 26,160,46
R 42,28672
v_cvt_pk_bf16_f32 v114, v74, v75
mvv 30,164,46
v_cvt_pk_bf16_f32 v115, v76, v77
buffer_store_dwordx4 v[112:115], v81, s[36:39], s49 offen sc1
mvv 34,46,168
s_add_u32 s49, s49, 346112
s_waitcnt lgkmcnt(2)
mvv 26,172,50
R 46,29696
mvv 30,176,50
mvv 34,50,180
mvv 26,184,54
R 50,30720
mvv 30,188,54
mvv 34,54,192
s_waitcnt lgkmcnt(2)
mvv 26,196,38
R 54,31744
v_xor_b32 v80, 0x10000, v80
mvv 30,200,38
mvv 34,38,204
mvv 26,208,42
R 38,32768
mvv 30,212,42
ds_read_b64 v[120:121], v82 offset:128
ds_read_b128 v[70:73], v83 offset:128
mvv 34,42,216
ds_read_b128 v[74:77], v83 offset:144
s_waitcnt lgkmcnt(5)
mvv 26,220,46
R 42,33792
mvv 30,224,46
mvv 34,46,228
mvv 26,232,50
R 46,34816
mvv 30,236,50
mvv 34,50,240
s_waitcnt lgkmcnt(2)
mvv 26,244,54
R 50,35840
mvv 30,248,54
mvv 34,54,252
s_add_u32 s60, s60, 1
s_cbranch_scc0 LQA_1
v_mfma_f32_16x16x32_bf16 v[14:17], a[0:3], v[38:41], 0
R 54,36864
v_mul_f32 v71, v71, v97
v_mfma_f32_16x16x32_bf16 v[18:21], a[4:7], v[38:41], 0
v_mul_f32 v73, v73, v97
v_mul_f32 v75, v75, v97
v_mfma_f32_16x16x32_bf16 v[22:25], v[38:41], a[8:11], 0
v_mul_f32 v77, v77, v97
s_waitcnt lgkmcnt(2)
mav 14,12,42
R 58,37888
v_fma_f32 v34, v34, v70, v71
mav 18,16,42
v_fma_f32 v35, v35, v72, v73
v_fma_f32 v36, v36, v74, v75
mva 22,42,20
v_fma_f32 v37, v37, v76, v77
v_cvt_pk_bf16_f32 v10, v34, v35
mav 14,24,46
R 38,38912
v_cvt_pk_bf16_f32 v11, v36, v37
mav 18,28,46
v_mul_f32 v122, v121, v62
v_mfma_f32_16x16x32_bf16 v[70:73], v[98:101], v[0:3], v[84:87]
v_fma_f32 v26, v26, v120, v122
v_mul_f32 v123, v121, v63
mva 22,46,32
v_fma_f32 v27, v27, v120, v123
s_waitcnt lgkmcnt(2)
mav 14,36,50
R 42,39936
v_mul_f32 v122, v121, v64
mav 18,40,50
v_fma_f32 v28, v28, v120, v122
v_mul_f32 v123, v121, v65
mva 22,50,44
v_fma_f32 v29, v29, v120, v123
v_mul_f32 v122, v121, v66
mav 14,48,54
R 46,40960
v_fma_f32 v30, v30, v120, v122
mav 18,52,54
v_mul_f32 v123, v121, v67
v_fma_f32 v31, v31, v120, v123
mva 22,54,56
v_mul_f32 v122, v121, v68
s_waitcnt lgkmcnt(2)
mav 14,60,58
R 50,41984
v_fma_f32 v32, v32, v120, v122
mav 18,64,58
v_mul_f32 v123, v121, v69
v_fma_f32 v33, v33, v120, v123
mva 22,58,68
v_add_u32 v78, 1, v78
mav 14,72,38
R 54,43008
mav 18,76,38
mva 22,38,80
s_waitcnt lgkmcnt(2)
mav 14,84,42
R 58,44032
mav 18,88,42
mva 22,42,92
mav 14,96,46
R 38,45056
mav 18,100,46
s_barrier
ds_read_b128 v[102:105], v81 offset:4096
mva 22,46,104
ds_read_b64 v[108:109], v81 offset:5120
s_waitcnt lgkmcnt(4)
mav 14,108,50
R 42,46080
mav 18,112,50
mva 22,50,116
mav 14,120,54
R 46,47104
mav 18,124,54
s_waitcnt vmcnt(0)
buffer_store_dwordx2 v[78:79], off, s[36:39], s52 sc1
mva 22,54,128
s_waitcnt lgkmcnt(3)
v_mfma_f32_16x16x32_bf16 v[74:77], v[102:105], v[0:3], v[88:91]
v_cvt_pk_bf16_f32 v0, v26, v27
v_cvt_pk_bf16_f32 v1, v28, v29
mav 14,132,58
R 50,48128
v_cvt_pk_bf16_f32 v2, v30, v31
mav 18,136,58
v_cvt_pk_bf16_f32 v3, v32, v33
mva 22,58,140
v_max3_f32 v116, v70, v71, v74
v_max3_f32 v117, v72, v73, v76
mav 14,144,38
R 54,49152
v_max_f32 v116, v116, v75
mav 18,148,38
v_max_f32 v117, v117, v77
v_max_f32 v118, v116, v117
mva 22,38,152
v_max_f32 v119, v116, v117
s_waitcnt lgkmcnt(2)
mav 14,156,42
R 38,50176
v_permlane32_swap_b32 v118, v119
mav 18,160,42
v_max_f32 v112, v118, v119
v_max_f32 v113, v118, v119
mva 22,42,164
mav 14,168,46
R 42,51200
v_permlane16_swap_b32 v112, v113
mav 18,172,46
v_max_f32 v118, v112, v113
v_mul_f32 v118, s65, v118
mva 22,46,176
v_fma_f32 v116, v116, s65, -v118
s_waitcnt lgkmcnt(2)
mav 14,180,50
R 46,52224
v_fma_f32 v117, v117, s65, -v118
mav 18,184,50
v_exp_f32 v116, v116
mva 22,50,188
v_exp_f32 v117, v117
mav 14,192,54
R 50,53248
v_add_f32 v118, v116, v117
mav 18,196,54
v_add_f32 v119, v116, v117
mva 22,54,200
s_waitcnt lgkmcnt(2)
mav 14,204,38
R 54,54272
v_permlane32_swap_b32 v118, v119
mav 18,208,38
v_add_f32 v112, v118, v119
v_add_f32 v113, v118, v119
mva 22,38,212
mav 14,216,42
R 38,55296
v_permlane16_swap_b32 v112, v113
mav 18,220,42
v_add_f32 v118, v112, v113
mva 22,42,224
s_waitcnt lgkmcnt(2)
mav 14,228,46
R 42,56320
mav 18,232,46
v_rcp_f32 v118, v118
mva 22,46,236
v_mul_f32 v116, v116, v118
v_mul_f32 v117, v117, v118
mav 14,240,50
R 46,57344
v_cvt_pk_bf16_f32 v116, v116, v116
mav 18,244,50
v_cvt_pk_bf16_f32 v117, v117, v117
v_and_b32 v112, v116, v92
mva 22,50,248
v_and_b32 v113, v117, v93
s_waitcnt lgkmcnt(2)
mav 14,252,54
R 50,58368
v_and_b32 v114, v116, v94
mvv 18,128,54
v_and_b32 v115, v117, v95
mvv 22,54,132
mvv 14,136,38
R 54,59392
v_mfma_f32_16x16x32_bf16 v[70:73], v[8:11], v[112:115], 0
v_mfma_f32_16x16x32_bf16 v[74:77], v[106:109], v[112:115], 0
mvv 18,140,38
mvv 22,38,144
s_waitcnt lgkmcnt(2)
mvv 14,148,42
R 38,60416
mvv 18,152,42
mvv 22,42,156
v_cvt_pk_bf16_f32 v112, v70, v71
v_cvt_pk_bf16_f32 v113, v72, v73
mvv 14,160,46
R 42,61440
v_cvt_pk_bf16_f32 v114, v74, v75
mvv 18,164,46
v_cvt_pk_bf16_f32 v115, v76, v77
buffer_store_dwordx4 v[112:115], v81, s[36:39], s49 offen sc1
mvv 22,46,168
s_add_u32 s49, s49, 346112
s_waitcnt lgkmcnt(2)
mvv 14,172,50
R 46,62464
mvv 18,176,50
mvv 22,50,180
mvv 14,184,54
R 50,63488
mvv 18,188,54
mvv 22,54,192
s_waitcnt lgkmcnt(2)
mvv 14,196,38
R 54,64512
mvv 18,200,38
mvv 22,38,204
mvv 14,208,42
R 38,0
mvv 18,212,42
ds_read_b64 v[120:121], v82 offset:0
ds_read_b128 v[70:73], v83 offset:0
mvv 22,42,216
ds_read_b128 v[74:77], v83 offset:16
s_waitcnt lgkmcnt(5)
mvv 14,220,46
R 42,1024
mvv 18,224,46
mvv 22,46,228
mvv 14,232,50
R 46,2048
mvv 18,236,50
mvv 22,50,240
s_waitcnt lgkmcnt(2)
mvv 14,244,54
R 50,3072
mvv 18,248,54
mvv 22,54,252
v_mfma_f32_16x16x32_bf16 v[26:29], a[0:3], v[38:41], 0
R 54,4096
v_mul_f32 v71, v71, v97
v_mfma_f32_16x16x32_bf16 v[30:33], a[4:7], v[38:41], 0
v_mul_f32 v73, v73, v97
v_mul_f32 v75, v75, v97
v_mfma_f32_16x16x32_bf16 v[34:37], v[38:41], a[8:11], 0
v_mul_f32 v77, v77, v97
s_waitcnt lgkmcnt(2)
mav 26,12,42
R 58,5120
v_fma_f32 v22, v22, v70, v71
mav 30,16,42
v_fma_f32 v23, v23, v72, v73
v_fma_f32 v24, v24, v74, v75
mva 34,42,20
v_fma_f32 v25, v25, v76, v77
v_cvt_pk_bf16_f32 v8, v22, v23
mav 26,24,46
R 38,6144
v_cvt_pk_bf16_f32 v9, v24, v25
mav 30,28,46
v_mul_f32 v122, v121, v62
v_mfma_f32_16x16x32_bf16 v[70:73], v[102:105], v[0:3], v[84:87]
v_fma_f32 v14, v14, v120, v122
v_mul_f32 v123, v121, v63
mva 34,46,32
v_fma_f32 v15, v15, v120, v123
s_waitcnt lgkmcnt(2)
mav 26,36,50
R 42,7168
v_mul_f32 v122, v121, v64
mav 30,40,50
buffer_load_dwordx4 v[4:7], v81, s[36:39], s55 offen sc1
buffer_load_dwordx2 v[12:13], v96, s[36:39], s55 offen offset:1024 sc1
mva 34,50,44
buffer_load_dwordx2 v[110:111], v96, s[36:39], s55 offen offset:1536 sc1
v_fma_f32 v16, v16, v120, v122
mav 26,48,54
R 46,8192
v_mul_f32 v123, v121, v65
mav 30,52,54
v_fma_f32 v17, v17, v120, v123
v_mul_f32 v122, v121, v66
mva 34,54,56
v_fma_f32 v18, v18, v120, v122
s_waitcnt lgkmcnt(2)
mav 26,60,58
R 50,9216
v_mul_f32 v123, v121, v67
mav 30,64,58
v_fma_f32 v19, v19, v120, v123
v_mul_f32 v122, v121, v68
mva 34,58,68
v_fma_f32 v20, v20, v120, v122
v_mul_f32 v123, v121, v69
mav 26,72,38
R 54,10240
v_fma_f32 v21, v21, v120, v123
)"
R"(
mav 30,76,38
v_add_u32 v78, 1, v78
mva 34,38,80
s_waitcnt lgkmcnt(2)
mav 26,84,42
R 58,11264
mav 30,88,42
mva 34,42,92
mav 26,96,46
R 38,12288
mav 30,100,46
s_barrier
ds_read_b128 v[98:101], v81 offset:0
mva 34,46,104
ds_read_b64 v[106:107], v81 offset:1024
s_waitcnt lgkmcnt(4)
mav 26,108,50
R 42,13312
mav 30,112,50
mva 34,50,116
mav 26,120,54
R 46,14336
mav 30,124,54
s_waitcnt vmcnt(3)
buffer_store_dwordx2 v[78:79], off, s[36:39], s52 sc1
mva 34,54,128
s_waitcnt lgkmcnt(3)
v_mfma_f32_16x16x32_bf16 v[74:77], v[98:101], v[0:3], v[88:91]
v_cvt_pk_bf16_f32 v0, v14, v15
v_cvt_pk_bf16_f32 v1, v16, v17
mav 26,132,58
R 50,15360
v_cvt_pk_bf16_f32 v2, v18, v19
mav 30,136,58
v_cvt_pk_bf16_f32 v3, v20, v21
mva 34,58,140
v_max3_f32 v116, v70, v71, v74
v_max3_f32 v117, v72, v73, v76
mav 26,144,38
R 54,16384
v_max_f32 v116, v116, v75
mav 30,148,38
v_max_f32 v117, v117, v77
v_max_f32 v118, v116, v117
mva 34,38,152
v_max_f32 v119, v116, v117
s_waitcnt lgkmcnt(2)
mav 26,156,42
R 38,17408
v_permlane32_swap_b32 v118, v119
mav 30,160,42
v_max_f32 v112, v118, v119
v_max_f32 v113, v118, v119
mva 34,42,164
mav 26,168,46
R 42,18432
v_permlane16_swap_b32 v112, v113
mav 30,172,46
v_max_f32 v118, v112, v113
v_mul_f32 v118, s65, v118
mva 34,46,176
LsQ4:
s_cbranch_execz LsQ8
v_fma_f32 v116, v116, s65, -v118
s_waitcnt lgkmcnt(2)
mav 26,180,50
R 46,19456
v_fma_f32 v117, v117, s65, -v118
mav 30,184,50
v_exp_f32 v116, v116
mva 34,50,188
v_exp_f32 v117, v117
mav 26,192,54
R 50,20480
v_add_f32 v118, v116, v117
mav 30,196,54
v_add_f32 v119, v116, v117
mva 34,54,200
s_waitcnt lgkmcnt(2)
mav 26,204,38
R 54,21504
v_permlane32_swap_b32 v118, v119
mav 30,208,38
v_add_f32 v112, v118, v119
v_add_f32 v113, v118, v119
mva 34,38,212
mav 26,216,42
R 38,22528
v_permlane16_swap_b32 v112, v113
mav 30,220,42
v_add_f32 v118, v112, v113
mva 34,42,224
s_waitcnt lgkmcnt(2)
mav 26,228,46
R 42,23552
mav 30,232,46
v_rcp_f32 v118, v118
mva 34,46,236
v_mul_f32 v116, v116, v118
v_mul_f32 v117, v117, v118
mav 26,240,50
R 46,24576
v_cvt_pk_bf16_f32 v116, v116, v116
mav 30,244,50
v_cvt_pk_bf16_f32 v117, v117, v117
v_and_b32 v112, v116, v94
mva 34,50,248
v_and_b32 v113, v117, v95
s_waitcnt lgkmcnt(2)
mav 26,252,54
R 50,25600
v_and_b32 v114, v116, v92
mvv 30,128,54
v_and_b32 v115, v117, v93
mvv 34,54,132
mvv 26,136,38
R 54,26624
v_mfma_f32_16x16x32_bf16 v[70:73], v[8:11], v[112:115], 0
v_mfma_f32_16x16x32_bf16 v[74:77], v[106:109], v[112:115], 0
mvv 30,140,38
mvv 34,38,144
s_waitcnt lgkmcnt(2)
mvv 26,148,42
R 38,27648
mvv 30,152,42
mvv 34,42,156
v_cvt_pk_bf16_f32 v112, v70, v71
v_cvt_pk_bf16_f32 v113, v72, v73
mvv 26,160,46
R 42,28672
v_cvt_pk_bf16_f32 v114, v74, v75
mvv 30,164,46
v_cvt_pk_bf16_f32 v115, v76, v77
buffer_store_dwordx4 v[112:115], v81, s[36:39], s49 offen sc1
mvv 34,46,168
s_add_u32 s49, s49, 346112
s_waitcnt lgkmcnt(2)
mvv 26,172,50
R 46,29696
mvv 30,176,50
mvv 34,50,180
mvv 26,184,54
R 50,30720
mvv 30,188,54
mvv 34,54,192
s_waitcnt lgkmcnt(2)
mvv 26,196,38
R 54,31744
v_xor_b32 v80, 0x10000, v80
mvv 30,200,38
mvv 34,38,204
mvv 26,208,42
R 38,32768
mvv 30,212,42
ds_read_b64 v[120:121], v82 offset:128
ds_read_b128 v[70:73], v83 offset:128
mvv 34,42,216
ds_read_b128 v[74:77], v83 offset:144
s_waitcnt lgkmcnt(5)
mvv 26,220,46
R 42,33792
mvv 30,224,46
mvv 34,46,228
mvv 26,232,50
R 46,34816
mvv 30,236,50
mvv 34,50,240
s_waitcnt lgkmcnt(2)
mvv 26,244,54
R 50,35840
mvv 30,248,54
mvv 34,54,252
v_mfma_f32_16x16x32_bf16 v[14:17], a[0:3], v[38:41], 0
R 54,36864
v_mul_f32 v71, v71, v97
v_mfma_f32_16x16x32_bf16 v[18:21], a[4:7], v[38:41], 0
v_mul_f32 v73, v73, v97
v_mul_f32 v75, v75, v97
v_mfma_f32_16x16x32_bf16 v[22:25], v[38:41], a[8:11], 0
v_mul_f32 v77, v77, v97
s_waitcnt lgkmcnt(2)
mav 14,12,42
R 58,37888
v_fma_f32 v34, v34, v70, v71
mav 18,16,42
v_fma_f32 v35, v35, v72, v73
v_fma_f32 v36, v36, v74, v75
mva 22,42,20
v_fma_f32 v37, v37, v76, v77
v_cvt_pk_bf16_f32 v10, v34, v35
mav 14,24,46
R 38,38912
v_cvt_pk_bf16_f32 v11, v36, v37
mav 18,28,46
v_mul_f32 v122, v121, v62
v_mfma_f32_16x16x32_bf16 v[70:73], v[98:101], v[0:3], v[84:87]
v_fma_f32 v26, v26, v120, v122
v_mul_f32 v123, v121, v63
mva 22,46,32
v_fma_f32 v27, v27, v120, v123
s_waitcnt lgkmcnt(2)
mav 14,36,50
R 42,39936
v_mul_f32 v122, v121, v64
mav 18,40,50
v_fma_f32 v28, v28, v120, v122
v_mul_f32 v123, v121, v65
mva 22,50,44
v_fma_f32 v29, v29, v120, v123
v_mul_f32 v122, v121, v66
mav 14,48,54
R 46,40960
v_fma_f32 v30, v30, v120, v122
mav 18,52,54
v_mul_f32 v123, v121, v67
v_fma_f32 v31, v31, v120, v123
mva 22,54,56
v_mul_f32 v122, v121, v68
s_waitcnt lgkmcnt(2)
mav 14,60,58
R 50,41984
v_fma_f32 v32, v32, v120, v122
mav 18,64,58
v_mul_f32 v123, v121, v69
v_fma_f32 v33, v33, v120, v123
mva 22,58,68
v_add_u32 v78, 1, v78
mav 14,72,38
R 54,43008
mav 18,76,38
mva 22,38,80
s_waitcnt lgkmcnt(2)
mav 14,84,42
R 58,44032
mav 18,88,42
mva 22,42,92
mav 14,96,46
R 38,45056
mav 18,100,46
s_barrier
ds_read_b128 v[102:105], v81 offset:4096
mva 22,46,104
ds_read_b64 v[108:109], v81 offset:5120
s_waitcnt lgkmcnt(4)
mav 14,108,50
R 42,46080
mav 18,112,50
mva 22,50,116
mav 14,120,54
R 46,47104
mav 18,124,54
s_waitcnt vmcnt(0)
buffer_store_dwordx2 v[78:79], off, s[36:39], s52 sc1
mva 22,54,128
s_waitcnt lgkmcnt(3)
v_mfma_f32_16x16x32_bf16 v[74:77], v[102:105], v[0:3], v[88:91]
v_cvt_pk_bf16_f32 v0, v26, v27
v_cvt_pk_bf16_f32 v1, v28, v29
mav 14,132,58
R 50,48128
v_cvt_pk_bf16_f32 v2, v30, v31
mav 18,136,58
v_cvt_pk_bf16_f32 v3, v32, v33
mva 22,58,140
v_max3_f32 v116, v70, v71, v74
v_max3_f32 v117, v72, v73, v76
mav 14,144,38
R 54,49152
v_max_f32 v116, v116, v75
mav 18,148,38
v_max_f32 v117, v117, v77
v_max_f32 v118, v116, v117
mva 22,38,152
v_max_f32 v119, v116, v117
s_waitcnt lgkmcnt(2)
mav 14,156,42
R 38,50176
v_permlane32_swap_b32 v118, v119
mav 18,160,42
v_max_f32 v112, v118, v119
v_max_f32 v113, v118, v119
mva 22,42,164
mav 14,168,46
R 42,51200
v_permlane16_swap_b32 v112, v113
mav 18,172,46
v_max_f32 v118, v112, v113
v_mul_f32 v118, s65, v118
mva 22,46,176
v_fma_f32 v116, v116, s65, -v118
s_waitcnt lgkmcnt(2)
mav 14,180,50
R 46,52224
v_fma_f32 v117, v117, s65, -v118
mav 18,184,50
v_exp_f32 v116, v116
mva 22,50,188
v_exp_f32 v117, v117
mav 14,192,54
R 50,53248
v_add_f32 v118, v116, v117
mav 18,196,54
v_add_f32 v119, v116, v117
mva 22,54,200
s_waitcnt lgkmcnt(2)
mav 14,204,38
R 54,54272
v_permlane32_swap_b32 v118, v119
mav 18,208,38
v_add_f32 v112, v118, v119
v_add_f32 v113, v118, v119
mva 22,38,212
mav 14,216,42
R 38,55296
v_permlane16_swap_b32 v112, v113
mav 18,220,42
v_add_f32 v118, v112, v113
mva 22,42,224
s_waitcnt lgkmcnt(2)
mav 14,228,46
R 42,56320
mav 18,232,46
v_rcp_f32 v118, v118
mva 22,46,236
v_mul_f32 v116, v116, v118
v_mul_f32 v117, v117, v118
mav 14,240,50
R 46,57344
v_cvt_pk_bf16_f32 v116, v116, v116
mav 18,244,50
v_cvt_pk_bf16_f32 v117, v117, v117
v_and_b32 v112, v116, v92
mva 22,50,248
v_and_b32 v113, v117, v93
s_waitcnt lgkmcnt(2)
mav 14,252,54
R 50,58368
v_and_b32 v114, v116, v94
mvv 18,128,54
v_and_b32 v115, v117, v95
mvv 22,54,132
mvv 14,136,38
R 54,59392
v_mfma_f32_16x16x32_bf16 v[70:73], v[8:11], v[112:115], 0
v_mfma_f32_16x16x32_bf16 v[74:77], v[106:109], v[112:115], 0
mvv 18,140,38
mvv 22,38,144
s_waitcnt lgkmcnt(2)
mvv 14,148,42
R 38,60416
mvv 18,152,42
mvv 22,42,156
v_cvt_pk_bf16_f32 v112, v70, v71
v_cvt_pk_bf16_f32 v113, v72, v73
mvv 14,160,46
R 42,61440
v_cvt_pk_bf16_f32 v114, v74, v75
mvv 18,164,46
v_cvt_pk_bf16_f32 v115, v76, v77
buffer_store_dwordx4 v[112:115], v81, s[36:39], s49 offen sc1
mvv 22,46,168
s_add_u32 s49, s49, 346112
s_waitcnt lgkmcnt(2)
mvv 14,172,50
R 46,62464
mvv 18,176,50
mvv 22,50,180
mvv 14,184,54
R 50,63488
mvv 18,188,54
mvv 22,54,192
s_waitcnt lgkmcnt(2)
mvv 14,196,38
R 54,64512
mvv 18,200,38
mvv 22,38,204
mvv 14,208,42
R 38,0
mvv 18,212,42
ds_read_b64 v[120:121], v82 offset:0
ds_read_b128 v[70:73], v83 offset:0
mvv 22,42,216
ds_read_b128 v[74:77], v83 offset:16
s_waitcnt lgkmcnt(5)
mvv 14,220,46
R 42,1024
mvv 18,224,46
mvv 22,46,228
mvv 14,232,50
R 46,2048
mvv 18,236,50
mvv 22,50,240
s_waitcnt lgkmcnt(2)
mvv 14,244,54
R 50,3072
mvv 18,248,54
mvv 22,54,252
v_mfma_f32_16x16x32_bf16 v[26:29], a[0:3], v[38:41], 0
R 54,4096
v_mul_f32 v71, v71, v97
v_mfma_f32_16x16x32_bf16 v[30:33], a[4:7], v[38:41], 0
v_mul_f32 v73, v73, v97
v_mul_f32 v75, v75, v97
v_mfma_f32_16x16x32_bf16 v[34:37], v[38:41], a[8:11], 0
v_mul_f32 v77, v77, v97
s_waitcnt lgkmcnt(2)
mav 26,12,42
R 58,5120
v_fma_f32 v22, v22, v70, v71
mav 30,16,42
v_fma_f32 v23, v23, v72, v73
v_fma_f32 v24, v24, v74, v75
mva 34,42,20
v_fma_f32 v25, v25, v76, v77
v_cvt_pk_bf16_f32 v8, v22, v23
mav 26,24,46
R 38,6144
v_cvt_pk_bf16_f32 v9, v24, v25
mav 30,28,46
v_min_f32 v74, s56, v88
v_mfma_f32_16x16x32_bf16 v[70:73], v[102:105], v[0:3], v[84:87]
v_min_f32 v75, s56, v89
v_min_f32 v76, s56, v90
mva 34,46,32
v_min_f32 v77, s56, v91
s_waitcnt lgkmcnt(2)
mav 26,36,50
R 42,7168
mvv 74,4,0
v_mul_f32 v122, v121, v62
v_fma_f32 v14, v14, v120, v122
mav 30,40,50
v_mul_f32 v123, v121, v63
v_fma_f32 v15, v15, v120, v123
mva 34,50,44
v_mul_f32 v122, v121, v64
v_fma_f32 v16, v16, v120, v122
mav 26,48,54
R 46,8192
v_max3_f32 v116, v70, v71, v74
mav 30,52,54
v_max3_f32 v117, v72, v73, v76
v_max_f32 v116, v116, v75
mva 34,54,56
v_max_f32 v117, v117, v77
s_waitcnt lgkmcnt(2)
mav 26,60,58
R 50,9216
v_max_f32 v118, v116, v117
mav 30,64,58
v_max_f32 v119, v116, v117
v_mul_f32 v123, v121, v65
mva 34,58,68
v_permlane32_swap_b32 v118, v119
v_max_f32 v112, v118, v119
mav 26,72,38
R 54,10240
v_max_f32 v113, v118, v119
mav 30,76,38
v_fma_f32 v17, v17, v120, v123
v_permlane16_swap_b32 v112, v113
mva 34,38,80
v_max_f32 v118, v112, v113
s_waitcnt lgkmcnt(2)
mav 26,84,42
R 58,11264
v_mul_f32 v118, s65, v118
mav 30,88,42
v_fma_f32 v116, v116, s65, -v118
v_fma_f32 v117, v117, s65, -v118
mva 34,42,92
v_exp_f32 v116, v116
mav 26,96,46
R 38,12288
v_mul_f32 v122, v121, v66
mav 30,100,46
s_barrier
ds_read_b128 v[98:101], v81 offset:0
mva 34,46,104
ds_read_b64 v[106:107], v81 offset:1024
s_waitcnt lgkmcnt(4)
mav 26,108,50
R 42,13312
v_fma_f32 v18, v18, v120, v122
mav 30,112,50
v_exp_f32 v117, v117
mva 34,50,116
v_add_f32 v118, v116, v117
v_add_f32 v119, v116, v117
mav 26,120,54
R 46,14336
v_permlane32_swap_b32 v118, v119
mav 30,124,54
v_add_f32 v112, v118, v119
v_add_f32 v113, v118, v119
mva 34,54,128
v_mul_f32 v123, v121, v67
s_waitcnt lgkmcnt(2)
mav 26,132,58
R 50,15360
v_permlane16_swap_b32 v112, v113
mav 30,136,58
v_add_f32 v118, v112, v113
v_fma_f32 v19, v19, v120, v123
mva 34,58,140
v_rcp_f32 v118, v118
mav 26,144,38
R 54,16384
v_mul_f32 v116, v116, v118
mav 30,148,38
v_mul_f32 v117, v117, v118
v_cvt_pk_bf16_f32 v116, v116, v116
mva 34,38,152
v_cvt_pk_bf16_f32 v117, v117, v117
s_waitcnt lgkmcnt(2)
mav 26,156,42
R 38,17408
v_and_b32 v112, v116, v92
mav 30,160,42
v_and_b32 v113, v117, v93
v_and_b32 v114, v116, v94
mva 34,42,164
v_and_b32 v115, v117, v95
buffer_store_dwordx4 v[98:101], v81, s[36:39], s54 offen offset:2048 sc1
mav 26,168,46
R 42,18432
v_mfma_f32_16x16x32_bf16 v[70:73], v[10:13], v[112:115], 0
v_mfma_f32_16x16x32_bf16 v[74:77], v[108:111], v[112:115], 0
buffer_store_dwordx2 v[8:9], v96, s[36:39], s54 offen offset:3072 sc1
buffer_store_dwordx2 v[106:107], v96, s[36:39], s54 offen offset:3584 sc1
mav 30,172,46
v_mul_f32 v122, v121, v68
v_fma_f32 v20, v20, v120, v122
mva 34,46,176
v_mul_f32 v123, v121, v69
s_waitcnt lgkmcnt(2)
mav 26,180,50
R 46,19456
v_cvt_pk_bf16_f32 v112, v70, v71
mav 30,184,50
v_cvt_pk_bf16_f32 v113, v72, v73
v_cvt_pk_bf16_f32 v114, v74, v75
mva 34,50,188
v_cvt_pk_bf16_f32 v115, v76, v77
buffer_store_dwordx4 v[112:115], v81, s[36:39], s49 offen sc1
mav 26,192,54
R 50,20480
s_add_u32 s49, s49, 346112
mav 30,196,54
v_fma_f32 v21, v21, v120, v123
v_cvt_pk_bf16_f32 v0, v14, v15
mva 34,54,200
v_cvt_pk_bf16_f32 v1, v16, v17
s_waitcnt lgkmcnt(2)
mav 26,204,38
R 54,21504
v_cvt_pk_bf16_f32 v2, v18, v19
mav 30,208,38
v_cvt_pk_bf16_f32 v3, v20, v21
v_add_u32 v78, 1, v78
mva 34,38,212
s_waitcnt vmcnt(4)
buffer_store_dwordx2 v[78:79], off, s[36:39], s52 sc1
mav 26,216,42
R 38,22528
mav 30,220,42
mva 34,42,224
s_waitcnt lgkmcnt(2)
mav 26,228,46
R 42,23552
mav 30,232,46
mva 34,46,236
mav 26,240,50
R 46,24576
mav 30,244,50
mva 34,50,248
s_waitcnt lgkmcnt(2)
mav 26,252,54
R 50,25600
mvv 30,128,54
mvv 34,54,132
mvv 26,136,38
R 54,26624
mvv 30,140,38
mvv 34,38,144
s_waitcnt lgkmcnt(2)
mvv 26,148,42
R 38,27648
mvv 30,152,42
mvv 34,42,156
mvv 26,160,46
R 42,28672
mvv 30,164,46
mvv 34,46,168
s_waitcnt lgkmcnt(2)
mvv 26,172,50
R 46,29696
mvv 30,176,50
mvv 34,50,180
mvv 26,184,54
R 50,30720
mvv 30,188,54
mvv 34,54,192
s_waitcnt lgkmcnt(2)
mvv 26,196,38
R 54,31744
v_xor_b32 v80, 0x10000, v80
mvv 30,200,38
mvv 34,38,204
mvv 26,208,42
R 38,32768
mvv 30,212,42
ds_read_b64 v[120:121], v82 offset:128
ds_read_b128 v[70:73], v83 offset:128
mvv 34,42,216
ds_read_b128 v[74:77], v83 offset:144
s_waitcnt lgkmcnt(5)
mvv 26,220,46
R 42,33792
mvv 30,224,46
mvv 34,46,228
mvv 26,232,50
R 46,34816
mvv 30,236,50
mvv 34,50,240
s_waitcnt lgkmcnt(2)
mvv 26,244,54
R 50,35840
mvv 30,248,54
mvv 34,54,252
v_mfma_f32_16x16x32_bf16 v[14:17], a[0:3], v[38:41], 0
R 54,36864
v_mul_f32 v71, v71, v97
v_mfma_f32_16x16x32_bf16 v[18:21], a[4:7], v[38:41], 0
v_mul_f32 v73, v73, v97
v_mul_f32 v75, v75, v97
v_mfma_f32_16x16x32_bf16 v[22:25], v[38:41], a[8:11], 0
v_mul_f32 v77, v77, v97
s_waitcnt lgkmcnt(2)
mav 14,12,42
R 58,37888
v_fma_f32 v34, v34, v70, v71
mav 18,16,42
v_fma_f32 v35, v35, v72, v73
v_fma_f32 v36, v36, v74, v75
mva 22,42,20
v_fma_f32 v37, v37, v76, v77
v_cvt_pk_bf16_f32 v10, v34, v35
mav 14,24,46
LsQ5:
s_cbranch_execz LsQ9
R 38,38912
v_cvt_pk_bf16_f32 v11, v36, v37
mav 18,28,46
v_mul_f32 v122, v121, v62
v_mfma_f32_16x16x32_bf16 v[70:73], v[98:101], v[0:3], v[84:87]
v_fma_f32 v26, v26, v120, v122
v_mul_f32 v123, v121, v63
mva 22,46,32
v_fma_f32 v27, v27, v120, v123
s_waitcnt lgkmcnt(2)
mav 14,36,50
R 42,39936
v_mul_f32 v122, v121, v64
mav 18,40,50
v_fma_f32 v28, v28, v120, v122
v_mul_f32 v123, v121, v65
mva 22,50,44
v_fma_f32 v29, v29, v120, v123
v_mul_f32 v122, v121, v66
mav 14,48,54
R 46,40960
v_fma_f32 v30, v30, v120, v122
mav 18,52,54
v_mul_f32 v123, v121, v67
v_fma_f32 v31, v31, v120, v123
mva 22,54,56
v_mul_f32 v122, v121, v68
s_waitcnt lgkmcnt(2)
mav 14,60,58
R 50,41984
v_fma_f32 v32, v32, v120, v122
mav 18,64,58
v_mul_f32 v123, v121, v69
v_fma_f32 v33, v33, v120, v123
mva 22,58,68
v_add_u32 v78, 1, v78
mav 14,72,38
R 54,43008
mav 18,76,38
mva 22,38,80
s_waitcnt lgkmcnt(2)
mav 14,84,42
R 58,44032
mav 18,88,42
mva 22,42,92
mav 14,96,46
R 38,45056
mav 18,100,46
s_barrier
ds_read_b128 v[102:105], v81 offset:4096
mva 22,46,104
ds_read_b64 v[108:109], v81 offset:5120
s_waitcnt lgkmcnt(4)
mav 14,108,50
R 42,46080
mav 18,112,50
mva 22,50,116
mav 14,120,54
R 46,47104
mav 18,124,54
s_waitcnt vmcnt(1)
buffer_store_dwordx2 v[78:79], off, s[36:39], s52 sc1
mva 22,54,128
s_waitcnt lgkmcnt(3)
v_mfma_f32_16x16x32_bf16 v[74:77], v[102:105], v[0:3], v[88:91]
v_cvt_pk_bf16_f32 v0, v26, v27
v_cvt_pk_bf16_f32 v1, v28, v29
mav 14,132,58
R 50,48128
v_cvt_pk_bf16_f32 v2, v30, v31
mav 18,136,58
v_cvt_pk_bf16_f32 v3, v32, v33
mva 22,58,140
v_max3_f32 v116, v70, v71, v74
v_max3_f32 v117, v72, v73, v76
mav 14,144,38
R 54,49152
v_max_f32 v116, v116, v75
mav 18,148,38
v_max_f32 v117, v117, v77
v_max_f32 v118, v116, v117
mva 22,38,152
v_max_f32 v119, v116, v117
s_waitcnt lgkmcnt(2)
mav 14,156,42
R 38,50176
v_permlane32_swap_b32 v118, v119
mav 18,160,42
v_max_f32 v112, v118, v119
v_max_f32 v113, v118, v119
mva 22,42,164
mav 14,168,46
R 42,51200
v_permlane16_swap_b32 v112, v113
mav 18,172,46
v_max_f32 v118, v112, v113
v_mul_f32 v118, s65, v118
mva 22,46,176
v_fma_f32 v116, v116, s65, -v118
s_waitcnt lgkmcnt(2)
mav 14,180,50
R 46,52224
v_fma_f32 v117, v117, s65, -v118
mav 18,184,50
v_exp_f32 v116, v116
mva 22,50,188
v_exp_f32 v117, v117
mav 14,192,54
R 50,53248
v_add_f32 v118, v116, v117
mav 18,196,54
v_add_f32 v119, v116, v117
mva 22,54,200
s_waitcnt lgkmcnt(2)
mav 14,204,38
R 54,54272
v_permlane32_swap_b32 v118, v119
mav 18,208,38
v_add_f32 v112, v118, v119
v_add_f32 v113, v118, v119
mva 22,38,212
mav 14,216,42
R 38,55296
v_permlane16_swap_b32 v112, v113
mav 18,220,42
v_add_f32 v118, v112, v113
mva 22,42,224
s_waitcnt lgkmcnt(2)
mav 14,228,46
R 42,56320
mav 18,232,46
v_rcp_f32 v118, v118
mva 22,46,236
v_mul_f32 v116, v116, v118
v_mul_f32 v117, v117, v118
mav 14,240,50
R 46,57344
v_cvt_pk_bf16_f32 v116, v116, v116
mav 18,244,50
v_cvt_pk_bf16_f32 v117, v117, v117
v_and_b32 v112, v116, v92
mva 22,50,248
v_and_b32 v113, v117, v93
s_waitcnt lgkmcnt(2)
mav 14,252,54
R 50,58368
v_and_b32 v114, v116, v94
mvv 18,128,54
v_and_b32 v115, v117, v95
mvv 22,54,132
mvv 14,136,38
R 54,59392
v_mfma_f32_16x16x32_bf16 v[70:73], v[8:11], v[112:115], 0
v_mfma_f32_16x16x32_bf16 v[74:77], v[106:109], v[112:115], 0
mvv 18,140,38
mvv 22,38,144
s_waitcnt lgkmcnt(2)
mvv 14,148,42
R 38,60416
mvv 18,152,42
mvv 22,42,156
v_cvt_pk_bf16_f32 v112, v70, v71
v_cvt_pk_bf16_f32 v113, v72, v73
mvv 14,160,46
R 42,61440
v_cvt_pk_bf16_f32 v114, v74, v75
mvv 18,164,46
v_cvt_pk_bf16_f32 v115, v76, v77
buffer_store_dwordx4 v[112:115], v81, s[36:39], s49 offen sc1
mvv 22,46,168
s_add_u32 s49, s49, 346112
s_waitcnt lgkmcnt(2)
mvv 14,172,50
R 46,62464
mvv 18,176,50
mvv 22,50,180
mvv 14,184,54
R 50,63488
mvv 18,188,54
mvv 22,54,192
s_waitcnt lgkmcnt(2)
mvv 14,196,38
R 54,64512
mvv 18,200,38
mvv 22,38,204
mvv 14,208,42
R 38,0
mvv 18,212,42
ds_read_b64 v[120:121], v82 offset:0
ds_read_b128 v[70:73], v83 offset:0
mvv 22,42,216
ds_read_b128 v[74:77], v83 offset:16
s_waitcnt lgkmcnt(5)
mvv 14,220,46
R 42,1024
mvv 18,224,46
mvv 22,46,228
mvv 14,232,50
R 46,2048
mvv 18,236,50
mvv 22,50,240
s_waitcnt lgkmcnt(2)
mvv 14,244,54
R 50,3072
mvv 18,248,54
mvv 22,54,252
v_mfma_f32_16x16x32_bf16 v[26:29], a[0:3], v[38:41], 0
R 54,4096
v_mul_f32 v71, v71, v97
v_mfma_f32_16x16x32_bf16 v[30:33], a[4:7], v[38:41], 0
v_mul_f32 v73, v73, v97
v_mul_f32 v75, v75, v97
v_mfma_f32_16x16x32_bf16 v[34:37], v[38:41], a[8:11], 0
v_mul_f32 v77, v77, v97
s_waitcnt lgkmcnt(2)
mav 26,12,42
R 58,5120
v_fma_f32 v22, v22, v70, v71
mav 30,16,42
v_fma_f32 v23, v23, v72, v73
v_fma_f32 v24, v24, v74, v75
mva 34,42,20
v_fma_f32 v25, v25, v76, v77
v_cvt_pk_bf16_f32 v8, v22, v23
mav 26,24,46
R 38,6144
v_cvt_pk_bf16_f32 v9, v24, v25
mav 30,28,46
v_mul_f32 v122, v121, v62
v_mfma_f32_16x16x32_bf16 v[70:73], v[102:105], v[0:3], v[84:87]
v_fma_f32 v14, v14, v120, v122
v_mul_f32 v123, v121, v63
mva 34,46,32
v_fma_f32 v15, v15, v120, v123
s_waitcnt lgkmcnt(2)
mav 26,36,50
R 42,7168
v_mul_f32 v122, v121, v64
mav 30,40,50
v_fma_f32 v16, v16, v120, v122
v_mul_f32 v123, v121, v65
mva 34,50,44
v_fma_f32 v17, v17, v120, v123
v_mul_f32 v122, v121, v66
mav 26,48,54
R 46,8192
v_fma_f32 v18, v18, v120, v122
mav 30,52,54
v_mul_f32 v123, v121, v67
v_fma_f32 v19, v19, v120, v123
mva 34,54,56
v_mul_f32 v122, v121, v68
s_waitcnt lgkmcnt(2)
mav 26,60,58
R 50,9216
v_fma_f32 v20, v20, v120, v122
mav 30,64,58
v_mul_f32 v123, v121, v69
v_fma_f32 v21, v21, v120, v123
mva 34,58,68
v_add_u32 v78, 1, v78
mav 26,72,38
R 54,10240
mav 30,76,38
mva 34,38,80
s_waitcnt lgkmcnt(2)
mav 26,84,42
R 58,11264
mav 30,88,42
mva 34,42,92
mav 26,96,46
R 38,12288
mav 30,100,46
s_barrier
ds_read_b128 v[98:101], v81 offset:0
mva 34,46,104
ds_read_b64 v[106:107], v81 offset:1024
s_waitcnt lgkmcnt(4)
mav 26,108,50
R 42,13312
mav 30,112,50
mva 34,50,116
mav 26,120,54
R 46,14336
mav 30,124,54
s_waitcnt vmcnt(0)
buffer_store_dwordx2 v[78:79], off, s[36:39], s52 sc1
mva 34,54,128
s_waitcnt lgkmcnt(3)
v_mfma_f32_16x16x32_bf16 v[74:77], v[98:101], v[0:3], v[88:91]
v_cvt_pk_bf16_f32 v0, v14, v15
v_cvt_pk_bf16_f32 v1, v16, v17
mav 26,132,58
R 50,15360
v_cvt_pk_bf16_f32 v2, v18, v19
mav 30,136,58
v_cvt_pk_bf16_f32 v3, v20, v21
mva 34,58,140
v_max3_f32 v116, v70, v71, v74
v_max3_f32 v117, v72, v73, v76
mav 26,144,38
R 54,16384
v_max_f32 v116, v116, v75
mav 30,148,38
v_max_f32 v117, v117, v77
v_max_f32 v118, v116, v117
mva 34,38,152
v_max_f32 v119, v116, v117
s_waitcnt lgkmcnt(2)
mav 26,156,42
R 38,17408
v_permlane32_swap_b32 v118, v119
mav 30,160,42
v_max_f32 v112, v118, v119
v_max_f32 v113, v118, v119
mva 34,42,164
mav 26,168,46
R 42,18432
v_permlane16_swap_b32 v112, v113
mav 30,172,46
v_max_f32 v118, v112, v113
v_mul_f32 v118, s65, v118
mva 34,46,176
v_fma_f32 v116, v116, s65, -v118
s_waitcnt lgkmcnt(2)
mav 26,180,50
R 46,19456
v_fma_f32 v117, v117, s65, -v118
mav 30,184,50
v_exp_f32 v116, v116
mva 34,50,188
v_exp_f32 v117, v117
mav 26,192,54
R 50,20480
v_add_f32 v118, v116, v117
mav 30,196,54
v_add_f32 v119, v116, v117
mva 34,54,200
s_waitcnt lgkmcnt(2)
mav 26,204,38
R 54,21504
v_permlane32_swap_b32 v118, v119
mav 30,208,38
v_add_f32 v112, v118, v119
v_add_f32 v113, v118, v119
mva 34,38,212
mav 26,216,42
R 38,22528
v_permlane16_swap_b32 v112, v113
mav 30,220,42
v_add_f32 v118, v112, v113
mva 34,42,224
s_waitcnt lgkmcnt(2)
mav 26,228,46
R 42,23552
mav 30,232,46
v_rcp_f32 v118, v118
mva 34,46,236
v_mul_f32 v116, v116, v118
v_mul_f32 v117, v117, v118
mav 26,240,50
R 46,24576
v_cvt_pk_bf16_f32 v116, v116, v116
mav 30,244,50
v_cvt_pk_bf16_f32 v117, v117, v117
v_and_b32 v112, v116, v94
mva 34,50,248
v_and_b32 v113, v117, v95
s_waitcnt lgkmcnt(2)
mav 26,252,54
R 50,25600
v_and_b32 v114, v116, v92
mvv 30,128,54
v_and_b32 v115, v117, v93
mvv 34,54,132
mvv 26,136,38
R 54,26624
v_mfma_f32_16x16x32_bf16 v[70:73], v[8:11], v[112:115], 0
v_mfma_f32_16x16x32_bf16 v[74:77], v[106:109], v[112:115], 0
mvv 30,140,38
mvv 34,38,144
s_waitcnt lgkmcnt(2)
mvv 26,148,42
R 38,27648
mvv 30,152,42
mvv 34,42,156
v_cvt_pk_bf16_f32 v112, v70, v71
v_cvt_pk_bf16_f32 v113, v72, v73
mvv 26,160,46
R 42,28672
v_cvt_pk_bf16_f32 v114, v74, v75
mvv 30,164,46
v_cvt_pk_bf16_f32 v115, v76, v77
buffer_store_dwordx4 v[112:115], v81, s[36:39], s49 offen sc1
mvv 34,46,168
s_add_u32 s49, s49, 346112
s_waitcnt lgkmcnt(2)
mvv 26,172,50
R 46,29696
mvv 30,176,50
mvv 34,50,180
mvv 26,184,54
R 50,30720
mvv 30,188,54
mvv 34,54,192
s_waitcnt lgkmcnt(2)
mvv 26,196,38
R 54,31744
v_xor_b32 v80, 0x10000, v80
mvv 30,200,38
mvv 34,38,204
mvv 26,208,42
R 38,32768
mvv 30,212,42
ds_read_b64 v[120:121], v82 offset:128
ds_read_b128 v[70:73], v83 offset:128
mvv 34,42,216
ds_read_b128 v[74:77], v83 offset:144
s_waitcnt lgkmcnt(5)
mvv 26,220,46
R 42,33792
mvv 30,224,46
mvv 34,46,228
mvv 26,232,50
R 46,34816
mvv 30,236,50
mvv 34,50,240
s_waitcnt lgkmcnt(2)
mvv 26,244,54
R 50,35840
mvv 30,248,54
mvv 34,54,252
s_mov_b32 s60, -13
.p2align 6
LQA_2:
v_mfma_f32_16x16x32_bf16 v[14:17], a[0:3], v[38:41], 0
R 54,36864
v_mul_f32 v71, v71, v97
v_mfma_f32_16x16x32_bf16 v[18:21], a[4:7], v[38:41], 0
v_mul_f32 v73, v73, v97
v_mul_f32 v75, v75, v97
v_mfma_f32_16x16x32_bf16 v[22:25], v[38:41], a[8:11], 0
v_mul_f32 v77, v77, v97
s_waitcnt lgkmcnt(2)
mav 14,12,42
R 58,37888
v_fma_f32 v34, v34, v70, v71
mav 18,16,42
v_fma_f32 v35, v35, v72, v73
v_fma_f32 v36, v36, v74, v75
mva 22,42,20
v_fma_f32 v37, v37, v76, v77
v_cvt_pk_bf16_f32 v10, v34, v35
mav 14,24,46
R 38,38912
v_cvt_pk_bf16_f32 v11, v36, v37
mav 18,28,46
v_mul_f32 v122, v121, v62
v_mfma_f32_16x16x32_bf16 v[70:73], v[98:101], v[0:3], v[84:87]
v_fma_f32 v26, v26, v120, v122
v_mul_f32 v123, v121, v63
mva 22,46,32
v_fma_f32 v27, v27, v120, v123
s_waitcnt lgkmcnt(2)
mav 14,36,50
R 42,39936
v_mul_f32 v122, v121, v64
mav 18,40,50
v_fma_f32 v28, v28, v120, v122
v_mul_f32 v123, v121, v65
mva 22,50,44
v_fma_f32 v29, v29, v120, v123
v_mul_f32 v122, v121, v66
mav 14,48,54
R 46,40960
v_fma_f32 v30, v30, v120, v122
mav 18,52,54
v_mul_f32 v123, v121, v67
v_fma_f32 v31, v31, v120, v123
mva 22,54,56
v_mul_f32 v122, v121, v68
s_waitcnt lgkmcnt(2)
mav 14,60,58
R 50,41984
v_fma_f32 v32, v32, v120, v122
mav 18,64,58
v_mul_f32 v123, v121, v69
v_fma_f32 v33, v33, v120, v123
mva 22,58,68
v_add_u32 v78, 1, v78
mav 14,72,38
R 54,43008
mav 18,76,38
mva 22,38,80
s_waitcnt lgkmcnt(2)
mav 14,84,42
R 58,44032
mav 18,88,42
mva 22,42,92
mav 14,96,46
R 38,45056
mav 18,100,46
s_barrier
ds_read_b128 v[102:105], v81 offset:4096
mva 22,46,104
ds_read_b64 v[108:109], v81 offset:5120
s_waitcnt lgkmcnt(4)
mav 14,108,50
R 42,46080
mav 18,112,50
mva 22,50,116
mav 14,120,54
R 46,47104
mav 18,124,54
s_waitcnt vmcnt(0)
buffer_store_dwordx2 v[78:79], off, s[36:39], s52 sc1
mva 22,54,128
s_waitcnt lgkmcnt(3)
v_mfma_f32_16x16x32_bf16 v[74:77], v[102:105], v[0:3], v[88:91]
v_cvt_pk_bf16_f32 v0, v26, v27
v_cvt_pk_bf16_f32 v1, v28, v29
mav 14,132,58
R 50,48128
v_cvt_pk_bf16_f32 v2, v30, v31
mav 18,136,58
v_cvt_pk_bf16_f32 v3, v32, v33
mva 22,58,140
v_max3_f32 v116, v70, v71, v74
v_max3_f32 v117, v72, v73, v76
mav 14,144,38
R 54,49152
v_max_f32 v116, v116, v75
mav 18,148,38
v_max_f32 v117, v117, v77
v_max_f32 v118, v116, v117
mva 22,38,152
v_max_f32 v119, v116, v117
s_waitcnt lgkmcnt(2)
mav 14,156,42
R 38,50176
v_permlane32_swap_b32 v118, v119
mav 18,160,42
v_max_f32 v112, v118, v119
v_max_f32 v113, v118, v119
mva 22,42,164
mav 14,168,46
R 42,51200
v_permlane16_swap_b32 v112, v113
mav 18,172,46
v_max_f32 v118, v112, v113
v_mul_f32 v118, s65, v118
mva 22,46,176
v_fma_f32 v116, v116, s65, -v118
s_waitcnt lgkmcnt(2)
mav 14,180,50
R 46,52224
v_fma_f32 v117, v117, s65, -v118
mav 18,184,50
v_exp_f32 v116, v116
mva 22,50,188
v_exp_f32 v117, v117
mav 14,192,54
R 50,53248
v_add_f32 v118, v116, v117
mav 18,196,54
v_add_f32 v119, v116, v117
mva 22,54,200
s_waitcnt lgkmcnt(2)
mav 14,204,38
R 54,54272
v_permlane32_swap_b32 v118, v119
mav 18,208,38
v_add_f32 v112, v118, v119
v_add_f32 v113, v118, v119
mva 22,38,212
mav 14,216,42
R 38,55296
v_permlane16_swap_b32 v112, v113
mav 18,220,42
v_add_f32 v118, v112, v113
mva 22,42,224
s_waitcnt lgkmcnt(2)
mav 14,228,46
R 42,56320
mav 18,232,46
LsQ6:
s_cbranch_execz LsQ10
v_rcp_f32 v118, v118
mva 22,46,236
v_mul_f32 v116, v116, v118
v_mul_f32 v117, v117, v118
mav 14,240,50
R 46,57344
v_cvt_pk_bf16_f32 v116, v116, v116
mav 18,244,50
v_cvt_pk_bf16_f32 v117, v117, v117
v_and_b32 v112, v116, v92
mva 22,50,248
v_and_b32 v113, v117, v93
s_waitcnt lgkmcnt(2)
mav 14,252,54
R 50,58368
v_and_b32 v114, v116, v94
mvv 18,128,54
v_and_b32 v115, v117, v95
mvv 22,54,132
mvv 14,136,38
R 54,59392
v_mfma_f32_16x16x32_bf16 v[70:73], v[8:11], v[112:115], 0
v_mfma_f32_16x16x32_bf16 v[74:77], v[106:109], v[112:115], 0
mvv 18,140,38
mvv 22,38,144
s_waitcnt lgkmcnt(2)
mvv 14,148,42
R 38,60416
mvv 18,152,42
mvv 22,42,156
v_cvt_pk_bf16_f32 v112, v70, v71
v_cvt_pk_bf16_f32 v113, v72, v73
mvv 14,160,46
R 42,61440
v_cvt_pk_bf16_f32 v114, v74, v75
mvv 18,164,46
v_cvt_pk_bf16_f32 v115, v76, v77
buffer_store_dwordx4 v[112:115], v81, s[36:39], s49 offen sc1
mvv 22,46,168
s_add_u32 s49, s49, 346112
s_waitcnt lgkmcnt(2)
mvv 14,172,50
R 46,62464
mvv 18,176,50
mvv 22,50,180
mvv 14,184,54
R 50,63488
mvv 18,188,54
mvv 22,54,192
s_waitcnt lgkmcnt(2)
mvv 14,196,38
R 54,64512
mvv 18,200,38
mvv 22,38,204
mvv 14,208,42
R 38,0
mvv 18,212,42
ds_read_b64 v[120:121], v82 offset:0
ds_read_b128 v[70:73], v83 offset:0
mvv 22,42,216
ds_read_b128 v[74:77], v83 offset:16
s_waitcnt lgkmcnt(5)
mvv 14,220,46
R 42,1024
mvv 18,224,46
mvv 22,46,228
mvv 14,232,50
R 46,2048
mvv 18,236,50
mvv 22,50,240
s_waitcnt lgkmcnt(2)
mvv 14,244,54
R 50,3072
mvv 18,248,54
mvv 22,54,252
v_mfma_f32_16x16x32_bf16 v[26:29], a[0:3], v[38:41], 0
R 54,4096
v_mul_f32 v71, v71, v97
v_mfma_f32_16x16x32_bf16 v[30:33], a[4:7], v[38:41], 0
v_mul_f32 v73, v73, v97
v_mul_f32 v75, v75, v97
v_mfma_f32_16x16x32_bf16 v[34:37], v[38:41], a[8:11], 0
v_mul_f32 v77, v77, v97
s_waitcnt lgkmcnt(2)
mav 26,12,42
R 58,5120
v_fma_f32 v22, v22, v70, v71
mav 30,16,42
v_fma_f32 v23, v23, v72, v73
v_fma_f32 v24, v24, v74, v75
mva 34,42,20
v_fma_f32 v25, v25, v76, v77
v_cvt_pk_bf16_f32 v8, v22, v23
mav 26,24,46
R 38,6144
v_cvt_pk_bf16_f32 v9, v24, v25
mav 30,28,46
v_mul_f32 v122, v121, v62
v_mfma_f32_16x16x32_bf16 v[70:73], v[102:105], v[0:3], v[84:87]
v_fma_f32 v14, v14, v120, v122
v_mul_f32 v123, v121, v63
mva 34,46,32
v_fma_f32 v15, v15, v120, v123
s_waitcnt lgkmcnt(2)
mav 26,36,50
R 42,7168
v_mul_f32 v122, v121, v64
mav 30,40,50
v_fma_f32 v16, v16, v120, v122
v_mul_f32 v123, v121, v65
mva 34,50,44
v_fma_f32 v17, v17, v120, v123
v_mul_f32 v122, v121, v66
mav 26,48,54
R 46,8192
v_fma_f32 v18, v18, v120, v122
mav 30,52,54
v_mul_f32 v123, v121, v67
v_fma_f32 v19, v19, v120, v123
mva 34,54,56
v_mul_f32 v122, v121, v68
s_waitcnt lgkmcnt(2)
mav 26,60,58
R 50,9216
v_fma_f32 v20, v20, v120, v122
mav 30,64,58
v_mul_f32 v123, v121, v69
v_fma_f32 v21, v21, v120, v123
mva 34,58,68
v_add_u32 v78, 1, v78
mav 26,72,38
R 54,10240
mav 30,76,38
mva 34,38,80
s_waitcnt lgkmcnt(2)
mav 26,84,42
R 58,11264
mav 30,88,42
mva 34,42,92
mav 26,96,46
R 38,12288
mav 30,100,46
s_barrier
ds_read_b128 v[98:101], v81 offset:0
mva 34,46,104
ds_read_b64 v[106:107], v81 offset:1024
s_waitcnt lgkmcnt(4)
mav 26,108,50
R 42,13312
mav 30,112,50
mva 34,50,116
mav 26,120,54
R 46,14336
mav 30,124,54
s_waitcnt vmcnt(0)
buffer_store_dwordx2 v[78:79], off, s[36:39], s52 sc1
mva 34,54,128
s_waitcnt lgkmcnt(3)
v_mfma_f32_16x16x32_bf16 v[74:77], v[98:101], v[0:3], v[88:91]
v_cvt_pk_bf16_f32 v0, v14, v15
v_cvt_pk_bf16_f32 v1, v16, v17
mav 26,132,58
R 50,15360
v_cvt_pk_bf16_f32 v2, v18, v19
mav 30,136,58
v_cvt_pk_bf16_f32 v3, v20, v21
mva 34,58,140
v_max3_f32 v116, v70, v71, v74
v_max3_f32 v117, v72, v73, v76
mav 26,144,38
R 54,16384
v_max_f32 v116, v116, v75
mav 30,148,38
v_max_f32 v117, v117, v77
v_max_f32 v118, v116, v117
mva 34,38,152
v_max_f32 v119, v116, v117
s_waitcnt lgkmcnt(2)
mav 26,156,42
R 38,17408
v_permlane32_swap_b32 v118, v119
mav 30,160,42
v_max_f32 v112, v118, v119
v_max_f32 v113, v118, v119
mva 34,42,164
mav 26,168,46
R 42,18432
v_permlane16_swap_b32 v112, v113
mav 30,172,46
v_max_f32 v118, v112, v113
v_mul_f32 v118, s65, v118
mva 34,46,176
v_fma_f32 v116, v116, s65, -v118
s_waitcnt lgkmcnt(2)
mav 26,180,50
R 46,19456
v_fma_f32 v117, v117, s65, -v118
mav 30,184,50
v_exp_f32 v116, v116
mva 34,50,188
v_exp_f32 v117, v117
mav 26,192,54
R 50,20480
v_add_f32 v118, v116, v117
mav 30,196,54
v_add_f32 v119, v116, v117
mva 34,54,200
s_waitcnt lgkmcnt(2)
mav 26,204,38
R 54,21504
v_permlane32_swap_b32 v118, v119
mav 30,208,38
v_add_f32 v112, v118, v119
v_add_f32 v113, v118, v119
mva 34,38,212
mav 26,216,42
R 38,22528
v_permlane16_swap_b32 v112, v113
mav 30,220,42
v_add_f32 v118, v112, v113
mva 34,42,224
s_waitcnt lgkmcnt(2)
mav 26,228,46
R 42,23552
mav 30,232,46
v_rcp_f32 v118, v118
mva 34,46,236
v_mul_f32 v116, v116, v118
v_mul_f32 v117, v117, v118
mav 26,240,50
R 46,24576
v_cvt_pk_bf16_f32 v116, v116, v116
mav 30,244,50
v_cvt_pk_bf16_f32 v117, v117, v117
v_and_b32 v112, v116, v94
mva 34,50,248
v_and_b32 v113, v117, v95
s_waitcnt lgkmcnt(2)
mav 26,252,54
R 50,25600
v_and_b32 v114, v116, v92
mvv 30,128,54
v_and_b32 v115, v117, v93
mvv 34,54,132
mvv 26,136,38
R 54,26624
v_mfma_f32_16x16x32_bf16 v[70:73], v[8:11], v[112:115], 0
v_mfma_f32_16x16x32_bf16 v[74:77], v[106:109], v[112:115], 0
mvv 30,140,38
mvv 34,38,144
s_waitcnt lgkmcnt(2)
mvv 26,148,42
R 38,27648
mvv 30,152,42
mvv 34,42,156
v_cvt_pk_bf16_f32 v112, v70, v71
v_cvt_pk_bf16_f32 v113, v72, v73
mvv 26,160,46
R 42,28672
v_cvt_pk_bf16_f32 v114, v74, v75
mvv 30,164,46
v_cvt_pk_bf16_f32 v115, v76, v77
buffer_store_dwordx4 v[112:115], v81, s[36:39], s49 offen sc1
mvv 34,46,168
s_add_u32 s49, s49, 346112
s_waitcnt lgkmcnt(2)
mvv 26,172,50
R 46,29696
mvv 30,176,50
mvv 34,50,180
mvv 26,184,54
R 50,30720
mvv 30,188,54
mvv 34,54,192
s_waitcnt lgkmcnt(2)
mvv 26,196,38
R 54,31744
v_xor_b32 v80, 0x10000, v80
mvv 30,200,38
mvv 34,38,204
mvv 26,208,42
R 38,32768
mvv 30,212,42
ds_read_b64 v[120:121], v82 offset:128
ds_read_b128 v[70:73], v83 offset:128
mvv 34,42,216
ds_read_b128 v[74:77], v83 offset:144
s_waitcnt lgkmcnt(5)
mvv 26,220,46
R 42,33792
mvv 30,224,46
mvv 34,46,228
mvv 26,232,50
R 46,34816
mvv 30,236,50
mvv 34,50,240
s_waitcnt lgkmcnt(2)
mvv 26,244,54
R 50,35840
mvv 30,248,54
mvv 34,54,252
s_add_u32 s60, s60, 1
s_cbranch_scc0 LQA_2
v_mfma_f32_16x16x32_bf16 v[14:17], a[0:3], v[38:41], 0
R 54,36864
v_mul_f32 v71, v71, v97
v_mfma_f32_16x16x32_bf16 v[18:21], a[4:7], v[38:41], 0
v_mul_f32 v73, v73, v97
v_mul_f32 v75, v75, v97
v_mfma_f32_16x16x32_bf16 v[22:25], v[38:41], a[8:11], 0
v_mul_f32 v77, v77, v97
s_waitcnt lgkmcnt(2)
mav 14,12,42
R 58,37888
v_fma_f32 v34, v34, v70, v71
mav 18,16,42
v_fma_f32 v35, v35, v72, v73
v_fma_f32 v36, v36, v74, v75
mva 22,42,20
v_fma_f32 v37, v37, v76, v77
v_cvt_pk_bf16_f32 v10, v34, v35
mav 14,24,46
R 38,38912
v_cvt_pk_bf16_f32 v11, v36, v37
mav 18,28,46
v_mul_f32 v122, v121, v62
v_mfma_f32_16x16x32_bf16 v[70:73], v[98:101], v[0:3], v[84:87]
v_fma_f32 v26, v26, v120, v122
v_mul_f32 v123, v121, v63
mva 22,46,32
v_fma_f32 v27, v27, v120, v123
s_waitcnt lgkmcnt(2)
mav 14,36,50
R 42,39936
v_mul_f32 v122, v121, v64
mav 18,40,50
v_fma_f32 v28, v28, v120, v122
v_mul_f32 v123, v121, v65
mva 22,50,44
v_fma_f32 v29, v29, v120, v123
v_mul_f32 v122, v121, v66
mav 14,48,54
R 46,40960
v_fma_f32 v30, v30, v120, v122
mav 18,52,54
v_mul_f32 v123, v121, v67
v_fma_f32 v31, v31, v120, v123
mva 22,54,56
v_mul_f32 v122, v121, v68
s_waitcnt lgkmcnt(2)
mav 14,60,58
R 50,41984
v_fma_f32 v32, v32, v120, v122
mav 18,64,58
v_mul_f32 v123, v121, v69
v_fma_f32 v33, v33, v120, v123
mva 22,58,68
v_add_u32 v78, 1, v78
mav 14,72,38
R 54,43008
mav 18,76,38
mva 22,38,80
s_waitcnt lgkmcnt(2)
mav 14,84,42
R 58,44032
mav 18,88,42
mva 22,42,92
mav 14,96,46
R 38,45056
mav 18,100,46
s_barrier
ds_read_b128 v[102:105], v81 offset:4096
mva 22,46,104
ds_read_b64 v[108:109], v81 offset:5120
s_waitcnt lgkmcnt(4)
mav 14,108,50
R 42,46080
mav 18,112,50
mva 22,50,116
mav 14,120,54
R 46,47104
mav 18,124,54
s_waitcnt vmcnt(0)
buffer_store_dwordx2 v[78:79], off, s[36:39], s52 sc1
mva 22,54,128
s_waitcnt lgkmcnt(3)
v_mfma_f32_16x16x32_bf16 v[74:77], v[102:105], v[0:3], v[88:91]
v_cvt_pk_bf16_f32 v0, v26, v27
v_cvt_pk_bf16_f32 v1, v28, v29
mav 14,132,58
R 50,48128
v_cvt_pk_bf16_f32 v2, v30, v31
mav 18,136,58
v_cvt_pk_bf16_f32 v3, v32, v33
mva 22,58,140
v_max3_f32 v116, v70, v71, v74
v_max3_f32 v117, v72, v73, v76
mav 14,144,38
R 54,49152
v_max_f32 v116, v116, v75
mav 18,148,38
v_max_f32 v117, v117, v77
v_max_f32 v118, v116, v117
mva 22,38,152
v_max_f32 v119, v116, v117
s_waitcnt lgkmcnt(2)
mav 14,156,42
R 38,50176
v_permlane32_swap_b32 v118, v119
mav 18,160,42
v_max_f32 v112, v118, v119
v_max_f32 v113, v118, v119
mva 22,42,164
mav 14,168,46
R 42,51200
v_permlane16_swap_b32 v112, v113
mav 18,172,46
v_max_f32 v118, v112, v113
v_mul_f32 v118, s65, v118
mva 22,46,176
v_fma_f32 v116, v116, s65, -v118
s_waitcnt lgkmcnt(2)
mav 14,180,50
R 46,52224
v_fma_f32 v117, v117, s65, -v118
mav 18,184,50
v_exp_f32 v116, v116
mva 22,50,188
v_exp_f32 v117, v117
mav 14,192,54
R 50,53248
v_add_f32 v118, v116, v117
mav 18,196,54
v_add_f32 v119, v116, v117
mva 22,54,200
s_waitcnt lgkmcnt(2)
mav 14,204,38
R 54,54272
v_permlane32_swap_b32 v118, v119
mav 18,208,38
v_add_f32 v112, v118, v119
v_add_f32 v113, v118, v119
mva 22,38,212
mav 14,216,42
R 38,55296
v_permlane16_swap_b32 v112, v113
mav 18,220,42
v_add_f32 v118, v112, v113
mva 22,42,224
s_waitcnt lgkmcnt(2)
mav 14,228,46
R 42,56320
mav 18,232,46
v_rcp_f32 v118, v118
mva 22,46,236
v_mul_f32 v116, v116, v118
v_mul_f32 v117, v117, v118
mav 14,240,50
R 46,57344
v_cvt_pk_bf16_f32 v116, v116, v116
mav 18,244,50
v_cvt_pk_bf16_f32 v117, v117, v117
v_and_b32 v112, v116, v92
mva 22,50,248
v_and_b32 v113, v117, v93
s_waitcnt lgkmcnt(2)
mav 14,252,54
R 50,58368
v_and_b32 v114, v116, v94
mvv 18,128,54
v_and_b32 v115, v117, v95
mvv 22,54,132
mvv 14,136,38
R 54,59392
v_mfma_f32_16x16x32_bf16 v[70:73], v[8:11], v[112:115], 0
v_mfma_f32_16x16x32_bf16 v[74:77], v[106:109], v[112:115], 0
mvv 18,140,38
mvv 22,38,144
s_waitcnt lgkmcnt(2)
mvv 14,148,42
R 38,60416
mvv 18,152,42
mvv 22,42,156
v_cvt_pk_bf16_f32 v112, v70, v71
v_cvt_pk_bf16_f32 v113, v72, v73
mvv 14,160,46
R 42,61440
v_cvt_pk_bf16_f32 v114, v74, v75
mvv 18,164,46
v_cvt_pk_bf16_f32 v115, v76, v77
buffer_store_dwordx4 v[112:115], v81, s[36:39], s49 offen sc1
mvv 22,46,168
s_add_u32 s49, s49, 346112
s_waitcnt lgkmcnt(2)
mvv 14,172,50
R 46,62464
mvv 18,176,50
mvv 22,50,180
mvv 14,184,54
R 50,63488
mvv 18,188,54
mvv 22,54,192
s_waitcnt lgkmcnt(2)
mvv 14,196,38
R 54,64512
mvv 18,200,38
mvv 22,38,204
mvv 14,208,42
R 38,0
mvv 18,212,42
ds_read_b64 v[120:121], v82 offset:0
ds_read_b128 v[70:73], v83 offset:0
mvv 22,42,216
ds_read_b128 v[74:77], v83 offset:16
s_waitcnt lgkmcnt(5)
mvv 14,220,46
R 42,1024
mvv 18,224,46
mvv 22,46,228
mvv 14,232,50
R 46,2048
mvv 18,236,50
mvv 22,50,240
s_waitcnt lgkmcnt(2)
mvv 14,244,54
R 50,3072
mvv 18,248,54
mvv 22,54,252
v_mfma_f32_16x16x32_bf16 v[26:29], a[0:3], v[38:41], 0
R 54,4096
v_mul_f32 v71, v71, v97
v_mfma_f32_16x16x32_bf16 v[30:33], a[4:7], v[38:41], 0
v_mul_f32 v73, v73, v97
v_mul_f32 v75, v75, v97
v_mfma_f32_16x16x32_bf16 v[34:37], v[38:41], a[8:11], 0
v_mul_f32 v77, v77, v97
s_waitcnt lgkmcnt(2)
mav 26,12,42
R 58,5120
v_fma_f32 v22, v22, v70, v71
mav 30,16,42
v_fma_f32 v23, v23, v72, v73
v_fma_f32 v24, v24, v74, v75
mva 34,42,20
v_fma_f32 v25, v25, v76, v77
v_cvt_pk_bf16_f32 v8, v22, v23
mav 26,24,46
R 38,6144
v_cvt_pk_bf16_f32 v9, v24, v25
mav 30,28,46
v_mul_f32 v122, v121, v62
v_mfma_f32_16x16x32_bf16 v[70:73], v[102:105], v[0:3], v[84:87]
v_fma_f32 v14, v14, v120, v122
v_mul_f32 v123, v121, v63
mva 34,46,32
v_fma_f32 v15, v15, v120, v123
s_waitcnt lgkmcnt(2)
mav 26,36,50
R 42,7168
v_mul_f32 v122, v121, v64
mav 30,40,50
buffer_load_dwordx4 v[4:7], v81, s[36:39], s55 offen offset:2048 sc1
buffer_load_dwordx2 v[12:13], v96, s[36:39], s55 offen offset:3072 sc1
mva 34,50,44
buffer_load_dwordx2 v[110:111], v96, s[36:39], s55 offen offset:3584 sc1
v_fma_f32 v16, v16, v120, v122
mav 26,48,54
R 46,8192
v_mul_f32 v123, v121, v65
mav 30,52,54
v_fma_f32 v17, v17, v120, v123
v_mul_f32 v122, v121, v66
mva 34,54,56
v_fma_f32 v18, v18, v120, v122
s_waitcnt lgkmcnt(2)
mav 26,60,58
R 50,9216
v_mul_f32 v123, v121, v67
mav 30,64,58
v_fma_f32 v19, v19, v120, v123
v_mul_f32 v122, v121, v68
mva 34,58,68
v_fma_f32 v20, v20, v120, v122
LsQ7:
s_cbranch_execz LsQ11
v_mul_f32 v123, v121, v69
mav 26,72,38
R 54,10240
v_fma_f32 v21, v21, v120, v123
mav 30,76,38
v_add_u32 v78, 1, v78
mva 34,38,80
s_waitcnt lgkmcnt(2)
mav 26,84,42
R 58,11264
mav 30,88,42
mva 34,42,92
mav 26,96,46
R 38,12288
mav 30,100,46
s_barrier
ds_read_b128 v[98:101], v81 offset:0
mva 34,46,104
ds_read_b64 v[106:107], v81 offset:1024
s_waitcnt lgkmcnt(4)
mav 26,108,50
R 42,13312
mav 30,112,50
mva 34,50,116
mav 26,120,54
R 46,14336
mav 30,124,54
s_waitcnt vmcnt(3)
buffer_store_dwordx2 v[78:79], off, s[36:39], s52 sc1
mva 34,54,128
s_waitcnt lgkmcnt(3)
v_mfma_f32_16x16x32_bf16 v[74:77], v[98:101], v[0:3], v[88:91]
v_cvt_pk_bf16_f32 v0, v14, v15
v_cvt_pk_bf16_f32 v1, v16, v17
mav 26,132,58
R 50,15360
v_cvt_pk_bf16_f32 v2, v18, v19
mav 30,136,58
v_cvt_pk_bf16_f32 v3, v20, v21
mva 34,58,140
v_max3_f32 v116, v70, v71, v74
v_max3_f32 v117, v72, v73, v76
mav 26,144,38
R 54,16384
v_max_f32 v116, v116, v75
mav 30,148,38
v_max_f32 v117, v117, v77
v_max_f32 v118, v116, v117
mva 34,38,152
v_max_f32 v119, v116, v117
s_waitcnt lgkmcnt(2)
mav 26,156,42
R 38,17408
v_permlane32_swap_b32 v118, v119
mav 30,160,42
v_max_f32 v112, v118, v119
v_max_f32 v113, v118, v119
mva 34,42,164
mav 26,168,46
R 42,18432
v_permlane16_swap_b32 v112, v113
mav 30,172,46
v_max_f32 v118, v112, v113
v_mul_f32 v118, s65, v118
mva 34,46,176
v_fma_f32 v116, v116, s65, -v118
s_waitcnt lgkmcnt(2)
mav 26,180,50
R 46,19456
v_fma_f32 v117, v117, s65, -v118
mav 30,184,50
v_exp_f32 v116, v116
mva 34,50,188
v_exp_f32 v117, v117
mav 26,192,54
R 50,20480
v_add_f32 v118, v116, v117
mav 30,196,54
v_add_f32 v119, v116, v117
mva 34,54,200
s_waitcnt lgkmcnt(2)
mav 26,204,38
R 54,21504
v_permlane32_swap_b32 v118, v119
mav 30,208,38
v_add_f32 v112, v118, v119
v_add_f32 v113, v118, v119
mva 34,38,212
mav 26,216,42
R 38,22528
v_permlane16_swap_b32 v112, v113
mav 30,220,42
v_add_f32 v118, v112, v113
mva 34,42,224
s_waitcnt lgkmcnt(2)
mav 26,228,46
R 42,23552
mav 30,232,46
v_rcp_f32 v118, v118
mva 34,46,236
v_mul_f32 v116, v116, v118
v_mul_f32 v117, v117, v118
mav 26,240,50
R 46,24576
v_cvt_pk_bf16_f32 v116, v116, v116
mav 30,244,50
v_cvt_pk_bf16_f32 v117, v117, v117
v_and_b32 v112, v116, v94
mva 34,50,248
v_and_b32 v113, v117, v95
s_waitcnt lgkmcnt(2)
mav 26,252,54
R 50,25600
v_and_b32 v114, v116, v92
mvv 30,128,54
v_and_b32 v115, v117, v93
mvv 34,54,132
mvv 26,136,38
R 54,26624
v_mfma_f32_16x16x32_bf16 v[70:73], v[8:11], v[112:115], 0
v_mfma_f32_16x16x32_bf16 v[74:77], v[106:109], v[112:115], 0
mvv 30,140,38
mvv 34,38,144
s_waitcnt lgkmcnt(2)
mvv 26,148,42
R 38,27648
mvv 30,152,42
mvv 34,42,156
v_cvt_pk_bf16_f32 v112, v70, v71
v_cvt_pk_bf16_f32 v113, v72, v73
mvv 26,160,46
R 42,28672
v_cvt_pk_bf16_f32 v114, v74, v75
mvv 30,164,46
v_cvt_pk_bf16_f32 v115, v76, v77
buffer_store_dwordx4 v[112:115], v81, s[36:39], s49 offen sc1
mvv 34,46,168
s_add_u32 s49, s49, 346112
s_waitcnt lgkmcnt(2)
mvv 26,172,50
R 46,29696
mvv 30,176,50
mvv 34,50,180
mvv 26,184,54
R 50,30720
mvv 30,188,54
mvv 34,54,192
s_waitcnt lgkmcnt(2)
mvv 26,196,38
R 54,31744
mvv 30,200,38
mvv 34,38,204
mvv 26,208,42
mvv 30,212,42
ds_read_b64 v[120:121], v82 offset:128
ds_read_b128 v[70:73], v83 offset:128
mvv 34,42,216
ds_read_b128 v[74:77], v83 offset:144
s_waitcnt lgkmcnt(4)
mvv 26,220,46
mvv 30,224,46
mvv 34,46,228
mvv 26,232,50
mvv 30,236,50
mvv 34,50,240
s_waitcnt lgkmcnt(0)
mvv 26,244,54
mvv 30,248,54
mvv 34,54,252
v_mul_f32 v71, v71, v97
s_nop 6
v_fma_f32 v34, v34, v70, v71
v_mul_f32 v73, v73, v97
v_fma_f32 v35, v35, v72, v73
v_mul_f32 v75, v75, v97
v_fma_f32 v36, v36, v74, v75
v_mul_f32 v77, v77, v97
)"
R"(
v_fma_f32 v37, v37, v76, v77
v_cvt_pk_bf16_f32 v10, v34, v35
v_cvt_pk_bf16_f32 v11, v36, v37
s_nop 1
v_mfma_f32_16x16x32_bf16 v[70:73], v[98:101], v[0:3], v[84:87]
v_mul_f32 v122, v121, v62
v_fma_f32 v26, v26, v120, v122
v_mul_f32 v123, v121, v63
v_fma_f32 v27, v27, v120, v123
v_mul_f32 v122, v121, v64
v_fma_f32 v28, v28, v120, v122
v_mul_f32 v123, v121, v65
v_fma_f32 v29, v29, v120, v123
v_mul_f32 v122, v121, v66
v_fma_f32 v30, v30, v120, v122
v_mul_f32 v123, v121, v67
v_fma_f32 v31, v31, v120, v123
v_mul_f32 v122, v121, v68
v_fma_f32 v32, v32, v120, v122
v_mul_f32 v123, v121, v69
v_fma_f32 v33, v33, v120, v123
s_barrier
ds_read_b128 v[102:105], v81 offset:4096
ds_read_b64 v[108:109], v81 offset:5120
s_waitcnt lgkmcnt(0)
v_mfma_f32_16x16x32_bf16 v[74:77], v[102:105], v[0:3], v[88:91]
s_nop 7
v_max3_f32 v116, v70, v71, v74
v_max3_f32 v117, v72, v73, v76
v_max_f32 v116, v116, v75
v_max_f32 v117, v117, v77
v_max_f32 v118, v116, v117
v_max_f32 v119, v116, v117
s_nop 1
v_permlane32_swap_b32 v118, v119
v_max_f32 v112, v118, v119
v_max_f32 v113, v118, v119
s_nop 1
v_permlane16_swap_b32 v112, v113
v_max_f32 v118, v112, v113
v_mul_f32 v118, s65, v118
v_fma_f32 v116, v116, s65, -v118
v_fma_f32 v117, v117, s65, -v118
v_exp_f32 v116, v116
v_exp_f32 v117, v117
s_nop 0
v_add_f32 v118, v116, v117
v_add_f32 v119, v116, v117
s_nop 1
v_permlane32_swap_b32 v118, v119
v_add_f32 v112, v118, v119
v_add_f32 v113, v118, v119
s_nop 1
v_permlane16_swap_b32 v112, v113
v_add_f32 v118, v112, v113
v_rcp_f32 v118, v118
s_nop 0
v_mul_f32 v116, v116, v118
v_mul_f32 v117, v117, v118
v_cvt_pk_bf16_f32 v116, v116, v116
v_cvt_pk_bf16_f32 v117, v117, v117
v_and_b32 v112, v116, v92
v_and_b32 v113, v117, v93
v_and_b32 v114, v116, v94
v_and_b32 v115, v117, v95
s_nop 1
v_mfma_f32_16x16x32_bf16 v[70:73], v[8:11], v[112:115], 0
v_mfma_f32_16x16x32_bf16 v[74:77], v[106:109], v[112:115], 0
s_nop 6
v_cvt_pk_bf16_f32 v112, v70, v71
v_cvt_pk_bf16_f32 v113, v72, v73
v_cvt_pk_bf16_f32 v114, v74, v75
v_cvt_pk_bf16_f32 v115, v76, v77
buffer_store_dwordx4 v[112:115], v81, s[36:39], s49 offen sc1
s_add_u32 s49, s49, 346112
v_cvt_pk_bf16_f32 v0, v26, v27
v_cvt_pk_bf16_f32 v1, v28, v29
v_cvt_pk_bf16_f32 v2, v30, v31
v_cvt_pk_bf16_f32 v3, v32, v33
v_add_u32 v78, 1, v78
s_waitcnt vmcnt(1)
buffer_store_dwordx2 v[78:79], off, s[36:39], s52 sc1
v_mfma_f32_16x16x32_bf16 v[70:73], v[102:105], v[0:3], v[84:87]
v_min_f32 v74, s56, v88
v_min_f32 v75, s56, v89
v_min_f32 v76, s56, v90
v_min_f32 v77, s56, v91
s_nop 1
mvv 74,4,0
s_nop 7
v_max3_f32 v116, v70, v71, v74
v_max3_f32 v117, v72, v73, v76
v_max_f32 v116, v116, v75
v_max_f32 v117, v117, v77
v_max_f32 v118, v116, v117
v_max_f32 v119, v116, v117
s_nop 1
v_permlane32_swap_b32 v118, v119
v_max_f32 v112, v118, v119
v_max_f32 v113, v118, v119
s_nop 1
v_permlane16_swap_b32 v112, v113
v_max_f32 v118, v112, v113
v_mul_f32 v118, s65, v118
v_fma_f32 v116, v116, s65, -v118
v_fma_f32 v117, v117, s65, -v118
v_exp_f32 v116, v116
v_exp_f32 v117, v117
s_nop 0
v_add_f32 v118, v116, v117
v_add_f32 v119, v116, v117
s_nop 1
v_permlane32_swap_b32 v118, v119
v_add_f32 v112, v118, v119
v_add_f32 v113, v118, v119
s_nop 1
v_permlane16_swap_b32 v112, v113
v_add_f32 v118, v112, v113
v_rcp_f32 v118, v118
s_nop 0
v_mul_f32 v116, v116, v118
v_mul_f32 v117, v117, v118
v_cvt_pk_bf16_f32 v116, v116, v116
v_cvt_pk_bf16_f32 v117, v117, v117
v_and_b32 v112, v116, v92
v_and_b32 v113, v117, v93
v_and_b32 v114, v116, v94
v_and_b32 v115, v117, v95
s_nop 1
v_mfma_f32_16x16x32_bf16 v[70:73], v[10:13], v[112:115], 0
v_mfma_f32_16x16x32_bf16 v[74:77], v[108:111], v[112:115], 0
s_nop 6
v_cvt_pk_bf16_f32 v112, v70, v71
v_cvt_pk_bf16_f32 v113, v72, v73
v_cvt_pk_bf16_f32 v114, v74, v75
v_cvt_pk_bf16_f32 v115, v76, v77
buffer_store_dwordx4 v[112:115], v81, s[36:39], s49 offen sc1
v_add_u32 v78, 1, v78
s_waitcnt vmcnt(2)
buffer_store_dwordx2 v[78:79], off, s[36:39], s52 sc1
v_add_u32 v78, 37, v78
s_waitcnt vmcnt(1)
buffer_store_dwordx2 v[78:79], off, s[36:39], s52 sc1
s_endpgm
Lr1:
s_cmp_eq_u32 s60, 1
s_cbranch_scc0 Lr2
s_lshr_b32 s66, s53, 10
s_mul_i32 s66, s66, 8208
s_mov_b32 s67, 0x8000
s_mov_b64 exec, 0
s_cmp_eq_u32 s53, 1024
s_cbranch_scc1 LsQ1
s_branch LsQ3
LwQB:
s_mov_b64 exec, -1
E 0,86016
E 1,90112
E 2,94208
s_add_u32 s51, s51, 196608
E 0,98304
E 1,102400
E 2,106496
s_add_u32 s51, s51, 196608
E 0,61440
E 1,65536
s_add_u32 m0, s66, 2816
s_nop 0
buffer_load_dwordx4 v3, s[44:47], s67 offen offset:256 lds
E 2,69632
s_add_u32 s51, s51, 196608
E 0,73728
s_add_u32 m0, s66, 6912
s_nop 0
buffer_load_dwordx4 v3, s[44:47], 0 offen offset:256 lds
E 1,77824
E 2,81920
s_add_u32 s51, s51, 196608
v_lshlrev_b32 v12, 2, v80
v_and_b32 v12, 0xc0, v12
v_xor_b32 v13, 0, v12
v_add_u32 v8, v7, v13
v_xor_b32 v13, 64, v12
v_add_u32 v9, v7, v13
v_xor_b32 v13, 128, v12
v_add_u32 v10, v7, v13
v_xor_b32 v13, 192, v12
v_add_u32 v11, v7, v13
v_mov_b32 v110, s64
v_mov_b32 v111, s64
v_mov_b32 v112, s64
v_mov_b32 v113, s64
s_waitcnt vmcnt(14)
s_barrier
v_add_u32 v4, 69632, v4
v_add_u32 v5, 69632, v5
v_add_u32 v6, 69632, v6
Ta 0,4,40960
Ta 4,5,40960
Ta 8,6,40960
Ta 12,4,53248
Ta 16,5,53248
Ta 20,6,53248
ds_read_b128 v[46:49], v8 offset:0
ds_read_b128 v[50:53], v9 offset:0
ds_read_b128 v[54:57], v8 offset:4096
ds_read_b128 v[58:61], v9 offset:4096
v_accvgpr_read_b32 v115, a255
v_mov_b32 v114, 0x100
v_add_u32 v115, 0x9e3779b9, v115
buffer_store_dwordx2 v[114:115], v81, s[36:39], s52 offen sc1
v_add_u32 v92, v92, v114
E 0,36864
E 1,40960
E 2,45056
s_add_u32 s51, s51, 196608
E 0,49152
E 1,53248
E 2,57344
s_add_u32 s51, s51, 196608
s_waitcnt lgkmcnt(0)
s_waitcnt vmcnt(15)
s_barrier
E 0,110592
Ta 24,4,16384
v_mfma_f32_16x16x32_bf16 v[94:97], a[0:3], v[110:113], 0
v_mfma_f32_16x16x32_bf16 v[98:101], a[4:7], v[110:113], 0
Ta 28,5,16384
v_mfma_f32_16x16x32_bf16 v[102:105], v[110:113], a[8:11], 0
Ta 32,6,16384
E 1,114688
v_mfma_f32_16x16x32_bf16 v[22:25], a[0:3], v[46:49], 0
Ta 36,4,28672
v_mfma_f32_16x16x32_bf16 v[26:29], a[4:7], v[46:49], 0
Ta 40,5,28672
s_add_u32 m0, s66, 3584
s_nop 0
buffer_load_dwordx4 v3, s[44:47], s67 offen offset:512 lds
v_mfma_f32_16x16x32_bf16 v[30:33], v[46:49], a[8:11], 0
Ta 44,6,28672
v_mfma_f32_16x16x32_bf16 v[34:37], a[0:3], v[54:57], 0
ds_read_b128 v[62:65], v10 offset:0
E 2,118784
s_add_u32 s51, s51, 196608
v_mfma_f32_16x16x32_bf16 v[38:41], a[4:7], v[54:57], 0
ds_read_b128 v[66:69], v11 offset:0
v_mfma_f32_16x16x32_bf16 v[42:45], v[54:57], a[8:11], 0
ds_read_b128 v[70:73], v10 offset:4096
E 0,122880
mav 94,12,110
ds_read_b128 v[74:77], v11 offset:4096
mav 98,16,110
mva 102,110,20
s_add_u32 m0, s66, 7680
s_nop 0
buffer_load_dwordx4 v3, s[44:47], 0 offen offset:512 lds
mav 22,12,50
mav 26,16,50
E 1,126976
mva 30,50,20
mav 34,12,58
E 2,131072
s_add_u32 s51, s51, 196608
mav 38,16,58
mva 42,58,20
s_waitcnt lgkmcnt(0)
s_waitcnt vmcnt(15)
s_barrier
v_add_u32 v4, -49152, v4
v_add_u32 v5, -49152, v5
v_add_u32 v6, -49152, v6
E 0,86016
Ta 48,4,40960
mav 94,24,110
mav 98,28,110
Ta 52,5,40960
mva 102,110,32
Ta 56,6,40960
E 1,90112
mav 22,24,62
Ta 60,4,53248
mav 26,28,62
Ta 64,5,53248
mva 30,62,32
Ta 68,6,53248
E 2,94208
s_add_u32 s51, s51, 196608
mav 34,24,70
ds_read_b128 v[46:49], v8 offset:1024
mav 38,28,70
ds_read_b128 v[50:53], v9 offset:1024
mva 42,70,32
ds_read_b128 v[54:57], v8 offset:5120
E 0,98304
mav 94,36,110
ds_read_b128 v[58:61], v9 offset:5120
mav 98,40,110
mva 102,110,44
E 1,102400
mav 22,36,66
mav 26,40,66
mva 30,66,44
E 2,106496
s_add_u32 s51, s51, 196608
mav 34,36,74
mav 38,40,74
mva 42,74,44
s_waitcnt lgkmcnt(0)
s_waitcnt vmcnt(14)
s_barrier
E 0,61440
Ta 72,4,16384
mav 94,48,110
mav 98,52,110
Ta 76,5,16384
mva 102,110,56
Ta 80,6,16384
E 1,65536
mav 22,48,46
Ta 84,4,28672
mav 26,52,46
Ta 88,5,28672
s_add_u32 m0, s66, 4352
s_nop 0
buffer_load_dwordx4 v3, s[44:47], s67 offen offset:768 lds
mva 30,46,56
Ta 92,6,28672
mav 34,48,54
ds_read_b128 v[62:65], v10 offset:1024
E 2,69632
s_add_u32 s51, s51, 196608
mav 38,52,54
ds_read_b128 v[66:69], v11 offset:1024
mva 42,54,56
ds_read_b128 v[70:73], v10 offset:5120
E 0,73728
mav 94,60,110
ds_read_b128 v[74:77], v11 offset:5120
mav 98,64,110
mva 102,110,68
s_add_u32 m0, s66, 8448
s_nop 0
buffer_load_dwordx4 v3, s[44:47], 0 offen offset:768 lds
mav 22,60,50
mav 26,64,50
E 1,77824
mva 30,50,68
mav 34,60,58
E 2,81920
LsQ8:
s_cbranch_execz LsQ12
s_add_u32 s51, s51, 196608
mav 38,64,58
mva 42,58,68
s_waitcnt lgkmcnt(0)
s_waitcnt vmcnt(14)
s_barrier
v_add_u32 v4, 49152, v4
v_add_u32 v5, 49152, v5
v_add_u32 v6, 49152, v6
E 0,36864
Ta 96,4,40960
mav 94,72,110
mav 98,76,110
Ta 100,5,40960
mva 102,110,80
Ta 104,6,40960
E 1,40960
mav 22,72,62
Ta 108,4,53248
mav 26,76,62
Ta 112,5,53248
mva 30,62,80
Ta 116,6,53248
E 2,45056
s_add_u32 s51, s51, 196608
mav 34,72,70
ds_read_b128 v[46:49], v8 offset:2048
mav 38,76,70
ds_read_b128 v[50:53], v9 offset:2048
mva 42,70,80
ds_read_b128 v[54:57], v8 offset:6144
E 0,49152
mav 94,84,110
ds_read_b128 v[58:61], v9 offset:6144
mav 98,88,110
mva 102,110,92
E 1,53248
mav 22,84,66
mav 26,88,66
mva 30,66,92
E 2,57344
s_add_u32 s51, s51, 196608
mav 34,84,74
mav 38,88,74
mva 42,74,92
s_waitcnt lgkmcnt(0)
s_waitcnt vmcnt(14)
s_barrier
E 0,110592
Ta 120,4,16384
mav 94,96,110
mav 98,100,110
Ta 124,5,16384
mva 102,110,104
Ta 128,6,16384
E 1,114688
mav 22,96,46
Ta 132,4,28672
mav 26,100,46
Ta 136,5,28672
s_add_u32 m0, s66, 1024
s_nop 0
buffer_load_dwordx4 v3, s[44:47], s67 offen offset:1024 lds
mva 30,46,104
Ta 140,6,28672
mav 34,96,54
ds_read_b128 v[62:65], v10 offset:2048
E 2,118784
s_add_u32 s51, s51, 196608
mav 38,100,54
ds_read_b128 v[66:69], v11 offset:2048
mva 42,54,104
ds_read_b128 v[70:73], v10 offset:6144
E 0,122880
mav 94,108,110
ds_read_b128 v[74:77], v11 offset:6144
mav 98,112,110
mva 102,110,116
s_add_u32 m0, s66, 5120
s_nop 0
buffer_load_dwordx4 v3, s[44:47], 0 offen offset:1024 lds
mav 22,108,50
mav 26,112,50
E 1,126976
mva 30,50,116
mav 34,108,58
E 2,131072
s_add_u32 s51, s51, 196608
mav 38,112,58
mva 42,58,116
s_waitcnt lgkmcnt(0)
s_waitcnt vmcnt(14)
s_barrier
v_add_u32 v4, -49152, v4
v_add_u32 v5, -49152, v5
v_add_u32 v6, -49152, v6
E 0,86016
Ta 144,4,40960
mav 94,120,110
mav 98,124,110
Ta 148,5,40960
mva 102,110,128
Ta 152,6,40960
E 1,90112
mav 22,120,62
Ta 156,4,53248
mav 26,124,62
Ta 160,5,53248
mva 30,62,128
Ta 164,6,53248
E 2,94208
s_add_u32 s51, s51, 196608
mav 34,120,70
ds_read_b128 v[46:49], v8 offset:3072
mav 38,124,70
ds_read_b128 v[50:53], v9 offset:3072
mva 42,70,128
ds_read_b128 v[54:57], v8 offset:7168
E 0,98304
mav 94,132,110
ds_read_b128 v[58:61], v9 offset:7168
mav 98,136,110
mva 102,110,140
E 1,102400
mav 22,132,66
mav 26,136,66
mva 30,66,140
E 2,106496
s_add_u32 s51, s51, 196608
mav 34,132,74
mav 38,136,74
mva 42,74,140
s_waitcnt lgkmcnt(0)
s_waitcnt vmcnt(14)
s_barrier
E 0,61440
Ta 168,4,16384
mav 94,144,110
mav 98,148,110
Ta 172,5,16384
mva 102,110,152
Ta 176,6,16384
E 1,65536
mav 22,144,46
Ta 180,4,28672
mav 26,148,46
Ta 184,5,28672
s_add_u32 m0, s66, 1792
s_nop 0
buffer_load_dwordx4 v3, s[44:47], s67 offen offset:1280 lds
mva 30,46,152
Ta 188,6,28672
mav 34,144,54
ds_read_b128 v[62:65], v10 offset:3072
E 2,69632
s_add_u32 s51, s51, 196608
mav 38,148,54
ds_read_b128 v[66:69], v11 offset:3072
mva 42,54,152
ds_read_b128 v[70:73], v10 offset:7168
E 0,73728
mav 94,156,110
ds_read_b128 v[74:77], v11 offset:7168
mav 98,160,110
mva 102,110,164
s_add_u32 m0, s66, 5888
s_nop 0
buffer_load_dwordx4 v3, s[44:47], 0 offen offset:1280 lds
mav 22,156,50
mav 26,160,50
E 1,77824
mva 30,50,164
mav 34,156,58
E 2,81920
s_add_u32 s51, s51, 196608
mav 38,160,58
mva 42,58,164
s_waitcnt lgkmcnt(0)
s_waitcnt vmcnt(14)
s_barrier
v_add_u32 v4, 49152, v4
v_add_u32 v5, 49152, v5
v_add_u32 v6, 49152, v6
E 0,36864
Ta 192,4,40960
mav 94,168,110
mav 98,172,110
Ta 196,5,40960
mva 102,110,176
Ta 200,6,40960
E 1,40960
mav 22,168,62
Ta 204,4,53248
mav 26,172,62
Ta 208,5,53248
mva 30,62,176
Ta 212,6,53248
E 2,45056
s_add_u32 s51, s51, 196608
mav 34,168,70
ds_read_b128 v[46:49], v8 offset:0
mav 38,172,70
ds_read_b128 v[50:53], v9 offset:0
mva 42,70,176
ds_read_b128 v[54:57], v8 offset:4096
E 0,49152
mav 94,180,110
ds_read_b128 v[58:61], v9 offset:4096
mav 98,184,110
mva 102,110,188
E 1,53248
mav 22,180,66
mav 26,184,66
mva 30,66,188
E 2,57344
s_add_u32 s51, s51, 196608
mav 34,180,74
mav 38,184,74
mva 42,74,188
s_waitcnt lgkmcnt(0)
s_waitcnt vmcnt(14)
s_barrier
E 0,110592
Ta 216,4,16384
mav 94,192,110
mav 98,196,110
Ta 220,5,16384
mva 102,110,200
Ta 224,6,16384
E 1,114688
mav 22,192,46
Ta 228,4,28672
mav 26,196,46
Ta 232,5,28672
s_add_u32 m0, s66, 2560
s_nop 0
buffer_load_dwordx4 v3, s[44:47], s67 offen offset:1536 lds
mva 30,46,200
Ta 236,6,28672
mav 34,192,54
ds_read_b128 v[62:65], v10 offset:0
E 2,118784
s_add_u32 s51, s51, 196608
mav 38,196,54
ds_read_b128 v[66:69], v11 offset:0
mva 42,54,200
ds_read_b128 v[70:73], v10 offset:4096
E 0,122880
mav 94,204,110
ds_read_b128 v[74:77], v11 offset:4096
mav 98,208,110
mva 102,110,212
s_add_u32 m0, s66, 6656
s_nop 0
buffer_load_dwordx4 v3, s[44:47], 0 offen offset:1536 lds
mav 22,204,50
mav 26,208,50
E 1,126976
mva 30,50,212
mav 34,204,58
E 2,131072
s_add_u32 s51, s51, 196608
mav 38,208,58
mva 42,58,212
s_waitcnt lgkmcnt(0)
s_waitcnt vmcnt(14)
s_barrier
v_add_u32 v4, -49152, v4
v_add_u32 v5, -49152, v5
v_add_u32 v6, -49152, v6
E 0,86016
Ta 240,4,40960
mav 94,216,110
mav 98,220,110
Ta 244,5,40960
mva 102,110,224
Ta 248,6,40960
E 1,90112
mav 22,216,62
Ta 252,4,53248
mav 26,220,62
Tv 128,5,53248
mva 30,62,224
E 2,94208
s_add_u32 s51, s51, 196608
Tv 132,6,53248
mav 34,216,70
ds_read_b128 v[46:49], v8 offset:1024
mav 38,220,70
ds_read_b128 v[50:53], v9 offset:1024
E 0,98304
mva 42,70,224
ds_read_b128 v[54:57], v8 offset:5120
mav 94,228,110
ds_read_b128 v[58:61], v9 offset:5120
mav 98,232,110
E 1,102400
mva 102,110,236
mav 22,228,66
E 2,106496
s_add_u32 s51, s51, 196608
mav 26,232,66
mva 30,66,236
mav 34,228,74
buffer_load_dwordx2 v[122:123], v91, s[36:39], 0 offen sc1
mav 38,232,74
mva 42,74,236
s_waitcnt lgkmcnt(0)
s_waitcnt vmcnt(15)
s_barrier
E 0,61440
Tv 136,4,16384
mav 94,240,110
mav 98,244,110
Tv 140,5,16384
mva 102,110,248
Tv 144,6,16384
E 1,65536
mav 22,240,46
Tv 148,4,28672
mav 26,244,46
Tv 152,5,28672
s_add_u32 m0, s66, 3328
s_nop 0
buffer_load_dwordx4 v3, s[44:47], s67 offen offset:1792 lds
mva 30,46,248
Tv 156,6,28672
mav 34,240,54
ds_read_b128 v[62:65], v10 offset:1024
E 2,69632
s_add_u32 s51, s51, 196608
mav 38,244,54
ds_read_b128 v[66:69], v11 offset:1024
mva 42,54,248
ds_read_b128 v[70:73], v10 offset:5120
E 0,73728
mav 94,252,110
ds_read_b128 v[74:77], v11 offset:5120
mvv 98,128,110
mvv 102,110,132
s_add_u32 m0, s66, 7424
s_nop 0
buffer_load_dwordx4 v3, s[44:47], 0 offen offset:1792 lds
mav 22,252,50
mvv 26,128,50
E 1,77824
mvv 30,50,132
mav 34,252,58
E 2,81920
s_add_u32 s51, s51, 196608
mvv 38,128,58
mvv 42,58,132
s_waitcnt lgkmcnt(0)
s_waitcnt vmcnt(15)
s_barrier
v_add_u32 v4, 49152, v4
v_add_u32 v5, 49152, v5
v_add_u32 v6, 49152, v6
E 0,36864
Tv 160,4,40960
mvv 94,136,110
mvv 98,140,110
Tv 164,5,40960
mvv 102,110,144
Tv 168,6,40960
E 1,40960
mvv 22,136,62
Tv 172,4,53248
mvv 26,140,62
Tv 176,5,53248
mvv 30,62,144
Tv 180,6,53248
E 2,45056
s_add_u32 s51, s51, 196608
mvv 34,136,70
ds_read_b128 v[46:49], v8 offset:2048
mvv 38,140,70
ds_read_b128 v[50:53], v9 offset:2048
mvv 42,70,144
ds_read_b128 v[54:57], v8 offset:6144
E 0,49152
mvv 94,148,110
ds_read_b128 v[58:61], v9 offset:6144
mvv 98,152,110
mvv 102,110,156
E 1,53248
mvv 22,148,66
mvv 26,152,66
mvv 30,66,156
E 2,57344
s_add_u32 s51, s51, 196608
mvv 34,148,74
LsQ9:
s_cbranch_execz LsQ13
mvv 38,152,74
mvv 42,74,156
s_waitcnt lgkmcnt(0)
s_waitcnt vmcnt(15)
s_barrier
Tv 184,4,16384
Tv 188,5,16384
Tv 192,6,16384
Tv 196,4,28672
Tv 200,5,28672
Tv 204,6,28672
ds_read_b128 v[62:65], v10 offset:2048
ds_read_b128 v[66:69], v11 offset:2048
ds_read_b128 v[70:73], v10 offset:6144
ds_read_b128 v[74:77], v11 offset:6144
s_waitcnt vmcnt(14)
ds_write_b32 v82, v122
v_sub_u32 v122, v122, v92
v_xor_b32 v123, v123, v115
v_cmp_lt_u64 vcc, s[62:63], v[122:123]
s_cbranch_vccnz Cq
Bq:
v_add_u32 v92, 1, v92
s_add_u32 s60, s49, 346112
s_add_u32 s56, s54, 692224
v_add_u32 v12, s55, v84
buffer_load_dwordx2 v[14:15], v93, s[36:39], s54 offen sc1
mvv 94,160,110
mvv 98,164,110
buffer_load_dwordx2 v[18:19], v93, s[36:39], s56 offen sc1
mvv 102,110,168
mvv 22,160,46
s_add_u32 m0, s50, 49152
s_nop 0
buffer_load_dwordx4 v88, s[36:39], s49 offen offset:0 sc1 lds
mvv 26,164,46
mvv 30,46,168
buffer_load_dwordx4 v88, s[36:39], s49 offen offset:1024 sc1 lds
mvv 34,160,54
mvv 38,164,54
buffer_load_dwordx4 v88, s[36:39], s49 offen offset:2048 sc1 lds
mvv 42,54,168
buffer_load_dwordx4 v88, s[36:39], s49 offen offset:3072 sc1 lds
mvv 94,172,110
mvv 98,176,110
s_add_u32 m0, s50, 57344
s_nop 0
buffer_load_dwordx4 v89, s[36:39], s49 offen offset:0 sc1 lds
mvv 102,110,180
mvv 22,172,50
buffer_load_dwordx4 v89, s[36:39], s49 offen offset:1024 sc1 lds
mvv 26,176,50
mvv 30,50,180
buffer_load_dwordx4 v89, s[36:39], s49 offen offset:2048 sc1 lds
mvv 34,172,58
mvv 38,176,58
buffer_load_dwordx4 v89, s[36:39], s49 offen offset:3072 sc1 lds
mvv 42,58,180
s_waitcnt lgkmcnt(0)
s_waitcnt vmcnt(16)
s_barrier
v_add_u32 v4, -49152, v4
v_add_u32 v5, -49152, v5
v_add_u32 v6, -49152, v6
s_add_u32 m0, s50, 32768
s_nop 0
buffer_load_dwordx4 v86, s[36:39], s49 offen offset:0 sc1 lds
Tv 208,4,40960
mvv 94,184,110
mvv 98,188,110
Tv 212,5,40960
mvv 102,110,192
Tv 216,6,40960
buffer_load_dwordx4 v86, s[36:39], s49 offen offset:1024 sc1 lds
mvv 22,184,62
Tv 220,4,53248
mvv 26,188,62
Tv 224,5,53248
buffer_load_dwordx4 v86, s[36:39], s49 offen offset:2048 sc1 lds
mvv 30,62,192
Tv 228,6,53248
mvv 34,184,70
ds_read_b128 v[46:49], v8 offset:3072
buffer_load_dwordx4 v86, s[36:39], s49 offen offset:3072 sc1 lds
mvv 38,188,70
ds_read_b128 v[50:53], v9 offset:3072
mvv 42,70,192
ds_read_b128 v[54:57], v8 offset:7168
s_add_u32 m0, s50, 40960
s_nop 0
buffer_load_dwordx4 v87, s[36:39], s49 offen offset:0 sc1 lds
mvv 94,196,110
ds_read_b128 v[58:61], v9 offset:7168
mvv 98,200,110
mvv 102,110,204
buffer_load_dwordx4 v87, s[36:39], s49 offen offset:1024 sc1 lds
mvv 22,196,66
mvv 26,200,66
buffer_load_dwordx4 v87, s[36:39], s49 offen offset:2048 sc1 lds
mvv 30,66,204
mvv 34,196,74
buffer_load_dwordx4 v87, s[36:39], s49 offen offset:3072 sc1 lds
mvv 38,200,74
mvv 42,74,204
s_waitcnt lgkmcnt(0)
s_waitcnt vmcnt(18)
s_barrier
s_waitcnt vmcnt(16)
s_add_u32 m0, s50, 0
s_nop 0
buffer_load_dwordx4 v86, s[36:39], s60 offen offset:0 sc1 lds
Tv 232,4,16384
mvv 94,208,110
v_permlane32_swap_b32 v14, v15
mvv 98,212,110
Tv 236,5,16384
v_add_f32 v16, v14, v15
mvv 102,110,216
Tv 240,6,16384
v_add_f32 v17, v14, v15
buffer_load_dwordx4 v86, s[36:39], s60 offen offset:1024 sc1 lds
mvv 22,208,46
Tv 244,4,28672
s_nop 1
v_permlane16_swap_b32 v16, v17
mvv 26,212,46
Tv 248,5,28672
v_add_f32 v14, v16, v17
buffer_load_dwordx4 v86, s[36:39], s60 offen offset:2048 sc1 lds
mvv 30,46,216
Tv 252,6,28672
v_add_f32 v15, v16, v17
mvv 34,208,54
ds_read_b128 v[62:65], v10 offset:3072
s_nop 1
v_permlane32_swap_b32 v14, v15
buffer_load_dwordx4 v86, s[36:39], s60 offen offset:3072 sc1 lds
mvv 38,212,54
v_mul_f32 v16, 0x3a800000, v14
ds_read_b128 v[66:69], v11 offset:3072
mvv 42,54,216
ds_read_b128 v[70:73], v10 offset:7168
v_mul_f32 v17, 0x3a800000, v15
s_add_u32 m0, s50, 8192
s_nop 0
buffer_load_dwordx4 v87, s[36:39], s60 offen offset:0 sc1 lds
mvv 94,220,110
ds_read_b128 v[74:77], v11 offset:7168
v_fma_f32 v17, -v16, v16, v17
mvv 98,224,110
v_max_f32 v17, 0, v17
mvv 102,110,228
buffer_load_dwordx4 v87, s[36:39], s60 offen offset:1024 sc1 lds
v_add_f32 v17, 0x3727c5ac, v17
mvv 22,220,50
v_rsq_f32 v14, v17
mvv 26,224,50
buffer_load_dwordx4 v87, s[36:39], s60 offen offset:2048 sc1 lds
mvv 30,50,228
s_nop 0
v_mul_f32_e64 v15, -v16, v14
mvv 34,220,58
ds_write_b64 v12, v[14:15]
buffer_load_dwordx4 v87, s[36:39], s60 offen offset:3072 sc1 lds
mvv 38,224,58
mvv 42,58,228
s_waitcnt lgkmcnt(0)
s_add_u32 m0, s50, 16384
s_nop 0
buffer_load_dwordx4 v88, s[36:39], s60 offen offset:0 sc1 lds
mvv 94,232,110
v_permlane32_swap_b32 v18, v19
mvv 98,236,110
v_add_f32 v20, v18, v19
mvv 102,110,240
buffer_load_dwordx4 v88, s[36:39], s60 offen offset:1024 sc1 lds
v_add_f32 v21, v18, v19
mvv 22,232,62
s_nop 1
v_permlane16_swap_b32 v20, v21
mvv 26,236,62
buffer_load_dwordx4 v88, s[36:39], s60 offen offset:2048 sc1 lds
mvv 30,62,240
v_add_f32 v18, v20, v21
mvv 34,232,70
v_add_f32 v19, v20, v21
buffer_load_dwordx4 v88, s[36:39], s60 offen offset:3072 sc1 lds
mvv 38,236,70
s_nop 1
v_permlane32_swap_b32 v18, v19
mvv 42,70,240
s_add_u32 m0, s50, 24576
s_nop 0
buffer_load_dwordx4 v89, s[36:39], s60 offen offset:0 sc1 lds
mvv 94,244,110
v_mul_f32 v20, 0x3a800000, v18
mvv 98,248,110
v_mul_f32 v21, 0x3a800000, v19
mvv 102,110,252
buffer_load_dwordx4 v89, s[36:39], s60 offen offset:1024 sc1 lds
v_fma_f32 v21, -v20, v20, v21
mvv 22,244,66
v_max_f32 v21, 0, v21
mvv 26,248,66
buffer_load_dwordx4 v89, s[36:39], s60 offen offset:2048 sc1 lds
mvv 30,66,252
v_add_f32 v21, 0x3727c5ac, v21
mvv 34,244,74
v_rsq_f32 v18, v21
buffer_load_dwordx4 v89, s[36:39], s60 offen offset:3072 sc1 lds
mvv 38,248,74
s_nop 0
v_mul_f32_e64 v19, -v20, v18
mvv 42,74,252
s_waitcnt vmcnt(16)
s_barrier
s_nop 7
v_mov_b32 v116, v102
s_and_b32 s60, s48, 3
s_cmp_eq_u32 s60, 1
s_cbranch_scc1 LvQB1
LvQB0:
LsQ10:
s_cbranch_execz LsQ14
v_xor_b32 v80, 0x10000, v80
ds_read_b64 v[118:119], v84 offset:0
ds_read_b128 v[102:105], v85 offset:0
ds_read_b128 v[106:109], v85 offset:16
s_add_u32 s49, s49, 346112
R 46,32768
R 50,33792
R 54,34816
R 58,35840
s_waitcnt lgkmcnt(2)
v_mul_f32 v120, v119, v94
v_fma_f32 v22, v22, v118, v120
v_mul_f32 v121, v119, v95
v_fma_f32 v23, v23, v118, v121
v_mul_f32 v120, v119, v96
v_fma_f32 v24, v24, v118, v120
v_mul_f32 v121, v119, v97
v_fma_f32 v25, v25, v118, v121
v_mul_f32 v120, v119, v98
v_fma_f32 v26, v26, v118, v120
v_mul_f32 v121, v119, v99
v_fma_f32 v27, v27, v118, v121
v_mul_f32 v120, v119, v100
v_fma_f32 v28, v28, v118, v120
v_mul_f32 v121, v119, v101
v_fma_f32 v29, v29, v118, v121
v_cvt_pk_bf16_f32 v110, v22, v23
v_cvt_pk_bf16_f32 v111, v24, v25
v_cvt_pk_bf16_f32 v112, v26, v27
v_cvt_pk_bf16_f32 v113, v28, v29
ds_write_b128 v83, v[110:113] offset:0
v_mul_f32 v103, v103, v116
v_fma_f32 v30, v30, v102, v103
v_mul_f32 v105, v105, v116
v_fma_f32 v31, v31, v104, v105
v_mul_f32 v107, v107, v116
v_fma_f32 v32, v32, v106, v107
v_mul_f32 v109, v109, v116
v_fma_f32 v33, v33, v108, v109
v_cvt_pk_bf16_f32 v78, v30, v31
v_cvt_pk_bf16_f32 v79, v32, v33
ds_write_b64 v83, v[78:79] offset:1024
s_xor_b32 s50, s50, 0x10000
s_add_u32 s49, s49, 346112
s_waitcnt lgkmcnt(0)
s_barrier
ds_write_b64 v84, v[18:19] offset:0
ds_read_b64 v[118:119], v84 offset:128
ds_read_b128 v[102:105], v85 offset:128
ds_read_b128 v[106:109], v85 offset:144
v_mfma_f32_16x16x32_bf16 v[22:25], a[0:3], v[46:49], 0
R 62,36864
v_mfma_f32_16x16x32_bf16 v[26:29], a[4:7], v[46:49], 0
s_waitcnt lgkmcnt(1)
v_mul_f32 v120, v119, v94
v_mfma_f32_16x16x32_bf16 v[30:33], v[46:49], a[8:11], 0
v_fma_f32 v34, v34, v118, v120
v_mul_f32 v121, v119, v95
mav 22,12,50
R 66,37888
v_fma_f32 v35, v35, v118, v121
mav 26,16,50
v_mul_f32 v120, v119, v96
v_fma_f32 v36, v36, v118, v120
mva 30,50,20
v_mul_f32 v121, v119, v97
v_fma_f32 v37, v37, v118, v121
mav 22,24,54
R 70,38912
v_mul_f32 v120, v119, v98
mav 26,28,54
v_fma_f32 v38, v38, v118, v120
v_mul_f32 v121, v119, v99
mva 30,54,32
v_fma_f32 v39, v39, v118, v121
v_mul_f32 v120, v119, v100
mav 22,36,58
R 74,39936
v_fma_f32 v40, v40, v118, v120
mav 26,40,58
v_mul_f32 v121, v119, v101
v_fma_f32 v41, v41, v118, v121
mva 30,58,44
v_cvt_pk_bf16_f32 v110, v34, v35
s_waitcnt lgkmcnt(2)
mav 22,48,62
R 46,40960
v_cvt_pk_bf16_f32 v111, v36, v37
mav 26,52,62
v_cvt_pk_bf16_f32 v112, v38, v39
v_cvt_pk_bf16_f32 v113, v40, v41
mva 30,62,56
ds_write_b128 v83, v[110:113] offset:4096
mav 22,60,66
R 50,41984
v_mul_f32 v103, v103, v116
mav 26,64,66
v_fma_f32 v42, v42, v102, v103
v_mul_f32 v105, v105, v116
mva 30,66,68
v_fma_f32 v43, v43, v104, v105
s_waitcnt lgkmcnt(3)
mav 22,72,70
R 54,43008
v_mul_f32 v107, v107, v116
mav 26,76,70
v_fma_f32 v44, v44, v106, v107
v_mul_f32 v109, v109, v116
mva 30,70,80
v_fma_f32 v45, v45, v108, v109
v_cvt_pk_bf16_f32 v78, v42, v43
mav 22,84,74
R 58,44032
v_cvt_pk_bf16_f32 v79, v44, v45
mav 26,88,74
ds_write_b64 v83, v[78:79] offset:5120
mva 30,74,92
s_waitcnt lgkmcnt(3)
mav 22,96,46
R 62,45056
s_mov_b32 m0, 142336
mav 26,100,46
s_waitcnt vmcnt(0) lgkmcnt(1)
s_barrier
mva 30,46,104
buffer_load_dword v90, s[36:39], 0 offen sc1 lds
mav 22,108,50
R 66,46080
s_add_u32 m0, s50, 32768
mav 26,112,50
mva 30,50,116
D 86,0
mav 22,120,54
R 70,47104
mav 26,124,54
D 86,1024
mva 30,54,128
mav 22,132,58
R 74,48128
D 86,2048
mav 26,136,58
mva 30,58,140
D 86,3072
s_waitcnt lgkmcnt(2)
mav 22,144,62
R 46,49152
s_add_u32 m0, s50, 40960
mav 26,148,62
D 87,0
mva 30,62,152
mav 22,156,66
R 50,50176
D 87,1024
mav 26,160,66
mva 30,66,164
D 87,2048
s_waitcnt lgkmcnt(2)
mav 22,168,70
R 54,51200
mav 26,172,70
D 87,3072
s_add_u32 m0, s50, 49152
mva 30,70,176
mav 22,180,74
R 58,52224
D 88,0
mav 26,184,74
mva 30,74,188
D 88,1024
s_waitcnt lgkmcnt(2)
mav 22,192,46
R 62,53248
mav 26,196,46
D 88,2048
mva 30,46,200
mav 22,204,50
R 66,54272
D 88,3072
mav 26,208,50
s_add_u32 m0, s50, 57344
mva 30,50,212
D 89,0
s_waitcnt lgkmcnt(2)
mav 22,216,54
R 70,55296
mav 26,220,54
D 89,1024
mva 30,54,224
mav 22,228,58
R 74,56320
D 89,2048
mav 26,232,58
mva 30,58,236
D 89,3072
s_waitcnt lgkmcnt(2)
mav 22,240,62
R 46,57344
s_add_u32 s49, s49, 346112
mav 26,244,62
s_add_u32 m0, s50, 0
mva 30,62,248
D 86,0
mav 22,252,66
R 50,58368
mvv 26,128,66
D 86,1024
mvv 30,66,132
s_waitcnt lgkmcnt(2)
mvv 22,136,70
R 54,59392
D 86,2048
mvv 26,140,70
mvv 30,70,144
D 86,3072
s_add_u32 m0, s50, 8192
mvv 22,148,74
R 58,60416
mvv 26,152,74
D 87,0
mvv 30,74,156
s_waitcnt lgkmcnt(2)
mvv 22,160,46
R 62,61440
D 87,1024
mvv 26,164,46
mvv 30,46,168
D 87,2048
mvv 22,172,50
R 66,62464
mvv 26,176,50
D 87,3072
s_add_u32 m0, s50, 16384
mvv 30,50,180
s_waitcnt lgkmcnt(2)
mvv 22,184,54
R 70,63488
D 88,0
mvv 26,188,54
mvv 30,54,192
D 88,1024
mvv 22,196,58
R 74,64512
mvv 26,200,58
D 88,2048
mvv 30,58,204
s_waitcnt lgkmcnt(2)
mvv 22,208,62
R 46,0
D 88,3072
mvv 26,212,62
ds_read_b32 v122, v82 offset:0
s_add_u32 m0, s50, 24576
mvv 30,62,216
ds_read_b64 v[118:119], v84 offset:0
ds_read_b128 v[102:105], v85 offset:0
mvv 22,220,66
R 50,1024
ds_read_b128 v[106:109], v85 offset:16
mvv 26,224,66
D 89,0
mvv 30,66,228
s_waitcnt lgkmcnt(6)
mvv 22,232,70
R 54,2048
D 89,1024
mvv 26,236,70
mvv 30,70,240
D 89,2048
mvv 22,244,74
R 58,3072
mvv 26,248,74
s_waitcnt lgkmcnt(2)
v_cmp_gt_i32 vcc, v92, v122
mvv 30,74,252
s_cbranch_vccnz CQB0_1
BQB0_1:
D 89,3072
v_mfma_f32_16x16x32_bf16 v[34:37], a[0:3], v[46:49], 0
R 62,4096
v_mul_f32 v120, v119, v94
v_mfma_f32_16x16x32_bf16 v[38:41], a[4:7], v[46:49], 0
v_fma_f32 v22, v22, v118, v120
v_mul_f32 v121, v119, v95
v_mfma_f32_16x16x32_bf16 v[42:45], v[46:49], a[8:11], 0
v_fma_f32 v23, v23, v118, v121
v_mul_f32 v120, v119, v96
mav 34,12,50
R 66,5120
v_fma_f32 v24, v24, v118, v120
mav 38,16,50
v_mul_f32 v121, v119, v97
v_fma_f32 v25, v25, v118, v121
mva 42,50,20
v_mul_f32 v120, v119, v98
s_waitcnt lgkmcnt(2)
mav 34,24,54
R 70,6144
v_fma_f32 v26, v26, v118, v120
mav 38,28,54
v_mul_f32 v121, v119, v99
v_fma_f32 v27, v27, v118, v121
mva 42,54,32
v_mul_f32 v120, v119, v100
v_fma_f32 v28, v28, v118, v120
mav 34,36,58
R 74,7168
v_mul_f32 v121, v119, v101
mav 38,40,58
v_fma_f32 v29, v29, v118, v121
v_cvt_pk_bf16_f32 v110, v22, v23
mva 42,58,44
v_cvt_pk_bf16_f32 v111, v24, v25
s_waitcnt lgkmcnt(2)
mav 34,48,62
R 46,8192
v_cvt_pk_bf16_f32 v112, v26, v27
mav 38,52,62
v_cvt_pk_bf16_f32 v113, v28, v29
v_mul_f32 v103, v103, v116
mva 42,62,56
ds_write_b128 v83, v[110:113] offset:0
mav 34,60,66
R 50,9216
v_fma_f32 v30, v30, v102, v103
mav 38,64,66
v_mul_f32 v105, v105, v116
v_fma_f32 v31, v31, v104, v105
mva 42,66,68
v_mul_f32 v107, v107, v116
s_waitcnt lgkmcnt(3)
mav 34,72,70
R 54,10240
v_fma_f32 v32, v32, v106, v107
mav 38,76,70
LsQ11:
s_cbranch_execz LsQ15
v_mul_f32 v109, v109, v116
v_fma_f32 v33, v33, v108, v109
mva 42,70,80
s_xor_b32 s50, s50, 0x10000
v_cvt_pk_bf16_f32 v78, v30, v31
mav 34,84,74
R 58,11264
v_cvt_pk_bf16_f32 v79, v32, v33
mav 38,88,74
ds_write_b64 v83, v[78:79] offset:1024
mva 42,74,92
s_waitcnt lgkmcnt(3)
mav 34,96,46
R 62,12288
s_mov_b32 m0, 142336
mav 38,100,46
s_waitcnt vmcnt(16) lgkmcnt(1)
s_barrier
mva 42,46,104
s_add_u32 s49, s49, 346112
buffer_load_dword v90, s[36:39], 0 offen sc1 lds
mav 34,108,50
R 66,13312
s_add_u32 m0, s50, 32768
mav 38,112,50
mva 42,50,116
D 86,0
mav 34,120,54
R 70,14336
mav 38,124,54
D 86,1024
mva 42,54,128
mav 34,132,58
R 74,15360
mav 38,136,58
D 86,2048
mva 42,58,140
s_waitcnt lgkmcnt(2)
mav 34,144,62
R 46,16384
D 86,3072
mav 38,148,62
s_add_u32 m0, s50, 40960
mva 42,62,152
mav 34,156,66
R 50,17408
D 87,0
mav 38,160,66
v_add_u32 v92, 1, v92
mva 42,66,164
D 87,1024
s_waitcnt lgkmcnt(2)
mav 34,168,70
R 54,18432
mav 38,172,70
mva 42,70,176
D 87,2048
mav 34,180,74
R 58,19456
mav 38,184,74
D 87,3072
mva 42,74,188
s_add_u32 m0, s50, 49152
s_waitcnt lgkmcnt(2)
mav 34,192,46
R 62,20480
mav 38,196,46
D 88,0
mva 42,46,200
mav 34,204,50
R 66,21504
D 88,1024
mav 38,208,50
mva 42,50,212
s_waitcnt lgkmcnt(2)
mav 34,216,54
R 70,22528
D 88,2048
mav 38,220,54
mva 42,54,224
D 88,3072
mav 34,228,58
R 74,23552
s_add_u32 m0, s50, 57344
mav 38,232,58
mva 42,58,236
D 89,0
s_waitcnt lgkmcnt(2)
mav 34,240,62
R 46,24576
mav 38,244,62
D 89,1024
mva 42,62,248
mav 34,252,66
R 50,25600
mvv 38,128,66
D 89,2048
mvv 42,66,132
s_waitcnt lgkmcnt(2)
mvv 34,136,70
R 54,26624
D 89,3072
mvv 38,140,70
mvv 42,70,144
mvv 34,148,74
R 58,27648
mvv 38,152,74
mvv 42,74,156
s_waitcnt lgkmcnt(2)
mvv 34,160,46
R 62,28672
mvv 38,164,46
mvv 42,46,168
mvv 34,172,50
R 66,29696
mvv 38,176,50
mvv 42,50,180
s_waitcnt lgkmcnt(2)
mvv 34,184,54
R 70,30720
mvv 38,188,54
mvv 42,54,192
mvv 34,196,58
R 74,31744
v_xor_b32 v80, 0x10000, v80
mvv 38,200,58
mvv 42,58,204
s_waitcnt lgkmcnt(2)
mvv 34,208,62
R 46,32768
mvv 38,212,62
ds_read_b32 v122, v82 offset:0
ds_read_b64 v[118:119], v84 offset:128
mvv 42,62,216
ds_read_b128 v[102:105], v85 offset:128
ds_read_b128 v[106:109], v85 offset:144
mvv 34,220,66
R 50,33792
mvv 38,224,66
mvv 42,66,228
s_waitcnt lgkmcnt(6)
mvv 34,232,70
R 54,34816
mvv 38,236,70
mvv 42,70,240
mvv 34,244,74
R 58,35840
mvv 38,248,74
s_waitcnt lgkmcnt(2)
v_cmp_gt_i32 vcc, v92, v122
mvv 42,74,252
s_mov_b32 s60, -28
.p2align 6
LQB0_2:
v_mfma_f32_16x16x32_bf16 v[22:25], a[0:3], v[46:49], 0
s_cbranch_vccnz CQB0_3
BQB0_3:
R 62,36864
v_mfma_f32_16x16x32_bf16 v[26:29], a[4:7], v[46:49], 0
v_mul_f32 v120, v119, v94
v_fma_f32 v34, v34, v118, v120
v_mfma_f32_16x16x32_bf16 v[30:33], v[46:49], a[8:11], 0
v_mul_f32 v121, v119, v95
v_fma_f32 v35, v35, v118, v121
mav 22,12,50
R 66,37888
v_mul_f32 v120, v119, v96
mav 26,16,50
v_fma_f32 v36, v36, v118, v120
v_mul_f32 v121, v119, v97
mva 30,50,20
v_fma_f32 v37, v37, v118, v121
s_waitcnt lgkmcnt(2)
mav 22,24,54
R 70,38912
v_mul_f32 v120, v119, v98
mav 26,28,54
v_fma_f32 v38, v38, v118, v120
v_mul_f32 v121, v119, v99
mva 30,54,32
v_fma_f32 v39, v39, v118, v121
v_mul_f32 v120, v119, v100
mav 22,36,58
R 74,39936
v_fma_f32 v40, v40, v118, v120
mav 26,40,58
v_mul_f32 v121, v119, v101
v_fma_f32 v41, v41, v118, v121
mva 30,58,44
v_cvt_pk_bf16_f32 v110, v34, v35
s_waitcnt lgkmcnt(2)
mav 22,48,62
R 46,40960
v_cvt_pk_bf16_f32 v111, v36, v37
mav 26,52,62
v_cvt_pk_bf16_f32 v112, v38, v39
v_cvt_pk_bf16_f32 v113, v40, v41
mva 30,62,56
ds_write_b128 v83, v[110:113] offset:4096
mav 22,60,66
R 50,41984
v_mul_f32 v103, v103, v116
mav 26,64,66
v_fma_f32 v42, v42, v102, v103
v_mul_f32 v105, v105, v116
mva 30,66,68
v_fma_f32 v43, v43, v104, v105
s_waitcnt lgkmcnt(3)
mav 22,72,70
R 54,43008
v_mul_f32 v107, v107, v116
mav 26,76,70
v_fma_f32 v44, v44, v106, v107
v_mul_f32 v109, v109, v116
mva 30,70,80
v_fma_f32 v45, v45, v108, v109
v_cvt_pk_bf16_f32 v78, v42, v43
mav 22,84,74
R 58,44032
v_cvt_pk_bf16_f32 v79, v44, v45
mav 26,88,74
ds_write_b64 v83, v[78:79] offset:5120
mva 30,74,92
s_waitcnt lgkmcnt(3)
mav 22,96,46
R 62,45056
s_mov_b32 m0, 142336
mav 26,100,46
s_waitcnt vmcnt(17) lgkmcnt(1)
s_barrier
mva 30,46,104
s_add_u32 s49, s49, 346112
buffer_load_dword v90, s[36:39], 0 offen sc1 lds
mav 22,108,50
R 66,46080
s_add_u32 m0, s50, 0
mav 26,112,50
mva 30,50,116
D 86,0
mav 22,120,54
R 70,47104
mav 26,124,54
D 86,1024
mva 30,54,128
mav 22,132,58
R 74,48128
mav 26,136,58
D 86,2048
mva 30,58,140
s_waitcnt lgkmcnt(2)
mav 22,144,62
R 46,49152
D 86,3072
mav 26,148,62
s_add_u32 m0, s50, 8192
mva 30,62,152
mav 22,156,66
R 50,50176
D 87,0
mav 26,160,66
v_add_u32 v92, 1, v92
mva 30,66,164
D 87,1024
s_waitcnt lgkmcnt(2)
mav 22,168,70
R 54,51200
mav 26,172,70
mva 30,70,176
D 87,2048
mav 22,180,74
R 58,52224
mav 26,184,74
D 87,3072
mva 30,74,188
s_add_u32 m0, s50, 16384
s_waitcnt lgkmcnt(2)
mav 22,192,46
R 62,53248
mav 26,196,46
D 88,0
mva 30,46,200
mav 22,204,50
R 66,54272
D 88,1024
mav 26,208,50
mva 30,50,212
s_waitcnt lgkmcnt(2)
mav 22,216,54
R 70,55296
D 88,2048
mav 26,220,54
mva 30,54,224
D 88,3072
mav 22,228,58
R 74,56320
s_add_u32 m0, s50, 24576
mav 26,232,58
mva 30,58,236
D 89,0
s_waitcnt lgkmcnt(2)
mav 22,240,62
R 46,57344
mav 26,244,62
D 89,1024
mva 30,62,248
mav 22,252,66
R 50,58368
mvv 26,128,66
D 89,2048
mvv 30,66,132
s_waitcnt lgkmcnt(2)
mvv 22,136,70
R 54,59392
D 89,3072
mvv 26,140,70
mvv 30,70,144
mvv 22,148,74
R 58,60416
mvv 26,152,74
mvv 30,74,156
s_waitcnt lgkmcnt(2)
mvv 22,160,46
R 62,61440
mvv 26,164,46
mvv 30,46,168
mvv 22,172,50
R 66,62464
mvv 26,176,50
mvv 30,50,180
s_waitcnt lgkmcnt(2)
mvv 22,184,54
R 70,63488
mvv 26,188,54
mvv 30,54,192
mvv 22,196,58
R 74,64512
mvv 26,200,58
mvv 30,58,204
s_waitcnt lgkmcnt(2)
mvv 22,208,62
R 46,0
mvv 26,212,62
ds_read_b32 v122, v82 offset:0
ds_read_b64 v[118:119], v84 offset:0
mvv 30,62,216
ds_read_b128 v[102:105], v85 offset:0
ds_read_b128 v[106:109], v85 offset:16
mvv 22,220,66
R 50,1024
mvv 26,224,66
mvv 30,66,228
s_waitcnt lgkmcnt(6)
mvv 22,232,70
R 54,2048
mvv 26,236,70
mvv 30,70,240
mvv 22,244,74
R 58,3072
mvv 26,248,74
s_waitcnt lgkmcnt(2)
v_cmp_gt_i32 vcc, v92, v122
mvv 30,74,252
s_cbranch_vccnz CQB0_4
BQB0_4:
v_mfma_f32_16x16x32_bf16 v[34:37], a[0:3], v[46:49], 0
R 62,4096
v_mul_f32 v120, v119, v94
v_mfma_f32_16x16x32_bf16 v[38:41], a[4:7], v[46:49], 0
v_fma_f32 v22, v22, v118, v120
v_mul_f32 v121, v119, v95
v_mfma_f32_16x16x32_bf16 v[42:45], v[46:49], a[8:11], 0
v_fma_f32 v23, v23, v118, v121
v_mul_f32 v120, v119, v96
mav 34,12,50
R 66,5120
v_fma_f32 v24, v24, v118, v120
mav 38,16,50
v_mul_f32 v121, v119, v97
v_fma_f32 v25, v25, v118, v121
mva 42,50,20
v_mul_f32 v120, v119, v98
s_waitcnt lgkmcnt(2)
mav 34,24,54
R 70,6144
v_fma_f32 v26, v26, v118, v120
mav 38,28,54
v_mul_f32 v121, v119, v99
v_fma_f32 v27, v27, v118, v121
mva 42,54,32
v_mul_f32 v120, v119, v100
v_fma_f32 v28, v28, v118, v120
mav 34,36,58
R 74,7168
v_mul_f32 v121, v119, v101
mav 38,40,58
v_fma_f32 v29, v29, v118, v121
v_cvt_pk_bf16_f32 v110, v22, v23
mva 42,58,44
v_cvt_pk_bf16_f32 v111, v24, v25
s_waitcnt lgkmcnt(2)
mav 34,48,62
R 46,8192
v_cvt_pk_bf16_f32 v112, v26, v27
mav 38,52,62
v_cvt_pk_bf16_f32 v113, v28, v29
v_mul_f32 v103, v103, v116
mva 42,62,56
ds_write_b128 v83, v[110:113] offset:0
mav 34,60,66
R 50,9216
v_fma_f32 v30, v30, v102, v103
mav 38,64,66
v_mul_f32 v105, v105, v116
v_fma_f32 v31, v31, v104, v105
mva 42,66,68
v_mul_f32 v107, v107, v116
s_waitcnt lgkmcnt(3)
mav 34,72,70
R 54,10240
v_fma_f32 v32, v32, v106, v107
mav 38,76,70
v_mul_f32 v109, v109, v116
v_fma_f32 v33, v33, v108, v109
mva 42,70,80
s_xor_b32 s50, s50, 0x10000
v_cvt_pk_bf16_f32 v78, v30, v31
mav 34,84,74
R 58,11264
v_cvt_pk_bf16_f32 v79, v32, v33
mav 38,88,74
ds_write_b64 v83, v[78:79] offset:1024
mva 42,74,92
s_waitcnt lgkmcnt(3)
mav 34,96,46
R 62,12288
s_mov_b32 m0, 142336
mav 38,100,46
s_waitcnt vmcnt(17) lgkmcnt(1)
s_barrier
mva 42,46,104
s_add_u32 s49, s49, 346112
buffer_load_dword v90, s[36:39], 0 offen sc1 lds
mav 34,108,50
R 66,13312
s_add_u32 m0, s50, 32768
mav 38,112,50
mva 42,50,116
D 86,0
mav 34,120,54
R 70,14336
mav 38,124,54
D 86,1024
mva 42,54,128
mav 34,132,58
R 74,15360
mav 38,136,58
D 86,2048
mva 42,58,140
s_waitcnt lgkmcnt(2)
mav 34,144,62
R 46,16384
D 86,3072
mav 38,148,62
s_add_u32 m0, s50, 40960
mva 42,62,152
mav 34,156,66
R 50,17408
D 87,0
mav 38,160,66
v_add_u32 v92, 1, v92
mva 42,66,164
D 87,1024
s_waitcnt lgkmcnt(2)
mav 34,168,70
R 54,18432
mav 38,172,70
mva 42,70,176
D 87,2048
mav 34,180,74
R 58,19456
mav 38,184,74
D 87,3072
mva 42,74,188
s_add_u32 m0, s50, 49152
s_waitcnt lgkmcnt(2)
mav 34,192,46
R 62,20480
mav 38,196,46
D 88,0
mva 42,46,200
mav 34,204,50
R 66,21504
D 88,1024
mav 38,208,50
mva 42,50,212
s_waitcnt lgkmcnt(2)
mav 34,216,54
R 70,22528
D 88,2048
mav 38,220,54
mva 42,54,224
D 88,3072
mav 34,228,58
R 74,23552
s_add_u32 m0, s50, 57344
mav 38,232,58
mva 42,58,236
D 89,0
s_waitcnt lgkmcnt(2)
mav 34,240,62
R 46,24576
mav 38,244,62
D 89,1024
mva 42,62,248
mav 34,252,66
R 50,25600
mvv 38,128,66
D 89,2048
mvv 42,66,132
s_waitcnt lgkmcnt(2)
mvv 34,136,70
R 54,26624
D 89,3072
mvv 38,140,70
mvv 42,70,144
mvv 34,148,74
R 58,27648
mvv 38,152,74
mvv 42,74,156
s_waitcnt lgkmcnt(2)
mvv 34,160,46
R 62,28672
mvv 38,164,46
mvv 42,46,168
mvv 34,172,50
R 66,29696
mvv 38,176,50
mvv 42,50,180
s_waitcnt lgkmcnt(2)
mvv 34,184,54
R 70,30720
mvv 38,188,54
mvv 42,54,192
mvv 34,196,58
R 74,31744
v_xor_b32 v80, 0x10000, v80
mvv 38,200,58
LsQ12:
s_cbranch_execz LsQ16
mvv 42,58,204
s_waitcnt lgkmcnt(2)
mvv 34,208,62
R 46,32768
mvv 38,212,62
ds_read_b32 v122, v82 offset:0
ds_read_b64 v[118:119], v84 offset:128
mvv 42,62,216
ds_read_b128 v[102:105], v85 offset:128
ds_read_b128 v[106:109], v85 offset:144
mvv 34,220,66
R 50,33792
mvv 38,224,66
mvv 42,66,228
s_waitcnt lgkmcnt(6)
mvv 34,232,70
R 54,34816
mvv 38,236,70
mvv 42,70,240
mvv 34,244,74
R 58,35840
mvv 38,248,74
s_waitcnt lgkmcnt(2)
v_cmp_gt_i32 vcc, v92, v122
mvv 42,74,252
s_add_u32 s60, s60, 1
s_cbranch_scc0 LQB0_2
v_mfma_f32_16x16x32_bf16 v[22:25], a[0:3], v[46:49], 0
s_cbranch_vccnz CQB0_5
BQB0_5:
R 62,36864
v_mfma_f32_16x16x32_bf16 v[26:29], a[4:7], v[46:49], 0
v_mul_f32 v120, v119, v94
v_fma_f32 v34, v34, v118, v120
v_mfma_f32_16x16x32_bf16 v[30:33], v[46:49], a[8:11], 0
v_mul_f32 v121, v119, v95
v_fma_f32 v35, v35, v118, v121
mav 22,12,50
R 66,37888
v_mul_f32 v120, v119, v96
mav 26,16,50
v_fma_f32 v36, v36, v118, v120
v_mul_f32 v121, v119, v97
mva 30,50,20
v_fma_f32 v37, v37, v118, v121
s_waitcnt lgkmcnt(2)
mav 22,24,54
R 70,38912
v_mul_f32 v120, v119, v98
mav 26,28,54
v_fma_f32 v38, v38, v118, v120
v_mul_f32 v121, v119, v99
mva 30,54,32
v_fma_f32 v39, v39, v118, v121
v_mul_f32 v120, v119, v100
mav 22,36,58
R 74,39936
v_fma_f32 v40, v40, v118, v120
mav 26,40,58
v_mul_f32 v121, v119, v101
v_fma_f32 v41, v41, v118, v121
mva 30,58,44
v_cvt_pk_bf16_f32 v110, v34, v35
s_waitcnt lgkmcnt(2)
mav 22,48,62
R 46,40960
v_cvt_pk_bf16_f32 v111, v36, v37
mav 26,52,62
v_cvt_pk_bf16_f32 v112, v38, v39
v_cvt_pk_bf16_f32 v113, v40, v41
mva 30,62,56
ds_write_b128 v83, v[110:113] offset:4096
mav 22,60,66
R 50,41984
v_mul_f32 v103, v103, v116
mav 26,64,66
v_fma_f32 v42, v42, v102, v103
v_mul_f32 v105, v105, v116
mva 30,66,68
v_fma_f32 v43, v43, v104, v105
s_waitcnt lgkmcnt(3)
mav 22,72,70
R 54,43008
v_mul_f32 v107, v107, v116
mav 26,76,70
v_fma_f32 v44, v44, v106, v107
v_mul_f32 v109, v109, v116
mva 30,70,80
v_fma_f32 v45, v45, v108, v109
v_cvt_pk_bf16_f32 v78, v42, v43
mav 22,84,74
R 58,44032
v_cvt_pk_bf16_f32 v79, v44, v45
mav 26,88,74
ds_write_b64 v83, v[78:79] offset:5120
mva 30,74,92
s_waitcnt lgkmcnt(3)
mav 22,96,46
R 62,45056
s_mov_b32 m0, 142336
mav 26,100,46
s_waitcnt vmcnt(17) lgkmcnt(1)
s_barrier
mva 30,46,104
s_add_u32 s49, s49, 346112
buffer_load_dword v90, s[36:39], 0 offen sc1 lds
mav 22,108,50
R 66,46080
s_add_u32 m0, s50, 0
mav 26,112,50
mva 30,50,116
D 86,0
mav 22,120,54
R 70,47104
mav 26,124,54
D 86,1024
mva 30,54,128
mav 22,132,58
R 74,48128
mav 26,136,58
D 86,2048
mva 30,58,140
s_waitcnt lgkmcnt(2)
mav 22,144,62
R 46,49152
D 86,3072
mav 26,148,62
s_add_u32 m0, s50, 8192
mva 30,62,152
mav 22,156,66
R 50,50176
D 87,0
mav 26,160,66
v_add_u32 v92, 1, v92
mva 30,66,164
D 87,1024
s_waitcnt lgkmcnt(2)
mav 22,168,70
R 54,51200
mav 26,172,70
mva 30,70,176
D 87,2048
mav 22,180,74
R 58,52224
mav 26,184,74
D 87,3072
)"
R"(
mva 30,74,188
s_add_u32 m0, s50, 16384
s_waitcnt lgkmcnt(2)
mav 22,192,46
R 62,53248
mav 26,196,46
D 88,0
mva 30,46,200
mav 22,204,50
R 66,54272
D 88,1024
mav 26,208,50
mva 30,50,212
s_waitcnt lgkmcnt(2)
mav 22,216,54
R 70,55296
D 88,2048
mav 26,220,54
mva 30,54,224
D 88,3072
mav 22,228,58
R 74,56320
s_add_u32 m0, s50, 24576
mav 26,232,58
mva 30,58,236
D 89,0
s_waitcnt lgkmcnt(2)
mav 22,240,62
R 46,57344
mav 26,244,62
D 89,1024
mva 30,62,248
mav 22,252,66
R 50,58368
mvv 26,128,66
D 89,2048
mvv 30,66,132
s_waitcnt lgkmcnt(2)
mvv 22,136,70
R 54,59392
D 89,3072
mvv 26,140,70
mvv 30,70,144
mvv 22,148,74
R 58,60416
mvv 26,152,74
mvv 30,74,156
s_waitcnt lgkmcnt(2)
mvv 22,160,46
R 62,61440
mvv 26,164,46
mvv 30,46,168
mvv 22,172,50
R 66,62464
mvv 26,176,50
mvv 30,50,180
s_waitcnt lgkmcnt(2)
mvv 22,184,54
R 70,63488
mvv 26,188,54
mvv 30,54,192
mvv 22,196,58
R 74,64512
mvv 26,200,58
mvv 30,58,204
s_waitcnt lgkmcnt(2)
mvv 22,208,62
R 46,0
mvv 26,212,62
ds_read_b32 v122, v82 offset:0
ds_read_b64 v[118:119], v84 offset:0
mvv 30,62,216
ds_read_b128 v[102:105], v85 offset:0
ds_read_b128 v[106:109], v85 offset:16
mvv 22,220,66
R 50,1024
mvv 26,224,66
mvv 30,66,228
s_waitcnt lgkmcnt(6)
mvv 22,232,70
R 54,2048
mvv 26,236,70
mvv 30,70,240
mvv 22,244,74
R 58,3072
mvv 26,248,74
s_waitcnt lgkmcnt(2)
v_cmp_gt_i32 vcc, v92, v122
mvv 30,74,252
s_cbranch_vccnz CQB0_6
BQB0_6:
v_mfma_f32_16x16x32_bf16 v[34:37], a[0:3], v[46:49], 0
R 62,4096
v_mul_f32 v120, v119, v94
v_mfma_f32_16x16x32_bf16 v[38:41], a[4:7], v[46:49], 0
v_fma_f32 v22, v22, v118, v120
v_mul_f32 v121, v119, v95
v_mfma_f32_16x16x32_bf16 v[42:45], v[46:49], a[8:11], 0
v_fma_f32 v23, v23, v118, v121
v_mul_f32 v120, v119, v96
mav 34,12,50
R 66,5120
v_fma_f32 v24, v24, v118, v120
mav 38,16,50
v_mul_f32 v121, v119, v97
v_fma_f32 v25, v25, v118, v121
mva 42,50,20
v_mul_f32 v120, v119, v98
s_waitcnt lgkmcnt(2)
mav 34,24,54
R 70,6144
v_fma_f32 v26, v26, v118, v120
mav 38,28,54
v_mul_f32 v121, v119, v99
v_fma_f32 v27, v27, v118, v121
mva 42,54,32
v_mul_f32 v120, v119, v100
v_fma_f32 v28, v28, v118, v120
mav 34,36,58
R 74,7168
v_mul_f32 v121, v119, v101
mav 38,40,58
v_fma_f32 v29, v29, v118, v121
v_cvt_pk_bf16_f32 v110, v22, v23
mva 42,58,44
v_cvt_pk_bf16_f32 v111, v24, v25
s_waitcnt lgkmcnt(2)
mav 34,48,62
R 46,8192
v_cvt_pk_bf16_f32 v112, v26, v27
mav 38,52,62
v_cvt_pk_bf16_f32 v113, v28, v29
v_mul_f32 v103, v103, v116
mva 42,62,56
ds_write_b128 v83, v[110:113] offset:0
mav 34,60,66
R 50,9216
v_fma_f32 v30, v30, v102, v103
mav 38,64,66
v_mul_f32 v105, v105, v116
v_fma_f32 v31, v31, v104, v105
mva 42,66,68
v_mul_f32 v107, v107, v116
s_waitcnt lgkmcnt(3)
mav 34,72,70
R 54,10240
v_fma_f32 v32, v32, v106, v107
mav 38,76,70
v_mul_f32 v109, v109, v116
v_fma_f32 v33, v33, v108, v109
mva 42,70,80
v_cvt_pk_bf16_f32 v78, v30, v31
v_cvt_pk_bf16_f32 v79, v32, v33
mav 34,84,74
R 58,11264
mav 38,88,74
ds_write_b64 v83, v[78:79] offset:1024
mva 42,74,92
s_waitcnt lgkmcnt(3)
mav 34,96,46
R 62,12288
s_add_u32 s49, s49, 346112
mav 38,100,46
s_waitcnt vmcnt(17) lgkmcnt(1)
s_barrier
mva 42,46,104
mav 34,108,50
R 66,13312
mav 38,112,50
mva 42,50,116
mav 34,120,54
R 70,14336
mav 38,124,54
mva 42,54,128
mav 34,132,58
R 74,15360
mav 38,136,58
mva 42,58,140
s_waitcnt lgkmcnt(2)
mav 34,144,62
R 46,16384
mav 38,148,62
mva 42,62,152
mav 34,156,66
R 50,17408
mav 38,160,66
mva 42,66,164
s_waitcnt lgkmcnt(2)
mav 34,168,70
R 54,18432
mav 38,172,70
mva 42,70,176
mav 34,180,74
R 58,19456
mav 38,184,74
mva 42,74,188
s_waitcnt lgkmcnt(2)
mav 34,192,46
R 62,20480
mav 38,196,46
mva 42,46,200
mav 34,204,50
R 66,21504
mav 38,208,50
mva 42,50,212
s_waitcnt lgkmcnt(2)
mav 34,216,54
R 70,22528
mav 38,220,54
mva 42,54,224
mav 34,228,58
R 74,23552
mav 38,232,58
mva 42,58,236
s_waitcnt lgkmcnt(2)
mav 34,240,62
R 46,24576
mav 38,244,62
mva 42,62,248
mav 34,252,66
R 50,25600
mvv 38,128,66
mvv 42,66,132
s_waitcnt lgkmcnt(2)
mvv 34,136,70
R 54,26624
mvv 38,140,70
mvv 42,70,144
mvv 34,148,74
R 58,27648
mvv 38,152,74
mvv 42,74,156
s_waitcnt lgkmcnt(2)
mvv 34,160,46
R 62,28672
mvv 38,164,46
mvv 42,46,168
mvv 34,172,50
R 66,29696
mvv 38,176,50
mvv 42,50,180
s_waitcnt lgkmcnt(2)
mvv 34,184,54
R 70,30720
mvv 38,188,54
mvv 42,54,192
mvv 34,196,58
R 74,31744
v_xor_b32 v80, 0x10000, v80
mvv 38,200,58
mvv 42,58,204
s_waitcnt lgkmcnt(2)
mvv 34,208,62
R 46,32768
mvv 38,212,62
ds_read_b64 v[118:119], v84 offset:128
ds_read_b128 v[102:105], v85 offset:128
mvv 42,62,216
ds_read_b128 v[106:109], v85 offset:144
mvv 34,220,66
R 50,33792
mvv 38,224,66
mvv 42,66,228
s_waitcnt lgkmcnt(5)
mvv 34,232,70
R 54,34816
mvv 38,236,70
mvv 42,70,240
mvv 34,244,74
R 58,35840
mvv 38,248,74
mvv 42,74,252
s_waitcnt lgkmcnt(2)
v_mfma_f32_16x16x32_bf16 v[22:25], a[0:3], v[46:49], 0
R 62,36864
v_mul_f32 v120, v119, v94
v_mfma_f32_16x16x32_bf16 v[26:29], a[4:7], v[46:49], 0
v_fma_f32 v34, v34, v118, v120
v_mul_f32 v121, v119, v95
v_mfma_f32_16x16x32_bf16 v[30:33], v[46:49], a[8:11], 0
v_fma_f32 v35, v35, v118, v121
v_mul_f32 v120, v119, v96
mav 22,12,50
R 66,37888
v_fma_f32 v36, v36, v118, v120
mav 26,16,50
v_mul_f32 v121, v119, v97
v_fma_f32 v37, v37, v118, v121
mva 30,50,20
v_mul_f32 v120, v119, v98
s_waitcnt lgkmcnt(2)
mav 22,24,54
R 70,38912
v_fma_f32 v38, v38, v118, v120
mav 26,28,54
v_mul_f32 v121, v119, v99
v_fma_f32 v39, v39, v118, v121
mva 30,54,32
v_mul_f32 v120, v119, v100
v_fma_f32 v40, v40, v118, v120
mav 22,36,58
R 74,39936
v_mul_f32 v121, v119, v101
mav 26,40,58
v_fma_f32 v41, v41, v118, v121
v_cvt_pk_bf16_f32 v110, v34, v35
mva 30,58,44
v_cvt_pk_bf16_f32 v111, v36, v37
s_waitcnt lgkmcnt(2)
mav 22,48,62
R 46,40960
v_cvt_pk_bf16_f32 v112, v38, v39
mav 26,52,62
v_cvt_pk_bf16_f32 v113, v40, v41
v_mul_f32 v103, v103, v116
mva 30,62,56
ds_write_b128 v83, v[110:113] offset:4096
mav 22,60,66
R 50,41984
v_fma_f32 v42, v42, v102, v103
mav 26,64,66
v_mul_f32 v105, v105, v116
v_fma_f32 v43, v43, v104, v105
mva 30,66,68
v_mul_f32 v107, v107, v116
s_waitcnt lgkmcnt(3)
mav 22,72,70
R 54,43008
v_fma_f32 v44, v44, v106, v107
mav 26,76,70
v_mul_f32 v109, v109, v116
v_fma_f32 v45, v45, v108, v109
mva 30,70,80
v_cvt_pk_bf16_f32 v78, v42, v43
v_cvt_pk_bf16_f32 v79, v44, v45
mav 22,84,74
R 58,44032
mav 26,88,74
ds_write_b64 v83, v[78:79] offset:5120
mva 30,74,92
s_waitcnt lgkmcnt(3)
mav 22,96,46
R 62,45056
s_add_u32 s49, s49, 346112
mav 26,100,46
s_waitcnt vmcnt(0) lgkmcnt(1)
s_barrier
mva 30,46,104
mav 22,108,50
R 66,46080
mav 26,112,50
mva 30,50,116
mav 22,120,54
R 70,47104
mav 26,124,54
mva 30,54,128
mav 22,132,58
R 74,48128
mav 26,136,58
mva 30,58,140
s_waitcnt lgkmcnt(2)
mav 22,144,62
R 46,49152
mav 26,148,62
mva 30,62,152
mav 22,156,66
R 50,50176
mav 26,160,66
mva 30,66,164
s_waitcnt lgkmcnt(2)
mav 22,168,70
R 54,51200
mav 26,172,70
mva 30,70,176
mav 22,180,74
R 58,52224
mav 26,184,74
mva 30,74,188
s_waitcnt lgkmcnt(2)
mav 22,192,46
R 62,53248
mav 26,196,46
mva 30,46,200
mav 22,204,50
R 66,54272
mav 26,208,50
mva 30,50,212
s_waitcnt lgkmcnt(2)
mav 22,216,54
R 70,55296
mav 26,220,54
mva 30,54,224
mav 22,228,58
R 74,56320
mav 26,232,58
mva 30,58,236
s_waitcnt lgkmcnt(2)
mav 22,240,62
R 46,57344
mav 26,244,62
mva 30,62,248
mav 22,252,66
R 50,58368
mvv 26,128,66
mvv 30,66,132
s_waitcnt lgkmcnt(2)
mvv 22,136,70
R 54,59392
mvv 26,140,70
mvv 30,70,144
mvv 22,148,74
R 58,60416
mvv 26,152,74
mvv 30,74,156
LsQ13:
s_cbranch_execz LsQ17
s_waitcnt lgkmcnt(2)
mvv 22,160,46
R 62,61440
mvv 26,164,46
mvv 30,46,168
mvv 22,172,50
R 66,62464
mvv 26,176,50
mvv 30,50,180
s_waitcnt lgkmcnt(2)
mvv 22,184,54
R 70,63488
mvv 26,188,54
mvv 30,54,192
mvv 22,196,58
R 74,64512
mvv 26,200,58
mvv 30,58,204
s_waitcnt lgkmcnt(2)
mvv 22,208,62
R 46,0
mvv 26,212,62
ds_read_b64 v[118:119], v84 offset:0
ds_read_b128 v[102:105], v85 offset:0
mvv 30,62,216
ds_read_b128 v[106:109], v85 offset:16
mvv 22,220,66
R 50,1024
mvv 26,224,66
mvv 30,66,228
s_waitcnt lgkmcnt(5)
mvv 22,232,70
R 54,2048
mvv 26,236,70
mvv 30,70,240
mvv 22,244,74
R 58,3072
mvv 26,248,74
mvv 30,74,252
s_waitcnt lgkmcnt(2)
v_mfma_f32_16x16x32_bf16 v[34:37], a[0:3], v[46:49], 0
R 62,4096
v_mul_f32 v120, v119, v94
v_mfma_f32_16x16x32_bf16 v[38:41], a[4:7], v[46:49], 0
v_fma_f32 v22, v22, v118, v120
v_mul_f32 v121, v119, v95
v_mfma_f32_16x16x32_bf16 v[42:45], v[46:49], a[8:11], 0
v_fma_f32 v23, v23, v118, v121
v_mul_f32 v120, v119, v96
mav 34,12,50
R 66,5120
v_fma_f32 v24, v24, v118, v120
mav 38,16,50
v_mul_f32 v121, v119, v97
v_fma_f32 v25, v25, v118, v121
mva 42,50,20
v_mul_f32 v120, v119, v98
s_waitcnt lgkmcnt(2)
mav 34,24,54
R 70,6144
v_fma_f32 v26, v26, v118, v120
mav 38,28,54
v_mul_f32 v121, v119, v99
v_fma_f32 v27, v27, v118, v121
mva 42,54,32
v_mul_f32 v120, v119, v100
v_fma_f32 v28, v28, v118, v120
mav 34,36,58
R 74,7168
v_mul_f32 v121, v119, v101
mav 38,40,58
v_fma_f32 v29, v29, v118, v121
v_cvt_pk_bf16_f32 v110, v22, v23
mva 42,58,44
v_cvt_pk_bf16_f32 v111, v24, v25
s_waitcnt lgkmcnt(2)
mav 34,48,62
R 46,8192
v_cvt_pk_bf16_f32 v112, v26, v27
mav 38,52,62
v_cvt_pk_bf16_f32 v113, v28, v29
v_mul_f32 v103, v103, v116
mva 42,62,56
ds_write_b128 v83, v[110:113] offset:0
mav 34,60,66
R 50,9216
v_fma_f32 v30, v30, v102, v103
mav 38,64,66
v_mul_f32 v105, v105, v116
v_fma_f32 v31, v31, v104, v105
mva 42,66,68
v_mul_f32 v107, v107, v116
s_waitcnt lgkmcnt(3)
mav 34,72,70
R 54,10240
v_fma_f32 v32, v32, v106, v107
mav 38,76,70
v_mul_f32 v109, v109, v116
v_fma_f32 v33, v33, v108, v109
mva 42,70,80
v_cvt_pk_bf16_f32 v78, v30, v31
v_cvt_pk_bf16_f32 v79, v32, v33
mav 34,84,74
R 58,11264
mav 38,88,74
ds_write_b64 v83, v[78:79] offset:1024
mva 42,74,92
s_waitcnt lgkmcnt(3)
mav 34,96,46
R 62,12288
mav 38,100,46
s_waitcnt lgkmcnt(1)
s_barrier
mva 42,46,104
mav 34,108,50
R 66,13312
mav 38,112,50
mva 42,50,116
mav 34,120,54
R 70,14336
mav 38,124,54
mva 42,54,128
mav 34,132,58
R 74,15360
mav 38,136,58
mva 42,58,140
s_waitcnt lgkmcnt(2)
mav 34,144,62
R 46,16384
mav 38,148,62
mva 42,62,152
mav 34,156,66
R 50,17408
mav 38,160,66
mva 42,66,164
s_waitcnt lgkmcnt(2)
mav 34,168,70
R 54,18432
mav 38,172,70
mva 42,70,176
mav 34,180,74
R 58,19456
mav 38,184,74
mva 42,74,188
s_waitcnt lgkmcnt(2)
mav 34,192,46
R 62,20480
mav 38,196,46
mva 42,46,200
mav 34,204,50
R 66,21504
mav 38,208,50
mva 42,50,212
s_waitcnt lgkmcnt(2)
mav 34,216,54
R 70,22528
mav 38,220,54
mva 42,54,224
mav 34,228,58
R 74,23552
mav 38,232,58
mva 42,58,236
s_waitcnt lgkmcnt(2)
mav 34,240,62
R 46,24576
mav 38,244,62
mva 42,62,248
mav 34,252,66
R 50,25600
mvv 38,128,66
mvv 42,66,132
s_waitcnt lgkmcnt(2)
mvv 34,136,70
R 54,26624
mvv 38,140,70
mvv 42,70,144
mvv 34,148,74
R 58,27648
mvv 38,152,74
mvv 42,74,156
s_waitcnt lgkmcnt(2)
mvv 34,160,46
R 62,28672
mvv 38,164,46
mvv 42,46,168
mvv 34,172,50
R 66,29696
mvv 38,176,50
mvv 42,50,180
s_waitcnt lgkmcnt(2)
mvv 34,184,54
R 70,30720
mvv 38,188,54
mvv 42,54,192
mvv 34,196,58
R 74,31744
mvv 38,200,58
mvv 42,58,204
s_waitcnt lgkmcnt(2)
mvv 34,208,62
mvv 38,212,62
ds_read_b64 v[118:119], v84 offset:128
ds_read_b128 v[102:105], v85 offset:128
mvv 42,62,216
ds_read_b128 v[106:109], v85 offset:144
mvv 34,220,66
mvv 38,224,66
mvv 42,66,228
s_waitcnt lgkmcnt(3)
mvv 34,232,70
mvv 38,236,70
mvv 42,70,240
mvv 34,244,74
mvv 38,248,74
mvv 42,74,252
s_waitcnt lgkmcnt(0)
v_mul_f32 v120, v119, v94
s_nop 3
v_fma_f32 v34, v34, v118, v120
v_mul_f32 v121, v119, v95
v_fma_f32 v35, v35, v118, v121
v_mul_f32 v120, v119, v96
v_fma_f32 v36, v36, v118, v120
v_mul_f32 v121, v119, v97
v_fma_f32 v37, v37, v118, v121
v_mul_f32 v120, v119, v98
v_fma_f32 v38, v38, v118, v120
v_mul_f32 v121, v119, v99
v_fma_f32 v39, v39, v118, v121
v_mul_f32 v120, v119, v100
v_fma_f32 v40, v40, v118, v120
v_mul_f32 v121, v119, v101
v_fma_f32 v41, v41, v118, v121
v_cvt_pk_bf16_f32 v110, v34, v35
v_cvt_pk_bf16_f32 v111, v36, v37
v_cvt_pk_bf16_f32 v112, v38, v39
v_cvt_pk_bf16_f32 v113, v40, v41
ds_write_b128 v83, v[110:113] offset:4096
v_mul_f32 v103, v103, v116
v_fma_f32 v42, v42, v102, v103
v_mul_f32 v105, v105, v116
v_fma_f32 v43, v43, v104, v105
v_mul_f32 v107, v107, v116
v_fma_f32 v44, v44, v106, v107
v_mul_f32 v109, v109, v116
v_fma_f32 v45, v45, v108, v109
v_cvt_pk_bf16_f32 v78, v42, v43
v_cvt_pk_bf16_f32 v79, v44, v45
ds_write_b64 v83, v[78:79] offset:5120
s_waitcnt lgkmcnt(0)
s_barrier
s_endpgm
CQB0_1:
s_mov_b32 s58, m0
s_mov_b32 m0, 142336
DQB0_1:
s_cmp_eq_u32 s61, 0
s_cbranch_scc1 EQB0_1
s_sub_u32 s61, s61, 1
buffer_load_dword v90, s[36:39], 0 offen sc1 lds
s_sleep 1
ds_read_b32 v122, v82
s_waitcnt lgkmcnt(0)
v_cmp_gt_i32 vcc, v92, v122
s_cbranch_vccnz DQB0_1
EQB0_1:
s_mov_b32 m0, s58
s_branch BQB0_1
CQB0_3:
s_mov_b32 s58, m0
s_mov_b32 m0, 142336
DQB0_3:
s_cmp_eq_u32 s61, 0
s_cbranch_scc1 EQB0_3
s_sub_u32 s61, s61, 1
buffer_load_dword v90, s[36:39], 0 offen sc1 lds
s_sleep 1
ds_read_b32 v122, v82
s_waitcnt lgkmcnt(0)
v_cmp_gt_i32 vcc, v92, v122
s_cbranch_vccnz DQB0_3
EQB0_3:
s_mov_b32 m0, s58
s_branch BQB0_3
CQB0_4:
s_mov_b32 s58, m0
s_mov_b32 m0, 142336
DQB0_4:
s_cmp_eq_u32 s61, 0
s_cbranch_scc1 EQB0_4
s_sub_u32 s61, s61, 1
buffer_load_dword v90, s[36:39], 0 offen sc1 lds
s_sleep 1
ds_read_b32 v122, v82
s_waitcnt lgkmcnt(0)
v_cmp_gt_i32 vcc, v92, v122
s_cbranch_vccnz DQB0_4
EQB0_4:
s_mov_b32 m0, s58
s_branch BQB0_4
CQB0_5:
s_mov_b32 s58, m0
s_mov_b32 m0, 142336
DQB0_5:
s_cmp_eq_u32 s61, 0
s_cbranch_scc1 EQB0_5
s_sub_u32 s61, s61, 1
buffer_load_dword v90, s[36:39], 0 offen sc1 lds
s_sleep 1
ds_read_b32 v122, v82
s_waitcnt lgkmcnt(0)
v_cmp_gt_i32 vcc, v92, v122
s_cbranch_vccnz DQB0_5
EQB0_5:
s_mov_b32 m0, s58
s_branch BQB0_5
CQB0_6:
s_mov_b32 s58, m0
s_mov_b32 m0, 142336
DQB0_6:
s_cmp_eq_u32 s61, 0
s_cbranch_scc1 EQB0_6
s_sub_u32 s61, s61, 1
buffer_load_dword v90, s[36:39], 0 offen sc1 lds
s_sleep 1
ds_read_b32 v122, v82
s_waitcnt lgkmcnt(0)
v_cmp_gt_i32 vcc, v92, v122
s_cbranch_vccnz DQB0_6
EQB0_6:
s_mov_b32 m0, s58
s_branch BQB0_6
LvQB1:
LsQ14:
s_cbranch_execz LsQ18
v_xor_b32 v80, 0x10000, v80
ds_read_b64 v[118:119], v84 offset:0
ds_read_b128 v[102:105], v85 offset:0
ds_read_b128 v[106:109], v85 offset:16
s_add_u32 s49, s49, 346112
R 46,32768
R 50,33792
R 54,34816
R 58,35840
s_waitcnt lgkmcnt(2)
v_mul_f32 v120, v119, v94
v_fma_f32 v22, v22, v118, v120
v_mul_f32 v121, v119, v95
v_fma_f32 v23, v23, v118, v121
v_mul_f32 v120, v119, v96
v_fma_f32 v24, v24, v118, v120
v_mul_f32 v121, v119, v97
v_fma_f32 v25, v25, v118, v121
v_mul_f32 v120, v119, v98
v_fma_f32 v26, v26, v118, v120
v_mul_f32 v121, v119, v99
v_fma_f32 v27, v27, v118, v121
v_mul_f32 v120, v119, v100
v_fma_f32 v28, v28, v118, v120
v_mul_f32 v121, v119, v101
v_fma_f32 v29, v29, v118, v121
v_cvt_pk_bf16_f32 v110, v22, v23
v_cvt_pk_bf16_f32 v111, v24, v25
v_cvt_pk_bf16_f32 v112, v26, v27
v_cvt_pk_bf16_f32 v113, v28, v29
ds_write_b128 v83, v[110:113] offset:0
v_mul_f32 v103, v103, v116
v_fma_f32 v30, v30, v102, v103
v_mul_f32 v105, v105, v116
v_fma_f32 v31, v31, v104, v105
v_mul_f32 v107, v107, v116
v_fma_f32 v32, v32, v106, v107
v_mul_f32 v109, v109, v116
v_fma_f32 v33, v33, v108, v109
v_cvt_pk_bf16_f32 v78, v30, v31
v_cvt_pk_bf16_f32 v79, v32, v33
ds_write_b64 v83, v[78:79] offset:1024
s_xor_b32 s50, s50, 0x10000
s_add_u32 s49, s49, 346112
s_waitcnt lgkmcnt(0)
s_barrier
ds_read_b64 v[118:119], v84 offset:128
ds_read_b128 v[102:105], v85 offset:128
ds_read_b128 v[106:109], v85 offset:144
v_mfma_f32_16x16x32_bf16 v[22:25], a[0:3], v[46:49], 0
R 62,36864
v_mfma_f32_16x16x32_bf16 v[26:29], a[4:7], v[46:49], 0
s_waitcnt lgkmcnt(1)
v_mul_f32 v120, v119, v94
v_mfma_f32_16x16x32_bf16 v[30:33], v[46:49], a[8:11], 0
v_fma_f32 v34, v34, v118, v120
v_mul_f32 v121, v119, v95
mav 22,12,50
R 66,37888
v_fma_f32 v35, v35, v118, v121
mav 26,16,50
v_mul_f32 v120, v119, v96
v_fma_f32 v36, v36, v118, v120
mva 30,50,20
v_mul_f32 v121, v119, v97
v_fma_f32 v37, v37, v118, v121
mav 22,24,54
R 70,38912
v_mul_f32 v120, v119, v98
mav 26,28,54
v_fma_f32 v38, v38, v118, v120
v_mul_f32 v121, v119, v99
mva 30,54,32
v_fma_f32 v39, v39, v118, v121
v_mul_f32 v120, v119, v100
mav 22,36,58
R 74,39936
v_fma_f32 v40, v40, v118, v120
mav 26,40,58
v_mul_f32 v121, v119, v101
v_fma_f32 v41, v41, v118, v121
mva 30,58,44
v_cvt_pk_bf16_f32 v110, v34, v35
s_waitcnt lgkmcnt(2)
mav 22,48,62
R 46,40960
v_cvt_pk_bf16_f32 v111, v36, v37
mav 26,52,62
v_cvt_pk_bf16_f32 v112, v38, v39
v_cvt_pk_bf16_f32 v113, v40, v41
mva 30,62,56
ds_write_b128 v83, v[110:113] offset:4096
mav 22,60,66
R 50,41984
v_mul_f32 v103, v103, v116
mav 26,64,66
v_fma_f32 v42, v42, v102, v103
v_mul_f32 v105, v105, v116
mva 30,66,68
v_fma_f32 v43, v43, v104, v105
s_waitcnt lgkmcnt(3)
mav 22,72,70
R 54,43008
v_mul_f32 v107, v107, v116
mav 26,76,70
v_fma_f32 v44, v44, v106, v107
v_mul_f32 v109, v109, v116
mva 30,70,80
v_fma_f32 v45, v45, v108, v109
v_cvt_pk_bf16_f32 v78, v42, v43
mav 22,84,74
R 58,44032
v_cvt_pk_bf16_f32 v79, v44, v45
mav 26,88,74
ds_write_b64 v83, v[78:79] offset:5120
mva 30,74,92
s_waitcnt lgkmcnt(3)
mav 22,96,46
R 62,45056
mav 26,100,46
s_waitcnt vmcnt(0) lgkmcnt(1)
s_barrier
mva 30,46,104
ds_write_b64 v84, v[18:19] offset:128
mav 22,108,50
R 66,46080
s_add_u32 m0, s50, 32768
mav 26,112,50
mva 30,50,116
D 86,0
mav 22,120,54
R 70,47104
mav 26,124,54
D 86,1024
mva 30,54,128
mav 22,132,58
R 74,48128
D 86,2048
mav 26,136,58
mva 30,58,140
D 86,3072
s_waitcnt lgkmcnt(2)
mav 22,144,62
R 46,49152
s_add_u32 m0, s50, 40960
mav 26,148,62
D 87,0
mva 30,62,152
mav 22,156,66
R 50,50176
D 87,1024
mav 26,160,66
mva 30,66,164
D 87,2048
s_waitcnt lgkmcnt(2)
mav 22,168,70
R 54,51200
mav 26,172,70
D 87,3072
s_add_u32 m0, s50, 49152
mva 30,70,176
mav 22,180,74
R 58,52224
D 88,0
mav 26,184,74
mva 30,74,188
D 88,1024
s_waitcnt lgkmcnt(2)
mav 22,192,46
R 62,53248
mav 26,196,46
D 88,2048
mva 30,46,200
mav 22,204,50
R 66,54272
D 88,3072
mav 26,208,50
s_add_u32 m0, s50, 57344
mva 30,50,212
D 89,0
s_waitcnt lgkmcnt(2)
mav 22,216,54
R 70,55296
mav 26,220,54
D 89,1024
mva 30,54,224
mav 22,228,58
R 74,56320
D 89,2048
mav 26,232,58
mva 30,58,236
D 89,3072
s_waitcnt lgkmcnt(2)
mav 22,240,62
R 46,57344
s_add_u32 s49, s49, 346112
mav 26,244,62
s_add_u32 m0, s50, 0
mva 30,62,248
D 86,0
mav 22,252,66
R 50,58368
mvv 26,128,66
D 86,1024
mvv 30,66,132
s_waitcnt lgkmcnt(2)
mvv 22,136,70
R 54,59392
D 86,2048
mvv 26,140,70
mvv 30,70,144
D 86,3072
s_add_u32 m0, s50, 8192
mvv 22,148,74
R 58,60416
mvv 26,152,74
D 87,0
mvv 30,74,156
s_waitcnt lgkmcnt(2)
mvv 22,160,46
R 62,61440
D 87,1024
mvv 26,164,46
mvv 30,46,168
D 87,2048
mvv 22,172,50
R 66,62464
mvv 26,176,50
D 87,3072
s_add_u32 m0, s50, 16384
mvv 30,50,180
s_waitcnt lgkmcnt(2)
mvv 22,184,54
R 70,63488
D 88,0
mvv 26,188,54
mvv 30,54,192
D 88,1024
mvv 22,196,58
R 74,64512
mvv 26,200,58
D 88,2048
mvv 30,58,204
s_waitcnt lgkmcnt(2)
mvv 22,208,62
R 46,0
D 88,3072
mvv 26,212,62
s_add_u32 m0, s50, 24576
ds_read_b64 v[118:119], v84 offset:0
mvv 30,62,216
ds_read_b128 v[102:105], v85 offset:0
ds_read_b128 v[106:109], v85 offset:16
mvv 22,220,66
R 50,1024
D 89,0
mvv 26,224,66
mvv 30,66,228
D 89,1024
s_waitcnt lgkmcnt(5)
mvv 22,232,70
R 54,2048
mvv 26,236,70
D 89,2048
mvv 30,70,240
mvv 22,244,74
LsQ15:
s_cbranch_execz LwQB
R 58,3072
D 89,3072
mvv 26,248,74
mvv 30,74,252
s_waitcnt lgkmcnt(2)
v_mfma_f32_16x16x32_bf16 v[34:37], a[0:3], v[46:49], 0
R 62,4096
v_mul_f32 v120, v119, v94
v_mfma_f32_16x16x32_bf16 v[38:41], a[4:7], v[46:49], 0
v_fma_f32 v22, v22, v118, v120
v_mul_f32 v121, v119, v95
v_mfma_f32_16x16x32_bf16 v[42:45], v[46:49], a[8:11], 0
v_fma_f32 v23, v23, v118, v121
v_mul_f32 v120, v119, v96
mav 34,12,50
R 66,5120
v_fma_f32 v24, v24, v118, v120
mav 38,16,50
v_mul_f32 v121, v119, v97
v_fma_f32 v25, v25, v118, v121
mva 42,50,20
v_mul_f32 v120, v119, v98
s_waitcnt lgkmcnt(2)
mav 34,24,54
R 70,6144
v_fma_f32 v26, v26, v118, v120
mav 38,28,54
v_mul_f32 v121, v119, v99
v_fma_f32 v27, v27, v118, v121
mva 42,54,32
v_mul_f32 v120, v119, v100
v_fma_f32 v28, v28, v118, v120
mav 34,36,58
R 74,7168
v_mul_f32 v121, v119, v101
mav 38,40,58
v_fma_f32 v29, v29, v118, v121
v_cvt_pk_bf16_f32 v110, v22, v23
mva 42,58,44
v_cvt_pk_bf16_f32 v111, v24, v25
s_waitcnt lgkmcnt(2)
mav 34,48,62
R 46,8192
v_cvt_pk_bf16_f32 v112, v26, v27
mav 38,52,62
v_cvt_pk_bf16_f32 v113, v28, v29
v_mul_f32 v103, v103, v116
mva 42,62,56
ds_write_b128 v83, v[110:113] offset:0
mav 34,60,66
R 50,9216
v_fma_f32 v30, v30, v102, v103
mav 38,64,66
v_mul_f32 v105, v105, v116
v_fma_f32 v31, v31, v104, v105
mva 42,66,68
v_mul_f32 v107, v107, v116
s_waitcnt lgkmcnt(3)
mav 34,72,70
R 54,10240
v_fma_f32 v32, v32, v106, v107
mav 38,76,70
v_mul_f32 v109, v109, v116
v_fma_f32 v33, v33, v108, v109
mva 42,70,80
s_xor_b32 s50, s50, 0x10000
v_cvt_pk_bf16_f32 v78, v30, v31
mav 34,84,74
R 58,11264
v_cvt_pk_bf16_f32 v79, v32, v33
mav 38,88,74
ds_write_b64 v83, v[78:79] offset:1024
mva 42,74,92
s_waitcnt lgkmcnt(3)
mav 34,96,46
R 62,12288
s_add_u32 s49, s49, 346112
mav 38,100,46
s_waitcnt vmcnt(16) lgkmcnt(1)
s_barrier
mva 42,46,104
buffer_load_dwordx2 v[14:15], v93, s[36:39], s49 offen sc1
mav 34,108,50
R 66,13312
s_add_u32 m0, s50, 32768
mav 38,112,50
mva 42,50,116
D 86,0
mav 34,120,54
R 70,14336
mav 38,124,54
D 86,1024
mva 42,54,128
mav 34,132,58
R 74,15360
mav 38,136,58
D 86,2048
mva 42,58,140
s_waitcnt lgkmcnt(2)
mav 34,144,62
R 46,16384
D 86,3072
mav 38,148,62
s_add_u32 m0, s50, 40960
mva 42,62,152
mav 34,156,66
R 50,17408
D 87,0
mav 38,160,66
mva 42,66,164
D 87,1024
s_waitcnt lgkmcnt(2)
mav 34,168,70
R 54,18432
mav 38,172,70
mva 42,70,176
D 87,2048
mav 34,180,74
R 58,19456
mav 38,184,74
D 87,3072
mva 42,74,188
s_add_u32 m0, s50, 49152
s_waitcnt lgkmcnt(2)
mav 34,192,46
R 62,20480
mav 38,196,46
D 88,0
mva 42,46,200
mav 34,204,50
R 66,21504
D 88,1024
mav 38,208,50
mva 42,50,212
s_waitcnt lgkmcnt(2)
mav 34,216,54
R 70,22528
D 88,2048
mav 38,220,54
mva 42,54,224
D 88,3072
mav 34,228,58
R 74,23552
s_add_u32 m0, s50, 57344
mav 38,232,58
mva 42,58,236
D 89,0
s_waitcnt lgkmcnt(2)
mav 34,240,62
R 46,24576
mav 38,244,62
D 89,1024
mva 42,62,248
mav 34,252,66
R 50,25600
mvv 38,128,66
D 89,2048
mvv 42,66,132
s_waitcnt lgkmcnt(2)
mvv 34,136,70
R 54,26624
D 89,3072
mvv 38,140,70
mvv 42,70,144
mvv 34,148,74
R 58,27648
mvv 38,152,74
mvv 42,74,156
s_waitcnt lgkmcnt(2)
mvv 34,160,46
R 62,28672
mvv 38,164,46
mvv 42,46,168
s_waitcnt vmcnt(16)
v_permlane32_swap_b32 v14, v15
mvv 34,172,50
R 66,29696
v_add_f32 v16, v14, v15
mvv 38,176,50
v_add_f32 v17, v14, v15
mvv 42,50,180
s_waitcnt lgkmcnt(2)
mvv 34,184,54
R 70,30720
v_permlane16_swap_b32 v16, v17
mvv 38,188,54
v_add_f32 v14, v16, v17
v_add_f32 v15, v16, v17
mvv 42,54,192
mvv 34,196,58
R 74,31744
v_xor_b32 v80, 0x10000, v80
mvv 38,200,58
v_permlane32_swap_b32 v14, v15
v_mul_f32 v16, 0x3a800000, v14
mvv 42,58,204
v_mul_f32 v17, 0x3a800000, v15
s_waitcnt lgkmcnt(2)
mvv 34,208,62
R 46,32768
v_fma_f32 v17, -v16, v16, v17
mvv 38,212,62
ds_read_b64 v[118:119], v84 offset:128
ds_read_b128 v[102:105], v85 offset:128
mvv 42,62,216
ds_read_b128 v[106:109], v85 offset:144
v_max_f32 v17, 0, v17
mvv 34,220,66
R 50,33792
v_add_f32 v17, 0x3727c5ac, v17
mvv 38,224,66
v_rsq_f32 v14, v17
mvv 42,66,228
v_mul_f32_e64 v15, -v16, v14
s_waitcnt lgkmcnt(5)
mvv 34,232,70
R 54,34816
mvv 38,236,70
mvv 42,70,240
mvv 34,244,74
R 58,35840
mvv 38,248,74
mvv 42,74,252
s_waitcnt lgkmcnt(2)
v_mfma_f32_16x16x32_bf16 v[22:25], a[0:3], v[46:49], 0
R 62,36864
v_mul_f32 v120, v119, v94
v_mfma_f32_16x16x32_bf16 v[26:29], a[4:7], v[46:49], 0
v_fma_f32 v34, v34, v118, v120
v_mul_f32 v121, v119, v95
v_mfma_f32_16x16x32_bf16 v[30:33], v[46:49], a[8:11], 0
v_fma_f32 v35, v35, v118, v121
v_mul_f32 v120, v119, v96
mav 22,12,50
R 66,37888
v_fma_f32 v36, v36, v118, v120
mav 26,16,50
v_mul_f32 v121, v119, v97
v_fma_f32 v37, v37, v118, v121
mva 30,50,20
v_mul_f32 v120, v119, v98
s_waitcnt lgkmcnt(2)
mav 22,24,54
R 70,38912
v_fma_f32 v38, v38, v118, v120
mav 26,28,54
v_mul_f32 v121, v119, v99
v_fma_f32 v39, v39, v118, v121
mva 30,54,32
v_mul_f32 v120, v119, v100
v_fma_f32 v40, v40, v118, v120
mav 22,36,58
R 74,39936
v_mul_f32 v121, v119, v101
mav 26,40,58
v_fma_f32 v41, v41, v118, v121
v_cvt_pk_bf16_f32 v110, v34, v35
mva 30,58,44
v_cvt_pk_bf16_f32 v111, v36, v37
s_waitcnt lgkmcnt(2)
mav 22,48,62
R 46,40960
v_cvt_pk_bf16_f32 v112, v38, v39
mav 26,52,62
v_cvt_pk_bf16_f32 v113, v40, v41
v_mul_f32 v103, v103, v116
mva 30,62,56
ds_write_b128 v83, v[110:113] offset:4096
mav 22,60,66
R 50,41984
v_fma_f32 v42, v42, v102, v103
mav 26,64,66
v_mul_f32 v105, v105, v116
v_fma_f32 v43, v43, v104, v105
mva 30,66,68
v_mul_f32 v107, v107, v116
s_waitcnt lgkmcnt(3)
mav 22,72,70
R 54,43008
v_fma_f32 v44, v44, v106, v107
mav 26,76,70
v_mul_f32 v109, v109, v116
v_fma_f32 v45, v45, v108, v109
mva 30,70,80
v_cvt_pk_bf16_f32 v78, v42, v43
v_cvt_pk_bf16_f32 v79, v44, v45
mav 22,84,74
R 58,44032
mav 26,88,74
ds_write_b64 v83, v[78:79] offset:5120
mva 30,74,92
s_waitcnt lgkmcnt(3)
mav 22,96,46
R 62,45056
s_add_u32 s49, s49, 346112
mav 26,100,46
ds_write_b64 v84, v[14:15] offset:0
mva 30,46,104
mav 22,108,50
R 66,46080
s_add_u32 m0, s50, 0
mav 26,112,50
s_waitcnt lgkmcnt(1)
s_barrier
mva 30,50,116
buffer_load_dwordx2 v[14:15], v93, s[36:39], s49 offen sc1
D 86,0
mav 22,120,54
R 70,47104
mav 26,124,54
D 86,1024
mva 30,54,128
mav 22,132,58
R 74,48128
mav 26,136,58
D 86,2048
mva 30,58,140
mav 22,144,62
R 46,49152
D 86,3072
mav 26,148,62
s_add_u32 m0, s50, 8192
mva 30,62,152
s_waitcnt lgkmcnt(2)
mav 22,156,66
R 50,50176
D 87,0
mav 26,160,66
mva 30,66,164
D 87,1024
mav 22,168,70
R 54,51200
mav 26,172,70
mva 30,70,176
D 87,2048
s_waitcnt lgkmcnt(2)
mav 22,180,74
R 58,52224
mav 26,184,74
D 87,3072
mva 30,74,188
s_add_u32 m0, s50, 16384
mav 22,192,46
R 62,53248
mav 26,196,46
D 88,0
mva 30,46,200
s_waitcnt lgkmcnt(2)
mav 22,204,50
R 66,54272
D 88,1024
mav 26,208,50
mva 30,50,212
mav 22,216,54
R 70,55296
D 88,2048
mav 26,220,54
mva 30,54,224
D 88,3072
s_waitcnt lgkmcnt(2)
mav 22,228,58
R 74,56320
s_add_u32 m0, s50, 24576
mav 26,232,58
mva 30,58,236
D 89,0
mav 22,240,62
R 46,57344
mav 26,244,62
D 89,1024
mva 30,62,248
s_waitcnt lgkmcnt(2)
mav 22,252,66
R 50,58368
mvv 26,128,66
D 89,2048
mvv 30,66,132
mvv 22,136,70
R 54,59392
D 89,3072
mvv 26,140,70
mvv 30,70,144
s_waitcnt lgkmcnt(2)
mvv 22,148,74
R 58,60416
mvv 26,152,74
mvv 30,74,156
mvv 22,160,46
R 62,61440
mvv 26,164,46
mvv 30,46,168
s_waitcnt lgkmcnt(2)
mvv 22,172,50
R 66,62464
mvv 26,176,50
s_waitcnt vmcnt(16)
v_permlane32_swap_b32 v14, v15
mvv 30,50,180
v_add_f32 v16, v14, v15
v_add_f32 v17, v14, v15
mvv 22,184,54
R 70,63488
v_permlane16_swap_b32 v16, v17
mvv 26,188,54
v_add_f32 v14, v16, v17
v_add_f32 v15, v16, v17
mvv 30,54,192
s_waitcnt lgkmcnt(2)
mvv 22,196,58
R 74,64512
v_permlane32_swap_b32 v14, v15
mvv 26,200,58
v_mul_f32 v16, 0x3a800000, v14
v_mul_f32 v17, 0x3a800000, v15
mvv 30,58,204
v_fma_f32 v17, -v16, v16, v17
v_max_f32 v17, 0, v17
mvv 22,208,62
R 46,0
v_add_f32 v17, 0x3727c5ac, v17
mvv 26,212,62
ds_read_b64 v[118:119], v84 offset:0
ds_read_b128 v[102:105], v85 offset:0
mvv 30,62,216
ds_read_b128 v[106:109], v85 offset:16
s_waitcnt lgkmcnt(5)
mvv 22,220,66
R 50,1024
mvv 26,224,66
v_rsq_f32 v14, v17
mvv 30,66,228
v_mul_f32_e64 v15, -v16, v14
mvv 22,232,70
R 54,2048
mvv 26,236,70
mvv 30,70,240
s_waitcnt lgkmcnt(2)
mvv 22,244,74
R 58,3072
mvv 26,248,74
mvv 30,74,252
v_mfma_f32_16x16x32_bf16 v[34:37], a[0:3], v[46:49], 0
R 62,4096
v_mul_f32 v120, v119, v94
v_mfma_f32_16x16x32_bf16 v[38:41], a[4:7], v[46:49], 0
v_mul_f32 v121, v119, v95
v_fma_f32 v22, v22, v118, v120
v_mfma_f32_16x16x32_bf16 v[42:45], v[46:49], a[8:11], 0
v_fma_f32 v23, v23, v118, v121
s_waitcnt lgkmcnt(2)
mav 34,12,50
R 66,5120
v_mul_f32 v120, v119, v96
mav 38,16,50
v_fma_f32 v24, v24, v118, v120
v_mul_f32 v121, v119, v97
mva 42,50,20
v_fma_f32 v25, v25, v118, v121
v_mul_f32 v120, v119, v98
mav 34,24,54
R 70,6144
v_fma_f32 v26, v26, v118, v120
mav 38,28,54
v_mul_f32 v121, v119, v99
v_fma_f32 v27, v27, v118, v121
mva 42,54,32
v_mul_f32 v120, v119, v100
s_waitcnt lgkmcnt(2)
mav 34,36,58
R 74,7168
v_fma_f32 v28, v28, v118, v120
mav 38,40,58
v_mul_f32 v121, v119, v101
v_fma_f32 v29, v29, v118, v121
mva 42,58,44
v_cvt_pk_bf16_f32 v110, v22, v23
v_cvt_pk_bf16_f32 v111, v24, v25
mav 34,48,62
R 46,8192
v_cvt_pk_bf16_f32 v112, v26, v27
mav 38,52,62
v_cvt_pk_bf16_f32 v113, v28, v29
v_mul_f32 v103, v103, v116
mva 42,62,56
v_fma_f32 v30, v30, v102, v103
s_waitcnt lgkmcnt(2)
mav 34,60,66
R 50,9216
v_mul_f32 v105, v105, v116
mav 38,64,66
ds_write_b128 v83, v[110:113] offset:0
mva 42,66,68
v_fma_f32 v31, v31, v104, v105
v_mul_f32 v107, v107, v116
mav 34,72,70
R 54,10240
v_fma_f32 v32, v32, v106, v107
mav 38,76,70
v_mul_f32 v109, v109, v116
v_fma_f32 v33, v33, v108, v109
mva 42,70,80
s_xor_b32 s50, s50, 0x10000
s_waitcnt lgkmcnt(3)
mav 34,84,74
R 58,11264
v_cvt_pk_bf16_f32 v78, v30, v31
mav 38,88,74
ds_write_b64 v84, v[14:15] offset:128
mva 42,74,92
v_cvt_pk_bf16_f32 v79, v32, v33
mav 34,96,46
R 62,12288
s_add_u32 s49, s49, 346112
mav 38,100,46
ds_write_b64 v83, v[78:79] offset:1024
mva 42,46,104
s_waitcnt lgkmcnt(4)
mav 34,108,50
R 66,13312
s_add_u32 m0, s50, 32768
mav 38,112,50
s_waitcnt lgkmcnt(1)
s_barrier
mva 42,50,116
buffer_load_dwordx2 v[14:15], v93, s[36:39], s49 offen sc1
D 86,0
mav 34,120,54
R 70,14336
mav 38,124,54
D 86,1024
mva 42,54,128
mav 34,132,58
R 74,15360
mav 38,136,58
D 86,2048
mva 42,58,140
mav 34,144,62
R 46,16384
D 86,3072
mav 38,148,62
s_add_u32 m0, s50, 40960
mva 42,62,152
s_waitcnt lgkmcnt(2)
mav 34,156,66
R 50,17408
D 87,0
mav 38,160,66
mva 42,66,164
D 87,1024
mav 34,168,70
R 54,18432
mav 38,172,70
mva 42,70,176
D 87,2048
s_waitcnt lgkmcnt(2)
mav 34,180,74
R 58,19456
mav 38,184,74
D 87,3072
mva 42,74,188
s_add_u32 m0, s50, 49152
LsQ16:
s_cbranch_execz LwQA
mav 34,192,46
R 62,20480
mav 38,196,46
D 88,0
mva 42,46,200
s_waitcnt lgkmcnt(2)
mav 34,204,50
R 66,21504
D 88,1024
mav 38,208,50
mva 42,50,212
mav 34,216,54
R 70,22528
D 88,2048
mav 38,220,54
mva 42,54,224
D 88,3072
s_waitcnt lgkmcnt(2)
mav 34,228,58
R 74,23552
s_add_u32 m0, s50, 57344
mav 38,232,58
mva 42,58,236
D 89,0
mav 34,240,62
R 46,24576
mav 38,244,62
D 89,1024
mva 42,62,248
s_waitcnt lgkmcnt(2)
mav 34,252,66
R 50,25600
mvv 38,128,66
D 89,2048
mvv 42,66,132
mvv 34,136,70
R 54,26624
D 89,3072
mvv 38,140,70
mvv 42,70,144
s_waitcnt lgkmcnt(2)
mvv 34,148,74
R 58,27648
mvv 38,152,74
mvv 42,74,156
mvv 34,160,46
R 62,28672
mvv 38,164,46
mvv 42,46,168
s_waitcnt lgkmcnt(2)
mvv 34,172,50
R 66,29696
mvv 38,176,50
s_waitcnt vmcnt(16)
v_permlane32_swap_b32 v14, v15
mvv 42,50,180
v_add_f32 v16, v14, v15
v_add_f32 v17, v14, v15
mvv 34,184,54
R 70,30720
v_permlane16_swap_b32 v16, v17
mvv 38,188,54
v_add_f32 v14, v16, v17
v_add_f32 v15, v16, v17
mvv 42,54,192
s_waitcnt lgkmcnt(2)
mvv 34,196,58
R 74,31744
v_xor_b32 v80, 0x10000, v80
mvv 38,200,58
v_permlane32_swap_b32 v14, v15
v_mul_f32 v16, 0x3a800000, v14
mvv 42,58,204
v_mul_f32 v17, 0x3a800000, v15
v_fma_f32 v17, -v16, v16, v17
mvv 34,208,62
R 46,32768
v_max_f32 v17, 0, v17
mvv 38,212,62
ds_read_b64 v[118:119], v84 offset:128
ds_read_b128 v[102:105], v85 offset:128
mvv 42,62,216
ds_read_b128 v[106:109], v85 offset:144
s_waitcnt lgkmcnt(5)
mvv 34,220,66
R 50,33792
v_add_f32 v17, 0x3727c5ac, v17
mvv 38,224,66
v_rsq_f32 v14, v17
mvv 42,66,228
v_mul_f32_e64 v15, -v16, v14
mvv 34,232,70
R 54,34816
mvv 38,236,70
mvv 42,70,240
s_waitcnt lgkmcnt(2)
mvv 34,244,74
R 58,35840
mvv 38,248,74
mvv 42,74,252
s_mov_b32 s60, -27
.p2align 6
LQB1_1:
v_mfma_f32_16x16x32_bf16 v[22:25], a[0:3], v[46:49], 0
R 62,36864
v_mul_f32 v120, v119, v94
v_mfma_f32_16x16x32_bf16 v[26:29], a[4:7], v[46:49], 0
v_mul_f32 v121, v119, v95
v_fma_f32 v34, v34, v118, v120
v_mfma_f32_16x16x32_bf16 v[30:33], v[46:49], a[8:11], 0
v_fma_f32 v35, v35, v118, v121
s_waitcnt lgkmcnt(2)
mav 22,12,50
R 66,37888
v_mul_f32 v120, v119, v96
mav 26,16,50
v_fma_f32 v36, v36, v118, v120
v_mul_f32 v121, v119, v97
mva 30,50,20
v_fma_f32 v37, v37, v118, v121
v_mul_f32 v120, v119, v98
mav 22,24,54
R 70,38912
v_fma_f32 v38, v38, v118, v120
mav 26,28,54
v_mul_f32 v121, v119, v99
v_fma_f32 v39, v39, v118, v121
mva 30,54,32
v_mul_f32 v120, v119, v100
s_waitcnt lgkmcnt(2)
mav 22,36,58
R 74,39936
v_fma_f32 v40, v40, v118, v120
mav 26,40,58
v_mul_f32 v121, v119, v101
v_fma_f32 v41, v41, v118, v121
mva 30,58,44
v_cvt_pk_bf16_f32 v110, v34, v35
v_cvt_pk_bf16_f32 v111, v36, v37
mav 22,48,62
R 46,40960
v_cvt_pk_bf16_f32 v112, v38, v39
mav 26,52,62
v_cvt_pk_bf16_f32 v113, v40, v41
v_mul_f32 v103, v103, v116
mva 30,62,56
v_fma_f32 v42, v42, v102, v103
s_waitcnt lgkmcnt(2)
mav 22,60,66
R 50,41984
v_mul_f32 v105, v105, v116
mav 26,64,66
ds_write_b128 v83, v[110:113] offset:4096
mva 30,66,68
v_fma_f32 v43, v43, v104, v105
v_mul_f32 v107, v107, v116
mav 22,72,70
R 54,43008
v_fma_f32 v44, v44, v106, v107
mav 26,76,70
v_mul_f32 v109, v109, v116
v_fma_f32 v45, v45, v108, v109
mva 30,70,80
v_cvt_pk_bf16_f32 v78, v42, v43
s_waitcnt lgkmcnt(3)
mav 22,84,74
R 58,44032
v_cvt_pk_bf16_f32 v79, v44, v45
mav 26,88,74
ds_write_b64 v83, v[78:79] offset:5120
mva 30,74,92
ds_write_b64 v84, v[14:15] offset:0
mav 22,96,46
R 62,45056
s_add_u32 s49, s49, 346112
mav 26,100,46
mva 30,46,104
s_waitcnt lgkmcnt(1)
s_barrier
mav 22,108,50
buffer_load_dwordx2 v[14:15], v93, s[36:39], s49 offen sc1
R 66,46080
mav 26,112,50
s_add_u32 m0, s50, 0
mva 30,50,116
D 86,0
mav 22,120,54
R 70,47104
mav 26,124,54
D 86,1024
mva 30,54,128
mav 22,132,58
R 74,48128
mav 26,136,58
D 86,2048
mva 30,58,140
s_waitcnt lgkmcnt(2)
mav 22,144,62
R 46,49152
D 86,3072
mav 26,148,62
s_add_u32 m0, s50, 8192
mva 30,62,152
mav 22,156,66
R 50,50176
D 87,0
mav 26,160,66
mva 30,66,164
D 87,1024
s_waitcnt lgkmcnt(2)
mav 22,168,70
R 54,51200
mav 26,172,70
mva 30,70,176
D 87,2048
mav 22,180,74
R 58,52224
mav 26,184,74
D 87,3072
mva 30,74,188
s_add_u32 m0, s50, 16384
s_waitcnt lgkmcnt(2)
mav 22,192,46
R 62,53248
mav 26,196,46
D 88,0
mva 30,46,200
mav 22,204,50
R 66,54272
D 88,1024
mav 26,208,50
mva 30,50,212
s_waitcnt lgkmcnt(2)
mav 22,216,54
R 70,55296
D 88,2048
mav 26,220,54
mva 30,54,224
D 88,3072
mav 22,228,58
R 74,56320
s_add_u32 m0, s50, 24576
mav 26,232,58
mva 30,58,236
D 89,0
s_waitcnt lgkmcnt(2)
mav 22,240,62
R 46,57344
mav 26,244,62
D 89,1024
mva 30,62,248
mav 22,252,66
R 50,58368
mvv 26,128,66
D 89,2048
mvv 30,66,132
s_waitcnt lgkmcnt(2)
mvv 22,136,70
R 54,59392
D 89,3072
mvv 26,140,70
mvv 30,70,144
mvv 22,148,74
R 58,60416
mvv 26,152,74
mvv 30,74,156
s_waitcnt lgkmcnt(2)
mvv 22,160,46
R 62,61440
mvv 26,164,46
mvv 30,46,168
s_waitcnt vmcnt(16)
v_permlane32_swap_b32 v14, v15
mvv 22,172,50
R 66,62464
v_add_f32 v16, v14, v15
mvv 26,176,50
v_add_f32 v17, v14, v15
mvv 30,50,180
s_waitcnt lgkmcnt(2)
mvv 22,184,54
R 70,63488
v_permlane16_swap_b32 v16, v17
mvv 26,188,54
v_add_f32 v14, v16, v17
v_add_f32 v15, v16, v17
mvv 30,54,192
mvv 22,196,58
R 74,64512
v_permlane32_swap_b32 v14, v15
mvv 26,200,58
v_mul_f32 v16, 0x3a800000, v14
v_mul_f32 v17, 0x3a800000, v15
mvv 30,58,204
v_fma_f32 v17, -v16, v16, v17
s_waitcnt lgkmcnt(2)
mvv 22,208,62
R 46,0
v_max_f32 v17, 0, v17
mvv 26,212,62
ds_read_b64 v[118:119], v84 offset:0
ds_read_b128 v[102:105], v85 offset:0
mvv 30,62,216
ds_read_b128 v[106:109], v85 offset:16
v_add_f32 v17, 0x3727c5ac, v17
mvv 22,220,66
R 50,1024
mvv 26,224,66
v_rsq_f32 v14, v17
mvv 30,66,228
v_mul_f32_e64 v15, -v16, v14
s_waitcnt lgkmcnt(5)
mvv 22,232,70
R 54,2048
mvv 26,236,70
mvv 30,70,240
mvv 22,244,74
R 58,3072
mvv 26,248,74
mvv 30,74,252
s_waitcnt lgkmcnt(2)
v_mfma_f32_16x16x32_bf16 v[34:37], a[0:3], v[46:49], 0
R 62,4096
v_mul_f32 v120, v119, v94
v_mfma_f32_16x16x32_bf16 v[38:41], a[4:7], v[46:49], 0
v_fma_f32 v22, v22, v118, v120
v_mul_f32 v121, v119, v95
v_mfma_f32_16x16x32_bf16 v[42:45], v[46:49], a[8:11], 0
v_fma_f32 v23, v23, v118, v121
v_mul_f32 v120, v119, v96
mav 34,12,50
R 66,5120
v_fma_f32 v24, v24, v118, v120
mav 38,16,50
v_mul_f32 v121, v119, v97
v_fma_f32 v25, v25, v118, v121
mva 42,50,20
v_mul_f32 v120, v119, v98
s_waitcnt lgkmcnt(2)
mav 34,24,54
R 70,6144
v_fma_f32 v26, v26, v118, v120
mav 38,28,54
v_mul_f32 v121, v119, v99
v_fma_f32 v27, v27, v118, v121
mva 42,54,32
v_mul_f32 v120, v119, v100
v_fma_f32 v28, v28, v118, v120
mav 34,36,58
R 74,7168
v_mul_f32 v121, v119, v101
mav 38,40,58
v_fma_f32 v29, v29, v118, v121
v_cvt_pk_bf16_f32 v110, v22, v23
mva 42,58,44
v_cvt_pk_bf16_f32 v111, v24, v25
s_waitcnt lgkmcnt(2)
mav 34,48,62
R 46,8192
v_cvt_pk_bf16_f32 v112, v26, v27
mav 38,52,62
v_cvt_pk_bf16_f32 v113, v28, v29
v_mul_f32 v103, v103, v116
mva 42,62,56
ds_write_b128 v83, v[110:113] offset:0
mav 34,60,66
R 50,9216
v_fma_f32 v30, v30, v102, v103
mav 38,64,66
v_mul_f32 v105, v105, v116
v_fma_f32 v31, v31, v104, v105
mva 42,66,68
v_mul_f32 v107, v107, v116
s_waitcnt lgkmcnt(3)
mav 34,72,70
R 54,10240
v_fma_f32 v32, v32, v106, v107
mav 38,76,70
v_mul_f32 v109, v109, v116
v_fma_f32 v33, v33, v108, v109
mva 42,70,80
s_xor_b32 s50, s50, 0x10000
v_cvt_pk_bf16_f32 v78, v30, v31
mav 34,84,74
R 58,11264
v_cvt_pk_bf16_f32 v79, v32, v33
mav 38,88,74
ds_write_b64 v83, v[78:79] offset:1024
mva 42,74,92
s_waitcnt lgkmcnt(3)
mav 34,96,46
R 62,12288
s_add_u32 s49, s49, 346112
mav 38,100,46
ds_write_b64 v84, v[14:15] offset:128
mva 42,46,104
mav 34,108,50
R 66,13312
s_add_u32 m0, s50, 32768
mav 38,112,50
s_waitcnt lgkmcnt(1)
s_barrier
mva 42,50,116
buffer_load_dwordx2 v[14:15], v93, s[36:39], s49 offen sc1
D 86,0
mav 34,120,54
R 70,14336
mav 38,124,54
D 86,1024
mva 42,54,128
mav 34,132,58
R 74,15360
mav 38,136,58
D 86,2048
mva 42,58,140
mav 34,144,62
R 46,16384
D 86,3072
)"
R"(
mav 38,148,62
s_add_u32 m0, s50, 40960
mva 42,62,152
s_waitcnt lgkmcnt(2)
mav 34,156,66
R 50,17408
D 87,0
mav 38,160,66
mva 42,66,164
D 87,1024
mav 34,168,70
R 54,18432
mav 38,172,70
mva 42,70,176
D 87,2048
s_waitcnt lgkmcnt(2)
mav 34,180,74
R 58,19456
mav 38,184,74
D 87,3072
mva 42,74,188
s_add_u32 m0, s50, 49152
mav 34,192,46
R 62,20480
mav 38,196,46
D 88,0
mva 42,46,200
s_waitcnt lgkmcnt(2)
mav 34,204,50
R 66,21504
D 88,1024
mav 38,208,50
mva 42,50,212
mav 34,216,54
R 70,22528
D 88,2048
mav 38,220,54
mva 42,54,224
D 88,3072
s_waitcnt lgkmcnt(2)
mav 34,228,58
R 74,23552
s_add_u32 m0, s50, 57344
mav 38,232,58
mva 42,58,236
D 89,0
mav 34,240,62
R 46,24576
mav 38,244,62
D 89,1024
mva 42,62,248
s_waitcnt lgkmcnt(2)
mav 34,252,66
R 50,25600
mvv 38,128,66
D 89,2048
mvv 42,66,132
mvv 34,136,70
R 54,26624
D 89,3072
mvv 38,140,70
mvv 42,70,144
s_waitcnt lgkmcnt(2)
mvv 34,148,74
R 58,27648
mvv 38,152,74
mvv 42,74,156
mvv 34,160,46
R 62,28672
mvv 38,164,46
mvv 42,46,168
s_waitcnt lgkmcnt(2)
mvv 34,172,50
R 66,29696
mvv 38,176,50
s_waitcnt vmcnt(16)
v_permlane32_swap_b32 v14, v15
mvv 42,50,180
v_add_f32 v16, v14, v15
v_add_f32 v17, v14, v15
mvv 34,184,54
R 70,30720
v_permlane16_swap_b32 v16, v17
mvv 38,188,54
v_add_f32 v14, v16, v17
v_add_f32 v15, v16, v17
mvv 42,54,192
s_waitcnt lgkmcnt(2)
mvv 34,196,58
R 74,31744
v_xor_b32 v80, 0x10000, v80
mvv 38,200,58
v_permlane32_swap_b32 v14, v15
v_mul_f32 v16, 0x3a800000, v14
mvv 42,58,204
v_mul_f32 v17, 0x3a800000, v15
v_fma_f32 v17, -v16, v16, v17
mvv 34,208,62
R 46,32768
v_max_f32 v17, 0, v17
mvv 38,212,62
ds_read_b64 v[118:119], v84 offset:128
ds_read_b128 v[102:105], v85 offset:128
mvv 42,62,216
ds_read_b128 v[106:109], v85 offset:144
s_waitcnt lgkmcnt(5)
mvv 34,220,66
R 50,33792
v_add_f32 v17, 0x3727c5ac, v17
mvv 38,224,66
v_rsq_f32 v14, v17
mvv 42,66,228
v_mul_f32_e64 v15, -v16, v14
mvv 34,232,70
R 54,34816
mvv 38,236,70
mvv 42,70,240
s_waitcnt lgkmcnt(2)
mvv 34,244,74
R 58,35840
mvv 38,248,74
mvv 42,74,252
s_add_u32 s60, s60, 1
s_cbranch_scc0 LQB1_1
v_mfma_f32_16x16x32_bf16 v[22:25], a[0:3], v[46:49], 0
R 62,36864
v_mul_f32 v120, v119, v94
v_mfma_f32_16x16x32_bf16 v[26:29], a[4:7], v[46:49], 0
v_mul_f32 v121, v119, v95
v_fma_f32 v34, v34, v118, v120
v_mfma_f32_16x16x32_bf16 v[30:33], v[46:49], a[8:11], 0
v_fma_f32 v35, v35, v118, v121
s_waitcnt lgkmcnt(2)
mav 22,12,50
R 66,37888
v_mul_f32 v120, v119, v96
mav 26,16,50
v_fma_f32 v36, v36, v118, v120
v_mul_f32 v121, v119, v97
mva 30,50,20
v_fma_f32 v37, v37, v118, v121
v_mul_f32 v120, v119, v98
mav 22,24,54
R 70,38912
LsQ17:
s_cbranch_execz LwQB
v_fma_f32 v38, v38, v118, v120
mav 26,28,54
v_mul_f32 v121, v119, v99
v_fma_f32 v39, v39, v118, v121
mva 30,54,32
v_mul_f32 v120, v119, v100
s_waitcnt lgkmcnt(2)
mav 22,36,58
R 74,39936
v_fma_f32 v40, v40, v118, v120
mav 26,40,58
v_mul_f32 v121, v119, v101
v_fma_f32 v41, v41, v118, v121
mva 30,58,44
v_cvt_pk_bf16_f32 v110, v34, v35
v_cvt_pk_bf16_f32 v111, v36, v37
mav 22,48,62
R 46,40960
v_cvt_pk_bf16_f32 v112, v38, v39
mav 26,52,62
v_cvt_pk_bf16_f32 v113, v40, v41
v_mul_f32 v103, v103, v116
mva 30,62,56
v_fma_f32 v42, v42, v102, v103
s_waitcnt lgkmcnt(2)
mav 22,60,66
R 50,41984
v_mul_f32 v105, v105, v116
mav 26,64,66
ds_write_b128 v83, v[110:113] offset:4096
mva 30,66,68
v_fma_f32 v43, v43, v104, v105
v_mul_f32 v107, v107, v116
mav 22,72,70
R 54,43008
v_fma_f32 v44, v44, v106, v107
mav 26,76,70
v_mul_f32 v109, v109, v116
v_fma_f32 v45, v45, v108, v109
mva 30,70,80
v_cvt_pk_bf16_f32 v78, v42, v43
s_waitcnt lgkmcnt(3)
mav 22,84,74
R 58,44032
v_cvt_pk_bf16_f32 v79, v44, v45
mav 26,88,74
ds_write_b64 v83, v[78:79] offset:5120
mva 30,74,92
ds_write_b64 v84, v[14:15] offset:0
mav 22,96,46
R 62,45056
s_add_u32 s49, s49, 346112
mav 26,100,46
mva 30,46,104
s_waitcnt lgkmcnt(1)
s_barrier
mav 22,108,50
buffer_load_dwordx2 v[14:15], v93, s[36:39], s49 offen sc1
R 66,46080
mav 26,112,50
s_add_u32 m0, s50, 0
mva 30,50,116
D 86,0
mav 22,120,54
R 70,47104
mav 26,124,54
D 86,1024
mva 30,54,128
mav 22,132,58
R 74,48128
mav 26,136,58
D 86,2048
mva 30,58,140
s_waitcnt lgkmcnt(2)
mav 22,144,62
R 46,49152
D 86,3072
mav 26,148,62
s_add_u32 m0, s50, 8192
mva 30,62,152
mav 22,156,66
R 50,50176
D 87,0
mav 26,160,66
mva 30,66,164
D 87,1024
s_waitcnt lgkmcnt(2)
mav 22,168,70
R 54,51200
mav 26,172,70
mva 30,70,176
D 87,2048
mav 22,180,74
R 58,52224
mav 26,184,74
D 87,3072
mva 30,74,188
s_add_u32 m0, s50, 16384
s_waitcnt lgkmcnt(2)
mav 22,192,46
R 62,53248
mav 26,196,46
D 88,0
mva 30,46,200
mav 22,204,50
R 66,54272
D 88,1024
mav 26,208,50
mva 30,50,212
s_waitcnt lgkmcnt(2)
mav 22,216,54
R 70,55296
D 88,2048
mav 26,220,54
mva 30,54,224
D 88,3072
mav 22,228,58
R 74,56320
s_add_u32 m0, s50, 24576
mav 26,232,58
mva 30,58,236
D 89,0
s_waitcnt lgkmcnt(2)
mav 22,240,62
R 46,57344
mav 26,244,62
D 89,1024
mva 30,62,248
mav 22,252,66
R 50,58368
mvv 26,128,66
D 89,2048
mvv 30,66,132
s_waitcnt lgkmcnt(2)
mvv 22,136,70
R 54,59392
D 89,3072
mvv 26,140,70
mvv 30,70,144
mvv 22,148,74
R 58,60416
mvv 26,152,74
mvv 30,74,156
s_waitcnt lgkmcnt(2)
mvv 22,160,46
R 62,61440
mvv 26,164,46
mvv 30,46,168
s_waitcnt vmcnt(16)
v_permlane32_swap_b32 v14, v15
mvv 22,172,50
R 66,62464
v_add_f32 v16, v14, v15
mvv 26,176,50
v_add_f32 v17, v14, v15
mvv 30,50,180
s_waitcnt lgkmcnt(2)
mvv 22,184,54
R 70,63488
v_permlane16_swap_b32 v16, v17
mvv 26,188,54
v_add_f32 v14, v16, v17
v_add_f32 v15, v16, v17
mvv 30,54,192
mvv 22,196,58
R 74,64512
v_permlane32_swap_b32 v14, v15
mvv 26,200,58
v_mul_f32 v16, 0x3a800000, v14
v_mul_f32 v17, 0x3a800000, v15
mvv 30,58,204
v_fma_f32 v17, -v16, v16, v17
s_waitcnt lgkmcnt(2)
mvv 22,208,62
R 46,0
v_max_f32 v17, 0, v17
mvv 26,212,62
ds_read_b64 v[118:119], v84 offset:0
ds_read_b128 v[102:105], v85 offset:0
mvv 30,62,216
ds_read_b128 v[106:109], v85 offset:16
v_add_f32 v17, 0x3727c5ac, v17
mvv 22,220,66
R 50,1024
mvv 26,224,66
v_rsq_f32 v14, v17
mvv 30,66,228
v_mul_f32_e64 v15, -v16, v14
s_waitcnt lgkmcnt(5)
mvv 22,232,70
R 54,2048
mvv 26,236,70
mvv 30,70,240
mvv 22,244,74
R 58,3072
mvv 26,248,74
mvv 30,74,252
s_waitcnt lgkmcnt(2)
v_mfma_f32_16x16x32_bf16 v[34:37], a[0:3], v[46:49], 0
R 62,4096
v_mul_f32 v120, v119, v94
v_mfma_f32_16x16x32_bf16 v[38:41], a[4:7], v[46:49], 0
v_fma_f32 v22, v22, v118, v120
v_mul_f32 v121, v119, v95
v_mfma_f32_16x16x32_bf16 v[42:45], v[46:49], a[8:11], 0
v_fma_f32 v23, v23, v118, v121
v_mul_f32 v120, v119, v96
mav 34,12,50
R 66,5120
v_fma_f32 v24, v24, v118, v120
mav 38,16,50
v_mul_f32 v121, v119, v97
v_fma_f32 v25, v25, v118, v121
mva 42,50,20
v_mul_f32 v120, v119, v98
s_waitcnt lgkmcnt(2)
mav 34,24,54
R 70,6144
v_fma_f32 v26, v26, v118, v120
mav 38,28,54
v_mul_f32 v121, v119, v99
v_fma_f32 v27, v27, v118, v121
mva 42,54,32
v_mul_f32 v120, v119, v100
v_fma_f32 v28, v28, v118, v120
mav 34,36,58
R 74,7168
v_mul_f32 v121, v119, v101
mav 38,40,58
v_fma_f32 v29, v29, v118, v121
v_cvt_pk_bf16_f32 v110, v22, v23
mva 42,58,44
v_cvt_pk_bf16_f32 v111, v24, v25
s_waitcnt lgkmcnt(2)
mav 34,48,62
R 46,8192
v_cvt_pk_bf16_f32 v112, v26, v27
mav 38,52,62
v_cvt_pk_bf16_f32 v113, v28, v29
v_mul_f32 v103, v103, v116
mva 42,62,56
ds_write_b128 v83, v[110:113] offset:0
mav 34,60,66
R 50,9216
v_fma_f32 v30, v30, v102, v103
mav 38,64,66
v_mul_f32 v105, v105, v116
v_fma_f32 v31, v31, v104, v105
mva 42,66,68
v_mul_f32 v107, v107, v116
s_waitcnt lgkmcnt(3)
mav 34,72,70
R 54,10240
v_fma_f32 v32, v32, v106, v107
mav 38,76,70
v_mul_f32 v109, v109, v116
v_fma_f32 v33, v33, v108, v109
mva 42,70,80
v_cvt_pk_bf16_f32 v78, v30, v31
v_cvt_pk_bf16_f32 v79, v32, v33
mav 34,84,74
R 58,11264
mav 38,88,74
ds_write_b64 v83, v[78:79] offset:1024
mva 42,74,92
s_waitcnt lgkmcnt(3)
mav 34,96,46
R 62,12288
s_add_u32 s49, s49, 346112
mav 38,100,46
ds_write_b64 v84, v[14:15] offset:128
mva 42,46,104
mav 34,108,50
R 66,13312
mav 38,112,50
s_waitcnt lgkmcnt(1)
s_barrier
mva 42,50,116
buffer_load_dwordx2 v[14:15], v93, s[36:39], s49 offen sc1
mav 34,120,54
R 70,14336
mav 38,124,54
mva 42,54,128
mav 34,132,58
R 74,15360
mav 38,136,58
mva 42,58,140
mav 34,144,62
R 46,16384
mav 38,148,62
mva 42,62,152
s_waitcnt lgkmcnt(2)
mav 34,156,66
R 50,17408
mav 38,160,66
mva 42,66,164
mav 34,168,70
R 54,18432
mav 38,172,70
mva 42,70,176
s_waitcnt lgkmcnt(2)
mav 34,180,74
R 58,19456
mav 38,184,74
mva 42,74,188
mav 34,192,46
R 62,20480
mav 38,196,46
mva 42,46,200
s_waitcnt lgkmcnt(2)
mav 34,204,50
R 66,21504
mav 38,208,50
mva 42,50,212
mav 34,216,54
R 70,22528
mav 38,220,54
mva 42,54,224
s_waitcnt lgkmcnt(2)
mav 34,228,58
R 74,23552
mav 38,232,58
mva 42,58,236
mav 34,240,62
R 46,24576
mav 38,244,62
mva 42,62,248
s_waitcnt lgkmcnt(2)
mav 34,252,66
R 50,25600
mvv 38,128,66
mvv 42,66,132
mvv 34,136,70
R 54,26624
mvv 38,140,70
mvv 42,70,144
s_waitcnt lgkmcnt(2)
mvv 34,148,74
R 58,27648
mvv 38,152,74
mvv 42,74,156
mvv 34,160,46
R 62,28672
mvv 38,164,46
mvv 42,46,168
s_waitcnt lgkmcnt(2)
mvv 34,172,50
R 66,29696
mvv 38,176,50
s_waitcnt vmcnt(0)
v_permlane32_swap_b32 v14, v15
mvv 42,50,180
v_add_f32 v16, v14, v15
v_add_f32 v17, v14, v15
mvv 34,184,54
R 70,30720
v_permlane16_swap_b32 v16, v17
mvv 38,188,54
v_add_f32 v14, v16, v17
v_add_f32 v15, v16, v17
mvv 42,54,192
s_waitcnt lgkmcnt(2)
mvv 34,196,58
R 74,31744
v_xor_b32 v80, 0x10000, v80
mvv 38,200,58
v_permlane32_swap_b32 v14, v15
v_mul_f32 v16, 0x3a800000, v14
mvv 42,58,204
v_mul_f32 v17, 0x3a800000, v15
v_fma_f32 v17, -v16, v16, v17
mvv 34,208,62
R 46,32768
v_max_f32 v17, 0, v17
mvv 38,212,62
ds_read_b64 v[118:119], v84 offset:128
ds_read_b128 v[102:105], v85 offset:128
mvv 42,62,216
ds_read_b128 v[106:109], v85 offset:144
s_waitcnt lgkmcnt(5)
mvv 34,220,66
R 50,33792
v_add_f32 v17, 0x3727c5ac, v17
mvv 38,224,66
v_rsq_f32 v14, v17
mvv 42,66,228
v_mul_f32_e64 v15, -v16, v14
mvv 34,232,70
R 54,34816
mvv 38,236,70
mvv 42,70,240
s_waitcnt lgkmcnt(2)
mvv 34,244,74
R 58,35840
mvv 38,248,74
mvv 42,74,252
v_mfma_f32_16x16x32_bf16 v[22:25], a[0:3], v[46:49], 0
R 62,36864
v_mul_f32 v120, v119, v94
v_mfma_f32_16x16x32_bf16 v[26:29], a[4:7], v[46:49], 0
v_mul_f32 v121, v119, v95
v_fma_f32 v34, v34, v118, v120
v_mfma_f32_16x16x32_bf16 v[30:33], v[46:49], a[8:11], 0
v_fma_f32 v35, v35, v118, v121
s_waitcnt lgkmcnt(2)
mav 22,12,50
R 66,37888
v_mul_f32 v120, v119, v96
mav 26,16,50
v_fma_f32 v36, v36, v118, v120
v_mul_f32 v121, v119, v97
mva 30,50,20
v_fma_f32 v37, v37, v118, v121
v_mul_f32 v120, v119, v98
mav 22,24,54
R 70,38912
v_fma_f32 v38, v38, v118, v120
mav 26,28,54
v_mul_f32 v121, v119, v99
v_fma_f32 v39, v39, v118, v121
mva 30,54,32
v_mul_f32 v120, v119, v100
s_waitcnt lgkmcnt(2)
mav 22,36,58
R 74,39936
v_fma_f32 v40, v40, v118, v120
mav 26,40,58
v_mul_f32 v121, v119, v101
v_fma_f32 v41, v41, v118, v121
mva 30,58,44
v_cvt_pk_bf16_f32 v110, v34, v35
v_cvt_pk_bf16_f32 v111, v36, v37
mav 22,48,62
R 46,40960
v_cvt_pk_bf16_f32 v112, v38, v39
mav 26,52,62
v_cvt_pk_bf16_f32 v113, v40, v41
v_mul_f32 v103, v103, v116
mva 30,62,56
v_fma_f32 v42, v42, v102, v103
s_waitcnt lgkmcnt(2)
mav 22,60,66
R 50,41984
v_mul_f32 v105, v105, v116
mav 26,64,66
ds_write_b128 v83, v[110:113] offset:4096
mva 30,66,68
v_fma_f32 v43, v43, v104, v105
v_mul_f32 v107, v107, v116
mav 22,72,70
R 54,43008
v_fma_f32 v44, v44, v106, v107
mav 26,76,70
v_mul_f32 v109, v109, v116
v_fma_f32 v45, v45, v108, v109
mva 30,70,80
v_cvt_pk_bf16_f32 v78, v42, v43
s_waitcnt lgkmcnt(3)
mav 22,84,74
R 58,44032
v_cvt_pk_bf16_f32 v79, v44, v45
mav 26,88,74
ds_write_b64 v83, v[78:79] offset:5120
mva 30,74,92
ds_write_b64 v84, v[14:15] offset:0
mav 22,96,46
R 62,45056
s_add_u32 s49, s49, 346112
mav 26,100,46
mva 30,46,104
s_waitcnt lgkmcnt(1)
s_barrier
mav 22,108,50
buffer_load_dwordx2 v[14:15], v93, s[36:39], s49 offen sc1
R 66,46080
mav 26,112,50
mva 30,50,116
mav 22,120,54
R 70,47104
mav 26,124,54
mva 30,54,128
mav 22,132,58
R 74,48128
mav 26,136,58
mva 30,58,140
s_waitcnt lgkmcnt(2)
mav 22,144,62
R 46,49152
mav 26,148,62
mva 30,62,152
mav 22,156,66
R 50,50176
mav 26,160,66
mva 30,66,164
s_waitcnt lgkmcnt(2)
mav 22,168,70
R 54,51200
mav 26,172,70
mva 30,70,176
mav 22,180,74
R 58,52224
mav 26,184,74
mva 30,74,188
s_waitcnt lgkmcnt(2)
mav 22,192,46
R 62,53248
mav 26,196,46
mva 30,46,200
mav 22,204,50
R 66,54272
mav 26,208,50
mva 30,50,212
s_waitcnt lgkmcnt(2)
mav 22,216,54
R 70,55296
mav 26,220,54
mva 30,54,224
mav 22,228,58
R 74,56320
mav 26,232,58
mva 30,58,236
s_waitcnt lgkmcnt(2)
mav 22,240,62
R 46,57344
mav 26,244,62
mva 30,62,248
mav 22,252,66
R 50,58368
mvv 26,128,66
mvv 30,66,132
s_waitcnt lgkmcnt(2)
mvv 22,136,70
R 54,59392
mvv 26,140,70
mvv 30,70,144
mvv 22,148,74
R 58,60416
mvv 26,152,74
mvv 30,74,156
s_waitcnt lgkmcnt(2)
mvv 22,160,46
R 62,61440
mvv 26,164,46
mvv 30,46,168
s_waitcnt vmcnt(0)
v_permlane32_swap_b32 v14, v15
mvv 22,172,50
R 66,62464
v_add_f32 v16, v14, v15
mvv 26,176,50
v_add_f32 v17, v14, v15
mvv 30,50,180
s_waitcnt lgkmcnt(2)
mvv 22,184,54
R 70,63488
v_permlane16_swap_b32 v16, v17
mvv 26,188,54
v_add_f32 v14, v16, v17
v_add_f32 v15, v16, v17
mvv 30,54,192
mvv 22,196,58
R 74,64512
v_permlane32_swap_b32 v14, v15
mvv 26,200,58
v_mul_f32 v16, 0x3a800000, v14
LsQ18:
s_cbranch_execz LwQA
v_mul_f32 v17, 0x3a800000, v15
mvv 30,58,204
v_fma_f32 v17, -v16, v16, v17
s_waitcnt lgkmcnt(2)
mvv 22,208,62
R 46,0
v_max_f32 v17, 0, v17
mvv 26,212,62
ds_read_b64 v[118:119], v84 offset:0
ds_read_b128 v[102:105], v85 offset:0
mvv 30,62,216
ds_read_b128 v[106:109], v85 offset:16
v_add_f32 v17, 0x3727c5ac, v17
mvv 22,220,66
R 50,1024
mvv 26,224,66
v_rsq_f32 v14, v17
mvv 30,66,228
v_mul_f32_e64 v15, -v16, v14
s_waitcnt lgkmcnt(5)
mvv 22,232,70
R 54,2048
mvv 26,236,70
mvv 30,70,240
mvv 22,244,74
R 58,3072
mvv 26,248,74
mvv 30,74,252
s_waitcnt lgkmcnt(2)
v_mfma_f32_16x16x32_bf16 v[34:37], a[0:3], v[46:49], 0
R 62,4096
v_mul_f32 v120, v119, v94
v_mfma_f32_16x16x32_bf16 v[38:41], a[4:7], v[46:49], 0
v_fma_f32 v22, v22, v118, v120
v_mul_f32 v121, v119, v95
v_mfma_f32_16x16x32_bf16 v[42:45], v[46:49], a[8:11], 0
v_fma_f32 v23, v23, v118, v121
v_mul_f32 v120, v119, v96
mav 34,12,50
R 66,5120
v_fma_f32 v24, v24, v118, v120
mav 38,16,50
v_mul_f32 v121, v119, v97
v_fma_f32 v25, v25, v118, v121
mva 42,50,20
v_mul_f32 v120, v119, v98
s_waitcnt lgkmcnt(2)
mav 34,24,54
R 70,6144
v_fma_f32 v26, v26, v118, v120
mav 38,28,54
v_mul_f32 v121, v119, v99
v_fma_f32 v27, v27, v118, v121
mva 42,54,32
v_mul_f32 v120, v119, v100
v_fma_f32 v28, v28, v118, v120
mav 34,36,58
R 74,7168
v_mul_f32 v121, v119, v101
mav 38,40,58
v_fma_f32 v29, v29, v118, v121
v_cvt_pk_bf16_f32 v110, v22, v23
mva 42,58,44
v_cvt_pk_bf16_f32 v111, v24, v25
s_waitcnt lgkmcnt(2)
mav 34,48,62
R 46,8192
v_cvt_pk_bf16_f32 v112, v26, v27
mav 38,52,62
v_cvt_pk_bf16_f32 v113, v28, v29
v_mul_f32 v103, v103, v116
mva 42,62,56
ds_write_b128 v83, v[110:113] offset:0
mav 34,60,66
R 50,9216
v_fma_f32 v30, v30, v102, v103
mav 38,64,66
v_mul_f32 v105, v105, v116
v_fma_f32 v31, v31, v104, v105
mva 42,66,68
v_mul_f32 v107, v107, v116
s_waitcnt lgkmcnt(3)
mav 34,72,70
R 54,10240
v_fma_f32 v32, v32, v106, v107
mav 38,76,70
v_mul_f32 v109, v109, v116
v_fma_f32 v33, v33, v108, v109
mva 42,70,80
v_cvt_pk_bf16_f32 v78, v30, v31
v_cvt_pk_bf16_f32 v79, v32, v33
mav 34,84,74
R 58,11264
mav 38,88,74
ds_write_b64 v83, v[78:79] offset:1024
mva 42,74,92
s_waitcnt lgkmcnt(3)
mav 34,96,46
R 62,12288
mav 38,100,46
ds_write_b64 v84, v[14:15] offset:128
mva 42,46,104
mav 34,108,50
R 66,13312
mav 38,112,50
s_waitcnt lgkmcnt(1)
s_barrier
mva 42,50,116
mav 34,120,54
R 70,14336
mav 38,124,54
mva 42,54,128
mav 34,132,58
R 74,15360
mav 38,136,58
mva 42,58,140
mav 34,144,62
R 46,16384
mav 38,148,62
mva 42,62,152
s_waitcnt lgkmcnt(2)
mav 34,156,66
R 50,17408
mav 38,160,66
mva 42,66,164
mav 34,168,70
R 54,18432
mav 38,172,70
mva 42,70,176
s_waitcnt lgkmcnt(2)
mav 34,180,74
R 58,19456
mav 38,184,74
mva 42,74,188
mav 34,192,46
R 62,20480
mav 38,196,46
mva 42,46,200
s_waitcnt lgkmcnt(2)
mav 34,204,50
R 66,21504
mav 38,208,50
mva 42,50,212
mav 34,216,54
R 70,22528
mav 38,220,54
mva 42,54,224
s_waitcnt lgkmcnt(2)
mav 34,228,58
R 74,23552
mav 38,232,58
mva 42,58,236
mav 34,240,62
R 46,24576
mav 38,244,62
mva 42,62,248
s_waitcnt lgkmcnt(2)
mav 34,252,66
R 50,25600
mvv 38,128,66
mvv 42,66,132
mvv 34,136,70
R 54,26624
mvv 38,140,70
mvv 42,70,144
s_waitcnt lgkmcnt(2)
mvv 34,148,74
R 58,27648
mvv 38,152,74
mvv 42,74,156
mvv 34,160,46
R 62,28672
mvv 38,164,46
mvv 42,46,168
s_waitcnt lgkmcnt(2)
mvv 34,172,50
R 66,29696
mvv 38,176,50
mvv 42,50,180
mvv 34,184,54
R 70,30720
mvv 38,188,54
mvv 42,54,192
s_waitcnt lgkmcnt(2)
mvv 34,196,58
R 74,31744
mvv 38,200,58
mvv 42,58,204
mvv 34,208,62
mvv 38,212,62
ds_read_b64 v[118:119], v84 offset:128
ds_read_b128 v[102:105], v85 offset:128
mvv 42,62,216
ds_read_b128 v[106:109], v85 offset:144
s_waitcnt lgkmcnt(4)
mvv 34,220,66
mvv 38,224,66
mvv 42,66,228
mvv 34,232,70
mvv 38,236,70
mvv 42,70,240
s_waitcnt lgkmcnt(0)
mvv 34,244,74
mvv 38,248,74
mvv 42,74,252
v_mul_f32 v120, v119, v94
s_nop 4
v_fma_f32 v34, v34, v118, v120
v_mul_f32 v121, v119, v95
v_fma_f32 v35, v35, v118, v121
v_mul_f32 v120, v119, v96
v_fma_f32 v36, v36, v118, v120
v_mul_f32 v121, v119, v97
v_fma_f32 v37, v37, v118, v121
v_mul_f32 v120, v119, v98
v_fma_f32 v38, v38, v118, v120
v_mul_f32 v121, v119, v99
v_fma_f32 v39, v39, v118, v121
v_mul_f32 v120, v119, v100
v_fma_f32 v40, v40, v118, v120
v_mul_f32 v121, v119, v101
v_fma_f32 v41, v41, v118, v121
v_cvt_pk_bf16_f32 v110, v34, v35
v_cvt_pk_bf16_f32 v111, v36, v37
v_cvt_pk_bf16_f32 v112, v38, v39
v_cvt_pk_bf16_f32 v113, v40, v41
ds_write_b128 v83, v[110:113] offset:4096
v_mul_f32 v103, v103, v116
v_fma_f32 v42, v42, v102, v103
v_mul_f32 v105, v105, v116
v_fma_f32 v43, v43, v104, v105
v_mul_f32 v107, v107, v116
v_fma_f32 v44, v44, v106, v107
v_mul_f32 v109, v109, v116
v_fma_f32 v45, v45, v108, v109
v_cvt_pk_bf16_f32 v78, v42, v43
v_cvt_pk_bf16_f32 v79, v44, v45
ds_write_b64 v83, v[78:79] offset:5120
s_waitcnt lgkmcnt(0)
s_barrier
s_endpgm
Cq:
s_cmp_eq_u32 s61, 0
s_cbranch_scc1 Bq
s_sub_u32 s61, s61, 1
s_sleep 2
buffer_load_dwordx2 v[122:123], v91, s[36:39], 0 offen sc1
s_waitcnt vmcnt(0)
ds_write_b32 v82, v122
v_sub_u32 v122, v122, v92
v_xor_b32 v123, v123, v115
v_cmp_lt_u64 vcc, s[62:63], v[122:123]
s_cbranch_vccnz Cq
s_branch Bq
Lr2:
s_cmp_eq_u32 s60, 2
s_cbranch_scc0 Lr3
s_waitcnt vmcnt(0)
s_barrier
v_accvgpr_read_b32 v71, a255
v_mov_b32 v70, 0x100
v_add_u32 v71, 0x9e3779b9, v71
buffer_store_dwordx2 v[70:71], v81, s[36:39], s52 offen sc1
v_add_u32 v88, v88, v70
s_sleep 32
E 0,65536
E 1,69632
E 2,73728
s_add_u32 s51, s51, 65536
E 0,77824
E 1,81920
E 2,86016
s_add_u32 s51, s51, 65536
E 0,90112
E 1,94208
E 2,98304
s_add_u32 s51, s51, 65536
E 0,102400
E 1,106496
E 2,110592
s_add_u32 s51, s51, 65536
s_mov_b64 exec, 0
s_cmp_eq_u32 s53, 0
s_cbranch_scc1 LsO0
s_cmp_eq_u32 s53, 1024
s_cbranch_scc1 LsO1
s_cmp_eq_u32 s53, 2048
s_cbranch_scc1 LsO2
s_branch LsO3
LwO:
s_mov_b64 exec, -1
s_waitcnt vmcnt(6)
s_barrier
v_add_u32 v4, 65536, v4
v_add_u32 v5, 65536, v5
v_add_u32 v6, 65536, v6
Ta 0,4,0
Ta 4,5,0
Ta 8,6,0
Ta 12,4,12288
Ta 16,5,12288
Ta 20,6,12288
E 0,114688
E 1,118784
E 2,122880
s_add_u32 s51, s51, 65536
E 0,126976
E 1,131072
E 2,135168
s_add_u32 s51, s51, 65536
E 0,139264
E 1,143360
E 2,147456
s_add_u32 s51, s51, 65536
E 0,151552
E 1,155648
E 2,159744
s_add_u32 s51, s51, 65536
s_cmp_ge_u32 s51, s57
s_cselect_b32 s60, 2097152, 0
s_sub_u32 s51, s51, s60
s_waitcnt lgkmcnt(0)
s_waitcnt vmcnt(12)
s_barrier
E 0,65536
Ta 24,4,24576
Ta 28,5,24576
E 1,69632
Ta 32,6,24576
Ta 36,4,36864
E 2,73728
s_add_u32 s51, s51, 65536
Ta 40,5,36864
E 0,77824
Ta 44,6,36864
E 1,81920
E 2,86016
s_add_u32 s51, s51, 65536
s_waitcnt lgkmcnt(0)
s_waitcnt vmcnt(12)
s_barrier
v_add_u32 v4, 49152, v4
v_add_u32 v5, 49152, v5
v_add_u32 v6, 49152, v6
E 0,90112
Ta 48,4,0
Ta 52,5,0
E 1,94208
Ta 56,6,0
Ta 60,4,12288
E 2,98304
s_add_u32 s51, s51, 65536
Ta 64,5,12288
E 0,102400
Ta 68,6,12288
E 1,106496
E 2,110592
s_add_u32 s51, s51, 65536
s_waitcnt lgkmcnt(0)
s_waitcnt vmcnt(12)
s_barrier
E 0,114688
Ta 72,4,24576
Ta 76,5,24576
E 1,118784
Ta 80,6,24576
Ta 84,4,36864
E 2,122880
s_add_u32 s51, s51, 65536
Ta 88,5,36864
E 0,126976
Ta 92,6,36864
E 1,131072
E 2,135168
s_add_u32 s51, s51, 65536
s_waitcnt lgkmcnt(0)
s_waitcnt vmcnt(12)
s_barrier
v_add_u32 v4, -49152, v4
v_add_u32 v5, -49152, v5
v_add_u32 v6, -49152, v6
E 0,139264
Ta 96,4,0
Ta 100,5,0
E 1,143360
Ta 104,6,0
Ta 108,4,12288
E 2,147456
s_add_u32 s51, s51, 65536
Ta 112,5,12288
E 0,151552
Ta 116,6,12288
E 1,155648
E 2,159744
s_add_u32 s51, s51, 65536
s_cmp_ge_u32 s51, s57
s_cselect_b32 s60, 2097152, 0
s_sub_u32 s51, s51, s60
s_waitcnt lgkmcnt(0)
s_waitcnt vmcnt(12)
s_barrier
E 0,65536
Ta 120,4,24576
Ta 124,5,24576
E 1,69632
Ta 128,6,24576
Ta 132,4,36864
E 2,73728
s_add_u32 s51, s51, 65536
Ta 136,5,36864
E 0,77824
Ta 140,6,36864
E 1,81920
E 2,86016
s_add_u32 s51, s51, 65536
s_waitcnt lgkmcnt(0)
s_waitcnt vmcnt(12)
s_barrier
v_add_u32 v4, 49152, v4
v_add_u32 v5, 49152, v5
v_add_u32 v6, 49152, v6
E 0,90112
Ta 144,4,0
Ta 148,5,0
E 1,94208
Ta 152,6,0
Ta 156,4,12288
E 2,98304
s_add_u32 s51, s51, 65536
Ta 160,5,12288
E 0,102400
Ta 164,6,12288
E 1,106496
LsO0:
s_cbranch_execz LsO4
E 2,110592
s_add_u32 s51, s51, 65536
s_waitcnt lgkmcnt(0)
s_waitcnt vmcnt(12)
s_barrier
E 0,114688
Ta 168,4,24576
Ta 172,5,24576
E 1,118784
Ta 176,6,24576
Ta 180,4,36864
E 2,122880
s_add_u32 s51, s51, 65536
Ta 184,5,36864
E 0,126976
Ta 188,6,36864
E 1,131072
E 2,135168
s_add_u32 s51, s51, 65536
s_waitcnt lgkmcnt(0)
s_waitcnt vmcnt(12)
s_barrier
v_add_u32 v4, -49152, v4
v_add_u32 v5, -49152, v5
v_add_u32 v6, -49152, v6
E 0,139264
Ta 192,4,0
Ta 196,5,0
E 1,143360
Ta 200,6,0
Ta 204,4,12288
E 2,147456
s_add_u32 s51, s51, 65536
Ta 208,5,12288
E 0,151552
Ta 212,6,12288
E 1,155648
E 2,159744
s_add_u32 s51, s51, 65536
s_cmp_ge_u32 s51, s57
s_cselect_b32 s60, 2097152, 0
s_sub_u32 s51, s51, s60
s_waitcnt lgkmcnt(0)
s_waitcnt vmcnt(12)
s_barrier
E 0,65536
Ta 216,4,24576
Ta 220,5,24576
E 1,69632
Ta 224,6,24576
Ta 228,4,36864
E 2,73728
s_add_u32 s51, s51, 65536
Ta 232,5,36864
E 0,77824
Ta 236,6,36864
E 1,81920
E 2,86016
s_add_u32 s51, s51, 65536
s_waitcnt lgkmcnt(0)
s_waitcnt vmcnt(12)
s_barrier
v_add_u32 v4, 49152, v4
v_add_u32 v5, 49152, v5
v_add_u32 v6, 49152, v6
E 0,90112
Ta 240,4,0
Ta 244,5,0
E 1,94208
Ta 248,6,0
Ta 252,4,12288
E 2,98304
s_add_u32 s51, s51, 65536
Tv 128,5,12288
E 0,102400
Tv 132,6,12288
E 1,106496
E 2,110592
s_add_u32 s51, s51, 65536
s_waitcnt lgkmcnt(0)
s_waitcnt vmcnt(12)
s_barrier
E 0,114688
Tv 136,4,24576
Tv 140,5,24576
E 1,118784
Tv 144,6,24576
Tv 148,4,36864
E 2,122880
s_add_u32 s51, s51, 65536
Tv 152,5,36864
E 0,126976
Tv 156,6,36864
E 1,131072
E 2,135168
s_add_u32 s51, s51, 65536
s_waitcnt lgkmcnt(0)
s_waitcnt vmcnt(12)
s_barrier
v_add_u32 v4, -49152, v4
v_add_u32 v5, -49152, v5
v_add_u32 v6, -49152, v6
E 0,139264
Tv 160,4,0
Tv 164,5,0
E 1,143360
Tv 168,6,0
Tv 172,4,12288
E 2,147456
s_add_u32 s51, s51, 65536
Tv 176,5,12288
E 0,151552
Tv 180,6,12288
E 1,155648
E 2,159744
s_add_u32 s51, s51, 65536
s_cmp_ge_u32 s51, s57
s_cselect_b32 s60, 2097152, 0
s_sub_u32 s51, s51, s60
s_waitcnt lgkmcnt(0)
s_waitcnt vmcnt(12)
s_barrier
Tv 184,4,24576
Tv 188,5,24576
Tv 192,6,24576
Tv 196,4,36864
Tv 200,5,36864
Tv 204,6,36864
s_waitcnt lgkmcnt(0)
s_waitcnt vmcnt(6)
s_barrier
v_add_u32 v4, 49152, v4
v_add_u32 v5, 49152, v5
v_add_u32 v6, 49152, v6
Tv 208,4,0
Tv 212,5,0
Tv 216,6,0
Tv 220,4,12288
Tv 224,5,12288
Tv 228,6,12288
s_waitcnt lgkmcnt(0)
s_waitcnt vmcnt(0)
s_barrier
Tv 232,4,24576
Tv 236,5,24576
Tv 240,6,24576
Tv 244,4,36864
Tv 248,5,36864
Tv 252,6,36864
s_waitcnt lgkmcnt(0)
s_waitcnt vmcnt(0)
s_barrier
s_nop 7
s_and_b32 s60, s48, 3
s_cmp_eq_u32 s60, 1
s_cbranch_scc1 LvO1
s_cmp_eq_u32 s60, 2
s_cbranch_scc1 LvO2
LvO0:
LsO1:
s_cbranch_execz LsO5
buffer_load_dwordx2 v[74:75], v87, s[36:39], 0 offen sc1
s_waitcnt vmcnt(0)
ds_write_b32 v82, v74
v_sub_u32 v74, v74, v88
v_xor_b32 v75, v75, v71
v_cmp_lt_u64 vcc, s[62:63], v[74:75]
s_waitcnt lgkmcnt(0)
s_cbranch_vccnz CO0_1
BO0_1:
s_barrier
buffer_load_dwordx4 v[56:59], v89, s[36:39], s49 offen sc1
buffer_load_dwordx2 v[60:61], v90, s[36:39], s49 offen sc1
s_add_u32 s49, s49, 346112
s_add_u32 m0, s50, 0
s_nop 0
D 85,0
D 85,1024
D 85,2048
D 85,3072
s_add_u32 m0, s50, 4096
s_nop 0
D 86,0
D 86,1024
D 86,2048
D 86,3072
s_mov_b32 m0, 142336
s_nop 0
buffer_load_dword v87, s[36:39], 0 offen sc1 lds
v_add_u32 v88, 1, v88
s_waitcnt vmcnt(10)
v_lshlrev_b32 v0, 16, v56
v_and_b32 v1, 0xffff0000, v56
v_lshlrev_b32 v2, 16, v57
v_and_b32 v3, 0xffff0000, v57
v_lshlrev_b32 v4, 16, v58
v_and_b32 v5, 0xffff0000, v58
v_lshlrev_b32 v6, 16, v59
v_and_b32 v7, 0xffff0000, v59
s_waitcnt vmcnt(9)
v_lshlrev_b32 v8, 16, v60
v_and_b32 v9, 0xffff0000, v60
v_lshlrev_b32 v10, 16, v61
v_and_b32 v11, 0xffff0000, v61
buffer_load_dwordx4 v[56:59], v89, s[36:39], s49 offen sc1
buffer_load_dwordx2 v[60:61], v90, s[36:39], s49 offen sc1
ds_read_b32 v74, v82 offset:0
s_waitcnt lgkmcnt(0)
v_cmp_gt_i32 vcc, v88, v74
s_waitcnt vmcnt(3)
s_cbranch_vccnz CO0_2
BO0_2:
s_add_u32 s49, s49, 346112
s_barrier
s_add_u32 m0, s50, 32768
R 24,0
D 85,0
R 28,1024
D 85,1024
R 32,2048
R 36,3072
D 85,2048
s_waitcnt lgkmcnt(2)
mav 0,0,24
R 40,4096
mav 4,4,24
mav 8,8,24
D 85,3072
mav 0,12,28
R 44,5120
mav 4,16,28
mav 8,20,28
s_add_u32 m0, s50, 36864
s_waitcnt lgkmcnt(2)
mav 0,24,32
R 48,6144
mav 4,28,32
D 86,0
mav 8,32,32
mav 0,36,36
R 52,7168
mav 4,40,36
mav 8,44,36
D 86,1024
s_waitcnt lgkmcnt(2)
mav 0,48,40
R 24,8192
mav 4,52,40
mav 8,56,40
mav 0,60,44
R 28,9216
mav 4,64,44
D 86,2048
mav 8,68,44
s_waitcnt lgkmcnt(2)
mav 0,72,48
R 32,10240
mav 4,76,48
mav 8,80,48
D 86,3072
mav 0,84,52
R 36,11264
mav 4,88,52
mav 8,92,52
s_waitcnt lgkmcnt(2)
mav 0,96,24
R 40,12288
mav 4,100,24
mav 8,104,24
mav 0,108,28
R 44,13312
mav 4,112,28
mav 8,116,28
s_waitcnt lgkmcnt(2)
mav 0,120,32
R 48,14336
mav 4,124,32
mav 8,128,32
mav 0,132,36
R 52,15360
mav 4,136,36
s_mov_b32 m0, 142336
mav 8,140,36
s_waitcnt lgkmcnt(2)
mav 0,144,40
R 24,16384
buffer_load_dword v87, s[36:39], 0 offen sc1 lds
mav 4,148,40
mav 8,152,40
mav 0,156,44
R 28,17408
mav 4,160,44
v_add_u32 v88, 1, v88
mav 8,164,44
s_waitcnt lgkmcnt(2)
mav 0,168,48
R 32,18432
mav 4,172,48
mav 8,176,48
mav 0,180,52
R 36,19456
mav 4,184,52
mav 8,188,52
s_waitcnt lgkmcnt(2)
mav 0,192,24
R 40,20480
mav 4,196,24
mav 8,200,24
s_waitcnt vmcnt(10)
v_lshlrev_b32 v12, 16, v56
mav 0,204,28
R 44,21504
v_and_b32 v13, 0xffff0000, v56
mav 4,208,28
v_lshlrev_b32 v14, 16, v57
v_and_b32 v15, 0xffff0000, v57
mav 8,212,28
v_lshlrev_b32 v16, 16, v58
s_waitcnt lgkmcnt(2)
mav 0,216,32
R 48,22528
v_and_b32 v17, 0xffff0000, v58
mav 4,220,32
v_lshlrev_b32 v18, 16, v59
v_and_b32 v19, 0xffff0000, v59
mav 8,224,32
s_waitcnt vmcnt(9)
v_lshlrev_b32 v20, 16, v60
mav 0,228,36
R 52,23552
v_and_b32 v21, 0xffff0000, v60
mav 4,232,36
v_lshlrev_b32 v22, 16, v61
v_and_b32 v23, 0xffff0000, v61
mav 8,236,36
s_waitcnt lgkmcnt(2)
mav 0,240,40
R 24,24576
mav 4,244,40
mav 8,248,40
mav 0,252,44
R 28,25600
mvv 4,128,44
buffer_load_dwordx4 v[56:59], v89, s[36:39], s49 offen sc1
mvv 8,132,44
s_waitcnt lgkmcnt(2)
mvv 0,136,48
R 32,26624
mvv 4,140,48
mvv 8,144,48
buffer_load_dwordx2 v[60:61], v90, s[36:39], s49 offen sc1
mvv 0,148,52
R 36,27648
mvv 4,152,52
mvv 8,156,52
s_waitcnt lgkmcnt(2)
mvv 0,160,24
R 40,28672
mvv 4,164,24
ds_read_b32 v74, v82 offset:0
mvv 8,168,24
mvv 0,172,28
R 44,29696
mvv 4,176,28
mvv 8,180,28
s_waitcnt lgkmcnt(3)
mvv 0,184,32
R 48,30720
s_xor_b32 s50, s50, 0x10000
mvv 4,188,32
mvv 8,192,32
mvv 0,196,36
R 52,31744
mvv 4,200,36
s_waitcnt lgkmcnt(2)
v_cmp_gt_i32 vcc, v88, v74
mvv 8,204,36
s_waitcnt vmcnt(3)
s_cbranch_vccnz CO0_3
BO0_3:
mvv 0,208,40
s_barrier
R 24,32768
mvv 4,212,40
s_add_u32 s49, s49, 346112
s_add_u32 m0, s50, 0
mvv 8,216,40
D 85,0
mvv 0,220,44
R 28,33792
mvv 4,224,44
mvv 8,228,44
s_waitcnt lgkmcnt(2)
mvv 0,232,48
R 32,34816
D 85,1024
mvv 4,236,48
mvv 8,240,48
mvv 0,244,52
R 36,35840
mvv 4,248,52
D 85,2048
mvv 8,252,52
s_waitcnt lgkmcnt(2)
mav 12,0,24
R 40,36864
mav 16,4,24
v_cvt_pk_bf16_f32 v62, v0, v1
v_cvt_pk_bf16_f32 v63, v2, v3
mav 20,8,24
D 85,3072
v_cvt_pk_bf16_f32 v64, v4, v5
mav 12,12,28
R 44,37888
v_cvt_pk_bf16_f32 v65, v6, v7
mav 16,16,28
v_cvt_pk_bf16_f32 v66, v8, v9
v_cvt_pk_bf16_f32 v67, v10, v11
mav 20,20,28
s_add_u32 m0, s50, 4096
s_waitcnt lgkmcnt(2)
mav 12,24,32
R 48,38912
buffer_store_dwordx4 v[62:65], v83, s[36:39], s49 offen sc1
mav 16,28,32
v_cvt_pk_bf16_f32 v68, v8, v9
v_cvt_pk_bf16_f32 v69, v10, v11
mav 20,32,32
v_permlane16_swap_b32 v66, v68
v_permlane16_swap_b32 v67, v69
mav 12,36,36
R 52,39936
v_mul_f32 v73, v0, v0
mav 16,40,36
buffer_store_dwordx4 v[66:69], v84, s[36:39], s49 offen sc1
v_add_f32 v72, v1, v0
mav 20,44,36
v_fmac_f32 v73, v1, v1
s_waitcnt lgkmcnt(2)
mav 12,48,40
R 24,40960
v_add_f32 v72, v2, v72
mav 16,52,40
v_fmac_f32 v73, v2, v2
v_add_f32 v72, v3, v72
mav 20,56,40
D 86,0
v_fmac_f32 v73, v3, v3
mav 12,60,44
R 28,41984
v_add_f32 v72, v4, v72
mav 16,64,44
v_fmac_f32 v73, v4, v4
v_add_f32 v72, v5, v72
mav 20,68,44
v_fmac_f32 v73, v5, v5
s_waitcnt lgkmcnt(2)
mav 12,72,48
R 32,43008
D 86,1024
mav 16,76,48
v_add_f32 v72, v6, v72
v_fmac_f32 v73, v6, v6
LsO2:
s_cbranch_execz LsO6
mav 20,80,48
v_add_f32 v72, v7, v72
v_fmac_f32 v73, v7, v7
mav 12,84,52
R 36,44032
v_add_f32 v72, v8, v72
mav 16,88,52
D 86,2048
v_fmac_f32 v73, v8, v8
mav 20,92,52
v_add_f32 v72, v9, v72
s_waitcnt lgkmcnt(2)
mav 12,96,24
R 40,45056
v_fmac_f32 v73, v9, v9
mav 16,100,24
v_add_f32 v72, v10, v72
v_fmac_f32 v73, v10, v10
mav 20,104,24
D 86,3072
v_add_f32 v72, v11, v72
mav 12,108,28
R 44,46080
v_fmac_f32 v73, v11, v11
mav 16,112,28
mav 20,116,28
v_permlane32_swap_b32 v72, v73
s_waitcnt lgkmcnt(2)
mav 12,120,32
R 48,47104
v_add_f32 v72, v72, v73
mav 16,124,32
mav 20,128,32
mav 12,132,36
R 52,48128
mav 16,136,36
s_mov_b32 m0, 142336
mav 20,140,36
s_waitcnt lgkmcnt(2)
mav 12,144,40
R 24,49152
buffer_load_dword v87, s[36:39], 0 offen sc1 lds
mav 16,148,40
mav 20,152,40
mav 12,156,44
R 28,50176
mav 16,160,44
v_add_u32 v88, 1, v88
mav 20,164,44
s_waitcnt lgkmcnt(2)
mav 12,168,48
R 32,51200
mav 16,172,48
mav 20,176,48
mav 12,180,52
R 36,52224
mav 16,184,52
v_add_u32 v70, 1, v70
mav 20,188,52
s_waitcnt lgkmcnt(2)
mav 12,192,24
R 40,53248
mav 16,196,24
mav 20,200,24
s_waitcnt vmcnt(12)
v_lshlrev_b32 v0, 16, v56
mav 12,204,28
R 44,54272
v_and_b32 v1, 0xffff0000, v56
mav 16,208,28
v_lshlrev_b32 v2, 16, v57
v_and_b32 v3, 0xffff0000, v57
mav 20,212,28
v_lshlrev_b32 v4, 16, v58
s_waitcnt lgkmcnt(2)
mav 12,216,32
R 48,55296
v_and_b32 v5, 0xffff0000, v58
mav 16,220,32
v_lshlrev_b32 v6, 16, v59
v_and_b32 v7, 0xffff0000, v59
mav 20,224,32
s_waitcnt vmcnt(11)
v_lshlrev_b32 v8, 16, v60
mav 12,228,36
R 52,56320
v_and_b32 v9, 0xffff0000, v60
mav 16,232,36
v_lshlrev_b32 v10, 16, v61
v_and_b32 v11, 0xffff0000, v61
mav 20,236,36
s_waitcnt lgkmcnt(2)
mav 12,240,40
R 24,57344
mav 16,244,40
mav 20,248,40
mav 12,252,44
R 28,58368
mvv 16,128,44
buffer_load_dwordx4 v[56:59], v89, s[36:39], s49 offen sc1
mvv 20,132,44
s_waitcnt lgkmcnt(2)
mvv 12,136,48
R 32,59392
mvv 16,140,48
mvv 20,144,48
buffer_load_dwordx2 v[60:61], v90, s[36:39], s49 offen sc1
mvv 12,148,52
R 36,60416
mvv 16,152,52
s_waitcnt vmcnt(7)
buffer_store_dwordx2 v[70:71], v81, s[36:39], s52 offen sc1
mvv 20,156,52
s_waitcnt lgkmcnt(2)
mvv 12,160,24
R 40,61440
mvv 16,164,24
ds_read_b32 v74, v82 offset:0
mvv 20,168,24
ds_write_b32 v91, v72 offset:0
mvv 12,172,28
R 44,62464
mvv 16,176,28
mvv 20,180,28
s_waitcnt lgkmcnt(4)
mvv 12,184,32
R 48,63488
mvv 16,188,32
mvv 20,192,32
mvv 12,196,36
R 52,64512
v_xor_b32 v80, 0x10000, v80
mvv 16,200,36
s_waitcnt lgkmcnt(2)
v_cmp_gt_i32 vcc, v88, v74
mvv 20,204,36
s_waitcnt vmcnt(4)
s_cbranch_vccnz CO0_4
BO0_4:
mvv 12,208,40
s_barrier
R 24,0
mvv 16,212,40
s_add_u32 s49, s49, 346112
s_add_u32 m0, s50, 32768
mvv 20,216,40
D 85,0
mvv 12,220,44
R 28,1024
mvv 16,224,44
mvv 20,228,44
s_waitcnt lgkmcnt(2)
mvv 12,232,48
R 32,2048
D 85,1024
mvv 16,236,48
mvv 20,240,48
mvv 12,244,52
R 36,3072
mvv 16,248,52
D 85,2048
mvv 20,252,52
s_mov_b32 s60, -30
.p2align 6
LO0_5:
s_waitcnt lgkmcnt(2)
mav 0,0,24
R 40,4096
mav 4,4,24
v_cvt_pk_bf16_f32 v62, v12, v13
v_cvt_pk_bf16_f32 v63, v14, v15
mav 8,8,24
D 85,3072
v_cvt_pk_bf16_f32 v64, v16, v17
mav 0,12,28
R 44,5120
v_cvt_pk_bf16_f32 v65, v18, v19
mav 4,16,28
v_cvt_pk_bf16_f32 v66, v20, v21
v_cvt_pk_bf16_f32 v67, v22, v23
mav 8,20,28
s_add_u32 m0, s50, 36864
s_waitcnt lgkmcnt(2)
mav 0,24,32
R 48,6144
buffer_store_dwordx4 v[62:65], v83, s[36:39], s49 offen sc1
mav 4,28,32
v_cvt_pk_bf16_f32 v68, v20, v21
v_cvt_pk_bf16_f32 v69, v22, v23
mav 8,32,32
v_permlane16_swap_b32 v66, v68
v_permlane16_swap_b32 v67, v69
mav 0,36,36
R 52,7168
v_mul_f32 v73, v12, v12
mav 4,40,36
buffer_store_dwordx4 v[66:69], v84, s[36:39], s49 offen sc1
v_add_f32 v72, v13, v12
mav 8,44,36
v_fmac_f32 v73, v13, v13
s_waitcnt lgkmcnt(2)
mav 0,48,40
R 24,8192
v_add_f32 v72, v14, v72
mav 4,52,40
v_fmac_f32 v73, v14, v14
v_add_f32 v72, v15, v72
mav 8,56,40
D 86,0
v_fmac_f32 v73, v15, v15
mav 0,60,44
R 28,9216
v_add_f32 v72, v16, v72
mav 4,64,44
v_fmac_f32 v73, v16, v16
v_add_f32 v72, v17, v72
mav 8,68,44
v_fmac_f32 v73, v17, v17
s_waitcnt lgkmcnt(2)
mav 0,72,48
R 32,10240
D 86,1024
mav 4,76,48
v_add_f32 v72, v18, v72
v_fmac_f32 v73, v18, v18
mav 8,80,48
v_add_f32 v72, v19, v72
v_fmac_f32 v73, v19, v19
mav 0,84,52
R 36,11264
v_add_f32 v72, v20, v72
mav 4,88,52
D 86,2048
v_fmac_f32 v73, v20, v20
mav 8,92,52
v_add_f32 v72, v21, v72
s_waitcnt lgkmcnt(2)
mav 0,96,24
R 40,12288
v_fmac_f32 v73, v21, v21
mav 4,100,24
v_add_f32 v72, v22, v72
v_fmac_f32 v73, v22, v22
mav 8,104,24
D 86,3072
v_add_f32 v72, v23, v72
mav 0,108,28
R 44,13312
v_fmac_f32 v73, v23, v23
mav 4,112,28
mav 8,116,28
v_permlane32_swap_b32 v72, v73
s_waitcnt lgkmcnt(2)
mav 0,120,32
R 48,14336
v_add_f32 v72, v72, v73
mav 4,124,32
mav 8,128,32
mav 0,132,36
R 52,15360
mav 4,136,36
s_mov_b32 m0, 142336
mav 8,140,36
s_waitcnt lgkmcnt(2)
mav 0,144,40
R 24,16384
buffer_load_dword v87, s[36:39], 0 offen sc1 lds
mav 4,148,40
mav 8,152,40
mav 0,156,44
R 28,17408
mav 4,160,44
v_add_u32 v88, 1, v88
mav 8,164,44
s_waitcnt lgkmcnt(2)
mav 0,168,48
R 32,18432
mav 4,172,48
mav 8,176,48
mav 0,180,52
R 36,19456
mav 4,184,52
v_add_u32 v70, 1, v70
mav 8,188,52
s_waitcnt lgkmcnt(2)
mav 0,192,24
R 40,20480
mav 4,196,24
mav 8,200,24
s_waitcnt vmcnt(13)
v_lshlrev_b32 v12, 16, v56
mav 0,204,28
R 44,21504
v_and_b32 v13, 0xffff0000, v56
mav 4,208,28
v_lshlrev_b32 v14, 16, v57
v_and_b32 v15, 0xffff0000, v57
mav 8,212,28
v_lshlrev_b32 v16, 16, v58
s_waitcnt lgkmcnt(2)
mav 0,216,32
R 48,22528
v_and_b32 v17, 0xffff0000, v58
mav 4,220,32
v_lshlrev_b32 v18, 16, v59
v_and_b32 v19, 0xffff0000, v59
mav 8,224,32
s_waitcnt vmcnt(12)
v_lshlrev_b32 v20, 16, v60
mav 0,228,36
R 52,23552
v_and_b32 v21, 0xffff0000, v60
mav 4,232,36
v_lshlrev_b32 v22, 16, v61
v_and_b32 v23, 0xffff0000, v61
mav 8,236,36
s_waitcnt lgkmcnt(2)
mav 0,240,40
R 24,24576
mav 4,244,40
mav 8,248,40
mav 0,252,44
R 28,25600
mvv 4,128,44
buffer_load_dwordx4 v[56:59], v89, s[36:39], s49 offen sc1
mvv 8,132,44
s_waitcnt lgkmcnt(2)
mvv 0,136,48
R 32,26624
mvv 4,140,48
mvv 8,144,48
buffer_load_dwordx2 v[60:61], v90, s[36:39], s49 offen sc1
mvv 0,148,52
R 36,27648
mvv 4,152,52
s_waitcnt vmcnt(7)
buffer_store_dwordx2 v[70:71], v81, s[36:39], s52 offen sc1
mvv 8,156,52
s_waitcnt lgkmcnt(2)
mvv 0,160,24
R 40,28672
mvv 4,164,24
ds_read_b32 v74, v82 offset:0
mvv 8,168,24
ds_write_b32 v91, v72 offset:1024
mvv 0,172,28
R 44,29696
mvv 4,176,28
mvv 8,180,28
s_waitcnt lgkmcnt(4)
mvv 0,184,32
R 48,30720
s_xor_b32 s50, s50, 0x10000
mvv 4,188,32
mvv 8,192,32
mvv 0,196,36
R 52,31744
mvv 4,200,36
s_waitcnt lgkmcnt(2)
v_cmp_gt_i32 vcc, v88, v74
mvv 8,204,36
s_waitcnt vmcnt(4)
s_cbranch_vccnz CO0_6
BO0_6:
mvv 0,208,40
s_barrier
R 24,32768
mvv 4,212,40
s_add_u32 s49, s49, 346112
s_add_u32 m0, s50, 0
mvv 8,216,40
D 85,0
mvv 0,220,44
R 28,33792
mvv 4,224,44
mvv 8,228,44
s_waitcnt lgkmcnt(2)
mvv 0,232,48
R 32,34816
D 85,1024
mvv 4,236,48
mvv 8,240,48
mvv 0,244,52
R 36,35840
mvv 4,248,52
D 85,2048
mvv 8,252,52
)"
R"(
s_waitcnt lgkmcnt(2)
mav 12,0,24
R 40,36864
mav 16,4,24
v_cvt_pk_bf16_f32 v62, v0, v1
v_cvt_pk_bf16_f32 v63, v2, v3
mav 20,8,24
D 85,3072
v_cvt_pk_bf16_f32 v64, v4, v5
mav 12,12,28
R 44,37888
v_cvt_pk_bf16_f32 v65, v6, v7
mav 16,16,28
v_cvt_pk_bf16_f32 v66, v8, v9
v_cvt_pk_bf16_f32 v67, v10, v11
mav 20,20,28
s_add_u32 m0, s50, 4096
s_waitcnt lgkmcnt(2)
mav 12,24,32
R 48,38912
buffer_store_dwordx4 v[62:65], v83, s[36:39], s49 offen sc1
mav 16,28,32
v_cvt_pk_bf16_f32 v68, v8, v9
v_cvt_pk_bf16_f32 v69, v10, v11
mav 20,32,32
v_permlane16_swap_b32 v66, v68
v_permlane16_swap_b32 v67, v69
mav 12,36,36
R 52,39936
v_mul_f32 v73, v0, v0
mav 16,40,36
buffer_store_dwordx4 v[66:69], v84, s[36:39], s49 offen sc1
v_add_f32 v72, v1, v0
mav 20,44,36
v_fmac_f32 v73, v1, v1
s_waitcnt lgkmcnt(2)
mav 12,48,40
R 24,40960
v_add_f32 v72, v2, v72
mav 16,52,40
v_fmac_f32 v73, v2, v2
v_add_f32 v72, v3, v72
mav 20,56,40
D 86,0
v_fmac_f32 v73, v3, v3
mav 12,60,44
R 28,41984
v_add_f32 v72, v4, v72
mav 16,64,44
v_fmac_f32 v73, v4, v4
v_add_f32 v72, v5, v72
mav 20,68,44
v_fmac_f32 v73, v5, v5
s_waitcnt lgkmcnt(2)
mav 12,72,48
R 32,43008
D 86,1024
mav 16,76,48
v_add_f32 v72, v6, v72
v_fmac_f32 v73, v6, v6
mav 20,80,48
v_add_f32 v72, v7, v72
v_fmac_f32 v73, v7, v7
mav 12,84,52
R 36,44032
v_add_f32 v72, v8, v72
mav 16,88,52
D 86,2048
v_fmac_f32 v73, v8, v8
mav 20,92,52
v_add_f32 v72, v9, v72
s_waitcnt lgkmcnt(2)
mav 12,96,24
R 40,45056
v_fmac_f32 v73, v9, v9
mav 16,100,24
v_add_f32 v72, v10, v72
v_fmac_f32 v73, v10, v10
mav 20,104,24
D 86,3072
v_add_f32 v72, v11, v72
mav 12,108,28
R 44,46080
v_fmac_f32 v73, v11, v11
mav 16,112,28
mav 20,116,28
v_permlane32_swap_b32 v72, v73
s_waitcnt lgkmcnt(2)
mav 12,120,32
R 48,47104
v_add_f32 v72, v72, v73
mav 16,124,32
mav 20,128,32
mav 12,132,36
R 52,48128
mav 16,136,36
s_mov_b32 m0, 142336
mav 20,140,36
s_waitcnt lgkmcnt(2)
mav 12,144,40
R 24,49152
buffer_load_dword v87, s[36:39], 0 offen sc1 lds
mav 16,148,40
mav 20,152,40
mav 12,156,44
R 28,50176
mav 16,160,44
v_add_u32 v88, 1, v88
mav 20,164,44
s_waitcnt lgkmcnt(2)
mav 12,168,48
R 32,51200
mav 16,172,48
mav 20,176,48
mav 12,180,52
R 36,52224
mav 16,184,52
v_add_u32 v70, 1, v70
mav 20,188,52
s_waitcnt lgkmcnt(2)
mav 12,192,24
R 40,53248
mav 16,196,24
mav 20,200,24
s_waitcnt vmcnt(13)
v_lshlrev_b32 v0, 16, v56
mav 12,204,28
R 44,54272
v_and_b32 v1, 0xffff0000, v56
mav 16,208,28
v_lshlrev_b32 v2, 16, v57
v_and_b32 v3, 0xffff0000, v57
mav 20,212,28
v_lshlrev_b32 v4, 16, v58
s_waitcnt lgkmcnt(2)
mav 12,216,32
R 48,55296
v_and_b32 v5, 0xffff0000, v58
mav 16,220,32
v_lshlrev_b32 v6, 16, v59
v_and_b32 v7, 0xffff0000, v59
mav 20,224,32
s_waitcnt vmcnt(12)
v_lshlrev_b32 v8, 16, v60
mav 12,228,36
R 52,56320
v_and_b32 v9, 0xffff0000, v60
mav 16,232,36
v_lshlrev_b32 v10, 16, v61
v_and_b32 v11, 0xffff0000, v61
mav 20,236,36
s_waitcnt lgkmcnt(2)
mav 12,240,40
R 24,57344
mav 16,244,40
mav 20,248,40
mav 12,252,44
R 28,58368
mvv 16,128,44
buffer_load_dwordx4 v[56:59], v89, s[36:39], s49 offen sc1
mvv 20,132,44
s_waitcnt lgkmcnt(2)
mvv 12,136,48
R 32,59392
mvv 16,140,48
mvv 20,144,48
buffer_load_dwordx2 v[60:61], v90, s[36:39], s49 offen sc1
mvv 12,148,52
R 36,60416
mvv 16,152,52
s_waitcnt vmcnt(7)
buffer_store_dwordx2 v[70:71], v81, s[36:39], s52 offen sc1
mvv 20,156,52
s_waitcnt lgkmcnt(2)
mvv 12,160,24
R 40,61440
mvv 16,164,24
ds_read_b32 v74, v82 offset:0
mvv 20,168,24
ds_write_b32 v91, v72 offset:0
mvv 12,172,28
R 44,62464
mvv 16,176,28
mvv 20,180,28
s_waitcnt lgkmcnt(4)
mvv 12,184,32
R 48,63488
mvv 16,188,32
mvv 20,192,32
mvv 12,196,36
R 52,64512
v_xor_b32 v80, 0x10000, v80
mvv 16,200,36
s_waitcnt lgkmcnt(2)
v_cmp_gt_i32 vcc, v88, v74
mvv 20,204,36
s_waitcnt vmcnt(4)
s_cbranch_vccnz CO0_7
BO0_7:
mvv 12,208,40
s_barrier
R 24,0
mvv 16,212,40
s_add_u32 s49, s49, 346112
s_add_u32 m0, s50, 32768
mvv 20,216,40
LsO3:
s_cbranch_execz LsO7
D 85,0
mvv 12,220,44
R 28,1024
mvv 16,224,44
mvv 20,228,44
s_waitcnt lgkmcnt(2)
mvv 12,232,48
R 32,2048
D 85,1024
mvv 16,236,48
mvv 20,240,48
mvv 12,244,52
R 36,3072
mvv 16,248,52
D 85,2048
mvv 20,252,52
s_add_u32 s60, s60, 1
s_cbranch_scc0 LO0_5
s_waitcnt lgkmcnt(2)
mav 0,0,24
R 40,4096
mav 4,4,24
v_cvt_pk_bf16_f32 v62, v12, v13
v_cvt_pk_bf16_f32 v63, v14, v15
mav 8,8,24
D 85,3072
v_cvt_pk_bf16_f32 v64, v16, v17
mav 0,12,28
R 44,5120
v_cvt_pk_bf16_f32 v65, v18, v19
mav 4,16,28
v_cvt_pk_bf16_f32 v66, v20, v21
v_cvt_pk_bf16_f32 v67, v22, v23
mav 8,20,28
s_add_u32 m0, s50, 36864
s_waitcnt lgkmcnt(2)
mav 0,24,32
R 48,6144
buffer_store_dwordx4 v[62:65], v83, s[36:39], s49 offen sc1
mav 4,28,32
v_cvt_pk_bf16_f32 v68, v20, v21
v_cvt_pk_bf16_f32 v69, v22, v23
mav 8,32,32
v_permlane16_swap_b32 v66, v68
v_permlane16_swap_b32 v67, v69
mav 0,36,36
R 52,7168
v_mul_f32 v73, v12, v12
mav 4,40,36
buffer_store_dwordx4 v[66:69], v84, s[36:39], s49 offen sc1
v_add_f32 v72, v13, v12
mav 8,44,36
v_fmac_f32 v73, v13, v13
s_waitcnt lgkmcnt(2)
mav 0,48,40
R 24,8192
v_add_f32 v72, v14, v72
mav 4,52,40
v_fmac_f32 v73, v14, v14
v_add_f32 v72, v15, v72
mav 8,56,40
D 86,0
v_fmac_f32 v73, v15, v15
mav 0,60,44
R 28,9216
v_add_f32 v72, v16, v72
mav 4,64,44
v_fmac_f32 v73, v16, v16
v_add_f32 v72, v17, v72
mav 8,68,44
v_fmac_f32 v73, v17, v17
s_waitcnt lgkmcnt(2)
mav 0,72,48
R 32,10240
D 86,1024
mav 4,76,48
v_add_f32 v72, v18, v72
v_fmac_f32 v73, v18, v18
mav 8,80,48
v_add_f32 v72, v19, v72
v_fmac_f32 v73, v19, v19
mav 0,84,52
R 36,11264
v_add_f32 v72, v20, v72
mav 4,88,52
D 86,2048
v_fmac_f32 v73, v20, v20
mav 8,92,52
v_add_f32 v72, v21, v72
s_waitcnt lgkmcnt(2)
mav 0,96,24
R 40,12288
v_fmac_f32 v73, v21, v21
mav 4,100,24
v_add_f32 v72, v22, v72
v_fmac_f32 v73, v22, v22
mav 8,104,24
D 86,3072
v_add_f32 v72, v23, v72
mav 0,108,28
R 44,13312
v_fmac_f32 v73, v23, v23
mav 4,112,28
mav 8,116,28
v_permlane32_swap_b32 v72, v73
s_waitcnt lgkmcnt(2)
mav 0,120,32
R 48,14336
v_add_f32 v72, v72, v73
mav 4,124,32
mav 8,128,32
mav 0,132,36
R 52,15360
mav 4,136,36
mav 8,140,36
s_waitcnt lgkmcnt(2)
mav 0,144,40
R 24,16384
mav 4,148,40
mav 8,152,40
mav 0,156,44
R 28,17408
mav 4,160,44
mav 8,164,44
s_waitcnt lgkmcnt(2)
mav 0,168,48
R 32,18432
mav 4,172,48
mav 8,176,48
mav 0,180,52
R 36,19456
mav 4,184,52
v_add_u32 v70, 1, v70
mav 8,188,52
s_waitcnt lgkmcnt(2)
mav 0,192,24
R 40,20480
mav 4,196,24
mav 8,200,24
s_waitcnt vmcnt(12)
v_lshlrev_b32 v12, 16, v56
mav 0,204,28
R 44,21504
v_and_b32 v13, 0xffff0000, v56
mav 4,208,28
v_lshlrev_b32 v14, 16, v57
v_and_b32 v15, 0xffff0000, v57
mav 8,212,28
v_lshlrev_b32 v16, 16, v58
s_waitcnt lgkmcnt(2)
mav 0,216,32
R 48,22528
v_and_b32 v17, 0xffff0000, v58
mav 4,220,32
v_lshlrev_b32 v18, 16, v59
v_and_b32 v19, 0xffff0000, v59
mav 8,224,32
s_waitcnt vmcnt(11)
v_lshlrev_b32 v20, 16, v60
mav 0,228,36
R 52,23552
v_and_b32 v21, 0xffff0000, v60
mav 4,232,36
v_lshlrev_b32 v22, 16, v61
v_and_b32 v23, 0xffff0000, v61
mav 8,236,36
s_waitcnt lgkmcnt(2)
mav 0,240,40
R 24,24576
mav 4,244,40
mav 8,248,40
mav 0,252,44
R 28,25600
mvv 4,128,44
mvv 8,132,44
s_waitcnt lgkmcnt(2)
mvv 0,136,48
R 32,26624
mvv 4,140,48
mvv 8,144,48
mvv 0,148,52
R 36,27648
mvv 4,152,52
s_waitcnt vmcnt(4)
buffer_store_dwordx2 v[70:71], v81, s[36:39], s52 offen sc1
mvv 8,156,52
s_waitcnt lgkmcnt(2)
mvv 0,160,24
R 40,28672
mvv 4,164,24
mvv 8,168,24
ds_write_b32 v91, v72 offset:1024
mvv 0,172,28
R 44,29696
mvv 4,176,28
mvv 8,180,28
s_waitcnt lgkmcnt(3)
mvv 0,184,32
R 48,30720
mvv 4,188,32
mvv 8,192,32
mvv 0,196,36
R 52,31744
mvv 4,200,36
s_add_u32 s49, s49, 346112
mvv 8,204,36
s_waitcnt vmcnt(1) lgkmcnt(2)
s_barrier
mvv 0,208,40
R 24,32768
mvv 4,212,40
mvv 8,216,40
mvv 0,220,44
R 28,33792
mvv 4,224,44
mvv 8,228,44
s_waitcnt lgkmcnt(2)
mvv 0,232,48
R 32,34816
mvv 4,236,48
mvv 8,240,48
mvv 0,244,52
R 36,35840
mvv 4,248,52
mvv 8,252,52
s_waitcnt lgkmcnt(2)
mav 12,0,24
R 40,36864
mav 16,4,24
mav 20,8,24
v_cvt_pk_bf16_f32 v62, v0, v1
v_cvt_pk_bf16_f32 v63, v2, v3
mav 12,12,28
R 44,37888
v_cvt_pk_bf16_f32 v64, v4, v5
mav 16,16,28
v_cvt_pk_bf16_f32 v65, v6, v7
buffer_store_dwordx4 v[62:65], v83, s[36:39], s49 offen sc1
mav 20,20,28
v_cvt_pk_bf16_f32 v66, v8, v9
s_waitcnt lgkmcnt(2)
mav 12,24,32
R 48,38912
v_cvt_pk_bf16_f32 v67, v10, v11
mav 16,28,32
v_cvt_pk_bf16_f32 v68, v8, v9
v_cvt_pk_bf16_f32 v69, v10, v11
mav 20,32,32
v_permlane16_swap_b32 v66, v68
v_permlane16_swap_b32 v67, v69
mav 12,36,36
R 52,39936
buffer_store_dwordx4 v[66:69], v84, s[36:39], s49 offen sc1
mav 16,40,36
v_mul_f32 v73, v0, v0
v_add_f32 v72, v1, v0
mav 20,44,36
v_fmac_f32 v73, v1, v1
s_waitcnt lgkmcnt(2)
mav 12,48,40
R 24,40960
v_add_f32 v72, v2, v72
mav 16,52,40
v_fmac_f32 v73, v2, v2
v_add_f32 v72, v3, v72
mav 20,56,40
v_fmac_f32 v73, v3, v3
v_add_f32 v72, v4, v72
mav 12,60,44
R 28,41984
v_fmac_f32 v73, v4, v4
mav 16,64,44
v_add_f32 v72, v5, v72
v_fmac_f32 v73, v5, v5
mav 20,68,44
v_add_f32 v72, v6, v72
s_waitcnt lgkmcnt(2)
mav 12,72,48
R 32,43008
v_fmac_f32 v73, v6, v6
mav 16,76,48
v_add_f32 v72, v7, v72
v_fmac_f32 v73, v7, v7
mav 20,80,48
v_add_f32 v72, v8, v72
v_fmac_f32 v73, v8, v8
mav 12,84,52
R 36,44032
v_add_f32 v72, v9, v72
mav 16,88,52
v_fmac_f32 v73, v9, v9
v_add_f32 v72, v10, v72
mav 20,92,52
v_fmac_f32 v73, v10, v10
s_waitcnt lgkmcnt(2)
mav 12,96,24
R 40,45056
v_add_f32 v72, v11, v72
mav 16,100,24
v_fmac_f32 v73, v11, v11
mav 20,104,24
mav 12,108,28
R 44,46080
v_permlane32_swap_b32 v72, v73
mav 16,112,28
v_add_f32 v72, v72, v73
mav 20,116,28
s_waitcnt lgkmcnt(2)
mav 12,120,32
R 48,47104
mav 16,124,32
mav 20,128,32
mav 12,132,36
R 52,48128
mav 16,136,36
mav 20,140,36
s_waitcnt lgkmcnt(2)
mav 12,144,40
R 24,49152
mav 16,148,40
mav 20,152,40
mav 12,156,44
R 28,50176
mav 16,160,44
mav 20,164,44
s_waitcnt lgkmcnt(2)
mav 12,168,48
R 32,51200
mav 16,172,48
mav 20,176,48
mav 12,180,52
R 36,52224
mav 16,184,52
v_add_u32 v70, 1, v70
mav 20,188,52
s_waitcnt lgkmcnt(2)
mav 12,192,24
R 40,53248
mav 16,196,24
mav 20,200,24
mav 12,204,28
R 44,54272
mav 16,208,28
mav 20,212,28
s_waitcnt lgkmcnt(2)
mav 12,216,32
R 48,55296
mav 16,220,32
mav 20,224,32
mav 12,228,36
R 52,56320
mav 16,232,36
mav 20,236,36
s_waitcnt lgkmcnt(2)
mav 12,240,40
R 24,57344
mav 16,244,40
mav 20,248,40
mav 12,252,44
R 28,58368
mvv 16,128,44
mvv 20,132,44
s_waitcnt lgkmcnt(2)
mvv 12,136,48
R 32,59392
mvv 16,140,48
mvv 20,144,48
mvv 12,148,52
R 36,60416
mvv 16,152,52
s_waitcnt vmcnt(0)
buffer_store_dwordx2 v[70:71], v81, s[36:39], s52 offen sc1
mvv 20,156,52
s_waitcnt lgkmcnt(2)
mvv 12,160,24
R 40,61440
mvv 16,164,24
mvv 20,168,24
ds_write_b32 v91, v72 offset:0
mvv 12,172,28
R 44,62464
mvv 16,176,28
mvv 20,180,28
s_waitcnt lgkmcnt(3)
mvv 12,184,32
R 48,63488
mvv 16,188,32
mvv 20,192,32
mvv 12,196,36
R 52,64512
mvv 16,200,36
s_add_u32 s49, s49, 346112
mvv 20,204,36
s_waitcnt lgkmcnt(2)
s_barrier
mvv 12,208,40
mvv 16,212,40
mvv 20,216,40
mvv 12,220,44
mvv 16,224,44
mvv 20,228,44
s_waitcnt lgkmcnt(0)
mvv 12,232,48
mvv 16,236,48
mvv 20,240,48
mvv 12,244,52
mvv 16,248,52
mvv 20,252,52
s_nop 5
v_cvt_pk_bf16_f32 v62, v12, v13
v_cvt_pk_bf16_f32 v63, v14, v15
v_cvt_pk_bf16_f32 v64, v16, v17
v_cvt_pk_bf16_f32 v65, v18, v19
buffer_store_dwordx4 v[62:65], v83, s[36:39], s49 offen sc1
v_cvt_pk_bf16_f32 v66, v20, v21
v_cvt_pk_bf16_f32 v67, v22, v23
v_cvt_pk_bf16_f32 v68, v20, v21
v_cvt_pk_bf16_f32 v69, v22, v23
s_nop 0
v_permlane16_swap_b32 v66, v68
v_permlane16_swap_b32 v67, v69
buffer_store_dwordx4 v[66:69], v84, s[36:39], s49 offen sc1
v_mul_f32 v73, v12, v12
v_add_f32 v72, v13, v12
v_fmac_f32 v73, v13, v13
v_add_f32 v72, v14, v72
v_fmac_f32 v73, v14, v14
v_add_f32 v72, v15, v72
v_fmac_f32 v73, v15, v15
v_add_f32 v72, v16, v72
v_fmac_f32 v73, v16, v16
v_add_f32 v72, v17, v72
v_fmac_f32 v73, v17, v17
v_add_f32 v72, v18, v72
v_fmac_f32 v73, v18, v18
v_add_f32 v72, v19, v72
v_fmac_f32 v73, v19, v19
v_add_f32 v72, v20, v72
v_fmac_f32 v73, v20, v20
v_add_f32 v72, v21, v72
v_fmac_f32 v73, v21, v21
v_add_f32 v72, v22, v72
v_fmac_f32 v73, v22, v22
v_add_f32 v72, v23, v72
v_fmac_f32 v73, v23, v23
s_nop 1
v_permlane32_swap_b32 v72, v73
v_add_f32 v72, v72, v73
v_add_u32 v70, 37, v70
s_waitcnt vmcnt(0)
buffer_store_dwordx2 v[70:71], v81, s[36:39], s52 offen sc1
ds_write_b32 v91, v72 offset:1024
s_add_u32 s49, s49, 346112
s_waitcnt lgkmcnt(0)
s_barrier
s_endpgm
CO0_1:
s_cmp_eq_u32 s61, 0
s_cbranch_scc1 BO0_1
s_sub_u32 s61, s61, 1
s_sleep 8
buffer_load_dwordx2 v[74:75], v87, s[36:39], 0 offen sc1
s_waitcnt vmcnt(0)
ds_write_b32 v82, v74
v_sub_u32 v74, v74, v88
v_xor_b32 v75, v75, v71
v_cmp_lt_u64 vcc, s[62:63], v[74:75]
s_waitcnt lgkmcnt(0)
s_cbranch_vccnz CO0_1
s_branch BO0_1
CO0_2:
s_mov_b32 s58, m0
s_mov_b32 m0, 142336
DO0_2:
s_cmp_eq_u32 s61, 0
s_cbranch_scc1 EO0_2
s_sub_u32 s61, s61, 1
buffer_load_dword v87, s[36:39], 0 offen sc1 lds
s_sleep 1
ds_read_b32 v74, v82
s_waitcnt lgkmcnt(0)
v_cmp_gt_i32 vcc, v88, v74
s_cbranch_vccnz DO0_2
EO0_2:
s_mov_b32 m0, s58
s_branch BO0_2
CO0_3:
s_mov_b32 s58, m0
s_mov_b32 m0, 142336
DO0_3:
s_cmp_eq_u32 s61, 0
s_cbranch_scc1 EO0_3
s_sub_u32 s61, s61, 1
buffer_load_dword v87, s[36:39], 0 offen sc1 lds
s_sleep 1
ds_read_b32 v74, v82
s_waitcnt lgkmcnt(0)
v_cmp_gt_i32 vcc, v88, v74
s_cbranch_vccnz DO0_3
EO0_3:
s_mov_b32 m0, s58
s_branch BO0_3
CO0_4:
s_mov_b32 s58, m0
s_mov_b32 m0, 142336
DO0_4:
s_cmp_eq_u32 s61, 0
s_cbranch_scc1 EO0_4
s_sub_u32 s61, s61, 1
buffer_load_dword v87, s[36:39], 0 offen sc1 lds
s_sleep 1
ds_read_b32 v74, v82
s_waitcnt lgkmcnt(0)
v_cmp_gt_i32 vcc, v88, v74
s_cbranch_vccnz DO0_4
EO0_4:
s_mov_b32 m0, s58
s_branch BO0_4
CO0_6:
s_mov_b32 s58, m0
s_mov_b32 m0, 142336
DO0_6:
s_cmp_eq_u32 s61, 0
s_cbranch_scc1 EO0_6
s_sub_u32 s61, s61, 1
buffer_load_dword v87, s[36:39], 0 offen sc1 lds
s_sleep 1
ds_read_b32 v74, v82
s_waitcnt lgkmcnt(0)
v_cmp_gt_i32 vcc, v88, v74
s_cbranch_vccnz DO0_6
EO0_6:
s_mov_b32 m0, s58
s_branch BO0_6
CO0_7:
s_mov_b32 s58, m0
s_mov_b32 m0, 142336
DO0_7:
s_cmp_eq_u32 s61, 0
s_cbranch_scc1 EO0_7
s_sub_u32 s61, s61, 1
buffer_load_dword v87, s[36:39], 0 offen sc1 lds
s_sleep 1
ds_read_b32 v74, v82
s_waitcnt lgkmcnt(0)
v_cmp_gt_i32 vcc, v88, v74
s_cbranch_vccnz DO0_7
EO0_7:
s_mov_b32 m0, s58
s_branch BO0_7
LvO1:
LsO4:
s_cbranch_execz LsO8
s_barrier
buffer_load_dwordx4 v[56:59], v89, s[36:39], s49 offen sc1
buffer_load_dwordx2 v[60:61], v90, s[36:39], s49 offen sc1
s_add_u32 s49, s49, 346112
s_add_u32 m0, s50, 0
s_nop 0
D 85,0
D 85,1024
D 85,2048
D 85,3072
s_add_u32 m0, s50, 4096
s_nop 0
D 86,0
D 86,1024
D 86,2048
D 86,3072
s_waitcnt vmcnt(9)
v_lshlrev_b32 v0, 16, v56
v_and_b32 v1, 0xffff0000, v56
v_lshlrev_b32 v2, 16, v57
v_and_b32 v3, 0xffff0000, v57
v_lshlrev_b32 v4, 16, v58
v_and_b32 v5, 0xffff0000, v58
v_lshlrev_b32 v6, 16, v59
v_and_b32 v7, 0xffff0000, v59
s_waitcnt vmcnt(8)
v_lshlrev_b32 v8, 16, v60
v_and_b32 v9, 0xffff0000, v60
v_lshlrev_b32 v10, 16, v61
v_and_b32 v11, 0xffff0000, v61
buffer_load_dwordx4 v[56:59], v89, s[36:39], s49 offen sc1
buffer_load_dwordx2 v[60:61], v90, s[36:39], s49 offen sc1
s_add_u32 s49, s49, 346112
s_waitcnt vmcnt(2)
s_barrier
s_add_u32 m0, s50, 32768
R 24,0
D 85,0
R 28,1024
D 85,1024
R 32,2048
R 36,3072
D 85,2048
s_waitcnt lgkmcnt(2)
mav 0,0,24
R 40,4096
mav 4,4,24
mav 8,8,24
D 85,3072
mav 0,12,28
R 44,5120
mav 4,16,28
mav 8,20,28
s_add_u32 m0, s50, 36864
s_waitcnt lgkmcnt(2)
mav 0,24,32
R 48,6144
mav 4,28,32
D 86,0
mav 8,32,32
mav 0,36,36
R 52,7168
mav 4,40,36
mav 8,44,36
D 86,1024
s_waitcnt lgkmcnt(2)
mav 0,48,40
R 24,8192
mav 4,52,40
mav 8,56,40
mav 0,60,44
R 28,9216
mav 4,64,44
D 86,2048
mav 8,68,44
s_waitcnt lgkmcnt(2)
mav 0,72,48
R 32,10240
mav 4,76,48
mav 8,80,48
D 86,3072
mav 0,84,52
R 36,11264
mav 4,88,52
mav 8,92,52
s_waitcnt lgkmcnt(2)
LsO5:
s_cbranch_execz LsO9
mav 0,96,24
R 40,12288
mav 4,100,24
mav 8,104,24
mav 0,108,28
R 44,13312
mav 4,112,28
mav 8,116,28
s_waitcnt lgkmcnt(2)
mav 0,120,32
R 48,14336
mav 4,124,32
mav 8,128,32
mav 0,132,36
R 52,15360
mav 4,136,36
mav 8,140,36
s_waitcnt lgkmcnt(2)
mav 0,144,40
R 24,16384
mav 4,148,40
mav 8,152,40
mav 0,156,44
R 28,17408
mav 4,160,44
mav 8,164,44
s_waitcnt lgkmcnt(2)
mav 0,168,48
R 32,18432
mav 4,172,48
mav 8,176,48
mav 0,180,52
R 36,19456
mav 4,184,52
mav 8,188,52
s_waitcnt lgkmcnt(2)
mav 0,192,24
R 40,20480
mav 4,196,24
mav 8,200,24
s_waitcnt vmcnt(9)
v_lshlrev_b32 v12, 16, v56
mav 0,204,28
R 44,21504
v_and_b32 v13, 0xffff0000, v56
mav 4,208,28
v_lshlrev_b32 v14, 16, v57
v_and_b32 v15, 0xffff0000, v57
mav 8,212,28
v_lshlrev_b32 v16, 16, v58
s_waitcnt lgkmcnt(2)
mav 0,216,32
R 48,22528
v_and_b32 v17, 0xffff0000, v58
mav 4,220,32
v_lshlrev_b32 v18, 16, v59
v_and_b32 v19, 0xffff0000, v59
mav 8,224,32
s_waitcnt vmcnt(8)
v_lshlrev_b32 v20, 16, v60
mav 0,228,36
R 52,23552
v_and_b32 v21, 0xffff0000, v60
mav 4,232,36
v_lshlrev_b32 v22, 16, v61
v_and_b32 v23, 0xffff0000, v61
mav 8,236,36
s_waitcnt lgkmcnt(2)
mav 0,240,40
R 24,24576
mav 4,244,40
mav 8,248,40
mav 0,252,44
R 28,25600
mvv 4,128,44
buffer_load_dwordx4 v[56:59], v89, s[36:39], s49 offen sc1
mvv 8,132,44
s_waitcnt lgkmcnt(2)
mvv 0,136,48
R 32,26624
mvv 4,140,48
mvv 8,144,48
buffer_load_dwordx2 v[60:61], v90, s[36:39], s49 offen sc1
mvv 0,148,52
R 36,27648
mvv 4,152,52
mvv 8,156,52
s_waitcnt lgkmcnt(2)
mvv 0,160,24
R 40,28672
mvv 4,164,24
mvv 8,168,24
mvv 0,172,28
R 44,29696
mvv 4,176,28
mvv 8,180,28
s_waitcnt lgkmcnt(2)
mvv 0,184,32
R 48,30720
s_xor_b32 s50, s50, 0x10000
mvv 4,188,32
mvv 8,192,32
mvv 0,196,36
R 52,31744
mvv 4,200,36
s_add_u32 s49, s49, 346112
mvv 8,204,36
s_waitcnt vmcnt(2)
s_barrier
s_waitcnt lgkmcnt(2)
mvv 0,208,40
R 24,32768
s_add_u32 m0, s50, 0
mvv 4,212,40
D 85,0
mvv 8,216,40
mvv 0,220,44
R 28,33792
mvv 4,224,44
mvv 8,228,44
D 85,1024
s_waitcnt lgkmcnt(2)
mvv 0,232,48
R 32,34816
mvv 4,236,48
mvv 8,240,48
mvv 0,244,52
R 36,35840
mvv 4,248,52
D 85,2048
mvv 8,252,52
s_waitcnt lgkmcnt(2)
mav 12,0,24
R 40,36864
mav 16,4,24
v_cvt_pk_bf16_f32 v62, v0, v1
v_cvt_pk_bf16_f32 v63, v2, v3
mav 20,8,24
D 85,3072
v_cvt_pk_bf16_f32 v64, v4, v5
mav 12,12,28
R 44,37888
v_cvt_pk_bf16_f32 v65, v6, v7
mav 16,16,28
v_cvt_pk_bf16_f32 v66, v8, v9
v_cvt_pk_bf16_f32 v67, v10, v11
mav 20,20,28
s_add_u32 m0, s50, 4096
s_waitcnt lgkmcnt(2)
mav 12,24,32
R 48,38912
buffer_store_dwordx4 v[62:65], v83, s[36:39], s49 offen sc1
mav 16,28,32
v_cvt_pk_bf16_f32 v68, v8, v9
v_cvt_pk_bf16_f32 v69, v10, v11
mav 20,32,32
v_permlane16_swap_b32 v66, v68
v_permlane16_swap_b32 v67, v69
mav 12,36,36
R 52,39936
v_mul_f32 v73, v0, v0
mav 16,40,36
buffer_store_dwordx4 v[66:69], v84, s[36:39], s49 offen sc1
v_add_f32 v72, v1, v0
mav 20,44,36
v_fmac_f32 v73, v1, v1
s_waitcnt lgkmcnt(2)
mav 12,48,40
R 24,40960
v_add_f32 v72, v2, v72
mav 16,52,40
v_fmac_f32 v73, v2, v2
v_add_f32 v72, v3, v72
mav 20,56,40
D 86,0
v_fmac_f32 v73, v3, v3
mav 12,60,44
R 28,41984
v_add_f32 v72, v4, v72
mav 16,64,44
v_fmac_f32 v73, v4, v4
v_add_f32 v72, v5, v72
mav 20,68,44
v_fmac_f32 v73, v5, v5
s_waitcnt lgkmcnt(2)
mav 12,72,48
R 32,43008
D 86,1024
mav 16,76,48
v_add_f32 v72, v6, v72
v_fmac_f32 v73, v6, v6
mav 20,80,48
v_add_f32 v72, v7, v72
v_fmac_f32 v73, v7, v7
mav 12,84,52
R 36,44032
v_add_f32 v72, v8, v72
mav 16,88,52
D 86,2048
v_fmac_f32 v73, v8, v8
mav 20,92,52
v_add_f32 v72, v9, v72
s_waitcnt lgkmcnt(2)
mav 12,96,24
R 40,45056
v_fmac_f32 v73, v9, v9
mav 16,100,24
v_add_f32 v72, v10, v72
v_fmac_f32 v73, v10, v10
mav 20,104,24
D 86,3072
v_add_f32 v72, v11, v72
mav 12,108,28
R 44,46080
v_fmac_f32 v73, v11, v11
mav 16,112,28
mav 20,116,28
v_permlane32_swap_b32 v72, v73
s_waitcnt lgkmcnt(2)
mav 12,120,32
R 48,47104
v_add_f32 v72, v72, v73
mav 16,124,32
mav 20,128,32
mav 12,132,36
R 52,48128
mav 16,136,36
mav 20,140,36
s_waitcnt lgkmcnt(2)
mav 12,144,40
R 24,49152
mav 16,148,40
mav 20,152,40
mav 12,156,44
R 28,50176
mav 16,160,44
mav 20,164,44
s_waitcnt lgkmcnt(2)
mav 12,168,48
R 32,51200
mav 16,172,48
mav 20,176,48
mav 12,180,52
R 36,52224
mav 16,184,52
v_add_u32 v70, 1, v70
mav 20,188,52
s_waitcnt lgkmcnt(2)
mav 12,192,24
R 40,53248
mav 16,196,24
mav 20,200,24
s_waitcnt vmcnt(11)
v_lshlrev_b32 v0, 16, v56
mav 12,204,28
R 44,54272
v_and_b32 v1, 0xffff0000, v56
mav 16,208,28
v_lshlrev_b32 v2, 16, v57
v_and_b32 v3, 0xffff0000, v57
mav 20,212,28
v_lshlrev_b32 v4, 16, v58
s_waitcnt lgkmcnt(2)
mav 12,216,32
R 48,55296
v_and_b32 v5, 0xffff0000, v58
mav 16,220,32
v_lshlrev_b32 v6, 16, v59
v_and_b32 v7, 0xffff0000, v59
mav 20,224,32
s_waitcnt vmcnt(10)
v_lshlrev_b32 v8, 16, v60
mav 12,228,36
R 52,56320
v_and_b32 v9, 0xffff0000, v60
mav 16,232,36
v_lshlrev_b32 v10, 16, v61
v_and_b32 v11, 0xffff0000, v61
mav 20,236,36
s_waitcnt lgkmcnt(2)
mav 12,240,40
R 24,57344
mav 16,244,40
mav 20,248,40
mav 12,252,44
R 28,58368
mvv 16,128,44
buffer_load_dwordx4 v[56:59], v89, s[36:39], s49 offen sc1
mvv 20,132,44
s_waitcnt lgkmcnt(2)
mvv 12,136,48
R 32,59392
mvv 16,140,48
mvv 20,144,48
buffer_load_dwordx2 v[60:61], v90, s[36:39], s49 offen sc1
mvv 12,148,52
R 36,60416
mvv 16,152,52
s_waitcnt vmcnt(6)
buffer_store_dwordx2 v[70:71], v81, s[36:39], s52 offen sc1
mvv 20,156,52
s_waitcnt lgkmcnt(2)
mvv 12,160,24
R 40,61440
mvv 16,164,24
mvv 20,168,24
ds_write_b32 v91, v72 offset:0
mvv 12,172,28
R 44,62464
mvv 16,176,28
mvv 20,180,28
s_waitcnt lgkmcnt(3)
mvv 12,184,32
R 48,63488
mvv 16,188,32
mvv 20,192,32
mvv 12,196,36
R 52,64512
v_xor_b32 v80, 0x10000, v80
mvv 16,200,36
s_add_u32 s49, s49, 346112
mvv 20,204,36
s_waitcnt vmcnt(3) lgkmcnt(2)
s_barrier
mvv 12,208,40
R 24,0
s_add_u32 m0, s50, 32768
mvv 16,212,40
ds_read_b128 v[76:79], v92 offset:0
D 85,0
mvv 20,216,40
mvv 12,220,44
R 28,1024
mvv 16,224,44
mvv 20,228,44
D 85,1024
s_waitcnt lgkmcnt(3)
mvv 12,232,48
R 32,2048
mvv 16,236,48
mvv 20,240,48
s_waitcnt lgkmcnt(2)
v_add_f32 v76, v76, v77
mvv 12,244,52
R 36,3072
v_add_f32 v78, v78, v79
mvv 16,248,52
D 85,2048
v_add_f32 v76, v76, v78
mvv 20,252,52
s_mov_b32 s60, -30
.p2align 6
LO1_1:
mav 0,0,24
R 40,4096
v_add_f32_dpp v77, v76, v76 quad_perm:[1,0,3,2] row_mask:0xf bank_mask:0xf
mav 4,4,24
buffer_store_dword v77, v93, s[36:39], s49 offen sc1
v_cvt_pk_bf16_f32 v62, v12, v13
mav 8,8,24
D 85,3072
s_waitcnt lgkmcnt(2)
mav 0,12,28
R 44,5120
v_cvt_pk_bf16_f32 v63, v14, v15
mav 4,16,28
v_cvt_pk_bf16_f32 v64, v16, v17
v_cvt_pk_bf16_f32 v65, v18, v19
mav 8,20,28
s_add_u32 m0, s50, 36864
v_cvt_pk_bf16_f32 v66, v20, v21
mav 0,24,32
R 48,6144
buffer_store_dwordx4 v[62:65], v83, s[36:39], s49 offen sc1
mav 4,28,32
v_cvt_pk_bf16_f32 v67, v22, v23
v_cvt_pk_bf16_f32 v68, v20, v21
mav 8,32,32
v_cvt_pk_bf16_f32 v69, v22, v23
s_waitcnt lgkmcnt(2)
mav 0,36,36
R 52,7168
v_permlane16_swap_b32 v66, v68
mav 4,40,36
D 86,0
v_permlane16_swap_b32 v67, v69
mav 8,44,36
v_mul_f32 v73, v12, v12
v_add_f32 v72, v13, v12
mav 0,48,40
R 24,8192
v_fmac_f32 v73, v13, v13
mav 4,52,40
v_add_f32 v72, v14, v72
v_fmac_f32 v73, v14, v14
mav 8,56,40
buffer_store_dwordx4 v[66:69], v84, s[36:39], s49 offen sc1
s_waitcnt lgkmcnt(2)
mav 0,60,44
R 28,9216
v_add_f32 v72, v15, v72
mav 4,64,44
v_fmac_f32 v73, v15, v15
v_add_f32 v72, v16, v72
mav 8,68,44
v_fmac_f32 v73, v16, v16
v_add_f32 v72, v17, v72
mav 0,72,48
R 32,10240
D 86,1024
mav 4,76,48
v_fmac_f32 v73, v17, v17
v_add_f32 v72, v18, v72
mav 8,80,48
v_fmac_f32 v73, v18, v18
s_waitcnt lgkmcnt(2)
mav 0,84,52
R 36,11264
v_add_f32 v72, v19, v72
mav 4,88,52
D 86,2048
v_fmac_f32 v73, v19, v19
mav 8,92,52
v_add_f32 v72, v20, v72
v_fmac_f32 v73, v20, v20
mav 0,96,24
R 40,12288
v_add_f32 v72, v21, v72
mav 4,100,24
v_fmac_f32 v73, v21, v21
v_add_f32 v72, v22, v72
mav 8,104,24
D 86,3072
s_waitcnt lgkmcnt(2)
mav 0,108,28
R 44,13312
v_fmac_f32 v73, v22, v22
mav 4,112,28
v_add_f32 v72, v23, v72
v_fmac_f32 v73, v23, v23
mav 8,116,28
mav 0,120,32
R 48,14336
v_permlane32_swap_b32 v72, v73
mav 4,124,32
v_add_f32 v72, v72, v73
mav 8,128,32
s_waitcnt lgkmcnt(2)
mav 0,132,36
R 52,15360
mav 4,136,36
mav 8,140,36
mav 0,144,40
R 24,16384
mav 4,148,40
mav 8,152,40
s_waitcnt lgkmcnt(2)
mav 0,156,44
R 28,17408
mav 4,160,44
mav 8,164,44
mav 0,168,48
R 32,18432
mav 4,172,48
mav 8,176,48
s_waitcnt lgkmcnt(2)
mav 0,180,52
R 36,19456
mav 4,184,52
v_add_u32 v70, 1, v70
mav 8,188,52
mav 0,192,24
R 40,20480
mav 4,196,24
mav 8,200,24
s_waitcnt lgkmcnt(2)
mav 0,204,28
R 44,21504
mav 4,208,28
s_waitcnt vmcnt(13)
v_lshlrev_b32 v12, 16, v56
mav 8,212,28
v_and_b32 v13, 0xffff0000, v56
v_lshlrev_b32 v14, 16, v57
mav 0,216,32
R 48,22528
v_and_b32 v15, 0xffff0000, v57
mav 4,220,32
v_lshlrev_b32 v16, 16, v58
v_and_b32 v17, 0xffff0000, v58
mav 8,224,32
v_lshlrev_b32 v18, 16, v59
s_waitcnt lgkmcnt(2)
mav 0,228,36
R 52,23552
v_and_b32 v19, 0xffff0000, v59
mav 4,232,36
s_waitcnt vmcnt(12)
v_lshlrev_b32 v20, 16, v60
mav 8,236,36
v_and_b32 v21, 0xffff0000, v60
v_lshlrev_b32 v22, 16, v61
mav 0,240,40
R 24,24576
v_and_b32 v23, 0xffff0000, v61
mav 4,244,40
mav 8,248,40
s_waitcnt lgkmcnt(2)
mav 0,252,44
R 28,25600
mvv 4,128,44
buffer_load_dwordx4 v[56:59], v89, s[36:39], s49 offen sc1
mvv 8,132,44
mvv 0,136,48
R 32,26624
mvv 4,140,48
mvv 8,144,48
buffer_load_dwordx2 v[60:61], v90, s[36:39], s49 offen sc1
s_waitcnt lgkmcnt(2)
mvv 0,148,52
R 36,27648
mvv 4,152,52
s_waitcnt vmcnt(5)
buffer_store_dwordx2 v[70:71], v81, s[36:39], s52 offen sc1
mvv 8,156,52
mvv 0,160,24
R 40,28672
mvv 4,164,24
mvv 8,168,24
s_waitcnt lgkmcnt(2)
mvv 0,172,28
R 44,29696
mvv 4,176,28
ds_write_b32 v91, v72 offset:1024
mvv 8,180,28
mvv 0,184,32
R 48,30720
s_xor_b32 s50, s50, 0x10000
mvv 4,188,32
mvv 8,192,32
s_waitcnt lgkmcnt(3)
mvv 0,196,36
R 52,31744
mvv 4,200,36
s_add_u32 s49, s49, 346112
mvv 8,204,36
s_waitcnt vmcnt(3) lgkmcnt(2)
s_barrier
mvv 0,208,40
R 24,32768
s_add_u32 m0, s50, 0
mvv 4,212,40
ds_read_b128 v[76:79], v92 offset:1024
D 85,0
mvv 8,216,40
mvv 0,220,44
R 28,33792
mvv 4,224,44
mvv 8,228,44
D 85,1024
s_waitcnt lgkmcnt(3)
mvv 0,232,48
R 32,34816
mvv 4,236,48
mvv 8,240,48
s_waitcnt lgkmcnt(2)
v_add_f32 v76, v76, v77
mvv 0,244,52
R 36,35840
v_add_f32 v78, v78, v79
mvv 4,248,52
D 85,2048
v_add_f32 v76, v76, v78
mvv 8,252,52
mav 12,0,24
R 40,36864
v_add_f32_dpp v77, v76, v76 quad_perm:[1,0,3,2] row_mask:0xf bank_mask:0xf
mav 16,4,24
buffer_store_dword v77, v93, s[36:39], s49 offen sc1
v_cvt_pk_bf16_f32 v62, v0, v1
mav 20,8,24
D 85,3072
s_waitcnt lgkmcnt(2)
LsO6:
s_cbranch_execz LsO10
mav 12,12,28
R 44,37888
v_cvt_pk_bf16_f32 v63, v2, v3
mav 16,16,28
v_cvt_pk_bf16_f32 v64, v4, v5
v_cvt_pk_bf16_f32 v65, v6, v7
mav 20,20,28
s_add_u32 m0, s50, 4096
v_cvt_pk_bf16_f32 v66, v8, v9
mav 12,24,32
R 48,38912
buffer_store_dwordx4 v[62:65], v83, s[36:39], s49 offen sc1
mav 16,28,32
v_cvt_pk_bf16_f32 v67, v10, v11
v_cvt_pk_bf16_f32 v68, v8, v9
mav 20,32,32
v_cvt_pk_bf16_f32 v69, v10, v11
s_waitcnt lgkmcnt(2)
mav 12,36,36
R 52,39936
v_permlane16_swap_b32 v66, v68
mav 16,40,36
D 86,0
v_permlane16_swap_b32 v67, v69
mav 20,44,36
v_mul_f32 v73, v0, v0
v_add_f32 v72, v1, v0
mav 12,48,40
R 24,40960
v_fmac_f32 v73, v1, v1
mav 16,52,40
v_add_f32 v72, v2, v72
v_fmac_f32 v73, v2, v2
mav 20,56,40
buffer_store_dwordx4 v[66:69], v84, s[36:39], s49 offen sc1
s_waitcnt lgkmcnt(2)
mav 12,60,44
R 28,41984
v_add_f32 v72, v3, v72
mav 16,64,44
v_fmac_f32 v73, v3, v3
v_add_f32 v72, v4, v72
mav 20,68,44
v_fmac_f32 v73, v4, v4
v_add_f32 v72, v5, v72
mav 12,72,48
R 32,43008
D 86,1024
mav 16,76,48
v_fmac_f32 v73, v5, v5
v_add_f32 v72, v6, v72
mav 20,80,48
v_fmac_f32 v73, v6, v6
s_waitcnt lgkmcnt(2)
mav 12,84,52
R 36,44032
v_add_f32 v72, v7, v72
mav 16,88,52
D 86,2048
v_fmac_f32 v73, v7, v7
mav 20,92,52
v_add_f32 v72, v8, v72
v_fmac_f32 v73, v8, v8
mav 12,96,24
R 40,45056
v_add_f32 v72, v9, v72
mav 16,100,24
v_fmac_f32 v73, v9, v9
v_add_f32 v72, v10, v72
mav 20,104,24
D 86,3072
s_waitcnt lgkmcnt(2)
mav 12,108,28
R 44,46080
v_fmac_f32 v73, v10, v10
mav 16,112,28
v_add_f32 v72, v11, v72
v_fmac_f32 v73, v11, v11
mav 20,116,28
mav 12,120,32
R 48,47104
v_permlane32_swap_b32 v72, v73
mav 16,124,32
v_add_f32 v72, v72, v73
mav 20,128,32
s_waitcnt lgkmcnt(2)
mav 12,132,36
R 52,48128
mav 16,136,36
mav 20,140,36
mav 12,144,40
R 24,49152
mav 16,148,40
mav 20,152,40
s_waitcnt lgkmcnt(2)
mav 12,156,44
R 28,50176
mav 16,160,44
mav 20,164,44
mav 12,168,48
R 32,51200
mav 16,172,48
mav 20,176,48
s_waitcnt lgkmcnt(2)
mav 12,180,52
R 36,52224
mav 16,184,52
v_add_u32 v70, 1, v70
mav 20,188,52
mav 12,192,24
R 40,53248
mav 16,196,24
mav 20,200,24
s_waitcnt lgkmcnt(2)
mav 12,204,28
R 44,54272
mav 16,208,28
s_waitcnt vmcnt(13)
v_lshlrev_b32 v0, 16, v56
mav 20,212,28
v_and_b32 v1, 0xffff0000, v56
v_lshlrev_b32 v2, 16, v57
mav 12,216,32
R 48,55296
v_and_b32 v3, 0xffff0000, v57
mav 16,220,32
v_lshlrev_b32 v4, 16, v58
v_and_b32 v5, 0xffff0000, v58
mav 20,224,32
v_lshlrev_b32 v6, 16, v59
s_waitcnt lgkmcnt(2)
mav 12,228,36
R 52,56320
v_and_b32 v7, 0xffff0000, v59
mav 16,232,36
s_waitcnt vmcnt(12)
v_lshlrev_b32 v8, 16, v60
mav 20,236,36
v_and_b32 v9, 0xffff0000, v60
v_lshlrev_b32 v10, 16, v61
mav 12,240,40
R 24,57344
v_and_b32 v11, 0xffff0000, v61
mav 16,244,40
mav 20,248,40
s_waitcnt lgkmcnt(2)
mav 12,252,44
R 28,58368
mvv 16,128,44
buffer_load_dwordx4 v[56:59], v89, s[36:39], s49 offen sc1
mvv 20,132,44
mvv 12,136,48
R 32,59392
mvv 16,140,48
mvv 20,144,48
buffer_load_dwordx2 v[60:61], v90, s[36:39], s49 offen sc1
s_waitcnt lgkmcnt(2)
mvv 12,148,52
R 36,60416
mvv 16,152,52
s_waitcnt vmcnt(5)
buffer_store_dwordx2 v[70:71], v81, s[36:39], s52 offen sc1
mvv 20,156,52
mvv 12,160,24
R 40,61440
mvv 16,164,24
mvv 20,168,24
s_waitcnt lgkmcnt(2)
mvv 12,172,28
R 44,62464
mvv 16,176,28
ds_write_b32 v91, v72 offset:0
mvv 20,180,28
mvv 12,184,32
R 48,63488
mvv 16,188,32
mvv 20,192,32
s_waitcnt lgkmcnt(3)
mvv 12,196,36
R 52,64512
v_xor_b32 v80, 0x10000, v80
mvv 16,200,36
s_add_u32 s49, s49, 346112
mvv 20,204,36
s_waitcnt vmcnt(3) lgkmcnt(2)
s_barrier
mvv 12,208,40
R 24,0
s_add_u32 m0, s50, 32768
mvv 16,212,40
ds_read_b128 v[76:79], v92 offset:0
D 85,0
mvv 20,216,40
mvv 12,220,44
R 28,1024
mvv 16,224,44
mvv 20,228,44
D 85,1024
s_waitcnt lgkmcnt(3)
mvv 12,232,48
R 32,2048
mvv 16,236,48
mvv 20,240,48
s_waitcnt lgkmcnt(2)
v_add_f32 v76, v76, v77
mvv 12,244,52
R 36,3072
v_add_f32 v78, v78, v79
mvv 16,248,52
D 85,2048
v_add_f32 v76, v76, v78
mvv 20,252,52
s_add_u32 s60, s60, 1
s_cbranch_scc0 LO1_1
mav 0,0,24
R 40,4096
v_add_f32_dpp v77, v76, v76 quad_perm:[1,0,3,2] row_mask:0xf bank_mask:0xf
mav 4,4,24
buffer_store_dword v77, v93, s[36:39], s49 offen sc1
v_cvt_pk_bf16_f32 v62, v12, v13
mav 8,8,24
D 85,3072
s_waitcnt lgkmcnt(2)
mav 0,12,28
R 44,5120
v_cvt_pk_bf16_f32 v63, v14, v15
mav 4,16,28
v_cvt_pk_bf16_f32 v64, v16, v17
v_cvt_pk_bf16_f32 v65, v18, v19
mav 8,20,28
s_add_u32 m0, s50, 36864
v_cvt_pk_bf16_f32 v66, v20, v21
mav 0,24,32
R 48,6144
buffer_store_dwordx4 v[62:65], v83, s[36:39], s49 offen sc1
mav 4,28,32
v_cvt_pk_bf16_f32 v67, v22, v23
v_cvt_pk_bf16_f32 v68, v20, v21
mav 8,32,32
v_cvt_pk_bf16_f32 v69, v22, v23
s_waitcnt lgkmcnt(2)
mav 0,36,36
R 52,7168
v_permlane16_swap_b32 v66, v68
mav 4,40,36
D 86,0
v_permlane16_swap_b32 v67, v69
mav 8,44,36
v_mul_f32 v73, v12, v12
v_add_f32 v72, v13, v12
mav 0,48,40
R 24,8192
v_fmac_f32 v73, v13, v13
mav 4,52,40
v_add_f32 v72, v14, v72
v_fmac_f32 v73, v14, v14
mav 8,56,40
buffer_store_dwordx4 v[66:69], v84, s[36:39], s49 offen sc1
s_waitcnt lgkmcnt(2)
mav 0,60,44
R 28,9216
v_add_f32 v72, v15, v72
mav 4,64,44
v_fmac_f32 v73, v15, v15
v_add_f32 v72, v16, v72
mav 8,68,44
v_fmac_f32 v73, v16, v16
v_add_f32 v72, v17, v72
mav 0,72,48
R 32,10240
D 86,1024
mav 4,76,48
v_fmac_f32 v73, v17, v17
v_add_f32 v72, v18, v72
mav 8,80,48
v_fmac_f32 v73, v18, v18
s_waitcnt lgkmcnt(2)
mav 0,84,52
R 36,11264
v_add_f32 v72, v19, v72
mav 4,88,52
D 86,2048
v_fmac_f32 v73, v19, v19
mav 8,92,52
v_add_f32 v72, v20, v72
v_fmac_f32 v73, v20, v20
mav 0,96,24
R 40,12288
v_add_f32 v72, v21, v72
mav 4,100,24
v_fmac_f32 v73, v21, v21
v_add_f32 v72, v22, v72
mav 8,104,24
D 86,3072
s_waitcnt lgkmcnt(2)
mav 0,108,28
R 44,13312
v_fmac_f32 v73, v22, v22
mav 4,112,28
v_add_f32 v72, v23, v72
v_fmac_f32 v73, v23, v23
mav 8,116,28
mav 0,120,32
R 48,14336
v_permlane32_swap_b32 v72, v73
mav 4,124,32
v_add_f32 v72, v72, v73
mav 8,128,32
s_waitcnt lgkmcnt(2)
mav 0,132,36
R 52,15360
mav 4,136,36
mav 8,140,36
mav 0,144,40
R 24,16384
mav 4,148,40
mav 8,152,40
s_waitcnt lgkmcnt(2)
mav 0,156,44
R 28,17408
mav 4,160,44
mav 8,164,44
mav 0,168,48
R 32,18432
mav 4,172,48
mav 8,176,48
s_waitcnt lgkmcnt(2)
mav 0,180,52
R 36,19456
mav 4,184,52
v_add_u32 v70, 1, v70
mav 8,188,52
mav 0,192,24
R 40,20480
mav 4,196,24
mav 8,200,24
s_waitcnt lgkmcnt(2)
mav 0,204,28
R 44,21504
mav 4,208,28
s_waitcnt vmcnt(13)
v_lshlrev_b32 v12, 16, v56
mav 8,212,28
v_and_b32 v13, 0xffff0000, v56
v_lshlrev_b32 v14, 16, v57
mav 0,216,32
R 48,22528
v_and_b32 v15, 0xffff0000, v57
mav 4,220,32
v_lshlrev_b32 v16, 16, v58
v_and_b32 v17, 0xffff0000, v58
mav 8,224,32
v_lshlrev_b32 v18, 16, v59
s_waitcnt lgkmcnt(2)
mav 0,228,36
R 52,23552
v_and_b32 v19, 0xffff0000, v59
mav 4,232,36
s_waitcnt vmcnt(12)
v_lshlrev_b32 v20, 16, v60
mav 8,236,36
v_and_b32 v21, 0xffff0000, v60
v_lshlrev_b32 v22, 16, v61
mav 0,240,40
R 24,24576
v_and_b32 v23, 0xffff0000, v61
mav 4,244,40
mav 8,248,40
s_waitcnt lgkmcnt(2)
mav 0,252,44
R 28,25600
mvv 4,128,44
mvv 8,132,44
mvv 0,136,48
R 32,26624
mvv 4,140,48
mvv 8,144,48
s_waitcnt lgkmcnt(2)
mvv 0,148,52
R 36,27648
mvv 4,152,52
s_waitcnt vmcnt(3)
buffer_store_dwordx2 v[70:71], v81, s[36:39], s52 offen sc1
mvv 8,156,52
mvv 0,160,24
R 40,28672
mvv 4,164,24
mvv 8,168,24
s_waitcnt lgkmcnt(2)
mvv 0,172,28
R 44,29696
mvv 4,176,28
ds_write_b32 v91, v72 offset:1024
mvv 8,180,28
mvv 0,184,32
R 48,30720
mvv 4,188,32
mvv 8,192,32
s_waitcnt lgkmcnt(3)
mvv 0,196,36
R 52,31744
mvv 4,200,36
s_add_u32 s49, s49, 346112
mvv 8,204,36
s_waitcnt vmcnt(1) lgkmcnt(2)
s_barrier
mvv 0,208,40
R 24,32768
ds_read_b128 v[76:79], v92 offset:1024
mvv 4,212,40
mvv 8,216,40
mvv 0,220,44
R 28,33792
mvv 4,224,44
mvv 8,228,44
s_waitcnt lgkmcnt(3)
mvv 0,232,48
R 32,34816
mvv 4,236,48
s_waitcnt lgkmcnt(2)
v_add_f32 v76, v76, v77
mvv 8,240,48
v_add_f32 v78, v78, v79
v_add_f32 v76, v76, v78
mvv 0,244,52
R 36,35840
v_add_f32_dpp v77, v76, v76 quad_perm:[1,0,3,2] row_mask:0xf bank_mask:0xf
mvv 4,248,52
buffer_store_dword v77, v93, s[36:39], s49 offen sc1
mvv 8,252,52
mav 12,0,24
R 40,36864
mav 16,4,24
v_cvt_pk_bf16_f32 v62, v0, v1
v_cvt_pk_bf16_f32 v63, v2, v3
mav 20,8,24
v_cvt_pk_bf16_f32 v64, v4, v5
s_waitcnt lgkmcnt(2)
mav 12,12,28
R 44,37888
v_cvt_pk_bf16_f32 v65, v6, v7
mav 16,16,28
buffer_store_dwordx4 v[62:65], v83, s[36:39], s49 offen sc1
v_cvt_pk_bf16_f32 v66, v8, v9
mav 20,20,28
v_cvt_pk_bf16_f32 v67, v10, v11
v_cvt_pk_bf16_f32 v68, v8, v9
mav 12,24,32
R 48,38912
v_cvt_pk_bf16_f32 v69, v10, v11
mav 16,28,32
v_permlane16_swap_b32 v66, v68
v_permlane16_swap_b32 v67, v69
mav 20,32,32
buffer_store_dwordx4 v[66:69], v84, s[36:39], s49 offen sc1
s_waitcnt lgkmcnt(2)
mav 12,36,36
R 52,39936
v_mul_f32 v73, v0, v0
mav 16,40,36
v_add_f32 v72, v1, v0
v_fmac_f32 v73, v1, v1
mav 20,44,36
v_add_f32 v72, v2, v72
v_fmac_f32 v73, v2, v2
mav 12,48,40
R 24,40960
v_add_f32 v72, v3, v72
mav 16,52,40
v_fmac_f32 v73, v3, v3
v_add_f32 v72, v4, v72
mav 20,56,40
v_fmac_f32 v73, v4, v4
s_waitcnt lgkmcnt(2)
mav 12,60,44
R 28,41984
v_add_f32 v72, v5, v72
mav 16,64,44
v_fmac_f32 v73, v5, v5
v_add_f32 v72, v6, v72
mav 20,68,44
v_fmac_f32 v73, v6, v6
v_add_f32 v72, v7, v72
mav 12,72,48
R 32,43008
v_fmac_f32 v73, v7, v7
mav 16,76,48
v_add_f32 v72, v8, v72
v_fmac_f32 v73, v8, v8
mav 20,80,48
v_add_f32 v72, v9, v72
s_waitcnt lgkmcnt(2)
mav 12,84,52
R 36,44032
v_fmac_f32 v73, v9, v9
mav 16,88,52
v_add_f32 v72, v10, v72
v_fmac_f32 v73, v10, v10
mav 20,92,52
v_add_f32 v72, v11, v72
v_fmac_f32 v73, v11, v11
mav 12,96,24
R 40,45056
v_permlane32_swap_b32 v72, v73
mav 16,100,24
v_add_f32 v72, v72, v73
mav 20,104,24
s_waitcnt lgkmcnt(2)
mav 12,108,28
R 44,46080
mav 16,112,28
mav 20,116,28
mav 12,120,32
R 48,47104
mav 16,124,32
mav 20,128,32
s_waitcnt lgkmcnt(2)
mav 12,132,36
R 52,48128
mav 16,136,36
mav 20,140,36
mav 12,144,40
R 24,49152
mav 16,148,40
mav 20,152,40
s_waitcnt lgkmcnt(2)
mav 12,156,44
R 28,50176
mav 16,160,44
mav 20,164,44
mav 12,168,48
R 32,51200
mav 16,172,48
mav 20,176,48
s_waitcnt lgkmcnt(2)
mav 12,180,52
R 36,52224
mav 16,184,52
v_add_u32 v70, 1, v70
mav 20,188,52
mav 12,192,24
R 40,53248
mav 16,196,24
mav 20,200,24
s_waitcnt lgkmcnt(2)
mav 12,204,28
R 44,54272
mav 16,208,28
mav 20,212,28
mav 12,216,32
R 48,55296
mav 16,220,32
mav 20,224,32
s_waitcnt lgkmcnt(2)
mav 12,228,36
R 52,56320
mav 16,232,36
mav 20,236,36
mav 12,240,40
R 24,57344
mav 16,244,40
mav 20,248,40
s_waitcnt lgkmcnt(2)
mav 12,252,44
R 28,58368
mvv 16,128,44
)"
R"(
mvv 20,132,44
mvv 12,136,48
R 32,59392
mvv 16,140,48
mvv 20,144,48
s_waitcnt lgkmcnt(2)
mvv 12,148,52
R 36,60416
mvv 16,152,52
s_waitcnt vmcnt(0)
buffer_store_dwordx2 v[70:71], v81, s[36:39], s52 offen sc1
mvv 20,156,52
mvv 12,160,24
R 40,61440
mvv 16,164,24
mvv 20,168,24
s_waitcnt lgkmcnt(2)
mvv 12,172,28
R 44,62464
mvv 16,176,28
ds_write_b32 v91, v72 offset:0
mvv 20,180,28
mvv 12,184,32
R 48,63488
mvv 16,188,32
LsO7:
s_cbranch_execz LwO
mvv 20,192,32
s_waitcnt lgkmcnt(3)
mvv 12,196,36
R 52,64512
mvv 16,200,36
s_add_u32 s49, s49, 346112
mvv 20,204,36
s_waitcnt lgkmcnt(2)
s_barrier
mvv 12,208,40
ds_read_b128 v[76:79], v92 offset:0
mvv 16,212,40
mvv 20,216,40
mvv 12,220,44
mvv 16,224,44
mvv 20,228,44
s_waitcnt lgkmcnt(1)
mvv 12,232,48
mvv 16,236,48
s_waitcnt lgkmcnt(0)
v_add_f32 v76, v76, v77
mvv 20,240,48
v_add_f32 v78, v78, v79
v_add_f32 v76, v76, v78
mvv 12,244,52
mvv 16,248,52
v_add_f32_dpp v77, v76, v76 quad_perm:[1,0,3,2] row_mask:0xf bank_mask:0xf
buffer_store_dword v77, v93, s[36:39], s49 offen sc1
mvv 20,252,52
s_nop 3
v_cvt_pk_bf16_f32 v62, v12, v13
v_cvt_pk_bf16_f32 v63, v14, v15
v_cvt_pk_bf16_f32 v64, v16, v17
v_cvt_pk_bf16_f32 v65, v18, v19
buffer_store_dwordx4 v[62:65], v83, s[36:39], s49 offen sc1
v_cvt_pk_bf16_f32 v66, v20, v21
v_cvt_pk_bf16_f32 v67, v22, v23
v_cvt_pk_bf16_f32 v68, v20, v21
v_cvt_pk_bf16_f32 v69, v22, v23
s_nop 0
v_permlane16_swap_b32 v66, v68
v_permlane16_swap_b32 v67, v69
buffer_store_dwordx4 v[66:69], v84, s[36:39], s49 offen sc1
v_mul_f32 v73, v12, v12
v_add_f32 v72, v13, v12
v_fmac_f32 v73, v13, v13
v_add_f32 v72, v14, v72
v_fmac_f32 v73, v14, v14
v_add_f32 v72, v15, v72
v_fmac_f32 v73, v15, v15
v_add_f32 v72, v16, v72
v_fmac_f32 v73, v16, v16
v_add_f32 v72, v17, v72
v_fmac_f32 v73, v17, v17
v_add_f32 v72, v18, v72
v_fmac_f32 v73, v18, v18
v_add_f32 v72, v19, v72
v_fmac_f32 v73, v19, v19
v_add_f32 v72, v20, v72
v_fmac_f32 v73, v20, v20
v_add_f32 v72, v21, v72
v_fmac_f32 v73, v21, v21
v_add_f32 v72, v22, v72
v_fmac_f32 v73, v22, v22
v_add_f32 v72, v23, v72
v_fmac_f32 v73, v23, v23
s_nop 1
v_permlane32_swap_b32 v72, v73
v_add_f32 v72, v72, v73
ds_write_b32 v91, v72 offset:1024
s_add_u32 s49, s49, 346112
s_waitcnt lgkmcnt(0)
s_barrier
ds_read_b128 v[76:79], v92 offset:1024
s_waitcnt lgkmcnt(0)
v_add_f32 v76, v76, v77
v_add_f32 v78, v78, v79
v_add_f32 v76, v76, v78
s_nop 1
v_add_f32_dpp v77, v76, v76 quad_perm:[1,0,3,2] row_mask:0xf bank_mask:0xf
buffer_store_dword v77, v93, s[36:39], s49 offen sc1
v_add_u32 v70, 37, v70
s_waitcnt vmcnt(0)
buffer_store_dwordx2 v[70:71], v81, s[36:39], s52 offen sc1
s_endpgm
LvO2:
LsO8:
s_cbranch_execz LwO
s_barrier
buffer_load_dwordx4 v[56:59], v89, s[36:39], s49 offen sc1
buffer_load_dwordx2 v[60:61], v90, s[36:39], s49 offen sc1
s_add_u32 s49, s49, 346112
s_add_u32 m0, s50, 0
s_nop 0
D 85,0
D 85,1024
D 85,2048
D 85,3072
s_add_u32 m0, s50, 4096
s_nop 0
D 86,0
D 86,1024
D 86,2048
D 86,3072
s_waitcnt vmcnt(9)
v_lshlrev_b32 v0, 16, v56
v_and_b32 v1, 0xffff0000, v56
v_lshlrev_b32 v2, 16, v57
v_and_b32 v3, 0xffff0000, v57
v_lshlrev_b32 v4, 16, v58
v_and_b32 v5, 0xffff0000, v58
v_lshlrev_b32 v6, 16, v59
v_and_b32 v7, 0xffff0000, v59
s_waitcnt vmcnt(8)
v_lshlrev_b32 v8, 16, v60
v_and_b32 v9, 0xffff0000, v60
v_lshlrev_b32 v10, 16, v61
v_and_b32 v11, 0xffff0000, v61
buffer_load_dwordx4 v[56:59], v89, s[36:39], s49 offen sc1
buffer_load_dwordx2 v[60:61], v90, s[36:39], s49 offen sc1
s_add_u32 s49, s49, 346112
s_waitcnt vmcnt(2)
s_barrier
s_add_u32 m0, s50, 32768
R 24,0
D 85,0
R 28,1024
D 85,1024
R 32,2048
R 36,3072
D 85,2048
s_waitcnt lgkmcnt(2)
mav 0,0,24
R 40,4096
mav 4,4,24
mav 8,8,24
D 85,3072
mav 0,12,28
R 44,5120
mav 4,16,28
mav 8,20,28
s_add_u32 m0, s50, 36864
s_waitcnt lgkmcnt(2)
mav 0,24,32
R 48,6144
mav 4,28,32
D 86,0
mav 8,32,32
mav 0,36,36
R 52,7168
mav 4,40,36
mav 8,44,36
D 86,1024
s_waitcnt lgkmcnt(2)
mav 0,48,40
R 24,8192
mav 4,52,40
mav 8,56,40
mav 0,60,44
R 28,9216
mav 4,64,44
D 86,2048
mav 8,68,44
s_waitcnt lgkmcnt(2)
mav 0,72,48
R 32,10240
mav 4,76,48
mav 8,80,48
D 86,3072
mav 0,84,52
R 36,11264
mav 4,88,52
mav 8,92,52
s_waitcnt lgkmcnt(2)
mav 0,96,24
R 40,12288
mav 4,100,24
mav 8,104,24
mav 0,108,28
R 44,13312
mav 4,112,28
mav 8,116,28
s_waitcnt lgkmcnt(2)
mav 0,120,32
R 48,14336
mav 4,124,32
mav 8,128,32
mav 0,132,36
R 52,15360
mav 4,136,36
mav 8,140,36
s_waitcnt lgkmcnt(2)
mav 0,144,40
R 24,16384
mav 4,148,40
mav 8,152,40
mav 0,156,44
R 28,17408
mav 4,160,44
mav 8,164,44
s_waitcnt lgkmcnt(2)
mav 0,168,48
R 32,18432
mav 4,172,48
mav 8,176,48
mav 0,180,52
R 36,19456
mav 4,184,52
mav 8,188,52
s_waitcnt lgkmcnt(2)
mav 0,192,24
R 40,20480
mav 4,196,24
mav 8,200,24
s_waitcnt vmcnt(9)
v_lshlrev_b32 v12, 16, v56
mav 0,204,28
R 44,21504
v_and_b32 v13, 0xffff0000, v56
mav 4,208,28
v_lshlrev_b32 v14, 16, v57
v_and_b32 v15, 0xffff0000, v57
mav 8,212,28
v_lshlrev_b32 v16, 16, v58
s_waitcnt lgkmcnt(2)
mav 0,216,32
R 48,22528
v_and_b32 v17, 0xffff0000, v58
mav 4,220,32
v_lshlrev_b32 v18, 16, v59
v_and_b32 v19, 0xffff0000, v59
mav 8,224,32
s_waitcnt vmcnt(8)
v_lshlrev_b32 v20, 16, v60
mav 0,228,36
R 52,23552
v_and_b32 v21, 0xffff0000, v60
mav 4,232,36
v_lshlrev_b32 v22, 16, v61
v_and_b32 v23, 0xffff0000, v61
mav 8,236,36
s_waitcnt lgkmcnt(2)
mav 0,240,40
R 24,24576
mav 4,244,40
mav 8,248,40
mav 0,252,44
R 28,25600
mvv 4,128,44
buffer_load_dwordx4 v[56:59], v89, s[36:39], s49 offen sc1
mvv 8,132,44
s_waitcnt lgkmcnt(2)
mvv 0,136,48
R 32,26624
mvv 4,140,48
mvv 8,144,48
buffer_load_dwordx2 v[60:61], v90, s[36:39], s49 offen sc1
mvv 0,148,52
R 36,27648
mvv 4,152,52
mvv 8,156,52
s_waitcnt lgkmcnt(2)
mvv 0,160,24
R 40,28672
mvv 4,164,24
mvv 8,168,24
mvv 0,172,28
R 44,29696
mvv 4,176,28
mvv 8,180,28
s_waitcnt lgkmcnt(2)
mvv 0,184,32
R 48,30720
s_xor_b32 s50, s50, 0x10000
mvv 4,188,32
mvv 8,192,32
mvv 0,196,36
R 52,31744
mvv 4,200,36
s_add_u32 s49, s49, 346112
mvv 8,204,36
s_waitcnt vmcnt(2)
s_barrier
s_waitcnt lgkmcnt(2)
mvv 0,208,40
R 24,32768
s_add_u32 m0, s50, 0
mvv 4,212,40
D 85,0
mvv 8,216,40
mvv 0,220,44
R 28,33792
mvv 4,224,44
mvv 8,228,44
D 85,1024
s_waitcnt lgkmcnt(2)
mvv 0,232,48
R 32,34816
mvv 4,236,48
mvv 8,240,48
mvv 0,244,52
R 36,35840
mvv 4,248,52
D 85,2048
mvv 8,252,52
s_waitcnt lgkmcnt(2)
mav 12,0,24
R 40,36864
mav 16,4,24
v_cvt_pk_bf16_f32 v62, v0, v1
v_cvt_pk_bf16_f32 v63, v2, v3
mav 20,8,24
D 85,3072
v_cvt_pk_bf16_f32 v64, v4, v5
mav 12,12,28
R 44,37888
v_cvt_pk_bf16_f32 v65, v6, v7
mav 16,16,28
v_cvt_pk_bf16_f32 v66, v8, v9
v_cvt_pk_bf16_f32 v67, v10, v11
mav 20,20,28
s_add_u32 m0, s50, 4096
s_waitcnt lgkmcnt(2)
mav 12,24,32
R 48,38912
buffer_store_dwordx4 v[62:65], v83, s[36:39], s49 offen sc1
mav 16,28,32
v_cvt_pk_bf16_f32 v68, v8, v9
v_cvt_pk_bf16_f32 v69, v10, v11
mav 20,32,32
v_permlane16_swap_b32 v66, v68
v_permlane16_swap_b32 v67, v69
mav 12,36,36
R 52,39936
v_mul_f32 v73, v0, v0
mav 16,40,36
buffer_store_dwordx4 v[66:69], v84, s[36:39], s49 offen sc1
v_add_f32 v72, v1, v0
mav 20,44,36
v_fmac_f32 v73, v1, v1
s_waitcnt lgkmcnt(2)
mav 12,48,40
R 24,40960
v_add_f32 v72, v2, v72
mav 16,52,40
v_fmac_f32 v73, v2, v2
v_add_f32 v72, v3, v72
mav 20,56,40
D 86,0
v_fmac_f32 v73, v3, v3
mav 12,60,44
R 28,41984
v_add_f32 v72, v4, v72
mav 16,64,44
v_fmac_f32 v73, v4, v4
v_add_f32 v72, v5, v72
mav 20,68,44
v_fmac_f32 v73, v5, v5
s_waitcnt lgkmcnt(2)
mav 12,72,48
R 32,43008
D 86,1024
mav 16,76,48
v_add_f32 v72, v6, v72
v_fmac_f32 v73, v6, v6
mav 20,80,48
v_add_f32 v72, v7, v72
v_fmac_f32 v73, v7, v7
mav 12,84,52
R 36,44032
v_add_f32 v72, v8, v72
mav 16,88,52
D 86,2048
v_fmac_f32 v73, v8, v8
mav 20,92,52
v_add_f32 v72, v9, v72
s_waitcnt lgkmcnt(2)
mav 12,96,24
R 40,45056
v_fmac_f32 v73, v9, v9
mav 16,100,24
v_add_f32 v72, v10, v72
v_fmac_f32 v73, v10, v10
mav 20,104,24
D 86,3072
v_add_f32 v72, v11, v72
mav 12,108,28
R 44,46080
v_fmac_f32 v73, v11, v11
mav 16,112,28
mav 20,116,28
v_permlane32_swap_b32 v72, v73
s_waitcnt lgkmcnt(2)
mav 12,120,32
R 48,47104
v_add_f32 v72, v72, v73
mav 16,124,32
mav 20,128,32
mav 12,132,36
R 52,48128
mav 16,136,36
mav 20,140,36
s_waitcnt lgkmcnt(2)
mav 12,144,40
R 24,49152
mav 16,148,40
mav 20,152,40
mav 12,156,44
R 28,50176
mav 16,160,44
mav 20,164,44
s_waitcnt lgkmcnt(2)
mav 12,168,48
R 32,51200
mav 16,172,48
mav 20,176,48
mav 12,180,52
R 36,52224
mav 16,184,52
v_add_u32 v70, 1, v70
mav 20,188,52
s_waitcnt lgkmcnt(2)
mav 12,192,24
R 40,53248
mav 16,196,24
mav 20,200,24
s_waitcnt vmcnt(11)
v_lshlrev_b32 v0, 16, v56
mav 12,204,28
R 44,54272
v_and_b32 v1, 0xffff0000, v56
mav 16,208,28
v_lshlrev_b32 v2, 16, v57
v_and_b32 v3, 0xffff0000, v57
mav 20,212,28
v_lshlrev_b32 v4, 16, v58
s_waitcnt lgkmcnt(2)
mav 12,216,32
R 48,55296
v_and_b32 v5, 0xffff0000, v58
mav 16,220,32
v_lshlrev_b32 v6, 16, v59
v_and_b32 v7, 0xffff0000, v59
mav 20,224,32
s_waitcnt vmcnt(10)
v_lshlrev_b32 v8, 16, v60
mav 12,228,36
R 52,56320
v_and_b32 v9, 0xffff0000, v60
mav 16,232,36
v_lshlrev_b32 v10, 16, v61
v_and_b32 v11, 0xffff0000, v61
mav 20,236,36
s_waitcnt lgkmcnt(2)
mav 12,240,40
R 24,57344
mav 16,244,40
mav 20,248,40
mav 12,252,44
R 28,58368
mvv 16,128,44
buffer_load_dwordx4 v[56:59], v89, s[36:39], s49 offen sc1
mvv 20,132,44
s_waitcnt lgkmcnt(2)
mvv 12,136,48
R 32,59392
mvv 16,140,48
mvv 20,144,48
buffer_load_dwordx2 v[60:61], v90, s[36:39], s49 offen sc1
mvv 12,148,52
R 36,60416
mvv 16,152,52
s_waitcnt vmcnt(6)
buffer_store_dwordx2 v[70:71], v81, s[36:39], s52 offen sc1
mvv 20,156,52
s_waitcnt lgkmcnt(2)
mvv 12,160,24
R 40,61440
mvv 16,164,24
mvv 20,168,24
ds_write_b32 v91, v72 offset:0
mvv 12,172,28
R 44,62464
mvv 16,176,28
mvv 20,180,28
s_waitcnt lgkmcnt(3)
mvv 12,184,32
R 48,63488
mvv 16,188,32
mvv 20,192,32
mvv 12,196,36
R 52,64512
v_xor_b32 v80, 0x10000, v80
mvv 16,200,36
s_add_u32 s49, s49, 346112
mvv 20,204,36
s_waitcnt vmcnt(3) lgkmcnt(2)
s_barrier
mvv 12,208,40
R 24,0
s_add_u32 m0, s50, 32768
mvv 16,212,40
D 85,0
mvv 20,216,40
mvv 12,220,44
R 28,1024
mvv 16,224,44
mvv 20,228,44
D 85,1024
s_waitcnt lgkmcnt(2)
mvv 12,232,48
R 32,2048
mvv 16,236,48
mvv 20,240,48
mvv 12,244,52
R 36,3072
mvv 16,248,52
D 85,2048
mvv 20,252,52
s_mov_b32 s60, -30
.p2align 6
LO2_1:
s_waitcnt lgkmcnt(2)
mav 0,0,24
R 40,4096
mav 4,4,24
v_cvt_pk_bf16_f32 v62, v12, v13
v_cvt_pk_bf16_f32 v63, v14, v15
mav 8,8,24
D 85,3072
v_cvt_pk_bf16_f32 v64, v16, v17
mav 0,12,28
R 44,5120
v_cvt_pk_bf16_f32 v65, v18, v19
mav 4,16,28
v_cvt_pk_bf16_f32 v66, v20, v21
v_cvt_pk_bf16_f32 v67, v22, v23
mav 8,20,28
s_add_u32 m0, s50, 36864
s_waitcnt lgkmcnt(2)
mav 0,24,32
R 48,6144
buffer_store_dwordx4 v[62:65], v83, s[36:39], s49 offen sc1
mav 4,28,32
v_cvt_pk_bf16_f32 v68, v20, v21
v_cvt_pk_bf16_f32 v69, v22, v23
mav 8,32,32
v_permlane16_swap_b32 v66, v68
v_permlane16_swap_b32 v67, v69
mav 0,36,36
R 52,7168
v_mul_f32 v73, v12, v12
mav 4,40,36
buffer_store_dwordx4 v[66:69], v84, s[36:39], s49 offen sc1
v_add_f32 v72, v13, v12
mav 8,44,36
v_fmac_f32 v73, v13, v13
s_waitcnt lgkmcnt(2)
mav 0,48,40
R 24,8192
v_add_f32 v72, v14, v72
mav 4,52,40
v_fmac_f32 v73, v14, v14
v_add_f32 v72, v15, v72
mav 8,56,40
D 86,0
v_fmac_f32 v73, v15, v15
mav 0,60,44
R 28,9216
v_add_f32 v72, v16, v72
mav 4,64,44
v_fmac_f32 v73, v16, v16
v_add_f32 v72, v17, v72
mav 8,68,44
v_fmac_f32 v73, v17, v17
s_waitcnt lgkmcnt(2)
mav 0,72,48
R 32,10240
D 86,1024
mav 4,76,48
v_add_f32 v72, v18, v72
v_fmac_f32 v73, v18, v18
mav 8,80,48
v_add_f32 v72, v19, v72
v_fmac_f32 v73, v19, v19
mav 0,84,52
R 36,11264
v_add_f32 v72, v20, v72
mav 4,88,52
D 86,2048
v_fmac_f32 v73, v20, v20
mav 8,92,52
v_add_f32 v72, v21, v72
s_waitcnt lgkmcnt(2)
mav 0,96,24
R 40,12288
v_fmac_f32 v73, v21, v21
mav 4,100,24
v_add_f32 v72, v22, v72
v_fmac_f32 v73, v22, v22
LsO9:
s_cbranch_execz LwO
mav 8,104,24
D 86,3072
v_add_f32 v72, v23, v72
mav 0,108,28
R 44,13312
v_fmac_f32 v73, v23, v23
mav 4,112,28
mav 8,116,28
v_permlane32_swap_b32 v72, v73
s_waitcnt lgkmcnt(2)
mav 0,120,32
R 48,14336
v_add_f32 v72, v72, v73
mav 4,124,32
mav 8,128,32
mav 0,132,36
R 52,15360
mav 4,136,36
mav 8,140,36
s_waitcnt lgkmcnt(2)
mav 0,144,40
R 24,16384
mav 4,148,40
mav 8,152,40
mav 0,156,44
R 28,17408
mav 4,160,44
mav 8,164,44
s_waitcnt lgkmcnt(2)
mav 0,168,48
R 32,18432
mav 4,172,48
mav 8,176,48
mav 0,180,52
R 36,19456
mav 4,184,52
v_add_u32 v70, 1, v70
mav 8,188,52
s_waitcnt lgkmcnt(2)
mav 0,192,24
R 40,20480
mav 4,196,24
mav 8,200,24
s_waitcnt vmcnt(12)
v_lshlrev_b32 v12, 16, v56
mav 0,204,28
R 44,21504
v_and_b32 v13, 0xffff0000, v56
mav 4,208,28
v_lshlrev_b32 v14, 16, v57
v_and_b32 v15, 0xffff0000, v57
mav 8,212,28
v_lshlrev_b32 v16, 16, v58
s_waitcnt lgkmcnt(2)
mav 0,216,32
R 48,22528
v_and_b32 v17, 0xffff0000, v58
mav 4,220,32
v_lshlrev_b32 v18, 16, v59
v_and_b32 v19, 0xffff0000, v59
mav 8,224,32
s_waitcnt vmcnt(11)
v_lshlrev_b32 v20, 16, v60
mav 0,228,36
R 52,23552
v_and_b32 v21, 0xffff0000, v60
mav 4,232,36
v_lshlrev_b32 v22, 16, v61
v_and_b32 v23, 0xffff0000, v61
mav 8,236,36
s_waitcnt lgkmcnt(2)
mav 0,240,40
R 24,24576
mav 4,244,40
mav 8,248,40
mav 0,252,44
R 28,25600
mvv 4,128,44
buffer_load_dwordx4 v[56:59], v89, s[36:39], s49 offen sc1
mvv 8,132,44
s_waitcnt lgkmcnt(2)
mvv 0,136,48
R 32,26624
mvv 4,140,48
mvv 8,144,48
buffer_load_dwordx2 v[60:61], v90, s[36:39], s49 offen sc1
mvv 0,148,52
R 36,27648
mvv 4,152,52
s_waitcnt vmcnt(6)
buffer_store_dwordx2 v[70:71], v81, s[36:39], s52 offen sc1
mvv 8,156,52
s_waitcnt lgkmcnt(2)
mvv 0,160,24
R 40,28672
mvv 4,164,24
mvv 8,168,24
ds_write_b32 v91, v72 offset:1024
mvv 0,172,28
R 44,29696
mvv 4,176,28
mvv 8,180,28
s_waitcnt lgkmcnt(3)
mvv 0,184,32
R 48,30720
s_xor_b32 s50, s50, 0x10000
mvv 4,188,32
mvv 8,192,32
mvv 0,196,36
R 52,31744
mvv 4,200,36
s_add_u32 s49, s49, 346112
mvv 8,204,36
s_waitcnt vmcnt(3) lgkmcnt(2)
s_barrier
mvv 0,208,40
R 24,32768
s_add_u32 m0, s50, 0
mvv 4,212,40
D 85,0
mvv 8,216,40
mvv 0,220,44
R 28,33792
mvv 4,224,44
mvv 8,228,44
D 85,1024
s_waitcnt lgkmcnt(2)
mvv 0,232,48
R 32,34816
mvv 4,236,48
mvv 8,240,48
mvv 0,244,52
R 36,35840
mvv 4,248,52
D 85,2048
mvv 8,252,52
s_waitcnt lgkmcnt(2)
mav 12,0,24
R 40,36864
mav 16,4,24
v_cvt_pk_bf16_f32 v62, v0, v1
v_cvt_pk_bf16_f32 v63, v2, v3
mav 20,8,24
D 85,3072
v_cvt_pk_bf16_f32 v64, v4, v5
mav 12,12,28
R 44,37888
v_cvt_pk_bf16_f32 v65, v6, v7
mav 16,16,28
v_cvt_pk_bf16_f32 v66, v8, v9
v_cvt_pk_bf16_f32 v67, v10, v11
mav 20,20,28
s_add_u32 m0, s50, 4096
s_waitcnt lgkmcnt(2)
mav 12,24,32
R 48,38912
buffer_store_dwordx4 v[62:65], v83, s[36:39], s49 offen sc1
mav 16,28,32
v_cvt_pk_bf16_f32 v68, v8, v9
v_cvt_pk_bf16_f32 v69, v10, v11
mav 20,32,32
v_permlane16_swap_b32 v66, v68
v_permlane16_swap_b32 v67, v69
mav 12,36,36
R 52,39936
v_mul_f32 v73, v0, v0
mav 16,40,36
buffer_store_dwordx4 v[66:69], v84, s[36:39], s49 offen sc1
v_add_f32 v72, v1, v0
mav 20,44,36
v_fmac_f32 v73, v1, v1
s_waitcnt lgkmcnt(2)
mav 12,48,40
R 24,40960
v_add_f32 v72, v2, v72
mav 16,52,40
v_fmac_f32 v73, v2, v2
v_add_f32 v72, v3, v72
mav 20,56,40
D 86,0
v_fmac_f32 v73, v3, v3
mav 12,60,44
R 28,41984
v_add_f32 v72, v4, v72
mav 16,64,44
v_fmac_f32 v73, v4, v4
v_add_f32 v72, v5, v72
mav 20,68,44
v_fmac_f32 v73, v5, v5
s_waitcnt lgkmcnt(2)
mav 12,72,48
R 32,43008
D 86,1024
mav 16,76,48
v_add_f32 v72, v6, v72
v_fmac_f32 v73, v6, v6
mav 20,80,48
v_add_f32 v72, v7, v72
v_fmac_f32 v73, v7, v7
mav 12,84,52
R 36,44032
v_add_f32 v72, v8, v72
mav 16,88,52
D 86,2048
v_fmac_f32 v73, v8, v8
mav 20,92,52
v_add_f32 v72, v9, v72
s_waitcnt lgkmcnt(2)
mav 12,96,24
R 40,45056
v_fmac_f32 v73, v9, v9
mav 16,100,24
v_add_f32 v72, v10, v72
v_fmac_f32 v73, v10, v10
mav 20,104,24
D 86,3072
v_add_f32 v72, v11, v72
mav 12,108,28
R 44,46080
v_fmac_f32 v73, v11, v11
mav 16,112,28
mav 20,116,28
v_permlane32_swap_b32 v72, v73
s_waitcnt lgkmcnt(2)
mav 12,120,32
R 48,47104
v_add_f32 v72, v72, v73
mav 16,124,32
mav 20,128,32
mav 12,132,36
R 52,48128
mav 16,136,36
mav 20,140,36
s_waitcnt lgkmcnt(2)
mav 12,144,40
R 24,49152
mav 16,148,40
mav 20,152,40
mav 12,156,44
R 28,50176
mav 16,160,44
mav 20,164,44
s_waitcnt lgkmcnt(2)
mav 12,168,48
R 32,51200
mav 16,172,48
mav 20,176,48
mav 12,180,52
R 36,52224
mav 16,184,52
v_add_u32 v70, 1, v70
mav 20,188,52
s_waitcnt lgkmcnt(2)
mav 12,192,24
R 40,53248
mav 16,196,24
mav 20,200,24
s_waitcnt vmcnt(12)
v_lshlrev_b32 v0, 16, v56
mav 12,204,28
R 44,54272
v_and_b32 v1, 0xffff0000, v56
mav 16,208,28
v_lshlrev_b32 v2, 16, v57
v_and_b32 v3, 0xffff0000, v57
mav 20,212,28
v_lshlrev_b32 v4, 16, v58
s_waitcnt lgkmcnt(2)
mav 12,216,32
R 48,55296
v_and_b32 v5, 0xffff0000, v58
mav 16,220,32
v_lshlrev_b32 v6, 16, v59
v_and_b32 v7, 0xffff0000, v59
mav 20,224,32
s_waitcnt vmcnt(11)
v_lshlrev_b32 v8, 16, v60
mav 12,228,36
R 52,56320
v_and_b32 v9, 0xffff0000, v60
mav 16,232,36
v_lshlrev_b32 v10, 16, v61
v_and_b32 v11, 0xffff0000, v61
mav 20,236,36
s_waitcnt lgkmcnt(2)
mav 12,240,40
R 24,57344
mav 16,244,40
mav 20,248,40
mav 12,252,44
R 28,58368
mvv 16,128,44
buffer_load_dwordx4 v[56:59], v89, s[36:39], s49 offen sc1
mvv 20,132,44
s_waitcnt lgkmcnt(2)
mvv 12,136,48
R 32,59392
mvv 16,140,48
mvv 20,144,48
buffer_load_dwordx2 v[60:61], v90, s[36:39], s49 offen sc1
mvv 12,148,52
R 36,60416
mvv 16,152,52
s_waitcnt vmcnt(6)
buffer_store_dwordx2 v[70:71], v81, s[36:39], s52 offen sc1
mvv 20,156,52
s_waitcnt lgkmcnt(2)
mvv 12,160,24
R 40,61440
mvv 16,164,24
mvv 20,168,24
ds_write_b32 v91, v72 offset:0
mvv 12,172,28
R 44,62464
mvv 16,176,28
mvv 20,180,28
s_waitcnt lgkmcnt(3)
mvv 12,184,32
R 48,63488
mvv 16,188,32
mvv 20,192,32
mvv 12,196,36
R 52,64512
v_xor_b32 v80, 0x10000, v80
mvv 16,200,36
s_add_u32 s49, s49, 346112
mvv 20,204,36
s_waitcnt vmcnt(3) lgkmcnt(2)
s_barrier
mvv 12,208,40
R 24,0
s_add_u32 m0, s50, 32768
mvv 16,212,40
D 85,0
mvv 20,216,40
mvv 12,220,44
R 28,1024
mvv 16,224,44
mvv 20,228,44
D 85,1024
s_waitcnt lgkmcnt(2)
mvv 12,232,48
R 32,2048
mvv 16,236,48
mvv 20,240,48
mvv 12,244,52
R 36,3072
mvv 16,248,52
D 85,2048
mvv 20,252,52
s_add_u32 s60, s60, 1
s_cbranch_scc0 LO2_1
s_waitcnt lgkmcnt(2)
mav 0,0,24
R 40,4096
mav 4,4,24
v_cvt_pk_bf16_f32 v62, v12, v13
v_cvt_pk_bf16_f32 v63, v14, v15
mav 8,8,24
D 85,3072
v_cvt_pk_bf16_f32 v64, v16, v17
mav 0,12,28
R 44,5120
v_cvt_pk_bf16_f32 v65, v18, v19
mav 4,16,28
v_cvt_pk_bf16_f32 v66, v20, v21
v_cvt_pk_bf16_f32 v67, v22, v23
mav 8,20,28
s_add_u32 m0, s50, 36864
s_waitcnt lgkmcnt(2)
mav 0,24,32
R 48,6144
buffer_store_dwordx4 v[62:65], v83, s[36:39], s49 offen sc1
mav 4,28,32
v_cvt_pk_bf16_f32 v68, v20, v21
v_cvt_pk_bf16_f32 v69, v22, v23
mav 8,32,32
v_permlane16_swap_b32 v66, v68
v_permlane16_swap_b32 v67, v69
mav 0,36,36
R 52,7168
v_mul_f32 v73, v12, v12
mav 4,40,36
buffer_store_dwordx4 v[66:69], v84, s[36:39], s49 offen sc1
v_add_f32 v72, v13, v12
mav 8,44,36
v_fmac_f32 v73, v13, v13
s_waitcnt lgkmcnt(2)
mav 0,48,40
R 24,8192
v_add_f32 v72, v14, v72
mav 4,52,40
v_fmac_f32 v73, v14, v14
v_add_f32 v72, v15, v72
mav 8,56,40
D 86,0
v_fmac_f32 v73, v15, v15
mav 0,60,44
R 28,9216
v_add_f32 v72, v16, v72
mav 4,64,44
v_fmac_f32 v73, v16, v16
v_add_f32 v72, v17, v72
mav 8,68,44
v_fmac_f32 v73, v17, v17
s_waitcnt lgkmcnt(2)
mav 0,72,48
R 32,10240
D 86,1024
mav 4,76,48
v_add_f32 v72, v18, v72
v_fmac_f32 v73, v18, v18
mav 8,80,48
v_add_f32 v72, v19, v72
v_fmac_f32 v73, v19, v19
mav 0,84,52
R 36,11264
v_add_f32 v72, v20, v72
mav 4,88,52
D 86,2048
v_fmac_f32 v73, v20, v20
mav 8,92,52
v_add_f32 v72, v21, v72
s_waitcnt lgkmcnt(2)
mav 0,96,24
R 40,12288
v_fmac_f32 v73, v21, v21
mav 4,100,24
v_add_f32 v72, v22, v72
v_fmac_f32 v73, v22, v22
mav 8,104,24
D 86,3072
v_add_f32 v72, v23, v72
mav 0,108,28
R 44,13312
v_fmac_f32 v73, v23, v23
mav 4,112,28
mav 8,116,28
v_permlane32_swap_b32 v72, v73
s_waitcnt lgkmcnt(2)
mav 0,120,32
R 48,14336
v_add_f32 v72, v72, v73
mav 4,124,32
mav 8,128,32
mav 0,132,36
R 52,15360
mav 4,136,36
mav 8,140,36
s_waitcnt lgkmcnt(2)
mav 0,144,40
R 24,16384
mav 4,148,40
mav 8,152,40
mav 0,156,44
R 28,17408
mav 4,160,44
mav 8,164,44
s_waitcnt lgkmcnt(2)
mav 0,168,48
R 32,18432
mav 4,172,48
mav 8,176,48
mav 0,180,52
R 36,19456
mav 4,184,52
v_add_u32 v70, 1, v70
mav 8,188,52
s_waitcnt lgkmcnt(2)
mav 0,192,24
R 40,20480
mav 4,196,24
mav 8,200,24
s_waitcnt vmcnt(12)
v_lshlrev_b32 v12, 16, v56
mav 0,204,28
R 44,21504
v_and_b32 v13, 0xffff0000, v56
mav 4,208,28
v_lshlrev_b32 v14, 16, v57
v_and_b32 v15, 0xffff0000, v57
mav 8,212,28
v_lshlrev_b32 v16, 16, v58
s_waitcnt lgkmcnt(2)
mav 0,216,32
R 48,22528
v_and_b32 v17, 0xffff0000, v58
mav 4,220,32
v_lshlrev_b32 v18, 16, v59
v_and_b32 v19, 0xffff0000, v59
mav 8,224,32
s_waitcnt vmcnt(11)
v_lshlrev_b32 v20, 16, v60
mav 0,228,36
R 52,23552
v_and_b32 v21, 0xffff0000, v60
mav 4,232,36
v_lshlrev_b32 v22, 16, v61
v_and_b32 v23, 0xffff0000, v61
mav 8,236,36
s_waitcnt lgkmcnt(2)
mav 0,240,40
R 24,24576
mav 4,244,40
mav 8,248,40
mav 0,252,44
R 28,25600
mvv 4,128,44
mvv 8,132,44
s_waitcnt lgkmcnt(2)
mvv 0,136,48
R 32,26624
mvv 4,140,48
mvv 8,144,48
mvv 0,148,52
R 36,27648
mvv 4,152,52
s_waitcnt vmcnt(4)
buffer_store_dwordx2 v[70:71], v81, s[36:39], s52 offen sc1
mvv 8,156,52
s_waitcnt lgkmcnt(2)
mvv 0,160,24
R 40,28672
mvv 4,164,24
mvv 8,168,24
ds_write_b32 v91, v72 offset:1024
mvv 0,172,28
R 44,29696
mvv 4,176,28
mvv 8,180,28
s_waitcnt lgkmcnt(3)
mvv 0,184,32
R 48,30720
mvv 4,188,32
mvv 8,192,32
mvv 0,196,36
R 52,31744
mvv 4,200,36
s_add_u32 s49, s49, 346112
mvv 8,204,36
s_waitcnt vmcnt(1) lgkmcnt(2)
s_barrier
mvv 0,208,40
R 24,32768
mvv 4,212,40
mvv 8,216,40
mvv 0,220,44
R 28,33792
mvv 4,224,44
mvv 8,228,44
s_waitcnt lgkmcnt(2)
mvv 0,232,48
R 32,34816
mvv 4,236,48
mvv 8,240,48
mvv 0,244,52
R 36,35840
mvv 4,248,52
mvv 8,252,52
s_waitcnt lgkmcnt(2)
mav 12,0,24
R 40,36864
mav 16,4,24
mav 20,8,24
v_cvt_pk_bf16_f32 v62, v0, v1
v_cvt_pk_bf16_f32 v63, v2, v3
mav 12,12,28
R 44,37888
v_cvt_pk_bf16_f32 v64, v4, v5
mav 16,16,28
v_cvt_pk_bf16_f32 v65, v6, v7
buffer_store_dwordx4 v[62:65], v83, s[36:39], s49 offen sc1
mav 20,20,28
v_cvt_pk_bf16_f32 v66, v8, v9
s_waitcnt lgkmcnt(2)
mav 12,24,32
R 48,38912
v_cvt_pk_bf16_f32 v67, v10, v11
mav 16,28,32
v_cvt_pk_bf16_f32 v68, v8, v9
v_cvt_pk_bf16_f32 v69, v10, v11
LsO10:
s_cbranch_execz LwO
mav 20,32,32
v_permlane16_swap_b32 v66, v68
v_permlane16_swap_b32 v67, v69
mav 12,36,36
R 52,39936
buffer_store_dwordx4 v[66:69], v84, s[36:39], s49 offen sc1
mav 16,40,36
v_mul_f32 v73, v0, v0
v_add_f32 v72, v1, v0
mav 20,44,36
v_fmac_f32 v73, v1, v1
s_waitcnt lgkmcnt(2)
mav 12,48,40
R 24,40960
v_add_f32 v72, v2, v72
mav 16,52,40
v_fmac_f32 v73, v2, v2
v_add_f32 v72, v3, v72
mav 20,56,40
v_fmac_f32 v73, v3, v3
v_add_f32 v72, v4, v72
mav 12,60,44
R 28,41984
v_fmac_f32 v73, v4, v4
mav 16,64,44
v_add_f32 v72, v5, v72
v_fmac_f32 v73, v5, v5
mav 20,68,44
v_add_f32 v72, v6, v72
s_waitcnt lgkmcnt(2)
mav 12,72,48
R 32,43008
v_fmac_f32 v73, v6, v6
mav 16,76,48
v_add_f32 v72, v7, v72
v_fmac_f32 v73, v7, v7
mav 20,80,48
v_add_f32 v72, v8, v72
v_fmac_f32 v73, v8, v8
mav 12,84,52
R 36,44032
v_add_f32 v72, v9, v72
mav 16,88,52
v_fmac_f32 v73, v9, v9
v_add_f32 v72, v10, v72
mav 20,92,52
v_fmac_f32 v73, v10, v10
s_waitcnt lgkmcnt(2)
mav 12,96,24
R 40,45056
v_add_f32 v72, v11, v72
mav 16,100,24
v_fmac_f32 v73, v11, v11
mav 20,104,24
mav 12,108,28
R 44,46080
v_permlane32_swap_b32 v72, v73
mav 16,112,28
v_add_f32 v72, v72, v73
mav 20,116,28
s_waitcnt lgkmcnt(2)
mav 12,120,32
R 48,47104
mav 16,124,32
mav 20,128,32
mav 12,132,36
R 52,48128
mav 16,136,36
mav 20,140,36
s_waitcnt lgkmcnt(2)
mav 12,144,40
R 24,49152
mav 16,148,40
mav 20,152,40
mav 12,156,44
R 28,50176
mav 16,160,44
mav 20,164,44
s_waitcnt lgkmcnt(2)
mav 12,168,48
R 32,51200
mav 16,172,48
mav 20,176,48
mav 12,180,52
R 36,52224
mav 16,184,52
v_add_u32 v70, 1, v70
mav 20,188,52
s_waitcnt lgkmcnt(2)
mav 12,192,24
R 40,53248
mav 16,196,24
mav 20,200,24
mav 12,204,28
R 44,54272
mav 16,208,28
mav 20,212,28
s_waitcnt lgkmcnt(2)
mav 12,216,32
R 48,55296
mav 16,220,32
mav 20,224,32
mav 12,228,36
R 52,56320
mav 16,232,36
mav 20,236,36
s_waitcnt lgkmcnt(2)
mav 12,240,40
R 24,57344
mav 16,244,40
mav 20,248,40
mav 12,252,44
R 28,58368
mvv 16,128,44
mvv 20,132,44
s_waitcnt lgkmcnt(2)
mvv 12,136,48
R 32,59392
mvv 16,140,48
mvv 20,144,48
mvv 12,148,52
R 36,60416
mvv 16,152,52
s_waitcnt vmcnt(0)
buffer_store_dwordx2 v[70:71], v81, s[36:39], s52 offen sc1
mvv 20,156,52
s_waitcnt lgkmcnt(2)
mvv 12,160,24
R 40,61440
mvv 16,164,24
mvv 20,168,24
ds_write_b32 v91, v72 offset:0
mvv 12,172,28
R 44,62464
mvv 16,176,28
mvv 20,180,28
s_waitcnt lgkmcnt(3)
mvv 12,184,32
R 48,63488
mvv 16,188,32
mvv 20,192,32
mvv 12,196,36
R 52,64512
mvv 16,200,36
s_add_u32 s49, s49, 346112
mvv 20,204,36
s_waitcnt lgkmcnt(2)
s_barrier
mvv 12,208,40
mvv 16,212,40
mvv 20,216,40
mvv 12,220,44
mvv 16,224,44
mvv 20,228,44
s_waitcnt lgkmcnt(0)
mvv 12,232,48
mvv 16,236,48
mvv 20,240,48
mvv 12,244,52
mvv 16,248,52
mvv 20,252,52
s_nop 5
v_cvt_pk_bf16_f32 v62, v12, v13
v_cvt_pk_bf16_f32 v63, v14, v15
v_cvt_pk_bf16_f32 v64, v16, v17
v_cvt_pk_bf16_f32 v65, v18, v19
buffer_store_dwordx4 v[62:65], v83, s[36:39], s49 offen sc1
v_cvt_pk_bf16_f32 v66, v20, v21
v_cvt_pk_bf16_f32 v67, v22, v23
v_cvt_pk_bf16_f32 v68, v20, v21
v_cvt_pk_bf16_f32 v69, v22, v23
s_nop 0
v_permlane16_swap_b32 v66, v68
v_permlane16_swap_b32 v67, v69
buffer_store_dwordx4 v[66:69], v84, s[36:39], s49 offen sc1
v_mul_f32 v73, v12, v12
v_add_f32 v72, v13, v12
v_fmac_f32 v73, v13, v13
v_add_f32 v72, v14, v72
v_fmac_f32 v73, v14, v14
v_add_f32 v72, v15, v72
v_fmac_f32 v73, v15, v15
v_add_f32 v72, v16, v72
v_fmac_f32 v73, v16, v16
v_add_f32 v72, v17, v72
v_fmac_f32 v73, v17, v17
v_add_f32 v72, v18, v72
v_fmac_f32 v73, v18, v18
v_add_f32 v72, v19, v72
v_fmac_f32 v73, v19, v19
v_add_f32 v72, v20, v72
v_fmac_f32 v73, v20, v20
v_add_f32 v72, v21, v72
v_fmac_f32 v73, v21, v21
v_add_f32 v72, v22, v72
v_fmac_f32 v73, v22, v22
v_add_f32 v72, v23, v72
v_fmac_f32 v73, v23, v23
s_nop 1
v_permlane32_swap_b32 v72, v73
v_add_f32 v72, v72, v73
v_add_u32 v70, 37, v70
s_waitcnt vmcnt(0)
buffer_store_dwordx2 v[70:71], v81, s[36:39], s52 offen sc1
ds_write_b32 v91, v72 offset:1024
s_add_u32 s49, s49, 346112
s_waitcnt lgkmcnt(0)
s_barrier
s_endpgm
Lr3:
s_cmp_eq_u32 s60, 3
s_cbranch_scc0 Lr4
s_waitcnt vmcnt(0)
s_barrier
v_accvgpr_read_b32 v77, a255
v_mov_b32 v76, 0x100
v_add_u32 v77, 0x9e3779b9, v77
buffer_store_dwordx2 v[76:77], v81, s[36:39], s52 offen sc1
v_add_u32 v88, v88, v76
s_sleep 32
E 0,65536
E 1,69632
E 2,73728
s_add_u32 s51, s51, 229376
E 0,77824
E 1,81920
E 2,86016
s_add_u32 s51, s51, 229376
E 0,90112
E 1,94208
E 2,98304
s_add_u32 s51, s51, 229376
E 0,102400
E 1,106496
E 2,110592
s_add_u32 s51, s51, 229376
s_mov_b64 exec, 0
s_cmp_eq_u32 s53, 0
s_cbranch_scc1 LsF0
s_cmp_eq_u32 s53, 1024
s_cbranch_scc1 LsF1
s_cmp_eq_u32 s53, 2048
s_cbranch_scc1 LsF2
s_branch LsF3
LwF:
s_mov_b64 exec, -1
v_mov_b32 v24, s64
v_mov_b32 v25, s64
v_mov_b32 v26, s64
v_mov_b32 v27, s64
s_waitcnt vmcnt(6)
s_barrier
v_add_u32 v4, 65536, v4
v_add_u32 v5, 65536, v5
v_add_u32 v6, 65536, v6
Ta 0,4,0
Ta 4,5,0
Ta 8,6,0
Ta 12,4,12288
Ta 16,5,12288
Ta 20,6,12288
E 0,114688
E 1,118784
E 2,122880
s_add_u32 s51, s51, 229376
E 0,126976
E 1,131072
E 2,135168
s_add_u32 s51, s51, 229376
E 0,139264
E 1,143360
E 2,147456
s_add_u32 s51, s51, 229376
E 0,151552
E 1,155648
E 2,159744
s_add_u32 s51, s51, 229376
s_cmp_ge_u32 s51, s57
s_cselect_b32 s60, 7340032, 0
s_sub_u32 s51, s51, s60
s_waitcnt lgkmcnt(0)
s_waitcnt vmcnt(12)
s_barrier
E 0,65536
Ta 24,4,24576
v_mfma_f32_16x16x32_bf16 v[56:59], a[0:3], v[24:27], 0
Ta 28,5,24576
E 1,69632
v_mfma_f32_16x16x32_bf16 v[60:63], a[4:7], v[24:27], 0
Ta 32,6,24576
Ta 36,4,36864
E 2,73728
s_add_u32 s51, s51, 229376
v_mfma_f32_16x16x32_bf16 v[64:67], a[8:11], v[24:27], 0
Ta 40,5,36864
E 0,77824
Ta 44,6,36864
mav 56,12,24
E 1,81920
mav 60,16,24
E 2,86016
s_add_u32 s51, s51, 229376
mav 64,20,24
s_waitcnt lgkmcnt(0)
s_waitcnt vmcnt(12)
s_barrier
v_add_u32 v4, 49152, v4
v_add_u32 v5, 49152, v5
v_add_u32 v6, 49152, v6
E 0,90112
Ta 48,4,0
mav 56,24,24
Ta 52,5,0
E 1,94208
mav 60,28,24
Ta 56,6,0
Ta 60,4,12288
E 2,98304
s_add_u32 s51, s51, 229376
mav 64,32,24
Ta 64,5,12288
E 0,102400
Ta 68,6,12288
mav 56,36,24
E 1,106496
mav 60,40,24
E 2,110592
s_add_u32 s51, s51, 229376
mav 64,44,24
s_waitcnt lgkmcnt(0)
s_waitcnt vmcnt(12)
s_barrier
E 0,114688
Ta 72,4,24576
mav 56,48,24
Ta 76,5,24576
E 1,118784
mav 60,52,24
Ta 80,6,24576
Ta 84,4,36864
E 2,122880
s_add_u32 s51, s51, 229376
mav 64,56,24
Ta 88,5,36864
E 0,126976
Ta 92,6,36864
mav 56,60,24
E 1,131072
mav 60,64,24
E 2,135168
s_add_u32 s51, s51, 229376
mav 64,68,24
s_waitcnt lgkmcnt(0)
s_waitcnt vmcnt(12)
s_barrier
v_add_u32 v4, -49152, v4
v_add_u32 v5, -49152, v5
v_add_u32 v6, -49152, v6
E 0,139264
Ta 96,4,0
mav 56,72,24
Ta 100,5,0
E 1,143360
mav 60,76,24
Ta 104,6,0
Ta 108,4,12288
E 2,147456
s_add_u32 s51, s51, 229376
mav 64,80,24
Ta 112,5,12288
E 0,151552
Ta 116,6,12288
mav 56,84,24
E 1,155648
mav 60,88,24
E 2,159744
s_add_u32 s51, s51, 229376
s_cmp_ge_u32 s51, s57
s_cselect_b32 s60, 7340032, 0
s_sub_u32 s51, s51, s60
mav 64,92,24
s_waitcnt lgkmcnt(0)
s_waitcnt vmcnt(12)
s_barrier
E 0,65536
Ta 120,4,24576
mav 56,96,24
Ta 124,5,24576
E 1,69632
mav 60,100,24
Ta 128,6,24576
Ta 132,4,36864
E 2,73728
s_add_u32 s51, s51, 229376
mav 64,104,24
Ta 136,5,36864
E 0,77824
Ta 140,6,36864
mav 56,108,24
E 1,81920
mav 60,112,24
E 2,86016
s_add_u32 s51, s51, 229376
mav 64,116,24
s_waitcnt lgkmcnt(0)
s_waitcnt vmcnt(12)
s_barrier
v_add_u32 v4, 49152, v4
v_add_u32 v5, 49152, v5
v_add_u32 v6, 49152, v6
E 0,90112
Ta 144,4,0
mav 56,120,24
Ta 148,5,0
E 1,94208
mav 60,124,24
Ta 152,6,0
Ta 156,4,12288
E 2,98304
s_add_u32 s51, s51, 229376
mav 64,128,24
Ta 160,5,12288
E 0,102400
Ta 164,6,12288
mav 56,132,24
E 1,106496
mav 60,136,24
E 2,110592
s_add_u32 s51, s51, 229376
mav 64,140,24
s_waitcnt lgkmcnt(0)
s_waitcnt vmcnt(12)
s_barrier
E 0,114688
Ta 168,4,24576
mav 56,144,24
Ta 172,5,24576
E 1,118784
LsF0:
s_cbranch_execz LsF4
mav 60,148,24
Ta 176,6,24576
Ta 180,4,36864
E 2,122880
s_add_u32 s51, s51, 229376
mav 64,152,24
Ta 184,5,36864
E 0,126976
Ta 188,6,36864
mav 56,156,24
E 1,131072
mav 60,160,24
E 2,135168
s_add_u32 s51, s51, 229376
mav 64,164,24
s_waitcnt lgkmcnt(0)
s_waitcnt vmcnt(12)
s_barrier
v_add_u32 v4, -49152, v4
v_add_u32 v5, -49152, v5
v_add_u32 v6, -49152, v6
E 0,139264
Ta 192,4,0
mav 56,168,24
Ta 196,5,0
E 1,143360
mav 60,172,24
Ta 200,6,0
Ta 204,4,12288
E 2,147456
s_add_u32 s51, s51, 229376
mav 64,176,24
Ta 208,5,12288
E 0,151552
Ta 212,6,12288
mav 56,180,24
E 1,155648
mav 60,184,24
E 2,159744
s_add_u32 s51, s51, 229376
s_cmp_ge_u32 s51, s57
s_cselect_b32 s60, 7340032, 0
s_sub_u32 s51, s51, s60
mav 64,188,24
s_waitcnt lgkmcnt(0)
s_waitcnt vmcnt(12)
s_barrier
E 0,65536
Ta 216,4,24576
mav 56,192,24
Ta 220,5,24576
E 1,69632
mav 60,196,24
Ta 224,6,24576
Ta 228,4,36864
E 2,73728
s_add_u32 s51, s51, 229376
mav 64,200,24
Ta 232,5,36864
E 0,77824
Ta 236,6,36864
mav 56,204,24
E 1,81920
mav 60,208,24
E 2,86016
s_add_u32 s51, s51, 229376
mav 64,212,24
s_waitcnt lgkmcnt(0)
s_waitcnt vmcnt(12)
s_barrier
v_add_u32 v4, 49152, v4
v_add_u32 v5, 49152, v5
v_add_u32 v6, 49152, v6
E 0,90112
Ta 240,4,0
mav 56,216,24
Ta 244,5,0
E 1,94208
mav 60,220,24
Ta 248,6,0
Ta 252,4,12288
E 2,98304
s_add_u32 s51, s51, 229376
mav 64,224,24
Tv 128,5,12288
E 0,102400
Tv 132,6,12288
mav 56,228,24
E 1,106496
mav 60,232,24
E 2,110592
s_add_u32 s51, s51, 229376
mav 64,236,24
s_waitcnt lgkmcnt(0)
s_waitcnt vmcnt(12)
s_barrier
E 0,114688
Tv 136,4,24576
mav 56,240,24
Tv 140,5,24576
E 1,118784
mav 60,244,24
Tv 144,6,24576
Tv 148,4,36864
E 2,122880
s_add_u32 s51, s51, 229376
mav 64,248,24
Tv 152,5,36864
E 0,126976
Tv 156,6,36864
mav 56,252,24
E 1,131072
mvv 60,128,24
E 2,135168
s_add_u32 s51, s51, 229376
mvv 64,132,24
s_waitcnt lgkmcnt(0)
s_waitcnt vmcnt(12)
s_barrier
v_add_u32 v4, -49152, v4
v_add_u32 v5, -49152, v5
v_add_u32 v6, -49152, v6
E 0,139264
Tv 160,4,0
mvv 56,136,24
Tv 164,5,0
E 1,143360
mvv 60,140,24
Tv 168,6,0
Tv 172,4,12288
E 2,147456
s_add_u32 s51, s51, 229376
mvv 64,144,24
Tv 176,5,12288
E 0,151552
Tv 180,6,12288
mvv 56,148,24
E 1,155648
mvv 60,152,24
E 2,159744
s_add_u32 s51, s51, 229376
s_cmp_ge_u32 s51, s57
s_cselect_b32 s60, 7340032, 0
s_sub_u32 s51, s51, s60
mvv 64,156,24
s_waitcnt lgkmcnt(0)
s_waitcnt vmcnt(12)
s_barrier
Tv 184,4,24576
mvv 56,160,24
Tv 188,5,24576
mvv 60,164,24
Tv 192,6,24576
Tv 196,4,36864
mvv 64,168,24
Tv 200,5,36864
Tv 204,6,36864
mvv 56,172,24
mvv 60,176,24
mvv 64,180,24
s_waitcnt lgkmcnt(0)
s_waitcnt vmcnt(6)
s_barrier
v_add_u32 v4, 49152, v4
v_add_u32 v5, 49152, v5
v_add_u32 v6, 49152, v6
Tv 208,4,0
mvv 56,184,24
Tv 212,5,0
mvv 60,188,24
Tv 216,6,0
Tv 220,4,12288
mvv 64,192,24
Tv 224,5,12288
Tv 228,6,12288
mvv 56,196,24
mvv 60,200,24
mvv 64,204,24
s_waitcnt lgkmcnt(0)
s_waitcnt vmcnt(0)
s_barrier
Tv 232,4,24576
mvv 56,208,24
Tv 236,5,24576
mvv 60,212,24
Tv 240,6,24576
Tv 244,4,36864
mvv 64,216,24
Tv 248,5,36864
Tv 252,6,36864
mvv 56,220,24
mvv 60,224,24
mvv 64,228,24
s_waitcnt lgkmcnt(0)
mvv 56,232,24
mvv 60,236,24
mvv 64,240,24
mvv 56,244,24
mvv 60,248,24
mvv 64,252,24
s_waitcnt vmcnt(0)
s_barrier
s_nop 7
s_and_b32 s60, s48, 3
s_cmp_eq_u32 s60, 1
s_cbranch_scc1 LvF1
s_cmp_eq_u32 s60, 2
s_cbranch_scc1 LvF2
LvF0:
LsF1:
s_cbranch_execz LsF5
buffer_load_dwordx2 v[106:107], v87, s[36:39], 0 offen sc1
s_waitcnt vmcnt(0)
ds_write_b32 v82, v106
v_sub_u32 v106, v106, v88
v_xor_b32 v107, v107, v77
v_cmp_lt_u64 vcc, s[62:63], v[106:107]
s_waitcnt lgkmcnt(0)
s_cbranch_vccnz CF0_1
BF0_1:
s_add_u32 s49, s49, 346112
s_barrier
s_add_u32 m0, s50, 0
s_nop 0
D 85,0
D 85,1024
D 85,2048
D 85,3072
s_add_u32 m0, s50, 4096
s_nop 0
D 86,0
D 86,1024
D 86,2048
D 86,3072
s_mov_b32 m0, 142336
s_nop 0
buffer_load_dword v87, s[36:39], 0 offen sc1 lds
v_add_u32 v88, 1, v88
ds_read_b32 v106, v82 offset:0
s_waitcnt lgkmcnt(0)
v_cmp_gt_i32 vcc, v88, v106
s_waitcnt vmcnt(1)
s_cbranch_vccnz CF0_2
BF0_2:
s_add_u32 s49, s49, 346112
s_barrier
R 24,0
s_add_u32 m0, s50, 32768
R 28,1024
D 85,0
R 32,2048
D 85,1024
R 36,3072
s_waitcnt lgkmcnt(2)
v_mfma_f32_16x16x32_bf16 v[0:3], a[0:3], v[24:27], 0
R 40,4096
D 85,2048
v_mfma_f32_16x16x32_bf16 v[4:7], a[4:7], v[24:27], 0
v_mfma_f32_16x16x32_bf16 v[8:11], a[8:11], v[24:27], 0
mav 0,12,28
R 44,5120
mav 4,16,28
D 85,3072
mav 8,20,28
s_waitcnt lgkmcnt(2)
mav 0,24,32
R 48,6144
mav 4,28,32
s_add_u32 m0, s50, 36864
mav 8,32,32
mav 0,36,36
R 52,7168
D 86,0
mav 4,40,36
mav 8,44,36
s_waitcnt lgkmcnt(2)
mav 0,48,40
R 24,8192
mav 4,52,40
D 86,1024
mav 8,56,40
mav 0,60,44
R 28,9216
mav 4,64,44
mav 8,68,44
s_waitcnt lgkmcnt(2)
mav 0,72,48
R 32,10240
D 86,2048
mav 4,76,48
mav 8,80,48
mav 0,84,52
R 36,11264
mav 4,88,52
D 86,3072
mav 8,92,52
s_waitcnt lgkmcnt(2)
mav 0,96,24
R 40,12288
mav 4,100,24
mav 8,104,24
mav 0,108,28
R 44,13312
mav 4,112,28
mav 8,116,28
s_waitcnt lgkmcnt(2)
mav 0,120,32
R 48,14336
mav 4,124,32
mav 8,128,32
mav 0,132,36
R 52,15360
mav 4,136,36
s_mov_b32 m0, 142336
mav 8,140,36
s_waitcnt lgkmcnt(2)
mav 0,144,40
R 24,16384
buffer_load_dword v87, s[36:39], 0 offen sc1 lds
mav 4,148,40
mav 8,152,40
mav 0,156,44
R 28,17408
mav 4,160,44
v_add_u32 v88, 1, v88
mav 8,164,44
s_waitcnt lgkmcnt(2)
mav 0,168,48
R 32,18432
mav 4,172,48
mav 8,176,48
mav 0,180,52
R 36,19456
mav 4,184,52
mav 8,188,52
s_waitcnt lgkmcnt(2)
mav 0,192,24
R 40,20480
mav 4,196,24
mav 8,200,24
mav 0,204,28
R 44,21504
mav 4,208,28
mav 8,212,28
s_waitcnt lgkmcnt(2)
mav 0,216,32
R 48,22528
mav 4,220,32
)"
R"(
mav 8,224,32
mav 0,228,36
R 52,23552
mav 4,232,36
mav 8,236,36
s_waitcnt lgkmcnt(2)
mav 0,240,40
R 24,24576
mav 4,244,40
mav 8,248,40
mav 0,252,44
R 28,25600
mvv 4,128,44
mvv 8,132,44
s_waitcnt lgkmcnt(2)
mvv 0,136,48
R 32,26624
mvv 4,140,48
mvv 8,144,48
mvv 0,148,52
R 36,27648
mvv 4,152,52
mvv 8,156,52
s_waitcnt lgkmcnt(2)
mvv 0,160,24
R 40,28672
mvv 4,164,24
ds_read_b32 v106, v82 offset:0
mvv 8,168,24
mvv 0,172,28
R 44,29696
mvv 4,176,28
mvv 8,180,28
s_waitcnt lgkmcnt(3)
mvv 0,184,32
R 48,30720
s_xor_b32 s50, s50, 0x10000
mvv 4,188,32
mvv 8,192,32
mvv 0,196,36
R 52,31744
mvv 4,200,36
s_waitcnt lgkmcnt(2)
v_cmp_gt_i32 vcc, v88, v106
mvv 8,204,36
s_waitcnt vmcnt(1)
s_cbranch_vccnz CF0_3
BF0_3:
mvv 0,208,40
s_barrier
R 24,32768
mvv 4,212,40
s_add_u32 s49, s49, 346112
s_add_u32 m0, s50, 0
mvv 8,216,40
ds_read_b64 v[78:79], v89 offset:0
mvv 0,220,44
R 28,33792
D 85,0
mvv 4,224,44
mvv 8,228,44
s_waitcnt lgkmcnt(3)
mvv 0,232,48
R 32,34816
mvv 4,236,48
D 85,1024
mvv 8,240,48
mvv 0,244,52
R 36,35840
mvv 4,248,52
mvv 8,252,52
s_waitcnt lgkmcnt(2)
v_mfma_f32_16x16x32_bf16 v[12:15], a[0:3], v[24:27], 0
R 40,36864
D 85,2048
v_mfma_f32_16x16x32_bf16 v[16:19], a[4:7], v[24:27], 0
v_fma_f32 v104, v79, v56, v92
v_fma_f32 v0, v0, v78, v104
v_mfma_f32_16x16x32_bf16 v[20:23], a[8:11], v[24:27], 0
v_fma_f32 v105, v79, v57, v93
v_fma_f32 v1, v1, v78, v105
mav 12,12,28
R 44,37888
v_mul_f32_e64 v0, v0, |v0|
mav 16,16,28
D 85,3072
v_mul_f32_e64 v1, v1, |v1|
mav 20,20,28
v_cvt_pk_bf16_f32 v0, v0, v1
s_waitcnt lgkmcnt(2)
mav 12,24,32
R 48,38912
v_pk_max_i16 v68, v0, 0
mav 16,28,32
s_add_u32 m0, s50, 4096
v_fma_f32 v104, v79, v58, v94
mav 20,32,32
v_fma_f32 v2, v2, v78, v104
v_fma_f32 v105, v79, v59, v95
mav 12,36,36
R 52,39936
D 86,0
mav 16,40,36
v_fma_f32 v3, v3, v78, v105
v_mul_f32_e64 v2, v2, |v2|
LsF2:
s_cbranch_execz LsF6
mav 20,44,36
v_mul_f32_e64 v3, v3, |v3|
s_waitcnt lgkmcnt(2)
mav 12,48,40
R 24,40960
v_cvt_pk_bf16_f32 v2, v2, v3
mav 16,52,40
D 86,1024
v_pk_max_i16 v69, v2, 0
mav 20,56,40
v_fma_f32 v104, v79, v60, v96
v_fma_f32 v4, v4, v78, v104
mav 12,60,44
R 28,41984
v_fma_f32 v105, v79, v61, v97
mav 16,64,44
v_fma_f32 v5, v5, v78, v105
v_mul_f32_e64 v4, v4, |v4|
mav 20,68,44
v_mul_f32_e64 v5, v5, |v5|
s_waitcnt lgkmcnt(2)
mav 12,72,48
R 32,43008
D 86,2048
mav 16,76,48
v_cvt_pk_bf16_f32 v4, v4, v5
v_pk_max_i16 v70, v4, 0
mav 20,80,48
v_fma_f32 v104, v79, v62, v98
v_fma_f32 v6, v6, v78, v104
mav 12,84,52
R 36,44032
v_fma_f32 v105, v79, v63, v99
mav 16,88,52
D 86,3072
v_fma_f32 v7, v7, v78, v105
mav 20,92,52
v_mul_f32_e64 v6, v6, |v6|
s_waitcnt lgkmcnt(2)
mav 12,96,24
R 40,45056
v_mul_f32_e64 v7, v7, |v7|
mav 16,100,24
v_cvt_pk_bf16_f32 v6, v6, v7
v_pk_max_i16 v71, v6, 0
mav 20,104,24
buffer_store_dwordx4 v[68:71], v83, s[36:39], s49 offen sc1
v_fma_f32 v104, v79, v64, v100
mav 12,108,28
R 44,46080
v_fma_f32 v8, v8, v78, v104
mav 16,112,28
v_fma_f32 v105, v79, v65, v101
v_fma_f32 v9, v9, v78, v105
mav 20,116,28
v_mul_f32_e64 v8, v8, |v8|
s_waitcnt lgkmcnt(2)
mav 12,120,32
R 48,47104
v_mul_f32_e64 v9, v9, |v9|
mav 16,124,32
v_cvt_pk_bf16_f32 v8, v8, v9
v_pk_max_i16 v72, v8, 0
mav 20,128,32
v_pk_max_i16 v74, v8, 0
v_fma_f32 v104, v79, v66, v102
mav 12,132,36
R 52,48128
v_fma_f32 v10, v10, v78, v104
mav 16,136,36
s_mov_b32 m0, 142336
v_fma_f32 v105, v79, v67, v103
mav 20,140,36
v_fma_f32 v11, v11, v78, v105
s_waitcnt lgkmcnt(2)
mav 12,144,40
R 24,49152
buffer_load_dword v87, s[36:39], 0 offen sc1 lds
mav 16,148,40
v_mul_f32_e64 v10, v10, |v10|
v_mul_f32_e64 v11, v11, |v11|
mav 20,152,40
v_cvt_pk_bf16_f32 v10, v10, v11
v_pk_max_i16 v73, v10, 0
mav 12,156,44
R 28,50176
v_pk_max_i16 v75, v10, 0
mav 16,160,44
v_permlane16_swap_b32 v72, v74
v_permlane16_swap_b32 v73, v75
mav 20,164,44
buffer_store_dwordx4 v[72:75], v84, s[36:39], s49 offen sc1
s_waitcnt lgkmcnt(2)
mav 12,168,48
R 32,51200
v_add_u32 v88, 1, v88
mav 16,172,48
mav 20,176,48
mav 12,180,52
R 36,52224
mav 16,184,52
mav 20,188,52
s_waitcnt lgkmcnt(2)
mav 12,192,24
R 40,53248
mav 16,196,24
mav 20,200,24
mav 12,204,28
R 44,54272
mav 16,208,28
mav 20,212,28
s_waitcnt lgkmcnt(2)
mav 12,216,32
R 48,55296
mav 16,220,32
mav 20,224,32
mav 12,228,36
R 52,56320
mav 16,232,36
mav 20,236,36
s_waitcnt lgkmcnt(2)
mav 12,240,40
R 24,57344
mav 16,244,40
mav 20,248,40
mav 12,252,44
R 28,58368
mvv 16,128,44
mvv 20,132,44
s_waitcnt lgkmcnt(2)
mvv 12,136,48
R 32,59392
mvv 16,140,48
mvv 20,144,48
mvv 12,148,52
R 36,60416
mvv 16,152,52
mvv 20,156,52
s_waitcnt lgkmcnt(2)
mvv 12,160,24
R 40,61440
mvv 16,164,24
ds_read_b32 v106, v82 offset:0
mvv 20,168,24
mvv 12,172,28
R 44,62464
mvv 16,176,28
v_add_u32 v76, 1, v76
mvv 20,180,28
s_waitcnt lgkmcnt(3)
mvv 12,184,32
R 48,63488
mvv 16,188,32
mvv 20,192,32
mvv 12,196,36
R 52,64512
v_xor_b32 v80, 0x10000, v80
mvv 16,200,36
s_waitcnt lgkmcnt(2)
v_cmp_gt_i32 vcc, v88, v106
mvv 20,204,36
s_waitcnt vmcnt(3)
s_cbranch_vccnz CF0_4
BF0_4:
mvv 12,208,40
s_barrier
R 24,0
mvv 16,212,40
s_add_u32 s49, s49, 346112
s_add_u32 m0, s50, 32768
mvv 20,216,40
ds_read_b64 v[78:79], v89 offset:128
mvv 12,220,44
R 28,1024
D 85,0
mvv 16,224,44
mvv 20,228,44
s_waitcnt lgkmcnt(3)
mvv 12,232,48
R 32,2048
mvv 16,236,48
D 85,1024
mvv 20,240,48
mvv 12,244,52
R 36,3072
mvv 16,248,52
mvv 20,252,52
s_mov_b32 s60, -30
.p2align 6
LF0_5:
s_waitcnt lgkmcnt(2)
v_mfma_f32_16x16x32_bf16 v[0:3], a[0:3], v[24:27], 0
R 40,4096
D 85,2048
v_mfma_f32_16x16x32_bf16 v[4:7], a[4:7], v[24:27], 0
v_fma_f32 v104, v79, v56, v92
v_fma_f32 v12, v12, v78, v104
v_mfma_f32_16x16x32_bf16 v[8:11], a[8:11], v[24:27], 0
v_fma_f32 v105, v79, v57, v93
v_fma_f32 v13, v13, v78, v105
mav 0,12,28
R 44,5120
v_mul_f32_e64 v12, v12, |v12|
mav 4,16,28
s_waitcnt vmcnt(3)
buffer_store_dwordx2 v[76:77], v81, s[36:39], s52 offen sc1
mav 8,20,28
D 85,3072
s_waitcnt lgkmcnt(2)
mav 0,24,32
R 48,6144
v_mul_f32_e64 v13, v13, |v13|
mav 4,28,32
s_add_u32 m0, s50, 36864
v_cvt_pk_bf16_f32 v12, v12, v13
mav 8,32,32
v_pk_max_i16 v68, v12, 0
v_fma_f32 v104, v79, v58, v94
mav 0,36,36
R 52,7168
D 86,0
mav 4,40,36
v_fma_f32 v14, v14, v78, v104
v_fma_f32 v105, v79, v59, v95
mav 8,44,36
v_fma_f32 v15, v15, v78, v105
s_waitcnt lgkmcnt(2)
mav 0,48,40
R 24,8192
v_mul_f32_e64 v14, v14, |v14|
mav 4,52,40
D 86,1024
v_mul_f32_e64 v15, v15, |v15|
mav 8,56,40
v_cvt_pk_bf16_f32 v14, v14, v15
v_pk_max_i16 v69, v14, 0
mav 0,60,44
R 28,9216
v_fma_f32 v104, v79, v60, v96
mav 4,64,44
v_fma_f32 v16, v16, v78, v104
v_fma_f32 v105, v79, v61, v97
mav 8,68,44
v_fma_f32 v17, v17, v78, v105
s_waitcnt lgkmcnt(2)
mav 0,72,48
R 32,10240
D 86,2048
mav 4,76,48
v_mul_f32_e64 v16, v16, |v16|
v_mul_f32_e64 v17, v17, |v17|
mav 8,80,48
v_cvt_pk_bf16_f32 v16, v16, v17
v_pk_max_i16 v70, v16, 0
mav 0,84,52
R 36,11264
v_fma_f32 v104, v79, v62, v98
mav 4,88,52
D 86,3072
v_fma_f32 v18, v18, v78, v104
mav 8,92,52
v_fma_f32 v105, v79, v63, v99
s_waitcnt lgkmcnt(2)
mav 0,96,24
R 40,12288
v_fma_f32 v19, v19, v78, v105
mav 4,100,24
v_mul_f32_e64 v18, v18, |v18|
v_mul_f32_e64 v19, v19, |v19|
mav 8,104,24
v_cvt_pk_bf16_f32 v18, v18, v19
v_pk_max_i16 v71, v18, 0
mav 0,108,28
R 44,13312
buffer_store_dwordx4 v[68:71], v83, s[36:39], s49 offen sc1
mav 4,112,28
v_fma_f32 v104, v79, v64, v100
v_fma_f32 v20, v20, v78, v104
mav 8,116,28
v_fma_f32 v105, v79, v65, v101
s_waitcnt lgkmcnt(2)
mav 0,120,32
R 48,14336
v_fma_f32 v21, v21, v78, v105
mav 4,124,32
v_mul_f32_e64 v20, v20, |v20|
v_mul_f32_e64 v21, v21, |v21|
mav 8,128,32
v_cvt_pk_bf16_f32 v20, v20, v21
v_pk_max_i16 v72, v20, 0
mav 0,132,36
R 52,15360
v_pk_max_i16 v74, v20, 0
mav 4,136,36
s_mov_b32 m0, 142336
v_fma_f32 v104, v79, v66, v102
mav 8,140,36
v_fma_f32 v22, v22, v78, v104
s_waitcnt lgkmcnt(2)
mav 0,144,40
R 24,16384
buffer_load_dword v87, s[36:39], 0 offen sc1 lds
mav 4,148,40
v_fma_f32 v105, v79, v67, v103
v_fma_f32 v23, v23, v78, v105
mav 8,152,40
v_mul_f32_e64 v22, v22, |v22|
v_mul_f32_e64 v23, v23, |v23|
mav 0,156,44
R 28,17408
v_cvt_pk_bf16_f32 v22, v22, v23
mav 4,160,44
v_pk_max_i16 v73, v22, 0
v_pk_max_i16 v75, v22, 0
mav 8,164,44
v_permlane16_swap_b32 v72, v74
s_waitcnt lgkmcnt(2)
mav 0,168,48
R 32,18432
v_permlane16_swap_b32 v73, v75
mav 4,172,48
buffer_store_dwordx4 v[72:75], v84, s[36:39], s49 offen sc1
v_add_u32 v88, 1, v88
mav 8,176,48
mav 0,180,52
R 36,19456
mav 4,184,52
mav 8,188,52
s_waitcnt lgkmcnt(2)
mav 0,192,24
R 40,20480
mav 4,196,24
mav 8,200,24
mav 0,204,28
R 44,21504
mav 4,208,28
mav 8,212,28
s_waitcnt lgkmcnt(2)
mav 0,216,32
R 48,22528
mav 4,220,32
mav 8,224,32
mav 0,228,36
R 52,23552
mav 4,232,36
mav 8,236,36
s_waitcnt lgkmcnt(2)
mav 0,240,40
R 24,24576
mav 4,244,40
mav 8,248,40
mav 0,252,44
R 28,25600
mvv 4,128,44
mvv 8,132,44
s_waitcnt lgkmcnt(2)
mvv 0,136,48
R 32,26624
mvv 4,140,48
mvv 8,144,48
mvv 0,148,52
R 36,27648
mvv 4,152,52
mvv 8,156,52
s_waitcnt lgkmcnt(2)
mvv 0,160,24
R 40,28672
mvv 4,164,24
ds_read_b32 v106, v82 offset:0
mvv 8,168,24
mvv 0,172,28
R 44,29696
mvv 4,176,28
v_add_u32 v76, 1, v76
mvv 8,180,28
s_waitcnt lgkmcnt(3)
mvv 0,184,32
R 48,30720
s_xor_b32 s50, s50, 0x10000
mvv 4,188,32
mvv 8,192,32
mvv 0,196,36
R 52,31744
mvv 4,200,36
s_waitcnt lgkmcnt(2)
v_cmp_gt_i32 vcc, v88, v106
mvv 8,204,36
s_waitcnt vmcnt(3)
s_cbranch_vccnz CF0_6
BF0_6:
mvv 0,208,40
s_barrier
R 24,32768
mvv 4,212,40
s_add_u32 s49, s49, 346112
s_add_u32 m0, s50, 0
mvv 8,216,40
ds_read_b64 v[78:79], v89 offset:0
mvv 0,220,44
R 28,33792
D 85,0
mvv 4,224,44
mvv 8,228,44
s_waitcnt lgkmcnt(3)
mvv 0,232,48
R 32,34816
mvv 4,236,48
D 85,1024
mvv 8,240,48
mvv 0,244,52
R 36,35840
mvv 4,248,52
mvv 8,252,52
s_waitcnt lgkmcnt(2)
v_mfma_f32_16x16x32_bf16 v[12:15], a[0:3], v[24:27], 0
R 40,36864
D 85,2048
v_mfma_f32_16x16x32_bf16 v[16:19], a[4:7], v[24:27], 0
v_fma_f32 v104, v79, v56, v92
v_fma_f32 v0, v0, v78, v104
v_mfma_f32_16x16x32_bf16 v[20:23], a[8:11], v[24:27], 0
v_fma_f32 v105, v79, v57, v93
v_fma_f32 v1, v1, v78, v105
mav 12,12,28
R 44,37888
v_mul_f32_e64 v0, v0, |v0|
mav 16,16,28
s_waitcnt vmcnt(3)
buffer_store_dwordx2 v[76:77], v81, s[36:39], s52 offen sc1
mav 20,20,28
D 85,3072
s_waitcnt lgkmcnt(2)
mav 12,24,32
R 48,38912
v_mul_f32_e64 v1, v1, |v1|
mav 16,28,32
s_add_u32 m0, s50, 4096
v_cvt_pk_bf16_f32 v0, v0, v1
mav 20,32,32
v_pk_max_i16 v68, v0, 0
v_fma_f32 v104, v79, v58, v94
mav 12,36,36
R 52,39936
D 86,0
mav 16,40,36
v_fma_f32 v2, v2, v78, v104
v_fma_f32 v105, v79, v59, v95
mav 20,44,36
v_fma_f32 v3, v3, v78, v105
s_waitcnt lgkmcnt(2)
mav 12,48,40
R 24,40960
v_mul_f32_e64 v2, v2, |v2|
mav 16,52,40
D 86,1024
v_mul_f32_e64 v3, v3, |v3|
mav 20,56,40
v_cvt_pk_bf16_f32 v2, v2, v3
v_pk_max_i16 v69, v2, 0
mav 12,60,44
R 28,41984
v_fma_f32 v104, v79, v60, v96
mav 16,64,44
v_fma_f32 v4, v4, v78, v104
v_fma_f32 v105, v79, v61, v97
mav 20,68,44
v_fma_f32 v5, v5, v78, v105
s_waitcnt lgkmcnt(2)
mav 12,72,48
R 32,43008
D 86,2048
mav 16,76,48
v_mul_f32_e64 v4, v4, |v4|
v_mul_f32_e64 v5, v5, |v5|
mav 20,80,48
v_cvt_pk_bf16_f32 v4, v4, v5
v_pk_max_i16 v70, v4, 0
mav 12,84,52
R 36,44032
v_fma_f32 v104, v79, v62, v98
mav 16,88,52
D 86,3072
v_fma_f32 v6, v6, v78, v104
mav 20,92,52
v_fma_f32 v105, v79, v63, v99
s_waitcnt lgkmcnt(2)
mav 12,96,24
R 40,45056
v_fma_f32 v7, v7, v78, v105
mav 16,100,24
v_mul_f32_e64 v6, v6, |v6|
v_mul_f32_e64 v7, v7, |v7|
mav 20,104,24
v_cvt_pk_bf16_f32 v6, v6, v7
v_pk_max_i16 v71, v6, 0
mav 12,108,28
R 44,46080
buffer_store_dwordx4 v[68:71], v83, s[36:39], s49 offen sc1
mav 16,112,28
v_fma_f32 v104, v79, v64, v100
v_fma_f32 v8, v8, v78, v104
mav 20,116,28
v_fma_f32 v105, v79, v65, v101
s_waitcnt lgkmcnt(2)
mav 12,120,32
R 48,47104
v_fma_f32 v9, v9, v78, v105
mav 16,124,32
v_mul_f32_e64 v8, v8, |v8|
v_mul_f32_e64 v9, v9, |v9|
mav 20,128,32
v_cvt_pk_bf16_f32 v8, v8, v9
v_pk_max_i16 v72, v8, 0
mav 12,132,36
R 52,48128
v_pk_max_i16 v74, v8, 0
mav 16,136,36
s_mov_b32 m0, 142336
v_fma_f32 v104, v79, v66, v102
mav 20,140,36
v_fma_f32 v10, v10, v78, v104
s_waitcnt lgkmcnt(2)
mav 12,144,40
R 24,49152
buffer_load_dword v87, s[36:39], 0 offen sc1 lds
mav 16,148,40
v_fma_f32 v105, v79, v67, v103
v_fma_f32 v11, v11, v78, v105
mav 20,152,40
v_mul_f32_e64 v10, v10, |v10|
v_mul_f32_e64 v11, v11, |v11|
mav 12,156,44
R 28,50176
v_cvt_pk_bf16_f32 v10, v10, v11
mav 16,160,44
v_pk_max_i16 v73, v10, 0
LsF3:
s_cbranch_execz LsF7
v_pk_max_i16 v75, v10, 0
mav 20,164,44
v_permlane16_swap_b32 v72, v74
s_waitcnt lgkmcnt(2)
mav 12,168,48
R 32,51200
v_permlane16_swap_b32 v73, v75
mav 16,172,48
buffer_store_dwordx4 v[72:75], v84, s[36:39], s49 offen sc1
v_add_u32 v88, 1, v88
mav 20,176,48
mav 12,180,52
R 36,52224
mav 16,184,52
mav 20,188,52
s_waitcnt lgkmcnt(2)
mav 12,192,24
R 40,53248
mav 16,196,24
mav 20,200,24
mav 12,204,28
R 44,54272
mav 16,208,28
mav 20,212,28
s_waitcnt lgkmcnt(2)
mav 12,216,32
R 48,55296
mav 16,220,32
mav 20,224,32
mav 12,228,36
R 52,56320
mav 16,232,36
mav 20,236,36
s_waitcnt lgkmcnt(2)
mav 12,240,40
R 24,57344
mav 16,244,40
mav 20,248,40
mav 12,252,44
R 28,58368
mvv 16,128,44
mvv 20,132,44
s_waitcnt lgkmcnt(2)
mvv 12,136,48
R 32,59392
mvv 16,140,48
mvv 20,144,48
mvv 12,148,52
R 36,60416
mvv 16,152,52
mvv 20,156,52
s_waitcnt lgkmcnt(2)
mvv 12,160,24
R 40,61440
mvv 16,164,24
ds_read_b32 v106, v82 offset:0
mvv 20,168,24
mvv 12,172,28
R 44,62464
mvv 16,176,28
v_add_u32 v76, 1, v76
mvv 20,180,28
s_waitcnt lgkmcnt(3)
mvv 12,184,32
R 48,63488
mvv 16,188,32
mvv 20,192,32
mvv 12,196,36
R 52,64512
v_xor_b32 v80, 0x10000, v80
mvv 16,200,36
s_waitcnt lgkmcnt(2)
v_cmp_gt_i32 vcc, v88, v106
mvv 20,204,36
s_waitcnt vmcnt(3)
s_cbranch_vccnz CF0_7
BF0_7:
mvv 12,208,40
s_barrier
R 24,0
mvv 16,212,40
s_add_u32 s49, s49, 346112
s_add_u32 m0, s50, 32768
mvv 20,216,40
ds_read_b64 v[78:79], v89 offset:128
mvv 12,220,44
R 28,1024
D 85,0
mvv 16,224,44
mvv 20,228,44
s_waitcnt lgkmcnt(3)
mvv 12,232,48
R 32,2048
mvv 16,236,48
D 85,1024
mvv 20,240,48
mvv 12,244,52
R 36,3072
mvv 16,248,52
mvv 20,252,52
s_add_u32 s60, s60, 1
s_cbranch_scc0 LF0_5
s_waitcnt lgkmcnt(2)
v_mfma_f32_16x16x32_bf16 v[0:3], a[0:3], v[24:27], 0
R 40,4096
D 85,2048
v_mfma_f32_16x16x32_bf16 v[4:7], a[4:7], v[24:27], 0
v_fma_f32 v104, v79, v56, v92
v_fma_f32 v12, v12, v78, v104
v_mfma_f32_16x16x32_bf16 v[8:11], a[8:11], v[24:27], 0
v_fma_f32 v105, v79, v57, v93
v_fma_f32 v13, v13, v78, v105
mav 0,12,28
R 44,5120
v_mul_f32_e64 v12, v12, |v12|
mav 4,16,28
s_waitcnt vmcnt(3)
buffer_store_dwordx2 v[76:77], v81, s[36:39], s52 offen sc1
mav 8,20,28
D 85,3072
s_waitcnt lgkmcnt(2)
mav 0,24,32
R 48,6144
v_mul_f32_e64 v13, v13, |v13|
mav 4,28,32
s_add_u32 m0, s50, 36864
v_cvt_pk_bf16_f32 v12, v12, v13
mav 8,32,32
v_pk_max_i16 v68, v12, 0
v_fma_f32 v104, v79, v58, v94
mav 0,36,36
R 52,7168
D 86,0
mav 4,40,36
v_fma_f32 v14, v14, v78, v104
v_fma_f32 v105, v79, v59, v95
mav 8,44,36
v_fma_f32 v15, v15, v78, v105
s_waitcnt lgkmcnt(2)
mav 0,48,40
R 24,8192
v_mul_f32_e64 v14, v14, |v14|
mav 4,52,40
D 86,1024
v_mul_f32_e64 v15, v15, |v15|
mav 8,56,40
v_cvt_pk_bf16_f32 v14, v14, v15
v_pk_max_i16 v69, v14, 0
mav 0,60,44
R 28,9216
v_fma_f32 v104, v79, v60, v96
mav 4,64,44
v_fma_f32 v16, v16, v78, v104
v_fma_f32 v105, v79, v61, v97
mav 8,68,44
v_fma_f32 v17, v17, v78, v105
s_waitcnt lgkmcnt(2)
mav 0,72,48
R 32,10240
D 86,2048
mav 4,76,48
v_mul_f32_e64 v16, v16, |v16|
v_mul_f32_e64 v17, v17, |v17|
mav 8,80,48
v_cvt_pk_bf16_f32 v16, v16, v17
v_pk_max_i16 v70, v16, 0
mav 0,84,52
R 36,11264
v_fma_f32 v104, v79, v62, v98
mav 4,88,52
D 86,3072
v_fma_f32 v18, v18, v78, v104
mav 8,92,52
v_fma_f32 v105, v79, v63, v99
s_waitcnt lgkmcnt(2)
mav 0,96,24
R 40,12288
v_fma_f32 v19, v19, v78, v105
mav 4,100,24
v_mul_f32_e64 v18, v18, |v18|
v_mul_f32_e64 v19, v19, |v19|
mav 8,104,24
v_cvt_pk_bf16_f32 v18, v18, v19
v_pk_max_i16 v71, v18, 0
mav 0,108,28
R 44,13312
buffer_store_dwordx4 v[68:71], v83, s[36:39], s49 offen sc1
mav 4,112,28
v_fma_f32 v104, v79, v64, v100
v_fma_f32 v20, v20, v78, v104
mav 8,116,28
v_fma_f32 v105, v79, v65, v101
s_waitcnt lgkmcnt(2)
mav 0,120,32
R 48,14336
v_fma_f32 v21, v21, v78, v105
mav 4,124,32
v_mul_f32_e64 v20, v20, |v20|
v_mul_f32_e64 v21, v21, |v21|
mav 8,128,32
v_cvt_pk_bf16_f32 v20, v20, v21
v_pk_max_i16 v72, v20, 0
mav 0,132,36
R 52,15360
v_pk_max_i16 v74, v20, 0
mav 4,136,36
s_mov_b32 m0, 142336
v_fma_f32 v104, v79, v66, v102
mav 8,140,36
v_fma_f32 v22, v22, v78, v104
s_waitcnt lgkmcnt(2)
mav 0,144,40
R 24,16384
buffer_load_dword v87, s[36:39], 0 offen sc1 lds
mav 4,148,40
v_fma_f32 v105, v79, v67, v103
v_fma_f32 v23, v23, v78, v105
mav 8,152,40
v_mul_f32_e64 v22, v22, |v22|
v_mul_f32_e64 v23, v23, |v23|
mav 0,156,44
R 28,17408
v_cvt_pk_bf16_f32 v22, v22, v23
mav 4,160,44
v_pk_max_i16 v73, v22, 0
v_pk_max_i16 v75, v22, 0
mav 8,164,44
v_permlane16_swap_b32 v72, v74
s_waitcnt lgkmcnt(2)
mav 0,168,48
R 32,18432
v_permlane16_swap_b32 v73, v75
mav 4,172,48
buffer_store_dwordx4 v[72:75], v84, s[36:39], s49 offen sc1
v_add_u32 v88, 1, v88
mav 8,176,48
mav 0,180,52
R 36,19456
mav 4,184,52
mav 8,188,52
s_waitcnt lgkmcnt(2)
mav 0,192,24
R 40,20480
mav 4,196,24
mav 8,200,24
mav 0,204,28
R 44,21504
mav 4,208,28
mav 8,212,28
s_waitcnt lgkmcnt(2)
mav 0,216,32
R 48,22528
mav 4,220,32
mav 8,224,32
mav 0,228,36
R 52,23552
mav 4,232,36
mav 8,236,36
s_waitcnt lgkmcnt(2)
mav 0,240,40
R 24,24576
mav 4,244,40
mav 8,248,40
mav 0,252,44
R 28,25600
mvv 4,128,44
mvv 8,132,44
s_waitcnt lgkmcnt(2)
mvv 0,136,48
R 32,26624
mvv 4,140,48
mvv 8,144,48
mvv 0,148,52
R 36,27648
mvv 4,152,52
mvv 8,156,52
s_waitcnt lgkmcnt(2)
mvv 0,160,24
R 40,28672
mvv 4,164,24
ds_read_b32 v106, v82 offset:0
mvv 8,168,24
mvv 0,172,28
R 44,29696
mvv 4,176,28
v_add_u32 v76, 1, v76
mvv 8,180,28
s_waitcnt lgkmcnt(3)
mvv 0,184,32
R 48,30720
mvv 4,188,32
mvv 8,192,32
mvv 0,196,36
R 52,31744
mvv 4,200,36
s_waitcnt lgkmcnt(2)
v_cmp_gt_i32 vcc, v88, v106
mvv 8,204,36
s_waitcnt vmcnt(3)
s_cbranch_vccnz CF0_8
BF0_8:
mvv 0,208,40
s_barrier
R 24,32768
mvv 4,212,40
s_add_u32 s49, s49, 346112
ds_read_b64 v[78:79], v89 offset:0
mvv 8,216,40
mvv 0,220,44
R 28,33792
mvv 4,224,44
mvv 8,228,44
s_waitcnt lgkmcnt(3)
mvv 0,232,48
R 32,34816
mvv 4,236,48
mvv 8,240,48
mvv 0,244,52
R 36,35840
mvv 4,248,52
mvv 8,252,52
s_waitcnt lgkmcnt(2)
v_mfma_f32_16x16x32_bf16 v[12:15], a[0:3], v[24:27], 0
R 40,36864
v_fma_f32 v104, v79, v56, v92
v_mfma_f32_16x16x32_bf16 v[16:19], a[4:7], v[24:27], 0
v_fma_f32 v0, v0, v78, v104
v_fma_f32 v105, v79, v57, v93
v_mfma_f32_16x16x32_bf16 v[20:23], a[8:11], v[24:27], 0
v_fma_f32 v1, v1, v78, v105
v_mul_f32_e64 v0, v0, |v0|
mav 12,12,28
R 44,37888
v_mul_f32_e64 v1, v1, |v1|
mav 16,16,28
s_waitcnt vmcnt(0)
buffer_store_dwordx2 v[76:77], v81, s[36:39], s52 offen sc1
mav 20,20,28
v_cvt_pk_bf16_f32 v0, v0, v1
s_waitcnt lgkmcnt(2)
mav 12,24,32
R 48,38912
v_pk_max_i16 v68, v0, 0
mav 16,28,32
v_fma_f32 v104, v79, v58, v94
v_fma_f32 v2, v2, v78, v104
mav 20,32,32
v_fma_f32 v105, v79, v59, v95
v_fma_f32 v3, v3, v78, v105
mav 12,36,36
R 52,39936
v_mul_f32_e64 v2, v2, |v2|
mav 16,40,36
v_mul_f32_e64 v3, v3, |v3|
v_cvt_pk_bf16_f32 v2, v2, v3
mav 20,44,36
v_pk_max_i16 v69, v2, 0
s_waitcnt lgkmcnt(2)
mav 12,48,40
R 24,40960
v_fma_f32 v104, v79, v60, v96
mav 16,52,40
v_fma_f32 v4, v4, v78, v104
v_fma_f32 v105, v79, v61, v97
mav 20,56,40
v_fma_f32 v5, v5, v78, v105
v_mul_f32_e64 v4, v4, |v4|
mav 12,60,44
R 28,41984
v_mul_f32_e64 v5, v5, |v5|
mav 16,64,44
v_cvt_pk_bf16_f32 v4, v4, v5
v_pk_max_i16 v70, v4, 0
mav 20,68,44
v_fma_f32 v104, v79, v62, v98
s_waitcnt lgkmcnt(2)
mav 12,72,48
R 32,43008
v_fma_f32 v6, v6, v78, v104
mav 16,76,48
v_fma_f32 v105, v79, v63, v99
v_fma_f32 v7, v7, v78, v105
mav 20,80,48
v_mul_f32_e64 v6, v6, |v6|
v_mul_f32_e64 v7, v7, |v7|
mav 12,84,52
R 36,44032
v_cvt_pk_bf16_f32 v6, v6, v7
mav 16,88,52
v_pk_max_i16 v71, v6, 0
buffer_store_dwordx4 v[68:71], v83, s[36:39], s49 offen sc1
mav 20,92,52
v_fma_f32 v104, v79, v64, v100
s_waitcnt lgkmcnt(2)
mav 12,96,24
R 40,45056
v_fma_f32 v8, v8, v78, v104
mav 16,100,24
v_fma_f32 v105, v79, v65, v101
v_fma_f32 v9, v9, v78, v105
mav 20,104,24
v_mul_f32_e64 v8, v8, |v8|
v_mul_f32_e64 v9, v9, |v9|
mav 12,108,28
R 44,46080
v_cvt_pk_bf16_f32 v8, v8, v9
mav 16,112,28
v_pk_max_i16 v72, v8, 0
v_pk_max_i16 v74, v8, 0
mav 20,116,28
v_fma_f32 v104, v79, v66, v102
s_waitcnt lgkmcnt(2)
mav 12,120,32
R 48,47104
v_fma_f32 v10, v10, v78, v104
mav 16,124,32
v_fma_f32 v105, v79, v67, v103
v_fma_f32 v11, v11, v78, v105
mav 20,128,32
v_mul_f32_e64 v10, v10, |v10|
v_mul_f32_e64 v11, v11, |v11|
mav 12,132,36
R 52,48128
v_cvt_pk_bf16_f32 v10, v10, v11
mav 16,136,36
v_pk_max_i16 v73, v10, 0
v_pk_max_i16 v75, v10, 0
mav 20,140,36
v_permlane16_swap_b32 v72, v74
s_waitcnt lgkmcnt(2)
mav 12,144,40
R 24,49152
v_permlane16_swap_b32 v73, v75
mav 16,148,40
buffer_store_dwordx4 v[72:75], v84, s[36:39], s49 offen sc1
mav 20,152,40
mav 12,156,44
R 28,50176
mav 16,160,44
mav 20,164,44
s_waitcnt lgkmcnt(2)
mav 12,168,48
R 32,51200
mav 16,172,48
mav 20,176,48
mav 12,180,52
R 36,52224
mav 16,184,52
mav 20,188,52
s_waitcnt lgkmcnt(2)
mav 12,192,24
R 40,53248
mav 16,196,24
mav 20,200,24
mav 12,204,28
R 44,54272
mav 16,208,28
mav 20,212,28
s_waitcnt lgkmcnt(2)
mav 12,216,32
R 48,55296
mav 16,220,32
mav 20,224,32
mav 12,228,36
R 52,56320
mav 16,232,36
mav 20,236,36
s_waitcnt lgkmcnt(2)
mav 12,240,40
R 24,57344
mav 16,244,40
mav 20,248,40
mav 12,252,44
R 28,58368
mvv 16,128,44
mvv 20,132,44
s_waitcnt lgkmcnt(2)
mvv 12,136,48
R 32,59392
mvv 16,140,48
mvv 20,144,48
mvv 12,148,52
R 36,60416
mvv 16,152,52
mvv 20,156,52
s_waitcnt lgkmcnt(2)
mvv 12,160,24
R 40,61440
mvv 16,164,24
mvv 20,168,24
mvv 12,172,28
R 44,62464
mvv 16,176,28
v_add_u32 v76, 1, v76
mvv 20,180,28
s_waitcnt lgkmcnt(2)
mvv 12,184,32
R 48,63488
mvv 16,188,32
mvv 20,192,32
mvv 12,196,36
R 52,64512
mvv 16,200,36
s_add_u32 s49, s49, 346112
mvv 20,204,36
s_barrier
s_waitcnt lgkmcnt(2)
mvv 12,208,40
mvv 16,212,40
ds_read_b64 v[78:79], v89 offset:128
mvv 20,216,40
mvv 12,220,44
mvv 16,224,44
mvv 20,228,44
s_waitcnt lgkmcnt(1)
mvv 12,232,48
mvv 16,236,48
mvv 20,240,48
mvv 12,244,52
mvv 16,248,52
mvv 20,252,52
s_waitcnt lgkmcnt(0)
v_fma_f32 v104, v79, v56, v92
s_nop 3
v_fma_f32 v12, v12, v78, v104
v_fma_f32 v105, v79, v57, v93
v_fma_f32 v13, v13, v78, v105
v_mul_f32_e64 v12, v12, |v12|
v_mul_f32_e64 v13, v13, |v13|
v_cvt_pk_bf16_f32 v12, v12, v13
v_pk_max_i16 v68, v12, 0
v_fma_f32 v104, v79, v58, v94
v_fma_f32 v14, v14, v78, v104
v_fma_f32 v105, v79, v59, v95
v_fma_f32 v15, v15, v78, v105
v_mul_f32_e64 v14, v14, |v14|
v_mul_f32_e64 v15, v15, |v15|
v_cvt_pk_bf16_f32 v14, v14, v15
v_pk_max_i16 v69, v14, 0
v_fma_f32 v104, v79, v60, v96
v_fma_f32 v16, v16, v78, v104
v_fma_f32 v105, v79, v61, v97
v_fma_f32 v17, v17, v78, v105
v_mul_f32_e64 v16, v16, |v16|
v_mul_f32_e64 v17, v17, |v17|
v_cvt_pk_bf16_f32 v16, v16, v17
v_pk_max_i16 v70, v16, 0
LsF4:
s_cbranch_execz LsF8
v_fma_f32 v104, v79, v62, v98
v_fma_f32 v18, v18, v78, v104
v_fma_f32 v105, v79, v63, v99
v_fma_f32 v19, v19, v78, v105
v_mul_f32_e64 v18, v18, |v18|
v_mul_f32_e64 v19, v19, |v19|
v_cvt_pk_bf16_f32 v18, v18, v19
v_pk_max_i16 v71, v18, 0
buffer_store_dwordx4 v[68:71], v83, s[36:39], s49 offen sc1
v_fma_f32 v104, v79, v64, v100
v_fma_f32 v20, v20, v78, v104
v_fma_f32 v105, v79, v65, v101
v_fma_f32 v21, v21, v78, v105
v_mul_f32_e64 v20, v20, |v20|
v_mul_f32_e64 v21, v21, |v21|
v_cvt_pk_bf16_f32 v20, v20, v21
v_pk_max_i16 v72, v20, 0
v_pk_max_i16 v74, v20, 0
v_fma_f32 v104, v79, v66, v102
v_fma_f32 v22, v22, v78, v104
v_fma_f32 v105, v79, v67, v103
v_fma_f32 v23, v23, v78, v105
v_mul_f32_e64 v22, v22, |v22|
v_mul_f32_e64 v23, v23, |v23|
v_cvt_pk_bf16_f32 v22, v22, v23
v_pk_max_i16 v73, v22, 0
v_pk_max_i16 v75, v22, 0
v_permlane16_swap_b32 v72, v74
s_nop 0
v_permlane16_swap_b32 v73, v75
buffer_store_dwordx4 v[72:75], v84, s[36:39], s49 offen sc1
s_waitcnt vmcnt(2)
buffer_store_dwordx2 v[76:77], v81, s[36:39], s52 offen sc1
v_add_u32 v76, 37, v76
s_waitcnt vmcnt(1)
buffer_store_dwordx2 v[76:77], v81, s[36:39], s52 offen sc1
s_endpgm
CF0_1:
s_cmp_eq_u32 s61, 0
s_cbranch_scc1 BF0_1
s_sub_u32 s61, s61, 1
s_sleep 8
buffer_load_dwordx2 v[106:107], v87, s[36:39], 0 offen sc1
s_waitcnt vmcnt(0)
ds_write_b32 v82, v106
v_sub_u32 v106, v106, v88
v_xor_b32 v107, v107, v77
v_cmp_lt_u64 vcc, s[62:63], v[106:107]
s_waitcnt lgkmcnt(0)
s_cbranch_vccnz CF0_1
s_branch BF0_1
CF0_2:
s_mov_b32 s58, m0
s_mov_b32 m0, 142336
DF0_2:
s_cmp_eq_u32 s61, 0
s_cbranch_scc1 EF0_2
s_sub_u32 s61, s61, 1
buffer_load_dword v87, s[36:39], 0 offen sc1 lds
s_sleep 1
ds_read_b32 v106, v82
s_waitcnt lgkmcnt(0)
v_cmp_gt_i32 vcc, v88, v106
s_cbranch_vccnz DF0_2
EF0_2:
s_mov_b32 m0, s58
s_branch BF0_2
CF0_3:
s_mov_b32 s58, m0
s_mov_b32 m0, 142336
DF0_3:
s_cmp_eq_u32 s61, 0
s_cbranch_scc1 EF0_3
s_sub_u32 s61, s61, 1
buffer_load_dword v87, s[36:39], 0 offen sc1 lds
s_sleep 1
ds_read_b32 v106, v82
s_waitcnt lgkmcnt(0)
v_cmp_gt_i32 vcc, v88, v106
s_cbranch_vccnz DF0_3
EF0_3:
s_mov_b32 m0, s58
s_branch BF0_3
CF0_4:
s_mov_b32 s58, m0
s_mov_b32 m0, 142336
DF0_4:
s_cmp_eq_u32 s61, 0
s_cbranch_scc1 EF0_4
s_sub_u32 s61, s61, 1
buffer_load_dword v87, s[36:39], 0 offen sc1 lds
s_sleep 1
ds_read_b32 v106, v82
s_waitcnt lgkmcnt(0)
v_cmp_gt_i32 vcc, v88, v106
s_cbranch_vccnz DF0_4
EF0_4:
s_mov_b32 m0, s58
s_branch BF0_4
CF0_6:
s_mov_b32 s58, m0
s_mov_b32 m0, 142336
DF0_6:
s_cmp_eq_u32 s61, 0
s_cbranch_scc1 EF0_6
s_sub_u32 s61, s61, 1
buffer_load_dword v87, s[36:39], 0 offen sc1 lds
s_sleep 1
ds_read_b32 v106, v82
s_waitcnt lgkmcnt(0)
v_cmp_gt_i32 vcc, v88, v106
s_cbranch_vccnz DF0_6
EF0_6:
s_mov_b32 m0, s58
s_branch BF0_6
CF0_7:
s_mov_b32 s58, m0
s_mov_b32 m0, 142336
DF0_7:
s_cmp_eq_u32 s61, 0
s_cbranch_scc1 EF0_7
s_sub_u32 s61, s61, 1
buffer_load_dword v87, s[36:39], 0 offen sc1 lds
s_sleep 1
ds_read_b32 v106, v82
s_waitcnt lgkmcnt(0)
v_cmp_gt_i32 vcc, v88, v106
s_cbranch_vccnz DF0_7
EF0_7:
s_mov_b32 m0, s58
s_branch BF0_7
CF0_8:
s_mov_b32 s58, m0
s_mov_b32 m0, 142336
DF0_8:
s_cmp_eq_u32 s61, 0
s_cbranch_scc1 EF0_8
s_sub_u32 s61, s61, 1
buffer_load_dword v87, s[36:39], 0 offen sc1 lds
s_sleep 1
ds_read_b32 v106, v82
s_waitcnt lgkmcnt(0)
v_cmp_gt_i32 vcc, v88, v106
s_cbranch_vccnz DF0_8
EF0_8:
s_mov_b32 m0, s58
s_branch BF0_8
LvF1:
LsF5:
s_cbranch_execz LsF9
s_add_u32 s49, s49, 346112
s_barrier
s_add_u32 m0, s50, 0
s_nop 0
D 85,0
D 85,1024
D 85,2048
D 85,3072
s_add_u32 m0, s50, 4096
s_nop 0
D 86,0
D 86,1024
D 86,2048
D 86,3072
s_add_u32 s49, s49, 346112
s_waitcnt vmcnt(0)
s_barrier
R 24,0
buffer_load_dwordx2 v[108:109], v90, s[36:39], s49 offen sc1
buffer_load_dwordx2 v[110:111], v91, s[36:39], s49 offen sc1
s_add_u32 m0, s50, 32768
R 28,1024
D 85,0
R 32,2048
D 85,1024
R 36,3072
s_waitcnt lgkmcnt(2)
v_mfma_f32_16x16x32_bf16 v[0:3], a[0:3], v[24:27], 0
R 40,4096
D 85,2048
v_mfma_f32_16x16x32_bf16 v[4:7], a[4:7], v[24:27], 0
v_mfma_f32_16x16x32_bf16 v[8:11], a[8:11], v[24:27], 0
mav 0,12,28
R 44,5120
mav 4,16,28
D 85,3072
mav 8,20,28
s_waitcnt lgkmcnt(2)
mav 0,24,32
R 48,6144
mav 4,28,32
s_add_u32 m0, s50, 36864
mav 8,32,32
mav 0,36,36
R 52,7168
D 86,0
mav 4,40,36
mav 8,44,36
s_waitcnt lgkmcnt(2)
mav 0,48,40
R 24,8192
mav 4,52,40
D 86,1024
mav 8,56,40
mav 0,60,44
R 28,9216
mav 4,64,44
mav 8,68,44
s_waitcnt lgkmcnt(2)
mav 0,72,48
R 32,10240
D 86,2048
mav 4,76,48
mav 8,80,48
mav 0,84,52
R 36,11264
mav 4,88,52
D 86,3072
mav 8,92,52
s_waitcnt lgkmcnt(2)
mav 0,96,24
R 40,12288
mav 4,100,24
mav 8,104,24
mav 0,108,28
R 44,13312
mav 4,112,28
mav 8,116,28
s_waitcnt lgkmcnt(2)
mav 0,120,32
R 48,14336
mav 4,124,32
mav 8,128,32
mav 0,132,36
R 52,15360
mav 4,136,36
mav 8,140,36
s_waitcnt lgkmcnt(2)
mav 0,144,40
R 24,16384
mav 4,148,40
mav 8,152,40
mav 0,156,44
R 28,17408
mav 4,160,44
mav 8,164,44
s_waitcnt lgkmcnt(2)
mav 0,168,48
R 32,18432
mav 4,172,48
mav 8,176,48
mav 0,180,52
R 36,19456
mav 4,184,52
mav 8,188,52
s_waitcnt lgkmcnt(2)
mav 0,192,24
R 40,20480
mav 4,196,24
mav 8,200,24
s_waitcnt vmcnt(8)
v_add_f32 v108, v108, v110
mav 0,204,28
R 44,21504
v_add_f32 v109, v109, v111
mav 4,208,28
mav 8,212,28
v_permlane32_swap_b32 v108, v109
s_waitcnt lgkmcnt(2)
mav 0,216,32
R 48,22528
v_add_f32 v110, v108, v109
mav 4,220,32
v_add_f32 v111, v108, v109
mav 8,224,32
mav 0,228,36
R 52,23552
v_permlane16_swap_b32 v110, v111
mav 4,232,36
v_add_f32 v108, v110, v111
v_add_f32 v109, v110, v111
mav 8,236,36
s_waitcnt lgkmcnt(2)
mav 0,240,40
R 24,24576
v_permlane32_swap_b32 v108, v109
mav 4,244,40
v_mul_f32 v110, 0x3a800000, v108
v_mul_f32 v111, 0x3a800000, v109
mav 8,248,40
v_fma_f32 v111, -v110, v110, v111
v_max_f32 v111, 0, v111
mav 0,252,44
R 28,25600
v_add_f32 v111, 0x3727c5ac, v111
mvv 4,128,44
v_rsq_f32 v108, v111
mvv 8,132,44
v_mul_f32_e64 v109, -v110, v108
s_waitcnt lgkmcnt(2)
mvv 0,136,48
R 32,26624
mvv 4,140,48
ds_write_b64 v89, v[108:109] offset:0
mvv 8,144,48
mvv 0,148,52
R 36,27648
mvv 4,152,52
mvv 8,156,52
s_waitcnt lgkmcnt(3)
mvv 0,160,24
R 40,28672
mvv 4,164,24
mvv 8,168,24
mvv 0,172,28
R 44,29696
mvv 4,176,28
mvv 8,180,28
s_waitcnt lgkmcnt(2)
mvv 0,184,32
R 48,30720
s_xor_b32 s50, s50, 0x10000
mvv 4,188,32
mvv 8,192,32
mvv 0,196,36
R 52,31744
mvv 4,200,36
s_add_u32 s49, s49, 346112
mvv 8,204,36
s_waitcnt vmcnt(0)
s_barrier
s_waitcnt lgkmcnt(2)
mvv 0,208,40
R 24,32768
buffer_load_dwordx2 v[108:109], v90, s[36:39], s49 offen sc1
mvv 4,212,40
buffer_load_dwordx2 v[110:111], v91, s[36:39], s49 offen sc1
s_add_u32 m0, s50, 0
mvv 8,216,40
ds_read_b64 v[78:79], v89 offset:0
mvv 0,220,44
R 28,33792
D 85,0
mvv 4,224,44
mvv 8,228,44
s_waitcnt lgkmcnt(3)
mvv 0,232,48
R 32,34816
mvv 4,236,48
D 85,1024
mvv 8,240,48
mvv 0,244,52
R 36,35840
mvv 4,248,52
mvv 8,252,52
s_waitcnt lgkmcnt(2)
v_mfma_f32_16x16x32_bf16 v[12:15], a[0:3], v[24:27], 0
R 40,36864
D 85,2048
v_mfma_f32_16x16x32_bf16 v[16:19], a[4:7], v[24:27], 0
v_fma_f32 v104, v79, v56, v92
v_fma_f32 v0, v0, v78, v104
v_mfma_f32_16x16x32_bf16 v[20:23], a[8:11], v[24:27], 0
v_fma_f32 v105, v79, v57, v93
v_fma_f32 v1, v1, v78, v105
mav 12,12,28
R 44,37888
v_mul_f32_e64 v0, v0, |v0|
mav 16,16,28
D 85,3072
v_mul_f32_e64 v1, v1, |v1|
mav 20,20,28
v_cvt_pk_bf16_f32 v0, v0, v1
s_waitcnt lgkmcnt(2)
mav 12,24,32
R 48,38912
v_pk_max_i16 v68, v0, 0
mav 16,28,32
s_add_u32 m0, s50, 4096
v_fma_f32 v104, v79, v58, v94
mav 20,32,32
v_fma_f32 v2, v2, v78, v104
v_fma_f32 v105, v79, v59, v95
mav 12,36,36
R 52,39936
D 86,0
mav 16,40,36
v_fma_f32 v3, v3, v78, v105
v_mul_f32_e64 v2, v2, |v2|
mav 20,44,36
v_mul_f32_e64 v3, v3, |v3|
s_waitcnt lgkmcnt(2)
mav 12,48,40
R 24,40960
v_cvt_pk_bf16_f32 v2, v2, v3
mav 16,52,40
D 86,1024
v_pk_max_i16 v69, v2, 0
mav 20,56,40
v_fma_f32 v104, v79, v60, v96
v_fma_f32 v4, v4, v78, v104
mav 12,60,44
R 28,41984
v_fma_f32 v105, v79, v61, v97
mav 16,64,44
v_fma_f32 v5, v5, v78, v105
v_mul_f32_e64 v4, v4, |v4|
mav 20,68,44
v_mul_f32_e64 v5, v5, |v5|
s_waitcnt lgkmcnt(2)
mav 12,72,48
R 32,43008
D 86,2048
mav 16,76,48
v_cvt_pk_bf16_f32 v4, v4, v5
v_pk_max_i16 v70, v4, 0
mav 20,80,48
v_fma_f32 v104, v79, v62, v98
v_fma_f32 v6, v6, v78, v104
mav 12,84,52
R 36,44032
v_fma_f32 v105, v79, v63, v99
mav 16,88,52
D 86,3072
v_fma_f32 v7, v7, v78, v105
mav 20,92,52
v_mul_f32_e64 v6, v6, |v6|
s_waitcnt lgkmcnt(2)
mav 12,96,24
R 40,45056
v_mul_f32_e64 v7, v7, |v7|
mav 16,100,24
v_cvt_pk_bf16_f32 v6, v6, v7
v_pk_max_i16 v71, v6, 0
mav 20,104,24
buffer_store_dwordx4 v[68:71], v83, s[36:39], s49 offen sc1
v_fma_f32 v104, v79, v64, v100
mav 12,108,28
R 44,46080
v_fma_f32 v8, v8, v78, v104
mav 16,112,28
v_fma_f32 v105, v79, v65, v101
v_fma_f32 v9, v9, v78, v105
mav 20,116,28
v_mul_f32_e64 v8, v8, |v8|
s_waitcnt lgkmcnt(2)
mav 12,120,32
R 48,47104
v_mul_f32_e64 v9, v9, |v9|
mav 16,124,32
v_cvt_pk_bf16_f32 v8, v8, v9
v_pk_max_i16 v72, v8, 0
mav 20,128,32
v_pk_max_i16 v74, v8, 0
v_fma_f32 v104, v79, v66, v102
mav 12,132,36
R 52,48128
v_fma_f32 v10, v10, v78, v104
mav 16,136,36
v_fma_f32 v105, v79, v67, v103
v_fma_f32 v11, v11, v78, v105
mav 20,140,36
v_mul_f32_e64 v10, v10, |v10|
s_waitcnt lgkmcnt(2)
mav 12,144,40
R 24,49152
v_mul_f32_e64 v11, v11, |v11|
mav 16,148,40
v_cvt_pk_bf16_f32 v10, v10, v11
v_pk_max_i16 v73, v10, 0
mav 20,152,40
v_pk_max_i16 v75, v10, 0
v_permlane16_swap_b32 v72, v74
mav 12,156,44
R 28,50176
v_permlane16_swap_b32 v73, v75
mav 16,160,44
buffer_store_dwordx4 v[72:75], v84, s[36:39], s49 offen sc1
mav 20,164,44
s_waitcnt lgkmcnt(2)
mav 12,168,48
R 32,51200
mav 16,172,48
mav 20,176,48
mav 12,180,52
R 36,52224
mav 16,184,52
mav 20,188,52
s_waitcnt lgkmcnt(2)
mav 12,192,24
R 40,53248
mav 16,196,24
mav 20,200,24
s_waitcnt vmcnt(10)
v_add_f32 v108, v108, v110
mav 12,204,28
R 44,54272
v_add_f32 v109, v109, v111
mav 16,208,28
mav 20,212,28
v_permlane32_swap_b32 v108, v109
s_waitcnt lgkmcnt(2)
mav 12,216,32
R 48,55296
v_add_f32 v110, v108, v109
mav 16,220,32
v_add_f32 v111, v108, v109
mav 20,224,32
mav 12,228,36
R 52,56320
v_permlane16_swap_b32 v110, v111
mav 16,232,36
v_add_f32 v108, v110, v111
v_add_f32 v109, v110, v111
mav 20,236,36
s_waitcnt lgkmcnt(2)
mav 12,240,40
R 24,57344
v_permlane32_swap_b32 v108, v109
mav 16,244,40
v_mul_f32 v110, 0x3a800000, v108
v_mul_f32 v111, 0x3a800000, v109
mav 20,248,40
v_fma_f32 v111, -v110, v110, v111
v_max_f32 v111, 0, v111
mav 12,252,44
R 28,58368
v_add_f32 v111, 0x3727c5ac, v111
mvv 16,128,44
v_rsq_f32 v108, v111
mvv 20,132,44
v_mul_f32_e64 v109, -v110, v108
s_waitcnt lgkmcnt(2)
mvv 12,136,48
R 32,59392
mvv 16,140,48
ds_write_b64 v89, v[108:109] offset:128
mvv 20,144,48
mvv 12,148,52
R 36,60416
mvv 16,152,52
mvv 20,156,52
s_waitcnt lgkmcnt(3)
mvv 12,160,24
R 40,61440
mvv 16,164,24
mvv 20,168,24
mvv 12,172,28
R 44,62464
mvv 16,176,28
v_add_u32 v76, 1, v76
mvv 20,180,28
s_waitcnt lgkmcnt(2)
mvv 12,184,32
R 48,63488
mvv 16,188,32
mvv 20,192,32
mvv 12,196,36
R 52,64512
v_xor_b32 v80, 0x10000, v80
mvv 16,200,36
s_add_u32 s49, s49, 346112
mvv 20,204,36
s_waitcnt vmcnt(2)
s_barrier
s_waitcnt lgkmcnt(2)
mvv 12,208,40
R 24,0
buffer_load_dwordx2 v[108:109], v90, s[36:39], s49 offen sc1
mvv 16,212,40
buffer_load_dwordx2 v[110:111], v91, s[36:39], s49 offen sc1
s_add_u32 m0, s50, 32768
mvv 20,216,40
ds_read_b64 v[78:79], v89 offset:128
mvv 12,220,44
R 28,1024
D 85,0
mvv 16,224,44
mvv 20,228,44
s_waitcnt lgkmcnt(3)
mvv 12,232,48
R 32,2048
mvv 16,236,48
D 85,1024
mvv 20,240,48
mvv 12,244,52
R 36,3072
mvv 16,248,52
mvv 20,252,52
s_mov_b32 s60, -30
.p2align 6
LF1_1:
s_waitcnt lgkmcnt(2)
v_mfma_f32_16x16x32_bf16 v[0:3], a[0:3], v[24:27], 0
R 40,4096
D 85,2048
v_mfma_f32_16x16x32_bf16 v[4:7], a[4:7], v[24:27], 0
v_fma_f32 v104, v79, v56, v92
v_fma_f32 v12, v12, v78, v104
v_mfma_f32_16x16x32_bf16 v[8:11], a[8:11], v[24:27], 0
v_fma_f32 v105, v79, v57, v93
v_fma_f32 v13, v13, v78, v105
mav 0,12,28
R 44,5120
v_mul_f32_e64 v12, v12, |v12|
mav 4,16,28
s_waitcnt vmcnt(5)
buffer_store_dwordx2 v[76:77], v81, s[36:39], s52 offen sc1
mav 8,20,28
LsF6:
s_cbranch_execz LsF10
D 85,3072
s_waitcnt lgkmcnt(2)
mav 0,24,32
R 48,6144
v_mul_f32_e64 v13, v13, |v13|
mav 4,28,32
s_add_u32 m0, s50, 36864
v_cvt_pk_bf16_f32 v12, v12, v13
mav 8,32,32
v_pk_max_i16 v68, v12, 0
v_fma_f32 v104, v79, v58, v94
mav 0,36,36
R 52,7168
D 86,0
mav 4,40,36
v_fma_f32 v14, v14, v78, v104
v_fma_f32 v105, v79, v59, v95
mav 8,44,36
v_fma_f32 v15, v15, v78, v105
s_waitcnt lgkmcnt(2)
mav 0,48,40
R 24,8192
v_mul_f32_e64 v14, v14, |v14|
mav 4,52,40
D 86,1024
v_mul_f32_e64 v15, v15, |v15|
mav 8,56,40
v_cvt_pk_bf16_f32 v14, v14, v15
v_pk_max_i16 v69, v14, 0
mav 0,60,44
R 28,9216
v_fma_f32 v104, v79, v60, v96
mav 4,64,44
v_fma_f32 v16, v16, v78, v104
v_fma_f32 v105, v79, v61, v97
mav 8,68,44
v_fma_f32 v17, v17, v78, v105
s_waitcnt lgkmcnt(2)
mav 0,72,48
R 32,10240
D 86,2048
mav 4,76,48
v_mul_f32_e64 v16, v16, |v16|
v_mul_f32_e64 v17, v17, |v17|
mav 8,80,48
v_cvt_pk_bf16_f32 v16, v16, v17
v_pk_max_i16 v70, v16, 0
mav 0,84,52
R 36,11264
v_fma_f32 v104, v79, v62, v98
mav 4,88,52
D 86,3072
v_fma_f32 v18, v18, v78, v104
mav 8,92,52
v_fma_f32 v105, v79, v63, v99
s_waitcnt lgkmcnt(2)
mav 0,96,24
R 40,12288
v_fma_f32 v19, v19, v78, v105
mav 4,100,24
v_mul_f32_e64 v18, v18, |v18|
v_mul_f32_e64 v19, v19, |v19|
mav 8,104,24
v_cvt_pk_bf16_f32 v18, v18, v19
v_pk_max_i16 v71, v18, 0
mav 0,108,28
R 44,13312
buffer_store_dwordx4 v[68:71], v83, s[36:39], s49 offen sc1
mav 4,112,28
v_fma_f32 v104, v79, v64, v100
v_fma_f32 v20, v20, v78, v104
mav 8,116,28
v_fma_f32 v105, v79, v65, v101
s_waitcnt lgkmcnt(2)
mav 0,120,32
R 48,14336
v_fma_f32 v21, v21, v78, v105
mav 4,124,32
v_mul_f32_e64 v20, v20, |v20|
v_mul_f32_e64 v21, v21, |v21|
mav 8,128,32
v_cvt_pk_bf16_f32 v20, v20, v21
v_pk_max_i16 v72, v20, 0
mav 0,132,36
R 52,15360
v_pk_max_i16 v74, v20, 0
mav 4,136,36
v_fma_f32 v104, v79, v66, v102
v_fma_f32 v22, v22, v78, v104
mav 8,140,36
v_fma_f32 v105, v79, v67, v103
s_waitcnt lgkmcnt(2)
mav 0,144,40
R 24,16384
v_fma_f32 v23, v23, v78, v105
mav 4,148,40
v_mul_f32_e64 v22, v22, |v22|
v_mul_f32_e64 v23, v23, |v23|
mav 8,152,40
v_cvt_pk_bf16_f32 v22, v22, v23
v_pk_max_i16 v73, v22, 0
mav 0,156,44
R 28,17408
v_pk_max_i16 v75, v22, 0
mav 4,160,44
v_permlane16_swap_b32 v72, v74
v_permlane16_swap_b32 v73, v75
mav 8,164,44
buffer_store_dwordx4 v[72:75], v84, s[36:39], s49 offen sc1
s_waitcnt lgkmcnt(2)
mav 0,168,48
R 32,18432
mav 4,172,48
mav 8,176,48
mav 0,180,52
R 36,19456
mav 4,184,52
mav 8,188,52
s_waitcnt lgkmcnt(2)
mav 0,192,24
R 40,20480
mav 4,196,24
mav 8,200,24
s_waitcnt vmcnt(11)
v_add_f32 v108, v108, v110
mav 0,204,28
R 44,21504
v_add_f32 v109, v109, v111
mav 4,208,28
mav 8,212,28
v_permlane32_swap_b32 v108, v109
s_waitcnt lgkmcnt(2)
mav 0,216,32
R 48,22528
v_add_f32 v110, v108, v109
mav 4,220,32
v_add_f32 v111, v108, v109
mav 8,224,32
mav 0,228,36
R 52,23552
v_permlane16_swap_b32 v110, v111
mav 4,232,36
v_add_f32 v108, v110, v111
v_add_f32 v109, v110, v111
mav 8,236,36
s_waitcnt lgkmcnt(2)
mav 0,240,40
R 24,24576
v_permlane32_swap_b32 v108, v109
mav 4,244,40
v_mul_f32 v110, 0x3a800000, v108
v_mul_f32 v111, 0x3a800000, v109
mav 8,248,40
v_fma_f32 v111, -v110, v110, v111
v_max_f32 v111, 0, v111
mav 0,252,44
R 28,25600
v_add_f32 v111, 0x3727c5ac, v111
mvv 4,128,44
v_rsq_f32 v108, v111
mvv 8,132,44
v_mul_f32_e64 v109, -v110, v108
s_waitcnt lgkmcnt(2)
mvv 0,136,48
R 32,26624
mvv 4,140,48
ds_write_b64 v89, v[108:109] offset:0
mvv 8,144,48
mvv 0,148,52
R 36,27648
mvv 4,152,52
mvv 8,156,52
s_waitcnt lgkmcnt(3)
mvv 0,160,24
R 40,28672
mvv 4,164,24
mvv 8,168,24
mvv 0,172,28
R 44,29696
mvv 4,176,28
v_add_u32 v76, 1, v76
mvv 8,180,28
s_waitcnt lgkmcnt(2)
mvv 0,184,32
R 48,30720
s_xor_b32 s50, s50, 0x10000
mvv 4,188,32
mvv 8,192,32
mvv 0,196,36
R 52,31744
mvv 4,200,36
s_add_u32 s49, s49, 346112
mvv 8,204,36
s_waitcnt vmcnt(2)
s_barrier
)"
R"(
s_waitcnt lgkmcnt(2)
mvv 0,208,40
R 24,32768
buffer_load_dwordx2 v[108:109], v90, s[36:39], s49 offen sc1
mvv 4,212,40
buffer_load_dwordx2 v[110:111], v91, s[36:39], s49 offen sc1
s_add_u32 m0, s50, 0
mvv 8,216,40
ds_read_b64 v[78:79], v89 offset:0
mvv 0,220,44
R 28,33792
D 85,0
mvv 4,224,44
mvv 8,228,44
s_waitcnt lgkmcnt(3)
mvv 0,232,48
R 32,34816
mvv 4,236,48
D 85,1024
mvv 8,240,48
mvv 0,244,52
R 36,35840
mvv 4,248,52
mvv 8,252,52
s_waitcnt lgkmcnt(2)
v_mfma_f32_16x16x32_bf16 v[12:15], a[0:3], v[24:27], 0
R 40,36864
D 85,2048
v_mfma_f32_16x16x32_bf16 v[16:19], a[4:7], v[24:27], 0
v_fma_f32 v104, v79, v56, v92
v_fma_f32 v0, v0, v78, v104
v_mfma_f32_16x16x32_bf16 v[20:23], a[8:11], v[24:27], 0
v_fma_f32 v105, v79, v57, v93
v_fma_f32 v1, v1, v78, v105
mav 12,12,28
R 44,37888
v_mul_f32_e64 v0, v0, |v0|
mav 16,16,28
s_waitcnt vmcnt(5)
buffer_store_dwordx2 v[76:77], v81, s[36:39], s52 offen sc1
mav 20,20,28
D 85,3072
s_waitcnt lgkmcnt(2)
mav 12,24,32
R 48,38912
v_mul_f32_e64 v1, v1, |v1|
mav 16,28,32
s_add_u32 m0, s50, 4096
v_cvt_pk_bf16_f32 v0, v0, v1
mav 20,32,32
v_pk_max_i16 v68, v0, 0
v_fma_f32 v104, v79, v58, v94
mav 12,36,36
R 52,39936
D 86,0
mav 16,40,36
v_fma_f32 v2, v2, v78, v104
v_fma_f32 v105, v79, v59, v95
mav 20,44,36
v_fma_f32 v3, v3, v78, v105
s_waitcnt lgkmcnt(2)
mav 12,48,40
R 24,40960
v_mul_f32_e64 v2, v2, |v2|
mav 16,52,40
D 86,1024
v_mul_f32_e64 v3, v3, |v3|
mav 20,56,40
v_cvt_pk_bf16_f32 v2, v2, v3
v_pk_max_i16 v69, v2, 0
mav 12,60,44
R 28,41984
v_fma_f32 v104, v79, v60, v96
mav 16,64,44
v_fma_f32 v4, v4, v78, v104
v_fma_f32 v105, v79, v61, v97
mav 20,68,44
v_fma_f32 v5, v5, v78, v105
s_waitcnt lgkmcnt(2)
mav 12,72,48
R 32,43008
D 86,2048
mav 16,76,48
v_mul_f32_e64 v4, v4, |v4|
v_mul_f32_e64 v5, v5, |v5|
mav 20,80,48
v_cvt_pk_bf16_f32 v4, v4, v5
v_pk_max_i16 v70, v4, 0
mav 12,84,52
R 36,44032
v_fma_f32 v104, v79, v62, v98
mav 16,88,52
D 86,3072
v_fma_f32 v6, v6, v78, v104
mav 20,92,52
v_fma_f32 v105, v79, v63, v99
s_waitcnt lgkmcnt(2)
mav 12,96,24
R 40,45056
v_fma_f32 v7, v7, v78, v105
mav 16,100,24
v_mul_f32_e64 v6, v6, |v6|
v_mul_f32_e64 v7, v7, |v7|
mav 20,104,24
v_cvt_pk_bf16_f32 v6, v6, v7
v_pk_max_i16 v71, v6, 0
mav 12,108,28
R 44,46080
buffer_store_dwordx4 v[68:71], v83, s[36:39], s49 offen sc1
mav 16,112,28
v_fma_f32 v104, v79, v64, v100
v_fma_f32 v8, v8, v78, v104
mav 20,116,28
v_fma_f32 v105, v79, v65, v101
s_waitcnt lgkmcnt(2)
mav 12,120,32
R 48,47104
v_fma_f32 v9, v9, v78, v105
mav 16,124,32
v_mul_f32_e64 v8, v8, |v8|
v_mul_f32_e64 v9, v9, |v9|
mav 20,128,32
v_cvt_pk_bf16_f32 v8, v8, v9
v_pk_max_i16 v72, v8, 0
mav 12,132,36
R 52,48128
v_pk_max_i16 v74, v8, 0
mav 16,136,36
v_fma_f32 v104, v79, v66, v102
v_fma_f32 v10, v10, v78, v104
mav 20,140,36
v_fma_f32 v105, v79, v67, v103
s_waitcnt lgkmcnt(2)
mav 12,144,40
R 24,49152
v_fma_f32 v11, v11, v78, v105
mav 16,148,40
v_mul_f32_e64 v10, v10, |v10|
v_mul_f32_e64 v11, v11, |v11|
mav 20,152,40
v_cvt_pk_bf16_f32 v10, v10, v11
v_pk_max_i16 v73, v10, 0
mav 12,156,44
R 28,50176
v_pk_max_i16 v75, v10, 0
mav 16,160,44
v_permlane16_swap_b32 v72, v74
v_permlane16_swap_b32 v73, v75
mav 20,164,44
buffer_store_dwordx4 v[72:75], v84, s[36:39], s49 offen sc1
s_waitcnt lgkmcnt(2)
mav 12,168,48
R 32,51200
mav 16,172,48
mav 20,176,48
mav 12,180,52
R 36,52224
mav 16,184,52
mav 20,188,52
s_waitcnt lgkmcnt(2)
mav 12,192,24
R 40,53248
mav 16,196,24
mav 20,200,24
s_waitcnt vmcnt(11)
v_add_f32 v108, v108, v110
mav 12,204,28
R 44,54272
v_add_f32 v109, v109, v111
mav 16,208,28
mav 20,212,28
v_permlane32_swap_b32 v108, v109
s_waitcnt lgkmcnt(2)
mav 12,216,32
R 48,55296
v_add_f32 v110, v108, v109
mav 16,220,32
v_add_f32 v111, v108, v109
mav 20,224,32
mav 12,228,36
R 52,56320
v_permlane16_swap_b32 v110, v111
mav 16,232,36
v_add_f32 v108, v110, v111
v_add_f32 v109, v110, v111
mav 20,236,36
s_waitcnt lgkmcnt(2)
mav 12,240,40
R 24,57344
v_permlane32_swap_b32 v108, v109
mav 16,244,40
v_mul_f32 v110, 0x3a800000, v108
v_mul_f32 v111, 0x3a800000, v109
mav 20,248,40
v_fma_f32 v111, -v110, v110, v111
v_max_f32 v111, 0, v111
mav 12,252,44
R 28,58368
v_add_f32 v111, 0x3727c5ac, v111
mvv 16,128,44
v_rsq_f32 v108, v111
mvv 20,132,44
v_mul_f32_e64 v109, -v110, v108
s_waitcnt lgkmcnt(2)
mvv 12,136,48
R 32,59392
mvv 16,140,48
ds_write_b64 v89, v[108:109] offset:128
mvv 20,144,48
mvv 12,148,52
R 36,60416
mvv 16,152,52
mvv 20,156,52
s_waitcnt lgkmcnt(3)
mvv 12,160,24
R 40,61440
mvv 16,164,24
mvv 20,168,24
mvv 12,172,28
R 44,62464
mvv 16,176,28
v_add_u32 v76, 1, v76
mvv 20,180,28
s_waitcnt lgkmcnt(2)
mvv 12,184,32
R 48,63488
mvv 16,188,32
mvv 20,192,32
mvv 12,196,36
R 52,64512
v_xor_b32 v80, 0x10000, v80
mvv 16,200,36
s_add_u32 s49, s49, 346112
mvv 20,204,36
s_waitcnt vmcnt(2)
s_barrier
s_waitcnt lgkmcnt(2)
mvv 12,208,40
R 24,0
buffer_load_dwordx2 v[108:109], v90, s[36:39], s49 offen sc1
mvv 16,212,40
buffer_load_dwordx2 v[110:111], v91, s[36:39], s49 offen sc1
s_add_u32 m0, s50, 32768
mvv 20,216,40
ds_read_b64 v[78:79], v89 offset:128
mvv 12,220,44
R 28,1024
D 85,0
mvv 16,224,44
mvv 20,228,44
s_waitcnt lgkmcnt(3)
mvv 12,232,48
R 32,2048
mvv 16,236,48
D 85,1024
mvv 20,240,48
mvv 12,244,52
R 36,3072
mvv 16,248,52
mvv 20,252,52
s_add_u32 s60, s60, 1
s_cbranch_scc0 LF1_1
s_waitcnt lgkmcnt(2)
v_mfma_f32_16x16x32_bf16 v[0:3], a[0:3], v[24:27], 0
R 40,4096
D 85,2048
v_mfma_f32_16x16x32_bf16 v[4:7], a[4:7], v[24:27], 0
v_fma_f32 v104, v79, v56, v92
v_fma_f32 v12, v12, v78, v104
v_mfma_f32_16x16x32_bf16 v[8:11], a[8:11], v[24:27], 0
v_fma_f32 v105, v79, v57, v93
v_fma_f32 v13, v13, v78, v105
mav 0,12,28
R 44,5120
v_mul_f32_e64 v12, v12, |v12|
mav 4,16,28
s_waitcnt vmcnt(5)
buffer_store_dwordx2 v[76:77], v81, s[36:39], s52 offen sc1
mav 8,20,28
D 85,3072
s_waitcnt lgkmcnt(2)
mav 0,24,32
R 48,6144
v_mul_f32_e64 v13, v13, |v13|
mav 4,28,32
s_add_u32 m0, s50, 36864
v_cvt_pk_bf16_f32 v12, v12, v13
mav 8,32,32
v_pk_max_i16 v68, v12, 0
v_fma_f32 v104, v79, v58, v94
mav 0,36,36
R 52,7168
D 86,0
mav 4,40,36
v_fma_f32 v14, v14, v78, v104
v_fma_f32 v105, v79, v59, v95
mav 8,44,36
v_fma_f32 v15, v15, v78, v105
s_waitcnt lgkmcnt(2)
mav 0,48,40
R 24,8192
v_mul_f32_e64 v14, v14, |v14|
mav 4,52,40
D 86,1024
v_mul_f32_e64 v15, v15, |v15|
mav 8,56,40
v_cvt_pk_bf16_f32 v14, v14, v15
v_pk_max_i16 v69, v14, 0
mav 0,60,44
R 28,9216
v_fma_f32 v104, v79, v60, v96
mav 4,64,44
v_fma_f32 v16, v16, v78, v104
v_fma_f32 v105, v79, v61, v97
mav 8,68,44
v_fma_f32 v17, v17, v78, v105
s_waitcnt lgkmcnt(2)
mav 0,72,48
R 32,10240
D 86,2048
mav 4,76,48
v_mul_f32_e64 v16, v16, |v16|
v_mul_f32_e64 v17, v17, |v17|
mav 8,80,48
v_cvt_pk_bf16_f32 v16, v16, v17
v_pk_max_i16 v70, v16, 0
mav 0,84,52
R 36,11264
v_fma_f32 v104, v79, v62, v98
mav 4,88,52
D 86,3072
v_fma_f32 v18, v18, v78, v104
mav 8,92,52
v_fma_f32 v105, v79, v63, v99
s_waitcnt lgkmcnt(2)
mav 0,96,24
R 40,12288
v_fma_f32 v19, v19, v78, v105
mav 4,100,24
v_mul_f32_e64 v18, v18, |v18|
v_mul_f32_e64 v19, v19, |v19|
mav 8,104,24
v_cvt_pk_bf16_f32 v18, v18, v19
v_pk_max_i16 v71, v18, 0
mav 0,108,28
R 44,13312
buffer_store_dwordx4 v[68:71], v83, s[36:39], s49 offen sc1
mav 4,112,28
v_fma_f32 v104, v79, v64, v100
v_fma_f32 v20, v20, v78, v104
mav 8,116,28
v_fma_f32 v105, v79, v65, v101
s_waitcnt lgkmcnt(2)
mav 0,120,32
R 48,14336
LsF7:
s_cbranch_execz LsF11
v_fma_f32 v21, v21, v78, v105
mav 4,124,32
v_mul_f32_e64 v20, v20, |v20|
v_mul_f32_e64 v21, v21, |v21|
mav 8,128,32
v_cvt_pk_bf16_f32 v20, v20, v21
v_pk_max_i16 v72, v20, 0
mav 0,132,36
R 52,15360
v_pk_max_i16 v74, v20, 0
mav 4,136,36
v_fma_f32 v104, v79, v66, v102
v_fma_f32 v22, v22, v78, v104
mav 8,140,36
v_fma_f32 v105, v79, v67, v103
s_waitcnt lgkmcnt(2)
mav 0,144,40
R 24,16384
v_fma_f32 v23, v23, v78, v105
mav 4,148,40
v_mul_f32_e64 v22, v22, |v22|
v_mul_f32_e64 v23, v23, |v23|
mav 8,152,40
v_cvt_pk_bf16_f32 v22, v22, v23
v_pk_max_i16 v73, v22, 0
mav 0,156,44
R 28,17408
v_pk_max_i16 v75, v22, 0
mav 4,160,44
v_permlane16_swap_b32 v72, v74
v_permlane16_swap_b32 v73, v75
mav 8,164,44
buffer_store_dwordx4 v[72:75], v84, s[36:39], s49 offen sc1
s_waitcnt lgkmcnt(2)
mav 0,168,48
R 32,18432
mav 4,172,48
mav 8,176,48
mav 0,180,52
R 36,19456
mav 4,184,52
mav 8,188,52
s_waitcnt lgkmcnt(2)
mav 0,192,24
R 40,20480
mav 4,196,24
mav 8,200,24
s_waitcnt vmcnt(11)
v_add_f32 v108, v108, v110
mav 0,204,28
R 44,21504
v_add_f32 v109, v109, v111
mav 4,208,28
mav 8,212,28
v_permlane32_swap_b32 v108, v109
s_waitcnt lgkmcnt(2)
mav 0,216,32
R 48,22528
v_add_f32 v110, v108, v109
mav 4,220,32
v_add_f32 v111, v108, v109
mav 8,224,32
mav 0,228,36
R 52,23552
v_permlane16_swap_b32 v110, v111
mav 4,232,36
v_add_f32 v108, v110, v111
v_add_f32 v109, v110, v111
mav 8,236,36
s_waitcnt lgkmcnt(2)
mav 0,240,40
R 24,24576
v_permlane32_swap_b32 v108, v109
mav 4,244,40
v_mul_f32 v110, 0x3a800000, v108
v_mul_f32 v111, 0x3a800000, v109
mav 8,248,40
v_fma_f32 v111, -v110, v110, v111
v_max_f32 v111, 0, v111
mav 0,252,44
R 28,25600
v_add_f32 v111, 0x3727c5ac, v111
mvv 4,128,44
v_rsq_f32 v108, v111
mvv 8,132,44
v_mul_f32_e64 v109, -v110, v108
s_waitcnt lgkmcnt(2)
mvv 0,136,48
R 32,26624
mvv 4,140,48
ds_write_b64 v89, v[108:109] offset:0
mvv 8,144,48
mvv 0,148,52
R 36,27648
mvv 4,152,52
mvv 8,156,52
s_waitcnt lgkmcnt(3)
mvv 0,160,24
R 40,28672
mvv 4,164,24
mvv 8,168,24
mvv 0,172,28
R 44,29696
mvv 4,176,28
v_add_u32 v76, 1, v76
mvv 8,180,28
s_waitcnt lgkmcnt(2)
mvv 0,184,32
R 48,30720
mvv 4,188,32
mvv 8,192,32
mvv 0,196,36
R 52,31744
mvv 4,200,36
s_add_u32 s49, s49, 346112
mvv 8,204,36
s_waitcnt vmcnt(2)
s_barrier
s_waitcnt lgkmcnt(2)
mvv 0,208,40
R 24,32768
buffer_load_dwordx2 v[108:109], v90, s[36:39], s49 offen sc1
mvv 4,212,40
buffer_load_dwordx2 v[110:111], v91, s[36:39], s49 offen sc1
ds_read_b64 v[78:79], v89 offset:0
mvv 8,216,40
mvv 0,220,44
R 28,33792
mvv 4,224,44
mvv 8,228,44
s_waitcnt lgkmcnt(3)
mvv 0,232,48
R 32,34816
mvv 4,236,48
mvv 8,240,48
mvv 0,244,52
R 36,35840
mvv 4,248,52
mvv 8,252,52
s_waitcnt lgkmcnt(2)
v_mfma_f32_16x16x32_bf16 v[12:15], a[0:3], v[24:27], 0
R 40,36864
v_fma_f32 v104, v79, v56, v92
v_mfma_f32_16x16x32_bf16 v[16:19], a[4:7], v[24:27], 0
v_fma_f32 v0, v0, v78, v104
v_fma_f32 v105, v79, v57, v93
v_mfma_f32_16x16x32_bf16 v[20:23], a[8:11], v[24:27], 0
v_fma_f32 v1, v1, v78, v105
v_mul_f32_e64 v0, v0, |v0|
mav 12,12,28
R 44,37888
v_mul_f32_e64 v1, v1, |v1|
mav 16,16,28
s_waitcnt vmcnt(2)
buffer_store_dwordx2 v[76:77], v81, s[36:39], s52 offen sc1
mav 20,20,28
v_cvt_pk_bf16_f32 v0, v0, v1
s_waitcnt lgkmcnt(2)
mav 12,24,32
R 48,38912
v_pk_max_i16 v68, v0, 0
mav 16,28,32
v_fma_f32 v104, v79, v58, v94
v_fma_f32 v2, v2, v78, v104
mav 20,32,32
v_fma_f32 v105, v79, v59, v95
v_fma_f32 v3, v3, v78, v105
mav 12,36,36
R 52,39936
v_mul_f32_e64 v2, v2, |v2|
mav 16,40,36
v_mul_f32_e64 v3, v3, |v3|
v_cvt_pk_bf16_f32 v2, v2, v3
mav 20,44,36
v_pk_max_i16 v69, v2, 0
s_waitcnt lgkmcnt(2)
mav 12,48,40
R 24,40960
v_fma_f32 v104, v79, v60, v96
mav 16,52,40
v_fma_f32 v4, v4, v78, v104
v_fma_f32 v105, v79, v61, v97
mav 20,56,40
v_fma_f32 v5, v5, v78, v105
v_mul_f32_e64 v4, v4, |v4|
mav 12,60,44
R 28,41984
v_mul_f32_e64 v5, v5, |v5|
mav 16,64,44
v_cvt_pk_bf16_f32 v4, v4, v5
v_pk_max_i16 v70, v4, 0
mav 20,68,44
v_fma_f32 v104, v79, v62, v98
s_waitcnt lgkmcnt(2)
mav 12,72,48
R 32,43008
v_fma_f32 v6, v6, v78, v104
mav 16,76,48
v_fma_f32 v105, v79, v63, v99
v_fma_f32 v7, v7, v78, v105
mav 20,80,48
v_mul_f32_e64 v6, v6, |v6|
v_mul_f32_e64 v7, v7, |v7|
mav 12,84,52
R 36,44032
v_cvt_pk_bf16_f32 v6, v6, v7
mav 16,88,52
v_pk_max_i16 v71, v6, 0
buffer_store_dwordx4 v[68:71], v83, s[36:39], s49 offen sc1
mav 20,92,52
v_fma_f32 v104, v79, v64, v100
s_waitcnt lgkmcnt(2)
mav 12,96,24
R 40,45056
v_fma_f32 v8, v8, v78, v104
mav 16,100,24
v_fma_f32 v105, v79, v65, v101
v_fma_f32 v9, v9, v78, v105
mav 20,104,24
v_mul_f32_e64 v8, v8, |v8|
v_mul_f32_e64 v9, v9, |v9|
mav 12,108,28
R 44,46080
v_cvt_pk_bf16_f32 v8, v8, v9
mav 16,112,28
v_pk_max_i16 v72, v8, 0
v_pk_max_i16 v74, v8, 0
mav 20,116,28
v_fma_f32 v104, v79, v66, v102
s_waitcnt lgkmcnt(2)
mav 12,120,32
R 48,47104
v_fma_f32 v10, v10, v78, v104
mav 16,124,32
v_fma_f32 v105, v79, v67, v103
v_fma_f32 v11, v11, v78, v105
mav 20,128,32
v_mul_f32_e64 v10, v10, |v10|
v_mul_f32_e64 v11, v11, |v11|
mav 12,132,36
R 52,48128
v_cvt_pk_bf16_f32 v10, v10, v11
mav 16,136,36
v_pk_max_i16 v73, v10, 0
v_pk_max_i16 v75, v10, 0
mav 20,140,36
v_permlane16_swap_b32 v72, v74
s_waitcnt lgkmcnt(2)
mav 12,144,40
R 24,49152
v_permlane16_swap_b32 v73, v75
mav 16,148,40
buffer_store_dwordx4 v[72:75], v84, s[36:39], s49 offen sc1
mav 20,152,40
mav 12,156,44
R 28,50176
mav 16,160,44
mav 20,164,44
s_waitcnt lgkmcnt(2)
mav 12,168,48
R 32,51200
mav 16,172,48
mav 20,176,48
mav 12,180,52
R 36,52224
mav 16,184,52
mav 20,188,52
s_waitcnt lgkmcnt(2)
mav 12,192,24
R 40,53248
mav 16,196,24
mav 20,200,24
s_waitcnt vmcnt(3)
v_add_f32 v108, v108, v110
mav 12,204,28
R 44,54272
v_add_f32 v109, v109, v111
mav 16,208,28
mav 20,212,28
v_permlane32_swap_b32 v108, v109
s_waitcnt lgkmcnt(2)
mav 12,216,32
R 48,55296
v_add_f32 v110, v108, v109
mav 16,220,32
v_add_f32 v111, v108, v109
mav 20,224,32
mav 12,228,36
R 52,56320
v_permlane16_swap_b32 v110, v111
mav 16,232,36
v_add_f32 v108, v110, v111
v_add_f32 v109, v110, v111
mav 20,236,36
s_waitcnt lgkmcnt(2)
mav 12,240,40
R 24,57344
v_permlane32_swap_b32 v108, v109
mav 16,244,40
v_mul_f32 v110, 0x3a800000, v108
v_mul_f32 v111, 0x3a800000, v109
mav 20,248,40
v_fma_f32 v111, -v110, v110, v111
v_max_f32 v111, 0, v111
mav 12,252,44
R 28,58368
v_add_f32 v111, 0x3727c5ac, v111
mvv 16,128,44
v_rsq_f32 v108, v111
mvv 20,132,44
v_mul_f32_e64 v109, -v110, v108
s_waitcnt lgkmcnt(2)
mvv 12,136,48
R 32,59392
mvv 16,140,48
ds_write_b64 v89, v[108:109] offset:128
mvv 20,144,48
mvv 12,148,52
R 36,60416
mvv 16,152,52
mvv 20,156,52
s_waitcnt lgkmcnt(3)
mvv 12,160,24
R 40,61440
mvv 16,164,24
mvv 20,168,24
mvv 12,172,28
R 44,62464
mvv 16,176,28
v_add_u32 v76, 1, v76
mvv 20,180,28
s_waitcnt lgkmcnt(2)
mvv 12,184,32
R 48,63488
mvv 16,188,32
mvv 20,192,32
mvv 12,196,36
R 52,64512
mvv 16,200,36
s_add_u32 s49, s49, 346112
mvv 20,204,36
s_barrier
s_waitcnt lgkmcnt(2)
mvv 12,208,40
mvv 16,212,40
ds_read_b64 v[78:79], v89 offset:128
mvv 20,216,40
mvv 12,220,44
mvv 16,224,44
mvv 20,228,44
s_waitcnt lgkmcnt(1)
mvv 12,232,48
mvv 16,236,48
mvv 20,240,48
mvv 12,244,52
mvv 16,248,52
mvv 20,252,52
s_waitcnt lgkmcnt(0)
v_fma_f32 v104, v79, v56, v92
s_nop 3
v_fma_f32 v12, v12, v78, v104
v_fma_f32 v105, v79, v57, v93
v_fma_f32 v13, v13, v78, v105
v_mul_f32_e64 v12, v12, |v12|
v_mul_f32_e64 v13, v13, |v13|
v_cvt_pk_bf16_f32 v12, v12, v13
v_pk_max_i16 v68, v12, 0
v_fma_f32 v104, v79, v58, v94
v_fma_f32 v14, v14, v78, v104
v_fma_f32 v105, v79, v59, v95
v_fma_f32 v15, v15, v78, v105
v_mul_f32_e64 v14, v14, |v14|
v_mul_f32_e64 v15, v15, |v15|
v_cvt_pk_bf16_f32 v14, v14, v15
v_pk_max_i16 v69, v14, 0
v_fma_f32 v104, v79, v60, v96
v_fma_f32 v16, v16, v78, v104
v_fma_f32 v105, v79, v61, v97
v_fma_f32 v17, v17, v78, v105
v_mul_f32_e64 v16, v16, |v16|
v_mul_f32_e64 v17, v17, |v17|
v_cvt_pk_bf16_f32 v16, v16, v17
v_pk_max_i16 v70, v16, 0
v_fma_f32 v104, v79, v62, v98
v_fma_f32 v18, v18, v78, v104
v_fma_f32 v105, v79, v63, v99
v_fma_f32 v19, v19, v78, v105
v_mul_f32_e64 v18, v18, |v18|
v_mul_f32_e64 v19, v19, |v19|
v_cvt_pk_bf16_f32 v18, v18, v19
v_pk_max_i16 v71, v18, 0
buffer_store_dwordx4 v[68:71], v83, s[36:39], s49 offen sc1
v_fma_f32 v104, v79, v64, v100
v_fma_f32 v20, v20, v78, v104
v_fma_f32 v105, v79, v65, v101
v_fma_f32 v21, v21, v78, v105
v_mul_f32_e64 v20, v20, |v20|
v_mul_f32_e64 v21, v21, |v21|
v_cvt_pk_bf16_f32 v20, v20, v21
v_pk_max_i16 v72, v20, 0
v_pk_max_i16 v74, v20, 0
v_fma_f32 v104, v79, v66, v102
v_fma_f32 v22, v22, v78, v104
v_fma_f32 v105, v79, v67, v103
v_fma_f32 v23, v23, v78, v105
v_mul_f32_e64 v22, v22, |v22|
v_mul_f32_e64 v23, v23, |v23|
v_cvt_pk_bf16_f32 v22, v22, v23
v_pk_max_i16 v73, v22, 0
v_pk_max_i16 v75, v22, 0
v_permlane16_swap_b32 v72, v74
s_nop 0
v_permlane16_swap_b32 v73, v75
buffer_store_dwordx4 v[72:75], v84, s[36:39], s49 offen sc1
s_waitcnt vmcnt(2)
buffer_store_dwordx2 v[76:77], v81, s[36:39], s52 offen sc1
v_add_u32 v76, 37, v76
s_waitcnt vmcnt(1)
buffer_store_dwordx2 v[76:77], v81, s[36:39], s52 offen sc1
s_endpgm
LvF2:
LsF8:
s_cbranch_execz LwF
s_add_u32 s49, s49, 346112
s_barrier
s_add_u32 m0, s50, 0
s_nop 0
D 85,0
D 85,1024
D 85,2048
D 85,3072
s_add_u32 m0, s50, 4096
s_nop 0
D 86,0
D 86,1024
D 86,2048
D 86,3072
s_add_u32 s49, s49, 346112
s_waitcnt vmcnt(0)
s_barrier
R 24,0
s_add_u32 m0, s50, 32768
R 28,1024
D 85,0
R 32,2048
D 85,1024
R 36,3072
s_waitcnt lgkmcnt(2)
v_mfma_f32_16x16x32_bf16 v[0:3], a[0:3], v[24:27], 0
R 40,4096
D 85,2048
v_mfma_f32_16x16x32_bf16 v[4:7], a[4:7], v[24:27], 0
v_mfma_f32_16x16x32_bf16 v[8:11], a[8:11], v[24:27], 0
mav 0,12,28
R 44,5120
mav 4,16,28
D 85,3072
mav 8,20,28
s_waitcnt lgkmcnt(2)
mav 0,24,32
R 48,6144
mav 4,28,32
s_add_u32 m0, s50, 36864
mav 8,32,32
mav 0,36,36
R 52,7168
D 86,0
mav 4,40,36
mav 8,44,36
s_waitcnt lgkmcnt(2)
mav 0,48,40
R 24,8192
mav 4,52,40
D 86,1024
mav 8,56,40
mav 0,60,44
R 28,9216
mav 4,64,44
mav 8,68,44
s_waitcnt lgkmcnt(2)
mav 0,72,48
R 32,10240
D 86,2048
mav 4,76,48
mav 8,80,48
mav 0,84,52
R 36,11264
mav 4,88,52
D 86,3072
mav 8,92,52
s_waitcnt lgkmcnt(2)
mav 0,96,24
R 40,12288
mav 4,100,24
mav 8,104,24
mav 0,108,28
R 44,13312
mav 4,112,28
mav 8,116,28
s_waitcnt lgkmcnt(2)
mav 0,120,32
R 48,14336
mav 4,124,32
mav 8,128,32
mav 0,132,36
R 52,15360
mav 4,136,36
mav 8,140,36
s_waitcnt lgkmcnt(2)
mav 0,144,40
R 24,16384
mav 4,148,40
mav 8,152,40
mav 0,156,44
R 28,17408
mav 4,160,44
mav 8,164,44
s_waitcnt lgkmcnt(2)
mav 0,168,48
R 32,18432
mav 4,172,48
mav 8,176,48
mav 0,180,52
R 36,19456
mav 4,184,52
mav 8,188,52
s_waitcnt lgkmcnt(2)
mav 0,192,24
R 40,20480
mav 4,196,24
mav 8,200,24
mav 0,204,28
R 44,21504
mav 4,208,28
mav 8,212,28
s_waitcnt lgkmcnt(2)
mav 0,216,32
R 48,22528
mav 4,220,32
mav 8,224,32
mav 0,228,36
R 52,23552
mav 4,232,36
mav 8,236,36
s_waitcnt lgkmcnt(2)
mav 0,240,40
R 24,24576
mav 4,244,40
mav 8,248,40
mav 0,252,44
R 28,25600
mvv 4,128,44
mvv 8,132,44
s_waitcnt lgkmcnt(2)
mvv 0,136,48
R 32,26624
mvv 4,140,48
LsF9:
s_cbranch_execz LwF
mvv 8,144,48
mvv 0,148,52
R 36,27648
mvv 4,152,52
mvv 8,156,52
s_waitcnt lgkmcnt(2)
mvv 0,160,24
R 40,28672
mvv 4,164,24
mvv 8,168,24
mvv 0,172,28
R 44,29696
mvv 4,176,28
mvv 8,180,28
s_waitcnt lgkmcnt(2)
mvv 0,184,32
R 48,30720
s_xor_b32 s50, s50, 0x10000
mvv 4,188,32
mvv 8,192,32
mvv 0,196,36
R 52,31744
mvv 4,200,36
s_add_u32 s49, s49, 346112
mvv 8,204,36
s_waitcnt vmcnt(0)
s_barrier
s_waitcnt lgkmcnt(2)
mvv 0,208,40
R 24,32768
mvv 4,212,40
s_add_u32 m0, s50, 0
ds_read_b64 v[78:79], v89 offset:0
mvv 8,216,40
mvv 0,220,44
R 28,33792
D 85,0
mvv 4,224,44
mvv 8,228,44
s_waitcnt lgkmcnt(3)
mvv 0,232,48
R 32,34816
mvv 4,236,48
D 85,1024
mvv 8,240,48
mvv 0,244,52
R 36,35840
mvv 4,248,52
mvv 8,252,52
s_waitcnt lgkmcnt(2)
v_mfma_f32_16x16x32_bf16 v[12:15], a[0:3], v[24:27], 0
R 40,36864
D 85,2048
v_mfma_f32_16x16x32_bf16 v[16:19], a[4:7], v[24:27], 0
v_fma_f32 v104, v79, v56, v92
v_fma_f32 v0, v0, v78, v104
v_mfma_f32_16x16x32_bf16 v[20:23], a[8:11], v[24:27], 0
v_fma_f32 v105, v79, v57, v93
v_fma_f32 v1, v1, v78, v105
mav 12,12,28
R 44,37888
v_mul_f32_e64 v0, v0, |v0|
mav 16,16,28
D 85,3072
v_mul_f32_e64 v1, v1, |v1|
mav 20,20,28
v_cvt_pk_bf16_f32 v0, v0, v1
s_waitcnt lgkmcnt(2)
mav 12,24,32
R 48,38912
v_pk_max_i16 v68, v0, 0
mav 16,28,32
s_add_u32 m0, s50, 4096
v_fma_f32 v104, v79, v58, v94
mav 20,32,32
v_fma_f32 v2, v2, v78, v104
v_fma_f32 v105, v79, v59, v95
mav 12,36,36
R 52,39936
D 86,0
mav 16,40,36
v_fma_f32 v3, v3, v78, v105
v_mul_f32_e64 v2, v2, |v2|
mav 20,44,36
v_mul_f32_e64 v3, v3, |v3|
s_waitcnt lgkmcnt(2)
mav 12,48,40
R 24,40960
v_cvt_pk_bf16_f32 v2, v2, v3
mav 16,52,40
D 86,1024
v_pk_max_i16 v69, v2, 0
mav 20,56,40
v_fma_f32 v104, v79, v60, v96
v_fma_f32 v4, v4, v78, v104
mav 12,60,44
R 28,41984
v_fma_f32 v105, v79, v61, v97
mav 16,64,44
v_fma_f32 v5, v5, v78, v105
v_mul_f32_e64 v4, v4, |v4|
mav 20,68,44
v_mul_f32_e64 v5, v5, |v5|
s_waitcnt lgkmcnt(2)
mav 12,72,48
R 32,43008
D 86,2048
mav 16,76,48
v_cvt_pk_bf16_f32 v4, v4, v5
v_pk_max_i16 v70, v4, 0
mav 20,80,48
v_fma_f32 v104, v79, v62, v98
v_fma_f32 v6, v6, v78, v104
mav 12,84,52
R 36,44032
v_fma_f32 v105, v79, v63, v99
mav 16,88,52
D 86,3072
v_fma_f32 v7, v7, v78, v105
mav 20,92,52
v_mul_f32_e64 v6, v6, |v6|
s_waitcnt lgkmcnt(2)
mav 12,96,24
R 40,45056
v_mul_f32_e64 v7, v7, |v7|
mav 16,100,24
v_cvt_pk_bf16_f32 v6, v6, v7
v_pk_max_i16 v71, v6, 0
mav 20,104,24
buffer_store_dwordx4 v[68:71], v83, s[36:39], s49 offen sc1
v_fma_f32 v104, v79, v64, v100
mav 12,108,28
R 44,46080
v_fma_f32 v8, v8, v78, v104
mav 16,112,28
v_fma_f32 v105, v79, v65, v101
v_fma_f32 v9, v9, v78, v105
mav 20,116,28
v_mul_f32_e64 v8, v8, |v8|
s_waitcnt lgkmcnt(2)
mav 12,120,32
R 48,47104
v_mul_f32_e64 v9, v9, |v9|
mav 16,124,32
v_cvt_pk_bf16_f32 v8, v8, v9
v_pk_max_i16 v72, v8, 0
mav 20,128,32
v_pk_max_i16 v74, v8, 0
v_fma_f32 v104, v79, v66, v102
mav 12,132,36
R 52,48128
v_fma_f32 v10, v10, v78, v104
mav 16,136,36
v_fma_f32 v105, v79, v67, v103
v_fma_f32 v11, v11, v78, v105
mav 20,140,36
v_mul_f32_e64 v10, v10, |v10|
s_waitcnt lgkmcnt(2)
mav 12,144,40
R 24,49152
v_mul_f32_e64 v11, v11, |v11|
mav 16,148,40
v_cvt_pk_bf16_f32 v10, v10, v11
v_pk_max_i16 v73, v10, 0
mav 20,152,40
v_pk_max_i16 v75, v10, 0
v_permlane16_swap_b32 v72, v74
mav 12,156,44
R 28,50176
v_permlane16_swap_b32 v73, v75
mav 16,160,44
buffer_store_dwordx4 v[72:75], v84, s[36:39], s49 offen sc1
mav 20,164,44
s_waitcnt lgkmcnt(2)
mav 12,168,48
R 32,51200
mav 16,172,48
mav 20,176,48
mav 12,180,52
R 36,52224
mav 16,184,52
mav 20,188,52
s_waitcnt lgkmcnt(2)
mav 12,192,24
R 40,53248
mav 16,196,24
mav 20,200,24
mav 12,204,28
R 44,54272
mav 16,208,28
mav 20,212,28
s_waitcnt lgkmcnt(2)
mav 12,216,32
R 48,55296
mav 16,220,32
mav 20,224,32
mav 12,228,36
R 52,56320
mav 16,232,36
mav 20,236,36
s_waitcnt lgkmcnt(2)
mav 12,240,40
R 24,57344
mav 16,244,40
mav 20,248,40
mav 12,252,44
R 28,58368
mvv 16,128,44
mvv 20,132,44
s_waitcnt lgkmcnt(2)
mvv 12,136,48
R 32,59392
mvv 16,140,48
mvv 20,144,48
mvv 12,148,52
R 36,60416
mvv 16,152,52
mvv 20,156,52
s_waitcnt lgkmcnt(2)
mvv 12,160,24
R 40,61440
mvv 16,164,24
mvv 20,168,24
mvv 12,172,28
R 44,62464
mvv 16,176,28
v_add_u32 v76, 1, v76
mvv 20,180,28
s_waitcnt lgkmcnt(2)
mvv 12,184,32
R 48,63488
mvv 16,188,32
mvv 20,192,32
mvv 12,196,36
R 52,64512
v_xor_b32 v80, 0x10000, v80
mvv 16,200,36
s_add_u32 s49, s49, 346112
mvv 20,204,36
s_waitcnt vmcnt(2)
s_barrier
s_waitcnt lgkmcnt(2)
mvv 12,208,40
R 24,0
mvv 16,212,40
s_add_u32 m0, s50, 32768
ds_read_b64 v[78:79], v89 offset:128
mvv 20,216,40
mvv 12,220,44
R 28,1024
D 85,0
mvv 16,224,44
mvv 20,228,44
s_waitcnt lgkmcnt(3)
mvv 12,232,48
R 32,2048
mvv 16,236,48
D 85,1024
mvv 20,240,48
mvv 12,244,52
R 36,3072
mvv 16,248,52
mvv 20,252,52
s_mov_b32 s60, -30
.p2align 6
LF2_1:
s_waitcnt lgkmcnt(2)
v_mfma_f32_16x16x32_bf16 v[0:3], a[0:3], v[24:27], 0
R 40,4096
D 85,2048
v_mfma_f32_16x16x32_bf16 v[4:7], a[4:7], v[24:27], 0
v_fma_f32 v104, v79, v56, v92
v_fma_f32 v12, v12, v78, v104
v_mfma_f32_16x16x32_bf16 v[8:11], a[8:11], v[24:27], 0
v_fma_f32 v105, v79, v57, v93
v_fma_f32 v13, v13, v78, v105
mav 0,12,28
R 44,5120
v_mul_f32_e64 v12, v12, |v12|
mav 4,16,28
s_waitcnt vmcnt(3)
buffer_store_dwordx2 v[76:77], v81, s[36:39], s52 offen sc1
mav 8,20,28
D 85,3072
s_waitcnt lgkmcnt(2)
mav 0,24,32
R 48,6144
v_mul_f32_e64 v13, v13, |v13|
mav 4,28,32
s_add_u32 m0, s50, 36864
v_cvt_pk_bf16_f32 v12, v12, v13
mav 8,32,32
v_pk_max_i16 v68, v12, 0
v_fma_f32 v104, v79, v58, v94
mav 0,36,36
R 52,7168
D 86,0
mav 4,40,36
v_fma_f32 v14, v14, v78, v104
v_fma_f32 v105, v79, v59, v95
mav 8,44,36
v_fma_f32 v15, v15, v78, v105
s_waitcnt lgkmcnt(2)
mav 0,48,40
R 24,8192
v_mul_f32_e64 v14, v14, |v14|
mav 4,52,40
D 86,1024
v_mul_f32_e64 v15, v15, |v15|
mav 8,56,40
v_cvt_pk_bf16_f32 v14, v14, v15
v_pk_max_i16 v69, v14, 0
mav 0,60,44
R 28,9216
v_fma_f32 v104, v79, v60, v96
mav 4,64,44
v_fma_f32 v16, v16, v78, v104
v_fma_f32 v105, v79, v61, v97
mav 8,68,44
v_fma_f32 v17, v17, v78, v105
s_waitcnt lgkmcnt(2)
mav 0,72,48
R 32,10240
D 86,2048
mav 4,76,48
v_mul_f32_e64 v16, v16, |v16|
v_mul_f32_e64 v17, v17, |v17|
mav 8,80,48
v_cvt_pk_bf16_f32 v16, v16, v17
v_pk_max_i16 v70, v16, 0
mav 0,84,52
R 36,11264
v_fma_f32 v104, v79, v62, v98
mav 4,88,52
D 86,3072
v_fma_f32 v18, v18, v78, v104
mav 8,92,52
v_fma_f32 v105, v79, v63, v99
s_waitcnt lgkmcnt(2)
mav 0,96,24
R 40,12288
v_fma_f32 v19, v19, v78, v105
mav 4,100,24
v_mul_f32_e64 v18, v18, |v18|
v_mul_f32_e64 v19, v19, |v19|
mav 8,104,24
v_cvt_pk_bf16_f32 v18, v18, v19
v_pk_max_i16 v71, v18, 0
mav 0,108,28
R 44,13312
buffer_store_dwordx4 v[68:71], v83, s[36:39], s49 offen sc1
mav 4,112,28
v_fma_f32 v104, v79, v64, v100
v_fma_f32 v20, v20, v78, v104
mav 8,116,28
v_fma_f32 v105, v79, v65, v101
s_waitcnt lgkmcnt(2)
mav 0,120,32
R 48,14336
v_fma_f32 v21, v21, v78, v105
mav 4,124,32
v_mul_f32_e64 v20, v20, |v20|
v_mul_f32_e64 v21, v21, |v21|
mav 8,128,32
v_cvt_pk_bf16_f32 v20, v20, v21
v_pk_max_i16 v72, v20, 0
mav 0,132,36
R 52,15360
v_pk_max_i16 v74, v20, 0
mav 4,136,36
v_fma_f32 v104, v79, v66, v102
v_fma_f32 v22, v22, v78, v104
mav 8,140,36
v_fma_f32 v105, v79, v67, v103
s_waitcnt lgkmcnt(2)
mav 0,144,40
R 24,16384
v_fma_f32 v23, v23, v78, v105
mav 4,148,40
v_mul_f32_e64 v22, v22, |v22|
v_mul_f32_e64 v23, v23, |v23|
mav 8,152,40
v_cvt_pk_bf16_f32 v22, v22, v23
v_pk_max_i16 v73, v22, 0
mav 0,156,44
R 28,17408
v_pk_max_i16 v75, v22, 0
mav 4,160,44
v_permlane16_swap_b32 v72, v74
v_permlane16_swap_b32 v73, v75
mav 8,164,44
buffer_store_dwordx4 v[72:75], v84, s[36:39], s49 offen sc1
s_waitcnt lgkmcnt(2)
mav 0,168,48
R 32,18432
mav 4,172,48
mav 8,176,48
mav 0,180,52
R 36,19456
mav 4,184,52
mav 8,188,52
s_waitcnt lgkmcnt(2)
mav 0,192,24
R 40,20480
mav 4,196,24
mav 8,200,24
mav 0,204,28
R 44,21504
mav 4,208,28
mav 8,212,28
s_waitcnt lgkmcnt(2)
mav 0,216,32
R 48,22528
mav 4,220,32
mav 8,224,32
mav 0,228,36
R 52,23552
mav 4,232,36
mav 8,236,36
s_waitcnt lgkmcnt(2)
mav 0,240,40
R 24,24576
mav 4,244,40
mav 8,248,40
mav 0,252,44
R 28,25600
mvv 4,128,44
mvv 8,132,44
s_waitcnt lgkmcnt(2)
mvv 0,136,48
R 32,26624
mvv 4,140,48
mvv 8,144,48
mvv 0,148,52
R 36,27648
mvv 4,152,52
mvv 8,156,52
s_waitcnt lgkmcnt(2)
mvv 0,160,24
R 40,28672
mvv 4,164,24
mvv 8,168,24
mvv 0,172,28
R 44,29696
mvv 4,176,28
v_add_u32 v76, 1, v76
mvv 8,180,28
s_waitcnt lgkmcnt(2)
mvv 0,184,32
R 48,30720
s_xor_b32 s50, s50, 0x10000
mvv 4,188,32
mvv 8,192,32
mvv 0,196,36
R 52,31744
mvv 4,200,36
s_add_u32 s49, s49, 346112
mvv 8,204,36
s_waitcnt vmcnt(2)
s_barrier
s_waitcnt lgkmcnt(2)
mvv 0,208,40
R 24,32768
mvv 4,212,40
s_add_u32 m0, s50, 0
ds_read_b64 v[78:79], v89 offset:0
mvv 8,216,40
mvv 0,220,44
R 28,33792
D 85,0
mvv 4,224,44
mvv 8,228,44
s_waitcnt lgkmcnt(3)
mvv 0,232,48
R 32,34816
mvv 4,236,48
D 85,1024
mvv 8,240,48
mvv 0,244,52
R 36,35840
mvv 4,248,52
mvv 8,252,52
s_waitcnt lgkmcnt(2)
v_mfma_f32_16x16x32_bf16 v[12:15], a[0:3], v[24:27], 0
R 40,36864
D 85,2048
v_mfma_f32_16x16x32_bf16 v[16:19], a[4:7], v[24:27], 0
v_fma_f32 v104, v79, v56, v92
v_fma_f32 v0, v0, v78, v104
v_mfma_f32_16x16x32_bf16 v[20:23], a[8:11], v[24:27], 0
v_fma_f32 v105, v79, v57, v93
v_fma_f32 v1, v1, v78, v105
mav 12,12,28
R 44,37888
v_mul_f32_e64 v0, v0, |v0|
mav 16,16,28
s_waitcnt vmcnt(3)
buffer_store_dwordx2 v[76:77], v81, s[36:39], s52 offen sc1
mav 20,20,28
D 85,3072
s_waitcnt lgkmcnt(2)
mav 12,24,32
R 48,38912
v_mul_f32_e64 v1, v1, |v1|
mav 16,28,32
s_add_u32 m0, s50, 4096
v_cvt_pk_bf16_f32 v0, v0, v1
mav 20,32,32
v_pk_max_i16 v68, v0, 0
v_fma_f32 v104, v79, v58, v94
mav 12,36,36
R 52,39936
D 86,0
mav 16,40,36
v_fma_f32 v2, v2, v78, v104
v_fma_f32 v105, v79, v59, v95
mav 20,44,36
v_fma_f32 v3, v3, v78, v105
s_waitcnt lgkmcnt(2)
mav 12,48,40
R 24,40960
v_mul_f32_e64 v2, v2, |v2|
mav 16,52,40
D 86,1024
v_mul_f32_e64 v3, v3, |v3|
mav 20,56,40
v_cvt_pk_bf16_f32 v2, v2, v3
v_pk_max_i16 v69, v2, 0
mav 12,60,44
R 28,41984
v_fma_f32 v104, v79, v60, v96
mav 16,64,44
v_fma_f32 v4, v4, v78, v104
v_fma_f32 v105, v79, v61, v97
mav 20,68,44
v_fma_f32 v5, v5, v78, v105
LsF10:
s_cbranch_execz LwF
s_waitcnt lgkmcnt(2)
mav 12,72,48
R 32,43008
D 86,2048
mav 16,76,48
v_mul_f32_e64 v4, v4, |v4|
v_mul_f32_e64 v5, v5, |v5|
mav 20,80,48
v_cvt_pk_bf16_f32 v4, v4, v5
v_pk_max_i16 v70, v4, 0
mav 12,84,52
R 36,44032
v_fma_f32 v104, v79, v62, v98
mav 16,88,52
D 86,3072
v_fma_f32 v6, v6, v78, v104
mav 20,92,52
v_fma_f32 v105, v79, v63, v99
s_waitcnt lgkmcnt(2)
mav 12,96,24
R 40,45056
v_fma_f32 v7, v7, v78, v105
mav 16,100,24
v_mul_f32_e64 v6, v6, |v6|
v_mul_f32_e64 v7, v7, |v7|
mav 20,104,24
v_cvt_pk_bf16_f32 v6, v6, v7
v_pk_max_i16 v71, v6, 0
mav 12,108,28
R 44,46080
buffer_store_dwordx4 v[68:71], v83, s[36:39], s49 offen sc1
mav 16,112,28
v_fma_f32 v104, v79, v64, v100
v_fma_f32 v8, v8, v78, v104
mav 20,116,28
v_fma_f32 v105, v79, v65, v101
s_waitcnt lgkmcnt(2)
mav 12,120,32
R 48,47104
v_fma_f32 v9, v9, v78, v105
mav 16,124,32
v_mul_f32_e64 v8, v8, |v8|
v_mul_f32_e64 v9, v9, |v9|
mav 20,128,32
v_cvt_pk_bf16_f32 v8, v8, v9
v_pk_max_i16 v72, v8, 0
mav 12,132,36
R 52,48128
v_pk_max_i16 v74, v8, 0
mav 16,136,36
v_fma_f32 v104, v79, v66, v102
v_fma_f32 v10, v10, v78, v104
mav 20,140,36
v_fma_f32 v105, v79, v67, v103
s_waitcnt lgkmcnt(2)
mav 12,144,40
R 24,49152
v_fma_f32 v11, v11, v78, v105
mav 16,148,40
v_mul_f32_e64 v10, v10, |v10|
v_mul_f32_e64 v11, v11, |v11|
mav 20,152,40
v_cvt_pk_bf16_f32 v10, v10, v11
v_pk_max_i16 v73, v10, 0
mav 12,156,44
R 28,50176
v_pk_max_i16 v75, v10, 0
mav 16,160,44
v_permlane16_swap_b32 v72, v74
v_permlane16_swap_b32 v73, v75
mav 20,164,44
buffer_store_dwordx4 v[72:75], v84, s[36:39], s49 offen sc1
s_waitcnt lgkmcnt(2)
mav 12,168,48
R 32,51200
mav 16,172,48
mav 20,176,48
mav 12,180,52
R 36,52224
mav 16,184,52
mav 20,188,52
s_waitcnt lgkmcnt(2)
mav 12,192,24
R 40,53248
mav 16,196,24
mav 20,200,24
mav 12,204,28
R 44,54272
mav 16,208,28
mav 20,212,28
s_waitcnt lgkmcnt(2)
mav 12,216,32
R 48,55296
mav 16,220,32
mav 20,224,32
mav 12,228,36
R 52,56320
mav 16,232,36
mav 20,236,36
s_waitcnt lgkmcnt(2)
mav 12,240,40
R 24,57344
mav 16,244,40
mav 20,248,40
mav 12,252,44
R 28,58368
mvv 16,128,44
mvv 20,132,44
s_waitcnt lgkmcnt(2)
mvv 12,136,48
R 32,59392
mvv 16,140,48
mvv 20,144,48
mvv 12,148,52
R 36,60416
mvv 16,152,52
mvv 20,156,52
s_waitcnt lgkmcnt(2)
mvv 12,160,24
R 40,61440
mvv 16,164,24
mvv 20,168,24
mvv 12,172,28
R 44,62464
mvv 16,176,28
v_add_u32 v76, 1, v76
mvv 20,180,28
s_waitcnt lgkmcnt(2)
mvv 12,184,32
R 48,63488
mvv 16,188,32
mvv 20,192,32
mvv 12,196,36
R 52,64512
v_xor_b32 v80, 0x10000, v80
mvv 16,200,36
s_add_u32 s49, s49, 346112
mvv 20,204,36
s_waitcnt vmcnt(2)
s_barrier
s_waitcnt lgkmcnt(2)
mvv 12,208,40
R 24,0
mvv 16,212,40
s_add_u32 m0, s50, 32768
ds_read_b64 v[78:79], v89 offset:128
mvv 20,216,40
mvv 12,220,44
R 28,1024
D 85,0
mvv 16,224,44
mvv 20,228,44
s_waitcnt lgkmcnt(3)
mvv 12,232,48
R 32,2048
mvv 16,236,48
D 85,1024
mvv 20,240,48
mvv 12,244,52
R 36,3072
mvv 16,248,52
mvv 20,252,52
s_add_u32 s60, s60, 1
s_cbranch_scc0 LF2_1
s_waitcnt lgkmcnt(2)
v_mfma_f32_16x16x32_bf16 v[0:3], a[0:3], v[24:27], 0
R 40,4096
D 85,2048
v_mfma_f32_16x16x32_bf16 v[4:7], a[4:7], v[24:27], 0
v_fma_f32 v104, v79, v56, v92
v_fma_f32 v12, v12, v78, v104
v_mfma_f32_16x16x32_bf16 v[8:11], a[8:11], v[24:27], 0
v_fma_f32 v105, v79, v57, v93
v_fma_f32 v13, v13, v78, v105
mav 0,12,28
R 44,5120
v_mul_f32_e64 v12, v12, |v12|
mav 4,16,28
s_waitcnt vmcnt(3)
buffer_store_dwordx2 v[76:77], v81, s[36:39], s52 offen sc1
mav 8,20,28
D 85,3072
s_waitcnt lgkmcnt(2)
mav 0,24,32
R 48,6144
v_mul_f32_e64 v13, v13, |v13|
mav 4,28,32
s_add_u32 m0, s50, 36864
v_cvt_pk_bf16_f32 v12, v12, v13
mav 8,32,32
v_pk_max_i16 v68, v12, 0
v_fma_f32 v104, v79, v58, v94
mav 0,36,36
R 52,7168
D 86,0
mav 4,40,36
v_fma_f32 v14, v14, v78, v104
v_fma_f32 v105, v79, v59, v95
mav 8,44,36
v_fma_f32 v15, v15, v78, v105
s_waitcnt lgkmcnt(2)
mav 0,48,40
R 24,8192
v_mul_f32_e64 v14, v14, |v14|
mav 4,52,40
D 86,1024
v_mul_f32_e64 v15, v15, |v15|
mav 8,56,40
v_cvt_pk_bf16_f32 v14, v14, v15
v_pk_max_i16 v69, v14, 0
mav 0,60,44
R 28,9216
v_fma_f32 v104, v79, v60, v96
mav 4,64,44
v_fma_f32 v16, v16, v78, v104
v_fma_f32 v105, v79, v61, v97
mav 8,68,44
v_fma_f32 v17, v17, v78, v105
s_waitcnt lgkmcnt(2)
mav 0,72,48
R 32,10240
D 86,2048
mav 4,76,48
v_mul_f32_e64 v16, v16, |v16|
v_mul_f32_e64 v17, v17, |v17|
mav 8,80,48
v_cvt_pk_bf16_f32 v16, v16, v17
v_pk_max_i16 v70, v16, 0
mav 0,84,52
R 36,11264
v_fma_f32 v104, v79, v62, v98
mav 4,88,52
D 86,3072
v_fma_f32 v18, v18, v78, v104
mav 8,92,52
v_fma_f32 v105, v79, v63, v99
s_waitcnt lgkmcnt(2)
mav 0,96,24
R 40,12288
v_fma_f32 v19, v19, v78, v105
mav 4,100,24
v_mul_f32_e64 v18, v18, |v18|
v_mul_f32_e64 v19, v19, |v19|
mav 8,104,24
v_cvt_pk_bf16_f32 v18, v18, v19
v_pk_max_i16 v71, v18, 0
mav 0,108,28
R 44,13312
buffer_store_dwordx4 v[68:71], v83, s[36:39], s49 offen sc1
mav 4,112,28
v_fma_f32 v104, v79, v64, v100
v_fma_f32 v20, v20, v78, v104
mav 8,116,28
v_fma_f32 v105, v79, v65, v101
s_waitcnt lgkmcnt(2)
mav 0,120,32
R 48,14336
v_fma_f32 v21, v21, v78, v105
mav 4,124,32
v_mul_f32_e64 v20, v20, |v20|
v_mul_f32_e64 v21, v21, |v21|
mav 8,128,32
v_cvt_pk_bf16_f32 v20, v20, v21
v_pk_max_i16 v72, v20, 0
mav 0,132,36
R 52,15360
v_pk_max_i16 v74, v20, 0
mav 4,136,36
v_fma_f32 v104, v79, v66, v102
v_fma_f32 v22, v22, v78, v104
mav 8,140,36
v_fma_f32 v105, v79, v67, v103
s_waitcnt lgkmcnt(2)
mav 0,144,40
R 24,16384
v_fma_f32 v23, v23, v78, v105
mav 4,148,40
v_mul_f32_e64 v22, v22, |v22|
v_mul_f32_e64 v23, v23, |v23|
mav 8,152,40
v_cvt_pk_bf16_f32 v22, v22, v23
v_pk_max_i16 v73, v22, 0
mav 0,156,44
R 28,17408
v_pk_max_i16 v75, v22, 0
mav 4,160,44
v_permlane16_swap_b32 v72, v74
v_permlane16_swap_b32 v73, v75
mav 8,164,44
buffer_store_dwordx4 v[72:75], v84, s[36:39], s49 offen sc1
s_waitcnt lgkmcnt(2)
mav 0,168,48
R 32,18432
mav 4,172,48
mav 8,176,48
mav 0,180,52
R 36,19456
mav 4,184,52
mav 8,188,52
s_waitcnt lgkmcnt(2)
mav 0,192,24
R 40,20480
mav 4,196,24
mav 8,200,24
mav 0,204,28
R 44,21504
mav 4,208,28
mav 8,212,28
s_waitcnt lgkmcnt(2)
mav 0,216,32
R 48,22528
mav 4,220,32
mav 8,224,32
mav 0,228,36
R 52,23552
mav 4,232,36
mav 8,236,36
s_waitcnt lgkmcnt(2)
mav 0,240,40
R 24,24576
mav 4,244,40
mav 8,248,40
mav 0,252,44
R 28,25600
mvv 4,128,44
mvv 8,132,44
s_waitcnt lgkmcnt(2)
mvv 0,136,48
R 32,26624
mvv 4,140,48
mvv 8,144,48
mvv 0,148,52
R 36,27648
mvv 4,152,52
mvv 8,156,52
s_waitcnt lgkmcnt(2)
mvv 0,160,24
R 40,28672
mvv 4,164,24
mvv 8,168,24
mvv 0,172,28
R 44,29696
mvv 4,176,28
v_add_u32 v76, 1, v76
mvv 8,180,28
s_waitcnt lgkmcnt(2)
mvv 0,184,32
R 48,30720
mvv 4,188,32
mvv 8,192,32
mvv 0,196,36
R 52,31744
mvv 4,200,36
s_add_u32 s49, s49, 346112
mvv 8,204,36
s_waitcnt vmcnt(2)
s_barrier
s_waitcnt lgkmcnt(2)
mvv 0,208,40
R 24,32768
mvv 4,212,40
ds_read_b64 v[78:79], v89 offset:0
mvv 8,216,40
mvv 0,220,44
R 28,33792
mvv 4,224,44
mvv 8,228,44
s_waitcnt lgkmcnt(3)
mvv 0,232,48
R 32,34816
mvv 4,236,48
mvv 8,240,48
mvv 0,244,52
R 36,35840
mvv 4,248,52
mvv 8,252,52
s_waitcnt lgkmcnt(2)
v_mfma_f32_16x16x32_bf16 v[12:15], a[0:3], v[24:27], 0
R 40,36864
v_fma_f32 v104, v79, v56, v92
v_mfma_f32_16x16x32_bf16 v[16:19], a[4:7], v[24:27], 0
v_fma_f32 v0, v0, v78, v104
v_fma_f32 v105, v79, v57, v93
v_mfma_f32_16x16x32_bf16 v[20:23], a[8:11], v[24:27], 0
v_fma_f32 v1, v1, v78, v105
v_mul_f32_e64 v0, v0, |v0|
mav 12,12,28
R 44,37888
v_mul_f32_e64 v1, v1, |v1|
mav 16,16,28
s_waitcnt vmcnt(0)
buffer_store_dwordx2 v[76:77], v81, s[36:39], s52 offen sc1
mav 20,20,28
v_cvt_pk_bf16_f32 v0, v0, v1
s_waitcnt lgkmcnt(2)
mav 12,24,32
R 48,38912
v_pk_max_i16 v68, v0, 0
mav 16,28,32
v_fma_f32 v104, v79, v58, v94
v_fma_f32 v2, v2, v78, v104
mav 20,32,32
v_fma_f32 v105, v79, v59, v95
v_fma_f32 v3, v3, v78, v105
mav 12,36,36
R 52,39936
v_mul_f32_e64 v2, v2, |v2|
mav 16,40,36
v_mul_f32_e64 v3, v3, |v3|
v_cvt_pk_bf16_f32 v2, v2, v3
mav 20,44,36
v_pk_max_i16 v69, v2, 0
s_waitcnt lgkmcnt(2)
mav 12,48,40
R 24,40960
v_fma_f32 v104, v79, v60, v96
mav 16,52,40
v_fma_f32 v4, v4, v78, v104
v_fma_f32 v105, v79, v61, v97
mav 20,56,40
v_fma_f32 v5, v5, v78, v105
v_mul_f32_e64 v4, v4, |v4|
mav 12,60,44
R 28,41984
v_mul_f32_e64 v5, v5, |v5|
mav 16,64,44
v_cvt_pk_bf16_f32 v4, v4, v5
v_pk_max_i16 v70, v4, 0
mav 20,68,44
v_fma_f32 v104, v79, v62, v98
s_waitcnt lgkmcnt(2)
mav 12,72,48
R 32,43008
v_fma_f32 v6, v6, v78, v104
mav 16,76,48
v_fma_f32 v105, v79, v63, v99
v_fma_f32 v7, v7, v78, v105
mav 20,80,48
v_mul_f32_e64 v6, v6, |v6|
v_mul_f32_e64 v7, v7, |v7|
mav 12,84,52
R 36,44032
v_cvt_pk_bf16_f32 v6, v6, v7
mav 16,88,52
v_pk_max_i16 v71, v6, 0
buffer_store_dwordx4 v[68:71], v83, s[36:39], s49 offen sc1
mav 20,92,52
v_fma_f32 v104, v79, v64, v100
s_waitcnt lgkmcnt(2)
mav 12,96,24
R 40,45056
v_fma_f32 v8, v8, v78, v104
mav 16,100,24
v_fma_f32 v105, v79, v65, v101
v_fma_f32 v9, v9, v78, v105
mav 20,104,24
v_mul_f32_e64 v8, v8, |v8|
v_mul_f32_e64 v9, v9, |v9|
mav 12,108,28
R 44,46080
v_cvt_pk_bf16_f32 v8, v8, v9
mav 16,112,28
v_pk_max_i16 v72, v8, 0
v_pk_max_i16 v74, v8, 0
mav 20,116,28
v_fma_f32 v104, v79, v66, v102
s_waitcnt lgkmcnt(2)
mav 12,120,32
R 48,47104
v_fma_f32 v10, v10, v78, v104
mav 16,124,32
v_fma_f32 v105, v79, v67, v103
v_fma_f32 v11, v11, v78, v105
mav 20,128,32
v_mul_f32_e64 v10, v10, |v10|
v_mul_f32_e64 v11, v11, |v11|
mav 12,132,36
R 52,48128
v_cvt_pk_bf16_f32 v10, v10, v11
mav 16,136,36
v_pk_max_i16 v73, v10, 0
v_pk_max_i16 v75, v10, 0
mav 20,140,36
v_permlane16_swap_b32 v72, v74
s_waitcnt lgkmcnt(2)
mav 12,144,40
R 24,49152
v_permlane16_swap_b32 v73, v75
mav 16,148,40
buffer_store_dwordx4 v[72:75], v84, s[36:39], s49 offen sc1
mav 20,152,40
mav 12,156,44
R 28,50176
mav 16,160,44
mav 20,164,44
s_waitcnt lgkmcnt(2)
mav 12,168,48
R 32,51200
mav 16,172,48
mav 20,176,48
mav 12,180,52
R 36,52224
mav 16,184,52
mav 20,188,52
s_waitcnt lgkmcnt(2)
mav 12,192,24
R 40,53248
mav 16,196,24
mav 20,200,24
mav 12,204,28
R 44,54272
mav 16,208,28
mav 20,212,28
s_waitcnt lgkmcnt(2)
mav 12,216,32
R 48,55296
mav 16,220,32
mav 20,224,32
mav 12,228,36
R 52,56320
mav 16,232,36
mav 20,236,36
s_waitcnt lgkmcnt(2)
mav 12,240,40
R 24,57344
mav 16,244,40
mav 20,248,40
mav 12,252,44
R 28,58368
mvv 16,128,44
mvv 20,132,44
s_waitcnt lgkmcnt(2)
mvv 12,136,48
R 32,59392
mvv 16,140,48
mvv 20,144,48
mvv 12,148,52
R 36,60416
mvv 16,152,52
mvv 20,156,52
LsF11:
s_cbranch_execz LwF
s_waitcnt lgkmcnt(2)
mvv 12,160,24
R 40,61440
mvv 16,164,24
)"
R"(
mvv 20,168,24
mvv 12,172,28
R 44,62464
mvv 16,176,28
v_add_u32 v76, 1, v76
mvv 20,180,28
s_waitcnt lgkmcnt(2)
mvv 12,184,32
R 48,63488
mvv 16,188,32
mvv 20,192,32
mvv 12,196,36
R 52,64512
mvv 16,200,36
s_add_u32 s49, s49, 346112
mvv 20,204,36
s_barrier
s_waitcnt lgkmcnt(2)
mvv 12,208,40
mvv 16,212,40
ds_read_b64 v[78:79], v89 offset:128
mvv 20,216,40
mvv 12,220,44
mvv 16,224,44
mvv 20,228,44
s_waitcnt lgkmcnt(1)
mvv 12,232,48
mvv 16,236,48
mvv 20,240,48
mvv 12,244,52
mvv 16,248,52
mvv 20,252,52
s_waitcnt lgkmcnt(0)
v_fma_f32 v104, v79, v56, v92
s_nop 3
v_fma_f32 v12, v12, v78, v104
v_fma_f32 v105, v79, v57, v93
v_fma_f32 v13, v13, v78, v105
v_mul_f32_e64 v12, v12, |v12|
v_mul_f32_e64 v13, v13, |v13|
v_cvt_pk_bf16_f32 v12, v12, v13
v_pk_max_i16 v68, v12, 0
v_fma_f32 v104, v79, v58, v94
v_fma_f32 v14, v14, v78, v104
v_fma_f32 v105, v79, v59, v95
v_fma_f32 v15, v15, v78, v105
v_mul_f32_e64 v14, v14, |v14|
v_mul_f32_e64 v15, v15, |v15|
v_cvt_pk_bf16_f32 v14, v14, v15
v_pk_max_i16 v69, v14, 0
v_fma_f32 v104, v79, v60, v96
v_fma_f32 v16, v16, v78, v104
v_fma_f32 v105, v79, v61, v97
v_fma_f32 v17, v17, v78, v105
v_mul_f32_e64 v16, v16, |v16|
v_mul_f32_e64 v17, v17, |v17|
v_cvt_pk_bf16_f32 v16, v16, v17
v_pk_max_i16 v70, v16, 0
v_fma_f32 v104, v79, v62, v98
v_fma_f32 v18, v18, v78, v104
v_fma_f32 v105, v79, v63, v99
v_fma_f32 v19, v19, v78, v105
v_mul_f32_e64 v18, v18, |v18|
v_mul_f32_e64 v19, v19, |v19|
v_cvt_pk_bf16_f32 v18, v18, v19
v_pk_max_i16 v71, v18, 0
buffer_store_dwordx4 v[68:71], v83, s[36:39], s49 offen sc1
v_fma_f32 v104, v79, v64, v100
v_fma_f32 v20, v20, v78, v104
v_fma_f32 v105, v79, v65, v101
v_fma_f32 v21, v21, v78, v105
v_mul_f32_e64 v20, v20, |v20|
v_mul_f32_e64 v21, v21, |v21|
v_cvt_pk_bf16_f32 v20, v20, v21
v_pk_max_i16 v72, v20, 0
v_pk_max_i16 v74, v20, 0
v_fma_f32 v104, v79, v66, v102
v_fma_f32 v22, v22, v78, v104
v_fma_f32 v105, v79, v67, v103
v_fma_f32 v23, v23, v78, v105
v_mul_f32_e64 v22, v22, |v22|
v_mul_f32_e64 v23, v23, |v23|
v_cvt_pk_bf16_f32 v22, v22, v23
v_pk_max_i16 v73, v22, 0
v_pk_max_i16 v75, v22, 0
v_permlane16_swap_b32 v72, v74
s_nop 0
v_permlane16_swap_b32 v73, v75
buffer_store_dwordx4 v[72:75], v84, s[36:39], s49 offen sc1
s_waitcnt vmcnt(2)
buffer_store_dwordx2 v[76:77], v81, s[36:39], s52 offen sc1
v_add_u32 v76, 37, v76
s_waitcnt vmcnt(1)
buffer_store_dwordx2 v[76:77], v81, s[36:39], s52 offen sc1
s_endpgm
Lr4:
s_cmp_eq_u32 s60, 4
s_cbranch_scc0 Lr5
s_waitcnt vmcnt(0)
v_accvgpr_read_b32 v73, a255
v_mov_b32 v72, 0x100
v_add_u32 v73, 0x9e3779b9, v73
buffer_store_dwordx2 v[72:73], v81, s[36:39], s52 offen sc1
v_add_u32 v86, v86, v72
s_sleep 32
s_sleep 32
s_sleep 32
E 0,65536
E 1,69632
E 2,73728
E 3,77824
s_add_u32 s51, s51, 65536
E 0,81920
E 1,86016
E 2,90112
E 3,94208
s_add_u32 s51, s51, 65536
E 0,98304
E 1,102400
E 2,106496
E 3,110592
s_add_u32 s51, s51, 65536
E 0,114688
E 1,118784
E 2,122880
E 3,126976
s_add_u32 s51, s51, 65536
s_mov_b64 exec, 0
s_cmp_eq_u32 s53, 0
s_cbranch_scc1 LsG0
s_cmp_eq_u32 s53, 1024
s_cbranch_scc1 LsG1
s_cmp_eq_u32 s53, 2048
s_cbranch_scc1 LsG2
s_branch LsG3
LwG:
s_mov_b64 exec, -1
s_waitcnt vmcnt(8)
s_barrier
v_add_u32 v4, 65536, v4
v_add_u32 v5, 65536, v5
v_add_u32 v6, 65536, v6
v_add_u32 v7, 65536, v7
Ta 0,4,0
Ta 4,5,0
Ta 8,6,0
Ta 12,7,0
Ta 16,4,16384
Ta 20,5,16384
Ta 24,6,16384
Ta 28,7,16384
E 0,131072
E 1,135168
E 2,139264
E 3,143360
s_add_u32 s51, s51, 65536
E 0,147456
E 1,151552
E 2,155648
E 3,159744
s_add_u32 s51, s51, 65536
s_cmp_ge_u32 s51, s57
s_cselect_b32 s60, 1572864, 0
s_sub_u32 s51, s51, s60
s_waitcnt lgkmcnt(0)
s_waitcnt vmcnt(8)
s_barrier
E 0,65536
Ta 32,4,32768
Ta 36,5,32768
E 1,69632
Ta 40,6,32768
Ta 44,7,32768
E 2,73728
Ta 48,4,49152
E 3,77824
s_add_u32 s51, s51, 65536
Ta 52,5,49152
Ta 56,6,49152
E 0,81920
Ta 60,7,49152
E 1,86016
E 2,90112
E 3,94208
s_add_u32 s51, s51, 65536
s_waitcnt lgkmcnt(0)
s_waitcnt vmcnt(8)
s_barrier
v_add_u32 v4, 65536, v4
v_add_u32 v5, 65536, v5
v_add_u32 v6, 65536, v6
v_add_u32 v7, 65536, v7
E 0,98304
Ta 64,4,0
Ta 68,5,0
E 1,102400
Ta 72,6,0
Ta 76,7,0
E 2,106496
Ta 80,4,16384
E 3,110592
s_add_u32 s51, s51, 65536
Ta 84,5,16384
Ta 88,6,16384
E 0,114688
Ta 92,7,16384
E 1,118784
E 2,122880
E 3,126976
s_add_u32 s51, s51, 65536
s_waitcnt lgkmcnt(0)
s_waitcnt vmcnt(8)
s_barrier
v_add_u32 v4, -65536, v4
v_add_u32 v5, -65536, v5
v_add_u32 v6, -65536, v6
v_add_u32 v7, -65536, v7
E 0,131072
Ta 96,4,0
Ta 100,5,0
E 1,135168
Ta 104,6,0
Ta 108,7,0
E 2,139264
Ta 112,4,16384
E 3,143360
s_add_u32 s51, s51, 65536
Ta 116,5,16384
Ta 120,6,16384
E 0,147456
Ta 124,7,16384
E 1,151552
E 2,155648
E 3,159744
s_add_u32 s51, s51, 65536
s_cmp_ge_u32 s51, s57
s_cselect_b32 s60, 1572864, 0
s_sub_u32 s51, s51, s60
s_waitcnt lgkmcnt(0)
s_waitcnt vmcnt(8)
s_barrier
E 0,65536
Ta 128,4,32768
Ta 132,5,32768
E 1,69632
Ta 136,6,32768
Ta 140,7,32768
E 2,73728
Ta 144,4,49152
E 3,77824
s_add_u32 s51, s51, 65536
Ta 148,5,49152
Ta 152,6,49152
E 0,81920
Ta 156,7,49152
E 1,86016
E 2,90112
E 3,94208
s_add_u32 s51, s51, 65536
s_waitcnt lgkmcnt(0)
s_waitcnt vmcnt(8)
s_barrier
v_add_u32 v4, 65536, v4
v_add_u32 v5, 65536, v5
v_add_u32 v6, 65536, v6
v_add_u32 v7, 65536, v7
E 0,98304
Ta 160,4,0
Ta 164,5,0
E 1,102400
Ta 168,6,0
Ta 172,7,0
E 2,106496
Ta 176,4,16384
E 3,110592
s_add_u32 s51, s51, 65536
Ta 180,5,16384
Ta 184,6,16384
E 0,114688
Ta 188,7,16384
E 1,118784
E 2,122880
E 3,126976
s_add_u32 s51, s51, 65536
s_waitcnt lgkmcnt(0)
s_waitcnt vmcnt(8)
s_barrier
v_add_u32 v4, -65536, v4
v_add_u32 v5, -65536, v5
v_add_u32 v6, -65536, v6
v_add_u32 v7, -65536, v7
E 0,131072
Ta 192,4,0
Ta 196,5,0
E 1,135168
Ta 200,6,0
Ta 204,7,0
E 2,139264
Ta 208,4,16384
E 3,143360
s_add_u32 s51, s51, 65536
Ta 212,5,16384
Ta 216,6,16384
E 0,147456
Ta 220,7,16384
E 1,151552
E 2,155648
E 3,159744
s_add_u32 s51, s51, 65536
s_cmp_ge_u32 s51, s57
s_cselect_b32 s60, 1572864, 0
s_sub_u32 s51, s51, s60
s_waitcnt lgkmcnt(0)
s_waitcnt vmcnt(8)
s_barrier
E 0,65536
Ta 224,4,32768
Ta 228,5,32768
E 1,69632
Ta 232,6,32768
Ta 236,7,32768
E 2,73728
Ta 240,4,49152
E 3,77824
s_add_u32 s51, s51, 65536
Ta 244,5,49152
Ta 248,6,49152
E 0,81920
Ta 252,7,49152
E 1,86016
E 2,90112
E 3,94208
s_add_u32 s51, s51, 65536
s_waitcnt lgkmcnt(0)
s_waitcnt vmcnt(8)
s_barrier
v_add_u32 v4, 65536, v4
v_add_u32 v5, 65536, v5
v_add_u32 v6, 65536, v6
v_add_u32 v7, 65536, v7
E 0,98304
Tv 128,4,0
Tv 132,5,0
E 1,102400
Tv 136,6,0
Tv 140,7,0
E 2,106496
LsG0:
s_cbranch_execz LsG4
Tv 144,4,16384
E 3,110592
s_add_u32 s51, s51, 65536
Tv 148,5,16384
Tv 152,6,16384
E 0,114688
Tv 156,7,16384
E 1,118784
E 2,122880
E 3,126976
s_add_u32 s51, s51, 65536
s_waitcnt lgkmcnt(0)
s_waitcnt vmcnt(8)
s_barrier
v_add_u32 v4, -65536, v4
v_add_u32 v5, -65536, v5
v_add_u32 v6, -65536, v6
v_add_u32 v7, -65536, v7
E 0,131072
Tv 160,4,0
Tv 164,5,0
E 1,135168
Tv 168,6,0
Tv 172,7,0
E 2,139264
Tv 176,4,16384
E 3,143360
s_add_u32 s51, s51, 65536
Tv 180,5,16384
Tv 184,6,16384
E 0,147456
Tv 188,7,16384
E 1,151552
E 2,155648
E 3,159744
s_add_u32 s51, s51, 65536
s_cmp_ge_u32 s51, s57
s_cselect_b32 s60, 1572864, 0
s_sub_u32 s51, s51, s60
s_waitcnt lgkmcnt(0)
s_waitcnt vmcnt(8)
s_barrier
Tv 192,4,32768
Tv 196,5,32768
Tv 200,6,32768
Tv 204,7,32768
Tv 208,4,49152
Tv 212,5,49152
Tv 216,6,49152
Tv 220,7,49152
s_waitcnt lgkmcnt(0)
s_waitcnt vmcnt(0)
s_barrier
v_add_u32 v4, 65536, v4
v_add_u32 v5, 65536, v5
v_add_u32 v6, 65536, v6
v_add_u32 v7, 65536, v7
Tv 224,4,0
Tv 228,5,0
Tv 232,6,0
Tv 236,7,0
Tv 240,4,16384
Tv 244,5,16384
Tv 248,6,16384
Tv 252,7,16384
s_waitcnt lgkmcnt(0)
s_waitcnt vmcnt(0)
s_barrier
s_nop 7
s_and_b32 s60, s48, 3
s_cmp_eq_u32 s60, 1
s_cbranch_scc1 LvG1
LvG0:
LsG1:
s_cbranch_execz LsG5
v_add_u32 v72, 1, v72
buffer_load_dwordx2 v[74:75], v85, s[36:39], 0 offen sc1
s_waitcnt vmcnt(0)
ds_write_b32 v82, v74
v_sub_u32 v74, v74, v86
v_xor_b32 v75, v75, v73
v_cmp_lt_u64 vcc, s[62:63], v[74:75]
s_waitcnt lgkmcnt(0)
s_cbranch_vccnz CG0_1
BG0_1:
s_add_u32 s49, s49, 346112
s_barrier
s_add_u32 m0, s50, 0
buffer_store_dwordx2 v[72:73], v81, s[36:39], s52 offen sc1
D 83,0
D 83,1024
D 83,2048
D 83,3072
s_add_u32 m0, s50, 4096
s_nop 0
D 84,0
D 84,1024
s_mov_b32 m0, 142336
s_nop 0
buffer_load_dword v85, s[36:39], 0 offen sc1 lds
v_add_u32 v86, 1, v86
v_add_u32 v72, 1, v72
ds_read_b32 v74, v82 offset:0
s_waitcnt lgkmcnt(0)
v_cmp_gt_i32 vcc, v86, v74
s_waitcnt vmcnt(1)
s_cbranch_vccnz CG0_2
BG0_2:
s_add_u32 s49, s49, 346112
s_barrier
s_add_u32 m0, s50, 32768
R 32,0
buffer_store_dwordx2 v[72:73], v81, s[36:39], s52 offen sc1
D 83,0
R 36,1024
D 83,1024
R 40,2048
D 83,2048
s_waitcnt lgkmcnt(1)
v_mfma_f32_16x16x32_bf16 v[0:3], a[0:3], v[32:35], 0
R 44,3072
v_mfma_f32_16x16x32_bf16 v[4:7], a[4:7], v[32:35], 0
v_mfma_f32_16x16x32_bf16 v[8:11], a[8:11], v[32:35], 0
D 83,3072
v_mfma_f32_16x16x32_bf16 v[12:15], a[12:15], v[32:35], 0
mav 0,16,36
R 48,4096
mav 4,20,36
s_add_u32 m0, s50, 36864
mav 8,24,36
mav 12,28,36
D 84,0
s_waitcnt lgkmcnt(1)
mav 0,32,40
R 52,5120
mav 4,36,40
mav 8,40,40
mav 12,44,40
D 84,1024
mav 0,48,44
R 56,6144
mav 4,52,44
mav 8,56,44
mav 12,60,44
s_waitcnt lgkmcnt(1)
mav 0,64,48
R 60,7168
mav 4,68,48
mav 8,72,48
mav 12,76,48
mav 0,80,52
R 32,8192
mav 4,84,52
mav 8,88,52
mav 12,92,52
s_waitcnt lgkmcnt(1)
mav 0,96,56
R 36,9216
mav 4,100,56
mav 8,104,56
mav 12,108,56
mav 0,112,60
R 40,10240
mav 4,116,60
mav 8,120,60
mav 12,124,60
s_waitcnt lgkmcnt(1)
mav 0,128,32
R 44,11264
mav 4,132,32
mav 8,136,32
s_mov_b32 m0, 142336
mav 12,140,32
mav 0,144,36
R 48,12288
buffer_load_dword v85, s[36:39], 0 offen sc1 lds
mav 4,148,36
mav 8,152,36
mav 12,156,36
s_waitcnt lgkmcnt(1)
mav 0,160,40
R 52,13312
v_add_u32 v86, 1, v86
mav 4,164,40
mav 8,168,40
mav 12,172,40
mav 0,176,44
R 56,14336
mav 4,180,44
mav 8,184,44
mav 12,188,44
s_waitcnt lgkmcnt(1)
mav 0,192,48
R 60,15360
mav 4,196,48
mav 8,200,48
mav 12,204,48
mav 0,208,52
R 32,16384
mav 4,212,52
mav 8,216,52
mav 12,220,52
s_waitcnt lgkmcnt(1)
mav 0,224,56
R 36,17408
mav 4,228,56
mav 8,232,56
mav 12,236,56
mav 0,240,60
R 40,18432
mav 4,244,60
mav 8,248,60
mav 12,252,60
s_waitcnt lgkmcnt(1)
mvv 0,128,32
R 44,19456
mvv 4,132,32
mvv 8,136,32
mvv 12,140,32
mvv 0,144,36
R 48,20480
mvv 4,148,36
mvv 8,152,36
mvv 12,156,36
s_waitcnt lgkmcnt(1)
mvv 0,160,40
R 52,21504
mvv 4,164,40
ds_read_b32 v74, v82 offset:0
mvv 8,168,40
mvv 12,172,40
mvv 0,176,44
R 56,22528
mvv 4,180,44
mvv 8,184,44
s_xor_b32 s50, s50, 0x10000
mvv 12,188,44
s_waitcnt lgkmcnt(1)
mvv 0,192,48
R 60,23552
mvv 4,196,48
v_cmp_gt_i32 vcc, v86, v74
mvv 8,200,48
s_waitcnt vmcnt(1)
s_cbranch_vccnz CG0_3
BG0_3:
mvv 12,204,48
s_barrier
s_add_u32 s49, s49, 346112
mvv 0,208,52
R 32,32768
s_add_u32 m0, s50, 0
mvv 4,212,52
D 83,0
mvv 8,216,52
mvv 12,220,52
s_waitcnt lgkmcnt(1)
mvv 0,224,56
R 36,33792
mvv 4,228,56
D 83,1024
mvv 8,232,56
mvv 12,236,56
mvv 0,240,60
R 40,34816
mvv 4,244,60
mvv 8,248,60
D 83,2048
mvv 12,252,60
s_waitcnt lgkmcnt(1)
v_mfma_f32_16x16x32_bf16 v[16:19], a[0:3], v[32:35], 0
R 44,35840
v_cvt_pk_bf16_f32 v64, v0, v1
v_mfma_f32_16x16x32_bf16 v[20:23], a[4:7], v[32:35], 0
v_cvt_pk_bf16_f32 v65, v2, v3
v_cvt_pk_bf16_f32 v66, v4, v5
v_mfma_f32_16x16x32_bf16 v[24:27], a[8:11], v[32:35], 0
D 83,3072
v_cvt_pk_bf16_f32 v67, v6, v7
v_mfma_f32_16x16x32_bf16 v[28:31], a[12:15], v[32:35], 0
v_cvt_pk_bf16_f32 v68, v8, v9
v_cvt_pk_bf16_f32 v69, v10, v11
mav 16,16,36
R 48,36864
v_cvt_pk_bf16_f32 v70, v12, v13
mav 20,20,36
s_add_u32 m0, s50, 4096
v_cvt_pk_bf16_f32 v71, v14, v15
mav 24,24,36
buffer_store_dwordx4 v[64:67], v87, s[36:39], s49 offen sc1
mav 28,28,36
s_waitcnt lgkmcnt(1)
mav 16,32,40
R 52,37888
mav 20,36,40
mav 24,40,40
buffer_store_dwordx4 v[68:71], v87, s[36:39], s49 offen offset:1024 sc1
mav 28,44,40
mav 16,48,44
R 56,38912
mav 20,52,44
mav 24,56,44
D 84,0
mav 28,60,44
s_waitcnt lgkmcnt(1)
mav 16,64,48
R 60,39936
mav 20,68,48
mav 24,72,48
D 84,1024
mav 28,76,48
mav 16,80,52
R 32,40960
mav 20,84,52
mav 24,88,52
mav 28,92,52
s_waitcnt lgkmcnt(1)
mav 16,96,56
R 36,41984
mav 20,100,56
mav 24,104,56
mav 28,108,56
mav 16,112,60
R 40,43008
mav 20,116,60
mav 24,120,60
mav 28,124,60
s_waitcnt lgkmcnt(1)
mav 16,128,32
R 44,44032
mav 20,132,32
mav 24,136,32
s_mov_b32 m0, 142336
mav 28,140,32
mav 16,144,36
R 48,45056
buffer_load_dword v85, s[36:39], 0 offen sc1 lds
mav 20,148,36
v_add_u32 v72, 1, v72
mav 24,152,36
mav 28,156,36
s_waitcnt lgkmcnt(1)
mav 16,160,40
R 52,46080
v_add_u32 v86, 1, v86
mav 20,164,40
mav 24,168,40
mav 28,172,40
mav 16,176,44
R 56,47104
mav 20,180,44
mav 24,184,44
mav 28,188,44
s_waitcnt lgkmcnt(1)
mav 16,192,48
R 60,48128
mav 20,196,48
mav 24,200,48
mav 28,204,48
mav 16,208,52
R 32,49152
mav 20,212,52
mav 24,216,52
mav 28,220,52
s_waitcnt lgkmcnt(1)
mav 16,224,56
R 36,50176
mav 20,228,56
mav 24,232,56
mav 28,236,56
mav 16,240,60
R 40,51200
mav 20,244,60
s_waitcnt vmcnt(3)
buffer_store_dwordx2 v[72:73], v81, s[36:39], s52 offen sc1
mav 24,248,60
mav 28,252,60
s_waitcnt lgkmcnt(1)
mvv 16,128,32
R 44,52224
mvv 20,132,32
mvv 24,136,32
mvv 28,140,32
mvv 16,144,36
R 48,53248
mvv 20,148,36
mvv 24,152,36
mvv 28,156,36
s_waitcnt lgkmcnt(1)
mvv 16,160,40
R 52,54272
mvv 20,164,40
ds_read_b32 v74, v82 offset:0
mvv 24,168,40
mvv 28,172,40
mvv 16,176,44
R 56,55296
mvv 20,180,44
mvv 24,184,44
mvv 28,188,44
s_waitcnt lgkmcnt(1)
mvv 16,192,48
R 60,56320
mvv 20,196,48
v_xor_b32 v80, 0x10000, v80
v_cmp_gt_i32 vcc, v86, v74
mvv 24,200,48
s_waitcnt vmcnt(2)
s_cbranch_vccnz CG0_4
BG0_4:
mvv 28,204,48
s_barrier
s_add_u32 s49, s49, 346112
mvv 16,208,52
R 32,0
s_add_u32 m0, s50, 32768
mvv 20,212,52
D 83,0
mvv 24,216,52
mvv 28,220,52
s_waitcnt lgkmcnt(1)
mvv 16,224,56
R 36,1024
mvv 20,228,56
D 83,1024
mvv 24,232,56
mvv 28,236,56
mvv 16,240,60
R 40,2048
mvv 20,244,60
mvv 24,248,60
D 83,2048
mvv 28,252,60
s_mov_b32 s60, -30
.p2align 6
LG0_5:
s_waitcnt lgkmcnt(1)
v_mfma_f32_16x16x32_bf16 v[0:3], a[0:3], v[32:35], 0
R 44,3072
v_cvt_pk_bf16_f32 v64, v16, v17
v_mfma_f32_16x16x32_bf16 v[4:7], a[4:7], v[32:35], 0
v_cvt_pk_bf16_f32 v65, v18, v19
v_cvt_pk_bf16_f32 v66, v20, v21
v_mfma_f32_16x16x32_bf16 v[8:11], a[8:11], v[32:35], 0
D 83,3072
v_cvt_pk_bf16_f32 v67, v22, v23
v_mfma_f32_16x16x32_bf16 v[12:15], a[12:15], v[32:35], 0
v_cvt_pk_bf16_f32 v68, v24, v25
v_cvt_pk_bf16_f32 v69, v26, v27
mav 0,16,36
R 48,4096
v_cvt_pk_bf16_f32 v70, v28, v29
mav 4,20,36
s_add_u32 m0, s50, 36864
v_cvt_pk_bf16_f32 v71, v30, v31
mav 8,24,36
buffer_store_dwordx4 v[64:67], v87, s[36:39], s49 offen sc1
mav 12,28,36
s_waitcnt lgkmcnt(1)
mav 0,32,40
R 52,5120
mav 4,36,40
mav 8,40,40
buffer_store_dwordx4 v[68:71], v87, s[36:39], s49 offen offset:1024 sc1
mav 12,44,40
mav 0,48,44
R 56,6144
mav 4,52,44
mav 8,56,44
D 84,0
mav 12,60,44
s_waitcnt lgkmcnt(1)
mav 0,64,48
R 60,7168
mav 4,68,48
mav 8,72,48
D 84,1024
mav 12,76,48
mav 0,80,52
R 32,8192
mav 4,84,52
mav 8,88,52
mav 12,92,52
s_waitcnt lgkmcnt(1)
mav 0,96,56
R 36,9216
mav 4,100,56
mav 8,104,56
mav 12,108,56
mav 0,112,60
R 40,10240
mav 4,116,60
mav 8,120,60
mav 12,124,60
s_waitcnt lgkmcnt(1)
mav 0,128,32
R 44,11264
mav 4,132,32
mav 8,136,32
s_mov_b32 m0, 142336
mav 12,140,32
mav 0,144,36
R 48,12288
buffer_load_dword v85, s[36:39], 0 offen sc1 lds
mav 4,148,36
v_add_u32 v72, 1, v72
mav 8,152,36
mav 12,156,36
s_waitcnt lgkmcnt(1)
mav 0,160,40
R 52,13312
v_add_u32 v86, 1, v86
LsG2:
s_cbranch_execz LwG
mav 4,164,40
mav 8,168,40
mav 12,172,40
mav 0,176,44
R 56,14336
mav 4,180,44
mav 8,184,44
mav 12,188,44
s_waitcnt lgkmcnt(1)
mav 0,192,48
R 60,15360
mav 4,196,48
mav 8,200,48
mav 12,204,48
mav 0,208,52
R 32,16384
mav 4,212,52
mav 8,216,52
mav 12,220,52
s_waitcnt lgkmcnt(1)
mav 0,224,56
R 36,17408
mav 4,228,56
mav 8,232,56
mav 12,236,56
mav 0,240,60
R 40,18432
mav 4,244,60
s_waitcnt vmcnt(3)
buffer_store_dwordx2 v[72:73], v81, s[36:39], s52 offen sc1
mav 8,248,60
mav 12,252,60
s_waitcnt lgkmcnt(1)
mvv 0,128,32
R 44,19456
mvv 4,132,32
mvv 8,136,32
mvv 12,140,32
mvv 0,144,36
R 48,20480
mvv 4,148,36
mvv 8,152,36
mvv 12,156,36
s_waitcnt lgkmcnt(1)
mvv 0,160,40
R 52,21504
mvv 4,164,40
ds_read_b32 v74, v82 offset:0
mvv 8,168,40
mvv 12,172,40
mvv 0,176,44
R 56,22528
mvv 4,180,44
mvv 8,184,44
s_xor_b32 s50, s50, 0x10000
mvv 12,188,44
s_waitcnt lgkmcnt(1)
mvv 0,192,48
R 60,23552
mvv 4,196,48
v_cmp_gt_i32 vcc, v86, v74
mvv 8,200,48
s_waitcnt vmcnt(2)
s_cbranch_vccnz CG0_6
BG0_6:
mvv 12,204,48
s_barrier
s_add_u32 s49, s49, 346112
mvv 0,208,52
R 32,32768
s_add_u32 m0, s50, 0
mvv 4,212,52
D 83,0
mvv 8,216,52
mvv 12,220,52
s_waitcnt lgkmcnt(1)
mvv 0,224,56
R 36,33792
mvv 4,228,56
D 83,1024
mvv 8,232,56
mvv 12,236,56
mvv 0,240,60
R 40,34816
mvv 4,244,60
mvv 8,248,60
D 83,2048
mvv 12,252,60
s_waitcnt lgkmcnt(1)
v_mfma_f32_16x16x32_bf16 v[16:19], a[0:3], v[32:35], 0
R 44,35840
v_cvt_pk_bf16_f32 v64, v0, v1
v_mfma_f32_16x16x32_bf16 v[20:23], a[4:7], v[32:35], 0
v_cvt_pk_bf16_f32 v65, v2, v3
v_cvt_pk_bf16_f32 v66, v4, v5
v_mfma_f32_16x16x32_bf16 v[24:27], a[8:11], v[32:35], 0
D 83,3072
v_cvt_pk_bf16_f32 v67, v6, v7
v_mfma_f32_16x16x32_bf16 v[28:31], a[12:15], v[32:35], 0
v_cvt_pk_bf16_f32 v68, v8, v9
v_cvt_pk_bf16_f32 v69, v10, v11
mav 16,16,36
R 48,36864
v_cvt_pk_bf16_f32 v70, v12, v13
mav 20,20,36
s_add_u32 m0, s50, 4096
v_cvt_pk_bf16_f32 v71, v14, v15
mav 24,24,36
buffer_store_dwordx4 v[64:67], v87, s[36:39], s49 offen sc1
mav 28,28,36
s_waitcnt lgkmcnt(1)
mav 16,32,40
R 52,37888
mav 20,36,40
mav 24,40,40
buffer_store_dwordx4 v[68:71], v87, s[36:39], s49 offen offset:1024 sc1
mav 28,44,40
mav 16,48,44
R 56,38912
mav 20,52,44
mav 24,56,44
D 84,0
mav 28,60,44
s_waitcnt lgkmcnt(1)
mav 16,64,48
R 60,39936
mav 20,68,48
mav 24,72,48
D 84,1024
mav 28,76,48
mav 16,80,52
R 32,40960
mav 20,84,52
mav 24,88,52
mav 28,92,52
s_waitcnt lgkmcnt(1)
mav 16,96,56
R 36,41984
mav 20,100,56
mav 24,104,56
mav 28,108,56
mav 16,112,60
R 40,43008
mav 20,116,60
mav 24,120,60
mav 28,124,60
s_waitcnt lgkmcnt(1)
mav 16,128,32
R 44,44032
mav 20,132,32
mav 24,136,32
s_mov_b32 m0, 142336
mav 28,140,32
mav 16,144,36
R 48,45056
buffer_load_dword v85, s[36:39], 0 offen sc1 lds
mav 20,148,36
v_add_u32 v72, 1, v72
mav 24,152,36
mav 28,156,36
s_waitcnt lgkmcnt(1)
mav 16,160,40
R 52,46080
v_add_u32 v86, 1, v86
mav 20,164,40
mav 24,168,40
mav 28,172,40
mav 16,176,44
R 56,47104
mav 20,180,44
mav 24,184,44
mav 28,188,44
s_waitcnt lgkmcnt(1)
mav 16,192,48
R 60,48128
mav 20,196,48
mav 24,200,48
mav 28,204,48
mav 16,208,52
R 32,49152
mav 20,212,52
mav 24,216,52
mav 28,220,52
s_waitcnt lgkmcnt(1)
mav 16,224,56
R 36,50176
mav 20,228,56
mav 24,232,56
mav 28,236,56
mav 16,240,60
R 40,51200
mav 20,244,60
s_waitcnt vmcnt(3)
buffer_store_dwordx2 v[72:73], v81, s[36:39], s52 offen sc1
mav 24,248,60
mav 28,252,60
s_waitcnt lgkmcnt(1)
mvv 16,128,32
R 44,52224
mvv 20,132,32
mvv 24,136,32
mvv 28,140,32
mvv 16,144,36
R 48,53248
mvv 20,148,36
mvv 24,152,36
mvv 28,156,36
s_waitcnt lgkmcnt(1)
mvv 16,160,40
R 52,54272
mvv 20,164,40
ds_read_b32 v74, v82 offset:0
mvv 24,168,40
mvv 28,172,40
mvv 16,176,44
R 56,55296
mvv 20,180,44
mvv 24,184,44
mvv 28,188,44
s_waitcnt lgkmcnt(1)
mvv 16,192,48
R 60,56320
mvv 20,196,48
v_xor_b32 v80, 0x10000, v80
v_cmp_gt_i32 vcc, v86, v74
mvv 24,200,48
s_waitcnt vmcnt(2)
s_cbranch_vccnz CG0_7
BG0_7:
mvv 28,204,48
s_barrier
s_add_u32 s49, s49, 346112
mvv 16,208,52
R 32,0
s_add_u32 m0, s50, 32768
mvv 20,212,52
D 83,0
mvv 24,216,52
mvv 28,220,52
s_waitcnt lgkmcnt(1)
mvv 16,224,56
R 36,1024
mvv 20,228,56
D 83,1024
mvv 24,232,56
mvv 28,236,56
mvv 16,240,60
R 40,2048
mvv 20,244,60
mvv 24,248,60
D 83,2048
mvv 28,252,60
s_add_u32 s60, s60, 1
s_cbranch_scc0 LG0_5
s_waitcnt lgkmcnt(1)
v_mfma_f32_16x16x32_bf16 v[0:3], a[0:3], v[32:35], 0
R 44,3072
v_cvt_pk_bf16_f32 v64, v16, v17
v_mfma_f32_16x16x32_bf16 v[4:7], a[4:7], v[32:35], 0
v_cvt_pk_bf16_f32 v65, v18, v19
v_cvt_pk_bf16_f32 v66, v20, v21
v_mfma_f32_16x16x32_bf16 v[8:11], a[8:11], v[32:35], 0
D 83,3072
v_cvt_pk_bf16_f32 v67, v22, v23
v_mfma_f32_16x16x32_bf16 v[12:15], a[12:15], v[32:35], 0
v_cvt_pk_bf16_f32 v68, v24, v25
v_cvt_pk_bf16_f32 v69, v26, v27
mav 0,16,36
R 48,4096
v_cvt_pk_bf16_f32 v70, v28, v29
mav 4,20,36
s_add_u32 m0, s50, 36864
v_cvt_pk_bf16_f32 v71, v30, v31
mav 8,24,36
buffer_store_dwordx4 v[64:67], v87, s[36:39], s49 offen sc1
mav 12,28,36
s_waitcnt lgkmcnt(1)
mav 0,32,40
R 52,5120
mav 4,36,40
mav 8,40,40
buffer_store_dwordx4 v[68:71], v87, s[36:39], s49 offen offset:1024 sc1
mav 12,44,40
mav 0,48,44
R 56,6144
mav 4,52,44
mav 8,56,44
D 84,0
mav 12,60,44
s_waitcnt lgkmcnt(1)
mav 0,64,48
R 60,7168
mav 4,68,48
mav 8,72,48
D 84,1024
mav 12,76,48
mav 0,80,52
R 32,8192
mav 4,84,52
mav 8,88,52
mav 12,92,52
s_waitcnt lgkmcnt(1)
mav 0,96,56
R 36,9216
mav 4,100,56
mav 8,104,56
mav 12,108,56
mav 0,112,60
R 40,10240
mav 4,116,60
mav 8,120,60
mav 12,124,60
s_waitcnt lgkmcnt(1)
mav 0,128,32
R 44,11264
mav 4,132,32
mav 8,136,32
mav 12,140,32
mav 0,144,36
R 48,12288
v_add_u32 v72, 1, v72
mav 4,148,36
mav 8,152,36
mav 12,156,36
s_waitcnt lgkmcnt(1)
mav 0,160,40
R 52,13312
mav 4,164,40
mav 8,168,40
mav 12,172,40
mav 0,176,44
R 56,14336
mav 4,180,44
mav 8,184,44
mav 12,188,44
s_waitcnt lgkmcnt(1)
mav 0,192,48
R 60,15360
mav 4,196,48
mav 8,200,48
mav 12,204,48
mav 0,208,52
R 32,16384
mav 4,212,52
mav 8,216,52
mav 12,220,52
s_waitcnt lgkmcnt(1)
mav 0,224,56
R 36,17408
mav 4,228,56
mav 8,232,56
mav 12,236,56
mav 0,240,60
R 40,18432
mav 4,244,60
s_waitcnt vmcnt(2)
buffer_store_dwordx2 v[72:73], v81, s[36:39], s52 offen sc1
mav 8,248,60
mav 12,252,60
s_waitcnt lgkmcnt(1)
mvv 0,128,32
R 44,19456
mvv 4,132,32
mvv 8,136,32
mvv 12,140,32
mvv 0,144,36
R 48,20480
mvv 4,148,36
mvv 8,152,36
mvv 12,156,36
s_waitcnt lgkmcnt(1)
mvv 0,160,40
R 52,21504
mvv 4,164,40
mvv 8,168,40
mvv 12,172,40
mvv 0,176,44
R 56,22528
mvv 4,180,44
mvv 8,184,44
mvv 12,188,44
s_waitcnt lgkmcnt(1)
mvv 0,192,48
R 60,23552
mvv 4,196,48
mvv 8,200,48
s_add_u32 s49, s49, 346112
mvv 12,204,48
s_waitcnt vmcnt(1)
s_barrier
mvv 0,208,52
R 32,32768
mvv 4,212,52
mvv 8,216,52
mvv 12,220,52
s_waitcnt lgkmcnt(1)
mvv 0,224,56
R 36,33792
mvv 4,228,56
mvv 8,232,56
mvv 12,236,56
mvv 0,240,60
R 40,34816
mvv 4,244,60
mvv 8,248,60
mvv 12,252,60
s_waitcnt lgkmcnt(1)
v_mfma_f32_16x16x32_bf16 v[16:19], a[0:3], v[32:35], 0
R 44,35840
v_mfma_f32_16x16x32_bf16 v[20:23], a[4:7], v[32:35], 0
v_cvt_pk_bf16_f32 v64, v0, v1
v_cvt_pk_bf16_f32 v65, v2, v3
v_mfma_f32_16x16x32_bf16 v[24:27], a[8:11], v[32:35], 0
v_cvt_pk_bf16_f32 v66, v4, v5
v_cvt_pk_bf16_f32 v67, v6, v7
v_mfma_f32_16x16x32_bf16 v[28:31], a[12:15], v[32:35], 0
buffer_store_dwordx4 v[64:67], v87, s[36:39], s49 offen sc1
v_cvt_pk_bf16_f32 v68, v8, v9
mav 16,16,36
R 48,36864
v_cvt_pk_bf16_f32 v69, v10, v11
mav 20,20,36
v_cvt_pk_bf16_f32 v70, v12, v13
v_cvt_pk_bf16_f32 v71, v14, v15
mav 24,24,36
mav 28,28,36
buffer_store_dwordx4 v[68:71], v87, s[36:39], s49 offen offset:1024 sc1
s_waitcnt lgkmcnt(1)
mav 16,32,40
R 52,37888
mav 20,36,40
mav 24,40,40
mav 28,44,40
mav 16,48,44
R 56,38912
mav 20,52,44
mav 24,56,44
mav 28,60,44
s_waitcnt lgkmcnt(1)
mav 16,64,48
R 60,39936
mav 20,68,48
mav 24,72,48
mav 28,76,48
mav 16,80,52
R 32,40960
mav 20,84,52
mav 24,88,52
mav 28,92,52
s_waitcnt lgkmcnt(1)
mav 16,96,56
R 36,41984
mav 20,100,56
mav 24,104,56
mav 28,108,56
mav 16,112,60
R 40,43008
mav 20,116,60
mav 24,120,60
mav 28,124,60
s_waitcnt lgkmcnt(1)
mav 16,128,32
R 44,44032
mav 20,132,32
mav 24,136,32
mav 28,140,32
mav 16,144,36
R 48,45056
v_add_u32 v72, 1, v72
mav 20,148,36
mav 24,152,36
mav 28,156,36
s_waitcnt lgkmcnt(1)
mav 16,160,40
R 52,46080
mav 20,164,40
mav 24,168,40
mav 28,172,40
mav 16,176,44
R 56,47104
mav 20,180,44
mav 24,184,44
mav 28,188,44
s_waitcnt lgkmcnt(1)
mav 16,192,48
R 60,48128
mav 20,196,48
mav 24,200,48
mav 28,204,48
mav 16,208,52
R 32,49152
mav 20,212,52
mav 24,216,52
mav 28,220,52
s_waitcnt lgkmcnt(1)
mav 16,224,56
R 36,50176
mav 20,228,56
mav 24,232,56
mav 28,236,56
mav 16,240,60
R 40,51200
mav 20,244,60
s_waitcnt vmcnt(0)
buffer_store_dwordx2 v[72:73], v81, s[36:39], s52 offen sc1
mav 24,248,60
mav 28,252,60
s_waitcnt lgkmcnt(1)
mvv 16,128,32
R 44,52224
mvv 20,132,32
mvv 24,136,32
mvv 28,140,32
mvv 16,144,36
R 48,53248
mvv 20,148,36
mvv 24,152,36
mvv 28,156,36
s_waitcnt lgkmcnt(1)
mvv 16,160,40
R 52,54272
mvv 20,164,40
mvv 24,168,40
mvv 28,172,40
mvv 16,176,44
R 56,55296
mvv 20,180,44
mvv 24,184,44
mvv 28,188,44
s_waitcnt lgkmcnt(1)
mvv 16,192,48
R 60,56320
mvv 20,196,48
mvv 24,200,48
s_add_u32 s49, s49, 346112
mvv 28,204,48
mvv 16,208,52
mvv 20,212,52
mvv 24,216,52
mvv 28,220,52
s_waitcnt lgkmcnt(0)
mvv 16,224,56
mvv 20,228,56
mvv 24,232,56
mvv 28,236,56
LsG3:
s_cbranch_execz LwG
mvv 16,240,60
mvv 20,244,60
mvv 24,248,60
mvv 28,252,60
s_nop 4
v_cvt_pk_bf16_f32 v64, v16, v17
v_cvt_pk_bf16_f32 v65, v18, v19
v_cvt_pk_bf16_f32 v66, v20, v21
v_cvt_pk_bf16_f32 v67, v22, v23
buffer_store_dwordx4 v[64:67], v87, s[36:39], s49 offen sc1
v_cvt_pk_bf16_f32 v68, v24, v25
v_cvt_pk_bf16_f32 v69, v26, v27
v_cvt_pk_bf16_f32 v70, v28, v29
v_cvt_pk_bf16_f32 v71, v30, v31
buffer_store_dwordx4 v[68:71], v87, s[36:39], s49 offen offset:1024 sc1
v_add_u32 v72, 35, v72
s_waitcnt vmcnt(0)
buffer_store_dwordx2 v[72:73], v81, s[36:39], s52 offen sc1
s_endpgm
CG0_1:
s_cmp_eq_u32 s61, 0
s_cbranch_scc1 BG0_1
s_sub_u32 s61, s61, 1
s_sleep 8
buffer_load_dwordx2 v[74:75], v85, s[36:39], 0 offen sc1
s_waitcnt vmcnt(0)
ds_write_b32 v82, v74
v_sub_u32 v74, v74, v86
v_xor_b32 v75, v75, v73
v_cmp_lt_u64 vcc, s[62:63], v[74:75]
s_waitcnt lgkmcnt(0)
s_cbranch_vccnz CG0_1
s_branch BG0_1
CG0_2:
s_mov_b32 s58, m0
s_mov_b32 m0, 142336
DG0_2:
s_cmp_eq_u32 s61, 0
s_cbranch_scc1 EG0_2
s_sub_u32 s61, s61, 1
buffer_load_dword v85, s[36:39], 0 offen sc1 lds
s_sleep 1
ds_read_b32 v74, v82
s_waitcnt lgkmcnt(0)
v_cmp_gt_i32 vcc, v86, v74
s_cbranch_vccnz DG0_2
EG0_2:
s_mov_b32 m0, s58
s_branch BG0_2
CG0_3:
s_mov_b32 s58, m0
s_mov_b32 m0, 142336
DG0_3:
s_cmp_eq_u32 s61, 0
s_cbranch_scc1 EG0_3
s_sub_u32 s61, s61, 1
buffer_load_dword v85, s[36:39], 0 offen sc1 lds
s_sleep 1
ds_read_b32 v74, v82
s_waitcnt lgkmcnt(0)
v_cmp_gt_i32 vcc, v86, v74
s_cbranch_vccnz DG0_3
EG0_3:
s_mov_b32 m0, s58
s_branch BG0_3
CG0_4:
s_mov_b32 s58, m0
s_mov_b32 m0, 142336
DG0_4:
s_cmp_eq_u32 s61, 0
s_cbranch_scc1 EG0_4
s_sub_u32 s61, s61, 1
buffer_load_dword v85, s[36:39], 0 offen sc1 lds
s_sleep 1
ds_read_b32 v74, v82
s_waitcnt lgkmcnt(0)
v_cmp_gt_i32 vcc, v86, v74
s_cbranch_vccnz DG0_4
EG0_4:
s_mov_b32 m0, s58
s_branch BG0_4
CG0_6:
s_mov_b32 s58, m0
s_mov_b32 m0, 142336
DG0_6:
s_cmp_eq_u32 s61, 0
s_cbranch_scc1 EG0_6
s_sub_u32 s61, s61, 1
buffer_load_dword v85, s[36:39], 0 offen sc1 lds
s_sleep 1
ds_read_b32 v74, v82
s_waitcnt lgkmcnt(0)
v_cmp_gt_i32 vcc, v86, v74
s_cbranch_vccnz DG0_6
EG0_6:
s_mov_b32 m0, s58
s_branch BG0_6
CG0_7:
s_mov_b32 s58, m0
s_mov_b32 m0, 142336
DG0_7:
s_cmp_eq_u32 s61, 0
s_cbranch_scc1 EG0_7
s_sub_u32 s61, s61, 1
buffer_load_dword v85, s[36:39], 0 offen sc1 lds
s_sleep 1
ds_read_b32 v74, v82
s_waitcnt lgkmcnt(0)
v_cmp_gt_i32 vcc, v86, v74
s_cbranch_vccnz DG0_7
EG0_7:
s_mov_b32 m0, s58
s_branch BG0_7
LvG1:
LsG4:
s_cbranch_execz LwG
v_add_u32 v72, 1, v72
s_add_u32 s49, s49, 346112
s_barrier
s_add_u32 m0, s50, 0
buffer_store_dwordx2 v[72:73], v81, s[36:39], s52 offen sc1
D 83,0
D 83,1024
D 83,2048
D 83,3072
s_add_u32 m0, s50, 4096
s_nop 0
D 84,0
D 84,1024
v_add_u32 v72, 1, v72
s_add_u32 s49, s49, 346112
s_waitcnt vmcnt(0)
s_barrier
s_add_u32 m0, s50, 32768
R 32,0
buffer_store_dwordx2 v[72:73], v81, s[36:39], s52 offen sc1
D 83,0
R 36,1024
D 83,1024
R 40,2048
D 83,2048
s_waitcnt lgkmcnt(1)
v_mfma_f32_16x16x32_bf16 v[0:3], a[0:3], v[32:35], 0
R 44,3072
v_mfma_f32_16x16x32_bf16 v[4:7], a[4:7], v[32:35], 0
v_mfma_f32_16x16x32_bf16 v[8:11], a[8:11], v[32:35], 0
D 83,3072
v_mfma_f32_16x16x32_bf16 v[12:15], a[12:15], v[32:35], 0
mav 0,16,36
R 48,4096
mav 4,20,36
s_add_u32 m0, s50, 36864
mav 8,24,36
mav 12,28,36
D 84,0
s_waitcnt lgkmcnt(1)
mav 0,32,40
R 52,5120
mav 4,36,40
mav 8,40,40
mav 12,44,40
D 84,1024
mav 0,48,44
R 56,6144
mav 4,52,44
mav 8,56,44
mav 12,60,44
s_waitcnt lgkmcnt(1)
mav 0,64,48
R 60,7168
mav 4,68,48
mav 8,72,48
mav 12,76,48
mav 0,80,52
R 32,8192
mav 4,84,52
mav 8,88,52
mav 12,92,52
s_waitcnt lgkmcnt(1)
mav 0,96,56
R 36,9216
mav 4,100,56
mav 8,104,56
mav 12,108,56
mav 0,112,60
R 40,10240
mav 4,116,60
mav 8,120,60
mav 12,124,60
s_waitcnt lgkmcnt(1)
mav 0,128,32
R 44,11264
mav 4,132,32
mav 8,136,32
mav 12,140,32
mav 0,144,36
R 48,12288
mav 4,148,36
mav 8,152,36
mav 12,156,36
s_waitcnt lgkmcnt(1)
mav 0,160,40
R 52,13312
mav 4,164,40
mav 8,168,40
mav 12,172,40
mav 0,176,44
R 56,14336
mav 4,180,44
mav 8,184,44
mav 12,188,44
s_waitcnt lgkmcnt(1)
mav 0,192,48
R 60,15360
mav 4,196,48
mav 8,200,48
mav 12,204,48
mav 0,208,52
R 32,16384
mav 4,212,52
mav 8,216,52
mav 12,220,52
s_waitcnt lgkmcnt(1)
mav 0,224,56
R 36,17408
mav 4,228,56
mav 8,232,56
mav 12,236,56
mav 0,240,60
R 40,18432
mav 4,244,60
mav 8,248,60
mav 12,252,60
s_waitcnt lgkmcnt(1)
mvv 0,128,32
R 44,19456
mvv 4,132,32
mvv 8,136,32
mvv 12,140,32
mvv 0,144,36
R 48,20480
mvv 4,148,36
mvv 8,152,36
mvv 12,156,36
s_waitcnt lgkmcnt(1)
mvv 0,160,40
R 52,21504
mvv 4,164,40
mvv 8,168,40
mvv 12,172,40
mvv 0,176,44
R 56,22528
mvv 4,180,44
mvv 8,184,44
s_xor_b32 s50, s50, 0x10000
mvv 12,188,44
s_waitcnt lgkmcnt(1)
mvv 0,192,48
R 60,23552
mvv 4,196,48
mvv 8,200,48
s_add_u32 s49, s49, 346112
mvv 12,204,48
s_waitcnt vmcnt(0)
s_barrier
mvv 0,208,52
R 32,32768
s_add_u32 m0, s50, 0
mvv 4,212,52
D 83,0
mvv 8,216,52
mvv 12,220,52
s_waitcnt lgkmcnt(1)
mvv 0,224,56
R 36,33792
mvv 4,228,56
D 83,1024
mvv 8,232,56
mvv 12,236,56
mvv 0,240,60
R 40,34816
mvv 4,244,60
mvv 8,248,60
D 83,2048
mvv 12,252,60
s_waitcnt lgkmcnt(1)
v_mfma_f32_16x16x32_bf16 v[16:19], a[0:3], v[32:35], 0
R 44,35840
v_cvt_pk_bf16_f32 v64, v0, v1
v_mfma_f32_16x16x32_bf16 v[20:23], a[4:7], v[32:35], 0
v_cvt_pk_bf16_f32 v65, v2, v3
v_cvt_pk_bf16_f32 v66, v4, v5
v_mfma_f32_16x16x32_bf16 v[24:27], a[8:11], v[32:35], 0
D 83,3072
v_cvt_pk_bf16_f32 v67, v6, v7
v_mfma_f32_16x16x32_bf16 v[28:31], a[12:15], v[32:35], 0
v_cvt_pk_bf16_f32 v68, v8, v9
v_cvt_pk_bf16_f32 v69, v10, v11
mav 16,16,36
R 48,36864
v_cvt_pk_bf16_f32 v70, v12, v13
mav 20,20,36
s_add_u32 m0, s50, 4096
v_cvt_pk_bf16_f32 v71, v14, v15
mav 24,24,36
buffer_store_dwordx4 v[64:67], v87, s[36:39], s49 offen sc1
mav 28,28,36
s_waitcnt lgkmcnt(1)
mav 16,32,40
R 52,37888
mav 20,36,40
mav 24,40,40
buffer_store_dwordx4 v[68:71], v87, s[36:39], s49 offen offset:1024 sc1
mav 28,44,40
mav 16,48,44
R 56,38912
mav 20,52,44
mav 24,56,44
D 84,0
mav 28,60,44
s_waitcnt lgkmcnt(1)
mav 16,64,48
R 60,39936
mav 20,68,48
mav 24,72,48
D 84,1024
mav 28,76,48
mav 16,80,52
R 32,40960
mav 20,84,52
mav 24,88,52
mav 28,92,52
s_waitcnt lgkmcnt(1)
mav 16,96,56
R 36,41984
mav 20,100,56
mav 24,104,56
mav 28,108,56
mav 16,112,60
R 40,43008
mav 20,116,60
mav 24,120,60
mav 28,124,60
s_waitcnt lgkmcnt(1)
mav 16,128,32
R 44,44032
mav 20,132,32
mav 24,136,32
mav 28,140,32
mav 16,144,36
R 48,45056
v_add_u32 v72, 1, v72
mav 20,148,36
mav 24,152,36
mav 28,156,36
s_waitcnt lgkmcnt(1)
mav 16,160,40
R 52,46080
mav 20,164,40
mav 24,168,40
mav 28,172,40
mav 16,176,44
R 56,47104
mav 20,180,44
mav 24,184,44
mav 28,188,44
s_waitcnt lgkmcnt(1)
mav 16,192,48
R 60,48128
mav 20,196,48
mav 24,200,48
mav 28,204,48
mav 16,208,52
R 32,49152
mav 20,212,52
mav 24,216,52
mav 28,220,52
s_waitcnt lgkmcnt(1)
mav 16,224,56
R 36,50176
mav 20,228,56
mav 24,232,56
mav 28,236,56
mav 16,240,60
R 40,51200
mav 20,244,60
s_waitcnt vmcnt(2)
buffer_store_dwordx2 v[72:73], v81, s[36:39], s52 offen sc1
mav 24,248,60
mav 28,252,60
s_waitcnt lgkmcnt(1)
mvv 16,128,32
R 44,52224
mvv 20,132,32
mvv 24,136,32
mvv 28,140,32
mvv 16,144,36
R 48,53248
mvv 20,148,36
mvv 24,152,36
mvv 28,156,36
s_waitcnt lgkmcnt(1)
mvv 16,160,40
R 52,54272
mvv 20,164,40
mvv 24,168,40
mvv 28,172,40
mvv 16,176,44
R 56,55296
mvv 20,180,44
mvv 24,184,44
mvv 28,188,44
s_waitcnt lgkmcnt(1)
mvv 16,192,48
R 60,56320
mvv 20,196,48
v_xor_b32 v80, 0x10000, v80
mvv 24,200,48
s_add_u32 s49, s49, 346112
mvv 28,204,48
s_waitcnt vmcnt(1)
s_barrier
mvv 16,208,52
R 32,0
s_add_u32 m0, s50, 32768
mvv 20,212,52
D 83,0
mvv 24,216,52
mvv 28,220,52
s_waitcnt lgkmcnt(1)
mvv 16,224,56
R 36,1024
mvv 20,228,56
D 83,1024
mvv 24,232,56
mvv 28,236,56
mvv 16,240,60
R 40,2048
mvv 20,244,60
mvv 24,248,60
D 83,2048
mvv 28,252,60
s_mov_b32 s60, -30
.p2align 6
LG1_1:
s_waitcnt lgkmcnt(1)
v_mfma_f32_16x16x32_bf16 v[0:3], a[0:3], v[32:35], 0
R 44,3072
v_cvt_pk_bf16_f32 v64, v16, v17
v_mfma_f32_16x16x32_bf16 v[4:7], a[4:7], v[32:35], 0
v_cvt_pk_bf16_f32 v65, v18, v19
v_cvt_pk_bf16_f32 v66, v20, v21
v_mfma_f32_16x16x32_bf16 v[8:11], a[8:11], v[32:35], 0
D 83,3072
v_cvt_pk_bf16_f32 v67, v22, v23
v_mfma_f32_16x16x32_bf16 v[12:15], a[12:15], v[32:35], 0
v_cvt_pk_bf16_f32 v68, v24, v25
v_cvt_pk_bf16_f32 v69, v26, v27
mav 0,16,36
R 48,4096
v_cvt_pk_bf16_f32 v70, v28, v29
mav 4,20,36
s_add_u32 m0, s50, 36864
v_cvt_pk_bf16_f32 v71, v30, v31
mav 8,24,36
buffer_store_dwordx4 v[64:67], v87, s[36:39], s49 offen sc1
mav 12,28,36
s_waitcnt lgkmcnt(1)
mav 0,32,40
R 52,5120
mav 4,36,40
mav 8,40,40
buffer_store_dwordx4 v[68:71], v87, s[36:39], s49 offen offset:1024 sc1
mav 12,44,40
mav 0,48,44
R 56,6144
mav 4,52,44
mav 8,56,44
D 84,0
mav 12,60,44
s_waitcnt lgkmcnt(1)
mav 0,64,48
R 60,7168
mav 4,68,48
mav 8,72,48
D 84,1024
mav 12,76,48
mav 0,80,52
R 32,8192
mav 4,84,52
mav 8,88,52
mav 12,92,52
s_waitcnt lgkmcnt(1)
mav 0,96,56
R 36,9216
mav 4,100,56
mav 8,104,56
mav 12,108,56
mav 0,112,60
R 40,10240
mav 4,116,60
mav 8,120,60
mav 12,124,60
s_waitcnt lgkmcnt(1)
mav 0,128,32
R 44,11264
mav 4,132,32
mav 8,136,32
mav 12,140,32
mav 0,144,36
R 48,12288
v_add_u32 v72, 1, v72
mav 4,148,36
mav 8,152,36
mav 12,156,36
s_waitcnt lgkmcnt(1)
mav 0,160,40
R 52,13312
mav 4,164,40
mav 8,168,40
mav 12,172,40
mav 0,176,44
R 56,14336
mav 4,180,44
mav 8,184,44
mav 12,188,44
s_waitcnt lgkmcnt(1)
mav 0,192,48
R 60,15360
mav 4,196,48
mav 8,200,48
mav 12,204,48
mav 0,208,52
R 32,16384
mav 4,212,52
mav 8,216,52
mav 12,220,52
s_waitcnt lgkmcnt(1)
mav 0,224,56
R 36,17408
mav 4,228,56
mav 8,232,56
mav 12,236,56
mav 0,240,60
R 40,18432
mav 4,244,60
s_waitcnt vmcnt(2)
buffer_store_dwordx2 v[72:73], v81, s[36:39], s52 offen sc1
mav 8,248,60
mav 12,252,60
s_waitcnt lgkmcnt(1)
mvv 0,128,32
R 44,19456
mvv 4,132,32
mvv 8,136,32
mvv 12,140,32
mvv 0,144,36
R 48,20480
mvv 4,148,36
mvv 8,152,36
mvv 12,156,36
s_waitcnt lgkmcnt(1)
mvv 0,160,40
R 52,21504
mvv 4,164,40
mvv 8,168,40
mvv 12,172,40
mvv 0,176,44
R 56,22528
mvv 4,180,44
mvv 8,184,44
s_xor_b32 s50, s50, 0x10000
mvv 12,188,44
s_waitcnt lgkmcnt(1)
mvv 0,192,48
R 60,23552
mvv 4,196,48
mvv 8,200,48
s_add_u32 s49, s49, 346112
mvv 12,204,48
s_waitcnt vmcnt(1)
s_barrier
mvv 0,208,52
R 32,32768
s_add_u32 m0, s50, 0
mvv 4,212,52
D 83,0
mvv 8,216,52
mvv 12,220,52
LsG5:
)"
R"(
s_cbranch_execz LwG
s_waitcnt lgkmcnt(1)
mvv 0,224,56
R 36,33792
mvv 4,228,56
D 83,1024
mvv 8,232,56
mvv 12,236,56
mvv 0,240,60
R 40,34816
mvv 4,244,60
mvv 8,248,60
D 83,2048
mvv 12,252,60
s_waitcnt lgkmcnt(1)
v_mfma_f32_16x16x32_bf16 v[16:19], a[0:3], v[32:35], 0
R 44,35840
v_cvt_pk_bf16_f32 v64, v0, v1
v_mfma_f32_16x16x32_bf16 v[20:23], a[4:7], v[32:35], 0
v_cvt_pk_bf16_f32 v65, v2, v3
v_cvt_pk_bf16_f32 v66, v4, v5
v_mfma_f32_16x16x32_bf16 v[24:27], a[8:11], v[32:35], 0
D 83,3072
v_cvt_pk_bf16_f32 v67, v6, v7
v_mfma_f32_16x16x32_bf16 v[28:31], a[12:15], v[32:35], 0
v_cvt_pk_bf16_f32 v68, v8, v9
v_cvt_pk_bf16_f32 v69, v10, v11
mav 16,16,36
R 48,36864
v_cvt_pk_bf16_f32 v70, v12, v13
mav 20,20,36
s_add_u32 m0, s50, 4096
v_cvt_pk_bf16_f32 v71, v14, v15
mav 24,24,36
buffer_store_dwordx4 v[64:67], v87, s[36:39], s49 offen sc1
mav 28,28,36
s_waitcnt lgkmcnt(1)
mav 16,32,40
R 52,37888
mav 20,36,40
mav 24,40,40
buffer_store_dwordx4 v[68:71], v87, s[36:39], s49 offen offset:1024 sc1
mav 28,44,40
mav 16,48,44
R 56,38912
mav 20,52,44
mav 24,56,44
D 84,0
mav 28,60,44
s_waitcnt lgkmcnt(1)
mav 16,64,48
R 60,39936
mav 20,68,48
mav 24,72,48
D 84,1024
mav 28,76,48
mav 16,80,52
R 32,40960
mav 20,84,52
mav 24,88,52
mav 28,92,52
s_waitcnt lgkmcnt(1)
mav 16,96,56
R 36,41984
mav 20,100,56
mav 24,104,56
mav 28,108,56
mav 16,112,60
R 40,43008
mav 20,116,60
mav 24,120,60
mav 28,124,60
s_waitcnt lgkmcnt(1)
mav 16,128,32
R 44,44032
mav 20,132,32
mav 24,136,32
mav 28,140,32
mav 16,144,36
R 48,45056
v_add_u32 v72, 1, v72
mav 20,148,36
mav 24,152,36
mav 28,156,36
s_waitcnt lgkmcnt(1)
mav 16,160,40
R 52,46080
mav 20,164,40
mav 24,168,40
mav 28,172,40
mav 16,176,44
R 56,47104
mav 20,180,44
mav 24,184,44
mav 28,188,44
s_waitcnt lgkmcnt(1)
mav 16,192,48
R 60,48128
mav 20,196,48
mav 24,200,48
mav 28,204,48
mav 16,208,52
R 32,49152
mav 20,212,52
mav 24,216,52
mav 28,220,52
s_waitcnt lgkmcnt(1)
mav 16,224,56
R 36,50176
mav 20,228,56
mav 24,232,56
mav 28,236,56
mav 16,240,60
R 40,51200
mav 20,244,60
s_waitcnt vmcnt(2)
buffer_store_dwordx2 v[72:73], v81, s[36:39], s52 offen sc1
mav 24,248,60
mav 28,252,60
s_waitcnt lgkmcnt(1)
mvv 16,128,32
R 44,52224
mvv 20,132,32
mvv 24,136,32
mvv 28,140,32
mvv 16,144,36
R 48,53248
mvv 20,148,36
mvv 24,152,36
mvv 28,156,36
s_waitcnt lgkmcnt(1)
mvv 16,160,40
R 52,54272
mvv 20,164,40
mvv 24,168,40
mvv 28,172,40
mvv 16,176,44
R 56,55296
mvv 20,180,44
mvv 24,184,44
mvv 28,188,44
s_waitcnt lgkmcnt(1)
mvv 16,192,48
R 60,56320
mvv 20,196,48
v_xor_b32 v80, 0x10000, v80
mvv 24,200,48
s_add_u32 s49, s49, 346112
mvv 28,204,48
s_waitcnt vmcnt(1)
s_barrier
mvv 16,208,52
R 32,0
s_add_u32 m0, s50, 32768
mvv 20,212,52
D 83,0
mvv 24,216,52
mvv 28,220,52
s_waitcnt lgkmcnt(1)
mvv 16,224,56
R 36,1024
mvv 20,228,56
D 83,1024
mvv 24,232,56
mvv 28,236,56
mvv 16,240,60
R 40,2048
mvv 20,244,60
mvv 24,248,60
D 83,2048
mvv 28,252,60
s_add_u32 s60, s60, 1
s_cbranch_scc0 LG1_1
s_waitcnt lgkmcnt(1)
v_mfma_f32_16x16x32_bf16 v[0:3], a[0:3], v[32:35], 0
R 44,3072
v_cvt_pk_bf16_f32 v64, v16, v17
v_mfma_f32_16x16x32_bf16 v[4:7], a[4:7], v[32:35], 0
v_cvt_pk_bf16_f32 v65, v18, v19
v_cvt_pk_bf16_f32 v66, v20, v21
v_mfma_f32_16x16x32_bf16 v[8:11], a[8:11], v[32:35], 0
D 83,3072
v_cvt_pk_bf16_f32 v67, v22, v23
v_mfma_f32_16x16x32_bf16 v[12:15], a[12:15], v[32:35], 0
v_cvt_pk_bf16_f32 v68, v24, v25
v_cvt_pk_bf16_f32 v69, v26, v27
mav 0,16,36
R 48,4096
v_cvt_pk_bf16_f32 v70, v28, v29
mav 4,20,36
s_add_u32 m0, s50, 36864
v_cvt_pk_bf16_f32 v71, v30, v31
mav 8,24,36
buffer_store_dwordx4 v[64:67], v87, s[36:39], s49 offen sc1
mav 12,28,36
s_waitcnt lgkmcnt(1)
mav 0,32,40
R 52,5120
mav 4,36,40
mav 8,40,40
buffer_store_dwordx4 v[68:71], v87, s[36:39], s49 offen offset:1024 sc1
mav 12,44,40
mav 0,48,44
R 56,6144
mav 4,52,44
mav 8,56,44
D 84,0
mav 12,60,44
s_waitcnt lgkmcnt(1)
mav 0,64,48
R 60,7168
mav 4,68,48
mav 8,72,48
D 84,1024
mav 12,76,48
mav 0,80,52
R 32,8192
mav 4,84,52
mav 8,88,52
mav 12,92,52
s_waitcnt lgkmcnt(1)
mav 0,96,56
R 36,9216
mav 4,100,56
mav 8,104,56
mav 12,108,56
mav 0,112,60
R 40,10240
mav 4,116,60
mav 8,120,60
mav 12,124,60
s_waitcnt lgkmcnt(1)
mav 0,128,32
R 44,11264
mav 4,132,32
mav 8,136,32
mav 12,140,32
mav 0,144,36
R 48,12288
v_add_u32 v72, 1, v72
mav 4,148,36
mav 8,152,36
mav 12,156,36
s_waitcnt lgkmcnt(1)
mav 0,160,40
R 52,13312
mav 4,164,40
mav 8,168,40
mav 12,172,40
mav 0,176,44
R 56,14336
mav 4,180,44
mav 8,184,44
mav 12,188,44
s_waitcnt lgkmcnt(1)
mav 0,192,48
R 60,15360
mav 4,196,48
mav 8,200,48
mav 12,204,48
mav 0,208,52
R 32,16384
mav 4,212,52
mav 8,216,52
mav 12,220,52
s_waitcnt lgkmcnt(1)
mav 0,224,56
R 36,17408
mav 4,228,56
mav 8,232,56
mav 12,236,56
mav 0,240,60
R 40,18432
mav 4,244,60
s_waitcnt vmcnt(2)
buffer_store_dwordx2 v[72:73], v81, s[36:39], s52 offen sc1
mav 8,248,60
mav 12,252,60
s_waitcnt lgkmcnt(1)
mvv 0,128,32
R 44,19456
mvv 4,132,32
mvv 8,136,32
mvv 12,140,32
mvv 0,144,36
R 48,20480
mvv 4,148,36
mvv 8,152,36
mvv 12,156,36
s_waitcnt lgkmcnt(1)
mvv 0,160,40
R 52,21504
mvv 4,164,40
mvv 8,168,40
mvv 12,172,40
mvv 0,176,44
R 56,22528
mvv 4,180,44
mvv 8,184,44
mvv 12,188,44
s_waitcnt lgkmcnt(1)
mvv 0,192,48
R 60,23552
mvv 4,196,48
mvv 8,200,48
s_add_u32 s49, s49, 346112
mvv 12,204,48
s_waitcnt vmcnt(1)
s_barrier
mvv 0,208,52
R 32,32768
mvv 4,212,52
mvv 8,216,52
mvv 12,220,52
s_waitcnt lgkmcnt(1)
mvv 0,224,56
R 36,33792
mvv 4,228,56
mvv 8,232,56
mvv 12,236,56
mvv 0,240,60
R 40,34816
mvv 4,244,60
mvv 8,248,60
mvv 12,252,60
s_waitcnt lgkmcnt(1)
v_mfma_f32_16x16x32_bf16 v[16:19], a[0:3], v[32:35], 0
R 44,35840
v_mfma_f32_16x16x32_bf16 v[20:23], a[4:7], v[32:35], 0
v_cvt_pk_bf16_f32 v64, v0, v1
v_cvt_pk_bf16_f32 v65, v2, v3
v_mfma_f32_16x16x32_bf16 v[24:27], a[8:11], v[32:35], 0
v_cvt_pk_bf16_f32 v66, v4, v5
v_cvt_pk_bf16_f32 v67, v6, v7
v_mfma_f32_16x16x32_bf16 v[28:31], a[12:15], v[32:35], 0
buffer_store_dwordx4 v[64:67], v87, s[36:39], s49 offen sc1
v_cvt_pk_bf16_f32 v68, v8, v9
mav 16,16,36
R 48,36864
v_cvt_pk_bf16_f32 v69, v10, v11
mav 20,20,36
v_cvt_pk_bf16_f32 v70, v12, v13
v_cvt_pk_bf16_f32 v71, v14, v15
mav 24,24,36
mav 28,28,36
buffer_store_dwordx4 v[68:71], v87, s[36:39], s49 offen offset:1024 sc1
s_waitcnt lgkmcnt(1)
mav 16,32,40
R 52,37888
mav 20,36,40
mav 24,40,40
mav 28,44,40
mav 16,48,44
R 56,38912
mav 20,52,44
mav 24,56,44
mav 28,60,44
s_waitcnt lgkmcnt(1)
mav 16,64,48
R 60,39936
mav 20,68,48
mav 24,72,48
mav 28,76,48
mav 16,80,52
R 32,40960
mav 20,84,52
mav 24,88,52
mav 28,92,52
s_waitcnt lgkmcnt(1)
mav 16,96,56
R 36,41984
mav 20,100,56
mav 24,104,56
mav 28,108,56
mav 16,112,60
R 40,43008
mav 20,116,60
mav 24,120,60
mav 28,124,60
s_waitcnt lgkmcnt(1)
mav 16,128,32
R 44,44032
mav 20,132,32
mav 24,136,32
mav 28,140,32
mav 16,144,36
R 48,45056
v_add_u32 v72, 1, v72
mav 20,148,36
mav 24,152,36
mav 28,156,36
s_waitcnt lgkmcnt(1)
mav 16,160,40
R 52,46080
mav 20,164,40
mav 24,168,40
mav 28,172,40
mav 16,176,44
R 56,47104
mav 20,180,44
mav 24,184,44
mav 28,188,44
s_waitcnt lgkmcnt(1)
mav 16,192,48
R 60,48128
mav 20,196,48
mav 24,200,48
mav 28,204,48
mav 16,208,52
R 32,49152
mav 20,212,52
mav 24,216,52
mav 28,220,52
s_waitcnt lgkmcnt(1)
mav 16,224,56
R 36,50176
mav 20,228,56
mav 24,232,56
mav 28,236,56
mav 16,240,60
R 40,51200
mav 20,244,60
s_waitcnt vmcnt(0)
buffer_store_dwordx2 v[72:73], v81, s[36:39], s52 offen sc1
mav 24,248,60
mav 28,252,60
s_waitcnt lgkmcnt(1)
mvv 16,128,32
R 44,52224
mvv 20,132,32
mvv 24,136,32
mvv 28,140,32
mvv 16,144,36
R 48,53248
mvv 20,148,36
mvv 24,152,36
mvv 28,156,36
s_waitcnt lgkmcnt(1)
mvv 16,160,40
R 52,54272
mvv 20,164,40
mvv 24,168,40
mvv 28,172,40
mvv 16,176,44
R 56,55296
mvv 20,180,44
mvv 24,184,44
mvv 28,188,44
s_waitcnt lgkmcnt(1)
mvv 16,192,48
R 60,56320
mvv 20,196,48
mvv 24,200,48
s_add_u32 s49, s49, 346112
mvv 28,204,48
mvv 16,208,52
mvv 20,212,52
mvv 24,216,52
mvv 28,220,52
s_waitcnt lgkmcnt(0)
mvv 16,224,56
mvv 20,228,56
mvv 24,232,56
mvv 28,236,56
mvv 16,240,60
mvv 20,244,60
mvv 24,248,60
mvv 28,252,60
s_nop 4
v_cvt_pk_bf16_f32 v64, v16, v17
v_cvt_pk_bf16_f32 v65, v18, v19
v_cvt_pk_bf16_f32 v66, v20, v21
v_cvt_pk_bf16_f32 v67, v22, v23
buffer_store_dwordx4 v[64:67], v87, s[36:39], s49 offen sc1
v_cvt_pk_bf16_f32 v68, v24, v25
v_cvt_pk_bf16_f32 v69, v26, v27
v_cvt_pk_bf16_f32 v70, v28, v29
v_cvt_pk_bf16_f32 v71, v30, v31
buffer_store_dwordx4 v[68:71], v87, s[36:39], s49 offen offset:1024 sc1
v_add_u32 v72, 35, v72
s_waitcnt vmcnt(0)
buffer_store_dwordx2 v[72:73], v81, s[36:39], s52 offen sc1
s_endpgm
Lr5:
s_waitcnt vmcnt(0)
s_barrier
v_accvgpr_read_b32 v73, a255
v_mov_b32 v72, 0x100
v_add_u32 v73, 0x9e3779b9, v73
buffer_store_dwordx2 v[72:73], v81, s[36:39], s52 offen sc1
v_add_u32 v85, v85, v72
s_sleep 32
s_sleep 32
s_sleep 32
E 0,65536
E 1,69632
E 2,73728
E 3,77824
s_add_u32 s51, s51, 65536
E 0,81920
E 1,86016
E 2,90112
E 3,94208
s_add_u32 s51, s51, 65536
E 0,98304
E 1,102400
E 2,106496
E 3,110592
s_add_u32 s51, s51, 65536
E 0,114688
E 1,118784
E 2,122880
E 3,126976
s_add_u32 s51, s51, 65536
s_cmp_ge_u32 s51, s57
s_cselect_b32 s60, 1048576, 0
s_sub_u32 s51, s51, s60
s_mov_b64 exec, 0
s_cmp_eq_u32 s53, 0
s_cbranch_scc1 LsR0
s_cmp_eq_u32 s53, 1024
s_cbranch_scc1 LsR1
s_cmp_eq_u32 s53, 2048
s_cbranch_scc1 LsR2
s_branch LsR3
LwR:
s_mov_b64 exec, -1
LsR0:
s_cbranch_execz LsR4
s_waitcnt vmcnt(8)
s_barrier
v_add_u32 v4, 65536, v4
v_add_u32 v5, 65536, v5
v_add_u32 v6, 65536, v6
v_add_u32 v7, 65536, v7
Ta 0,4,0
Ta 4,5,0
Ta 8,6,0
Ta 12,7,0
Ta 16,4,16384
Ta 20,5,16384
Ta 24,6,16384
Ta 28,7,16384
E 0,131072
E 1,135168
E 2,139264
E 3,143360
s_add_u32 s51, s51, 65536
E 0,147456
E 1,151552
E 2,155648
E 3,159744
s_add_u32 s51, s51, 65536
s_waitcnt lgkmcnt(0)
s_waitcnt vmcnt(8)
s_barrier
E 0,65536
Ta 32,4,32768
Ta 36,5,32768
E 1,69632
Ta 40,6,32768
Ta 44,7,32768
E 2,73728
Ta 48,4,49152
E 3,77824
s_add_u32 s51, s51, 65536
Ta 52,5,49152
Ta 56,6,49152
E 0,81920
Ta 60,7,49152
E 1,86016
E 2,90112
E 3,94208
s_add_u32 s51, s51, 65536
s_cmp_ge_u32 s51, s57
s_cselect_b32 s60, 1048576, 0
s_sub_u32 s51, s51, s60
s_waitcnt lgkmcnt(0)
s_waitcnt vmcnt(8)
s_barrier
v_add_u32 v4, 65536, v4
v_add_u32 v5, 65536, v5
v_add_u32 v6, 65536, v6
v_add_u32 v7, 65536, v7
E 0,98304
Ta 64,4,0
Ta 68,5,0
E 1,102400
Ta 72,6,0
Ta 76,7,0
E 2,106496
Ta 80,4,16384
E 3,110592
s_add_u32 s51, s51, 65536
Ta 84,5,16384
Ta 88,6,16384
E 0,114688
Ta 92,7,16384
E 1,118784
E 2,122880
E 3,126976
s_add_u32 s51, s51, 65536
s_waitcnt lgkmcnt(0)
s_waitcnt vmcnt(8)
s_barrier
v_add_u32 v4, -65536, v4
v_add_u32 v5, -65536, v5
v_add_u32 v6, -65536, v6
v_add_u32 v7, -65536, v7
E 0,131072
Ta 96,4,0
Ta 100,5,0
E 1,135168
Ta 104,6,0
Ta 108,7,0
E 2,139264
Ta 112,4,16384
E 3,143360
s_add_u32 s51, s51, 65536
Ta 116,5,16384
Ta 120,6,16384
E 0,147456
Ta 124,7,16384
E 1,151552
E 2,155648
E 3,159744
s_add_u32 s51, s51, 65536
s_cmp_ge_u32 s51, s57
s_cselect_b32 s60, 1048576, 0
s_sub_u32 s51, s51, s60
s_waitcnt lgkmcnt(0)
s_waitcnt vmcnt(8)
s_barrier
E 0,65536
Ta 128,4,32768
Ta 132,5,32768
E 1,69632
Ta 136,6,32768
Ta 140,7,32768
E 2,73728
Ta 144,4,49152
E 3,77824
s_add_u32 s51, s51, 65536
Ta 148,5,49152
Ta 152,6,49152
E 0,81920
Ta 156,7,49152
E 1,86016
E 2,90112
E 3,94208
s_add_u32 s51, s51, 65536
s_waitcnt lgkmcnt(0)
s_waitcnt vmcnt(8)
s_barrier
v_add_u32 v4, 65536, v4
v_add_u32 v5, 65536, v5
v_add_u32 v6, 65536, v6
v_add_u32 v7, 65536, v7
E 0,98304
Ta 160,4,0
Ta 164,5,0
E 1,102400
Ta 168,6,0
Ta 172,7,0
E 2,106496
Ta 176,4,16384
E 3,110592
s_add_u32 s51, s51, 65536
Ta 180,5,16384
Ta 184,6,16384
E 0,114688
Ta 188,7,16384
E 1,118784
E 2,122880
E 3,126976
s_add_u32 s51, s51, 65536
s_cmp_ge_u32 s51, s57
s_cselect_b32 s60, 1048576, 0
s_sub_u32 s51, s51, s60
s_waitcnt lgkmcnt(0)
s_waitcnt vmcnt(8)
s_barrier
v_add_u32 v4, -65536, v4
v_add_u32 v5, -65536, v5
v_add_u32 v6, -65536, v6
v_add_u32 v7, -65536, v7
Ta 192,4,0
Ta 196,5,0
Ta 200,6,0
Ta 204,7,0
Ta 208,4,16384
Ta 212,5,16384
Ta 216,6,16384
Ta 220,7,16384
s_waitcnt lgkmcnt(0)
s_waitcnt vmcnt(0)
s_barrier
Ta 224,4,32768
Ta 228,5,32768
Ta 232,6,32768
Ta 236,7,32768
Ta 240,4,49152
Ta 244,5,49152
Ta 248,6,49152
Ta 252,7,49152
s_waitcnt lgkmcnt(0)
s_waitcnt vmcnt(0)
s_barrier
s_nop 7
s_and_b32 s60, s48, 3
s_cmp_eq_u32 s60, 1
s_cbranch_scc1 LvR1
s_cmp_eq_u32 s60, 2
s_cbranch_scc1 LvR2
LvR0:
LsR1:
s_cbranch_execz LsR5
buffer_load_dwordx2 v[76:77], v84, s[36:39], 0 offen sc1
s_waitcnt vmcnt(0)
ds_write_b32 v82, v76
v_sub_u32 v76, v76, v85
v_xor_b32 v77, v77, v73
v_cmp_lt_u64 vcc, s[62:63], v[76:77]
s_waitcnt lgkmcnt(0)
s_cbranch_vccnz CR0_1
BR0_1:
s_add_u32 s49, s49, 346112
s_barrier
s_add_u32 m0, s50, 0
s_nop 0
D 83,0
D 83,1024
D 83,2048
D 83,3072
s_mov_b32 m0, 142336
s_nop 0
buffer_load_dword v84, s[36:39], 0 offen sc1 lds
v_add_u32 v85, 1, v85
ds_read_b32 v76, v82 offset:0
s_waitcnt lgkmcnt(0)
v_cmp_gt_i32 vcc, v85, v76
s_cbranch_vccnz CR0_2
BR0_2:
s_add_u32 s49, s49, 346112
s_barrier
s_add_u32 m0, s50, 32768
s_nop 0
D 83,0
D 83,1024
D 83,2048
D 83,3072
s_mov_b32 m0, 142336
s_nop 0
buffer_load_dword v84, s[36:39], 0 offen sc1 lds
v_add_u32 v85, 1, v85
buffer_load_dwordx4 v[124:127], v90, s[36:39], s49 offen sc1
buffer_load_dwordx4 v[128:131], v90, s[36:39], s49 offen offset:1024 sc1
ds_read_b32 v76, v82 offset:0
s_xor_b32 s50, s50, 0x10000
s_waitcnt lgkmcnt(0)
v_cmp_gt_i32 vcc, v85, v76
s_waitcnt vmcnt(8)
s_cbranch_vccnz CR0_3
BR0_3:
s_add_u32 s49, s49, 346112
s_barrier
R 32,0
buffer_load_dwordx4 v[132:135], v86, s[36:39], s49 offen sc1
R 36,1024
buffer_load_dwordx4 v[136:139], v86, s[36:39], s49 offen offset:1024 sc1
R 40,2048
buffer_load_dwordx4 v[140:143], v87, s[36:39], s49 offen sc1
s_waitcnt lgkmcnt(1)
v_mfma_f32_16x16x32_bf16 v[0:3], a[0:3], v[32:35], v[100:103]
R 44,3072
v_mfma_f32_16x16x32_bf16 v[4:7], a[4:7], v[32:35], v[104:107]
v_mfma_f32_16x16x32_bf16 v[8:11], a[8:11], v[32:35], v[108:111]
buffer_load_dwordx4 v[144:147], v87, s[36:39], s49 offen offset:1024 sc1
v_mfma_f32_16x16x32_bf16 v[12:15], a[12:15], v[32:35], v[112:115]
mav 0,16,36
R 48,4096
mav 4,20,36
mav 8,24,36
mav 12,28,36
buffer_load_dwordx4 v[148:151], v88, s[36:39], s49 offen sc1
s_waitcnt lgkmcnt(1)
mav 0,32,40
R 52,5120
mav 4,36,40
mav 8,40,40
mav 12,44,40
buffer_load_dwordx4 v[152:155], v88, s[36:39], s49 offen offset:1024 sc1
mav 0,48,44
R 56,6144
mav 4,52,44
mav 8,56,44
mav 12,60,44
s_waitcnt lgkmcnt(1)
mav 0,64,48
R 60,7168
buffer_load_dwordx4 v[156:159], v89, s[36:39], s49 offen sc1
mav 4,68,48
mav 8,72,48
mav 12,76,48
mav 0,80,52
R 32,8192
buffer_load_dwordx4 v[160:163], v89, s[36:39], s49 offen offset:1024 sc1
mav 4,84,52
mav 8,88,52
mav 12,92,52
s_add_u32 m0, s50, 0
s_waitcnt lgkmcnt(1)
mav 0,96,56
R 36,9216
mav 4,100,56
D 83,0
mav 8,104,56
mav 12,108,56
mav 0,112,60
R 40,10240
mav 4,116,60
D 83,1024
mav 8,120,60
mav 12,124,60
s_waitcnt lgkmcnt(1)
mav 0,128,32
R 44,11264
mav 4,132,32
mav 8,136,32
D 83,2048
mav 12,140,32
mav 0,144,36
R 48,12288
mav 4,148,36
mav 8,152,36
D 83,3072
s_mov_b32 m0, 142336
mav 12,156,36
buffer_load_dword v84, s[36:39], 0 offen sc1 lds
s_waitcnt lgkmcnt(1)
mav 0,160,40
R 52,13312
v_add_u32 v85, 1, v85
mav 4,164,40
mav 8,168,40
mav 12,172,40
buffer_load_dwordx4 v[164:167], v90, s[36:39], s49 offen sc1
mav 0,176,44
R 56,14336
mav 4,180,44
mav 8,184,44
mav 12,188,44
buffer_load_dwordx4 v[168:171], v90, s[36:39], s49 offen offset:1024 sc1
s_waitcnt lgkmcnt(1)
mav 0,192,48
R 60,15360
mav 4,196,48
mav 8,200,48
mav 12,204,48
mav 0,208,52
mav 4,212,52
mav 8,216,52
mav 12,220,52
s_waitcnt lgkmcnt(0)
mav 0,224,56
mav 4,228,56
mav 8,232,56
mav 12,236,56
mav 0,240,60
mav 4,244,60
ds_read_b32 v76, v82 offset:0
mav 8,248,60
mav 12,252,60
s_waitcnt vmcnt(16)
mvv 0,116,124
mvv 4,120,124
s_waitcnt vmcnt(15)
mvv 8,116,128
mvv 12,120,128
s_waitcnt vmcnt(14)
mvv 0,116,132
mvv 4,120,132
s_waitcnt lgkmcnt(0)
v_cmp_gt_i32 vcc, v85, v76
s_waitcnt vmcnt(13)
mvv 8,116,136
s_cbranch_vccnz CR0_4
BR0_4:
s_add_u32 s49, s49, 346112
mvv 12,120,136
s_barrier
s_waitcnt vmcnt(12)
mvv 0,116,140
R 32,32768
mvv 4,120,140
buffer_load_dwordx4 v[172:175], v86, s[36:39], s49 offen sc1
s_waitcnt vmcnt(12)
mvv 8,116,144
mvv 12,120,144
s_waitcnt vmcnt(11)
mvv 0,116,148
R 36,33792
mvv 4,120,148
buffer_load_dwordx4 v[176:179], v86, s[36:39], s49 offen offset:1024 sc1
s_waitcnt vmcnt(11)
mvv 8,116,152
mvv 12,120,152
s_waitcnt vmcnt(10)
mvv 0,116,156
R 40,34816
mvv 4,120,156
s_waitcnt vmcnt(9)
mvv 8,116,160
buffer_load_dwordx4 v[180:183], v87, s[36:39], s49 offen sc1
mvv 12,120,160
s_waitcnt lgkmcnt(1)
v_mfma_f32_16x16x32_bf16 v[16:19], a[0:3], v[32:35], v[100:103]
R 44,35840
v_cvt_pk_bf16_f32 v64, v0, v1
v_mfma_f32_16x16x32_bf16 v[20:23], a[4:7], v[32:35], v[104:107]
v_cvt_pk_bf16_f32 v65, v2, v3
v_cvt_pk_bf16_f32 v66, v4, v5
v_mfma_f32_16x16x32_bf16 v[24:27], a[8:11], v[32:35], v[108:111]
buffer_load_dwordx4 v[184:187], v87, s[36:39], s49 offen offset:1024 sc1
v_cvt_pk_bf16_f32 v67, v6, v7
v_mfma_f32_16x16x32_bf16 v[28:31], a[12:15], v[32:35], v[112:115]
v_cvt_pk_bf16_f32 v68, v8, v9
v_cvt_pk_bf16_f32 v69, v10, v11
mav 16,16,36
R 48,36864
v_cvt_pk_bf16_f32 v70, v12, v13
mav 20,20,36
v_cvt_pk_bf16_f32 v71, v14, v15
v_mul_f32 v75, v0, v0
mav 24,24,36
buffer_store_dwordx4 v[64:67], v91, s[36:39], s49 offen sc1
v_add_f32 v74, v1, v0
mav 28,28,36
v_fmac_f32 v75, v1, v1
s_waitcnt lgkmcnt(1)
mav 16,32,40
R 52,37888
v_add_f32 v74, v2, v74
mav 20,36,40
v_fmac_f32 v75, v2, v2
v_add_f32 v74, v3, v74
mav 24,40,40
buffer_store_dwordx4 v[68:71], v91, s[36:39], s49 offen offset:1024 sc1
v_fmac_f32 v75, v3, v3
mav 28,44,40
v_add_f32 v74, v4, v74
v_fmac_f32 v75, v4, v4
mav 16,48,44
R 56,38912
v_add_f32 v74, v5, v74
mav 20,52,44
v_fmac_f32 v75, v5, v5
v_add_f32 v74, v6, v74
LsR2:
s_cbranch_execz LsR6
mav 24,56,44
buffer_load_dwordx4 v[188:191], v88, s[36:39], s49 offen sc1
v_fmac_f32 v75, v6, v6
mav 28,60,44
v_add_f32 v74, v7, v74
s_waitcnt lgkmcnt(1)
mav 16,64,48
R 60,39936
v_fmac_f32 v75, v7, v7
mav 20,68,48
v_add_f32 v74, v8, v74
v_fmac_f32 v75, v8, v8
mav 24,72,48
buffer_load_dwordx4 v[192:195], v88, s[36:39], s49 offen offset:1024 sc1
v_add_f32 v74, v9, v74
mav 28,76,48
v_fmac_f32 v75, v9, v9
v_add_f32 v74, v10, v74
mav 16,80,52
R 32,40960
v_fmac_f32 v75, v10, v10
mav 20,84,52
v_add_f32 v74, v11, v74
v_fmac_f32 v75, v11, v11
mav 24,88,52
buffer_load_dwordx4 v[196:199], v89, s[36:39], s49 offen sc1
v_add_f32 v74, v12, v74
mav 28,92,52
s_add_u32 m0, s50, 32768
s_waitcnt lgkmcnt(1)
mav 16,96,56
R 36,41984
v_fmac_f32 v75, v12, v12
mav 20,100,56
v_add_f32 v74, v13, v74
v_fmac_f32 v75, v13, v13
mav 24,104,56
buffer_load_dwordx4 v[200:203], v89, s[36:39], s49 offen offset:1024 sc1
v_add_f32 v74, v14, v74
mav 28,108,56
v_fmac_f32 v75, v14, v14
v_add_f32 v74, v15, v74
mav 16,112,60
R 40,43008
v_fmac_f32 v75, v15, v15
mav 20,116,60
mav 24,120,60
D 83,0
v_permlane32_swap_b32 v74, v75
mav 28,124,60
v_add_f32 v74, v74, v75
s_waitcnt lgkmcnt(1)
mav 16,128,32
R 44,44032
mav 20,132,32
mav 24,136,32
D 83,1024
mav 28,140,32
mav 16,144,36
R 48,45056
v_add_u32 v72, 1, v72
mav 20,148,36
mav 24,152,36
D 83,2048
mav 28,156,36
s_waitcnt lgkmcnt(1)
mav 16,160,40
R 52,46080
v_add_u32 v85, 1, v85
mav 20,164,40
mav 24,168,40
D 83,3072
s_mov_b32 m0, 142336
mav 28,172,40
buffer_load_dword v84, s[36:39], 0 offen sc1 lds
mav 16,176,44
R 56,47104
mav 20,180,44
mav 24,184,44
buffer_load_dwordx4 v[124:127], v90, s[36:39], s49 offen sc1
mav 28,188,44
s_waitcnt lgkmcnt(1)
mav 16,192,48
R 60,48128
mav 20,196,48
mav 24,200,48
buffer_load_dwordx4 v[128:131], v90, s[36:39], s49 offen offset:1024 sc1
mav 28,204,48
mav 16,208,52
mav 20,212,52
mav 24,216,52
mav 28,220,52
s_waitcnt lgkmcnt(0)
mav 16,224,56
mav 20,228,56
mav 24,232,56
mav 28,236,56
mav 16,240,60
s_waitcnt vmcnt(11)
buffer_store_dwordx2 v[72:73], v81, s[36:39], s52 offen sc1
mav 20,244,60
ds_read_b32 v76, v82 offset:0
mav 24,248,60
ds_write_b32 v97, v74 offset:0
mav 28,252,60
mvv 16,116,164
mvv 20,120,164
mvv 24,116,168
s_xor_b32 s50, s50, 0x10000
mvv 28,120,168
mvv 16,116,172
mvv 20,120,172
v_xor_b32 v80, 0x10000, v80
mvv 24,116,176
s_waitcnt lgkmcnt(0)
v_cmp_gt_i32 vcc, v85, v76
mvv 28,120,176
s_cbranch_vccnz CR0_5
BR0_5:
s_barrier
mvv 16,116,180
R 32,0
s_add_u32 s49, s49, 346112
mvv 20,120,180
buffer_load_dwordx4 v[132:135], v86, s[36:39], s49 offen sc1
mvv 24,116,184
mvv 28,120,184
s_waitcnt vmcnt(12)
mvv 16,116,188
R 36,1024
mvv 20,120,188
buffer_load_dwordx4 v[136:139], v86, s[36:39], s49 offen offset:1024 sc1
s_waitcnt vmcnt(12)
mvv 24,116,192
mvv 28,120,192
s_waitcnt vmcnt(11)
mvv 16,116,196
R 40,2048
mvv 20,120,196
s_waitcnt vmcnt(10)
mvv 24,116,200
buffer_load_dwordx4 v[140:143], v87, s[36:39], s49 offen sc1
mvv 28,120,200
s_mov_b32 s60, -15
.p2align 6
LR0_6:
s_waitcnt lgkmcnt(1)
v_mfma_f32_16x16x32_bf16 v[0:3], a[0:3], v[32:35], v[100:103]
R 44,3072
v_cvt_pk_bf16_f32 v64, v16, v17
v_mfma_f32_16x16x32_bf16 v[4:7], a[4:7], v[32:35], v[104:107]
v_cvt_pk_bf16_f32 v65, v18, v19
v_cvt_pk_bf16_f32 v66, v20, v21
v_mfma_f32_16x16x32_bf16 v[8:11], a[8:11], v[32:35], v[108:111]
buffer_load_dwordx4 v[144:147], v87, s[36:39], s49 offen offset:1024 sc1
v_cvt_pk_bf16_f32 v67, v22, v23
v_mfma_f32_16x16x32_bf16 v[12:15], a[12:15], v[32:35], v[112:115]
v_cvt_pk_bf16_f32 v68, v24, v25
v_cvt_pk_bf16_f32 v69, v26, v27
mav 0,16,36
R 48,4096
v_cvt_pk_bf16_f32 v70, v28, v29
mav 4,20,36
v_cvt_pk_bf16_f32 v71, v30, v31
v_mul_f32 v75, v16, v16
mav 8,24,36
buffer_store_dwordx4 v[64:67], v91, s[36:39], s49 offen sc1
v_add_f32 v74, v17, v16
mav 12,28,36
v_fmac_f32 v75, v17, v17
s_waitcnt lgkmcnt(1)
mav 0,32,40
R 52,5120
v_add_f32 v74, v18, v74
mav 4,36,40
v_fmac_f32 v75, v18, v18
v_add_f32 v74, v19, v74
mav 8,40,40
buffer_store_dwordx4 v[68:71], v91, s[36:39], s49 offen offset:1024 sc1
v_fmac_f32 v75, v19, v19
mav 12,44,40
v_add_f32 v74, v20, v74
v_fmac_f32 v75, v20, v20
mav 0,48,44
R 56,6144
v_add_f32 v74, v21, v74
mav 4,52,44
v_fmac_f32 v75, v21, v21
v_add_f32 v74, v22, v74
mav 8,56,44
buffer_load_dwordx4 v[148:151], v88, s[36:39], s49 offen sc1
v_fmac_f32 v75, v22, v22
mav 12,60,44
v_add_f32 v74, v23, v74
s_waitcnt lgkmcnt(1)
mav 0,64,48
R 60,7168
v_fmac_f32 v75, v23, v23
mav 4,68,48
v_add_f32 v74, v24, v74
v_fmac_f32 v75, v24, v24
mav 8,72,48
buffer_load_dwordx4 v[152:155], v88, s[36:39], s49 offen offset:1024 sc1
v_add_f32 v74, v25, v74
mav 12,76,48
v_fmac_f32 v75, v25, v25
v_add_f32 v74, v26, v74
mav 0,80,52
R 32,8192
v_fmac_f32 v75, v26, v26
mav 4,84,52
v_add_f32 v74, v27, v74
v_fmac_f32 v75, v27, v27
mav 8,88,52
buffer_load_dwordx4 v[156:159], v89, s[36:39], s49 offen sc1
v_add_f32 v74, v28, v74
mav 12,92,52
s_add_u32 m0, s50, 0
s_waitcnt lgkmcnt(1)
mav 0,96,56
R 36,9216
v_fmac_f32 v75, v28, v28
mav 4,100,56
v_add_f32 v74, v29, v74
v_fmac_f32 v75, v29, v29
mav 8,104,56
buffer_load_dwordx4 v[160:163], v89, s[36:39], s49 offen offset:1024 sc1
v_add_f32 v74, v30, v74
mav 12,108,56
v_fmac_f32 v75, v30, v30
v_add_f32 v74, v31, v74
mav 0,112,60
R 40,10240
v_fmac_f32 v75, v31, v31
mav 4,116,60
mav 8,120,60
D 83,0
v_permlane32_swap_b32 v74, v75
mav 12,124,60
v_add_f32 v74, v74, v75
s_waitcnt lgkmcnt(1)
mav 0,128,32
R 44,11264
mav 4,132,32
mav 8,136,32
D 83,1024
mav 12,140,32
mav 0,144,36
R 48,12288
v_add_u32 v72, 1, v72
mav 4,148,36
mav 8,152,36
D 83,2048
mav 12,156,36
s_waitcnt lgkmcnt(1)
mav 0,160,40
R 52,13312
v_add_u32 v85, 1, v85
mav 4,164,40
mav 8,168,40
D 83,3072
s_mov_b32 m0, 142336
mav 12,172,40
buffer_load_dword v84, s[36:39], 0 offen sc1 lds
mav 0,176,44
R 56,14336
mav 4,180,44
mav 8,184,44
buffer_load_dwordx4 v[164:167], v90, s[36:39], s49 offen sc1
mav 12,188,44
s_waitcnt lgkmcnt(1)
mav 0,192,48
R 60,15360
mav 4,196,48
mav 8,200,48
buffer_load_dwordx4 v[168:171], v90, s[36:39], s49 offen offset:1024 sc1
mav 12,204,48
mav 0,208,52
mav 4,212,52
mav 8,216,52
mav 12,220,52
s_waitcnt lgkmcnt(0)
mav 0,224,56
mav 4,228,56
mav 8,232,56
mav 12,236,56
mav 0,240,60
s_waitcnt vmcnt(11)
buffer_store_dwordx2 v[72:73], v81, s[36:39], s52 offen sc1
mav 4,244,60
ds_read_b32 v76, v82 offset:0
mav 8,248,60
ds_write_b32 v97, v74 offset:1024
mav 12,252,60
mvv 0,116,124
mvv 4,120,124
mvv 8,116,128
mvv 12,120,128
mvv 0,116,132
mvv 4,120,132
s_waitcnt lgkmcnt(0)
v_cmp_gt_i32 vcc, v85, v76
mvv 8,116,136
s_cbranch_vccnz CR0_7
BR0_7:
s_add_u32 s49, s49, 346112
mvv 12,120,136
s_barrier
mvv 0,116,140
R 32,32768
mvv 4,120,140
buffer_load_dwordx4 v[172:175], v86, s[36:39], s49 offen sc1
mvv 8,116,144
mvv 12,120,144
s_waitcnt vmcnt(12)
mvv 0,116,148
R 36,33792
mvv 4,120,148
buffer_load_dwordx4 v[176:179], v86, s[36:39], s49 offen offset:1024 sc1
s_waitcnt vmcnt(12)
mvv 8,116,152
mvv 12,120,152
s_waitcnt vmcnt(11)
mvv 0,116,156
R 40,34816
mvv 4,120,156
s_waitcnt vmcnt(10)
mvv 8,116,160
buffer_load_dwordx4 v[180:183], v87, s[36:39], s49 offen sc1
mvv 12,120,160
s_waitcnt lgkmcnt(1)
v_mfma_f32_16x16x32_bf16 v[16:19], a[0:3], v[32:35], v[100:103]
R 44,35840
v_cvt_pk_bf16_f32 v64, v0, v1
v_mfma_f32_16x16x32_bf16 v[20:23], a[4:7], v[32:35], v[104:107]
v_cvt_pk_bf16_f32 v65, v2, v3
v_cvt_pk_bf16_f32 v66, v4, v5
v_mfma_f32_16x16x32_bf16 v[24:27], a[8:11], v[32:35], v[108:111]
buffer_load_dwordx4 v[184:187], v87, s[36:39], s49 offen offset:1024 sc1
v_cvt_pk_bf16_f32 v67, v6, v7
v_mfma_f32_16x16x32_bf16 v[28:31], a[12:15], v[32:35], v[112:115]
v_cvt_pk_bf16_f32 v68, v8, v9
v_cvt_pk_bf16_f32 v69, v10, v11
mav 16,16,36
R 48,36864
v_cvt_pk_bf16_f32 v70, v12, v13
mav 20,20,36
v_cvt_pk_bf16_f32 v71, v14, v15
v_mul_f32 v75, v0, v0
mav 24,24,36
buffer_store_dwordx4 v[64:67], v91, s[36:39], s49 offen sc1
v_add_f32 v74, v1, v0
mav 28,28,36
v_fmac_f32 v75, v1, v1
s_waitcnt lgkmcnt(1)
mav 16,32,40
R 52,37888
v_add_f32 v74, v2, v74
mav 20,36,40
v_fmac_f32 v75, v2, v2
v_add_f32 v74, v3, v74
mav 24,40,40
buffer_store_dwordx4 v[68:71], v91, s[36:39], s49 offen offset:1024 sc1
v_fmac_f32 v75, v3, v3
mav 28,44,40
v_add_f32 v74, v4, v74
v_fmac_f32 v75, v4, v4
mav 16,48,44
R 56,38912
v_add_f32 v74, v5, v74
mav 20,52,44
v_fmac_f32 v75, v5, v5
v_add_f32 v74, v6, v74
mav 24,56,44
buffer_load_dwordx4 v[188:191], v88, s[36:39], s49 offen sc1
v_fmac_f32 v75, v6, v6
mav 28,60,44
v_add_f32 v74, v7, v74
s_waitcnt lgkmcnt(1)
mav 16,64,48
R 60,39936
v_fmac_f32 v75, v7, v7
mav 20,68,48
v_add_f32 v74, v8, v74
v_fmac_f32 v75, v8, v8
mav 24,72,48
buffer_load_dwordx4 v[192:195], v88, s[36:39], s49 offen offset:1024 sc1
v_add_f32 v74, v9, v74
mav 28,76,48
v_fmac_f32 v75, v9, v9
v_add_f32 v74, v10, v74
mav 16,80,52
R 32,40960
v_fmac_f32 v75, v10, v10
mav 20,84,52
v_add_f32 v74, v11, v74
v_fmac_f32 v75, v11, v11
mav 24,88,52
buffer_load_dwordx4 v[196:199], v89, s[36:39], s49 offen sc1
v_add_f32 v74, v12, v74
mav 28,92,52
s_add_u32 m0, s50, 32768
s_waitcnt lgkmcnt(1)
mav 16,96,56
R 36,41984
v_fmac_f32 v75, v12, v12
mav 20,100,56
v_add_f32 v74, v13, v74
v_fmac_f32 v75, v13, v13
mav 24,104,56
buffer_load_dwordx4 v[200:203], v89, s[36:39], s49 offen offset:1024 sc1
v_add_f32 v74, v14, v74
mav 28,108,56
v_fmac_f32 v75, v14, v14
v_add_f32 v74, v15, v74
mav 16,112,60
R 40,43008
v_fmac_f32 v75, v15, v15
mav 20,116,60
mav 24,120,60
D 83,0
v_permlane32_swap_b32 v74, v75
mav 28,124,60
v_add_f32 v74, v74, v75
s_waitcnt lgkmcnt(1)
mav 16,128,32
R 44,44032
mav 20,132,32
mav 24,136,32
D 83,1024
mav 28,140,32
mav 16,144,36
R 48,45056
v_add_u32 v72, 1, v72
mav 20,148,36
mav 24,152,36
D 83,2048
mav 28,156,36
s_waitcnt lgkmcnt(1)
mav 16,160,40
R 52,46080
v_add_u32 v85, 1, v85
mav 20,164,40
mav 24,168,40
D 83,3072
s_mov_b32 m0, 142336
mav 28,172,40
buffer_load_dword v84, s[36:39], 0 offen sc1 lds
mav 16,176,44
R 56,47104
mav 20,180,44
mav 24,184,44
buffer_load_dwordx4 v[124:127], v90, s[36:39], s49 offen sc1
mav 28,188,44
s_waitcnt lgkmcnt(1)
mav 16,192,48
R 60,48128
mav 20,196,48
mav 24,200,48
buffer_load_dwordx4 v[128:131], v90, s[36:39], s49 offen offset:1024 sc1
mav 28,204,48
mav 16,208,52
mav 20,212,52
mav 24,216,52
mav 28,220,52
s_waitcnt lgkmcnt(0)
mav 16,224,56
mav 20,228,56
mav 24,232,56
mav 28,236,56
mav 16,240,60
s_waitcnt vmcnt(11)
buffer_store_dwordx2 v[72:73], v81, s[36:39], s52 offen sc1
mav 20,244,60
ds_read_b32 v76, v82 offset:0
mav 24,248,60
ds_write_b32 v97, v74 offset:0
mav 28,252,60
mvv 16,116,164
mvv 20,120,164
mvv 24,116,168
s_xor_b32 s50, s50, 0x10000
mvv 28,120,168
mvv 16,116,172
mvv 20,120,172
v_xor_b32 v80, 0x10000, v80
mvv 24,116,176
s_waitcnt lgkmcnt(0)
v_cmp_gt_i32 vcc, v85, v76
mvv 28,120,176
s_cbranch_vccnz CR0_8
BR0_8:
s_barrier
mvv 16,116,180
R 32,0
s_add_u32 s49, s49, 346112
mvv 20,120,180
buffer_load_dwordx4 v[132:135], v86, s[36:39], s49 offen sc1
mvv 24,116,184
mvv 28,120,184
s_waitcnt vmcnt(12)
mvv 16,116,188
R 36,1024
mvv 20,120,188
buffer_load_dwordx4 v[136:139], v86, s[36:39], s49 offen offset:1024 sc1
s_waitcnt vmcnt(12)
mvv 24,116,192
mvv 28,120,192
s_waitcnt vmcnt(11)
mvv 16,116,196
R 40,2048
mvv 20,120,196
s_waitcnt vmcnt(10)
mvv 24,116,200
buffer_load_dwordx4 v[140:143], v87, s[36:39], s49 offen sc1
mvv 28,120,200
s_add_u32 s60, s60, 1
s_cbranch_scc0 LR0_6
s_waitcnt lgkmcnt(1)
v_mfma_f32_16x16x32_bf16 v[0:3], a[0:3], v[32:35], v[100:103]
R 44,3072
v_cvt_pk_bf16_f32 v64, v16, v17
v_mfma_f32_16x16x32_bf16 v[4:7], a[4:7], v[32:35], v[104:107]
v_cvt_pk_bf16_f32 v65, v18, v19
v_cvt_pk_bf16_f32 v66, v20, v21
v_mfma_f32_16x16x32_bf16 v[8:11], a[8:11], v[32:35], v[108:111]
buffer_load_dwordx4 v[144:147], v87, s[36:39], s49 offen offset:1024 sc1
v_cvt_pk_bf16_f32 v67, v22, v23
v_mfma_f32_16x16x32_bf16 v[12:15], a[12:15], v[32:35], v[112:115]
v_cvt_pk_bf16_f32 v68, v24, v25
v_cvt_pk_bf16_f32 v69, v26, v27
mav 0,16,36
R 48,4096
v_cvt_pk_bf16_f32 v70, v28, v29
mav 4,20,36
v_cvt_pk_bf16_f32 v71, v30, v31
v_mul_f32 v75, v16, v16
mav 8,24,36
buffer_store_dwordx4 v[64:67], v91, s[36:39], s49 offen sc1
v_add_f32 v74, v17, v16
mav 12,28,36
v_fmac_f32 v75, v17, v17
s_waitcnt lgkmcnt(1)
mav 0,32,40
R 52,5120
v_add_f32 v74, v18, v74
mav 4,36,40
v_fmac_f32 v75, v18, v18
v_add_f32 v74, v19, v74
mav 8,40,40
buffer_store_dwordx4 v[68:71], v91, s[36:39], s49 offen offset:1024 sc1
v_fmac_f32 v75, v19, v19
mav 12,44,40
v_add_f32 v74, v20, v74
v_fmac_f32 v75, v20, v20
mav 0,48,44
R 56,6144
v_add_f32 v74, v21, v74
mav 4,52,44
v_fmac_f32 v75, v21, v21
v_add_f32 v74, v22, v74
mav 8,56,44
buffer_load_dwordx4 v[148:151], v88, s[36:39], s49 offen sc1
v_fmac_f32 v75, v22, v22
mav 12,60,44
v_add_f32 v74, v23, v74
s_waitcnt lgkmcnt(1)
mav 0,64,48
R 60,7168
v_fmac_f32 v75, v23, v23
mav 4,68,48
v_add_f32 v74, v24, v74
v_fmac_f32 v75, v24, v24
mav 8,72,48
buffer_load_dwordx4 v[152:155], v88, s[36:39], s49 offen offset:1024 sc1
v_add_f32 v74, v25, v74
mav 12,76,48
v_fmac_f32 v75, v25, v25
v_add_f32 v74, v26, v74
mav 0,80,52
R 32,8192
v_fmac_f32 v75, v26, v26
mav 4,84,52
v_add_f32 v74, v27, v74
v_fmac_f32 v75, v27, v27
mav 8,88,52
buffer_load_dwordx4 v[156:159], v89, s[36:39], s49 offen sc1
v_add_f32 v74, v28, v74
mav 12,92,52
s_add_u32 m0, s50, 0
s_waitcnt lgkmcnt(1)
mav 0,96,56
R 36,9216
v_fmac_f32 v75, v28, v28
mav 4,100,56
v_add_f32 v74, v29, v74
v_fmac_f32 v75, v29, v29
mav 8,104,56
buffer_load_dwordx4 v[160:163], v89, s[36:39], s49 offen offset:1024 sc1
v_add_f32 v74, v30, v74
mav 12,108,56
v_fmac_f32 v75, v30, v30
v_add_f32 v74, v31, v74
LsR3:
s_cbranch_execz LsR7
mav 0,112,60
R 40,10240
v_fmac_f32 v75, v31, v31
mav 4,116,60
mav 8,120,60
D 83,0
v_permlane32_swap_b32 v74, v75
mav 12,124,60
v_add_f32 v74, v74, v75
s_waitcnt lgkmcnt(1)
mav 0,128,32
R 44,11264
mav 4,132,32
mav 8,136,32
D 83,1024
mav 12,140,32
mav 0,144,36
R 48,12288
v_add_u32 v72, 69, v72
mav 4,148,36
mav 8,152,36
D 83,2048
mav 12,156,36
s_waitcnt lgkmcnt(1)
mav 0,160,40
R 52,13312
v_add_u32 v85, 1, v85
mav 4,164,40
mav 8,168,40
D 83,3072
s_mov_b32 m0, 142336
mav 12,172,40
buffer_load_dword v84, s[36:39], 0 offen sc1 lds
mav 0,176,44
R 56,14336
mav 4,180,44
mav 8,184,44
buffer_load_dwordx4 v[164:167], v90, s[36:39], s49 offen sc1
mav 12,188,44
s_waitcnt lgkmcnt(1)
mav 0,192,48
R 60,15360
mav 4,196,48
mav 8,200,48
buffer_load_dwordx4 v[168:171], v90, s[36:39], s49 offen offset:1024 sc1
mav 12,204,48
mav 0,208,52
mav 4,212,52
mav 8,216,52
mav 12,220,52
s_waitcnt lgkmcnt(0)
mav 0,224,56
mav 4,228,56
mav 8,232,56
mav 12,236,56
mav 0,240,60
s_waitcnt vmcnt(11)
buffer_store_dwordx2 v[72:73], v81, s[36:39], s52 offen sc1
mav 4,244,60
ds_read_b32 v76, v82 offset:0
mav 8,248,60
ds_write_b32 v97, v74 offset:1024
mav 12,252,60
mvv 0,116,124
mvv 4,120,124
mvv 8,116,128
mvv 12,120,128
mvv 0,116,132
mvv 4,120,132
s_waitcnt lgkmcnt(0)
v_cmp_gt_i32 vcc, v85, v76
mvv 8,116,136
s_cbranch_vccnz CR0_9
BR0_9:
s_add_u32 s49, s49, 346112
mvv 12,120,136
s_barrier
mvv 0,116,140
R 32,32768
mvv 4,120,140
buffer_load_dwordx4 v[172:175], v86, s[36:39], s49 offen sc1
mvv 8,116,144
mvv 12,120,144
s_waitcnt vmcnt(12)
mvv 0,116,148
R 36,33792
mvv 4,120,148
buffer_load_dwordx4 v[176:179], v86, s[36:39], s49 offen offset:1024 sc1
s_waitcnt vmcnt(12)
mvv 8,116,152
mvv 12,120,152
s_waitcnt vmcnt(11)
mvv 0,116,156
R 40,34816
mvv 4,120,156
s_waitcnt vmcnt(10)
mvv 8,116,160
buffer_load_dwordx4 v[180:183], v87, s[36:39], s49 offen sc1
mvv 12,120,160
s_waitcnt lgkmcnt(1)
v_mfma_f32_16x16x32_bf16 v[16:19], a[0:3], v[32:35], v[100:103]
R 44,35840
v_cvt_pk_bf16_f32 v64, v0, v1
v_mfma_f32_16x16x32_bf16 v[20:23], a[4:7], v[32:35], v[104:107]
v_cvt_pk_bf16_f32 v65, v2, v3
v_cvt_pk_bf16_f32 v66, v4, v5
v_mfma_f32_16x16x32_bf16 v[24:27], a[8:11], v[32:35], v[108:111]
buffer_load_dwordx4 v[184:187], v87, s[36:39], s49 offen offset:1024 sc1
v_cvt_pk_bf16_f32 v67, v6, v7
v_mfma_f32_16x16x32_bf16 v[28:31], a[12:15], v[32:35], v[112:115]
ds_write_b128 v94, v[64:67] offset:0
mav 16,16,36
R 48,36864
v_cvt_pk_bf16_f32 v68, v8, v9
mav 20,20,36
v_cvt_pk_bf16_f32 v69, v10, v11
v_cvt_pk_bf16_f32 v70, v12, v13
mav 24,24,36
v_cvt_pk_bf16_f32 v71, v14, v15
mav 28,28,36
buffer_load_dwordx4 v[188:191], v88, s[36:39], s49 offen sc1
s_waitcnt lgkmcnt(2)
mav 16,32,40
R 52,37888
mav 20,36,40
ds_write_b128 v95, v[68:71] offset:0
mav 24,40,40
mav 28,44,40
buffer_load_dwordx4 v[192:195], v88, s[36:39], s49 offen offset:1024 sc1
mav 16,48,44
R 56,38912
mav 20,52,44
s_waitcnt lgkmcnt(1)
ds_read_b128 v[64:67], v96 offset:0
mav 24,56,44
ds_read_b128 v[68:71], v96 offset:1024
mav 28,60,44
mav 16,64,48
R 60,39936
buffer_load_dwordx4 v[196:199], v89, s[36:39], s49 offen sc1
mav 20,68,48
mav 24,72,48
mav 28,76,48
mav 16,80,52
R 32,40960
buffer_load_dwordx4 v[200:203], v89, s[36:39], s49 offen offset:1024 sc1
mav 20,84,52
mav 24,88,52
mav 28,92,52
s_add_u32 m0, s50, 32768
s_waitcnt lgkmcnt(1)
mav 16,96,56
R 36,41984
buffer_store_dwordx4 v[64:67], v92, s[44:47], s54 offen
mav 20,100,56
mav 24,104,56
mav 28,108,56
mav 16,112,60
R 40,43008
buffer_store_dwordx4 v[68:71], v93, s[44:47], s54 offen
mav 20,116,60
mav 24,120,60
mav 28,124,60
s_waitcnt lgkmcnt(1)
mav 16,128,32
R 44,44032
D 83,0
mav 20,132,32
mav 24,136,32
mav 28,140,32
mav 16,144,36
R 48,45056
D 83,1024
mav 20,148,36
mav 24,152,36
mav 28,156,36
s_waitcnt lgkmcnt(1)
mav 16,160,40
R 52,46080
D 83,2048
mav 20,164,40
s_sub_u32 s54, s54, 0x8000
v_add_u32 v85, 1, v85
mav 24,168,40
mav 28,172,40
mav 16,176,44
R 56,47104
D 83,3072
mav 20,180,44
s_mov_b32 m0, 142336
mav 24,184,44
buffer_load_dword v84, s[36:39], 0 offen sc1 lds
mav 28,188,44
s_waitcnt lgkmcnt(1)
mav 16,192,48
R 60,48128
buffer_load_dwordx4 v[124:127], v90, s[36:39], s49 offen sc1
mav 20,196,48
mav 24,200,48
mav 28,204,48
mav 16,208,52
buffer_load_dwordx4 v[128:131], v90, s[36:39], s49 offen offset:1024 sc1
mav 20,212,52
mav 24,216,52
mav 28,220,52
s_waitcnt lgkmcnt(0)
mav 16,224,56
mav 20,228,56
mav 24,232,56
mav 28,236,56
mav 16,240,60
mav 20,244,60
ds_read_b32 v76, v82 offset:0
mav 24,248,60
mav 28,252,60
s_waitcnt vmcnt(19)
mvv 16,116,164
mvv 20,120,164
s_waitcnt vmcnt(18)
mvv 24,116,168
s_xor_b32 s50, s50, 0x10000
mvv 28,120,168
s_waitcnt vmcnt(16)
mvv 16,116,172
mvv 20,120,172
v_xor_b32 v80, 0x10000, v80
s_waitcnt vmcnt(15)
mvv 24,116,176
s_waitcnt lgkmcnt(0)
v_cmp_gt_i32 vcc, v85, v76
mvv 28,120,176
s_cbranch_vccnz CR0_10
BR0_10:
s_waitcnt vmcnt(14)
mvv 16,116,180
s_barrier
R 32,0
mvv 20,120,180
s_add_u32 s49, s49, 346112
s_waitcnt vmcnt(13)
mvv 24,116,184
buffer_load_dwordx4 v[132:135], v86, s[36:39], s49 offen sc1
mvv 28,120,184
s_waitcnt vmcnt(13)
mvv 16,116,188
R 36,1024
mvv 20,120,188
s_waitcnt vmcnt(12)
mvv 24,116,192
buffer_load_dwordx4 v[136:139], v86, s[36:39], s49 offen offset:1024 sc1
mvv 28,120,192
s_waitcnt vmcnt(12)
mvv 16,116,196
R 40,2048
mvv 20,120,196
s_waitcnt vmcnt(11)
mvv 24,116,200
buffer_load_dwordx4 v[140:143], v87, s[36:39], s49 offen sc1
mvv 28,120,200
s_mov_b32 s60, -13
.p2align 6
LR0_11:
s_waitcnt lgkmcnt(1)
v_mfma_f32_16x16x32_bf16 v[0:3], a[0:3], v[32:35], v[100:103]
R 44,3072
v_cvt_pk_bf16_f32 v64, v16, v17
v_mfma_f32_16x16x32_bf16 v[4:7], a[4:7], v[32:35], v[104:107]
v_cvt_pk_bf16_f32 v65, v18, v19
v_cvt_pk_bf16_f32 v66, v20, v21
v_mfma_f32_16x16x32_bf16 v[8:11], a[8:11], v[32:35], v[108:111]
buffer_load_dwordx4 v[144:147], v87, s[36:39], s49 offen offset:1024 sc1
v_cvt_pk_bf16_f32 v67, v22, v23
v_mfma_f32_16x16x32_bf16 v[12:15], a[12:15], v[32:35], v[112:115]
ds_write_b128 v94, v[64:67] offset:0
mav 0,16,36
R 48,4096
v_cvt_pk_bf16_f32 v68, v24, v25
mav 4,20,36
v_cvt_pk_bf16_f32 v69, v26, v27
v_cvt_pk_bf16_f32 v70, v28, v29
mav 8,24,36
v_cvt_pk_bf16_f32 v71, v30, v31
mav 12,28,36
buffer_load_dwordx4 v[148:151], v88, s[36:39], s49 offen sc1
s_waitcnt lgkmcnt(2)
mav 0,32,40
R 52,5120
mav 4,36,40
ds_write_b128 v95, v[68:71] offset:0
mav 8,40,40
mav 12,44,40
buffer_load_dwordx4 v[152:155], v88, s[36:39], s49 offen offset:1024 sc1
mav 0,48,44
R 56,6144
mav 4,52,44
s_waitcnt lgkmcnt(1)
ds_read_b128 v[64:67], v96 offset:0
mav 8,56,44
ds_read_b128 v[68:71], v96 offset:1024
mav 12,60,44
mav 0,64,48
R 60,7168
buffer_load_dwordx4 v[156:159], v89, s[36:39], s49 offen sc1
mav 4,68,48
mav 8,72,48
mav 12,76,48
mav 0,80,52
R 32,8192
buffer_load_dwordx4 v[160:163], v89, s[36:39], s49 offen offset:1024 sc1
mav 4,84,52
mav 8,88,52
mav 12,92,52
s_add_u32 m0, s50, 0
s_waitcnt lgkmcnt(1)
mav 0,96,56
R 36,9216
buffer_store_dwordx4 v[64:67], v92, s[44:47], s54 offen
mav 4,100,56
mav 8,104,56
mav 12,108,56
mav 0,112,60
R 40,10240
buffer_store_dwordx4 v[68:71], v93, s[44:47], s54 offen
mav 4,116,60
mav 8,120,60
mav 12,124,60
s_waitcnt lgkmcnt(1)
mav 0,128,32
R 44,11264
D 83,0
mav 4,132,32
mav 8,136,32
mav 12,140,32
mav 0,144,36
R 48,12288
D 83,1024
mav 4,148,36
mav 8,152,36
mav 12,156,36
s_waitcnt lgkmcnt(1)
mav 0,160,40
R 52,13312
D 83,2048
mav 4,164,40
s_sub_u32 s54, s54, 0x8000
v_add_u32 v85, 1, v85
mav 8,168,40
mav 12,172,40
mav 0,176,44
R 56,14336
D 83,3072
mav 4,180,44
s_mov_b32 m0, 142336
mav 8,184,44
buffer_load_dword v84, s[36:39], 0 offen sc1 lds
mav 12,188,44
s_waitcnt lgkmcnt(1)
mav 0,192,48
R 60,15360
buffer_load_dwordx4 v[164:167], v90, s[36:39], s49 offen sc1
mav 4,196,48
mav 8,200,48
mav 12,204,48
mav 0,208,52
buffer_load_dwordx4 v[168:171], v90, s[36:39], s49 offen offset:1024 sc1
mav 4,212,52
mav 8,216,52
mav 12,220,52
s_waitcnt lgkmcnt(0)
mav 0,224,56
mav 4,228,56
mav 8,232,56
mav 12,236,56
mav 0,240,60
mav 4,244,60
ds_read_b32 v76, v82 offset:0
mav 8,248,60
mav 12,252,60
s_waitcnt vmcnt(18)
mvv 0,116,124
mvv 4,120,124
s_waitcnt vmcnt(17)
mvv 8,116,128
mvv 12,120,128
s_waitcnt vmcnt(16)
mvv 0,116,132
mvv 4,120,132
s_waitcnt lgkmcnt(0)
v_cmp_gt_i32 vcc, v85, v76
s_waitcnt vmcnt(15)
mvv 8,116,136
s_cbranch_vccnz CR0_12
BR0_12:
s_add_u32 s49, s49, 346112
mvv 12,120,136
s_barrier
s_waitcnt vmcnt(14)
mvv 0,116,140
R 32,32768
mvv 4,120,140
buffer_load_dwordx4 v[172:175], v86, s[36:39], s49 offen sc1
s_waitcnt vmcnt(14)
mvv 8,116,144
mvv 12,120,144
s_waitcnt vmcnt(13)
mvv 0,116,148
R 36,33792
mvv 4,120,148
buffer_load_dwordx4 v[176:179], v86, s[36:39], s49 offen offset:1024 sc1
s_waitcnt vmcnt(13)
mvv 8,116,152
mvv 12,120,152
s_waitcnt vmcnt(12)
mvv 0,116,156
R 40,34816
mvv 4,120,156
s_waitcnt vmcnt(11)
mvv 8,116,160
buffer_load_dwordx4 v[180:183], v87, s[36:39], s49 offen sc1
mvv 12,120,160
s_waitcnt lgkmcnt(1)
v_mfma_f32_16x16x32_bf16 v[16:19], a[0:3], v[32:35], v[100:103]
R 44,35840
v_cvt_pk_bf16_f32 v64, v0, v1
v_mfma_f32_16x16x32_bf16 v[20:23], a[4:7], v[32:35], v[104:107]
v_cvt_pk_bf16_f32 v65, v2, v3
v_cvt_pk_bf16_f32 v66, v4, v5
v_mfma_f32_16x16x32_bf16 v[24:27], a[8:11], v[32:35], v[108:111]
buffer_load_dwordx4 v[184:187], v87, s[36:39], s49 offen offset:1024 sc1
v_cvt_pk_bf16_f32 v67, v6, v7
v_mfma_f32_16x16x32_bf16 v[28:31], a[12:15], v[32:35], v[112:115]
ds_write_b128 v94, v[64:67] offset:0
mav 16,16,36
R 48,36864
v_cvt_pk_bf16_f32 v68, v8, v9
)"
R"(
mav 20,20,36
v_cvt_pk_bf16_f32 v69, v10, v11
v_cvt_pk_bf16_f32 v70, v12, v13
mav 24,24,36
v_cvt_pk_bf16_f32 v71, v14, v15
mav 28,28,36
buffer_load_dwordx4 v[188:191], v88, s[36:39], s49 offen sc1
s_waitcnt lgkmcnt(2)
mav 16,32,40
R 52,37888
mav 20,36,40
ds_write_b128 v95, v[68:71] offset:0
mav 24,40,40
mav 28,44,40
buffer_load_dwordx4 v[192:195], v88, s[36:39], s49 offen offset:1024 sc1
mav 16,48,44
R 56,38912
mav 20,52,44
s_waitcnt lgkmcnt(1)
ds_read_b128 v[64:67], v96 offset:0
mav 24,56,44
ds_read_b128 v[68:71], v96 offset:1024
mav 28,60,44
mav 16,64,48
R 60,39936
buffer_load_dwordx4 v[196:199], v89, s[36:39], s49 offen sc1
mav 20,68,48
mav 24,72,48
mav 28,76,48
mav 16,80,52
R 32,40960
buffer_load_dwordx4 v[200:203], v89, s[36:39], s49 offen offset:1024 sc1
mav 20,84,52
mav 24,88,52
mav 28,92,52
s_add_u32 m0, s50, 32768
s_waitcnt lgkmcnt(1)
mav 16,96,56
R 36,41984
buffer_store_dwordx4 v[64:67], v92, s[44:47], s54 offen
mav 20,100,56
mav 24,104,56
mav 28,108,56
mav 16,112,60
R 40,43008
buffer_store_dwordx4 v[68:71], v93, s[44:47], s54 offen
mav 20,116,60
mav 24,120,60
mav 28,124,60
s_waitcnt lgkmcnt(1)
mav 16,128,32
R 44,44032
D 83,0
mav 20,132,32
mav 24,136,32
mav 28,140,32
mav 16,144,36
R 48,45056
D 83,1024
mav 20,148,36
mav 24,152,36
mav 28,156,36
s_waitcnt lgkmcnt(1)
mav 16,160,40
R 52,46080
D 83,2048
mav 20,164,40
s_sub_u32 s54, s54, 0x8000
v_add_u32 v85, 1, v85
mav 24,168,40
mav 28,172,40
mav 16,176,44
R 56,47104
D 83,3072
mav 20,180,44
s_mov_b32 m0, 142336
mav 24,184,44
buffer_load_dword v84, s[36:39], 0 offen sc1 lds
mav 28,188,44
s_waitcnt lgkmcnt(1)
mav 16,192,48
R 60,48128
buffer_load_dwordx4 v[124:127], v90, s[36:39], s49 offen sc1
mav 20,196,48
mav 24,200,48
mav 28,204,48
mav 16,208,52
buffer_load_dwordx4 v[128:131], v90, s[36:39], s49 offen offset:1024 sc1
mav 20,212,52
mav 24,216,52
mav 28,220,52
s_waitcnt lgkmcnt(0)
mav 16,224,56
mav 20,228,56
mav 24,232,56
mav 28,236,56
mav 16,240,60
mav 20,244,60
ds_read_b32 v76, v82 offset:0
mav 24,248,60
mav 28,252,60
s_waitcnt vmcnt(18)
mvv 16,116,164
mvv 20,120,164
s_waitcnt vmcnt(17)
mvv 24,116,168
s_xor_b32 s50, s50, 0x10000
mvv 28,120,168
s_waitcnt vmcnt(16)
mvv 16,116,172
mvv 20,120,172
v_xor_b32 v80, 0x10000, v80
s_waitcnt vmcnt(15)
mvv 24,116,176
s_waitcnt lgkmcnt(0)
v_cmp_gt_i32 vcc, v85, v76
mvv 28,120,176
s_cbranch_vccnz CR0_13
BR0_13:
s_waitcnt vmcnt(14)
mvv 16,116,180
s_barrier
LsR4:
s_cbranch_execz LsR8
R 32,0
mvv 20,120,180
s_add_u32 s49, s49, 346112
s_waitcnt vmcnt(13)
mvv 24,116,184
buffer_load_dwordx4 v[132:135], v86, s[36:39], s49 offen sc1
mvv 28,120,184
s_waitcnt vmcnt(13)
mvv 16,116,188
R 36,1024
mvv 20,120,188
s_waitcnt vmcnt(12)
mvv 24,116,192
buffer_load_dwordx4 v[136:139], v86, s[36:39], s49 offen offset:1024 sc1
mvv 28,120,192
s_waitcnt vmcnt(12)
mvv 16,116,196
R 40,2048
mvv 20,120,196
s_waitcnt vmcnt(11)
mvv 24,116,200
buffer_load_dwordx4 v[140:143], v87, s[36:39], s49 offen sc1
mvv 28,120,200
s_add_u32 s60, s60, 1
s_cbranch_scc0 LR0_11
s_waitcnt lgkmcnt(1)
v_mfma_f32_16x16x32_bf16 v[0:3], a[0:3], v[32:35], v[100:103]
R 44,3072
v_cvt_pk_bf16_f32 v64, v16, v17
v_mfma_f32_16x16x32_bf16 v[4:7], a[4:7], v[32:35], v[104:107]
v_cvt_pk_bf16_f32 v65, v18, v19
v_cvt_pk_bf16_f32 v66, v20, v21
v_mfma_f32_16x16x32_bf16 v[8:11], a[8:11], v[32:35], v[108:111]
buffer_load_dwordx4 v[144:147], v87, s[36:39], s49 offen offset:1024 sc1
v_cvt_pk_bf16_f32 v67, v22, v23
v_mfma_f32_16x16x32_bf16 v[12:15], a[12:15], v[32:35], v[112:115]
ds_write_b128 v94, v[64:67] offset:0
mav 0,16,36
R 48,4096
v_cvt_pk_bf16_f32 v68, v24, v25
mav 4,20,36
v_cvt_pk_bf16_f32 v69, v26, v27
v_cvt_pk_bf16_f32 v70, v28, v29
mav 8,24,36
v_cvt_pk_bf16_f32 v71, v30, v31
mav 12,28,36
buffer_load_dwordx4 v[148:151], v88, s[36:39], s49 offen sc1
s_waitcnt lgkmcnt(2)
mav 0,32,40
R 52,5120
mav 4,36,40
ds_write_b128 v95, v[68:71] offset:0
mav 8,40,40
mav 12,44,40
buffer_load_dwordx4 v[152:155], v88, s[36:39], s49 offen offset:1024 sc1
mav 0,48,44
R 56,6144
mav 4,52,44
s_waitcnt lgkmcnt(1)
ds_read_b128 v[64:67], v96 offset:0
mav 8,56,44
ds_read_b128 v[68:71], v96 offset:1024
mav 12,60,44
mav 0,64,48
R 60,7168
buffer_load_dwordx4 v[156:159], v89, s[36:39], s49 offen sc1
mav 4,68,48
mav 8,72,48
mav 12,76,48
mav 0,80,52
R 32,8192
buffer_load_dwordx4 v[160:163], v89, s[36:39], s49 offen offset:1024 sc1
mav 4,84,52
mav 8,88,52
mav 12,92,52
s_add_u32 m0, s50, 0
s_waitcnt lgkmcnt(1)
mav 0,96,56
R 36,9216
buffer_store_dwordx4 v[64:67], v92, s[44:47], s54 offen
mav 4,100,56
mav 8,104,56
mav 12,108,56
mav 0,112,60
R 40,10240
buffer_store_dwordx4 v[68:71], v93, s[44:47], s54 offen
mav 4,116,60
mav 8,120,60
mav 12,124,60
s_waitcnt lgkmcnt(1)
mav 0,128,32
R 44,11264
D 83,0
mav 4,132,32
mav 8,136,32
mav 12,140,32
mav 0,144,36
R 48,12288
D 83,1024
mav 4,148,36
mav 8,152,36
mav 12,156,36
s_waitcnt lgkmcnt(1)
mav 0,160,40
R 52,13312
D 83,2048
mav 4,164,40
s_sub_u32 s54, s54, 0x8000
v_add_u32 v85, 1, v85
mav 8,168,40
mav 12,172,40
mav 0,176,44
R 56,14336
D 83,3072
mav 4,180,44
s_mov_b32 m0, 142336
mav 8,184,44
buffer_load_dword v84, s[36:39], 0 offen sc1 lds
mav 12,188,44
s_waitcnt lgkmcnt(1)
mav 0,192,48
R 60,15360
buffer_load_dwordx4 v[164:167], v90, s[36:39], s49 offen sc1
mav 4,196,48
mav 8,200,48
mav 12,204,48
mav 0,208,52
buffer_load_dwordx4 v[168:171], v90, s[36:39], s49 offen offset:1024 sc1
mav 4,212,52
mav 8,216,52
mav 12,220,52
s_waitcnt lgkmcnt(0)
mav 0,224,56
mav 4,228,56
mav 8,232,56
mav 12,236,56
mav 0,240,60
mav 4,244,60
ds_read_b32 v76, v82 offset:0
mav 8,248,60
mav 12,252,60
s_waitcnt vmcnt(18)
mvv 0,116,124
mvv 4,120,124
s_waitcnt vmcnt(17)
mvv 8,116,128
mvv 12,120,128
s_waitcnt vmcnt(16)
mvv 0,116,132
mvv 4,120,132
s_waitcnt lgkmcnt(0)
v_cmp_gt_i32 vcc, v85, v76
s_waitcnt vmcnt(15)
mvv 8,116,136
s_cbranch_vccnz CR0_14
BR0_14:
s_add_u32 s49, s49, 346112
mvv 12,120,136
s_barrier
s_waitcnt vmcnt(14)
mvv 0,116,140
R 32,32768
mvv 4,120,140
buffer_load_dwordx4 v[172:175], v86, s[36:39], s49 offen sc1
s_waitcnt vmcnt(14)
mvv 8,116,144
mvv 12,120,144
s_waitcnt vmcnt(13)
mvv 0,116,148
R 36,33792
mvv 4,120,148
buffer_load_dwordx4 v[176:179], v86, s[36:39], s49 offen offset:1024 sc1
s_waitcnt vmcnt(13)
mvv 8,116,152
mvv 12,120,152
s_waitcnt vmcnt(12)
mvv 0,116,156
R 40,34816
mvv 4,120,156
s_waitcnt vmcnt(11)
mvv 8,116,160
buffer_load_dwordx4 v[180:183], v87, s[36:39], s49 offen sc1
mvv 12,120,160
s_waitcnt lgkmcnt(1)
v_mfma_f32_16x16x32_bf16 v[16:19], a[0:3], v[32:35], v[100:103]
R 44,35840
v_cvt_pk_bf16_f32 v64, v0, v1
v_mfma_f32_16x16x32_bf16 v[20:23], a[4:7], v[32:35], v[104:107]
v_cvt_pk_bf16_f32 v65, v2, v3
v_cvt_pk_bf16_f32 v66, v4, v5
v_mfma_f32_16x16x32_bf16 v[24:27], a[8:11], v[32:35], v[108:111]
buffer_load_dwordx4 v[184:187], v87, s[36:39], s49 offen offset:1024 sc1
v_cvt_pk_bf16_f32 v67, v6, v7
v_mfma_f32_16x16x32_bf16 v[28:31], a[12:15], v[32:35], v[112:115]
ds_write_b128 v94, v[64:67] offset:0
mav 16,16,36
R 48,36864
v_cvt_pk_bf16_f32 v68, v8, v9
mav 20,20,36
v_cvt_pk_bf16_f32 v69, v10, v11
v_cvt_pk_bf16_f32 v70, v12, v13
mav 24,24,36
v_cvt_pk_bf16_f32 v71, v14, v15
mav 28,28,36
buffer_load_dwordx4 v[188:191], v88, s[36:39], s49 offen sc1
s_waitcnt lgkmcnt(2)
mav 16,32,40
R 52,37888
mav 20,36,40
ds_write_b128 v95, v[68:71] offset:0
mav 24,40,40
mav 28,44,40
buffer_load_dwordx4 v[192:195], v88, s[36:39], s49 offen offset:1024 sc1
mav 16,48,44
R 56,38912
mav 20,52,44
s_waitcnt lgkmcnt(1)
ds_read_b128 v[64:67], v96 offset:0
mav 24,56,44
ds_read_b128 v[68:71], v96 offset:1024
mav 28,60,44
mav 16,64,48
R 60,39936
buffer_load_dwordx4 v[196:199], v89, s[36:39], s49 offen sc1
mav 20,68,48
mav 24,72,48
mav 28,76,48
mav 16,80,52
R 32,40960
buffer_load_dwordx4 v[200:203], v89, s[36:39], s49 offen offset:1024 sc1
mav 20,84,52
mav 24,88,52
mav 28,92,52
s_add_u32 m0, s50, 32768
s_waitcnt lgkmcnt(1)
mav 16,96,56
R 36,41984
buffer_store_dwordx4 v[64:67], v92, s[44:47], s54 offen
mav 20,100,56
mav 24,104,56
mav 28,108,56
mav 16,112,60
R 40,43008
buffer_store_dwordx4 v[68:71], v93, s[44:47], s54 offen
mav 20,116,60
mav 24,120,60
mav 28,124,60
s_waitcnt lgkmcnt(1)
mav 16,128,32
R 44,44032
D 83,0
mav 20,132,32
mav 24,136,32
mav 28,140,32
mav 16,144,36
R 48,45056
D 83,1024
mav 20,148,36
mav 24,152,36
mav 28,156,36
s_waitcnt lgkmcnt(1)
mav 16,160,40
R 52,46080
D 83,2048
mav 20,164,40
s_sub_u32 s54, s54, 0x8000
v_add_u32 v85, 1, v85
mav 24,168,40
mav 28,172,40
mav 16,176,44
R 56,47104
D 83,3072
mav 20,180,44
s_mov_b32 m0, 142336
mav 24,184,44
buffer_load_dword v84, s[36:39], 0 offen sc1 lds
mav 28,188,44
s_waitcnt lgkmcnt(1)
mav 16,192,48
R 60,48128
buffer_load_dwordx4 v[124:127], v90, s[36:39], s49 offen sc1
mav 20,196,48
mav 24,200,48
mav 28,204,48
mav 16,208,52
buffer_load_dwordx4 v[128:131], v90, s[36:39], s49 offen offset:1024 sc1
mav 20,212,52
mav 24,216,52
mav 28,220,52
s_waitcnt lgkmcnt(0)
mav 16,224,56
mav 20,228,56
mav 24,232,56
mav 28,236,56
mav 16,240,60
mav 20,244,60
ds_read_b32 v76, v82 offset:0
mav 24,248,60
mav 28,252,60
s_waitcnt vmcnt(18)
mvv 16,116,164
mvv 20,120,164
s_waitcnt vmcnt(17)
mvv 24,116,168
mvv 28,120,168
s_waitcnt vmcnt(16)
mvv 16,116,172
mvv 20,120,172
v_xor_b32 v80, 0x10000, v80
s_waitcnt vmcnt(15)
mvv 24,116,176
s_waitcnt lgkmcnt(0)
v_cmp_gt_i32 vcc, v85, v76
mvv 28,120,176
s_cbranch_vccnz CR0_15
BR0_15:
s_waitcnt vmcnt(14)
mvv 16,116,180
s_barrier
R 32,0
mvv 20,120,180
s_add_u32 s49, s49, 346112
s_waitcnt vmcnt(13)
mvv 24,116,184
buffer_load_dwordx4 v[132:135], v86, s[36:39], s49 offen sc1
mvv 28,120,184
s_waitcnt vmcnt(13)
mvv 16,116,188
R 36,1024
mvv 20,120,188
s_waitcnt vmcnt(12)
mvv 24,116,192
buffer_load_dwordx4 v[136:139], v86, s[36:39], s49 offen offset:1024 sc1
mvv 28,120,192
s_waitcnt vmcnt(12)
mvv 16,116,196
R 40,2048
mvv 20,120,196
s_waitcnt vmcnt(11)
mvv 24,116,200
buffer_load_dwordx4 v[140:143], v87, s[36:39], s49 offen sc1
mvv 28,120,200
s_waitcnt lgkmcnt(1)
v_mfma_f32_16x16x32_bf16 v[0:3], a[0:3], v[32:35], v[100:103]
R 44,3072
v_cvt_pk_bf16_f32 v64, v16, v17
v_mfma_f32_16x16x32_bf16 v[4:7], a[4:7], v[32:35], v[104:107]
v_cvt_pk_bf16_f32 v65, v18, v19
v_cvt_pk_bf16_f32 v66, v20, v21
v_mfma_f32_16x16x32_bf16 v[8:11], a[8:11], v[32:35], v[108:111]
buffer_load_dwordx4 v[144:147], v87, s[36:39], s49 offen offset:1024 sc1
v_cvt_pk_bf16_f32 v67, v22, v23
v_mfma_f32_16x16x32_bf16 v[12:15], a[12:15], v[32:35], v[112:115]
ds_write_b128 v94, v[64:67] offset:0
mav 0,16,36
R 48,4096
v_cvt_pk_bf16_f32 v68, v24, v25
mav 4,20,36
v_cvt_pk_bf16_f32 v69, v26, v27
v_cvt_pk_bf16_f32 v70, v28, v29
mav 8,24,36
v_cvt_pk_bf16_f32 v71, v30, v31
mav 12,28,36
buffer_load_dwordx4 v[148:151], v88, s[36:39], s49 offen sc1
s_waitcnt lgkmcnt(2)
mav 0,32,40
R 52,5120
mav 4,36,40
ds_write_b128 v95, v[68:71] offset:0
mav 8,40,40
mav 12,44,40
buffer_load_dwordx4 v[152:155], v88, s[36:39], s49 offen offset:1024 sc1
mav 0,48,44
R 56,6144
mav 4,52,44
s_waitcnt lgkmcnt(1)
ds_read_b128 v[64:67], v96 offset:0
mav 8,56,44
ds_read_b128 v[68:71], v96 offset:1024
mav 12,60,44
mav 0,64,48
R 60,7168
buffer_load_dwordx4 v[156:159], v89, s[36:39], s49 offen sc1
mav 4,68,48
mav 8,72,48
mav 12,76,48
mav 0,80,52
R 32,8192
buffer_load_dwordx4 v[160:163], v89, s[36:39], s49 offen offset:1024 sc1
mav 4,84,52
mav 8,88,52
mav 12,92,52
s_waitcnt lgkmcnt(1)
mav 0,96,56
R 36,9216
buffer_store_dwordx4 v[64:67], v92, s[44:47], s54 offen
mav 4,100,56
mav 8,104,56
mav 12,108,56
mav 0,112,60
R 40,10240
buffer_store_dwordx4 v[68:71], v93, s[44:47], s54 offen
mav 4,116,60
s_mov_b32 m0, 142336
mav 8,120,60
buffer_load_dword v84, s[36:39], 0 offen sc1 lds
mav 12,124,60
s_waitcnt lgkmcnt(1)
mav 0,128,32
R 44,11264
mav 4,132,32
mav 8,136,32
mav 12,140,32
mav 0,144,36
R 48,12288
mav 4,148,36
mav 8,152,36
mav 12,156,36
s_waitcnt lgkmcnt(1)
mav 0,160,40
R 52,13312
s_sub_u32 s54, s54, 0x8000
mav 4,164,40
v_add_u32 v85, 1, v85
mav 8,168,40
mav 12,172,40
buffer_load_dwordx4 v[164:167], v90, s[36:39], s49 offen sc1
mav 0,176,44
R 56,14336
mav 4,180,44
mav 8,184,44
mav 12,188,44
buffer_load_dwordx4 v[168:171], v90, s[36:39], s49 offen offset:1024 sc1
s_waitcnt lgkmcnt(1)
mav 0,192,48
R 60,15360
mav 4,196,48
mav 8,200,48
mav 12,204,48
mav 0,208,52
mav 4,212,52
mav 8,216,52
mav 12,220,52
s_waitcnt lgkmcnt(0)
mav 0,224,56
mav 4,228,56
mav 8,232,56
mav 12,236,56
mav 0,240,60
mav 4,244,60
ds_read_b32 v76, v82 offset:0
mav 8,248,60
mav 12,252,60
s_waitcnt vmcnt(14)
mvv 0,116,124
mvv 4,120,124
s_waitcnt vmcnt(13)
mvv 8,116,128
mvv 12,120,128
s_waitcnt vmcnt(12)
mvv 0,116,132
mvv 4,120,132
s_waitcnt lgkmcnt(0)
v_cmp_gt_i32 vcc, v85, v76
s_waitcnt vmcnt(11)
mvv 8,116,136
s_cbranch_vccnz CR0_16
BR0_16:
s_add_u32 s49, s49, 346112
mvv 12,120,136
s_barrier
s_waitcnt vmcnt(10)
mvv 0,116,140
R 32,32768
mvv 4,120,140
buffer_load_dwordx4 v[172:175], v86, s[36:39], s49 offen sc1
s_waitcnt vmcnt(10)
mvv 8,116,144
mvv 12,120,144
s_waitcnt vmcnt(9)
mvv 0,116,148
R 36,33792
mvv 4,120,148
buffer_load_dwordx4 v[176:179], v86, s[36:39], s49 offen offset:1024 sc1
s_waitcnt vmcnt(9)
mvv 8,116,152
mvv 12,120,152
s_waitcnt vmcnt(8)
mvv 0,116,156
R 40,34816
mvv 4,120,156
s_waitcnt vmcnt(7)
mvv 8,116,160
buffer_load_dwordx4 v[180:183], v87, s[36:39], s49 offen sc1
mvv 12,120,160
s_waitcnt lgkmcnt(1)
v_mfma_f32_16x16x32_bf16 v[16:19], a[0:3], v[32:35], v[100:103]
R 44,35840
v_cvt_pk_bf16_f32 v64, v0, v1
v_mfma_f32_16x16x32_bf16 v[20:23], a[4:7], v[32:35], v[104:107]
v_cvt_pk_bf16_f32 v65, v2, v3
v_cvt_pk_bf16_f32 v66, v4, v5
v_mfma_f32_16x16x32_bf16 v[24:27], a[8:11], v[32:35], v[108:111]
buffer_load_dwordx4 v[184:187], v87, s[36:39], s49 offen offset:1024 sc1
v_cvt_pk_bf16_f32 v67, v6, v7
v_mfma_f32_16x16x32_bf16 v[28:31], a[12:15], v[32:35], v[112:115]
ds_write_b128 v94, v[64:67] offset:0
mav 16,16,36
R 48,36864
v_cvt_pk_bf16_f32 v68, v8, v9
mav 20,20,36
v_cvt_pk_bf16_f32 v69, v10, v11
v_cvt_pk_bf16_f32 v70, v12, v13
mav 24,24,36
v_cvt_pk_bf16_f32 v71, v14, v15
mav 28,28,36
buffer_load_dwordx4 v[188:191], v88, s[36:39], s49 offen sc1
s_waitcnt lgkmcnt(2)
mav 16,32,40
R 52,37888
mav 20,36,40
ds_write_b128 v95, v[68:71] offset:0
mav 24,40,40
mav 28,44,40
buffer_load_dwordx4 v[192:195], v88, s[36:39], s49 offen offset:1024 sc1
mav 16,48,44
R 56,38912
mav 20,52,44
s_waitcnt lgkmcnt(1)
ds_read_b128 v[64:67], v96 offset:0
mav 24,56,44
ds_read_b128 v[68:71], v96 offset:1024
mav 28,60,44
mav 16,64,48
R 60,39936
buffer_load_dwordx4 v[196:199], v89, s[36:39], s49 offen sc1
mav 20,68,48
mav 24,72,48
mav 28,76,48
mav 16,80,52
R 32,40960
buffer_load_dwordx4 v[200:203], v89, s[36:39], s49 offen offset:1024 sc1
mav 20,84,52
mav 24,88,52
mav 28,92,52
s_waitcnt lgkmcnt(1)
mav 16,96,56
R 36,41984
buffer_store_dwordx4 v[64:67], v92, s[44:47], s54 offen
mav 20,100,56
mav 24,104,56
mav 28,108,56
mav 16,112,60
R 40,43008
buffer_store_dwordx4 v[68:71], v93, s[44:47], s54 offen
LsR5:
s_cbranch_execz LsR9
mav 20,116,60
mav 24,120,60
mav 28,124,60
s_waitcnt lgkmcnt(1)
mav 16,128,32
R 44,44032
mav 20,132,32
mav 24,136,32
mav 28,140,32
mav 16,144,36
R 48,45056
mav 20,148,36
mav 24,152,36
mav 28,156,36
s_waitcnt lgkmcnt(1)
mav 16,160,40
R 52,46080
s_sub_u32 s54, s54, 0x8000
mav 20,164,40
mav 24,168,40
mav 28,172,40
mav 16,176,44
R 56,47104
mav 20,180,44
mav 24,184,44
mav 28,188,44
s_waitcnt lgkmcnt(1)
mav 16,192,48
R 60,48128
mav 20,196,48
mav 24,200,48
mav 28,204,48
mav 16,208,52
mav 20,212,52
mav 24,216,52
mav 28,220,52
s_waitcnt lgkmcnt(0)
mav 16,224,56
mav 20,228,56
mav 24,232,56
mav 28,236,56
mav 16,240,60
mav 20,244,60
mav 24,248,60
mav 28,252,60
s_waitcnt vmcnt(11)
mvv 16,116,164
mvv 20,120,164
s_waitcnt vmcnt(10)
mvv 24,116,168
mvv 28,120,168
s_waitcnt vmcnt(9)
mvv 16,116,172
mvv 20,120,172
s_waitcnt vmcnt(8)
mvv 24,116,176
mvv 28,120,176
s_waitcnt vmcnt(7)
mvv 16,116,180
mvv 20,120,180
s_waitcnt vmcnt(6)
mvv 24,116,184
mvv 28,120,184
s_waitcnt vmcnt(5)
mvv 16,116,188
mvv 20,120,188
s_waitcnt vmcnt(4)
mvv 24,116,192
mvv 28,120,192
s_waitcnt vmcnt(3)
mvv 16,116,196
mvv 20,120,196
s_waitcnt vmcnt(2)
mvv 24,116,200
mvv 28,120,200
s_nop 3
v_cvt_pk_bf16_f32 v64, v16, v17
v_cvt_pk_bf16_f32 v65, v18, v19
v_cvt_pk_bf16_f32 v66, v20, v21
v_cvt_pk_bf16_f32 v67, v22, v23
ds_write_b128 v94, v[64:67] offset:0
v_cvt_pk_bf16_f32 v68, v24, v25
v_cvt_pk_bf16_f32 v69, v26, v27
v_cvt_pk_bf16_f32 v70, v28, v29
v_cvt_pk_bf16_f32 v71, v30, v31
ds_write_b128 v95, v[68:71] offset:0
s_waitcnt lgkmcnt(0)
ds_read_b128 v[64:67], v96 offset:0
ds_read_b128 v[68:71], v96 offset:1024
s_waitcnt lgkmcnt(0)
buffer_store_dwordx4 v[64:67], v92, s[44:47], s54 offen
buffer_store_dwordx4 v[68:71], v93, s[44:47], s54 offen
s_endpgm
CR0_1:
s_cmp_eq_u32 s61, 0
s_cbranch_scc1 BR0_1
s_sub_u32 s61, s61, 1
s_sleep 8
buffer_load_dwordx2 v[76:77], v84, s[36:39], 0 offen sc1
s_waitcnt vmcnt(0)
ds_write_b32 v82, v76
v_sub_u32 v76, v76, v85
v_xor_b32 v77, v77, v73
v_cmp_lt_u64 vcc, s[62:63], v[76:77]
s_waitcnt lgkmcnt(0)
s_cbranch_vccnz CR0_1
s_branch BR0_1
CR0_2:
s_mov_b32 s58, m0
s_mov_b32 m0, 142336
DR0_2:
s_cmp_eq_u32 s61, 0
s_cbranch_scc1 ER0_2
s_sub_u32 s61, s61, 1
buffer_load_dword v84, s[36:39], 0 offen sc1 lds
s_sleep 1
ds_read_b32 v76, v82
s_waitcnt lgkmcnt(0)
v_cmp_gt_i32 vcc, v85, v76
s_cbranch_vccnz DR0_2
ER0_2:
s_mov_b32 m0, s58
s_branch BR0_2
CR0_3:
s_mov_b32 s58, m0
s_mov_b32 m0, 142336
DR0_3:
s_cmp_eq_u32 s61, 0
s_cbranch_scc1 ER0_3
s_sub_u32 s61, s61, 1
buffer_load_dword v84, s[36:39], 0 offen sc1 lds
s_sleep 1
ds_read_b32 v76, v82
s_waitcnt lgkmcnt(0)
v_cmp_gt_i32 vcc, v85, v76
s_cbranch_vccnz DR0_3
ER0_3:
s_mov_b32 m0, s58
s_branch BR0_3
CR0_4:
s_mov_b32 s58, m0
s_mov_b32 m0, 142336
DR0_4:
s_cmp_eq_u32 s61, 0
s_cbranch_scc1 ER0_4
s_sub_u32 s61, s61, 1
buffer_load_dword v84, s[36:39], 0 offen sc1 lds
s_sleep 1
ds_read_b32 v76, v82
s_waitcnt lgkmcnt(0)
v_cmp_gt_i32 vcc, v85, v76
s_cbranch_vccnz DR0_4
ER0_4:
s_mov_b32 m0, s58
s_branch BR0_4
CR0_5:
s_mov_b32 s58, m0
s_mov_b32 m0, 142336
DR0_5:
s_cmp_eq_u32 s61, 0
s_cbranch_scc1 ER0_5
s_sub_u32 s61, s61, 1
buffer_load_dword v84, s[36:39], 0 offen sc1 lds
s_sleep 1
ds_read_b32 v76, v82
s_waitcnt lgkmcnt(0)
v_cmp_gt_i32 vcc, v85, v76
s_cbranch_vccnz DR0_5
ER0_5:
s_mov_b32 m0, s58
s_branch BR0_5
CR0_7:
s_mov_b32 s58, m0
s_mov_b32 m0, 142336
DR0_7:
s_cmp_eq_u32 s61, 0
s_cbranch_scc1 ER0_7
s_sub_u32 s61, s61, 1
buffer_load_dword v84, s[36:39], 0 offen sc1 lds
s_sleep 1
ds_read_b32 v76, v82
s_waitcnt lgkmcnt(0)
v_cmp_gt_i32 vcc, v85, v76
s_cbranch_vccnz DR0_7
ER0_7:
s_mov_b32 m0, s58
s_branch BR0_7
CR0_8:
s_mov_b32 s58, m0
s_mov_b32 m0, 142336
DR0_8:
s_cmp_eq_u32 s61, 0
s_cbranch_scc1 ER0_8
s_sub_u32 s61, s61, 1
buffer_load_dword v84, s[36:39], 0 offen sc1 lds
s_sleep 1
ds_read_b32 v76, v82
s_waitcnt lgkmcnt(0)
v_cmp_gt_i32 vcc, v85, v76
s_cbranch_vccnz DR0_8
ER0_8:
s_mov_b32 m0, s58
s_branch BR0_8
CR0_9:
s_mov_b32 s58, m0
s_mov_b32 m0, 142336
DR0_9:
s_cmp_eq_u32 s61, 0
s_cbranch_scc1 ER0_9
s_sub_u32 s61, s61, 1
buffer_load_dword v84, s[36:39], 0 offen sc1 lds
s_sleep 1
ds_read_b32 v76, v82
s_waitcnt lgkmcnt(0)
v_cmp_gt_i32 vcc, v85, v76
s_cbranch_vccnz DR0_9
ER0_9:
s_mov_b32 m0, s58
s_branch BR0_9
CR0_10:
s_mov_b32 s58, m0
s_mov_b32 m0, 142336
DR0_10:
s_cmp_eq_u32 s61, 0
s_cbranch_scc1 ER0_10
s_sub_u32 s61, s61, 1
buffer_load_dword v84, s[36:39], 0 offen sc1 lds
s_sleep 1
ds_read_b32 v76, v82
s_waitcnt lgkmcnt(0)
v_cmp_gt_i32 vcc, v85, v76
s_cbranch_vccnz DR0_10
ER0_10:
s_mov_b32 m0, s58
s_branch BR0_10
CR0_12:
s_mov_b32 s58, m0
s_mov_b32 m0, 142336
DR0_12:
s_cmp_eq_u32 s61, 0
s_cbranch_scc1 ER0_12
s_sub_u32 s61, s61, 1
buffer_load_dword v84, s[36:39], 0 offen sc1 lds
s_sleep 1
ds_read_b32 v76, v82
s_waitcnt lgkmcnt(0)
v_cmp_gt_i32 vcc, v85, v76
s_cbranch_vccnz DR0_12
ER0_12:
s_mov_b32 m0, s58
s_branch BR0_12
CR0_13:
s_mov_b32 s58, m0
s_mov_b32 m0, 142336
DR0_13:
s_cmp_eq_u32 s61, 0
s_cbranch_scc1 ER0_13
s_sub_u32 s61, s61, 1
buffer_load_dword v84, s[36:39], 0 offen sc1 lds
s_sleep 1
ds_read_b32 v76, v82
s_waitcnt lgkmcnt(0)
v_cmp_gt_i32 vcc, v85, v76
s_cbranch_vccnz DR0_13
ER0_13:
s_mov_b32 m0, s58
s_branch BR0_13
CR0_14:
s_mov_b32 s58, m0
s_mov_b32 m0, 142336
DR0_14:
s_cmp_eq_u32 s61, 0
s_cbranch_scc1 ER0_14
s_sub_u32 s61, s61, 1
buffer_load_dword v84, s[36:39], 0 offen sc1 lds
s_sleep 1
ds_read_b32 v76, v82
s_waitcnt lgkmcnt(0)
v_cmp_gt_i32 vcc, v85, v76
s_cbranch_vccnz DR0_14
ER0_14:
s_mov_b32 m0, s58
s_branch BR0_14
CR0_15:
s_mov_b32 s58, m0
s_mov_b32 m0, 142336
DR0_15:
s_cmp_eq_u32 s61, 0
s_cbranch_scc1 ER0_15
s_sub_u32 s61, s61, 1
buffer_load_dword v84, s[36:39], 0 offen sc1 lds
s_sleep 1
ds_read_b32 v76, v82
s_waitcnt lgkmcnt(0)
v_cmp_gt_i32 vcc, v85, v76
s_cbranch_vccnz DR0_15
ER0_15:
s_mov_b32 m0, s58
s_branch BR0_15
CR0_16:
s_mov_b32 s58, m0
s_mov_b32 m0, 142336
DR0_16:
s_cmp_eq_u32 s61, 0
s_cbranch_scc1 ER0_16
s_sub_u32 s61, s61, 1
buffer_load_dword v84, s[36:39], 0 offen sc1 lds
s_sleep 1
ds_read_b32 v76, v82
s_waitcnt lgkmcnt(0)
v_cmp_gt_i32 vcc, v85, v76
s_cbranch_vccnz DR0_16
ER0_16:
s_mov_b32 m0, s58
s_branch BR0_16
LvR1:
LsR6:
s_cbranch_execz LsR10
s_add_u32 s49, s49, 346112
s_barrier
s_add_u32 m0, s50, 0
s_nop 0
D 83,0
D 83,1024
D 83,2048
D 83,3072
s_add_u32 s49, s49, 346112
s_barrier
s_add_u32 m0, s50, 32768
s_nop 0
D 83,0
D 83,1024
D 83,2048
D 83,3072
buffer_load_dwordx4 v[124:127], v90, s[36:39], s49 offen sc1
buffer_load_dwordx4 v[128:131], v90, s[36:39], s49 offen offset:1024 sc1
s_xor_b32 s50, s50, 0x10000
s_add_u32 s49, s49, 346112
s_waitcnt vmcnt(6)
s_barrier
R 32,0
buffer_load_dwordx4 v[132:135], v86, s[36:39], s49 offen sc1
R 36,1024
buffer_load_dwordx4 v[136:139], v86, s[36:39], s49 offen offset:1024 sc1
R 40,2048
buffer_load_dwordx4 v[140:143], v87, s[36:39], s49 offen sc1
s_waitcnt lgkmcnt(1)
v_mfma_f32_16x16x32_bf16 v[0:3], a[0:3], v[32:35], v[100:103]
R 44,3072
v_mfma_f32_16x16x32_bf16 v[4:7], a[4:7], v[32:35], v[104:107]
v_mfma_f32_16x16x32_bf16 v[8:11], a[8:11], v[32:35], v[108:111]
buffer_load_dwordx4 v[144:147], v87, s[36:39], s49 offen offset:1024 sc1
v_mfma_f32_16x16x32_bf16 v[12:15], a[12:15], v[32:35], v[112:115]
mav 0,16,36
R 48,4096
mav 4,20,36
mav 8,24,36
mav 12,28,36
buffer_load_dwordx4 v[148:151], v88, s[36:39], s49 offen sc1
s_waitcnt lgkmcnt(1)
mav 0,32,40
R 52,5120
mav 4,36,40
mav 8,40,40
mav 12,44,40
buffer_load_dwordx4 v[152:155], v88, s[36:39], s49 offen offset:1024 sc1
mav 0,48,44
R 56,6144
mav 4,52,44
mav 8,56,44
mav 12,60,44
s_waitcnt lgkmcnt(1)
mav 0,64,48
R 60,7168
buffer_load_dwordx4 v[156:159], v89, s[36:39], s49 offen sc1
mav 4,68,48
mav 8,72,48
mav 12,76,48
mav 0,80,52
R 32,8192
buffer_load_dwordx4 v[160:163], v89, s[36:39], s49 offen offset:1024 sc1
mav 4,84,52
mav 8,88,52
mav 12,92,52
s_add_u32 m0, s50, 0
s_waitcnt lgkmcnt(1)
mav 0,96,56
R 36,9216
mav 4,100,56
D 83,0
mav 8,104,56
mav 12,108,56
mav 0,112,60
R 40,10240
mav 4,116,60
D 83,1024
mav 8,120,60
mav 12,124,60
s_waitcnt lgkmcnt(1)
mav 0,128,32
R 44,11264
mav 4,132,32
mav 8,136,32
D 83,2048
mav 12,140,32
mav 0,144,36
R 48,12288
mav 4,148,36
mav 8,152,36
D 83,3072
mav 12,156,36
s_waitcnt lgkmcnt(1)
mav 0,160,40
R 52,13312
mav 4,164,40
mav 8,168,40
mav 12,172,40
buffer_load_dwordx4 v[164:167], v90, s[36:39], s49 offen sc1
mav 0,176,44
R 56,14336
mav 4,180,44
mav 8,184,44
mav 12,188,44
buffer_load_dwordx4 v[168:171], v90, s[36:39], s49 offen offset:1024 sc1
s_waitcnt lgkmcnt(1)
mav 0,192,48
R 60,15360
mav 4,196,48
mav 8,200,48
mav 12,204,48
mav 0,208,52
mav 4,212,52
mav 8,216,52
mav 12,220,52
s_waitcnt lgkmcnt(0)
mav 0,224,56
mav 4,228,56
mav 8,232,56
mav 12,236,56
mav 0,240,60
mav 4,244,60
mav 8,248,60
mav 12,252,60
s_waitcnt vmcnt(15)
mvv 0,116,124
mvv 4,120,124
s_waitcnt vmcnt(14)
mvv 8,116,128
mvv 12,120,128
s_waitcnt vmcnt(13)
mvv 0,116,132
mvv 4,120,132
s_waitcnt vmcnt(12)
mvv 8,116,136
s_add_u32 s49, s49, 346112
mvv 12,120,136
s_barrier
s_waitcnt vmcnt(11)
mvv 0,116,140
R 32,32768
mvv 4,120,140
buffer_load_dwordx4 v[172:175], v86, s[36:39], s49 offen sc1
s_waitcnt vmcnt(11)
mvv 8,116,144
mvv 12,120,144
s_waitcnt vmcnt(10)
mvv 0,116,148
R 36,33792
mvv 4,120,148
buffer_load_dwordx4 v[176:179], v86, s[36:39], s49 offen offset:1024 sc1
s_waitcnt vmcnt(10)
mvv 8,116,152
mvv 12,120,152
s_waitcnt vmcnt(9)
mvv 0,116,156
R 40,34816
mvv 4,120,156
s_waitcnt vmcnt(8)
mvv 8,116,160
buffer_load_dwordx4 v[180:183], v87, s[36:39], s49 offen sc1
mvv 12,120,160
s_waitcnt lgkmcnt(1)
v_mfma_f32_16x16x32_bf16 v[16:19], a[0:3], v[32:35], v[100:103]
R 44,35840
v_cvt_pk_bf16_f32 v64, v0, v1
v_mfma_f32_16x16x32_bf16 v[20:23], a[4:7], v[32:35], v[104:107]
v_cvt_pk_bf16_f32 v65, v2, v3
v_cvt_pk_bf16_f32 v66, v4, v5
v_mfma_f32_16x16x32_bf16 v[24:27], a[8:11], v[32:35], v[108:111]
buffer_load_dwordx4 v[184:187], v87, s[36:39], s49 offen offset:1024 sc1
v_cvt_pk_bf16_f32 v67, v6, v7
v_mfma_f32_16x16x32_bf16 v[28:31], a[12:15], v[32:35], v[112:115]
v_cvt_pk_bf16_f32 v68, v8, v9
v_cvt_pk_bf16_f32 v69, v10, v11
mav 16,16,36
R 48,36864
v_cvt_pk_bf16_f32 v70, v12, v13
mav 20,20,36
v_cvt_pk_bf16_f32 v71, v14, v15
v_mul_f32 v75, v0, v0
mav 24,24,36
buffer_store_dwordx4 v[64:67], v91, s[36:39], s49 offen sc1
v_add_f32 v74, v1, v0
mav 28,28,36
v_fmac_f32 v75, v1, v1
s_waitcnt lgkmcnt(1)
mav 16,32,40
R 52,37888
v_add_f32 v74, v2, v74
mav 20,36,40
v_fmac_f32 v75, v2, v2
v_add_f32 v74, v3, v74
mav 24,40,40
buffer_store_dwordx4 v[68:71], v91, s[36:39], s49 offen offset:1024 sc1
v_fmac_f32 v75, v3, v3
mav 28,44,40
v_add_f32 v74, v4, v74
v_fmac_f32 v75, v4, v4
mav 16,48,44
R 56,38912
v_add_f32 v74, v5, v74
mav 20,52,44
v_fmac_f32 v75, v5, v5
v_add_f32 v74, v6, v74
mav 24,56,44
buffer_load_dwordx4 v[188:191], v88, s[36:39], s49 offen sc1
v_fmac_f32 v75, v6, v6
mav 28,60,44
v_add_f32 v74, v7, v74
s_waitcnt lgkmcnt(1)
mav 16,64,48
R 60,39936
v_fmac_f32 v75, v7, v7
mav 20,68,48
v_add_f32 v74, v8, v74
v_fmac_f32 v75, v8, v8
mav 24,72,48
buffer_load_dwordx4 v[192:195], v88, s[36:39], s49 offen offset:1024 sc1
v_add_f32 v74, v9, v74
mav 28,76,48
v_fmac_f32 v75, v9, v9
v_add_f32 v74, v10, v74
mav 16,80,52
R 32,40960
v_fmac_f32 v75, v10, v10
mav 20,84,52
v_add_f32 v74, v11, v74
v_fmac_f32 v75, v11, v11
mav 24,88,52
buffer_load_dwordx4 v[196:199], v89, s[36:39], s49 offen sc1
v_add_f32 v74, v12, v74
mav 28,92,52
s_add_u32 m0, s50, 32768
s_waitcnt lgkmcnt(1)
mav 16,96,56
R 36,41984
v_fmac_f32 v75, v12, v12
mav 20,100,56
v_add_f32 v74, v13, v74
v_fmac_f32 v75, v13, v13
mav 24,104,56
buffer_load_dwordx4 v[200:203], v89, s[36:39], s49 offen offset:1024 sc1
v_add_f32 v74, v14, v74
mav 28,108,56
v_fmac_f32 v75, v14, v14
v_add_f32 v74, v15, v74
mav 16,112,60
R 40,43008
v_fmac_f32 v75, v15, v15
mav 20,116,60
mav 24,120,60
D 83,0
v_permlane32_swap_b32 v74, v75
mav 28,124,60
v_add_f32 v74, v74, v75
s_waitcnt lgkmcnt(1)
mav 16,128,32
R 44,44032
mav 20,132,32
mav 24,136,32
D 83,1024
mav 28,140,32
mav 16,144,36
R 48,45056
v_add_u32 v72, 1, v72
mav 20,148,36
mav 24,152,36
D 83,2048
mav 28,156,36
s_waitcnt lgkmcnt(1)
mav 16,160,40
R 52,46080
mav 20,164,40
mav 24,168,40
D 83,3072
mav 28,172,40
mav 16,176,44
R 56,47104
mav 20,180,44
mav 24,184,44
buffer_load_dwordx4 v[124:127], v90, s[36:39], s49 offen sc1
mav 28,188,44
s_waitcnt lgkmcnt(1)
mav 16,192,48
R 60,48128
mav 20,196,48
mav 24,200,48
buffer_load_dwordx4 v[128:131], v90, s[36:39], s49 offen offset:1024 sc1
mav 28,204,48
mav 16,208,52
mav 20,212,52
mav 24,216,52
mav 28,220,52
s_waitcnt lgkmcnt(0)
mav 16,224,56
mav 20,228,56
mav 24,232,56
mav 28,236,56
mav 16,240,60
s_waitcnt vmcnt(10)
buffer_store_dwordx2 v[72:73], v81, s[36:39], s52 offen sc1
mav 20,244,60
mav 24,248,60
ds_write_b32 v97, v74 offset:0
mav 28,252,60
mvv 16,116,164
mvv 20,120,164
mvv 24,116,168
s_xor_b32 s50, s50, 0x10000
mvv 28,120,168
mvv 16,116,172
mvv 20,120,172
v_xor_b32 v80, 0x10000, v80
mvv 24,116,176
s_add_u32 s49, s49, 346112
mvv 28,120,176
s_waitcnt lgkmcnt(0)
s_barrier
mvv 16,116,180
R 32,0
ds_read_b128 v[204:207], v98 offset:0
mvv 20,120,180
buffer_load_dwordx4 v[132:135], v86, s[36:39], s49 offen sc1
mvv 24,116,184
mvv 28,120,184
s_waitcnt vmcnt(11)
mvv 16,116,188
R 36,1024
mvv 20,120,188
buffer_load_dwordx4 v[136:139], v86, s[36:39], s49 offen offset:1024 sc1
s_waitcnt vmcnt(11)
mvv 24,116,192
mvv 28,120,192
s_waitcnt vmcnt(10)
mvv 16,116,196
R 40,2048
mvv 20,120,196
s_waitcnt vmcnt(9)
mvv 24,116,200
buffer_load_dwordx4 v[140:143], v87, s[36:39], s49 offen sc1
mvv 28,120,200
s_mov_b32 s60, -15
.p2align 6
LR1_1:
s_waitcnt lgkmcnt(1)
v_mfma_f32_16x16x32_bf16 v[0:3], a[0:3], v[32:35], v[100:103]
R 44,3072
v_add_f32 v204, v204, v205
v_mfma_f32_16x16x32_bf16 v[4:7], a[4:7], v[32:35], v[104:107]
v_add_f32 v206, v206, v207
v_add_f32 v204, v204, v206
v_mfma_f32_16x16x32_bf16 v[8:11], a[8:11], v[32:35], v[108:111]
buffer_load_dwordx4 v[144:147], v87, s[36:39], s49 offen offset:1024 sc1
v_add_f32_dpp v205, v204, v204 quad_perm:[1,0,3,2] row_mask:0xf bank_mask:0xf
LsR7:
s_cbranch_execz LsR11
v_mfma_f32_16x16x32_bf16 v[12:15], a[12:15], v[32:35], v[112:115]
buffer_store_dword v205, v99, s[36:39], s49 offen sc1
v_cvt_pk_bf16_f32 v64, v16, v17
mav 0,16,36
R 48,4096
v_cvt_pk_bf16_f32 v65, v18, v19
mav 4,20,36
v_cvt_pk_bf16_f32 v66, v20, v21
v_cvt_pk_bf16_f32 v67, v22, v23
mav 8,24,36
buffer_store_dwordx4 v[64:67], v91, s[36:39], s49 offen sc1
v_cvt_pk_bf16_f32 v68, v24, v25
mav 12,28,36
v_cvt_pk_bf16_f32 v69, v26, v27
s_waitcnt lgkmcnt(1)
mav 0,32,40
R 52,5120
v_cvt_pk_bf16_f32 v70, v28, v29
mav 4,36,40
v_cvt_pk_bf16_f32 v71, v30, v31
v_mul_f32 v75, v16, v16
mav 8,40,40
buffer_store_dwordx4 v[68:71], v91, s[36:39], s49 offen offset:1024 sc1
v_add_f32 v74, v17, v16
mav 12,44,40
v_fmac_f32 v75, v17, v17
v_add_f32 v74, v18, v74
mav 0,48,44
R 56,6144
v_fmac_f32 v75, v18, v18
mav 4,52,44
v_add_f32 v74, v19, v74
v_fmac_f32 v75, v19, v19
mav 8,56,44
buffer_load_dwordx4 v[148:151], v88, s[36:39], s49 offen sc1
v_add_f32 v74, v20, v74
mav 12,60,44
v_fmac_f32 v75, v20, v20
s_waitcnt lgkmcnt(1)
mav 0,64,48
R 60,7168
v_add_f32 v74, v21, v74
mav 4,68,48
v_fmac_f32 v75, v21, v21
v_add_f32 v74, v22, v74
mav 8,72,48
buffer_load_dwordx4 v[152:155], v88, s[36:39], s49 offen offset:1024 sc1
v_fmac_f32 v75, v22, v22
mav 12,76,48
v_add_f32 v74, v23, v74
v_fmac_f32 v75, v23, v23
mav 0,80,52
R 32,8192
v_add_f32 v74, v24, v74
mav 4,84,52
v_fmac_f32 v75, v24, v24
v_add_f32 v74, v25, v74
mav 8,88,52
buffer_load_dwordx4 v[156:159], v89, s[36:39], s49 offen sc1
v_fmac_f32 v75, v25, v25
mav 12,92,52
s_add_u32 m0, s50, 0
s_waitcnt lgkmcnt(1)
mav 0,96,56
R 36,9216
v_add_f32 v74, v26, v74
mav 4,100,56
v_fmac_f32 v75, v26, v26
v_add_f32 v74, v27, v74
mav 8,104,56
buffer_load_dwordx4 v[160:163], v89, s[36:39], s49 offen offset:1024 sc1
v_fmac_f32 v75, v27, v27
mav 12,108,56
v_add_f32 v74, v28, v74
v_fmac_f32 v75, v28, v28
mav 0,112,60
R 40,10240
v_add_f32 v74, v29, v74
mav 4,116,60
v_fmac_f32 v75, v29, v29
v_add_f32 v74, v30, v74
mav 8,120,60
D 83,0
v_fmac_f32 v75, v30, v30
mav 12,124,60
v_add_f32 v74, v31, v74
s_waitcnt lgkmcnt(1)
mav 0,128,32
R 44,11264
v_fmac_f32 v75, v31, v31
mav 4,132,32
mav 8,136,32
D 83,1024
v_permlane32_swap_b32 v74, v75
mav 12,140,32
v_add_f32 v74, v74, v75
mav 0,144,36
R 48,12288
v_add_u32 v72, 1, v72
mav 4,148,36
mav 8,152,36
D 83,2048
mav 12,156,36
s_waitcnt lgkmcnt(1)
mav 0,160,40
R 52,13312
mav 4,164,40
mav 8,168,40
D 83,3072
mav 12,172,40
mav 0,176,44
R 56,14336
mav 4,180,44
mav 8,184,44
buffer_load_dwordx4 v[164:167], v90, s[36:39], s49 offen sc1
mav 12,188,44
s_waitcnt lgkmcnt(1)
mav 0,192,48
R 60,15360
mav 4,196,48
mav 8,200,48
buffer_load_dwordx4 v[168:171], v90, s[36:39], s49 offen offset:1024 sc1
mav 12,204,48
mav 0,208,52
mav 4,212,52
mav 8,216,52
mav 12,220,52
s_waitcnt lgkmcnt(0)
mav 0,224,56
mav 4,228,56
mav 8,232,56
mav 12,236,56
mav 0,240,60
s_waitcnt vmcnt(10)
buffer_store_dwordx2 v[72:73], v81, s[36:39], s52 offen sc1
mav 4,244,60
mav 8,248,60
ds_write_b32 v97, v74 offset:1024
mav 12,252,60
mvv 0,116,124
mvv 4,120,124
mvv 8,116,128
mvv 12,120,128
mvv 0,116,132
mvv 4,120,132
mvv 8,116,136
s_add_u32 s49, s49, 346112
mvv 12,120,136
s_waitcnt lgkmcnt(0)
s_barrier
mvv 0,116,140
R 32,32768
ds_read_b128 v[204:207], v98 offset:1024
mvv 4,120,140
buffer_load_dwordx4 v[172:175], v86, s[36:39], s49 offen sc1
mvv 8,116,144
mvv 12,120,144
s_waitcnt vmcnt(11)
mvv 0,116,148
R 36,33792
mvv 4,120,148
buffer_load_dwordx4 v[176:179], v86, s[36:39], s49 offen offset:1024 sc1
s_waitcnt vmcnt(11)
mvv 8,116,152
mvv 12,120,152
s_waitcnt vmcnt(10)
mvv 0,116,156
R 40,34816
mvv 4,120,156
s_waitcnt vmcnt(9)
mvv 8,116,160
buffer_load_dwordx4 v[180:183], v87, s[36:39], s49 offen sc1
mvv 12,120,160
s_waitcnt lgkmcnt(1)
v_mfma_f32_16x16x32_bf16 v[16:19], a[0:3], v[32:35], v[100:103]
R 44,35840
v_add_f32 v204, v204, v205
v_mfma_f32_16x16x32_bf16 v[20:23], a[4:7], v[32:35], v[104:107]
v_add_f32 v206, v206, v207
v_add_f32 v204, v204, v206
v_mfma_f32_16x16x32_bf16 v[24:27], a[8:11], v[32:35], v[108:111]
buffer_load_dwordx4 v[184:187], v87, s[36:39], s49 offen offset:1024 sc1
v_add_f32_dpp v205, v204, v204 quad_perm:[1,0,3,2] row_mask:0xf bank_mask:0xf
v_mfma_f32_16x16x32_bf16 v[28:31], a[12:15], v[32:35], v[112:115]
buffer_store_dword v205, v99, s[36:39], s49 offen sc1
v_cvt_pk_bf16_f32 v64, v0, v1
mav 16,16,36
R 48,36864
v_cvt_pk_bf16_f32 v65, v2, v3
mav 20,20,36
v_cvt_pk_bf16_f32 v66, v4, v5
v_cvt_pk_bf16_f32 v67, v6, v7
mav 24,24,36
buffer_store_dwordx4 v[64:67], v91, s[36:39], s49 offen sc1
v_cvt_pk_bf16_f32 v68, v8, v9
mav 28,28,36
v_cvt_pk_bf16_f32 v69, v10, v11
s_waitcnt lgkmcnt(1)
mav 16,32,40
R 52,37888
v_cvt_pk_bf16_f32 v70, v12, v13
mav 20,36,40
v_cvt_pk_bf16_f32 v71, v14, v15
v_mul_f32 v75, v0, v0
mav 24,40,40
buffer_store_dwordx4 v[68:71], v91, s[36:39], s49 offen offset:1024 sc1
v_add_f32 v74, v1, v0
mav 28,44,40
v_fmac_f32 v75, v1, v1
v_add_f32 v74, v2, v74
mav 16,48,44
R 56,38912
v_fmac_f32 v75, v2, v2
mav 20,52,44
v_add_f32 v74, v3, v74
v_fmac_f32 v75, v3, v3
mav 24,56,44
buffer_load_dwordx4 v[188:191], v88, s[36:39], s49 offen sc1
v_add_f32 v74, v4, v74
mav 28,60,44
v_fmac_f32 v75, v4, v4
s_waitcnt lgkmcnt(1)
mav 16,64,48
R 60,39936
v_add_f32 v74, v5, v74
mav 20,68,48
v_fmac_f32 v75, v5, v5
v_add_f32 v74, v6, v74
mav 24,72,48
buffer_load_dwordx4 v[192:195], v88, s[36:39], s49 offen offset:1024 sc1
v_fmac_f32 v75, v6, v6
mav 28,76,48
v_add_f32 v74, v7, v74
v_fmac_f32 v75, v7, v7
mav 16,80,52
R 32,40960
v_add_f32 v74, v8, v74
mav 20,84,52
v_fmac_f32 v75, v8, v8
v_add_f32 v74, v9, v74
mav 24,88,52
buffer_load_dwordx4 v[196:199], v89, s[36:39], s49 offen sc1
v_fmac_f32 v75, v9, v9
mav 28,92,52
s_add_u32 m0, s50, 32768
s_waitcnt lgkmcnt(1)
mav 16,96,56
R 36,41984
v_add_f32 v74, v10, v74
mav 20,100,56
v_fmac_f32 v75, v10, v10
v_add_f32 v74, v11, v74
mav 24,104,56
buffer_load_dwordx4 v[200:203], v89, s[36:39], s49 offen offset:1024 sc1
v_fmac_f32 v75, v11, v11
mav 28,108,56
v_add_f32 v74, v12, v74
v_fmac_f32 v75, v12, v12
mav 16,112,60
R 40,43008
v_add_f32 v74, v13, v74
mav 20,116,60
v_fmac_f32 v75, v13, v13
v_add_f32 v74, v14, v74
mav 24,120,60
D 83,0
v_fmac_f32 v75, v14, v14
mav 28,124,60
v_add_f32 v74, v15, v74
s_waitcnt lgkmcnt(1)
mav 16,128,32
R 44,44032
v_fmac_f32 v75, v15, v15
mav 20,132,32
mav 24,136,32
D 83,1024
v_permlane32_swap_b32 v74, v75
mav 28,140,32
v_add_f32 v74, v74, v75
mav 16,144,36
R 48,45056
v_add_u32 v72, 1, v72
mav 20,148,36
mav 24,152,36
D 83,2048
mav 28,156,36
s_waitcnt lgkmcnt(1)
mav 16,160,40
R 52,46080
mav 20,164,40
mav 24,168,40
D 83,3072
mav 28,172,40
mav 16,176,44
R 56,47104
mav 20,180,44
mav 24,184,44
buffer_load_dwordx4 v[124:127], v90, s[36:39], s49 offen sc1
mav 28,188,44
s_waitcnt lgkmcnt(1)
mav 16,192,48
R 60,48128
mav 20,196,48
mav 24,200,48
buffer_load_dwordx4 v[128:131], v90, s[36:39], s49 offen offset:1024 sc1
mav 28,204,48
mav 16,208,52
mav 20,212,52
mav 24,216,52
mav 28,220,52
s_waitcnt lgkmcnt(0)
mav 16,224,56
mav 20,228,56
mav 24,232,56
mav 28,236,56
mav 16,240,60
s_waitcnt vmcnt(10)
buffer_store_dwordx2 v[72:73], v81, s[36:39], s52 offen sc1
mav 20,244,60
mav 24,248,60
ds_write_b32 v97, v74 offset:0
mav 28,252,60
mvv 16,116,164
mvv 20,120,164
mvv 24,116,168
s_xor_b32 s50, s50, 0x10000
mvv 28,120,168
mvv 16,116,172
mvv 20,120,172
v_xor_b32 v80, 0x10000, v80
mvv 24,116,176
s_add_u32 s49, s49, 346112
mvv 28,120,176
s_waitcnt lgkmcnt(0)
s_barrier
mvv 16,116,180
R 32,0
ds_read_b128 v[204:207], v98 offset:0
mvv 20,120,180
buffer_load_dwordx4 v[132:135], v86, s[36:39], s49 offen sc1
mvv 24,116,184
mvv 28,120,184
s_waitcnt vmcnt(11)
mvv 16,116,188
R 36,1024
mvv 20,120,188
buffer_load_dwordx4 v[136:139], v86, s[36:39], s49 offen offset:1024 sc1
s_waitcnt vmcnt(11)
mvv 24,116,192
mvv 28,120,192
s_waitcnt vmcnt(10)
mvv 16,116,196
R 40,2048
mvv 20,120,196
s_waitcnt vmcnt(9)
mvv 24,116,200
buffer_load_dwordx4 v[140:143], v87, s[36:39], s49 offen sc1
mvv 28,120,200
s_add_u32 s60, s60, 1
s_cbranch_scc0 LR1_1
s_waitcnt lgkmcnt(1)
v_mfma_f32_16x16x32_bf16 v[0:3], a[0:3], v[32:35], v[100:103]
R 44,3072
v_add_f32 v204, v204, v205
v_mfma_f32_16x16x32_bf16 v[4:7], a[4:7], v[32:35], v[104:107]
v_add_f32 v206, v206, v207
v_add_f32 v204, v204, v206
v_mfma_f32_16x16x32_bf16 v[8:11], a[8:11], v[32:35], v[108:111]
buffer_load_dwordx4 v[144:147], v87, s[36:39], s49 offen offset:1024 sc1
v_add_f32_dpp v205, v204, v204 quad_perm:[1,0,3,2] row_mask:0xf bank_mask:0xf
v_mfma_f32_16x16x32_bf16 v[12:15], a[12:15], v[32:35], v[112:115]
buffer_store_dword v205, v99, s[36:39], s49 offen sc1
v_cvt_pk_bf16_f32 v64, v16, v17
mav 0,16,36
R 48,4096
v_cvt_pk_bf16_f32 v65, v18, v19
mav 4,20,36
v_cvt_pk_bf16_f32 v66, v20, v21
v_cvt_pk_bf16_f32 v67, v22, v23
mav 8,24,36
buffer_store_dwordx4 v[64:67], v91, s[36:39], s49 offen sc1
v_cvt_pk_bf16_f32 v68, v24, v25
mav 12,28,36
v_cvt_pk_bf16_f32 v69, v26, v27
s_waitcnt lgkmcnt(1)
mav 0,32,40
R 52,5120
v_cvt_pk_bf16_f32 v70, v28, v29
mav 4,36,40
v_cvt_pk_bf16_f32 v71, v30, v31
v_mul_f32 v75, v16, v16
mav 8,40,40
buffer_store_dwordx4 v[68:71], v91, s[36:39], s49 offen offset:1024 sc1
v_add_f32 v74, v17, v16
mav 12,44,40
v_fmac_f32 v75, v17, v17
v_add_f32 v74, v18, v74
mav 0,48,44
R 56,6144
v_fmac_f32 v75, v18, v18
mav 4,52,44
v_add_f32 v74, v19, v74
v_fmac_f32 v75, v19, v19
mav 8,56,44
buffer_load_dwordx4 v[148:151], v88, s[36:39], s49 offen sc1
v_add_f32 v74, v20, v74
mav 12,60,44
v_fmac_f32 v75, v20, v20
s_waitcnt lgkmcnt(1)
mav 0,64,48
R 60,7168
v_add_f32 v74, v21, v74
mav 4,68,48
v_fmac_f32 v75, v21, v21
v_add_f32 v74, v22, v74
mav 8,72,48
buffer_load_dwordx4 v[152:155], v88, s[36:39], s49 offen offset:1024 sc1
v_fmac_f32 v75, v22, v22
mav 12,76,48
v_add_f32 v74, v23, v74
v_fmac_f32 v75, v23, v23
mav 0,80,52
R 32,8192
v_add_f32 v74, v24, v74
mav 4,84,52
v_fmac_f32 v75, v24, v24
v_add_f32 v74, v25, v74
mav 8,88,52
buffer_load_dwordx4 v[156:159], v89, s[36:39], s49 offen sc1
v_fmac_f32 v75, v25, v25
mav 12,92,52
s_add_u32 m0, s50, 0
s_waitcnt lgkmcnt(1)
mav 0,96,56
R 36,9216
v_add_f32 v74, v26, v74
mav 4,100,56
v_fmac_f32 v75, v26, v26
v_add_f32 v74, v27, v74
mav 8,104,56
buffer_load_dwordx4 v[160:163], v89, s[36:39], s49 offen offset:1024 sc1
v_fmac_f32 v75, v27, v27
mav 12,108,56
v_add_f32 v74, v28, v74
v_fmac_f32 v75, v28, v28
mav 0,112,60
R 40,10240
v_add_f32 v74, v29, v74
mav 4,116,60
v_fmac_f32 v75, v29, v29
v_add_f32 v74, v30, v74
mav 8,120,60
D 83,0
v_fmac_f32 v75, v30, v30
mav 12,124,60
v_add_f32 v74, v31, v74
s_waitcnt lgkmcnt(1)
mav 0,128,32
R 44,11264
v_fmac_f32 v75, v31, v31
mav 4,132,32
mav 8,136,32
D 83,1024
v_permlane32_swap_b32 v74, v75
mav 12,140,32
v_add_f32 v74, v74, v75
mav 0,144,36
R 48,12288
v_add_u32 v72, 1, v72
mav 4,148,36
mav 8,152,36
D 83,2048
mav 12,156,36
s_waitcnt lgkmcnt(1)
mav 0,160,40
R 52,13312
mav 4,164,40
mav 8,168,40
D 83,3072
mav 12,172,40
mav 0,176,44
R 56,14336
mav 4,180,44
mav 8,184,44
buffer_load_dwordx4 v[164:167], v90, s[36:39], s49 offen sc1
mav 12,188,44
s_waitcnt lgkmcnt(1)
mav 0,192,48
R 60,15360
mav 4,196,48
mav 8,200,48
buffer_load_dwordx4 v[168:171], v90, s[36:39], s49 offen offset:1024 sc1
mav 12,204,48
mav 0,208,52
mav 4,212,52
mav 8,216,52
mav 12,220,52
s_waitcnt lgkmcnt(0)
mav 0,224,56
mav 4,228,56
mav 8,232,56
mav 12,236,56
mav 0,240,60
s_waitcnt vmcnt(10)
buffer_store_dwordx2 v[72:73], v81, s[36:39], s52 offen sc1
mav 4,244,60
mav 8,248,60
ds_write_b32 v97, v74 offset:1024
mav 12,252,60
mvv 0,116,124
mvv 4,120,124
mvv 8,116,128
mvv 12,120,128
mvv 0,116,132
mvv 4,120,132
mvv 8,116,136
s_add_u32 s49, s49, 346112
mvv 12,120,136
s_waitcnt lgkmcnt(0)
s_barrier
mvv 0,116,140
R 32,32768
ds_read_b128 v[204:207], v98 offset:1024
mvv 4,120,140
buffer_load_dwordx4 v[172:175], v86, s[36:39], s49 offen sc1
mvv 8,116,144
mvv 12,120,144
s_waitcnt vmcnt(11)
mvv 0,116,148
R 36,33792
mvv 4,120,148
buffer_load_dwordx4 v[176:179], v86, s[36:39], s49 offen offset:1024 sc1
s_waitcnt vmcnt(11)
mvv 8,116,152
mvv 12,120,152
s_waitcnt vmcnt(10)
mvv 0,116,156
R 40,34816
mvv 4,120,156
s_waitcnt vmcnt(9)
mvv 8,116,160
buffer_load_dwordx4 v[180:183], v87, s[36:39], s49 offen sc1
mvv 12,120,160
s_waitcnt lgkmcnt(1)
v_mfma_f32_16x16x32_bf16 v[16:19], a[0:3], v[32:35], v[100:103]
R 44,35840
v_add_f32 v204, v204, v205
v_mfma_f32_16x16x32_bf16 v[20:23], a[4:7], v[32:35], v[104:107]
v_add_f32 v206, v206, v207
v_add_f32 v204, v204, v206
v_mfma_f32_16x16x32_bf16 v[24:27], a[8:11], v[32:35], v[108:111]
buffer_load_dwordx4 v[184:187], v87, s[36:39], s49 offen offset:1024 sc1
v_add_f32_dpp v205, v204, v204 quad_perm:[1,0,3,2] row_mask:0xf bank_mask:0xf
v_mfma_f32_16x16x32_bf16 v[28:31], a[12:15], v[32:35], v[112:115]
buffer_store_dword v205, v99, s[36:39], s49 offen sc1
v_cvt_pk_bf16_f32 v64, v0, v1
mav 16,16,36
R 48,36864
v_cvt_pk_bf16_f32 v65, v2, v3
mav 20,20,36
v_cvt_pk_bf16_f32 v66, v4, v5
v_cvt_pk_bf16_f32 v67, v6, v7
mav 24,24,36
ds_write_b128 v94, v[64:67] offset:0
mav 28,28,36
buffer_load_dwordx4 v[188:191], v88, s[36:39], s49 offen sc1
s_waitcnt lgkmcnt(2)
mav 16,32,40
R 52,37888
v_cvt_pk_bf16_f32 v68, v8, v9
mav 20,36,40
v_cvt_pk_bf16_f32 v69, v10, v11
v_cvt_pk_bf16_f32 v70, v12, v13
mav 24,40,40
v_cvt_pk_bf16_f32 v71, v14, v15
mav 28,44,40
ds_write_b128 v95, v[68:71] offset:0
mav 16,48,44
R 56,38912
buffer_load_dwordx4 v[192:195], v88, s[36:39], s49 offen offset:1024 sc1
mav 20,52,44
mav 24,56,44
s_waitcnt lgkmcnt(1)
ds_read_b128 v[64:67], v96 offset:0
mav 28,60,44
ds_read_b128 v[68:71], v96 offset:1024
mav 16,64,48
R 60,39936
buffer_load_dwordx4 v[196:199], v89, s[36:39], s49 offen sc1
mav 20,68,48
mav 24,72,48
mav 28,76,48
mav 16,80,52
LsR8:
s_cbranch_execz LsR12
R 32,40960
buffer_load_dwordx4 v[200:203], v89, s[36:39], s49 offen offset:1024 sc1
mav 20,84,52
mav 24,88,52
mav 28,92,52
s_add_u32 m0, s50, 32768
s_waitcnt lgkmcnt(1)
mav 16,96,56
R 36,41984
buffer_store_dwordx4 v[64:67], v92, s[44:47], s54 offen
mav 20,100,56
mav 24,104,56
mav 28,108,56
mav 16,112,60
R 40,43008
buffer_store_dwordx4 v[68:71], v93, s[44:47], s54 offen
mav 20,116,60
mav 24,120,60
mav 28,124,60
s_waitcnt lgkmcnt(1)
mav 16,128,32
R 44,44032
D 83,0
mav 20,132,32
mav 24,136,32
mav 28,140,32
mav 16,144,36
R 48,45056
D 83,1024
mav 20,148,36
v_add_u32 v72, 68, v72
mav 24,152,36
mav 28,156,36
s_waitcnt lgkmcnt(1)
mav 16,160,40
R 52,46080
D 83,2048
mav 20,164,40
s_sub_u32 s54, s54, 0x8000
mav 24,168,40
mav 28,172,40
mav 16,176,44
R 56,47104
D 83,3072
mav 20,180,44
mav 24,184,44
mav 28,188,44
s_waitcnt lgkmcnt(1)
)"
R"(
mav 16,192,48
R 60,48128
buffer_load_dwordx4 v[124:127], v90, s[36:39], s49 offen sc1
mav 20,196,48
mav 24,200,48
mav 28,204,48
mav 16,208,52
buffer_load_dwordx4 v[128:131], v90, s[36:39], s49 offen offset:1024 sc1
mav 20,212,52
mav 24,216,52
mav 28,220,52
s_waitcnt lgkmcnt(0)
mav 16,224,56
mav 20,228,56
mav 24,232,56
mav 28,236,56
mav 16,240,60
s_waitcnt vmcnt(12)
buffer_store_dwordx2 v[72:73], v81, s[36:39], s52 offen sc1
mav 20,244,60
mav 24,248,60
mav 28,252,60
mvv 16,116,164
mvv 20,120,164
mvv 24,116,168
s_xor_b32 s50, s50, 0x10000
mvv 28,120,168
mvv 16,116,172
mvv 20,120,172
v_xor_b32 v80, 0x10000, v80
mvv 24,116,176
s_add_u32 s49, s49, 346112
mvv 28,120,176
s_barrier
mvv 16,116,180
R 32,0
mvv 20,120,180
buffer_load_dwordx4 v[132:135], v86, s[36:39], s49 offen sc1
mvv 24,116,184
mvv 28,120,184
s_waitcnt vmcnt(13)
mvv 16,116,188
R 36,1024
mvv 20,120,188
buffer_load_dwordx4 v[136:139], v86, s[36:39], s49 offen offset:1024 sc1
s_waitcnt vmcnt(13)
mvv 24,116,192
mvv 28,120,192
s_waitcnt vmcnt(12)
mvv 16,116,196
R 40,2048
mvv 20,120,196
s_waitcnt vmcnt(11)
mvv 24,116,200
buffer_load_dwordx4 v[140:143], v87, s[36:39], s49 offen sc1
mvv 28,120,200
s_waitcnt lgkmcnt(1)
v_mfma_f32_16x16x32_bf16 v[0:3], a[0:3], v[32:35], v[100:103]
R 44,3072
v_cvt_pk_bf16_f32 v64, v16, v17
v_mfma_f32_16x16x32_bf16 v[4:7], a[4:7], v[32:35], v[104:107]
v_cvt_pk_bf16_f32 v65, v18, v19
v_cvt_pk_bf16_f32 v66, v20, v21
v_mfma_f32_16x16x32_bf16 v[8:11], a[8:11], v[32:35], v[108:111]
buffer_load_dwordx4 v[144:147], v87, s[36:39], s49 offen offset:1024 sc1
v_cvt_pk_bf16_f32 v67, v22, v23
v_mfma_f32_16x16x32_bf16 v[12:15], a[12:15], v[32:35], v[112:115]
ds_write_b128 v94, v[64:67] offset:0
mav 0,16,36
R 48,4096
v_cvt_pk_bf16_f32 v68, v24, v25
mav 4,20,36
v_cvt_pk_bf16_f32 v69, v26, v27
v_cvt_pk_bf16_f32 v70, v28, v29
mav 8,24,36
v_cvt_pk_bf16_f32 v71, v30, v31
mav 12,28,36
buffer_load_dwordx4 v[148:151], v88, s[36:39], s49 offen sc1
s_waitcnt lgkmcnt(2)
mav 0,32,40
R 52,5120
mav 4,36,40
ds_write_b128 v95, v[68:71] offset:0
mav 8,40,40
mav 12,44,40
buffer_load_dwordx4 v[152:155], v88, s[36:39], s49 offen offset:1024 sc1
mav 0,48,44
R 56,6144
mav 4,52,44
s_waitcnt lgkmcnt(1)
ds_read_b128 v[64:67], v96 offset:0
mav 8,56,44
ds_read_b128 v[68:71], v96 offset:1024
mav 12,60,44
mav 0,64,48
R 60,7168
buffer_load_dwordx4 v[156:159], v89, s[36:39], s49 offen sc1
mav 4,68,48
mav 8,72,48
mav 12,76,48
mav 0,80,52
R 32,8192
buffer_load_dwordx4 v[160:163], v89, s[36:39], s49 offen offset:1024 sc1
mav 4,84,52
mav 8,88,52
mav 12,92,52
s_add_u32 m0, s50, 0
s_waitcnt lgkmcnt(1)
mav 0,96,56
R 36,9216
buffer_store_dwordx4 v[64:67], v92, s[44:47], s54 offen
mav 4,100,56
mav 8,104,56
mav 12,108,56
mav 0,112,60
R 40,10240
buffer_store_dwordx4 v[68:71], v93, s[44:47], s54 offen
mav 4,116,60
mav 8,120,60
mav 12,124,60
s_waitcnt lgkmcnt(1)
mav 0,128,32
R 44,11264
D 83,0
mav 4,132,32
mav 8,136,32
mav 12,140,32
mav 0,144,36
R 48,12288
D 83,1024
mav 4,148,36
mav 8,152,36
mav 12,156,36
s_waitcnt lgkmcnt(1)
mav 0,160,40
R 52,13312
D 83,2048
mav 4,164,40
s_sub_u32 s54, s54, 0x8000
mav 8,168,40
mav 12,172,40
mav 0,176,44
R 56,14336
D 83,3072
mav 4,180,44
mav 8,184,44
mav 12,188,44
s_waitcnt lgkmcnt(1)
mav 0,192,48
R 60,15360
buffer_load_dwordx4 v[164:167], v90, s[36:39], s49 offen sc1
mav 4,196,48
mav 8,200,48
mav 12,204,48
mav 0,208,52
buffer_load_dwordx4 v[168:171], v90, s[36:39], s49 offen offset:1024 sc1
mav 4,212,52
mav 8,216,52
mav 12,220,52
s_waitcnt lgkmcnt(0)
mav 0,224,56
mav 4,228,56
mav 8,232,56
mav 12,236,56
mav 0,240,60
mav 4,244,60
mav 8,248,60
mav 12,252,60
s_waitcnt vmcnt(18)
mvv 0,116,124
mvv 4,120,124
s_waitcnt vmcnt(17)
mvv 8,116,128
mvv 12,120,128
s_waitcnt vmcnt(15)
mvv 0,116,132
mvv 4,120,132
s_waitcnt vmcnt(14)
mvv 8,116,136
s_add_u32 s49, s49, 346112
mvv 12,120,136
s_barrier
s_waitcnt vmcnt(13)
mvv 0,116,140
R 32,32768
mvv 4,120,140
buffer_load_dwordx4 v[172:175], v86, s[36:39], s49 offen sc1
s_waitcnt vmcnt(13)
mvv 8,116,144
mvv 12,120,144
s_waitcnt vmcnt(12)
mvv 0,116,148
R 36,33792
mvv 4,120,148
buffer_load_dwordx4 v[176:179], v86, s[36:39], s49 offen offset:1024 sc1
s_waitcnt vmcnt(12)
mvv 8,116,152
mvv 12,120,152
s_waitcnt vmcnt(11)
mvv 0,116,156
R 40,34816
mvv 4,120,156
s_waitcnt vmcnt(10)
mvv 8,116,160
buffer_load_dwordx4 v[180:183], v87, s[36:39], s49 offen sc1
mvv 12,120,160
s_waitcnt lgkmcnt(1)
v_mfma_f32_16x16x32_bf16 v[16:19], a[0:3], v[32:35], v[100:103]
R 44,35840
v_cvt_pk_bf16_f32 v64, v0, v1
v_mfma_f32_16x16x32_bf16 v[20:23], a[4:7], v[32:35], v[104:107]
v_cvt_pk_bf16_f32 v65, v2, v3
v_cvt_pk_bf16_f32 v66, v4, v5
v_mfma_f32_16x16x32_bf16 v[24:27], a[8:11], v[32:35], v[108:111]
buffer_load_dwordx4 v[184:187], v87, s[36:39], s49 offen offset:1024 sc1
v_cvt_pk_bf16_f32 v67, v6, v7
v_mfma_f32_16x16x32_bf16 v[28:31], a[12:15], v[32:35], v[112:115]
ds_write_b128 v94, v[64:67] offset:0
mav 16,16,36
R 48,36864
v_cvt_pk_bf16_f32 v68, v8, v9
mav 20,20,36
v_cvt_pk_bf16_f32 v69, v10, v11
v_cvt_pk_bf16_f32 v70, v12, v13
mav 24,24,36
v_cvt_pk_bf16_f32 v71, v14, v15
mav 28,28,36
buffer_load_dwordx4 v[188:191], v88, s[36:39], s49 offen sc1
s_waitcnt lgkmcnt(2)
mav 16,32,40
R 52,37888
mav 20,36,40
ds_write_b128 v95, v[68:71] offset:0
mav 24,40,40
mav 28,44,40
buffer_load_dwordx4 v[192:195], v88, s[36:39], s49 offen offset:1024 sc1
mav 16,48,44
R 56,38912
mav 20,52,44
s_waitcnt lgkmcnt(1)
ds_read_b128 v[64:67], v96 offset:0
mav 24,56,44
ds_read_b128 v[68:71], v96 offset:1024
mav 28,60,44
mav 16,64,48
R 60,39936
buffer_load_dwordx4 v[196:199], v89, s[36:39], s49 offen sc1
mav 20,68,48
mav 24,72,48
mav 28,76,48
mav 16,80,52
R 32,40960
buffer_load_dwordx4 v[200:203], v89, s[36:39], s49 offen offset:1024 sc1
mav 20,84,52
mav 24,88,52
mav 28,92,52
s_add_u32 m0, s50, 32768
s_waitcnt lgkmcnt(1)
mav 16,96,56
R 36,41984
buffer_store_dwordx4 v[64:67], v92, s[44:47], s54 offen
mav 20,100,56
mav 24,104,56
mav 28,108,56
mav 16,112,60
R 40,43008
buffer_store_dwordx4 v[68:71], v93, s[44:47], s54 offen
mav 20,116,60
mav 24,120,60
mav 28,124,60
s_waitcnt lgkmcnt(1)
mav 16,128,32
R 44,44032
D 83,0
mav 20,132,32
mav 24,136,32
mav 28,140,32
mav 16,144,36
R 48,45056
D 83,1024
mav 20,148,36
mav 24,152,36
mav 28,156,36
s_waitcnt lgkmcnt(1)
mav 16,160,40
R 52,46080
D 83,2048
mav 20,164,40
s_sub_u32 s54, s54, 0x8000
mav 24,168,40
mav 28,172,40
mav 16,176,44
R 56,47104
D 83,3072
mav 20,180,44
mav 24,184,44
mav 28,188,44
s_waitcnt lgkmcnt(1)
mav 16,192,48
R 60,48128
buffer_load_dwordx4 v[124:127], v90, s[36:39], s49 offen sc1
mav 20,196,48
mav 24,200,48
mav 28,204,48
mav 16,208,52
buffer_load_dwordx4 v[128:131], v90, s[36:39], s49 offen offset:1024 sc1
mav 20,212,52
mav 24,216,52
mav 28,220,52
s_waitcnt lgkmcnt(0)
mav 16,224,56
mav 20,228,56
mav 24,232,56
mav 28,236,56
mav 16,240,60
mav 20,244,60
mav 24,248,60
mav 28,252,60
s_waitcnt vmcnt(17)
mvv 16,116,164
mvv 20,120,164
s_waitcnt vmcnt(16)
mvv 24,116,168
s_xor_b32 s50, s50, 0x10000
mvv 28,120,168
s_waitcnt vmcnt(15)
mvv 16,116,172
mvv 20,120,172
v_xor_b32 v80, 0x10000, v80
s_waitcnt vmcnt(14)
mvv 24,116,176
s_add_u32 s49, s49, 346112
mvv 28,120,176
s_barrier
s_waitcnt vmcnt(13)
mvv 16,116,180
R 32,0
mvv 20,120,180
buffer_load_dwordx4 v[132:135], v86, s[36:39], s49 offen sc1
s_waitcnt vmcnt(13)
mvv 24,116,184
mvv 28,120,184
s_waitcnt vmcnt(12)
mvv 16,116,188
R 36,1024
mvv 20,120,188
buffer_load_dwordx4 v[136:139], v86, s[36:39], s49 offen offset:1024 sc1
s_waitcnt vmcnt(12)
mvv 24,116,192
mvv 28,120,192
s_waitcnt vmcnt(11)
mvv 16,116,196
R 40,2048
mvv 20,120,196
s_waitcnt vmcnt(10)
mvv 24,116,200
buffer_load_dwordx4 v[140:143], v87, s[36:39], s49 offen sc1
mvv 28,120,200
s_mov_b32 s60, -12
.p2align 6
LR1_2:
s_waitcnt lgkmcnt(1)
v_mfma_f32_16x16x32_bf16 v[0:3], a[0:3], v[32:35], v[100:103]
R 44,3072
v_cvt_pk_bf16_f32 v64, v16, v17
v_mfma_f32_16x16x32_bf16 v[4:7], a[4:7], v[32:35], v[104:107]
v_cvt_pk_bf16_f32 v65, v18, v19
v_cvt_pk_bf16_f32 v66, v20, v21
v_mfma_f32_16x16x32_bf16 v[8:11], a[8:11], v[32:35], v[108:111]
buffer_load_dwordx4 v[144:147], v87, s[36:39], s49 offen offset:1024 sc1
v_cvt_pk_bf16_f32 v67, v22, v23
v_mfma_f32_16x16x32_bf16 v[12:15], a[12:15], v[32:35], v[112:115]
ds_write_b128 v94, v[64:67] offset:0
mav 0,16,36
R 48,4096
v_cvt_pk_bf16_f32 v68, v24, v25
mav 4,20,36
v_cvt_pk_bf16_f32 v69, v26, v27
v_cvt_pk_bf16_f32 v70, v28, v29
mav 8,24,36
v_cvt_pk_bf16_f32 v71, v30, v31
mav 12,28,36
buffer_load_dwordx4 v[148:151], v88, s[36:39], s49 offen sc1
s_waitcnt lgkmcnt(2)
mav 0,32,40
R 52,5120
mav 4,36,40
ds_write_b128 v95, v[68:71] offset:0
mav 8,40,40
mav 12,44,40
buffer_load_dwordx4 v[152:155], v88, s[36:39], s49 offen offset:1024 sc1
mav 0,48,44
R 56,6144
mav 4,52,44
s_waitcnt lgkmcnt(1)
ds_read_b128 v[64:67], v96 offset:0
mav 8,56,44
ds_read_b128 v[68:71], v96 offset:1024
mav 12,60,44
mav 0,64,48
R 60,7168
buffer_load_dwordx4 v[156:159], v89, s[36:39], s49 offen sc1
mav 4,68,48
mav 8,72,48
mav 12,76,48
mav 0,80,52
R 32,8192
buffer_load_dwordx4 v[160:163], v89, s[36:39], s49 offen offset:1024 sc1
mav 4,84,52
mav 8,88,52
mav 12,92,52
s_add_u32 m0, s50, 0
s_waitcnt lgkmcnt(1)
mav 0,96,56
R 36,9216
buffer_store_dwordx4 v[64:67], v92, s[44:47], s54 offen
mav 4,100,56
mav 8,104,56
mav 12,108,56
mav 0,112,60
R 40,10240
buffer_store_dwordx4 v[68:71], v93, s[44:47], s54 offen
mav 4,116,60
mav 8,120,60
mav 12,124,60
s_waitcnt lgkmcnt(1)
mav 0,128,32
R 44,11264
D 83,0
mav 4,132,32
mav 8,136,32
mav 12,140,32
mav 0,144,36
R 48,12288
D 83,1024
mav 4,148,36
mav 8,152,36
mav 12,156,36
s_waitcnt lgkmcnt(1)
mav 0,160,40
R 52,13312
D 83,2048
mav 4,164,40
s_sub_u32 s54, s54, 0x8000
mav 8,168,40
mav 12,172,40
mav 0,176,44
R 56,14336
D 83,3072
mav 4,180,44
mav 8,184,44
mav 12,188,44
s_waitcnt lgkmcnt(1)
mav 0,192,48
R 60,15360
buffer_load_dwordx4 v[164:167], v90, s[36:39], s49 offen sc1
mav 4,196,48
mav 8,200,48
mav 12,204,48
mav 0,208,52
buffer_load_dwordx4 v[168:171], v90, s[36:39], s49 offen offset:1024 sc1
mav 4,212,52
mav 8,216,52
mav 12,220,52
s_waitcnt lgkmcnt(0)
mav 0,224,56
mav 4,228,56
mav 8,232,56
mav 12,236,56
mav 0,240,60
mav 4,244,60
mav 8,248,60
mav 12,252,60
s_waitcnt vmcnt(17)
mvv 0,116,124
mvv 4,120,124
s_waitcnt vmcnt(16)
mvv 8,116,128
mvv 12,120,128
s_waitcnt vmcnt(15)
mvv 0,116,132
mvv 4,120,132
s_waitcnt vmcnt(14)
mvv 8,116,136
s_add_u32 s49, s49, 346112
mvv 12,120,136
s_barrier
s_waitcnt vmcnt(13)
mvv 0,116,140
R 32,32768
mvv 4,120,140
buffer_load_dwordx4 v[172:175], v86, s[36:39], s49 offen sc1
s_waitcnt vmcnt(13)
mvv 8,116,144
mvv 12,120,144
s_waitcnt vmcnt(12)
mvv 0,116,148
R 36,33792
LsR9:
s_cbranch_execz LsR13
mvv 4,120,148
buffer_load_dwordx4 v[176:179], v86, s[36:39], s49 offen offset:1024 sc1
s_waitcnt vmcnt(12)
mvv 8,116,152
mvv 12,120,152
s_waitcnt vmcnt(11)
mvv 0,116,156
R 40,34816
mvv 4,120,156
s_waitcnt vmcnt(10)
mvv 8,116,160
buffer_load_dwordx4 v[180:183], v87, s[36:39], s49 offen sc1
mvv 12,120,160
s_waitcnt lgkmcnt(1)
v_mfma_f32_16x16x32_bf16 v[16:19], a[0:3], v[32:35], v[100:103]
R 44,35840
v_cvt_pk_bf16_f32 v64, v0, v1
v_mfma_f32_16x16x32_bf16 v[20:23], a[4:7], v[32:35], v[104:107]
v_cvt_pk_bf16_f32 v65, v2, v3
v_cvt_pk_bf16_f32 v66, v4, v5
v_mfma_f32_16x16x32_bf16 v[24:27], a[8:11], v[32:35], v[108:111]
buffer_load_dwordx4 v[184:187], v87, s[36:39], s49 offen offset:1024 sc1
v_cvt_pk_bf16_f32 v67, v6, v7
v_mfma_f32_16x16x32_bf16 v[28:31], a[12:15], v[32:35], v[112:115]
ds_write_b128 v94, v[64:67] offset:0
mav 16,16,36
R 48,36864
v_cvt_pk_bf16_f32 v68, v8, v9
mav 20,20,36
v_cvt_pk_bf16_f32 v69, v10, v11
v_cvt_pk_bf16_f32 v70, v12, v13
mav 24,24,36
v_cvt_pk_bf16_f32 v71, v14, v15
mav 28,28,36
buffer_load_dwordx4 v[188:191], v88, s[36:39], s49 offen sc1
s_waitcnt lgkmcnt(2)
mav 16,32,40
R 52,37888
mav 20,36,40
ds_write_b128 v95, v[68:71] offset:0
mav 24,40,40
mav 28,44,40
buffer_load_dwordx4 v[192:195], v88, s[36:39], s49 offen offset:1024 sc1
mav 16,48,44
R 56,38912
mav 20,52,44
s_waitcnt lgkmcnt(1)
ds_read_b128 v[64:67], v96 offset:0
mav 24,56,44
ds_read_b128 v[68:71], v96 offset:1024
mav 28,60,44
mav 16,64,48
R 60,39936
buffer_load_dwordx4 v[196:199], v89, s[36:39], s49 offen sc1
mav 20,68,48
mav 24,72,48
mav 28,76,48
mav 16,80,52
R 32,40960
buffer_load_dwordx4 v[200:203], v89, s[36:39], s49 offen offset:1024 sc1
mav 20,84,52
mav 24,88,52
mav 28,92,52
s_add_u32 m0, s50, 32768
s_waitcnt lgkmcnt(1)
mav 16,96,56
R 36,41984
buffer_store_dwordx4 v[64:67], v92, s[44:47], s54 offen
mav 20,100,56
mav 24,104,56
mav 28,108,56
mav 16,112,60
R 40,43008
buffer_store_dwordx4 v[68:71], v93, s[44:47], s54 offen
mav 20,116,60
mav 24,120,60
mav 28,124,60
s_waitcnt lgkmcnt(1)
mav 16,128,32
R 44,44032
D 83,0
mav 20,132,32
mav 24,136,32
mav 28,140,32
mav 16,144,36
R 48,45056
D 83,1024
mav 20,148,36
mav 24,152,36
mav 28,156,36
s_waitcnt lgkmcnt(1)
mav 16,160,40
R 52,46080
D 83,2048
mav 20,164,40
s_sub_u32 s54, s54, 0x8000
mav 24,168,40
mav 28,172,40
mav 16,176,44
R 56,47104
D 83,3072
mav 20,180,44
mav 24,184,44
mav 28,188,44
s_waitcnt lgkmcnt(1)
mav 16,192,48
R 60,48128
buffer_load_dwordx4 v[124:127], v90, s[36:39], s49 offen sc1
mav 20,196,48
mav 24,200,48
mav 28,204,48
mav 16,208,52
buffer_load_dwordx4 v[128:131], v90, s[36:39], s49 offen offset:1024 sc1
mav 20,212,52
mav 24,216,52
mav 28,220,52
s_waitcnt lgkmcnt(0)
mav 16,224,56
mav 20,228,56
mav 24,232,56
mav 28,236,56
mav 16,240,60
mav 20,244,60
mav 24,248,60
mav 28,252,60
s_waitcnt vmcnt(17)
mvv 16,116,164
mvv 20,120,164
s_waitcnt vmcnt(16)
mvv 24,116,168
s_xor_b32 s50, s50, 0x10000
mvv 28,120,168
s_waitcnt vmcnt(15)
mvv 16,116,172
mvv 20,120,172
v_xor_b32 v80, 0x10000, v80
s_waitcnt vmcnt(14)
mvv 24,116,176
s_add_u32 s49, s49, 346112
mvv 28,120,176
s_barrier
s_waitcnt vmcnt(13)
mvv 16,116,180
R 32,0
mvv 20,120,180
buffer_load_dwordx4 v[132:135], v86, s[36:39], s49 offen sc1
s_waitcnt vmcnt(13)
mvv 24,116,184
mvv 28,120,184
s_waitcnt vmcnt(12)
mvv 16,116,188
R 36,1024
mvv 20,120,188
buffer_load_dwordx4 v[136:139], v86, s[36:39], s49 offen offset:1024 sc1
s_waitcnt vmcnt(12)
mvv 24,116,192
mvv 28,120,192
s_waitcnt vmcnt(11)
mvv 16,116,196
R 40,2048
mvv 20,120,196
s_waitcnt vmcnt(10)
mvv 24,116,200
buffer_load_dwordx4 v[140:143], v87, s[36:39], s49 offen sc1
mvv 28,120,200
s_add_u32 s60, s60, 1
s_cbranch_scc0 LR1_2
s_waitcnt lgkmcnt(1)
v_mfma_f32_16x16x32_bf16 v[0:3], a[0:3], v[32:35], v[100:103]
R 44,3072
v_cvt_pk_bf16_f32 v64, v16, v17
v_mfma_f32_16x16x32_bf16 v[4:7], a[4:7], v[32:35], v[104:107]
v_cvt_pk_bf16_f32 v65, v18, v19
v_cvt_pk_bf16_f32 v66, v20, v21
v_mfma_f32_16x16x32_bf16 v[8:11], a[8:11], v[32:35], v[108:111]
buffer_load_dwordx4 v[144:147], v87, s[36:39], s49 offen offset:1024 sc1
v_cvt_pk_bf16_f32 v67, v22, v23
v_mfma_f32_16x16x32_bf16 v[12:15], a[12:15], v[32:35], v[112:115]
ds_write_b128 v94, v[64:67] offset:0
mav 0,16,36
R 48,4096
v_cvt_pk_bf16_f32 v68, v24, v25
mav 4,20,36
v_cvt_pk_bf16_f32 v69, v26, v27
v_cvt_pk_bf16_f32 v70, v28, v29
mav 8,24,36
v_cvt_pk_bf16_f32 v71, v30, v31
mav 12,28,36
buffer_load_dwordx4 v[148:151], v88, s[36:39], s49 offen sc1
s_waitcnt lgkmcnt(2)
mav 0,32,40
R 52,5120
mav 4,36,40
ds_write_b128 v95, v[68:71] offset:0
mav 8,40,40
mav 12,44,40
buffer_load_dwordx4 v[152:155], v88, s[36:39], s49 offen offset:1024 sc1
mav 0,48,44
R 56,6144
mav 4,52,44
s_waitcnt lgkmcnt(1)
ds_read_b128 v[64:67], v96 offset:0
mav 8,56,44
ds_read_b128 v[68:71], v96 offset:1024
mav 12,60,44
mav 0,64,48
R 60,7168
buffer_load_dwordx4 v[156:159], v89, s[36:39], s49 offen sc1
mav 4,68,48
mav 8,72,48
mav 12,76,48
mav 0,80,52
R 32,8192
buffer_load_dwordx4 v[160:163], v89, s[36:39], s49 offen offset:1024 sc1
mav 4,84,52
mav 8,88,52
mav 12,92,52
s_add_u32 m0, s50, 0
s_waitcnt lgkmcnt(1)
mav 0,96,56
R 36,9216
buffer_store_dwordx4 v[64:67], v92, s[44:47], s54 offen
mav 4,100,56
mav 8,104,56
mav 12,108,56
mav 0,112,60
R 40,10240
buffer_store_dwordx4 v[68:71], v93, s[44:47], s54 offen
mav 4,116,60
mav 8,120,60
mav 12,124,60
s_waitcnt lgkmcnt(1)
mav 0,128,32
R 44,11264
D 83,0
mav 4,132,32
mav 8,136,32
mav 12,140,32
mav 0,144,36
R 48,12288
D 83,1024
mav 4,148,36
mav 8,152,36
mav 12,156,36
s_waitcnt lgkmcnt(1)
mav 0,160,40
R 52,13312
D 83,2048
mav 4,164,40
s_sub_u32 s54, s54, 0x8000
mav 8,168,40
mav 12,172,40
mav 0,176,44
R 56,14336
D 83,3072
mav 4,180,44
mav 8,184,44
mav 12,188,44
s_waitcnt lgkmcnt(1)
mav 0,192,48
R 60,15360
buffer_load_dwordx4 v[164:167], v90, s[36:39], s49 offen sc1
mav 4,196,48
mav 8,200,48
mav 12,204,48
mav 0,208,52
buffer_load_dwordx4 v[168:171], v90, s[36:39], s49 offen offset:1024 sc1
mav 4,212,52
mav 8,216,52
mav 12,220,52
s_waitcnt lgkmcnt(0)
mav 0,224,56
mav 4,228,56
mav 8,232,56
mav 12,236,56
mav 0,240,60
mav 4,244,60
mav 8,248,60
mav 12,252,60
s_waitcnt vmcnt(17)
mvv 0,116,124
mvv 4,120,124
s_waitcnt vmcnt(16)
mvv 8,116,128
mvv 12,120,128
s_waitcnt vmcnt(15)
mvv 0,116,132
mvv 4,120,132
s_waitcnt vmcnt(14)
mvv 8,116,136
s_add_u32 s49, s49, 346112
mvv 12,120,136
s_barrier
s_waitcnt vmcnt(13)
mvv 0,116,140
R 32,32768
mvv 4,120,140
buffer_load_dwordx4 v[172:175], v86, s[36:39], s49 offen sc1
s_waitcnt vmcnt(13)
mvv 8,116,144
mvv 12,120,144
s_waitcnt vmcnt(12)
mvv 0,116,148
R 36,33792
mvv 4,120,148
buffer_load_dwordx4 v[176:179], v86, s[36:39], s49 offen offset:1024 sc1
s_waitcnt vmcnt(12)
mvv 8,116,152
mvv 12,120,152
s_waitcnt vmcnt(11)
mvv 0,116,156
R 40,34816
mvv 4,120,156
s_waitcnt vmcnt(10)
mvv 8,116,160
buffer_load_dwordx4 v[180:183], v87, s[36:39], s49 offen sc1
mvv 12,120,160
s_waitcnt lgkmcnt(1)
v_mfma_f32_16x16x32_bf16 v[16:19], a[0:3], v[32:35], v[100:103]
R 44,35840
v_cvt_pk_bf16_f32 v64, v0, v1
v_mfma_f32_16x16x32_bf16 v[20:23], a[4:7], v[32:35], v[104:107]
v_cvt_pk_bf16_f32 v65, v2, v3
v_cvt_pk_bf16_f32 v66, v4, v5
v_mfma_f32_16x16x32_bf16 v[24:27], a[8:11], v[32:35], v[108:111]
buffer_load_dwordx4 v[184:187], v87, s[36:39], s49 offen offset:1024 sc1
v_cvt_pk_bf16_f32 v67, v6, v7
v_mfma_f32_16x16x32_bf16 v[28:31], a[12:15], v[32:35], v[112:115]
ds_write_b128 v94, v[64:67] offset:0
mav 16,16,36
R 48,36864
v_cvt_pk_bf16_f32 v68, v8, v9
mav 20,20,36
v_cvt_pk_bf16_f32 v69, v10, v11
v_cvt_pk_bf16_f32 v70, v12, v13
mav 24,24,36
v_cvt_pk_bf16_f32 v71, v14, v15
mav 28,28,36
buffer_load_dwordx4 v[188:191], v88, s[36:39], s49 offen sc1
s_waitcnt lgkmcnt(2)
mav 16,32,40
R 52,37888
mav 20,36,40
ds_write_b128 v95, v[68:71] offset:0
mav 24,40,40
mav 28,44,40
buffer_load_dwordx4 v[192:195], v88, s[36:39], s49 offen offset:1024 sc1
mav 16,48,44
R 56,38912
mav 20,52,44
s_waitcnt lgkmcnt(1)
ds_read_b128 v[64:67], v96 offset:0
mav 24,56,44
ds_read_b128 v[68:71], v96 offset:1024
mav 28,60,44
mav 16,64,48
R 60,39936
buffer_load_dwordx4 v[196:199], v89, s[36:39], s49 offen sc1
mav 20,68,48
mav 24,72,48
mav 28,76,48
mav 16,80,52
R 32,40960
buffer_load_dwordx4 v[200:203], v89, s[36:39], s49 offen offset:1024 sc1
mav 20,84,52
mav 24,88,52
mav 28,92,52
s_add_u32 m0, s50, 32768
s_waitcnt lgkmcnt(1)
mav 16,96,56
R 36,41984
buffer_store_dwordx4 v[64:67], v92, s[44:47], s54 offen
mav 20,100,56
mav 24,104,56
mav 28,108,56
mav 16,112,60
R 40,43008
buffer_store_dwordx4 v[68:71], v93, s[44:47], s54 offen
mav 20,116,60
mav 24,120,60
mav 28,124,60
s_waitcnt lgkmcnt(1)
mav 16,128,32
R 44,44032
D 83,0
mav 20,132,32
mav 24,136,32
mav 28,140,32
mav 16,144,36
R 48,45056
D 83,1024
mav 20,148,36
mav 24,152,36
mav 28,156,36
s_waitcnt lgkmcnt(1)
mav 16,160,40
R 52,46080
D 83,2048
mav 20,164,40
s_sub_u32 s54, s54, 0x8000
mav 24,168,40
mav 28,172,40
mav 16,176,44
R 56,47104
D 83,3072
mav 20,180,44
mav 24,184,44
mav 28,188,44
s_waitcnt lgkmcnt(1)
mav 16,192,48
R 60,48128
buffer_load_dwordx4 v[124:127], v90, s[36:39], s49 offen sc1
mav 20,196,48
mav 24,200,48
mav 28,204,48
mav 16,208,52
buffer_load_dwordx4 v[128:131], v90, s[36:39], s49 offen offset:1024 sc1
mav 20,212,52
mav 24,216,52
mav 28,220,52
s_waitcnt lgkmcnt(0)
mav 16,224,56
mav 20,228,56
mav 24,232,56
mav 28,236,56
mav 16,240,60
mav 20,244,60
mav 24,248,60
mav 28,252,60
s_waitcnt vmcnt(17)
mvv 16,116,164
mvv 20,120,164
s_waitcnt vmcnt(16)
mvv 24,116,168
mvv 28,120,168
s_waitcnt vmcnt(15)
mvv 16,116,172
mvv 20,120,172
v_xor_b32 v80, 0x10000, v80
s_waitcnt vmcnt(14)
mvv 24,116,176
s_add_u32 s49, s49, 346112
mvv 28,120,176
s_barrier
s_waitcnt vmcnt(13)
mvv 16,116,180
R 32,0
mvv 20,120,180
buffer_load_dwordx4 v[132:135], v86, s[36:39], s49 offen sc1
s_waitcnt vmcnt(13)
mvv 24,116,184
mvv 28,120,184
s_waitcnt vmcnt(12)
mvv 16,116,188
R 36,1024
mvv 20,120,188
buffer_load_dwordx4 v[136:139], v86, s[36:39], s49 offen offset:1024 sc1
s_waitcnt vmcnt(12)
mvv 24,116,192
mvv 28,120,192
s_waitcnt vmcnt(11)
mvv 16,116,196
R 40,2048
mvv 20,120,196
s_waitcnt vmcnt(10)
mvv 24,116,200
buffer_load_dwordx4 v[140:143], v87, s[36:39], s49 offen sc1
mvv 28,120,200
s_waitcnt lgkmcnt(1)
v_mfma_f32_16x16x32_bf16 v[0:3], a[0:3], v[32:35], v[100:103]
R 44,3072
v_cvt_pk_bf16_f32 v64, v16, v17
v_mfma_f32_16x16x32_bf16 v[4:7], a[4:7], v[32:35], v[104:107]
v_cvt_pk_bf16_f32 v65, v18, v19
v_cvt_pk_bf16_f32 v66, v20, v21
v_mfma_f32_16x16x32_bf16 v[8:11], a[8:11], v[32:35], v[108:111]
buffer_load_dwordx4 v[144:147], v87, s[36:39], s49 offen offset:1024 sc1
v_cvt_pk_bf16_f32 v67, v22, v23
v_mfma_f32_16x16x32_bf16 v[12:15], a[12:15], v[32:35], v[112:115]
ds_write_b128 v94, v[64:67] offset:0
mav 0,16,36
R 48,4096
v_cvt_pk_bf16_f32 v68, v24, v25
mav 4,20,36
v_cvt_pk_bf16_f32 v69, v26, v27
v_cvt_pk_bf16_f32 v70, v28, v29
mav 8,24,36
v_cvt_pk_bf16_f32 v71, v30, v31
mav 12,28,36
buffer_load_dwordx4 v[148:151], v88, s[36:39], s49 offen sc1
s_waitcnt lgkmcnt(2)
mav 0,32,40
R 52,5120
mav 4,36,40
ds_write_b128 v95, v[68:71] offset:0
mav 8,40,40
mav 12,44,40
buffer_load_dwordx4 v[152:155], v88, s[36:39], s49 offen offset:1024 sc1
mav 0,48,44
R 56,6144
mav 4,52,44
s_waitcnt lgkmcnt(1)
ds_read_b128 v[64:67], v96 offset:0
mav 8,56,44
ds_read_b128 v[68:71], v96 offset:1024
mav 12,60,44
mav 0,64,48
R 60,7168
buffer_load_dwordx4 v[156:159], v89, s[36:39], s49 offen sc1
mav 4,68,48
mav 8,72,48
mav 12,76,48
mav 0,80,52
R 32,8192
buffer_load_dwordx4 v[160:163], v89, s[36:39], s49 offen offset:1024 sc1
mav 4,84,52
mav 8,88,52
mav 12,92,52
s_waitcnt lgkmcnt(1)
mav 0,96,56
R 36,9216
buffer_store_dwordx4 v[64:67], v92, s[44:47], s54 offen
mav 4,100,56
mav 8,104,56
mav 12,108,56
mav 0,112,60
R 40,10240
buffer_store_dwordx4 v[68:71], v93, s[44:47], s54 offen
mav 4,116,60
mav 8,120,60
mav 12,124,60
s_waitcnt lgkmcnt(1)
mav 0,128,32
R 44,11264
mav 4,132,32
mav 8,136,32
mav 12,140,32
mav 0,144,36
R 48,12288
mav 4,148,36
mav 8,152,36
mav 12,156,36
s_waitcnt lgkmcnt(1)
mav 0,160,40
R 52,13312
s_sub_u32 s54, s54, 0x8000
LsR10:
s_cbranch_execz LsR14
mav 4,164,40
mav 8,168,40
mav 12,172,40
buffer_load_dwordx4 v[164:167], v90, s[36:39], s49 offen sc1
mav 0,176,44
R 56,14336
mav 4,180,44
mav 8,184,44
mav 12,188,44
buffer_load_dwordx4 v[168:171], v90, s[36:39], s49 offen offset:1024 sc1
s_waitcnt lgkmcnt(1)
mav 0,192,48
R 60,15360
mav 4,196,48
mav 8,200,48
mav 12,204,48
mav 0,208,52
mav 4,212,52
mav 8,216,52
mav 12,220,52
s_waitcnt lgkmcnt(0)
mav 0,224,56
mav 4,228,56
mav 8,232,56
mav 12,236,56
mav 0,240,60
mav 4,244,60
mav 8,248,60
mav 12,252,60
s_waitcnt vmcnt(13)
mvv 0,116,124
mvv 4,120,124
s_waitcnt vmcnt(12)
mvv 8,116,128
mvv 12,120,128
s_waitcnt vmcnt(11)
mvv 0,116,132
mvv 4,120,132
s_waitcnt vmcnt(10)
mvv 8,116,136
s_add_u32 s49, s49, 346112
mvv 12,120,136
s_barrier
s_waitcnt vmcnt(9)
mvv 0,116,140
R 32,32768
mvv 4,120,140
buffer_load_dwordx4 v[172:175], v86, s[36:39], s49 offen sc1
s_waitcnt vmcnt(9)
mvv 8,116,144
mvv 12,120,144
s_waitcnt vmcnt(8)
mvv 0,116,148
R 36,33792
mvv 4,120,148
buffer_load_dwordx4 v[176:179], v86, s[36:39], s49 offen offset:1024 sc1
s_waitcnt vmcnt(8)
mvv 8,116,152
mvv 12,120,152
s_waitcnt vmcnt(7)
mvv 0,116,156
R 40,34816
mvv 4,120,156
s_waitcnt vmcnt(6)
mvv 8,116,160
buffer_load_dwordx4 v[180:183], v87, s[36:39], s49 offen sc1
mvv 12,120,160
s_waitcnt lgkmcnt(1)
v_mfma_f32_16x16x32_bf16 v[16:19], a[0:3], v[32:35], v[100:103]
R 44,35840
v_cvt_pk_bf16_f32 v64, v0, v1
v_mfma_f32_16x16x32_bf16 v[20:23], a[4:7], v[32:35], v[104:107]
v_cvt_pk_bf16_f32 v65, v2, v3
v_cvt_pk_bf16_f32 v66, v4, v5
v_mfma_f32_16x16x32_bf16 v[24:27], a[8:11], v[32:35], v[108:111]
buffer_load_dwordx4 v[184:187], v87, s[36:39], s49 offen offset:1024 sc1
v_cvt_pk_bf16_f32 v67, v6, v7
v_mfma_f32_16x16x32_bf16 v[28:31], a[12:15], v[32:35], v[112:115]
ds_write_b128 v94, v[64:67] offset:0
mav 16,16,36
R 48,36864
v_cvt_pk_bf16_f32 v68, v8, v9
mav 20,20,36
v_cvt_pk_bf16_f32 v69, v10, v11
v_cvt_pk_bf16_f32 v70, v12, v13
mav 24,24,36
v_cvt_pk_bf16_f32 v71, v14, v15
mav 28,28,36
buffer_load_dwordx4 v[188:191], v88, s[36:39], s49 offen sc1
s_waitcnt lgkmcnt(2)
mav 16,32,40
R 52,37888
mav 20,36,40
ds_write_b128 v95, v[68:71] offset:0
mav 24,40,40
mav 28,44,40
buffer_load_dwordx4 v[192:195], v88, s[36:39], s49 offen offset:1024 sc1
mav 16,48,44
R 56,38912
mav 20,52,44
s_waitcnt lgkmcnt(1)
ds_read_b128 v[64:67], v96 offset:0
mav 24,56,44
ds_read_b128 v[68:71], v96 offset:1024
mav 28,60,44
mav 16,64,48
R 60,39936
buffer_load_dwordx4 v[196:199], v89, s[36:39], s49 offen sc1
mav 20,68,48
mav 24,72,48
mav 28,76,48
mav 16,80,52
R 32,40960
buffer_load_dwordx4 v[200:203], v89, s[36:39], s49 offen offset:1024 sc1
mav 20,84,52
mav 24,88,52
mav 28,92,52
s_waitcnt lgkmcnt(1)
mav 16,96,56
R 36,41984
buffer_store_dwordx4 v[64:67], v92, s[44:47], s54 offen
mav 20,100,56
mav 24,104,56
mav 28,108,56
mav 16,112,60
R 40,43008
buffer_store_dwordx4 v[68:71], v93, s[44:47], s54 offen
mav 20,116,60
mav 24,120,60
mav 28,124,60
s_waitcnt lgkmcnt(1)
mav 16,128,32
R 44,44032
mav 20,132,32
mav 24,136,32
mav 28,140,32
mav 16,144,36
R 48,45056
mav 20,148,36
mav 24,152,36
mav 28,156,36
s_waitcnt lgkmcnt(1)
mav 16,160,40
R 52,46080
s_sub_u32 s54, s54, 0x8000
mav 20,164,40
mav 24,168,40
mav 28,172,40
mav 16,176,44
R 56,47104
mav 20,180,44
mav 24,184,44
mav 28,188,44
s_waitcnt lgkmcnt(1)
mav 16,192,48
R 60,48128
mav 20,196,48
mav 24,200,48
mav 28,204,48
mav 16,208,52
mav 20,212,52
mav 24,216,52
mav 28,220,52
s_waitcnt lgkmcnt(0)
mav 16,224,56
mav 20,228,56
mav 24,232,56
mav 28,236,56
mav 16,240,60
mav 20,244,60
mav 24,248,60
mav 28,252,60
s_waitcnt vmcnt(11)
mvv 16,116,164
mvv 20,120,164
s_waitcnt vmcnt(10)
mvv 24,116,168
mvv 28,120,168
s_waitcnt vmcnt(9)
mvv 16,116,172
mvv 20,120,172
s_waitcnt vmcnt(8)
mvv 24,116,176
mvv 28,120,176
s_waitcnt vmcnt(7)
mvv 16,116,180
mvv 20,120,180
s_waitcnt vmcnt(6)
mvv 24,116,184
mvv 28,120,184
s_waitcnt vmcnt(5)
mvv 16,116,188
mvv 20,120,188
s_waitcnt vmcnt(4)
mvv 24,116,192
mvv 28,120,192
s_waitcnt vmcnt(3)
mvv 16,116,196
mvv 20,120,196
s_waitcnt vmcnt(2)
mvv 24,116,200
mvv 28,120,200
s_nop 3
v_cvt_pk_bf16_f32 v64, v16, v17
v_cvt_pk_bf16_f32 v65, v18, v19
v_cvt_pk_bf16_f32 v66, v20, v21
v_cvt_pk_bf16_f32 v67, v22, v23
ds_write_b128 v94, v[64:67] offset:0
v_cvt_pk_bf16_f32 v68, v24, v25
v_cvt_pk_bf16_f32 v69, v26, v27
v_cvt_pk_bf16_f32 v70, v28, v29
v_cvt_pk_bf16_f32 v71, v30, v31
ds_write_b128 v95, v[68:71] offset:0
s_waitcnt lgkmcnt(0)
ds_read_b128 v[64:67], v96 offset:0
ds_read_b128 v[68:71], v96 offset:1024
s_waitcnt lgkmcnt(0)
buffer_store_dwordx4 v[64:67], v92, s[44:47], s54 offen
buffer_store_dwordx4 v[68:71], v93, s[44:47], s54 offen
s_endpgm
LvR2:
LsR11:
s_cbranch_execz LwR
s_add_u32 s49, s49, 346112
s_barrier
s_add_u32 m0, s50, 0
s_nop 0
D 83,0
D 83,1024
D 83,2048
D 83,3072
s_add_u32 s49, s49, 346112
s_barrier
s_add_u32 m0, s50, 32768
s_nop 0
D 83,0
D 83,1024
D 83,2048
D 83,3072
buffer_load_dwordx4 v[124:127], v90, s[36:39], s49 offen sc1
buffer_load_dwordx4 v[128:131], v90, s[36:39], s49 offen offset:1024 sc1
s_xor_b32 s50, s50, 0x10000
s_add_u32 s49, s49, 346112
s_waitcnt vmcnt(6)
s_barrier
R 32,0
buffer_load_dwordx4 v[132:135], v86, s[36:39], s49 offen sc1
R 36,1024
buffer_load_dwordx4 v[136:139], v86, s[36:39], s49 offen offset:1024 sc1
R 40,2048
buffer_load_dwordx4 v[140:143], v87, s[36:39], s49 offen sc1
s_waitcnt lgkmcnt(1)
v_mfma_f32_16x16x32_bf16 v[0:3], a[0:3], v[32:35], v[100:103]
R 44,3072
v_mfma_f32_16x16x32_bf16 v[4:7], a[4:7], v[32:35], v[104:107]
v_mfma_f32_16x16x32_bf16 v[8:11], a[8:11], v[32:35], v[108:111]
buffer_load_dwordx4 v[144:147], v87, s[36:39], s49 offen offset:1024 sc1
v_mfma_f32_16x16x32_bf16 v[12:15], a[12:15], v[32:35], v[112:115]
mav 0,16,36
R 48,4096
mav 4,20,36
mav 8,24,36
mav 12,28,36
buffer_load_dwordx4 v[148:151], v88, s[36:39], s49 offen sc1
s_waitcnt lgkmcnt(1)
mav 0,32,40
R 52,5120
mav 4,36,40
mav 8,40,40
mav 12,44,40
buffer_load_dwordx4 v[152:155], v88, s[36:39], s49 offen offset:1024 sc1
mav 0,48,44
R 56,6144
mav 4,52,44
mav 8,56,44
mav 12,60,44
s_waitcnt lgkmcnt(1)
mav 0,64,48
R 60,7168
buffer_load_dwordx4 v[156:159], v89, s[36:39], s49 offen sc1
mav 4,68,48
mav 8,72,48
mav 12,76,48
mav 0,80,52
R 32,8192
buffer_load_dwordx4 v[160:163], v89, s[36:39], s49 offen offset:1024 sc1
mav 4,84,52
mav 8,88,52
mav 12,92,52
s_add_u32 m0, s50, 0
s_waitcnt lgkmcnt(1)
mav 0,96,56
R 36,9216
mav 4,100,56
D 83,0
mav 8,104,56
mav 12,108,56
mav 0,112,60
R 40,10240
mav 4,116,60
D 83,1024
mav 8,120,60
mav 12,124,60
s_waitcnt lgkmcnt(1)
mav 0,128,32
R 44,11264
mav 4,132,32
mav 8,136,32
D 83,2048
mav 12,140,32
mav 0,144,36
R 48,12288
mav 4,148,36
mav 8,152,36
D 83,3072
mav 12,156,36
s_waitcnt lgkmcnt(1)
mav 0,160,40
R 52,13312
mav 4,164,40
mav 8,168,40
mav 12,172,40
buffer_load_dwordx4 v[164:167], v90, s[36:39], s49 offen sc1
mav 0,176,44
R 56,14336
mav 4,180,44
mav 8,184,44
mav 12,188,44
buffer_load_dwordx4 v[168:171], v90, s[36:39], s49 offen offset:1024 sc1
s_waitcnt lgkmcnt(1)
mav 0,192,48
R 60,15360
mav 4,196,48
mav 8,200,48
mav 12,204,48
mav 0,208,52
mav 4,212,52
mav 8,216,52
mav 12,220,52
s_waitcnt lgkmcnt(0)
mav 0,224,56
mav 4,228,56
mav 8,232,56
mav 12,236,56
mav 0,240,60
mav 4,244,60
mav 8,248,60
mav 12,252,60
s_waitcnt vmcnt(15)
mvv 0,116,124
mvv 4,120,124
s_waitcnt vmcnt(14)
mvv 8,116,128
mvv 12,120,128
s_waitcnt vmcnt(13)
mvv 0,116,132
mvv 4,120,132
s_waitcnt vmcnt(12)
mvv 8,116,136
s_add_u32 s49, s49, 346112
mvv 12,120,136
s_barrier
s_waitcnt vmcnt(11)
mvv 0,116,140
R 32,32768
mvv 4,120,140
buffer_load_dwordx4 v[172:175], v86, s[36:39], s49 offen sc1
s_waitcnt vmcnt(11)
mvv 8,116,144
mvv 12,120,144
s_waitcnt vmcnt(10)
mvv 0,116,148
R 36,33792
mvv 4,120,148
buffer_load_dwordx4 v[176:179], v86, s[36:39], s49 offen offset:1024 sc1
s_waitcnt vmcnt(10)
mvv 8,116,152
mvv 12,120,152
s_waitcnt vmcnt(9)
mvv 0,116,156
R 40,34816
mvv 4,120,156
s_waitcnt vmcnt(8)
mvv 8,116,160
buffer_load_dwordx4 v[180:183], v87, s[36:39], s49 offen sc1
mvv 12,120,160
s_waitcnt lgkmcnt(1)
v_mfma_f32_16x16x32_bf16 v[16:19], a[0:3], v[32:35], v[100:103]
R 44,35840
v_cvt_pk_bf16_f32 v64, v0, v1
v_mfma_f32_16x16x32_bf16 v[20:23], a[4:7], v[32:35], v[104:107]
v_cvt_pk_bf16_f32 v65, v2, v3
v_cvt_pk_bf16_f32 v66, v4, v5
v_mfma_f32_16x16x32_bf16 v[24:27], a[8:11], v[32:35], v[108:111]
buffer_load_dwordx4 v[184:187], v87, s[36:39], s49 offen offset:1024 sc1
v_cvt_pk_bf16_f32 v67, v6, v7
v_mfma_f32_16x16x32_bf16 v[28:31], a[12:15], v[32:35], v[112:115]
v_cvt_pk_bf16_f32 v68, v8, v9
v_cvt_pk_bf16_f32 v69, v10, v11
mav 16,16,36
R 48,36864
v_cvt_pk_bf16_f32 v70, v12, v13
mav 20,20,36
v_cvt_pk_bf16_f32 v71, v14, v15
v_mul_f32 v75, v0, v0
mav 24,24,36
buffer_store_dwordx4 v[64:67], v91, s[36:39], s49 offen sc1
v_add_f32 v74, v1, v0
mav 28,28,36
v_fmac_f32 v75, v1, v1
s_waitcnt lgkmcnt(1)
mav 16,32,40
R 52,37888
v_add_f32 v74, v2, v74
mav 20,36,40
v_fmac_f32 v75, v2, v2
v_add_f32 v74, v3, v74
mav 24,40,40
buffer_store_dwordx4 v[68:71], v91, s[36:39], s49 offen offset:1024 sc1
v_fmac_f32 v75, v3, v3
mav 28,44,40
v_add_f32 v74, v4, v74
v_fmac_f32 v75, v4, v4
mav 16,48,44
R 56,38912
v_add_f32 v74, v5, v74
mav 20,52,44
v_fmac_f32 v75, v5, v5
v_add_f32 v74, v6, v74
mav 24,56,44
buffer_load_dwordx4 v[188:191], v88, s[36:39], s49 offen sc1
v_fmac_f32 v75, v6, v6
mav 28,60,44
v_add_f32 v74, v7, v74
s_waitcnt lgkmcnt(1)
mav 16,64,48
R 60,39936
v_fmac_f32 v75, v7, v7
mav 20,68,48
v_add_f32 v74, v8, v74
v_fmac_f32 v75, v8, v8
mav 24,72,48
buffer_load_dwordx4 v[192:195], v88, s[36:39], s49 offen offset:1024 sc1
v_add_f32 v74, v9, v74
mav 28,76,48
v_fmac_f32 v75, v9, v9
v_add_f32 v74, v10, v74
mav 16,80,52
R 32,40960
v_fmac_f32 v75, v10, v10
mav 20,84,52
v_add_f32 v74, v11, v74
v_fmac_f32 v75, v11, v11
mav 24,88,52
buffer_load_dwordx4 v[196:199], v89, s[36:39], s49 offen sc1
v_add_f32 v74, v12, v74
mav 28,92,52
s_add_u32 m0, s50, 32768
s_waitcnt lgkmcnt(1)
mav 16,96,56
R 36,41984
v_fmac_f32 v75, v12, v12
mav 20,100,56
v_add_f32 v74, v13, v74
v_fmac_f32 v75, v13, v13
mav 24,104,56
buffer_load_dwordx4 v[200:203], v89, s[36:39], s49 offen offset:1024 sc1
v_add_f32 v74, v14, v74
mav 28,108,56
v_fmac_f32 v75, v14, v14
v_add_f32 v74, v15, v74
mav 16,112,60
R 40,43008
v_fmac_f32 v75, v15, v15
mav 20,116,60
mav 24,120,60
D 83,0
v_permlane32_swap_b32 v74, v75
mav 28,124,60
v_add_f32 v74, v74, v75
s_waitcnt lgkmcnt(1)
mav 16,128,32
R 44,44032
mav 20,132,32
mav 24,136,32
D 83,1024
mav 28,140,32
mav 16,144,36
R 48,45056
v_add_u32 v72, 1, v72
mav 20,148,36
mav 24,152,36
D 83,2048
mav 28,156,36
s_waitcnt lgkmcnt(1)
mav 16,160,40
R 52,46080
mav 20,164,40
mav 24,168,40
D 83,3072
mav 28,172,40
mav 16,176,44
R 56,47104
mav 20,180,44
mav 24,184,44
buffer_load_dwordx4 v[124:127], v90, s[36:39], s49 offen sc1
mav 28,188,44
s_waitcnt lgkmcnt(1)
mav 16,192,48
R 60,48128
mav 20,196,48
mav 24,200,48
buffer_load_dwordx4 v[128:131], v90, s[36:39], s49 offen offset:1024 sc1
mav 28,204,48
mav 16,208,52
mav 20,212,52
mav 24,216,52
mav 28,220,52
s_waitcnt lgkmcnt(0)
mav 16,224,56
mav 20,228,56
mav 24,232,56
mav 28,236,56
mav 16,240,60
s_waitcnt vmcnt(10)
buffer_store_dwordx2 v[72:73], v81, s[36:39], s52 offen sc1
mav 20,244,60
mav 24,248,60
ds_write_b32 v97, v74 offset:0
mav 28,252,60
mvv 16,116,164
mvv 20,120,164
mvv 24,116,168
s_xor_b32 s50, s50, 0x10000
mvv 28,120,168
mvv 16,116,172
mvv 20,120,172
v_xor_b32 v80, 0x10000, v80
mvv 24,116,176
s_add_u32 s49, s49, 346112
mvv 28,120,176
s_waitcnt lgkmcnt(0)
s_barrier
mvv 16,116,180
R 32,0
mvv 20,120,180
buffer_load_dwordx4 v[132:135], v86, s[36:39], s49 offen sc1
mvv 24,116,184
mvv 28,120,184
s_waitcnt vmcnt(11)
mvv 16,116,188
R 36,1024
mvv 20,120,188
buffer_load_dwordx4 v[136:139], v86, s[36:39], s49 offen offset:1024 sc1
s_waitcnt vmcnt(11)
mvv 24,116,192
mvv 28,120,192
s_waitcnt vmcnt(10)
mvv 16,116,196
R 40,2048
mvv 20,120,196
s_waitcnt vmcnt(9)
mvv 24,116,200
buffer_load_dwordx4 v[140:143], v87, s[36:39], s49 offen sc1
mvv 28,120,200
s_mov_b32 s60, -15
.p2align 6
LR2_1:
LsR12:
s_cbranch_execz LwR
s_waitcnt lgkmcnt(1)
v_mfma_f32_16x16x32_bf16 v[0:3], a[0:3], v[32:35], v[100:103]
R 44,3072
v_cvt_pk_bf16_f32 v64, v16, v17
v_mfma_f32_16x16x32_bf16 v[4:7], a[4:7], v[32:35], v[104:107]
v_cvt_pk_bf16_f32 v65, v18, v19
v_cvt_pk_bf16_f32 v66, v20, v21
v_mfma_f32_16x16x32_bf16 v[8:11], a[8:11], v[32:35], v[108:111]
buffer_load_dwordx4 v[144:147], v87, s[36:39], s49 offen offset:1024 sc1
v_cvt_pk_bf16_f32 v67, v22, v23
v_mfma_f32_16x16x32_bf16 v[12:15], a[12:15], v[32:35], v[112:115]
v_cvt_pk_bf16_f32 v68, v24, v25
v_cvt_pk_bf16_f32 v69, v26, v27
mav 0,16,36
R 48,4096
v_cvt_pk_bf16_f32 v70, v28, v29
mav 4,20,36
v_cvt_pk_bf16_f32 v71, v30, v31
v_mul_f32 v75, v16, v16
mav 8,24,36
buffer_store_dwordx4 v[64:67], v91, s[36:39], s49 offen sc1
v_add_f32 v74, v17, v16
mav 12,28,36
v_fmac_f32 v75, v17, v17
s_waitcnt lgkmcnt(1)
mav 0,32,40
R 52,5120
v_add_f32 v74, v18, v74
mav 4,36,40
v_fmac_f32 v75, v18, v18
v_add_f32 v74, v19, v74
mav 8,40,40
buffer_store_dwordx4 v[68:71], v91, s[36:39], s49 offen offset:1024 sc1
v_fmac_f32 v75, v19, v19
mav 12,44,40
v_add_f32 v74, v20, v74
v_fmac_f32 v75, v20, v20
mav 0,48,44
R 56,6144
v_add_f32 v74, v21, v74
mav 4,52,44
v_fmac_f32 v75, v21, v21
v_add_f32 v74, v22, v74
mav 8,56,44
buffer_load_dwordx4 v[148:151], v88, s[36:39], s49 offen sc1
v_fmac_f32 v75, v22, v22
mav 12,60,44
v_add_f32 v74, v23, v74
s_waitcnt lgkmcnt(1)
mav 0,64,48
R 60,7168
v_fmac_f32 v75, v23, v23
mav 4,68,48
v_add_f32 v74, v24, v74
v_fmac_f32 v75, v24, v24
mav 8,72,48
buffer_load_dwordx4 v[152:155], v88, s[36:39], s49 offen offset:1024 sc1
v_add_f32 v74, v25, v74
mav 12,76,48
v_fmac_f32 v75, v25, v25
v_add_f32 v74, v26, v74
mav 0,80,52
R 32,8192
v_fmac_f32 v75, v26, v26
mav 4,84,52
v_add_f32 v74, v27, v74
v_fmac_f32 v75, v27, v27
mav 8,88,52
buffer_load_dwordx4 v[156:159], v89, s[36:39], s49 offen sc1
v_add_f32 v74, v28, v74
mav 12,92,52
s_add_u32 m0, s50, 0
s_waitcnt lgkmcnt(1)
mav 0,96,56
R 36,9216
v_fmac_f32 v75, v28, v28
mav 4,100,56
v_add_f32 v74, v29, v74
v_fmac_f32 v75, v29, v29
mav 8,104,56
buffer_load_dwordx4 v[160:163], v89, s[36:39], s49 offen offset:1024 sc1
v_add_f32 v74, v30, v74
mav 12,108,56
v_fmac_f32 v75, v30, v30
v_add_f32 v74, v31, v74
mav 0,112,60
R 40,10240
v_fmac_f32 v75, v31, v31
mav 4,116,60
mav 8,120,60
D 83,0
v_permlane32_swap_b32 v74, v75
mav 12,124,60
v_add_f32 v74, v74, v75
s_waitcnt lgkmcnt(1)
mav 0,128,32
R 44,11264
mav 4,132,32
mav 8,136,32
D 83,1024
mav 12,140,32
mav 0,144,36
R 48,12288
v_add_u32 v72, 1, v72
mav 4,148,36
mav 8,152,36
D 83,2048
mav 12,156,36
s_waitcnt lgkmcnt(1)
mav 0,160,40
R 52,13312
mav 4,164,40
mav 8,168,40
D 83,3072
mav 12,172,40
mav 0,176,44
R 56,14336
mav 4,180,44
mav 8,184,44
buffer_load_dwordx4 v[164:167], v90, s[36:39], s49 offen sc1
mav 12,188,44
s_waitcnt lgkmcnt(1)
mav 0,192,48
R 60,15360
mav 4,196,48
mav 8,200,48
buffer_load_dwordx4 v[168:171], v90, s[36:39], s49 offen offset:1024 sc1
mav 12,204,48
mav 0,208,52
mav 4,212,52
mav 8,216,52
mav 12,220,52
s_waitcnt lgkmcnt(0)
mav 0,224,56
mav 4,228,56
mav 8,232,56
mav 12,236,56
mav 0,240,60
s_waitcnt vmcnt(10)
buffer_store_dwordx2 v[72:73], v81, s[36:39], s52 offen sc1
mav 4,244,60
mav 8,248,60
ds_write_b32 v97, v74 offset:1024
mav 12,252,60
mvv 0,116,124
mvv 4,120,124
mvv 8,116,128
mvv 12,120,128
mvv 0,116,132
mvv 4,120,132
mvv 8,116,136
s_add_u32 s49, s49, 346112
mvv 12,120,136
s_waitcnt lgkmcnt(0)
s_barrier
mvv 0,116,140
R 32,32768
mvv 4,120,140
buffer_load_dwordx4 v[172:175], v86, s[36:39], s49 offen sc1
mvv 8,116,144
mvv 12,120,144
s_waitcnt vmcnt(11)
mvv 0,116,148
R 36,33792
mvv 4,120,148
buffer_load_dwordx4 v[176:179], v86, s[36:39], s49 offen offset:1024 sc1
s_waitcnt vmcnt(11)
mvv 8,116,152
mvv 12,120,152
s_waitcnt vmcnt(10)
mvv 0,116,156
R 40,34816
mvv 4,120,156
s_waitcnt vmcnt(9)
mvv 8,116,160
buffer_load_dwordx4 v[180:183], v87, s[36:39], s49 offen sc1
mvv 12,120,160
s_waitcnt lgkmcnt(1)
v_mfma_f32_16x16x32_bf16 v[16:19], a[0:3], v[32:35], v[100:103]
R 44,35840
v_cvt_pk_bf16_f32 v64, v0, v1
v_mfma_f32_16x16x32_bf16 v[20:23], a[4:7], v[32:35], v[104:107]
v_cvt_pk_bf16_f32 v65, v2, v3
v_cvt_pk_bf16_f32 v66, v4, v5
v_mfma_f32_16x16x32_bf16 v[24:27], a[8:11], v[32:35], v[108:111]
buffer_load_dwordx4 v[184:187], v87, s[36:39], s49 offen offset:1024 sc1
v_cvt_pk_bf16_f32 v67, v6, v7
v_mfma_f32_16x16x32_bf16 v[28:31], a[12:15], v[32:35], v[112:115]
v_cvt_pk_bf16_f32 v68, v8, v9
v_cvt_pk_bf16_f32 v69, v10, v11
mav 16,16,36
R 48,36864
v_cvt_pk_bf16_f32 v70, v12, v13
mav 20,20,36
v_cvt_pk_bf16_f32 v71, v14, v15
v_mul_f32 v75, v0, v0
mav 24,24,36
buffer_store_dwordx4 v[64:67], v91, s[36:39], s49 offen sc1
v_add_f32 v74, v1, v0
mav 28,28,36
v_fmac_f32 v75, v1, v1
s_waitcnt lgkmcnt(1)
mav 16,32,40
R 52,37888
v_add_f32 v74, v2, v74
mav 20,36,40
v_fmac_f32 v75, v2, v2
v_add_f32 v74, v3, v74
mav 24,40,40
buffer_store_dwordx4 v[68:71], v91, s[36:39], s49 offen offset:1024 sc1
v_fmac_f32 v75, v3, v3
mav 28,44,40
v_add_f32 v74, v4, v74
v_fmac_f32 v75, v4, v4
mav 16,48,44
R 56,38912
v_add_f32 v74, v5, v74
mav 20,52,44
v_fmac_f32 v75, v5, v5
v_add_f32 v74, v6, v74
mav 24,56,44
buffer_load_dwordx4 v[188:191], v88, s[36:39], s49 offen sc1
v_fmac_f32 v75, v6, v6
mav 28,60,44
v_add_f32 v74, v7, v74
s_waitcnt lgkmcnt(1)
mav 16,64,48
R 60,39936
v_fmac_f32 v75, v7, v7
mav 20,68,48
v_add_f32 v74, v8, v74
v_fmac_f32 v75, v8, v8
mav 24,72,48
buffer_load_dwordx4 v[192:195], v88, s[36:39], s49 offen offset:1024 sc1
v_add_f32 v74, v9, v74
mav 28,76,48
v_fmac_f32 v75, v9, v9
v_add_f32 v74, v10, v74
mav 16,80,52
R 32,40960
v_fmac_f32 v75, v10, v10
mav 20,84,52
v_add_f32 v74, v11, v74
v_fmac_f32 v75, v11, v11
mav 24,88,52
buffer_load_dwordx4 v[196:199], v89, s[36:39], s49 offen sc1
v_add_f32 v74, v12, v74
mav 28,92,52
s_add_u32 m0, s50, 32768
s_waitcnt lgkmcnt(1)
mav 16,96,56
R 36,41984
v_fmac_f32 v75, v12, v12
mav 20,100,56
v_add_f32 v74, v13, v74
v_fmac_f32 v75, v13, v13
mav 24,104,56
buffer_load_dwordx4 v[200:203], v89, s[36:39], s49 offen offset:1024 sc1
v_add_f32 v74, v14, v74
mav 28,108,56
v_fmac_f32 v75, v14, v14
v_add_f32 v74, v15, v74
mav 16,112,60
R 40,43008
v_fmac_f32 v75, v15, v15
mav 20,116,60
mav 24,120,60
D 83,0
v_permlane32_swap_b32 v74, v75
mav 28,124,60
v_add_f32 v74, v74, v75
s_waitcnt lgkmcnt(1)
mav 16,128,32
R 44,44032
mav 20,132,32
mav 24,136,32
D 83,1024
mav 28,140,32
mav 16,144,36
R 48,45056
v_add_u32 v72, 1, v72
mav 20,148,36
mav 24,152,36
D 83,2048
mav 28,156,36
s_waitcnt lgkmcnt(1)
mav 16,160,40
R 52,46080
mav 20,164,40
mav 24,168,40
D 83,3072
mav 28,172,40
mav 16,176,44
R 56,47104
mav 20,180,44
mav 24,184,44
buffer_load_dwordx4 v[124:127], v90, s[36:39], s49 offen sc1
mav 28,188,44
s_waitcnt lgkmcnt(1)
mav 16,192,48
R 60,48128
mav 20,196,48
mav 24,200,48
buffer_load_dwordx4 v[128:131], v90, s[36:39], s49 offen offset:1024 sc1
mav 28,204,48
mav 16,208,52
mav 20,212,52
mav 24,216,52
mav 28,220,52
s_waitcnt lgkmcnt(0)
mav 16,224,56
mav 20,228,56
mav 24,232,56
mav 28,236,56
mav 16,240,60
s_waitcnt vmcnt(10)
buffer_store_dwordx2 v[72:73], v81, s[36:39], s52 offen sc1
mav 20,244,60
mav 24,248,60
ds_write_b32 v97, v74 offset:0
mav 28,252,60
mvv 16,116,164
mvv 20,120,164
mvv 24,116,168
s_xor_b32 s50, s50, 0x10000
mvv 28,120,168
mvv 16,116,172
mvv 20,120,172
v_xor_b32 v80, 0x10000, v80
mvv 24,116,176
s_add_u32 s49, s49, 346112
mvv 28,120,176
s_waitcnt lgkmcnt(0)
s_barrier
mvv 16,116,180
R 32,0
mvv 20,120,180
buffer_load_dwordx4 v[132:135], v86, s[36:39], s49 offen sc1
mvv 24,116,184
mvv 28,120,184
s_waitcnt vmcnt(11)
mvv 16,116,188
R 36,1024
mvv 20,120,188
buffer_load_dwordx4 v[136:139], v86, s[36:39], s49 offen offset:1024 sc1
s_waitcnt vmcnt(11)
mvv 24,116,192
mvv 28,120,192
s_waitcnt vmcnt(10)
mvv 16,116,196
R 40,2048
mvv 20,120,196
s_waitcnt vmcnt(9)
mvv 24,116,200
buffer_load_dwordx4 v[140:143], v87, s[36:39], s49 offen sc1
mvv 28,120,200
s_add_u32 s60, s60, 1
s_cbranch_scc0 LR2_1
s_waitcnt lgkmcnt(1)
v_mfma_f32_16x16x32_bf16 v[0:3], a[0:3], v[32:35], v[100:103]
R 44,3072
v_cvt_pk_bf16_f32 v64, v16, v17
v_mfma_f32_16x16x32_bf16 v[4:7], a[4:7], v[32:35], v[104:107]
v_cvt_pk_bf16_f32 v65, v18, v19
v_cvt_pk_bf16_f32 v66, v20, v21
v_mfma_f32_16x16x32_bf16 v[8:11], a[8:11], v[32:35], v[108:111]
buffer_load_dwordx4 v[144:147], v87, s[36:39], s49 offen offset:1024 sc1
v_cvt_pk_bf16_f32 v67, v22, v23
v_mfma_f32_16x16x32_bf16 v[12:15], a[12:15], v[32:35], v[112:115]
v_cvt_pk_bf16_f32 v68, v24, v25
v_cvt_pk_bf16_f32 v69, v26, v27
mav 0,16,36
R 48,4096
v_cvt_pk_bf16_f32 v70, v28, v29
mav 4,20,36
v_cvt_pk_bf16_f32 v71, v30, v31
v_mul_f32 v75, v16, v16
mav 8,24,36
buffer_store_dwordx4 v[64:67], v91, s[36:39], s49 offen sc1
v_add_f32 v74, v17, v16
mav 12,28,36
v_fmac_f32 v75, v17, v17
)"
R"(
s_waitcnt lgkmcnt(1)
mav 0,32,40
R 52,5120
v_add_f32 v74, v18, v74
mav 4,36,40
v_fmac_f32 v75, v18, v18
v_add_f32 v74, v19, v74
mav 8,40,40
buffer_store_dwordx4 v[68:71], v91, s[36:39], s49 offen offset:1024 sc1
v_fmac_f32 v75, v19, v19
mav 12,44,40
v_add_f32 v74, v20, v74
v_fmac_f32 v75, v20, v20
mav 0,48,44
R 56,6144
v_add_f32 v74, v21, v74
mav 4,52,44
v_fmac_f32 v75, v21, v21
v_add_f32 v74, v22, v74
mav 8,56,44
buffer_load_dwordx4 v[148:151], v88, s[36:39], s49 offen sc1
v_fmac_f32 v75, v22, v22
mav 12,60,44
v_add_f32 v74, v23, v74
s_waitcnt lgkmcnt(1)
mav 0,64,48
R 60,7168
v_fmac_f32 v75, v23, v23
mav 4,68,48
v_add_f32 v74, v24, v74
v_fmac_f32 v75, v24, v24
mav 8,72,48
buffer_load_dwordx4 v[152:155], v88, s[36:39], s49 offen offset:1024 sc1
v_add_f32 v74, v25, v74
mav 12,76,48
v_fmac_f32 v75, v25, v25
v_add_f32 v74, v26, v74
mav 0,80,52
R 32,8192
v_fmac_f32 v75, v26, v26
mav 4,84,52
v_add_f32 v74, v27, v74
v_fmac_f32 v75, v27, v27
mav 8,88,52
buffer_load_dwordx4 v[156:159], v89, s[36:39], s49 offen sc1
v_add_f32 v74, v28, v74
mav 12,92,52
s_add_u32 m0, s50, 0
s_waitcnt lgkmcnt(1)
mav 0,96,56
R 36,9216
v_fmac_f32 v75, v28, v28
mav 4,100,56
v_add_f32 v74, v29, v74
v_fmac_f32 v75, v29, v29
mav 8,104,56
buffer_load_dwordx4 v[160:163], v89, s[36:39], s49 offen offset:1024 sc1
v_add_f32 v74, v30, v74
mav 12,108,56
v_fmac_f32 v75, v30, v30
v_add_f32 v74, v31, v74
mav 0,112,60
R 40,10240
v_fmac_f32 v75, v31, v31
mav 4,116,60
mav 8,120,60
D 83,0
v_permlane32_swap_b32 v74, v75
mav 12,124,60
v_add_f32 v74, v74, v75
s_waitcnt lgkmcnt(1)
mav 0,128,32
R 44,11264
mav 4,132,32
mav 8,136,32
D 83,1024
mav 12,140,32
mav 0,144,36
R 48,12288
v_add_u32 v72, 69, v72
mav 4,148,36
mav 8,152,36
D 83,2048
mav 12,156,36
s_waitcnt lgkmcnt(1)
mav 0,160,40
R 52,13312
mav 4,164,40
mav 8,168,40
D 83,3072
mav 12,172,40
mav 0,176,44
R 56,14336
mav 4,180,44
mav 8,184,44
buffer_load_dwordx4 v[164:167], v90, s[36:39], s49 offen sc1
mav 12,188,44
s_waitcnt lgkmcnt(1)
mav 0,192,48
R 60,15360
mav 4,196,48
mav 8,200,48
buffer_load_dwordx4 v[168:171], v90, s[36:39], s49 offen offset:1024 sc1
mav 12,204,48
mav 0,208,52
mav 4,212,52
mav 8,216,52
mav 12,220,52
s_waitcnt lgkmcnt(0)
mav 0,224,56
mav 4,228,56
mav 8,232,56
mav 12,236,56
mav 0,240,60
s_waitcnt vmcnt(10)
buffer_store_dwordx2 v[72:73], v81, s[36:39], s52 offen sc1
mav 4,244,60
mav 8,248,60
ds_write_b32 v97, v74 offset:1024
mav 12,252,60
mvv 0,116,124
mvv 4,120,124
mvv 8,116,128
mvv 12,120,128
mvv 0,116,132
mvv 4,120,132
mvv 8,116,136
s_add_u32 s49, s49, 346112
mvv 12,120,136
s_waitcnt lgkmcnt(0)
s_barrier
mvv 0,116,140
R 32,32768
mvv 4,120,140
buffer_load_dwordx4 v[172:175], v86, s[36:39], s49 offen sc1
mvv 8,116,144
mvv 12,120,144
s_waitcnt vmcnt(11)
mvv 0,116,148
R 36,33792
mvv 4,120,148
buffer_load_dwordx4 v[176:179], v86, s[36:39], s49 offen offset:1024 sc1
s_waitcnt vmcnt(11)
mvv 8,116,152
mvv 12,120,152
s_waitcnt vmcnt(10)
mvv 0,116,156
R 40,34816
mvv 4,120,156
s_waitcnt vmcnt(9)
mvv 8,116,160
buffer_load_dwordx4 v[180:183], v87, s[36:39], s49 offen sc1
mvv 12,120,160
s_waitcnt lgkmcnt(1)
v_mfma_f32_16x16x32_bf16 v[16:19], a[0:3], v[32:35], v[100:103]
R 44,35840
v_cvt_pk_bf16_f32 v64, v0, v1
v_mfma_f32_16x16x32_bf16 v[20:23], a[4:7], v[32:35], v[104:107]
v_cvt_pk_bf16_f32 v65, v2, v3
v_cvt_pk_bf16_f32 v66, v4, v5
v_mfma_f32_16x16x32_bf16 v[24:27], a[8:11], v[32:35], v[108:111]
buffer_load_dwordx4 v[184:187], v87, s[36:39], s49 offen offset:1024 sc1
v_cvt_pk_bf16_f32 v67, v6, v7
v_mfma_f32_16x16x32_bf16 v[28:31], a[12:15], v[32:35], v[112:115]
ds_write_b128 v94, v[64:67] offset:0
mav 16,16,36
R 48,36864
v_cvt_pk_bf16_f32 v68, v8, v9
mav 20,20,36
v_cvt_pk_bf16_f32 v69, v10, v11
v_cvt_pk_bf16_f32 v70, v12, v13
mav 24,24,36
v_cvt_pk_bf16_f32 v71, v14, v15
mav 28,28,36
buffer_load_dwordx4 v[188:191], v88, s[36:39], s49 offen sc1
s_waitcnt lgkmcnt(2)
mav 16,32,40
R 52,37888
mav 20,36,40
ds_write_b128 v95, v[68:71] offset:0
mav 24,40,40
mav 28,44,40
buffer_load_dwordx4 v[192:195], v88, s[36:39], s49 offen offset:1024 sc1
mav 16,48,44
R 56,38912
mav 20,52,44
s_waitcnt lgkmcnt(1)
ds_read_b128 v[64:67], v96 offset:0
mav 24,56,44
ds_read_b128 v[68:71], v96 offset:1024
mav 28,60,44
mav 16,64,48
R 60,39936
buffer_load_dwordx4 v[196:199], v89, s[36:39], s49 offen sc1
mav 20,68,48
mav 24,72,48
mav 28,76,48
mav 16,80,52
R 32,40960
buffer_load_dwordx4 v[200:203], v89, s[36:39], s49 offen offset:1024 sc1
mav 20,84,52
mav 24,88,52
mav 28,92,52
s_add_u32 m0, s50, 32768
s_waitcnt lgkmcnt(1)
mav 16,96,56
R 36,41984
LsR13:
s_cbranch_execz LwR
buffer_store_dwordx4 v[64:67], v92, s[44:47], s54 offen
mav 20,100,56
mav 24,104,56
mav 28,108,56
mav 16,112,60
R 40,43008
buffer_store_dwordx4 v[68:71], v93, s[44:47], s54 offen
mav 20,116,60
mav 24,120,60
mav 28,124,60
s_waitcnt lgkmcnt(1)
mav 16,128,32
R 44,44032
D 83,0
mav 20,132,32
mav 24,136,32
mav 28,140,32
mav 16,144,36
R 48,45056
D 83,1024
mav 20,148,36
mav 24,152,36
mav 28,156,36
s_waitcnt lgkmcnt(1)
mav 16,160,40
R 52,46080
D 83,2048
mav 20,164,40
s_sub_u32 s54, s54, 0x8000
mav 24,168,40
mav 28,172,40
mav 16,176,44
R 56,47104
D 83,3072
mav 20,180,44
mav 24,184,44
mav 28,188,44
s_waitcnt lgkmcnt(1)
mav 16,192,48
R 60,48128
buffer_load_dwordx4 v[124:127], v90, s[36:39], s49 offen sc1
mav 20,196,48
mav 24,200,48
mav 28,204,48
mav 16,208,52
buffer_load_dwordx4 v[128:131], v90, s[36:39], s49 offen offset:1024 sc1
mav 20,212,52
mav 24,216,52
mav 28,220,52
s_waitcnt lgkmcnt(0)
mav 16,224,56
mav 20,228,56
mav 24,232,56
mav 28,236,56
mav 16,240,60
mav 20,244,60
mav 24,248,60
mav 28,252,60
s_waitcnt vmcnt(18)
mvv 16,116,164
mvv 20,120,164
s_waitcnt vmcnt(17)
mvv 24,116,168
s_xor_b32 s50, s50, 0x10000
mvv 28,120,168
s_waitcnt vmcnt(15)
mvv 16,116,172
mvv 20,120,172
v_xor_b32 v80, 0x10000, v80
s_waitcnt vmcnt(14)
mvv 24,116,176
s_add_u32 s49, s49, 346112
mvv 28,120,176
s_barrier
s_waitcnt vmcnt(13)
mvv 16,116,180
R 32,0
mvv 20,120,180
buffer_load_dwordx4 v[132:135], v86, s[36:39], s49 offen sc1
s_waitcnt vmcnt(13)
mvv 24,116,184
mvv 28,120,184
s_waitcnt vmcnt(12)
mvv 16,116,188
R 36,1024
mvv 20,120,188
buffer_load_dwordx4 v[136:139], v86, s[36:39], s49 offen offset:1024 sc1
s_waitcnt vmcnt(12)
mvv 24,116,192
mvv 28,120,192
s_waitcnt vmcnt(11)
mvv 16,116,196
R 40,2048
mvv 20,120,196
s_waitcnt vmcnt(10)
mvv 24,116,200
buffer_load_dwordx4 v[140:143], v87, s[36:39], s49 offen sc1
mvv 28,120,200
s_mov_b32 s60, -13
.p2align 6
LR2_2:
s_waitcnt lgkmcnt(1)
v_mfma_f32_16x16x32_bf16 v[0:3], a[0:3], v[32:35], v[100:103]
R 44,3072
v_cvt_pk_bf16_f32 v64, v16, v17
v_mfma_f32_16x16x32_bf16 v[4:7], a[4:7], v[32:35], v[104:107]
v_cvt_pk_bf16_f32 v65, v18, v19
v_cvt_pk_bf16_f32 v66, v20, v21
v_mfma_f32_16x16x32_bf16 v[8:11], a[8:11], v[32:35], v[108:111]
buffer_load_dwordx4 v[144:147], v87, s[36:39], s49 offen offset:1024 sc1
v_cvt_pk_bf16_f32 v67, v22, v23
v_mfma_f32_16x16x32_bf16 v[12:15], a[12:15], v[32:35], v[112:115]
ds_write_b128 v94, v[64:67] offset:0
mav 0,16,36
R 48,4096
v_cvt_pk_bf16_f32 v68, v24, v25
mav 4,20,36
v_cvt_pk_bf16_f32 v69, v26, v27
v_cvt_pk_bf16_f32 v70, v28, v29
mav 8,24,36
v_cvt_pk_bf16_f32 v71, v30, v31
mav 12,28,36
buffer_load_dwordx4 v[148:151], v88, s[36:39], s49 offen sc1
s_waitcnt lgkmcnt(2)
mav 0,32,40
R 52,5120
mav 4,36,40
ds_write_b128 v95, v[68:71] offset:0
mav 8,40,40
mav 12,44,40
buffer_load_dwordx4 v[152:155], v88, s[36:39], s49 offen offset:1024 sc1
mav 0,48,44
R 56,6144
mav 4,52,44
s_waitcnt lgkmcnt(1)
ds_read_b128 v[64:67], v96 offset:0
mav 8,56,44
ds_read_b128 v[68:71], v96 offset:1024
mav 12,60,44
mav 0,64,48
R 60,7168
buffer_load_dwordx4 v[156:159], v89, s[36:39], s49 offen sc1
mav 4,68,48
mav 8,72,48
mav 12,76,48
mav 0,80,52
R 32,8192
buffer_load_dwordx4 v[160:163], v89, s[36:39], s49 offen offset:1024 sc1
mav 4,84,52
mav 8,88,52
mav 12,92,52
s_add_u32 m0, s50, 0
s_waitcnt lgkmcnt(1)
mav 0,96,56
R 36,9216
buffer_store_dwordx4 v[64:67], v92, s[44:47], s54 offen
mav 4,100,56
mav 8,104,56
mav 12,108,56
mav 0,112,60
R 40,10240
buffer_store_dwordx4 v[68:71], v93, s[44:47], s54 offen
mav 4,116,60
mav 8,120,60
mav 12,124,60
s_waitcnt lgkmcnt(1)
mav 0,128,32
R 44,11264
D 83,0
mav 4,132,32
mav 8,136,32
mav 12,140,32
mav 0,144,36
R 48,12288
D 83,1024
mav 4,148,36
mav 8,152,36
mav 12,156,36
s_waitcnt lgkmcnt(1)
mav 0,160,40
R 52,13312
D 83,2048
mav 4,164,40
s_sub_u32 s54, s54, 0x8000
mav 8,168,40
mav 12,172,40
mav 0,176,44
R 56,14336
D 83,3072
mav 4,180,44
mav 8,184,44
mav 12,188,44
s_waitcnt lgkmcnt(1)
mav 0,192,48
R 60,15360
buffer_load_dwordx4 v[164:167], v90, s[36:39], s49 offen sc1
mav 4,196,48
mav 8,200,48
mav 12,204,48
mav 0,208,52
buffer_load_dwordx4 v[168:171], v90, s[36:39], s49 offen offset:1024 sc1
mav 4,212,52
mav 8,216,52
mav 12,220,52
s_waitcnt lgkmcnt(0)
mav 0,224,56
mav 4,228,56
mav 8,232,56
mav 12,236,56
mav 0,240,60
mav 4,244,60
mav 8,248,60
mav 12,252,60
s_waitcnt vmcnt(17)
mvv 0,116,124
mvv 4,120,124
s_waitcnt vmcnt(16)
mvv 8,116,128
mvv 12,120,128
s_waitcnt vmcnt(15)
mvv 0,116,132
mvv 4,120,132
s_waitcnt vmcnt(14)
mvv 8,116,136
s_add_u32 s49, s49, 346112
mvv 12,120,136
s_barrier
s_waitcnt vmcnt(13)
mvv 0,116,140
R 32,32768
mvv 4,120,140
buffer_load_dwordx4 v[172:175], v86, s[36:39], s49 offen sc1
s_waitcnt vmcnt(13)
mvv 8,116,144
mvv 12,120,144
s_waitcnt vmcnt(12)
mvv 0,116,148
R 36,33792
mvv 4,120,148
buffer_load_dwordx4 v[176:179], v86, s[36:39], s49 offen offset:1024 sc1
s_waitcnt vmcnt(12)
mvv 8,116,152
mvv 12,120,152
s_waitcnt vmcnt(11)
mvv 0,116,156
R 40,34816
mvv 4,120,156
s_waitcnt vmcnt(10)
mvv 8,116,160
buffer_load_dwordx4 v[180:183], v87, s[36:39], s49 offen sc1
mvv 12,120,160
s_waitcnt lgkmcnt(1)
v_mfma_f32_16x16x32_bf16 v[16:19], a[0:3], v[32:35], v[100:103]
R 44,35840
v_cvt_pk_bf16_f32 v64, v0, v1
v_mfma_f32_16x16x32_bf16 v[20:23], a[4:7], v[32:35], v[104:107]
v_cvt_pk_bf16_f32 v65, v2, v3
v_cvt_pk_bf16_f32 v66, v4, v5
v_mfma_f32_16x16x32_bf16 v[24:27], a[8:11], v[32:35], v[108:111]
buffer_load_dwordx4 v[184:187], v87, s[36:39], s49 offen offset:1024 sc1
v_cvt_pk_bf16_f32 v67, v6, v7
v_mfma_f32_16x16x32_bf16 v[28:31], a[12:15], v[32:35], v[112:115]
ds_write_b128 v94, v[64:67] offset:0
mav 16,16,36
R 48,36864
v_cvt_pk_bf16_f32 v68, v8, v9
mav 20,20,36
v_cvt_pk_bf16_f32 v69, v10, v11
v_cvt_pk_bf16_f32 v70, v12, v13
mav 24,24,36
v_cvt_pk_bf16_f32 v71, v14, v15
mav 28,28,36
buffer_load_dwordx4 v[188:191], v88, s[36:39], s49 offen sc1
s_waitcnt lgkmcnt(2)
mav 16,32,40
R 52,37888
mav 20,36,40
ds_write_b128 v95, v[68:71] offset:0
mav 24,40,40
mav 28,44,40
buffer_load_dwordx4 v[192:195], v88, s[36:39], s49 offen offset:1024 sc1
mav 16,48,44
R 56,38912
mav 20,52,44
s_waitcnt lgkmcnt(1)
ds_read_b128 v[64:67], v96 offset:0
mav 24,56,44
ds_read_b128 v[68:71], v96 offset:1024
mav 28,60,44
mav 16,64,48
R 60,39936
buffer_load_dwordx4 v[196:199], v89, s[36:39], s49 offen sc1
mav 20,68,48
mav 24,72,48
mav 28,76,48
mav 16,80,52
R 32,40960
buffer_load_dwordx4 v[200:203], v89, s[36:39], s49 offen offset:1024 sc1
mav 20,84,52
mav 24,88,52
mav 28,92,52
s_add_u32 m0, s50, 32768
s_waitcnt lgkmcnt(1)
mav 16,96,56
R 36,41984
buffer_store_dwordx4 v[64:67], v92, s[44:47], s54 offen
mav 20,100,56
mav 24,104,56
mav 28,108,56
mav 16,112,60
R 40,43008
buffer_store_dwordx4 v[68:71], v93, s[44:47], s54 offen
mav 20,116,60
mav 24,120,60
mav 28,124,60
s_waitcnt lgkmcnt(1)
mav 16,128,32
R 44,44032
D 83,0
mav 20,132,32
mav 24,136,32
mav 28,140,32
mav 16,144,36
R 48,45056
D 83,1024
mav 20,148,36
mav 24,152,36
mav 28,156,36
s_waitcnt lgkmcnt(1)
mav 16,160,40
R 52,46080
D 83,2048
mav 20,164,40
s_sub_u32 s54, s54, 0x8000
mav 24,168,40
mav 28,172,40
mav 16,176,44
R 56,47104
D 83,3072
mav 20,180,44
mav 24,184,44
mav 28,188,44
s_waitcnt lgkmcnt(1)
mav 16,192,48
R 60,48128
buffer_load_dwordx4 v[124:127], v90, s[36:39], s49 offen sc1
mav 20,196,48
mav 24,200,48
mav 28,204,48
mav 16,208,52
buffer_load_dwordx4 v[128:131], v90, s[36:39], s49 offen offset:1024 sc1
mav 20,212,52
mav 24,216,52
mav 28,220,52
s_waitcnt lgkmcnt(0)
mav 16,224,56
mav 20,228,56
mav 24,232,56
mav 28,236,56
mav 16,240,60
mav 20,244,60
mav 24,248,60
mav 28,252,60
s_waitcnt vmcnt(17)
mvv 16,116,164
mvv 20,120,164
s_waitcnt vmcnt(16)
mvv 24,116,168
s_xor_b32 s50, s50, 0x10000
mvv 28,120,168
s_waitcnt vmcnt(15)
mvv 16,116,172
mvv 20,120,172
v_xor_b32 v80, 0x10000, v80
s_waitcnt vmcnt(14)
mvv 24,116,176
s_add_u32 s49, s49, 346112
mvv 28,120,176
s_barrier
s_waitcnt vmcnt(13)
mvv 16,116,180
R 32,0
mvv 20,120,180
buffer_load_dwordx4 v[132:135], v86, s[36:39], s49 offen sc1
s_waitcnt vmcnt(13)
mvv 24,116,184
mvv 28,120,184
s_waitcnt vmcnt(12)
mvv 16,116,188
R 36,1024
mvv 20,120,188
buffer_load_dwordx4 v[136:139], v86, s[36:39], s49 offen offset:1024 sc1
s_waitcnt vmcnt(12)
mvv 24,116,192
mvv 28,120,192
s_waitcnt vmcnt(11)
mvv 16,116,196
R 40,2048
mvv 20,120,196
s_waitcnt vmcnt(10)
mvv 24,116,200
buffer_load_dwordx4 v[140:143], v87, s[36:39], s49 offen sc1
mvv 28,120,200
s_add_u32 s60, s60, 1
s_cbranch_scc0 LR2_2
s_waitcnt lgkmcnt(1)
v_mfma_f32_16x16x32_bf16 v[0:3], a[0:3], v[32:35], v[100:103]
R 44,3072
v_cvt_pk_bf16_f32 v64, v16, v17
v_mfma_f32_16x16x32_bf16 v[4:7], a[4:7], v[32:35], v[104:107]
v_cvt_pk_bf16_f32 v65, v18, v19
v_cvt_pk_bf16_f32 v66, v20, v21
v_mfma_f32_16x16x32_bf16 v[8:11], a[8:11], v[32:35], v[108:111]
buffer_load_dwordx4 v[144:147], v87, s[36:39], s49 offen offset:1024 sc1
v_cvt_pk_bf16_f32 v67, v22, v23
v_mfma_f32_16x16x32_bf16 v[12:15], a[12:15], v[32:35], v[112:115]
ds_write_b128 v94, v[64:67] offset:0
mav 0,16,36
R 48,4096
v_cvt_pk_bf16_f32 v68, v24, v25
mav 4,20,36
v_cvt_pk_bf16_f32 v69, v26, v27
v_cvt_pk_bf16_f32 v70, v28, v29
mav 8,24,36
v_cvt_pk_bf16_f32 v71, v30, v31
mav 12,28,36
buffer_load_dwordx4 v[148:151], v88, s[36:39], s49 offen sc1
s_waitcnt lgkmcnt(2)
mav 0,32,40
R 52,5120
mav 4,36,40
ds_write_b128 v95, v[68:71] offset:0
mav 8,40,40
mav 12,44,40
buffer_load_dwordx4 v[152:155], v88, s[36:39], s49 offen offset:1024 sc1
mav 0,48,44
R 56,6144
mav 4,52,44
s_waitcnt lgkmcnt(1)
ds_read_b128 v[64:67], v96 offset:0
mav 8,56,44
ds_read_b128 v[68:71], v96 offset:1024
mav 12,60,44
mav 0,64,48
R 60,7168
buffer_load_dwordx4 v[156:159], v89, s[36:39], s49 offen sc1
mav 4,68,48
mav 8,72,48
mav 12,76,48
mav 0,80,52
R 32,8192
buffer_load_dwordx4 v[160:163], v89, s[36:39], s49 offen offset:1024 sc1
mav 4,84,52
mav 8,88,52
mav 12,92,52
s_add_u32 m0, s50, 0
s_waitcnt lgkmcnt(1)
mav 0,96,56
R 36,9216
buffer_store_dwordx4 v[64:67], v92, s[44:47], s54 offen
mav 4,100,56
mav 8,104,56
mav 12,108,56
mav 0,112,60
R 40,10240
buffer_store_dwordx4 v[68:71], v93, s[44:47], s54 offen
mav 4,116,60
mav 8,120,60
mav 12,124,60
s_waitcnt lgkmcnt(1)
mav 0,128,32
R 44,11264
D 83,0
mav 4,132,32
mav 8,136,32
mav 12,140,32
mav 0,144,36
R 48,12288
D 83,1024
mav 4,148,36
mav 8,152,36
mav 12,156,36
s_waitcnt lgkmcnt(1)
mav 0,160,40
R 52,13312
D 83,2048
mav 4,164,40
s_sub_u32 s54, s54, 0x8000
mav 8,168,40
mav 12,172,40
mav 0,176,44
R 56,14336
D 83,3072
mav 4,180,44
mav 8,184,44
mav 12,188,44
s_waitcnt lgkmcnt(1)
mav 0,192,48
R 60,15360
buffer_load_dwordx4 v[164:167], v90, s[36:39], s49 offen sc1
mav 4,196,48
mav 8,200,48
mav 12,204,48
mav 0,208,52
buffer_load_dwordx4 v[168:171], v90, s[36:39], s49 offen offset:1024 sc1
mav 4,212,52
mav 8,216,52
mav 12,220,52
s_waitcnt lgkmcnt(0)
mav 0,224,56
mav 4,228,56
mav 8,232,56
mav 12,236,56
mav 0,240,60
mav 4,244,60
mav 8,248,60
mav 12,252,60
s_waitcnt vmcnt(17)
mvv 0,116,124
mvv 4,120,124
s_waitcnt vmcnt(16)
mvv 8,116,128
mvv 12,120,128
s_waitcnt vmcnt(15)
mvv 0,116,132
mvv 4,120,132
s_waitcnt vmcnt(14)
mvv 8,116,136
s_add_u32 s49, s49, 346112
mvv 12,120,136
s_barrier
s_waitcnt vmcnt(13)
mvv 0,116,140
R 32,32768
mvv 4,120,140
buffer_load_dwordx4 v[172:175], v86, s[36:39], s49 offen sc1
s_waitcnt vmcnt(13)
mvv 8,116,144
mvv 12,120,144
s_waitcnt vmcnt(12)
mvv 0,116,148
R 36,33792
mvv 4,120,148
buffer_load_dwordx4 v[176:179], v86, s[36:39], s49 offen offset:1024 sc1
s_waitcnt vmcnt(12)
mvv 8,116,152
mvv 12,120,152
LsR14:
s_cbranch_execz LwR
s_waitcnt vmcnt(11)
mvv 0,116,156
R 40,34816
mvv 4,120,156
s_waitcnt vmcnt(10)
mvv 8,116,160
buffer_load_dwordx4 v[180:183], v87, s[36:39], s49 offen sc1
mvv 12,120,160
s_waitcnt lgkmcnt(1)
v_mfma_f32_16x16x32_bf16 v[16:19], a[0:3], v[32:35], v[100:103]
R 44,35840
v_cvt_pk_bf16_f32 v64, v0, v1
v_mfma_f32_16x16x32_bf16 v[20:23], a[4:7], v[32:35], v[104:107]
v_cvt_pk_bf16_f32 v65, v2, v3
v_cvt_pk_bf16_f32 v66, v4, v5
v_mfma_f32_16x16x32_bf16 v[24:27], a[8:11], v[32:35], v[108:111]
buffer_load_dwordx4 v[184:187], v87, s[36:39], s49 offen offset:1024 sc1
v_cvt_pk_bf16_f32 v67, v6, v7
v_mfma_f32_16x16x32_bf16 v[28:31], a[12:15], v[32:35], v[112:115]
ds_write_b128 v94, v[64:67] offset:0
mav 16,16,36
R 48,36864
v_cvt_pk_bf16_f32 v68, v8, v9
mav 20,20,36
v_cvt_pk_bf16_f32 v69, v10, v11
v_cvt_pk_bf16_f32 v70, v12, v13
mav 24,24,36
v_cvt_pk_bf16_f32 v71, v14, v15
mav 28,28,36
buffer_load_dwordx4 v[188:191], v88, s[36:39], s49 offen sc1
s_waitcnt lgkmcnt(2)
mav 16,32,40
R 52,37888
mav 20,36,40
ds_write_b128 v95, v[68:71] offset:0
mav 24,40,40
mav 28,44,40
buffer_load_dwordx4 v[192:195], v88, s[36:39], s49 offen offset:1024 sc1
mav 16,48,44
R 56,38912
mav 20,52,44
s_waitcnt lgkmcnt(1)
ds_read_b128 v[64:67], v96 offset:0
mav 24,56,44
ds_read_b128 v[68:71], v96 offset:1024
mav 28,60,44
mav 16,64,48
R 60,39936
buffer_load_dwordx4 v[196:199], v89, s[36:39], s49 offen sc1
mav 20,68,48
mav 24,72,48
mav 28,76,48
mav 16,80,52
R 32,40960
buffer_load_dwordx4 v[200:203], v89, s[36:39], s49 offen offset:1024 sc1
mav 20,84,52
mav 24,88,52
mav 28,92,52
s_add_u32 m0, s50, 32768
s_waitcnt lgkmcnt(1)
mav 16,96,56
R 36,41984
buffer_store_dwordx4 v[64:67], v92, s[44:47], s54 offen
mav 20,100,56
mav 24,104,56
mav 28,108,56
mav 16,112,60
R 40,43008
buffer_store_dwordx4 v[68:71], v93, s[44:47], s54 offen
mav 20,116,60
mav 24,120,60
mav 28,124,60
s_waitcnt lgkmcnt(1)
mav 16,128,32
R 44,44032
D 83,0
mav 20,132,32
mav 24,136,32
mav 28,140,32
mav 16,144,36
R 48,45056
D 83,1024
mav 20,148,36
mav 24,152,36
mav 28,156,36
s_waitcnt lgkmcnt(1)
mav 16,160,40
R 52,46080
D 83,2048
mav 20,164,40
s_sub_u32 s54, s54, 0x8000
mav 24,168,40
mav 28,172,40
mav 16,176,44
R 56,47104
D 83,3072
mav 20,180,44
mav 24,184,44
mav 28,188,44
s_waitcnt lgkmcnt(1)
mav 16,192,48
R 60,48128
buffer_load_dwordx4 v[124:127], v90, s[36:39], s49 offen sc1
mav 20,196,48
mav 24,200,48
mav 28,204,48
mav 16,208,52
buffer_load_dwordx4 v[128:131], v90, s[36:39], s49 offen offset:1024 sc1
mav 20,212,52
mav 24,216,52
mav 28,220,52
s_waitcnt lgkmcnt(0)
mav 16,224,56
mav 20,228,56
mav 24,232,56
mav 28,236,56
mav 16,240,60
mav 20,244,60
mav 24,248,60
mav 28,252,60
s_waitcnt vmcnt(17)
mvv 16,116,164
mvv 20,120,164
s_waitcnt vmcnt(16)
mvv 24,116,168
mvv 28,120,168
s_waitcnt vmcnt(15)
mvv 16,116,172
mvv 20,120,172
v_xor_b32 v80, 0x10000, v80
s_waitcnt vmcnt(14)
mvv 24,116,176
s_add_u32 s49, s49, 346112
mvv 28,120,176
s_barrier
s_waitcnt vmcnt(13)
mvv 16,116,180
R 32,0
mvv 20,120,180
buffer_load_dwordx4 v[132:135], v86, s[36:39], s49 offen sc1
s_waitcnt vmcnt(13)
mvv 24,116,184
mvv 28,120,184
s_waitcnt vmcnt(12)
mvv 16,116,188
R 36,1024
mvv 20,120,188
buffer_load_dwordx4 v[136:139], v86, s[36:39], s49 offen offset:1024 sc1
s_waitcnt vmcnt(12)
mvv 24,116,192
mvv 28,120,192
s_waitcnt vmcnt(11)
mvv 16,116,196
R 40,2048
mvv 20,120,196
s_waitcnt vmcnt(10)
mvv 24,116,200
buffer_load_dwordx4 v[140:143], v87, s[36:39], s49 offen sc1
mvv 28,120,200
s_waitcnt lgkmcnt(1)
v_mfma_f32_16x16x32_bf16 v[0:3], a[0:3], v[32:35], v[100:103]
R 44,3072
v_cvt_pk_bf16_f32 v64, v16, v17
v_mfma_f32_16x16x32_bf16 v[4:7], a[4:7], v[32:35], v[104:107]
v_cvt_pk_bf16_f32 v65, v18, v19
v_cvt_pk_bf16_f32 v66, v20, v21
v_mfma_f32_16x16x32_bf16 v[8:11], a[8:11], v[32:35], v[108:111]
buffer_load_dwordx4 v[144:147], v87, s[36:39], s49 offen offset:1024 sc1
v_cvt_pk_bf16_f32 v67, v22, v23
v_mfma_f32_16x16x32_bf16 v[12:15], a[12:15], v[32:35], v[112:115]
ds_write_b128 v94, v[64:67] offset:0
mav 0,16,36
R 48,4096
v_cvt_pk_bf16_f32 v68, v24, v25
mav 4,20,36
v_cvt_pk_bf16_f32 v69, v26, v27
v_cvt_pk_bf16_f32 v70, v28, v29
mav 8,24,36
v_cvt_pk_bf16_f32 v71, v30, v31
mav 12,28,36
buffer_load_dwordx4 v[148:151], v88, s[36:39], s49 offen sc1
s_waitcnt lgkmcnt(2)
mav 0,32,40
R 52,5120
mav 4,36,40
ds_write_b128 v95, v[68:71] offset:0
mav 8,40,40
mav 12,44,40
buffer_load_dwordx4 v[152:155], v88, s[36:39], s49 offen offset:1024 sc1
mav 0,48,44
R 56,6144
mav 4,52,44
s_waitcnt lgkmcnt(1)
ds_read_b128 v[64:67], v96 offset:0
mav 8,56,44
ds_read_b128 v[68:71], v96 offset:1024
mav 12,60,44
mav 0,64,48
R 60,7168
buffer_load_dwordx4 v[156:159], v89, s[36:39], s49 offen sc1
mav 4,68,48
mav 8,72,48
mav 12,76,48
mav 0,80,52
R 32,8192
buffer_load_dwordx4 v[160:163], v89, s[36:39], s49 offen offset:1024 sc1
mav 4,84,52
mav 8,88,52
mav 12,92,52
s_waitcnt lgkmcnt(1)
mav 0,96,56
R 36,9216
buffer_store_dwordx4 v[64:67], v92, s[44:47], s54 offen
mav 4,100,56
mav 8,104,56
mav 12,108,56
mav 0,112,60
R 40,10240
buffer_store_dwordx4 v[68:71], v93, s[44:47], s54 offen
mav 4,116,60
mav 8,120,60
mav 12,124,60
s_waitcnt lgkmcnt(1)
mav 0,128,32
R 44,11264
mav 4,132,32
mav 8,136,32
mav 12,140,32
mav 0,144,36
R 48,12288
mav 4,148,36
mav 8,152,36
mav 12,156,36
s_waitcnt lgkmcnt(1)
mav 0,160,40
R 52,13312
s_sub_u32 s54, s54, 0x8000
mav 4,164,40
mav 8,168,40
mav 12,172,40
buffer_load_dwordx4 v[164:167], v90, s[36:39], s49 offen sc1
mav 0,176,44
R 56,14336
mav 4,180,44
mav 8,184,44
mav 12,188,44
buffer_load_dwordx4 v[168:171], v90, s[36:39], s49 offen offset:1024 sc1
s_waitcnt lgkmcnt(1)
mav 0,192,48
R 60,15360
mav 4,196,48
mav 8,200,48
mav 12,204,48
mav 0,208,52
mav 4,212,52
mav 8,216,52
mav 12,220,52
s_waitcnt lgkmcnt(0)
mav 0,224,56
mav 4,228,56
mav 8,232,56
mav 12,236,56
mav 0,240,60
mav 4,244,60
mav 8,248,60
mav 12,252,60
s_waitcnt vmcnt(13)
mvv 0,116,124
mvv 4,120,124
s_waitcnt vmcnt(12)
mvv 8,116,128
mvv 12,120,128
s_waitcnt vmcnt(11)
mvv 0,116,132
mvv 4,120,132
s_waitcnt vmcnt(10)
mvv 8,116,136
s_add_u32 s49, s49, 346112
mvv 12,120,136
s_barrier
s_waitcnt vmcnt(9)
mvv 0,116,140
R 32,32768
mvv 4,120,140
buffer_load_dwordx4 v[172:175], v86, s[36:39], s49 offen sc1
s_waitcnt vmcnt(9)
mvv 8,116,144
mvv 12,120,144
s_waitcnt vmcnt(8)
mvv 0,116,148
R 36,33792
mvv 4,120,148
buffer_load_dwordx4 v[176:179], v86, s[36:39], s49 offen offset:1024 sc1
s_waitcnt vmcnt(8)
mvv 8,116,152
mvv 12,120,152
s_waitcnt vmcnt(7)
mvv 0,116,156
R 40,34816
mvv 4,120,156
s_waitcnt vmcnt(6)
mvv 8,116,160
buffer_load_dwordx4 v[180:183], v87, s[36:39], s49 offen sc1
mvv 12,120,160
s_waitcnt lgkmcnt(1)
v_mfma_f32_16x16x32_bf16 v[16:19], a[0:3], v[32:35], v[100:103]
R 44,35840
v_cvt_pk_bf16_f32 v64, v0, v1
v_mfma_f32_16x16x32_bf16 v[20:23], a[4:7], v[32:35], v[104:107]
v_cvt_pk_bf16_f32 v65, v2, v3
v_cvt_pk_bf16_f32 v66, v4, v5
v_mfma_f32_16x16x32_bf16 v[24:27], a[8:11], v[32:35], v[108:111]
buffer_load_dwordx4 v[184:187], v87, s[36:39], s49 offen offset:1024 sc1
v_cvt_pk_bf16_f32 v67, v6, v7
v_mfma_f32_16x16x32_bf16 v[28:31], a[12:15], v[32:35], v[112:115]
ds_write_b128 v94, v[64:67] offset:0
mav 16,16,36
R 48,36864
v_cvt_pk_bf16_f32 v68, v8, v9
mav 20,20,36
v_cvt_pk_bf16_f32 v69, v10, v11
v_cvt_pk_bf16_f32 v70, v12, v13
mav 24,24,36
v_cvt_pk_bf16_f32 v71, v14, v15
mav 28,28,36
buffer_load_dwordx4 v[188:191], v88, s[36:39], s49 offen sc1
s_waitcnt lgkmcnt(2)
mav 16,32,40
R 52,37888
mav 20,36,40
ds_write_b128 v95, v[68:71] offset:0
mav 24,40,40
mav 28,44,40
buffer_load_dwordx4 v[192:195], v88, s[36:39], s49 offen offset:1024 sc1
mav 16,48,44
R 56,38912
mav 20,52,44
s_waitcnt lgkmcnt(1)
ds_read_b128 v[64:67], v96 offset:0
mav 24,56,44
ds_read_b128 v[68:71], v96 offset:1024
mav 28,60,44
mav 16,64,48
R 60,39936
buffer_load_dwordx4 v[196:199], v89, s[36:39], s49 offen sc1
mav 20,68,48
mav 24,72,48
mav 28,76,48
mav 16,80,52
R 32,40960
buffer_load_dwordx4 v[200:203], v89, s[36:39], s49 offen offset:1024 sc1
mav 20,84,52
mav 24,88,52
mav 28,92,52
s_waitcnt lgkmcnt(1)
mav 16,96,56
R 36,41984
buffer_store_dwordx4 v[64:67], v92, s[44:47], s54 offen
mav 20,100,56
mav 24,104,56
mav 28,108,56
mav 16,112,60
R 40,43008
buffer_store_dwordx4 v[68:71], v93, s[44:47], s54 offen
mav 20,116,60
mav 24,120,60
mav 28,124,60
s_waitcnt lgkmcnt(1)
mav 16,128,32
R 44,44032
mav 20,132,32
mav 24,136,32
mav 28,140,32
mav 16,144,36
R 48,45056
mav 20,148,36
mav 24,152,36
mav 28,156,36
s_waitcnt lgkmcnt(1)
mav 16,160,40
R 52,46080
s_sub_u32 s54, s54, 0x8000
mav 20,164,40
mav 24,168,40
mav 28,172,40
mav 16,176,44
R 56,47104
mav 20,180,44
mav 24,184,44
mav 28,188,44
s_waitcnt lgkmcnt(1)
mav 16,192,48
R 60,48128
mav 20,196,48
mav 24,200,48
mav 28,204,48
mav 16,208,52
mav 20,212,52
mav 24,216,52
mav 28,220,52
s_waitcnt lgkmcnt(0)
mav 16,224,56
mav 20,228,56
mav 24,232,56
mav 28,236,56
mav 16,240,60
mav 20,244,60
mav 24,248,60
mav 28,252,60
s_waitcnt vmcnt(11)
mvv 16,116,164
mvv 20,120,164
s_waitcnt vmcnt(10)
mvv 24,116,168
mvv 28,120,168
s_waitcnt vmcnt(9)
mvv 16,116,172
mvv 20,120,172
s_waitcnt vmcnt(8)
mvv 24,116,176
mvv 28,120,176
s_waitcnt vmcnt(7)
mvv 16,116,180
mvv 20,120,180
s_waitcnt vmcnt(6)
mvv 24,116,184
mvv 28,120,184
s_waitcnt vmcnt(5)
mvv 16,116,188
mvv 20,120,188
s_waitcnt vmcnt(4)
mvv 24,116,192
mvv 28,120,192
s_waitcnt vmcnt(3)
mvv 16,116,196
mvv 20,120,196
s_waitcnt vmcnt(2)
mvv 24,116,200
mvv 28,120,200
s_nop 3
v_cvt_pk_bf16_f32 v64, v16, v17
v_cvt_pk_bf16_f32 v65, v18, v19
v_cvt_pk_bf16_f32 v66, v20, v21
v_cvt_pk_bf16_f32 v67, v22, v23
ds_write_b128 v94, v[64:67] offset:0
v_cvt_pk_bf16_f32 v68, v24, v25
v_cvt_pk_bf16_f32 v69, v26, v27
v_cvt_pk_bf16_f32 v70, v28, v29
v_cvt_pk_bf16_f32 v71, v30, v31
ds_write_b128 v95, v[68:71] offset:0
s_waitcnt lgkmcnt(0)
ds_read_b128 v[64:67], v96 offset:0
ds_read_b128 v[68:71], v96 offset:1024
s_waitcnt lgkmcnt(0)
buffer_store_dwordx4 v[64:67], v92, s[44:47], s54 offen
buffer_store_dwordx4 v[68:71], v93, s[44:47], s54 offen
s_endpgm
.purgem mav
.purgem Ta
.purgem mva
.purgem mvv
.purgem Tv
.purgem R
.purgem D
.purgem E
)"
    : "+{v[0:7]}"(v0), "+{v[80:95]}"(c0), "+{v[96:111]}"(c1), "+{v[112:127]}"(c2), "+{s[36:51]}"(s0), "+{s[52:59]}"(s1)
    :
    : "v8", "v9", "v10", "v11", "v12", "v13", "v14", "v15", "v16", "v17", "v18", "v19", "v20", "v21", "v22", "v23", "v24", "v25", "v26", "v27", "v28", "v29", "v30", "v31", "v32", "v33", "v34", "v35", "v36", "v37", "v38", "v39", "v40", "v41", "v42", "v43", "v44", "v45", "v46", "v47", "v48", "v49", "v50", "v51", "v52", "v53", "v54", "v55", "v56", "v57", "v58", "v59", "v60", "v61", "v62", "v63", "v64", "v65", "v66", "v67", "v68", "v69", "v70", "v71", "v72", "v73", "v74", "v75", "v76", "v77", "v78", "v79", "v128", "v129", "v130", "v131", "v132", "v133", "v134", "v135", "v136", "v137", "v138", "v139", "v140", "v141", "v142", "v143", "v144", "v145", "v146", "v147", "v148", "v149", "v150", "v151", "v152", "v153", "v154", "v155", "v156", "v157", "v158", "v159", "v160", "v161", "v162", "v163", "v164", "v165", "v166", "v167", "v168", "v169", "v170", "v171", "v172", "v173", "v174", "v175", "v176", "v177", "v178", "v179", "v180", "v181", "v182", "v183", "v184", "v185", "v186", "v187", "v188", "v189", "v190", "v191", "v192", "v193", "v194", "v195", "v196", "v197", "v198", "v199", "v200", "v201", "v202", "v203", "v204", "v205", "v206", "v207", "v208", "v209", "v210", "v211", "v212", "v213", "v214", "v215", "v216", "v217", "v218", "v219", "v220", "v221", "v222", "v223", "v224", "v225", "v226", "v227", "v228", "v229", "v230", "v231", "v232", "v233", "v234", "v235", "v236", "v237", "v238", "v239", "v240", "v241", "v242", "v243", "v244", "v245", "v246", "v247", "v248", "v249", "v250", "v251", "v252", "v253", "v254", "v255", "a0", "a1", "a2", "a3", "a4", "a5", "a6", "a7", "a8", "a9", "a10", "a11", "a12", "a13", "a14", "a15", "a16", "a17", "a18", "a19", "a20", "a21", "a22", "a23", "a24", "a25", "a26", "a27", "a28", "a29", "a30", "a31", "a32", "a33", "a34", "a35", "a36", "a37", "a38", "a39", "a40", "a41", "a42", "a43", "a44", "a45", "a46", "a47", "a48", "a49", "a50", "a51", "a52", "a53", "a54", "a55", "a56", "a57", "a58", "a59", "a60", "a61", "a62", "a63", "a64", "a65", "a66", "a67", "a68", "a69", "a70", "a71", "a72", "a73", "a74", "a75", "a76", "a77", "a78", "a79", "a80", "a81", "a82", "a83", "a84", "a85", "a86", "a87", "a88", "a89", "a90", "a91", "a92", "a93", "a94", "a95", "a96", "a97", "a98", "a99", "a100", "a101", "a102", "a103", "a104", "a105", "a106", "a107", "a108", "a109", "a110", "a111", "a112", "a113", "a114", "a115", "a116", "a117", "a118", "a119", "a120", "a121", "a122", "a123", "a124", "a125", "a126", "a127", "a128", "a129", "a130", "a131", "a132", "a133", "a134", "a135", "a136", "a137", "a138", "a139", "a140", "a141", "a142", "a143", "a144", "a145", "a146", "a147", "a148", "a149", "a150", "a151", "a152", "a153", "a154", "a155", "a156", "a157", "a158", "a159", "a160", "a161", "a162", "a163", "a164", "a165", "a166", "a167", "a168", "a169", "a170", "a171", "a172", "a173", "a174", "a175", "a176", "a177", "a178", "a179", "a180", "a181", "a182", "a183", "a184", "a185", "a186", "a187", "a188", "a189", "a190", "a191", "a192", "a193", "a194", "a195", "a196", "a197", "a198", "a199", "a200", "a201", "a202", "a203", "a204", "a205", "a206", "a207", "a208", "a209", "a210", "a211", "a212", "a213", "a214", "a215", "a216", "a217", "a218", "a219", "a220", "a221", "a222", "a223", "a224", "a225", "a226", "a227", "a228", "a229", "a230", "a231", "a232", "a233", "a234", "a235", "a236", "a237", "a238", "a239", "a240", "a241", "a242", "a243", "a244", "a245", "a246", "a247", "a248", "a249", "a250", "a251", "a252", "a253", "a254", "a255", "s60", "s61", "s62", "s63", "s64", "s65", "s66", "s67", "s68", "s69", "s70", "s71", "s72", "s73", "s74", "s75", "s76", "s77", "s78", "s79", "s80", "s81", "s82", "s83", "s84", "s85", "s86", "s87", "s88", "s89", "s90", "s91", "s92", "s93", "s94", "s95", "s96", "s97", "s98", "s99", "vcc", "scc", "memory");
}

extern "C" void kernel_launch(void* const* d_in, const int* in_sizes, int n_in, void* d_out, int out_size,
                              void* d_ws, size_t ws_size, hipStream_t stream) {
  Args m{};
  m.x = (const u16*)d_in[0]; m.wqkv = (const u16*)d_in[1]; m.wo = (const u16*)d_in[2]; m.w1 = (const u16*)d_in[4];
  m.b1 = (const u16*)d_in[5]; m.w2 = (const u16*)d_in[6]; m.b2 = (const u16*)d_in[7];
  m.out = (u16*)d_out; m.ws = (char*)d_ws;
  const unsigned tab[32] = {0x9c983000u, 0x94903404u, 0xa29e3808u, 0xa8a43c0cu, 0xdcd87040u, 0xd4d07444u, 0xe2de7848u, 0xe8e47c4cu, 0x9d99ab01u, 0x9591ad05u, 0xa39fae09u, 0xa9a5ac0du, 0xddd93141u, 0xd5d13545u, 0xe3df3949u, 0xe9e53d4du, 0x969a7102u, 0x2a927506u, 0x97a0790au, 0xafa67d0eu, 0x33daeb42u, 0x37d2ed46u, 0x3be0ee4au, 0x3fe6ec4eu, 0x739b3203u, 0x77933607u, 0x7ba13a0bu, 0x7fa73e0fu, 0xd6db7243u, 0x6ad37647u, 0xd7e17a4bu, 0xefe77e4fu};
  for (int i = 0; i < 32; ++i) m.tab[i] = tab[i];
  (void)hipFuncSetAttribute((const void*)df_kernel, hipFuncAttributeMaxDynamicSharedMemorySize, LDS_BYTES);
  hipLaunchKernelGGL(df_kernel, dim3(256), dim3(256), LDS_BYTES, stream, m);
}
```

```cpp
#include <hip/hip_runtime.h>
#include <stdint.h>

typedef __attribute__((ext_vector_type(16))) unsigned u32x16;
typedef __attribute__((ext_vector_type(8))) unsigned u32x8;
typedef __attribute__((ext_vector_type(4))) unsigned u32x4;
typedef __attribute__((ext_vector_type(2))) unsigned u32x2;
typedef __attribute__((ext_vector_type(2))) __bf16 bf2_t;
typedef unsigned short u16;
typedef __amdgpu_buffer_rsrc_t rsrc_t;
typedef __attribute__((address_space(3))) void lds_t;

constexpr unsigned FLAGS = 4096, RB = 8388608, REC = 313344, SX = 311296, WS_END = 88604672, DBG = 1048576;
constexpr int LDS_BYTES = 163840;
#define NEGB 0xff61b1e6u
#define FW(p, s, w) (FLAGS + (unsigned)(((p) * 64 + (s)) * 128 + (w) * 8))
#define blk0(s) (RB + (unsigned)(p * 64 + (s) - 2) * REC)
#define TA(seg, hf, sub) (unsigned)((seg) * 4096 + (8 * lg + (li >> 2)) * 128 + ((((hf) * 4 + (li & 3)) ^ ((li >> 1) & 4)) << 4) + (sub))
#define TO(seg, hf, half) (unsigned)((seg) * 4096 + (8 * lg + (li >> 2)) * 128 + ((((hf) * 4 + (half) * 2 + ((li & 3) >> 1)) ^ ((li >> 1) & 4)) << 4) + (li & 1) * 8)
#define BIAS(ptr, n, i) ((unsigned)__builtin_amdgcn_raw_buffer_load_b16(__builtin_amdgcn_make_buffer_rsrc((void*)(ptr), 0, (n) * 2, 0x00020000), (i) * 2, 0, 0) << 16)
#define XRD (unsigned)((li & 7) * 4128 + (li >> 3) * 2048 + lg * 16)

struct Args {
  const u16 *x, *wqkv, *wo, *w1, *w2;
  char* ws;
  u16* out;
  const u16 *b1, *b2;
  unsigned tab[64];
};

__device__ __forceinline__ float rows_sum(float v) {
  const auto a = __builtin_amdgcn_permlane16_swap(__float_as_uint(v), __float_as_uint(v), false, false);
  const float m = __uint_as_float(a[0]) + __uint_as_float(a[1]);
  const auto b = __builtin_amdgcn_permlane32_swap(__float_as_uint(m), __float_as_uint(m), false, false);
  return __uint_as_float(b[0]) + __uint_as_float(b[1]);
}

__device__ __forceinline__ const u16* arg_b2() {
  auto ka = (const __attribute__((address_space(4))) Args*)__builtin_amdgcn_kernarg_segment_ptr();
  asm volatile("" : "+s"(ka));
  return ka->b2;
}

__global__ __launch_bounds__(256, 1) void df_kernel(Args m) {
  const int w = __builtin_amdgcn_readfirstlane(threadIdx.x >> 6);
  const int bid = blockIdx.x, xc = bid & 7, j_ = bid >> 3;
  const int te_ = ((const __attribute__((address_space(4))) Args*)__builtin_amdgcn_kernarg_segment_ptr())->tab[(j_ & 28) * 2 + xc] >> ((j_ & 3) * 8), slot = te_ & 63, p = (te_ >> 6) & 3;
  {
    const unsigned tid = threadIdx.x;
    lds_t* const sink = (lds_t*)(size_t)(65536 + w * 1024);
    if (w & 1) {
      unsigned long long pc; unsigned o = tid < 112 ? (tid & 63) * 128 : (tid < 128 ? tid - 48 : tid & 63) * 4096 + j_ * 128;
      asm volatile("s_mov_b32 m0, %2\n\ts_getpc_b64 %0\nLbeg:\n\tv_min_u32 %1, Lfin - Lbeg + 60, %1\n\tglobal_load_lds_dword %1, %0" : "=&s"(pc), "+v"(o) : "s"(65536 + w * 1024) : "memory");
    }
    {
      const u32x4 rs = {(unsigned)(size_t)m.ws, (unsigned)((size_t)m.ws >> 32) & 0xffffu, WS_END, 0x00020000};
      asm volatile("s_nop 4\n\tbuffer_load_dword a255, off, %0, 0 sc1" :: "s"(rs) : "a255", "memory");
    }
    const int ts = slot, tp = p;
    if (ts >> 4 == 2) {
      const rsrc_t rT = __builtin_amdgcn_make_buffer_rsrc((void*)m.wqkv, 0, 1024 * 3072 * 2, 0x00020000);
      const rsrc_t rX = __builtin_amdgcn_make_buffer_rsrc((void*)m.x, 0, 2048 * 1024 * 2, 0x00020000);
      const unsigned t = ((slot & 3) * 4 + p) * 256 + tid;
#pragma unroll
      for (int i = 0; i < 3; ++i) {
        __builtin_amdgcn_raw_ptr_buffer_load_lds(rT, sink, 4, (t >> 2) * 2048 + (t & 3) * 128, (ts & 12) * 128 + i * 2048 * 1024, 0, 0);
        if (i == 0 && 1) __builtin_amdgcn_raw_ptr_buffer_load_lds(rX, sink, 4, (t >> 10) * 1048576 + (t & 1023) * 128, 28 * 32768, 0, 0);
      }
    }
    if (1 && ts >= 56) {
      const rsrc_t rU = __builtin_amdgcn_make_buffer_rsrc((void*)m.wo, 0, 1024 * 1024 * 2, 0x00020000);
#pragma unroll
      for (int i = 0; i < 4; ++i) __builtin_amdgcn_raw_ptr_buffer_load_lds(rU, sink, 4, ((ts - 56) * 256 + tid) * 128, ((tp * 1 * 2 + 4 + i) & 7) * 262144, 0, 0);
    }
    if (slot < 16) {
      const rsrc_t rW = __builtin_amdgcn_make_buffer_rsrc((void*)m.wqkv, 0, 1024 * 3072 * 2, 0x00020000);
      const rsrc_t rX = __builtin_amdgcn_make_buffer_rsrc((void*)m.x, 0, 2048 * 1024 * 2, 0x00020000);
      const unsigned ln = tid & 63, vw = (ln >> 3) * 6144 + (((ln & 7) ^ ((ln >> 2) & 4)) << 4), vx = (ln >> 4) * 2048 + (((ln & 15) ^ ((ln >> 4) * 4)) << 4);
#pragma unroll
      for (int g = 0; g < 1; ++g) {
#pragma unroll
        for (int i = 0; i < 6; ++i) {
          __builtin_amdgcn_raw_ptr_buffer_load_lds(rW, (lds_t*)(size_t)(36864 + (3 - g) * 24576 + i * 4096 + w * 1024), 16, vw, w * 8 * 6144 + slot * 128 + (2 * g + i / 3) * 32 * 6144 + i % 3 * 2048, 0, 0);
          if (!(g & 1) && (i == 1 || i == 3))
            __builtin_amdgcn_raw_ptr_buffer_load_lds(rX, (lds_t*)(size_t)(w * 8208 + 2048 + (i >> 1) * 4096 + g * 512), 16, vx, (p * 32 + 31 - (i >> 1)) * 32768 + w * 8192 + g * 128, 0, 0);
        }
      }
    }
  }
  if (slot >> 4 == 1) {
    int lane;
    asm volatile("v_mbcnt_lo_u32_b32 %0, -1, 0\n\tv_mbcnt_hi_u32_b32 %0, -1, %0" : "=v"(lane));
    const int lg = lane >> 4, li = lane & 15;
    const rsrc_t rS = __builtin_amdgcn_make_buffer_rsrc((void*)m.ws, 0, WS_END, 0x00020000);
    const rsrc_t rX = __builtin_amdgcn_make_buffer_rsrc((void*)m.x, 0, 2048 * 1024 * 2, 0x00020000);
    const bf2_t one2 = {(__bf16)1.0f, (__bf16)1.0f};
    const unsigned go = lg * 2048 + w * 512 + ((li ^ (lg * 4)) << 4), lb = 73728 + w * 16512, lo = lb + (li >> 2) * 2064 + (li & 3) * 256 + lg * 16;
#pragma unroll
    for (int i = 0; i < 16; ++i)
      __builtin_amdgcn_raw_ptr_buffer_load_lds(rX, (lds_t*)(size_t)(lb + (i >> 1) * 2064 + (i & 1) * 1024), 16, go + ((i >> 1) * 8192 + (i & 1) * 256), (p * 16 + 15 - (slot & 15)) * 65536, 0, 0);
    asm volatile("s_waitcnt vmcnt(0)" ::: "memory");
#pragma unroll
    for (int b = 0; b < 2; ++b) {
      float s = 0.f, q = 0.f;
#pragma unroll
      for (int k = 0; k < 8; ++k) {
        const u32x4 v = *(const __attribute__((address_space(3))) u32x4*)(size_t)(lo + b * 8256 + (k >> 2) * 1024 + (((k ^ li) & 3) << 6));
#pragma unroll
        for (int e = 0; e < 4; ++e) {
          const unsigned xe = v[e];
          const bf2_t x = __builtin_bit_cast(bf2_t, xe);
          s = __builtin_amdgcn_fdot2_f32_bf16(x, one2, s, false);
          q = __builtin_amdgcn_fdot2_f32_bf16(x, x, q, false);
        }
      }
      s = rows_sum(s); q = rows_sum(q);
      __builtin_amdgcn_raw_buffer_store_b64((u32x2){__float_as_uint(s), __float_as_uint(q)}, rS, lg ? 0x80000000u : w * 128 + li * 8 + SX, RB + (unsigned)(p * 64 + (slot & 15) * 2 + 1 - b) * REC, 16);
    }
  }
  const int lane = threadIdx.x & 63, lg = lane >> 4, li = lane & 15;
  const unsigned l16 = lane * 16;
  u32x16 c0 = 0, c1 = 0, c2 = 0, s0 = 0;
  u32x8 v0 = 0, s1 = 0;
  const u16 *wp, *op = m.out; unsigned wn, on = 4 << 20;
  const u16 *rQp = m.wqkv, *rOp = m.wo, *r1p = m.w1, *r2p = m.w2;
  constexpr unsigned rQn = 1024 * 3072 * 2, rOn = 1024 * 1024 * 2, r1n = 1024 * 3584 * 2, r2n = r1n;
  const int rot = p * 1, wr = (w + rot) & 3;
  if (slot < 16 && !(w & 1)) {
    s0[12] = 0 + (w >> 1); wp = rQp; wn = rQn;
    op = m.x - 512 * 1024; on = 5 << 20;
    v0[3] = (p * 32 + 62) * 32768 + (w * 4 + lg) * 2048 + ((li ^ (lg * 4)) << 4); v0[7] = 2048 + (li >> 2) * 8208 + (li & 3) * 256 + lg * 16;
    s0[13] = blk0(0) + 0u + (slot * 2 + (w >> 1)) * 1024 - (133120u + (w >> 1) * 2048);
    s0[15] = w * 8 * 6144 + slot * 128 + 393216;
    s1[2] = 262144u + (p * 32 + slot * 2 + (w >> 1)) * 4096 - (133120u + (w >> 1) * 2048);
    s1[3] = p ? 262144u + ((p - 1) * 32 + slot * 2 + (w >> 1)) * 4096 - (133120u + (w >> 1) * 2048) : 0x80000000u;
    s1[4] = p ? 0u : NEGB;
    v0[0] = (lane >> 3) * 6144 + (((lane & 7) ^ ((lane >> 2) & 4)) << 4) + 0u;
    v0[1] = (lane >> 3) * 6144 + (((lane & 7) ^ ((lane >> 2) & 4)) << 4) + 2048u;
    v0[2] = (lane >> 3) * 6144 + (((lane & 7) ^ ((lane >> 2) & 4)) << 4) + 4096u;
    v0[4] = TA(0, w >> 1, 0);
    v0[5] = TA(0, w >> 1, 8);
    v0[6] = TA(2, w >> 1, 0);
    c0[0] = 66048u + XRD;
    c0[1] = (66048u + XRD) ^ XRD;
    c0[2] = 133120u + (w >> 1) * 2048 + l16;
    c0[3] = 141312u + li * 8;
    c0[4] = 141312u + lg * 32;
    c0[6] = (((4 * lg + 0) ^ li) & 1) == 0 && (4 * lg + 0 <= li) ? 0u : NEGB;
    c0[7] = (((4 * lg + 1) ^ li) & 1) == 0 && (4 * lg + 1 <= li) ? 0u : NEGB;
    c0[8] = (((4 * lg + 2) ^ li) & 1) == 0 && (4 * lg + 2 <= li) ? 0u : NEGB;
    c0[9] = (((4 * lg + 3) ^ li) & 1) == 0 && (4 * lg + 3 <= li) ? 0u : NEGB;
    c0[10] = (((4 * lg + 0) ^ li) & 1) == 0 && (4 * lg + 0 > li) ? 0u : NEGB;
    c0[11] = (((4 * lg + 1) ^ li) & 1) == 0 && (4 * lg + 1 > li) ? 0u : NEGB;
    c0[12] = (((4 * lg + 2) ^ li) & 1) == 0 && (4 * lg + 2 > li) ? 0u : NEGB;
    c0[13] = (((4 * lg + 3) ^ li) & 1) == 0 && (4 * lg + 3 > li) ? 0u : NEGB;
    c0[14] = (4 * lg + 0 + (li & 1) <= li) ? ((li & 1) ? 0xffff0000u : 0xffffu) : 0u;
    c0[15] = (4 * lg + 2 + (li & 1) <= li) ? ((li & 1) ? 0xffff0000u : 0xffffu) : 0u;
    c1[0] = (4 * lg + 0 + (li & 1) > li) ? ((li & 1) ? 0xffff0000u : 0xffffu) : 0u;
    c1[1] = (4 * lg + 2 + (li & 1) > li) ? ((li & 1) ? 0xffff0000u : 0xffffu) : 0u;
    c1[2] = 133120u + (w >> 1) * 2048 + lane * 8;
  } else if (slot < 16) {
    s0[12] = 4 + (w >> 1); wp = rQp; wn = rQn;
    op = m.x - 512 * 1024; on = 5 << 20;
    v0[3] = (p * 32 + 62) * 32768 + (w * 4 + lg) * 2048 + ((li ^ (lg * 4)) << 4); v0[7] = 2048 + (li >> 2) * 8208 + (li & 3) * 256 + lg * 16;
    s0[13] = blk0(0);
    s0[14] = 66048 + (w >> 1) * 4128;
    s0[15] = w * 8 * 6144 + slot * 128 + 393216;
    s1[2] = (p * 32 + 61) * 32768;
    s1[3] = (p * 32 + 61) * 32768 + 14336;
    s1[4] = (p * 32 + 32) * 32768;
    s1[5] = (66048 + (w >> 1) * 4128) ^ ((w >> 1) * 4128);
    v0[0] = (lane >> 3) * 6144 + (((lane & 7) ^ ((lane >> 2) & 4)) << 4) + 0u;
    v0[1] = (lane >> 3) * 6144 + (((lane & 7) ^ ((lane >> 2) & 4)) << 4) + 2048u;
    v0[2] = (lane >> 3) * 6144 + (((lane & 7) ^ ((lane >> 2) & 4)) << 4) + 4096u;
    v0[4] = TA(1, w >> 1, 0);
    v0[5] = TA(1, w >> 1, 8);
    v0[6] = TA(2, w >> 1, 8);
    c0[0] = 66048u + XRD;
    c0[1] = (66048u + XRD) ^ XRD;
    c0[2] = lane ? 0x80000000u : 0u;
    c0[3] = 144384u + lane * 4;
    c0[4] = 133120u + (w >> 1) * 2048 + l16;
    c0[5] = 141312u + li * 8;
    c0[6] = 141312u + lg * 32;
    c0[7] = (0 + (w >> 1)) * 2048 + l16;
    c0[8] = (2 + (w >> 1)) * 2048 + l16;
    c0[9] = (4 + (w >> 1)) * 2048 + l16;
    c0[10] = (6 + (w >> 1)) * 2048 + l16;
    c0[11] = (lane >> 1) == 24 && p ? FW(p - 1, 48 + (slot & 7), 2) : FW(p, 60 + ((lane & 15) >> 2), lane & 3);
    c0[12] = lg == 1 ? FW(p, 16 + li, 0) : (lane >> 1) == 24 && p ? FW(p - 1, 48 + (slot & 7), 2) : FW(p, 60 + ((lane & 15) >> 2), lane & 3);
    c0[13] = (unsigned)-26;
    c0[14] = 937984u + lane * 8;
  } else if (slot >> 3 == 6) {
    s0[12] = 8 + (w); wp = rOp; wn = rOn;
    op = m.x - 512 * 1024; on = 5 << 20;
    s0[13] = blk0(-2);
    s0[14] = w * 8192;
    s0[15] = w * 8 * 2048 + (slot - 48) * 256 + rot * 524288;
    s1[2] = (p * 32 + 63) * 32768;
    s1[4] = (p * 32 + 32) * 32768;
    s1[5] = w * 8 * 2048 + (slot - 48) * 256 + 2097152u;
    v0[0] = (lane >> 3) * 2048 + (((lane & 7) ^ ((lane >> 2) & 4)) << 4) + 0u;
    v0[1] = (lane >> 3) * 2048 + (((lane & 7) ^ ((lane >> 2) & 4)) << 4) + 128u;
    v0[4] = TA(w >> 1, w & 1, 0);
    v0[5] = TA(w >> 1, w & 1, 8);
    c0[0] = l16;
    c0[1] = lane ? 0x80000000u : 0u;
    c0[2] = 144384u + lane * 4;
    c0[3] = 659456u + ((slot - 48) * 4 + w) * 1024 + l16;
    c0[4] = 940032u + wr * 8192 + l16;
    c0[5] = 944128u + wr * 8192 + l16;
    c0[6] = FW(p, (lane & 31) >> 1, (lane & 1) * 2);
    c0[7] = 1u;
    c0[8] = li * 2048 + ((slot - 48) * 4 + w) * 64 + lg * 16;
    c0[9] = 142336u + ((li * 2 + (lg >> 1)) * 8 + w * 2 + (lg & 1)) * 4;
    c0[10] = 142336u + l16;
    c0[11] = (lane & 1) ? 0x80000000u : 625664u + (slot - 48) * 128 + (lane >> 1) * 4;
  } else if (slot < 32) {
    s0[12] = 12 + (w); wp = r1p; wn = r1n;
    s0[13] = blk0(-2);
    s0[14] = w * 8192;
    s0[15] = w * 8 * 7168 + (slot - 16) * 384 + rot * 1835008;
    s1[5] = w * 8 * 7168 + (slot - 16) * 384 + 7340032u;
    v0[0] = (lane >> 3) * 7168 + (((lane & 7) ^ ((lane >> 2) & 4)) << 4) + 0u;
    v0[1] = (lane >> 3) * 7168 + (((lane & 7) ^ ((lane >> 2) & 4)) << 4) + 128u;
    v0[2] = (lane >> 3) * 7168 + (((lane & 7) ^ ((lane >> 2) & 4)) << 4) + 256u;
    v0[4] = TA(((w >> 1) * 3 + (w & 1)) >> 1, ((w >> 1) * 3 + (w & 1)) & 1, 0);
    v0[5] = TA(((w >> 1) * 3 + (w & 1)) >> 1, ((w >> 1) * 3 + (w & 1)) & 1, 8);
    v0[6] = TO(((w >> 1) * 3 + 2) >> 1, ((w >> 1) * 3 + 2) & 1, w & 1);
    c0[0] = l16;
    c0[1] = lane ? 0x80000000u : 0u;
    c0[2] = 144384u + lane * 4;
    c0[3] = 378880u + ((slot - 16) * 6 + (w >> 1) * 3 + (w & 1)) * 1024 + l16;
    c0[4] = !(lg & 1) ? 378880u + ((slot - 16) * 6 + (w >> 1) * 3 + 2) * 1024 + ((w & 1) * 2 + (lg >> 1)) * 256 + li * 16 : 0x80000000u;
    c0[5] = 972800u + wr * 8192 + l16;
    c0[6] = 976896u + wr * 8192 + l16;
    c0[7] = FW(p, 48 + ((lane & 31) >> 2), lane & 3);
    c0[8] = 1u;
    c0[9] = 141312u + li * 8;
    c0[10] = 939008u + lane * 8;
    c0[12] = BIAS(m.b1, 3584, ((slot - 16) * 6 + (w >> 1) * 3 + (w & 1)) * 32 + lg * 8 + 0);
    c0[13] = BIAS(m.b1, 3584, ((slot - 16) * 6 + (w >> 1) * 3 + (w & 1)) * 32 + lg * 8 + 1);
    c0[14] = BIAS(m.b1, 3584, ((slot - 16) * 6 + (w >> 1) * 3 + (w & 1)) * 32 + lg * 8 + 2);
    c0[15] = BIAS(m.b1, 3584, ((slot - 16) * 6 + (w >> 1) * 3 + (w & 1)) * 32 + lg * 8 + 3);
    c1[0] = BIAS(m.b1, 3584, ((slot - 16) * 6 + (w >> 1) * 3 + (w & 1)) * 32 + lg * 8 + 4);
    c1[1] = BIAS(m.b1, 3584, ((slot - 16) * 6 + (w >> 1) * 3 + (w & 1)) * 32 + lg * 8 + 5);
    c1[2] = BIAS(m.b1, 3584, ((slot - 16) * 6 + (w >> 1) * 3 + (w & 1)) * 32 + lg * 8 + 6);
    c1[3] = BIAS(m.b1, 3584, ((slot - 16) * 6 + (w >> 1) * 3 + (w & 1)) * 32 + lg * 8 + 7);
    c1[4] = BIAS(m.b1, 3584, ((slot - 16) * 6 + (w >> 1) * 3 + 2) * 32 + (w & 1) * 16 + lg * 4 + 0);
    c1[5] = BIAS(m.b1, 3584, ((slot - 16) * 6 + (w >> 1) * 3 + 2) * 32 + (w & 1) * 16 + lg * 4 + 1);
    c1[6] = BIAS(m.b1, 3584, ((slot - 16) * 6 + (w >> 1) * 3 + 2) * 32 + (w & 1) * 16 + lg * 4 + 2);
    c1[7] = BIAS(m.b1, 3584, ((slot - 16) * 6 + (w >> 1) * 3 + 2) * 32 + (w & 1) * 16 + lg * 4 + 3);
  } else if (slot >> 2 == 14) {
    s0[12] = 16 + (w); wp = r1p; wn = r1n;
    s0[13] = blk0(-2);
    s0[14] = w * 8192;
    s0[15] = w * 8 * 7168 + (slot - 32) * 256 + rot * 1835008;
    s1[5] = w * 8 * 7168 + (slot - 32) * 256 + 7340032u;
    v0[0] = (lane >> 3) * 7168 + (((lane & 7) ^ ((lane >> 2) & 4)) << 4) + 0u;
    v0[1] = (lane >> 3) * 7168 + (((lane & 7) ^ ((lane >> 2) & 4)) << 4) + 128u;
    v0[4] = TA(w >> 1, w & 1, 0);
    v0[5] = TA(w >> 1, w & 1, 8);
    c0[0] = l16;
    c0[1] = lane ? 0x80000000u : 0u;
    c0[2] = 144384u + lane * 4;
    c0[3] = 378880u + ((slot - 32) * 4 + w) * 1024 + l16;
    c0[4] = 972800u + wr * 8192 + l16;
    c0[5] = 976896u + wr * 8192 + l16;
    c0[6] = FW(p, 48 + ((lane & 31) >> 2), lane & 3);
    c0[7] = 1u;
    c0[8] = 141312u + li * 8;
    c0[9] = 939008u + lane * 8;
    c0[10] = BIAS(m.b1, 3584, ((slot - 32) * 4 + w) * 32 + lg * 8 + 0);
    c0[11] = BIAS(m.b1, 3584, ((slot - 32) * 4 + w) * 32 + lg * 8 + 1);
    c0[12] = BIAS(m.b1, 3584, ((slot - 32) * 4 + w) * 32 + lg * 8 + 2);
    c0[13] = BIAS(m.b1, 3584, ((slot - 32) * 4 + w) * 32 + lg * 8 + 3);
    c0[14] = BIAS(m.b1, 3584, ((slot - 32) * 4 + w) * 32 + lg * 8 + 4);
    c0[15] = BIAS(m.b1, 3584, ((slot - 32) * 4 + w) * 32 + lg * 8 + 5);
    c1[0] = BIAS(m.b1, 3584, ((slot - 32) * 4 + w) * 32 + lg * 8 + 6);
    c1[1] = BIAS(m.b1, 3584, ((slot - 32) * 4 + w) * 32 + lg * 8 + 7);
  } else if (slot < 48) {
    s0[12] = 20 + (w); wp = r2p; wn = r2n;
    s0[13] = blk0(-2);
    s0[14] = w * 6144;
    s0[15] = (((slot - 32) >> 2) * 768 + w * 8) * 2048 + (slot & 3) * 512 + rot * 393216;
    s1[5] = (((slot - 32) >> 2) * 768 + w * 8) * 2048 + (slot & 3) * 512 + 1572864u;
    v0[0] = (lane >> 3) * 2048 + (((lane & 7) ^ ((lane >> 2) & 4)) << 4) + 0u;
    v0[1] = (lane >> 3) * 2048 + (((lane & 7) ^ ((lane >> 2) & 4)) << 4) + 128u;
    v0[2] = (lane >> 3) * 2048 + (((lane & 7) ^ ((lane >> 2) & 4)) << 4) + 256u;
    v0[3] = (lane >> 3) * 2048 + (((lane & 7) ^ ((lane >> 2) & 4)) << 4) + 384u;
    v0[4] = TA(w, 0, 0);
    v0[5] = TA(w, 0, 8);
    v0[6] = TA(w, 1, 0);
    v0[7] = TA(w, 1, 8);
    c0[0] = l16;
    c0[1] = lane ? 0x80000000u : 0u;
    c0[2] = 144384u + lane * 4;
    c0[3] = 1005568u + ((slot - 32) >> 2) * 24576 + wr * 6144 + l16;
    c0[4] = 1009664u + ((slot - 32) >> 2) * 24576 + wr * 6144 + l16;
    c0[5] = FW(p, (lane >= 48 && slot >= 44 ? 56 : (slot - 16) & ~3) + (li >> 2), lane & 3);
    c0[6] = 1u;
    c0[7] = 493568u + (slot - 32) * 8192 + w * 2048 + l16;
  } else if (true) {
    s0[12] = 24 + (w); wp = r2p; wn = r2n;
    const u16* const b2p = arg_b2();
    s0[13] = blk0(-3);
    s0[14] = w * 4096;
    s0[15] = (3072 + w * 8) * 2048 + (slot - 60) * 512 + rot * 262144;
    s1[2] = (p * 32 + 31) * 32768 + 4194304;
    s1[5] = (3072 + w * 8) * 2048 + (slot - 60) * 512 + 1048576u;
    v0[0] = (lane >> 3) * 2048 + (((lane & 7) ^ ((lane >> 2) & 4)) << 4) + 0u;
    v0[1] = (lane >> 3) * 2048 + (((lane & 7) ^ ((lane >> 2) & 4)) << 4) + 128u;
    v0[2] = (lane >> 3) * 2048 + (((lane & 7) ^ ((lane >> 2) & 4)) << 4) + 256u;
    v0[3] = (lane >> 3) * 2048 + (((lane & 7) ^ ((lane >> 2) & 4)) << 4) + 384u;
    v0[4] = TA(w, 0, 0);
    v0[5] = TA(w, 0, 8);
    v0[6] = TA(w, 1, 0);
    v0[7] = TA(w, 1, 8);
    c0[0] = l16;
    c0[1] = lane ? 0x80000000u : 0u;
    c0[2] = 144384u + lane * 4;
    c0[3] = 1417216u + wr * 4096 + l16;
    c0[4] = FW(p, 32 + (lane & 12) + slot - 60, lane & 3);
    c0[5] = 1u;
    c0[6] = 806912u + (slot - 60) * 8192 + w * 2048 + l16;
    c0[7] = 839680u + (slot - 60) * 8192 + w * 2048 + l16;
    c0[8] = 872448u + (slot - 60) * 8192 + w * 2048 + l16;
    c0[9] = 905216u + (slot - 60) * 8192 + w * 2048 + l16;
    c0[10] = 972800u + (slot - 60) * 8192 + w * 2048 + l16;
    c0[11] = (lane >> 3) * 2048 + (lane & 7) * 16 + (slot - 60) * 512 + w * 128;
    c0[12] = ((lane >> 3) + 8) * 2048 + (lane & 7) * 16 + (slot - 60) * 512 + w * 128;
    c0[14] = 133120u + w * 2048 + li * 128 + (((0 + lg) ^ (li & 7)) << 4);
    c0[15] = 133120u + w * 2048 + li * 128 + (((4 + lg) ^ (li & 7)) << 4);
    c1[0] = 133120u + w * 2048 + (lane >> 3) * 128 + (((lane ^ (lane >> 3)) & 7) << 4);
    c1[1] = 142336u + ((li * 2 + (lg >> 1)) * 8 + w * 2 + (lg & 1)) * 4;
    c1[2] = 142336u + l16;
    c1[3] = (lane & 1) ? 0x80000000u : 10024960u + (slot - 60) * 128 + (lane >> 1) * 4;
    c1[4] = BIAS(b2p, 1024, (slot - 60) * 256 + w * 64 + 0 + lg * 8);
    c1[5] = BIAS(b2p, 1024, (slot - 60) * 256 + w * 64 + 1 + lg * 8);
    c1[6] = BIAS(b2p, 1024, (slot - 60) * 256 + w * 64 + 2 + lg * 8);
    c1[7] = BIAS(b2p, 1024, (slot - 60) * 256 + w * 64 + 3 + lg * 8);
    c1[8] = BIAS(b2p, 1024, (slot - 60) * 256 + w * 64 + 4 + lg * 8);
    c1[9] = BIAS(b2p, 1024, (slot - 60) * 256 + w * 64 + 5 + lg * 8);
    c1[10] = BIAS(b2p, 1024, (slot - 60) * 256 + w * 64 + 6 + lg * 8);
    c1[11] = BIAS(b2p, 1024, (slot - 60) * 256 + w * 64 + 7 + lg * 8);
    c1[12] = BIAS(b2p, 1024, (slot - 60) * 256 + w * 64 + 32 + lg * 8);
    c1[13] = BIAS(b2p, 1024, (slot - 60) * 256 + w * 64 + 33 + lg * 8);
    c1[14] = BIAS(b2p, 1024, (slot - 60) * 256 + w * 64 + 34 + lg * 8);
    c1[15] = BIAS(b2p, 1024, (slot - 60) * 256 + w * 64 + 35 + lg * 8);
    c2[0] = BIAS(b2p, 1024, (slot - 60) * 256 + w * 64 + 36 + lg * 8);
    c2[1] = BIAS(b2p, 1024, (slot - 60) * 256 + w * 64 + 37 + lg * 8);
    c2[2] = BIAS(b2p, 1024, (slot - 60) * 256 + w * 64 + 38 + lg * 8);
    c2[3] = BIAS(b2p, 1024, (slot - 60) * 256 + w * 64 + 39 + lg * 8);
    c2[4] = (lg == (li >> 2) && ((li & 3) >> 1) + 0 == 0) ? ((li & 1) ? 0x3f800000u : 0x3f80u) : 0u;
    c2[5] = (lg == (li >> 2) && ((li & 3) >> 1) + 0 == 1) ? ((li & 1) ? 0x3f800000u : 0x3f80u) : 0u;
    c2[6] = (lg == (li >> 2) && ((li & 3) >> 1) + 0 == 2) ? ((li & 1) ? 0x3f800000u : 0x3f80u) : 0u;
    c2[7] = (lg == (li >> 2) && ((li & 3) >> 1) + 0 == 3) ? ((li & 1) ? 0x3f800000u : 0x3f80u) : 0u;
    c2[8] = (lg == (li >> 2) && ((li & 3) >> 1) + 2 == 0) ? ((li & 1) ? 0x3f800000u : 0x3f80u) : 0u;
    c2[9] = (lg == (li >> 2) && ((li & 3) >> 1) + 2 == 1) ? ((li & 1) ? 0x3f800000u : 0x3f80u) : 0u;
    c2[10] = (lg == (li >> 2) && ((li & 3) >> 1) + 2 == 2) ? ((li & 1) ? 0x3f800000u : 0x3f80u) : 0u;
    c2[11] = (lg == (li >> 2) && ((li & 3) >> 1) + 2 == 3) ? ((li & 1) ? 0x3f800000u : 0x3f80u) : 0u;
  }
  s0[0] = (unsigned)(size_t)m.ws; s0[1] = (unsigned)((size_t)m.ws >> 32) & 0xffffu; s0[2] = WS_END; s0[3] = 0x00020000;
  s0[4] = (unsigned)(size_t)wp; s0[5] = (unsigned)((size_t)wp >> 32) & 0xffffu; s0[6] = wn; s0[7] = 0x00020000;
  s0[8] = (unsigned)(size_t)op; s0[9] = (unsigned)((size_t)op >> 32) & 0xffffu; s0[10] = on; s0[11] = 0x00020000;
  s1[0] = FW(p, slot, w); s1[1] = w * 1024; s1[7] = DBG + ((p * 64 + slot) * 4 + w) * 1024;
#pragma unroll
  for (int i = 0; i < 16; ++i) s0[i] = __builtin_amdgcn_readfirstlane(s0[i]);
#pragma unroll
  for (int i = 0; i < 8; ++i) s1[i] = __builtin_amdgcn_readfirstlane(s1[i]);
  asm volatile(
R"(
.macro mav d,a,b
v_mfma_f32_16x16x32_bf16 v[\d:\d+3], a[\a:\a+3], v[\b:\b+3], v[\d:\d+3]
.endm
.macro Ta d,a,o
ds_read_b64_tr_b16 a[\d:\d+1], v\a offset:\o
ds_read_b64_tr_b16 a[\d+2:\d+3], v\a offset:\o+512
.endm
.macro mva d,a,b
v_mfma_f32_16x16x32_bf16 v[\d:\d+3], v[\a:\a+3], a[\b:\b+3], v[\d:\d+3]
.endm
.macro mvv d,a,b
v_mfma_f32_16x16x32_bf16 v[\d:\d+3], v[\a:\a+3], v[\b:\b+3], v[\d:\d+3]
.endm
.macro Tv d,a,o
ds_read_b64_tr_b16 v[\d:\d+1], v\a offset:\o
ds_read_b64_tr_b16 v[\d+2:\d+3], v\a offset:\o+512
.endm
.macro R d,o
ds_read_b128 v[\d:\d+3], v80 offset:\o
.endm
.macro D v,o
buffer_load_dwordx4 v\v, s[36:39], s49 offen offset:\o sc1 lds
.endm
.macro D2 v,s,o
buffer_load_dwordx4 v\v, s[44:47], s\s offen offset:\o sc1 lds
.endm
.macro E v,o
s_add_u32 m0, s53, \o
s_nop 0
buffer_load_dwordx4 v\v, s[40:43], s51 offen lds
.endm
s_branch La
.p2align 12
La:
s_nop 4
s_mov_b32 s61, 200000
s_mov_b64 s[62:63], 255
s_mov_b32 s64, 0x3f803f80
s_mov_b32 s65, 0x3e8293ee
s_lshr_b32 s60, s48, 2
Lr0:
s_cmp_eq_u32 s60, 0
s_cbranch_scc0 Lr1
s_lshr_b32 s66, s53, 10
s_mul_i32 s66, s66, 8208
s_mov_b32 s67, 0x8000
s_mov_b64 exec, 0
s_cmp_eq_u32 s53, 0
s_cbranch_scc1 LsQ0
s_branch LsQ2
LwQA:
s_mov_b64 exec, -1
E 0,86016
E 1,90112
E 2,94208
s_add_u32 s51, s51, 196608
E 0,98304
E 1,102400
E 2,106496
s_add_u32 s51, s51, 196608
E 0,61440
E 1,65536
s_add_u32 m0, s66, 2816
s_nop 0
buffer_load_dwordx4 v3, s[44:47], s67 offen offset:256 lds
E 2,69632
s_add_u32 s51, s51, 196608
E 0,73728
s_add_u32 m0, s66, 6912
s_nop 0
buffer_load_dwordx4 v3, s[44:47], 0 offen offset:256 lds
E 1,77824
E 2,81920
s_add_u32 s51, s51, 196608
v_lshrrev_b32 v12, 2, v7
v_and_b32 v12, 0xc0, v12
v_xor_b32 v13, 0, v12
v_add_u32 v8, v7, v13
v_xor_b32 v13, 64, v12
v_add_u32 v9, v7, v13
v_xor_b32 v13, 128, v12
v_add_u32 v10, v7, v13
v_xor_b32 v13, 192, v12
v_add_u32 v11, v7, v13
v_mov_b32 v114, s64
v_mov_b32 v115, s64
v_mov_b32 v116, s64
v_mov_b32 v117, s64
s_mov_b32 s92, 0xaaaaaaaa
s_mov_b32 s93, s92
s_mov_b32 s94, 0xcccccccc
s_mov_b32 s95, s94
s_mov_b32 s96, 0xf0000f
s_mov_b32 s97, 0xf0000f00
s_waitcnt vmcnt(14)
s_barrier
v_add_u32 v4, 69632, v4
v_add_u32 v5, 69632, v5
v_add_u32 v6, 69632, v6
Ta 0,4,40960
Ta 4,5,40960
Ta 8,6,40960
Ta 12,4,53248
Ta 16,5,53248
Ta 20,6,53248
ds_read_b128 v[38:41], v8 offset:0
ds_read_b128 v[42:45], v9 offset:0
ds_read_b128 v[46:49], v8 offset:4096
ds_read_b128 v[50:53], v9 offset:4096
v_accvgpr_read_b32 v79, a255
v_mov_b32 v78, 0x100
v_add_u32 v79, 0x9e3779b9, v79
buffer_store_dwordx2 v[78:79], off, s[36:39], s52 sc1
E 0,36864
E 1,40960
E 2,45056
s_add_u32 s51, s51, 196608
E 0,49152
E 1,53248
E 2,57344
s_add_u32 s51, s51, 196608
s_waitcnt lgkmcnt(0)
s_waitcnt vmcnt(15)
s_barrier
E 0,110592
Ta 24,4,16384
v_mfma_f32_16x16x32_bf16 v[74:77], v[114:117], v[38:41], 0
v_mfma_f32_16x16x32_bf16 v[118:121], v[38:41], v[38:41], 0
Ta 28,5,16384
mvv 74,114,42
Ta 32,6,16384
E 1,114688
mvv 118,42,42
Ta 36,4,28672
v_mfma_f32_16x16x32_bf16 v[62:65], a[0:3], v[114:117], 0
v_mfma_f32_16x16x32_bf16 v[66:69], a[4:7], v[114:117], 0
Ta 40,5,28672
s_add_u32 m0, s66, 3584
s_nop 0
buffer_load_dwordx4 v3, s[44:47], s67 offen offset:512 lds
v_mfma_f32_16x16x32_bf16 v[70:73], v[114:117], a[8:11], 0
Ta 44,6,28672
v_mfma_f32_16x16x32_bf16 v[14:17], a[0:3], v[38:41], 0
ds_read_b128 v[54:57], v10 offset:0
v_mfma_f32_16x16x32_bf16 v[18:21], a[4:7], v[38:41], 0
E 2,118784
s_add_u32 s51, s51, 196608
v_mfma_f32_16x16x32_bf16 v[22:25], v[38:41], a[8:11], 0
ds_read_b128 v[58:61], v11 offset:0
v_mfma_f32_16x16x32_bf16 v[26:29], a[0:3], v[46:49], 0
ds_read_b128 v[100:103], v10 offset:4096
E 0,122880
v_mfma_f32_16x16x32_bf16 v[30:33], a[4:7], v[46:49], 0
ds_read_b128 v[104:107], v11 offset:4096
v_mfma_f32_16x16x32_bf16 v[34:37], v[46:49], a[8:11], 0
mav 62,12,114
s_add_u32 m0, s66, 7680
s_nop 0
buffer_load_dwordx4 v3, s[44:47], 0 offen offset:512 lds
mav 66,16,114
mva 70,114,20
mav 14,12,42
E 1,126976
mav 18,16,42
mva 22,42,20
mav 26,12,50
E 2,131072
s_add_u32 s51, s51, 196608
mav 30,16,50
mva 34,50,20
s_waitcnt lgkmcnt(0)
s_waitcnt vmcnt(15)
s_barrier
v_add_u32 v4, -49152, v4
v_add_u32 v5, -49152, v5
v_add_u32 v6, -49152, v6
E 0,86016
Ta 48,4,40960
mvv 74,114,54
mvv 118,54,54
Ta 52,5,40960
mvv 74,114,58
Ta 56,6,40960
mvv 118,58,58
E 1,90112
Ta 60,4,53248
mav 62,24,114
mav 66,28,114
Ta 64,5,53248
mva 70,114,32
Ta 68,6,53248
mav 14,24,54
E 2,94208
s_add_u32 s51, s51, 196608
ds_read_b128 v[38:41], v8 offset:1024
mav 18,28,54
mva 22,54,32
ds_read_b128 v[42:45], v9 offset:1024
mav 26,24,100
ds_read_b128 v[46:49], v8 offset:5120
E 0,98304
mav 30,28,100
ds_read_b128 v[50:53], v9 offset:5120
mva 34,100,32
mav 62,36,114
mav 66,40,114
E 1,102400
mva 70,114,44
mav 14,36,58
mav 18,40,58
mva 22,58,44
E 2,106496
s_add_u32 s51, s51, 196608
mav 26,36,104
mav 30,40,104
mva 34,104,44
s_waitcnt lgkmcnt(0)
s_waitcnt vmcnt(14)
s_barrier
E 0,61440
Ta 72,4,16384
mvv 74,114,38
mvv 118,38,38
Ta 76,5,16384
mvv 74,114,42
Ta 80,6,16384
E 1,65536
mvv 118,42,42
Ta 84,4,28672
mav 62,48,114
mav 66,52,114
Ta 88,5,28672
s_add_u32 m0, s66, 4352
s_nop 0
buffer_load_dwordx4 v3, s[44:47], s67 offen offset:768 lds
mva 70,114,56
Ta 92,6,28672
mav 14,48,38
ds_read_b128 v[54:57], v10 offset:1024
mav 18,52,38
E 2,69632
s_add_u32 s51, s51, 196608
mva 22,38,56
ds_read_b128 v[58:61], v11 offset:1024
mav 26,48,46
ds_read_b128 v[100:103], v10 offset:5120
E 0,73728
mav 30,52,46
ds_read_b128 v[104:107], v11 offset:5120
mva 34,46,56
mav 62,60,114
s_add_u32 m0, s66, 8448
s_nop 0
buffer_load_dwordx4 v3, s[44:47], 0 offen offset:768 lds
mav 66,64,114
mva 70,114,68
mav 14,60,42
E 1,77824
mav 18,64,42
mva 22,42,68
mav 26,60,50
E 2,81920
s_add_u32 s51, s51, 196608
mav 30,64,50
mva 34,50,68
s_waitcnt lgkmcnt(0)
s_waitcnt vmcnt(14)
s_barrier
v_add_u32 v4, 49152, v4
v_add_u32 v5, 49152, v5
v_add_u32 v6, 49152, v6
E 0,36864
Ta 96,4,40960
mvv 74,114,54
mvv 118,54,54
Ta 100,5,40960
mvv 74,114,58
Ta 104,6,40960
mvv 118,58,58
E 1,40960
Ta 108,4,53248
mav 62,72,114
mav 66,76,114
Ta 112,5,53248
mva 70,114,80
Ta 116,6,53248
mav 14,72,54
E 2,45056
s_add_u32 s51, s51, 196608
ds_read_b128 v[38:41], v8 offset:2048
mav 18,76,54
mva 22,54,80
ds_read_b128 v[42:45], v9 offset:2048
mav 26,72,100
ds_read_b128 v[46:49], v8 offset:6144
E 0,49152
mav 30,76,100
ds_read_b128 v[50:53], v9 offset:6144
mva 34,100,80
mav 62,84,114
mav 66,88,114
E 1,53248
mva 70,114,92
mav 14,84,58
mav 18,88,58
mva 22,58,92
E 2,57344
s_add_u32 s51, s51, 196608
mav 26,84,104
mav 30,88,104
mva 34,104,92
s_waitcnt lgkmcnt(0)
s_waitcnt vmcnt(14)
s_barrier
E 0,110592
Ta 120,4,16384
mvv 74,114,38
mvv 118,38,38
Ta 124,5,16384
mvv 74,114,42
Ta 128,6,16384
E 1,114688
mvv 118,42,42
Ta 132,4,28672
mav 62,96,114
mav 66,100,114
Ta 136,5,28672
s_add_u32 m0, s66, 1024
s_nop 0
buffer_load_dwordx4 v3, s[44:47], s67 offen offset:1024 lds
mva 70,114,104
Ta 140,6,28672
mav 14,96,38
ds_read_b128 v[54:57], v10 offset:2048
mav 18,100,38
E 2,118784
s_add_u32 s51, s51, 196608
mva 22,38,104
ds_read_b128 v[58:61], v11 offset:2048
mav 26,96,46
ds_read_b128 v[100:103], v10 offset:6144
E 0,122880
mav 30,100,46
ds_read_b128 v[104:107], v11 offset:6144
mva 34,46,104
mav 62,108,114
s_add_u32 m0, s66, 5120
s_nop 0
buffer_load_dwordx4 v3, s[44:47], 0 offen offset:1024 lds
mav 66,112,114
mva 70,114,116
mav 14,108,42
E 1,126976
mav 18,112,42
mva 22,42,116
mav 26,108,50
E 2,131072
s_add_u32 s51, s51, 196608
mav 30,112,50
mva 34,50,116
s_waitcnt lgkmcnt(0)
s_waitcnt vmcnt(14)
s_barrier
v_add_u32 v4, -49152, v4
v_add_u32 v5, -49152, v5
v_add_u32 v6, -49152, v6
E 0,86016
Ta 144,4,40960
mvv 74,114,54
mvv 118,54,54
Ta 148,5,40960
mvv 74,114,58
Ta 152,6,40960
mvv 118,58,58
E 1,90112
Ta 156,4,53248
mav 62,120,114
mav 66,124,114
Ta 160,5,53248
mva 70,114,128
Ta 164,6,53248
mav 14,120,54
E 2,94208
s_add_u32 s51, s51, 196608
ds_read_b128 v[38:41], v8 offset:3072
mav 18,124,54
mva 22,54,128
ds_read_b128 v[42:45], v9 offset:3072
mav 26,120,100
ds_read_b128 v[46:49], v8 offset:7168
E 0,98304
mav 30,124,100
ds_read_b128 v[50:53], v9 offset:7168
mva 34,100,128
mav 62,132,114
mav 66,136,114
E 1,102400
mva 70,114,140
mav 14,132,58
mav 18,136,58
mva 22,58,140
E 2,106496
s_add_u32 s51, s51, 196608
mav 26,132,104
mav 30,136,104
mva 34,104,140
s_waitcnt lgkmcnt(0)
s_waitcnt vmcnt(14)
s_barrier
E 0,61440
Ta 168,4,16384
mvv 74,114,38
mvv 118,38,38
Ta 172,5,16384
mvv 74,114,42
Ta 176,6,16384
E 1,65536
mvv 118,42,42
Ta 180,4,28672
mav 62,144,114
mav 66,148,114
Ta 184,5,28672
s_add_u32 m0, s66, 1792
s_nop 0
buffer_load_dwordx4 v3, s[44:47], s67 offen offset:1280 lds
mva 70,114,152
Ta 188,6,28672
mav 14,144,38
ds_read_b128 v[54:57], v10 offset:3072
mav 18,148,38
E 2,69632
s_add_u32 s51, s51, 196608
mva 22,38,152
ds_read_b128 v[58:61], v11 offset:3072
mav 26,144,46
ds_read_b128 v[100:103], v10 offset:7168
E 0,73728
mav 30,148,46
ds_read_b128 v[104:107], v11 offset:7168
mva 34,46,152
mav 62,156,114
LsQ0:
s_cbranch_execz LsQ4
s_add_u32 m0, s66, 5888
s_nop 0
buffer_load_dwordx4 v3, s[44:47], 0 offen offset:1280 lds
mav 66,160,114
mva 70,114,164
mav 14,156,42
E 1,77824
mav 18,160,42
mva 22,42,164
mav 26,156,50
E 2,81920
s_add_u32 s51, s51, 196608
mav 30,160,50
mva 34,50,164
s_waitcnt lgkmcnt(0)
s_waitcnt vmcnt(14)
s_barrier
v_add_u32 v4, 49152, v4
v_add_u32 v5, 49152, v5
v_add_u32 v6, 49152, v6
E 0,36864
Ta 192,4,40960
mvv 74,114,54
mvv 118,54,54
Ta 196,5,40960
mvv 74,114,58
Ta 200,6,40960
mvv 118,58,58
E 1,40960
Ta 204,4,53248
mav 62,168,114
mav 66,172,114
Ta 208,5,53248
mva 70,114,176
Ta 212,6,53248
mav 14,168,54
E 2,45056
s_add_u32 s51, s51, 196608
ds_read_b128 v[38:41], v8 offset:0
mav 18,172,54
mva 22,54,176
ds_read_b128 v[42:45], v9 offset:0
mav 26,168,100
ds_read_b128 v[46:49], v8 offset:4096
E 0,49152
mav 30,172,100
ds_read_b128 v[50:53], v9 offset:4096
mva 34,100,176
mav 62,180,114
mav 66,184,114
E 1,53248
mva 70,114,188
mav 14,180,58
mav 18,184,58
mva 22,58,188
E 2,57344
s_add_u32 s51, s51, 196608
mav 26,180,104
mav 30,184,104
mva 34,104,188
s_waitcnt lgkmcnt(0)
s_waitcnt vmcnt(14)
s_barrier
E 0,110592
Ta 216,4,16384
mvv 74,114,38
mvv 118,38,38
Ta 220,5,16384
mvv 74,114,42
Ta 224,6,16384
E 1,114688
mvv 118,42,42
Ta 228,4,28672
mav 62,192,114
mav 66,196,114
Ta 232,5,28672
s_add_u32 m0, s66, 2560
s_nop 0
buffer_load_dwordx4 v3, s[44:47], s67 offen offset:1536 lds
mva 70,114,200
Ta 236,6,28672
mav 14,192,38
ds_read_b128 v[54:57], v10 offset:0
mav 18,196,38
E 2,118784
s_add_u32 s51, s51, 196608
mva 22,38,200
ds_read_b128 v[58:61], v11 offset:0
mav 26,192,46
ds_read_b128 v[100:103], v10 offset:4096
E 0,122880
mav 30,196,46
ds_read_b128 v[104:107], v11 offset:4096
mva 34,46,200
mav 62,204,114
s_add_u32 m0, s66, 6656
s_nop 0
buffer_load_dwordx4 v3, s[44:47], 0 offen offset:1536 lds
mav 66,208,114
mva 70,114,212
mav 14,204,42
E 1,126976
mav 18,208,42
mva 22,42,212
mav 26,204,50
E 2,131072
s_add_u32 s51, s51, 196608
mav 30,208,50
mva 34,50,212
s_waitcnt lgkmcnt(0)
s_waitcnt vmcnt(14)
s_barrier
v_add_u32 v4, -49152, v4
v_add_u32 v5, -49152, v5
v_add_u32 v6, -49152, v6
E 0,86016
Ta 240,4,40960
mvv 74,114,54
mvv 118,54,54
Ta 244,5,40960
mvv 74,114,58
Ta 248,6,40960
mvv 118,58,58
E 1,90112
Ta 252,4,53248
mav 62,216,114
mav 66,220,114
Tv 128,5,53248
mva 70,114,224
Tv 132,6,53248
mav 14,216,54
E 2,94208
s_add_u32 s51, s51, 196608
ds_read_b128 v[38:41], v8 offset:1024
mav 18,220,54
mva 22,54,224
ds_read_b128 v[42:45], v9 offset:1024
mav 26,216,100
ds_read_b128 v[46:49], v8 offset:5120
E 0,98304
mav 30,220,100
ds_read_b128 v[50:53], v9 offset:5120
mva 34,100,224
mav 62,228,114
mav 66,232,114
E 1,102400
mva 70,114,236
mav 14,228,58
mav 18,232,58
mva 22,58,236
E 2,106496
s_add_u32 s51, s51, 196608
mav 26,228,104
mav 30,232,104
mva 34,104,236
s_waitcnt lgkmcnt(0)
s_waitcnt vmcnt(14)
s_barrier
E 0,61440
Tv 136,4,16384
mvv 74,114,38
mvv 118,38,38
Tv 140,5,16384
mvv 74,114,42
Tv 144,6,16384
E 1,65536
mvv 118,42,42
Tv 148,4,28672
mav 62,240,114
mav 66,244,114
Tv 152,5,28672
s_add_u32 m0, s66, 3328
s_nop 0
buffer_load_dwordx4 v3, s[44:47], s67 offen offset:1792 lds
mva 70,114,248
Tv 156,6,28672
mav 14,240,38
ds_read_b128 v[54:57], v10 offset:1024
mav 18,244,38
E 2,69632
s_add_u32 s51, s51, 196608
mva 22,38,248
ds_read_b128 v[58:61], v11 offset:1024
mav 26,240,46
ds_read_b128 v[100:103], v10 offset:5120
E 0,73728
mav 30,244,46
ds_read_b128 v[104:107], v11 offset:5120
mva 34,46,248
mav 62,252,114
s_add_u32 m0, s66, 7424
s_nop 0
buffer_load_dwordx4 v3, s[44:47], 0 offen offset:1792 lds
mvv 66,128,114
mvv 70,114,132
mav 14,252,42
E 1,77824
mvv 18,128,42
mvv 22,42,132
mav 26,252,50
E 2,81920
s_add_u32 s51, s51, 196608
mvv 30,128,50
mvv 34,50,132
s_waitcnt lgkmcnt(0)
s_waitcnt vmcnt(14)
s_barrier
v_add_u32 v4, 49152, v4
v_add_u32 v5, 49152, v5
v_add_u32 v6, 49152, v6
E 0,36864
Tv 160,4,40960
mvv 74,114,54
mvv 118,54,54
Tv 164,5,40960
mvv 74,114,58
Tv 168,6,40960
mvv 118,58,58
E 1,40960
Tv 172,4,53248
mvv 62,136,114
mvv 66,140,114
Tv 176,5,53248
mvv 70,114,144
Tv 180,6,53248
mvv 14,136,54
E 2,45056
s_add_u32 s51, s51, 196608
ds_read_b128 v[38:41], v8 offset:2048
mvv 18,140,54
mvv 22,54,144
ds_read_b128 v[42:45], v9 offset:2048
mvv 26,136,100
ds_read_b128 v[46:49], v8 offset:6144
E 0,49152
mvv 30,140,100
ds_read_b128 v[50:53], v9 offset:6144
mvv 34,100,144
mvv 62,148,114
mvv 66,152,114
E 1,53248
mvv 70,114,156
mvv 14,148,58
mvv 18,152,58
mvv 22,58,156
E 2,57344
s_add_u32 s51, s51, 196608
mvv 26,148,104
mvv 30,152,104
mvv 34,104,156
s_waitcnt lgkmcnt(0)
s_waitcnt vmcnt(14)
s_barrier
Tv 184,4,16384
mvv 74,114,38
mvv 118,38,38
Tv 188,5,16384
mvv 74,114,42
Tv 192,6,16384
mvv 118,42,42
Tv 196,4,28672
mvv 62,160,114
mvv 66,164,114
Tv 200,5,28672
mvv 70,114,168
Tv 204,6,28672
mvv 14,160,38
ds_read_b128 v[54:57], v10 offset:2048
mvv 18,164,38
mvv 22,38,168
ds_read_b128 v[58:61], v11 offset:2048
mvv 26,160,46
ds_read_b128 v[100:103], v10 offset:6144
mvv 30,164,46
ds_read_b128 v[104:107], v11 offset:6144
mvv 34,46,168
mvv 62,172,114
mvv 66,176,114
mvv 70,114,180
mvv 14,172,42
mvv 18,176,42
mvv 22,42,180
mvv 26,172,50
mvv 30,176,50
mvv 34,50,180
s_waitcnt lgkmcnt(0)
s_waitcnt vmcnt(6)
s_barrier
v_add_u32 v4, -49152, v4
v_add_u32 v5, -49152, v5
v_add_u32 v6, -49152, v6
Tv 208,4,40960
mvv 74,114,54
mvv 118,54,54
Tv 212,5,40960
mvv 74,114,58
Tv 216,6,40960
mvv 118,58,58
Tv 220,4,53248
mvv 62,184,114
mvv 66,188,114
Tv 224,5,53248
mvv 70,114,192
Tv 228,6,53248
mvv 14,184,54
ds_read_b128 v[38:41], v8 offset:3072
mvv 18,188,54
mvv 22,54,192
ds_read_b128 v[42:45], v9 offset:3072
mvv 26,184,100
ds_read_b128 v[46:49], v8 offset:7168
mvv 30,188,100
ds_read_b128 v[50:53], v9 offset:7168
mvv 34,100,192
mvv 62,196,114
mvv 66,200,114
mvv 70,114,204
mvv 14,196,58
mvv 18,200,58
mvv 22,58,204
mvv 26,196,104
mvv 30,200,104
mvv 34,104,204
s_waitcnt lgkmcnt(0)
s_waitcnt vmcnt(0)
s_barrier
Tv 232,4,16384
mvv 74,114,38
mvv 118,38,38
Tv 236,5,16384
mvv 74,114,42
Tv 240,6,16384
mvv 118,42,42
Tv 244,4,28672
mvv 62,208,114
mvv 66,212,114
Tv 248,5,28672
mvv 70,114,216
Tv 252,6,28672
mvv 14,208,38
ds_read_b128 v[54:57], v10 offset:3072
mvv 18,212,38
mvv 22,38,216
ds_read_b128 v[58:61], v11 offset:3072
mvv 26,208,46
ds_read_b128 v[100:103], v10 offset:7168
mvv 30,212,46
ds_read_b128 v[104:107], v11 offset:7168
mvv 34,46,216
mvv 62,220,114
mvv 66,224,114
mvv 70,114,228
mvv 14,220,42
mvv 18,224,42
mvv 22,42,228
mvv 26,220,50
mvv 30,224,50
mvv 34,50,228
s_waitcnt lgkmcnt(0)
mvv 74,114,54
mvv 118,54,54
mvv 74,114,58
mvv 118,58,58
mvv 62,232,114
mvv 66,236,114
mvv 70,114,240
mvv 14,232,54
mvv 18,236,54
mvv 22,54,240
mvv 26,232,100
mvv 30,236,100
mvv 34,100,240
v_cndmask_b32 v118, v118, v119, s[92:93]
mvv 62,244,114
v_cndmask_b32 v120, v120, v121, s[92:93]
mvv 66,248,114
v_mul_f32 v74, 0x3a800000, v74
v_cndmask_b32 v118, v118, v120, s[94:95]
mvv 70,114,252
v_mul_f32 v118, 0x3a800000, v118
mvv 14,244,58
v_fma_f32 v118, -v74, v74, v118
mvv 18,248,58
v_max_f32 v118, 0, v118
mvv 22,58,252
v_add_f32 v118, 0x3727c5ac, v118
mvv 26,244,104
v_rsq_f32 v118, v118
mvv 30,248,104
s_nop 0
v_mul_f32_e64 v119, -v74, v118
mvv 34,104,252
s_mov_b64 exec, s[96:97]
ds_write_b64 v83, v[118:119] offset:0
s_mov_b64 exec, -1
s_waitcnt lgkmcnt(0)
s_barrier
s_nop 7
v_mov_b32 v99, v70
s_and_b32 s60, s48, 3
s_cmp_eq_u32 s60, 1
s_cbranch_scc1 LvQA1
LvQA0:
LsQ1:
s_cbranch_execz LsQ5
ds_read_b64 v[122:123], v83 offset:0
ds_read_b128 v[70:73], v84 offset:0
ds_read_b128 v[74:77], v84 offset:16
s_add_u32 s49, s49, 313344
s_waitcnt lgkmcnt(0)
v_mul_f32 v71, v71, v99
v_fma_f32 v22, v22, v70, v71
v_mul_f32 v73, v73, v99
v_fma_f32 v23, v23, v72, v73
v_mul_f32 v75, v75, v99
v_fma_f32 v24, v24, v74, v75
v_mul_f32 v77, v77, v99
v_fma_f32 v25, v25, v76, v77
v_cvt_pk_bf16_f32 v8, v22, v23
v_cvt_pk_bf16_f32 v9, v24, v25
v_mul_f32 v124, v123, v62
v_fma_f32 v14, v14, v122, v124
v_mul_f32 v125, v123, v63
v_fma_f32 v15, v15, v122, v125
v_mul_f32 v124, v123, v64
v_fma_f32 v16, v16, v122, v124
v_mul_f32 v125, v123, v65
v_fma_f32 v17, v17, v122, v125
v_mul_f32 v124, v123, v66
v_fma_f32 v18, v18, v122, v124
v_mul_f32 v125, v123, v67
v_fma_f32 v19, v19, v122, v125
v_mul_f32 v124, v123, v68
v_fma_f32 v20, v20, v122, v124
v_mul_f32 v125, v123, v69
v_fma_f32 v21, v21, v122, v125
v_cvt_pk_bf16_f32 v0, v14, v15
v_cvt_pk_bf16_f32 v1, v16, v17
v_cvt_pk_bf16_f32 v2, v18, v19
v_cvt_pk_bf16_f32 v3, v20, v21
s_add_u32 s49, s49, 313344
s_barrier
ds_read_b128 v[100:103], v82 offset:0
ds_read_b64 v[108:109], v82 offset:1024
R 38,33024
R 42,33088
LsQ2:
s_cbranch_execz LsQ6
R 46,33152
R 50,33216
ds_read_b64 v[122:123], v83 offset:128
ds_read_b128 v[70:73], v84 offset:128
ds_read_b128 v[74:77], v84 offset:144
s_waitcnt lgkmcnt(0)
v_mfma_f32_16x16x32_bf16 v[14:17], a[0:3], v[38:41], 0
R 54,33280
v_mul_f32 v71, v71, v99
v_mfma_f32_16x16x32_bf16 v[18:21], a[4:7], v[38:41], 0
v_fma_f32 v34, v34, v70, v71
v_mul_f32 v73, v73, v99
v_mfma_f32_16x16x32_bf16 v[22:25], v[38:41], a[8:11], 0
v_fma_f32 v35, v35, v72, v73
v_mul_f32 v75, v75, v99
mav 14,12,42
R 58,33344
v_fma_f32 v36, v36, v74, v75
mav 18,16,42
v_mul_f32 v77, v77, v99
v_fma_f32 v37, v37, v76, v77
mva 22,42,20
v_cvt_pk_bf16_f32 v10, v34, v35
v_cvt_pk_bf16_f32 v11, v36, v37
mav 14,24,46
R 38,33408
v_mul_f32 v124, v123, v62
v_mfma_f32_16x16x32_bf16 v[70:73], v[100:103], v[0:3], v[86:89]
v_fma_f32 v26, v26, v122, v124
v_mul_f32 v125, v123, v63
mav 18,28,46
v_fma_f32 v27, v27, v122, v125
v_mul_f32 v124, v123, v64
mva 22,46,32
v_fma_f32 v28, v28, v122, v124
v_mul_f32 v125, v123, v65
mav 14,36,50
R 42,33472
v_fma_f32 v29, v29, v122, v125
mav 18,40,50
v_mul_f32 v124, v123, v66
v_fma_f32 v30, v30, v122, v124
mva 22,50,44
s_waitcnt lgkmcnt(2)
v_mul_f32 v125, v123, v67
v_fma_f32 v31, v31, v122, v125
mav 14,48,54
R 46,33536
v_mul_f32 v124, v123, v68
mav 18,52,54
v_fma_f32 v32, v32, v122, v124
v_mul_f32 v125, v123, v69
mva 22,54,56
v_fma_f32 v33, v33, v122, v125
mav 14,60,58
R 50,33600
mav 18,64,58
mva 22,58,68
s_waitcnt lgkmcnt(2)
mav 14,72,38
R 54,33664
mav 18,76,38
mva 22,38,80
buffer_store_dwordx4 v[100:103], v82, s[36:39], s54 offen sc1
mav 14,84,42
R 58,33728
mav 18,88,42
buffer_store_dwordx2 v[8:9], v98, s[36:39], s54 offen offset:1024 sc1
mva 22,42,92
s_waitcnt lgkmcnt(2)
mav 14,96,46
buffer_store_dwordx2 v[108:109], v98, s[36:39], s54 offen offset:1536 sc1
R 38,33792
mav 18,100,46
s_barrier
mva 22,46,104
ds_read_b128 v[104:107], v82 offset:4096
mav 14,108,50
ds_read_b64 v[110:111], v82 offset:5120
mav 18,112,50
R 42,33856
mva 22,50,116
s_waitcnt lgkmcnt(4)
mav 14,120,54
R 46,33920
mav 18,124,54
mva 22,54,128
mav 14,132,58
s_waitcnt lgkmcnt(2)
R 50,33984
v_mfma_f32_16x16x32_bf16 v[74:77], v[104:107], v[0:3], v[90:93]
v_cvt_pk_bf16_f32 v0, v26, v27
v_cvt_pk_bf16_f32 v1, v28, v29
mav 18,136,58
v_cvt_pk_bf16_f32 v2, v30, v31
v_cvt_pk_bf16_f32 v3, v32, v33
mva 22,58,140
mav 14,144,38
R 54,34048
v_max3_f32 v118, v70, v71, v74
mav 18,148,38
v_max3_f32 v119, v72, v73, v76
v_max_f32 v118, v118, v75
mva 22,38,152
s_waitcnt lgkmcnt(2)
v_max_f32 v119, v119, v77
v_max_f32 v120, v118, v119
mav 14,156,42
R 38,34112
v_max_f32 v121, v118, v119
mav 18,160,42
mva 22,42,164
v_permlane32_swap_b32 v120, v121
mav 14,168,46
R 42,34176
v_max_f32 v114, v120, v121
mav 18,172,46
v_max_f32 v115, v120, v121
mva 22,46,176
s_waitcnt lgkmcnt(2)
v_permlane16_swap_b32 v114, v115
mav 14,180,50
R 46,34240
v_max_f32 v120, v114, v115
mav 18,184,50
v_mul_f32 v120, s65, v120
v_fma_f32 v118, v118, s65, -v120
mva 22,50,188
v_fma_f32 v119, v119, s65, -v120
mav 14,192,54
R 50,34304
mav 18,196,54
v_exp_f32 v118, v118
mva 22,54,200
s_waitcnt lgkmcnt(2)
v_exp_f32 v119, v119
mav 14,204,38
R 54,34368
v_add_f32 v120, v118, v119
mav 18,208,38
v_add_f32 v121, v118, v119
mva 22,38,212
mav 14,216,42
R 38,34432
mav 18,220,42
v_permlane32_swap_b32 v120, v121
mva 22,42,224
s_waitcnt lgkmcnt(2)
v_add_f32 v114, v120, v121
v_add_f32 v115, v120, v121
mav 14,228,46
R 42,34496
mav 18,232,46
v_permlane16_swap_b32 v114, v115
mva 22,46,236
v_add_f32 v120, v114, v115
mav 14,240,50
R 46,34560
mav 18,244,50
v_rcp_f32 v120, v120
mva 22,50,248
s_waitcnt lgkmcnt(2)
v_mul_f32 v118, v118, v120
v_mul_f32 v119, v119, v120
mav 14,252,54
R 50,34624
v_cvt_pk_bf16_f32 v118, v118, v118
mvv 18,128,54
v_cvt_pk_bf16_f32 v119, v119, v119
v_and_b32 v114, v118, v94
mvv 22,54,132
v_and_b32 v115, v119, v95
v_and_b32 v116, v118, v96
mvv 14,136,38
R 54,34688
v_and_b32 v117, v119, v97
mvv 18,140,38
mvv 22,38,144
s_waitcnt lgkmcnt(2)
v_mfma_f32_16x16x32_bf16 v[70:73], v[8:11], v[114:117], 0
v_mfma_f32_16x16x32_bf16 v[74:77], v[108:111], v[114:117], 0
mvv 14,148,42
R 38,34752
mvv 18,152,42
mvv 22,42,156
mvv 14,160,46
R 42,34816
mvv 18,164,46
v_cvt_pk_bf16_f32 v114, v70, v71
v_cvt_pk_bf16_f32 v115, v72, v73
mvv 22,46,168
s_waitcnt lgkmcnt(2)
v_cvt_pk_bf16_f32 v116, v74, v75
v_cvt_pk_bf16_f32 v117, v76, v77
mvv 14,172,50
R 46,34880
mvv 18,176,50
buffer_store_dwordx4 v[114:117], v82, s[36:39], s49 offen sc1
s_add_u32 s49, s49, 313344
mvv 22,50,180
mvv 14,184,54
R 50,34944
mvv 18,188,54
mvv 22,54,192
s_waitcnt lgkmcnt(2)
mvv 14,196,38
R 54,35008
mvv 18,200,38
mvv 22,38,204
mvv 14,208,42
R 38,0
mvv 18,212,42
ds_read_b64 v[122:123], v83 offset:0
mvv 22,42,216
s_waitcnt lgkmcnt(3)
ds_read_b128 v[70:73], v84 offset:0
mvv 14,220,46
R 42,64
mvv 18,224,46
ds_read_b128 v[74:77], v84 offset:16
mvv 22,46,228
mvv 14,232,50
R 46,128
mvv 18,236,50
mvv 22,50,240
s_waitcnt lgkmcnt(3)
mvv 14,244,54
R 50,192
mvv 18,248,54
mvv 22,54,252
v_mfma_f32_16x16x32_bf16 v[26:29], a[0:3], v[38:41], 0
s_waitcnt lgkmcnt(1)
R 54,256
v_mul_f32 v71, v71, v99
v_mfma_f32_16x16x32_bf16 v[30:33], a[4:7], v[38:41], 0
v_mul_f32 v73, v73, v99
v_mul_f32 v75, v75, v99
v_mfma_f32_16x16x32_bf16 v[34:37], v[38:41], a[8:11], 0
v_fma_f32 v22, v22, v70, v71
v_fma_f32 v23, v23, v72, v73
mav 26,12,42
R 58,320
v_fma_f32 v24, v24, v74, v75
mav 30,16,42
v_mul_f32 v77, v77, v99
v_fma_f32 v25, v25, v76, v77
mva 34,42,20
v_cvt_pk_bf16_f32 v8, v22, v23
v_cvt_pk_bf16_f32 v9, v24, v25
mav 26,24,46
R 38,384
v_mul_f32 v124, v123, v62
v_mfma_f32_16x16x32_bf16 v[70:73], v[104:107], v[0:3], v[86:89]
v_fma_f32 v14, v14, v122, v124
v_mul_f32 v125, v123, v63
mav 30,28,46
v_fma_f32 v15, v15, v122, v125
v_mul_f32 v124, v123, v64
mva 34,46,32
s_waitcnt lgkmcnt(2)
v_fma_f32 v16, v16, v122, v124
v_mul_f32 v125, v123, v65
mav 26,36,50
R 42,448
v_fma_f32 v17, v17, v122, v125
mav 30,40,50
v_mul_f32 v124, v123, v66
v_fma_f32 v18, v18, v122, v124
mva 34,50,44
v_mul_f32 v125, v123, v67
v_fma_f32 v19, v19, v122, v125
mav 26,48,54
R 46,512
v_mul_f32 v124, v123, v68
mav 30,52,54
v_fma_f32 v20, v20, v122, v124
v_mul_f32 v125, v123, v69
mva 34,54,56
s_waitcnt lgkmcnt(2)
v_fma_f32 v21, v21, v122, v125
v_add_u32 v78, 1, v78
mav 26,60,58
R 50,576
mav 30,64,58
mva 34,58,68
mav 26,72,38
R 54,640
mav 30,76,38
mva 34,38,80
s_waitcnt lgkmcnt(2)
mav 26,84,42
R 58,704
mav 30,88,42
mva 34,42,92
mav 26,96,46
R 38,768
mav 30,100,46
s_barrier
mva 34,46,104
s_waitcnt lgkmcnt(2)
ds_read_b128 v[100:103], v82 offset:0
mav 26,108,50
ds_read_b64 v[108:109], v82 offset:1024
mav 30,112,50
R 42,832
mva 34,50,116
mav 26,120,54
s_waitcnt vmcnt(0)
buffer_store_dword v78, off, s[36:39], s52 sc1
mav 30,124,54
R 46,896
mva 34,54,128
s_waitcnt lgkmcnt(3)
v_mfma_f32_16x16x32_bf16 v[74:77], v[100:103], v[0:3], v[90:93]
v_cvt_pk_bf16_f32 v0, v14, v15
v_cvt_pk_bf16_f32 v1, v16, v17
mav 26,132,58
R 50,960
v_cvt_pk_bf16_f32 v2, v18, v19
mav 30,136,58
v_cvt_pk_bf16_f32 v3, v20, v21
mva 34,58,140
v_max3_f32 v118, v70, v71, v74
v_max3_f32 v119, v72, v73, v76
mav 26,144,38
R 54,1024
v_max_f32 v118, v118, v75
mav 30,148,38
v_max_f32 v119, v119, v77
v_max_f32 v120, v118, v119
mva 34,38,152
s_waitcnt lgkmcnt(2)
v_max_f32 v121, v118, v119
mav 26,156,42
R 38,1088
mav 30,160,42
v_permlane32_swap_b32 v120, v121
mva 34,42,164
v_max_f32 v114, v120, v121
v_max_f32 v115, v120, v121
mav 26,168,46
R 42,1152
mav 30,172,46
v_permlane16_swap_b32 v114, v115
mva 34,46,176
s_waitcnt lgkmcnt(2)
v_max_f32 v120, v114, v115
v_mul_f32 v120, s65, v120
mav 26,180,50
R 46,1216
v_fma_f32 v118, v118, s65, -v120
mav 30,184,50
v_fma_f32 v119, v119, s65, -v120
mva 34,50,188
v_exp_f32 v118, v118
mav 26,192,54
R 50,1280
mav 30,196,54
v_exp_f32 v119, v119
mva 34,54,200
s_waitcnt lgkmcnt(2)
v_add_f32 v120, v118, v119
v_add_f32 v121, v118, v119
mav 26,204,38
R 54,1344
mav 30,208,38
v_permlane32_swap_b32 v120, v121
mva 34,38,212
v_add_f32 v114, v120, v121
v_add_f32 v115, v120, v121
mav 26,216,42
R 38,1408
mav 30,220,42
v_permlane16_swap_b32 v114, v115
mva 34,42,224
s_waitcnt lgkmcnt(2)
v_add_f32 v120, v114, v115
mav 26,228,46
R 42,1472
mav 30,232,46
v_rcp_f32 v120, v120
mva 34,46,236
v_mul_f32 v118, v118, v120
v_mul_f32 v119, v119, v120
mav 26,240,50
R 46,1536
v_cvt_pk_bf16_f32 v118, v118, v118
mav 30,244,50
v_cvt_pk_bf16_f32 v119, v119, v119
v_and_b32 v114, v118, v96
mva 34,50,248
s_waitcnt lgkmcnt(2)
v_and_b32 v115, v119, v97
v_and_b32 v116, v118, v94
mav 26,252,54
R 50,1600
v_and_b32 v117, v119, v95
mvv 30,128,54
mvv 34,54,132
v_mfma_f32_16x16x32_bf16 v[70:73], v[8:11], v[114:117], 0
v_mfma_f32_16x16x32_bf16 v[74:77], v[108:111], v[114:117], 0
mvv 26,136,38
R 54,1664
mvv 30,140,38
mvv 34,38,144
s_waitcnt lgkmcnt(2)
mvv 26,148,42
R 38,1728
v_cvt_pk_bf16_f32 v114, v70, v71
mvv 30,152,42
v_cvt_pk_bf16_f32 v115, v72, v73
v_cvt_pk_bf16_f32 v116, v74, v75
mvv 34,42,156
v_cvt_pk_bf16_f32 v117, v76, v77
mvv 26,160,46
buffer_store_dwordx4 v[114:117], v82, s[36:39], s49 offen sc1
R 42,1792
s_add_u32 s49, s49, 313344
mvv 30,164,46
mvv 34,46,168
s_waitcnt lgkmcnt(2)
mvv 26,172,50
R 46,1856
mvv 30,176,50
mvv 34,50,180
mvv 26,184,54
R 50,1920
mvv 30,188,54
s_waitcnt lgkmcnt(2)
mvv 34,54,192
s_mov_b32 s60, -13
.p2align 6
LQA0_1:
mvv 26,196,38
R 54,1984
v_xor_b32 v80, v81, v80
mvv 30,200,38
mvv 34,38,204
mvv 26,208,42
R 38,33024
mvv 30,212,42
ds_read_b64 v[122:123], v83 offset:128
mvv 34,42,216
s_waitcnt lgkmcnt(3)
ds_read_b128 v[70:73], v84 offset:128
mvv 26,220,46
R 42,33088
mvv 30,224,46
ds_read_b128 v[74:77], v84 offset:144
mvv 34,46,228
mvv 26,232,50
R 46,33152
mvv 30,236,50
mvv 34,50,240
s_waitcnt lgkmcnt(3)
mvv 26,244,54
R 50,33216
mvv 30,248,54
mvv 34,54,252
v_mfma_f32_16x16x32_bf16 v[14:17], a[0:3], v[38:41], 0
s_waitcnt lgkmcnt(1)
R 54,33280
v_mul_f32 v71, v71, v99
v_mfma_f32_16x16x32_bf16 v[18:21], a[4:7], v[38:41], 0
v_mul_f32 v73, v73, v99
v_mul_f32 v75, v75, v99
v_mfma_f32_16x16x32_bf16 v[22:25], v[38:41], a[8:11], 0
v_fma_f32 v34, v34, v70, v71
v_fma_f32 v35, v35, v72, v73
mav 14,12,42
R 58,33344
v_fma_f32 v36, v36, v74, v75
mav 18,16,42
v_mul_f32 v77, v77, v99
v_fma_f32 v37, v37, v76, v77
mva 22,42,20
v_cvt_pk_bf16_f32 v10, v34, v35
v_cvt_pk_bf16_f32 v11, v36, v37
mav 14,24,46
R 38,33408
v_mul_f32 v124, v123, v62
v_mfma_f32_16x16x32_bf16 v[70:73], v[100:103], v[0:3], v[86:89]
v_fma_f32 v26, v26, v122, v124
v_mul_f32 v125, v123, v63
mav 18,28,46
v_fma_f32 v27, v27, v122, v125
v_mul_f32 v124, v123, v64
mva 22,46,32
s_waitcnt lgkmcnt(2)
v_fma_f32 v28, v28, v122, v124
v_mul_f32 v125, v123, v65
mav 14,36,50
R 42,33472
v_fma_f32 v29, v29, v122, v125
mav 18,40,50
v_mul_f32 v124, v123, v66
v_fma_f32 v30, v30, v122, v124
mva 22,50,44
v_mul_f32 v125, v123, v67
v_fma_f32 v31, v31, v122, v125
mav 14,48,54
R 46,33536
v_mul_f32 v124, v123, v68
mav 18,52,54
v_fma_f32 v32, v32, v122, v124
v_mul_f32 v125, v123, v69
mva 22,54,56
s_waitcnt lgkmcnt(2)
v_fma_f32 v33, v33, v122, v125
v_add_u32 v78, 1, v78
mav 14,60,58
R 50,33600
mav 18,64,58
mva 22,58,68
mav 14,72,38
R 54,33664
mav 18,76,38
mva 22,38,80
s_waitcnt lgkmcnt(2)
mav 14,84,42
R 58,33728
mav 18,88,42
mva 22,42,92
mav 14,96,46
R 38,33792
mav 18,100,46
s_barrier
mva 22,46,104
s_waitcnt lgkmcnt(2)
ds_read_b128 v[104:107], v82 offset:4096
mav 14,108,50
ds_read_b64 v[110:111], v82 offset:5120
mav 18,112,50
R 42,33856
mva 22,50,116
mav 14,120,54
s_waitcnt vmcnt(0)
buffer_store_dword v78, off, s[36:39], s52 sc1
mav 18,124,54
R 46,33920
mva 22,54,128
s_waitcnt lgkmcnt(3)
v_mfma_f32_16x16x32_bf16 v[74:77], v[104:107], v[0:3], v[90:93]
v_cvt_pk_bf16_f32 v0, v26, v27
v_cvt_pk_bf16_f32 v1, v28, v29
mav 14,132,58
R 50,33984
v_cvt_pk_bf16_f32 v2, v30, v31
mav 18,136,58
v_cvt_pk_bf16_f32 v3, v32, v33
mva 22,58,140
v_max3_f32 v118, v70, v71, v74
v_max3_f32 v119, v72, v73, v76
mav 14,144,38
R 54,34048
v_max_f32 v118, v118, v75
mav 18,148,38
v_max_f32 v119, v119, v77
v_max_f32 v120, v118, v119
mva 22,38,152
s_waitcnt lgkmcnt(2)
v_max_f32 v121, v118, v119
mav 14,156,42
R 38,34112
mav 18,160,42
v_permlane32_swap_b32 v120, v121
mva 22,42,164
v_max_f32 v114, v120, v121
v_max_f32 v115, v120, v121
mav 14,168,46
R 42,34176
mav 18,172,46
v_permlane16_swap_b32 v114, v115
mva 22,46,176
s_waitcnt lgkmcnt(2)
v_max_f32 v120, v114, v115
v_mul_f32 v120, s65, v120
mav 14,180,50
R 46,34240
LsQ3:
s_cbranch_execz LsQ7
v_fma_f32 v118, v118, s65, -v120
mav 18,184,50
v_fma_f32 v119, v119, s65, -v120
mva 22,50,188
v_exp_f32 v118, v118
mav 14,192,54
R 50,34304
mav 18,196,54
v_exp_f32 v119, v119
mva 22,54,200
s_waitcnt lgkmcnt(2)
v_add_f32 v120, v118, v119
v_add_f32 v121, v118, v119
mav 14,204,38
R 54,34368
mav 18,208,38
v_permlane32_swap_b32 v120, v121
mva 22,38,212
v_add_f32 v114, v120, v121
v_add_f32 v115, v120, v121
mav 14,216,42
R 38,34432
mav 18,220,42
v_permlane16_swap_b32 v114, v115
mva 22,42,224
s_waitcnt lgkmcnt(2)
v_add_f32 v120, v114, v115
mav 14,228,46
R 42,34496
mav 18,232,46
v_rcp_f32 v120, v120
mva 22,46,236
v_mul_f32 v118, v118, v120
v_mul_f32 v119, v119, v120
mav 14,240,50
R 46,34560
v_cvt_pk_bf16_f32 v118, v118, v118
mav 18,244,50
v_cvt_pk_bf16_f32 v119, v119, v119
v_and_b32 v114, v118, v94
mva 22,50,248
s_waitcnt lgkmcnt(2)
v_and_b32 v115, v119, v95
v_and_b32 v116, v118, v96
mav 14,252,54
R 50,34624
v_and_b32 v117, v119, v97
mvv 18,128,54
mvv 22,54,132
v_mfma_f32_16x16x32_bf16 v[70:73], v[8:11], v[114:117], 0
v_mfma_f32_16x16x32_bf16 v[74:77], v[108:111], v[114:117], 0
mvv 14,136,38
R 54,34688
mvv 18,140,38
mvv 22,38,144
s_waitcnt lgkmcnt(2)
mvv 14,148,42
R 38,34752
v_cvt_pk_bf16_f32 v114, v70, v71
mvv 18,152,42
v_cvt_pk_bf16_f32 v115, v72, v73
v_cvt_pk_bf16_f32 v116, v74, v75
mvv 22,42,156
v_cvt_pk_bf16_f32 v117, v76, v77
mvv 14,160,46
buffer_store_dwordx4 v[114:117], v82, s[36:39], s49 offen sc1
R 42,34816
s_add_u32 s49, s49, 313344
mvv 18,164,46
mvv 22,46,168
s_waitcnt lgkmcnt(2)
mvv 14,172,50
R 46,34880
mvv 18,176,50
mvv 22,50,180
mvv 14,184,54
R 50,34944
mvv 18,188,54
mvv 22,54,192
s_waitcnt lgkmcnt(2)
mvv 14,196,38
R 54,35008
mvv 18,200,38
mvv 22,38,204
mvv 14,208,42
R 38,0
mvv 18,212,42
ds_read_b64 v[122:123], v83 offset:0
mvv 22,42,216
s_waitcnt lgkmcnt(3)
ds_read_b128 v[70:73], v84 offset:0
mvv 14,220,46
R 42,64
mvv 18,224,46
ds_read_b128 v[74:77], v84 offset:16
mvv 22,46,228
mvv 14,232,50
R 46,128
mvv 18,236,50
mvv 22,50,240
s_waitcnt lgkmcnt(3)
mvv 14,244,54
R 50,192
mvv 18,248,54
mvv 22,54,252
v_mfma_f32_16x16x32_bf16 v[26:29], a[0:3], v[38:41], 0
s_waitcnt lgkmcnt(1)
R 54,256
v_mul_f32 v71, v71, v99
v_mfma_f32_16x16x32_bf16 v[30:33], a[4:7], v[38:41], 0
v_mul_f32 v73, v73, v99
v_mul_f32 v75, v75, v99
v_mfma_f32_16x16x32_bf16 v[34:37], v[38:41], a[8:11], 0
v_fma_f32 v22, v22, v70, v71
v_fma_f32 v23, v23, v72, v73
mav 26,12,42
R 58,320
v_fma_f32 v24, v24, v74, v75
mav 30,16,42
v_mul_f32 v77, v77, v99
v_fma_f32 v25, v25, v76, v77
mva 34,42,20
v_cvt_pk_bf16_f32 v8, v22, v23
v_cvt_pk_bf16_f32 v9, v24, v25
mav 26,24,46
R 38,384
v_mul_f32 v124, v123, v62
v_mfma_f32_16x16x32_bf16 v[70:73], v[104:107], v[0:3], v[86:89]
v_fma_f32 v14, v14, v122, v124
v_mul_f32 v125, v123, v63
mav 30,28,46
v_fma_f32 v15, v15, v122, v125
v_mul_f32 v124, v123, v64
mva 34,46,32
s_waitcnt lgkmcnt(2)
v_fma_f32 v16, v16, v122, v124
v_mul_f32 v125, v123, v65
mav 26,36,50
R 42,448
v_fma_f32 v17, v17, v122, v125
mav 30,40,50
v_mul_f32 v124, v123, v66
v_fma_f32 v18, v18, v122, v124
mva 34,50,44
v_mul_f32 v125, v123, v67
v_fma_f32 v19, v19, v122, v125
mav 26,48,54
R 46,512
v_mul_f32 v124, v123, v68
mav 30,52,54
v_fma_f32 v20, v20, v122, v124
v_mul_f32 v125, v123, v69
mva 34,54,56
s_waitcnt lgkmcnt(2)
v_fma_f32 v21, v21, v122, v125
v_add_u32 v78, 1, v78
mav 26,60,58
R 50,576
mav 30,64,58
mva 34,58,68
mav 26,72,38
R 54,640
mav 30,76,38
mva 34,38,80
s_waitcnt lgkmcnt(2)
mav 26,84,42
R 58,704
mav 30,88,42
mva 34,42,92
mav 26,96,46
R 38,768
mav 30,100,46
s_barrier
mva 34,46,104
s_waitcnt lgkmcnt(2)
ds_read_b128 v[100:103], v82 offset:0
mav 26,108,50
ds_read_b64 v[108:109], v82 offset:1024
mav 30,112,50
R 42,832
mva 34,50,116
mav 26,120,54
s_waitcnt vmcnt(0)
buffer_store_dword v78, off, s[36:39], s52 sc1
mav 30,124,54
R 46,896
mva 34,54,128
s_waitcnt lgkmcnt(3)
v_mfma_f32_16x16x32_bf16 v[74:77], v[100:103], v[0:3], v[90:93]
v_cvt_pk_bf16_f32 v0, v14, v15
v_cvt_pk_bf16_f32 v1, v16, v17
mav 26,132,58
R 50,960
v_cvt_pk_bf16_f32 v2, v18, v19
mav 30,136,58
v_cvt_pk_bf16_f32 v3, v20, v21
mva 34,58,140
v_max3_f32 v118, v70, v71, v74
v_max3_f32 v119, v72, v73, v76
mav 26,144,38
R 54,1024
v_max_f32 v118, v118, v75
mav 30,148,38
v_max_f32 v119, v119, v77
v_max_f32 v120, v118, v119
mva 34,38,152
s_waitcnt lgkmcnt(2)
v_max_f32 v121, v118, v119
mav 26,156,42
R 38,1088
mav 30,160,42
v_permlane32_swap_b32 v120, v121
mva 34,42,164
v_max_f32 v114, v120, v121
v_max_f32 v115, v120, v121
mav 26,168,46
R 42,1152
mav 30,172,46
v_permlane16_swap_b32 v114, v115
mva 34,46,176
s_waitcnt lgkmcnt(2)
v_max_f32 v120, v114, v115
v_mul_f32 v120, s65, v120
mav 26,180,50
R 46,1216
v_fma_f32 v118, v118, s65, -v120
mav 30,184,50
v_fma_f32 v119, v119, s65, -v120
mva 34,50,188
v_exp_f32 v118, v118
mav 26,192,54
R 50,1280
mav 30,196,54
v_exp_f32 v119, v119
mva 34,54,200
s_waitcnt lgkmcnt(2)
v_add_f32 v120, v118, v119
v_add_f32 v121, v118, v119
mav 26,204,38
R 54,1344
mav 30,208,38
v_permlane32_swap_b32 v120, v121
mva 34,38,212
v_add_f32 v114, v120, v121
v_add_f32 v115, v120, v121
mav 26,216,42
R 38,1408
mav 30,220,42
v_permlane16_swap_b32 v114, v115
mva 34,42,224
s_waitcnt lgkmcnt(2)
v_add_f32 v120, v114, v115
mav 26,228,46
R 42,1472
mav 30,232,46
v_rcp_f32 v120, v120
mva 34,46,236
v_mul_f32 v118, v118, v120
v_mul_f32 v119, v119, v120
mav 26,240,50
R 46,1536
v_cvt_pk_bf16_f32 v118, v118, v118
mav 30,244,50
v_cvt_pk_bf16_f32 v119, v119, v119
v_and_b32 v114, v118, v96
mva 34,50,248
s_waitcnt lgkmcnt(2)
v_and_b32 v115, v119, v97
v_and_b32 v116, v118, v94
mav 26,252,54
R 50,1600
v_and_b32 v117, v119, v95
mvv 30,128,54
mvv 34,54,132
v_mfma_f32_16x16x32_bf16 v[70:73], v[8:11], v[114:117], 0
v_mfma_f32_16x16x32_bf16 v[74:77], v[108:111], v[114:117], 0
mvv 26,136,38
R 54,1664
mvv 30,140,38
mvv 34,38,144
s_waitcnt lgkmcnt(2)
mvv 26,148,42
R 38,1728
v_cvt_pk_bf16_f32 v114, v70, v71
mvv 30,152,42
v_cvt_pk_bf16_f32 v115, v72, v73
v_cvt_pk_bf16_f32 v116, v74, v75
mvv 34,42,156
v_cvt_pk_bf16_f32 v117, v76, v77
mvv 26,160,46
buffer_store_dwordx4 v[114:117], v82, s[36:39], s49 offen sc1
R 42,1792
s_add_u32 s49, s49, 313344
mvv 30,164,46
mvv 34,46,168
s_waitcnt lgkmcnt(2)
mvv 26,172,50
R 46,1856
mvv 30,176,50
mvv 34,50,180
mvv 26,184,54
R 50,1920
mvv 30,188,54
s_waitcnt lgkmcnt(2)
s_add_u32 s60, s60, 1
mvv 34,54,192
s_cbranch_scc0 LQA0_1
mvv 26,196,38
R 54,1984
v_xor_b32 v80, v81, v80
mvv 30,200,38
mvv 34,38,204
mvv 26,208,42
R 38,33024
mvv 30,212,42
ds_read_b64 v[122:123], v83 offset:128
mvv 34,42,216
s_waitcnt lgkmcnt(3)
ds_read_b128 v[70:73], v84 offset:128
mvv 26,220,46
R 42,33088
mvv 30,224,46
ds_read_b128 v[74:77], v84 offset:144
mvv 34,46,228
mvv 26,232,50
R 46,33152
mvv 30,236,50
mvv 34,50,240
s_waitcnt lgkmcnt(3)
mvv 26,244,54
R 50,33216
mvv 30,248,54
mvv 34,54,252
v_mfma_f32_16x16x32_bf16 v[14:17], a[0:3], v[38:41], 0
s_waitcnt lgkmcnt(1)
R 54,33280
v_mul_f32 v71, v71, v99
v_mfma_f32_16x16x32_bf16 v[18:21], a[4:7], v[38:41], 0
v_mul_f32 v73, v73, v99
v_mul_f32 v75, v75, v99
v_mfma_f32_16x16x32_bf16 v[22:25], v[38:41], a[8:11], 0
v_fma_f32 v34, v34, v70, v71
v_fma_f32 v35, v35, v72, v73
mav 14,12,42
R 58,33344
v_fma_f32 v36, v36, v74, v75
mav 18,16,42
v_mul_f32 v77, v77, v99
v_fma_f32 v37, v37, v76, v77
mva 22,42,20
v_cvt_pk_bf16_f32 v10, v34, v35
v_cvt_pk_bf16_f32 v11, v36, v37
mav 14,24,46
R 38,33408
v_mul_f32 v124, v123, v62
v_mfma_f32_16x16x32_bf16 v[70:73], v[100:103], v[0:3], v[86:89]
v_fma_f32 v26, v26, v122, v124
v_mul_f32 v125, v123, v63
mav 18,28,46
v_fma_f32 v27, v27, v122, v125
v_mul_f32 v124, v123, v64
mva 22,46,32
s_waitcnt lgkmcnt(2)
v_fma_f32 v28, v28, v122, v124
v_mul_f32 v125, v123, v65
mav 14,36,50
R 42,33472
v_fma_f32 v29, v29, v122, v125
mav 18,40,50
v_mul_f32 v124, v123, v66
v_fma_f32 v30, v30, v122, v124
mva 22,50,44
v_mul_f32 v125, v123, v67
v_fma_f32 v31, v31, v122, v125
mav 14,48,54
R 46,33536
v_mul_f32 v124, v123, v68
mav 18,52,54
v_fma_f32 v32, v32, v122, v124
v_mul_f32 v125, v123, v69
mva 22,54,56
s_waitcnt lgkmcnt(2)
v_fma_f32 v33, v33, v122, v125
v_add_u32 v78, 1, v78
mav 14,60,58
R 50,33600
mav 18,64,58
mva 22,58,68
mav 14,72,38
R 54,33664
mav 18,76,38
mva 22,38,80
s_waitcnt lgkmcnt(2)
mav 14,84,42
R 58,33728
mav 18,88,42
mva 22,42,92
mav 14,96,46
R 38,33792
mav 18,100,46
s_barrier
mva 22,46,104
s_waitcnt lgkmcnt(2)
ds_read_b128 v[104:107], v82 offset:4096
mav 14,108,50
ds_read_b64 v[110:111], v82 offset:5120
mav 18,112,50
R 42,33856
mva 22,50,116
mav 14,120,54
s_waitcnt vmcnt(0)
buffer_store_dword v78, off, s[36:39], s52 sc1
mav 18,124,54
R 46,33920
mva 22,54,128
s_waitcnt lgkmcnt(3)
v_mfma_f32_16x16x32_bf16 v[74:77], v[104:107], v[0:3], v[90:93]
v_cvt_pk_bf16_f32 v0, v26, v27
v_cvt_pk_bf16_f32 v1, v28, v29
mav 14,132,58
R 50,33984
v_cvt_pk_bf16_f32 v2, v30, v31
mav 18,136,58
v_cvt_pk_bf16_f32 v3, v32, v33
mva 22,58,140
v_max3_f32 v118, v70, v71, v74
v_max3_f32 v119, v72, v73, v76
mav 14,144,38
R 54,34048
v_max_f32 v118, v118, v75
mav 18,148,38
v_max_f32 v119, v119, v77
v_max_f32 v120, v118, v119
mva 22,38,152
s_waitcnt lgkmcnt(2)
v_max_f32 v121, v118, v119
mav 14,156,42
R 38,34112
mav 18,160,42
v_permlane32_swap_b32 v120, v121
mva 22,42,164
v_max_f32 v114, v120, v121
v_max_f32 v115, v120, v121
mav 14,168,46
R 42,34176
mav 18,172,46
v_permlane16_swap_b32 v114, v115
mva 22,46,176
s_waitcnt lgkmcnt(2)
v_max_f32 v120, v114, v115
v_mul_f32 v120, s65, v120
mav 14,180,50
R 46,34240
v_fma_f32 v118, v118, s65, -v120
mav 18,184,50
v_fma_f32 v119, v119, s65, -v120
mva 22,50,188
v_exp_f32 v118, v118
mav 14,192,54
R 50,34304
mav 18,196,54
v_exp_f32 v119, v119
mva 22,54,200
s_waitcnt lgkmcnt(2)
v_add_f32 v120, v118, v119
v_add_f32 v121, v118, v119
mav 14,204,38
R 54,34368
mav 18,208,38
v_permlane32_swap_b32 v120, v121
mva 22,38,212
v_add_f32 v114, v120, v121
v_add_f32 v115, v120, v121
mav 14,216,42
R 38,34432
mav 18,220,42
v_permlane16_swap_b32 v114, v115
mva 22,42,224
s_waitcnt lgkmcnt(2)
v_add_f32 v120, v114, v115
mav 14,228,46
R 42,34496
mav 18,232,46
v_rcp_f32 v120, v120
mva 22,46,236
v_mul_f32 v118, v118, v120
v_mul_f32 v119, v119, v120
mav 14,240,50
R 46,34560
v_cvt_pk_bf16_f32 v118, v118, v118
mav 18,244,50
v_cvt_pk_bf16_f32 v119, v119, v119
v_and_b32 v114, v118, v94
mva 22,50,248
s_waitcnt lgkmcnt(2)
v_and_b32 v115, v119, v95
v_and_b32 v116, v118, v96
mav 14,252,54
R 50,34624
v_and_b32 v117, v119, v97
mvv 18,128,54
mvv 22,54,132
v_mfma_f32_16x16x32_bf16 v[70:73], v[8:11], v[114:117], 0
v_mfma_f32_16x16x32_bf16 v[74:77], v[108:111], v[114:117], 0
mvv 14,136,38
R 54,34688
mvv 18,140,38
mvv 22,38,144
s_waitcnt lgkmcnt(2)
mvv 14,148,42
R 38,34752
v_cvt_pk_bf16_f32 v114, v70, v71
mvv 18,152,42
v_cvt_pk_bf16_f32 v115, v72, v73
v_cvt_pk_bf16_f32 v116, v74, v75
mvv 22,42,156
v_cvt_pk_bf16_f32 v117, v76, v77
mvv 14,160,46
buffer_store_dwordx4 v[114:117], v82, s[36:39], s49 offen sc1
R 42,34816
s_add_u32 s49, s49, 313344
mvv 18,164,46
mvv 22,46,168
s_waitcnt lgkmcnt(2)
mvv 14,172,50
R 46,34880
mvv 18,176,50
mvv 22,50,180
)"
R"(
mvv 14,184,54
R 50,34944
mvv 18,188,54
mvv 22,54,192
s_waitcnt lgkmcnt(2)
mvv 14,196,38
R 54,35008
mvv 18,200,38
mvv 22,38,204
mvv 14,208,42
R 38,0
mvv 18,212,42
ds_read_b64 v[122:123], v83 offset:0
mvv 22,42,216
s_waitcnt lgkmcnt(3)
ds_read_b128 v[70:73], v84 offset:0
mvv 14,220,46
R 42,64
mvv 18,224,46
ds_read_b128 v[74:77], v84 offset:16
mvv 22,46,228
mvv 14,232,50
R 46,128
mvv 18,236,50
mvv 22,50,240
s_waitcnt lgkmcnt(3)
mvv 14,244,54
R 50,192
mvv 18,248,54
mvv 22,54,252
v_mfma_f32_16x16x32_bf16 v[26:29], a[0:3], v[38:41], 0
s_waitcnt lgkmcnt(1)
R 54,256
v_mul_f32 v71, v71, v99
v_mfma_f32_16x16x32_bf16 v[30:33], a[4:7], v[38:41], 0
v_mul_f32 v73, v73, v99
v_mul_f32 v75, v75, v99
v_mfma_f32_16x16x32_bf16 v[34:37], v[38:41], a[8:11], 0
v_fma_f32 v22, v22, v70, v71
v_fma_f32 v23, v23, v72, v73
mav 26,12,42
R 58,320
v_fma_f32 v24, v24, v74, v75
mav 30,16,42
v_mul_f32 v77, v77, v99
v_fma_f32 v25, v25, v76, v77
mva 34,42,20
v_cvt_pk_bf16_f32 v8, v22, v23
v_cvt_pk_bf16_f32 v9, v24, v25
mav 26,24,46
R 38,384
v_mul_f32 v124, v123, v62
v_mfma_f32_16x16x32_bf16 v[70:73], v[104:107], v[0:3], v[86:89]
v_fma_f32 v14, v14, v122, v124
v_mul_f32 v125, v123, v63
mav 30,28,46
v_fma_f32 v15, v15, v122, v125
v_mul_f32 v124, v123, v64
LsQ4:
s_cbranch_execz LsQ8
mva 34,46,32
s_waitcnt lgkmcnt(2)
v_fma_f32 v16, v16, v122, v124
v_mul_f32 v125, v123, v65
mav 26,36,50
R 42,448
v_fma_f32 v17, v17, v122, v125
mav 30,40,50
buffer_load_dwordx4 v[4:7], v82, s[36:39], s55 offen sc1
mva 34,50,44
v_mul_f32 v124, v123, v66
v_fma_f32 v18, v18, v122, v124
mav 26,48,54
buffer_load_dwordx2 v[12:13], v98, s[36:39], s55 offen offset:1024 sc1
mav 30,52,54
R 46,512
v_mul_f32 v125, v123, v67
mva 34,54,56
s_waitcnt lgkmcnt(2)
buffer_load_dwordx2 v[112:113], v98, s[36:39], s55 offen offset:1536 sc1
mav 26,60,58
R 50,576
v_fma_f32 v19, v19, v122, v125
mav 30,64,58
v_mul_f32 v124, v123, v68
v_fma_f32 v20, v20, v122, v124
mva 34,58,68
v_mul_f32 v125, v123, v69
v_fma_f32 v21, v21, v122, v125
mav 26,72,38
R 54,640
v_add_u32 v78, 1, v78
mav 30,76,38
mva 34,38,80
s_waitcnt lgkmcnt(2)
mav 26,84,42
R 58,704
mav 30,88,42
mva 34,42,92
mav 26,96,46
R 38,768
mav 30,100,46
s_barrier
mva 34,46,104
s_waitcnt lgkmcnt(2)
ds_read_b128 v[100:103], v82 offset:0
mav 26,108,50
ds_read_b64 v[108:109], v82 offset:1024
mav 30,112,50
R 42,832
mva 34,50,116
mav 26,120,54
s_waitcnt vmcnt(3)
buffer_store_dword v78, off, s[36:39], s52 sc1
mav 30,124,54
R 46,896
mva 34,54,128
s_waitcnt lgkmcnt(3)
v_mfma_f32_16x16x32_bf16 v[74:77], v[100:103], v[0:3], v[90:93]
v_cvt_pk_bf16_f32 v0, v14, v15
v_cvt_pk_bf16_f32 v1, v16, v17
mav 26,132,58
R 50,960
v_cvt_pk_bf16_f32 v2, v18, v19
mav 30,136,58
v_cvt_pk_bf16_f32 v3, v20, v21
mva 34,58,140
v_max3_f32 v118, v70, v71, v74
v_max3_f32 v119, v72, v73, v76
mav 26,144,38
R 54,1024
v_max_f32 v118, v118, v75
mav 30,148,38
v_max_f32 v119, v119, v77
v_max_f32 v120, v118, v119
mva 34,38,152
s_waitcnt lgkmcnt(2)
v_max_f32 v121, v118, v119
mav 26,156,42
R 38,1088
mav 30,160,42
v_permlane32_swap_b32 v120, v121
mva 34,42,164
v_max_f32 v114, v120, v121
v_max_f32 v115, v120, v121
mav 26,168,46
R 42,1152
mav 30,172,46
v_permlane16_swap_b32 v114, v115
mva 34,46,176
s_waitcnt lgkmcnt(2)
v_max_f32 v120, v114, v115
v_mul_f32 v120, s65, v120
mav 26,180,50
R 46,1216
v_fma_f32 v118, v118, s65, -v120
mav 30,184,50
v_fma_f32 v119, v119, s65, -v120
mva 34,50,188
v_exp_f32 v118, v118
mav 26,192,54
R 50,1280
mav 30,196,54
v_exp_f32 v119, v119
mva 34,54,200
s_waitcnt lgkmcnt(2)
v_add_f32 v120, v118, v119
v_add_f32 v121, v118, v119
mav 26,204,38
R 54,1344
mav 30,208,38
v_permlane32_swap_b32 v120, v121
mva 34,38,212
v_add_f32 v114, v120, v121
v_add_f32 v115, v120, v121
mav 26,216,42
R 38,1408
mav 30,220,42
v_permlane16_swap_b32 v114, v115
mva 34,42,224
s_waitcnt lgkmcnt(2)
v_add_f32 v120, v114, v115
mav 26,228,46
R 42,1472
mav 30,232,46
v_rcp_f32 v120, v120
mva 34,46,236
v_mul_f32 v118, v118, v120
v_mul_f32 v119, v119, v120
mav 26,240,50
R 46,1536
v_cvt_pk_bf16_f32 v118, v118, v118
mav 30,244,50
v_cvt_pk_bf16_f32 v119, v119, v119
v_and_b32 v114, v118, v96
mva 34,50,248
s_waitcnt lgkmcnt(2)
v_and_b32 v115, v119, v97
v_and_b32 v116, v118, v94
mav 26,252,54
R 50,1600
v_and_b32 v117, v119, v95
mvv 30,128,54
mvv 34,54,132
v_mfma_f32_16x16x32_bf16 v[70:73], v[8:11], v[114:117], 0
v_mfma_f32_16x16x32_bf16 v[74:77], v[108:111], v[114:117], 0
mvv 26,136,38
R 54,1664
mvv 30,140,38
mvv 34,38,144
s_waitcnt lgkmcnt(2)
mvv 26,148,42
R 38,1728
v_cvt_pk_bf16_f32 v114, v70, v71
mvv 30,152,42
v_cvt_pk_bf16_f32 v115, v72, v73
v_cvt_pk_bf16_f32 v116, v74, v75
mvv 34,42,156
v_cvt_pk_bf16_f32 v117, v76, v77
mvv 26,160,46
buffer_store_dwordx4 v[114:117], v82, s[36:39], s49 offen sc1
R 42,1792
s_add_u32 s49, s49, 313344
mvv 30,164,46
mvv 34,46,168
s_waitcnt lgkmcnt(2)
mvv 26,172,50
R 46,1856
mvv 30,176,50
mvv 34,50,180
mvv 26,184,54
R 50,1920
mvv 30,188,54
s_waitcnt lgkmcnt(2)
mvv 34,54,192
mvv 26,196,38
R 54,1984
v_xor_b32 v80, v81, v80
mvv 30,200,38
mvv 34,38,204
mvv 26,208,42
R 38,33024
mvv 30,212,42
ds_read_b64 v[122:123], v83 offset:128
mvv 34,42,216
s_waitcnt lgkmcnt(3)
ds_read_b128 v[70:73], v84 offset:128
mvv 26,220,46
R 42,33088
mvv 30,224,46
ds_read_b128 v[74:77], v84 offset:144
mvv 34,46,228
mvv 26,232,50
R 46,33152
mvv 30,236,50
mvv 34,50,240
s_waitcnt lgkmcnt(3)
mvv 26,244,54
R 50,33216
mvv 30,248,54
mvv 34,54,252
v_mfma_f32_16x16x32_bf16 v[14:17], a[0:3], v[38:41], 0
s_waitcnt lgkmcnt(1)
R 54,33280
v_mul_f32 v71, v71, v99
v_mfma_f32_16x16x32_bf16 v[18:21], a[4:7], v[38:41], 0
v_mul_f32 v73, v73, v99
v_mul_f32 v75, v75, v99
v_mfma_f32_16x16x32_bf16 v[22:25], v[38:41], a[8:11], 0
v_fma_f32 v34, v34, v70, v71
v_fma_f32 v35, v35, v72, v73
mav 14,12,42
R 58,33344
v_fma_f32 v36, v36, v74, v75
mav 18,16,42
v_mul_f32 v77, v77, v99
v_fma_f32 v37, v37, v76, v77
mva 22,42,20
v_cvt_pk_bf16_f32 v10, v34, v35
v_cvt_pk_bf16_f32 v11, v36, v37
mav 14,24,46
R 38,33408
v_mul_f32 v124, v123, v62
v_mfma_f32_16x16x32_bf16 v[70:73], v[100:103], v[0:3], v[86:89]
v_fma_f32 v26, v26, v122, v124
v_mul_f32 v125, v123, v63
mav 18,28,46
v_fma_f32 v27, v27, v122, v125
v_mul_f32 v124, v123, v64
mva 22,46,32
s_waitcnt lgkmcnt(2)
v_fma_f32 v28, v28, v122, v124
v_mul_f32 v125, v123, v65
mav 14,36,50
R 42,33472
v_fma_f32 v29, v29, v122, v125
mav 18,40,50
v_mul_f32 v124, v123, v66
v_fma_f32 v30, v30, v122, v124
mva 22,50,44
v_mul_f32 v125, v123, v67
v_fma_f32 v31, v31, v122, v125
mav 14,48,54
R 46,33536
v_mul_f32 v124, v123, v68
mav 18,52,54
v_fma_f32 v32, v32, v122, v124
v_mul_f32 v125, v123, v69
mva 22,54,56
s_waitcnt lgkmcnt(2)
v_fma_f32 v33, v33, v122, v125
v_add_u32 v78, 1, v78
mav 14,60,58
R 50,33600
mav 18,64,58
mva 22,58,68
mav 14,72,38
R 54,33664
mav 18,76,38
mva 22,38,80
s_waitcnt lgkmcnt(2)
mav 14,84,42
R 58,33728
mav 18,88,42
mva 22,42,92
mav 14,96,46
R 38,33792
mav 18,100,46
s_barrier
mva 22,46,104
s_waitcnt lgkmcnt(2)
ds_read_b128 v[104:107], v82 offset:4096
mav 14,108,50
ds_read_b64 v[110:111], v82 offset:5120
mav 18,112,50
R 42,33856
mva 22,50,116
mav 14,120,54
s_waitcnt vmcnt(0)
buffer_store_dword v78, off, s[36:39], s52 sc1
mav 18,124,54
R 46,33920
mva 22,54,128
s_waitcnt lgkmcnt(3)
v_mfma_f32_16x16x32_bf16 v[74:77], v[104:107], v[0:3], v[90:93]
v_cvt_pk_bf16_f32 v0, v26, v27
v_cvt_pk_bf16_f32 v1, v28, v29
mav 14,132,58
R 50,33984
v_cvt_pk_bf16_f32 v2, v30, v31
mav 18,136,58
v_cvt_pk_bf16_f32 v3, v32, v33
mva 22,58,140
v_max3_f32 v118, v70, v71, v74
v_max3_f32 v119, v72, v73, v76
mav 14,144,38
R 54,34048
v_max_f32 v118, v118, v75
mav 18,148,38
v_max_f32 v119, v119, v77
v_max_f32 v120, v118, v119
mva 22,38,152
s_waitcnt lgkmcnt(2)
v_max_f32 v121, v118, v119
mav 14,156,42
R 38,34112
mav 18,160,42
v_permlane32_swap_b32 v120, v121
mva 22,42,164
v_max_f32 v114, v120, v121
v_max_f32 v115, v120, v121
mav 14,168,46
R 42,34176
mav 18,172,46
v_permlane16_swap_b32 v114, v115
mva 22,46,176
s_waitcnt lgkmcnt(2)
v_max_f32 v120, v114, v115
v_mul_f32 v120, s65, v120
mav 14,180,50
R 46,34240
v_fma_f32 v118, v118, s65, -v120
mav 18,184,50
v_fma_f32 v119, v119, s65, -v120
mva 22,50,188
v_exp_f32 v118, v118
mav 14,192,54
R 50,34304
mav 18,196,54
v_exp_f32 v119, v119
mva 22,54,200
s_waitcnt lgkmcnt(2)
v_add_f32 v120, v118, v119
v_add_f32 v121, v118, v119
mav 14,204,38
R 54,34368
mav 18,208,38
v_permlane32_swap_b32 v120, v121
mva 22,38,212
v_add_f32 v114, v120, v121
v_add_f32 v115, v120, v121
mav 14,216,42
R 38,34432
mav 18,220,42
v_permlane16_swap_b32 v114, v115
mva 22,42,224
s_waitcnt lgkmcnt(2)
v_add_f32 v120, v114, v115
mav 14,228,46
R 42,34496
mav 18,232,46
v_rcp_f32 v120, v120
mva 22,46,236
v_mul_f32 v118, v118, v120
v_mul_f32 v119, v119, v120
mav 14,240,50
R 46,34560
v_cvt_pk_bf16_f32 v118, v118, v118
mav 18,244,50
v_cvt_pk_bf16_f32 v119, v119, v119
v_and_b32 v114, v118, v94
mva 22,50,248
s_waitcnt lgkmcnt(2)
v_and_b32 v115, v119, v95
v_and_b32 v116, v118, v96
mav 14,252,54
R 50,34624
v_and_b32 v117, v119, v97
mvv 18,128,54
mvv 22,54,132
v_mfma_f32_16x16x32_bf16 v[70:73], v[8:11], v[114:117], 0
v_mfma_f32_16x16x32_bf16 v[74:77], v[108:111], v[114:117], 0
mvv 14,136,38
R 54,34688
mvv 18,140,38
mvv 22,38,144
s_waitcnt lgkmcnt(2)
mvv 14,148,42
R 38,34752
v_cvt_pk_bf16_f32 v114, v70, v71
mvv 18,152,42
v_cvt_pk_bf16_f32 v115, v72, v73
v_cvt_pk_bf16_f32 v116, v74, v75
mvv 22,42,156
v_cvt_pk_bf16_f32 v117, v76, v77
mvv 14,160,46
buffer_store_dwordx4 v[114:117], v82, s[36:39], s49 offen sc1
R 42,34816
s_add_u32 s49, s49, 313344
mvv 18,164,46
mvv 22,46,168
s_waitcnt lgkmcnt(2)
mvv 14,172,50
R 46,34880
mvv 18,176,50
mvv 22,50,180
mvv 14,184,54
R 50,34944
mvv 18,188,54
mvv 22,54,192
s_waitcnt lgkmcnt(2)
mvv 14,196,38
R 54,35008
mvv 18,200,38
mvv 22,38,204
mvv 14,208,42
R 38,0
mvv 18,212,42
ds_read_b64 v[122:123], v83 offset:0
mvv 22,42,216
s_waitcnt lgkmcnt(3)
ds_read_b128 v[70:73], v84 offset:0
mvv 14,220,46
R 42,64
mvv 18,224,46
ds_read_b128 v[74:77], v84 offset:16
mvv 22,46,228
mvv 14,232,50
R 46,128
mvv 18,236,50
mvv 22,50,240
s_waitcnt lgkmcnt(3)
mvv 14,244,54
R 50,192
mvv 18,248,54
mvv 22,54,252
v_mfma_f32_16x16x32_bf16 v[26:29], a[0:3], v[38:41], 0
s_waitcnt lgkmcnt(1)
R 54,256
v_mul_f32 v71, v71, v99
v_mfma_f32_16x16x32_bf16 v[30:33], a[4:7], v[38:41], 0
v_mul_f32 v73, v73, v99
v_mul_f32 v75, v75, v99
v_mfma_f32_16x16x32_bf16 v[34:37], v[38:41], a[8:11], 0
v_fma_f32 v22, v22, v70, v71
v_fma_f32 v23, v23, v72, v73
mav 26,12,42
R 58,320
v_fma_f32 v24, v24, v74, v75
mav 30,16,42
v_mul_f32 v77, v77, v99
v_fma_f32 v25, v25, v76, v77
mva 34,42,20
v_cvt_pk_bf16_f32 v8, v22, v23
v_cvt_pk_bf16_f32 v9, v24, v25
mav 26,24,46
R 38,384
v_min_f32 v74, s56, v90
v_mfma_f32_16x16x32_bf16 v[70:73], v[104:107], v[0:3], v[86:89]
v_min_f32 v75, s56, v91
v_min_f32 v76, s56, v92
mav 30,28,46
v_min_f32 v77, s56, v93
v_mul_f32 v124, v123, v62
mva 34,46,32
s_waitcnt lgkmcnt(2)
v_fma_f32 v14, v14, v122, v124
v_mul_f32 v125, v123, v63
mvv 74,4,0
v_fma_f32 v15, v15, v122, v125
v_mul_f32 v124, v123, v64
mav 26,36,50
R 42,448
v_fma_f32 v16, v16, v122, v124
mav 30,40,50
v_mul_f32 v125, v123, v65
v_fma_f32 v17, v17, v122, v125
mva 34,50,44
v_max3_f32 v118, v70, v71, v74
v_max3_f32 v119, v72, v73, v76
mav 26,48,54
R 46,512
v_max_f32 v118, v118, v75
mav 30,52,54
v_max_f32 v119, v119, v77
v_max_f32 v120, v118, v119
mva 34,54,56
s_waitcnt lgkmcnt(2)
v_max_f32 v121, v118, v119
v_mul_f32 v124, v123, v66
mav 26,60,58
R 50,576
v_fma_f32 v18, v18, v122, v124
mav 30,64,58
v_permlane32_swap_b32 v120, v121
mva 34,58,68
v_max_f32 v114, v120, v121
v_max_f32 v115, v120, v121
mav 26,72,38
R 54,640
v_mul_f32 v125, v123, v67
mav 30,76,38
v_permlane16_swap_b32 v114, v115
mva 34,38,80
s_waitcnt lgkmcnt(2)
v_max_f32 v120, v114, v115
v_mul_f32 v120, s65, v120
mav 26,84,42
R 58,704
v_fma_f32 v118, v118, s65, -v120
mav 30,88,42
v_fma_f32 v119, v119, s65, -v120
v_fma_f32 v19, v19, v122, v125
mva 34,42,92
v_exp_f32 v118, v118
mav 26,96,46
R 38,768
v_mul_f32 v124, v123, v68
mav 30,100,46
s_barrier
mva 34,46,104
s_waitcnt lgkmcnt(2)
ds_read_b128 v[100:103], v82 offset:0
v_fma_f32 v20, v20, v122, v124
mav 26,108,50
ds_read_b64 v[108:109], v82 offset:1024
v_mul_f32 v125, v123, v69
mav 30,112,50
R 42,832
v_fma_f32 v21, v21, v122, v125
mva 34,50,116
v_exp_f32 v119, v119
mav 26,120,54
R 46,896
v_add_f32 v120, v118, v119
mav 30,124,54
v_add_f32 v121, v118, v119
v_cvt_pk_bf16_f32 v0, v14, v15
mva 34,54,128
s_waitcnt lgkmcnt(3)
v_permlane32_swap_b32 v120, v121
mav 26,132,58
R 50,960
v_add_f32 v114, v120, v121
mav 30,136,58
v_add_f32 v115, v120, v121
v_cvt_pk_bf16_f32 v1, v16, v17
mva 34,58,140
v_permlane16_swap_b32 v114, v115
mav 26,144,38
R 54,1024
v_add_f32 v120, v114, v115
mav 30,148,38
v_rcp_f32 v120, v120
mva 34,38,152
s_waitcnt lgkmcnt(2)
v_mul_f32 v118, v118, v120
v_mul_f32 v119, v119, v120
mav 26,156,42
R 38,1088
v_cvt_pk_bf16_f32 v118, v118, v118
mav 30,160,42
buffer_store_dwordx4 v[100:103], v82, s[36:39], s54 offen offset:2048 sc1
v_cvt_pk_bf16_f32 v119, v119, v119
v_and_b32 v114, v118, v94
v_and_b32 v115, v119, v95
mva 34,42,164
v_and_b32 v116, v118, v96
v_and_b32 v117, v119, v97
mav 26,168,46
buffer_store_dwordx2 v[8:9], v98, s[36:39], s54 offen offset:3072 sc1
R 42,1152
v_mfma_f32_16x16x32_bf16 v[70:73], v[10:13], v[114:117], 0
v_cvt_pk_bf16_f32 v2, v18, v19
v_cvt_pk_bf16_f32 v3, v20, v21
v_mfma_f32_16x16x32_bf16 v[74:77], v[110:113], v[114:117], 0
v_add_u32 v78, 1, v78
mav 30,172,46
s_waitcnt vmcnt(2)
mva 34,46,176
s_waitcnt lgkmcnt(2)
buffer_store_dword v78, off, s[36:39], s52 sc1
mav 26,180,50
R 46,1216
v_cvt_pk_bf16_f32 v114, v70, v71
mav 30,184,50
buffer_store_dwordx2 v[108:109], v98, s[36:39], s54 offen offset:3584 sc1
v_cvt_pk_bf16_f32 v115, v72, v73
mva 34,50,188
v_cvt_pk_bf16_f32 v116, v74, v75
v_cvt_pk_bf16_f32 v117, v76, v77
mav 26,192,54
buffer_store_dwordx4 v[114:117], v82, s[36:39], s49 offen sc1
R 50,1280
s_add_u32 s49, s49, 313344
mav 30,196,54
mva 34,54,200
s_waitcnt lgkmcnt(2)
mav 26,204,38
R 54,1344
mav 30,208,38
mva 34,38,212
LsQ5:
s_cbranch_execz LsQ9
mav 26,216,42
R 38,1408
mav 30,220,42
mva 34,42,224
s_waitcnt lgkmcnt(2)
mav 26,228,46
R 42,1472
mav 30,232,46
mva 34,46,236
mav 26,240,50
R 46,1536
mav 30,244,50
mva 34,50,248
s_waitcnt lgkmcnt(2)
mav 26,252,54
R 50,1600
mvv 30,128,54
mvv 34,54,132
mvv 26,136,38
R 54,1664
mvv 30,140,38
mvv 34,38,144
s_waitcnt lgkmcnt(2)
mvv 26,148,42
R 38,1728
mvv 30,152,42
mvv 34,42,156
mvv 26,160,46
R 42,1792
mvv 30,164,46
mvv 34,46,168
s_waitcnt lgkmcnt(2)
mvv 26,172,50
R 46,1856
mvv 30,176,50
mvv 34,50,180
mvv 26,184,54
R 50,1920
mvv 30,188,54
s_waitcnt lgkmcnt(2)
mvv 34,54,192
s_mov_b32 s60, -14
.p2align 6
LQA0_2:
mvv 26,196,38
R 54,1984
v_xor_b32 v80, v81, v80
mvv 30,200,38
mvv 34,38,204
mvv 26,208,42
R 38,33024
mvv 30,212,42
ds_read_b64 v[122:123], v83 offset:128
mvv 34,42,216
s_waitcnt lgkmcnt(3)
ds_read_b128 v[70:73], v84 offset:128
mvv 26,220,46
R 42,33088
mvv 30,224,46
ds_read_b128 v[74:77], v84 offset:144
mvv 34,46,228
mvv 26,232,50
R 46,33152
mvv 30,236,50
mvv 34,50,240
s_waitcnt lgkmcnt(3)
mvv 26,244,54
R 50,33216
mvv 30,248,54
mvv 34,54,252
v_mfma_f32_16x16x32_bf16 v[14:17], a[0:3], v[38:41], 0
s_waitcnt lgkmcnt(1)
R 54,33280
v_mul_f32 v71, v71, v99
v_mfma_f32_16x16x32_bf16 v[18:21], a[4:7], v[38:41], 0
v_mul_f32 v73, v73, v99
v_mul_f32 v75, v75, v99
v_mfma_f32_16x16x32_bf16 v[22:25], v[38:41], a[8:11], 0
v_fma_f32 v34, v34, v70, v71
v_fma_f32 v35, v35, v72, v73
mav 14,12,42
R 58,33344
v_fma_f32 v36, v36, v74, v75
mav 18,16,42
v_mul_f32 v77, v77, v99
v_fma_f32 v37, v37, v76, v77
mva 22,42,20
v_cvt_pk_bf16_f32 v10, v34, v35
v_cvt_pk_bf16_f32 v11, v36, v37
mav 14,24,46
R 38,33408
v_mul_f32 v124, v123, v62
v_mfma_f32_16x16x32_bf16 v[70:73], v[100:103], v[0:3], v[86:89]
v_fma_f32 v26, v26, v122, v124
v_mul_f32 v125, v123, v63
mav 18,28,46
v_fma_f32 v27, v27, v122, v125
v_mul_f32 v124, v123, v64
mva 22,46,32
s_waitcnt lgkmcnt(2)
v_fma_f32 v28, v28, v122, v124
v_mul_f32 v125, v123, v65
mav 14,36,50
R 42,33472
v_fma_f32 v29, v29, v122, v125
mav 18,40,50
v_mul_f32 v124, v123, v66
v_fma_f32 v30, v30, v122, v124
mva 22,50,44
v_mul_f32 v125, v123, v67
v_fma_f32 v31, v31, v122, v125
mav 14,48,54
R 46,33536
v_mul_f32 v124, v123, v68
mav 18,52,54
v_fma_f32 v32, v32, v122, v124
v_mul_f32 v125, v123, v69
mva 22,54,56
s_waitcnt lgkmcnt(2)
v_fma_f32 v33, v33, v122, v125
v_add_u32 v78, 1, v78
mav 14,60,58
R 50,33600
mav 18,64,58
mva 22,58,68
mav 14,72,38
R 54,33664
mav 18,76,38
mva 22,38,80
s_waitcnt lgkmcnt(2)
mav 14,84,42
R 58,33728
mav 18,88,42
mva 22,42,92
mav 14,96,46
R 38,33792
mav 18,100,46
s_barrier
mva 22,46,104
s_waitcnt lgkmcnt(2)
ds_read_b128 v[104:107], v82 offset:4096
mav 14,108,50
ds_read_b64 v[110:111], v82 offset:5120
mav 18,112,50
R 42,33856
mva 22,50,116
mav 14,120,54
s_waitcnt vmcnt(0)
buffer_store_dword v78, off, s[36:39], s52 sc1
mav 18,124,54
R 46,33920
mva 22,54,128
s_waitcnt lgkmcnt(3)
v_mfma_f32_16x16x32_bf16 v[74:77], v[104:107], v[0:3], v[90:93]
v_cvt_pk_bf16_f32 v0, v26, v27
v_cvt_pk_bf16_f32 v1, v28, v29
mav 14,132,58
R 50,33984
v_cvt_pk_bf16_f32 v2, v30, v31
mav 18,136,58
v_cvt_pk_bf16_f32 v3, v32, v33
mva 22,58,140
v_max3_f32 v118, v70, v71, v74
v_max3_f32 v119, v72, v73, v76
mav 14,144,38
R 54,34048
v_max_f32 v118, v118, v75
mav 18,148,38
v_max_f32 v119, v119, v77
v_max_f32 v120, v118, v119
mva 22,38,152
s_waitcnt lgkmcnt(2)
v_max_f32 v121, v118, v119
mav 14,156,42
R 38,34112
mav 18,160,42
v_permlane32_swap_b32 v120, v121
mva 22,42,164
v_max_f32 v114, v120, v121
v_max_f32 v115, v120, v121
mav 14,168,46
R 42,34176
mav 18,172,46
v_permlane16_swap_b32 v114, v115
mva 22,46,176
s_waitcnt lgkmcnt(2)
v_max_f32 v120, v114, v115
v_mul_f32 v120, s65, v120
mav 14,180,50
R 46,34240
v_fma_f32 v118, v118, s65, -v120
mav 18,184,50
v_fma_f32 v119, v119, s65, -v120
mva 22,50,188
v_exp_f32 v118, v118
mav 14,192,54
R 50,34304
mav 18,196,54
v_exp_f32 v119, v119
mva 22,54,200
s_waitcnt lgkmcnt(2)
v_add_f32 v120, v118, v119
v_add_f32 v121, v118, v119
mav 14,204,38
R 54,34368
mav 18,208,38
v_permlane32_swap_b32 v120, v121
mva 22,38,212
v_add_f32 v114, v120, v121
v_add_f32 v115, v120, v121
mav 14,216,42
R 38,34432
mav 18,220,42
v_permlane16_swap_b32 v114, v115
mva 22,42,224
s_waitcnt lgkmcnt(2)
v_add_f32 v120, v114, v115
mav 14,228,46
R 42,34496
mav 18,232,46
v_rcp_f32 v120, v120
mva 22,46,236
v_mul_f32 v118, v118, v120
v_mul_f32 v119, v119, v120
mav 14,240,50
R 46,34560
v_cvt_pk_bf16_f32 v118, v118, v118
mav 18,244,50
v_cvt_pk_bf16_f32 v119, v119, v119
v_and_b32 v114, v118, v94
mva 22,50,248
s_waitcnt lgkmcnt(2)
v_and_b32 v115, v119, v95
v_and_b32 v116, v118, v96
mav 14,252,54
R 50,34624
v_and_b32 v117, v119, v97
mvv 18,128,54
mvv 22,54,132
v_mfma_f32_16x16x32_bf16 v[70:73], v[8:11], v[114:117], 0
v_mfma_f32_16x16x32_bf16 v[74:77], v[108:111], v[114:117], 0
mvv 14,136,38
R 54,34688
mvv 18,140,38
mvv 22,38,144
s_waitcnt lgkmcnt(2)
mvv 14,148,42
R 38,34752
v_cvt_pk_bf16_f32 v114, v70, v71
mvv 18,152,42
v_cvt_pk_bf16_f32 v115, v72, v73
v_cvt_pk_bf16_f32 v116, v74, v75
mvv 22,42,156
v_cvt_pk_bf16_f32 v117, v76, v77
mvv 14,160,46
buffer_store_dwordx4 v[114:117], v82, s[36:39], s49 offen sc1
R 42,34816
s_add_u32 s49, s49, 313344
mvv 18,164,46
mvv 22,46,168
s_waitcnt lgkmcnt(2)
mvv 14,172,50
R 46,34880
mvv 18,176,50
mvv 22,50,180
mvv 14,184,54
R 50,34944
mvv 18,188,54
mvv 22,54,192
s_waitcnt lgkmcnt(2)
mvv 14,196,38
R 54,35008
mvv 18,200,38
mvv 22,38,204
mvv 14,208,42
R 38,0
mvv 18,212,42
ds_read_b64 v[122:123], v83 offset:0
mvv 22,42,216
s_waitcnt lgkmcnt(3)
ds_read_b128 v[70:73], v84 offset:0
mvv 14,220,46
R 42,64
mvv 18,224,46
ds_read_b128 v[74:77], v84 offset:16
mvv 22,46,228
mvv 14,232,50
R 46,128
mvv 18,236,50
mvv 22,50,240
s_waitcnt lgkmcnt(3)
mvv 14,244,54
R 50,192
mvv 18,248,54
mvv 22,54,252
v_mfma_f32_16x16x32_bf16 v[26:29], a[0:3], v[38:41], 0
s_waitcnt lgkmcnt(1)
R 54,256
v_mul_f32 v71, v71, v99
v_mfma_f32_16x16x32_bf16 v[30:33], a[4:7], v[38:41], 0
v_mul_f32 v73, v73, v99
v_mul_f32 v75, v75, v99
v_mfma_f32_16x16x32_bf16 v[34:37], v[38:41], a[8:11], 0
v_fma_f32 v22, v22, v70, v71
v_fma_f32 v23, v23, v72, v73
mav 26,12,42
R 58,320
v_fma_f32 v24, v24, v74, v75
mav 30,16,42
v_mul_f32 v77, v77, v99
v_fma_f32 v25, v25, v76, v77
mva 34,42,20
v_cvt_pk_bf16_f32 v8, v22, v23
v_cvt_pk_bf16_f32 v9, v24, v25
mav 26,24,46
R 38,384
v_mul_f32 v124, v123, v62
v_mfma_f32_16x16x32_bf16 v[70:73], v[104:107], v[0:3], v[86:89]
v_fma_f32 v14, v14, v122, v124
v_mul_f32 v125, v123, v63
mav 30,28,46
v_fma_f32 v15, v15, v122, v125
v_mul_f32 v124, v123, v64
mva 34,46,32
s_waitcnt lgkmcnt(2)
v_fma_f32 v16, v16, v122, v124
v_mul_f32 v125, v123, v65
mav 26,36,50
R 42,448
v_fma_f32 v17, v17, v122, v125
mav 30,40,50
v_mul_f32 v124, v123, v66
v_fma_f32 v18, v18, v122, v124
mva 34,50,44
v_mul_f32 v125, v123, v67
v_fma_f32 v19, v19, v122, v125
mav 26,48,54
R 46,512
v_mul_f32 v124, v123, v68
mav 30,52,54
v_fma_f32 v20, v20, v122, v124
v_mul_f32 v125, v123, v69
mva 34,54,56
s_waitcnt lgkmcnt(2)
v_fma_f32 v21, v21, v122, v125
v_add_u32 v78, 1, v78
mav 26,60,58
R 50,576
mav 30,64,58
mva 34,58,68
mav 26,72,38
R 54,640
mav 30,76,38
mva 34,38,80
s_waitcnt lgkmcnt(2)
mav 26,84,42
R 58,704
mav 30,88,42
mva 34,42,92
mav 26,96,46
R 38,768
mav 30,100,46
s_barrier
mva 34,46,104
s_waitcnt lgkmcnt(2)
ds_read_b128 v[100:103], v82 offset:0
mav 26,108,50
ds_read_b64 v[108:109], v82 offset:1024
mav 30,112,50
R 42,832
mva 34,50,116
mav 26,120,54
s_waitcnt vmcnt(0)
buffer_store_dword v78, off, s[36:39], s52 sc1
mav 30,124,54
R 46,896
mva 34,54,128
s_waitcnt lgkmcnt(3)
v_mfma_f32_16x16x32_bf16 v[74:77], v[100:103], v[0:3], v[90:93]
v_cvt_pk_bf16_f32 v0, v14, v15
v_cvt_pk_bf16_f32 v1, v16, v17
mav 26,132,58
R 50,960
v_cvt_pk_bf16_f32 v2, v18, v19
mav 30,136,58
v_cvt_pk_bf16_f32 v3, v20, v21
mva 34,58,140
v_max3_f32 v118, v70, v71, v74
v_max3_f32 v119, v72, v73, v76
mav 26,144,38
R 54,1024
v_max_f32 v118, v118, v75
mav 30,148,38
v_max_f32 v119, v119, v77
v_max_f32 v120, v118, v119
mva 34,38,152
s_waitcnt lgkmcnt(2)
v_max_f32 v121, v118, v119
mav 26,156,42
R 38,1088
mav 30,160,42
v_permlane32_swap_b32 v120, v121
mva 34,42,164
v_max_f32 v114, v120, v121
v_max_f32 v115, v120, v121
mav 26,168,46
R 42,1152
mav 30,172,46
v_permlane16_swap_b32 v114, v115
mva 34,46,176
s_waitcnt lgkmcnt(2)
v_max_f32 v120, v114, v115
v_mul_f32 v120, s65, v120
mav 26,180,50
R 46,1216
v_fma_f32 v118, v118, s65, -v120
mav 30,184,50
v_fma_f32 v119, v119, s65, -v120
mva 34,50,188
v_exp_f32 v118, v118
mav 26,192,54
R 50,1280
mav 30,196,54
v_exp_f32 v119, v119
mva 34,54,200
s_waitcnt lgkmcnt(2)
v_add_f32 v120, v118, v119
v_add_f32 v121, v118, v119
mav 26,204,38
R 54,1344
mav 30,208,38
v_permlane32_swap_b32 v120, v121
mva 34,38,212
v_add_f32 v114, v120, v121
v_add_f32 v115, v120, v121
mav 26,216,42
R 38,1408
mav 30,220,42
v_permlane16_swap_b32 v114, v115
mva 34,42,224
s_waitcnt lgkmcnt(2)
v_add_f32 v120, v114, v115
mav 26,228,46
R 42,1472
mav 30,232,46
v_rcp_f32 v120, v120
mva 34,46,236
v_mul_f32 v118, v118, v120
v_mul_f32 v119, v119, v120
mav 26,240,50
R 46,1536
v_cvt_pk_bf16_f32 v118, v118, v118
mav 30,244,50
v_cvt_pk_bf16_f32 v119, v119, v119
v_and_b32 v114, v118, v96
mva 34,50,248
s_waitcnt lgkmcnt(2)
v_and_b32 v115, v119, v97
v_and_b32 v116, v118, v94
mav 26,252,54
R 50,1600
v_and_b32 v117, v119, v95
mvv 30,128,54
mvv 34,54,132
v_mfma_f32_16x16x32_bf16 v[70:73], v[8:11], v[114:117], 0
v_mfma_f32_16x16x32_bf16 v[74:77], v[108:111], v[114:117], 0
mvv 26,136,38
R 54,1664
mvv 30,140,38
mvv 34,38,144
s_waitcnt lgkmcnt(2)
mvv 26,148,42
R 38,1728
v_cvt_pk_bf16_f32 v114, v70, v71
mvv 30,152,42
v_cvt_pk_bf16_f32 v115, v72, v73
v_cvt_pk_bf16_f32 v116, v74, v75
mvv 34,42,156
v_cvt_pk_bf16_f32 v117, v76, v77
mvv 26,160,46
buffer_store_dwordx4 v[114:117], v82, s[36:39], s49 offen sc1
R 42,1792
s_add_u32 s49, s49, 313344
mvv 30,164,46
mvv 34,46,168
s_waitcnt lgkmcnt(2)
mvv 26,172,50
R 46,1856
mvv 30,176,50
mvv 34,50,180
mvv 26,184,54
R 50,1920
mvv 30,188,54
s_waitcnt lgkmcnt(2)
s_add_u32 s60, s60, 1
mvv 34,54,192
s_cbranch_scc0 LQA0_2
mvv 26,196,38
R 54,1984
v_xor_b32 v80, v81, v80
mvv 30,200,38
mvv 34,38,204
mvv 26,208,42
R 38,33024
mvv 30,212,42
ds_read_b64 v[122:123], v83 offset:128
mvv 34,42,216
s_waitcnt lgkmcnt(3)
ds_read_b128 v[70:73], v84 offset:128
mvv 26,220,46
R 42,33088
mvv 30,224,46
ds_read_b128 v[74:77], v84 offset:144
mvv 34,46,228
mvv 26,232,50
R 46,33152
mvv 30,236,50
mvv 34,50,240
s_waitcnt lgkmcnt(3)
mvv 26,244,54
R 50,33216
mvv 30,248,54
mvv 34,54,252
v_mfma_f32_16x16x32_bf16 v[14:17], a[0:3], v[38:41], 0
s_waitcnt lgkmcnt(1)
R 54,33280
v_mul_f32 v71, v71, v99
v_mfma_f32_16x16x32_bf16 v[18:21], a[4:7], v[38:41], 0
v_mul_f32 v73, v73, v99
v_mul_f32 v75, v75, v99
v_mfma_f32_16x16x32_bf16 v[22:25], v[38:41], a[8:11], 0
v_fma_f32 v34, v34, v70, v71
v_fma_f32 v35, v35, v72, v73
mav 14,12,42
R 58,33344
v_fma_f32 v36, v36, v74, v75
mav 18,16,42
v_mul_f32 v77, v77, v99
v_fma_f32 v37, v37, v76, v77
mva 22,42,20
v_cvt_pk_bf16_f32 v10, v34, v35
v_cvt_pk_bf16_f32 v11, v36, v37
mav 14,24,46
R 38,33408
v_mul_f32 v124, v123, v62
v_mfma_f32_16x16x32_bf16 v[70:73], v[100:103], v[0:3], v[86:89]
v_fma_f32 v26, v26, v122, v124
v_mul_f32 v125, v123, v63
mav 18,28,46
v_fma_f32 v27, v27, v122, v125
v_mul_f32 v124, v123, v64
mva 22,46,32
s_waitcnt lgkmcnt(2)
v_fma_f32 v28, v28, v122, v124
v_mul_f32 v125, v123, v65
mav 14,36,50
R 42,33472
v_fma_f32 v29, v29, v122, v125
mav 18,40,50
v_mul_f32 v124, v123, v66
v_fma_f32 v30, v30, v122, v124
mva 22,50,44
v_mul_f32 v125, v123, v67
v_fma_f32 v31, v31, v122, v125
mav 14,48,54
R 46,33536
v_mul_f32 v124, v123, v68
mav 18,52,54
v_fma_f32 v32, v32, v122, v124
v_mul_f32 v125, v123, v69
mva 22,54,56
s_waitcnt lgkmcnt(2)
v_fma_f32 v33, v33, v122, v125
v_add_u32 v78, 1, v78
mav 14,60,58
R 50,33600
mav 18,64,58
mva 22,58,68
mav 14,72,38
R 54,33664
mav 18,76,38
mva 22,38,80
s_waitcnt lgkmcnt(2)
mav 14,84,42
R 58,33728
LsQ6:
s_cbranch_execz LsQ10
mav 18,88,42
mva 22,42,92
mav 14,96,46
R 38,33792
mav 18,100,46
s_barrier
mva 22,46,104
s_waitcnt lgkmcnt(2)
ds_read_b128 v[104:107], v82 offset:4096
mav 14,108,50
ds_read_b64 v[110:111], v82 offset:5120
mav 18,112,50
R 42,33856
mva 22,50,116
mav 14,120,54
s_waitcnt vmcnt(0)
buffer_store_dword v78, off, s[36:39], s52 sc1
mav 18,124,54
R 46,33920
mva 22,54,128
s_waitcnt lgkmcnt(3)
v_mfma_f32_16x16x32_bf16 v[74:77], v[104:107], v[0:3], v[90:93]
v_cvt_pk_bf16_f32 v0, v26, v27
v_cvt_pk_bf16_f32 v1, v28, v29
mav 14,132,58
R 50,33984
v_cvt_pk_bf16_f32 v2, v30, v31
mav 18,136,58
v_cvt_pk_bf16_f32 v3, v32, v33
mva 22,58,140
v_max3_f32 v118, v70, v71, v74
v_max3_f32 v119, v72, v73, v76
mav 14,144,38
R 54,34048
v_max_f32 v118, v118, v75
mav 18,148,38
v_max_f32 v119, v119, v77
v_max_f32 v120, v118, v119
mva 22,38,152
s_waitcnt lgkmcnt(2)
v_max_f32 v121, v118, v119
mav 14,156,42
R 38,34112
mav 18,160,42
v_permlane32_swap_b32 v120, v121
mva 22,42,164
v_max_f32 v114, v120, v121
v_max_f32 v115, v120, v121
mav 14,168,46
R 42,34176
mav 18,172,46
v_permlane16_swap_b32 v114, v115
mva 22,46,176
s_waitcnt lgkmcnt(2)
v_max_f32 v120, v114, v115
v_mul_f32 v120, s65, v120
mav 14,180,50
R 46,34240
v_fma_f32 v118, v118, s65, -v120
mav 18,184,50
v_fma_f32 v119, v119, s65, -v120
mva 22,50,188
v_exp_f32 v118, v118
mav 14,192,54
R 50,34304
mav 18,196,54
v_exp_f32 v119, v119
mva 22,54,200
s_waitcnt lgkmcnt(2)
v_add_f32 v120, v118, v119
v_add_f32 v121, v118, v119
mav 14,204,38
R 54,34368
mav 18,208,38
v_permlane32_swap_b32 v120, v121
mva 22,38,212
v_add_f32 v114, v120, v121
v_add_f32 v115, v120, v121
mav 14,216,42
R 38,34432
mav 18,220,42
v_permlane16_swap_b32 v114, v115
mva 22,42,224
s_waitcnt lgkmcnt(2)
v_add_f32 v120, v114, v115
mav 14,228,46
R 42,34496
mav 18,232,46
v_rcp_f32 v120, v120
mva 22,46,236
v_mul_f32 v118, v118, v120
v_mul_f32 v119, v119, v120
mav 14,240,50
R 46,34560
v_cvt_pk_bf16_f32 v118, v118, v118
mav 18,244,50
v_cvt_pk_bf16_f32 v119, v119, v119
v_and_b32 v114, v118, v94
mva 22,50,248
s_waitcnt lgkmcnt(2)
v_and_b32 v115, v119, v95
v_and_b32 v116, v118, v96
mav 14,252,54
R 50,34624
v_and_b32 v117, v119, v97
mvv 18,128,54
mvv 22,54,132
v_mfma_f32_16x16x32_bf16 v[70:73], v[8:11], v[114:117], 0
v_mfma_f32_16x16x32_bf16 v[74:77], v[108:111], v[114:117], 0
mvv 14,136,38
R 54,34688
mvv 18,140,38
mvv 22,38,144
s_waitcnt lgkmcnt(2)
mvv 14,148,42
R 38,34752
v_cvt_pk_bf16_f32 v114, v70, v71
mvv 18,152,42
v_cvt_pk_bf16_f32 v115, v72, v73
v_cvt_pk_bf16_f32 v116, v74, v75
mvv 22,42,156
v_cvt_pk_bf16_f32 v117, v76, v77
mvv 14,160,46
buffer_store_dwordx4 v[114:117], v82, s[36:39], s49 offen sc1
R 42,34816
s_add_u32 s49, s49, 313344
mvv 18,164,46
mvv 22,46,168
s_waitcnt lgkmcnt(2)
mvv 14,172,50
R 46,34880
mvv 18,176,50
mvv 22,50,180
mvv 14,184,54
R 50,34944
mvv 18,188,54
mvv 22,54,192
s_waitcnt lgkmcnt(2)
mvv 14,196,38
R 54,35008
mvv 18,200,38
mvv 22,38,204
mvv 14,208,42
R 38,0
mvv 18,212,42
ds_read_b64 v[122:123], v83 offset:0
mvv 22,42,216
s_waitcnt lgkmcnt(3)
ds_read_b128 v[70:73], v84 offset:0
mvv 14,220,46
R 42,64
mvv 18,224,46
ds_read_b128 v[74:77], v84 offset:16
mvv 22,46,228
mvv 14,232,50
R 46,128
mvv 18,236,50
mvv 22,50,240
s_waitcnt lgkmcnt(3)
mvv 14,244,54
R 50,192
mvv 18,248,54
mvv 22,54,252
v_mfma_f32_16x16x32_bf16 v[26:29], a[0:3], v[38:41], 0
s_waitcnt lgkmcnt(1)
R 54,256
v_mul_f32 v71, v71, v99
v_mfma_f32_16x16x32_bf16 v[30:33], a[4:7], v[38:41], 0
v_mul_f32 v73, v73, v99
v_mul_f32 v75, v75, v99
v_mfma_f32_16x16x32_bf16 v[34:37], v[38:41], a[8:11], 0
v_fma_f32 v22, v22, v70, v71
v_fma_f32 v23, v23, v72, v73
mav 26,12,42
R 58,320
v_fma_f32 v24, v24, v74, v75
mav 30,16,42
v_mul_f32 v77, v77, v99
v_fma_f32 v25, v25, v76, v77
mva 34,42,20
v_cvt_pk_bf16_f32 v8, v22, v23
v_cvt_pk_bf16_f32 v9, v24, v25
mav 26,24,46
R 38,384
v_mul_f32 v124, v123, v62
v_mfma_f32_16x16x32_bf16 v[70:73], v[104:107], v[0:3], v[86:89]
v_fma_f32 v14, v14, v122, v124
v_mul_f32 v125, v123, v63
mav 30,28,46
v_fma_f32 v15, v15, v122, v125
v_mul_f32 v124, v123, v64
mva 34,46,32
s_waitcnt lgkmcnt(2)
v_fma_f32 v16, v16, v122, v124
v_mul_f32 v125, v123, v65
mav 26,36,50
R 42,448
v_fma_f32 v17, v17, v122, v125
mav 30,40,50
buffer_load_dwordx4 v[4:7], v82, s[36:39], s55 offen offset:2048 sc1
mva 34,50,44
v_mul_f32 v124, v123, v66
v_fma_f32 v18, v18, v122, v124
mav 26,48,54
buffer_load_dwordx2 v[12:13], v98, s[36:39], s55 offen offset:3072 sc1
mav 30,52,54
R 46,512
v_mul_f32 v125, v123, v67
mva 34,54,56
s_waitcnt lgkmcnt(2)
buffer_load_dwordx2 v[112:113], v98, s[36:39], s55 offen offset:3584 sc1
mav 26,60,58
R 50,576
v_fma_f32 v19, v19, v122, v125
mav 30,64,58
v_mul_f32 v124, v123, v68
v_fma_f32 v20, v20, v122, v124
mva 34,58,68
v_mul_f32 v125, v123, v69
v_fma_f32 v21, v21, v122, v125
mav 26,72,38
R 54,640
v_add_u32 v78, 1, v78
mav 30,76,38
mva 34,38,80
s_waitcnt lgkmcnt(2)
mav 26,84,42
R 58,704
mav 30,88,42
mva 34,42,92
mav 26,96,46
R 38,768
mav 30,100,46
s_barrier
mva 34,46,104
s_waitcnt lgkmcnt(2)
ds_read_b128 v[100:103], v82 offset:0
mav 26,108,50
ds_read_b64 v[108:109], v82 offset:1024
mav 30,112,50
R 42,832
mva 34,50,116
mav 26,120,54
s_waitcnt vmcnt(3)
buffer_store_dword v78, off, s[36:39], s52 sc1
mav 30,124,54
R 46,896
mva 34,54,128
s_waitcnt lgkmcnt(3)
v_mfma_f32_16x16x32_bf16 v[74:77], v[100:103], v[0:3], v[90:93]
v_cvt_pk_bf16_f32 v0, v14, v15
v_cvt_pk_bf16_f32 v1, v16, v17
mav 26,132,58
R 50,960
v_cvt_pk_bf16_f32 v2, v18, v19
mav 30,136,58
v_cvt_pk_bf16_f32 v3, v20, v21
mva 34,58,140
v_max3_f32 v118, v70, v71, v74
v_max3_f32 v119, v72, v73, v76
mav 26,144,38
R 54,1024
v_max_f32 v118, v118, v75
mav 30,148,38
v_max_f32 v119, v119, v77
v_max_f32 v120, v118, v119
mva 34,38,152
s_waitcnt lgkmcnt(2)
v_max_f32 v121, v118, v119
mav 26,156,42
R 38,1088
mav 30,160,42
v_permlane32_swap_b32 v120, v121
mva 34,42,164
v_max_f32 v114, v120, v121
v_max_f32 v115, v120, v121
mav 26,168,46
R 42,1152
mav 30,172,46
v_permlane16_swap_b32 v114, v115
mva 34,46,176
s_waitcnt lgkmcnt(2)
v_max_f32 v120, v114, v115
v_mul_f32 v120, s65, v120
mav 26,180,50
R 46,1216
v_fma_f32 v118, v118, s65, -v120
mav 30,184,50
v_fma_f32 v119, v119, s65, -v120
mva 34,50,188
v_exp_f32 v118, v118
mav 26,192,54
R 50,1280
mav 30,196,54
v_exp_f32 v119, v119
mva 34,54,200
s_waitcnt lgkmcnt(2)
v_add_f32 v120, v118, v119
v_add_f32 v121, v118, v119
mav 26,204,38
R 54,1344
mav 30,208,38
v_permlane32_swap_b32 v120, v121
mva 34,38,212
v_add_f32 v114, v120, v121
v_add_f32 v115, v120, v121
mav 26,216,42
R 38,1408
mav 30,220,42
v_permlane16_swap_b32 v114, v115
mva 34,42,224
s_waitcnt lgkmcnt(2)
v_add_f32 v120, v114, v115
mav 26,228,46
R 42,1472
mav 30,232,46
v_rcp_f32 v120, v120
mva 34,46,236
v_mul_f32 v118, v118, v120
v_mul_f32 v119, v119, v120
mav 26,240,50
R 46,1536
v_cvt_pk_bf16_f32 v118, v118, v118
mav 30,244,50
v_cvt_pk_bf16_f32 v119, v119, v119
v_and_b32 v114, v118, v96
mva 34,50,248
s_waitcnt lgkmcnt(2)
v_and_b32 v115, v119, v97
v_and_b32 v116, v118, v94
mav 26,252,54
R 50,1600
v_and_b32 v117, v119, v95
mvv 30,128,54
mvv 34,54,132
v_mfma_f32_16x16x32_bf16 v[70:73], v[8:11], v[114:117], 0
v_mfma_f32_16x16x32_bf16 v[74:77], v[108:111], v[114:117], 0
mvv 26,136,38
R 54,1664
mvv 30,140,38
mvv 34,38,144
s_waitcnt lgkmcnt(2)
mvv 26,148,42
R 38,1728
v_cvt_pk_bf16_f32 v114, v70, v71
mvv 30,152,42
v_cvt_pk_bf16_f32 v115, v72, v73
v_cvt_pk_bf16_f32 v116, v74, v75
mvv 34,42,156
v_cvt_pk_bf16_f32 v117, v76, v77
mvv 26,160,46
buffer_store_dwordx4 v[114:117], v82, s[36:39], s49 offen sc1
R 42,1792
s_add_u32 s49, s49, 313344
mvv 30,164,46
mvv 34,46,168
s_waitcnt lgkmcnt(2)
mvv 26,172,50
R 46,1856
mvv 30,176,50
mvv 34,50,180
mvv 26,184,54
R 50,1920
mvv 30,188,54
s_waitcnt lgkmcnt(2)
mvv 34,54,192
mvv 26,196,38
R 54,1984
mvv 30,200,38
mvv 34,38,204
mvv 26,208,42
mvv 30,212,42
ds_read_b64 v[122:123], v83 offset:128
mvv 34,42,216
s_waitcnt lgkmcnt(2)
ds_read_b128 v[70:73], v84 offset:128
mvv 26,220,46
ds_read_b128 v[74:77], v84 offset:144
mvv 30,224,46
mvv 34,46,228
mvv 26,232,50
mvv 30,236,50
mvv 34,50,240
s_waitcnt lgkmcnt(1)
mvv 26,244,54
mvv 30,248,54
mvv 34,54,252
v_mul_f32 v71, v71, v99
s_nop 6
v_fma_f32 v34, v34, v70, v71
v_mul_f32 v73, v73, v99
v_fma_f32 v35, v35, v72, v73
s_waitcnt lgkmcnt(0)
v_mul_f32 v75, v75, v99
v_fma_f32 v36, v36, v74, v75
v_mul_f32 v77, v77, v99
v_fma_f32 v37, v37, v76, v77
v_cvt_pk_bf16_f32 v10, v34, v35
v_cvt_pk_bf16_f32 v11, v36, v37
s_nop 1
v_mfma_f32_16x16x32_bf16 v[70:73], v[100:103], v[0:3], v[86:89]
v_mul_f32 v124, v123, v62
v_fma_f32 v26, v26, v122, v124
v_mul_f32 v125, v123, v63
v_fma_f32 v27, v27, v122, v125
v_mul_f32 v124, v123, v64
v_fma_f32 v28, v28, v122, v124
v_mul_f32 v125, v123, v65
v_fma_f32 v29, v29, v122, v125
v_mul_f32 v124, v123, v66
v_fma_f32 v30, v30, v122, v124
v_mul_f32 v125, v123, v67
v_fma_f32 v31, v31, v122, v125
v_mul_f32 v124, v123, v68
v_fma_f32 v32, v32, v122, v124
v_mul_f32 v125, v123, v69
v_fma_f32 v33, v33, v122, v125
s_barrier
ds_read_b128 v[104:107], v82 offset:4096
ds_read_b64 v[110:111], v82 offset:5120
s_waitcnt lgkmcnt(0)
v_mfma_f32_16x16x32_bf16 v[74:77], v[104:107], v[0:3], v[90:93]
s_nop 7
v_max3_f32 v118, v70, v71, v74
v_max3_f32 v119, v72, v73, v76
v_max_f32 v118, v118, v75
v_max_f32 v119, v119, v77
v_max_f32 v120, v118, v119
v_max_f32 v121, v118, v119
s_nop 1
v_permlane32_swap_b32 v120, v121
v_max_f32 v114, v120, v121
v_max_f32 v115, v120, v121
s_nop 1
v_permlane16_swap_b32 v114, v115
v_max_f32 v120, v114, v115
v_mul_f32 v120, s65, v120
v_fma_f32 v118, v118, s65, -v120
v_fma_f32 v119, v119, s65, -v120
v_exp_f32 v118, v118
v_exp_f32 v119, v119
s_nop 0
v_add_f32 v120, v118, v119
v_add_f32 v121, v118, v119
s_nop 1
v_permlane32_swap_b32 v120, v121
v_add_f32 v114, v120, v121
v_add_f32 v115, v120, v121
s_nop 1
v_permlane16_swap_b32 v114, v115
v_add_f32 v120, v114, v115
v_rcp_f32 v120, v120
s_nop 0
v_mul_f32 v118, v118, v120
v_mul_f32 v119, v119, v120
v_cvt_pk_bf16_f32 v118, v118, v118
v_cvt_pk_bf16_f32 v119, v119, v119
v_and_b32 v114, v118, v94
v_and_b32 v115, v119, v95
v_and_b32 v116, v118, v96
v_and_b32 v117, v119, v97
s_nop 1
v_mfma_f32_16x16x32_bf16 v[70:73], v[8:11], v[114:117], 0
v_mfma_f32_16x16x32_bf16 v[74:77], v[108:111], v[114:117], 0
s_nop 6
v_cvt_pk_bf16_f32 v114, v70, v71
v_cvt_pk_bf16_f32 v115, v72, v73
v_cvt_pk_bf16_f32 v116, v74, v75
v_cvt_pk_bf16_f32 v117, v76, v77
buffer_store_dwordx4 v[114:117], v82, s[36:39], s49 offen sc1
s_add_u32 s49, s49, 313344
v_cvt_pk_bf16_f32 v0, v26, v27
v_cvt_pk_bf16_f32 v1, v28, v29
v_cvt_pk_bf16_f32 v2, v30, v31
v_cvt_pk_bf16_f32 v3, v32, v33
v_add_u32 v78, 1, v78
s_waitcnt vmcnt(1)
buffer_store_dword v78, off, s[36:39], s52 sc1
v_mfma_f32_16x16x32_bf16 v[70:73], v[104:107], v[0:3], v[86:89]
v_min_f32 v74, s56, v90
v_min_f32 v75, s56, v91
v_min_f32 v76, s56, v92
v_min_f32 v77, s56, v93
s_nop 1
mvv 74,4,0
s_nop 7
v_max3_f32 v118, v70, v71, v74
v_max3_f32 v119, v72, v73, v76
v_max_f32 v118, v118, v75
v_max_f32 v119, v119, v77
v_max_f32 v120, v118, v119
v_max_f32 v121, v118, v119
s_nop 1
v_permlane32_swap_b32 v120, v121
v_max_f32 v114, v120, v121
v_max_f32 v115, v120, v121
s_nop 1
v_permlane16_swap_b32 v114, v115
v_max_f32 v120, v114, v115
v_mul_f32 v120, s65, v120
v_fma_f32 v118, v118, s65, -v120
v_fma_f32 v119, v119, s65, -v120
v_exp_f32 v118, v118
v_exp_f32 v119, v119
s_nop 0
v_add_f32 v120, v118, v119
v_add_f32 v121, v118, v119
s_nop 1
v_permlane32_swap_b32 v120, v121
v_add_f32 v114, v120, v121
v_add_f32 v115, v120, v121
s_nop 1
v_permlane16_swap_b32 v114, v115
v_add_f32 v120, v114, v115
v_rcp_f32 v120, v120
s_nop 0
v_mul_f32 v118, v118, v120
v_mul_f32 v119, v119, v120
v_cvt_pk_bf16_f32 v118, v118, v118
v_cvt_pk_bf16_f32 v119, v119, v119
v_and_b32 v114, v118, v94
v_and_b32 v115, v119, v95
v_and_b32 v116, v118, v96
v_and_b32 v117, v119, v97
s_nop 1
v_mfma_f32_16x16x32_bf16 v[70:73], v[10:13], v[114:117], 0
v_mfma_f32_16x16x32_bf16 v[74:77], v[110:113], v[114:117], 0
s_nop 6
v_cvt_pk_bf16_f32 v114, v70, v71
v_cvt_pk_bf16_f32 v115, v72, v73
v_cvt_pk_bf16_f32 v116, v74, v75
v_cvt_pk_bf16_f32 v117, v76, v77
buffer_store_dwordx4 v[114:117], v82, s[36:39], s49 offen sc1
v_add_u32 v78, 1, v78
s_waitcnt vmcnt(2)
buffer_store_dword v78, off, s[36:39], s52 sc1
v_add_u32 v78, 37, v78
s_waitcnt vmcnt(1)
buffer_store_dword v78, off, s[36:39], s52 sc1
s_endpgm
LvQA1:
LsQ7:
s_cbranch_execz LsQ11
ds_read_b64 v[122:123], v83 offset:0
ds_read_b128 v[70:73], v84 offset:0
ds_read_b128 v[74:77], v84 offset:16
s_add_u32 s49, s49, 313344
s_waitcnt lgkmcnt(0)
v_mul_f32 v71, v71, v99
v_fma_f32 v22, v22, v70, v71
v_mul_f32 v73, v73, v99
v_fma_f32 v23, v23, v72, v73
v_mul_f32 v75, v75, v99
v_fma_f32 v24, v24, v74, v75
v_mul_f32 v77, v77, v99
v_fma_f32 v25, v25, v76, v77
v_cvt_pk_bf16_f32 v8, v22, v23
v_cvt_pk_bf16_f32 v9, v24, v25
v_mul_f32 v124, v123, v62
v_fma_f32 v14, v14, v122, v124
v_mul_f32 v125, v123, v63
v_fma_f32 v15, v15, v122, v125
v_mul_f32 v124, v123, v64
v_fma_f32 v16, v16, v122, v124
v_mul_f32 v125, v123, v65
v_fma_f32 v17, v17, v122, v125
v_mul_f32 v124, v123, v66
v_fma_f32 v18, v18, v122, v124
v_mul_f32 v125, v123, v67
v_fma_f32 v19, v19, v122, v125
v_mul_f32 v124, v123, v68
v_fma_f32 v20, v20, v122, v124
v_mul_f32 v125, v123, v69
v_fma_f32 v21, v21, v122, v125
v_cvt_pk_bf16_f32 v0, v14, v15
v_cvt_pk_bf16_f32 v1, v16, v17
v_cvt_pk_bf16_f32 v2, v18, v19
v_cvt_pk_bf16_f32 v3, v20, v21
s_add_u32 s49, s49, 313344
s_barrier
ds_read_b128 v[100:103], v82 offset:0
ds_read_b64 v[108:109], v82 offset:1024
R 38,33024
R 42,33088
R 46,33152
R 50,33216
ds_read_b64 v[122:123], v83 offset:128
ds_read_b128 v[70:73], v84 offset:128
ds_read_b128 v[74:77], v84 offset:144
s_waitcnt lgkmcnt(0)
v_mfma_f32_16x16x32_bf16 v[14:17], a[0:3], v[38:41], 0
R 54,33280
v_mul_f32 v71, v71, v99
v_mfma_f32_16x16x32_bf16 v[18:21], a[4:7], v[38:41], 0
v_fma_f32 v34, v34, v70, v71
v_mul_f32 v73, v73, v99
v_mfma_f32_16x16x32_bf16 v[22:25], v[38:41], a[8:11], 0
v_fma_f32 v35, v35, v72, v73
v_mul_f32 v75, v75, v99
mav 14,12,42
R 58,33344
v_fma_f32 v36, v36, v74, v75
mav 18,16,42
v_mul_f32 v77, v77, v99
v_fma_f32 v37, v37, v76, v77
mva 22,42,20
v_cvt_pk_bf16_f32 v10, v34, v35
v_cvt_pk_bf16_f32 v11, v36, v37
mav 14,24,46
R 38,33408
v_mul_f32 v124, v123, v62
v_mfma_f32_16x16x32_bf16 v[70:73], v[100:103], v[0:3], v[86:89]
v_fma_f32 v26, v26, v122, v124
v_mul_f32 v125, v123, v63
mav 18,28,46
v_fma_f32 v27, v27, v122, v125
v_mul_f32 v124, v123, v64
mva 22,46,32
v_fma_f32 v28, v28, v122, v124
v_mul_f32 v125, v123, v65
mav 14,36,50
R 42,33472
v_fma_f32 v29, v29, v122, v125
LsQ8:
s_cbranch_execz LsQ12
mav 18,40,50
v_mul_f32 v124, v123, v66
v_fma_f32 v30, v30, v122, v124
mva 22,50,44
s_waitcnt lgkmcnt(2)
v_mul_f32 v125, v123, v67
v_fma_f32 v31, v31, v122, v125
mav 14,48,54
R 46,33536
v_mul_f32 v124, v123, v68
mav 18,52,54
v_fma_f32 v32, v32, v122, v124
v_mul_f32 v125, v123, v69
mva 22,54,56
v_fma_f32 v33, v33, v122, v125
mav 14,60,58
R 50,33600
mav 18,64,58
mva 22,58,68
s_waitcnt lgkmcnt(2)
mav 14,72,38
R 54,33664
mav 18,76,38
buffer_store_dwordx4 v[100:103], v82, s[36:39], s54 offen sc1
mva 22,38,80
mav 14,84,42
buffer_store_dwordx2 v[8:9], v98, s[36:39], s54 offen offset:1024 sc1
R 58,33728
mav 18,88,42
mva 22,42,92
s_waitcnt lgkmcnt(2)
buffer_store_dwordx2 v[108:109], v98, s[36:39], s54 offen offset:1536 sc1
mav 14,96,46
R 38,33792
mav 18,100,46
s_barrier
mva 22,46,104
ds_read_b128 v[104:107], v82 offset:4096
mav 14,108,50
ds_read_b64 v[110:111], v82 offset:5120
mav 18,112,50
R 42,33856
mva 22,50,116
s_waitcnt lgkmcnt(4)
mav 14,120,54
R 46,33920
mav 18,124,54
mva 22,54,128
mav 14,132,58
s_waitcnt lgkmcnt(2)
R 50,33984
v_mfma_f32_16x16x32_bf16 v[74:77], v[104:107], v[0:3], v[90:93]
v_cvt_pk_bf16_f32 v0, v26, v27
v_cvt_pk_bf16_f32 v1, v28, v29
mav 18,136,58
v_cvt_pk_bf16_f32 v2, v30, v31
v_cvt_pk_bf16_f32 v3, v32, v33
mva 22,58,140
mav 14,144,38
R 54,34048
v_max3_f32 v118, v70, v71, v74
mav 18,148,38
v_max3_f32 v119, v72, v73, v76
v_max_f32 v118, v118, v75
mva 22,38,152
s_waitcnt lgkmcnt(2)
v_max_f32 v119, v119, v77
v_max_f32 v120, v118, v119
mav 14,156,42
R 38,34112
v_max_f32 v121, v118, v119
mav 18,160,42
mva 22,42,164
v_permlane32_swap_b32 v120, v121
mav 14,168,46
R 42,34176
v_max_f32 v114, v120, v121
mav 18,172,46
v_max_f32 v115, v120, v121
mva 22,46,176
s_waitcnt lgkmcnt(2)
v_permlane16_swap_b32 v114, v115
mav 14,180,50
R 46,34240
v_max_f32 v120, v114, v115
mav 18,184,50
v_mul_f32 v120, s65, v120
v_fma_f32 v118, v118, s65, -v120
mva 22,50,188
v_fma_f32 v119, v119, s65, -v120
mav 14,192,54
R 50,34304
mav 18,196,54
v_exp_f32 v118, v118
mva 22,54,200
s_waitcnt lgkmcnt(2)
v_exp_f32 v119, v119
mav 14,204,38
R 54,34368
v_add_f32 v120, v118, v119
mav 18,208,38
v_add_f32 v121, v118, v119
mva 22,38,212
mav 14,216,42
R 38,34432
mav 18,220,42
v_permlane32_swap_b32 v120, v121
mva 22,42,224
s_waitcnt lgkmcnt(2)
v_add_f32 v114, v120, v121
v_add_f32 v115, v120, v121
mav 14,228,46
R 42,34496
mav 18,232,46
v_permlane16_swap_b32 v114, v115
mva 22,46,236
v_add_f32 v120, v114, v115
mav 14,240,50
R 46,34560
mav 18,244,50
v_rcp_f32 v120, v120
mva 22,50,248
s_waitcnt lgkmcnt(2)
v_mul_f32 v118, v118, v120
v_mul_f32 v119, v119, v120
mav 14,252,54
R 50,34624
v_cvt_pk_bf16_f32 v118, v118, v118
mvv 18,128,54
v_cvt_pk_bf16_f32 v119, v119, v119
v_and_b32 v114, v118, v94
mvv 22,54,132
v_and_b32 v115, v119, v95
v_and_b32 v116, v118, v96
)"
R"(
mvv 14,136,38
R 54,34688
v_and_b32 v117, v119, v97
mvv 18,140,38
mvv 22,38,144
s_waitcnt lgkmcnt(2)
v_mfma_f32_16x16x32_bf16 v[70:73], v[8:11], v[114:117], 0
v_mfma_f32_16x16x32_bf16 v[74:77], v[108:111], v[114:117], 0
mvv 14,148,42
R 38,34752
mvv 18,152,42
mvv 22,42,156
mvv 14,160,46
R 42,34816
mvv 18,164,46
v_cvt_pk_bf16_f32 v114, v70, v71
v_cvt_pk_bf16_f32 v115, v72, v73
mvv 22,46,168
s_waitcnt lgkmcnt(2)
v_cvt_pk_bf16_f32 v116, v74, v75
v_cvt_pk_bf16_f32 v117, v76, v77
mvv 14,172,50
buffer_store_dwordx4 v[114:117], v82, s[36:39], s49 offen sc1
R 46,34880
s_add_u32 s49, s49, 313344
mvv 18,176,50
mvv 22,50,180
mvv 14,184,54
R 50,34944
mvv 18,188,54
mvv 22,54,192
s_waitcnt lgkmcnt(2)
mvv 14,196,38
R 54,35008
mvv 18,200,38
mvv 22,38,204
mvv 14,208,42
R 38,0
mvv 18,212,42
ds_read_b64 v[122:123], v83 offset:0
mvv 22,42,216
s_waitcnt lgkmcnt(3)
ds_read_b128 v[70:73], v84 offset:0
mvv 14,220,46
R 42,64
mvv 18,224,46
ds_read_b128 v[74:77], v84 offset:16
mvv 22,46,228
mvv 14,232,50
R 46,128
mvv 18,236,50
mvv 22,50,240
s_waitcnt lgkmcnt(3)
mvv 14,244,54
R 50,192
mvv 18,248,54
mvv 22,54,252
v_mfma_f32_16x16x32_bf16 v[26:29], a[0:3], v[38:41], 0
s_waitcnt lgkmcnt(1)
R 54,256
v_mul_f32 v71, v71, v99
v_mfma_f32_16x16x32_bf16 v[30:33], a[4:7], v[38:41], 0
v_mul_f32 v73, v73, v99
v_mul_f32 v75, v75, v99
v_mfma_f32_16x16x32_bf16 v[34:37], v[38:41], a[8:11], 0
v_fma_f32 v22, v22, v70, v71
v_fma_f32 v23, v23, v72, v73
mav 26,12,42
R 58,320
v_fma_f32 v24, v24, v74, v75
mav 30,16,42
v_mul_f32 v77, v77, v99
v_fma_f32 v25, v25, v76, v77
mva 34,42,20
v_cvt_pk_bf16_f32 v8, v22, v23
v_cvt_pk_bf16_f32 v9, v24, v25
mav 26,24,46
R 38,384
v_mul_f32 v124, v123, v62
v_mfma_f32_16x16x32_bf16 v[70:73], v[104:107], v[0:3], v[86:89]
v_fma_f32 v14, v14, v122, v124
v_mul_f32 v125, v123, v63
mav 30,28,46
v_fma_f32 v15, v15, v122, v125
v_mul_f32 v124, v123, v64
mva 34,46,32
s_waitcnt lgkmcnt(2)
v_fma_f32 v16, v16, v122, v124
v_mul_f32 v125, v123, v65
mav 26,36,50
R 42,448
v_fma_f32 v17, v17, v122, v125
mav 30,40,50
v_mul_f32 v124, v123, v66
v_fma_f32 v18, v18, v122, v124
mva 34,50,44
v_mul_f32 v125, v123, v67
v_fma_f32 v19, v19, v122, v125
mav 26,48,54
R 46,512
v_mul_f32 v124, v123, v68
mav 30,52,54
v_fma_f32 v20, v20, v122, v124
v_mul_f32 v125, v123, v69
mva 34,54,56
s_waitcnt lgkmcnt(2)
v_fma_f32 v21, v21, v122, v125
v_add_u32 v78, 1, v78
mav 26,60,58
R 50,576
mav 30,64,58
mva 34,58,68
mav 26,72,38
R 54,640
mav 30,76,38
mva 34,38,80
s_waitcnt lgkmcnt(2)
mav 26,84,42
R 58,704
mav 30,88,42
mva 34,42,92
mav 26,96,46
R 38,768
mav 30,100,46
s_barrier
mva 34,46,104
s_waitcnt lgkmcnt(2)
ds_read_b128 v[100:103], v82 offset:0
mav 26,108,50
ds_read_b64 v[108:109], v82 offset:1024
mav 30,112,50
R 42,832
mva 34,50,116
mav 26,120,54
s_waitcnt vmcnt(0)
R 46,896
mav 30,124,54
buffer_store_dword v78, off, s[36:39], s52 sc1
mva 34,54,128
s_waitcnt lgkmcnt(3)
v_mfma_f32_16x16x32_bf16 v[74:77], v[100:103], v[0:3], v[90:93]
v_cvt_pk_bf16_f32 v0, v14, v15
v_cvt_pk_bf16_f32 v1, v16, v17
mav 26,132,58
R 50,960
v_cvt_pk_bf16_f32 v2, v18, v19
mav 30,136,58
v_cvt_pk_bf16_f32 v3, v20, v21
mva 34,58,140
v_max3_f32 v118, v70, v71, v74
v_max3_f32 v119, v72, v73, v76
mav 26,144,38
R 54,1024
v_max_f32 v118, v118, v75
mav 30,148,38
v_max_f32 v119, v119, v77
v_max_f32 v120, v118, v119
mva 34,38,152
s_waitcnt lgkmcnt(2)
v_max_f32 v121, v118, v119
mav 26,156,42
R 38,1088
mav 30,160,42
v_permlane32_swap_b32 v120, v121
mva 34,42,164
v_max_f32 v114, v120, v121
v_max_f32 v115, v120, v121
mav 26,168,46
R 42,1152
mav 30,172,46
v_permlane16_swap_b32 v114, v115
mva 34,46,176
s_waitcnt lgkmcnt(2)
v_max_f32 v120, v114, v115
v_mul_f32 v120, s65, v120
mav 26,180,50
R 46,1216
v_fma_f32 v118, v118, s65, -v120
mav 30,184,50
v_fma_f32 v119, v119, s65, -v120
mva 34,50,188
v_exp_f32 v118, v118
mav 26,192,54
R 50,1280
mav 30,196,54
v_exp_f32 v119, v119
mva 34,54,200
s_waitcnt lgkmcnt(2)
v_add_f32 v120, v118, v119
v_add_f32 v121, v118, v119
mav 26,204,38
R 54,1344
mav 30,208,38
v_permlane32_swap_b32 v120, v121
mva 34,38,212
v_add_f32 v114, v120, v121
v_add_f32 v115, v120, v121
mav 26,216,42
R 38,1408
mav 30,220,42
v_permlane16_swap_b32 v114, v115
mva 34,42,224
s_waitcnt lgkmcnt(2)
v_add_f32 v120, v114, v115
mav 26,228,46
R 42,1472
mav 30,232,46
v_rcp_f32 v120, v120
mva 34,46,236
v_mul_f32 v118, v118, v120
v_mul_f32 v119, v119, v120
mav 26,240,50
R 46,1536
v_cvt_pk_bf16_f32 v118, v118, v118
mav 30,244,50
v_cvt_pk_bf16_f32 v119, v119, v119
v_and_b32 v114, v118, v96
mva 34,50,248
s_waitcnt lgkmcnt(2)
v_and_b32 v115, v119, v97
v_and_b32 v116, v118, v94
mav 26,252,54
R 50,1600
v_and_b32 v117, v119, v95
mvv 30,128,54
mvv 34,54,132
v_mfma_f32_16x16x32_bf16 v[70:73], v[8:11], v[114:117], 0
v_mfma_f32_16x16x32_bf16 v[74:77], v[108:111], v[114:117], 0
mvv 26,136,38
R 54,1664
mvv 30,140,38
mvv 34,38,144
s_waitcnt lgkmcnt(2)
mvv 26,148,42
R 38,1728
v_cvt_pk_bf16_f32 v114, v70, v71
mvv 30,152,42
v_cvt_pk_bf16_f32 v115, v72, v73
v_cvt_pk_bf16_f32 v116, v74, v75
mvv 34,42,156
v_cvt_pk_bf16_f32 v117, v76, v77
mvv 26,160,46
R 42,1792
mvv 30,164,46
buffer_store_dwordx4 v[114:117], v82, s[36:39], s49 offen sc1
s_add_u32 s49, s49, 313344
mvv 34,46,168
s_waitcnt lgkmcnt(2)
mvv 26,172,50
R 46,1856
mvv 30,176,50
mvv 34,50,180
mvv 26,184,54
R 50,1920
mvv 30,188,54
s_waitcnt lgkmcnt(2)
mvv 34,54,192
s_mov_b32 s60, -13
.p2align 6
LQA1_1:
mvv 26,196,38
R 54,1984
v_xor_b32 v80, v81, v80
mvv 30,200,38
mvv 34,38,204
mvv 26,208,42
R 38,33024
mvv 30,212,42
ds_read_b64 v[122:123], v83 offset:128
mvv 34,42,216
s_waitcnt lgkmcnt(3)
ds_read_b128 v[70:73], v84 offset:128
mvv 26,220,46
R 42,33088
mvv 30,224,46
ds_read_b128 v[74:77], v84 offset:144
mvv 34,46,228
mvv 26,232,50
R 46,33152
mvv 30,236,50
mvv 34,50,240
s_waitcnt lgkmcnt(3)
mvv 26,244,54
R 50,33216
mvv 30,248,54
mvv 34,54,252
v_mfma_f32_16x16x32_bf16 v[14:17], a[0:3], v[38:41], 0
s_waitcnt lgkmcnt(1)
R 54,33280
v_mul_f32 v71, v71, v99
v_mfma_f32_16x16x32_bf16 v[18:21], a[4:7], v[38:41], 0
v_mul_f32 v73, v73, v99
v_mul_f32 v75, v75, v99
v_mfma_f32_16x16x32_bf16 v[22:25], v[38:41], a[8:11], 0
v_fma_f32 v34, v34, v70, v71
v_fma_f32 v35, v35, v72, v73
mav 14,12,42
R 58,33344
v_fma_f32 v36, v36, v74, v75
mav 18,16,42
v_mul_f32 v77, v77, v99
v_fma_f32 v37, v37, v76, v77
mva 22,42,20
v_cvt_pk_bf16_f32 v10, v34, v35
v_cvt_pk_bf16_f32 v11, v36, v37
mav 14,24,46
R 38,33408
v_mul_f32 v124, v123, v62
v_mfma_f32_16x16x32_bf16 v[70:73], v[100:103], v[0:3], v[86:89]
v_fma_f32 v26, v26, v122, v124
v_mul_f32 v125, v123, v63
mav 18,28,46
v_fma_f32 v27, v27, v122, v125
v_mul_f32 v124, v123, v64
mva 22,46,32
s_waitcnt lgkmcnt(2)
v_fma_f32 v28, v28, v122, v124
v_mul_f32 v125, v123, v65
mav 14,36,50
R 42,33472
v_fma_f32 v29, v29, v122, v125
mav 18,40,50
v_mul_f32 v124, v123, v66
v_fma_f32 v30, v30, v122, v124
mva 22,50,44
v_mul_f32 v125, v123, v67
v_fma_f32 v31, v31, v122, v125
mav 14,48,54
R 46,33536
v_mul_f32 v124, v123, v68
mav 18,52,54
v_fma_f32 v32, v32, v122, v124
v_mul_f32 v125, v123, v69
mva 22,54,56
s_waitcnt lgkmcnt(2)
v_fma_f32 v33, v33, v122, v125
v_add_u32 v78, 1, v78
mav 14,60,58
R 50,33600
mav 18,64,58
mva 22,58,68
mav 14,72,38
R 54,33664
mav 18,76,38
mva 22,38,80
s_waitcnt lgkmcnt(2)
mav 14,84,42
R 58,33728
mav 18,88,42
mva 22,42,92
mav 14,96,46
R 38,33792
mav 18,100,46
s_barrier
mva 22,46,104
s_waitcnt lgkmcnt(2)
ds_read_b128 v[104:107], v82 offset:4096
mav 14,108,50
ds_read_b64 v[110:111], v82 offset:5120
mav 18,112,50
R 42,33856
mva 22,50,116
mav 14,120,54
s_waitcnt vmcnt(0)
R 46,33920
mav 18,124,54
buffer_store_dword v78, off, s[36:39], s52 sc1
mva 22,54,128
s_waitcnt lgkmcnt(3)
v_mfma_f32_16x16x32_bf16 v[74:77], v[104:107], v[0:3], v[90:93]
v_cvt_pk_bf16_f32 v0, v26, v27
v_cvt_pk_bf16_f32 v1, v28, v29
mav 14,132,58
R 50,33984
v_cvt_pk_bf16_f32 v2, v30, v31
mav 18,136,58
v_cvt_pk_bf16_f32 v3, v32, v33
mva 22,58,140
v_max3_f32 v118, v70, v71, v74
v_max3_f32 v119, v72, v73, v76
mav 14,144,38
R 54,34048
v_max_f32 v118, v118, v75
mav 18,148,38
v_max_f32 v119, v119, v77
v_max_f32 v120, v118, v119
mva 22,38,152
s_waitcnt lgkmcnt(2)
v_max_f32 v121, v118, v119
mav 14,156,42
R 38,34112
mav 18,160,42
v_permlane32_swap_b32 v120, v121
mva 22,42,164
v_max_f32 v114, v120, v121
v_max_f32 v115, v120, v121
mav 14,168,46
R 42,34176
mav 18,172,46
v_permlane16_swap_b32 v114, v115
mva 22,46,176
s_waitcnt lgkmcnt(2)
v_max_f32 v120, v114, v115
v_mul_f32 v120, s65, v120
mav 14,180,50
R 46,34240
v_fma_f32 v118, v118, s65, -v120
mav 18,184,50
v_fma_f32 v119, v119, s65, -v120
mva 22,50,188
v_exp_f32 v118, v118
mav 14,192,54
R 50,34304
mav 18,196,54
v_exp_f32 v119, v119
mva 22,54,200
s_waitcnt lgkmcnt(2)
v_add_f32 v120, v118, v119
v_add_f32 v121, v118, v119
mav 14,204,38
R 54,34368
mav 18,208,38
v_permlane32_swap_b32 v120, v121
mva 22,38,212
v_add_f32 v114, v120, v121
v_add_f32 v115, v120, v121
mav 14,216,42
R 38,34432
mav 18,220,42
v_permlane16_swap_b32 v114, v115
mva 22,42,224
s_waitcnt lgkmcnt(2)
v_add_f32 v120, v114, v115
mav 14,228,46
R 42,34496
mav 18,232,46
v_rcp_f32 v120, v120
mva 22,46,236
v_mul_f32 v118, v118, v120
v_mul_f32 v119, v119, v120
mav 14,240,50
R 46,34560
v_cvt_pk_bf16_f32 v118, v118, v118
mav 18,244,50
v_cvt_pk_bf16_f32 v119, v119, v119
v_and_b32 v114, v118, v94
mva 22,50,248
s_waitcnt lgkmcnt(2)
v_and_b32 v115, v119, v95
v_and_b32 v116, v118, v96
mav 14,252,54
R 50,34624
v_and_b32 v117, v119, v97
mvv 18,128,54
mvv 22,54,132
v_mfma_f32_16x16x32_bf16 v[70:73], v[8:11], v[114:117], 0
LsQ9:
s_cbranch_execz LsQ13
v_mfma_f32_16x16x32_bf16 v[74:77], v[108:111], v[114:117], 0
mvv 14,136,38
R 54,34688
mvv 18,140,38
mvv 22,38,144
s_waitcnt lgkmcnt(2)
mvv 14,148,42
R 38,34752
v_cvt_pk_bf16_f32 v114, v70, v71
mvv 18,152,42
v_cvt_pk_bf16_f32 v115, v72, v73
v_cvt_pk_bf16_f32 v116, v74, v75
mvv 22,42,156
v_cvt_pk_bf16_f32 v117, v76, v77
mvv 14,160,46
R 42,34816
mvv 18,164,46
buffer_store_dwordx4 v[114:117], v82, s[36:39], s49 offen sc1
s_add_u32 s49, s49, 313344
mvv 22,46,168
s_waitcnt lgkmcnt(2)
mvv 14,172,50
R 46,34880
mvv 18,176,50
mvv 22,50,180
mvv 14,184,54
R 50,34944
mvv 18,188,54
mvv 22,54,192
s_waitcnt lgkmcnt(2)
mvv 14,196,38
R 54,35008
mvv 18,200,38
mvv 22,38,204
mvv 14,208,42
R 38,0
mvv 18,212,42
ds_read_b64 v[122:123], v83 offset:0
mvv 22,42,216
s_waitcnt lgkmcnt(3)
ds_read_b128 v[70:73], v84 offset:0
mvv 14,220,46
R 42,64
mvv 18,224,46
ds_read_b128 v[74:77], v84 offset:16
mvv 22,46,228
mvv 14,232,50
R 46,128
mvv 18,236,50
mvv 22,50,240
s_waitcnt lgkmcnt(3)
mvv 14,244,54
R 50,192
mvv 18,248,54
mvv 22,54,252
v_mfma_f32_16x16x32_bf16 v[26:29], a[0:3], v[38:41], 0
s_waitcnt lgkmcnt(1)
R 54,256
v_mul_f32 v71, v71, v99
v_mfma_f32_16x16x32_bf16 v[30:33], a[4:7], v[38:41], 0
v_mul_f32 v73, v73, v99
v_mul_f32 v75, v75, v99
v_mfma_f32_16x16x32_bf16 v[34:37], v[38:41], a[8:11], 0
v_fma_f32 v22, v22, v70, v71
v_fma_f32 v23, v23, v72, v73
mav 26,12,42
R 58,320
v_fma_f32 v24, v24, v74, v75
mav 30,16,42
v_mul_f32 v77, v77, v99
v_fma_f32 v25, v25, v76, v77
mva 34,42,20
v_cvt_pk_bf16_f32 v8, v22, v23
v_cvt_pk_bf16_f32 v9, v24, v25
mav 26,24,46
R 38,384
v_mul_f32 v124, v123, v62
v_mfma_f32_16x16x32_bf16 v[70:73], v[104:107], v[0:3], v[86:89]
v_fma_f32 v14, v14, v122, v124
v_mul_f32 v125, v123, v63
mav 30,28,46
v_fma_f32 v15, v15, v122, v125
v_mul_f32 v124, v123, v64
mva 34,46,32
s_waitcnt lgkmcnt(2)
v_fma_f32 v16, v16, v122, v124
v_mul_f32 v125, v123, v65
mav 26,36,50
R 42,448
v_fma_f32 v17, v17, v122, v125
mav 30,40,50
v_mul_f32 v124, v123, v66
v_fma_f32 v18, v18, v122, v124
mva 34,50,44
v_mul_f32 v125, v123, v67
v_fma_f32 v19, v19, v122, v125
mav 26,48,54
R 46,512
v_mul_f32 v124, v123, v68
mav 30,52,54
v_fma_f32 v20, v20, v122, v124
v_mul_f32 v125, v123, v69
mva 34,54,56
s_waitcnt lgkmcnt(2)
v_fma_f32 v21, v21, v122, v125
v_add_u32 v78, 1, v78
mav 26,60,58
R 50,576
mav 30,64,58
mva 34,58,68
mav 26,72,38
R 54,640
mav 30,76,38
mva 34,38,80
s_waitcnt lgkmcnt(2)
mav 26,84,42
R 58,704
mav 30,88,42
mva 34,42,92
mav 26,96,46
R 38,768
mav 30,100,46
s_barrier
mva 34,46,104
s_waitcnt lgkmcnt(2)
ds_read_b128 v[100:103], v82 offset:0
mav 26,108,50
ds_read_b64 v[108:109], v82 offset:1024
mav 30,112,50
R 42,832
mva 34,50,116
mav 26,120,54
s_waitcnt vmcnt(0)
R 46,896
mav 30,124,54
buffer_store_dword v78, off, s[36:39], s52 sc1
mva 34,54,128
s_waitcnt lgkmcnt(3)
v_mfma_f32_16x16x32_bf16 v[74:77], v[100:103], v[0:3], v[90:93]
v_cvt_pk_bf16_f32 v0, v14, v15
v_cvt_pk_bf16_f32 v1, v16, v17
mav 26,132,58
R 50,960
v_cvt_pk_bf16_f32 v2, v18, v19
mav 30,136,58
v_cvt_pk_bf16_f32 v3, v20, v21
mva 34,58,140
v_max3_f32 v118, v70, v71, v74
v_max3_f32 v119, v72, v73, v76
mav 26,144,38
R 54,1024
v_max_f32 v118, v118, v75
mav 30,148,38
v_max_f32 v119, v119, v77
v_max_f32 v120, v118, v119
mva 34,38,152
s_waitcnt lgkmcnt(2)
v_max_f32 v121, v118, v119
mav 26,156,42
R 38,1088
mav 30,160,42
v_permlane32_swap_b32 v120, v121
mva 34,42,164
v_max_f32 v114, v120, v121
v_max_f32 v115, v120, v121
mav 26,168,46
R 42,1152
mav 30,172,46
v_permlane16_swap_b32 v114, v115
mva 34,46,176
s_waitcnt lgkmcnt(2)
v_max_f32 v120, v114, v115
v_mul_f32 v120, s65, v120
mav 26,180,50
R 46,1216
v_fma_f32 v118, v118, s65, -v120
mav 30,184,50
v_fma_f32 v119, v119, s65, -v120
mva 34,50,188
v_exp_f32 v118, v118
mav 26,192,54
R 50,1280
mav 30,196,54
v_exp_f32 v119, v119
mva 34,54,200
s_waitcnt lgkmcnt(2)
v_add_f32 v120, v118, v119
v_add_f32 v121, v118, v119
mav 26,204,38
R 54,1344
mav 30,208,38
v_permlane32_swap_b32 v120, v121
mva 34,38,212
v_add_f32 v114, v120, v121
v_add_f32 v115, v120, v121
mav 26,216,42
R 38,1408
mav 30,220,42
v_permlane16_swap_b32 v114, v115
mva 34,42,224
s_waitcnt lgkmcnt(2)
v_add_f32 v120, v114, v115
mav 26,228,46
R 42,1472
mav 30,232,46
v_rcp_f32 v120, v120
mva 34,46,236
v_mul_f32 v118, v118, v120
v_mul_f32 v119, v119, v120
mav 26,240,50
R 46,1536
v_cvt_pk_bf16_f32 v118, v118, v118
mav 30,244,50
v_cvt_pk_bf16_f32 v119, v119, v119
v_and_b32 v114, v118, v96
mva 34,50,248
s_waitcnt lgkmcnt(2)
v_and_b32 v115, v119, v97
v_and_b32 v116, v118, v94
mav 26,252,54
R 50,1600
v_and_b32 v117, v119, v95
mvv 30,128,54
mvv 34,54,132
v_mfma_f32_16x16x32_bf16 v[70:73], v[8:11], v[114:117], 0
v_mfma_f32_16x16x32_bf16 v[74:77], v[108:111], v[114:117], 0
mvv 26,136,38
R 54,1664
mvv 30,140,38
mvv 34,38,144
s_waitcnt lgkmcnt(2)
mvv 26,148,42
R 38,1728
v_cvt_pk_bf16_f32 v114, v70, v71
mvv 30,152,42
v_cvt_pk_bf16_f32 v115, v72, v73
v_cvt_pk_bf16_f32 v116, v74, v75
mvv 34,42,156
v_cvt_pk_bf16_f32 v117, v76, v77
mvv 26,160,46
R 42,1792
mvv 30,164,46
buffer_store_dwordx4 v[114:117], v82, s[36:39], s49 offen sc1
s_add_u32 s49, s49, 313344
mvv 34,46,168
s_waitcnt lgkmcnt(2)
mvv 26,172,50
R 46,1856
mvv 30,176,50
mvv 34,50,180
mvv 26,184,54
R 50,1920
mvv 30,188,54
s_waitcnt lgkmcnt(2)
s_add_u32 s60, s60, 1
mvv 34,54,192
s_cbranch_scc0 LQA1_1
mvv 26,196,38
R 54,1984
v_xor_b32 v80, v81, v80
mvv 30,200,38
mvv 34,38,204
mvv 26,208,42
R 38,33024
mvv 30,212,42
ds_read_b64 v[122:123], v83 offset:128
mvv 34,42,216
s_waitcnt lgkmcnt(3)
ds_read_b128 v[70:73], v84 offset:128
mvv 26,220,46
R 42,33088
mvv 30,224,46
ds_read_b128 v[74:77], v84 offset:144
mvv 34,46,228
mvv 26,232,50
R 46,33152
mvv 30,236,50
mvv 34,50,240
s_waitcnt lgkmcnt(3)
mvv 26,244,54
R 50,33216
mvv 30,248,54
mvv 34,54,252
v_mfma_f32_16x16x32_bf16 v[14:17], a[0:3], v[38:41], 0
s_waitcnt lgkmcnt(1)
R 54,33280
v_mul_f32 v71, v71, v99
v_mfma_f32_16x16x32_bf16 v[18:21], a[4:7], v[38:41], 0
v_mul_f32 v73, v73, v99
v_mul_f32 v75, v75, v99
v_mfma_f32_16x16x32_bf16 v[22:25], v[38:41], a[8:11], 0
v_fma_f32 v34, v34, v70, v71
v_fma_f32 v35, v35, v72, v73
mav 14,12,42
R 58,33344
v_fma_f32 v36, v36, v74, v75
mav 18,16,42
v_mul_f32 v77, v77, v99
v_fma_f32 v37, v37, v76, v77
mva 22,42,20
v_cvt_pk_bf16_f32 v10, v34, v35
v_cvt_pk_bf16_f32 v11, v36, v37
mav 14,24,46
R 38,33408
v_mul_f32 v124, v123, v62
v_mfma_f32_16x16x32_bf16 v[70:73], v[100:103], v[0:3], v[86:89]
v_fma_f32 v26, v26, v122, v124
v_mul_f32 v125, v123, v63
mav 18,28,46
v_fma_f32 v27, v27, v122, v125
v_mul_f32 v124, v123, v64
mva 22,46,32
s_waitcnt lgkmcnt(2)
v_fma_f32 v28, v28, v122, v124
v_mul_f32 v125, v123, v65
mav 14,36,50
R 42,33472
v_fma_f32 v29, v29, v122, v125
mav 18,40,50
v_mul_f32 v124, v123, v66
v_fma_f32 v30, v30, v122, v124
mva 22,50,44
v_mul_f32 v125, v123, v67
v_fma_f32 v31, v31, v122, v125
mav 14,48,54
R 46,33536
v_mul_f32 v124, v123, v68
mav 18,52,54
v_fma_f32 v32, v32, v122, v124
v_mul_f32 v125, v123, v69
mva 22,54,56
s_waitcnt lgkmcnt(2)
v_fma_f32 v33, v33, v122, v125
v_add_u32 v78, 1, v78
mav 14,60,58
R 50,33600
mav 18,64,58
mva 22,58,68
mav 14,72,38
R 54,33664
mav 18,76,38
mva 22,38,80
s_waitcnt lgkmcnt(2)
mav 14,84,42
R 58,33728
mav 18,88,42
mva 22,42,92
mav 14,96,46
R 38,33792
mav 18,100,46
s_barrier
mva 22,46,104
s_waitcnt lgkmcnt(2)
ds_read_b128 v[104:107], v82 offset:4096
mav 14,108,50
ds_read_b64 v[110:111], v82 offset:5120
mav 18,112,50
R 42,33856
mva 22,50,116
mav 14,120,54
s_waitcnt vmcnt(0)
R 46,33920
mav 18,124,54
buffer_store_dword v78, off, s[36:39], s52 sc1
mva 22,54,128
s_waitcnt lgkmcnt(3)
v_mfma_f32_16x16x32_bf16 v[74:77], v[104:107], v[0:3], v[90:93]
v_cvt_pk_bf16_f32 v0, v26, v27
v_cvt_pk_bf16_f32 v1, v28, v29
mav 14,132,58
R 50,33984
v_cvt_pk_bf16_f32 v2, v30, v31
mav 18,136,58
v_cvt_pk_bf16_f32 v3, v32, v33
mva 22,58,140
v_max3_f32 v118, v70, v71, v74
v_max3_f32 v119, v72, v73, v76
mav 14,144,38
R 54,34048
v_max_f32 v118, v118, v75
mav 18,148,38
v_max_f32 v119, v119, v77
v_max_f32 v120, v118, v119
mva 22,38,152
s_waitcnt lgkmcnt(2)
v_max_f32 v121, v118, v119
mav 14,156,42
R 38,34112
mav 18,160,42
v_permlane32_swap_b32 v120, v121
mva 22,42,164
v_max_f32 v114, v120, v121
v_max_f32 v115, v120, v121
mav 14,168,46
R 42,34176
mav 18,172,46
v_permlane16_swap_b32 v114, v115
mva 22,46,176
s_waitcnt lgkmcnt(2)
v_max_f32 v120, v114, v115
v_mul_f32 v120, s65, v120
mav 14,180,50
R 46,34240
v_fma_f32 v118, v118, s65, -v120
mav 18,184,50
v_fma_f32 v119, v119, s65, -v120
mva 22,50,188
v_exp_f32 v118, v118
mav 14,192,54
R 50,34304
mav 18,196,54
v_exp_f32 v119, v119
mva 22,54,200
s_waitcnt lgkmcnt(2)
v_add_f32 v120, v118, v119
v_add_f32 v121, v118, v119
mav 14,204,38
R 54,34368
mav 18,208,38
v_permlane32_swap_b32 v120, v121
mva 22,38,212
v_add_f32 v114, v120, v121
v_add_f32 v115, v120, v121
mav 14,216,42
R 38,34432
mav 18,220,42
v_permlane16_swap_b32 v114, v115
mva 22,42,224
s_waitcnt lgkmcnt(2)
v_add_f32 v120, v114, v115
mav 14,228,46
R 42,34496
mav 18,232,46
v_rcp_f32 v120, v120
mva 22,46,236
v_mul_f32 v118, v118, v120
v_mul_f32 v119, v119, v120
mav 14,240,50
R 46,34560
v_cvt_pk_bf16_f32 v118, v118, v118
mav 18,244,50
v_cvt_pk_bf16_f32 v119, v119, v119
v_and_b32 v114, v118, v94
mva 22,50,248
s_waitcnt lgkmcnt(2)
v_and_b32 v115, v119, v95
v_and_b32 v116, v118, v96
mav 14,252,54
R 50,34624
v_and_b32 v117, v119, v97
mvv 18,128,54
mvv 22,54,132
v_mfma_f32_16x16x32_bf16 v[70:73], v[8:11], v[114:117], 0
v_mfma_f32_16x16x32_bf16 v[74:77], v[108:111], v[114:117], 0
mvv 14,136,38
R 54,34688
mvv 18,140,38
mvv 22,38,144
s_waitcnt lgkmcnt(2)
mvv 14,148,42
R 38,34752
v_cvt_pk_bf16_f32 v114, v70, v71
mvv 18,152,42
v_cvt_pk_bf16_f32 v115, v72, v73
v_cvt_pk_bf16_f32 v116, v74, v75
mvv 22,42,156
v_cvt_pk_bf16_f32 v117, v76, v77
mvv 14,160,46
R 42,34816
mvv 18,164,46
buffer_store_dwordx4 v[114:117], v82, s[36:39], s49 offen sc1
s_add_u32 s49, s49, 313344
mvv 22,46,168
s_waitcnt lgkmcnt(2)
mvv 14,172,50
R 46,34880
mvv 18,176,50
mvv 22,50,180
mvv 14,184,54
R 50,34944
mvv 18,188,54
mvv 22,54,192
s_waitcnt lgkmcnt(2)
mvv 14,196,38
R 54,35008
mvv 18,200,38
mvv 22,38,204
mvv 14,208,42
R 38,0
mvv 18,212,42
ds_read_b64 v[122:123], v83 offset:0
mvv 22,42,216
s_waitcnt lgkmcnt(3)
ds_read_b128 v[70:73], v84 offset:0
mvv 14,220,46
R 42,64
mvv 18,224,46
ds_read_b128 v[74:77], v84 offset:16
mvv 22,46,228
mvv 14,232,50
R 46,128
mvv 18,236,50
mvv 22,50,240
s_waitcnt lgkmcnt(3)
mvv 14,244,54
R 50,192
mvv 18,248,54
mvv 22,54,252
v_mfma_f32_16x16x32_bf16 v[26:29], a[0:3], v[38:41], 0
s_waitcnt lgkmcnt(1)
R 54,256
v_mul_f32 v71, v71, v99
v_mfma_f32_16x16x32_bf16 v[30:33], a[4:7], v[38:41], 0
v_mul_f32 v73, v73, v99
v_mul_f32 v75, v75, v99
v_mfma_f32_16x16x32_bf16 v[34:37], v[38:41], a[8:11], 0
v_fma_f32 v22, v22, v70, v71
v_fma_f32 v23, v23, v72, v73
mav 26,12,42
R 58,320
v_fma_f32 v24, v24, v74, v75
mav 30,16,42
v_mul_f32 v77, v77, v99
v_fma_f32 v25, v25, v76, v77
mva 34,42,20
v_cvt_pk_bf16_f32 v8, v22, v23
v_cvt_pk_bf16_f32 v9, v24, v25
mav 26,24,46
R 38,384
v_mul_f32 v124, v123, v62
v_mfma_f32_16x16x32_bf16 v[70:73], v[104:107], v[0:3], v[86:89]
v_fma_f32 v14, v14, v122, v124
v_mul_f32 v125, v123, v63
mav 30,28,46
v_fma_f32 v15, v15, v122, v125
v_mul_f32 v124, v123, v64
mva 34,46,32
s_waitcnt lgkmcnt(2)
v_fma_f32 v16, v16, v122, v124
v_mul_f32 v125, v123, v65
mav 26,36,50
R 42,448
v_fma_f32 v17, v17, v122, v125
mav 30,40,50
v_mul_f32 v124, v123, v66
v_fma_f32 v18, v18, v122, v124
mva 34,50,44
buffer_load_dwordx4 v[4:7], v82, s[36:39], s55 offen sc1
mav 26,48,54
R 46,512
v_mul_f32 v125, v123, v67
mav 30,52,54
buffer_load_dwordx2 v[12:13], v98, s[36:39], s55 offen offset:1024 sc1
mva 34,54,56
s_waitcnt lgkmcnt(2)
v_fma_f32 v19, v19, v122, v125
v_mul_f32 v124, v123, v68
mav 26,60,58
buffer_load_dwordx2 v[112:113], v98, s[36:39], s55 offen offset:1536 sc1
mav 30,64,58
R 50,576
v_fma_f32 v20, v20, v122, v124
mva 34,58,68
v_mul_f32 v125, v123, v69
v_fma_f32 v21, v21, v122, v125
mav 26,72,38
R 54,640
v_add_u32 v78, 1, v78
mav 30,76,38
mva 34,38,80
s_waitcnt lgkmcnt(2)
mav 26,84,42
R 58,704
mav 30,88,42
mva 34,42,92
mav 26,96,46
R 38,768
mav 30,100,46
s_barrier
mva 34,46,104
s_waitcnt lgkmcnt(2)
ds_read_b128 v[100:103], v82 offset:0
LsQ10:
s_cbranch_execz LsQ14
mav 26,108,50
ds_read_b64 v[108:109], v82 offset:1024
mav 30,112,50
R 42,832
mva 34,50,116
mav 26,120,54
s_waitcnt vmcnt(3)
R 46,896
mav 30,124,54
buffer_store_dword v78, off, s[36:39], s52 sc1
mva 34,54,128
s_waitcnt lgkmcnt(3)
v_mfma_f32_16x16x32_bf16 v[74:77], v[100:103], v[0:3], v[90:93]
v_cvt_pk_bf16_f32 v0, v14, v15
v_cvt_pk_bf16_f32 v1, v16, v17
mav 26,132,58
R 50,960
v_cvt_pk_bf16_f32 v2, v18, v19
mav 30,136,58
v_cvt_pk_bf16_f32 v3, v20, v21
mva 34,58,140
v_max3_f32 v118, v70, v71, v74
v_max3_f32 v119, v72, v73, v76
mav 26,144,38
R 54,1024
v_max_f32 v118, v118, v75
mav 30,148,38
v_max_f32 v119, v119, v77
v_max_f32 v120, v118, v119
mva 34,38,152
s_waitcnt lgkmcnt(2)
v_max_f32 v121, v118, v119
mav 26,156,42
R 38,1088
mav 30,160,42
v_permlane32_swap_b32 v120, v121
mva 34,42,164
v_max_f32 v114, v120, v121
v_max_f32 v115, v120, v121
mav 26,168,46
R 42,1152
mav 30,172,46
v_permlane16_swap_b32 v114, v115
mva 34,46,176
s_waitcnt lgkmcnt(2)
v_max_f32 v120, v114, v115
v_mul_f32 v120, s65, v120
mav 26,180,50
R 46,1216
v_fma_f32 v118, v118, s65, -v120
mav 30,184,50
v_fma_f32 v119, v119, s65, -v120
mva 34,50,188
v_exp_f32 v118, v118
mav 26,192,54
R 50,1280
mav 30,196,54
v_exp_f32 v119, v119
mva 34,54,200
s_waitcnt lgkmcnt(2)
v_add_f32 v120, v118, v119
v_add_f32 v121, v118, v119
mav 26,204,38
R 54,1344
mav 30,208,38
v_permlane32_swap_b32 v120, v121
mva 34,38,212
v_add_f32 v114, v120, v121
v_add_f32 v115, v120, v121
mav 26,216,42
R 38,1408
mav 30,220,42
v_permlane16_swap_b32 v114, v115
mva 34,42,224
s_waitcnt lgkmcnt(2)
v_add_f32 v120, v114, v115
mav 26,228,46
R 42,1472
mav 30,232,46
v_rcp_f32 v120, v120
mva 34,46,236
v_mul_f32 v118, v118, v120
v_mul_f32 v119, v119, v120
mav 26,240,50
R 46,1536
v_cvt_pk_bf16_f32 v118, v118, v118
mav 30,244,50
v_cvt_pk_bf16_f32 v119, v119, v119
v_and_b32 v114, v118, v96
mva 34,50,248
s_waitcnt lgkmcnt(2)
v_and_b32 v115, v119, v97
v_and_b32 v116, v118, v94
mav 26,252,54
R 50,1600
v_and_b32 v117, v119, v95
mvv 30,128,54
mvv 34,54,132
v_mfma_f32_16x16x32_bf16 v[70:73], v[8:11], v[114:117], 0
v_mfma_f32_16x16x32_bf16 v[74:77], v[108:111], v[114:117], 0
mvv 26,136,38
R 54,1664
mvv 30,140,38
mvv 34,38,144
s_waitcnt lgkmcnt(2)
mvv 26,148,42
R 38,1728
v_cvt_pk_bf16_f32 v114, v70, v71
mvv 30,152,42
v_cvt_pk_bf16_f32 v115, v72, v73
v_cvt_pk_bf16_f32 v116, v74, v75
mvv 34,42,156
v_cvt_pk_bf16_f32 v117, v76, v77
mvv 26,160,46
R 42,1792
mvv 30,164,46
buffer_store_dwordx4 v[114:117], v82, s[36:39], s49 offen sc1
s_add_u32 s49, s49, 313344
mvv 34,46,168
s_waitcnt lgkmcnt(2)
mvv 26,172,50
R 46,1856
mvv 30,176,50
mvv 34,50,180
mvv 26,184,54
R 50,1920
mvv 30,188,54
s_waitcnt lgkmcnt(2)
mvv 34,54,192
mvv 26,196,38
R 54,1984
v_xor_b32 v80, v81, v80
mvv 30,200,38
mvv 34,38,204
mvv 26,208,42
R 38,33024
mvv 30,212,42
ds_read_b64 v[122:123], v83 offset:128
mvv 34,42,216
s_waitcnt lgkmcnt(3)
ds_read_b128 v[70:73], v84 offset:128
mvv 26,220,46
R 42,33088
mvv 30,224,46
ds_read_b128 v[74:77], v84 offset:144
mvv 34,46,228
mvv 26,232,50
R 46,33152
mvv 30,236,50
mvv 34,50,240
s_waitcnt lgkmcnt(3)
mvv 26,244,54
R 50,33216
mvv 30,248,54
mvv 34,54,252
v_mfma_f32_16x16x32_bf16 v[14:17], a[0:3], v[38:41], 0
s_waitcnt lgkmcnt(1)
R 54,33280
v_mul_f32 v71, v71, v99
v_mfma_f32_16x16x32_bf16 v[18:21], a[4:7], v[38:41], 0
v_mul_f32 v73, v73, v99
v_mul_f32 v75, v75, v99
v_mfma_f32_16x16x32_bf16 v[22:25], v[38:41], a[8:11], 0
v_fma_f32 v34, v34, v70, v71
v_fma_f32 v35, v35, v72, v73
mav 14,12,42
R 58,33344
v_fma_f32 v36, v36, v74, v75
mav 18,16,42
v_mul_f32 v77, v77, v99
v_fma_f32 v37, v37, v76, v77
mva 22,42,20
v_cvt_pk_bf16_f32 v10, v34, v35
v_cvt_pk_bf16_f32 v11, v36, v37
mav 14,24,46
R 38,33408
v_mul_f32 v124, v123, v62
v_mfma_f32_16x16x32_bf16 v[70:73], v[100:103], v[0:3], v[86:89]
v_fma_f32 v26, v26, v122, v124
v_mul_f32 v125, v123, v63
mav 18,28,46
v_fma_f32 v27, v27, v122, v125
v_mul_f32 v124, v123, v64
mva 22,46,32
s_waitcnt lgkmcnt(2)
v_fma_f32 v28, v28, v122, v124
v_mul_f32 v125, v123, v65
mav 14,36,50
R 42,33472
v_fma_f32 v29, v29, v122, v125
mav 18,40,50
v_mul_f32 v124, v123, v66
v_fma_f32 v30, v30, v122, v124
mva 22,50,44
v_mul_f32 v125, v123, v67
v_fma_f32 v31, v31, v122, v125
mav 14,48,54
R 46,33536
v_mul_f32 v124, v123, v68
mav 18,52,54
v_fma_f32 v32, v32, v122, v124
v_mul_f32 v125, v123, v69
mva 22,54,56
s_waitcnt lgkmcnt(2)
v_fma_f32 v33, v33, v122, v125
v_add_u32 v78, 1, v78
mav 14,60,58
R 50,33600
mav 18,64,58
mva 22,58,68
mav 14,72,38
R 54,33664
mav 18,76,38
mva 22,38,80
s_waitcnt lgkmcnt(2)
mav 14,84,42
R 58,33728
mav 18,88,42
mva 22,42,92
mav 14,96,46
R 38,33792
mav 18,100,46
s_barrier
mva 22,46,104
s_waitcnt lgkmcnt(2)
ds_read_b128 v[104:107], v82 offset:4096
mav 14,108,50
ds_read_b64 v[110:111], v82 offset:5120
mav 18,112,50
R 42,33856
mva 22,50,116
mav 14,120,54
s_waitcnt vmcnt(0)
R 46,33920
mav 18,124,54
buffer_store_dword v78, off, s[36:39], s52 sc1
mva 22,54,128
s_waitcnt lgkmcnt(3)
v_mfma_f32_16x16x32_bf16 v[74:77], v[104:107], v[0:3], v[90:93]
v_cvt_pk_bf16_f32 v0, v26, v27
v_cvt_pk_bf16_f32 v1, v28, v29
mav 14,132,58
R 50,33984
v_cvt_pk_bf16_f32 v2, v30, v31
mav 18,136,58
v_cvt_pk_bf16_f32 v3, v32, v33
mva 22,58,140
v_max3_f32 v118, v70, v71, v74
v_max3_f32 v119, v72, v73, v76
mav 14,144,38
R 54,34048
v_max_f32 v118, v118, v75
mav 18,148,38
v_max_f32 v119, v119, v77
v_max_f32 v120, v118, v119
mva 22,38,152
s_waitcnt lgkmcnt(2)
v_max_f32 v121, v118, v119
mav 14,156,42
R 38,34112
mav 18,160,42
v_permlane32_swap_b32 v120, v121
mva 22,42,164
v_max_f32 v114, v120, v121
v_max_f32 v115, v120, v121
mav 14,168,46
R 42,34176
mav 18,172,46
v_permlane16_swap_b32 v114, v115
mva 22,46,176
s_waitcnt lgkmcnt(2)
v_max_f32 v120, v114, v115
v_mul_f32 v120, s65, v120
mav 14,180,50
R 46,34240
v_fma_f32 v118, v118, s65, -v120
mav 18,184,50
v_fma_f32 v119, v119, s65, -v120
mva 22,50,188
v_exp_f32 v118, v118
mav 14,192,54
R 50,34304
mav 18,196,54
v_exp_f32 v119, v119
mva 22,54,200
s_waitcnt lgkmcnt(2)
v_add_f32 v120, v118, v119
v_add_f32 v121, v118, v119
mav 14,204,38
R 54,34368
mav 18,208,38
v_permlane32_swap_b32 v120, v121
mva 22,38,212
v_add_f32 v114, v120, v121
v_add_f32 v115, v120, v121
mav 14,216,42
R 38,34432
mav 18,220,42
v_permlane16_swap_b32 v114, v115
mva 22,42,224
s_waitcnt lgkmcnt(2)
v_add_f32 v120, v114, v115
mav 14,228,46
R 42,34496
mav 18,232,46
v_rcp_f32 v120, v120
mva 22,46,236
v_mul_f32 v118, v118, v120
v_mul_f32 v119, v119, v120
mav 14,240,50
R 46,34560
v_cvt_pk_bf16_f32 v118, v118, v118
mav 18,244,50
v_cvt_pk_bf16_f32 v119, v119, v119
v_and_b32 v114, v118, v94
mva 22,50,248
s_waitcnt lgkmcnt(2)
v_and_b32 v115, v119, v95
v_and_b32 v116, v118, v96
mav 14,252,54
R 50,34624
v_and_b32 v117, v119, v97
mvv 18,128,54
mvv 22,54,132
v_mfma_f32_16x16x32_bf16 v[70:73], v[8:11], v[114:117], 0
v_mfma_f32_16x16x32_bf16 v[74:77], v[108:111], v[114:117], 0
mvv 14,136,38
R 54,34688
mvv 18,140,38
mvv 22,38,144
s_waitcnt lgkmcnt(2)
mvv 14,148,42
R 38,34752
v_cvt_pk_bf16_f32 v114, v70, v71
mvv 18,152,42
v_cvt_pk_bf16_f32 v115, v72, v73
v_cvt_pk_bf16_f32 v116, v74, v75
mvv 22,42,156
v_cvt_pk_bf16_f32 v117, v76, v77
mvv 14,160,46
R 42,34816
mvv 18,164,46
buffer_store_dwordx4 v[114:117], v82, s[36:39], s49 offen sc1
s_add_u32 s49, s49, 313344
mvv 22,46,168
s_waitcnt lgkmcnt(2)
mvv 14,172,50
R 46,34880
mvv 18,176,50
mvv 22,50,180
mvv 14,184,54
R 50,34944
mvv 18,188,54
mvv 22,54,192
s_waitcnt lgkmcnt(2)
mvv 14,196,38
R 54,35008
mvv 18,200,38
mvv 22,38,204
mvv 14,208,42
R 38,0
mvv 18,212,42
ds_read_b64 v[122:123], v83 offset:0
mvv 22,42,216
s_waitcnt lgkmcnt(3)
ds_read_b128 v[70:73], v84 offset:0
mvv 14,220,46
R 42,64
mvv 18,224,46
ds_read_b128 v[74:77], v84 offset:16
mvv 22,46,228
mvv 14,232,50
R 46,128
mvv 18,236,50
mvv 22,50,240
s_waitcnt lgkmcnt(3)
mvv 14,244,54
R 50,192
mvv 18,248,54
mvv 22,54,252
v_mfma_f32_16x16x32_bf16 v[26:29], a[0:3], v[38:41], 0
s_waitcnt lgkmcnt(1)
R 54,256
v_mul_f32 v71, v71, v99
v_mfma_f32_16x16x32_bf16 v[30:33], a[4:7], v[38:41], 0
v_mul_f32 v73, v73, v99
v_mul_f32 v75, v75, v99
v_mfma_f32_16x16x32_bf16 v[34:37], v[38:41], a[8:11], 0
v_fma_f32 v22, v22, v70, v71
v_fma_f32 v23, v23, v72, v73
mav 26,12,42
R 58,320
v_fma_f32 v24, v24, v74, v75
mav 30,16,42
v_mul_f32 v77, v77, v99
v_fma_f32 v25, v25, v76, v77
mva 34,42,20
v_cvt_pk_bf16_f32 v8, v22, v23
v_cvt_pk_bf16_f32 v9, v24, v25
mav 26,24,46
R 38,384
v_min_f32 v74, s56, v90
v_mfma_f32_16x16x32_bf16 v[70:73], v[104:107], v[0:3], v[86:89]
v_min_f32 v75, s56, v91
v_min_f32 v76, s56, v92
mav 30,28,46
v_min_f32 v77, s56, v93
v_mul_f32 v124, v123, v62
mva 34,46,32
s_waitcnt lgkmcnt(2)
v_fma_f32 v14, v14, v122, v124
v_mul_f32 v125, v123, v63
mvv 74,4,0
v_fma_f32 v15, v15, v122, v125
v_mul_f32 v124, v123, v64
mav 26,36,50
R 42,448
v_fma_f32 v16, v16, v122, v124
mav 30,40,50
v_mul_f32 v125, v123, v65
v_fma_f32 v17, v17, v122, v125
mva 34,50,44
v_max3_f32 v118, v70, v71, v74
v_max3_f32 v119, v72, v73, v76
mav 26,48,54
R 46,512
v_max_f32 v118, v118, v75
mav 30,52,54
v_max_f32 v119, v119, v77
v_max_f32 v120, v118, v119
mva 34,54,56
s_waitcnt lgkmcnt(2)
v_max_f32 v121, v118, v119
v_mul_f32 v124, v123, v66
mav 26,60,58
R 50,576
v_fma_f32 v18, v18, v122, v124
mav 30,64,58
v_permlane32_swap_b32 v120, v121
mva 34,58,68
v_max_f32 v114, v120, v121
v_max_f32 v115, v120, v121
mav 26,72,38
R 54,640
v_mul_f32 v125, v123, v67
mav 30,76,38
v_permlane16_swap_b32 v114, v115
mva 34,38,80
s_waitcnt lgkmcnt(2)
v_max_f32 v120, v114, v115
v_mul_f32 v120, s65, v120
mav 26,84,42
R 58,704
v_fma_f32 v118, v118, s65, -v120
mav 30,88,42
v_fma_f32 v119, v119, s65, -v120
v_fma_f32 v19, v19, v122, v125
mva 34,42,92
v_exp_f32 v118, v118
mav 26,96,46
R 38,768
v_mul_f32 v124, v123, v68
mav 30,100,46
s_barrier
mva 34,46,104
s_waitcnt lgkmcnt(2)
ds_read_b128 v[100:103], v82 offset:0
v_fma_f32 v20, v20, v122, v124
mav 26,108,50
ds_read_b64 v[108:109], v82 offset:1024
v_mul_f32 v125, v123, v69
mav 30,112,50
R 42,832
v_fma_f32 v21, v21, v122, v125
mva 34,50,116
v_exp_f32 v119, v119
mav 26,120,54
R 46,896
v_add_f32 v120, v118, v119
mav 30,124,54
v_add_f32 v121, v118, v119
v_cvt_pk_bf16_f32 v0, v14, v15
mva 34,54,128
s_waitcnt lgkmcnt(3)
v_permlane32_swap_b32 v120, v121
mav 26,132,58
R 50,960
v_add_f32 v114, v120, v121
mav 30,136,58
v_add_f32 v115, v120, v121
v_cvt_pk_bf16_f32 v1, v16, v17
mva 34,58,140
v_permlane16_swap_b32 v114, v115
mav 26,144,38
R 54,1024
v_add_f32 v120, v114, v115
mav 30,148,38
v_rcp_f32 v120, v120
mva 34,38,152
s_waitcnt lgkmcnt(2)
v_mul_f32 v118, v118, v120
v_mul_f32 v119, v119, v120
mav 26,156,42
R 38,1088
v_cvt_pk_bf16_f32 v118, v118, v118
mav 30,160,42
v_cvt_pk_bf16_f32 v119, v119, v119
v_and_b32 v114, v118, v94
mva 34,42,164
buffer_store_dwordx4 v[100:103], v82, s[36:39], s54 offen offset:2048 sc1
v_and_b32 v115, v119, v95
v_and_b32 v116, v118, v96
v_and_b32 v117, v119, v97
mav 26,168,46
R 42,1152
v_cvt_pk_bf16_f32 v2, v18, v19
v_mfma_f32_16x16x32_bf16 v[70:73], v[10:13], v[114:117], 0
v_cvt_pk_bf16_f32 v3, v20, v21
v_add_u32 v78, 1, v78
v_mfma_f32_16x16x32_bf16 v[74:77], v[110:113], v[114:117], 0
s_waitcnt vmcnt(1)
mav 30,172,46
buffer_store_dword v78, off, s[36:39], s52 sc1
mva 34,46,176
s_waitcnt lgkmcnt(2)
v_cvt_pk_bf16_f32 v114, v70, v71
v_cvt_pk_bf16_f32 v115, v72, v73
mav 26,180,50
buffer_store_dwordx2 v[8:9], v98, s[36:39], s54 offen offset:3072 sc1
R 46,1216
mav 30,184,50
v_cvt_pk_bf16_f32 v116, v74, v75
v_cvt_pk_bf16_f32 v117, v76, v77
mva 34,50,188
buffer_store_dwordx4 v[114:117], v82, s[36:39], s49 offen sc1
s_add_u32 s49, s49, 313344
mav 26,192,54
R 50,1280
mav 30,196,54
buffer_store_dwordx2 v[108:109], v98, s[36:39], s54 offen offset:3584 sc1
mva 34,54,200
s_waitcnt lgkmcnt(2)
mav 26,204,38
R 54,1344
mav 30,208,38
mva 34,38,212
mav 26,216,42
R 38,1408
mav 30,220,42
mva 34,42,224
s_waitcnt lgkmcnt(2)
mav 26,228,46
R 42,1472
mav 30,232,46
mva 34,46,236
mav 26,240,50
R 46,1536
mav 30,244,50
mva 34,50,248
s_waitcnt lgkmcnt(2)
mav 26,252,54
R 50,1600
mvv 30,128,54
mvv 34,54,132
mvv 26,136,38
R 54,1664
mvv 30,140,38
mvv 34,38,144
s_waitcnt lgkmcnt(2)
mvv 26,148,42
R 38,1728
mvv 30,152,42
mvv 34,42,156
mvv 26,160,46
R 42,1792
mvv 30,164,46
mvv 34,46,168
s_waitcnt lgkmcnt(2)
mvv 26,172,50
R 46,1856
mvv 30,176,50
mvv 34,50,180
mvv 26,184,54
R 50,1920
mvv 30,188,54
s_waitcnt lgkmcnt(2)
mvv 34,54,192
mvv 26,196,38
R 54,1984
LsQ11:
s_cbranch_execz LsQ15
v_xor_b32 v80, v81, v80
mvv 30,200,38
mvv 34,38,204
mvv 26,208,42
R 38,33024
mvv 30,212,42
ds_read_b64 v[122:123], v83 offset:128
mvv 34,42,216
s_waitcnt lgkmcnt(3)
ds_read_b128 v[70:73], v84 offset:128
mvv 26,220,46
R 42,33088
mvv 30,224,46
ds_read_b128 v[74:77], v84 offset:144
mvv 34,46,228
mvv 26,232,50
R 46,33152
mvv 30,236,50
mvv 34,50,240
s_waitcnt lgkmcnt(3)
mvv 26,244,54
R 50,33216
mvv 30,248,54
mvv 34,54,252
v_mfma_f32_16x16x32_bf16 v[14:17], a[0:3], v[38:41], 0
s_waitcnt lgkmcnt(1)
R 54,33280
v_mul_f32 v71, v71, v99
v_mfma_f32_16x16x32_bf16 v[18:21], a[4:7], v[38:41], 0
v_mul_f32 v73, v73, v99
v_mul_f32 v75, v75, v99
v_mfma_f32_16x16x32_bf16 v[22:25], v[38:41], a[8:11], 0
v_fma_f32 v34, v34, v70, v71
v_fma_f32 v35, v35, v72, v73
mav 14,12,42
R 58,33344
v_fma_f32 v36, v36, v74, v75
mav 18,16,42
v_mul_f32 v77, v77, v99
v_fma_f32 v37, v37, v76, v77
mva 22,42,20
v_cvt_pk_bf16_f32 v10, v34, v35
v_cvt_pk_bf16_f32 v11, v36, v37
mav 14,24,46
R 38,33408
v_mul_f32 v124, v123, v62
v_mfma_f32_16x16x32_bf16 v[70:73], v[100:103], v[0:3], v[86:89]
v_fma_f32 v26, v26, v122, v124
v_mul_f32 v125, v123, v63
mav 18,28,46
v_fma_f32 v27, v27, v122, v125
v_mul_f32 v124, v123, v64
mva 22,46,32
s_waitcnt lgkmcnt(2)
v_fma_f32 v28, v28, v122, v124
v_mul_f32 v125, v123, v65
mav 14,36,50
R 42,33472
v_fma_f32 v29, v29, v122, v125
mav 18,40,50
v_mul_f32 v124, v123, v66
v_fma_f32 v30, v30, v122, v124
mva 22,50,44
v_mul_f32 v125, v123, v67
v_fma_f32 v31, v31, v122, v125
mav 14,48,54
R 46,33536
v_mul_f32 v124, v123, v68
mav 18,52,54
v_fma_f32 v32, v32, v122, v124
v_mul_f32 v125, v123, v69
mva 22,54,56
s_waitcnt lgkmcnt(2)
v_fma_f32 v33, v33, v122, v125
v_add_u32 v78, 1, v78
mav 14,60,58
R 50,33600
mav 18,64,58
mva 22,58,68
mav 14,72,38
R 54,33664
mav 18,76,38
mva 22,38,80
s_waitcnt lgkmcnt(2)
mav 14,84,42
R 58,33728
mav 18,88,42
mva 22,42,92
mav 14,96,46
R 38,33792
mav 18,100,46
s_barrier
mva 22,46,104
s_waitcnt lgkmcnt(2)
ds_read_b128 v[104:107], v82 offset:4096
mav 14,108,50
ds_read_b64 v[110:111], v82 offset:5120
mav 18,112,50
R 42,33856
mva 22,50,116
mav 14,120,54
s_waitcnt vmcnt(1)
R 46,33920
mav 18,124,54
buffer_store_dword v78, off, s[36:39], s52 sc1
mva 22,54,128
s_waitcnt lgkmcnt(3)
v_mfma_f32_16x16x32_bf16 v[74:77], v[104:107], v[0:3], v[90:93]
v_cvt_pk_bf16_f32 v0, v26, v27
v_cvt_pk_bf16_f32 v1, v28, v29
mav 14,132,58
R 50,33984
v_cvt_pk_bf16_f32 v2, v30, v31
mav 18,136,58
v_cvt_pk_bf16_f32 v3, v32, v33
mva 22,58,140
v_max3_f32 v118, v70, v71, v74
v_max3_f32 v119, v72, v73, v76
mav 14,144,38
R 54,34048
v_max_f32 v118, v118, v75
mav 18,148,38
v_max_f32 v119, v119, v77
v_max_f32 v120, v118, v119
mva 22,38,152
s_waitcnt lgkmcnt(2)
v_max_f32 v121, v118, v119
mav 14,156,42
R 38,34112
mav 18,160,42
v_permlane32_swap_b32 v120, v121
mva 22,42,164
v_max_f32 v114, v120, v121
v_max_f32 v115, v120, v121
mav 14,168,46
R 42,34176
mav 18,172,46
v_permlane16_swap_b32 v114, v115
mva 22,46,176
s_waitcnt lgkmcnt(2)
v_max_f32 v120, v114, v115
v_mul_f32 v120, s65, v120
mav 14,180,50
R 46,34240
v_fma_f32 v118, v118, s65, -v120
mav 18,184,50
v_fma_f32 v119, v119, s65, -v120
mva 22,50,188
v_exp_f32 v118, v118
mav 14,192,54
R 50,34304
mav 18,196,54
v_exp_f32 v119, v119
mva 22,54,200
s_waitcnt lgkmcnt(2)
v_add_f32 v120, v118, v119
v_add_f32 v121, v118, v119
mav 14,204,38
R 54,34368
mav 18,208,38
v_permlane32_swap_b32 v120, v121
mva 22,38,212
v_add_f32 v114, v120, v121
v_add_f32 v115, v120, v121
mav 14,216,42
R 38,34432
mav 18,220,42
v_permlane16_swap_b32 v114, v115
mva 22,42,224
s_waitcnt lgkmcnt(2)
v_add_f32 v120, v114, v115
mav 14,228,46
R 42,34496
mav 18,232,46
v_rcp_f32 v120, v120
mva 22,46,236
v_mul_f32 v118, v118, v120
v_mul_f32 v119, v119, v120
mav 14,240,50
R 46,34560
v_cvt_pk_bf16_f32 v118, v118, v118
mav 18,244,50
v_cvt_pk_bf16_f32 v119, v119, v119
v_and_b32 v114, v118, v94
mva 22,50,248
s_waitcnt lgkmcnt(2)
v_and_b32 v115, v119, v95
v_and_b32 v116, v118, v96
mav 14,252,54
R 50,34624
v_and_b32 v117, v119, v97
mvv 18,128,54
mvv 22,54,132
v_mfma_f32_16x16x32_bf16 v[70:73], v[8:11], v[114:117], 0
v_mfma_f32_16x16x32_bf16 v[74:77], v[108:111], v[114:117], 0
mvv 14,136,38
R 54,34688
mvv 18,140,38
mvv 22,38,144
s_waitcnt lgkmcnt(2)
mvv 14,148,42
R 38,34752
v_cvt_pk_bf16_f32 v114, v70, v71
mvv 18,152,42
v_cvt_pk_bf16_f32 v115, v72, v73
v_cvt_pk_bf16_f32 v116, v74, v75
mvv 22,42,156
v_cvt_pk_bf16_f32 v117, v76, v77
mvv 14,160,46
R 42,34816
mvv 18,164,46
buffer_store_dwordx4 v[114:117], v82, s[36:39], s49 offen sc1
s_add_u32 s49, s49, 313344
mvv 22,46,168
s_waitcnt lgkmcnt(2)
mvv 14,172,50
R 46,34880
mvv 18,176,50
mvv 22,50,180
mvv 14,184,54
R 50,34944
mvv 18,188,54
mvv 22,54,192
s_waitcnt lgkmcnt(2)
mvv 14,196,38
R 54,35008
mvv 18,200,38
mvv 22,38,204
mvv 14,208,42
R 38,0
mvv 18,212,42
ds_read_b64 v[122:123], v83 offset:0
mvv 22,42,216
s_waitcnt lgkmcnt(3)
ds_read_b128 v[70:73], v84 offset:0
mvv 14,220,46
R 42,64
mvv 18,224,46
ds_read_b128 v[74:77], v84 offset:16
mvv 22,46,228
mvv 14,232,50
R 46,128
mvv 18,236,50
mvv 22,50,240
s_waitcnt lgkmcnt(3)
mvv 14,244,54
R 50,192
mvv 18,248,54
mvv 22,54,252
v_mfma_f32_16x16x32_bf16 v[26:29], a[0:3], v[38:41], 0
s_waitcnt lgkmcnt(1)
R 54,256
v_mul_f32 v71, v71, v99
v_mfma_f32_16x16x32_bf16 v[30:33], a[4:7], v[38:41], 0
v_mul_f32 v73, v73, v99
v_mul_f32 v75, v75, v99
v_mfma_f32_16x16x32_bf16 v[34:37], v[38:41], a[8:11], 0
v_fma_f32 v22, v22, v70, v71
v_fma_f32 v23, v23, v72, v73
mav 26,12,42
R 58,320
v_fma_f32 v24, v24, v74, v75
mav 30,16,42
v_mul_f32 v77, v77, v99
v_fma_f32 v25, v25, v76, v77
mva 34,42,20
v_cvt_pk_bf16_f32 v8, v22, v23
v_cvt_pk_bf16_f32 v9, v24, v25
mav 26,24,46
R 38,384
v_mul_f32 v124, v123, v62
v_mfma_f32_16x16x32_bf16 v[70:73], v[104:107], v[0:3], v[86:89]
v_fma_f32 v14, v14, v122, v124
v_mul_f32 v125, v123, v63
mav 30,28,46
v_fma_f32 v15, v15, v122, v125
v_mul_f32 v124, v123, v64
mva 34,46,32
s_waitcnt lgkmcnt(2)
v_fma_f32 v16, v16, v122, v124
v_mul_f32 v125, v123, v65
mav 26,36,50
R 42,448
v_fma_f32 v17, v17, v122, v125
mav 30,40,50
v_mul_f32 v124, v123, v66
v_fma_f32 v18, v18, v122, v124
mva 34,50,44
v_mul_f32 v125, v123, v67
v_fma_f32 v19, v19, v122, v125
mav 26,48,54
R 46,512
v_mul_f32 v124, v123, v68
mav 30,52,54
v_fma_f32 v20, v20, v122, v124
v_mul_f32 v125, v123, v69
mva 34,54,56
s_waitcnt lgkmcnt(2)
v_fma_f32 v21, v21, v122, v125
v_add_u32 v78, 1, v78
mav 26,60,58
R 50,576
mav 30,64,58
mva 34,58,68
mav 26,72,38
R 54,640
mav 30,76,38
mva 34,38,80
s_waitcnt lgkmcnt(2)
mav 26,84,42
R 58,704
mav 30,88,42
mva 34,42,92
mav 26,96,46
R 38,768
mav 30,100,46
s_barrier
mva 34,46,104
s_waitcnt lgkmcnt(2)
ds_read_b128 v[100:103], v82 offset:0
mav 26,108,50
ds_read_b64 v[108:109], v82 offset:1024
mav 30,112,50
R 42,832
mva 34,50,116
mav 26,120,54
s_waitcnt vmcnt(0)
R 46,896
mav 30,124,54
buffer_store_dword v78, off, s[36:39], s52 sc1
mva 34,54,128
s_waitcnt lgkmcnt(3)
v_mfma_f32_16x16x32_bf16 v[74:77], v[100:103], v[0:3], v[90:93]
v_cvt_pk_bf16_f32 v0, v14, v15
v_cvt_pk_bf16_f32 v1, v16, v17
mav 26,132,58
R 50,960
v_cvt_pk_bf16_f32 v2, v18, v19
mav 30,136,58
v_cvt_pk_bf16_f32 v3, v20, v21
mva 34,58,140
v_max3_f32 v118, v70, v71, v74
v_max3_f32 v119, v72, v73, v76
mav 26,144,38
R 54,1024
v_max_f32 v118, v118, v75
mav 30,148,38
v_max_f32 v119, v119, v77
v_max_f32 v120, v118, v119
mva 34,38,152
s_waitcnt lgkmcnt(2)
v_max_f32 v121, v118, v119
mav 26,156,42
R 38,1088
mav 30,160,42
v_permlane32_swap_b32 v120, v121
mva 34,42,164
v_max_f32 v114, v120, v121
v_max_f32 v115, v120, v121
mav 26,168,46
R 42,1152
mav 30,172,46
v_permlane16_swap_b32 v114, v115
mva 34,46,176
s_waitcnt lgkmcnt(2)
v_max_f32 v120, v114, v115
v_mul_f32 v120, s65, v120
)"
R"(
mav 26,180,50
R 46,1216
v_fma_f32 v118, v118, s65, -v120
mav 30,184,50
v_fma_f32 v119, v119, s65, -v120
mva 34,50,188
v_exp_f32 v118, v118
mav 26,192,54
R 50,1280
mav 30,196,54
v_exp_f32 v119, v119
mva 34,54,200
s_waitcnt lgkmcnt(2)
v_add_f32 v120, v118, v119
v_add_f32 v121, v118, v119
mav 26,204,38
R 54,1344
mav 30,208,38
v_permlane32_swap_b32 v120, v121
mva 34,38,212
v_add_f32 v114, v120, v121
v_add_f32 v115, v120, v121
mav 26,216,42
R 38,1408
mav 30,220,42
v_permlane16_swap_b32 v114, v115
mva 34,42,224
s_waitcnt lgkmcnt(2)
v_add_f32 v120, v114, v115
mav 26,228,46
R 42,1472
mav 30,232,46
v_rcp_f32 v120, v120
mva 34,46,236
v_mul_f32 v118, v118, v120
v_mul_f32 v119, v119, v120
mav 26,240,50
R 46,1536
v_cvt_pk_bf16_f32 v118, v118, v118
mav 30,244,50
v_cvt_pk_bf16_f32 v119, v119, v119
v_and_b32 v114, v118, v96
mva 34,50,248
s_waitcnt lgkmcnt(2)
v_and_b32 v115, v119, v97
v_and_b32 v116, v118, v94
mav 26,252,54
R 50,1600
v_and_b32 v117, v119, v95
mvv 30,128,54
mvv 34,54,132
v_mfma_f32_16x16x32_bf16 v[70:73], v[8:11], v[114:117], 0
v_mfma_f32_16x16x32_bf16 v[74:77], v[108:111], v[114:117], 0
mvv 26,136,38
R 54,1664
mvv 30,140,38
mvv 34,38,144
s_waitcnt lgkmcnt(2)
mvv 26,148,42
R 38,1728
v_cvt_pk_bf16_f32 v114, v70, v71
mvv 30,152,42
v_cvt_pk_bf16_f32 v115, v72, v73
v_cvt_pk_bf16_f32 v116, v74, v75
mvv 34,42,156
v_cvt_pk_bf16_f32 v117, v76, v77
mvv 26,160,46
R 42,1792
mvv 30,164,46
buffer_store_dwordx4 v[114:117], v82, s[36:39], s49 offen sc1
s_add_u32 s49, s49, 313344
mvv 34,46,168
s_waitcnt lgkmcnt(2)
mvv 26,172,50
R 46,1856
mvv 30,176,50
mvv 34,50,180
mvv 26,184,54
R 50,1920
mvv 30,188,54
s_waitcnt lgkmcnt(2)
mvv 34,54,192
s_mov_b32 s60, -13
.p2align 6
LQA1_2:
mvv 26,196,38
R 54,1984
v_xor_b32 v80, v81, v80
mvv 30,200,38
mvv 34,38,204
mvv 26,208,42
R 38,33024
mvv 30,212,42
ds_read_b64 v[122:123], v83 offset:128
mvv 34,42,216
s_waitcnt lgkmcnt(3)
ds_read_b128 v[70:73], v84 offset:128
mvv 26,220,46
R 42,33088
mvv 30,224,46
ds_read_b128 v[74:77], v84 offset:144
mvv 34,46,228
mvv 26,232,50
R 46,33152
mvv 30,236,50
mvv 34,50,240
s_waitcnt lgkmcnt(3)
mvv 26,244,54
R 50,33216
mvv 30,248,54
mvv 34,54,252
v_mfma_f32_16x16x32_bf16 v[14:17], a[0:3], v[38:41], 0
s_waitcnt lgkmcnt(1)
R 54,33280
v_mul_f32 v71, v71, v99
v_mfma_f32_16x16x32_bf16 v[18:21], a[4:7], v[38:41], 0
v_mul_f32 v73, v73, v99
v_mul_f32 v75, v75, v99
v_mfma_f32_16x16x32_bf16 v[22:25], v[38:41], a[8:11], 0
v_fma_f32 v34, v34, v70, v71
v_fma_f32 v35, v35, v72, v73
mav 14,12,42
R 58,33344
v_fma_f32 v36, v36, v74, v75
mav 18,16,42
v_mul_f32 v77, v77, v99
v_fma_f32 v37, v37, v76, v77
mva 22,42,20
v_cvt_pk_bf16_f32 v10, v34, v35
v_cvt_pk_bf16_f32 v11, v36, v37
mav 14,24,46
R 38,33408
v_mul_f32 v124, v123, v62
v_mfma_f32_16x16x32_bf16 v[70:73], v[100:103], v[0:3], v[86:89]
v_fma_f32 v26, v26, v122, v124
v_mul_f32 v125, v123, v63
mav 18,28,46
v_fma_f32 v27, v27, v122, v125
v_mul_f32 v124, v123, v64
mva 22,46,32
s_waitcnt lgkmcnt(2)
v_fma_f32 v28, v28, v122, v124
v_mul_f32 v125, v123, v65
mav 14,36,50
R 42,33472
v_fma_f32 v29, v29, v122, v125
mav 18,40,50
v_mul_f32 v124, v123, v66
v_fma_f32 v30, v30, v122, v124
mva 22,50,44
v_mul_f32 v125, v123, v67
v_fma_f32 v31, v31, v122, v125
mav 14,48,54
R 46,33536
v_mul_f32 v124, v123, v68
mav 18,52,54
v_fma_f32 v32, v32, v122, v124
v_mul_f32 v125, v123, v69
mva 22,54,56
s_waitcnt lgkmcnt(2)
v_fma_f32 v33, v33, v122, v125
v_add_u32 v78, 1, v78
mav 14,60,58
R 50,33600
mav 18,64,58
mva 22,58,68
mav 14,72,38
R 54,33664
mav 18,76,38
mva 22,38,80
s_waitcnt lgkmcnt(2)
mav 14,84,42
R 58,33728
mav 18,88,42
mva 22,42,92
mav 14,96,46
R 38,33792
mav 18,100,46
s_barrier
mva 22,46,104
s_waitcnt lgkmcnt(2)
ds_read_b128 v[104:107], v82 offset:4096
mav 14,108,50
ds_read_b64 v[110:111], v82 offset:5120
mav 18,112,50
R 42,33856
mva 22,50,116
mav 14,120,54
s_waitcnt vmcnt(0)
R 46,33920
mav 18,124,54
buffer_store_dword v78, off, s[36:39], s52 sc1
mva 22,54,128
s_waitcnt lgkmcnt(3)
v_mfma_f32_16x16x32_bf16 v[74:77], v[104:107], v[0:3], v[90:93]
v_cvt_pk_bf16_f32 v0, v26, v27
v_cvt_pk_bf16_f32 v1, v28, v29
mav 14,132,58
R 50,33984
v_cvt_pk_bf16_f32 v2, v30, v31
mav 18,136,58
v_cvt_pk_bf16_f32 v3, v32, v33
mva 22,58,140
v_max3_f32 v118, v70, v71, v74
v_max3_f32 v119, v72, v73, v76
mav 14,144,38
R 54,34048
v_max_f32 v118, v118, v75
mav 18,148,38
v_max_f32 v119, v119, v77
LsQ12:
s_cbranch_execz LsQ16
v_max_f32 v120, v118, v119
mva 22,38,152
s_waitcnt lgkmcnt(2)
v_max_f32 v121, v118, v119
mav 14,156,42
R 38,34112
mav 18,160,42
v_permlane32_swap_b32 v120, v121
mva 22,42,164
v_max_f32 v114, v120, v121
v_max_f32 v115, v120, v121
mav 14,168,46
R 42,34176
mav 18,172,46
v_permlane16_swap_b32 v114, v115
mva 22,46,176
s_waitcnt lgkmcnt(2)
v_max_f32 v120, v114, v115
v_mul_f32 v120, s65, v120
mav 14,180,50
R 46,34240
v_fma_f32 v118, v118, s65, -v120
mav 18,184,50
v_fma_f32 v119, v119, s65, -v120
mva 22,50,188
v_exp_f32 v118, v118
mav 14,192,54
R 50,34304
mav 18,196,54
v_exp_f32 v119, v119
mva 22,54,200
s_waitcnt lgkmcnt(2)
v_add_f32 v120, v118, v119
v_add_f32 v121, v118, v119
mav 14,204,38
R 54,34368
mav 18,208,38
v_permlane32_swap_b32 v120, v121
mva 22,38,212
v_add_f32 v114, v120, v121
v_add_f32 v115, v120, v121
mav 14,216,42
R 38,34432
mav 18,220,42
v_permlane16_swap_b32 v114, v115
mva 22,42,224
s_waitcnt lgkmcnt(2)
v_add_f32 v120, v114, v115
mav 14,228,46
R 42,34496
mav 18,232,46
v_rcp_f32 v120, v120
mva 22,46,236
v_mul_f32 v118, v118, v120
v_mul_f32 v119, v119, v120
mav 14,240,50
R 46,34560
v_cvt_pk_bf16_f32 v118, v118, v118
mav 18,244,50
v_cvt_pk_bf16_f32 v119, v119, v119
v_and_b32 v114, v118, v94
mva 22,50,248
s_waitcnt lgkmcnt(2)
v_and_b32 v115, v119, v95
v_and_b32 v116, v118, v96
mav 14,252,54
R 50,34624
v_and_b32 v117, v119, v97
mvv 18,128,54
mvv 22,54,132
v_mfma_f32_16x16x32_bf16 v[70:73], v[8:11], v[114:117], 0
v_mfma_f32_16x16x32_bf16 v[74:77], v[108:111], v[114:117], 0
mvv 14,136,38
R 54,34688
mvv 18,140,38
mvv 22,38,144
s_waitcnt lgkmcnt(2)
mvv 14,148,42
R 38,34752
v_cvt_pk_bf16_f32 v114, v70, v71
mvv 18,152,42
v_cvt_pk_bf16_f32 v115, v72, v73
v_cvt_pk_bf16_f32 v116, v74, v75
mvv 22,42,156
v_cvt_pk_bf16_f32 v117, v76, v77
mvv 14,160,46
R 42,34816
mvv 18,164,46
buffer_store_dwordx4 v[114:117], v82, s[36:39], s49 offen sc1
s_add_u32 s49, s49, 313344
mvv 22,46,168
s_waitcnt lgkmcnt(2)
mvv 14,172,50
R 46,34880
mvv 18,176,50
mvv 22,50,180
mvv 14,184,54
R 50,34944
mvv 18,188,54
mvv 22,54,192
s_waitcnt lgkmcnt(2)
mvv 14,196,38
R 54,35008
mvv 18,200,38
mvv 22,38,204
mvv 14,208,42
R 38,0
mvv 18,212,42
ds_read_b64 v[122:123], v83 offset:0
mvv 22,42,216
s_waitcnt lgkmcnt(3)
ds_read_b128 v[70:73], v84 offset:0
mvv 14,220,46
R 42,64
mvv 18,224,46
ds_read_b128 v[74:77], v84 offset:16
mvv 22,46,228
mvv 14,232,50
R 46,128
mvv 18,236,50
mvv 22,50,240
s_waitcnt lgkmcnt(3)
mvv 14,244,54
R 50,192
mvv 18,248,54
mvv 22,54,252
v_mfma_f32_16x16x32_bf16 v[26:29], a[0:3], v[38:41], 0
s_waitcnt lgkmcnt(1)
R 54,256
v_mul_f32 v71, v71, v99
v_mfma_f32_16x16x32_bf16 v[30:33], a[4:7], v[38:41], 0
v_mul_f32 v73, v73, v99
v_mul_f32 v75, v75, v99
v_mfma_f32_16x16x32_bf16 v[34:37], v[38:41], a[8:11], 0
v_fma_f32 v22, v22, v70, v71
v_fma_f32 v23, v23, v72, v73
mav 26,12,42
R 58,320
v_fma_f32 v24, v24, v74, v75
mav 30,16,42
v_mul_f32 v77, v77, v99
v_fma_f32 v25, v25, v76, v77
mva 34,42,20
v_cvt_pk_bf16_f32 v8, v22, v23
v_cvt_pk_bf16_f32 v9, v24, v25
mav 26,24,46
R 38,384
v_mul_f32 v124, v123, v62
v_mfma_f32_16x16x32_bf16 v[70:73], v[104:107], v[0:3], v[86:89]
v_fma_f32 v14, v14, v122, v124
v_mul_f32 v125, v123, v63
mav 30,28,46
v_fma_f32 v15, v15, v122, v125
v_mul_f32 v124, v123, v64
mva 34,46,32
s_waitcnt lgkmcnt(2)
v_fma_f32 v16, v16, v122, v124
v_mul_f32 v125, v123, v65
mav 26,36,50
R 42,448
v_fma_f32 v17, v17, v122, v125
mav 30,40,50
v_mul_f32 v124, v123, v66
v_fma_f32 v18, v18, v122, v124
mva 34,50,44
v_mul_f32 v125, v123, v67
v_fma_f32 v19, v19, v122, v125
mav 26,48,54
R 46,512
v_mul_f32 v124, v123, v68
mav 30,52,54
v_fma_f32 v20, v20, v122, v124
v_mul_f32 v125, v123, v69
mva 34,54,56
s_waitcnt lgkmcnt(2)
v_fma_f32 v21, v21, v122, v125
v_add_u32 v78, 1, v78
mav 26,60,58
R 50,576
mav 30,64,58
mva 34,58,68
mav 26,72,38
R 54,640
mav 30,76,38
mva 34,38,80
s_waitcnt lgkmcnt(2)
mav 26,84,42
R 58,704
mav 30,88,42
mva 34,42,92
mav 26,96,46
R 38,768
mav 30,100,46
s_barrier
mva 34,46,104
s_waitcnt lgkmcnt(2)
ds_read_b128 v[100:103], v82 offset:0
mav 26,108,50
ds_read_b64 v[108:109], v82 offset:1024
mav 30,112,50
R 42,832
mva 34,50,116
mav 26,120,54
s_waitcnt vmcnt(0)
R 46,896
mav 30,124,54
buffer_store_dword v78, off, s[36:39], s52 sc1
mva 34,54,128
s_waitcnt lgkmcnt(3)
v_mfma_f32_16x16x32_bf16 v[74:77], v[100:103], v[0:3], v[90:93]
v_cvt_pk_bf16_f32 v0, v14, v15
v_cvt_pk_bf16_f32 v1, v16, v17
mav 26,132,58
R 50,960
v_cvt_pk_bf16_f32 v2, v18, v19
mav 30,136,58
v_cvt_pk_bf16_f32 v3, v20, v21
mva 34,58,140
v_max3_f32 v118, v70, v71, v74
v_max3_f32 v119, v72, v73, v76
mav 26,144,38
R 54,1024
v_max_f32 v118, v118, v75
mav 30,148,38
v_max_f32 v119, v119, v77
v_max_f32 v120, v118, v119
mva 34,38,152
s_waitcnt lgkmcnt(2)
v_max_f32 v121, v118, v119
mav 26,156,42
R 38,1088
mav 30,160,42
v_permlane32_swap_b32 v120, v121
mva 34,42,164
v_max_f32 v114, v120, v121
v_max_f32 v115, v120, v121
mav 26,168,46
R 42,1152
mav 30,172,46
v_permlane16_swap_b32 v114, v115
mva 34,46,176
s_waitcnt lgkmcnt(2)
v_max_f32 v120, v114, v115
v_mul_f32 v120, s65, v120
mav 26,180,50
R 46,1216
v_fma_f32 v118, v118, s65, -v120
mav 30,184,50
v_fma_f32 v119, v119, s65, -v120
mva 34,50,188
v_exp_f32 v118, v118
mav 26,192,54
R 50,1280
mav 30,196,54
v_exp_f32 v119, v119
mva 34,54,200
s_waitcnt lgkmcnt(2)
v_add_f32 v120, v118, v119
v_add_f32 v121, v118, v119
mav 26,204,38
R 54,1344
mav 30,208,38
v_permlane32_swap_b32 v120, v121
mva 34,38,212
v_add_f32 v114, v120, v121
v_add_f32 v115, v120, v121
mav 26,216,42
R 38,1408
mav 30,220,42
v_permlane16_swap_b32 v114, v115
mva 34,42,224
s_waitcnt lgkmcnt(2)
v_add_f32 v120, v114, v115
mav 26,228,46
R 42,1472
mav 30,232,46
v_rcp_f32 v120, v120
mva 34,46,236
v_mul_f32 v118, v118, v120
v_mul_f32 v119, v119, v120
mav 26,240,50
R 46,1536
v_cvt_pk_bf16_f32 v118, v118, v118
mav 30,244,50
v_cvt_pk_bf16_f32 v119, v119, v119
v_and_b32 v114, v118, v96
mva 34,50,248
s_waitcnt lgkmcnt(2)
v_and_b32 v115, v119, v97
v_and_b32 v116, v118, v94
mav 26,252,54
R 50,1600
v_and_b32 v117, v119, v95
mvv 30,128,54
mvv 34,54,132
v_mfma_f32_16x16x32_bf16 v[70:73], v[8:11], v[114:117], 0
v_mfma_f32_16x16x32_bf16 v[74:77], v[108:111], v[114:117], 0
mvv 26,136,38
R 54,1664
mvv 30,140,38
mvv 34,38,144
s_waitcnt lgkmcnt(2)
mvv 26,148,42
R 38,1728
v_cvt_pk_bf16_f32 v114, v70, v71
mvv 30,152,42
v_cvt_pk_bf16_f32 v115, v72, v73
v_cvt_pk_bf16_f32 v116, v74, v75
mvv 34,42,156
v_cvt_pk_bf16_f32 v117, v76, v77
mvv 26,160,46
R 42,1792
mvv 30,164,46
buffer_store_dwordx4 v[114:117], v82, s[36:39], s49 offen sc1
s_add_u32 s49, s49, 313344
mvv 34,46,168
s_waitcnt lgkmcnt(2)
mvv 26,172,50
R 46,1856
mvv 30,176,50
mvv 34,50,180
mvv 26,184,54
R 50,1920
mvv 30,188,54
s_waitcnt lgkmcnt(2)
s_add_u32 s60, s60, 1
mvv 34,54,192
s_cbranch_scc0 LQA1_2
mvv 26,196,38
R 54,1984
v_xor_b32 v80, v81, v80
mvv 30,200,38
mvv 34,38,204
mvv 26,208,42
R 38,33024
mvv 30,212,42
ds_read_b64 v[122:123], v83 offset:128
mvv 34,42,216
s_waitcnt lgkmcnt(3)
ds_read_b128 v[70:73], v84 offset:128
mvv 26,220,46
R 42,33088
mvv 30,224,46
ds_read_b128 v[74:77], v84 offset:144
mvv 34,46,228
mvv 26,232,50
R 46,33152
mvv 30,236,50
mvv 34,50,240
s_waitcnt lgkmcnt(3)
mvv 26,244,54
R 50,33216
mvv 30,248,54
mvv 34,54,252
v_mfma_f32_16x16x32_bf16 v[14:17], a[0:3], v[38:41], 0
s_waitcnt lgkmcnt(1)
R 54,33280
v_mul_f32 v71, v71, v99
v_mfma_f32_16x16x32_bf16 v[18:21], a[4:7], v[38:41], 0
v_mul_f32 v73, v73, v99
v_mul_f32 v75, v75, v99
v_mfma_f32_16x16x32_bf16 v[22:25], v[38:41], a[8:11], 0
v_fma_f32 v34, v34, v70, v71
v_fma_f32 v35, v35, v72, v73
mav 14,12,42
R 58,33344
v_fma_f32 v36, v36, v74, v75
mav 18,16,42
v_mul_f32 v77, v77, v99
v_fma_f32 v37, v37, v76, v77
mva 22,42,20
v_cvt_pk_bf16_f32 v10, v34, v35
v_cvt_pk_bf16_f32 v11, v36, v37
mav 14,24,46
R 38,33408
v_mul_f32 v124, v123, v62
v_mfma_f32_16x16x32_bf16 v[70:73], v[100:103], v[0:3], v[86:89]
v_fma_f32 v26, v26, v122, v124
v_mul_f32 v125, v123, v63
mav 18,28,46
v_fma_f32 v27, v27, v122, v125
v_mul_f32 v124, v123, v64
mva 22,46,32
s_waitcnt lgkmcnt(2)
v_fma_f32 v28, v28, v122, v124
v_mul_f32 v125, v123, v65
mav 14,36,50
R 42,33472
v_fma_f32 v29, v29, v122, v125
mav 18,40,50
v_mul_f32 v124, v123, v66
v_fma_f32 v30, v30, v122, v124
mva 22,50,44
v_mul_f32 v125, v123, v67
v_fma_f32 v31, v31, v122, v125
mav 14,48,54
R 46,33536
v_mul_f32 v124, v123, v68
mav 18,52,54
v_fma_f32 v32, v32, v122, v124
v_mul_f32 v125, v123, v69
mva 22,54,56
s_waitcnt lgkmcnt(2)
v_fma_f32 v33, v33, v122, v125
v_add_u32 v78, 1, v78
mav 14,60,58
R 50,33600
mav 18,64,58
mva 22,58,68
mav 14,72,38
R 54,33664
mav 18,76,38
mva 22,38,80
s_waitcnt lgkmcnt(2)
mav 14,84,42
R 58,33728
mav 18,88,42
mva 22,42,92
mav 14,96,46
R 38,33792
mav 18,100,46
s_barrier
mva 22,46,104
s_waitcnt lgkmcnt(2)
ds_read_b128 v[104:107], v82 offset:4096
mav 14,108,50
ds_read_b64 v[110:111], v82 offset:5120
mav 18,112,50
R 42,33856
mva 22,50,116
mav 14,120,54
s_waitcnt vmcnt(0)
R 46,33920
mav 18,124,54
buffer_store_dword v78, off, s[36:39], s52 sc1
mva 22,54,128
s_waitcnt lgkmcnt(3)
v_mfma_f32_16x16x32_bf16 v[74:77], v[104:107], v[0:3], v[90:93]
v_cvt_pk_bf16_f32 v0, v26, v27
v_cvt_pk_bf16_f32 v1, v28, v29
mav 14,132,58
R 50,33984
v_cvt_pk_bf16_f32 v2, v30, v31
mav 18,136,58
v_cvt_pk_bf16_f32 v3, v32, v33
mva 22,58,140
v_max3_f32 v118, v70, v71, v74
v_max3_f32 v119, v72, v73, v76
mav 14,144,38
R 54,34048
v_max_f32 v118, v118, v75
mav 18,148,38
v_max_f32 v119, v119, v77
v_max_f32 v120, v118, v119
mva 22,38,152
s_waitcnt lgkmcnt(2)
v_max_f32 v121, v118, v119
mav 14,156,42
R 38,34112
mav 18,160,42
v_permlane32_swap_b32 v120, v121
mva 22,42,164
v_max_f32 v114, v120, v121
v_max_f32 v115, v120, v121
mav 14,168,46
R 42,34176
mav 18,172,46
v_permlane16_swap_b32 v114, v115
mva 22,46,176
s_waitcnt lgkmcnt(2)
v_max_f32 v120, v114, v115
v_mul_f32 v120, s65, v120
mav 14,180,50
R 46,34240
v_fma_f32 v118, v118, s65, -v120
mav 18,184,50
v_fma_f32 v119, v119, s65, -v120
mva 22,50,188
v_exp_f32 v118, v118
mav 14,192,54
R 50,34304
mav 18,196,54
v_exp_f32 v119, v119
mva 22,54,200
s_waitcnt lgkmcnt(2)
v_add_f32 v120, v118, v119
v_add_f32 v121, v118, v119
mav 14,204,38
R 54,34368
mav 18,208,38
v_permlane32_swap_b32 v120, v121
mva 22,38,212
v_add_f32 v114, v120, v121
v_add_f32 v115, v120, v121
mav 14,216,42
R 38,34432
mav 18,220,42
v_permlane16_swap_b32 v114, v115
mva 22,42,224
s_waitcnt lgkmcnt(2)
v_add_f32 v120, v114, v115
mav 14,228,46
R 42,34496
mav 18,232,46
v_rcp_f32 v120, v120
mva 22,46,236
v_mul_f32 v118, v118, v120
v_mul_f32 v119, v119, v120
mav 14,240,50
R 46,34560
v_cvt_pk_bf16_f32 v118, v118, v118
mav 18,244,50
v_cvt_pk_bf16_f32 v119, v119, v119
v_and_b32 v114, v118, v94
mva 22,50,248
s_waitcnt lgkmcnt(2)
v_and_b32 v115, v119, v95
v_and_b32 v116, v118, v96
mav 14,252,54
R 50,34624
v_and_b32 v117, v119, v97
mvv 18,128,54
mvv 22,54,132
v_mfma_f32_16x16x32_bf16 v[70:73], v[8:11], v[114:117], 0
v_mfma_f32_16x16x32_bf16 v[74:77], v[108:111], v[114:117], 0
mvv 14,136,38
R 54,34688
mvv 18,140,38
mvv 22,38,144
s_waitcnt lgkmcnt(2)
mvv 14,148,42
R 38,34752
v_cvt_pk_bf16_f32 v114, v70, v71
mvv 18,152,42
v_cvt_pk_bf16_f32 v115, v72, v73
v_cvt_pk_bf16_f32 v116, v74, v75
mvv 22,42,156
v_cvt_pk_bf16_f32 v117, v76, v77
mvv 14,160,46
R 42,34816
mvv 18,164,46
buffer_store_dwordx4 v[114:117], v82, s[36:39], s49 offen sc1
s_add_u32 s49, s49, 313344
mvv 22,46,168
s_waitcnt lgkmcnt(2)
mvv 14,172,50
R 46,34880
mvv 18,176,50
mvv 22,50,180
mvv 14,184,54
R 50,34944
mvv 18,188,54
mvv 22,54,192
s_waitcnt lgkmcnt(2)
mvv 14,196,38
R 54,35008
mvv 18,200,38
mvv 22,38,204
mvv 14,208,42
R 38,0
mvv 18,212,42
ds_read_b64 v[122:123], v83 offset:0
mvv 22,42,216
s_waitcnt lgkmcnt(3)
ds_read_b128 v[70:73], v84 offset:0
mvv 14,220,46
R 42,64
mvv 18,224,46
ds_read_b128 v[74:77], v84 offset:16
mvv 22,46,228
mvv 14,232,50
R 46,128
mvv 18,236,50
mvv 22,50,240
s_waitcnt lgkmcnt(3)
mvv 14,244,54
R 50,192
mvv 18,248,54
mvv 22,54,252
v_mfma_f32_16x16x32_bf16 v[26:29], a[0:3], v[38:41], 0
s_waitcnt lgkmcnt(1)
R 54,256
v_mul_f32 v71, v71, v99
v_mfma_f32_16x16x32_bf16 v[30:33], a[4:7], v[38:41], 0
v_mul_f32 v73, v73, v99
v_mul_f32 v75, v75, v99
v_mfma_f32_16x16x32_bf16 v[34:37], v[38:41], a[8:11], 0
v_fma_f32 v22, v22, v70, v71
v_fma_f32 v23, v23, v72, v73
LsQ13:
s_cbranch_execz LsQ17
mav 26,12,42
R 58,320
v_fma_f32 v24, v24, v74, v75
mav 30,16,42
v_mul_f32 v77, v77, v99
v_fma_f32 v25, v25, v76, v77
mva 34,42,20
v_cvt_pk_bf16_f32 v8, v22, v23
v_cvt_pk_bf16_f32 v9, v24, v25
mav 26,24,46
R 38,384
v_mul_f32 v124, v123, v62
v_mfma_f32_16x16x32_bf16 v[70:73], v[104:107], v[0:3], v[86:89]
v_fma_f32 v14, v14, v122, v124
v_mul_f32 v125, v123, v63
mav 30,28,46
v_fma_f32 v15, v15, v122, v125
v_mul_f32 v124, v123, v64
mva 34,46,32
s_waitcnt lgkmcnt(2)
v_fma_f32 v16, v16, v122, v124
v_mul_f32 v125, v123, v65
mav 26,36,50
R 42,448
v_fma_f32 v17, v17, v122, v125
mav 30,40,50
v_mul_f32 v124, v123, v66
v_fma_f32 v18, v18, v122, v124
mva 34,50,44
buffer_load_dwordx4 v[4:7], v82, s[36:39], s55 offen offset:2048 sc1
mav 26,48,54
R 46,512
v_mul_f32 v125, v123, v67
mav 30,52,54
buffer_load_dwordx2 v[12:13], v98, s[36:39], s55 offen offset:3072 sc1
mva 34,54,56
s_waitcnt lgkmcnt(2)
v_fma_f32 v19, v19, v122, v125
v_mul_f32 v124, v123, v68
mav 26,60,58
buffer_load_dwordx2 v[112:113], v98, s[36:39], s55 offen offset:3584 sc1
mav 30,64,58
R 50,576
v_fma_f32 v20, v20, v122, v124
mva 34,58,68
v_mul_f32 v125, v123, v69
v_fma_f32 v21, v21, v122, v125
mav 26,72,38
R 54,640
v_add_u32 v78, 1, v78
mav 30,76,38
mva 34,38,80
s_waitcnt lgkmcnt(2)
mav 26,84,42
R 58,704
mav 30,88,42
mva 34,42,92
mav 26,96,46
R 38,768
mav 30,100,46
s_barrier
mva 34,46,104
s_waitcnt lgkmcnt(2)
ds_read_b128 v[100:103], v82 offset:0
mav 26,108,50
ds_read_b64 v[108:109], v82 offset:1024
mav 30,112,50
R 42,832
mva 34,50,116
mav 26,120,54
s_waitcnt vmcnt(3)
R 46,896
mav 30,124,54
buffer_store_dword v78, off, s[36:39], s52 sc1
mva 34,54,128
s_waitcnt lgkmcnt(3)
v_mfma_f32_16x16x32_bf16 v[74:77], v[100:103], v[0:3], v[90:93]
v_cvt_pk_bf16_f32 v0, v14, v15
v_cvt_pk_bf16_f32 v1, v16, v17
mav 26,132,58
R 50,960
v_cvt_pk_bf16_f32 v2, v18, v19
mav 30,136,58
v_cvt_pk_bf16_f32 v3, v20, v21
mva 34,58,140
v_max3_f32 v118, v70, v71, v74
v_max3_f32 v119, v72, v73, v76
mav 26,144,38
R 54,1024
v_max_f32 v118, v118, v75
mav 30,148,38
v_max_f32 v119, v119, v77
v_max_f32 v120, v118, v119
mva 34,38,152
s_waitcnt lgkmcnt(2)
v_max_f32 v121, v118, v119
mav 26,156,42
R 38,1088
mav 30,160,42
v_permlane32_swap_b32 v120, v121
mva 34,42,164
v_max_f32 v114, v120, v121
v_max_f32 v115, v120, v121
mav 26,168,46
R 42,1152
mav 30,172,46
v_permlane16_swap_b32 v114, v115
mva 34,46,176
s_waitcnt lgkmcnt(2)
v_max_f32 v120, v114, v115
v_mul_f32 v120, s65, v120
mav 26,180,50
R 46,1216
v_fma_f32 v118, v118, s65, -v120
mav 30,184,50
v_fma_f32 v119, v119, s65, -v120
mva 34,50,188
v_exp_f32 v118, v118
mav 26,192,54
R 50,1280
mav 30,196,54
v_exp_f32 v119, v119
mva 34,54,200
s_waitcnt lgkmcnt(2)
v_add_f32 v120, v118, v119
v_add_f32 v121, v118, v119
mav 26,204,38
R 54,1344
mav 30,208,38
v_permlane32_swap_b32 v120, v121
mva 34,38,212
v_add_f32 v114, v120, v121
v_add_f32 v115, v120, v121
mav 26,216,42
R 38,1408
mav 30,220,42
v_permlane16_swap_b32 v114, v115
mva 34,42,224
s_waitcnt lgkmcnt(2)
v_add_f32 v120, v114, v115
mav 26,228,46
R 42,1472
mav 30,232,46
v_rcp_f32 v120, v120
mva 34,46,236
v_mul_f32 v118, v118, v120
v_mul_f32 v119, v119, v120
mav 26,240,50
R 46,1536
v_cvt_pk_bf16_f32 v118, v118, v118
mav 30,244,50
v_cvt_pk_bf16_f32 v119, v119, v119
v_and_b32 v114, v118, v96
mva 34,50,248
s_waitcnt lgkmcnt(2)
v_and_b32 v115, v119, v97
v_and_b32 v116, v118, v94
mav 26,252,54
R 50,1600
v_and_b32 v117, v119, v95
mvv 30,128,54
mvv 34,54,132
v_mfma_f32_16x16x32_bf16 v[70:73], v[8:11], v[114:117], 0
v_mfma_f32_16x16x32_bf16 v[74:77], v[108:111], v[114:117], 0
mvv 26,136,38
R 54,1664
mvv 30,140,38
mvv 34,38,144
s_waitcnt lgkmcnt(2)
mvv 26,148,42
R 38,1728
v_cvt_pk_bf16_f32 v114, v70, v71
mvv 30,152,42
v_cvt_pk_bf16_f32 v115, v72, v73
v_cvt_pk_bf16_f32 v116, v74, v75
mvv 34,42,156
v_cvt_pk_bf16_f32 v117, v76, v77
mvv 26,160,46
R 42,1792
mvv 30,164,46
buffer_store_dwordx4 v[114:117], v82, s[36:39], s49 offen sc1
s_add_u32 s49, s49, 313344
mvv 34,46,168
s_waitcnt lgkmcnt(2)
mvv 26,172,50
R 46,1856
mvv 30,176,50
mvv 34,50,180
mvv 26,184,54
R 50,1920
mvv 30,188,54
s_waitcnt lgkmcnt(2)
mvv 34,54,192
mvv 26,196,38
R 54,1984
mvv 30,200,38
mvv 34,38,204
mvv 26,208,42
mvv 30,212,42
ds_read_b64 v[122:123], v83 offset:128
mvv 34,42,216
s_waitcnt lgkmcnt(2)
ds_read_b128 v[70:73], v84 offset:128
mvv 26,220,46
ds_read_b128 v[74:77], v84 offset:144
mvv 30,224,46
mvv 34,46,228
mvv 26,232,50
mvv 30,236,50
mvv 34,50,240
s_waitcnt lgkmcnt(1)
mvv 26,244,54
mvv 30,248,54
mvv 34,54,252
v_mul_f32 v71, v71, v99
s_nop 6
v_fma_f32 v34, v34, v70, v71
v_mul_f32 v73, v73, v99
v_fma_f32 v35, v35, v72, v73
s_waitcnt lgkmcnt(0)
v_mul_f32 v75, v75, v99
v_fma_f32 v36, v36, v74, v75
v_mul_f32 v77, v77, v99
v_fma_f32 v37, v37, v76, v77
v_cvt_pk_bf16_f32 v10, v34, v35
v_cvt_pk_bf16_f32 v11, v36, v37
s_nop 1
v_mfma_f32_16x16x32_bf16 v[70:73], v[100:103], v[0:3], v[86:89]
v_mul_f32 v124, v123, v62
v_fma_f32 v26, v26, v122, v124
v_mul_f32 v125, v123, v63
v_fma_f32 v27, v27, v122, v125
v_mul_f32 v124, v123, v64
v_fma_f32 v28, v28, v122, v124
v_mul_f32 v125, v123, v65
v_fma_f32 v29, v29, v122, v125
v_mul_f32 v124, v123, v66
v_fma_f32 v30, v30, v122, v124
v_mul_f32 v125, v123, v67
v_fma_f32 v31, v31, v122, v125
v_mul_f32 v124, v123, v68
v_fma_f32 v32, v32, v122, v124
v_mul_f32 v125, v123, v69
v_fma_f32 v33, v33, v122, v125
s_barrier
ds_read_b128 v[104:107], v82 offset:4096
ds_read_b64 v[110:111], v82 offset:5120
s_waitcnt lgkmcnt(0)
v_mfma_f32_16x16x32_bf16 v[74:77], v[104:107], v[0:3], v[90:93]
s_nop 7
v_max3_f32 v118, v70, v71, v74
v_max3_f32 v119, v72, v73, v76
v_max_f32 v118, v118, v75
v_max_f32 v119, v119, v77
v_max_f32 v120, v118, v119
v_max_f32 v121, v118, v119
s_nop 1
v_permlane32_swap_b32 v120, v121
v_max_f32 v114, v120, v121
v_max_f32 v115, v120, v121
s_nop 1
v_permlane16_swap_b32 v114, v115
v_max_f32 v120, v114, v115
v_mul_f32 v120, s65, v120
v_fma_f32 v118, v118, s65, -v120
v_fma_f32 v119, v119, s65, -v120
v_exp_f32 v118, v118
v_exp_f32 v119, v119
s_nop 0
v_add_f32 v120, v118, v119
v_add_f32 v121, v118, v119
s_nop 1
v_permlane32_swap_b32 v120, v121
v_add_f32 v114, v120, v121
v_add_f32 v115, v120, v121
s_nop 1
v_permlane16_swap_b32 v114, v115
v_add_f32 v120, v114, v115
v_rcp_f32 v120, v120
s_nop 0
v_mul_f32 v118, v118, v120
v_mul_f32 v119, v119, v120
v_cvt_pk_bf16_f32 v118, v118, v118
v_cvt_pk_bf16_f32 v119, v119, v119
v_and_b32 v114, v118, v94
v_and_b32 v115, v119, v95
v_and_b32 v116, v118, v96
v_and_b32 v117, v119, v97
s_nop 1
v_mfma_f32_16x16x32_bf16 v[70:73], v[8:11], v[114:117], 0
v_mfma_f32_16x16x32_bf16 v[74:77], v[108:111], v[114:117], 0
s_nop 6
v_cvt_pk_bf16_f32 v114, v70, v71
v_cvt_pk_bf16_f32 v115, v72, v73
v_cvt_pk_bf16_f32 v116, v74, v75
v_cvt_pk_bf16_f32 v117, v76, v77
buffer_store_dwordx4 v[114:117], v82, s[36:39], s49 offen sc1
s_add_u32 s49, s49, 313344
v_cvt_pk_bf16_f32 v0, v26, v27
v_cvt_pk_bf16_f32 v1, v28, v29
v_cvt_pk_bf16_f32 v2, v30, v31
v_cvt_pk_bf16_f32 v3, v32, v33
v_add_u32 v78, 1, v78
s_waitcnt vmcnt(1)
buffer_store_dword v78, off, s[36:39], s52 sc1
v_mfma_f32_16x16x32_bf16 v[70:73], v[104:107], v[0:3], v[86:89]
v_min_f32 v74, s56, v90
v_min_f32 v75, s56, v91
v_min_f32 v76, s56, v92
v_min_f32 v77, s56, v93
s_nop 1
mvv 74,4,0
s_nop 7
v_max3_f32 v118, v70, v71, v74
v_max3_f32 v119, v72, v73, v76
v_max_f32 v118, v118, v75
v_max_f32 v119, v119, v77
v_max_f32 v120, v118, v119
v_max_f32 v121, v118, v119
s_nop 1
v_permlane32_swap_b32 v120, v121
v_max_f32 v114, v120, v121
v_max_f32 v115, v120, v121
s_nop 1
v_permlane16_swap_b32 v114, v115
v_max_f32 v120, v114, v115
v_mul_f32 v120, s65, v120
v_fma_f32 v118, v118, s65, -v120
v_fma_f32 v119, v119, s65, -v120
v_exp_f32 v118, v118
v_exp_f32 v119, v119
s_nop 0
v_add_f32 v120, v118, v119
v_add_f32 v121, v118, v119
s_nop 1
v_permlane32_swap_b32 v120, v121
v_add_f32 v114, v120, v121
v_add_f32 v115, v120, v121
s_nop 1
v_permlane16_swap_b32 v114, v115
v_add_f32 v120, v114, v115
v_rcp_f32 v120, v120
s_nop 0
v_mul_f32 v118, v118, v120
v_mul_f32 v119, v119, v120
v_cvt_pk_bf16_f32 v118, v118, v118
v_cvt_pk_bf16_f32 v119, v119, v119
v_and_b32 v114, v118, v94
v_and_b32 v115, v119, v95
v_and_b32 v116, v118, v96
v_and_b32 v117, v119, v97
s_nop 1
v_mfma_f32_16x16x32_bf16 v[70:73], v[10:13], v[114:117], 0
v_mfma_f32_16x16x32_bf16 v[74:77], v[110:113], v[114:117], 0
s_nop 6
v_cvt_pk_bf16_f32 v114, v70, v71
v_cvt_pk_bf16_f32 v115, v72, v73
v_cvt_pk_bf16_f32 v116, v74, v75
v_cvt_pk_bf16_f32 v117, v76, v77
buffer_store_dwordx4 v[114:117], v82, s[36:39], s49 offen sc1
v_add_u32 v78, 1, v78
s_waitcnt vmcnt(2)
buffer_store_dword v78, off, s[36:39], s52 sc1
v_add_u32 v78, 37, v78
s_waitcnt vmcnt(1)
buffer_store_dword v78, off, s[36:39], s52 sc1
s_endpgm
Lr1:
s_cmp_eq_u32 s60, 1
s_cbranch_scc0 Lr2
s_lshr_b32 s66, s53, 10
s_mul_i32 s66, s66, 8208
s_mov_b32 s67, 0x8000
s_mov_b64 exec, 0
s_cmp_eq_u32 s53, 1024
s_cbranch_scc1 LsQ1
s_branch LsQ3
LwQB:
s_mov_b64 exec, -1
E 0,86016
E 1,90112
E 2,94208
s_add_u32 s51, s51, 196608
E 0,98304
E 1,102400
E 2,106496
s_add_u32 s51, s51, 196608
E 0,61440
E 1,65536
s_add_u32 m0, s66, 2816
s_nop 0
buffer_load_dwordx4 v3, s[44:47], s67 offen offset:256 lds
E 2,69632
s_add_u32 s51, s51, 196608
E 0,73728
s_add_u32 m0, s66, 6912
s_nop 0
buffer_load_dwordx4 v3, s[44:47], 0 offen offset:256 lds
E 1,77824
E 2,81920
s_add_u32 s51, s51, 196608
v_lshrrev_b32 v12, 2, v7
v_and_b32 v12, 0xc0, v12
v_xor_b32 v13, 0, v12
v_add_u32 v8, v7, v13
v_xor_b32 v13, 64, v12
v_add_u32 v9, v7, v13
v_xor_b32 v13, 128, v12
v_add_u32 v10, v7, v13
v_xor_b32 v13, 192, v12
v_add_u32 v11, v7, v13
v_mov_b32 v112, s64
v_mov_b32 v113, s64
v_mov_b32 v114, s64
v_mov_b32 v115, s64
s_mov_b32 s92, 0xaaaaaaaa
s_mov_b32 s93, s92
s_mov_b32 s94, 0xcccccccc
s_mov_b32 s95, s94
s_mov_b32 s96, 0xf0000f
s_mov_b32 s97, 0xf0000f00
s_waitcnt vmcnt(14)
s_barrier
v_add_u32 v4, 69632, v4
v_add_u32 v5, 69632, v5
v_add_u32 v6, 69632, v6
Ta 0,4,40960
Ta 4,5,40960
Ta 8,6,40960
Ta 12,4,53248
Ta 16,5,53248
Ta 20,6,53248
ds_read_b128 v[46:49], v8 offset:0
ds_read_b128 v[50:53], v9 offset:0
ds_read_b128 v[54:57], v8 offset:4096
ds_read_b128 v[58:61], v9 offset:4096
v_accvgpr_read_b32 v117, a255
v_mov_b32 v116, 0x100
v_add_u32 v117, 0x9e3779b9, v117
buffer_store_dwordx2 v[116:117], v82, s[36:39], s52 offen sc1
v_add_u32 v93, v93, v116
E 0,36864
E 1,40960
E 2,45056
s_add_u32 s51, s51, 196608
E 0,49152
E 1,53248
E 2,57344
s_add_u32 s51, s51, 196608
s_waitcnt lgkmcnt(0)
s_waitcnt vmcnt(15)
s_barrier
E 0,110592
Ta 24,4,16384
v_mfma_f32_16x16x32_bf16 v[108:111], v[112:115], v[54:57], 0
v_mfma_f32_16x16x32_bf16 v[18:21], v[54:57], v[54:57], 0
Ta 28,5,16384
mvv 108,112,58
Ta 32,6,16384
E 1,114688
mvv 18,58,58
Ta 36,4,28672
v_mfma_f32_16x16x32_bf16 v[96:99], a[0:3], v[112:115], 0
v_mfma_f32_16x16x32_bf16 v[100:103], a[4:7], v[112:115], 0
Ta 40,5,28672
s_add_u32 m0, s66, 3584
s_nop 0
buffer_load_dwordx4 v3, s[44:47], s67 offen offset:512 lds
v_mfma_f32_16x16x32_bf16 v[104:107], v[112:115], a[8:11], 0
Ta 44,6,28672
v_mfma_f32_16x16x32_bf16 v[22:25], a[0:3], v[46:49], 0
ds_read_b128 v[62:65], v10 offset:0
v_mfma_f32_16x16x32_bf16 v[26:29], a[4:7], v[46:49], 0
E 2,118784
s_add_u32 s51, s51, 196608
v_mfma_f32_16x16x32_bf16 v[30:33], v[46:49], a[8:11], 0
ds_read_b128 v[66:69], v11 offset:0
v_mfma_f32_16x16x32_bf16 v[34:37], a[0:3], v[54:57], 0
ds_read_b128 v[70:73], v10 offset:4096
E 0,122880
v_mfma_f32_16x16x32_bf16 v[38:41], a[4:7], v[54:57], 0
ds_read_b128 v[74:77], v11 offset:4096
v_mfma_f32_16x16x32_bf16 v[42:45], v[54:57], a[8:11], 0
mav 96,12,112
s_add_u32 m0, s66, 7680
s_nop 0
buffer_load_dwordx4 v3, s[44:47], 0 offen offset:512 lds
mav 100,16,112
mva 104,112,20
mav 22,12,50
E 1,126976
mav 26,16,50
mva 30,50,20
mav 34,12,58
E 2,131072
s_add_u32 s51, s51, 196608
mav 38,16,58
mva 42,58,20
s_waitcnt lgkmcnt(0)
s_waitcnt vmcnt(15)
s_barrier
v_add_u32 v4, -49152, v4
v_add_u32 v5, -49152, v5
v_add_u32 v6, -49152, v6
E 0,86016
Ta 48,4,40960
mvv 108,112,70
mvv 18,70,70
Ta 52,5,40960
mvv 108,112,74
Ta 56,6,40960
mvv 18,74,74
E 1,90112
Ta 60,4,53248
mav 96,24,112
mav 100,28,112
Ta 64,5,53248
mva 104,112,32
Ta 68,6,53248
mav 22,24,62
E 2,94208
s_add_u32 s51, s51, 196608
ds_read_b128 v[46:49], v8 offset:1024
mav 26,28,62
mva 30,62,32
ds_read_b128 v[50:53], v9 offset:1024
mav 34,24,70
ds_read_b128 v[54:57], v8 offset:5120
E 0,98304
mav 38,28,70
ds_read_b128 v[58:61], v9 offset:5120
mva 42,70,32
mav 96,36,112
mav 100,40,112
E 1,102400
mva 104,112,44
mav 22,36,66
mav 26,40,66
mva 30,66,44
E 2,106496
s_add_u32 s51, s51, 196608
mav 34,36,74
mav 38,40,74
LsQ14:
s_cbranch_execz LsQ18
mva 42,74,44
s_waitcnt lgkmcnt(0)
s_waitcnt vmcnt(14)
s_barrier
E 0,61440
Ta 72,4,16384
mvv 108,112,54
mvv 18,54,54
Ta 76,5,16384
mvv 108,112,58
Ta 80,6,16384
E 1,65536
mvv 18,58,58
Ta 84,4,28672
mav 96,48,112
mav 100,52,112
Ta 88,5,28672
s_add_u32 m0, s66, 4352
s_nop 0
buffer_load_dwordx4 v3, s[44:47], s67 offen offset:768 lds
mva 104,112,56
Ta 92,6,28672
mav 22,48,46
ds_read_b128 v[62:65], v10 offset:1024
mav 26,52,46
E 2,69632
s_add_u32 s51, s51, 196608
mva 30,46,56
ds_read_b128 v[66:69], v11 offset:1024
mav 34,48,54
ds_read_b128 v[70:73], v10 offset:5120
E 0,73728
mav 38,52,54
ds_read_b128 v[74:77], v11 offset:5120
mva 42,54,56
mav 96,60,112
s_add_u32 m0, s66, 8448
s_nop 0
buffer_load_dwordx4 v3, s[44:47], 0 offen offset:768 lds
mav 100,64,112
mva 104,112,68
mav 22,60,50
E 1,77824
mav 26,64,50
mva 30,50,68
mav 34,60,58
E 2,81920
s_add_u32 s51, s51, 196608
mav 38,64,58
mva 42,58,68
s_waitcnt lgkmcnt(0)
s_waitcnt vmcnt(14)
s_barrier
v_add_u32 v4, 49152, v4
v_add_u32 v5, 49152, v5
v_add_u32 v6, 49152, v6
E 0,36864
Ta 96,4,40960
mvv 108,112,70
mvv 18,70,70
Ta 100,5,40960
mvv 108,112,74
Ta 104,6,40960
mvv 18,74,74
E 1,40960
Ta 108,4,53248
mav 96,72,112
mav 100,76,112
Ta 112,5,53248
mva 104,112,80
Ta 116,6,53248
mav 22,72,62
E 2,45056
s_add_u32 s51, s51, 196608
ds_read_b128 v[46:49], v8 offset:2048
mav 26,76,62
mva 30,62,80
ds_read_b128 v[50:53], v9 offset:2048
mav 34,72,70
ds_read_b128 v[54:57], v8 offset:6144
E 0,49152
mav 38,76,70
ds_read_b128 v[58:61], v9 offset:6144
mva 42,70,80
mav 96,84,112
mav 100,88,112
E 1,53248
mva 104,112,92
mav 22,84,66
mav 26,88,66
mva 30,66,92
E 2,57344
s_add_u32 s51, s51, 196608
mav 34,84,74
mav 38,88,74
mva 42,74,92
s_waitcnt lgkmcnt(0)
s_waitcnt vmcnt(14)
s_barrier
E 0,110592
Ta 120,4,16384
mvv 108,112,54
mvv 18,54,54
Ta 124,5,16384
mvv 108,112,58
Ta 128,6,16384
E 1,114688
mvv 18,58,58
Ta 132,4,28672
mav 96,96,112
mav 100,100,112
Ta 136,5,28672
s_add_u32 m0, s66, 1024
s_nop 0
buffer_load_dwordx4 v3, s[44:47], s67 offen offset:1024 lds
mva 104,112,104
Ta 140,6,28672
mav 22,96,46
ds_read_b128 v[62:65], v10 offset:2048
mav 26,100,46
E 2,118784
s_add_u32 s51, s51, 196608
mva 30,46,104
ds_read_b128 v[66:69], v11 offset:2048
mav 34,96,54
ds_read_b128 v[70:73], v10 offset:6144
E 0,122880
mav 38,100,54
ds_read_b128 v[74:77], v11 offset:6144
mva 42,54,104
mav 96,108,112
s_add_u32 m0, s66, 5120
s_nop 0
buffer_load_dwordx4 v3, s[44:47], 0 offen offset:1024 lds
mav 100,112,112
mva 104,112,116
mav 22,108,50
E 1,126976
mav 26,112,50
mva 30,50,116
mav 34,108,58
E 2,131072
s_add_u32 s51, s51, 196608
mav 38,112,58
mva 42,58,116
s_waitcnt lgkmcnt(0)
s_waitcnt vmcnt(14)
s_barrier
v_add_u32 v4, -49152, v4
v_add_u32 v5, -49152, v5
v_add_u32 v6, -49152, v6
E 0,86016
Ta 144,4,40960
mvv 108,112,70
mvv 18,70,70
Ta 148,5,40960
mvv 108,112,74
Ta 152,6,40960
mvv 18,74,74
E 1,90112
Ta 156,4,53248
mav 96,120,112
mav 100,124,112
Ta 160,5,53248
mva 104,112,128
Ta 164,6,53248
mav 22,120,62
E 2,94208
s_add_u32 s51, s51, 196608
ds_read_b128 v[46:49], v8 offset:3072
mav 26,124,62
mva 30,62,128
ds_read_b128 v[50:53], v9 offset:3072
mav 34,120,70
ds_read_b128 v[54:57], v8 offset:7168
E 0,98304
mav 38,124,70
ds_read_b128 v[58:61], v9 offset:7168
mva 42,70,128
mav 96,132,112
mav 100,136,112
E 1,102400
mva 104,112,140
mav 22,132,66
mav 26,136,66
mva 30,66,140
E 2,106496
s_add_u32 s51, s51, 196608
mav 34,132,74
mav 38,136,74
mva 42,74,140
s_waitcnt lgkmcnt(0)
s_waitcnt vmcnt(14)
s_barrier
E 0,61440
Ta 168,4,16384
mvv 108,112,54
mvv 18,54,54
Ta 172,5,16384
mvv 108,112,58
Ta 176,6,16384
E 1,65536
mvv 18,58,58
Ta 180,4,28672
mav 96,144,112
mav 100,148,112
Ta 184,5,28672
s_add_u32 m0, s66, 1792
s_nop 0
buffer_load_dwordx4 v3, s[44:47], s67 offen offset:1280 lds
mva 104,112,152
Ta 188,6,28672
mav 22,144,46
ds_read_b128 v[62:65], v10 offset:3072
mav 26,148,46
E 2,69632
s_add_u32 s51, s51, 196608
mva 30,46,152
ds_read_b128 v[66:69], v11 offset:3072
mav 34,144,54
ds_read_b128 v[70:73], v10 offset:7168
E 0,73728
mav 38,148,54
ds_read_b128 v[74:77], v11 offset:7168
mva 42,54,152
mav 96,156,112
s_add_u32 m0, s66, 5888
s_nop 0
buffer_load_dwordx4 v3, s[44:47], 0 offen offset:1280 lds
mav 100,160,112
mva 104,112,164
mav 22,156,50
E 1,77824
mav 26,160,50
mva 30,50,164
mav 34,156,58
E 2,81920
s_add_u32 s51, s51, 196608
mav 38,160,58
mva 42,58,164
s_waitcnt lgkmcnt(0)
s_waitcnt vmcnt(14)
s_barrier
v_add_u32 v4, 49152, v4
v_add_u32 v5, 49152, v5
v_add_u32 v6, 49152, v6
E 0,36864
Ta 192,4,40960
mvv 108,112,70
mvv 18,70,70
Ta 196,5,40960
mvv 108,112,74
Ta 200,6,40960
mvv 18,74,74
E 1,40960
Ta 204,4,53248
mav 96,168,112
mav 100,172,112
Ta 208,5,53248
mva 104,112,176
Ta 212,6,53248
mav 22,168,62
E 2,45056
s_add_u32 s51, s51, 196608
ds_read_b128 v[46:49], v8 offset:0
mav 26,172,62
mva 30,62,176
ds_read_b128 v[50:53], v9 offset:0
mav 34,168,70
ds_read_b128 v[54:57], v8 offset:4096
E 0,49152
mav 38,172,70
ds_read_b128 v[58:61], v9 offset:4096
mva 42,70,176
mav 96,180,112
mav 100,184,112
E 1,53248
mva 104,112,188
mav 22,180,66
mav 26,184,66
mva 30,66,188
E 2,57344
s_add_u32 s51, s51, 196608
mav 34,180,74
mav 38,184,74
mva 42,74,188
s_waitcnt lgkmcnt(0)
s_waitcnt vmcnt(14)
s_barrier
E 0,110592
Ta 216,4,16384
mvv 108,112,54
mvv 18,54,54
Ta 220,5,16384
mvv 108,112,58
Ta 224,6,16384
E 1,114688
mvv 18,58,58
Ta 228,4,28672
mav 96,192,112
mav 100,196,112
Ta 232,5,28672
s_add_u32 m0, s66, 2560
s_nop 0
buffer_load_dwordx4 v3, s[44:47], s67 offen offset:1536 lds
mva 104,112,200
Ta 236,6,28672
mav 22,192,46
ds_read_b128 v[62:65], v10 offset:0
mav 26,196,46
E 2,118784
s_add_u32 s51, s51, 196608
mva 30,46,200
ds_read_b128 v[66:69], v11 offset:0
mav 34,192,54
ds_read_b128 v[70:73], v10 offset:4096
E 0,122880
mav 38,196,54
ds_read_b128 v[74:77], v11 offset:4096
mva 42,54,200
mav 96,204,112
s_add_u32 m0, s66, 6656
s_nop 0
buffer_load_dwordx4 v3, s[44:47], 0 offen offset:1536 lds
mav 100,208,112
mva 104,112,212
mav 22,204,50
E 1,126976
mav 26,208,50
mva 30,50,212
mav 34,204,58
E 2,131072
s_add_u32 s51, s51, 196608
mav 38,208,58
mva 42,58,212
s_waitcnt lgkmcnt(0)
s_waitcnt vmcnt(14)
s_barrier
v_add_u32 v4, -49152, v4
v_add_u32 v5, -49152, v5
v_add_u32 v6, -49152, v6
E 0,86016
Ta 240,4,40960
mvv 108,112,70
mvv 18,70,70
Ta 244,5,40960
mvv 108,112,74
Ta 248,6,40960
mvv 18,74,74
E 1,90112
Ta 252,4,53248
mav 96,216,112
mav 100,220,112
Tv 128,5,53248
mva 104,112,224
Tv 132,6,53248
mav 22,216,62
E 2,94208
s_add_u32 s51, s51, 196608
ds_read_b128 v[46:49], v8 offset:1024
mav 26,220,62
mva 30,62,224
ds_read_b128 v[50:53], v9 offset:1024
mav 34,216,70
ds_read_b128 v[54:57], v8 offset:5120
E 0,98304
mav 38,220,70
ds_read_b128 v[58:61], v9 offset:5120
mva 42,70,224
mav 96,228,112
mav 100,232,112
E 1,102400
mva 104,112,236
mav 22,228,66
mav 26,232,66
mva 30,66,236
E 2,106496
s_add_u32 s51, s51, 196608
mav 34,228,74
mav 38,232,74
mva 42,74,236
s_waitcnt lgkmcnt(0)
s_waitcnt vmcnt(14)
s_barrier
E 0,61440
Tv 136,4,16384
mvv 108,112,54
mvv 18,54,54
Tv 140,5,16384
mvv 108,112,58
Tv 144,6,16384
E 1,65536
mvv 18,58,58
Tv 148,4,28672
mav 96,240,112
mav 100,244,112
Tv 152,5,28672
s_add_u32 m0, s66, 3328
s_nop 0
buffer_load_dwordx4 v3, s[44:47], s67 offen offset:1792 lds
mva 104,112,248
Tv 156,6,28672
LsQ15:
s_cbranch_execz LsQ19
mav 22,240,46
ds_read_b128 v[62:65], v10 offset:1024
mav 26,244,46
E 2,69632
s_add_u32 s51, s51, 196608
mva 30,46,248
ds_read_b128 v[66:69], v11 offset:1024
mav 34,240,54
ds_read_b128 v[70:73], v10 offset:5120
E 0,73728
mav 38,244,54
ds_read_b128 v[74:77], v11 offset:5120
mva 42,54,248
mav 96,252,112
s_add_u32 m0, s66, 7424
s_nop 0
buffer_load_dwordx4 v3, s[44:47], 0 offen offset:1792 lds
mvv 100,128,112
mvv 104,112,132
mav 22,252,50
E 1,77824
mvv 26,128,50
mvv 30,50,132
mav 34,252,58
E 2,81920
s_add_u32 s51, s51, 196608
mvv 38,128,58
mvv 42,58,132
s_waitcnt lgkmcnt(0)
s_waitcnt vmcnt(14)
s_barrier
v_add_u32 v4, 49152, v4
v_add_u32 v5, 49152, v5
v_add_u32 v6, 49152, v6
E 0,36864
Tv 160,4,40960
mvv 108,112,70
mvv 18,70,70
Tv 164,5,40960
mvv 108,112,74
Tv 168,6,40960
mvv 18,74,74
E 1,40960
Tv 172,4,53248
mvv 96,136,112
mvv 100,140,112
Tv 176,5,53248
mvv 104,112,144
E 2,45056
s_add_u32 s51, s51, 196608
Tv 180,6,53248
mvv 22,136,62
ds_read_b128 v[46:49], v8 offset:2048
mvv 26,140,62
mvv 30,62,144
ds_read_b128 v[50:53], v9 offset:2048
E 0,49152
mvv 34,136,70
ds_read_b128 v[54:57], v8 offset:6144
mvv 38,140,70
ds_read_b128 v[58:61], v9 offset:6144
mvv 42,70,144
E 1,53248
mvv 96,148,112
mvv 100,152,112
mvv 104,112,156
E 2,57344
s_add_u32 s51, s51, 196608
mvv 22,148,66
mvv 26,152,66
mvv 30,66,156
buffer_load_dwordx2 v[122:123], v92, s[36:39], 0 offen sc1
mvv 34,148,74
mvv 38,152,74
mvv 42,74,156
s_waitcnt lgkmcnt(0)
s_waitcnt vmcnt(15)
s_barrier
s_add_u32 m0, s50, 49536
s_nop 0
D2 89,54,0
Tv 184,4,16384
mvv 108,112,54
mvv 18,54,54
Tv 188,5,16384
mvv 108,112,58
Tv 192,6,16384
D2 89,54,1024
mvv 18,58,58
Tv 196,4,28672
mvv 96,160,112
mvv 100,164,112
Tv 200,5,28672
D2 89,55,2048
mvv 104,112,168
Tv 204,6,28672
mvv 22,160,46
ds_read_b128 v[62:65], v10 offset:2048
mvv 26,164,46
D2 89,55,3072
mvv 30,46,168
ds_read_b128 v[66:69], v11 offset:2048
mvv 34,160,54
ds_read_b128 v[70:73], v10 offset:6144
s_add_u32 m0, s50, 57792
s_nop 0
D2 90,54,0
mvv 38,164,54
ds_read_b128 v[74:77], v11 offset:6144
mvv 42,54,168
mvv 96,172,112
D2 90,54,1024
mvv 100,176,112
mvv 104,112,180
mvv 22,172,50
D2 90,55,2048
mvv 26,176,50
mvv 30,50,180
mvv 34,172,58
D2 90,55,3072
mvv 38,176,58
mvv 42,58,180
s_waitcnt lgkmcnt(0)
s_waitcnt vmcnt(15)
s_barrier
v_add_u32 v4, -49152, v4
v_add_u32 v5, -49152, v5
v_add_u32 v6, -49152, v6
s_add_u32 m0, s50, 33024
s_nop 0
D2 87,54,0
Tv 208,4,40960
mvv 108,112,70
mvv 18,70,70
Tv 212,5,40960
mvv 108,112,74
Tv 216,6,40960
D2 87,54,1024
mvv 18,74,74
Tv 220,4,53248
mvv 96,184,112
mvv 100,188,112
Tv 224,5,53248
D2 87,55,2048
mvv 104,112,192
Tv 228,6,53248
mvv 22,184,62
ds_read_b128 v[46:49], v8 offset:3072
mvv 26,188,62
D2 87,55,3072
mvv 30,62,192
ds_read_b128 v[50:53], v9 offset:3072
mvv 34,184,70
ds_read_b128 v[54:57], v8 offset:7168
s_add_u32 m0, s50, 41280
s_nop 0
D2 88,54,0
mvv 38,188,70
ds_read_b128 v[58:61], v9 offset:7168
mvv 42,70,192
mvv 96,196,112
D2 88,54,1024
mvv 100,200,112
mvv 104,112,204
mvv 22,196,66
D2 88,55,2048
mvv 26,200,66
mvv 30,66,204
mvv 34,196,74
D2 88,55,3072
mvv 38,200,74
mvv 42,74,204
s_waitcnt lgkmcnt(0)
s_waitcnt vmcnt(17)
s_barrier
Tv 232,4,16384
Tv 236,5,16384
Tv 240,6,16384
Tv 244,4,28672
Tv 248,5,28672
Tv 252,6,28672
ds_read_b128 v[62:65], v10 offset:3072
ds_read_b128 v[66:69], v11 offset:3072
ds_read_b128 v[70:73], v10 offset:7168
ds_read_b128 v[74:77], v11 offset:7168
s_lshr_b32 s60, s53, 11
s_mul_i32 s60, s60, 313344
s_add_u32 s60, s60, 626688
s_add_u32 s60, s60, s49
s_sub_u32 s54, s54, 0x8000
s_sub_u32 s55, s55, 0x8000
s_waitcnt vmcnt(16)
ds_write_b32 v83, v122
v_sub_u32 v122, v122, v93
v_xor_b32 v123, v123, v117
v_cmp_lt_u64 vcc, s[62:63], v[122:123]
s_cbranch_vccnz Cq
Bq:
v_add_u32 v93, 1, v93
buffer_load_dwordx2 v[14:15], v94, s[36:39], s60 offen sc1
mvv 108,112,54
mvv 18,54,54
mvv 108,112,58
s_add_u32 m0, s50, 0
s_nop 0
D2 87,54,0
mvv 18,58,58
mvv 96,208,112
D2 87,54,1024
mvv 100,212,112
mvv 104,112,216
mvv 22,208,46
D2 87,55,2048
mvv 26,212,46
mvv 30,46,216
D2 87,55,3072
mvv 34,208,54
mvv 38,212,54
mvv 42,54,216
s_add_u32 m0, s50, 8256
s_nop 0
D2 88,54,0
mvv 96,220,112
mvv 100,224,112
D2 88,54,1024
mvv 104,112,228
mvv 22,220,50
mvv 26,224,50
D2 88,55,2048
mvv 30,50,228
mvv 34,220,58
D2 88,55,3072
mvv 38,224,58
mvv 42,58,228
s_waitcnt lgkmcnt(0)
s_add_u32 m0, s50, 16512
s_nop 0
D2 89,54,0
mvv 108,112,70
mvv 18,70,70
mvv 108,112,74
D2 89,54,1024
mvv 18,74,74
mvv 96,232,112
mvv 100,236,112
D2 89,55,2048
mvv 104,112,240
mvv 22,232,62
)"
R"(
mvv 26,236,62
D2 89,55,3072
mvv 30,62,240
mvv 34,232,70
s_add_u32 m0, s50, 24768
s_nop 0
D2 90,54,0
mvv 38,236,70
mvv 42,70,240
v_cndmask_b32 v18, v18, v19, s[92:93]
mvv 96,244,112
v_cndmask_b32 v20, v20, v21, s[92:93]
D2 90,54,1024
mvv 100,248,112
v_mul_f32 v108, 0x3a800000, v108
v_cndmask_b32 v18, v18, v20, s[94:95]
mvv 104,112,252
v_mul_f32 v18, 0x3a800000, v18
mvv 22,244,66
D2 90,55,2048
v_fma_f32 v18, -v108, v108, v18
mvv 26,248,66
v_max_f32 v18, 0, v18
mvv 30,66,252
v_add_f32 v18, 0x3727c5ac, v18
mvv 34,244,74
D2 90,55,3072
v_rsq_f32 v18, v18
mvv 38,248,74
s_nop 0
v_mul_f32_e64 v19, -v108, v18
mvv 42,74,252
s_mov_b64 exec, s[96:97]
ds_write_b64 v85, v[18:19] offset:128
s_mov_b64 exec, -1
s_waitcnt lgkmcnt(0)
s_barrier
s_nop 7
v_mov_b32 v95, v104
s_add_u32 s40, s36, 4194304
s_addc_u32 s41, s37, 0
s_and_b32 s60, s48, 3
s_cmp_eq_u32 s60, 1
s_cbranch_scc1 LvQB1
LvQB0:
LsQ16:
s_cbranch_execz LsQ20
ds_read_b64 v[118:119], v85 offset:0
ds_read_b128 v[104:107], v86 offset:0
ds_read_b128 v[108:111], v86 offset:16
s_waitcnt lgkmcnt(0)
v_mul_f32 v120, v119, v96
v_fma_f32 v22, v22, v118, v120
v_mul_f32 v121, v119, v97
v_fma_f32 v23, v23, v118, v121
v_mul_f32 v120, v119, v98
v_fma_f32 v24, v24, v118, v120
v_mul_f32 v121, v119, v99
v_fma_f32 v25, v25, v118, v121
v_mul_f32 v120, v119, v100
v_fma_f32 v26, v26, v118, v120
v_mul_f32 v121, v119, v101
v_fma_f32 v27, v27, v118, v121
v_mul_f32 v120, v119, v102
v_fma_f32 v28, v28, v118, v120
v_mul_f32 v121, v119, v103
v_fma_f32 v29, v29, v118, v121
v_cvt_pk_bf16_f32 v112, v22, v23
v_cvt_pk_bf16_f32 v113, v24, v25
v_cvt_pk_bf16_f32 v114, v26, v27
v_cvt_pk_bf16_f32 v115, v28, v29
ds_write_b128 v84, v[112:115] offset:0
v_mul_f32 v105, v105, v95
v_fma_f32 v30, v30, v104, v105
v_mul_f32 v107, v107, v95
v_fma_f32 v31, v31, v106, v107
v_mul_f32 v109, v109, v95
v_fma_f32 v32, v32, v108, v109
v_mul_f32 v111, v111, v95
v_fma_f32 v33, v33, v110, v111
v_cvt_pk_bf16_f32 v78, v30, v31
v_cvt_pk_bf16_f32 v79, v32, v33
ds_write_b64 v84, v[78:79] offset:1024
s_xor_b32 s50, s50, s57
s_waitcnt vmcnt(17) lgkmcnt(0)
s_barrier
R 46,33024
R 50,33088
R 54,33152
R 58,33216
s_waitcnt vmcnt(16)
v_permlane32_swap_b32 v14, v15
v_add_f32 v16, v14, v15
v_add_f32 v17, v14, v15
s_nop 1
v_permlane16_swap_b32 v16, v17
v_add_f32 v14, v16, v17
v_add_f32 v15, v16, v17
s_nop 1
v_permlane32_swap_b32 v14, v15
v_mul_f32 v16, 0x3a800000, v14
v_mul_f32 v17, 0x3a800000, v15
v_fma_f32 v17, -v16, v16, v17
v_max_f32 v17, 0, v17
v_add_f32 v17, 0x3727c5ac, v17
v_rsq_f32 v14, v17
s_nop 0
v_mul_f32_e64 v15, -v16, v14
ds_write_b64 v85, v[14:15] offset:0
ds_read_b64 v[118:119], v85 offset:128
ds_read_b128 v[104:107], v86 offset:128
ds_read_b128 v[108:111], v86 offset:144
s_waitcnt lgkmcnt(0)
v_mfma_f32_16x16x32_bf16 v[22:25], a[0:3], v[46:49], 0
R 62,33280
v_mul_f32 v120, v119, v96
v_mfma_f32_16x16x32_bf16 v[26:29], a[4:7], v[46:49], 0
v_fma_f32 v34, v34, v118, v120
v_mul_f32 v121, v119, v97
v_mfma_f32_16x16x32_bf16 v[30:33], v[46:49], a[8:11], 0
v_fma_f32 v35, v35, v118, v121
v_mul_f32 v120, v119, v98
mav 22,12,50
R 66,33344
v_fma_f32 v36, v36, v118, v120
mav 26,16,50
v_mul_f32 v121, v119, v99
v_fma_f32 v37, v37, v118, v121
mva 30,50,20
v_mul_f32 v120, v119, v100
v_fma_f32 v38, v38, v118, v120
mav 22,24,54
R 70,33408
v_mul_f32 v121, v119, v101
mav 26,28,54
v_fma_f32 v39, v39, v118, v121
v_mul_f32 v120, v119, v102
mva 30,54,32
v_fma_f32 v40, v40, v118, v120
v_mul_f32 v121, v119, v103
mav 22,36,58
R 74,33472
v_fma_f32 v41, v41, v118, v121
mav 26,40,58
v_cvt_pk_bf16_f32 v112, v34, v35
v_cvt_pk_bf16_f32 v113, v36, v37
mva 30,58,44
s_waitcnt lgkmcnt(2)
v_cvt_pk_bf16_f32 v114, v38, v39
v_cvt_pk_bf16_f32 v115, v40, v41
mav 22,48,62
R 46,33536
v_mul_f32 v105, v105, v95
mav 26,52,62
ds_write_b128 v84, v[112:115] offset:4096
mva 30,62,56
v_fma_f32 v42, v42, v104, v105
v_mul_f32 v107, v107, v95
mav 22,60,66
R 50,33600
v_fma_f32 v43, v43, v106, v107
mav 26,64,66
v_mul_f32 v109, v109, v95
v_fma_f32 v44, v44, v108, v109
mva 30,66,68
s_waitcnt lgkmcnt(3)
v_mul_f32 v111, v111, v95
v_fma_f32 v45, v45, v110, v111
mav 22,72,70
R 54,33664
v_cvt_pk_bf16_f32 v78, v42, v43
mav 26,76,70
v_cvt_pk_bf16_f32 v79, v44, v45
mva 30,70,80
ds_write_b64 v84, v[78:79] offset:5120
mav 22,84,74
R 58,33728
mav 26,88,74
mva 30,74,92
s_waitcnt lgkmcnt(3)
mav 22,96,46
s_waitcnt vmcnt(0) lgkmcnt(1)
R 62,33792
s_mov_b32 m0, 144384
mav 26,100,46
s_barrier
mva 30,46,104
buffer_load_dword v91, s[36:39], 0 offen sc1 lds
mav 22,108,50
s_sub_u32 s54, s54, 0x8000
R 66,33856
s_add_u32 m0, s50, 33024
mav 26,112,50
s_cmp_lt_u32 s54, s56
s_cselect_b64 s[44:45], s[40:41], s[44:45]
mva 30,50,116
mav 22,120,54
D2 87,54,0
mav 26,124,54
s_add_u32 s55, s54, 14336
R 70,33920
mva 30,54,128
s_waitcnt lgkmcnt(2)
D2 87,54,1024
mav 22,132,58
R 74,33984
mav 26,136,58
D2 87,55,2048
mva 30,58,140
mav 22,144,62
D2 87,55,3072
mav 26,148,62
s_add_u32 m0, s50, 41280
R 46,34048
mva 30,62,152
s_waitcnt lgkmcnt(2)
D2 88,54,0
mav 22,156,66
R 50,34112
mav 26,160,66
D2 88,54,1024
mva 30,66,164
mav 22,168,70
D2 88,55,2048
mav 26,172,70
R 54,34176
mva 30,70,176
s_waitcnt lgkmcnt(2)
D2 88,55,3072
mav 22,180,74
s_add_u32 m0, s50, 49536
R 58,34240
mav 26,184,74
D2 89,54,0
mva 30,74,188
mav 22,192,46
D2 89,54,1024
mav 26,196,46
R 62,34304
mva 30,46,200
s_waitcnt lgkmcnt(2)
D2 89,55,2048
mav 22,204,50
R 66,34368
mav 26,208,50
D2 89,55,3072
mva 30,50,212
s_add_u32 m0, s50, 57792
mav 22,216,54
D2 90,54,0
mav 26,220,54
R 70,34432
mva 30,54,224
s_waitcnt lgkmcnt(2)
D2 90,54,1024
mav 22,228,58
R 74,34496
mav 26,232,58
D2 90,55,2048
mva 30,58,236
s_sub_u32 s54, s54, 0x8000
mav 22,240,62
D2 90,55,3072
mav 26,244,62
s_add_u32 m0, s50, 0
R 46,34560
mva 30,62,248
s_waitcnt lgkmcnt(2)
D2 87,54,0
mav 22,252,66
R 50,34624
mvv 26,128,66
D2 87,54,1024
mvv 30,66,132
s_add_u32 s55, s54, 14336
mvv 22,136,70
D2 87,55,2048
LsQ17:
s_cbranch_execz LsQ21
mvv 26,140,70
R 54,34688
mvv 30,70,144
s_waitcnt lgkmcnt(2)
D2 87,55,3072
mvv 22,148,74
s_add_u32 m0, s50, 8256
R 58,34752
mvv 26,152,74
D2 88,54,0
mvv 30,74,156
mvv 22,160,46
D2 88,54,1024
mvv 26,164,46
R 62,34816
mvv 30,46,168
s_waitcnt lgkmcnt(2)
D2 88,55,2048
mvv 22,172,50
R 66,34880
mvv 26,176,50
D2 88,55,3072
mvv 30,50,180
s_add_u32 m0, s50, 16512
mvv 22,184,54
D2 89,54,0
mvv 26,188,54
R 70,34944
mvv 30,54,192
s_waitcnt lgkmcnt(2)
D2 89,54,1024
mvv 22,196,58
R 74,35008
mvv 26,200,58
D2 89,55,2048
mvv 30,58,204
mvv 22,208,62
D2 89,55,3072
mvv 26,212,62
s_add_u32 m0, s50, 24768
R 46,0
mvv 30,62,216
s_waitcnt lgkmcnt(2)
D2 90,54,0
mvv 22,220,66
ds_read_b32 v122, v83 offset:0
mvv 26,224,66
D2 90,54,1024
mvv 30,66,228
R 50,64
mvv 22,232,70
D2 90,55,2048
mvv 26,236,70
R 54,128
mvv 30,70,240
s_waitcnt lgkmcnt(3)
D2 90,55,3072
mvv 22,244,74
R 58,192
mvv 26,248,74
s_waitcnt lgkmcnt(3)
v_cmp_gt_i32 vcc, v93, v122
mvv 30,74,252
ds_read_b64 v[118:119], v85 offset:0
v_mfma_f32_16x16x32_bf16 v[34:37], a[0:3], v[46:49], 0
s_cbranch_vccnz CQB0_1
BQB0_1:
v_mfma_f32_16x16x32_bf16 v[38:41], a[4:7], v[46:49], 0
R 62,256
v_mfma_f32_16x16x32_bf16 v[42:45], v[46:49], a[8:11], 0
s_waitcnt lgkmcnt(3)
ds_read_b128 v[104:107], v86 offset:0
mav 34,12,50
R 66,320
mav 38,16,50
ds_read_b128 v[108:111], v86 offset:16
mva 42,50,20
mav 34,24,54
s_waitcnt lgkmcnt(4)
R 70,384
v_mul_f32 v120, v119, v96
mav 38,28,54
v_fma_f32 v22, v22, v118, v120
v_mul_f32 v121, v119, v97
mva 42,54,32
v_fma_f32 v23, v23, v118, v121
v_mul_f32 v120, v119, v98
mav 34,36,58
s_waitcnt lgkmcnt(2)
R 74,448
v_fma_f32 v24, v24, v118, v120
mav 38,40,58
v_mul_f32 v121, v119, v99
v_fma_f32 v25, v25, v118, v121
mva 42,58,44
s_waitcnt lgkmcnt(2)
v_mul_f32 v120, v119, v100
v_fma_f32 v26, v26, v118, v120
mav 34,48,62
R 46,512
v_mul_f32 v121, v119, v101
mav 38,52,62
v_fma_f32 v27, v27, v118, v121
v_mul_f32 v120, v119, v102
mva 42,62,56
v_fma_f32 v28, v28, v118, v120
v_mul_f32 v121, v119, v103
mav 34,60,66
R 50,576
v_fma_f32 v29, v29, v118, v121
mav 38,64,66
v_cvt_pk_bf16_f32 v112, v22, v23
v_cvt_pk_bf16_f32 v113, v24, v25
mva 42,66,68
s_waitcnt lgkmcnt(2)
v_cvt_pk_bf16_f32 v114, v26, v27
v_cvt_pk_bf16_f32 v115, v28, v29
mav 34,72,70
R 54,640
v_mul_f32 v105, v105, v95
mav 38,76,70
ds_write_b128 v84, v[112:115] offset:0
mva 42,70,80
s_xor_b32 s50, s50, s57
v_fma_f32 v30, v30, v104, v105
v_mul_f32 v107, v107, v95
mav 34,84,74
R 58,704
v_fma_f32 v31, v31, v106, v107
mav 38,88,74
v_mul_f32 v109, v109, v95
v_fma_f32 v32, v32, v108, v109
mva 42,74,92
s_waitcnt lgkmcnt(3)
v_mul_f32 v111, v111, v95
v_fma_f32 v33, v33, v110, v111
mav 34,96,46
s_mov_b32 m0, 144384
R 62,768
v_cvt_pk_bf16_f32 v78, v30, v31
mav 38,100,46
s_sub_u32 s54, s54, 0x8000
v_cvt_pk_bf16_f32 v79, v32, v33
mva 42,46,104
buffer_load_dword v91, s[36:39], 0 offen sc1 lds
mav 34,108,50
s_add_u32 m0, s50, 33024
R 66,832
mav 38,112,50
ds_write_b64 v84, v[78:79] offset:1024
mva 42,50,116
s_waitcnt lgkmcnt(3)
s_cmp_lt_u32 s54, s56
s_cselect_b64 s[44:45], s[40:41], s[44:45]
mav 34,120,54
R 70,896
mav 38,124,54
s_waitcnt vmcnt(17) lgkmcnt(1)
s_add_u32 s55, s54, 14336
mva 42,54,128
s_barrier
mav 34,132,58
R 74,960
mav 38,136,58
D2 87,54,0
mva 42,58,140
mav 34,144,62
D2 87,54,1024
mav 38,148,62
R 46,1024
mva 42,62,152
D2 87,55,2048
mav 34,156,66
R 50,1088
mav 38,160,66
D2 87,55,3072
mva 42,66,164
s_waitcnt lgkmcnt(2)
s_add_u32 m0, s50, 41280
v_add_u32 v93, 1, v93
mav 34,168,70
D2 88,54,0
mav 38,172,70
R 54,1152
mva 42,70,176
D2 88,54,1024
mav 34,180,74
R 58,1216
mav 38,184,74
D2 88,55,2048
mva 42,74,188
s_waitcnt lgkmcnt(2)
mav 34,192,46
D2 88,55,3072
mav 38,196,46
s_add_u32 m0, s50, 49536
R 62,1280
mva 42,46,200
D2 89,54,0
mav 34,204,50
R 66,1344
mav 38,208,50
D2 89,54,1024
mva 42,50,212
s_waitcnt lgkmcnt(2)
mav 34,216,54
D2 89,55,2048
mav 38,220,54
R 70,1408
mva 42,54,224
D2 89,55,3072
mav 34,228,58
s_add_u32 m0, s50, 57792
R 74,1472
mav 38,232,58
D2 90,54,0
mva 42,58,236
s_waitcnt lgkmcnt(2)
mav 34,240,62
D2 90,54,1024
mav 38,244,62
R 46,1536
mva 42,62,248
D2 90,55,2048
mav 34,252,66
R 50,1600
mvv 38,128,66
D2 90,55,3072
mvv 42,66,132
s_waitcnt lgkmcnt(2)
mvv 34,136,70
R 54,1664
mvv 38,140,70
mvv 42,70,144
mvv 34,148,74
R 58,1728
mvv 38,152,74
mvv 42,74,156
s_waitcnt lgkmcnt(2)
mvv 34,160,46
R 62,1792
mvv 38,164,46
mvv 42,46,168
mvv 34,172,50
R 66,1856
mvv 38,176,50
mvv 42,50,180
s_waitcnt lgkmcnt(2)
mvv 34,184,54
R 70,1920
mvv 38,188,54
mvv 42,54,192
mvv 34,196,58
R 74,1984
v_xor_b32 v80, v81, v80
mvv 38,200,58
mvv 42,58,204
s_waitcnt lgkmcnt(2)
mvv 34,208,62
R 46,33024
mvv 38,212,62
ds_read_b32 v122, v83 offset:0
mvv 42,62,216
ds_read_b64 v[118:119], v85 offset:128
mvv 34,220,66
R 50,33088
mvv 38,224,66
ds_read_b128 v[104:107], v86 offset:128
mvv 42,66,228
s_waitcnt lgkmcnt(5)
ds_read_b128 v[108:111], v86 offset:144
mvv 34,232,70
R 54,33152
mvv 38,236,70
mvv 42,70,240
s_waitcnt lgkmcnt(4)
v_cmp_gt_i32 vcc, v93, v122
mvv 34,244,74
R 58,33216
mvv 38,248,74
mvv 42,74,252
s_cbranch_vccnz CQB0_2
BQB0_2:
v_mfma_f32_16x16x32_bf16 v[22:25], a[0:3], v[46:49], 0
R 62,33280
v_mul_f32 v120, v119, v96
v_mfma_f32_16x16x32_bf16 v[26:29], a[4:7], v[46:49], 0
s_waitcnt lgkmcnt(2)
v_fma_f32 v34, v34, v118, v120
v_mul_f32 v121, v119, v97
v_mfma_f32_16x16x32_bf16 v[30:33], v[46:49], a[8:11], 0
v_fma_f32 v35, v35, v118, v121
v_mul_f32 v120, v119, v98
mav 22,12,50
R 66,33344
v_fma_f32 v36, v36, v118, v120
mav 26,16,50
v_mul_f32 v121, v119, v99
v_fma_f32 v37, v37, v118, v121
mva 30,50,20
v_mul_f32 v120, v119, v100
v_fma_f32 v38, v38, v118, v120
mav 22,24,54
R 70,33408
v_mul_f32 v121, v119, v101
mav 26,28,54
v_fma_f32 v39, v39, v118, v121
v_mul_f32 v120, v119, v102
mva 30,54,32
s_waitcnt lgkmcnt(2)
v_fma_f32 v40, v40, v118, v120
v_mul_f32 v121, v119, v103
mav 22,36,58
R 74,33472
v_fma_f32 v41, v41, v118, v121
mav 26,40,58
v_cvt_pk_bf16_f32 v112, v34, v35
v_cvt_pk_bf16_f32 v113, v36, v37
mva 30,58,44
v_cvt_pk_bf16_f32 v114, v38, v39
v_cvt_pk_bf16_f32 v115, v40, v41
mav 22,48,62
R 46,33536
v_mul_f32 v105, v105, v95
mav 26,52,62
ds_write_b128 v84, v[112:115] offset:4096
mva 30,62,56
s_waitcnt lgkmcnt(3)
v_fma_f32 v42, v42, v104, v105
v_mul_f32 v107, v107, v95
mav 22,60,66
R 50,33600
v_fma_f32 v43, v43, v106, v107
mav 26,64,66
v_mul_f32 v109, v109, v95
v_fma_f32 v44, v44, v108, v109
mva 30,66,68
v_mul_f32 v111, v111, v95
v_fma_f32 v45, v45, v110, v111
mav 22,72,70
R 54,33664
v_cvt_pk_bf16_f32 v78, v42, v43
mav 26,76,70
v_cvt_pk_bf16_f32 v79, v44, v45
mva 30,70,80
s_waitcnt lgkmcnt(2)
ds_write_b64 v84, v[78:79] offset:5120
mav 22,84,74
R 58,33728
mav 26,88,74
mva 30,74,92
mav 22,96,46
s_waitcnt vmcnt(17) lgkmcnt(1)
R 62,33792
s_mov_b32 m0, 144384
mav 26,100,46
s_barrier
mva 30,46,104
buffer_load_dword v91, s[36:39], 0 offen sc1 lds
mav 22,108,50
s_sub_u32 s54, s54, 0x8000
R 66,33856
s_add_u32 m0, s50, 0
mav 26,112,50
mva 30,50,116
mav 22,120,54
D2 87,54,0
mav 26,124,54
s_add_u32 s55, s54, 14336
R 70,33920
mva 30,54,128
s_waitcnt lgkmcnt(2)
D2 87,54,1024
mav 22,132,58
R 74,33984
mav 26,136,58
D2 87,55,2048
mva 30,58,140
mav 22,144,62
D2 87,55,3072
mav 26,148,62
s_add_u32 m0, s50, 8256
R 46,34048
mva 30,62,152
s_waitcnt lgkmcnt(2)
D2 88,54,0
mav 22,156,66
R 50,34112
mav 26,160,66
D2 88,54,1024
mva 30,66,164
v_add_u32 v93, 1, v93
mav 22,168,70
D2 88,55,2048
mav 26,172,70
R 54,34176
mva 30,70,176
s_waitcnt lgkmcnt(2)
D2 88,55,3072
mav 22,180,74
s_add_u32 m0, s50, 16512
R 58,34240
mav 26,184,74
D2 89,54,0
mva 30,74,188
mav 22,192,46
D2 89,54,1024
mav 26,196,46
R 62,34304
mva 30,46,200
s_waitcnt lgkmcnt(2)
D2 89,55,2048
mav 22,204,50
R 66,34368
mav 26,208,50
D2 89,55,3072
mva 30,50,212
s_add_u32 m0, s50, 24768
mav 22,216,54
D2 90,54,0
mav 26,220,54
R 70,34432
mva 30,54,224
s_waitcnt lgkmcnt(2)
D2 90,54,1024
mav 22,228,58
R 74,34496
mav 26,232,58
D2 90,55,2048
mva 30,58,236
mav 22,240,62
D2 90,55,3072
mav 26,244,62
R 46,34560
mva 30,62,248
s_waitcnt lgkmcnt(2)
mav 22,252,66
R 50,34624
mvv 26,128,66
mvv 30,66,132
mvv 22,136,70
R 54,34688
mvv 26,140,70
mvv 30,70,144
s_waitcnt lgkmcnt(2)
mvv 22,148,74
R 58,34752
mvv 26,152,74
mvv 30,74,156
mvv 22,160,46
R 62,34816
mvv 26,164,46
mvv 30,46,168
s_waitcnt lgkmcnt(2)
mvv 22,172,50
R 66,34880
mvv 26,176,50
mvv 30,50,180
mvv 22,184,54
R 70,34944
mvv 26,188,54
mvv 30,54,192
s_waitcnt lgkmcnt(2)
mvv 22,196,58
R 74,35008
mvv 26,200,58
mvv 30,58,204
mvv 22,208,62
R 46,0
mvv 26,212,62
ds_read_b32 v122, v83 offset:0
mvv 30,62,216
s_waitcnt lgkmcnt(3)
ds_read_b64 v[118:119], v85 offset:0
mvv 22,220,66
R 50,64
mvv 26,224,66
ds_read_b128 v[104:107], v86 offset:0
mvv 30,66,228
ds_read_b128 v[108:111], v86 offset:16
mvv 22,232,70
R 54,128
mvv 26,236,70
mvv 30,70,240
s_waitcnt lgkmcnt(4)
v_cmp_gt_i32 vcc, v93, v122
mvv 22,244,74
R 58,192
mvv 26,248,74
mvv 30,74,252
s_cbranch_vccnz CQB0_3
BQB0_3:
v_mfma_f32_16x16x32_bf16 v[34:37], a[0:3], v[46:49], 0
R 62,256
v_mul_f32 v120, v119, v96
v_mfma_f32_16x16x32_bf16 v[38:41], a[4:7], v[46:49], 0
s_waitcnt lgkmcnt(2)
v_fma_f32 v22, v22, v118, v120
v_mul_f32 v121, v119, v97
v_mfma_f32_16x16x32_bf16 v[42:45], v[46:49], a[8:11], 0
v_fma_f32 v23, v23, v118, v121
v_mul_f32 v120, v119, v98
mav 34,12,50
R 66,320
v_fma_f32 v24, v24, v118, v120
mav 38,16,50
v_mul_f32 v121, v119, v99
v_fma_f32 v25, v25, v118, v121
mva 42,50,20
v_mul_f32 v120, v119, v100
v_fma_f32 v26, v26, v118, v120
mav 34,24,54
R 70,384
v_mul_f32 v121, v119, v101
mav 38,28,54
v_fma_f32 v27, v27, v118, v121
v_mul_f32 v120, v119, v102
mva 42,54,32
s_waitcnt lgkmcnt(2)
v_fma_f32 v28, v28, v118, v120
v_mul_f32 v121, v119, v103
mav 34,36,58
R 74,448
v_fma_f32 v29, v29, v118, v121
mav 38,40,58
v_cvt_pk_bf16_f32 v112, v22, v23
v_cvt_pk_bf16_f32 v113, v24, v25
mva 42,58,44
v_cvt_pk_bf16_f32 v114, v26, v27
v_cvt_pk_bf16_f32 v115, v28, v29
mav 34,48,62
R 46,512
v_mul_f32 v105, v105, v95
mav 38,52,62
ds_write_b128 v84, v[112:115] offset:0
mva 42,62,56
s_waitcnt lgkmcnt(3)
v_fma_f32 v30, v30, v104, v105
v_mul_f32 v107, v107, v95
mav 34,60,66
R 50,576
v_fma_f32 v31, v31, v106, v107
mav 38,64,66
v_mul_f32 v109, v109, v95
v_fma_f32 v32, v32, v108, v109
mva 42,66,68
v_mul_f32 v111, v111, v95
v_fma_f32 v33, v33, v110, v111
mav 34,72,70
R 54,640
v_cvt_pk_bf16_f32 v78, v30, v31
mav 38,76,70
v_cvt_pk_bf16_f32 v79, v32, v33
mva 42,70,80
s_waitcnt lgkmcnt(2)
ds_write_b64 v84, v[78:79] offset:1024
mav 34,84,74
s_xor_b32 s50, s50, s57
R 58,704
mav 38,88,74
mva 42,74,92
mav 34,96,46
LsQ18:
s_cbranch_execz LsQ22
s_waitcnt vmcnt(17) lgkmcnt(1)
R 62,768
s_mov_b32 m0, 144384
mav 38,100,46
s_barrier
mva 42,46,104
buffer_load_dword v91, s[36:39], 0 offen sc1 lds
mav 34,108,50
s_sub_u32 s54, s54, 0x8000
R 66,832
s_add_u32 m0, s50, 33024
mav 38,112,50
s_cmp_lt_u32 s54, s56
s_cselect_b64 s[44:45], s[40:41], s[44:45]
mva 42,50,116
mav 34,120,54
D2 87,54,0
mav 38,124,54
s_add_u32 s55, s54, 14336
R 70,896
mva 42,54,128
s_waitcnt lgkmcnt(2)
D2 87,54,1024
mav 34,132,58
R 74,960
mav 38,136,58
D2 87,55,2048
mva 42,58,140
mav 34,144,62
D2 87,55,3072
mav 38,148,62
s_add_u32 m0, s50, 41280
R 46,1024
mva 42,62,152
s_waitcnt lgkmcnt(2)
D2 88,54,0
mav 34,156,66
R 50,1088
mav 38,160,66
D2 88,54,1024
mva 42,66,164
v_add_u32 v93, 1, v93
mav 34,168,70
D2 88,55,2048
mav 38,172,70
R 54,1152
mva 42,70,176
s_waitcnt lgkmcnt(2)
D2 88,55,3072
mav 34,180,74
s_add_u32 m0, s50, 49536
R 58,1216
mav 38,184,74
D2 89,54,0
mva 42,74,188
mav 34,192,46
D2 89,54,1024
mav 38,196,46
R 62,1280
mva 42,46,200
s_waitcnt lgkmcnt(2)
D2 89,55,2048
mav 34,204,50
R 66,1344
mav 38,208,50
D2 89,55,3072
mva 42,50,212
s_add_u32 m0, s50, 57792
mav 34,216,54
D2 90,54,0
mav 38,220,54
R 70,1408
mva 42,54,224
s_waitcnt lgkmcnt(2)
D2 90,54,1024
mav 34,228,58
R 74,1472
mav 38,232,58
D2 90,55,2048
mva 42,58,236
mav 34,240,62
D2 90,55,3072
mav 38,244,62
R 46,1536
mva 42,62,248
s_waitcnt lgkmcnt(2)
mav 34,252,66
R 50,1600
mvv 38,128,66
mvv 42,66,132
mvv 34,136,70
R 54,1664
mvv 38,140,70
mvv 42,70,144
s_waitcnt lgkmcnt(2)
mvv 34,148,74
R 58,1728
mvv 38,152,74
mvv 42,74,156
mvv 34,160,46
R 62,1792
mvv 38,164,46
mvv 42,46,168
s_waitcnt lgkmcnt(2)
mvv 34,172,50
R 66,1856
mvv 38,176,50
s_mov_b32 s60, -27
.p2align 6
LQB0_4:
mvv 42,50,180
mvv 34,184,54
R 70,1920
mvv 38,188,54
mvv 42,54,192
s_waitcnt lgkmcnt(2)
mvv 34,196,58
R 74,1984
v_xor_b32 v80, v81, v80
mvv 38,200,58
mvv 42,58,204
mvv 34,208,62
R 46,33024
mvv 38,212,62
ds_read_b32 v122, v83 offset:0
mvv 42,62,216
s_waitcnt lgkmcnt(3)
ds_read_b64 v[118:119], v85 offset:128
mvv 34,220,66
R 50,33088
mvv 38,224,66
ds_read_b128 v[104:107], v86 offset:128
mvv 42,66,228
ds_read_b128 v[108:111], v86 offset:144
mvv 34,232,70
R 54,33152
mvv 38,236,70
mvv 42,70,240
s_waitcnt lgkmcnt(4)
v_cmp_gt_i32 vcc, v93, v122
mvv 34,244,74
R 58,33216
mvv 38,248,74
mvv 42,74,252
s_cbranch_vccnz CQB0_5
BQB0_5:
v_mfma_f32_16x16x32_bf16 v[22:25], a[0:3], v[46:49], 0
R 62,33280
v_mul_f32 v120, v119, v96
v_mfma_f32_16x16x32_bf16 v[26:29], a[4:7], v[46:49], 0
s_waitcnt lgkmcnt(2)
v_fma_f32 v34, v34, v118, v120
v_mul_f32 v121, v119, v97
v_mfma_f32_16x16x32_bf16 v[30:33], v[46:49], a[8:11], 0
v_fma_f32 v35, v35, v118, v121
v_mul_f32 v120, v119, v98
mav 22,12,50
R 66,33344
v_fma_f32 v36, v36, v118, v120
mav 26,16,50
v_mul_f32 v121, v119, v99
v_fma_f32 v37, v37, v118, v121
mva 30,50,20
v_mul_f32 v120, v119, v100
v_fma_f32 v38, v38, v118, v120
mav 22,24,54
R 70,33408
v_mul_f32 v121, v119, v101
mav 26,28,54
v_fma_f32 v39, v39, v118, v121
v_mul_f32 v120, v119, v102
mva 30,54,32
s_waitcnt lgkmcnt(2)
v_fma_f32 v40, v40, v118, v120
v_mul_f32 v121, v119, v103
mav 22,36,58
R 74,33472
v_fma_f32 v41, v41, v118, v121
mav 26,40,58
v_cvt_pk_bf16_f32 v112, v34, v35
v_cvt_pk_bf16_f32 v113, v36, v37
mva 30,58,44
v_cvt_pk_bf16_f32 v114, v38, v39
v_cvt_pk_bf16_f32 v115, v40, v41
mav 22,48,62
R 46,33536
v_mul_f32 v105, v105, v95
mav 26,52,62
ds_write_b128 v84, v[112:115] offset:4096
mva 30,62,56
s_waitcnt lgkmcnt(3)
v_fma_f32 v42, v42, v104, v105
v_mul_f32 v107, v107, v95
mav 22,60,66
R 50,33600
v_fma_f32 v43, v43, v106, v107
mav 26,64,66
v_mul_f32 v109, v109, v95
v_fma_f32 v44, v44, v108, v109
mva 30,66,68
v_mul_f32 v111, v111, v95
v_fma_f32 v45, v45, v110, v111
mav 22,72,70
R 54,33664
v_cvt_pk_bf16_f32 v78, v42, v43
mav 26,76,70
v_cvt_pk_bf16_f32 v79, v44, v45
mva 30,70,80
s_waitcnt lgkmcnt(2)
ds_write_b64 v84, v[78:79] offset:5120
mav 22,84,74
R 58,33728
mav 26,88,74
mva 30,74,92
mav 22,96,46
s_waitcnt vmcnt(17) lgkmcnt(1)
R 62,33792
s_mov_b32 m0, 144384
mav 26,100,46
s_barrier
mva 30,46,104
buffer_load_dword v91, s[36:39], 0 offen sc1 lds
mav 22,108,50
s_sub_u32 s54, s54, 0x8000
R 66,33856
s_add_u32 m0, s50, 0
mav 26,112,50
mva 30,50,116
mav 22,120,54
D2 87,54,0
mav 26,124,54
s_add_u32 s55, s54, 14336
R 70,33920
mva 30,54,128
s_waitcnt lgkmcnt(2)
D2 87,54,1024
mav 22,132,58
R 74,33984
mav 26,136,58
D2 87,55,2048
mva 30,58,140
mav 22,144,62
D2 87,55,3072
mav 26,148,62
s_add_u32 m0, s50, 8256
R 46,34048
mva 30,62,152
s_waitcnt lgkmcnt(2)
D2 88,54,0
mav 22,156,66
R 50,34112
mav 26,160,66
D2 88,54,1024
mva 30,66,164
v_add_u32 v93, 1, v93
mav 22,168,70
D2 88,55,2048
mav 26,172,70
R 54,34176
mva 30,70,176
s_waitcnt lgkmcnt(2)
D2 88,55,3072
mav 22,180,74
s_add_u32 m0, s50, 16512
R 58,34240
mav 26,184,74
D2 89,54,0
mva 30,74,188
mav 22,192,46
D2 89,54,1024
mav 26,196,46
R 62,34304
mva 30,46,200
s_waitcnt lgkmcnt(2)
D2 89,55,2048
mav 22,204,50
R 66,34368
mav 26,208,50
D2 89,55,3072
mva 30,50,212
s_add_u32 m0, s50, 24768
mav 22,216,54
D2 90,54,0
mav 26,220,54
R 70,34432
mva 30,54,224
s_waitcnt lgkmcnt(2)
D2 90,54,1024
mav 22,228,58
R 74,34496
mav 26,232,58
D2 90,55,2048
mva 30,58,236
mav 22,240,62
D2 90,55,3072
mav 26,244,62
R 46,34560
mva 30,62,248
s_waitcnt lgkmcnt(2)
mav 22,252,66
R 50,34624
mvv 26,128,66
mvv 30,66,132
mvv 22,136,70
R 54,34688
mvv 26,140,70
mvv 30,70,144
s_waitcnt lgkmcnt(2)
mvv 22,148,74
R 58,34752
mvv 26,152,74
mvv 30,74,156
mvv 22,160,46
R 62,34816
mvv 26,164,46
mvv 30,46,168
s_waitcnt lgkmcnt(2)
mvv 22,172,50
R 66,34880
mvv 26,176,50
mvv 30,50,180
mvv 22,184,54
R 70,34944
mvv 26,188,54
mvv 30,54,192
s_waitcnt lgkmcnt(2)
mvv 22,196,58
R 74,35008
mvv 26,200,58
mvv 30,58,204
mvv 22,208,62
R 46,0
mvv 26,212,62
ds_read_b32 v122, v83 offset:0
mvv 30,62,216
s_waitcnt lgkmcnt(3)
ds_read_b64 v[118:119], v85 offset:0
mvv 22,220,66
R 50,64
mvv 26,224,66
ds_read_b128 v[104:107], v86 offset:0
mvv 30,66,228
ds_read_b128 v[108:111], v86 offset:16
mvv 22,232,70
R 54,128
mvv 26,236,70
mvv 30,70,240
s_waitcnt lgkmcnt(4)
v_cmp_gt_i32 vcc, v93, v122
mvv 22,244,74
R 58,192
mvv 26,248,74
mvv 30,74,252
s_cbranch_vccnz CQB0_6
BQB0_6:
v_mfma_f32_16x16x32_bf16 v[34:37], a[0:3], v[46:49], 0
R 62,256
v_mul_f32 v120, v119, v96
v_mfma_f32_16x16x32_bf16 v[38:41], a[4:7], v[46:49], 0
s_waitcnt lgkmcnt(2)
v_fma_f32 v22, v22, v118, v120
v_mul_f32 v121, v119, v97
v_mfma_f32_16x16x32_bf16 v[42:45], v[46:49], a[8:11], 0
v_fma_f32 v23, v23, v118, v121
v_mul_f32 v120, v119, v98
mav 34,12,50
R 66,320
v_fma_f32 v24, v24, v118, v120
mav 38,16,50
v_mul_f32 v121, v119, v99
v_fma_f32 v25, v25, v118, v121
mva 42,50,20
v_mul_f32 v120, v119, v100
v_fma_f32 v26, v26, v118, v120
mav 34,24,54
R 70,384
v_mul_f32 v121, v119, v101
mav 38,28,54
v_fma_f32 v27, v27, v118, v121
v_mul_f32 v120, v119, v102
mva 42,54,32
s_waitcnt lgkmcnt(2)
v_fma_f32 v28, v28, v118, v120
v_mul_f32 v121, v119, v103
mav 34,36,58
R 74,448
v_fma_f32 v29, v29, v118, v121
mav 38,40,58
v_cvt_pk_bf16_f32 v112, v22, v23
v_cvt_pk_bf16_f32 v113, v24, v25
mva 42,58,44
v_cvt_pk_bf16_f32 v114, v26, v27
v_cvt_pk_bf16_f32 v115, v28, v29
mav 34,48,62
R 46,512
v_mul_f32 v105, v105, v95
mav 38,52,62
ds_write_b128 v84, v[112:115] offset:0
mva 42,62,56
s_waitcnt lgkmcnt(3)
v_fma_f32 v30, v30, v104, v105
v_mul_f32 v107, v107, v95
mav 34,60,66
R 50,576
v_fma_f32 v31, v31, v106, v107
mav 38,64,66
v_mul_f32 v109, v109, v95
v_fma_f32 v32, v32, v108, v109
mva 42,66,68
v_mul_f32 v111, v111, v95
v_fma_f32 v33, v33, v110, v111
mav 34,72,70
R 54,640
v_cvt_pk_bf16_f32 v78, v30, v31
mav 38,76,70
v_cvt_pk_bf16_f32 v79, v32, v33
mva 42,70,80
s_waitcnt lgkmcnt(2)
ds_write_b64 v84, v[78:79] offset:1024
mav 34,84,74
s_xor_b32 s50, s50, s57
R 58,704
mav 38,88,74
mva 42,74,92
mav 34,96,46
s_waitcnt vmcnt(17) lgkmcnt(1)
R 62,768
s_mov_b32 m0, 144384
mav 38,100,46
s_barrier
mva 42,46,104
buffer_load_dword v91, s[36:39], 0 offen sc1 lds
mav 34,108,50
s_sub_u32 s54, s54, 0x8000
R 66,832
s_add_u32 m0, s50, 33024
mav 38,112,50
s_cmp_lt_u32 s54, s56
s_cselect_b64 s[44:45], s[40:41], s[44:45]
mva 42,50,116
mav 34,120,54
D2 87,54,0
mav 38,124,54
s_add_u32 s55, s54, 14336
R 70,896
mva 42,54,128
s_waitcnt lgkmcnt(2)
D2 87,54,1024
mav 34,132,58
R 74,960
mav 38,136,58
D2 87,55,2048
mva 42,58,140
mav 34,144,62
D2 87,55,3072
mav 38,148,62
s_add_u32 m0, s50, 41280
R 46,1024
mva 42,62,152
s_waitcnt lgkmcnt(2)
D2 88,54,0
mav 34,156,66
R 50,1088
mav 38,160,66
D2 88,54,1024
mva 42,66,164
v_add_u32 v93, 1, v93
mav 34,168,70
D2 88,55,2048
mav 38,172,70
R 54,1152
mva 42,70,176
s_waitcnt lgkmcnt(2)
D2 88,55,3072
mav 34,180,74
s_add_u32 m0, s50, 49536
R 58,1216
mav 38,184,74
D2 89,54,0
mva 42,74,188
mav 34,192,46
D2 89,54,1024
mav 38,196,46
R 62,1280
mva 42,46,200
s_waitcnt lgkmcnt(2)
D2 89,55,2048
mav 34,204,50
R 66,1344
mav 38,208,50
D2 89,55,3072
mva 42,50,212
s_add_u32 m0, s50, 57792
mav 34,216,54
D2 90,54,0
mav 38,220,54
R 70,1408
mva 42,54,224
s_waitcnt lgkmcnt(2)
D2 90,54,1024
mav 34,228,58
R 74,1472
mav 38,232,58
D2 90,55,2048
mva 42,58,236
mav 34,240,62
D2 90,55,3072
mav 38,244,62
R 46,1536
mva 42,62,248
s_waitcnt lgkmcnt(2)
mav 34,252,66
R 50,1600
mvv 38,128,66
mvv 42,66,132
mvv 34,136,70
R 54,1664
mvv 38,140,70
mvv 42,70,144
s_waitcnt lgkmcnt(2)
mvv 34,148,74
R 58,1728
mvv 38,152,74
mvv 42,74,156
mvv 34,160,46
R 62,1792
mvv 38,164,46
mvv 42,46,168
s_waitcnt lgkmcnt(2)
mvv 34,172,50
s_add_u32 s60, s60, 1
R 66,1856
mvv 38,176,50
s_cbranch_scc0 LQB0_4
mvv 42,50,180
mvv 34,184,54
R 70,1920
mvv 38,188,54
mvv 42,54,192
s_waitcnt lgkmcnt(2)
mvv 34,196,58
R 74,1984
v_xor_b32 v80, v81, v80
mvv 38,200,58
mvv 42,58,204
mvv 34,208,62
R 46,33024
mvv 38,212,62
ds_read_b32 v122, v83 offset:0
mvv 42,62,216
s_waitcnt lgkmcnt(3)
ds_read_b64 v[118:119], v85 offset:128
mvv 34,220,66
R 50,33088
mvv 38,224,66
ds_read_b128 v[104:107], v86 offset:128
mvv 42,66,228
LsQ19:
s_cbranch_execz LsQ23
ds_read_b128 v[108:111], v86 offset:144
mvv 34,232,70
R 54,33152
mvv 38,236,70
mvv 42,70,240
s_waitcnt lgkmcnt(4)
v_cmp_gt_i32 vcc, v93, v122
mvv 34,244,74
R 58,33216
mvv 38,248,74
mvv 42,74,252
s_cbranch_vccnz CQB0_7
BQB0_7:
v_mfma_f32_16x16x32_bf16 v[22:25], a[0:3], v[46:49], 0
R 62,33280
v_mul_f32 v120, v119, v96
v_mfma_f32_16x16x32_bf16 v[26:29], a[4:7], v[46:49], 0
s_waitcnt lgkmcnt(2)
v_fma_f32 v34, v34, v118, v120
v_mul_f32 v121, v119, v97
v_mfma_f32_16x16x32_bf16 v[30:33], v[46:49], a[8:11], 0
v_fma_f32 v35, v35, v118, v121
v_mul_f32 v120, v119, v98
mav 22,12,50
R 66,33344
v_fma_f32 v36, v36, v118, v120
mav 26,16,50
v_mul_f32 v121, v119, v99
v_fma_f32 v37, v37, v118, v121
mva 30,50,20
v_mul_f32 v120, v119, v100
v_fma_f32 v38, v38, v118, v120
mav 22,24,54
R 70,33408
v_mul_f32 v121, v119, v101
mav 26,28,54
v_fma_f32 v39, v39, v118, v121
v_mul_f32 v120, v119, v102
mva 30,54,32
s_waitcnt lgkmcnt(2)
v_fma_f32 v40, v40, v118, v120
v_mul_f32 v121, v119, v103
mav 22,36,58
R 74,33472
v_fma_f32 v41, v41, v118, v121
mav 26,40,58
v_cvt_pk_bf16_f32 v112, v34, v35
v_cvt_pk_bf16_f32 v113, v36, v37
mva 30,58,44
v_cvt_pk_bf16_f32 v114, v38, v39
v_cvt_pk_bf16_f32 v115, v40, v41
mav 22,48,62
R 46,33536
v_mul_f32 v105, v105, v95
mav 26,52,62
ds_write_b128 v84, v[112:115] offset:4096
mva 30,62,56
s_waitcnt lgkmcnt(3)
v_fma_f32 v42, v42, v104, v105
v_mul_f32 v107, v107, v95
mav 22,60,66
R 50,33600
v_fma_f32 v43, v43, v106, v107
mav 26,64,66
v_mul_f32 v109, v109, v95
v_fma_f32 v44, v44, v108, v109
mva 30,66,68
v_mul_f32 v111, v111, v95
v_fma_f32 v45, v45, v110, v111
mav 22,72,70
R 54,33664
v_cvt_pk_bf16_f32 v78, v42, v43
mav 26,76,70
v_cvt_pk_bf16_f32 v79, v44, v45
mva 30,70,80
s_waitcnt lgkmcnt(2)
ds_write_b64 v84, v[78:79] offset:5120
mav 22,84,74
R 58,33728
mav 26,88,74
mva 30,74,92
mav 22,96,46
s_waitcnt vmcnt(17) lgkmcnt(1)
R 62,33792
s_mov_b32 m0, 144384
mav 26,100,46
s_barrier
mva 30,46,104
buffer_load_dword v91, s[36:39], 0 offen sc1 lds
mav 22,108,50
s_sub_u32 s54, s54, 0x8000
R 66,33856
s_add_u32 m0, s50, 0
mav 26,112,50
mva 30,50,116
mav 22,120,54
D2 87,54,0
mav 26,124,54
s_add_u32 s55, s54, 14336
R 70,33920
mva 30,54,128
s_waitcnt lgkmcnt(2)
D2 87,54,1024
mav 22,132,58
R 74,33984
mav 26,136,58
D2 87,55,2048
mva 30,58,140
mav 22,144,62
D2 87,55,3072
mav 26,148,62
s_add_u32 m0, s50, 8256
R 46,34048
mva 30,62,152
s_waitcnt lgkmcnt(2)
D2 88,54,0
mav 22,156,66
R 50,34112
mav 26,160,66
D2 88,54,1024
mva 30,66,164
v_add_u32 v93, 1, v93
mav 22,168,70
D2 88,55,2048
mav 26,172,70
R 54,34176
mva 30,70,176
s_waitcnt lgkmcnt(2)
D2 88,55,3072
mav 22,180,74
s_add_u32 m0, s50, 16512
R 58,34240
mav 26,184,74
D2 89,54,0
mva 30,74,188
mav 22,192,46
D2 89,54,1024
mav 26,196,46
R 62,34304
mva 30,46,200
s_waitcnt lgkmcnt(2)
D2 89,55,2048
mav 22,204,50
R 66,34368
mav 26,208,50
D2 89,55,3072
mva 30,50,212
s_add_u32 m0, s50, 24768
mav 22,216,54
D2 90,54,0
mav 26,220,54
R 70,34432
mva 30,54,224
s_waitcnt lgkmcnt(2)
D2 90,54,1024
mav 22,228,58
R 74,34496
mav 26,232,58
D2 90,55,2048
mva 30,58,236
mav 22,240,62
D2 90,55,3072
mav 26,244,62
R 46,34560
mva 30,62,248
s_waitcnt lgkmcnt(2)
mav 22,252,66
R 50,34624
mvv 26,128,66
mvv 30,66,132
mvv 22,136,70
R 54,34688
mvv 26,140,70
mvv 30,70,144
s_waitcnt lgkmcnt(2)
mvv 22,148,74
R 58,34752
mvv 26,152,74
mvv 30,74,156
mvv 22,160,46
R 62,34816
mvv 26,164,46
mvv 30,46,168
s_waitcnt lgkmcnt(2)
mvv 22,172,50
R 66,34880
mvv 26,176,50
mvv 30,50,180
mvv 22,184,54
R 70,34944
mvv 26,188,54
mvv 30,54,192
s_waitcnt lgkmcnt(2)
mvv 22,196,58
R 74,35008
mvv 26,200,58
mvv 30,58,204
mvv 22,208,62
R 46,0
mvv 26,212,62
ds_read_b32 v122, v83 offset:0
mvv 30,62,216
s_waitcnt lgkmcnt(3)
ds_read_b64 v[118:119], v85 offset:0
mvv 22,220,66
R 50,64
mvv 26,224,66
ds_read_b128 v[104:107], v86 offset:0
mvv 30,66,228
ds_read_b128 v[108:111], v86 offset:16
mvv 22,232,70
R 54,128
mvv 26,236,70
mvv 30,70,240
s_waitcnt lgkmcnt(4)
v_cmp_gt_i32 vcc, v93, v122
mvv 22,244,74
R 58,192
mvv 26,248,74
mvv 30,74,252
s_cbranch_vccnz CQB0_8
BQB0_8:
v_mfma_f32_16x16x32_bf16 v[34:37], a[0:3], v[46:49], 0
R 62,256
v_mul_f32 v120, v119, v96
v_mfma_f32_16x16x32_bf16 v[38:41], a[4:7], v[46:49], 0
s_waitcnt lgkmcnt(2)
v_fma_f32 v22, v22, v118, v120
v_mul_f32 v121, v119, v97
v_mfma_f32_16x16x32_bf16 v[42:45], v[46:49], a[8:11], 0
v_fma_f32 v23, v23, v118, v121
v_mul_f32 v120, v119, v98
mav 34,12,50
R 66,320
v_fma_f32 v24, v24, v118, v120
mav 38,16,50
v_mul_f32 v121, v119, v99
v_fma_f32 v25, v25, v118, v121
mva 42,50,20
v_mul_f32 v120, v119, v100
v_fma_f32 v26, v26, v118, v120
mav 34,24,54
R 70,384
v_mul_f32 v121, v119, v101
mav 38,28,54
v_fma_f32 v27, v27, v118, v121
v_mul_f32 v120, v119, v102
mva 42,54,32
s_waitcnt lgkmcnt(2)
v_fma_f32 v28, v28, v118, v120
v_mul_f32 v121, v119, v103
mav 34,36,58
R 74,448
v_fma_f32 v29, v29, v118, v121
mav 38,40,58
v_cvt_pk_bf16_f32 v112, v22, v23
v_cvt_pk_bf16_f32 v113, v24, v25
mva 42,58,44
v_cvt_pk_bf16_f32 v114, v26, v27
v_cvt_pk_bf16_f32 v115, v28, v29
mav 34,48,62
R 46,512
v_mul_f32 v105, v105, v95
mav 38,52,62
ds_write_b128 v84, v[112:115] offset:0
mva 42,62,56
s_waitcnt lgkmcnt(3)
v_fma_f32 v30, v30, v104, v105
v_mul_f32 v107, v107, v95
mav 34,60,66
R 50,576
v_fma_f32 v31, v31, v106, v107
mav 38,64,66
v_mul_f32 v109, v109, v95
v_fma_f32 v32, v32, v108, v109
mva 42,66,68
v_mul_f32 v111, v111, v95
v_fma_f32 v33, v33, v110, v111
mav 34,72,70
R 54,640
v_cvt_pk_bf16_f32 v78, v30, v31
mav 38,76,70
v_cvt_pk_bf16_f32 v79, v32, v33
mva 42,70,80
s_waitcnt lgkmcnt(2)
ds_write_b64 v84, v[78:79] offset:1024
mav 34,84,74
R 58,704
mav 38,88,74
mva 42,74,92
mav 34,96,46
s_waitcnt vmcnt(17) lgkmcnt(1)
R 62,768
mav 38,100,46
s_barrier
mva 42,46,104
mav 34,108,50
R 66,832
mav 38,112,50
mva 42,50,116
mav 34,120,54
R 70,896
mav 38,124,54
mva 42,54,128
s_waitcnt lgkmcnt(2)
mav 34,132,58
R 74,960
mav 38,136,58
mva 42,58,140
mav 34,144,62
R 46,1024
mav 38,148,62
mva 42,62,152
s_waitcnt lgkmcnt(2)
mav 34,156,66
R 50,1088
mav 38,160,66
mva 42,66,164
mav 34,168,70
R 54,1152
mav 38,172,70
mva 42,70,176
s_waitcnt lgkmcnt(2)
mav 34,180,74
R 58,1216
mav 38,184,74
mva 42,74,188
mav 34,192,46
R 62,1280
mav 38,196,46
mva 42,46,200
s_waitcnt lgkmcnt(2)
mav 34,204,50
R 66,1344
mav 38,208,50
mva 42,50,212
mav 34,216,54
R 70,1408
mav 38,220,54
mva 42,54,224
s_waitcnt lgkmcnt(2)
mav 34,228,58
R 74,1472
mav 38,232,58
mva 42,58,236
mav 34,240,62
R 46,1536
mav 38,244,62
mva 42,62,248
s_waitcnt lgkmcnt(2)
mav 34,252,66
R 50,1600
mvv 38,128,66
mvv 42,66,132
mvv 34,136,70
R 54,1664
mvv 38,140,70
mvv 42,70,144
s_waitcnt lgkmcnt(2)
mvv 34,148,74
R 58,1728
mvv 38,152,74
mvv 42,74,156
mvv 34,160,46
R 62,1792
mvv 38,164,46
mvv 42,46,168
s_waitcnt lgkmcnt(2)
mvv 34,172,50
R 66,1856
mvv 38,176,50
mvv 42,50,180
mvv 34,184,54
R 70,1920
mvv 38,188,54
mvv 42,54,192
s_waitcnt lgkmcnt(2)
mvv 34,196,58
R 74,1984
v_xor_b32 v80, v81, v80
mvv 38,200,58
mvv 42,58,204
mvv 34,208,62
R 46,33024
mvv 38,212,62
ds_read_b64 v[118:119], v85 offset:128
mvv 42,62,216
s_waitcnt lgkmcnt(3)
ds_read_b128 v[104:107], v86 offset:128
mvv 34,220,66
R 50,33088
mvv 38,224,66
ds_read_b128 v[108:111], v86 offset:144
mvv 42,66,228
mvv 34,232,70
R 54,33152
mvv 38,236,70
mvv 42,70,240
s_waitcnt lgkmcnt(3)
mvv 34,244,74
R 58,33216
mvv 38,248,74
mvv 42,74,252
v_mfma_f32_16x16x32_bf16 v[22:25], a[0:3], v[46:49], 0
R 62,33280
v_mul_f32 v120, v119, v96
v_mfma_f32_16x16x32_bf16 v[26:29], a[4:7], v[46:49], 0
s_waitcnt lgkmcnt(2)
v_fma_f32 v34, v34, v118, v120
v_mul_f32 v121, v119, v97
v_mfma_f32_16x16x32_bf16 v[30:33], v[46:49], a[8:11], 0
v_fma_f32 v35, v35, v118, v121
v_mul_f32 v120, v119, v98
mav 22,12,50
R 66,33344
v_fma_f32 v36, v36, v118, v120
mav 26,16,50
v_mul_f32 v121, v119, v99
v_fma_f32 v37, v37, v118, v121
mva 30,50,20
v_mul_f32 v120, v119, v100
v_fma_f32 v38, v38, v118, v120
mav 22,24,54
R 70,33408
v_mul_f32 v121, v119, v101
mav 26,28,54
v_fma_f32 v39, v39, v118, v121
v_mul_f32 v120, v119, v102
mva 30,54,32
s_waitcnt lgkmcnt(2)
v_fma_f32 v40, v40, v118, v120
v_mul_f32 v121, v119, v103
mav 22,36,58
R 74,33472
v_fma_f32 v41, v41, v118, v121
mav 26,40,58
v_cvt_pk_bf16_f32 v112, v34, v35
v_cvt_pk_bf16_f32 v113, v36, v37
mva 30,58,44
v_cvt_pk_bf16_f32 v114, v38, v39
v_cvt_pk_bf16_f32 v115, v40, v41
mav 22,48,62
R 46,33536
v_mul_f32 v105, v105, v95
mav 26,52,62
ds_write_b128 v84, v[112:115] offset:4096
mva 30,62,56
s_waitcnt lgkmcnt(3)
v_fma_f32 v42, v42, v104, v105
v_mul_f32 v107, v107, v95
mav 22,60,66
R 50,33600
v_fma_f32 v43, v43, v106, v107
mav 26,64,66
v_mul_f32 v109, v109, v95
v_fma_f32 v44, v44, v108, v109
mva 30,66,68
v_mul_f32 v111, v111, v95
v_fma_f32 v45, v45, v110, v111
mav 22,72,70
R 54,33664
v_cvt_pk_bf16_f32 v78, v42, v43
mav 26,76,70
v_cvt_pk_bf16_f32 v79, v44, v45
mva 30,70,80
s_waitcnt lgkmcnt(2)
ds_write_b64 v84, v[78:79] offset:5120
mav 22,84,74
R 58,33728
mav 26,88,74
mva 30,74,92
mav 22,96,46
s_waitcnt vmcnt(0) lgkmcnt(1)
R 62,33792
mav 26,100,46
s_barrier
mva 30,46,104
mav 22,108,50
R 66,33856
mav 26,112,50
mva 30,50,116
mav 22,120,54
R 70,33920
mav 26,124,54
mva 30,54,128
s_waitcnt lgkmcnt(2)
mav 22,132,58
R 74,33984
mav 26,136,58
mva 30,58,140
mav 22,144,62
R 46,34048
mav 26,148,62
mva 30,62,152
s_waitcnt lgkmcnt(2)
mav 22,156,66
R 50,34112
mav 26,160,66
mva 30,66,164
mav 22,168,70
R 54,34176
mav 26,172,70
mva 30,70,176
s_waitcnt lgkmcnt(2)
mav 22,180,74
R 58,34240
mav 26,184,74
mva 30,74,188
mav 22,192,46
R 62,34304
mav 26,196,46
mva 30,46,200
s_waitcnt lgkmcnt(2)
mav 22,204,50
R 66,34368
mav 26,208,50
mva 30,50,212
mav 22,216,54
R 70,34432
mav 26,220,54
mva 30,54,224
s_waitcnt lgkmcnt(2)
mav 22,228,58
R 74,34496
mav 26,232,58
mva 30,58,236
mav 22,240,62
R 46,34560
mav 26,244,62
mva 30,62,248
s_waitcnt lgkmcnt(2)
mav 22,252,66
R 50,34624
mvv 26,128,66
mvv 30,66,132
mvv 22,136,70
R 54,34688
mvv 26,140,70
mvv 30,70,144
s_waitcnt lgkmcnt(2)
mvv 22,148,74
R 58,34752
mvv 26,152,74
mvv 30,74,156
mvv 22,160,46
R 62,34816
mvv 26,164,46
mvv 30,46,168
s_waitcnt lgkmcnt(2)
mvv 22,172,50
R 66,34880
mvv 26,176,50
mvv 30,50,180
mvv 22,184,54
R 70,34944
mvv 26,188,54
mvv 30,54,192
s_waitcnt lgkmcnt(2)
mvv 22,196,58
R 74,35008
LsQ20:
s_cbranch_execz LsQ24
mvv 26,200,58
mvv 30,58,204
mvv 22,208,62
R 46,0
mvv 26,212,62
ds_read_b64 v[118:119], v85 offset:0
mvv 30,62,216
s_waitcnt lgkmcnt(3)
ds_read_b128 v[104:107], v86 offset:0
mvv 22,220,66
R 50,64
mvv 26,224,66
ds_read_b128 v[108:111], v86 offset:16
mvv 30,66,228
mvv 22,232,70
R 54,128
mvv 26,236,70
mvv 30,70,240
s_waitcnt lgkmcnt(3)
mvv 22,244,74
R 58,192
mvv 26,248,74
mvv 30,74,252
v_mfma_f32_16x16x32_bf16 v[34:37], a[0:3], v[46:49], 0
R 62,256
v_mul_f32 v120, v119, v96
v_mfma_f32_16x16x32_bf16 v[38:41], a[4:7], v[46:49], 0
s_waitcnt lgkmcnt(2)
v_fma_f32 v22, v22, v118, v120
v_mul_f32 v121, v119, v97
v_mfma_f32_16x16x32_bf16 v[42:45], v[46:49], a[8:11], 0
v_fma_f32 v23, v23, v118, v121
v_mul_f32 v120, v119, v98
mav 34,12,50
R 66,320
v_fma_f32 v24, v24, v118, v120
)"
R"(
mav 38,16,50
v_mul_f32 v121, v119, v99
v_fma_f32 v25, v25, v118, v121
mva 42,50,20
v_mul_f32 v120, v119, v100
v_fma_f32 v26, v26, v118, v120
mav 34,24,54
R 70,384
v_mul_f32 v121, v119, v101
mav 38,28,54
v_fma_f32 v27, v27, v118, v121
v_mul_f32 v120, v119, v102
mva 42,54,32
s_waitcnt lgkmcnt(2)
v_fma_f32 v28, v28, v118, v120
v_mul_f32 v121, v119, v103
mav 34,36,58
R 74,448
v_fma_f32 v29, v29, v118, v121
mav 38,40,58
v_cvt_pk_bf16_f32 v112, v22, v23
v_cvt_pk_bf16_f32 v113, v24, v25
mva 42,58,44
v_cvt_pk_bf16_f32 v114, v26, v27
v_cvt_pk_bf16_f32 v115, v28, v29
mav 34,48,62
R 46,512
v_mul_f32 v105, v105, v95
mav 38,52,62
ds_write_b128 v84, v[112:115] offset:0
mva 42,62,56
s_waitcnt lgkmcnt(3)
v_fma_f32 v30, v30, v104, v105
v_mul_f32 v107, v107, v95
mav 34,60,66
R 50,576
v_fma_f32 v31, v31, v106, v107
mav 38,64,66
v_mul_f32 v109, v109, v95
v_fma_f32 v32, v32, v108, v109
mva 42,66,68
v_mul_f32 v111, v111, v95
v_fma_f32 v33, v33, v110, v111
mav 34,72,70
R 54,640
v_cvt_pk_bf16_f32 v78, v30, v31
mav 38,76,70
v_cvt_pk_bf16_f32 v79, v32, v33
mva 42,70,80
s_waitcnt lgkmcnt(2)
ds_write_b64 v84, v[78:79] offset:1024
mav 34,84,74
R 58,704
mav 38,88,74
mva 42,74,92
mav 34,96,46
s_waitcnt lgkmcnt(1)
R 62,768
mav 38,100,46
s_barrier
mva 42,46,104
mav 34,108,50
R 66,832
mav 38,112,50
mva 42,50,116
mav 34,120,54
R 70,896
mav 38,124,54
mva 42,54,128
s_waitcnt lgkmcnt(2)
mav 34,132,58
R 74,960
mav 38,136,58
mva 42,58,140
mav 34,144,62
R 46,1024
mav 38,148,62
mva 42,62,152
s_waitcnt lgkmcnt(2)
mav 34,156,66
R 50,1088
mav 38,160,66
mva 42,66,164
mav 34,168,70
R 54,1152
mav 38,172,70
mva 42,70,176
s_waitcnt lgkmcnt(2)
mav 34,180,74
R 58,1216
mav 38,184,74
mva 42,74,188
mav 34,192,46
R 62,1280
mav 38,196,46
mva 42,46,200
s_waitcnt lgkmcnt(2)
mav 34,204,50
R 66,1344
mav 38,208,50
mva 42,50,212
mav 34,216,54
R 70,1408
mav 38,220,54
mva 42,54,224
s_waitcnt lgkmcnt(2)
mav 34,228,58
R 74,1472
mav 38,232,58
mva 42,58,236
mav 34,240,62
R 46,1536
mav 38,244,62
mva 42,62,248
s_waitcnt lgkmcnt(2)
mav 34,252,66
R 50,1600
mvv 38,128,66
mvv 42,66,132
mvv 34,136,70
R 54,1664
mvv 38,140,70
mvv 42,70,144
s_waitcnt lgkmcnt(2)
mvv 34,148,74
R 58,1728
mvv 38,152,74
mvv 42,74,156
mvv 34,160,46
R 62,1792
mvv 38,164,46
mvv 42,46,168
s_waitcnt lgkmcnt(2)
mvv 34,172,50
R 66,1856
mvv 38,176,50
mvv 42,50,180
mvv 34,184,54
R 70,1920
mvv 38,188,54
mvv 42,54,192
s_waitcnt lgkmcnt(2)
mvv 34,196,58
R 74,1984
mvv 38,200,58
mvv 42,58,204
mvv 34,208,62
mvv 38,212,62
ds_read_b64 v[118:119], v85 offset:128
mvv 42,62,216
s_waitcnt lgkmcnt(2)
ds_read_b128 v[104:107], v86 offset:128
mvv 34,220,66
ds_read_b128 v[108:111], v86 offset:144
mvv 38,224,66
mvv 42,66,228
mvv 34,232,70
mvv 38,236,70
mvv 42,70,240
s_waitcnt lgkmcnt(1)
mvv 34,244,74
mvv 38,248,74
mvv 42,74,252
v_mul_f32 v120, v119, v96
s_nop 4
v_fma_f32 v34, v34, v118, v120
v_mul_f32 v121, v119, v97
v_fma_f32 v35, v35, v118, v121
v_mul_f32 v120, v119, v98
v_fma_f32 v36, v36, v118, v120
v_mul_f32 v121, v119, v99
v_fma_f32 v37, v37, v118, v121
v_mul_f32 v120, v119, v100
v_fma_f32 v38, v38, v118, v120
v_mul_f32 v121, v119, v101
v_fma_f32 v39, v39, v118, v121
v_mul_f32 v120, v119, v102
v_fma_f32 v40, v40, v118, v120
v_mul_f32 v121, v119, v103
v_fma_f32 v41, v41, v118, v121
v_cvt_pk_bf16_f32 v112, v34, v35
v_cvt_pk_bf16_f32 v113, v36, v37
v_cvt_pk_bf16_f32 v114, v38, v39
v_cvt_pk_bf16_f32 v115, v40, v41
ds_write_b128 v84, v[112:115] offset:4096
v_mul_f32 v105, v105, v95
v_fma_f32 v42, v42, v104, v105
v_mul_f32 v107, v107, v95
v_fma_f32 v43, v43, v106, v107
s_waitcnt lgkmcnt(1)
v_mul_f32 v109, v109, v95
v_fma_f32 v44, v44, v108, v109
v_mul_f32 v111, v111, v95
v_fma_f32 v45, v45, v110, v111
v_cvt_pk_bf16_f32 v78, v42, v43
v_cvt_pk_bf16_f32 v79, v44, v45
ds_write_b64 v84, v[78:79] offset:5120
s_waitcnt lgkmcnt(0)
s_barrier
s_endpgm
CQB0_1:
s_mov_b32 s58, m0
s_mov_b32 m0, 144384
DQB0_1:
s_cmp_eq_u32 s61, 0
s_cbranch_scc1 EQB0_1
s_sub_u32 s61, s61, 1
buffer_load_dword v91, s[36:39], 0 offen sc1 lds
s_sleep 1
ds_read_b32 v122, v83
s_waitcnt lgkmcnt(0)
v_cmp_gt_i32 vcc, v93, v122
s_cbranch_vccnz DQB0_1
EQB0_1:
s_mov_b32 m0, s58
s_branch BQB0_1
CQB0_2:
s_mov_b32 s58, m0
s_mov_b32 m0, 144384
DQB0_2:
s_cmp_eq_u32 s61, 0
s_cbranch_scc1 EQB0_2
s_sub_u32 s61, s61, 1
buffer_load_dword v91, s[36:39], 0 offen sc1 lds
s_sleep 1
ds_read_b32 v122, v83
s_waitcnt lgkmcnt(0)
v_cmp_gt_i32 vcc, v93, v122
s_cbranch_vccnz DQB0_2
EQB0_2:
s_mov_b32 m0, s58
s_branch BQB0_2
CQB0_3:
s_mov_b32 s58, m0
s_mov_b32 m0, 144384
DQB0_3:
s_cmp_eq_u32 s61, 0
s_cbranch_scc1 EQB0_3
s_sub_u32 s61, s61, 1
buffer_load_dword v91, s[36:39], 0 offen sc1 lds
s_sleep 1
ds_read_b32 v122, v83
s_waitcnt lgkmcnt(0)
v_cmp_gt_i32 vcc, v93, v122
s_cbranch_vccnz DQB0_3
EQB0_3:
s_mov_b32 m0, s58
s_branch BQB0_3
CQB0_5:
s_mov_b32 s58, m0
s_mov_b32 m0, 144384
DQB0_5:
s_cmp_eq_u32 s61, 0
s_cbranch_scc1 EQB0_5
s_sub_u32 s61, s61, 1
buffer_load_dword v91, s[36:39], 0 offen sc1 lds
s_sleep 1
ds_read_b32 v122, v83
s_waitcnt lgkmcnt(0)
v_cmp_gt_i32 vcc, v93, v122
s_cbranch_vccnz DQB0_5
EQB0_5:
s_mov_b32 m0, s58
s_branch BQB0_5
CQB0_6:
s_mov_b32 s58, m0
s_mov_b32 m0, 144384
DQB0_6:
s_cmp_eq_u32 s61, 0
s_cbranch_scc1 EQB0_6
s_sub_u32 s61, s61, 1
buffer_load_dword v91, s[36:39], 0 offen sc1 lds
s_sleep 1
ds_read_b32 v122, v83
s_waitcnt lgkmcnt(0)
v_cmp_gt_i32 vcc, v93, v122
s_cbranch_vccnz DQB0_6
EQB0_6:
s_mov_b32 m0, s58
s_branch BQB0_6
CQB0_7:
s_mov_b32 s58, m0
s_mov_b32 m0, 144384
DQB0_7:
s_cmp_eq_u32 s61, 0
s_cbranch_scc1 EQB0_7
s_sub_u32 s61, s61, 1
buffer_load_dword v91, s[36:39], 0 offen sc1 lds
s_sleep 1
ds_read_b32 v122, v83
s_waitcnt lgkmcnt(0)
v_cmp_gt_i32 vcc, v93, v122
s_cbranch_vccnz DQB0_7
EQB0_7:
s_mov_b32 m0, s58
s_branch BQB0_7
CQB0_8:
s_mov_b32 s58, m0
s_mov_b32 m0, 144384
DQB0_8:
s_cmp_eq_u32 s61, 0
s_cbranch_scc1 EQB0_8
s_sub_u32 s61, s61, 1
buffer_load_dword v91, s[36:39], 0 offen sc1 lds
s_sleep 1
ds_read_b32 v122, v83
s_waitcnt lgkmcnt(0)
v_cmp_gt_i32 vcc, v93, v122
s_cbranch_vccnz DQB0_8
EQB0_8:
s_mov_b32 m0, s58
s_branch BQB0_8
LvQB1:
LsQ21:
s_cbranch_execz LsQ25
ds_read_b64 v[118:119], v85 offset:0
ds_read_b128 v[104:107], v86 offset:0
ds_read_b128 v[108:111], v86 offset:16
s_add_u32 s49, s49, 313344
s_waitcnt lgkmcnt(0)
v_mul_f32 v120, v119, v96
v_fma_f32 v22, v22, v118, v120
v_mul_f32 v121, v119, v97
v_fma_f32 v23, v23, v118, v121
v_mul_f32 v120, v119, v98
v_fma_f32 v24, v24, v118, v120
v_mul_f32 v121, v119, v99
v_fma_f32 v25, v25, v118, v121
v_mul_f32 v120, v119, v100
v_fma_f32 v26, v26, v118, v120
v_mul_f32 v121, v119, v101
v_fma_f32 v27, v27, v118, v121
v_mul_f32 v120, v119, v102
v_fma_f32 v28, v28, v118, v120
v_mul_f32 v121, v119, v103
v_fma_f32 v29, v29, v118, v121
v_cvt_pk_bf16_f32 v112, v22, v23
v_cvt_pk_bf16_f32 v113, v24, v25
v_cvt_pk_bf16_f32 v114, v26, v27
v_cvt_pk_bf16_f32 v115, v28, v29
ds_write_b128 v84, v[112:115] offset:0
v_mul_f32 v105, v105, v95
v_fma_f32 v30, v30, v104, v105
v_mul_f32 v107, v107, v95
v_fma_f32 v31, v31, v106, v107
v_mul_f32 v109, v109, v95
v_fma_f32 v32, v32, v108, v109
v_mul_f32 v111, v111, v95
v_fma_f32 v33, v33, v110, v111
v_cvt_pk_bf16_f32 v78, v30, v31
v_cvt_pk_bf16_f32 v79, v32, v33
ds_write_b64 v84, v[78:79] offset:1024
s_xor_b32 s50, s50, s57
s_waitcnt vmcnt(17) lgkmcnt(0)
s_add_u32 s49, s49, 313344
s_barrier
R 46,33024
R 50,33088
R 54,33152
R 58,33216
s_waitcnt vmcnt(16)
v_permlane32_swap_b32 v14, v15
v_add_f32 v16, v14, v15
v_add_f32 v17, v14, v15
s_nop 1
v_permlane16_swap_b32 v16, v17
v_add_f32 v14, v16, v17
v_add_f32 v15, v16, v17
s_nop 1
v_permlane32_swap_b32 v14, v15
v_mul_f32 v16, 0x3a800000, v14
v_mul_f32 v17, 0x3a800000, v15
v_fma_f32 v17, -v16, v16, v17
v_max_f32 v17, 0, v17
v_add_f32 v17, 0x3727c5ac, v17
v_rsq_f32 v14, v17
s_nop 0
v_mul_f32_e64 v15, -v16, v14
ds_read_b64 v[118:119], v85 offset:128
ds_read_b128 v[104:107], v86 offset:128
ds_read_b128 v[108:111], v86 offset:144
s_waitcnt lgkmcnt(0)
v_mfma_f32_16x16x32_bf16 v[22:25], a[0:3], v[46:49], 0
R 62,33280
v_mul_f32 v120, v119, v96
v_mfma_f32_16x16x32_bf16 v[26:29], a[4:7], v[46:49], 0
v_fma_f32 v34, v34, v118, v120
v_mul_f32 v121, v119, v97
v_mfma_f32_16x16x32_bf16 v[30:33], v[46:49], a[8:11], 0
v_fma_f32 v35, v35, v118, v121
v_mul_f32 v120, v119, v98
mav 22,12,50
R 66,33344
v_fma_f32 v36, v36, v118, v120
mav 26,16,50
v_mul_f32 v121, v119, v99
v_fma_f32 v37, v37, v118, v121
mva 30,50,20
v_mul_f32 v120, v119, v100
v_fma_f32 v38, v38, v118, v120
mav 22,24,54
R 70,33408
v_mul_f32 v121, v119, v101
mav 26,28,54
v_fma_f32 v39, v39, v118, v121
v_mul_f32 v120, v119, v102
mva 30,54,32
v_fma_f32 v40, v40, v118, v120
v_mul_f32 v121, v119, v103
mav 22,36,58
R 74,33472
v_fma_f32 v41, v41, v118, v121
mav 26,40,58
v_cvt_pk_bf16_f32 v112, v34, v35
v_cvt_pk_bf16_f32 v113, v36, v37
mva 30,58,44
s_waitcnt lgkmcnt(2)
v_cvt_pk_bf16_f32 v114, v38, v39
v_cvt_pk_bf16_f32 v115, v40, v41
mav 22,48,62
R 46,33536
v_mul_f32 v105, v105, v95
mav 26,52,62
ds_write_b128 v84, v[112:115] offset:4096
mva 30,62,56
v_fma_f32 v42, v42, v104, v105
v_mul_f32 v107, v107, v95
mav 22,60,66
R 50,33600
v_fma_f32 v43, v43, v106, v107
mav 26,64,66
v_mul_f32 v109, v109, v95
v_fma_f32 v44, v44, v108, v109
mva 30,66,68
s_waitcnt lgkmcnt(3)
v_mul_f32 v111, v111, v95
v_fma_f32 v45, v45, v110, v111
mav 22,72,70
R 54,33664
v_cvt_pk_bf16_f32 v78, v42, v43
mav 26,76,70
v_cvt_pk_bf16_f32 v79, v44, v45
mva 30,70,80
ds_write_b64 v84, v[78:79] offset:5120
mav 22,84,74
R 58,33728
mav 26,88,74
mva 30,74,92
s_waitcnt lgkmcnt(3)
mav 22,96,46
s_waitcnt vmcnt(0) lgkmcnt(1)
R 62,33792
s_add_u32 s49, s49, 313344
mav 26,100,46
s_barrier
mva 30,46,104
ds_write_b64 v85, v[14:15] offset:128
mav 22,108,50
s_sub_u32 s54, s54, 0x8000
R 66,33856
s_add_u32 m0, s50, 33024
mav 26,112,50
s_cmp_lt_u32 s54, s56
s_cselect_b64 s[44:45], s[40:41], s[44:45]
mva 30,50,116
D2 87,54,0
mav 22,120,54
R 70,33920
mav 26,124,54
D2 87,54,1024
mva 30,54,128
s_waitcnt lgkmcnt(2)
s_add_u32 s55, s54, 14336
mav 22,132,58
D2 87,55,2048
mav 26,136,58
R 74,33984
mva 30,58,140
D2 87,55,3072
mav 22,144,62
s_add_u32 m0, s50, 41280
R 46,34048
mav 26,148,62
D2 88,54,0
mva 30,62,152
s_waitcnt lgkmcnt(2)
mav 22,156,66
D2 88,54,1024
mav 26,160,66
R 50,34112
mva 30,66,164
D2 88,55,2048
mav 22,168,70
R 54,34176
mav 26,172,70
D2 88,55,3072
mva 30,70,176
s_waitcnt lgkmcnt(2)
s_add_u32 m0, s50, 49536
mav 22,180,74
D2 89,54,0
mav 26,184,74
R 58,34240
mva 30,74,188
D2 89,54,1024
mav 22,192,46
R 62,34304
mav 26,196,46
D2 89,55,2048
mva 30,46,200
s_waitcnt lgkmcnt(2)
mav 22,204,50
D2 89,55,3072
mav 26,208,50
s_add_u32 m0, s50, 57792
R 66,34368
mva 30,50,212
D2 90,54,0
mav 22,216,54
R 70,34432
mav 26,220,54
D2 90,54,1024
mva 30,54,224
s_waitcnt lgkmcnt(2)
mav 22,228,58
D2 90,55,2048
mav 26,232,58
R 74,34496
mva 30,58,236
D2 90,55,3072
mav 22,240,62
s_sub_u32 s54, s54, 0x8000
R 46,34560
s_add_u32 m0, s50, 0
mav 26,244,62
mva 30,62,248
s_waitcnt lgkmcnt(2)
mav 22,252,66
D2 87,54,0
mvv 26,128,66
s_add_u32 s55, s54, 14336
R 50,34624
mvv 30,66,132
D2 87,54,1024
mvv 22,136,70
R 54,34688
mvv 26,140,70
D2 87,55,2048
mvv 30,70,144
s_waitcnt lgkmcnt(2)
mvv 22,148,74
D2 87,55,3072
mvv 26,152,74
s_add_u32 m0, s50, 8256
R 58,34752
mvv 30,74,156
D2 88,54,0
mvv 22,160,46
R 62,34816
mvv 26,164,46
D2 88,54,1024
mvv 30,46,168
s_waitcnt lgkmcnt(2)
mvv 22,172,50
D2 88,55,2048
mvv 26,176,50
R 66,34880
mvv 30,50,180
D2 88,55,3072
mvv 22,184,54
s_add_u32 m0, s50, 16512
R 70,34944
mvv 26,188,54
D2 89,54,0
mvv 30,54,192
s_waitcnt lgkmcnt(2)
mvv 22,196,58
D2 89,54,1024
mvv 26,200,58
R 74,35008
mvv 30,58,204
D2 89,55,2048
mvv 22,208,62
R 46,0
mvv 26,212,62
D2 89,55,3072
mvv 30,62,216
s_waitcnt lgkmcnt(2)
LsQ22:
s_cbranch_execz LwQA
ds_read_b64 v[118:119], v85 offset:0
s_add_u32 m0, s50, 24768
mvv 22,220,66
D2 90,54,0
mvv 26,224,66
R 50,64
mvv 30,66,228
D2 90,54,1024
mvv 22,232,70
R 54,128
mvv 26,236,70
D2 90,55,2048
mvv 30,70,240
s_waitcnt lgkmcnt(2)
ds_read_b128 v[104:107], v86 offset:0
mvv 22,244,74
D2 90,55,3072
mvv 26,248,74
R 58,192
mvv 30,74,252
ds_read_b128 v[108:111], v86 offset:16
v_mfma_f32_16x16x32_bf16 v[34:37], a[0:3], v[46:49], 0
R 62,256
v_mul_f32 v120, v119, v96
v_mfma_f32_16x16x32_bf16 v[38:41], a[4:7], v[46:49], 0
v_fma_f32 v22, v22, v118, v120
v_mul_f32 v121, v119, v97
v_mfma_f32_16x16x32_bf16 v[42:45], v[46:49], a[8:11], 0
s_waitcnt lgkmcnt(3)
v_fma_f32 v23, v23, v118, v121
v_mul_f32 v120, v119, v98
mav 34,12,50
R 66,320
v_fma_f32 v24, v24, v118, v120
mav 38,16,50
v_mul_f32 v121, v119, v99
v_fma_f32 v25, v25, v118, v121
mva 42,50,20
v_mul_f32 v120, v119, v100
v_fma_f32 v26, v26, v118, v120
mav 34,24,54
s_waitcnt lgkmcnt(1)
R 70,384
v_mul_f32 v121, v119, v101
mav 38,28,54
v_fma_f32 v27, v27, v118, v121
v_mul_f32 v120, v119, v102
mva 42,54,32
v_fma_f32 v28, v28, v118, v120
v_mul_f32 v121, v119, v103
mav 34,36,58
R 74,448
v_fma_f32 v29, v29, v118, v121
mav 38,40,58
v_cvt_pk_bf16_f32 v112, v22, v23
v_cvt_pk_bf16_f32 v113, v24, v25
mva 42,58,44
v_cvt_pk_bf16_f32 v114, v26, v27
v_cvt_pk_bf16_f32 v115, v28, v29
mav 34,48,62
R 46,512
v_mul_f32 v105, v105, v95
mav 38,52,62
ds_write_b128 v84, v[112:115] offset:0
mva 42,62,56
s_waitcnt lgkmcnt(3)
v_fma_f32 v30, v30, v104, v105
v_mul_f32 v107, v107, v95
mav 34,60,66
R 50,576
v_fma_f32 v31, v31, v106, v107
mav 38,64,66
v_mul_f32 v109, v109, v95
v_fma_f32 v32, v32, v108, v109
mva 42,66,68
v_mul_f32 v111, v111, v95
v_fma_f32 v33, v33, v110, v111
mav 34,72,70
R 54,640
v_cvt_pk_bf16_f32 v78, v30, v31
mav 38,76,70
v_cvt_pk_bf16_f32 v79, v32, v33
mva 42,70,80
s_waitcnt lgkmcnt(2)
ds_write_b64 v84, v[78:79] offset:1024
mav 34,84,74
s_xor_b32 s50, s50, s57
R 58,704
mav 38,88,74
mva 42,74,92
mav 34,96,46
s_waitcnt vmcnt(16) lgkmcnt(1)
R 62,768
s_add_u32 s49, s49, 313344
mav 38,100,46
s_barrier
mva 42,46,104
s_sub_u32 s54, s54, 0x8000
s_cmp_lt_u32 s54, s56
s_cselect_b64 s[44:45], s[40:41], s[44:45]
mav 34,108,50
buffer_load_dwordx2 v[14:15], v94, s[36:39], s49 offen sc1
mav 38,112,50
s_add_u32 m0, s50, 33024
R 66,832
mva 42,50,116
D2 87,54,0
mav 34,120,54
R 70,896
mav 38,124,54
D2 87,54,1024
mva 42,54,128
s_waitcnt lgkmcnt(2)
s_add_u32 s55, s54, 14336
mav 34,132,58
D2 87,55,2048
mav 38,136,58
R 74,960
mva 42,58,140
D2 87,55,3072
mav 34,144,62
s_add_u32 m0, s50, 41280
R 46,1024
mav 38,148,62
D2 88,54,0
mva 42,62,152
s_waitcnt lgkmcnt(2)
mav 34,156,66
D2 88,54,1024
mav 38,160,66
R 50,1088
mva 42,66,164
D2 88,55,2048
mav 34,168,70
R 54,1152
mav 38,172,70
D2 88,55,3072
mva 42,70,176
s_waitcnt lgkmcnt(2)
s_add_u32 m0, s50, 49536
mav 34,180,74
D2 89,54,0
mav 38,184,74
R 58,1216
mva 42,74,188
D2 89,54,1024
mav 34,192,46
R 62,1280
mav 38,196,46
D2 89,55,2048
mva 42,46,200
s_waitcnt lgkmcnt(2)
mav 34,204,50
D2 89,55,3072
mav 38,208,50
s_add_u32 m0, s50, 57792
R 66,1344
mva 42,50,212
D2 90,54,0
mav 34,216,54
R 70,1408
mav 38,220,54
D2 90,54,1024
mva 42,54,224
s_waitcnt lgkmcnt(2)
mav 34,228,58
D2 90,55,2048
mav 38,232,58
R 74,1472
mva 42,58,236
D2 90,55,3072
mav 34,240,62
R 46,1536
mav 38,244,62
mva 42,62,248
s_waitcnt lgkmcnt(2)
mav 34,252,66
R 50,1600
mvv 38,128,66
mvv 42,66,132
mvv 34,136,70
R 54,1664
mvv 38,140,70
mvv 42,70,144
s_waitcnt lgkmcnt(2)
mvv 34,148,74
R 58,1728
mvv 38,152,74
mvv 42,74,156
mvv 34,160,46
R 62,1792
mvv 38,164,46
mvv 42,46,168
s_waitcnt vmcnt(16) lgkmcnt(2)
v_permlane32_swap_b32 v14, v15
mvv 34,172,50
R 66,1856
v_add_f32 v16, v14, v15
mvv 38,176,50
mvv 42,50,180
v_add_f32 v17, v14, v15
mvv 34,184,54
R 70,1920
mvv 38,188,54
v_permlane16_swap_b32 v16, v17
mvv 42,54,192
s_waitcnt lgkmcnt(2)
v_add_f32 v14, v16, v17
v_add_f32 v15, v16, v17
mvv 34,196,58
R 74,1984
v_xor_b32 v80, v81, v80
mvv 38,200,58
v_permlane32_swap_b32 v14, v15
mvv 42,58,204
v_mul_f32 v16, 0x3a800000, v14
v_mul_f32 v17, 0x3a800000, v15
mvv 34,208,62
R 46,33024
v_fma_f32 v17, -v16, v16, v17
mvv 38,212,62
ds_read_b64 v[118:119], v85 offset:128
v_max_f32 v17, 0, v17
mvv 42,62,216
s_waitcnt lgkmcnt(3)
ds_read_b128 v[104:107], v86 offset:128
v_add_f32 v17, 0x3727c5ac, v17
mvv 34,220,66
R 50,33088
mvv 38,224,66
ds_read_b128 v[108:111], v86 offset:144
mvv 42,66,228
v_rsq_f32 v14, v17
mvv 34,232,70
R 54,33152
v_mul_f32_e64 v15, -v16, v14
mvv 38,236,70
mvv 42,70,240
s_waitcnt lgkmcnt(3)
mvv 34,244,74
R 58,33216
mvv 38,248,74
mvv 42,74,252
v_mfma_f32_16x16x32_bf16 v[22:25], a[0:3], v[46:49], 0
R 62,33280
v_mul_f32 v120, v119, v96
v_mfma_f32_16x16x32_bf16 v[26:29], a[4:7], v[46:49], 0
s_waitcnt lgkmcnt(2)
v_fma_f32 v34, v34, v118, v120
v_mul_f32 v121, v119, v97
v_mfma_f32_16x16x32_bf16 v[30:33], v[46:49], a[8:11], 0
v_fma_f32 v35, v35, v118, v121
v_mul_f32 v120, v119, v98
mav 22,12,50
R 66,33344
v_fma_f32 v36, v36, v118, v120
mav 26,16,50
v_mul_f32 v121, v119, v99
v_fma_f32 v37, v37, v118, v121
mva 30,50,20
v_mul_f32 v120, v119, v100
v_fma_f32 v38, v38, v118, v120
mav 22,24,54
R 70,33408
v_mul_f32 v121, v119, v101
mav 26,28,54
v_fma_f32 v39, v39, v118, v121
v_mul_f32 v120, v119, v102
mva 30,54,32
s_waitcnt lgkmcnt(2)
v_fma_f32 v40, v40, v118, v120
v_mul_f32 v121, v119, v103
mav 22,36,58
R 74,33472
v_fma_f32 v41, v41, v118, v121
mav 26,40,58
v_cvt_pk_bf16_f32 v112, v34, v35
v_cvt_pk_bf16_f32 v113, v36, v37
mva 30,58,44
v_cvt_pk_bf16_f32 v114, v38, v39
v_cvt_pk_bf16_f32 v115, v40, v41
mav 22,48,62
R 46,33536
v_mul_f32 v105, v105, v95
mav 26,52,62
ds_write_b128 v84, v[112:115] offset:4096
mva 30,62,56
s_waitcnt lgkmcnt(3)
v_fma_f32 v42, v42, v104, v105
v_mul_f32 v107, v107, v95
mav 22,60,66
R 50,33600
v_fma_f32 v43, v43, v106, v107
mav 26,64,66
v_mul_f32 v109, v109, v95
v_fma_f32 v44, v44, v108, v109
mva 30,66,68
v_mul_f32 v111, v111, v95
v_fma_f32 v45, v45, v110, v111
mav 22,72,70
R 54,33664
v_cvt_pk_bf16_f32 v78, v42, v43
mav 26,76,70
v_cvt_pk_bf16_f32 v79, v44, v45
mva 30,70,80
s_waitcnt lgkmcnt(2)
ds_write_b64 v84, v[78:79] offset:5120
mav 22,84,74
R 58,33728
mav 26,88,74
ds_write_b64 v85, v[14:15] offset:0
mva 30,74,92
mav 22,96,46
s_add_u32 s49, s49, 313344
R 62,33792
mav 26,100,46
s_waitcnt lgkmcnt(1)
mva 30,46,104
s_barrier
mav 22,108,50
buffer_load_dwordx2 v[14:15], v94, s[36:39], s49 offen sc1
mav 26,112,50
s_sub_u32 s54, s54, 0x8000
R 66,33856
s_add_u32 m0, s50, 0
mva 30,50,116
D2 87,54,0
mav 22,120,54
R 70,33920
mav 26,124,54
D2 87,54,1024
mva 30,54,128
s_add_u32 s55, s54, 14336
mav 22,132,58
D2 87,55,2048
mav 26,136,58
R 74,33984
mva 30,58,140
s_waitcnt lgkmcnt(2)
D2 87,55,3072
mav 22,144,62
s_add_u32 m0, s50, 8256
R 46,34048
mav 26,148,62
D2 88,54,0
mva 30,62,152
mav 22,156,66
D2 88,54,1024
mav 26,160,66
R 50,34112
mva 30,66,164
s_waitcnt lgkmcnt(2)
D2 88,55,2048
mav 22,168,70
R 54,34176
mav 26,172,70
D2 88,55,3072
mva 30,70,176
s_add_u32 m0, s50, 16512
mav 22,180,74
D2 89,54,0
mav 26,184,74
R 58,34240
mva 30,74,188
s_waitcnt lgkmcnt(2)
D2 89,54,1024
mav 22,192,46
R 62,34304
mav 26,196,46
D2 89,55,2048
mva 30,46,200
mav 22,204,50
D2 89,55,3072
mav 26,208,50
s_add_u32 m0, s50, 24768
R 66,34368
mva 30,50,212
s_waitcnt lgkmcnt(2)
D2 90,54,0
mav 22,216,54
R 70,34432
mav 26,220,54
D2 90,54,1024
mva 30,54,224
mav 22,228,58
D2 90,55,2048
mav 26,232,58
R 74,34496
mva 30,58,236
s_waitcnt lgkmcnt(2)
D2 90,55,3072
mav 22,240,62
R 46,34560
mav 26,244,62
mva 30,62,248
mav 22,252,66
R 50,34624
mvv 26,128,66
mvv 30,66,132
s_waitcnt lgkmcnt(2)
mvv 22,136,70
R 54,34688
mvv 26,140,70
mvv 30,70,144
mvv 22,148,74
R 58,34752
mvv 26,152,74
mvv 30,74,156
s_waitcnt lgkmcnt(2)
mvv 22,160,46
R 62,34816
mvv 26,164,46
mvv 30,46,168
s_waitcnt vmcnt(16)
v_permlane32_swap_b32 v14, v15
mvv 22,172,50
R 66,34880
v_add_f32 v16, v14, v15
mvv 26,176,50
v_add_f32 v17, v14, v15
mvv 30,50,180
s_waitcnt lgkmcnt(2)
v_permlane16_swap_b32 v16, v17
mvv 22,184,54
R 70,34944
v_add_f32 v14, v16, v17
mvv 26,188,54
v_add_f32 v15, v16, v17
mvv 30,54,192
mvv 22,196,58
R 74,35008
mvv 26,200,58
v_permlane32_swap_b32 v14, v15
mvv 30,58,204
s_waitcnt lgkmcnt(2)
v_mul_f32 v16, 0x3a800000, v14
v_mul_f32 v17, 0x3a800000, v15
mvv 22,208,62
R 46,0
v_fma_f32 v17, -v16, v16, v17
mvv 26,212,62
ds_read_b64 v[118:119], v85 offset:0
v_max_f32 v17, 0, v17
mvv 30,62,216
ds_read_b128 v[104:107], v86 offset:0
v_add_f32 v17, 0x3727c5ac, v17
mvv 22,220,66
R 50,64
mvv 26,224,66
ds_read_b128 v[108:111], v86 offset:16
mvv 30,66,228
s_waitcnt lgkmcnt(5)
v_rsq_f32 v14, v17
mvv 22,232,70
R 54,128
v_mul_f32_e64 v15, -v16, v14
mvv 26,236,70
mvv 30,70,240
mvv 22,244,74
R 58,192
mvv 26,248,74
mvv 30,74,252
s_waitcnt lgkmcnt(2)
v_mfma_f32_16x16x32_bf16 v[34:37], a[0:3], v[46:49], 0
R 62,256
v_mul_f32 v120, v119, v96
v_mfma_f32_16x16x32_bf16 v[38:41], a[4:7], v[46:49], 0
v_fma_f32 v22, v22, v118, v120
v_mul_f32 v121, v119, v97
v_mfma_f32_16x16x32_bf16 v[42:45], v[46:49], a[8:11], 0
v_fma_f32 v23, v23, v118, v121
v_mul_f32 v120, v119, v98
mav 34,12,50
R 66,320
v_fma_f32 v24, v24, v118, v120
mav 38,16,50
v_mul_f32 v121, v119, v99
v_fma_f32 v25, v25, v118, v121
mva 42,50,20
s_waitcnt lgkmcnt(2)
v_mul_f32 v120, v119, v100
v_fma_f32 v26, v26, v118, v120
mav 34,24,54
R 70,384
v_mul_f32 v121, v119, v101
mav 38,28,54
v_fma_f32 v27, v27, v118, v121
v_mul_f32 v120, v119, v102
mva 42,54,32
v_fma_f32 v28, v28, v118, v120
v_mul_f32 v121, v119, v103
mav 34,36,58
R 74,448
v_fma_f32 v29, v29, v118, v121
mav 38,40,58
v_cvt_pk_bf16_f32 v112, v22, v23
v_cvt_pk_bf16_f32 v113, v24, v25
mva 42,58,44
s_waitcnt lgkmcnt(2)
v_cvt_pk_bf16_f32 v114, v26, v27
v_cvt_pk_bf16_f32 v115, v28, v29
mav 34,48,62
R 46,512
v_mul_f32 v105, v105, v95
mav 38,52,62
ds_write_b128 v84, v[112:115] offset:0
mva 42,62,56
v_fma_f32 v30, v30, v104, v105
v_mul_f32 v107, v107, v95
mav 34,60,66
R 50,576
v_fma_f32 v31, v31, v106, v107
mav 38,64,66
v_mul_f32 v109, v109, v95
v_fma_f32 v32, v32, v108, v109
mva 42,66,68
s_waitcnt lgkmcnt(3)
v_mul_f32 v111, v111, v95
v_fma_f32 v33, v33, v110, v111
mav 34,72,70
R 54,640
v_cvt_pk_bf16_f32 v78, v30, v31
mav 38,76,70
v_cvt_pk_bf16_f32 v79, v32, v33
mva 42,70,80
ds_write_b64 v84, v[78:79] offset:1024
mav 34,84,74
s_xor_b32 s50, s50, s57
R 58,704
mav 38,88,74
ds_write_b64 v85, v[14:15] offset:128
mva 42,74,92
s_waitcnt lgkmcnt(4)
mav 34,96,46
s_add_u32 s49, s49, 313344
R 62,768
mav 38,100,46
s_waitcnt lgkmcnt(1)
s_sub_u32 s54, s54, 0x8000
mva 42,46,104
s_barrier
mav 34,108,50
buffer_load_dwordx2 v[14:15], v94, s[36:39], s49 offen sc1
mav 38,112,50
s_add_u32 m0, s50, 33024
R 66,832
mva 42,50,116
s_cmp_lt_u32 s54, s56
s_cselect_b64 s[44:45], s[40:41], s[44:45]
mav 34,120,54
R 70,896
mav 38,124,54
D2 87,54,0
mva 42,54,128
s_add_u32 s55, s54, 14336
mav 34,132,58
D2 87,54,1024
mav 38,136,58
R 74,960
mva 42,58,140
s_waitcnt lgkmcnt(2)
D2 87,55,2048
mav 34,144,62
R 46,1024
LsQ23:
s_cbranch_execz LwQB
mav 38,148,62
D2 87,55,3072
mva 42,62,152
s_add_u32 m0, s50, 41280
mav 34,156,66
D2 88,54,0
mav 38,160,66
R 50,1088
mva 42,66,164
s_waitcnt lgkmcnt(2)
D2 88,54,1024
mav 34,168,70
R 54,1152
mav 38,172,70
D2 88,55,2048
mva 42,70,176
mav 34,180,74
D2 88,55,3072
mav 38,184,74
s_add_u32 m0, s50, 49536
R 58,1216
mva 42,74,188
s_waitcnt lgkmcnt(2)
D2 89,54,0
mav 34,192,46
R 62,1280
mav 38,196,46
D2 89,54,1024
mva 42,46,200
mav 34,204,50
D2 89,55,2048
mav 38,208,50
R 66,1344
mva 42,50,212
s_waitcnt lgkmcnt(2)
D2 89,55,3072
mav 34,216,54
s_add_u32 m0, s50, 57792
R 70,1408
mav 38,220,54
D2 90,54,0
mva 42,54,224
mav 34,228,58
D2 90,54,1024
mav 38,232,58
R 74,1472
mva 42,58,236
s_waitcnt lgkmcnt(2)
D2 90,55,2048
mav 34,240,62
R 46,1536
mav 38,244,62
D2 90,55,3072
mva 42,62,248
mav 34,252,66
R 50,1600
mvv 38,128,66
mvv 42,66,132
s_waitcnt lgkmcnt(2)
mvv 34,136,70
R 54,1664
mvv 38,140,70
mvv 42,70,144
mvv 34,148,74
R 58,1728
mvv 38,152,74
mvv 42,74,156
s_waitcnt lgkmcnt(2)
mvv 34,160,46
R 62,1792
mvv 38,164,46
mvv 42,46,168
s_waitcnt vmcnt(16)
v_permlane32_swap_b32 v14, v15
mvv 34,172,50
R 66,1856
v_add_f32 v16, v14, v15
mvv 38,176,50
s_mov_b32 s60, -27
.p2align 6
LQB1_1:
mvv 42,50,180
s_waitcnt lgkmcnt(2)
v_add_f32 v17, v14, v15
mvv 34,184,54
R 70,1920
mvv 38,188,54
v_permlane16_swap_b32 v16, v17
mvv 42,54,192
v_add_f32 v14, v16, v17
v_add_f32 v15, v16, v17
mvv 34,196,58
R 74,1984
v_xor_b32 v80, v81, v80
mvv 38,200,58
v_permlane32_swap_b32 v14, v15
mvv 42,58,204
s_waitcnt lgkmcnt(2)
v_mul_f32 v16, 0x3a800000, v14
v_mul_f32 v17, 0x3a800000, v15
mvv 34,208,62
R 46,33024
v_fma_f32 v17, -v16, v16, v17
mvv 38,212,62
ds_read_b64 v[118:119], v85 offset:128
v_max_f32 v17, 0, v17
mvv 42,62,216
ds_read_b128 v[104:107], v86 offset:128
v_add_f32 v17, 0x3727c5ac, v17
mvv 34,220,66
R 50,33088
mvv 38,224,66
ds_read_b128 v[108:111], v86 offset:144
mvv 42,66,228
s_waitcnt lgkmcnt(5)
v_rsq_f32 v14, v17
mvv 34,232,70
R 54,33152
v_mul_f32_e64 v15, -v16, v14
mvv 38,236,70
mvv 42,70,240
mvv 34,244,74
R 58,33216
mvv 38,248,74
mvv 42,74,252
s_waitcnt lgkmcnt(2)
v_mfma_f32_16x16x32_bf16 v[22:25], a[0:3], v[46:49], 0
R 62,33280
v_mul_f32 v120, v119, v96
v_mfma_f32_16x16x32_bf16 v[26:29], a[4:7], v[46:49], 0
v_fma_f32 v34, v34, v118, v120
v_mul_f32 v121, v119, v97
v_mfma_f32_16x16x32_bf16 v[30:33], v[46:49], a[8:11], 0
v_fma_f32 v35, v35, v118, v121
v_mul_f32 v120, v119, v98
mav 22,12,50
R 66,33344
v_fma_f32 v36, v36, v118, v120
mav 26,16,50
v_mul_f32 v121, v119, v99
v_fma_f32 v37, v37, v118, v121
mva 30,50,20
s_waitcnt lgkmcnt(2)
v_mul_f32 v120, v119, v100
v_fma_f32 v38, v38, v118, v120
mav 22,24,54
R 70,33408
v_mul_f32 v121, v119, v101
mav 26,28,54
v_fma_f32 v39, v39, v118, v121
v_mul_f32 v120, v119, v102
mva 30,54,32
v_fma_f32 v40, v40, v118, v120
v_mul_f32 v121, v119, v103
mav 22,36,58
R 74,33472
v_fma_f32 v41, v41, v118, v121
mav 26,40,58
v_cvt_pk_bf16_f32 v112, v34, v35
v_cvt_pk_bf16_f32 v113, v36, v37
mva 30,58,44
s_waitcnt lgkmcnt(2)
v_cvt_pk_bf16_f32 v114, v38, v39
v_cvt_pk_bf16_f32 v115, v40, v41
mav 22,48,62
R 46,33536
v_mul_f32 v105, v105, v95
mav 26,52,62
ds_write_b128 v84, v[112:115] offset:4096
mva 30,62,56
v_fma_f32 v42, v42, v104, v105
v_mul_f32 v107, v107, v95
mav 22,60,66
R 50,33600
v_fma_f32 v43, v43, v106, v107
mav 26,64,66
v_mul_f32 v109, v109, v95
v_fma_f32 v44, v44, v108, v109
mva 30,66,68
s_waitcnt lgkmcnt(3)
v_mul_f32 v111, v111, v95
v_fma_f32 v45, v45, v110, v111
mav 22,72,70
R 54,33664
v_cvt_pk_bf16_f32 v78, v42, v43
mav 26,76,70
v_cvt_pk_bf16_f32 v79, v44, v45
mva 30,70,80
ds_write_b64 v84, v[78:79] offset:5120
mav 22,84,74
R 58,33728
mav 26,88,74
ds_write_b64 v85, v[14:15] offset:0
mva 30,74,92
s_waitcnt lgkmcnt(4)
mav 22,96,46
s_add_u32 s49, s49, 313344
R 62,33792
mav 26,100,46
s_waitcnt lgkmcnt(1)
mva 30,46,104
s_barrier
mav 22,108,50
buffer_load_dwordx2 v[14:15], v94, s[36:39], s49 offen sc1
mav 26,112,50
s_sub_u32 s54, s54, 0x8000
R 66,33856
s_add_u32 m0, s50, 0
mva 30,50,116
D2 87,54,0
mav 22,120,54
R 70,33920
mav 26,124,54
D2 87,54,1024
mva 30,54,128
s_add_u32 s55, s54, 14336
mav 22,132,58
D2 87,55,2048
mav 26,136,58
R 74,33984
mva 30,58,140
s_waitcnt lgkmcnt(2)
D2 87,55,3072
mav 22,144,62
s_add_u32 m0, s50, 8256
R 46,34048
mav 26,148,62
D2 88,54,0
mva 30,62,152
mav 22,156,66
D2 88,54,1024
mav 26,160,66
R 50,34112
mva 30,66,164
s_waitcnt lgkmcnt(2)
D2 88,55,2048
mav 22,168,70
R 54,34176
mav 26,172,70
D2 88,55,3072
mva 30,70,176
s_add_u32 m0, s50, 16512
mav 22,180,74
D2 89,54,0
mav 26,184,74
R 58,34240
mva 30,74,188
s_waitcnt lgkmcnt(2)
D2 89,54,1024
mav 22,192,46
R 62,34304
mav 26,196,46
D2 89,55,2048
mva 30,46,200
mav 22,204,50
D2 89,55,3072
mav 26,208,50
s_add_u32 m0, s50, 24768
R 66,34368
mva 30,50,212
s_waitcnt lgkmcnt(2)
D2 90,54,0
mav 22,216,54
R 70,34432
mav 26,220,54
D2 90,54,1024
mva 30,54,224
mav 22,228,58
D2 90,55,2048
mav 26,232,58
R 74,34496
mva 30,58,236
s_waitcnt lgkmcnt(2)
D2 90,55,3072
mav 22,240,62
R 46,34560
mav 26,244,62
mva 30,62,248
mav 22,252,66
R 50,34624
mvv 26,128,66
mvv 30,66,132
s_waitcnt lgkmcnt(2)
mvv 22,136,70
R 54,34688
mvv 26,140,70
mvv 30,70,144
mvv 22,148,74
R 58,34752
mvv 26,152,74
mvv 30,74,156
s_waitcnt lgkmcnt(2)
mvv 22,160,46
R 62,34816
mvv 26,164,46
mvv 30,46,168
s_waitcnt vmcnt(16)
v_permlane32_swap_b32 v14, v15
mvv 22,172,50
R 66,34880
v_add_f32 v16, v14, v15
mvv 26,176,50
v_add_f32 v17, v14, v15
mvv 30,50,180
s_waitcnt lgkmcnt(2)
v_permlane16_swap_b32 v16, v17
mvv 22,184,54
R 70,34944
v_add_f32 v14, v16, v17
mvv 26,188,54
v_add_f32 v15, v16, v17
mvv 30,54,192
mvv 22,196,58
R 74,35008
mvv 26,200,58
v_permlane32_swap_b32 v14, v15
mvv 30,58,204
s_waitcnt lgkmcnt(2)
v_mul_f32 v16, 0x3a800000, v14
v_mul_f32 v17, 0x3a800000, v15
mvv 22,208,62
R 46,0
v_fma_f32 v17, -v16, v16, v17
mvv 26,212,62
ds_read_b64 v[118:119], v85 offset:0
v_max_f32 v17, 0, v17
mvv 30,62,216
ds_read_b128 v[104:107], v86 offset:0
v_add_f32 v17, 0x3727c5ac, v17
mvv 22,220,66
R 50,64
mvv 26,224,66
ds_read_b128 v[108:111], v86 offset:16
mvv 30,66,228
s_waitcnt lgkmcnt(5)
v_rsq_f32 v14, v17
mvv 22,232,70
R 54,128
v_mul_f32_e64 v15, -v16, v14
mvv 26,236,70
mvv 30,70,240
mvv 22,244,74
R 58,192
mvv 26,248,74
mvv 30,74,252
s_waitcnt lgkmcnt(2)
v_mfma_f32_16x16x32_bf16 v[34:37], a[0:3], v[46:49], 0
R 62,256
v_mul_f32 v120, v119, v96
v_mfma_f32_16x16x32_bf16 v[38:41], a[4:7], v[46:49], 0
v_fma_f32 v22, v22, v118, v120
v_mul_f32 v121, v119, v97
v_mfma_f32_16x16x32_bf16 v[42:45], v[46:49], a[8:11], 0
v_fma_f32 v23, v23, v118, v121
v_mul_f32 v120, v119, v98
mav 34,12,50
R 66,320
v_fma_f32 v24, v24, v118, v120
mav 38,16,50
v_mul_f32 v121, v119, v99
v_fma_f32 v25, v25, v118, v121
mva 42,50,20
s_waitcnt lgkmcnt(2)
v_mul_f32 v120, v119, v100
v_fma_f32 v26, v26, v118, v120
mav 34,24,54
R 70,384
v_mul_f32 v121, v119, v101
mav 38,28,54
v_fma_f32 v27, v27, v118, v121
v_mul_f32 v120, v119, v102
mva 42,54,32
v_fma_f32 v28, v28, v118, v120
v_mul_f32 v121, v119, v103
mav 34,36,58
R 74,448
v_fma_f32 v29, v29, v118, v121
mav 38,40,58
v_cvt_pk_bf16_f32 v112, v22, v23
v_cvt_pk_bf16_f32 v113, v24, v25
mva 42,58,44
s_waitcnt lgkmcnt(2)
v_cvt_pk_bf16_f32 v114, v26, v27
v_cvt_pk_bf16_f32 v115, v28, v29
mav 34,48,62
R 46,512
v_mul_f32 v105, v105, v95
mav 38,52,62
ds_write_b128 v84, v[112:115] offset:0
mva 42,62,56
v_fma_f32 v30, v30, v104, v105
v_mul_f32 v107, v107, v95
mav 34,60,66
R 50,576
v_fma_f32 v31, v31, v106, v107
mav 38,64,66
v_mul_f32 v109, v109, v95
v_fma_f32 v32, v32, v108, v109
mva 42,66,68
s_waitcnt lgkmcnt(3)
v_mul_f32 v111, v111, v95
v_fma_f32 v33, v33, v110, v111
mav 34,72,70
R 54,640
v_cvt_pk_bf16_f32 v78, v30, v31
mav 38,76,70
v_cvt_pk_bf16_f32 v79, v32, v33
mva 42,70,80
ds_write_b64 v84, v[78:79] offset:1024
mav 34,84,74
s_xor_b32 s50, s50, s57
R 58,704
mav 38,88,74
ds_write_b64 v85, v[14:15] offset:128
mva 42,74,92
s_waitcnt lgkmcnt(4)
mav 34,96,46
s_add_u32 s49, s49, 313344
R 62,768
mav 38,100,46
s_waitcnt lgkmcnt(1)
s_sub_u32 s54, s54, 0x8000
mva 42,46,104
s_barrier
mav 34,108,50
buffer_load_dwordx2 v[14:15], v94, s[36:39], s49 offen sc1
mav 38,112,50
s_add_u32 m0, s50, 33024
R 66,832
mva 42,50,116
s_cmp_lt_u32 s54, s56
s_cselect_b64 s[44:45], s[40:41], s[44:45]
mav 34,120,54
R 70,896
mav 38,124,54
D2 87,54,0
mva 42,54,128
s_add_u32 s55, s54, 14336
mav 34,132,58
D2 87,54,1024
mav 38,136,58
R 74,960
mva 42,58,140
s_waitcnt lgkmcnt(2)
D2 87,55,2048
mav 34,144,62
R 46,1024
mav 38,148,62
D2 87,55,3072
mva 42,62,152
s_add_u32 m0, s50, 41280
mav 34,156,66
D2 88,54,0
mav 38,160,66
R 50,1088
mva 42,66,164
s_waitcnt lgkmcnt(2)
D2 88,54,1024
mav 34,168,70
R 54,1152
mav 38,172,70
D2 88,55,2048
mva 42,70,176
mav 34,180,74
D2 88,55,3072
mav 38,184,74
s_add_u32 m0, s50, 49536
R 58,1216
mva 42,74,188
s_waitcnt lgkmcnt(2)
D2 89,54,0
mav 34,192,46
R 62,1280
mav 38,196,46
D2 89,54,1024
mva 42,46,200
mav 34,204,50
D2 89,55,2048
mav 38,208,50
R 66,1344
mva 42,50,212
s_waitcnt lgkmcnt(2)
D2 89,55,3072
mav 34,216,54
s_add_u32 m0, s50, 57792
R 70,1408
mav 38,220,54
D2 90,54,0
mva 42,54,224
mav 34,228,58
D2 90,54,1024
mav 38,232,58
R 74,1472
mva 42,58,236
s_waitcnt lgkmcnt(2)
D2 90,55,2048
mav 34,240,62
R 46,1536
mav 38,244,62
D2 90,55,3072
mva 42,62,248
mav 34,252,66
R 50,1600
mvv 38,128,66
mvv 42,66,132
s_waitcnt lgkmcnt(2)
mvv 34,136,70
R 54,1664
mvv 38,140,70
mvv 42,70,144
mvv 34,148,74
R 58,1728
mvv 38,152,74
mvv 42,74,156
s_waitcnt lgkmcnt(2)
mvv 34,160,46
R 62,1792
mvv 38,164,46
mvv 42,46,168
s_waitcnt vmcnt(16)
v_permlane32_swap_b32 v14, v15
mvv 34,172,50
s_add_u32 s60, s60, 1
R 66,1856
v_add_f32 v16, v14, v15
mvv 38,176,50
s_cbranch_scc0 LQB1_1
mvv 42,50,180
s_waitcnt lgkmcnt(2)
v_add_f32 v17, v14, v15
mvv 34,184,54
R 70,1920
mvv 38,188,54
v_permlane16_swap_b32 v16, v17
mvv 42,54,192
v_add_f32 v14, v16, v17
v_add_f32 v15, v16, v17
mvv 34,196,58
R 74,1984
v_xor_b32 v80, v81, v80
mvv 38,200,58
v_permlane32_swap_b32 v14, v15
mvv 42,58,204
s_waitcnt lgkmcnt(2)
v_mul_f32 v16, 0x3a800000, v14
v_mul_f32 v17, 0x3a800000, v15
mvv 34,208,62
R 46,33024
v_fma_f32 v17, -v16, v16, v17
mvv 38,212,62
ds_read_b64 v[118:119], v85 offset:128
v_max_f32 v17, 0, v17
mvv 42,62,216
ds_read_b128 v[104:107], v86 offset:128
v_add_f32 v17, 0x3727c5ac, v17
mvv 34,220,66
R 50,33088
mvv 38,224,66
ds_read_b128 v[108:111], v86 offset:144
mvv 42,66,228
LsQ24:
s_cbranch_execz LwQA
s_waitcnt lgkmcnt(5)
v_rsq_f32 v14, v17
mvv 34,232,70
R 54,33152
v_mul_f32_e64 v15, -v16, v14
mvv 38,236,70
mvv 42,70,240
mvv 34,244,74
R 58,33216
mvv 38,248,74
mvv 42,74,252
s_waitcnt lgkmcnt(2)
v_mfma_f32_16x16x32_bf16 v[22:25], a[0:3], v[46:49], 0
R 62,33280
v_mul_f32 v120, v119, v96
v_mfma_f32_16x16x32_bf16 v[26:29], a[4:7], v[46:49], 0
v_fma_f32 v34, v34, v118, v120
v_mul_f32 v121, v119, v97
v_mfma_f32_16x16x32_bf16 v[30:33], v[46:49], a[8:11], 0
v_fma_f32 v35, v35, v118, v121
v_mul_f32 v120, v119, v98
mav 22,12,50
R 66,33344
v_fma_f32 v36, v36, v118, v120
mav 26,16,50
v_mul_f32 v121, v119, v99
v_fma_f32 v37, v37, v118, v121
mva 30,50,20
s_waitcnt lgkmcnt(2)
v_mul_f32 v120, v119, v100
v_fma_f32 v38, v38, v118, v120
mav 22,24,54
R 70,33408
v_mul_f32 v121, v119, v101
mav 26,28,54
v_fma_f32 v39, v39, v118, v121
v_mul_f32 v120, v119, v102
mva 30,54,32
v_fma_f32 v40, v40, v118, v120
v_mul_f32 v121, v119, v103
mav 22,36,58
R 74,33472
v_fma_f32 v41, v41, v118, v121
mav 26,40,58
v_cvt_pk_bf16_f32 v112, v34, v35
v_cvt_pk_bf16_f32 v113, v36, v37
mva 30,58,44
s_waitcnt lgkmcnt(2)
v_cvt_pk_bf16_f32 v114, v38, v39
v_cvt_pk_bf16_f32 v115, v40, v41
mav 22,48,62
R 46,33536
v_mul_f32 v105, v105, v95
mav 26,52,62
ds_write_b128 v84, v[112:115] offset:4096
mva 30,62,56
v_fma_f32 v42, v42, v104, v105
v_mul_f32 v107, v107, v95
mav 22,60,66
R 50,33600
v_fma_f32 v43, v43, v106, v107
mav 26,64,66
v_mul_f32 v109, v109, v95
v_fma_f32 v44, v44, v108, v109
mva 30,66,68
s_waitcnt lgkmcnt(3)
v_mul_f32 v111, v111, v95
v_fma_f32 v45, v45, v110, v111
mav 22,72,70
R 54,33664
v_cvt_pk_bf16_f32 v78, v42, v43
mav 26,76,70
v_cvt_pk_bf16_f32 v79, v44, v45
mva 30,70,80
ds_write_b64 v84, v[78:79] offset:5120
mav 22,84,74
R 58,33728
mav 26,88,74
ds_write_b64 v85, v[14:15] offset:0
mva 30,74,92
s_waitcnt lgkmcnt(4)
mav 22,96,46
s_add_u32 s49, s49, 313344
R 62,33792
mav 26,100,46
s_waitcnt lgkmcnt(1)
mva 30,46,104
s_barrier
mav 22,108,50
buffer_load_dwordx2 v[14:15], v94, s[36:39], s49 offen sc1
mav 26,112,50
s_sub_u32 s54, s54, 0x8000
R 66,33856
s_add_u32 m0, s50, 0
mva 30,50,116
D2 87,54,0
mav 22,120,54
R 70,33920
mav 26,124,54
D2 87,54,1024
mva 30,54,128
s_add_u32 s55, s54, 14336
mav 22,132,58
D2 87,55,2048
mav 26,136,58
R 74,33984
mva 30,58,140
s_waitcnt lgkmcnt(2)
D2 87,55,3072
mav 22,144,62
s_add_u32 m0, s50, 8256
R 46,34048
mav 26,148,62
D2 88,54,0
mva 30,62,152
mav 22,156,66
D2 88,54,1024
mav 26,160,66
R 50,34112
mva 30,66,164
s_waitcnt lgkmcnt(2)
D2 88,55,2048
mav 22,168,70
R 54,34176
mav 26,172,70
D2 88,55,3072
mva 30,70,176
s_add_u32 m0, s50, 16512
mav 22,180,74
D2 89,54,0
mav 26,184,74
R 58,34240
mva 30,74,188
s_waitcnt lgkmcnt(2)
D2 89,54,1024
mav 22,192,46
R 62,34304
mav 26,196,46
D2 89,55,2048
mva 30,46,200
mav 22,204,50
D2 89,55,3072
mav 26,208,50
s_add_u32 m0, s50, 24768
R 66,34368
mva 30,50,212
s_waitcnt lgkmcnt(2)
D2 90,54,0
mav 22,216,54
R 70,34432
mav 26,220,54
D2 90,54,1024
mva 30,54,224
mav 22,228,58
D2 90,55,2048
mav 26,232,58
R 74,34496
mva 30,58,236
s_waitcnt lgkmcnt(2)
D2 90,55,3072
mav 22,240,62
R 46,34560
mav 26,244,62
mva 30,62,248
mav 22,252,66
R 50,34624
mvv 26,128,66
mvv 30,66,132
s_waitcnt lgkmcnt(2)
mvv 22,136,70
R 54,34688
mvv 26,140,70
mvv 30,70,144
mvv 22,148,74
R 58,34752
mvv 26,152,74
mvv 30,74,156
s_waitcnt lgkmcnt(2)
mvv 22,160,46
R 62,34816
mvv 26,164,46
mvv 30,46,168
s_waitcnt vmcnt(16)
v_permlane32_swap_b32 v14, v15
mvv 22,172,50
R 66,34880
v_add_f32 v16, v14, v15
mvv 26,176,50
v_add_f32 v17, v14, v15
mvv 30,50,180
s_waitcnt lgkmcnt(2)
v_permlane16_swap_b32 v16, v17
mvv 22,184,54
R 70,34944
v_add_f32 v14, v16, v17
mvv 26,188,54
v_add_f32 v15, v16, v17
mvv 30,54,192
mvv 22,196,58
R 74,35008
mvv 26,200,58
v_permlane32_swap_b32 v14, v15
mvv 30,58,204
s_waitcnt lgkmcnt(2)
v_mul_f32 v16, 0x3a800000, v14
v_mul_f32 v17, 0x3a800000, v15
mvv 22,208,62
R 46,0
v_fma_f32 v17, -v16, v16, v17
mvv 26,212,62
ds_read_b64 v[118:119], v85 offset:0
v_max_f32 v17, 0, v17
mvv 30,62,216
ds_read_b128 v[104:107], v86 offset:0
v_add_f32 v17, 0x3727c5ac, v17
mvv 22,220,66
R 50,64
mvv 26,224,66
ds_read_b128 v[108:111], v86 offset:16
mvv 30,66,228
s_waitcnt lgkmcnt(5)
v_rsq_f32 v14, v17
mvv 22,232,70
R 54,128
v_mul_f32_e64 v15, -v16, v14
mvv 26,236,70
mvv 30,70,240
mvv 22,244,74
R 58,192
mvv 26,248,74
mvv 30,74,252
s_waitcnt lgkmcnt(2)
v_mfma_f32_16x16x32_bf16 v[34:37], a[0:3], v[46:49], 0
R 62,256
v_mul_f32 v120, v119, v96
v_mfma_f32_16x16x32_bf16 v[38:41], a[4:7], v[46:49], 0
v_fma_f32 v22, v22, v118, v120
v_mul_f32 v121, v119, v97
v_mfma_f32_16x16x32_bf16 v[42:45], v[46:49], a[8:11], 0
v_fma_f32 v23, v23, v118, v121
v_mul_f32 v120, v119, v98
mav 34,12,50
R 66,320
v_fma_f32 v24, v24, v118, v120
mav 38,16,50
v_mul_f32 v121, v119, v99
v_fma_f32 v25, v25, v118, v121
mva 42,50,20
s_waitcnt lgkmcnt(2)
v_mul_f32 v120, v119, v100
v_fma_f32 v26, v26, v118, v120
mav 34,24,54
R 70,384
v_mul_f32 v121, v119, v101
mav 38,28,54
v_fma_f32 v27, v27, v118, v121
v_mul_f32 v120, v119, v102
mva 42,54,32
v_fma_f32 v28, v28, v118, v120
v_mul_f32 v121, v119, v103
mav 34,36,58
R 74,448
v_fma_f32 v29, v29, v118, v121
mav 38,40,58
v_cvt_pk_bf16_f32 v112, v22, v23
v_cvt_pk_bf16_f32 v113, v24, v25
mva 42,58,44
s_waitcnt lgkmcnt(2)
v_cvt_pk_bf16_f32 v114, v26, v27
v_cvt_pk_bf16_f32 v115, v28, v29
mav 34,48,62
R 46,512
v_mul_f32 v105, v105, v95
mav 38,52,62
ds_write_b128 v84, v[112:115] offset:0
mva 42,62,56
v_fma_f32 v30, v30, v104, v105
v_mul_f32 v107, v107, v95
mav 34,60,66
R 50,576
v_fma_f32 v31, v31, v106, v107
mav 38,64,66
)"
R"(
v_mul_f32 v109, v109, v95
v_fma_f32 v32, v32, v108, v109
mva 42,66,68
s_waitcnt lgkmcnt(3)
v_mul_f32 v111, v111, v95
v_fma_f32 v33, v33, v110, v111
mav 34,72,70
R 54,640
v_cvt_pk_bf16_f32 v78, v30, v31
mav 38,76,70
v_cvt_pk_bf16_f32 v79, v32, v33
mva 42,70,80
ds_write_b64 v84, v[78:79] offset:1024
mav 34,84,74
R 58,704
mav 38,88,74
ds_write_b64 v85, v[14:15] offset:128
mva 42,74,92
s_waitcnt lgkmcnt(4)
mav 34,96,46
s_add_u32 s49, s49, 313344
R 62,768
mav 38,100,46
s_waitcnt lgkmcnt(1)
mva 42,46,104
s_barrier
mav 34,108,50
buffer_load_dwordx2 v[14:15], v94, s[36:39], s49 offen sc1
mav 38,112,50
R 66,832
mva 42,50,116
mav 34,120,54
R 70,896
mav 38,124,54
mva 42,54,128
mav 34,132,58
R 74,960
mav 38,136,58
mva 42,58,140
s_waitcnt lgkmcnt(2)
mav 34,144,62
R 46,1024
mav 38,148,62
mva 42,62,152
mav 34,156,66
R 50,1088
mav 38,160,66
mva 42,66,164
s_waitcnt lgkmcnt(2)
mav 34,168,70
R 54,1152
mav 38,172,70
mva 42,70,176
mav 34,180,74
R 58,1216
mav 38,184,74
mva 42,74,188
s_waitcnt lgkmcnt(2)
mav 34,192,46
R 62,1280
mav 38,196,46
mva 42,46,200
mav 34,204,50
R 66,1344
mav 38,208,50
mva 42,50,212
s_waitcnt lgkmcnt(2)
mav 34,216,54
R 70,1408
mav 38,220,54
mva 42,54,224
mav 34,228,58
R 74,1472
mav 38,232,58
mva 42,58,236
s_waitcnt lgkmcnt(2)
mav 34,240,62
R 46,1536
mav 38,244,62
mva 42,62,248
mav 34,252,66
R 50,1600
mvv 38,128,66
mvv 42,66,132
s_waitcnt lgkmcnt(2)
mvv 34,136,70
R 54,1664
mvv 38,140,70
mvv 42,70,144
mvv 34,148,74
R 58,1728
mvv 38,152,74
mvv 42,74,156
s_waitcnt lgkmcnt(2)
mvv 34,160,46
R 62,1792
mvv 38,164,46
mvv 42,46,168
s_waitcnt vmcnt(0)
v_permlane32_swap_b32 v14, v15
mvv 34,172,50
R 66,1856
v_add_f32 v16, v14, v15
mvv 38,176,50
mvv 42,50,180
s_waitcnt lgkmcnt(2)
v_add_f32 v17, v14, v15
mvv 34,184,54
R 70,1920
mvv 38,188,54
v_permlane16_swap_b32 v16, v17
mvv 42,54,192
v_add_f32 v14, v16, v17
v_add_f32 v15, v16, v17
mvv 34,196,58
R 74,1984
v_xor_b32 v80, v81, v80
mvv 38,200,58
v_permlane32_swap_b32 v14, v15
mvv 42,58,204
s_waitcnt lgkmcnt(2)
v_mul_f32 v16, 0x3a800000, v14
v_mul_f32 v17, 0x3a800000, v15
mvv 34,208,62
R 46,33024
v_fma_f32 v17, -v16, v16, v17
mvv 38,212,62
ds_read_b64 v[118:119], v85 offset:128
v_max_f32 v17, 0, v17
mvv 42,62,216
ds_read_b128 v[104:107], v86 offset:128
v_add_f32 v17, 0x3727c5ac, v17
mvv 34,220,66
R 50,33088
mvv 38,224,66
ds_read_b128 v[108:111], v86 offset:144
mvv 42,66,228
s_waitcnt lgkmcnt(5)
v_rsq_f32 v14, v17
mvv 34,232,70
R 54,33152
v_mul_f32_e64 v15, -v16, v14
mvv 38,236,70
mvv 42,70,240
mvv 34,244,74
R 58,33216
mvv 38,248,74
mvv 42,74,252
s_waitcnt lgkmcnt(2)
v_mfma_f32_16x16x32_bf16 v[22:25], a[0:3], v[46:49], 0
R 62,33280
v_mul_f32 v120, v119, v96
v_mfma_f32_16x16x32_bf16 v[26:29], a[4:7], v[46:49], 0
v_fma_f32 v34, v34, v118, v120
v_mul_f32 v121, v119, v97
v_mfma_f32_16x16x32_bf16 v[30:33], v[46:49], a[8:11], 0
v_fma_f32 v35, v35, v118, v121
v_mul_f32 v120, v119, v98
mav 22,12,50
R 66,33344
v_fma_f32 v36, v36, v118, v120
mav 26,16,50
v_mul_f32 v121, v119, v99
v_fma_f32 v37, v37, v118, v121
mva 30,50,20
s_waitcnt lgkmcnt(2)
v_mul_f32 v120, v119, v100
v_fma_f32 v38, v38, v118, v120
mav 22,24,54
R 70,33408
v_mul_f32 v121, v119, v101
mav 26,28,54
v_fma_f32 v39, v39, v118, v121
v_mul_f32 v120, v119, v102
mva 30,54,32
v_fma_f32 v40, v40, v118, v120
v_mul_f32 v121, v119, v103
mav 22,36,58
R 74,33472
v_fma_f32 v41, v41, v118, v121
mav 26,40,58
v_cvt_pk_bf16_f32 v112, v34, v35
v_cvt_pk_bf16_f32 v113, v36, v37
mva 30,58,44
s_waitcnt lgkmcnt(2)
v_cvt_pk_bf16_f32 v114, v38, v39
v_cvt_pk_bf16_f32 v115, v40, v41
mav 22,48,62
R 46,33536
v_mul_f32 v105, v105, v95
mav 26,52,62
ds_write_b128 v84, v[112:115] offset:4096
mva 30,62,56
v_fma_f32 v42, v42, v104, v105
v_mul_f32 v107, v107, v95
mav 22,60,66
R 50,33600
v_fma_f32 v43, v43, v106, v107
mav 26,64,66
v_mul_f32 v109, v109, v95
v_fma_f32 v44, v44, v108, v109
mva 30,66,68
s_waitcnt lgkmcnt(3)
v_mul_f32 v111, v111, v95
v_fma_f32 v45, v45, v110, v111
mav 22,72,70
R 54,33664
v_cvt_pk_bf16_f32 v78, v42, v43
mav 26,76,70
v_cvt_pk_bf16_f32 v79, v44, v45
mva 30,70,80
ds_write_b64 v84, v[78:79] offset:5120
mav 22,84,74
R 58,33728
mav 26,88,74
ds_write_b64 v85, v[14:15] offset:0
mva 30,74,92
s_waitcnt lgkmcnt(4)
mav 22,96,46
s_add_u32 s49, s49, 313344
R 62,33792
mav 26,100,46
s_waitcnt lgkmcnt(1)
mva 30,46,104
s_barrier
mav 22,108,50
buffer_load_dwordx2 v[14:15], v94, s[36:39], s49 offen sc1
mav 26,112,50
R 66,33856
mva 30,50,116
mav 22,120,54
R 70,33920
mav 26,124,54
mva 30,54,128
mav 22,132,58
R 74,33984
mav 26,136,58
mva 30,58,140
s_waitcnt lgkmcnt(2)
mav 22,144,62
R 46,34048
mav 26,148,62
mva 30,62,152
mav 22,156,66
R 50,34112
mav 26,160,66
mva 30,66,164
s_waitcnt lgkmcnt(2)
mav 22,168,70
R 54,34176
mav 26,172,70
mva 30,70,176
mav 22,180,74
R 58,34240
mav 26,184,74
mva 30,74,188
s_waitcnt lgkmcnt(2)
mav 22,192,46
R 62,34304
mav 26,196,46
mva 30,46,200
mav 22,204,50
R 66,34368
mav 26,208,50
mva 30,50,212
s_waitcnt lgkmcnt(2)
mav 22,216,54
R 70,34432
mav 26,220,54
mva 30,54,224
mav 22,228,58
R 74,34496
mav 26,232,58
mva 30,58,236
s_waitcnt lgkmcnt(2)
mav 22,240,62
R 46,34560
mav 26,244,62
mva 30,62,248
mav 22,252,66
R 50,34624
mvv 26,128,66
mvv 30,66,132
LsQ25:
s_cbranch_execz LwQB
s_waitcnt lgkmcnt(2)
mvv 22,136,70
R 54,34688
mvv 26,140,70
mvv 30,70,144
mvv 22,148,74
R 58,34752
mvv 26,152,74
mvv 30,74,156
s_waitcnt lgkmcnt(2)
mvv 22,160,46
R 62,34816
mvv 26,164,46
mvv 30,46,168
s_waitcnt vmcnt(0)
v_permlane32_swap_b32 v14, v15
mvv 22,172,50
R 66,34880
v_add_f32 v16, v14, v15
mvv 26,176,50
v_add_f32 v17, v14, v15
mvv 30,50,180
s_waitcnt lgkmcnt(2)
v_permlane16_swap_b32 v16, v17
mvv 22,184,54
R 70,34944
v_add_f32 v14, v16, v17
mvv 26,188,54
v_add_f32 v15, v16, v17
mvv 30,54,192
mvv 22,196,58
R 74,35008
mvv 26,200,58
v_permlane32_swap_b32 v14, v15
mvv 30,58,204
s_waitcnt lgkmcnt(2)
v_mul_f32 v16, 0x3a800000, v14
v_mul_f32 v17, 0x3a800000, v15
mvv 22,208,62
R 46,0
v_fma_f32 v17, -v16, v16, v17
mvv 26,212,62
ds_read_b64 v[118:119], v85 offset:0
v_max_f32 v17, 0, v17
mvv 30,62,216
ds_read_b128 v[104:107], v86 offset:0
v_add_f32 v17, 0x3727c5ac, v17
mvv 22,220,66
R 50,64
mvv 26,224,66
ds_read_b128 v[108:111], v86 offset:16
mvv 30,66,228
s_waitcnt lgkmcnt(5)
v_rsq_f32 v14, v17
mvv 22,232,70
R 54,128
v_mul_f32_e64 v15, -v16, v14
mvv 26,236,70
mvv 30,70,240
mvv 22,244,74
R 58,192
mvv 26,248,74
mvv 30,74,252
s_waitcnt lgkmcnt(2)
v_mfma_f32_16x16x32_bf16 v[34:37], a[0:3], v[46:49], 0
R 62,256
v_mul_f32 v120, v119, v96
v_mfma_f32_16x16x32_bf16 v[38:41], a[4:7], v[46:49], 0
v_fma_f32 v22, v22, v118, v120
v_mul_f32 v121, v119, v97
v_mfma_f32_16x16x32_bf16 v[42:45], v[46:49], a[8:11], 0
v_fma_f32 v23, v23, v118, v121
v_mul_f32 v120, v119, v98
mav 34,12,50
R 66,320
v_fma_f32 v24, v24, v118, v120
mav 38,16,50
v_mul_f32 v121, v119, v99
v_fma_f32 v25, v25, v118, v121
mva 42,50,20
s_waitcnt lgkmcnt(2)
v_mul_f32 v120, v119, v100
v_fma_f32 v26, v26, v118, v120
mav 34,24,54
R 70,384
v_mul_f32 v121, v119, v101
mav 38,28,54
v_fma_f32 v27, v27, v118, v121
v_mul_f32 v120, v119, v102
mva 42,54,32
v_fma_f32 v28, v28, v118, v120
v_mul_f32 v121, v119, v103
mav 34,36,58
R 74,448
v_fma_f32 v29, v29, v118, v121
mav 38,40,58
v_cvt_pk_bf16_f32 v112, v22, v23
v_cvt_pk_bf16_f32 v113, v24, v25
mva 42,58,44
s_waitcnt lgkmcnt(2)
v_cvt_pk_bf16_f32 v114, v26, v27
v_cvt_pk_bf16_f32 v115, v28, v29
mav 34,48,62
R 46,512
v_mul_f32 v105, v105, v95
mav 38,52,62
ds_write_b128 v84, v[112:115] offset:0
mva 42,62,56
v_fma_f32 v30, v30, v104, v105
v_mul_f32 v107, v107, v95
mav 34,60,66
R 50,576
v_fma_f32 v31, v31, v106, v107
mav 38,64,66
v_mul_f32 v109, v109, v95
v_fma_f32 v32, v32, v108, v109
mva 42,66,68
s_waitcnt lgkmcnt(3)
v_mul_f32 v111, v111, v95
v_fma_f32 v33, v33, v110, v111
mav 34,72,70
R 54,640
v_cvt_pk_bf16_f32 v78, v30, v31
mav 38,76,70
v_cvt_pk_bf16_f32 v79, v32, v33
mva 42,70,80
ds_write_b64 v84, v[78:79] offset:1024
mav 34,84,74
R 58,704
mav 38,88,74
ds_write_b64 v85, v[14:15] offset:128
mva 42,74,92
s_waitcnt lgkmcnt(4)
mav 34,96,46
R 62,768
mav 38,100,46
s_waitcnt lgkmcnt(1)
mva 42,46,104
s_barrier
mav 34,108,50
R 66,832
mav 38,112,50
mva 42,50,116
mav 34,120,54
R 70,896
mav 38,124,54
mva 42,54,128
mav 34,132,58
R 74,960
mav 38,136,58
mva 42,58,140
s_waitcnt lgkmcnt(2)
mav 34,144,62
R 46,1024
mav 38,148,62
mva 42,62,152
mav 34,156,66
R 50,1088
mav 38,160,66
mva 42,66,164
s_waitcnt lgkmcnt(2)
mav 34,168,70
R 54,1152
mav 38,172,70
mva 42,70,176
mav 34,180,74
R 58,1216
mav 38,184,74
mva 42,74,188
s_waitcnt lgkmcnt(2)
mav 34,192,46
R 62,1280
mav 38,196,46
mva 42,46,200
mav 34,204,50
R 66,1344
mav 38,208,50
mva 42,50,212
s_waitcnt lgkmcnt(2)
mav 34,216,54
R 70,1408
mav 38,220,54
mva 42,54,224
mav 34,228,58
R 74,1472
mav 38,232,58
mva 42,58,236
s_waitcnt lgkmcnt(2)
mav 34,240,62
R 46,1536
mav 38,244,62
mva 42,62,248
mav 34,252,66
R 50,1600
mvv 38,128,66
mvv 42,66,132
s_waitcnt lgkmcnt(2)
mvv 34,136,70
R 54,1664
mvv 38,140,70
mvv 42,70,144
mvv 34,148,74
R 58,1728
mvv 38,152,74
mvv 42,74,156
s_waitcnt lgkmcnt(2)
mvv 34,160,46
R 62,1792
mvv 38,164,46
mvv 42,46,168
mvv 34,172,50
R 66,1856
mvv 38,176,50
mvv 42,50,180
s_waitcnt lgkmcnt(2)
mvv 34,184,54
R 70,1920
mvv 38,188,54
mvv 42,54,192
mvv 34,196,58
R 74,1984
mvv 38,200,58
mvv 42,58,204
s_waitcnt lgkmcnt(2)
mvv 34,208,62
mvv 38,212,62
ds_read_b64 v[118:119], v85 offset:128
mvv 42,62,216
ds_read_b128 v[104:107], v86 offset:128
mvv 34,220,66
ds_read_b128 v[108:111], v86 offset:144
mvv 38,224,66
mvv 42,66,228
s_waitcnt lgkmcnt(3)
mvv 34,232,70
mvv 38,236,70
mvv 42,70,240
mvv 34,244,74
mvv 38,248,74
mvv 42,74,252
s_waitcnt lgkmcnt(0)
v_mul_f32 v120, v119, v96
s_nop 3
v_fma_f32 v34, v34, v118, v120
v_mul_f32 v121, v119, v97
v_fma_f32 v35, v35, v118, v121
v_mul_f32 v120, v119, v98
v_fma_f32 v36, v36, v118, v120
v_mul_f32 v121, v119, v99
v_fma_f32 v37, v37, v118, v121
v_mul_f32 v120, v119, v100
v_fma_f32 v38, v38, v118, v120
v_mul_f32 v121, v119, v101
v_fma_f32 v39, v39, v118, v121
v_mul_f32 v120, v119, v102
v_fma_f32 v40, v40, v118, v120
v_mul_f32 v121, v119, v103
v_fma_f32 v41, v41, v118, v121
v_cvt_pk_bf16_f32 v112, v34, v35
v_cvt_pk_bf16_f32 v113, v36, v37
v_cvt_pk_bf16_f32 v114, v38, v39
v_cvt_pk_bf16_f32 v115, v40, v41
ds_write_b128 v84, v[112:115] offset:4096
v_mul_f32 v105, v105, v95
v_fma_f32 v42, v42, v104, v105
v_mul_f32 v107, v107, v95
v_fma_f32 v43, v43, v106, v107
v_mul_f32 v109, v109, v95
v_fma_f32 v44, v44, v108, v109
v_mul_f32 v111, v111, v95
v_fma_f32 v45, v45, v110, v111
v_cvt_pk_bf16_f32 v78, v42, v43
v_cvt_pk_bf16_f32 v79, v44, v45
ds_write_b64 v84, v[78:79] offset:5120
s_waitcnt lgkmcnt(0)
s_barrier
s_endpgm
Cq:
s_cmp_eq_u32 s61, 0
s_cbranch_scc1 Bq
s_sub_u32 s61, s61, 1
s_sleep 2
buffer_load_dwordx2 v[122:123], v92, s[36:39], 0 offen sc1
s_waitcnt vmcnt(0)
ds_write_b32 v83, v122
v_sub_u32 v122, v122, v93
v_xor_b32 v123, v123, v117
v_cmp_lt_u64 vcc, s[62:63], v[122:123]
s_cbranch_vccnz Cq
s_branch Bq
Lr2:
s_cmp_eq_u32 s60, 2
s_cbranch_scc0 Lr3
E 0,65536
E 1,69632
s_add_u32 s51, s51, 65536
E 0,73728
E 1,77824
s_add_u32 s51, s51, 65536
E 0,81920
E 1,86016
s_add_u32 s51, s51, 65536
E 0,90112
E 1,94208
s_add_u32 s51, s51, 65536
s_mov_b64 exec, 0
s_cmp_eq_u32 s53, 0
s_cbranch_scc1 LsO0
s_cmp_eq_u32 s53, 1024
s_cbranch_scc1 LsO1
s_cmp_eq_u32 s53, 2048
s_cbranch_scc1 LsO2
s_branch LsO3
LwO:
s_mov_b64 exec, -1
s_waitcnt vmcnt(4)
s_barrier
v_add_u32 v4, 65536, v4
v_add_u32 v5, 65536, v5
Ta 0,4,0
Ta 4,5,0
Ta 8,4,8192
Ta 12,5,8192
v_accvgpr_read_b32 v57, a255
v_mov_b32 v56, 0x100
v_add_u32 v57, 0x9e3779b9, v57
buffer_store_dwordx2 v[56:57], v81, s[36:39], s52 offen sc1
v_add_u32 v87, v87, v56
E 0,98304
E 1,102400
s_add_u32 s51, s51, 65536
E 0,106496
E 1,110592
s_add_u32 s51, s51, 65536
E 0,114688
E 1,118784
s_add_u32 s51, s51, 65536
E 0,122880
E 1,126976
s_add_u32 s51, s51, 65536
s_cmp_ge_u32 s51, s57
s_cselect_b32 s60, 2097152, 0
s_sub_u32 s51, s51, s60
E 0,131072
E 1,135168
s_add_u32 s51, s51, 65536
E 0,139264
E 1,143360
s_add_u32 s51, s51, 65536
E 0,147456
E 1,151552
s_add_u32 s51, s51, 65536
E 0,155648
E 1,159744
s_add_u32 s51, s51, 65536
s_waitcnt lgkmcnt(0)
s_waitcnt vmcnt(17)
s_barrier
E 0,65536
Ta 16,4,16384
Ta 20,5,16384
E 1,69632
s_add_u32 s51, s51, 65536
Ta 24,4,24576
Ta 28,5,24576
E 0,73728
E 1,77824
s_add_u32 s51, s51, 65536
s_waitcnt lgkmcnt(0)
s_waitcnt vmcnt(16)
s_barrier
E 0,81920
Ta 32,4,32768
Ta 36,5,32768
E 1,86016
s_add_u32 s51, s51, 65536
Ta 40,4,40960
Ta 44,5,40960
E 0,90112
E 1,94208
s_add_u32 s51, s51, 65536
s_cmp_ge_u32 s51, s57
s_cselect_b32 s60, 2097152, 0
s_sub_u32 s51, s51, s60
s_waitcnt lgkmcnt(0)
s_waitcnt vmcnt(16)
s_barrier
E 0,98304
Ta 48,4,49152
Ta 52,5,49152
E 1,102400
s_add_u32 s51, s51, 65536
Ta 56,4,57344
Ta 60,5,57344
E 0,106496
E 1,110592
s_add_u32 s51, s51, 65536
s_waitcnt lgkmcnt(0)
s_waitcnt vmcnt(16)
s_barrier
v_add_u32 v4, 65536, v4
v_add_u32 v5, 65536, v5
E 0,114688
Ta 64,4,0
Ta 68,5,0
E 1,118784
s_add_u32 s51, s51, 65536
Ta 72,4,8192
Ta 76,5,8192
E 0,122880
E 1,126976
s_add_u32 s51, s51, 65536
s_waitcnt lgkmcnt(0)
s_waitcnt vmcnt(16)
s_barrier
E 0,131072
Ta 80,4,16384
Ta 84,5,16384
E 1,135168
s_add_u32 s51, s51, 65536
Ta 88,4,24576
Ta 92,5,24576
E 0,139264
E 1,143360
s_add_u32 s51, s51, 65536
s_waitcnt lgkmcnt(0)
s_waitcnt vmcnt(16)
s_barrier
v_add_u32 v4, -65536, v4
v_add_u32 v5, -65536, v5
E 0,147456
Ta 96,4,0
Ta 100,5,0
E 1,151552
s_add_u32 s51, s51, 65536
Ta 104,4,8192
Ta 108,5,8192
E 0,155648
E 1,159744
s_add_u32 s51, s51, 65536
s_cmp_ge_u32 s51, s57
s_cselect_b32 s60, 2097152, 0
s_sub_u32 s51, s51, s60
s_waitcnt lgkmcnt(0)
s_waitcnt vmcnt(16)
s_barrier
E 0,65536
Ta 112,4,16384
Ta 116,5,16384
E 1,69632
s_add_u32 s51, s51, 65536
Ta 120,4,24576
Ta 124,5,24576
E 0,73728
E 1,77824
s_add_u32 s51, s51, 65536
s_waitcnt lgkmcnt(0)
s_waitcnt vmcnt(16)
s_barrier
E 0,81920
LsO0:
s_cbranch_execz LsO4
Ta 128,4,32768
Ta 132,5,32768
E 1,86016
s_add_u32 s51, s51, 65536
Ta 136,4,40960
Ta 140,5,40960
E 0,90112
E 1,94208
s_add_u32 s51, s51, 65536
s_waitcnt lgkmcnt(0)
s_waitcnt vmcnt(16)
s_barrier
E 0,98304
Ta 144,4,49152
Ta 148,5,49152
E 1,102400
s_add_u32 s51, s51, 65536
Ta 152,4,57344
Ta 156,5,57344
E 0,106496
E 1,110592
s_add_u32 s51, s51, 65536
s_waitcnt lgkmcnt(0)
s_waitcnt vmcnt(16)
s_barrier
v_add_u32 v4, 65536, v4
v_add_u32 v5, 65536, v5
E 0,114688
Ta 160,4,0
Ta 164,5,0
E 1,118784
s_add_u32 s51, s51, 65536
Ta 168,4,8192
Ta 172,5,8192
E 0,122880
E 1,126976
s_add_u32 s51, s51, 65536
s_cmp_ge_u32 s51, s57
s_cselect_b32 s60, 2097152, 0
s_sub_u32 s51, s51, s60
s_waitcnt lgkmcnt(0)
s_waitcnt vmcnt(16)
s_barrier
Ta 176,4,16384
Ta 180,5,16384
Ta 184,4,24576
Ta 188,5,24576
s_waitcnt lgkmcnt(0)
s_waitcnt vmcnt(12)
s_barrier
v_add_u32 v4, -65536, v4
v_add_u32 v5, -65536, v5
Ta 192,4,0
Ta 196,5,0
Ta 200,4,8192
Ta 204,5,8192
s_waitcnt lgkmcnt(0)
s_waitcnt vmcnt(8)
s_barrier
Ta 208,4,16384
Ta 212,5,16384
Ta 216,4,24576
Ta 220,5,24576
s_waitcnt lgkmcnt(0)
s_waitcnt vmcnt(4)
s_barrier
Ta 224,4,32768
Ta 228,5,32768
Ta 232,4,40960
Ta 236,5,40960
s_waitcnt lgkmcnt(0)
s_waitcnt vmcnt(0)
s_barrier
Ta 240,4,49152
Ta 244,5,49152
Ta 248,4,57344
Ta 252,5,57344
s_waitcnt lgkmcnt(0)
s_waitcnt vmcnt(0)
s_barrier
s_nop 7
s_add_u32 s40, s36, 4194304
s_addc_u32 s41, s37, 0
s_and_b32 s60, s48, 3
s_cmp_eq_u32 s60, 1
s_cbranch_scc1 LvO1
s_cmp_eq_u32 s60, 2
s_cbranch_scc1 LvO2
s_cmp_eq_u32 s60, 3
s_cbranch_scc1 LvO3
LvO0:
LsO1:
s_cbranch_execz LsO5
buffer_load_dwordx2 v[60:61], v86, s[36:39], 0 offen sc1
s_waitcnt vmcnt(0)
ds_write_b32 v82, v60
v_sub_u32 v60, v60, v87
v_xor_b32 v61, v61, v57
v_cmp_lt_u64 vcc, s[62:63], v[60:61]
s_waitcnt lgkmcnt(0)
s_cbranch_vccnz CO0_1
BO0_1:
s_add_u32 s49, s49, 313344
s_barrier
buffer_load_dwordx4 v[48:51], v88, s[44:47], s54 offen sc1
s_add_u32 m0, s50, 0
s_nop 0
D 84,0
D 84,1024
D 84,2048
D 84,3072
s_add_u32 m0, s50, 4096
s_nop 0
D 85,0
D 85,1024
D 85,2048
D 85,3072
s_mov_b32 m0, 144384
s_nop 0
buffer_load_dword v86, s[36:39], 0 offen sc1 lds
s_waitcnt vmcnt(9)
v_lshlrev_b32 v0, 16, v48
v_and_b32 v1, 0xffff0000, v48
v_lshlrev_b32 v2, 16, v49
v_and_b32 v3, 0xffff0000, v49
v_lshlrev_b32 v4, 16, v50
v_and_b32 v5, 0xffff0000, v50
v_lshlrev_b32 v6, 16, v51
v_and_b32 v7, 0xffff0000, v51
s_sub_u32 s54, s54, 0x8000
buffer_load_dwordx4 v[48:51], v88, s[44:47], s54 offen sc1
buffer_load_dword v86, s[36:39], 0 offen sc1 lds
v_add_u32 v87, 1, v87
ds_read_b32 v60, v82 offset:0
s_waitcnt lgkmcnt(0)
v_cmp_gt_i32 vcc, v87, v60
s_waitcnt vmcnt(3)
s_add_u32 s49, s49, 313344
s_cbranch_vccnz CO0_2
BO0_2:
s_barrier
s_add_u32 m0, s50, 32768
R 16,0
D 84,0
R 20,1024
R 24,2048
D 84,1024
R 28,3072
R 32,4096
D 84,2048
s_waitcnt lgkmcnt(3)
mav 0,0,16
R 36,5120
mav 4,4,16
mav 0,8,20
R 40,6144
mav 4,12,20
s_waitcnt lgkmcnt(3)
D 84,3072
mav 0,16,24
R 44,7168
mav 4,20,24
s_add_u32 m0, s50, 36864
mav 0,24,28
R 16,8192
mav 4,28,28
s_waitcnt lgkmcnt(3)
D 85,0
mav 0,32,32
R 20,9216
mav 4,36,32
mav 0,40,36
R 24,10240
mav 4,44,36
s_waitcnt lgkmcnt(3)
D 85,1024
mav 0,48,40
R 28,11264
mav 4,52,40
mav 0,56,44
R 32,12288
mav 4,60,44
s_waitcnt lgkmcnt(3)
D 85,2048
mav 0,64,16
R 36,13312
mav 4,68,16
mav 0,72,20
R 40,14336
mav 4,76,20
s_waitcnt lgkmcnt(3)
D 85,3072
mav 0,80,24
R 44,15360
mav 4,84,24
mav 0,88,28
R 16,16384
mav 4,92,28
s_waitcnt lgkmcnt(3)
mav 0,96,32
R 20,17408
mav 4,100,32
mav 0,104,36
s_mov_b32 m0, 144384
R 24,18432
mav 4,108,36
s_waitcnt lgkmcnt(3)
mav 0,112,40
R 28,19456
mav 4,116,40
mav 0,120,44
s_waitcnt vmcnt(9)
R 32,20480
v_lshlrev_b32 v8, 16, v48
mav 4,124,44
s_waitcnt lgkmcnt(3)
buffer_load_dword v86, s[36:39], 0 offen sc1 lds
mav 0,128,16
R 36,21504
v_and_b32 v9, 0xffff0000, v48
mav 4,132,16
v_lshlrev_b32 v10, 16, v49
v_and_b32 v11, 0xffff0000, v49
mav 0,136,20
R 40,22528
v_lshlrev_b32 v12, 16, v50
mav 4,140,20
s_waitcnt lgkmcnt(3)
v_and_b32 v13, 0xffff0000, v50
v_lshlrev_b32 v14, 16, v51
mav 0,144,24
R 44,23552
v_and_b32 v15, 0xffff0000, v51
mav 4,148,24
s_sub_u32 s54, s54, 0x8000
mav 0,152,28
R 16,24576
mav 4,156,28
s_waitcnt lgkmcnt(3)
s_cmp_lt_u32 s54, s56
s_cselect_b64 s[44:45], s[40:41], s[44:45]
mav 0,160,32
R 20,25600
v_add_u32 v87, 1, v87
mav 4,164,32
mav 0,168,36
R 24,26624
mav 4,172,36
s_waitcnt lgkmcnt(3)
buffer_load_dwordx4 v[48:51], v88, s[44:47], s54 offen sc1
mav 0,176,40
R 28,27648
mav 4,180,40
mav 0,184,44
R 32,28672
mav 4,188,44
s_waitcnt lgkmcnt(3)
buffer_load_dword v86, s[36:39], 0 offen sc1 lds
mav 0,192,16
R 36,29696
mav 4,196,16
mav 0,200,20
R 40,30720
mav 4,204,20
s_waitcnt lgkmcnt(3)
mav 0,208,24
R 44,31744
mav 4,212,24
mav 0,216,28
mav 4,220,28
s_waitcnt lgkmcnt(2)
mav 0,224,32
mav 4,228,32
mav 0,232,36
mav 4,236,36
s_waitcnt lgkmcnt(0)
mav 0,240,40
mav 4,244,40
mav 0,248,44
mav 4,252,44
ds_read_b32 v60, v82 offset:0
s_nop 5
v_cvt_pk_bf16_f32 v52, v0, v1
v_cvt_pk_bf16_f32 v53, v2, v3
v_cvt_pk_bf16_f32 v54, v4, v5
v_cvt_pk_bf16_f32 v55, v6, v7
buffer_store_dwordx4 v[52:55], v83, s[36:39], s49 offen sc1
v_mul_f32 v59, v0, v0
v_add_f32 v58, v1, v0
v_fmac_f32 v59, v1, v1
v_add_f32 v58, v2, v58
v_fmac_f32 v59, v2, v2
v_add_f32 v58, v3, v58
v_fmac_f32 v59, v3, v3
v_add_f32 v58, v4, v58
v_fmac_f32 v59, v4, v4
v_add_f32 v58, v5, v58
v_fmac_f32 v59, v5, v5
v_add_f32 v58, v6, v58
v_fmac_f32 v59, v6, v6
v_add_f32 v58, v7, v58
v_fmac_f32 v59, v7, v7
s_nop 1
v_permlane32_swap_b32 v58, v59
v_add_f32 v58, v58, v59
ds_write_b32 v89, v58 offset:0
s_xor_b32 s50, s50, 0x10000
s_waitcnt lgkmcnt(1)
v_cmp_gt_i32 vcc, v87, v60
s_waitcnt vmcnt(4) lgkmcnt(0)
s_add_u32 s49, s49, 313344
s_cbranch_vccnz CO0_3
BO0_3:
s_barrier
s_add_u32 m0, s50, 0
R 16,32768
D 84,0
R 20,33792
R 24,34816
D 84,1024
R 28,35840
R 32,36864
D 84,2048
s_waitcnt lgkmcnt(3)
mav 8,0,16
R 36,37888
mav 12,4,16
mav 8,8,20
R 40,38912
mav 12,12,20
s_waitcnt lgkmcnt(3)
D 84,3072
mav 8,16,24
R 44,39936
mav 12,20,24
s_add_u32 m0, s50, 4096
mav 8,24,28
R 16,40960
mav 12,28,28
s_waitcnt lgkmcnt(3)
D 85,0
mav 8,32,32
R 20,41984
mav 12,36,32
mav 8,40,36
R 24,43008
mav 12,44,36
s_waitcnt lgkmcnt(3)
D 85,1024
mav 8,48,40
R 28,44032
v_add_u32 v56, 1, v56
mav 12,52,40
mav 8,56,44
R 32,45056
mav 12,60,44
s_waitcnt lgkmcnt(3)
D 85,2048
mav 8,64,16
R 36,46080
mav 12,68,16
mav 8,72,20
R 40,47104
mav 12,76,20
s_waitcnt lgkmcnt(3)
D 85,3072
mav 8,80,24
R 44,48128
mav 12,84,24
mav 8,88,28
R 16,49152
mav 12,92,28
s_waitcnt lgkmcnt(3)
mav 8,96,32
R 20,50176
mav 12,100,32
mav 8,104,36
s_mov_b32 m0, 144384
R 24,51200
mav 12,108,36
s_waitcnt lgkmcnt(3)
mav 8,112,40
R 28,52224
mav 12,116,40
mav 8,120,44
s_waitcnt vmcnt(10)
R 32,53248
v_lshlrev_b32 v0, 16, v48
mav 12,124,44
s_waitcnt lgkmcnt(3)
buffer_load_dword v86, s[36:39], 0 offen sc1 lds
mav 8,128,16
R 36,54272
v_and_b32 v1, 0xffff0000, v48
mav 12,132,16
v_lshlrev_b32 v2, 16, v49
v_and_b32 v3, 0xffff0000, v49
mav 8,136,20
R 40,55296
v_lshlrev_b32 v4, 16, v50
mav 12,140,20
s_waitcnt lgkmcnt(3)
v_and_b32 v5, 0xffff0000, v50
v_lshlrev_b32 v6, 16, v51
mav 8,144,24
s_waitcnt vmcnt(9)
R 44,56320
v_and_b32 v7, 0xffff0000, v51
mav 12,148,24
mav 8,152,28
s_sub_u32 s54, s54, 0x8000
R 16,57344
mav 12,156,28
s_waitcnt lgkmcnt(3)
buffer_store_dword v56, v81, s[36:39], s52 offen sc1
mav 8,160,32
R 20,58368
v_add_u32 v87, 1, v87
mav 12,164,32
mav 8,168,36
R 24,59392
mav 12,172,36
s_waitcnt lgkmcnt(3)
buffer_load_dwordx4 v[48:51], v88, s[44:47], s54 offen sc1
mav 8,176,40
R 28,60416
mav 12,180,40
mav 8,184,44
R 32,61440
mav 12,188,44
s_waitcnt lgkmcnt(3)
buffer_load_dword v86, s[36:39], 0 offen sc1 lds
mav 8,192,16
R 36,62464
mav 12,196,16
mav 8,200,20
R 40,63488
mav 12,204,20
s_waitcnt lgkmcnt(3)
mav 8,208,24
R 44,64512
mav 12,212,24
mav 8,216,28
mav 12,220,28
s_waitcnt lgkmcnt(2)
mav 8,224,32
mav 12,228,32
mav 8,232,36
s_mov_b32 s60, -30
.p2align 6
LO0_4:
mav 12,236,36
s_waitcnt lgkmcnt(0)
mav 8,240,40
mav 12,244,40
mav 8,248,44
mav 12,252,44
ds_read_b32 v60, v82 offset:0
s_nop 5
v_cvt_pk_bf16_f32 v52, v8, v9
v_cvt_pk_bf16_f32 v53, v10, v11
v_cvt_pk_bf16_f32 v54, v12, v13
v_cvt_pk_bf16_f32 v55, v14, v15
buffer_store_dwordx4 v[52:55], v83, s[36:39], s49 offen sc1
v_mul_f32 v59, v8, v8
v_add_f32 v58, v9, v8
v_fmac_f32 v59, v9, v9
v_add_f32 v58, v10, v58
v_fmac_f32 v59, v10, v10
v_add_f32 v58, v11, v58
v_fmac_f32 v59, v11, v11
v_add_f32 v58, v12, v58
v_fmac_f32 v59, v12, v12
v_add_f32 v58, v13, v58
v_fmac_f32 v59, v13, v13
v_add_f32 v58, v14, v58
v_fmac_f32 v59, v14, v14
v_add_f32 v58, v15, v58
v_fmac_f32 v59, v15, v15
s_nop 1
v_permlane32_swap_b32 v58, v59
v_add_f32 v58, v58, v59
ds_write_b32 v89, v58 offset:1024
s_waitcnt lgkmcnt(1)
v_cmp_gt_i32 vcc, v87, v60
v_xor_b32 v80, 0x10000, v80
s_waitcnt vmcnt(5) lgkmcnt(0)
s_add_u32 s49, s49, 313344
s_cbranch_vccnz CO0_5
BO0_5:
s_barrier
s_add_u32 m0, s50, 32768
R 16,0
D 84,0
R 20,1024
R 24,2048
D 84,1024
R 28,3072
R 32,4096
D 84,2048
s_waitcnt lgkmcnt(3)
mav 0,0,16
R 36,5120
mav 4,4,16
mav 0,8,20
R 40,6144
mav 4,12,20
s_waitcnt lgkmcnt(3)
D 84,3072
mav 0,16,24
R 44,7168
mav 4,20,24
s_add_u32 m0, s50, 36864
mav 0,24,28
R 16,8192
mav 4,28,28
s_waitcnt lgkmcnt(3)
D 85,0
mav 0,32,32
R 20,9216
mav 4,36,32
mav 0,40,36
R 24,10240
mav 4,44,36
s_waitcnt lgkmcnt(3)
D 85,1024
mav 0,48,40
R 28,11264
v_add_u32 v56, 1, v56
mav 4,52,40
mav 0,56,44
R 32,12288
mav 4,60,44
s_waitcnt lgkmcnt(3)
D 85,2048
mav 0,64,16
R 36,13312
mav 4,68,16
mav 0,72,20
R 40,14336
mav 4,76,20
s_waitcnt lgkmcnt(3)
D 85,3072
mav 0,80,24
R 44,15360
mav 4,84,24
mav 0,88,28
R 16,16384
LsO2:
s_cbranch_execz LsO6
mav 4,92,28
s_waitcnt lgkmcnt(3)
mav 0,96,32
R 20,17408
mav 4,100,32
mav 0,104,36
s_mov_b32 m0, 144384
R 24,18432
mav 4,108,36
s_waitcnt lgkmcnt(3)
mav 0,112,40
R 28,19456
mav 4,116,40
mav 0,120,44
s_waitcnt vmcnt(10)
R 32,20480
v_lshlrev_b32 v8, 16, v48
mav 4,124,44
s_waitcnt lgkmcnt(3)
buffer_load_dword v86, s[36:39], 0 offen sc1 lds
mav 0,128,16
R 36,21504
v_and_b32 v9, 0xffff0000, v48
mav 4,132,16
v_lshlrev_b32 v10, 16, v49
v_and_b32 v11, 0xffff0000, v49
mav 0,136,20
R 40,22528
v_lshlrev_b32 v12, 16, v50
mav 4,140,20
s_waitcnt lgkmcnt(3)
v_and_b32 v13, 0xffff0000, v50
v_lshlrev_b32 v14, 16, v51
mav 0,144,24
s_waitcnt vmcnt(9)
R 44,23552
v_and_b32 v15, 0xffff0000, v51
mav 4,148,24
s_sub_u32 s54, s54, 0x8000
mav 0,152,28
R 16,24576
mav 4,156,28
s_waitcnt lgkmcnt(3)
buffer_store_dword v56, v81, s[36:39], s52 offen sc1
mav 0,160,32
R 20,25600
v_add_u32 v87, 1, v87
mav 4,164,32
s_cmp_lt_u32 s54, s56
s_cselect_b64 s[44:45], s[40:41], s[44:45]
mav 0,168,36
R 24,26624
mav 4,172,36
s_waitcnt lgkmcnt(3)
buffer_load_dwordx4 v[48:51], v88, s[44:47], s54 offen sc1
mav 0,176,40
R 28,27648
mav 4,180,40
mav 0,184,44
R 32,28672
mav 4,188,44
s_waitcnt lgkmcnt(3)
buffer_load_dword v86, s[36:39], 0 offen sc1 lds
mav 0,192,16
R 36,29696
mav 4,196,16
mav 0,200,20
R 40,30720
mav 4,204,20
s_waitcnt lgkmcnt(3)
mav 0,208,24
R 44,31744
mav 4,212,24
mav 0,216,28
mav 4,220,28
s_waitcnt lgkmcnt(2)
mav 0,224,32
mav 4,228,32
mav 0,232,36
mav 4,236,36
s_waitcnt lgkmcnt(0)
mav 0,240,40
mav 4,244,40
mav 0,248,44
mav 4,252,44
ds_read_b32 v60, v82 offset:0
s_nop 5
v_cvt_pk_bf16_f32 v52, v0, v1
v_cvt_pk_bf16_f32 v53, v2, v3
v_cvt_pk_bf16_f32 v54, v4, v5
v_cvt_pk_bf16_f32 v55, v6, v7
buffer_store_dwordx4 v[52:55], v83, s[36:39], s49 offen sc1
v_mul_f32 v59, v0, v0
v_add_f32 v58, v1, v0
v_fmac_f32 v59, v1, v1
v_add_f32 v58, v2, v58
v_fmac_f32 v59, v2, v2
v_add_f32 v58, v3, v58
v_fmac_f32 v59, v3, v3
v_add_f32 v58, v4, v58
v_fmac_f32 v59, v4, v4
v_add_f32 v58, v5, v58
v_fmac_f32 v59, v5, v5
v_add_f32 v58, v6, v58
v_fmac_f32 v59, v6, v6
v_add_f32 v58, v7, v58
v_fmac_f32 v59, v7, v7
s_nop 1
v_permlane32_swap_b32 v58, v59
v_add_f32 v58, v58, v59
ds_write_b32 v89, v58 offset:0
s_xor_b32 s50, s50, 0x10000
s_waitcnt lgkmcnt(1)
v_cmp_gt_i32 vcc, v87, v60
s_waitcnt vmcnt(5) lgkmcnt(0)
s_add_u32 s49, s49, 313344
s_cbranch_vccnz CO0_6
BO0_6:
s_barrier
s_add_u32 m0, s50, 0
R 16,32768
D 84,0
R 20,33792
R 24,34816
D 84,1024
R 28,35840
R 32,36864
D 84,2048
s_waitcnt lgkmcnt(3)
mav 8,0,16
R 36,37888
mav 12,4,16
mav 8,8,20
R 40,38912
mav 12,12,20
s_waitcnt lgkmcnt(3)
D 84,3072
mav 8,16,24
R 44,39936
mav 12,20,24
s_add_u32 m0, s50, 4096
mav 8,24,28
R 16,40960
mav 12,28,28
s_waitcnt lgkmcnt(3)
D 85,0
mav 8,32,32
R 20,41984
mav 12,36,32
mav 8,40,36
R 24,43008
mav 12,44,36
s_waitcnt lgkmcnt(3)
D 85,1024
mav 8,48,40
R 28,44032
v_add_u32 v56, 1, v56
mav 12,52,40
mav 8,56,44
R 32,45056
mav 12,60,44
s_waitcnt lgkmcnt(3)
D 85,2048
mav 8,64,16
R 36,46080
mav 12,68,16
mav 8,72,20
R 40,47104
mav 12,76,20
s_waitcnt lgkmcnt(3)
D 85,3072
mav 8,80,24
R 44,48128
mav 12,84,24
mav 8,88,28
R 16,49152
mav 12,92,28
s_waitcnt lgkmcnt(3)
mav 8,96,32
R 20,50176
mav 12,100,32
mav 8,104,36
s_mov_b32 m0, 144384
R 24,51200
mav 12,108,36
s_waitcnt lgkmcnt(3)
mav 8,112,40
R 28,52224
mav 12,116,40
mav 8,120,44
s_waitcnt vmcnt(10)
R 32,53248
v_lshlrev_b32 v0, 16, v48
mav 12,124,44
s_waitcnt lgkmcnt(3)
buffer_load_dword v86, s[36:39], 0 offen sc1 lds
mav 8,128,16
R 36,54272
v_and_b32 v1, 0xffff0000, v48
mav 12,132,16
v_lshlrev_b32 v2, 16, v49
v_and_b32 v3, 0xffff0000, v49
mav 8,136,20
R 40,55296
v_lshlrev_b32 v4, 16, v50
mav 12,140,20
s_waitcnt lgkmcnt(3)
v_and_b32 v5, 0xffff0000, v50
v_lshlrev_b32 v6, 16, v51
mav 8,144,24
s_waitcnt vmcnt(9)
R 44,56320
v_and_b32 v7, 0xffff0000, v51
mav 12,148,24
mav 8,152,28
s_sub_u32 s54, s54, 0x8000
R 16,57344
mav 12,156,28
s_waitcnt lgkmcnt(3)
buffer_store_dword v56, v81, s[36:39], s52 offen sc1
mav 8,160,32
R 20,58368
v_add_u32 v87, 1, v87
mav 12,164,32
mav 8,168,36
R 24,59392
mav 12,172,36
s_waitcnt lgkmcnt(3)
buffer_load_dwordx4 v[48:51], v88, s[44:47], s54 offen sc1
mav 8,176,40
R 28,60416
mav 12,180,40
mav 8,184,44
R 32,61440
mav 12,188,44
s_waitcnt lgkmcnt(3)
buffer_load_dword v86, s[36:39], 0 offen sc1 lds
mav 8,192,16
R 36,62464
mav 12,196,16
mav 8,200,20
R 40,63488
mav 12,204,20
s_waitcnt lgkmcnt(3)
mav 8,208,24
R 44,64512
mav 12,212,24
mav 8,216,28
mav 12,220,28
s_waitcnt lgkmcnt(2)
mav 8,224,32
mav 12,228,32
s_add_u32 s60, s60, 1
mav 8,232,36
s_cbranch_scc0 LO0_4
mav 12,236,36
s_waitcnt lgkmcnt(0)
mav 8,240,40
mav 12,244,40
mav 8,248,44
mav 12,252,44
ds_read_b32 v60, v82 offset:0
s_nop 5
v_cvt_pk_bf16_f32 v52, v8, v9
v_cvt_pk_bf16_f32 v53, v10, v11
v_cvt_pk_bf16_f32 v54, v12, v13
v_cvt_pk_bf16_f32 v55, v14, v15
buffer_store_dwordx4 v[52:55], v83, s[36:39], s49 offen sc1
v_mul_f32 v59, v8, v8
v_add_f32 v58, v9, v8
v_fmac_f32 v59, v9, v9
v_add_f32 v58, v10, v58
v_fmac_f32 v59, v10, v10
v_add_f32 v58, v11, v58
v_fmac_f32 v59, v11, v11
v_add_f32 v58, v12, v58
v_fmac_f32 v59, v12, v12
v_add_f32 v58, v13, v58
v_fmac_f32 v59, v13, v13
v_add_f32 v58, v14, v58
v_fmac_f32 v59, v14, v14
v_add_f32 v58, v15, v58
v_fmac_f32 v59, v15, v15
s_nop 1
v_permlane32_swap_b32 v58, v59
v_add_f32 v58, v58, v59
ds_write_b32 v89, v58 offset:1024
s_waitcnt lgkmcnt(1)
v_cmp_gt_i32 vcc, v87, v60
v_xor_b32 v80, 0x10000, v80
s_waitcnt vmcnt(5) lgkmcnt(0)
s_add_u32 s49, s49, 313344
s_cbranch_vccnz CO0_7
BO0_7:
s_barrier
s_add_u32 m0, s50, 32768
R 16,0
D 84,0
R 20,1024
R 24,2048
D 84,1024
R 28,3072
R 32,4096
D 84,2048
s_waitcnt lgkmcnt(3)
mav 0,0,16
R 36,5120
mav 4,4,16
mav 0,8,20
R 40,6144
mav 4,12,20
s_waitcnt lgkmcnt(3)
D 84,3072
mav 0,16,24
R 44,7168
mav 4,20,24
s_add_u32 m0, s50, 36864
mav 0,24,28
R 16,8192
mav 4,28,28
s_waitcnt lgkmcnt(3)
D 85,0
mav 0,32,32
R 20,9216
mav 4,36,32
mav 0,40,36
R 24,10240
mav 4,44,36
s_waitcnt lgkmcnt(3)
D 85,1024
mav 0,48,40
R 28,11264
v_add_u32 v56, 1, v56
mav 4,52,40
mav 0,56,44
R 32,12288
mav 4,60,44
s_waitcnt lgkmcnt(3)
D 85,2048
mav 0,64,16
R 36,13312
mav 4,68,16
mav 0,72,20
R 40,14336
mav 4,76,20
s_waitcnt lgkmcnt(3)
D 85,3072
mav 0,80,24
R 44,15360
mav 4,84,24
mav 0,88,28
R 16,16384
mav 4,92,28
s_waitcnt lgkmcnt(3)
mav 0,96,32
R 20,17408
mav 4,100,32
mav 0,104,36
R 24,18432
mav 4,108,36
s_waitcnt lgkmcnt(3)
mav 0,112,40
R 28,19456
mav 4,116,40
mav 0,120,44
s_waitcnt vmcnt(10)
R 32,20480
v_lshlrev_b32 v8, 16, v48
mav 4,124,44
s_waitcnt lgkmcnt(3)
v_and_b32 v9, 0xffff0000, v48
v_lshlrev_b32 v10, 16, v49
mav 0,128,16
R 36,21504
v_and_b32 v11, 0xffff0000, v49
mav 4,132,16
v_lshlrev_b32 v12, 16, v50
v_and_b32 v13, 0xffff0000, v50
mav 0,136,20
R 40,22528
v_lshlrev_b32 v14, 16, v51
mav 4,140,20
s_waitcnt lgkmcnt(3)
v_and_b32 v15, 0xffff0000, v51
mav 0,144,24
s_waitcnt vmcnt(8)
R 44,23552
mav 4,148,24
mav 0,152,28
R 16,24576
mav 4,156,28
s_waitcnt lgkmcnt(3)
buffer_store_dword v56, v81, s[36:39], s52 offen sc1
mav 0,160,32
R 20,25600
mav 4,164,32
mav 0,168,36
R 24,26624
mav 4,172,36
s_waitcnt lgkmcnt(3)
mav 0,176,40
R 28,27648
mav 4,180,40
mav 0,184,44
R 32,28672
mav 4,188,44
s_waitcnt lgkmcnt(3)
mav 0,192,16
R 36,29696
mav 4,196,16
mav 0,200,20
R 40,30720
mav 4,204,20
s_waitcnt lgkmcnt(3)
mav 0,208,24
R 44,31744
mav 4,212,24
mav 0,216,28
mav 4,220,28
s_waitcnt lgkmcnt(2)
mav 0,224,32
mav 4,228,32
mav 0,232,36
mav 4,236,36
s_waitcnt lgkmcnt(0)
mav 0,240,40
mav 4,244,40
mav 0,248,44
mav 4,252,44
s_nop 6
v_cvt_pk_bf16_f32 v52, v0, v1
v_cvt_pk_bf16_f32 v53, v2, v3
v_cvt_pk_bf16_f32 v54, v4, v5
v_cvt_pk_bf16_f32 v55, v6, v7
buffer_store_dwordx4 v[52:55], v83, s[36:39], s49 offen sc1
v_mul_f32 v59, v0, v0
v_add_f32 v58, v1, v0
v_fmac_f32 v59, v1, v1
v_add_f32 v58, v2, v58
v_fmac_f32 v59, v2, v2
v_add_f32 v58, v3, v58
v_fmac_f32 v59, v3, v3
v_add_f32 v58, v4, v58
v_fmac_f32 v59, v4, v4
v_add_f32 v58, v5, v58
v_fmac_f32 v59, v5, v5
v_add_f32 v58, v6, v58
v_fmac_f32 v59, v6, v6
v_add_f32 v58, v7, v58
v_fmac_f32 v59, v7, v7
s_nop 1
v_permlane32_swap_b32 v58, v59
v_add_f32 v58, v58, v59
ds_write_b32 v89, v58 offset:0
s_waitcnt vmcnt(2) lgkmcnt(0)
s_add_u32 s49, s49, 313344
s_barrier
R 16,32768
R 20,33792
R 24,34816
R 28,35840
R 32,36864
s_waitcnt lgkmcnt(3)
mav 8,0,16
R 36,37888
mav 12,4,16
mav 8,8,20
R 40,38912
mav 12,12,20
s_waitcnt lgkmcnt(3)
mav 8,16,24
R 44,39936
mav 12,20,24
mav 8,24,28
R 16,40960
mav 12,28,28
s_waitcnt lgkmcnt(3)
mav 8,32,32
R 20,41984
mav 12,36,32
mav 8,40,36
R 24,43008
mav 12,44,36
s_waitcnt lgkmcnt(3)
mav 8,48,40
R 28,44032
v_add_u32 v56, 1, v56
mav 12,52,40
mav 8,56,44
R 32,45056
mav 12,60,44
s_waitcnt lgkmcnt(3)
mav 8,64,16
R 36,46080
mav 12,68,16
mav 8,72,20
R 40,47104
mav 12,76,20
s_waitcnt lgkmcnt(3)
mav 8,80,24
R 44,48128
mav 12,84,24
mav 8,88,28
R 16,49152
mav 12,92,28
s_waitcnt lgkmcnt(3)
mav 8,96,32
R 20,50176
mav 12,100,32
mav 8,104,36
R 24,51200
mav 12,108,36
s_waitcnt lgkmcnt(3)
mav 8,112,40
R 28,52224
mav 12,116,40
mav 8,120,44
R 32,53248
mav 12,124,44
s_waitcnt lgkmcnt(3)
mav 8,128,16
R 36,54272
mav 12,132,16
mav 8,136,20
R 40,55296
mav 12,140,20
s_waitcnt lgkmcnt(3)
mav 8,144,24
s_waitcnt vmcnt(0)
R 44,56320
mav 12,148,24
mav 8,152,28
R 16,57344
mav 12,156,28
s_waitcnt lgkmcnt(3)
buffer_store_dword v56, v81, s[36:39], s52 offen sc1
mav 8,160,32
R 20,58368
mav 12,164,32
mav 8,168,36
R 24,59392
mav 12,172,36
s_waitcnt lgkmcnt(3)
mav 8,176,40
R 28,60416
mav 12,180,40
mav 8,184,44
R 32,61440
mav 12,188,44
s_waitcnt lgkmcnt(3)
mav 8,192,16
R 36,62464
mav 12,196,16
mav 8,200,20
R 40,63488
mav 12,204,20
s_waitcnt lgkmcnt(3)
mav 8,208,24
R 44,64512
mav 12,212,24
mav 8,216,28
mav 12,220,28
s_waitcnt lgkmcnt(2)
mav 8,224,32
mav 12,228,32
mav 8,232,36
mav 12,236,36
s_waitcnt lgkmcnt(0)
mav 8,240,40
mav 12,244,40
mav 8,248,44
mav 12,252,44
s_nop 6
v_cvt_pk_bf16_f32 v52, v8, v9
v_cvt_pk_bf16_f32 v53, v10, v11
v_cvt_pk_bf16_f32 v54, v12, v13
v_cvt_pk_bf16_f32 v55, v14, v15
buffer_store_dwordx4 v[52:55], v83, s[36:39], s49 offen sc1
v_mul_f32 v59, v8, v8
v_add_f32 v58, v9, v8
v_fmac_f32 v59, v9, v9
v_add_f32 v58, v10, v58
v_fmac_f32 v59, v10, v10
v_add_f32 v58, v11, v58
v_fmac_f32 v59, v11, v11
v_add_f32 v58, v12, v58
v_fmac_f32 v59, v12, v12
v_add_f32 v58, v13, v58
v_fmac_f32 v59, v13, v13
v_add_f32 v58, v14, v58
v_fmac_f32 v59, v14, v14
v_add_f32 v58, v15, v58
v_fmac_f32 v59, v15, v15
s_nop 1
v_permlane32_swap_b32 v58, v59
v_add_f32 v58, v58, v59
ds_write_b32 v89, v58 offset:1024
s_waitcnt lgkmcnt(0)
s_add_u32 s49, s49, 313344
s_barrier
v_add_u32 v56, 37, v56
s_waitcnt vmcnt(0)
buffer_store_dword v56, v81, s[36:39], s52 offen sc1
LsO3:
s_cbranch_execz LsO7
s_endpgm
CO0_1:
s_cmp_eq_u32 s61, 0
s_cbranch_scc1 BO0_1
s_sub_u32 s61, s61, 1
s_sleep 8
buffer_load_dwordx2 v[60:61], v86, s[36:39], 0 offen sc1
s_waitcnt vmcnt(0)
ds_write_b32 v82, v60
v_sub_u32 v60, v60, v87
v_xor_b32 v61, v61, v57
v_cmp_lt_u64 vcc, s[62:63], v[60:61]
s_waitcnt lgkmcnt(0)
s_cbranch_vccnz CO0_1
s_branch BO0_1
CO0_2:
s_mov_b32 s58, m0
s_mov_b32 m0, 144384
DO0_2:
s_cmp_eq_u32 s61, 0
s_cbranch_scc1 EO0_2
s_sub_u32 s61, s61, 1
buffer_load_dword v86, s[36:39], 0 offen sc1 lds
s_sleep 1
ds_read_b32 v60, v82
s_waitcnt lgkmcnt(0)
v_cmp_gt_i32 vcc, v87, v60
s_cbranch_vccnz DO0_2
EO0_2:
s_mov_b32 m0, s58
s_branch BO0_2
CO0_3:
s_mov_b32 s58, m0
s_mov_b32 m0, 144384
DO0_3:
s_cmp_eq_u32 s61, 0
s_cbranch_scc1 EO0_3
s_sub_u32 s61, s61, 1
buffer_load_dword v86, s[36:39], 0 offen sc1 lds
s_sleep 1
ds_read_b32 v60, v82
s_waitcnt lgkmcnt(0)
v_cmp_gt_i32 vcc, v87, v60
s_cbranch_vccnz DO0_3
EO0_3:
s_mov_b32 m0, s58
s_branch BO0_3
CO0_5:
s_mov_b32 s58, m0
s_mov_b32 m0, 144384
DO0_5:
s_cmp_eq_u32 s61, 0
s_cbranch_scc1 EO0_5
s_sub_u32 s61, s61, 1
buffer_load_dword v86, s[36:39], 0 offen sc1 lds
s_sleep 1
ds_read_b32 v60, v82
s_waitcnt lgkmcnt(0)
v_cmp_gt_i32 vcc, v87, v60
s_cbranch_vccnz DO0_5
EO0_5:
s_mov_b32 m0, s58
s_branch BO0_5
CO0_6:
s_mov_b32 s58, m0
s_mov_b32 m0, 144384
DO0_6:
s_cmp_eq_u32 s61, 0
s_cbranch_scc1 EO0_6
s_sub_u32 s61, s61, 1
buffer_load_dword v86, s[36:39], 0 offen sc1 lds
s_sleep 1
ds_read_b32 v60, v82
s_waitcnt lgkmcnt(0)
v_cmp_gt_i32 vcc, v87, v60
s_cbranch_vccnz DO0_6
EO0_6:
s_mov_b32 m0, s58
s_branch BO0_6
CO0_7:
s_mov_b32 s58, m0
s_mov_b32 m0, 144384
DO0_7:
s_cmp_eq_u32 s61, 0
s_cbranch_scc1 EO0_7
s_sub_u32 s61, s61, 1
buffer_load_dword v86, s[36:39], 0 offen sc1 lds
s_sleep 1
ds_read_b32 v60, v82
s_waitcnt lgkmcnt(0)
v_cmp_gt_i32 vcc, v87, v60
s_cbranch_vccnz DO0_7
EO0_7:
s_mov_b32 m0, s58
s_branch BO0_7
LvO1:
LsO4:
s_cbranch_execz LsO8
)"
R"(
s_add_u32 s49, s49, 313344
s_barrier
buffer_load_dwordx4 v[48:51], v88, s[44:47], s54 offen sc1
s_add_u32 m0, s50, 0
s_nop 0
D 84,0
D 84,1024
D 84,2048
D 84,3072
s_add_u32 m0, s50, 4096
s_nop 0
D 85,0
D 85,1024
D 85,2048
D 85,3072
s_waitcnt vmcnt(8)
v_lshlrev_b32 v0, 16, v48
v_and_b32 v1, 0xffff0000, v48
v_lshlrev_b32 v2, 16, v49
v_and_b32 v3, 0xffff0000, v49
v_lshlrev_b32 v4, 16, v50
v_and_b32 v5, 0xffff0000, v50
v_lshlrev_b32 v6, 16, v51
v_and_b32 v7, 0xffff0000, v51
s_sub_u32 s54, s54, 0x8000
buffer_load_dwordx4 v[48:51], v88, s[44:47], s54 offen sc1
s_waitcnt vmcnt(1)
s_add_u32 s49, s49, 313344
s_barrier
s_add_u32 m0, s50, 32768
R 16,0
D 84,0
R 20,1024
R 24,2048
D 84,1024
R 28,3072
R 32,4096
D 84,2048
s_waitcnt lgkmcnt(3)
mav 0,0,16
R 36,5120
mav 4,4,16
mav 0,8,20
R 40,6144
mav 4,12,20
s_waitcnt lgkmcnt(3)
mav 0,16,24
R 44,7168
mav 4,20,24
mav 0,24,28
D 84,3072
mav 4,28,28
s_waitcnt lgkmcnt(2)
R 16,8192
s_add_u32 m0, s50, 36864
mav 0,32,32
R 20,9216
mav 4,36,32
mav 0,40,36
D 85,0
mav 4,44,36
s_waitcnt lgkmcnt(2)
R 24,10240
mav 0,48,40
R 28,11264
mav 4,52,40
mav 0,56,44
D 85,1024
mav 4,60,44
s_waitcnt lgkmcnt(2)
R 32,12288
mav 0,64,16
R 36,13312
mav 4,68,16
mav 0,72,20
D 85,2048
mav 4,76,20
s_waitcnt lgkmcnt(2)
R 40,14336
mav 0,80,24
R 44,15360
mav 4,84,24
mav 0,88,28
D 85,3072
mav 4,92,28
s_waitcnt lgkmcnt(2)
R 16,16384
mav 0,96,32
R 20,17408
mav 4,100,32
mav 0,104,36
R 24,18432
mav 4,108,36
s_waitcnt lgkmcnt(3)
mav 0,112,40
R 28,19456
mav 4,116,40
mav 0,120,44
s_waitcnt vmcnt(8)
R 32,20480
v_lshlrev_b32 v8, 16, v48
mav 4,124,44
s_waitcnt lgkmcnt(3)
v_and_b32 v9, 0xffff0000, v48
v_lshlrev_b32 v10, 16, v49
mav 0,128,16
R 36,21504
v_and_b32 v11, 0xffff0000, v49
mav 4,132,16
v_lshlrev_b32 v12, 16, v50
v_and_b32 v13, 0xffff0000, v50
mav 0,136,20
R 40,22528
v_lshlrev_b32 v14, 16, v51
mav 4,140,20
s_waitcnt lgkmcnt(3)
v_and_b32 v15, 0xffff0000, v51
mav 0,144,24
R 44,23552
mav 4,148,24
s_sub_u32 s54, s54, 0x8000
mav 0,152,28
R 16,24576
mav 4,156,28
s_waitcnt lgkmcnt(3)
s_cmp_lt_u32 s54, s56
s_cselect_b64 s[44:45], s[40:41], s[44:45]
mav 0,160,32
R 20,25600
mav 4,164,32
mav 0,168,36
buffer_load_dwordx4 v[48:51], v88, s[44:47], s54 offen sc1
mav 4,172,36
s_waitcnt lgkmcnt(2)
R 24,26624
mav 0,176,40
R 28,27648
mav 4,180,40
mav 0,184,44
R 32,28672
mav 4,188,44
s_waitcnt lgkmcnt(3)
mav 0,192,16
R 36,29696
mav 4,196,16
mav 0,200,20
R 40,30720
mav 4,204,20
s_waitcnt lgkmcnt(3)
mav 0,208,24
R 44,31744
mav 4,212,24
mav 0,216,28
mav 4,220,28
s_waitcnt lgkmcnt(2)
mav 0,224,32
mav 4,228,32
mav 0,232,36
mav 4,236,36
s_waitcnt lgkmcnt(0)
mav 0,240,40
mav 4,244,40
mav 0,248,44
mav 4,252,44
s_nop 6
v_cvt_pk_bf16_f32 v52, v0, v1
v_cvt_pk_bf16_f32 v53, v2, v3
v_cvt_pk_bf16_f32 v54, v4, v5
v_cvt_pk_bf16_f32 v55, v6, v7
buffer_store_dwordx4 v[52:55], v83, s[36:39], s49 offen sc1
v_mul_f32 v59, v0, v0
v_add_f32 v58, v1, v0
v_fmac_f32 v59, v1, v1
v_add_f32 v58, v2, v58
v_fmac_f32 v59, v2, v2
v_add_f32 v58, v3, v58
v_fmac_f32 v59, v3, v3
v_add_f32 v58, v4, v58
v_fmac_f32 v59, v4, v4
v_add_f32 v58, v5, v58
v_fmac_f32 v59, v5, v5
v_add_f32 v58, v6, v58
v_fmac_f32 v59, v6, v6
v_add_f32 v58, v7, v58
v_fmac_f32 v59, v7, v7
s_nop 1
v_permlane32_swap_b32 v58, v59
v_add_f32 v58, v58, v59
ds_write_b32 v89, v58 offset:0
s_xor_b32 s50, s50, 0x10000
s_waitcnt vmcnt(2) lgkmcnt(0)
s_add_u32 s49, s49, 313344
s_barrier
s_add_u32 m0, s50, 0
R 16,32768
ds_read_b128 v[62:65], v90 offset:0
D 84,0
R 20,33792
R 24,34816
D 84,1024
R 28,35840
s_waitcnt lgkmcnt(3)
v_add_f32 v62, v62, v63
v_add_f32 v64, v64, v65
v_add_f32 v62, v62, v64
s_nop 1
v_add_f32_dpp v63, v62, v62 quad_perm:[1,0,3,2] row_mask:0xf bank_mask:0xf
buffer_store_dword v63, v91, s[36:39], s49 offen sc1
R 32,36864
D 84,2048
mav 8,0,16
R 36,37888
mav 12,4,16
s_waitcnt lgkmcnt(3)
mav 8,8,20
R 40,38912
mav 12,12,20
mav 8,16,24
R 44,39936
mav 12,20,24
s_waitcnt lgkmcnt(3)
mav 8,24,28
D 84,3072
mav 12,28,28
s_add_u32 m0, s50, 4096
R 16,40960
mav 8,32,32
R 20,41984
mav 12,36,32
s_waitcnt lgkmcnt(3)
mav 8,40,36
D 85,0
mav 12,44,36
R 24,43008
mav 8,48,40
R 28,44032
v_add_u32 v56, 1, v56
mav 12,52,40
s_waitcnt lgkmcnt(3)
mav 8,56,44
D 85,1024
mav 12,60,44
R 32,45056
mav 8,64,16
R 36,46080
mav 12,68,16
s_waitcnt lgkmcnt(3)
mav 8,72,20
D 85,2048
mav 12,76,20
R 40,47104
mav 8,80,24
R 44,48128
mav 12,84,24
s_waitcnt lgkmcnt(3)
mav 8,88,28
D 85,3072
mav 12,92,28
R 16,49152
mav 8,96,32
R 20,50176
mav 12,100,32
s_waitcnt lgkmcnt(3)
mav 8,104,36
R 24,51200
mav 12,108,36
mav 8,112,40
R 28,52224
mav 12,116,40
s_waitcnt lgkmcnt(3)
mav 8,120,44
s_waitcnt vmcnt(10)
R 32,53248
v_lshlrev_b32 v0, 16, v48
mav 12,124,44
v_and_b32 v1, 0xffff0000, v48
v_lshlrev_b32 v2, 16, v49
mav 8,128,16
R 36,54272
v_and_b32 v3, 0xffff0000, v49
mav 12,132,16
s_waitcnt lgkmcnt(3)
v_lshlrev_b32 v4, 16, v50
v_and_b32 v5, 0xffff0000, v50
mav 8,136,20
R 40,55296
v_lshlrev_b32 v6, 16, v51
mav 12,140,20
v_and_b32 v7, 0xffff0000, v51
mav 8,144,24
s_waitcnt vmcnt(9)
R 44,56320
mav 12,148,24
s_waitcnt lgkmcnt(3)
mav 8,152,28
buffer_store_dword v56, v81, s[36:39], s52 offen sc1
mav 12,156,28
s_sub_u32 s54, s54, 0x8000
R 16,57344
mav 8,160,32
R 20,58368
mav 12,164,32
s_waitcnt lgkmcnt(3)
mav 8,168,36
buffer_load_dwordx4 v[48:51], v88, s[44:47], s54 offen sc1
mav 12,172,36
R 24,59392
mav 8,176,40
R 28,60416
mav 12,180,40
s_waitcnt lgkmcnt(3)
mav 8,184,44
R 32,61440
mav 12,188,44
mav 8,192,16
R 36,62464
mav 12,196,16
s_waitcnt lgkmcnt(3)
mav 8,200,20
R 40,63488
mav 12,204,20
mav 8,208,24
R 44,64512
mav 12,212,24
s_waitcnt lgkmcnt(3)
mav 8,216,28
mav 12,220,28
mav 8,224,32
mav 12,228,32
s_waitcnt lgkmcnt(1)
mav 8,232,36
s_mov_b32 s60, -30
.p2align 6
LO1_1:
mav 12,236,36
mav 8,240,40
mav 12,244,40
s_waitcnt lgkmcnt(0)
mav 8,248,44
mav 12,252,44
s_nop 6
v_cvt_pk_bf16_f32 v52, v8, v9
v_cvt_pk_bf16_f32 v53, v10, v11
v_cvt_pk_bf16_f32 v54, v12, v13
v_cvt_pk_bf16_f32 v55, v14, v15
buffer_store_dwordx4 v[52:55], v83, s[36:39], s49 offen sc1
v_mul_f32 v59, v8, v8
v_add_f32 v58, v9, v8
v_fmac_f32 v59, v9, v9
v_add_f32 v58, v10, v58
v_fmac_f32 v59, v10, v10
v_add_f32 v58, v11, v58
v_fmac_f32 v59, v11, v11
v_add_f32 v58, v12, v58
v_fmac_f32 v59, v12, v12
v_add_f32 v58, v13, v58
v_fmac_f32 v59, v13, v13
v_add_f32 v58, v14, v58
v_fmac_f32 v59, v14, v14
v_add_f32 v58, v15, v58
v_fmac_f32 v59, v15, v15
s_nop 1
v_permlane32_swap_b32 v58, v59
v_add_f32 v58, v58, v59
ds_write_b32 v89, v58 offset:1024
v_xor_b32 v80, 0x10000, v80
s_waitcnt vmcnt(3) lgkmcnt(0)
s_add_u32 s49, s49, 313344
s_barrier
s_add_u32 m0, s50, 32768
R 16,0
ds_read_b128 v[62:65], v90 offset:1024
D 84,0
R 20,1024
R 24,2048
D 84,1024
R 28,3072
s_waitcnt lgkmcnt(3)
v_add_f32 v62, v62, v63
v_add_f32 v64, v64, v65
v_add_f32 v62, v62, v64
s_nop 1
v_add_f32_dpp v63, v62, v62 quad_perm:[1,0,3,2] row_mask:0xf bank_mask:0xf
buffer_store_dword v63, v91, s[36:39], s49 offen sc1
R 32,4096
D 84,2048
mav 0,0,16
R 36,5120
mav 4,4,16
s_waitcnt lgkmcnt(3)
mav 0,8,20
R 40,6144
mav 4,12,20
mav 0,16,24
R 44,7168
mav 4,20,24
s_waitcnt lgkmcnt(3)
mav 0,24,28
D 84,3072
mav 4,28,28
s_add_u32 m0, s50, 36864
R 16,8192
mav 0,32,32
R 20,9216
mav 4,36,32
s_waitcnt lgkmcnt(3)
mav 0,40,36
D 85,0
mav 4,44,36
R 24,10240
mav 0,48,40
R 28,11264
v_add_u32 v56, 1, v56
mav 4,52,40
s_waitcnt lgkmcnt(3)
mav 0,56,44
D 85,1024
mav 4,60,44
R 32,12288
mav 0,64,16
R 36,13312
mav 4,68,16
s_waitcnt lgkmcnt(3)
mav 0,72,20
D 85,2048
mav 4,76,20
R 40,14336
mav 0,80,24
R 44,15360
mav 4,84,24
s_waitcnt lgkmcnt(3)
mav 0,88,28
D 85,3072
mav 4,92,28
R 16,16384
mav 0,96,32
R 20,17408
mav 4,100,32
s_waitcnt lgkmcnt(3)
mav 0,104,36
R 24,18432
mav 4,108,36
mav 0,112,40
R 28,19456
mav 4,116,40
s_waitcnt lgkmcnt(3)
mav 0,120,44
s_waitcnt vmcnt(10)
R 32,20480
v_lshlrev_b32 v8, 16, v48
mav 4,124,44
v_and_b32 v9, 0xffff0000, v48
v_lshlrev_b32 v10, 16, v49
mav 0,128,16
R 36,21504
v_and_b32 v11, 0xffff0000, v49
mav 4,132,16
s_waitcnt lgkmcnt(3)
v_lshlrev_b32 v12, 16, v50
v_and_b32 v13, 0xffff0000, v50
mav 0,136,20
R 40,22528
v_lshlrev_b32 v14, 16, v51
mav 4,140,20
v_and_b32 v15, 0xffff0000, v51
mav 0,144,24
s_waitcnt vmcnt(9)
R 44,23552
mav 4,148,24
s_waitcnt lgkmcnt(3)
s_sub_u32 s54, s54, 0x8000
mav 0,152,28
buffer_store_dword v56, v81, s[36:39], s52 offen sc1
mav 4,156,28
R 16,24576
mav 0,160,32
R 20,25600
mav 4,164,32
s_waitcnt lgkmcnt(3)
s_cmp_lt_u32 s54, s56
s_cselect_b64 s[44:45], s[40:41], s[44:45]
mav 0,168,36
buffer_load_dwordx4 v[48:51], v88, s[44:47], s54 offen sc1
mav 4,172,36
R 24,26624
mav 0,176,40
R 28,27648
mav 4,180,40
s_waitcnt lgkmcnt(3)
mav 0,184,44
R 32,28672
mav 4,188,44
mav 0,192,16
R 36,29696
mav 4,196,16
s_waitcnt lgkmcnt(3)
mav 0,200,20
R 40,30720
mav 4,204,20
mav 0,208,24
R 44,31744
mav 4,212,24
s_waitcnt lgkmcnt(3)
mav 0,216,28
mav 4,220,28
mav 0,224,32
mav 4,228,32
s_waitcnt lgkmcnt(1)
mav 0,232,36
mav 4,236,36
mav 0,240,40
mav 4,244,40
s_waitcnt lgkmcnt(0)
mav 0,248,44
mav 4,252,44
s_nop 6
v_cvt_pk_bf16_f32 v52, v0, v1
v_cvt_pk_bf16_f32 v53, v2, v3
v_cvt_pk_bf16_f32 v54, v4, v5
v_cvt_pk_bf16_f32 v55, v6, v7
buffer_store_dwordx4 v[52:55], v83, s[36:39], s49 offen sc1
v_mul_f32 v59, v0, v0
v_add_f32 v58, v1, v0
v_fmac_f32 v59, v1, v1
v_add_f32 v58, v2, v58
v_fmac_f32 v59, v2, v2
v_add_f32 v58, v3, v58
v_fmac_f32 v59, v3, v3
v_add_f32 v58, v4, v58
v_fmac_f32 v59, v4, v4
v_add_f32 v58, v5, v58
v_fmac_f32 v59, v5, v5
v_add_f32 v58, v6, v58
v_fmac_f32 v59, v6, v6
v_add_f32 v58, v7, v58
v_fmac_f32 v59, v7, v7
LsO5:
s_cbranch_execz LsO9
s_nop 1
v_permlane32_swap_b32 v58, v59
v_add_f32 v58, v58, v59
ds_write_b32 v89, v58 offset:0
s_xor_b32 s50, s50, 0x10000
s_waitcnt vmcnt(3) lgkmcnt(0)
s_add_u32 s49, s49, 313344
s_barrier
s_add_u32 m0, s50, 0
R 16,32768
ds_read_b128 v[62:65], v90 offset:0
D 84,0
R 20,33792
R 24,34816
D 84,1024
R 28,35840
s_waitcnt lgkmcnt(3)
v_add_f32 v62, v62, v63
v_add_f32 v64, v64, v65
v_add_f32 v62, v62, v64
s_nop 1
v_add_f32_dpp v63, v62, v62 quad_perm:[1,0,3,2] row_mask:0xf bank_mask:0xf
buffer_store_dword v63, v91, s[36:39], s49 offen sc1
R 32,36864
D 84,2048
mav 8,0,16
R 36,37888
mav 12,4,16
s_waitcnt lgkmcnt(3)
mav 8,8,20
R 40,38912
mav 12,12,20
mav 8,16,24
R 44,39936
mav 12,20,24
s_waitcnt lgkmcnt(3)
mav 8,24,28
D 84,3072
mav 12,28,28
s_add_u32 m0, s50, 4096
R 16,40960
mav 8,32,32
R 20,41984
mav 12,36,32
s_waitcnt lgkmcnt(3)
mav 8,40,36
D 85,0
mav 12,44,36
R 24,43008
mav 8,48,40
R 28,44032
v_add_u32 v56, 1, v56
mav 12,52,40
s_waitcnt lgkmcnt(3)
mav 8,56,44
D 85,1024
mav 12,60,44
R 32,45056
mav 8,64,16
R 36,46080
mav 12,68,16
s_waitcnt lgkmcnt(3)
mav 8,72,20
D 85,2048
mav 12,76,20
R 40,47104
mav 8,80,24
R 44,48128
mav 12,84,24
s_waitcnt lgkmcnt(3)
mav 8,88,28
D 85,3072
mav 12,92,28
R 16,49152
mav 8,96,32
R 20,50176
mav 12,100,32
s_waitcnt lgkmcnt(3)
mav 8,104,36
R 24,51200
mav 12,108,36
mav 8,112,40
R 28,52224
mav 12,116,40
s_waitcnt lgkmcnt(3)
mav 8,120,44
s_waitcnt vmcnt(10)
R 32,53248
v_lshlrev_b32 v0, 16, v48
mav 12,124,44
v_and_b32 v1, 0xffff0000, v48
v_lshlrev_b32 v2, 16, v49
mav 8,128,16
R 36,54272
v_and_b32 v3, 0xffff0000, v49
mav 12,132,16
s_waitcnt lgkmcnt(3)
v_lshlrev_b32 v4, 16, v50
v_and_b32 v5, 0xffff0000, v50
mav 8,136,20
R 40,55296
v_lshlrev_b32 v6, 16, v51
mav 12,140,20
v_and_b32 v7, 0xffff0000, v51
mav 8,144,24
s_waitcnt vmcnt(9)
R 44,56320
mav 12,148,24
s_waitcnt lgkmcnt(3)
mav 8,152,28
buffer_store_dword v56, v81, s[36:39], s52 offen sc1
mav 12,156,28
s_sub_u32 s54, s54, 0x8000
R 16,57344
mav 8,160,32
R 20,58368
mav 12,164,32
s_waitcnt lgkmcnt(3)
mav 8,168,36
buffer_load_dwordx4 v[48:51], v88, s[44:47], s54 offen sc1
mav 12,172,36
R 24,59392
mav 8,176,40
R 28,60416
mav 12,180,40
s_waitcnt lgkmcnt(3)
mav 8,184,44
R 32,61440
mav 12,188,44
mav 8,192,16
R 36,62464
mav 12,196,16
s_waitcnt lgkmcnt(3)
mav 8,200,20
R 40,63488
mav 12,204,20
mav 8,208,24
R 44,64512
mav 12,212,24
s_waitcnt lgkmcnt(3)
mav 8,216,28
mav 12,220,28
mav 8,224,32
mav 12,228,32
s_waitcnt lgkmcnt(1)
s_add_u32 s60, s60, 1
mav 8,232,36
s_cbranch_scc0 LO1_1
mav 12,236,36
mav 8,240,40
mav 12,244,40
s_waitcnt lgkmcnt(0)
mav 8,248,44
mav 12,252,44
s_nop 6
v_cvt_pk_bf16_f32 v52, v8, v9
v_cvt_pk_bf16_f32 v53, v10, v11
v_cvt_pk_bf16_f32 v54, v12, v13
v_cvt_pk_bf16_f32 v55, v14, v15
buffer_store_dwordx4 v[52:55], v83, s[36:39], s49 offen sc1
v_mul_f32 v59, v8, v8
v_add_f32 v58, v9, v8
v_fmac_f32 v59, v9, v9
v_add_f32 v58, v10, v58
v_fmac_f32 v59, v10, v10
v_add_f32 v58, v11, v58
v_fmac_f32 v59, v11, v11
v_add_f32 v58, v12, v58
v_fmac_f32 v59, v12, v12
v_add_f32 v58, v13, v58
v_fmac_f32 v59, v13, v13
v_add_f32 v58, v14, v58
v_fmac_f32 v59, v14, v14
v_add_f32 v58, v15, v58
v_fmac_f32 v59, v15, v15
s_nop 1
v_permlane32_swap_b32 v58, v59
v_add_f32 v58, v58, v59
ds_write_b32 v89, v58 offset:1024
v_xor_b32 v80, 0x10000, v80
s_waitcnt vmcnt(3) lgkmcnt(0)
s_add_u32 s49, s49, 313344
s_barrier
s_add_u32 m0, s50, 32768
R 16,0
ds_read_b128 v[62:65], v90 offset:1024
D 84,0
R 20,1024
R 24,2048
D 84,1024
R 28,3072
s_waitcnt lgkmcnt(3)
v_add_f32 v62, v62, v63
v_add_f32 v64, v64, v65
v_add_f32 v62, v62, v64
s_nop 1
v_add_f32_dpp v63, v62, v62 quad_perm:[1,0,3,2] row_mask:0xf bank_mask:0xf
buffer_store_dword v63, v91, s[36:39], s49 offen sc1
R 32,4096
D 84,2048
mav 0,0,16
R 36,5120
mav 4,4,16
s_waitcnt lgkmcnt(3)
mav 0,8,20
R 40,6144
mav 4,12,20
mav 0,16,24
R 44,7168
mav 4,20,24
s_waitcnt lgkmcnt(3)
mav 0,24,28
D 84,3072
mav 4,28,28
s_add_u32 m0, s50, 36864
R 16,8192
mav 0,32,32
R 20,9216
mav 4,36,32
s_waitcnt lgkmcnt(3)
mav 0,40,36
D 85,0
mav 4,44,36
R 24,10240
mav 0,48,40
R 28,11264
v_add_u32 v56, 1, v56
mav 4,52,40
s_waitcnt lgkmcnt(3)
mav 0,56,44
D 85,1024
mav 4,60,44
R 32,12288
mav 0,64,16
R 36,13312
mav 4,68,16
s_waitcnt lgkmcnt(3)
mav 0,72,20
D 85,2048
mav 4,76,20
R 40,14336
mav 0,80,24
R 44,15360
mav 4,84,24
s_waitcnt lgkmcnt(3)
mav 0,88,28
D 85,3072
mav 4,92,28
R 16,16384
mav 0,96,32
R 20,17408
mav 4,100,32
s_waitcnt lgkmcnt(3)
mav 0,104,36
R 24,18432
mav 4,108,36
mav 0,112,40
R 28,19456
mav 4,116,40
s_waitcnt lgkmcnt(3)
mav 0,120,44
s_waitcnt vmcnt(10)
R 32,20480
v_lshlrev_b32 v8, 16, v48
mav 4,124,44
v_and_b32 v9, 0xffff0000, v48
v_lshlrev_b32 v10, 16, v49
mav 0,128,16
R 36,21504
v_and_b32 v11, 0xffff0000, v49
mav 4,132,16
s_waitcnt lgkmcnt(3)
v_lshlrev_b32 v12, 16, v50
v_and_b32 v13, 0xffff0000, v50
mav 0,136,20
R 40,22528
v_lshlrev_b32 v14, 16, v51
mav 4,140,20
v_and_b32 v15, 0xffff0000, v51
mav 0,144,24
s_waitcnt vmcnt(9)
R 44,23552
mav 4,148,24
s_waitcnt lgkmcnt(3)
mav 0,152,28
buffer_store_dword v56, v81, s[36:39], s52 offen sc1
mav 4,156,28
R 16,24576
mav 0,160,32
R 20,25600
mav 4,164,32
s_waitcnt lgkmcnt(3)
mav 0,168,36
R 24,26624
mav 4,172,36
mav 0,176,40
R 28,27648
mav 4,180,40
s_waitcnt lgkmcnt(3)
mav 0,184,44
R 32,28672
mav 4,188,44
mav 0,192,16
R 36,29696
mav 4,196,16
s_waitcnt lgkmcnt(3)
mav 0,200,20
R 40,30720
mav 4,204,20
mav 0,208,24
R 44,31744
mav 4,212,24
s_waitcnt lgkmcnt(3)
mav 0,216,28
mav 4,220,28
mav 0,224,32
mav 4,228,32
s_waitcnt lgkmcnt(1)
mav 0,232,36
mav 4,236,36
mav 0,240,40
mav 4,244,40
s_waitcnt lgkmcnt(0)
mav 0,248,44
mav 4,252,44
s_nop 6
v_cvt_pk_bf16_f32 v52, v0, v1
v_cvt_pk_bf16_f32 v53, v2, v3
v_cvt_pk_bf16_f32 v54, v4, v5
v_cvt_pk_bf16_f32 v55, v6, v7
buffer_store_dwordx4 v[52:55], v83, s[36:39], s49 offen sc1
v_mul_f32 v59, v0, v0
v_add_f32 v58, v1, v0
v_fmac_f32 v59, v1, v1
v_add_f32 v58, v2, v58
v_fmac_f32 v59, v2, v2
v_add_f32 v58, v3, v58
v_fmac_f32 v59, v3, v3
v_add_f32 v58, v4, v58
v_fmac_f32 v59, v4, v4
v_add_f32 v58, v5, v58
v_fmac_f32 v59, v5, v5
v_add_f32 v58, v6, v58
v_fmac_f32 v59, v6, v6
v_add_f32 v58, v7, v58
v_fmac_f32 v59, v7, v7
s_nop 1
v_permlane32_swap_b32 v58, v59
v_add_f32 v58, v58, v59
ds_write_b32 v89, v58 offset:0
s_waitcnt vmcnt(2) lgkmcnt(0)
s_add_u32 s49, s49, 313344
s_barrier
R 16,32768
ds_read_b128 v[62:65], v90 offset:0
R 20,33792
R 24,34816
R 28,35840
s_waitcnt lgkmcnt(3)
v_add_f32 v62, v62, v63
v_add_f32 v64, v64, v65
v_add_f32 v62, v62, v64
s_nop 1
v_add_f32_dpp v63, v62, v62 quad_perm:[1,0,3,2] row_mask:0xf bank_mask:0xf
buffer_store_dword v63, v91, s[36:39], s49 offen sc1
R 32,36864
mav 8,0,16
R 36,37888
mav 12,4,16
s_waitcnt lgkmcnt(3)
mav 8,8,20
R 40,38912
mav 12,12,20
mav 8,16,24
R 44,39936
mav 12,20,24
s_waitcnt lgkmcnt(3)
mav 8,24,28
R 16,40960
mav 12,28,28
mav 8,32,32
R 20,41984
mav 12,36,32
s_waitcnt lgkmcnt(3)
mav 8,40,36
R 24,43008
mav 12,44,36
mav 8,48,40
R 28,44032
v_add_u32 v56, 1, v56
mav 12,52,40
s_waitcnt lgkmcnt(3)
mav 8,56,44
R 32,45056
mav 12,60,44
mav 8,64,16
R 36,46080
mav 12,68,16
s_waitcnt lgkmcnt(3)
mav 8,72,20
R 40,47104
mav 12,76,20
mav 8,80,24
R 44,48128
mav 12,84,24
s_waitcnt lgkmcnt(3)
mav 8,88,28
R 16,49152
mav 12,92,28
mav 8,96,32
R 20,50176
mav 12,100,32
s_waitcnt lgkmcnt(3)
mav 8,104,36
R 24,51200
mav 12,108,36
mav 8,112,40
R 28,52224
mav 12,116,40
s_waitcnt lgkmcnt(3)
mav 8,120,44
R 32,53248
mav 12,124,44
mav 8,128,16
R 36,54272
mav 12,132,16
s_waitcnt lgkmcnt(3)
mav 8,136,20
R 40,55296
mav 12,140,20
mav 8,144,24
s_waitcnt vmcnt(1)
R 44,56320
mav 12,148,24
s_waitcnt lgkmcnt(3)
mav 8,152,28
buffer_store_dword v56, v81, s[36:39], s52 offen sc1
mav 12,156,28
R 16,57344
mav 8,160,32
R 20,58368
mav 12,164,32
s_waitcnt lgkmcnt(3)
mav 8,168,36
R 24,59392
mav 12,172,36
mav 8,176,40
R 28,60416
mav 12,180,40
s_waitcnt lgkmcnt(3)
mav 8,184,44
R 32,61440
mav 12,188,44
mav 8,192,16
R 36,62464
mav 12,196,16
s_waitcnt lgkmcnt(3)
mav 8,200,20
R 40,63488
mav 12,204,20
mav 8,208,24
R 44,64512
mav 12,212,24
s_waitcnt lgkmcnt(3)
mav 8,216,28
mav 12,220,28
mav 8,224,32
mav 12,228,32
s_waitcnt lgkmcnt(1)
mav 8,232,36
mav 12,236,36
mav 8,240,40
mav 12,244,40
s_waitcnt lgkmcnt(0)
mav 8,248,44
mav 12,252,44
s_nop 6
v_cvt_pk_bf16_f32 v52, v8, v9
v_cvt_pk_bf16_f32 v53, v10, v11
v_cvt_pk_bf16_f32 v54, v12, v13
v_cvt_pk_bf16_f32 v55, v14, v15
buffer_store_dwordx4 v[52:55], v83, s[36:39], s49 offen sc1
v_mul_f32 v59, v8, v8
v_add_f32 v58, v9, v8
v_fmac_f32 v59, v9, v9
v_add_f32 v58, v10, v58
v_fmac_f32 v59, v10, v10
v_add_f32 v58, v11, v58
v_fmac_f32 v59, v11, v11
v_add_f32 v58, v12, v58
v_fmac_f32 v59, v12, v12
v_add_f32 v58, v13, v58
v_fmac_f32 v59, v13, v13
v_add_f32 v58, v14, v58
v_fmac_f32 v59, v14, v14
v_add_f32 v58, v15, v58
v_fmac_f32 v59, v15, v15
s_nop 1
v_permlane32_swap_b32 v58, v59
v_add_f32 v58, v58, v59
ds_write_b32 v89, v58 offset:1024
s_waitcnt lgkmcnt(0)
s_add_u32 s49, s49, 313344
s_barrier
ds_read_b128 v[62:65], v90 offset:1024
s_waitcnt lgkmcnt(0)
v_add_f32 v62, v62, v63
v_add_f32 v64, v64, v65
v_add_f32 v62, v62, v64
s_nop 1
v_add_f32_dpp v63, v62, v62 quad_perm:[1,0,3,2] row_mask:0xf bank_mask:0xf
buffer_store_dword v63, v91, s[36:39], s49 offen sc1
v_add_u32 v56, 37, v56
s_waitcnt vmcnt(0)
buffer_store_dword v56, v81, s[36:39], s52 offen sc1
s_endpgm
LvO2:
LsO6:
s_cbranch_execz LsO10
s_add_u32 s49, s49, 313344
s_barrier
buffer_load_dwordx4 v[48:51], v88, s[44:47], s54 offen sc1
s_add_u32 m0, s50, 0
s_nop 0
D 84,0
D 84,1024
D 84,2048
D 84,3072
s_add_u32 m0, s50, 4096
s_nop 0
D 85,0
D 85,1024
D 85,2048
D 85,3072
s_waitcnt vmcnt(8)
v_lshlrev_b32 v0, 16, v48
v_and_b32 v1, 0xffff0000, v48
v_lshlrev_b32 v2, 16, v49
v_and_b32 v3, 0xffff0000, v49
v_lshlrev_b32 v4, 16, v50
v_and_b32 v5, 0xffff0000, v50
v_lshlrev_b32 v6, 16, v51
v_and_b32 v7, 0xffff0000, v51
s_sub_u32 s54, s54, 0x8000
buffer_load_dwordx4 v[48:51], v88, s[44:47], s54 offen sc1
s_waitcnt vmcnt(1)
s_add_u32 s49, s49, 313344
s_barrier
s_add_u32 m0, s50, 32768
R 16,0
D 84,0
R 20,1024
R 24,2048
D 84,1024
R 28,3072
R 32,4096
D 84,2048
s_waitcnt lgkmcnt(3)
mav 0,0,16
R 36,5120
mav 4,4,16
mav 0,8,20
R 40,6144
mav 4,12,20
s_waitcnt lgkmcnt(3)
mav 0,16,24
D 84,3072
mav 4,20,24
s_add_u32 m0, s50, 36864
R 44,7168
mav 0,24,28
R 16,8192
mav 4,28,28
s_waitcnt lgkmcnt(3)
mav 0,32,32
D 85,0
mav 4,36,32
R 20,9216
mav 0,40,36
R 24,10240
mav 4,44,36
s_waitcnt lgkmcnt(3)
mav 0,48,40
D 85,1024
mav 4,52,40
R 28,11264
mav 0,56,44
R 32,12288
mav 4,60,44
s_waitcnt lgkmcnt(3)
mav 0,64,16
D 85,2048
mav 4,68,16
R 36,13312
mav 0,72,20
R 40,14336
mav 4,76,20
s_waitcnt lgkmcnt(3)
mav 0,80,24
D 85,3072
mav 4,84,24
R 44,15360
LsO7:
s_cbranch_execz LsO11
mav 0,88,28
R 16,16384
mav 4,92,28
s_waitcnt lgkmcnt(3)
mav 0,96,32
R 20,17408
mav 4,100,32
mav 0,104,36
R 24,18432
mav 4,108,36
s_waitcnt lgkmcnt(3)
mav 0,112,40
R 28,19456
mav 4,116,40
mav 0,120,44
s_waitcnt vmcnt(8)
R 32,20480
v_lshlrev_b32 v8, 16, v48
mav 4,124,44
s_waitcnt lgkmcnt(3)
v_and_b32 v9, 0xffff0000, v48
v_lshlrev_b32 v10, 16, v49
mav 0,128,16
R 36,21504
v_and_b32 v11, 0xffff0000, v49
mav 4,132,16
v_lshlrev_b32 v12, 16, v50
v_and_b32 v13, 0xffff0000, v50
mav 0,136,20
R 40,22528
v_lshlrev_b32 v14, 16, v51
mav 4,140,20
s_waitcnt lgkmcnt(3)
v_and_b32 v15, 0xffff0000, v51
mav 0,144,24
R 44,23552
mav 4,148,24
s_sub_u32 s54, s54, 0x8000
mav 0,152,28
R 16,24576
mav 4,156,28
s_waitcnt lgkmcnt(3)
s_cmp_lt_u32 s54, s56
s_cselect_b64 s[44:45], s[40:41], s[44:45]
mav 0,160,32
buffer_load_dwordx4 v[48:51], v88, s[44:47], s54 offen sc1
mav 4,164,32
R 20,25600
mav 0,168,36
R 24,26624
mav 4,172,36
s_waitcnt lgkmcnt(3)
mav 0,176,40
R 28,27648
mav 4,180,40
mav 0,184,44
R 32,28672
mav 4,188,44
s_waitcnt lgkmcnt(3)
mav 0,192,16
R 36,29696
mav 4,196,16
mav 0,200,20
R 40,30720
mav 4,204,20
s_waitcnt lgkmcnt(3)
mav 0,208,24
R 44,31744
mav 4,212,24
mav 0,216,28
mav 4,220,28
s_waitcnt lgkmcnt(2)
mav 0,224,32
mav 4,228,32
mav 0,232,36
mav 4,236,36
s_waitcnt lgkmcnt(0)
mav 0,240,40
mav 4,244,40
mav 0,248,44
mav 4,252,44
s_nop 6
v_cvt_pk_bf16_f32 v52, v0, v1
v_cvt_pk_bf16_f32 v53, v2, v3
v_cvt_pk_bf16_f32 v54, v4, v5
v_cvt_pk_bf16_f32 v55, v6, v7
buffer_store_dwordx4 v[52:55], v83, s[36:39], s49 offen sc1
v_mul_f32 v59, v0, v0
v_add_f32 v58, v1, v0
v_fmac_f32 v59, v1, v1
v_add_f32 v58, v2, v58
v_fmac_f32 v59, v2, v2
v_add_f32 v58, v3, v58
v_fmac_f32 v59, v3, v3
v_add_f32 v58, v4, v58
v_fmac_f32 v59, v4, v4
v_add_f32 v58, v5, v58
v_fmac_f32 v59, v5, v5
v_add_f32 v58, v6, v58
v_fmac_f32 v59, v6, v6
v_add_f32 v58, v7, v58
v_fmac_f32 v59, v7, v7
s_nop 1
v_permlane32_swap_b32 v58, v59
v_add_f32 v58, v58, v59
ds_write_b32 v89, v58 offset:0
s_xor_b32 s50, s50, 0x10000
s_waitcnt vmcnt(2) lgkmcnt(0)
s_add_u32 s49, s49, 313344
s_barrier
s_add_u32 m0, s50, 0
R 16,32768
D 84,0
R 20,33792
R 24,34816
D 84,1024
R 28,35840
R 32,36864
D 84,2048
s_waitcnt lgkmcnt(3)
mav 8,0,16
R 36,37888
mav 12,4,16
mav 8,8,20
R 40,38912
mav 12,12,20
s_waitcnt lgkmcnt(3)
mav 8,16,24
D 84,3072
mav 12,20,24
s_add_u32 m0, s50, 4096
R 44,39936
mav 8,24,28
R 16,40960
mav 12,28,28
s_waitcnt lgkmcnt(3)
mav 8,32,32
D 85,0
mav 12,36,32
R 20,41984
mav 8,40,36
R 24,43008
mav 12,44,36
s_waitcnt lgkmcnt(3)
mav 8,48,40
D 85,1024
mav 12,52,40
R 28,44032
v_add_u32 v56, 1, v56
mav 8,56,44
R 32,45056
mav 12,60,44
s_waitcnt lgkmcnt(3)
mav 8,64,16
D 85,2048
mav 12,68,16
R 36,46080
mav 8,72,20
R 40,47104
mav 12,76,20
s_waitcnt lgkmcnt(3)
mav 8,80,24
D 85,3072
mav 12,84,24
R 44,48128
mav 8,88,28
R 16,49152
mav 12,92,28
s_waitcnt lgkmcnt(3)
mav 8,96,32
R 20,50176
mav 12,100,32
mav 8,104,36
R 24,51200
mav 12,108,36
s_waitcnt lgkmcnt(3)
mav 8,112,40
R 28,52224
mav 12,116,40
mav 8,120,44
s_waitcnt vmcnt(9)
R 32,53248
v_lshlrev_b32 v0, 16, v48
mav 12,124,44
s_waitcnt lgkmcnt(3)
v_and_b32 v1, 0xffff0000, v48
v_lshlrev_b32 v2, 16, v49
mav 8,128,16
R 36,54272
v_and_b32 v3, 0xffff0000, v49
mav 12,132,16
v_lshlrev_b32 v4, 16, v50
v_and_b32 v5, 0xffff0000, v50
mav 8,136,20
R 40,55296
v_lshlrev_b32 v6, 16, v51
mav 12,140,20
s_waitcnt lgkmcnt(3)
v_and_b32 v7, 0xffff0000, v51
mav 8,144,24
s_waitcnt vmcnt(8)
buffer_store_dword v56, v81, s[36:39], s52 offen sc1
mav 12,148,24
R 44,56320
mav 8,152,28
s_sub_u32 s54, s54, 0x8000
R 16,57344
mav 12,156,28
s_waitcnt lgkmcnt(3)
mav 8,160,32
buffer_load_dwordx4 v[48:51], v88, s[44:47], s54 offen sc1
mav 12,164,32
R 20,58368
mav 8,168,36
R 24,59392
mav 12,172,36
s_waitcnt lgkmcnt(3)
mav 8,176,40
R 28,60416
mav 12,180,40
mav 8,184,44
R 32,61440
mav 12,188,44
s_waitcnt lgkmcnt(3)
mav 8,192,16
R 36,62464
mav 12,196,16
mav 8,200,20
R 40,63488
mav 12,204,20
s_waitcnt lgkmcnt(3)
mav 8,208,24
R 44,64512
mav 12,212,24
mav 8,216,28
mav 12,220,28
s_waitcnt lgkmcnt(2)
mav 8,224,32
mav 12,228,32
mav 8,232,36
s_mov_b32 s60, -30
.p2align 6
LO2_1:
mav 12,236,36
s_waitcnt lgkmcnt(0)
mav 8,240,40
mav 12,244,40
mav 8,248,44
mav 12,252,44
s_nop 6
v_cvt_pk_bf16_f32 v52, v8, v9
v_cvt_pk_bf16_f32 v53, v10, v11
v_cvt_pk_bf16_f32 v54, v12, v13
v_cvt_pk_bf16_f32 v55, v14, v15
buffer_store_dwordx4 v[52:55], v83, s[36:39], s49 offen sc1
v_mul_f32 v59, v8, v8
v_add_f32 v58, v9, v8
v_fmac_f32 v59, v9, v9
v_add_f32 v58, v10, v58
v_fmac_f32 v59, v10, v10
v_add_f32 v58, v11, v58
v_fmac_f32 v59, v11, v11
v_add_f32 v58, v12, v58
v_fmac_f32 v59, v12, v12
v_add_f32 v58, v13, v58
v_fmac_f32 v59, v13, v13
v_add_f32 v58, v14, v58
v_fmac_f32 v59, v14, v14
v_add_f32 v58, v15, v58
v_fmac_f32 v59, v15, v15
s_nop 1
v_permlane32_swap_b32 v58, v59
v_add_f32 v58, v58, v59
ds_write_b32 v89, v58 offset:1024
v_xor_b32 v80, 0x10000, v80
s_waitcnt vmcnt(3) lgkmcnt(0)
s_add_u32 s49, s49, 313344
s_barrier
s_add_u32 m0, s50, 32768
R 16,0
D 84,0
R 20,1024
R 24,2048
D 84,1024
R 28,3072
R 32,4096
D 84,2048
s_waitcnt lgkmcnt(3)
mav 0,0,16
R 36,5120
mav 4,4,16
mav 0,8,20
R 40,6144
mav 4,12,20
s_waitcnt lgkmcnt(3)
mav 0,16,24
D 84,3072
mav 4,20,24
s_add_u32 m0, s50, 36864
R 44,7168
mav 0,24,28
R 16,8192
mav 4,28,28
s_waitcnt lgkmcnt(3)
mav 0,32,32
D 85,0
mav 4,36,32
R 20,9216
mav 0,40,36
R 24,10240
mav 4,44,36
s_waitcnt lgkmcnt(3)
mav 0,48,40
D 85,1024
mav 4,52,40
R 28,11264
v_add_u32 v56, 1, v56
mav 0,56,44
R 32,12288
mav 4,60,44
s_waitcnt lgkmcnt(3)
mav 0,64,16
D 85,2048
mav 4,68,16
R 36,13312
mav 0,72,20
R 40,14336
mav 4,76,20
s_waitcnt lgkmcnt(3)
mav 0,80,24
D 85,3072
mav 4,84,24
R 44,15360
mav 0,88,28
R 16,16384
mav 4,92,28
s_waitcnt lgkmcnt(3)
mav 0,96,32
R 20,17408
mav 4,100,32
mav 0,104,36
R 24,18432
mav 4,108,36
s_waitcnt lgkmcnt(3)
mav 0,112,40
R 28,19456
mav 4,116,40
mav 0,120,44
s_waitcnt vmcnt(9)
R 32,20480
v_lshlrev_b32 v8, 16, v48
mav 4,124,44
s_waitcnt lgkmcnt(3)
v_and_b32 v9, 0xffff0000, v48
v_lshlrev_b32 v10, 16, v49
mav 0,128,16
R 36,21504
v_and_b32 v11, 0xffff0000, v49
mav 4,132,16
v_lshlrev_b32 v12, 16, v50
v_and_b32 v13, 0xffff0000, v50
mav 0,136,20
R 40,22528
v_lshlrev_b32 v14, 16, v51
mav 4,140,20
s_waitcnt lgkmcnt(3)
v_and_b32 v15, 0xffff0000, v51
mav 0,144,24
s_waitcnt vmcnt(8)
buffer_store_dword v56, v81, s[36:39], s52 offen sc1
mav 4,148,24
s_sub_u32 s54, s54, 0x8000
R 44,23552
mav 0,152,28
R 16,24576
mav 4,156,28
s_waitcnt lgkmcnt(3)
s_cmp_lt_u32 s54, s56
s_cselect_b64 s[44:45], s[40:41], s[44:45]
mav 0,160,32
buffer_load_dwordx4 v[48:51], v88, s[44:47], s54 offen sc1
mav 4,164,32
R 20,25600
mav 0,168,36
R 24,26624
mav 4,172,36
s_waitcnt lgkmcnt(3)
mav 0,176,40
R 28,27648
mav 4,180,40
mav 0,184,44
R 32,28672
mav 4,188,44
s_waitcnt lgkmcnt(3)
mav 0,192,16
R 36,29696
mav 4,196,16
mav 0,200,20
R 40,30720
mav 4,204,20
s_waitcnt lgkmcnt(3)
mav 0,208,24
R 44,31744
mav 4,212,24
mav 0,216,28
mav 4,220,28
s_waitcnt lgkmcnt(2)
mav 0,224,32
mav 4,228,32
mav 0,232,36
mav 4,236,36
s_waitcnt lgkmcnt(0)
mav 0,240,40
mav 4,244,40
mav 0,248,44
mav 4,252,44
s_nop 6
v_cvt_pk_bf16_f32 v52, v0, v1
v_cvt_pk_bf16_f32 v53, v2, v3
v_cvt_pk_bf16_f32 v54, v4, v5
v_cvt_pk_bf16_f32 v55, v6, v7
buffer_store_dwordx4 v[52:55], v83, s[36:39], s49 offen sc1
v_mul_f32 v59, v0, v0
v_add_f32 v58, v1, v0
v_fmac_f32 v59, v1, v1
v_add_f32 v58, v2, v58
v_fmac_f32 v59, v2, v2
v_add_f32 v58, v3, v58
v_fmac_f32 v59, v3, v3
v_add_f32 v58, v4, v58
v_fmac_f32 v59, v4, v4
v_add_f32 v58, v5, v58
v_fmac_f32 v59, v5, v5
v_add_f32 v58, v6, v58
v_fmac_f32 v59, v6, v6
v_add_f32 v58, v7, v58
v_fmac_f32 v59, v7, v7
s_nop 1
v_permlane32_swap_b32 v58, v59
v_add_f32 v58, v58, v59
ds_write_b32 v89, v58 offset:0
s_xor_b32 s50, s50, 0x10000
s_waitcnt vmcnt(3) lgkmcnt(0)
s_add_u32 s49, s49, 313344
s_barrier
s_add_u32 m0, s50, 0
R 16,32768
D 84,0
R 20,33792
R 24,34816
D 84,1024
R 28,35840
R 32,36864
D 84,2048
s_waitcnt lgkmcnt(3)
mav 8,0,16
R 36,37888
mav 12,4,16
mav 8,8,20
R 40,38912
mav 12,12,20
s_waitcnt lgkmcnt(3)
mav 8,16,24
D 84,3072
mav 12,20,24
s_add_u32 m0, s50, 4096
R 44,39936
mav 8,24,28
R 16,40960
mav 12,28,28
s_waitcnt lgkmcnt(3)
mav 8,32,32
D 85,0
mav 12,36,32
R 20,41984
mav 8,40,36
R 24,43008
mav 12,44,36
s_waitcnt lgkmcnt(3)
mav 8,48,40
D 85,1024
mav 12,52,40
R 28,44032
v_add_u32 v56, 1, v56
mav 8,56,44
R 32,45056
mav 12,60,44
s_waitcnt lgkmcnt(3)
mav 8,64,16
D 85,2048
mav 12,68,16
R 36,46080
mav 8,72,20
R 40,47104
mav 12,76,20
s_waitcnt lgkmcnt(3)
mav 8,80,24
D 85,3072
mav 12,84,24
R 44,48128
mav 8,88,28
R 16,49152
mav 12,92,28
s_waitcnt lgkmcnt(3)
mav 8,96,32
R 20,50176
mav 12,100,32
mav 8,104,36
R 24,51200
mav 12,108,36
s_waitcnt lgkmcnt(3)
mav 8,112,40
R 28,52224
mav 12,116,40
mav 8,120,44
s_waitcnt vmcnt(9)
R 32,53248
v_lshlrev_b32 v0, 16, v48
mav 12,124,44
s_waitcnt lgkmcnt(3)
v_and_b32 v1, 0xffff0000, v48
v_lshlrev_b32 v2, 16, v49
mav 8,128,16
R 36,54272
v_and_b32 v3, 0xffff0000, v49
mav 12,132,16
v_lshlrev_b32 v4, 16, v50
v_and_b32 v5, 0xffff0000, v50
mav 8,136,20
R 40,55296
v_lshlrev_b32 v6, 16, v51
mav 12,140,20
s_waitcnt lgkmcnt(3)
v_and_b32 v7, 0xffff0000, v51
mav 8,144,24
s_waitcnt vmcnt(8)
buffer_store_dword v56, v81, s[36:39], s52 offen sc1
mav 12,148,24
R 44,56320
mav 8,152,28
s_sub_u32 s54, s54, 0x8000
R 16,57344
mav 12,156,28
s_waitcnt lgkmcnt(3)
mav 8,160,32
buffer_load_dwordx4 v[48:51], v88, s[44:47], s54 offen sc1
mav 12,164,32
R 20,58368
mav 8,168,36
R 24,59392
mav 12,172,36
s_waitcnt lgkmcnt(3)
mav 8,176,40
R 28,60416
mav 12,180,40
mav 8,184,44
R 32,61440
mav 12,188,44
s_waitcnt lgkmcnt(3)
mav 8,192,16
R 36,62464
mav 12,196,16
mav 8,200,20
R 40,63488
mav 12,204,20
s_waitcnt lgkmcnt(3)
mav 8,208,24
R 44,64512
mav 12,212,24
mav 8,216,28
mav 12,220,28
s_waitcnt lgkmcnt(2)
mav 8,224,32
mav 12,228,32
s_add_u32 s60, s60, 1
mav 8,232,36
s_cbranch_scc0 LO2_1
mav 12,236,36
s_waitcnt lgkmcnt(0)
mav 8,240,40
mav 12,244,40
mav 8,248,44
mav 12,252,44
s_nop 6
v_cvt_pk_bf16_f32 v52, v8, v9
v_cvt_pk_bf16_f32 v53, v10, v11
v_cvt_pk_bf16_f32 v54, v12, v13
LsO8:
s_cbranch_execz LwO
v_cvt_pk_bf16_f32 v55, v14, v15
buffer_store_dwordx4 v[52:55], v83, s[36:39], s49 offen sc1
v_mul_f32 v59, v8, v8
v_add_f32 v58, v9, v8
v_fmac_f32 v59, v9, v9
v_add_f32 v58, v10, v58
v_fmac_f32 v59, v10, v10
v_add_f32 v58, v11, v58
v_fmac_f32 v59, v11, v11
v_add_f32 v58, v12, v58
v_fmac_f32 v59, v12, v12
v_add_f32 v58, v13, v58
v_fmac_f32 v59, v13, v13
v_add_f32 v58, v14, v58
v_fmac_f32 v59, v14, v14
v_add_f32 v58, v15, v58
v_fmac_f32 v59, v15, v15
s_nop 1
v_permlane32_swap_b32 v58, v59
v_add_f32 v58, v58, v59
ds_write_b32 v89, v58 offset:1024
v_xor_b32 v80, 0x10000, v80
s_waitcnt vmcnt(3) lgkmcnt(0)
s_add_u32 s49, s49, 313344
s_barrier
s_add_u32 m0, s50, 32768
R 16,0
D 84,0
R 20,1024
R 24,2048
D 84,1024
R 28,3072
R 32,4096
D 84,2048
s_waitcnt lgkmcnt(3)
mav 0,0,16
R 36,5120
mav 4,4,16
mav 0,8,20
R 40,6144
mav 4,12,20
s_waitcnt lgkmcnt(3)
mav 0,16,24
D 84,3072
mav 4,20,24
s_add_u32 m0, s50, 36864
R 44,7168
mav 0,24,28
R 16,8192
mav 4,28,28
s_waitcnt lgkmcnt(3)
mav 0,32,32
D 85,0
mav 4,36,32
R 20,9216
mav 0,40,36
R 24,10240
mav 4,44,36
s_waitcnt lgkmcnt(3)
mav 0,48,40
D 85,1024
mav 4,52,40
R 28,11264
v_add_u32 v56, 1, v56
mav 0,56,44
R 32,12288
mav 4,60,44
s_waitcnt lgkmcnt(3)
mav 0,64,16
D 85,2048
mav 4,68,16
R 36,13312
mav 0,72,20
R 40,14336
mav 4,76,20
s_waitcnt lgkmcnt(3)
mav 0,80,24
D 85,3072
mav 4,84,24
R 44,15360
mav 0,88,28
R 16,16384
mav 4,92,28
s_waitcnt lgkmcnt(3)
mav 0,96,32
R 20,17408
mav 4,100,32
mav 0,104,36
R 24,18432
mav 4,108,36
s_waitcnt lgkmcnt(3)
mav 0,112,40
R 28,19456
mav 4,116,40
mav 0,120,44
s_waitcnt vmcnt(9)
R 32,20480
v_lshlrev_b32 v8, 16, v48
mav 4,124,44
s_waitcnt lgkmcnt(3)
v_and_b32 v9, 0xffff0000, v48
v_lshlrev_b32 v10, 16, v49
mav 0,128,16
R 36,21504
v_and_b32 v11, 0xffff0000, v49
mav 4,132,16
v_lshlrev_b32 v12, 16, v50
v_and_b32 v13, 0xffff0000, v50
mav 0,136,20
R 40,22528
v_lshlrev_b32 v14, 16, v51
mav 4,140,20
s_waitcnt lgkmcnt(3)
v_and_b32 v15, 0xffff0000, v51
mav 0,144,24
s_waitcnt vmcnt(8)
buffer_store_dword v56, v81, s[36:39], s52 offen sc1
mav 4,148,24
R 44,23552
mav 0,152,28
R 16,24576
mav 4,156,28
s_waitcnt lgkmcnt(3)
mav 0,160,32
R 20,25600
mav 4,164,32
mav 0,168,36
R 24,26624
mav 4,172,36
s_waitcnt lgkmcnt(3)
mav 0,176,40
R 28,27648
mav 4,180,40
mav 0,184,44
R 32,28672
mav 4,188,44
s_waitcnt lgkmcnt(3)
mav 0,192,16
R 36,29696
mav 4,196,16
mav 0,200,20
R 40,30720
mav 4,204,20
s_waitcnt lgkmcnt(3)
mav 0,208,24
R 44,31744
mav 4,212,24
mav 0,216,28
mav 4,220,28
s_waitcnt lgkmcnt(2)
mav 0,224,32
mav 4,228,32
mav 0,232,36
mav 4,236,36
s_waitcnt lgkmcnt(0)
mav 0,240,40
mav 4,244,40
mav 0,248,44
mav 4,252,44
s_nop 6
v_cvt_pk_bf16_f32 v52, v0, v1
v_cvt_pk_bf16_f32 v53, v2, v3
v_cvt_pk_bf16_f32 v54, v4, v5
v_cvt_pk_bf16_f32 v55, v6, v7
buffer_store_dwordx4 v[52:55], v83, s[36:39], s49 offen sc1
v_mul_f32 v59, v0, v0
v_add_f32 v58, v1, v0
v_fmac_f32 v59, v1, v1
v_add_f32 v58, v2, v58
v_fmac_f32 v59, v2, v2
v_add_f32 v58, v3, v58
v_fmac_f32 v59, v3, v3
v_add_f32 v58, v4, v58
v_fmac_f32 v59, v4, v4
v_add_f32 v58, v5, v58
v_fmac_f32 v59, v5, v5
v_add_f32 v58, v6, v58
v_fmac_f32 v59, v6, v6
v_add_f32 v58, v7, v58
v_fmac_f32 v59, v7, v7
s_nop 1
v_permlane32_swap_b32 v58, v59
v_add_f32 v58, v58, v59
ds_write_b32 v89, v58 offset:0
s_waitcnt vmcnt(2) lgkmcnt(0)
s_add_u32 s49, s49, 313344
s_barrier
R 16,32768
R 20,33792
R 24,34816
R 28,35840
R 32,36864
s_waitcnt lgkmcnt(3)
mav 8,0,16
R 36,37888
mav 12,4,16
mav 8,8,20
R 40,38912
mav 12,12,20
s_waitcnt lgkmcnt(3)
mav 8,16,24
R 44,39936
mav 12,20,24
mav 8,24,28
R 16,40960
mav 12,28,28
s_waitcnt lgkmcnt(3)
mav 8,32,32
R 20,41984
mav 12,36,32
mav 8,40,36
R 24,43008
mav 12,44,36
s_waitcnt lgkmcnt(3)
mav 8,48,40
R 28,44032
v_add_u32 v56, 1, v56
mav 12,52,40
mav 8,56,44
R 32,45056
mav 12,60,44
s_waitcnt lgkmcnt(3)
mav 8,64,16
R 36,46080
mav 12,68,16
mav 8,72,20
R 40,47104
mav 12,76,20
s_waitcnt lgkmcnt(3)
mav 8,80,24
R 44,48128
mav 12,84,24
mav 8,88,28
R 16,49152
mav 12,92,28
s_waitcnt lgkmcnt(3)
mav 8,96,32
R 20,50176
mav 12,100,32
mav 8,104,36
R 24,51200
mav 12,108,36
s_waitcnt lgkmcnt(3)
mav 8,112,40
R 28,52224
mav 12,116,40
mav 8,120,44
R 32,53248
mav 12,124,44
s_waitcnt lgkmcnt(3)
mav 8,128,16
R 36,54272
mav 12,132,16
mav 8,136,20
R 40,55296
mav 12,140,20
s_waitcnt lgkmcnt(3)
mav 8,144,24
s_waitcnt vmcnt(0)
buffer_store_dword v56, v81, s[36:39], s52 offen sc1
mav 12,148,24
R 44,56320
mav 8,152,28
R 16,57344
mav 12,156,28
s_waitcnt lgkmcnt(3)
mav 8,160,32
R 20,58368
mav 12,164,32
)"
R"(
mav 8,168,36
R 24,59392
mav 12,172,36
s_waitcnt lgkmcnt(3)
mav 8,176,40
R 28,60416
mav 12,180,40
mav 8,184,44
R 32,61440
mav 12,188,44
s_waitcnt lgkmcnt(3)
mav 8,192,16
R 36,62464
mav 12,196,16
mav 8,200,20
R 40,63488
mav 12,204,20
s_waitcnt lgkmcnt(3)
mav 8,208,24
R 44,64512
mav 12,212,24
mav 8,216,28
mav 12,220,28
s_waitcnt lgkmcnt(2)
mav 8,224,32
mav 12,228,32
mav 8,232,36
mav 12,236,36
s_waitcnt lgkmcnt(0)
mav 8,240,40
mav 12,244,40
mav 8,248,44
mav 12,252,44
s_nop 6
v_cvt_pk_bf16_f32 v52, v8, v9
v_cvt_pk_bf16_f32 v53, v10, v11
v_cvt_pk_bf16_f32 v54, v12, v13
v_cvt_pk_bf16_f32 v55, v14, v15
buffer_store_dwordx4 v[52:55], v83, s[36:39], s49 offen sc1
v_mul_f32 v59, v8, v8
v_add_f32 v58, v9, v8
v_fmac_f32 v59, v9, v9
v_add_f32 v58, v10, v58
v_fmac_f32 v59, v10, v10
v_add_f32 v58, v11, v58
v_fmac_f32 v59, v11, v11
v_add_f32 v58, v12, v58
v_fmac_f32 v59, v12, v12
v_add_f32 v58, v13, v58
v_fmac_f32 v59, v13, v13
v_add_f32 v58, v14, v58
v_fmac_f32 v59, v14, v14
v_add_f32 v58, v15, v58
v_fmac_f32 v59, v15, v15
s_nop 1
v_permlane32_swap_b32 v58, v59
v_add_f32 v58, v58, v59
ds_write_b32 v89, v58 offset:1024
s_waitcnt lgkmcnt(0)
s_add_u32 s49, s49, 313344
s_barrier
v_add_u32 v56, 37, v56
s_waitcnt vmcnt(0)
buffer_store_dword v56, v81, s[36:39], s52 offen sc1
s_endpgm
LvO3:
LsO9:
s_cbranch_execz LwO
s_add_u32 s49, s49, 313344
s_barrier
buffer_load_dwordx4 v[48:51], v88, s[44:47], s54 offen sc1
s_add_u32 m0, s50, 0
s_nop 0
D 84,0
D 84,1024
D 84,2048
D 84,3072
s_add_u32 m0, s50, 4096
s_nop 0
D 85,0
D 85,1024
D 85,2048
D 85,3072
s_waitcnt vmcnt(8)
v_lshlrev_b32 v0, 16, v48
v_and_b32 v1, 0xffff0000, v48
v_lshlrev_b32 v2, 16, v49
v_and_b32 v3, 0xffff0000, v49
v_lshlrev_b32 v4, 16, v50
v_and_b32 v5, 0xffff0000, v50
v_lshlrev_b32 v6, 16, v51
v_and_b32 v7, 0xffff0000, v51
s_sub_u32 s54, s54, 0x8000
buffer_load_dwordx4 v[48:51], v88, s[44:47], s54 offen sc1
s_waitcnt vmcnt(1)
s_add_u32 s49, s49, 313344
s_barrier
s_add_u32 m0, s50, 32768
R 16,0
D 84,0
R 20,1024
R 24,2048
D 84,1024
R 28,3072
R 32,4096
D 84,2048
s_waitcnt lgkmcnt(3)
mav 0,0,16
R 36,5120
mav 4,4,16
mav 0,8,20
R 40,6144
mav 4,12,20
s_waitcnt lgkmcnt(3)
mav 0,16,24
R 44,7168
mav 4,20,24
D 84,3072
mav 0,24,28
s_add_u32 m0, s50, 36864
R 16,8192
mav 4,28,28
s_waitcnt lgkmcnt(3)
mav 0,32,32
R 20,9216
mav 4,36,32
D 85,0
mav 0,40,36
R 24,10240
mav 4,44,36
s_waitcnt lgkmcnt(3)
mav 0,48,40
R 28,11264
mav 4,52,40
D 85,1024
mav 0,56,44
R 32,12288
mav 4,60,44
s_waitcnt lgkmcnt(3)
mav 0,64,16
R 36,13312
mav 4,68,16
D 85,2048
mav 0,72,20
R 40,14336
mav 4,76,20
s_waitcnt lgkmcnt(3)
mav 0,80,24
R 44,15360
mav 4,84,24
D 85,3072
mav 0,88,28
R 16,16384
mav 4,92,28
s_waitcnt lgkmcnt(3)
mav 0,96,32
R 20,17408
mav 4,100,32
mav 0,104,36
R 24,18432
mav 4,108,36
s_waitcnt lgkmcnt(3)
mav 0,112,40
R 28,19456
mav 4,116,40
mav 0,120,44
s_waitcnt vmcnt(8)
R 32,20480
v_lshlrev_b32 v8, 16, v48
mav 4,124,44
s_waitcnt lgkmcnt(3)
v_and_b32 v9, 0xffff0000, v48
v_lshlrev_b32 v10, 16, v49
mav 0,128,16
R 36,21504
v_and_b32 v11, 0xffff0000, v49
mav 4,132,16
v_lshlrev_b32 v12, 16, v50
v_and_b32 v13, 0xffff0000, v50
mav 0,136,20
R 40,22528
v_lshlrev_b32 v14, 16, v51
mav 4,140,20
s_waitcnt lgkmcnt(3)
v_and_b32 v15, 0xffff0000, v51
mav 0,144,24
R 44,23552
mav 4,148,24
s_sub_u32 s54, s54, 0x8000
mav 0,152,28
R 16,24576
mav 4,156,28
s_waitcnt lgkmcnt(3)
s_cmp_lt_u32 s54, s56
s_cselect_b64 s[44:45], s[40:41], s[44:45]
mav 0,160,32
R 20,25600
mav 4,164,32
buffer_load_dwordx4 v[48:51], v88, s[44:47], s54 offen sc1
mav 0,168,36
R 24,26624
mav 4,172,36
s_waitcnt lgkmcnt(3)
mav 0,176,40
R 28,27648
mav 4,180,40
mav 0,184,44
R 32,28672
mav 4,188,44
s_waitcnt lgkmcnt(3)
mav 0,192,16
R 36,29696
mav 4,196,16
mav 0,200,20
R 40,30720
mav 4,204,20
s_waitcnt lgkmcnt(3)
mav 0,208,24
R 44,31744
mav 4,212,24
mav 0,216,28
mav 4,220,28
s_waitcnt lgkmcnt(2)
mav 0,224,32
mav 4,228,32
mav 0,232,36
mav 4,236,36
s_waitcnt lgkmcnt(0)
mav 0,240,40
mav 4,244,40
mav 0,248,44
mav 4,252,44
s_nop 6
v_cvt_pk_bf16_f32 v52, v0, v1
v_cvt_pk_bf16_f32 v53, v2, v3
v_cvt_pk_bf16_f32 v54, v4, v5
v_cvt_pk_bf16_f32 v55, v6, v7
buffer_store_dwordx4 v[52:55], v83, s[36:39], s49 offen sc1
v_mul_f32 v59, v0, v0
v_add_f32 v58, v1, v0
v_fmac_f32 v59, v1, v1
v_add_f32 v58, v2, v58
v_fmac_f32 v59, v2, v2
v_add_f32 v58, v3, v58
v_fmac_f32 v59, v3, v3
v_add_f32 v58, v4, v58
v_fmac_f32 v59, v4, v4
v_add_f32 v58, v5, v58
v_fmac_f32 v59, v5, v5
v_add_f32 v58, v6, v58
v_fmac_f32 v59, v6, v6
v_add_f32 v58, v7, v58
v_fmac_f32 v59, v7, v7
s_nop 1
v_permlane32_swap_b32 v58, v59
v_add_f32 v58, v58, v59
ds_write_b32 v89, v58 offset:0
s_xor_b32 s50, s50, 0x10000
s_waitcnt vmcnt(2) lgkmcnt(0)
s_add_u32 s49, s49, 313344
s_barrier
s_add_u32 m0, s50, 0
R 16,32768
D 84,0
R 20,33792
R 24,34816
D 84,1024
R 28,35840
R 32,36864
D 84,2048
s_waitcnt lgkmcnt(3)
mav 8,0,16
R 36,37888
mav 12,4,16
mav 8,8,20
R 40,38912
mav 12,12,20
s_waitcnt lgkmcnt(3)
mav 8,16,24
R 44,39936
mav 12,20,24
D 84,3072
mav 8,24,28
s_add_u32 m0, s50, 4096
R 16,40960
mav 12,28,28
s_waitcnt lgkmcnt(3)
mav 8,32,32
R 20,41984
mav 12,36,32
D 85,0
mav 8,40,36
R 24,43008
mav 12,44,36
s_waitcnt lgkmcnt(3)
mav 8,48,40
R 28,44032
v_add_u32 v56, 1, v56
mav 12,52,40
D 85,1024
mav 8,56,44
R 32,45056
mav 12,60,44
s_waitcnt lgkmcnt(3)
mav 8,64,16
R 36,46080
mav 12,68,16
D 85,2048
mav 8,72,20
R 40,47104
mav 12,76,20
s_waitcnt lgkmcnt(3)
mav 8,80,24
R 44,48128
mav 12,84,24
D 85,3072
mav 8,88,28
R 16,49152
mav 12,92,28
s_waitcnt lgkmcnt(3)
mav 8,96,32
R 20,50176
mav 12,100,32
mav 8,104,36
R 24,51200
mav 12,108,36
s_waitcnt lgkmcnt(3)
mav 8,112,40
R 28,52224
mav 12,116,40
LsO10:
s_cbranch_execz LwO
mav 8,120,44
s_waitcnt vmcnt(9)
R 32,53248
v_lshlrev_b32 v0, 16, v48
mav 12,124,44
s_waitcnt lgkmcnt(3)
v_and_b32 v1, 0xffff0000, v48
v_lshlrev_b32 v2, 16, v49
mav 8,128,16
R 36,54272
v_and_b32 v3, 0xffff0000, v49
mav 12,132,16
v_lshlrev_b32 v4, 16, v50
v_and_b32 v5, 0xffff0000, v50
mav 8,136,20
R 40,55296
v_lshlrev_b32 v6, 16, v51
mav 12,140,20
s_waitcnt lgkmcnt(3)
v_and_b32 v7, 0xffff0000, v51
mav 8,144,24
s_waitcnt vmcnt(8)
R 44,56320
mav 12,148,24
buffer_store_dword v56, v81, s[36:39], s52 offen sc1
mav 8,152,28
s_sub_u32 s54, s54, 0x8000
R 16,57344
mav 12,156,28
s_waitcnt lgkmcnt(3)
mav 8,160,32
R 20,58368
mav 12,164,32
buffer_load_dwordx4 v[48:51], v88, s[44:47], s54 offen sc1
mav 8,168,36
R 24,59392
mav 12,172,36
s_waitcnt lgkmcnt(3)
mav 8,176,40
R 28,60416
mav 12,180,40
mav 8,184,44
R 32,61440
mav 12,188,44
s_waitcnt lgkmcnt(3)
mav 8,192,16
R 36,62464
mav 12,196,16
mav 8,200,20
R 40,63488
mav 12,204,20
s_waitcnt lgkmcnt(3)
mav 8,208,24
R 44,64512
mav 12,212,24
mav 8,216,28
mav 12,220,28
s_waitcnt lgkmcnt(2)
mav 8,224,32
mav 12,228,32
mav 8,232,36
s_mov_b32 s60, -30
.p2align 6
LO3_1:
mav 12,236,36
s_waitcnt lgkmcnt(0)
mav 8,240,40
mav 12,244,40
mav 8,248,44
mav 12,252,44
s_nop 6
v_cvt_pk_bf16_f32 v52, v8, v9
v_cvt_pk_bf16_f32 v53, v10, v11
v_cvt_pk_bf16_f32 v54, v12, v13
v_cvt_pk_bf16_f32 v55, v14, v15
buffer_store_dwordx4 v[52:55], v83, s[36:39], s49 offen sc1
v_mul_f32 v59, v8, v8
v_add_f32 v58, v9, v8
v_fmac_f32 v59, v9, v9
v_add_f32 v58, v10, v58
v_fmac_f32 v59, v10, v10
v_add_f32 v58, v11, v58
v_fmac_f32 v59, v11, v11
v_add_f32 v58, v12, v58
v_fmac_f32 v59, v12, v12
v_add_f32 v58, v13, v58
v_fmac_f32 v59, v13, v13
v_add_f32 v58, v14, v58
v_fmac_f32 v59, v14, v14
v_add_f32 v58, v15, v58
v_fmac_f32 v59, v15, v15
s_nop 1
v_permlane32_swap_b32 v58, v59
v_add_f32 v58, v58, v59
ds_write_b32 v89, v58 offset:1024
v_xor_b32 v80, 0x10000, v80
s_waitcnt vmcnt(3) lgkmcnt(0)
s_add_u32 s49, s49, 313344
s_barrier
s_add_u32 m0, s50, 32768
R 16,0
D 84,0
R 20,1024
R 24,2048
D 84,1024
R 28,3072
R 32,4096
D 84,2048
s_waitcnt lgkmcnt(3)
mav 0,0,16
R 36,5120
mav 4,4,16
mav 0,8,20
R 40,6144
mav 4,12,20
s_waitcnt lgkmcnt(3)
mav 0,16,24
R 44,7168
mav 4,20,24
D 84,3072
mav 0,24,28
s_add_u32 m0, s50, 36864
R 16,8192
mav 4,28,28
s_waitcnt lgkmcnt(3)
mav 0,32,32
R 20,9216
mav 4,36,32
D 85,0
mav 0,40,36
R 24,10240
mav 4,44,36
s_waitcnt lgkmcnt(3)
mav 0,48,40
R 28,11264
v_add_u32 v56, 1, v56
mav 4,52,40
D 85,1024
mav 0,56,44
R 32,12288
mav 4,60,44
s_waitcnt lgkmcnt(3)
mav 0,64,16
R 36,13312
mav 4,68,16
D 85,2048
mav 0,72,20
R 40,14336
mav 4,76,20
s_waitcnt lgkmcnt(3)
mav 0,80,24
R 44,15360
mav 4,84,24
D 85,3072
mav 0,88,28
R 16,16384
mav 4,92,28
s_waitcnt lgkmcnt(3)
mav 0,96,32
R 20,17408
mav 4,100,32
mav 0,104,36
R 24,18432
mav 4,108,36
s_waitcnt lgkmcnt(3)
mav 0,112,40
R 28,19456
mav 4,116,40
mav 0,120,44
s_waitcnt vmcnt(9)
R 32,20480
v_lshlrev_b32 v8, 16, v48
mav 4,124,44
s_waitcnt lgkmcnt(3)
v_and_b32 v9, 0xffff0000, v48
v_lshlrev_b32 v10, 16, v49
mav 0,128,16
R 36,21504
v_and_b32 v11, 0xffff0000, v49
mav 4,132,16
v_lshlrev_b32 v12, 16, v50
v_and_b32 v13, 0xffff0000, v50
mav 0,136,20
R 40,22528
v_lshlrev_b32 v14, 16, v51
mav 4,140,20
s_waitcnt lgkmcnt(3)
v_and_b32 v15, 0xffff0000, v51
mav 0,144,24
s_waitcnt vmcnt(8)
R 44,23552
mav 4,148,24
buffer_store_dword v56, v81, s[36:39], s52 offen sc1
mav 0,152,28
s_sub_u32 s54, s54, 0x8000
R 16,24576
mav 4,156,28
s_waitcnt lgkmcnt(3)
s_cmp_lt_u32 s54, s56
s_cselect_b64 s[44:45], s[40:41], s[44:45]
mav 0,160,32
R 20,25600
mav 4,164,32
buffer_load_dwordx4 v[48:51], v88, s[44:47], s54 offen sc1
mav 0,168,36
R 24,26624
mav 4,172,36
s_waitcnt lgkmcnt(3)
mav 0,176,40
R 28,27648
mav 4,180,40
mav 0,184,44
R 32,28672
mav 4,188,44
s_waitcnt lgkmcnt(3)
mav 0,192,16
R 36,29696
mav 4,196,16
mav 0,200,20
R 40,30720
mav 4,204,20
s_waitcnt lgkmcnt(3)
mav 0,208,24
R 44,31744
mav 4,212,24
mav 0,216,28
mav 4,220,28
s_waitcnt lgkmcnt(2)
mav 0,224,32
mav 4,228,32
mav 0,232,36
mav 4,236,36
s_waitcnt lgkmcnt(0)
mav 0,240,40
mav 4,244,40
mav 0,248,44
mav 4,252,44
s_nop 6
v_cvt_pk_bf16_f32 v52, v0, v1
v_cvt_pk_bf16_f32 v53, v2, v3
v_cvt_pk_bf16_f32 v54, v4, v5
v_cvt_pk_bf16_f32 v55, v6, v7
buffer_store_dwordx4 v[52:55], v83, s[36:39], s49 offen sc1
v_mul_f32 v59, v0, v0
v_add_f32 v58, v1, v0
v_fmac_f32 v59, v1, v1
v_add_f32 v58, v2, v58
v_fmac_f32 v59, v2, v2
v_add_f32 v58, v3, v58
v_fmac_f32 v59, v3, v3
v_add_f32 v58, v4, v58
v_fmac_f32 v59, v4, v4
v_add_f32 v58, v5, v58
v_fmac_f32 v59, v5, v5
v_add_f32 v58, v6, v58
v_fmac_f32 v59, v6, v6
v_add_f32 v58, v7, v58
v_fmac_f32 v59, v7, v7
s_nop 1
v_permlane32_swap_b32 v58, v59
v_add_f32 v58, v58, v59
ds_write_b32 v89, v58 offset:0
s_xor_b32 s50, s50, 0x10000
s_waitcnt vmcnt(3) lgkmcnt(0)
s_add_u32 s49, s49, 313344
s_barrier
s_add_u32 m0, s50, 0
R 16,32768
D 84,0
R 20,33792
R 24,34816
D 84,1024
R 28,35840
R 32,36864
D 84,2048
s_waitcnt lgkmcnt(3)
mav 8,0,16
R 36,37888
mav 12,4,16
mav 8,8,20
R 40,38912
mav 12,12,20
s_waitcnt lgkmcnt(3)
mav 8,16,24
R 44,39936
mav 12,20,24
D 84,3072
mav 8,24,28
s_add_u32 m0, s50, 4096
R 16,40960
mav 12,28,28
s_waitcnt lgkmcnt(3)
mav 8,32,32
R 20,41984
mav 12,36,32
D 85,0
mav 8,40,36
R 24,43008
mav 12,44,36
s_waitcnt lgkmcnt(3)
mav 8,48,40
R 28,44032
v_add_u32 v56, 1, v56
mav 12,52,40
D 85,1024
mav 8,56,44
R 32,45056
mav 12,60,44
s_waitcnt lgkmcnt(3)
mav 8,64,16
R 36,46080
mav 12,68,16
D 85,2048
mav 8,72,20
R 40,47104
mav 12,76,20
s_waitcnt lgkmcnt(3)
mav 8,80,24
R 44,48128
mav 12,84,24
D 85,3072
mav 8,88,28
R 16,49152
mav 12,92,28
s_waitcnt lgkmcnt(3)
mav 8,96,32
R 20,50176
mav 12,100,32
mav 8,104,36
R 24,51200
mav 12,108,36
s_waitcnt lgkmcnt(3)
mav 8,112,40
R 28,52224
mav 12,116,40
mav 8,120,44
s_waitcnt vmcnt(9)
R 32,53248
v_lshlrev_b32 v0, 16, v48
mav 12,124,44
s_waitcnt lgkmcnt(3)
v_and_b32 v1, 0xffff0000, v48
v_lshlrev_b32 v2, 16, v49
mav 8,128,16
R 36,54272
v_and_b32 v3, 0xffff0000, v49
mav 12,132,16
v_lshlrev_b32 v4, 16, v50
v_and_b32 v5, 0xffff0000, v50
mav 8,136,20
R 40,55296
v_lshlrev_b32 v6, 16, v51
mav 12,140,20
s_waitcnt lgkmcnt(3)
v_and_b32 v7, 0xffff0000, v51
mav 8,144,24
s_waitcnt vmcnt(8)
R 44,56320
mav 12,148,24
buffer_store_dword v56, v81, s[36:39], s52 offen sc1
mav 8,152,28
s_sub_u32 s54, s54, 0x8000
R 16,57344
mav 12,156,28
s_waitcnt lgkmcnt(3)
mav 8,160,32
R 20,58368
mav 12,164,32
buffer_load_dwordx4 v[48:51], v88, s[44:47], s54 offen sc1
mav 8,168,36
R 24,59392
mav 12,172,36
s_waitcnt lgkmcnt(3)
mav 8,176,40
R 28,60416
mav 12,180,40
mav 8,184,44
R 32,61440
mav 12,188,44
s_waitcnt lgkmcnt(3)
mav 8,192,16
R 36,62464
mav 12,196,16
mav 8,200,20
R 40,63488
mav 12,204,20
s_waitcnt lgkmcnt(3)
mav 8,208,24
R 44,64512
mav 12,212,24
mav 8,216,28
mav 12,220,28
s_waitcnt lgkmcnt(2)
mav 8,224,32
mav 12,228,32
s_add_u32 s60, s60, 1
mav 8,232,36
s_cbranch_scc0 LO3_1
mav 12,236,36
s_waitcnt lgkmcnt(0)
mav 8,240,40
mav 12,244,40
mav 8,248,44
mav 12,252,44
s_nop 6
v_cvt_pk_bf16_f32 v52, v8, v9
v_cvt_pk_bf16_f32 v53, v10, v11
v_cvt_pk_bf16_f32 v54, v12, v13
v_cvt_pk_bf16_f32 v55, v14, v15
buffer_store_dwordx4 v[52:55], v83, s[36:39], s49 offen sc1
v_mul_f32 v59, v8, v8
v_add_f32 v58, v9, v8
v_fmac_f32 v59, v9, v9
v_add_f32 v58, v10, v58
v_fmac_f32 v59, v10, v10
v_add_f32 v58, v11, v58
v_fmac_f32 v59, v11, v11
v_add_f32 v58, v12, v58
v_fmac_f32 v59, v12, v12
v_add_f32 v58, v13, v58
v_fmac_f32 v59, v13, v13
v_add_f32 v58, v14, v58
v_fmac_f32 v59, v14, v14
v_add_f32 v58, v15, v58
v_fmac_f32 v59, v15, v15
s_nop 1
v_permlane32_swap_b32 v58, v59
v_add_f32 v58, v58, v59
ds_write_b32 v89, v58 offset:1024
v_xor_b32 v80, 0x10000, v80
s_waitcnt vmcnt(3) lgkmcnt(0)
s_add_u32 s49, s49, 313344
s_barrier
s_add_u32 m0, s50, 32768
R 16,0
D 84,0
R 20,1024
R 24,2048
D 84,1024
R 28,3072
R 32,4096
D 84,2048
s_waitcnt lgkmcnt(3)
mav 0,0,16
R 36,5120
mav 4,4,16
mav 0,8,20
R 40,6144
mav 4,12,20
s_waitcnt lgkmcnt(3)
mav 0,16,24
R 44,7168
mav 4,20,24
D 84,3072
mav 0,24,28
s_add_u32 m0, s50, 36864
R 16,8192
mav 4,28,28
s_waitcnt lgkmcnt(3)
mav 0,32,32
R 20,9216
mav 4,36,32
D 85,0
mav 0,40,36
R 24,10240
mav 4,44,36
s_waitcnt lgkmcnt(3)
mav 0,48,40
R 28,11264
v_add_u32 v56, 1, v56
mav 4,52,40
D 85,1024
mav 0,56,44
R 32,12288
mav 4,60,44
s_waitcnt lgkmcnt(3)
mav 0,64,16
R 36,13312
mav 4,68,16
D 85,2048
mav 0,72,20
R 40,14336
mav 4,76,20
s_waitcnt lgkmcnt(3)
mav 0,80,24
R 44,15360
mav 4,84,24
D 85,3072
mav 0,88,28
R 16,16384
mav 4,92,28
s_waitcnt lgkmcnt(3)
mav 0,96,32
R 20,17408
mav 4,100,32
mav 0,104,36
R 24,18432
mav 4,108,36
s_waitcnt lgkmcnt(3)
mav 0,112,40
R 28,19456
mav 4,116,40
mav 0,120,44
s_waitcnt vmcnt(9)
R 32,20480
v_lshlrev_b32 v8, 16, v48
mav 4,124,44
s_waitcnt lgkmcnt(3)
v_and_b32 v9, 0xffff0000, v48
v_lshlrev_b32 v10, 16, v49
mav 0,128,16
R 36,21504
v_and_b32 v11, 0xffff0000, v49
mav 4,132,16
v_lshlrev_b32 v12, 16, v50
v_and_b32 v13, 0xffff0000, v50
mav 0,136,20
R 40,22528
v_lshlrev_b32 v14, 16, v51
mav 4,140,20
s_waitcnt lgkmcnt(3)
v_and_b32 v15, 0xffff0000, v51
mav 0,144,24
s_waitcnt vmcnt(8)
R 44,23552
mav 4,148,24
buffer_store_dword v56, v81, s[36:39], s52 offen sc1
mav 0,152,28
R 16,24576
mav 4,156,28
s_waitcnt lgkmcnt(3)
mav 0,160,32
R 20,25600
mav 4,164,32
mav 0,168,36
R 24,26624
mav 4,172,36
s_waitcnt lgkmcnt(3)
mav 0,176,40
R 28,27648
mav 4,180,40
mav 0,184,44
R 32,28672
mav 4,188,44
s_waitcnt lgkmcnt(3)
mav 0,192,16
R 36,29696
mav 4,196,16
mav 0,200,20
R 40,30720
mav 4,204,20
s_waitcnt lgkmcnt(3)
mav 0,208,24
R 44,31744
mav 4,212,24
mav 0,216,28
mav 4,220,28
s_waitcnt lgkmcnt(2)
mav 0,224,32
mav 4,228,32
mav 0,232,36
mav 4,236,36
s_waitcnt lgkmcnt(0)
mav 0,240,40
mav 4,244,40
mav 0,248,44
mav 4,252,44
s_nop 6
v_cvt_pk_bf16_f32 v52, v0, v1
v_cvt_pk_bf16_f32 v53, v2, v3
v_cvt_pk_bf16_f32 v54, v4, v5
v_cvt_pk_bf16_f32 v55, v6, v7
buffer_store_dwordx4 v[52:55], v83, s[36:39], s49 offen sc1
v_mul_f32 v59, v0, v0
v_add_f32 v58, v1, v0
v_fmac_f32 v59, v1, v1
v_add_f32 v58, v2, v58
v_fmac_f32 v59, v2, v2
v_add_f32 v58, v3, v58
v_fmac_f32 v59, v3, v3
v_add_f32 v58, v4, v58
v_fmac_f32 v59, v4, v4
v_add_f32 v58, v5, v58
v_fmac_f32 v59, v5, v5
v_add_f32 v58, v6, v58
v_fmac_f32 v59, v6, v6
v_add_f32 v58, v7, v58
v_fmac_f32 v59, v7, v7
s_nop 1
v_permlane32_swap_b32 v58, v59
v_add_f32 v58, v58, v59
ds_write_b32 v89, v58 offset:0
s_waitcnt vmcnt(2) lgkmcnt(0)
s_add_u32 s49, s49, 313344
s_barrier
R 16,32768
R 20,33792
R 24,34816
R 28,35840
R 32,36864
LsO11:
s_cbranch_execz LwO
s_waitcnt lgkmcnt(3)
mav 8,0,16
R 36,37888
mav 12,4,16
mav 8,8,20
R 40,38912
mav 12,12,20
s_waitcnt lgkmcnt(3)
mav 8,16,24
R 44,39936
mav 12,20,24
mav 8,24,28
R 16,40960
mav 12,28,28
s_waitcnt lgkmcnt(3)
mav 8,32,32
R 20,41984
mav 12,36,32
mav 8,40,36
R 24,43008
mav 12,44,36
s_waitcnt lgkmcnt(3)
mav 8,48,40
R 28,44032
v_add_u32 v56, 1, v56
mav 12,52,40
mav 8,56,44
R 32,45056
mav 12,60,44
s_waitcnt lgkmcnt(3)
mav 8,64,16
R 36,46080
mav 12,68,16
mav 8,72,20
R 40,47104
mav 12,76,20
s_waitcnt lgkmcnt(3)
mav 8,80,24
R 44,48128
mav 12,84,24
mav 8,88,28
R 16,49152
mav 12,92,28
s_waitcnt lgkmcnt(3)
mav 8,96,32
R 20,50176
mav 12,100,32
mav 8,104,36
R 24,51200
mav 12,108,36
s_waitcnt lgkmcnt(3)
mav 8,112,40
R 28,52224
mav 12,116,40
mav 8,120,44
R 32,53248
mav 12,124,44
s_waitcnt lgkmcnt(3)
mav 8,128,16
R 36,54272
mav 12,132,16
mav 8,136,20
R 40,55296
mav 12,140,20
s_waitcnt lgkmcnt(3)
mav 8,144,24
s_waitcnt vmcnt(0)
R 44,56320
mav 12,148,24
buffer_store_dword v56, v81, s[36:39], s52 offen sc1
mav 8,152,28
R 16,57344
mav 12,156,28
s_waitcnt lgkmcnt(3)
mav 8,160,32
R 20,58368
mav 12,164,32
mav 8,168,36
R 24,59392
mav 12,172,36
s_waitcnt lgkmcnt(3)
mav 8,176,40
R 28,60416
mav 12,180,40
mav 8,184,44
R 32,61440
mav 12,188,44
s_waitcnt lgkmcnt(3)
mav 8,192,16
R 36,62464
mav 12,196,16
mav 8,200,20
R 40,63488
mav 12,204,20
s_waitcnt lgkmcnt(3)
mav 8,208,24
R 44,64512
mav 12,212,24
mav 8,216,28
mav 12,220,28
s_waitcnt lgkmcnt(2)
mav 8,224,32
mav 12,228,32
mav 8,232,36
mav 12,236,36
s_waitcnt lgkmcnt(0)
mav 8,240,40
mav 12,244,40
mav 8,248,44
mav 12,252,44
s_nop 6
v_cvt_pk_bf16_f32 v52, v8, v9
v_cvt_pk_bf16_f32 v53, v10, v11
v_cvt_pk_bf16_f32 v54, v12, v13
v_cvt_pk_bf16_f32 v55, v14, v15
buffer_store_dwordx4 v[52:55], v83, s[36:39], s49 offen sc1
v_mul_f32 v59, v8, v8
v_add_f32 v58, v9, v8
v_fmac_f32 v59, v9, v9
v_add_f32 v58, v10, v58
v_fmac_f32 v59, v10, v10
v_add_f32 v58, v11, v58
v_fmac_f32 v59, v11, v11
v_add_f32 v58, v12, v58
v_fmac_f32 v59, v12, v12
v_add_f32 v58, v13, v58
v_fmac_f32 v59, v13, v13
v_add_f32 v58, v14, v58
v_fmac_f32 v59, v14, v14
v_add_f32 v58, v15, v58
v_fmac_f32 v59, v15, v15
s_nop 1
v_permlane32_swap_b32 v58, v59
v_add_f32 v58, v58, v59
ds_write_b32 v89, v58 offset:1024
s_waitcnt lgkmcnt(0)
s_add_u32 s49, s49, 313344
s_barrier
v_add_u32 v56, 37, v56
s_waitcnt vmcnt(0)
buffer_store_dword v56, v81, s[36:39], s52 offen sc1
s_endpgm
Lr3:
s_cmp_eq_u32 s60, 3
s_cbranch_scc0 Lr4
s_waitcnt vmcnt(0)
s_barrier
v_accvgpr_read_b32 v77, a255
v_mov_b32 v76, 0x100
v_add_u32 v77, 0x9e3779b9, v77
buffer_store_dwordx2 v[76:77], v81, s[36:39], s52 offen sc1
v_add_u32 v88, v88, v76
E 0,65536
E 1,69632
E 2,73728
s_add_u32 s51, s51, 229376
E 0,77824
E 1,81920
E 2,86016
s_add_u32 s51, s51, 229376
E 0,90112
E 1,94208
E 2,98304
s_add_u32 s51, s51, 229376
E 0,102400
E 1,106496
E 2,110592
s_add_u32 s51, s51, 229376
s_mov_b64 exec, 0
s_cmp_eq_u32 s53, 0
s_cbranch_scc1 LsF0
s_cmp_eq_u32 s53, 1024
s_cbranch_scc1 LsF1
s_cmp_eq_u32 s53, 2048
s_cbranch_scc1 LsF2
s_branch LsF3
LwF:
s_mov_b64 exec, -1
v_mov_b32 v24, s64
v_mov_b32 v25, s64
v_mov_b32 v26, s64
v_mov_b32 v27, s64
s_waitcnt vmcnt(6)
s_barrier
v_add_u32 v4, 65536, v4
v_add_u32 v5, 65536, v5
v_add_u32 v6, 65536, v6
Ta 0,4,0
Ta 4,5,0
Ta 8,6,0
Ta 12,4,12288
Ta 16,5,12288
Ta 20,6,12288
E 0,114688
E 1,118784
E 2,122880
s_add_u32 s51, s51, 229376
E 0,126976
E 1,131072
E 2,135168
s_add_u32 s51, s51, 229376
E 0,139264
E 1,143360
E 2,147456
s_add_u32 s51, s51, 229376
E 0,151552
E 1,155648
E 2,159744
s_add_u32 s51, s51, 229376
s_cmp_ge_u32 s51, s57
s_cselect_b32 s60, 7340032, 0
s_sub_u32 s51, s51, s60
s_waitcnt lgkmcnt(0)
s_waitcnt vmcnt(12)
s_barrier
E 0,65536
Ta 24,4,24576
v_mfma_f32_16x16x32_bf16 v[56:59], a[0:3], v[24:27], 0
Ta 28,5,24576
E 1,69632
v_mfma_f32_16x16x32_bf16 v[60:63], a[4:7], v[24:27], 0
Ta 32,6,24576
Ta 36,4,36864
E 2,73728
s_add_u32 s51, s51, 229376
v_mfma_f32_16x16x32_bf16 v[64:67], a[8:11], v[24:27], 0
Ta 40,5,36864
E 0,77824
Ta 44,6,36864
mav 56,12,24
E 1,81920
mav 60,16,24
E 2,86016
s_add_u32 s51, s51, 229376
mav 64,20,24
s_waitcnt lgkmcnt(0)
s_waitcnt vmcnt(12)
s_barrier
v_add_u32 v4, 49152, v4
v_add_u32 v5, 49152, v5
v_add_u32 v6, 49152, v6
E 0,90112
Ta 48,4,0
mav 56,24,24
Ta 52,5,0
E 1,94208
mav 60,28,24
Ta 56,6,0
Ta 60,4,12288
E 2,98304
s_add_u32 s51, s51, 229376
mav 64,32,24
Ta 64,5,12288
E 0,102400
Ta 68,6,12288
mav 56,36,24
E 1,106496
mav 60,40,24
E 2,110592
s_add_u32 s51, s51, 229376
mav 64,44,24
s_waitcnt lgkmcnt(0)
s_waitcnt vmcnt(12)
s_barrier
E 0,114688
Ta 72,4,24576
mav 56,48,24
Ta 76,5,24576
E 1,118784
mav 60,52,24
Ta 80,6,24576
Ta 84,4,36864
E 2,122880
s_add_u32 s51, s51, 229376
mav 64,56,24
Ta 88,5,36864
E 0,126976
Ta 92,6,36864
mav 56,60,24
E 1,131072
mav 60,64,24
E 2,135168
s_add_u32 s51, s51, 229376
mav 64,68,24
s_waitcnt lgkmcnt(0)
s_waitcnt vmcnt(12)
s_barrier
v_add_u32 v4, -49152, v4
v_add_u32 v5, -49152, v5
v_add_u32 v6, -49152, v6
E 0,139264
Ta 96,4,0
mav 56,72,24
Ta 100,5,0
E 1,143360
mav 60,76,24
Ta 104,6,0
Ta 108,4,12288
E 2,147456
s_add_u32 s51, s51, 229376
mav 64,80,24
Ta 112,5,12288
E 0,151552
Ta 116,6,12288
mav 56,84,24
E 1,155648
mav 60,88,24
E 2,159744
s_add_u32 s51, s51, 229376
s_cmp_ge_u32 s51, s57
s_cselect_b32 s60, 7340032, 0
s_sub_u32 s51, s51, s60
mav 64,92,24
s_waitcnt lgkmcnt(0)
s_waitcnt vmcnt(12)
s_barrier
E 0,65536
Ta 120,4,24576
mav 56,96,24
Ta 124,5,24576
E 1,69632
mav 60,100,24
Ta 128,6,24576
Ta 132,4,36864
E 2,73728
s_add_u32 s51, s51, 229376
mav 64,104,24
Ta 136,5,36864
E 0,77824
Ta 140,6,36864
mav 56,108,24
E 1,81920
mav 60,112,24
E 2,86016
s_add_u32 s51, s51, 229376
mav 64,116,24
s_waitcnt lgkmcnt(0)
s_waitcnt vmcnt(12)
s_barrier
v_add_u32 v4, 49152, v4
v_add_u32 v5, 49152, v5
v_add_u32 v6, 49152, v6
E 0,90112
Ta 144,4,0
mav 56,120,24
Ta 148,5,0
E 1,94208
mav 60,124,24
Ta 152,6,0
Ta 156,4,12288
E 2,98304
s_add_u32 s51, s51, 229376
mav 64,128,24
Ta 160,5,12288
E 0,102400
Ta 164,6,12288
mav 56,132,24
E 1,106496
mav 60,136,24
E 2,110592
s_add_u32 s51, s51, 229376
mav 64,140,24
s_waitcnt lgkmcnt(0)
s_waitcnt vmcnt(12)
s_barrier
E 0,114688
Ta 168,4,24576
mav 56,144,24
Ta 172,5,24576
E 1,118784
mav 60,148,24
Ta 176,6,24576
Ta 180,4,36864
E 2,122880
s_add_u32 s51, s51, 229376
mav 64,152,24
Ta 184,5,36864
E 0,126976
Ta 188,6,36864
mav 56,156,24
E 1,131072
mav 60,160,24
E 2,135168
s_add_u32 s51, s51, 229376
mav 64,164,24
s_waitcnt lgkmcnt(0)
s_waitcnt vmcnt(12)
s_barrier
v_add_u32 v4, -49152, v4
v_add_u32 v5, -49152, v5
v_add_u32 v6, -49152, v6
E 0,139264
Ta 192,4,0
mav 56,168,24
Ta 196,5,0
E 1,143360
mav 60,172,24
Ta 200,6,0
Ta 204,4,12288
E 2,147456
s_add_u32 s51, s51, 229376
mav 64,176,24
LsF0:
s_cbranch_execz LsF4
Ta 208,5,12288
E 0,151552
Ta 212,6,12288
mav 56,180,24
E 1,155648
mav 60,184,24
E 2,159744
s_add_u32 s51, s51, 229376
s_cmp_ge_u32 s51, s57
s_cselect_b32 s60, 7340032, 0
s_sub_u32 s51, s51, s60
mav 64,188,24
s_waitcnt lgkmcnt(0)
s_waitcnt vmcnt(12)
s_barrier
E 0,65536
Ta 216,4,24576
mav 56,192,24
Ta 220,5,24576
E 1,69632
mav 60,196,24
Ta 224,6,24576
Ta 228,4,36864
E 2,73728
s_add_u32 s51, s51, 229376
mav 64,200,24
Ta 232,5,36864
E 0,77824
Ta 236,6,36864
mav 56,204,24
E 1,81920
mav 60,208,24
E 2,86016
s_add_u32 s51, s51, 229376
mav 64,212,24
s_waitcnt lgkmcnt(0)
s_waitcnt vmcnt(12)
s_barrier
v_add_u32 v4, 49152, v4
v_add_u32 v5, 49152, v5
v_add_u32 v6, 49152, v6
E 0,90112
Ta 240,4,0
mav 56,216,24
Ta 244,5,0
E 1,94208
mav 60,220,24
Ta 248,6,0
Ta 252,4,12288
E 2,98304
s_add_u32 s51, s51, 229376
mav 64,224,24
Tv 128,5,12288
E 0,102400
Tv 132,6,12288
mav 56,228,24
E 1,106496
mav 60,232,24
E 2,110592
s_add_u32 s51, s51, 229376
mav 64,236,24
s_waitcnt lgkmcnt(0)
s_waitcnt vmcnt(12)
s_barrier
E 0,114688
Tv 136,4,24576
mav 56,240,24
Tv 140,5,24576
E 1,118784
mav 60,244,24
Tv 144,6,24576
Tv 148,4,36864
E 2,122880
s_add_u32 s51, s51, 229376
mav 64,248,24
Tv 152,5,36864
E 0,126976
Tv 156,6,36864
mav 56,252,24
E 1,131072
mvv 60,128,24
E 2,135168
s_add_u32 s51, s51, 229376
mvv 64,132,24
s_waitcnt lgkmcnt(0)
s_waitcnt vmcnt(12)
s_barrier
v_add_u32 v4, -49152, v4
v_add_u32 v5, -49152, v5
v_add_u32 v6, -49152, v6
E 0,139264
Tv 160,4,0
mvv 56,136,24
Tv 164,5,0
E 1,143360
mvv 60,140,24
Tv 168,6,0
Tv 172,4,12288
E 2,147456
s_add_u32 s51, s51, 229376
mvv 64,144,24
Tv 176,5,12288
E 0,151552
Tv 180,6,12288
mvv 56,148,24
E 1,155648
mvv 60,152,24
E 2,159744
s_add_u32 s51, s51, 229376
s_cmp_ge_u32 s51, s57
s_cselect_b32 s60, 7340032, 0
s_sub_u32 s51, s51, s60
mvv 64,156,24
s_waitcnt lgkmcnt(0)
s_waitcnt vmcnt(12)
s_barrier
Tv 184,4,24576
mvv 56,160,24
Tv 188,5,24576
mvv 60,164,24
Tv 192,6,24576
Tv 196,4,36864
mvv 64,168,24
Tv 200,5,36864
Tv 204,6,36864
mvv 56,172,24
mvv 60,176,24
mvv 64,180,24
s_waitcnt lgkmcnt(0)
s_waitcnt vmcnt(6)
s_barrier
v_add_u32 v4, 49152, v4
v_add_u32 v5, 49152, v5
v_add_u32 v6, 49152, v6
Tv 208,4,0
mvv 56,184,24
Tv 212,5,0
mvv 60,188,24
Tv 216,6,0
Tv 220,4,12288
mvv 64,192,24
Tv 224,5,12288
Tv 228,6,12288
mvv 56,196,24
mvv 60,200,24
mvv 64,204,24
s_waitcnt lgkmcnt(0)
s_waitcnt vmcnt(0)
s_barrier
Tv 232,4,24576
mvv 56,208,24
Tv 236,5,24576
mvv 60,212,24
Tv 240,6,24576
Tv 244,4,36864
mvv 64,216,24
Tv 248,5,36864
Tv 252,6,36864
mvv 56,220,24
mvv 60,224,24
mvv 64,228,24
s_waitcnt lgkmcnt(0)
mvv 56,232,24
mvv 60,236,24
mvv 64,240,24
mvv 56,244,24
mvv 60,248,24
mvv 64,252,24
s_waitcnt vmcnt(0)
s_barrier
s_nop 7
s_and_b32 s60, s48, 3
s_cmp_eq_u32 s60, 1
s_cbranch_scc1 LvF1
s_cmp_eq_u32 s60, 2
s_cbranch_scc1 LvF2
s_cmp_eq_u32 s60, 3
s_cbranch_scc1 LvF3
LvF0:
LsF1:
s_cbranch_execz LsF5
buffer_load_dwordx2 v[106:107], v87, s[36:39], 0 offen sc1
s_waitcnt vmcnt(0)
ds_write_b32 v82, v106
v_sub_u32 v106, v106, v88
v_xor_b32 v107, v107, v77
v_cmp_lt_u64 vcc, s[62:63], v[106:107]
s_waitcnt lgkmcnt(0)
s_cbranch_vccnz CF0_1
BF0_1:
s_add_u32 s49, s49, 313344
s_barrier
s_add_u32 m0, s50, 0
s_nop 0
D 85,0
D 85,1024
D 85,2048
D 85,3072
s_add_u32 m0, s50, 4096
s_nop 0
D 86,0
D 86,1024
D 86,2048
D 86,3072
s_mov_b32 m0, 144384
s_nop 0
buffer_load_dword v87, s[36:39], 0 offen sc1 lds
v_add_u32 v88, 1, v88
ds_read_b32 v106, v82 offset:0
s_waitcnt lgkmcnt(0)
v_cmp_gt_i32 vcc, v88, v106
s_waitcnt vmcnt(1)
s_add_u32 s49, s49, 313344
s_cbranch_vccnz CF0_2
BF0_2:
s_barrier
s_add_u32 m0, s50, 32768
R 24,0
D 85,0
R 28,1024
D 85,1024
R 32,2048
R 36,3072
D 85,2048
s_waitcnt lgkmcnt(2)
v_mfma_f32_16x16x32_bf16 v[0:3], a[0:3], v[24:27], 0
R 40,4096
v_mfma_f32_16x16x32_bf16 v[4:7], a[4:7], v[24:27], 0
D 85,3072
v_mfma_f32_16x16x32_bf16 v[8:11], a[8:11], v[24:27], 0
mav 0,12,28
s_add_u32 m0, s50, 36864
R 44,5120
mav 4,16,28
mav 8,20,28
s_waitcnt lgkmcnt(2)
D 86,0
mav 0,24,32
R 48,6144
mav 4,28,32
mav 8,32,32
mav 0,36,36
D 86,1024
mav 4,40,36
R 52,7168
mav 8,44,36
s_waitcnt lgkmcnt(2)
mav 0,48,40
R 24,8192
mav 4,52,40
D 86,2048
mav 8,56,40
mav 0,60,44
R 28,9216
mav 4,64,44
mav 8,68,44
s_waitcnt lgkmcnt(2)
D 86,3072
mav 0,72,48
R 32,10240
mav 4,76,48
mav 8,80,48
mav 0,84,52
R 36,11264
mav 4,88,52
mav 8,92,52
s_waitcnt lgkmcnt(2)
mav 0,96,24
R 40,12288
mav 4,100,24
mav 8,104,24
mav 0,108,28
R 44,13312
mav 4,112,28
mav 8,116,28
s_waitcnt lgkmcnt(2)
mav 0,120,32
R 48,14336
mav 4,124,32
mav 8,128,32
mav 0,132,36
R 52,15360
mav 4,136,36
s_mov_b32 m0, 144384
mav 8,140,36
s_waitcnt lgkmcnt(2)
mav 0,144,40
R 24,16384
mav 4,148,40
buffer_load_dword v87, s[36:39], 0 offen sc1 lds
mav 8,152,40
mav 0,156,44
R 28,17408
mav 4,160,44
v_add_u32 v88, 1, v88
mav 8,164,44
s_waitcnt lgkmcnt(2)
mav 0,168,48
R 32,18432
mav 4,172,48
mav 8,176,48
mav 0,180,52
R 36,19456
mav 4,184,52
mav 8,188,52
s_waitcnt lgkmcnt(2)
mav 0,192,24
R 40,20480
mav 4,196,24
mav 8,200,24
mav 0,204,28
R 44,21504
mav 4,208,28
mav 8,212,28
s_waitcnt lgkmcnt(2)
mav 0,216,32
R 48,22528
mav 4,220,32
mav 8,224,32
mav 0,228,36
R 52,23552
mav 4,232,36
mav 8,236,36
s_waitcnt lgkmcnt(2)
mav 0,240,40
R 24,24576
mav 4,244,40
mav 8,248,40
mav 0,252,44
R 28,25600
mvv 4,128,44
mvv 8,132,44
s_waitcnt lgkmcnt(2)
mvv 0,136,48
R 32,26624
mvv 4,140,48
mvv 8,144,48
mvv 0,148,52
R 36,27648
mvv 4,152,52
mvv 8,156,52
s_waitcnt lgkmcnt(2)
mvv 0,160,24
R 40,28672
mvv 4,164,24
ds_read_b32 v106, v82 offset:0
mvv 8,168,24
mvv 0,172,28
R 44,29696
mvv 4,176,28
mvv 8,180,28
s_waitcnt lgkmcnt(3)
mvv 0,184,32
s_xor_b32 s50, s50, 0x10000
R 48,30720
mvv 4,188,32
mvv 8,192,32
s_waitcnt lgkmcnt(2)
v_cmp_gt_i32 vcc, v88, v106
mvv 0,196,36
R 52,31744
mvv 4,200,36
s_waitcnt vmcnt(1)
s_add_u32 s49, s49, 313344
mvv 8,204,36
s_cbranch_vccnz CF0_3
BF0_3:
s_barrier
mvv 0,208,40
s_add_u32 m0, s50, 0
R 24,32768
mvv 4,212,40
D 85,0
mvv 8,216,40
s_waitcnt lgkmcnt(2)
ds_read_b64 v[78:79], v89 offset:0
mvv 0,220,44
R 28,33792
mvv 4,224,44
mvv 8,228,44
D 85,1024
mvv 0,232,48
R 32,34816
mvv 4,236,48
mvv 8,240,48
s_waitcnt lgkmcnt(2)
mvv 0,244,52
D 85,2048
mvv 4,248,52
R 36,35840
mvv 8,252,52
v_mfma_f32_16x16x32_bf16 v[12:15], a[0:3], v[24:27], 0
R 40,36864
v_fma_f32 v104, v79, v56, v92
v_mfma_f32_16x16x32_bf16 v[16:19], a[4:7], v[24:27], 0
D 85,3072
v_mfma_f32_16x16x32_bf16 v[20:23], a[8:11], v[24:27], 0
s_waitcnt lgkmcnt(2)
v_fma_f32 v0, v0, v78, v104
v_fma_f32 v105, v79, v57, v93
mav 12,12,28
s_add_u32 m0, s50, 4096
R 44,37888
v_fma_f32 v1, v1, v78, v105
mav 16,16,28
v_mul_f32_e64 v0, v0, |v0|
v_mul_f32_e64 v1, v1, |v1|
mav 20,20,28
D 86,0
mav 12,24,32
R 48,38912
v_cvt_pk_bf16_f32 v0, v0, v1
mav 16,28,32
v_pk_max_i16 v68, v0, 0
v_fma_f32 v104, v79, v58, v94
mav 20,32,32
s_waitcnt lgkmcnt(2)
v_fma_f32 v2, v2, v78, v104
v_fma_f32 v105, v79, v59, v95
mav 12,36,36
D 86,1024
mav 16,40,36
R 52,39936
v_fma_f32 v3, v3, v78, v105
mav 20,44,36
v_mul_f32_e64 v2, v2, |v2|
v_mul_f32_e64 v3, v3, |v3|
mav 12,48,40
R 24,40960
v_cvt_pk_bf16_f32 v2, v2, v3
mav 16,52,40
D 86,2048
mav 20,56,40
s_waitcnt lgkmcnt(2)
v_pk_max_i16 v69, v2, 0
v_fma_f32 v104, v79, v60, v96
mav 12,60,44
R 28,41984
v_fma_f32 v4, v4, v78, v104
mav 16,64,44
v_fma_f32 v105, v79, v61, v97
v_fma_f32 v5, v5, v78, v105
mav 20,68,44
D 86,3072
mav 12,72,48
R 32,43008
v_mul_f32_e64 v4, v4, |v4|
mav 16,76,48
v_mul_f32_e64 v5, v5, |v5|
v_cvt_pk_bf16_f32 v4, v4, v5
mav 20,80,48
s_waitcnt lgkmcnt(2)
v_pk_max_i16 v70, v4, 0
v_fma_f32 v104, v79, v62, v98
mav 12,84,52
R 36,44032
v_fma_f32 v6, v6, v78, v104
mav 16,88,52
v_fma_f32 v105, v79, v63, v99
v_fma_f32 v7, v7, v78, v105
mav 20,92,52
v_mul_f32_e64 v6, v6, |v6|
v_mul_f32_e64 v7, v7, |v7|
mav 12,96,24
R 40,45056
v_cvt_pk_bf16_f32 v6, v6, v7
mav 16,100,24
v_pk_max_i16 v71, v6, 0
v_fma_f32 v104, v79, v64, v100
mav 20,104,24
s_waitcnt lgkmcnt(2)
v_fma_f32 v8, v8, v78, v104
v_fma_f32 v105, v79, v65, v101
LsF2:
s_cbranch_execz LsF6
mav 12,108,28
R 44,46080
v_fma_f32 v9, v9, v78, v105
mav 16,112,28
v_mul_f32_e64 v8, v8, |v8|
v_mul_f32_e64 v9, v9, |v9|
mav 20,116,28
buffer_store_dwordx4 v[68:71], v83, s[36:39], s49 offen sc1
v_cvt_pk_bf16_f32 v8, v8, v9
v_pk_max_i16 v72, v8, 0
v_pk_max_i16 v74, v8, 0
mav 12,120,32
R 48,47104
v_fma_f32 v104, v79, v66, v102
mav 16,124,32
v_fma_f32 v10, v10, v78, v104
v_fma_f32 v105, v79, v67, v103
mav 20,128,32
s_waitcnt lgkmcnt(2)
v_fma_f32 v11, v11, v78, v105
v_mul_f32_e64 v10, v10, |v10|
mav 12,132,36
R 52,48128
v_mul_f32_e64 v11, v11, |v11|
mav 16,136,36
s_mov_b32 m0, 144384
v_cvt_pk_bf16_f32 v10, v10, v11
v_pk_max_i16 v73, v10, 0
mav 20,140,36
v_pk_max_i16 v75, v10, 0
mav 12,144,40
R 24,49152
mav 16,148,40
buffer_load_dword v87, s[36:39], 0 offen sc1 lds
mav 20,152,40
s_waitcnt lgkmcnt(2)
v_permlane16_swap_b32 v72, v74
mav 12,156,44
R 28,50176
mav 16,160,44
v_permlane16_swap_b32 v73, v75
mav 20,164,44
buffer_store_dwordx4 v[72:75], v84, s[36:39], s49 offen sc1
v_add_u32 v88, 1, v88
mav 12,168,48
R 32,51200
mav 16,172,48
mav 20,176,48
s_waitcnt lgkmcnt(2)
mav 12,180,52
R 36,52224
mav 16,184,52
mav 20,188,52
mav 12,192,24
R 40,53248
mav 16,196,24
mav 20,200,24
s_waitcnt lgkmcnt(2)
mav 12,204,28
R 44,54272
mav 16,208,28
mav 20,212,28
mav 12,216,32
R 48,55296
mav 16,220,32
mav 20,224,32
s_waitcnt lgkmcnt(2)
mav 12,228,36
R 52,56320
mav 16,232,36
mav 20,236,36
mav 12,240,40
R 24,57344
mav 16,244,40
mav 20,248,40
s_waitcnt lgkmcnt(2)
mav 12,252,44
R 28,58368
mvv 16,128,44
mvv 20,132,44
mvv 12,136,48
R 32,59392
mvv 16,140,48
mvv 20,144,48
s_waitcnt lgkmcnt(2)
mvv 12,148,52
R 36,60416
mvv 16,152,52
mvv 20,156,52
mvv 12,160,24
s_mov_b32 s60, -30
.p2align 6
LF0_4:
mvv 16,164,24
R 40,61440
mvv 20,168,24
s_waitcnt lgkmcnt(2)
ds_read_b32 v106, v82 offset:0
mvv 12,172,28
R 44,62464
mvv 16,176,28
v_add_u32 v76, 1, v76
mvv 20,180,28
mvv 12,184,32
R 48,63488
mvv 16,188,32
mvv 20,192,32
s_waitcnt lgkmcnt(2)
v_cmp_gt_i32 vcc, v88, v106
mvv 12,196,36
R 52,64512
v_xor_b32 v80, 0x10000, v80
mvv 16,200,36
s_waitcnt vmcnt(3)
s_add_u32 s49, s49, 313344
mvv 20,204,36
s_cbranch_vccnz CF0_5
BF0_5:
s_barrier
mvv 12,208,40
s_add_u32 m0, s50, 32768
R 24,0
mvv 16,212,40
D 85,0
mvv 20,216,40
s_waitcnt lgkmcnt(2)
ds_read_b64 v[78:79], v89 offset:128
mvv 12,220,44
R 28,1024
mvv 16,224,44
mvv 20,228,44
D 85,1024
mvv 12,232,48
R 32,2048
mvv 16,236,48
mvv 20,240,48
s_waitcnt lgkmcnt(2)
mvv 12,244,52
D 85,2048
mvv 16,248,52
R 36,3072
mvv 20,252,52
v_mfma_f32_16x16x32_bf16 v[0:3], a[0:3], v[24:27], 0
R 40,4096
v_fma_f32 v104, v79, v56, v92
v_mfma_f32_16x16x32_bf16 v[4:7], a[4:7], v[24:27], 0
D 85,3072
v_mfma_f32_16x16x32_bf16 v[8:11], a[8:11], v[24:27], 0
s_waitcnt lgkmcnt(2)
v_fma_f32 v12, v12, v78, v104
v_fma_f32 v105, v79, v57, v93
mav 0,12,28
s_add_u32 m0, s50, 36864
R 44,5120
v_fma_f32 v13, v13, v78, v105
mav 4,16,28
s_waitcnt vmcnt(4)
v_mul_f32_e64 v12, v12, |v12|
v_mul_f32_e64 v13, v13, |v13|
mav 8,20,28
buffer_store_dword v76, v81, s[36:39], s52 offen sc1
mav 0,24,32
R 48,6144
v_cvt_pk_bf16_f32 v12, v12, v13
mav 4,28,32
v_pk_max_i16 v68, v12, 0
v_fma_f32 v104, v79, v58, v94
mav 8,32,32
s_waitcnt lgkmcnt(2)
v_fma_f32 v14, v14, v78, v104
v_fma_f32 v105, v79, v59, v95
mav 0,36,36
D 86,0
mav 4,40,36
R 52,7168
v_fma_f32 v15, v15, v78, v105
mav 8,44,36
v_mul_f32_e64 v14, v14, |v14|
v_mul_f32_e64 v15, v15, |v15|
mav 0,48,40
R 24,8192
v_cvt_pk_bf16_f32 v14, v14, v15
mav 4,52,40
D 86,1024
mav 8,56,40
s_waitcnt lgkmcnt(2)
v_pk_max_i16 v69, v14, 0
)"
R"(
v_fma_f32 v104, v79, v60, v96
mav 0,60,44
R 28,9216
v_fma_f32 v16, v16, v78, v104
mav 4,64,44
v_fma_f32 v105, v79, v61, v97
v_fma_f32 v17, v17, v78, v105
mav 8,68,44
D 86,2048
mav 0,72,48
R 32,10240
v_mul_f32_e64 v16, v16, |v16|
mav 4,76,48
v_mul_f32_e64 v17, v17, |v17|
v_cvt_pk_bf16_f32 v16, v16, v17
mav 8,80,48
s_waitcnt lgkmcnt(2)
v_pk_max_i16 v70, v16, 0
v_fma_f32 v104, v79, v62, v98
mav 0,84,52
D 86,3072
mav 4,88,52
R 36,11264
v_fma_f32 v18, v18, v78, v104
mav 8,92,52
v_fma_f32 v105, v79, v63, v99
v_fma_f32 v19, v19, v78, v105
mav 0,96,24
R 40,12288
v_mul_f32_e64 v18, v18, |v18|
mav 4,100,24
v_mul_f32_e64 v19, v19, |v19|
v_cvt_pk_bf16_f32 v18, v18, v19
mav 8,104,24
s_waitcnt lgkmcnt(2)
v_pk_max_i16 v71, v18, 0
v_fma_f32 v104, v79, v64, v100
mav 0,108,28
R 44,13312
v_fma_f32 v20, v20, v78, v104
mav 4,112,28
v_fma_f32 v105, v79, v65, v101
v_fma_f32 v21, v21, v78, v105
mav 8,116,28
buffer_store_dwordx4 v[68:71], v83, s[36:39], s49 offen sc1
v_mul_f32_e64 v20, v20, |v20|
v_mul_f32_e64 v21, v21, |v21|
v_cvt_pk_bf16_f32 v20, v20, v21
mav 0,120,32
R 48,14336
v_pk_max_i16 v72, v20, 0
mav 4,124,32
v_pk_max_i16 v74, v20, 0
v_fma_f32 v104, v79, v66, v102
mav 8,128,32
s_waitcnt lgkmcnt(2)
v_fma_f32 v22, v22, v78, v104
v_fma_f32 v105, v79, v67, v103
mav 0,132,36
R 52,15360
v_fma_f32 v23, v23, v78, v105
mav 4,136,36
s_mov_b32 m0, 144384
v_mul_f32_e64 v22, v22, |v22|
v_mul_f32_e64 v23, v23, |v23|
mav 8,140,36
v_cvt_pk_bf16_f32 v22, v22, v23
v_pk_max_i16 v73, v22, 0
mav 0,144,40
R 24,16384
v_pk_max_i16 v75, v22, 0
mav 4,148,40
buffer_load_dword v87, s[36:39], 0 offen sc1 lds
mav 8,152,40
s_waitcnt lgkmcnt(2)
v_permlane16_swap_b32 v72, v74
mav 0,156,44
R 28,17408
mav 4,160,44
v_permlane16_swap_b32 v73, v75
mav 8,164,44
buffer_store_dwordx4 v[72:75], v84, s[36:39], s49 offen sc1
v_add_u32 v88, 1, v88
mav 0,168,48
R 32,18432
mav 4,172,48
mav 8,176,48
s_waitcnt lgkmcnt(2)
mav 0,180,52
R 36,19456
mav 4,184,52
mav 8,188,52
mav 0,192,24
R 40,20480
mav 4,196,24
mav 8,200,24
s_waitcnt lgkmcnt(2)
mav 0,204,28
R 44,21504
mav 4,208,28
mav 8,212,28
mav 0,216,32
R 48,22528
mav 4,220,32
mav 8,224,32
s_waitcnt lgkmcnt(2)
mav 0,228,36
R 52,23552
mav 4,232,36
mav 8,236,36
mav 0,240,40
R 24,24576
mav 4,244,40
mav 8,248,40
s_waitcnt lgkmcnt(2)
mav 0,252,44
R 28,25600
mvv 4,128,44
mvv 8,132,44
mvv 0,136,48
R 32,26624
mvv 4,140,48
mvv 8,144,48
s_waitcnt lgkmcnt(2)
mvv 0,148,52
R 36,27648
mvv 4,152,52
mvv 8,156,52
mvv 0,160,24
R 40,28672
mvv 4,164,24
ds_read_b32 v106, v82 offset:0
mvv 8,168,24
s_waitcnt lgkmcnt(3)
mvv 0,172,28
R 44,29696
mvv 4,176,28
v_add_u32 v76, 1, v76
mvv 8,180,28
mvv 0,184,32
s_xor_b32 s50, s50, 0x10000
R 48,30720
mvv 4,188,32
mvv 8,192,32
s_waitcnt lgkmcnt(2)
v_cmp_gt_i32 vcc, v88, v106
mvv 0,196,36
R 52,31744
mvv 4,200,36
s_waitcnt vmcnt(3)
s_add_u32 s49, s49, 313344
mvv 8,204,36
s_cbranch_vccnz CF0_6
BF0_6:
s_barrier
mvv 0,208,40
s_add_u32 m0, s50, 0
R 24,32768
mvv 4,212,40
D 85,0
mvv 8,216,40
s_waitcnt lgkmcnt(2)
ds_read_b64 v[78:79], v89 offset:0
mvv 0,220,44
R 28,33792
mvv 4,224,44
mvv 8,228,44
D 85,1024
mvv 0,232,48
R 32,34816
mvv 4,236,48
mvv 8,240,48
s_waitcnt lgkmcnt(2)
mvv 0,244,52
D 85,2048
mvv 4,248,52
R 36,35840
mvv 8,252,52
v_mfma_f32_16x16x32_bf16 v[12:15], a[0:3], v[24:27], 0
R 40,36864
v_fma_f32 v104, v79, v56, v92
v_mfma_f32_16x16x32_bf16 v[16:19], a[4:7], v[24:27], 0
D 85,3072
v_mfma_f32_16x16x32_bf16 v[20:23], a[8:11], v[24:27], 0
s_waitcnt lgkmcnt(2)
v_fma_f32 v0, v0, v78, v104
v_fma_f32 v105, v79, v57, v93
mav 12,12,28
s_add_u32 m0, s50, 4096
R 44,37888
v_fma_f32 v1, v1, v78, v105
mav 16,16,28
s_waitcnt vmcnt(4)
v_mul_f32_e64 v0, v0, |v0|
v_mul_f32_e64 v1, v1, |v1|
mav 20,20,28
buffer_store_dword v76, v81, s[36:39], s52 offen sc1
mav 12,24,32
R 48,38912
v_cvt_pk_bf16_f32 v0, v0, v1
mav 16,28,32
v_pk_max_i16 v68, v0, 0
v_fma_f32 v104, v79, v58, v94
mav 20,32,32
s_waitcnt lgkmcnt(2)
v_fma_f32 v2, v2, v78, v104
v_fma_f32 v105, v79, v59, v95
mav 12,36,36
D 86,0
mav 16,40,36
R 52,39936
v_fma_f32 v3, v3, v78, v105
mav 20,44,36
v_mul_f32_e64 v2, v2, |v2|
v_mul_f32_e64 v3, v3, |v3|
mav 12,48,40
R 24,40960
v_cvt_pk_bf16_f32 v2, v2, v3
mav 16,52,40
D 86,1024
mav 20,56,40
s_waitcnt lgkmcnt(2)
v_pk_max_i16 v69, v2, 0
v_fma_f32 v104, v79, v60, v96
mav 12,60,44
R 28,41984
v_fma_f32 v4, v4, v78, v104
mav 16,64,44
v_fma_f32 v105, v79, v61, v97
v_fma_f32 v5, v5, v78, v105
mav 20,68,44
D 86,2048
mav 12,72,48
R 32,43008
v_mul_f32_e64 v4, v4, |v4|
mav 16,76,48
v_mul_f32_e64 v5, v5, |v5|
v_cvt_pk_bf16_f32 v4, v4, v5
mav 20,80,48
s_waitcnt lgkmcnt(2)
v_pk_max_i16 v70, v4, 0
v_fma_f32 v104, v79, v62, v98
mav 12,84,52
D 86,3072
mav 16,88,52
R 36,44032
v_fma_f32 v6, v6, v78, v104
mav 20,92,52
v_fma_f32 v105, v79, v63, v99
v_fma_f32 v7, v7, v78, v105
mav 12,96,24
R 40,45056
v_mul_f32_e64 v6, v6, |v6|
mav 16,100,24
v_mul_f32_e64 v7, v7, |v7|
v_cvt_pk_bf16_f32 v6, v6, v7
mav 20,104,24
s_waitcnt lgkmcnt(2)
v_pk_max_i16 v71, v6, 0
v_fma_f32 v104, v79, v64, v100
mav 12,108,28
R 44,46080
v_fma_f32 v8, v8, v78, v104
mav 16,112,28
v_fma_f32 v105, v79, v65, v101
v_fma_f32 v9, v9, v78, v105
mav 20,116,28
buffer_store_dwordx4 v[68:71], v83, s[36:39], s49 offen sc1
v_mul_f32_e64 v8, v8, |v8|
v_mul_f32_e64 v9, v9, |v9|
v_cvt_pk_bf16_f32 v8, v8, v9
mav 12,120,32
R 48,47104
v_pk_max_i16 v72, v8, 0
mav 16,124,32
v_pk_max_i16 v74, v8, 0
v_fma_f32 v104, v79, v66, v102
mav 20,128,32
s_waitcnt lgkmcnt(2)
v_fma_f32 v10, v10, v78, v104
v_fma_f32 v105, v79, v67, v103
mav 12,132,36
R 52,48128
v_fma_f32 v11, v11, v78, v105
mav 16,136,36
s_mov_b32 m0, 144384
v_mul_f32_e64 v10, v10, |v10|
v_mul_f32_e64 v11, v11, |v11|
mav 20,140,36
v_cvt_pk_bf16_f32 v10, v10, v11
v_pk_max_i16 v73, v10, 0
mav 12,144,40
R 24,49152
v_pk_max_i16 v75, v10, 0
mav 16,148,40
buffer_load_dword v87, s[36:39], 0 offen sc1 lds
mav 20,152,40
s_waitcnt lgkmcnt(2)
v_permlane16_swap_b32 v72, v74
mav 12,156,44
R 28,50176
mav 16,160,44
v_permlane16_swap_b32 v73, v75
mav 20,164,44
buffer_store_dwordx4 v[72:75], v84, s[36:39], s49 offen sc1
v_add_u32 v88, 1, v88
mav 12,168,48
R 32,51200
mav 16,172,48
mav 20,176,48
s_waitcnt lgkmcnt(2)
mav 12,180,52
R 36,52224
mav 16,184,52
mav 20,188,52
mav 12,192,24
R 40,53248
mav 16,196,24
mav 20,200,24
s_waitcnt lgkmcnt(2)
mav 12,204,28
R 44,54272
mav 16,208,28
mav 20,212,28
mav 12,216,32
R 48,55296
mav 16,220,32
mav 20,224,32
s_waitcnt lgkmcnt(2)
mav 12,228,36
R 52,56320
mav 16,232,36
mav 20,236,36
mav 12,240,40
R 24,57344
mav 16,244,40
mav 20,248,40
s_waitcnt lgkmcnt(2)
mav 12,252,44
R 28,58368
mvv 16,128,44
mvv 20,132,44
mvv 12,136,48
R 32,59392
mvv 16,140,48
mvv 20,144,48
s_waitcnt lgkmcnt(2)
mvv 12,148,52
R 36,60416
mvv 16,152,52
mvv 20,156,52
s_add_u32 s60, s60, 1
mvv 12,160,24
s_cbranch_scc0 LF0_4
mvv 16,164,24
R 40,61440
mvv 20,168,24
s_waitcnt lgkmcnt(2)
ds_read_b32 v106, v82 offset:0
LsF3:
s_cbranch_execz LsF7
mvv 12,172,28
R 44,62464
mvv 16,176,28
v_add_u32 v76, 1, v76
mvv 20,180,28
mvv 12,184,32
R 48,63488
mvv 16,188,32
mvv 20,192,32
s_waitcnt lgkmcnt(2)
v_cmp_gt_i32 vcc, v88, v106
mvv 12,196,36
R 52,64512
v_xor_b32 v80, 0x10000, v80
mvv 16,200,36
s_waitcnt vmcnt(3)
s_add_u32 s49, s49, 313344
mvv 20,204,36
s_cbranch_vccnz CF0_7
BF0_7:
s_barrier
mvv 12,208,40
s_add_u32 m0, s50, 32768
R 24,0
mvv 16,212,40
D 85,0
mvv 20,216,40
s_waitcnt lgkmcnt(2)
ds_read_b64 v[78:79], v89 offset:128
mvv 12,220,44
R 28,1024
mvv 16,224,44
mvv 20,228,44
D 85,1024
mvv 12,232,48
R 32,2048
mvv 16,236,48
mvv 20,240,48
s_waitcnt lgkmcnt(2)
mvv 12,244,52
D 85,2048
mvv 16,248,52
R 36,3072
mvv 20,252,52
v_mfma_f32_16x16x32_bf16 v[0:3], a[0:3], v[24:27], 0
R 40,4096
v_fma_f32 v104, v79, v56, v92
v_mfma_f32_16x16x32_bf16 v[4:7], a[4:7], v[24:27], 0
D 85,3072
v_mfma_f32_16x16x32_bf16 v[8:11], a[8:11], v[24:27], 0
s_waitcnt lgkmcnt(2)
v_fma_f32 v12, v12, v78, v104
v_fma_f32 v105, v79, v57, v93
mav 0,12,28
s_add_u32 m0, s50, 36864
R 44,5120
v_fma_f32 v13, v13, v78, v105
mav 4,16,28
s_waitcnt vmcnt(4)
v_mul_f32_e64 v12, v12, |v12|
v_mul_f32_e64 v13, v13, |v13|
mav 8,20,28
buffer_store_dword v76, v81, s[36:39], s52 offen sc1
mav 0,24,32
R 48,6144
v_cvt_pk_bf16_f32 v12, v12, v13
mav 4,28,32
v_pk_max_i16 v68, v12, 0
v_fma_f32 v104, v79, v58, v94
mav 8,32,32
s_waitcnt lgkmcnt(2)
v_fma_f32 v14, v14, v78, v104
v_fma_f32 v105, v79, v59, v95
mav 0,36,36
D 86,0
mav 4,40,36
R 52,7168
v_fma_f32 v15, v15, v78, v105
mav 8,44,36
v_mul_f32_e64 v14, v14, |v14|
v_mul_f32_e64 v15, v15, |v15|
mav 0,48,40
R 24,8192
v_cvt_pk_bf16_f32 v14, v14, v15
mav 4,52,40
D 86,1024
mav 8,56,40
s_waitcnt lgkmcnt(2)
v_pk_max_i16 v69, v14, 0
v_fma_f32 v104, v79, v60, v96
mav 0,60,44
R 28,9216
v_fma_f32 v16, v16, v78, v104
mav 4,64,44
v_fma_f32 v105, v79, v61, v97
v_fma_f32 v17, v17, v78, v105
mav 8,68,44
D 86,2048
mav 0,72,48
R 32,10240
v_mul_f32_e64 v16, v16, |v16|
mav 4,76,48
v_mul_f32_e64 v17, v17, |v17|
v_cvt_pk_bf16_f32 v16, v16, v17
mav 8,80,48
s_waitcnt lgkmcnt(2)
v_pk_max_i16 v70, v16, 0
v_fma_f32 v104, v79, v62, v98
mav 0,84,52
D 86,3072
mav 4,88,52
R 36,11264
v_fma_f32 v18, v18, v78, v104
mav 8,92,52
v_fma_f32 v105, v79, v63, v99
v_fma_f32 v19, v19, v78, v105
mav 0,96,24
R 40,12288
v_mul_f32_e64 v18, v18, |v18|
mav 4,100,24
v_mul_f32_e64 v19, v19, |v19|
v_cvt_pk_bf16_f32 v18, v18, v19
mav 8,104,24
s_waitcnt lgkmcnt(2)
v_pk_max_i16 v71, v18, 0
v_fma_f32 v104, v79, v64, v100
mav 0,108,28
R 44,13312
v_fma_f32 v20, v20, v78, v104
mav 4,112,28
v_fma_f32 v105, v79, v65, v101
v_fma_f32 v21, v21, v78, v105
mav 8,116,28
buffer_store_dwordx4 v[68:71], v83, s[36:39], s49 offen sc1
v_mul_f32_e64 v20, v20, |v20|
v_mul_f32_e64 v21, v21, |v21|
v_cvt_pk_bf16_f32 v20, v20, v21
mav 0,120,32
R 48,14336
v_pk_max_i16 v72, v20, 0
mav 4,124,32
v_pk_max_i16 v74, v20, 0
v_fma_f32 v104, v79, v66, v102
mav 8,128,32
s_waitcnt lgkmcnt(2)
v_fma_f32 v22, v22, v78, v104
v_fma_f32 v105, v79, v67, v103
mav 0,132,36
R 52,15360
v_fma_f32 v23, v23, v78, v105
mav 4,136,36
s_mov_b32 m0, 144384
v_mul_f32_e64 v22, v22, |v22|
v_mul_f32_e64 v23, v23, |v23|
mav 8,140,36
v_cvt_pk_bf16_f32 v22, v22, v23
v_pk_max_i16 v73, v22, 0
mav 0,144,40
R 24,16384
v_pk_max_i16 v75, v22, 0
mav 4,148,40
buffer_load_dword v87, s[36:39], 0 offen sc1 lds
mav 8,152,40
s_waitcnt lgkmcnt(2)
v_permlane16_swap_b32 v72, v74
mav 0,156,44
R 28,17408
mav 4,160,44
v_permlane16_swap_b32 v73, v75
mav 8,164,44
buffer_store_dwordx4 v[72:75], v84, s[36:39], s49 offen sc1
v_add_u32 v88, 1, v88
mav 0,168,48
R 32,18432
mav 4,172,48
mav 8,176,48
s_waitcnt lgkmcnt(2)
mav 0,180,52
R 36,19456
mav 4,184,52
mav 8,188,52
mav 0,192,24
R 40,20480
mav 4,196,24
mav 8,200,24
s_waitcnt lgkmcnt(2)
mav 0,204,28
R 44,21504
mav 4,208,28
mav 8,212,28
mav 0,216,32
R 48,22528
mav 4,220,32
mav 8,224,32
s_waitcnt lgkmcnt(2)
mav 0,228,36
R 52,23552
mav 4,232,36
mav 8,236,36
mav 0,240,40
R 24,24576
mav 4,244,40
mav 8,248,40
s_waitcnt lgkmcnt(2)
mav 0,252,44
R 28,25600
mvv 4,128,44
mvv 8,132,44
mvv 0,136,48
R 32,26624
mvv 4,140,48
mvv 8,144,48
s_waitcnt lgkmcnt(2)
mvv 0,148,52
R 36,27648
mvv 4,152,52
mvv 8,156,52
mvv 0,160,24
R 40,28672
mvv 4,164,24
ds_read_b32 v106, v82 offset:0
mvv 8,168,24
s_waitcnt lgkmcnt(3)
mvv 0,172,28
R 44,29696
mvv 4,176,28
v_add_u32 v76, 1, v76
mvv 8,180,28
mvv 0,184,32
R 48,30720
mvv 4,188,32
mvv 8,192,32
s_waitcnt lgkmcnt(2)
v_cmp_gt_i32 vcc, v88, v106
mvv 0,196,36
R 52,31744
mvv 4,200,36
s_waitcnt vmcnt(3)
s_add_u32 s49, s49, 313344
mvv 8,204,36
s_cbranch_vccnz CF0_8
BF0_8:
s_barrier
mvv 0,208,40
R 24,32768
mvv 4,212,40
ds_read_b64 v[78:79], v89 offset:0
mvv 8,216,40
s_waitcnt lgkmcnt(3)
mvv 0,220,44
R 28,33792
mvv 4,224,44
mvv 8,228,44
mvv 0,232,48
R 32,34816
mvv 4,236,48
mvv 8,240,48
s_waitcnt lgkmcnt(2)
mvv 0,244,52
R 36,35840
mvv 4,248,52
mvv 8,252,52
v_mfma_f32_16x16x32_bf16 v[12:15], a[0:3], v[24:27], 0
R 40,36864
v_fma_f32 v104, v79, v56, v92
v_mfma_f32_16x16x32_bf16 v[16:19], a[4:7], v[24:27], 0
v_fma_f32 v105, v79, v57, v93
v_fma_f32 v0, v0, v78, v104
v_mfma_f32_16x16x32_bf16 v[20:23], a[8:11], v[24:27], 0
s_waitcnt lgkmcnt(2)
v_fma_f32 v1, v1, v78, v105
v_mul_f32_e64 v0, v0, |v0|
mav 12,12,28
R 44,37888
v_mul_f32_e64 v1, v1, |v1|
mav 16,16,28
s_waitcnt vmcnt(0)
v_cvt_pk_bf16_f32 v0, v0, v1
v_pk_max_i16 v68, v0, 0
mav 20,20,28
buffer_store_dword v76, v81, s[36:39], s52 offen sc1
mav 12,24,32
R 48,38912
v_fma_f32 v104, v79, v58, v94
mav 16,28,32
v_fma_f32 v2, v2, v78, v104
v_fma_f32 v105, v79, v59, v95
mav 20,32,32
s_waitcnt lgkmcnt(2)
v_fma_f32 v3, v3, v78, v105
v_mul_f32_e64 v2, v2, |v2|
mav 12,36,36
R 52,39936
v_mul_f32_e64 v3, v3, |v3|
mav 16,40,36
v_cvt_pk_bf16_f32 v2, v2, v3
v_pk_max_i16 v69, v2, 0
mav 20,44,36
v_fma_f32 v104, v79, v60, v96
v_fma_f32 v4, v4, v78, v104
mav 12,48,40
R 24,40960
v_fma_f32 v105, v79, v61, v97
mav 16,52,40
v_fma_f32 v5, v5, v78, v105
v_mul_f32_e64 v4, v4, |v4|
mav 20,56,40
s_waitcnt lgkmcnt(2)
v_mul_f32_e64 v5, v5, |v5|
v_cvt_pk_bf16_f32 v4, v4, v5
mav 12,60,44
R 28,41984
v_pk_max_i16 v70, v4, 0
mav 16,64,44
v_fma_f32 v104, v79, v62, v98
v_fma_f32 v6, v6, v78, v104
mav 20,68,44
v_fma_f32 v105, v79, v63, v99
v_fma_f32 v7, v7, v78, v105
mav 12,72,48
R 32,43008
v_mul_f32_e64 v6, v6, |v6|
mav 16,76,48
v_mul_f32_e64 v7, v7, |v7|
v_cvt_pk_bf16_f32 v6, v6, v7
mav 20,80,48
s_waitcnt lgkmcnt(2)
v_pk_max_i16 v71, v6, 0
v_fma_f32 v104, v79, v64, v100
mav 12,84,52
buffer_store_dwordx4 v[68:71], v83, s[36:39], s49 offen sc1
R 36,44032
v_fma_f32 v8, v8, v78, v104
mav 16,88,52
v_fma_f32 v105, v79, v65, v101
v_fma_f32 v9, v9, v78, v105
mav 20,92,52
v_mul_f32_e64 v8, v8, |v8|
v_mul_f32_e64 v9, v9, |v9|
mav 12,96,24
R 40,45056
v_cvt_pk_bf16_f32 v8, v8, v9
mav 16,100,24
v_pk_max_i16 v72, v8, 0
v_pk_max_i16 v74, v8, 0
mav 20,104,24
s_waitcnt lgkmcnt(2)
v_fma_f32 v104, v79, v66, v102
v_fma_f32 v10, v10, v78, v104
mav 12,108,28
R 44,46080
v_fma_f32 v105, v79, v67, v103
mav 16,112,28
v_fma_f32 v11, v11, v78, v105
v_mul_f32_e64 v10, v10, |v10|
mav 20,116,28
v_mul_f32_e64 v11, v11, |v11|
v_cvt_pk_bf16_f32 v10, v10, v11
mav 12,120,32
R 48,47104
v_pk_max_i16 v73, v10, 0
mav 16,124,32
v_pk_max_i16 v75, v10, 0
mav 20,128,32
s_waitcnt lgkmcnt(2)
v_permlane16_swap_b32 v72, v74
mav 12,132,36
R 52,48128
mav 16,136,36
v_permlane16_swap_b32 v73, v75
mav 20,140,36
mav 12,144,40
R 24,49152
mav 16,148,40
buffer_store_dwordx4 v[72:75], v84, s[36:39], s49 offen sc1
mav 20,152,40
s_waitcnt lgkmcnt(2)
mav 12,156,44
R 28,50176
mav 16,160,44
mav 20,164,44
mav 12,168,48
R 32,51200
mav 16,172,48
mav 20,176,48
s_waitcnt lgkmcnt(2)
mav 12,180,52
R 36,52224
mav 16,184,52
mav 20,188,52
mav 12,192,24
R 40,53248
mav 16,196,24
mav 20,200,24
s_waitcnt lgkmcnt(2)
mav 12,204,28
R 44,54272
mav 16,208,28
mav 20,212,28
mav 12,216,32
R 48,55296
mav 16,220,32
mav 20,224,32
s_waitcnt lgkmcnt(2)
mav 12,228,36
R 52,56320
mav 16,232,36
mav 20,236,36
mav 12,240,40
R 24,57344
mav 16,244,40
mav 20,248,40
s_waitcnt lgkmcnt(2)
mav 12,252,44
R 28,58368
mvv 16,128,44
mvv 20,132,44
mvv 12,136,48
R 32,59392
mvv 16,140,48
mvv 20,144,48
s_waitcnt lgkmcnt(2)
mvv 12,148,52
R 36,60416
mvv 16,152,52
mvv 20,156,52
mvv 12,160,24
mvv 16,164,24
R 40,61440
mvv 20,168,24
s_waitcnt lgkmcnt(2)
mvv 12,172,28
R 44,62464
mvv 16,176,28
v_add_u32 v76, 1, v76
mvv 20,180,28
mvv 12,184,32
R 48,63488
mvv 16,188,32
mvv 20,192,32
s_waitcnt lgkmcnt(2)
mvv 12,196,36
R 52,64512
mvv 16,200,36
s_add_u32 s49, s49, 313344
mvv 20,204,36
s_barrier
mvv 12,208,40
mvv 16,212,40
ds_read_b64 v[78:79], v89 offset:128
mvv 20,216,40
s_waitcnt lgkmcnt(2)
mvv 12,220,44
mvv 16,224,44
mvv 20,228,44
mvv 12,232,48
mvv 16,236,48
mvv 20,240,48
s_waitcnt lgkmcnt(0)
mvv 12,244,52
mvv 16,248,52
mvv 20,252,52
v_fma_f32 v104, v79, v56, v92
s_nop 4
v_fma_f32 v12, v12, v78, v104
v_fma_f32 v105, v79, v57, v93
v_fma_f32 v13, v13, v78, v105
v_mul_f32_e64 v12, v12, |v12|
v_mul_f32_e64 v13, v13, |v13|
v_cvt_pk_bf16_f32 v12, v12, v13
v_pk_max_i16 v68, v12, 0
v_fma_f32 v104, v79, v58, v94
v_fma_f32 v14, v14, v78, v104
v_fma_f32 v105, v79, v59, v95
v_fma_f32 v15, v15, v78, v105
v_mul_f32_e64 v14, v14, |v14|
v_mul_f32_e64 v15, v15, |v15|
v_cvt_pk_bf16_f32 v14, v14, v15
v_pk_max_i16 v69, v14, 0
v_fma_f32 v104, v79, v60, v96
v_fma_f32 v16, v16, v78, v104
v_fma_f32 v105, v79, v61, v97
v_fma_f32 v17, v17, v78, v105
v_mul_f32_e64 v16, v16, |v16|
v_mul_f32_e64 v17, v17, |v17|
v_cvt_pk_bf16_f32 v16, v16, v17
v_pk_max_i16 v70, v16, 0
v_fma_f32 v104, v79, v62, v98
v_fma_f32 v18, v18, v78, v104
v_fma_f32 v105, v79, v63, v99
v_fma_f32 v19, v19, v78, v105
v_mul_f32_e64 v18, v18, |v18|
v_mul_f32_e64 v19, v19, |v19|
v_cvt_pk_bf16_f32 v18, v18, v19
v_pk_max_i16 v71, v18, 0
buffer_store_dwordx4 v[68:71], v83, s[36:39], s49 offen sc1
v_fma_f32 v104, v79, v64, v100
v_fma_f32 v20, v20, v78, v104
v_fma_f32 v105, v79, v65, v101
v_fma_f32 v21, v21, v78, v105
v_mul_f32_e64 v20, v20, |v20|
v_mul_f32_e64 v21, v21, |v21|
v_cvt_pk_bf16_f32 v20, v20, v21
v_pk_max_i16 v72, v20, 0
v_pk_max_i16 v74, v20, 0
v_fma_f32 v104, v79, v66, v102
v_fma_f32 v22, v22, v78, v104
v_fma_f32 v105, v79, v67, v103
v_fma_f32 v23, v23, v78, v105
v_mul_f32_e64 v22, v22, |v22|
v_mul_f32_e64 v23, v23, |v23|
v_cvt_pk_bf16_f32 v22, v22, v23
v_pk_max_i16 v73, v22, 0
v_pk_max_i16 v75, v22, 0
v_permlane16_swap_b32 v72, v74
s_nop 0
v_permlane16_swap_b32 v73, v75
buffer_store_dwordx4 v[72:75], v84, s[36:39], s49 offen sc1
s_waitcnt vmcnt(2)
buffer_store_dword v76, v81, s[36:39], s52 offen sc1
v_add_u32 v76, 37, v76
s_waitcnt vmcnt(1)
buffer_store_dword v76, v81, s[36:39], s52 offen sc1
s_endpgm
CF0_1:
s_cmp_eq_u32 s61, 0
s_cbranch_scc1 BF0_1
s_sub_u32 s61, s61, 1
s_sleep 8
buffer_load_dwordx2 v[106:107], v87, s[36:39], 0 offen sc1
s_waitcnt vmcnt(0)
ds_write_b32 v82, v106
v_sub_u32 v106, v106, v88
v_xor_b32 v107, v107, v77
v_cmp_lt_u64 vcc, s[62:63], v[106:107]
s_waitcnt lgkmcnt(0)
s_cbranch_vccnz CF0_1
s_branch BF0_1
CF0_2:
s_mov_b32 s58, m0
s_mov_b32 m0, 144384
DF0_2:
s_cmp_eq_u32 s61, 0
s_cbranch_scc1 EF0_2
s_sub_u32 s61, s61, 1
buffer_load_dword v87, s[36:39], 0 offen sc1 lds
s_sleep 1
ds_read_b32 v106, v82
s_waitcnt lgkmcnt(0)
v_cmp_gt_i32 vcc, v88, v106
s_cbranch_vccnz DF0_2
EF0_2:
s_mov_b32 m0, s58
s_branch BF0_2
CF0_3:
s_mov_b32 s58, m0
s_mov_b32 m0, 144384
DF0_3:
s_cmp_eq_u32 s61, 0
s_cbranch_scc1 EF0_3
s_sub_u32 s61, s61, 1
buffer_load_dword v87, s[36:39], 0 offen sc1 lds
s_sleep 1
ds_read_b32 v106, v82
LsF4:
s_cbranch_execz LsF8
s_waitcnt lgkmcnt(0)
v_cmp_gt_i32 vcc, v88, v106
s_cbranch_vccnz DF0_3
EF0_3:
s_mov_b32 m0, s58
s_branch BF0_3
CF0_5:
s_mov_b32 s58, m0
s_mov_b32 m0, 144384
DF0_5:
s_cmp_eq_u32 s61, 0
s_cbranch_scc1 EF0_5
s_sub_u32 s61, s61, 1
buffer_load_dword v87, s[36:39], 0 offen sc1 lds
s_sleep 1
ds_read_b32 v106, v82
s_waitcnt lgkmcnt(0)
v_cmp_gt_i32 vcc, v88, v106
s_cbranch_vccnz DF0_5
EF0_5:
s_mov_b32 m0, s58
s_branch BF0_5
CF0_6:
s_mov_b32 s58, m0
s_mov_b32 m0, 144384
DF0_6:
s_cmp_eq_u32 s61, 0
s_cbranch_scc1 EF0_6
s_sub_u32 s61, s61, 1
buffer_load_dword v87, s[36:39], 0 offen sc1 lds
s_sleep 1
ds_read_b32 v106, v82
s_waitcnt lgkmcnt(0)
v_cmp_gt_i32 vcc, v88, v106
s_cbranch_vccnz DF0_6
EF0_6:
s_mov_b32 m0, s58
s_branch BF0_6
CF0_7:
s_mov_b32 s58, m0
s_mov_b32 m0, 144384
DF0_7:
s_cmp_eq_u32 s61, 0
s_cbranch_scc1 EF0_7
s_sub_u32 s61, s61, 1
buffer_load_dword v87, s[36:39], 0 offen sc1 lds
s_sleep 1
ds_read_b32 v106, v82
s_waitcnt lgkmcnt(0)
v_cmp_gt_i32 vcc, v88, v106
s_cbranch_vccnz DF0_7
EF0_7:
s_mov_b32 m0, s58
s_branch BF0_7
CF0_8:
s_mov_b32 s58, m0
s_mov_b32 m0, 144384
DF0_8:
s_cmp_eq_u32 s61, 0
s_cbranch_scc1 EF0_8
s_sub_u32 s61, s61, 1
buffer_load_dword v87, s[36:39], 0 offen sc1 lds
s_sleep 1
ds_read_b32 v106, v82
s_waitcnt lgkmcnt(0)
v_cmp_gt_i32 vcc, v88, v106
s_cbranch_vccnz DF0_8
EF0_8:
s_mov_b32 m0, s58
s_branch BF0_8
LvF1:
LsF5:
s_cbranch_execz LsF9
s_add_u32 s49, s49, 313344
s_barrier
s_add_u32 m0, s50, 0
s_nop 0
D 85,0
D 85,1024
D 85,2048
D 85,3072
s_add_u32 m0, s50, 4096
s_nop 0
D 86,0
D 86,1024
D 86,2048
D 86,3072
s_waitcnt vmcnt(0)
s_add_u32 s49, s49, 313344
s_barrier
s_add_u32 m0, s50, 32768
R 24,0
buffer_load_dwordx2 v[108:109], v90, s[36:39], s49 offen sc1
buffer_load_dwordx2 v[110:111], v90, s[36:39], s49 offen offset:512 sc1
D 85,0
R 28,1024
D 85,1024
R 32,2048
R 36,3072
D 85,2048
s_waitcnt lgkmcnt(2)
v_mfma_f32_16x16x32_bf16 v[0:3], a[0:3], v[24:27], 0
R 40,4096
v_mfma_f32_16x16x32_bf16 v[4:7], a[4:7], v[24:27], 0
v_mfma_f32_16x16x32_bf16 v[8:11], a[8:11], v[24:27], 0
mav 0,12,28
R 44,5120
mav 4,16,28
D 85,3072
mav 8,20,28
s_waitcnt lgkmcnt(2)
s_add_u32 m0, s50, 36864
mav 0,24,32
R 48,6144
mav 4,28,32
mav 8,32,32
D 86,0
mav 0,36,36
R 52,7168
mav 4,40,36
mav 8,44,36
s_waitcnt lgkmcnt(2)
mav 0,48,40
D 86,1024
mav 4,52,40
R 24,8192
mav 8,56,40
mav 0,60,44
R 28,9216
mav 4,64,44
D 86,2048
mav 8,68,44
s_waitcnt lgkmcnt(2)
mav 0,72,48
R 32,10240
mav 4,76,48
mav 8,80,48
D 86,3072
mav 0,84,52
R 36,11264
mav 4,88,52
mav 8,92,52
s_waitcnt lgkmcnt(2)
mav 0,96,24
R 40,12288
mav 4,100,24
mav 8,104,24
mav 0,108,28
R 44,13312
mav 4,112,28
mav 8,116,28
s_waitcnt lgkmcnt(2)
mav 0,120,32
R 48,14336
mav 4,124,32
mav 8,128,32
mav 0,132,36
R 52,15360
mav 4,136,36
mav 8,140,36
s_waitcnt lgkmcnt(2)
mav 0,144,40
R 24,16384
mav 4,148,40
mav 8,152,40
mav 0,156,44
R 28,17408
mav 4,160,44
mav 8,164,44
s_waitcnt lgkmcnt(2)
mav 0,168,48
R 32,18432
mav 4,172,48
mav 8,176,48
mav 0,180,52
R 36,19456
mav 4,184,52
mav 8,188,52
s_waitcnt lgkmcnt(2)
mav 0,192,24
R 40,20480
mav 4,196,24
mav 8,200,24
s_waitcnt vmcnt(8)
v_add_f32 v108, v108, v110
v_add_f32 v109, v109, v111
mav 0,204,28
R 44,21504
mav 4,208,28
v_permlane32_swap_b32 v108, v109
mav 8,212,28
s_waitcnt lgkmcnt(2)
v_add_f32 v110, v108, v109
v_add_f32 v111, v108, v109
mav 0,216,32
R 48,22528
mav 4,220,32
v_permlane16_swap_b32 v110, v111
mav 8,224,32
v_add_f32 v108, v110, v111
v_add_f32 v109, v110, v111
mav 0,228,36
R 52,23552
mav 4,232,36
v_permlane32_swap_b32 v108, v109
mav 8,236,36
s_waitcnt lgkmcnt(2)
v_mul_f32 v110, 0x3a800000, v108
v_mul_f32 v111, 0x3a800000, v109
mav 0,240,40
R 24,24576
v_fma_f32 v111, -v110, v110, v111
mav 4,244,40
v_max_f32 v111, 0, v111
v_add_f32 v111, 0x3727c5ac, v111
mav 8,248,40
v_rsq_f32 v108, v111
mav 0,252,44
R 28,25600
v_mul_f32_e64 v109, -v110, v108
mvv 4,128,44
mvv 8,132,44
s_waitcnt lgkmcnt(2)
mvv 0,136,48
R 32,26624
mvv 4,140,48
mvv 8,144,48
mvv 0,148,52
R 36,27648
mvv 4,152,52
mvv 8,156,52
s_waitcnt lgkmcnt(2)
mvv 0,160,24
R 40,28672
mvv 4,164,24
mvv 8,168,24
mvv 0,172,28
R 44,29696
mvv 4,176,28
mvv 8,180,28
s_waitcnt lgkmcnt(2)
ds_write_b64 v89, v[108:109] offset:0
mvv 0,184,32
s_xor_b32 s50, s50, 0x10000
R 48,30720
mvv 4,188,32
mvv 8,192,32
mvv 0,196,36
R 52,31744
mvv 4,200,36
s_waitcnt vmcnt(0) lgkmcnt(2)
s_add_u32 s49, s49, 313344
mvv 8,204,36
s_barrier
mvv 0,208,40
buffer_load_dwordx2 v[108:109], v90, s[36:39], s49 offen sc1
mvv 4,212,40
s_add_u32 m0, s50, 0
R 24,32768
mvv 8,216,40
ds_read_b64 v[78:79], v89 offset:0
mvv 0,220,44
R 28,33792
mvv 4,224,44
buffer_load_dwordx2 v[110:111], v90, s[36:39], s49 offen offset:512 sc1
mvv 8,228,44
s_waitcnt lgkmcnt(3)
mvv 0,232,48
R 32,34816
mvv 4,236,48
mvv 8,240,48
D 85,0
mvv 0,244,52
R 36,35840
mvv 4,248,52
mvv 8,252,52
s_waitcnt lgkmcnt(2)
v_mfma_f32_16x16x32_bf16 v[12:15], a[0:3], v[24:27], 0
D 85,1024
v_mfma_f32_16x16x32_bf16 v[16:19], a[4:7], v[24:27], 0
R 40,36864
v_fma_f32 v104, v79, v56, v92
v_mfma_f32_16x16x32_bf16 v[20:23], a[8:11], v[24:27], 0
v_fma_f32 v0, v0, v78, v104
v_fma_f32 v105, v79, v57, v93
mav 12,12,28
R 44,37888
v_fma_f32 v1, v1, v78, v105
mav 16,16,28
D 85,2048
mav 20,20,28
s_waitcnt lgkmcnt(2)
v_mul_f32_e64 v0, v0, |v0|
v_mul_f32_e64 v1, v1, |v1|
mav 12,24,32
R 48,38912
v_cvt_pk_bf16_f32 v0, v0, v1
mav 16,28,32
v_pk_max_i16 v68, v0, 0
v_fma_f32 v104, v79, v58, v94
mav 20,32,32
D 85,3072
mav 12,36,36
s_add_u32 m0, s50, 4096
R 52,39936
v_fma_f32 v2, v2, v78, v104
mav 16,40,36
v_fma_f32 v105, v79, v59, v95
v_fma_f32 v3, v3, v78, v105
mav 20,44,36
s_waitcnt lgkmcnt(2)
v_mul_f32_e64 v2, v2, |v2|
v_mul_f32_e64 v3, v3, |v3|
mav 12,48,40
D 86,0
mav 16,52,40
R 24,40960
v_cvt_pk_bf16_f32 v2, v2, v3
mav 20,56,40
v_pk_max_i16 v69, v2, 0
v_fma_f32 v104, v79, v60, v96
mav 12,60,44
R 28,41984
v_fma_f32 v4, v4, v78, v104
mav 16,64,44
D 86,1024
mav 20,68,44
s_waitcnt lgkmcnt(2)
v_fma_f32 v105, v79, v61, v97
v_fma_f32 v5, v5, v78, v105
mav 12,72,48
R 32,43008
v_mul_f32_e64 v4, v4, |v4|
mav 16,76,48
v_mul_f32_e64 v5, v5, |v5|
v_cvt_pk_bf16_f32 v4, v4, v5
mav 20,80,48
D 86,2048
mav 12,84,52
R 36,44032
v_pk_max_i16 v70, v4, 0
mav 16,88,52
v_fma_f32 v104, v79, v62, v98
v_fma_f32 v6, v6, v78, v104
mav 20,92,52
s_waitcnt lgkmcnt(2)
v_fma_f32 v105, v79, v63, v99
v_fma_f32 v7, v7, v78, v105
mav 12,96,24
D 86,3072
mav 16,100,24
R 40,45056
v_mul_f32_e64 v6, v6, |v6|
mav 20,104,24
v_mul_f32_e64 v7, v7, |v7|
v_cvt_pk_bf16_f32 v6, v6, v7
mav 12,108,28
R 44,46080
v_pk_max_i16 v71, v6, 0
mav 16,112,28
buffer_store_dwordx4 v[68:71], v83, s[36:39], s49 offen sc1
v_fma_f32 v104, v79, v64, v100
v_fma_f32 v8, v8, v78, v104
v_fma_f32 v105, v79, v65, v101
mav 20,116,28
s_waitcnt lgkmcnt(2)
v_fma_f32 v9, v9, v78, v105
v_mul_f32_e64 v8, v8, |v8|
mav 12,120,32
R 48,47104
v_mul_f32_e64 v9, v9, |v9|
mav 16,124,32
v_cvt_pk_bf16_f32 v8, v8, v9
v_pk_max_i16 v72, v8, 0
mav 20,128,32
v_pk_max_i16 v74, v8, 0
v_fma_f32 v104, v79, v66, v102
mav 12,132,36
R 52,48128
v_fma_f32 v10, v10, v78, v104
mav 16,136,36
v_fma_f32 v105, v79, v67, v103
v_fma_f32 v11, v11, v78, v105
mav 20,140,36
s_waitcnt lgkmcnt(2)
v_mul_f32_e64 v10, v10, |v10|
v_mul_f32_e64 v11, v11, |v11|
mav 12,144,40
R 24,49152
v_cvt_pk_bf16_f32 v10, v10, v11
mav 16,148,40
v_pk_max_i16 v73, v10, 0
v_pk_max_i16 v75, v10, 0
mav 20,152,40
v_permlane16_swap_b32 v72, v74
mav 12,156,44
R 28,50176
mav 16,160,44
v_permlane16_swap_b32 v73, v75
mav 20,164,44
s_waitcnt lgkmcnt(2)
mav 12,168,48
R 32,51200
mav 16,172,48
mav 20,176,48
buffer_store_dwordx4 v[72:75], v84, s[36:39], s49 offen sc1
mav 12,180,52
R 36,52224
mav 16,184,52
mav 20,188,52
s_waitcnt lgkmcnt(2)
mav 12,192,24
R 40,53248
mav 16,196,24
mav 20,200,24
s_waitcnt vmcnt(10)
v_add_f32 v108, v108, v110
v_add_f32 v109, v109, v111
mav 12,204,28
R 44,54272
mav 16,208,28
v_permlane32_swap_b32 v108, v109
mav 20,212,28
s_waitcnt lgkmcnt(2)
v_add_f32 v110, v108, v109
v_add_f32 v111, v108, v109
mav 12,216,32
R 48,55296
mav 16,220,32
v_permlane16_swap_b32 v110, v111
mav 20,224,32
v_add_f32 v108, v110, v111
v_add_f32 v109, v110, v111
mav 12,228,36
R 52,56320
mav 16,232,36
v_permlane32_swap_b32 v108, v109
mav 20,236,36
s_waitcnt lgkmcnt(2)
v_mul_f32 v110, 0x3a800000, v108
v_mul_f32 v111, 0x3a800000, v109
mav 12,240,40
R 24,57344
v_fma_f32 v111, -v110, v110, v111
mav 16,244,40
v_max_f32 v111, 0, v111
v_add_f32 v111, 0x3727c5ac, v111
mav 20,248,40
v_rsq_f32 v108, v111
mav 12,252,44
R 28,58368
v_mul_f32_e64 v109, -v110, v108
mvv 16,128,44
mvv 20,132,44
s_waitcnt lgkmcnt(2)
mvv 12,136,48
R 32,59392
mvv 16,140,48
mvv 20,144,48
mvv 12,148,52
R 36,60416
mvv 16,152,52
mvv 20,156,52
s_waitcnt lgkmcnt(2)
mvv 12,160,24
mvv 16,164,24
R 40,61440
mvv 20,168,24
mvv 12,172,28
R 44,62464
mvv 16,176,28
v_add_u32 v76, 1, v76
mvv 20,180,28
s_waitcnt lgkmcnt(2)
ds_write_b64 v89, v[108:109] offset:128
mvv 12,184,32
R 48,63488
mvv 16,188,32
mvv 20,192,32
mvv 12,196,36
R 52,64512
v_xor_b32 v80, 0x10000, v80
mvv 16,200,36
s_waitcnt vmcnt(2) lgkmcnt(2)
s_add_u32 s49, s49, 313344
mvv 20,204,36
s_barrier
mvv 12,208,40
buffer_load_dwordx2 v[108:109], v90, s[36:39], s49 offen sc1
mvv 16,212,40
s_add_u32 m0, s50, 32768
R 24,0
mvv 20,216,40
ds_read_b64 v[78:79], v89 offset:128
mvv 12,220,44
R 28,1024
mvv 16,224,44
buffer_load_dwordx2 v[110:111], v90, s[36:39], s49 offen offset:512 sc1
mvv 20,228,44
s_waitcnt lgkmcnt(3)
mvv 12,232,48
R 32,2048
mvv 16,236,48
mvv 20,240,48
D 85,0
mvv 12,244,52
R 36,3072
mvv 16,248,52
mvv 20,252,52
s_waitcnt lgkmcnt(2)
v_mfma_f32_16x16x32_bf16 v[0:3], a[0:3], v[24:27], 0
D 85,1024
v_mfma_f32_16x16x32_bf16 v[4:7], a[4:7], v[24:27], 0
R 40,4096
v_fma_f32 v104, v79, v56, v92
v_mfma_f32_16x16x32_bf16 v[8:11], a[8:11], v[24:27], 0
v_fma_f32 v12, v12, v78, v104
v_fma_f32 v105, v79, v57, v93
mav 0,12,28
R 44,5120
v_fma_f32 v13, v13, v78, v105
mav 4,16,28
s_waitcnt vmcnt(4)
buffer_store_dword v76, v81, s[36:39], s52 offen sc1
mav 8,20,28
s_waitcnt lgkmcnt(2)
v_mul_f32_e64 v12, v12, |v12|
v_mul_f32_e64 v13, v13, |v13|
mav 0,24,32
R 48,6144
v_cvt_pk_bf16_f32 v12, v12, v13
mav 4,28,32
v_pk_max_i16 v68, v12, 0
v_fma_f32 v104, v79, v58, v94
mav 8,32,32
D 85,2048
mav 0,36,36
R 52,7168
v_fma_f32 v14, v14, v78, v104
mav 4,40,36
v_fma_f32 v105, v79, v59, v95
v_fma_f32 v15, v15, v78, v105
mav 8,44,36
s_waitcnt lgkmcnt(2)
v_mul_f32_e64 v14, v14, |v14|
v_mul_f32_e64 v15, v15, |v15|
mav 0,48,40
D 85,3072
mav 4,52,40
s_add_u32 m0, s50, 36864
R 24,8192
v_cvt_pk_bf16_f32 v14, v14, v15
mav 8,56,40
v_pk_max_i16 v69, v14, 0
v_fma_f32 v104, v79, v60, v96
mav 0,60,44
R 28,9216
v_fma_f32 v16, v16, v78, v104
mav 4,64,44
D 86,0
mav 8,68,44
s_waitcnt lgkmcnt(2)
v_fma_f32 v105, v79, v61, v97
v_fma_f32 v17, v17, v78, v105
mav 0,72,48
R 32,10240
v_mul_f32_e64 v16, v16, |v16|
mav 4,76,48
v_mul_f32_e64 v17, v17, |v17|
v_cvt_pk_bf16_f32 v16, v16, v17
mav 8,80,48
D 86,1024
mav 0,84,52
R 36,11264
v_pk_max_i16 v70, v16, 0
mav 4,88,52
v_fma_f32 v104, v79, v62, v98
v_fma_f32 v18, v18, v78, v104
mav 8,92,52
s_waitcnt lgkmcnt(2)
v_fma_f32 v105, v79, v63, v99
v_fma_f32 v19, v19, v78, v105
mav 0,96,24
D 86,2048
mav 4,100,24
R 40,12288
LsF6:
s_cbranch_execz LsF10
v_mul_f32_e64 v18, v18, |v18|
mav 8,104,24
v_mul_f32_e64 v19, v19, |v19|
v_cvt_pk_bf16_f32 v18, v18, v19
mav 0,108,28
R 44,13312
v_pk_max_i16 v71, v18, 0
mav 4,112,28
buffer_store_dwordx4 v[68:71], v83, s[36:39], s49 offen sc1
D 86,3072
mav 8,116,28
s_waitcnt lgkmcnt(2)
v_fma_f32 v104, v79, v64, v100
v_fma_f32 v20, v20, v78, v104
mav 0,120,32
R 48,14336
v_fma_f32 v105, v79, v65, v101
mav 4,124,32
v_fma_f32 v21, v21, v78, v105
v_mul_f32_e64 v20, v20, |v20|
mav 8,128,32
v_mul_f32_e64 v21, v21, |v21|
v_cvt_pk_bf16_f32 v20, v20, v21
mav 0,132,36
R 52,15360
v_pk_max_i16 v72, v20, 0
mav 4,136,36
v_pk_max_i16 v74, v20, 0
v_fma_f32 v104, v79, v66, v102
mav 8,140,36
s_waitcnt lgkmcnt(2)
v_fma_f32 v22, v22, v78, v104
v_fma_f32 v105, v79, v67, v103
mav 0,144,40
R 24,16384
v_fma_f32 v23, v23, v78, v105
mav 4,148,40
v_mul_f32_e64 v22, v22, |v22|
v_mul_f32_e64 v23, v23, |v23|
mav 8,152,40
v_cvt_pk_bf16_f32 v22, v22, v23
v_pk_max_i16 v73, v22, 0
mav 0,156,44
R 28,17408
v_pk_max_i16 v75, v22, 0
mav 4,160,44
v_permlane16_swap_b32 v72, v74
mav 8,164,44
s_waitcnt lgkmcnt(2)
v_permlane16_swap_b32 v73, v75
mav 0,168,48
R 32,18432
mav 4,172,48
mav 8,176,48
buffer_store_dwordx4 v[72:75], v84, s[36:39], s49 offen sc1
mav 0,180,52
R 36,19456
mav 4,184,52
mav 8,188,52
s_waitcnt lgkmcnt(2)
mav 0,192,24
R 40,20480
mav 4,196,24
mav 8,200,24
s_waitcnt vmcnt(11)
v_add_f32 v108, v108, v110
v_add_f32 v109, v109, v111
mav 0,204,28
R 44,21504
mav 4,208,28
v_permlane32_swap_b32 v108, v109
mav 8,212,28
s_waitcnt lgkmcnt(2)
v_add_f32 v110, v108, v109
v_add_f32 v111, v108, v109
mav 0,216,32
R 48,22528
mav 4,220,32
v_permlane16_swap_b32 v110, v111
mav 8,224,32
v_add_f32 v108, v110, v111
v_add_f32 v109, v110, v111
mav 0,228,36
R 52,23552
mav 4,232,36
v_permlane32_swap_b32 v108, v109
mav 8,236,36
s_waitcnt lgkmcnt(2)
v_mul_f32 v110, 0x3a800000, v108
v_mul_f32 v111, 0x3a800000, v109
mav 0,240,40
R 24,24576
v_fma_f32 v111, -v110, v110, v111
mav 4,244,40
v_max_f32 v111, 0, v111
v_add_f32 v111, 0x3727c5ac, v111
mav 8,248,40
v_rsq_f32 v108, v111
mav 0,252,44
R 28,25600
v_mul_f32_e64 v109, -v110, v108
mvv 4,128,44
mvv 8,132,44
s_waitcnt lgkmcnt(2)
mvv 0,136,48
R 32,26624
mvv 4,140,48
mvv 8,144,48
mvv 0,148,52
R 36,27648
mvv 4,152,52
mvv 8,156,52
s_waitcnt lgkmcnt(2)
mvv 0,160,24
R 40,28672
mvv 4,164,24
mvv 8,168,24
mvv 0,172,28
R 44,29696
mvv 4,176,28
v_add_u32 v76, 1, v76
mvv 8,180,28
s_waitcnt lgkmcnt(2)
ds_write_b64 v89, v[108:109] offset:0
mvv 0,184,32
s_xor_b32 s50, s50, 0x10000
R 48,30720
mvv 4,188,32
mvv 8,192,32
mvv 0,196,36
R 52,31744
mvv 4,200,36
s_waitcnt vmcnt(1) lgkmcnt(2)
s_add_u32 s49, s49, 313344
mvv 8,204,36
s_barrier
mvv 0,208,40
buffer_load_dwordx2 v[108:109], v90, s[36:39], s49 offen sc1
mvv 4,212,40
s_add_u32 m0, s50, 0
R 24,32768
mvv 8,216,40
ds_read_b64 v[78:79], v89 offset:0
mvv 0,220,44
R 28,33792
mvv 4,224,44
buffer_load_dwordx2 v[110:111], v90, s[36:39], s49 offen offset:512 sc1
mvv 8,228,44
s_waitcnt lgkmcnt(3)
mvv 0,232,48
R 32,34816
mvv 4,236,48
mvv 8,240,48
D 85,0
mvv 0,244,52
R 36,35840
mvv 4,248,52
mvv 8,252,52
s_waitcnt lgkmcnt(2)
v_mfma_f32_16x16x32_bf16 v[12:15], a[0:3], v[24:27], 0
D 85,1024
v_mfma_f32_16x16x32_bf16 v[16:19], a[4:7], v[24:27], 0
R 40,36864
v_fma_f32 v104, v79, v56, v92
v_mfma_f32_16x16x32_bf16 v[20:23], a[8:11], v[24:27], 0
v_fma_f32 v0, v0, v78, v104
v_fma_f32 v105, v79, v57, v93
mav 12,12,28
R 44,37888
v_fma_f32 v1, v1, v78, v105
mav 16,16,28
s_waitcnt vmcnt(4)
buffer_store_dword v76, v81, s[36:39], s52 offen sc1
mav 20,20,28
s_waitcnt lgkmcnt(2)
v_mul_f32_e64 v0, v0, |v0|
v_mul_f32_e64 v1, v1, |v1|
mav 12,24,32
R 48,38912
v_cvt_pk_bf16_f32 v0, v0, v1
mav 16,28,32
v_pk_max_i16 v68, v0, 0
v_fma_f32 v104, v79, v58, v94
mav 20,32,32
D 85,2048
mav 12,36,36
R 52,39936
v_fma_f32 v2, v2, v78, v104
mav 16,40,36
v_fma_f32 v105, v79, v59, v95
v_fma_f32 v3, v3, v78, v105
mav 20,44,36
s_waitcnt lgkmcnt(2)
v_mul_f32_e64 v2, v2, |v2|
v_mul_f32_e64 v3, v3, |v3|
mav 12,48,40
D 85,3072
mav 16,52,40
s_add_u32 m0, s50, 4096
R 24,40960
v_cvt_pk_bf16_f32 v2, v2, v3
mav 20,56,40
v_pk_max_i16 v69, v2, 0
v_fma_f32 v104, v79, v60, v96
mav 12,60,44
R 28,41984
v_fma_f32 v4, v4, v78, v104
mav 16,64,44
D 86,0
mav 20,68,44
s_waitcnt lgkmcnt(2)
v_fma_f32 v105, v79, v61, v97
v_fma_f32 v5, v5, v78, v105
mav 12,72,48
R 32,43008
v_mul_f32_e64 v4, v4, |v4|
mav 16,76,48
v_mul_f32_e64 v5, v5, |v5|
v_cvt_pk_bf16_f32 v4, v4, v5
mav 20,80,48
D 86,1024
mav 12,84,52
R 36,44032
v_pk_max_i16 v70, v4, 0
mav 16,88,52
v_fma_f32 v104, v79, v62, v98
v_fma_f32 v6, v6, v78, v104
mav 20,92,52
s_waitcnt lgkmcnt(2)
v_fma_f32 v105, v79, v63, v99
v_fma_f32 v7, v7, v78, v105
mav 12,96,24
D 86,2048
mav 16,100,24
R 40,45056
v_mul_f32_e64 v6, v6, |v6|
mav 20,104,24
v_mul_f32_e64 v7, v7, |v7|
v_cvt_pk_bf16_f32 v6, v6, v7
mav 12,108,28
R 44,46080
v_pk_max_i16 v71, v6, 0
mav 16,112,28
buffer_store_dwordx4 v[68:71], v83, s[36:39], s49 offen sc1
D 86,3072
mav 20,116,28
s_waitcnt lgkmcnt(2)
v_fma_f32 v104, v79, v64, v100
v_fma_f32 v8, v8, v78, v104
mav 12,120,32
R 48,47104
v_fma_f32 v105, v79, v65, v101
mav 16,124,32
v_fma_f32 v9, v9, v78, v105
v_mul_f32_e64 v8, v8, |v8|
mav 20,128,32
v_mul_f32_e64 v9, v9, |v9|
v_cvt_pk_bf16_f32 v8, v8, v9
mav 12,132,36
R 52,48128
v_pk_max_i16 v72, v8, 0
mav 16,136,36
v_pk_max_i16 v74, v8, 0
v_fma_f32 v104, v79, v66, v102
mav 20,140,36
s_waitcnt lgkmcnt(2)
v_fma_f32 v10, v10, v78, v104
v_fma_f32 v105, v79, v67, v103
mav 12,144,40
R 24,49152
v_fma_f32 v11, v11, v78, v105
mav 16,148,40
v_mul_f32_e64 v10, v10, |v10|
v_mul_f32_e64 v11, v11, |v11|
mav 20,152,40
v_cvt_pk_bf16_f32 v10, v10, v11
v_pk_max_i16 v73, v10, 0
mav 12,156,44
R 28,50176
v_pk_max_i16 v75, v10, 0
mav 16,160,44
v_permlane16_swap_b32 v72, v74
mav 20,164,44
s_waitcnt lgkmcnt(2)
v_permlane16_swap_b32 v73, v75
mav 12,168,48
R 32,51200
mav 16,172,48
mav 20,176,48
buffer_store_dwordx4 v[72:75], v84, s[36:39], s49 offen sc1
mav 12,180,52
R 36,52224
mav 16,184,52
mav 20,188,52
s_waitcnt lgkmcnt(2)
mav 12,192,24
R 40,53248
mav 16,196,24
mav 20,200,24
s_waitcnt vmcnt(11)
v_add_f32 v108, v108, v110
v_add_f32 v109, v109, v111
mav 12,204,28
R 44,54272
mav 16,208,28
v_permlane32_swap_b32 v108, v109
mav 20,212,28
s_waitcnt lgkmcnt(2)
v_add_f32 v110, v108, v109
v_add_f32 v111, v108, v109
mav 12,216,32
R 48,55296
mav 16,220,32
v_permlane16_swap_b32 v110, v111
mav 20,224,32
v_add_f32 v108, v110, v111
v_add_f32 v109, v110, v111
mav 12,228,36
R 52,56320
mav 16,232,36
v_permlane32_swap_b32 v108, v109
mav 20,236,36
s_waitcnt lgkmcnt(2)
v_mul_f32 v110, 0x3a800000, v108
v_mul_f32 v111, 0x3a800000, v109
mav 12,240,40
R 24,57344
v_fma_f32 v111, -v110, v110, v111
mav 16,244,40
v_max_f32 v111, 0, v111
v_add_f32 v111, 0x3727c5ac, v111
mav 20,248,40
v_rsq_f32 v108, v111
mav 12,252,44
R 28,58368
v_mul_f32_e64 v109, -v110, v108
mvv 16,128,44
mvv 20,132,44
s_waitcnt lgkmcnt(2)
mvv 12,136,48
R 32,59392
mvv 16,140,48
mvv 20,144,48
mvv 12,148,52
R 36,60416
mvv 16,152,52
mvv 20,156,52
s_waitcnt lgkmcnt(2)
mvv 12,160,24
s_mov_b32 s60, -29
.p2align 6
LF1_1:
mvv 16,164,24
R 40,61440
mvv 20,168,24
mvv 12,172,28
R 44,62464
mvv 16,176,28
v_add_u32 v76, 1, v76
mvv 20,180,28
s_waitcnt lgkmcnt(2)
ds_write_b64 v89, v[108:109] offset:128
mvv 12,184,32
R 48,63488
mvv 16,188,32
mvv 20,192,32
mvv 12,196,36
R 52,64512
v_xor_b32 v80, 0x10000, v80
mvv 16,200,36
s_waitcnt vmcnt(1) lgkmcnt(2)
s_add_u32 s49, s49, 313344
mvv 20,204,36
s_barrier
mvv 12,208,40
buffer_load_dwordx2 v[108:109], v90, s[36:39], s49 offen sc1
mvv 16,212,40
s_add_u32 m0, s50, 32768
R 24,0
mvv 20,216,40
ds_read_b64 v[78:79], v89 offset:128
mvv 12,220,44
R 28,1024
mvv 16,224,44
buffer_load_dwordx2 v[110:111], v90, s[36:39], s49 offen offset:512 sc1
mvv 20,228,44
s_waitcnt lgkmcnt(3)
mvv 12,232,48
R 32,2048
mvv 16,236,48
mvv 20,240,48
D 85,0
mvv 12,244,52
R 36,3072
mvv 16,248,52
mvv 20,252,52
s_waitcnt lgkmcnt(2)
v_mfma_f32_16x16x32_bf16 v[0:3], a[0:3], v[24:27], 0
D 85,1024
v_mfma_f32_16x16x32_bf16 v[4:7], a[4:7], v[24:27], 0
R 40,4096
v_fma_f32 v104, v79, v56, v92
v_mfma_f32_16x16x32_bf16 v[8:11], a[8:11], v[24:27], 0
v_fma_f32 v12, v12, v78, v104
v_fma_f32 v105, v79, v57, v93
mav 0,12,28
R 44,5120
v_fma_f32 v13, v13, v78, v105
mav 4,16,28
s_waitcnt vmcnt(4)
buffer_store_dword v76, v81, s[36:39], s52 offen sc1
mav 8,20,28
s_waitcnt lgkmcnt(2)
v_mul_f32_e64 v12, v12, |v12|
v_mul_f32_e64 v13, v13, |v13|
mav 0,24,32
R 48,6144
v_cvt_pk_bf16_f32 v12, v12, v13
mav 4,28,32
v_pk_max_i16 v68, v12, 0
v_fma_f32 v104, v79, v58, v94
mav 8,32,32
D 85,2048
mav 0,36,36
R 52,7168
v_fma_f32 v14, v14, v78, v104
mav 4,40,36
v_fma_f32 v105, v79, v59, v95
v_fma_f32 v15, v15, v78, v105
mav 8,44,36
s_waitcnt lgkmcnt(2)
v_mul_f32_e64 v14, v14, |v14|
v_mul_f32_e64 v15, v15, |v15|
mav 0,48,40
D 85,3072
mav 4,52,40
s_add_u32 m0, s50, 36864
R 24,8192
v_cvt_pk_bf16_f32 v14, v14, v15
mav 8,56,40
v_pk_max_i16 v69, v14, 0
v_fma_f32 v104, v79, v60, v96
mav 0,60,44
R 28,9216
v_fma_f32 v16, v16, v78, v104
mav 4,64,44
D 86,0
mav 8,68,44
s_waitcnt lgkmcnt(2)
v_fma_f32 v105, v79, v61, v97
v_fma_f32 v17, v17, v78, v105
mav 0,72,48
R 32,10240
v_mul_f32_e64 v16, v16, |v16|
mav 4,76,48
v_mul_f32_e64 v17, v17, |v17|
v_cvt_pk_bf16_f32 v16, v16, v17
mav 8,80,48
D 86,1024
mav 0,84,52
R 36,11264
v_pk_max_i16 v70, v16, 0
mav 4,88,52
v_fma_f32 v104, v79, v62, v98
v_fma_f32 v18, v18, v78, v104
mav 8,92,52
s_waitcnt lgkmcnt(2)
v_fma_f32 v105, v79, v63, v99
v_fma_f32 v19, v19, v78, v105
mav 0,96,24
D 86,2048
mav 4,100,24
R 40,12288
v_mul_f32_e64 v18, v18, |v18|
mav 8,104,24
v_mul_f32_e64 v19, v19, |v19|
v_cvt_pk_bf16_f32 v18, v18, v19
mav 0,108,28
R 44,13312
v_pk_max_i16 v71, v18, 0
mav 4,112,28
buffer_store_dwordx4 v[68:71], v83, s[36:39], s49 offen sc1
D 86,3072
mav 8,116,28
s_waitcnt lgkmcnt(2)
)"
R"(
v_fma_f32 v104, v79, v64, v100
v_fma_f32 v20, v20, v78, v104
mav 0,120,32
R 48,14336
v_fma_f32 v105, v79, v65, v101
mav 4,124,32
v_fma_f32 v21, v21, v78, v105
v_mul_f32_e64 v20, v20, |v20|
mav 8,128,32
v_mul_f32_e64 v21, v21, |v21|
v_cvt_pk_bf16_f32 v20, v20, v21
mav 0,132,36
R 52,15360
v_pk_max_i16 v72, v20, 0
mav 4,136,36
v_pk_max_i16 v74, v20, 0
v_fma_f32 v104, v79, v66, v102
mav 8,140,36
s_waitcnt lgkmcnt(2)
v_fma_f32 v22, v22, v78, v104
v_fma_f32 v105, v79, v67, v103
mav 0,144,40
R 24,16384
v_fma_f32 v23, v23, v78, v105
mav 4,148,40
v_mul_f32_e64 v22, v22, |v22|
v_mul_f32_e64 v23, v23, |v23|
mav 8,152,40
v_cvt_pk_bf16_f32 v22, v22, v23
v_pk_max_i16 v73, v22, 0
mav 0,156,44
R 28,17408
v_pk_max_i16 v75, v22, 0
mav 4,160,44
v_permlane16_swap_b32 v72, v74
mav 8,164,44
s_waitcnt lgkmcnt(2)
v_permlane16_swap_b32 v73, v75
mav 0,168,48
R 32,18432
mav 4,172,48
mav 8,176,48
buffer_store_dwordx4 v[72:75], v84, s[36:39], s49 offen sc1
mav 0,180,52
R 36,19456
mav 4,184,52
mav 8,188,52
s_waitcnt lgkmcnt(2)
mav 0,192,24
R 40,20480
mav 4,196,24
mav 8,200,24
s_waitcnt vmcnt(11)
v_add_f32 v108, v108, v110
v_add_f32 v109, v109, v111
mav 0,204,28
R 44,21504
mav 4,208,28
v_permlane32_swap_b32 v108, v109
mav 8,212,28
s_waitcnt lgkmcnt(2)
v_add_f32 v110, v108, v109
v_add_f32 v111, v108, v109
LsF7:
s_cbranch_execz LsF11
mav 0,216,32
R 48,22528
mav 4,220,32
v_permlane16_swap_b32 v110, v111
mav 8,224,32
v_add_f32 v108, v110, v111
v_add_f32 v109, v110, v111
mav 0,228,36
R 52,23552
mav 4,232,36
v_permlane32_swap_b32 v108, v109
mav 8,236,36
s_waitcnt lgkmcnt(2)
v_mul_f32 v110, 0x3a800000, v108
v_mul_f32 v111, 0x3a800000, v109
mav 0,240,40
R 24,24576
v_fma_f32 v111, -v110, v110, v111
mav 4,244,40
v_max_f32 v111, 0, v111
v_add_f32 v111, 0x3727c5ac, v111
mav 8,248,40
v_rsq_f32 v108, v111
mav 0,252,44
R 28,25600
v_mul_f32_e64 v109, -v110, v108
mvv 4,128,44
mvv 8,132,44
s_waitcnt lgkmcnt(2)
mvv 0,136,48
R 32,26624
mvv 4,140,48
mvv 8,144,48
mvv 0,148,52
R 36,27648
mvv 4,152,52
mvv 8,156,52
s_waitcnt lgkmcnt(2)
mvv 0,160,24
R 40,28672
mvv 4,164,24
mvv 8,168,24
mvv 0,172,28
R 44,29696
mvv 4,176,28
v_add_u32 v76, 1, v76
mvv 8,180,28
s_waitcnt lgkmcnt(2)
ds_write_b64 v89, v[108:109] offset:0
mvv 0,184,32
s_xor_b32 s50, s50, 0x10000
R 48,30720
mvv 4,188,32
mvv 8,192,32
mvv 0,196,36
R 52,31744
mvv 4,200,36
s_waitcnt vmcnt(1) lgkmcnt(2)
s_add_u32 s49, s49, 313344
mvv 8,204,36
s_barrier
mvv 0,208,40
buffer_load_dwordx2 v[108:109], v90, s[36:39], s49 offen sc1
mvv 4,212,40
s_add_u32 m0, s50, 0
R 24,32768
mvv 8,216,40
ds_read_b64 v[78:79], v89 offset:0
mvv 0,220,44
R 28,33792
mvv 4,224,44
buffer_load_dwordx2 v[110:111], v90, s[36:39], s49 offen offset:512 sc1
mvv 8,228,44
s_waitcnt lgkmcnt(3)
mvv 0,232,48
R 32,34816
mvv 4,236,48
mvv 8,240,48
D 85,0
mvv 0,244,52
R 36,35840
mvv 4,248,52
mvv 8,252,52
s_waitcnt lgkmcnt(2)
v_mfma_f32_16x16x32_bf16 v[12:15], a[0:3], v[24:27], 0
D 85,1024
v_mfma_f32_16x16x32_bf16 v[16:19], a[4:7], v[24:27], 0
R 40,36864
v_fma_f32 v104, v79, v56, v92
v_mfma_f32_16x16x32_bf16 v[20:23], a[8:11], v[24:27], 0
v_fma_f32 v0, v0, v78, v104
v_fma_f32 v105, v79, v57, v93
mav 12,12,28
R 44,37888
v_fma_f32 v1, v1, v78, v105
mav 16,16,28
s_waitcnt vmcnt(4)
buffer_store_dword v76, v81, s[36:39], s52 offen sc1
mav 20,20,28
s_waitcnt lgkmcnt(2)
v_mul_f32_e64 v0, v0, |v0|
v_mul_f32_e64 v1, v1, |v1|
mav 12,24,32
R 48,38912
v_cvt_pk_bf16_f32 v0, v0, v1
mav 16,28,32
v_pk_max_i16 v68, v0, 0
v_fma_f32 v104, v79, v58, v94
mav 20,32,32
D 85,2048
mav 12,36,36
R 52,39936
v_fma_f32 v2, v2, v78, v104
mav 16,40,36
v_fma_f32 v105, v79, v59, v95
v_fma_f32 v3, v3, v78, v105
mav 20,44,36
s_waitcnt lgkmcnt(2)
v_mul_f32_e64 v2, v2, |v2|
v_mul_f32_e64 v3, v3, |v3|
mav 12,48,40
D 85,3072
mav 16,52,40
s_add_u32 m0, s50, 4096
R 24,40960
v_cvt_pk_bf16_f32 v2, v2, v3
mav 20,56,40
v_pk_max_i16 v69, v2, 0
v_fma_f32 v104, v79, v60, v96
mav 12,60,44
R 28,41984
v_fma_f32 v4, v4, v78, v104
mav 16,64,44
D 86,0
mav 20,68,44
s_waitcnt lgkmcnt(2)
v_fma_f32 v105, v79, v61, v97
v_fma_f32 v5, v5, v78, v105
mav 12,72,48
R 32,43008
v_mul_f32_e64 v4, v4, |v4|
mav 16,76,48
v_mul_f32_e64 v5, v5, |v5|
v_cvt_pk_bf16_f32 v4, v4, v5
mav 20,80,48
D 86,1024
mav 12,84,52
R 36,44032
v_pk_max_i16 v70, v4, 0
mav 16,88,52
v_fma_f32 v104, v79, v62, v98
v_fma_f32 v6, v6, v78, v104
mav 20,92,52
s_waitcnt lgkmcnt(2)
v_fma_f32 v105, v79, v63, v99
v_fma_f32 v7, v7, v78, v105
mav 12,96,24
D 86,2048
mav 16,100,24
R 40,45056
v_mul_f32_e64 v6, v6, |v6|
mav 20,104,24
v_mul_f32_e64 v7, v7, |v7|
v_cvt_pk_bf16_f32 v6, v6, v7
mav 12,108,28
R 44,46080
v_pk_max_i16 v71, v6, 0
mav 16,112,28
buffer_store_dwordx4 v[68:71], v83, s[36:39], s49 offen sc1
D 86,3072
mav 20,116,28
s_waitcnt lgkmcnt(2)
v_fma_f32 v104, v79, v64, v100
v_fma_f32 v8, v8, v78, v104
mav 12,120,32
R 48,47104
v_fma_f32 v105, v79, v65, v101
mav 16,124,32
v_fma_f32 v9, v9, v78, v105
v_mul_f32_e64 v8, v8, |v8|
mav 20,128,32
v_mul_f32_e64 v9, v9, |v9|
v_cvt_pk_bf16_f32 v8, v8, v9
mav 12,132,36
R 52,48128
v_pk_max_i16 v72, v8, 0
mav 16,136,36
v_pk_max_i16 v74, v8, 0
v_fma_f32 v104, v79, v66, v102
mav 20,140,36
s_waitcnt lgkmcnt(2)
v_fma_f32 v10, v10, v78, v104
v_fma_f32 v105, v79, v67, v103
mav 12,144,40
R 24,49152
v_fma_f32 v11, v11, v78, v105
mav 16,148,40
v_mul_f32_e64 v10, v10, |v10|
v_mul_f32_e64 v11, v11, |v11|
mav 20,152,40
v_cvt_pk_bf16_f32 v10, v10, v11
v_pk_max_i16 v73, v10, 0
mav 12,156,44
R 28,50176
v_pk_max_i16 v75, v10, 0
mav 16,160,44
v_permlane16_swap_b32 v72, v74
mav 20,164,44
s_waitcnt lgkmcnt(2)
v_permlane16_swap_b32 v73, v75
mav 12,168,48
R 32,51200
mav 16,172,48
mav 20,176,48
buffer_store_dwordx4 v[72:75], v84, s[36:39], s49 offen sc1
mav 12,180,52
R 36,52224
mav 16,184,52
mav 20,188,52
s_waitcnt lgkmcnt(2)
mav 12,192,24
R 40,53248
mav 16,196,24
mav 20,200,24
s_waitcnt vmcnt(11)
v_add_f32 v108, v108, v110
v_add_f32 v109, v109, v111
mav 12,204,28
R 44,54272
mav 16,208,28
v_permlane32_swap_b32 v108, v109
mav 20,212,28
s_waitcnt lgkmcnt(2)
v_add_f32 v110, v108, v109
v_add_f32 v111, v108, v109
mav 12,216,32
R 48,55296
mav 16,220,32
v_permlane16_swap_b32 v110, v111
mav 20,224,32
v_add_f32 v108, v110, v111
v_add_f32 v109, v110, v111
mav 12,228,36
R 52,56320
mav 16,232,36
v_permlane32_swap_b32 v108, v109
mav 20,236,36
s_waitcnt lgkmcnt(2)
v_mul_f32 v110, 0x3a800000, v108
v_mul_f32 v111, 0x3a800000, v109
mav 12,240,40
R 24,57344
v_fma_f32 v111, -v110, v110, v111
mav 16,244,40
v_max_f32 v111, 0, v111
v_add_f32 v111, 0x3727c5ac, v111
mav 20,248,40
v_rsq_f32 v108, v111
mav 12,252,44
R 28,58368
v_mul_f32_e64 v109, -v110, v108
mvv 16,128,44
mvv 20,132,44
s_waitcnt lgkmcnt(2)
mvv 12,136,48
R 32,59392
mvv 16,140,48
mvv 20,144,48
mvv 12,148,52
R 36,60416
mvv 16,152,52
mvv 20,156,52
s_waitcnt lgkmcnt(2)
s_add_u32 s60, s60, 1
mvv 12,160,24
s_cbranch_scc0 LF1_1
mvv 16,164,24
R 40,61440
mvv 20,168,24
mvv 12,172,28
R 44,62464
mvv 16,176,28
v_add_u32 v76, 1, v76
mvv 20,180,28
s_waitcnt lgkmcnt(2)
ds_write_b64 v89, v[108:109] offset:128
mvv 12,184,32
R 48,63488
mvv 16,188,32
mvv 20,192,32
mvv 12,196,36
R 52,64512
v_xor_b32 v80, 0x10000, v80
mvv 16,200,36
s_waitcnt vmcnt(1) lgkmcnt(2)
s_add_u32 s49, s49, 313344
mvv 20,204,36
s_barrier
mvv 12,208,40
buffer_load_dwordx2 v[108:109], v90, s[36:39], s49 offen sc1
mvv 16,212,40
s_add_u32 m0, s50, 32768
R 24,0
mvv 20,216,40
ds_read_b64 v[78:79], v89 offset:128
mvv 12,220,44
R 28,1024
mvv 16,224,44
buffer_load_dwordx2 v[110:111], v90, s[36:39], s49 offen offset:512 sc1
mvv 20,228,44
s_waitcnt lgkmcnt(3)
mvv 12,232,48
R 32,2048
mvv 16,236,48
mvv 20,240,48
D 85,0
mvv 12,244,52
R 36,3072
mvv 16,248,52
mvv 20,252,52
s_waitcnt lgkmcnt(2)
v_mfma_f32_16x16x32_bf16 v[0:3], a[0:3], v[24:27], 0
D 85,1024
v_mfma_f32_16x16x32_bf16 v[4:7], a[4:7], v[24:27], 0
R 40,4096
v_fma_f32 v104, v79, v56, v92
v_mfma_f32_16x16x32_bf16 v[8:11], a[8:11], v[24:27], 0
v_fma_f32 v12, v12, v78, v104
v_fma_f32 v105, v79, v57, v93
mav 0,12,28
R 44,5120
v_fma_f32 v13, v13, v78, v105
mav 4,16,28
s_waitcnt vmcnt(4)
buffer_store_dword v76, v81, s[36:39], s52 offen sc1
mav 8,20,28
s_waitcnt lgkmcnt(2)
v_mul_f32_e64 v12, v12, |v12|
v_mul_f32_e64 v13, v13, |v13|
mav 0,24,32
R 48,6144
v_cvt_pk_bf16_f32 v12, v12, v13
mav 4,28,32
v_pk_max_i16 v68, v12, 0
v_fma_f32 v104, v79, v58, v94
mav 8,32,32
D 85,2048
mav 0,36,36
R 52,7168
v_fma_f32 v14, v14, v78, v104
mav 4,40,36
v_fma_f32 v105, v79, v59, v95
v_fma_f32 v15, v15, v78, v105
mav 8,44,36
s_waitcnt lgkmcnt(2)
v_mul_f32_e64 v14, v14, |v14|
v_mul_f32_e64 v15, v15, |v15|
mav 0,48,40
D 85,3072
mav 4,52,40
s_add_u32 m0, s50, 36864
R 24,8192
v_cvt_pk_bf16_f32 v14, v14, v15
mav 8,56,40
v_pk_max_i16 v69, v14, 0
v_fma_f32 v104, v79, v60, v96
mav 0,60,44
R 28,9216
v_fma_f32 v16, v16, v78, v104
mav 4,64,44
D 86,0
mav 8,68,44
s_waitcnt lgkmcnt(2)
v_fma_f32 v105, v79, v61, v97
v_fma_f32 v17, v17, v78, v105
mav 0,72,48
R 32,10240
v_mul_f32_e64 v16, v16, |v16|
mav 4,76,48
v_mul_f32_e64 v17, v17, |v17|
v_cvt_pk_bf16_f32 v16, v16, v17
mav 8,80,48
D 86,1024
mav 0,84,52
R 36,11264
v_pk_max_i16 v70, v16, 0
mav 4,88,52
v_fma_f32 v104, v79, v62, v98
v_fma_f32 v18, v18, v78, v104
mav 8,92,52
s_waitcnt lgkmcnt(2)
v_fma_f32 v105, v79, v63, v99
v_fma_f32 v19, v19, v78, v105
mav 0,96,24
D 86,2048
mav 4,100,24
R 40,12288
v_mul_f32_e64 v18, v18, |v18|
mav 8,104,24
v_mul_f32_e64 v19, v19, |v19|
v_cvt_pk_bf16_f32 v18, v18, v19
mav 0,108,28
R 44,13312
v_pk_max_i16 v71, v18, 0
mav 4,112,28
buffer_store_dwordx4 v[68:71], v83, s[36:39], s49 offen sc1
D 86,3072
mav 8,116,28
s_waitcnt lgkmcnt(2)
v_fma_f32 v104, v79, v64, v100
v_fma_f32 v20, v20, v78, v104
mav 0,120,32
R 48,14336
v_fma_f32 v105, v79, v65, v101
mav 4,124,32
v_fma_f32 v21, v21, v78, v105
v_mul_f32_e64 v20, v20, |v20|
mav 8,128,32
v_mul_f32_e64 v21, v21, |v21|
v_cvt_pk_bf16_f32 v20, v20, v21
mav 0,132,36
R 52,15360
v_pk_max_i16 v72, v20, 0
mav 4,136,36
v_pk_max_i16 v74, v20, 0
v_fma_f32 v104, v79, v66, v102
mav 8,140,36
s_waitcnt lgkmcnt(2)
v_fma_f32 v22, v22, v78, v104
v_fma_f32 v105, v79, v67, v103
mav 0,144,40
R 24,16384
v_fma_f32 v23, v23, v78, v105
mav 4,148,40
v_mul_f32_e64 v22, v22, |v22|
v_mul_f32_e64 v23, v23, |v23|
mav 8,152,40
v_cvt_pk_bf16_f32 v22, v22, v23
v_pk_max_i16 v73, v22, 0
mav 0,156,44
R 28,17408
v_pk_max_i16 v75, v22, 0
mav 4,160,44
v_permlane16_swap_b32 v72, v74
mav 8,164,44
s_waitcnt lgkmcnt(2)
v_permlane16_swap_b32 v73, v75
mav 0,168,48
R 32,18432
mav 4,172,48
mav 8,176,48
buffer_store_dwordx4 v[72:75], v84, s[36:39], s49 offen sc1
mav 0,180,52
R 36,19456
mav 4,184,52
mav 8,188,52
s_waitcnt lgkmcnt(2)
mav 0,192,24
R 40,20480
mav 4,196,24
mav 8,200,24
s_waitcnt vmcnt(11)
v_add_f32 v108, v108, v110
v_add_f32 v109, v109, v111
mav 0,204,28
R 44,21504
mav 4,208,28
v_permlane32_swap_b32 v108, v109
mav 8,212,28
s_waitcnt lgkmcnt(2)
v_add_f32 v110, v108, v109
v_add_f32 v111, v108, v109
mav 0,216,32
R 48,22528
mav 4,220,32
v_permlane16_swap_b32 v110, v111
mav 8,224,32
v_add_f32 v108, v110, v111
v_add_f32 v109, v110, v111
mav 0,228,36
R 52,23552
mav 4,232,36
v_permlane32_swap_b32 v108, v109
mav 8,236,36
s_waitcnt lgkmcnt(2)
v_mul_f32 v110, 0x3a800000, v108
v_mul_f32 v111, 0x3a800000, v109
mav 0,240,40
R 24,24576
v_fma_f32 v111, -v110, v110, v111
mav 4,244,40
v_max_f32 v111, 0, v111
v_add_f32 v111, 0x3727c5ac, v111
mav 8,248,40
v_rsq_f32 v108, v111
mav 0,252,44
R 28,25600
v_mul_f32_e64 v109, -v110, v108
mvv 4,128,44
mvv 8,132,44
s_waitcnt lgkmcnt(2)
mvv 0,136,48
R 32,26624
mvv 4,140,48
mvv 8,144,48
mvv 0,148,52
R 36,27648
mvv 4,152,52
mvv 8,156,52
s_waitcnt lgkmcnt(2)
mvv 0,160,24
R 40,28672
mvv 4,164,24
mvv 8,168,24
mvv 0,172,28
R 44,29696
mvv 4,176,28
v_add_u32 v76, 1, v76
mvv 8,180,28
s_waitcnt lgkmcnt(2)
ds_write_b64 v89, v[108:109] offset:0
mvv 0,184,32
R 48,30720
mvv 4,188,32
mvv 8,192,32
mvv 0,196,36
R 52,31744
mvv 4,200,36
s_waitcnt vmcnt(1) lgkmcnt(2)
s_add_u32 s49, s49, 313344
mvv 8,204,36
s_barrier
mvv 0,208,40
buffer_load_dwordx2 v[108:109], v90, s[36:39], s49 offen sc1
mvv 4,212,40
R 24,32768
mvv 8,216,40
ds_read_b64 v[78:79], v89 offset:0
mvv 0,220,44
R 28,33792
mvv 4,224,44
buffer_load_dwordx2 v[110:111], v90, s[36:39], s49 offen offset:512 sc1
mvv 8,228,44
s_waitcnt lgkmcnt(3)
mvv 0,232,48
R 32,34816
mvv 4,236,48
mvv 8,240,48
mvv 0,244,52
R 36,35840
mvv 4,248,52
mvv 8,252,52
s_waitcnt lgkmcnt(2)
v_mfma_f32_16x16x32_bf16 v[12:15], a[0:3], v[24:27], 0
LsF8:
s_cbranch_execz LsF12
R 40,36864
v_fma_f32 v104, v79, v56, v92
v_mfma_f32_16x16x32_bf16 v[16:19], a[4:7], v[24:27], 0
v_fma_f32 v0, v0, v78, v104
v_fma_f32 v105, v79, v57, v93
v_mfma_f32_16x16x32_bf16 v[20:23], a[8:11], v[24:27], 0
v_fma_f32 v1, v1, v78, v105
v_mul_f32_e64 v0, v0, |v0|
mav 12,12,28
R 44,37888
v_mul_f32_e64 v1, v1, |v1|
mav 16,16,28
s_waitcnt vmcnt(2)
buffer_store_dword v76, v81, s[36:39], s52 offen sc1
mav 20,20,28
s_waitcnt lgkmcnt(2)
v_cvt_pk_bf16_f32 v0, v0, v1
v_pk_max_i16 v68, v0, 0
mav 12,24,32
R 48,38912
v_fma_f32 v104, v79, v58, v94
mav 16,28,32
v_fma_f32 v2, v2, v78, v104
v_fma_f32 v105, v79, v59, v95
mav 20,32,32
v_fma_f32 v3, v3, v78, v105
v_mul_f32_e64 v2, v2, |v2|
mav 12,36,36
R 52,39936
v_mul_f32_e64 v3, v3, |v3|
mav 16,40,36
v_cvt_pk_bf16_f32 v2, v2, v3
v_pk_max_i16 v69, v2, 0
mav 20,44,36
s_waitcnt lgkmcnt(2)
v_fma_f32 v104, v79, v60, v96
v_fma_f32 v4, v4, v78, v104
mav 12,48,40
R 24,40960
v_fma_f32 v105, v79, v61, v97
mav 16,52,40
v_fma_f32 v5, v5, v78, v105
v_mul_f32_e64 v4, v4, |v4|
mav 20,56,40
v_mul_f32_e64 v5, v5, |v5|
v_cvt_pk_bf16_f32 v4, v4, v5
mav 12,60,44
R 28,41984
v_pk_max_i16 v70, v4, 0
mav 16,64,44
v_fma_f32 v104, v79, v62, v98
v_fma_f32 v6, v6, v78, v104
mav 20,68,44
s_waitcnt lgkmcnt(2)
v_fma_f32 v105, v79, v63, v99
v_fma_f32 v7, v7, v78, v105
mav 12,72,48
R 32,43008
v_mul_f32_e64 v6, v6, |v6|
mav 16,76,48
v_mul_f32_e64 v7, v7, |v7|
v_cvt_pk_bf16_f32 v6, v6, v7
mav 20,80,48
v_pk_max_i16 v71, v6, 0
v_fma_f32 v104, v79, v64, v100
mav 12,84,52
R 36,44032
v_fma_f32 v8, v8, v78, v104
mav 16,88,52
v_fma_f32 v105, v79, v65, v101
v_fma_f32 v9, v9, v78, v105
mav 20,92,52
s_waitcnt lgkmcnt(2)
v_mul_f32_e64 v8, v8, |v8|
v_mul_f32_e64 v9, v9, |v9|
mav 12,96,24
buffer_store_dwordx4 v[68:71], v83, s[36:39], s49 offen sc1
R 40,45056
v_cvt_pk_bf16_f32 v8, v8, v9
mav 16,100,24
v_pk_max_i16 v72, v8, 0
v_pk_max_i16 v74, v8, 0
mav 20,104,24
v_fma_f32 v104, v79, v66, v102
v_fma_f32 v10, v10, v78, v104
mav 12,108,28
R 44,46080
v_fma_f32 v105, v79, v67, v103
mav 16,112,28
v_fma_f32 v11, v11, v78, v105
v_mul_f32_e64 v10, v10, |v10|
mav 20,116,28
s_waitcnt lgkmcnt(2)
v_mul_f32_e64 v11, v11, |v11|
v_cvt_pk_bf16_f32 v10, v10, v11
mav 12,120,32
R 48,47104
v_pk_max_i16 v73, v10, 0
mav 16,124,32
v_pk_max_i16 v75, v10, 0
mav 20,128,32
v_permlane16_swap_b32 v72, v74
mav 12,132,36
R 52,48128
mav 16,136,36
v_permlane16_swap_b32 v73, v75
mav 20,140,36
s_waitcnt lgkmcnt(2)
mav 12,144,40
buffer_store_dwordx4 v[72:75], v84, s[36:39], s49 offen sc1
R 24,49152
mav 16,148,40
mav 20,152,40
mav 12,156,44
R 28,50176
mav 16,160,44
mav 20,164,44
s_waitcnt lgkmcnt(2)
mav 12,168,48
R 32,51200
mav 16,172,48
mav 20,176,48
mav 12,180,52
R 36,52224
mav 16,184,52
mav 20,188,52
s_waitcnt lgkmcnt(2)
mav 12,192,24
R 40,53248
mav 16,196,24
mav 20,200,24
s_waitcnt vmcnt(3)
v_add_f32 v108, v108, v110
v_add_f32 v109, v109, v111
mav 12,204,28
R 44,54272
mav 16,208,28
v_permlane32_swap_b32 v108, v109
mav 20,212,28
s_waitcnt lgkmcnt(2)
v_add_f32 v110, v108, v109
v_add_f32 v111, v108, v109
mav 12,216,32
R 48,55296
mav 16,220,32
v_permlane16_swap_b32 v110, v111
mav 20,224,32
v_add_f32 v108, v110, v111
v_add_f32 v109, v110, v111
mav 12,228,36
R 52,56320
mav 16,232,36
v_permlane32_swap_b32 v108, v109
mav 20,236,36
s_waitcnt lgkmcnt(2)
v_mul_f32 v110, 0x3a800000, v108
v_mul_f32 v111, 0x3a800000, v109
mav 12,240,40
R 24,57344
v_fma_f32 v111, -v110, v110, v111
mav 16,244,40
v_max_f32 v111, 0, v111
v_add_f32 v111, 0x3727c5ac, v111
mav 20,248,40
v_rsq_f32 v108, v111
mav 12,252,44
R 28,58368
v_mul_f32_e64 v109, -v110, v108
mvv 16,128,44
mvv 20,132,44
s_waitcnt lgkmcnt(2)
mvv 12,136,48
R 32,59392
mvv 16,140,48
mvv 20,144,48
mvv 12,148,52
R 36,60416
mvv 16,152,52
mvv 20,156,52
s_waitcnt lgkmcnt(2)
mvv 12,160,24
mvv 16,164,24
R 40,61440
mvv 20,168,24
mvv 12,172,28
R 44,62464
mvv 16,176,28
v_add_u32 v76, 1, v76
mvv 20,180,28
s_waitcnt lgkmcnt(2)
ds_write_b64 v89, v[108:109] offset:128
mvv 12,184,32
R 48,63488
mvv 16,188,32
mvv 20,192,32
mvv 12,196,36
R 52,64512
mvv 16,200,36
s_waitcnt lgkmcnt(2)
s_add_u32 s49, s49, 313344
mvv 20,204,36
s_barrier
mvv 12,208,40
mvv 16,212,40
ds_read_b64 v[78:79], v89 offset:128
mvv 20,216,40
mvv 12,220,44
mvv 16,224,44
mvv 20,228,44
s_waitcnt lgkmcnt(1)
mvv 12,232,48
mvv 16,236,48
mvv 20,240,48
mvv 12,244,52
mvv 16,248,52
mvv 20,252,52
s_waitcnt lgkmcnt(0)
v_fma_f32 v104, v79, v56, v92
s_nop 3
v_fma_f32 v12, v12, v78, v104
v_fma_f32 v105, v79, v57, v93
v_fma_f32 v13, v13, v78, v105
v_mul_f32_e64 v12, v12, |v12|
v_mul_f32_e64 v13, v13, |v13|
v_cvt_pk_bf16_f32 v12, v12, v13
v_pk_max_i16 v68, v12, 0
v_fma_f32 v104, v79, v58, v94
v_fma_f32 v14, v14, v78, v104
v_fma_f32 v105, v79, v59, v95
v_fma_f32 v15, v15, v78, v105
v_mul_f32_e64 v14, v14, |v14|
v_mul_f32_e64 v15, v15, |v15|
v_cvt_pk_bf16_f32 v14, v14, v15
v_pk_max_i16 v69, v14, 0
v_fma_f32 v104, v79, v60, v96
v_fma_f32 v16, v16, v78, v104
v_fma_f32 v105, v79, v61, v97
v_fma_f32 v17, v17, v78, v105
v_mul_f32_e64 v16, v16, |v16|
v_mul_f32_e64 v17, v17, |v17|
v_cvt_pk_bf16_f32 v16, v16, v17
v_pk_max_i16 v70, v16, 0
v_fma_f32 v104, v79, v62, v98
v_fma_f32 v18, v18, v78, v104
v_fma_f32 v105, v79, v63, v99
v_fma_f32 v19, v19, v78, v105
v_mul_f32_e64 v18, v18, |v18|
v_mul_f32_e64 v19, v19, |v19|
v_cvt_pk_bf16_f32 v18, v18, v19
v_pk_max_i16 v71, v18, 0
buffer_store_dwordx4 v[68:71], v83, s[36:39], s49 offen sc1
v_fma_f32 v104, v79, v64, v100
v_fma_f32 v20, v20, v78, v104
v_fma_f32 v105, v79, v65, v101
v_fma_f32 v21, v21, v78, v105
v_mul_f32_e64 v20, v20, |v20|
v_mul_f32_e64 v21, v21, |v21|
v_cvt_pk_bf16_f32 v20, v20, v21
v_pk_max_i16 v72, v20, 0
v_pk_max_i16 v74, v20, 0
v_fma_f32 v104, v79, v66, v102
v_fma_f32 v22, v22, v78, v104
v_fma_f32 v105, v79, v67, v103
v_fma_f32 v23, v23, v78, v105
v_mul_f32_e64 v22, v22, |v22|
v_mul_f32_e64 v23, v23, |v23|
v_cvt_pk_bf16_f32 v22, v22, v23
v_pk_max_i16 v73, v22, 0
v_pk_max_i16 v75, v22, 0
v_permlane16_swap_b32 v72, v74
s_nop 0
v_permlane16_swap_b32 v73, v75
buffer_store_dwordx4 v[72:75], v84, s[36:39], s49 offen sc1
s_waitcnt vmcnt(2)
buffer_store_dword v76, v81, s[36:39], s52 offen sc1
v_add_u32 v76, 37, v76
s_waitcnt vmcnt(1)
buffer_store_dword v76, v81, s[36:39], s52 offen sc1
s_endpgm
LvF2:
LsF9:
s_cbranch_execz LsF13
s_add_u32 s49, s49, 313344
s_barrier
s_add_u32 m0, s50, 0
s_nop 0
D 85,0
D 85,1024
D 85,2048
D 85,3072
s_add_u32 m0, s50, 4096
s_nop 0
D 86,0
D 86,1024
D 86,2048
D 86,3072
s_waitcnt vmcnt(0)
s_add_u32 s49, s49, 313344
s_barrier
s_add_u32 m0, s50, 32768
R 24,0
D 85,0
R 28,1024
D 85,1024
R 32,2048
R 36,3072
D 85,2048
s_waitcnt lgkmcnt(2)
v_mfma_f32_16x16x32_bf16 v[0:3], a[0:3], v[24:27], 0
R 40,4096
v_mfma_f32_16x16x32_bf16 v[4:7], a[4:7], v[24:27], 0
v_mfma_f32_16x16x32_bf16 v[8:11], a[8:11], v[24:27], 0
D 85,3072
mav 0,12,28
s_add_u32 m0, s50, 36864
R 44,5120
mav 4,16,28
mav 8,20,28
s_waitcnt lgkmcnt(2)
mav 0,24,32
D 86,0
mav 4,28,32
R 48,6144
mav 8,32,32
mav 0,36,36
R 52,7168
mav 4,40,36
D 86,1024
mav 8,44,36
s_waitcnt lgkmcnt(2)
mav 0,48,40
R 24,8192
mav 4,52,40
mav 8,56,40
D 86,2048
mav 0,60,44
R 28,9216
mav 4,64,44
mav 8,68,44
s_waitcnt lgkmcnt(2)
mav 0,72,48
D 86,3072
mav 4,76,48
R 32,10240
mav 8,80,48
mav 0,84,52
R 36,11264
mav 4,88,52
mav 8,92,52
s_waitcnt lgkmcnt(2)
mav 0,96,24
R 40,12288
mav 4,100,24
mav 8,104,24
mav 0,108,28
R 44,13312
mav 4,112,28
mav 8,116,28
s_waitcnt lgkmcnt(2)
mav 0,120,32
R 48,14336
mav 4,124,32
mav 8,128,32
mav 0,132,36
R 52,15360
mav 4,136,36
mav 8,140,36
s_waitcnt lgkmcnt(2)
mav 0,144,40
R 24,16384
mav 4,148,40
mav 8,152,40
mav 0,156,44
R 28,17408
mav 4,160,44
mav 8,164,44
s_waitcnt lgkmcnt(2)
mav 0,168,48
R 32,18432
mav 4,172,48
mav 8,176,48
mav 0,180,52
R 36,19456
mav 4,184,52
mav 8,188,52
s_waitcnt lgkmcnt(2)
mav 0,192,24
R 40,20480
mav 4,196,24
mav 8,200,24
mav 0,204,28
R 44,21504
mav 4,208,28
mav 8,212,28
s_waitcnt lgkmcnt(2)
mav 0,216,32
R 48,22528
mav 4,220,32
mav 8,224,32
mav 0,228,36
R 52,23552
mav 4,232,36
mav 8,236,36
s_waitcnt lgkmcnt(2)
mav 0,240,40
R 24,24576
mav 4,244,40
mav 8,248,40
mav 0,252,44
R 28,25600
mvv 4,128,44
mvv 8,132,44
s_waitcnt lgkmcnt(2)
mvv 0,136,48
R 32,26624
mvv 4,140,48
mvv 8,144,48
mvv 0,148,52
R 36,27648
mvv 4,152,52
mvv 8,156,52
s_waitcnt lgkmcnt(2)
mvv 0,160,24
R 40,28672
mvv 4,164,24
mvv 8,168,24
mvv 0,172,28
R 44,29696
mvv 4,176,28
mvv 8,180,28
s_waitcnt lgkmcnt(2)
mvv 0,184,32
s_xor_b32 s50, s50, 0x10000
R 48,30720
mvv 4,188,32
mvv 8,192,32
mvv 0,196,36
R 52,31744
mvv 4,200,36
s_waitcnt vmcnt(0) lgkmcnt(2)
s_add_u32 s49, s49, 313344
mvv 8,204,36
s_barrier
mvv 0,208,40
s_add_u32 m0, s50, 0
R 24,32768
mvv 4,212,40
ds_read_b64 v[78:79], v89 offset:0
mvv 8,216,40
D 85,0
mvv 0,220,44
R 28,33792
mvv 4,224,44
mvv 8,228,44
s_waitcnt lgkmcnt(3)
mvv 0,232,48
D 85,1024
mvv 4,236,48
R 32,34816
mvv 8,240,48
mvv 0,244,52
R 36,35840
mvv 4,248,52
D 85,2048
mvv 8,252,52
s_waitcnt lgkmcnt(2)
v_mfma_f32_16x16x32_bf16 v[12:15], a[0:3], v[24:27], 0
R 40,36864
v_fma_f32 v104, v79, v56, v92
v_mfma_f32_16x16x32_bf16 v[16:19], a[4:7], v[24:27], 0
v_fma_f32 v0, v0, v78, v104
v_fma_f32 v105, v79, v57, v93
v_mfma_f32_16x16x32_bf16 v[20:23], a[8:11], v[24:27], 0
D 85,3072
mav 12,12,28
s_add_u32 m0, s50, 4096
R 44,37888
v_fma_f32 v1, v1, v78, v105
mav 16,16,28
v_mul_f32_e64 v0, v0, |v0|
v_mul_f32_e64 v1, v1, |v1|
mav 20,20,28
s_waitcnt lgkmcnt(2)
v_cvt_pk_bf16_f32 v0, v0, v1
v_pk_max_i16 v68, v0, 0
mav 12,24,32
D 86,0
mav 16,28,32
R 48,38912
v_fma_f32 v104, v79, v58, v94
mav 20,32,32
v_fma_f32 v2, v2, v78, v104
v_fma_f32 v105, v79, v59, v95
mav 12,36,36
R 52,39936
v_fma_f32 v3, v3, v78, v105
mav 16,40,36
D 86,1024
mav 20,44,36
s_waitcnt lgkmcnt(2)
v_mul_f32_e64 v2, v2, |v2|
v_mul_f32_e64 v3, v3, |v3|
mav 12,48,40
R 24,40960
v_cvt_pk_bf16_f32 v2, v2, v3
mav 16,52,40
v_pk_max_i16 v69, v2, 0
v_fma_f32 v104, v79, v60, v96
mav 20,56,40
D 86,2048
mav 12,60,44
R 28,41984
v_fma_f32 v4, v4, v78, v104
mav 16,64,44
v_fma_f32 v105, v79, v61, v97
v_fma_f32 v5, v5, v78, v105
mav 20,68,44
s_waitcnt lgkmcnt(2)
v_mul_f32_e64 v4, v4, |v4|
v_mul_f32_e64 v5, v5, |v5|
mav 12,72,48
D 86,3072
mav 16,76,48
R 32,43008
v_cvt_pk_bf16_f32 v4, v4, v5
mav 20,80,48
v_pk_max_i16 v70, v4, 0
v_fma_f32 v104, v79, v62, v98
mav 12,84,52
R 36,44032
v_fma_f32 v6, v6, v78, v104
mav 16,88,52
v_fma_f32 v105, v79, v63, v99
v_fma_f32 v7, v7, v78, v105
mav 20,92,52
s_waitcnt lgkmcnt(2)
v_mul_f32_e64 v6, v6, |v6|
v_mul_f32_e64 v7, v7, |v7|
mav 12,96,24
R 40,45056
v_cvt_pk_bf16_f32 v6, v6, v7
mav 16,100,24
v_pk_max_i16 v71, v6, 0
v_fma_f32 v104, v79, v64, v100
mav 20,104,24
buffer_store_dwordx4 v[68:71], v83, s[36:39], s49 offen sc1
v_fma_f32 v8, v8, v78, v104
v_fma_f32 v105, v79, v65, v101
v_fma_f32 v9, v9, v78, v105
LsF10:
s_cbranch_execz LsF14
mav 12,108,28
R 44,46080
v_mul_f32_e64 v8, v8, |v8|
mav 16,112,28
v_mul_f32_e64 v9, v9, |v9|
v_cvt_pk_bf16_f32 v8, v8, v9
mav 20,116,28
s_waitcnt lgkmcnt(2)
v_pk_max_i16 v72, v8, 0
v_pk_max_i16 v74, v8, 0
mav 12,120,32
R 48,47104
v_fma_f32 v104, v79, v66, v102
mav 16,124,32
v_fma_f32 v10, v10, v78, v104
v_fma_f32 v105, v79, v67, v103
mav 20,128,32
v_fma_f32 v11, v11, v78, v105
v_mul_f32_e64 v10, v10, |v10|
mav 12,132,36
R 52,48128
v_mul_f32_e64 v11, v11, |v11|
mav 16,136,36
v_cvt_pk_bf16_f32 v10, v10, v11
v_pk_max_i16 v73, v10, 0
mav 20,140,36
s_waitcnt lgkmcnt(2)
v_pk_max_i16 v75, v10, 0
mav 12,144,40
R 24,49152
mav 16,148,40
v_permlane16_swap_b32 v72, v74
mav 20,152,40
v_permlane16_swap_b32 v73, v75
mav 12,156,44
R 28,50176
mav 16,160,44
mav 20,164,44
s_waitcnt lgkmcnt(2)
mav 12,168,48
buffer_store_dwordx4 v[72:75], v84, s[36:39], s49 offen sc1
R 32,51200
mav 16,172,48
mav 20,176,48
mav 12,180,52
R 36,52224
mav 16,184,52
mav 20,188,52
s_waitcnt lgkmcnt(2)
mav 12,192,24
R 40,53248
mav 16,196,24
mav 20,200,24
mav 12,204,28
R 44,54272
mav 16,208,28
mav 20,212,28
s_waitcnt lgkmcnt(2)
mav 12,216,32
R 48,55296
mav 16,220,32
mav 20,224,32
mav 12,228,36
R 52,56320
mav 16,232,36
mav 20,236,36
s_waitcnt lgkmcnt(2)
mav 12,240,40
R 24,57344
mav 16,244,40
mav 20,248,40
mav 12,252,44
R 28,58368
mvv 16,128,44
mvv 20,132,44
s_waitcnt lgkmcnt(2)
mvv 12,136,48
R 32,59392
mvv 16,140,48
mvv 20,144,48
mvv 12,148,52
R 36,60416
mvv 16,152,52
mvv 20,156,52
s_waitcnt lgkmcnt(2)
mvv 12,160,24
s_mov_b32 s60, -30
.p2align 6
LF2_1:
mvv 16,164,24
R 40,61440
mvv 20,168,24
mvv 12,172,28
R 44,62464
mvv 16,176,28
v_add_u32 v76, 1, v76
mvv 20,180,28
s_waitcnt lgkmcnt(2)
mvv 12,184,32
R 48,63488
mvv 16,188,32
mvv 20,192,32
mvv 12,196,36
R 52,64512
v_xor_b32 v80, 0x10000, v80
mvv 16,200,36
s_waitcnt vmcnt(2) lgkmcnt(2)
s_add_u32 s49, s49, 313344
mvv 20,204,36
s_barrier
mvv 12,208,40
s_add_u32 m0, s50, 32768
R 24,0
mvv 16,212,40
ds_read_b64 v[78:79], v89 offset:128
mvv 20,216,40
D 85,0
mvv 12,220,44
R 28,1024
mvv 16,224,44
mvv 20,228,44
s_waitcnt lgkmcnt(3)
mvv 12,232,48
D 85,1024
mvv 16,236,48
R 32,2048
mvv 20,240,48
mvv 12,244,52
R 36,3072
mvv 16,248,52
D 85,2048
mvv 20,252,52
s_waitcnt lgkmcnt(2)
v_mfma_f32_16x16x32_bf16 v[0:3], a[0:3], v[24:27], 0
R 40,4096
v_fma_f32 v104, v79, v56, v92
v_mfma_f32_16x16x32_bf16 v[4:7], a[4:7], v[24:27], 0
v_fma_f32 v12, v12, v78, v104
v_fma_f32 v105, v79, v57, v93
v_mfma_f32_16x16x32_bf16 v[8:11], a[8:11], v[24:27], 0
D 85,3072
mav 0,12,28
s_add_u32 m0, s50, 36864
R 44,5120
v_fma_f32 v13, v13, v78, v105
mav 4,16,28
s_waitcnt vmcnt(4)
v_mul_f32_e64 v12, v12, |v12|
v_mul_f32_e64 v13, v13, |v13|
mav 8,20,28
s_waitcnt lgkmcnt(2)
v_cvt_pk_bf16_f32 v12, v12, v13
v_pk_max_i16 v68, v12, 0
mav 0,24,32
buffer_store_dword v76, v81, s[36:39], s52 offen sc1
mav 4,28,32
R 48,6144
v_fma_f32 v104, v79, v58, v94
mav 8,32,32
v_fma_f32 v14, v14, v78, v104
v_fma_f32 v105, v79, v59, v95
mav 0,36,36
R 52,7168
v_fma_f32 v15, v15, v78, v105
mav 4,40,36
D 86,0
mav 8,44,36
s_waitcnt lgkmcnt(2)
v_mul_f32_e64 v14, v14, |v14|
v_mul_f32_e64 v15, v15, |v15|
mav 0,48,40
R 24,8192
v_cvt_pk_bf16_f32 v14, v14, v15
mav 4,52,40
v_pk_max_i16 v69, v14, 0
v_fma_f32 v104, v79, v60, v96
mav 8,56,40
D 86,1024
mav 0,60,44
R 28,9216
v_fma_f32 v16, v16, v78, v104
mav 4,64,44
v_fma_f32 v105, v79, v61, v97
v_fma_f32 v17, v17, v78, v105
mav 8,68,44
s_waitcnt lgkmcnt(2)
v_mul_f32_e64 v16, v16, |v16|
v_mul_f32_e64 v17, v17, |v17|
mav 0,72,48
D 86,2048
mav 4,76,48
R 32,10240
v_cvt_pk_bf16_f32 v16, v16, v17
mav 8,80,48
v_pk_max_i16 v70, v16, 0
v_fma_f32 v104, v79, v62, v98
mav 0,84,52
R 36,11264
v_fma_f32 v18, v18, v78, v104
mav 4,88,52
D 86,3072
mav 8,92,52
s_waitcnt lgkmcnt(2)
v_fma_f32 v105, v79, v63, v99
v_fma_f32 v19, v19, v78, v105
mav 0,96,24
R 40,12288
v_mul_f32_e64 v18, v18, |v18|
mav 4,100,24
v_mul_f32_e64 v19, v19, |v19|
v_cvt_pk_bf16_f32 v18, v18, v19
mav 8,104,24
v_pk_max_i16 v71, v18, 0
v_fma_f32 v104, v79, v64, v100
mav 0,108,28
R 44,13312
v_fma_f32 v20, v20, v78, v104
mav 4,112,28
v_fma_f32 v105, v79, v65, v101
v_fma_f32 v21, v21, v78, v105
mav 8,116,28
s_waitcnt lgkmcnt(2)
v_mul_f32_e64 v20, v20, |v20|
v_mul_f32_e64 v21, v21, |v21|
mav 0,120,32
buffer_store_dwordx4 v[68:71], v83, s[36:39], s49 offen sc1
R 48,14336
v_cvt_pk_bf16_f32 v20, v20, v21
mav 4,124,32
v_pk_max_i16 v72, v20, 0
v_pk_max_i16 v74, v20, 0
mav 8,128,32
v_fma_f32 v104, v79, v66, v102
v_fma_f32 v22, v22, v78, v104
mav 0,132,36
R 52,15360
v_fma_f32 v105, v79, v67, v103
mav 4,136,36
v_fma_f32 v23, v23, v78, v105
v_mul_f32_e64 v22, v22, |v22|
mav 8,140,36
s_waitcnt lgkmcnt(2)
v_mul_f32_e64 v23, v23, |v23|
v_cvt_pk_bf16_f32 v22, v22, v23
mav 0,144,40
R 24,16384
v_pk_max_i16 v73, v22, 0
mav 4,148,40
v_pk_max_i16 v75, v22, 0
mav 8,152,40
v_permlane16_swap_b32 v72, v74
mav 0,156,44
R 28,17408
mav 4,160,44
v_permlane16_swap_b32 v73, v75
mav 8,164,44
s_waitcnt lgkmcnt(2)
mav 0,168,48
buffer_store_dwordx4 v[72:75], v84, s[36:39], s49 offen sc1
R 32,18432
mav 4,172,48
mav 8,176,48
mav 0,180,52
R 36,19456
mav 4,184,52
mav 8,188,52
s_waitcnt lgkmcnt(2)
mav 0,192,24
R 40,20480
mav 4,196,24
mav 8,200,24
mav 0,204,28
R 44,21504
mav 4,208,28
mav 8,212,28
s_waitcnt lgkmcnt(2)
mav 0,216,32
R 48,22528
mav 4,220,32
mav 8,224,32
mav 0,228,36
R 52,23552
mav 4,232,36
mav 8,236,36
s_waitcnt lgkmcnt(2)
mav 0,240,40
R 24,24576
mav 4,244,40
mav 8,248,40
mav 0,252,44
R 28,25600
mvv 4,128,44
mvv 8,132,44
s_waitcnt lgkmcnt(2)
mvv 0,136,48
R 32,26624
mvv 4,140,48
mvv 8,144,48
mvv 0,148,52
R 36,27648
mvv 4,152,52
mvv 8,156,52
s_waitcnt lgkmcnt(2)
mvv 0,160,24
R 40,28672
mvv 4,164,24
mvv 8,168,24
mvv 0,172,28
R 44,29696
mvv 4,176,28
v_add_u32 v76, 1, v76
mvv 8,180,28
s_waitcnt lgkmcnt(2)
mvv 0,184,32
s_xor_b32 s50, s50, 0x10000
R 48,30720
mvv 4,188,32
mvv 8,192,32
mvv 0,196,36
R 52,31744
mvv 4,200,36
s_waitcnt vmcnt(2) lgkmcnt(2)
s_add_u32 s49, s49, 313344
mvv 8,204,36
s_barrier
mvv 0,208,40
s_add_u32 m0, s50, 0
R 24,32768
mvv 4,212,40
ds_read_b64 v[78:79], v89 offset:0
mvv 8,216,40
D 85,0
mvv 0,220,44
R 28,33792
mvv 4,224,44
mvv 8,228,44
s_waitcnt lgkmcnt(3)
mvv 0,232,48
D 85,1024
mvv 4,236,48
R 32,34816
mvv 8,240,48
mvv 0,244,52
R 36,35840
mvv 4,248,52
D 85,2048
mvv 8,252,52
s_waitcnt lgkmcnt(2)
v_mfma_f32_16x16x32_bf16 v[12:15], a[0:3], v[24:27], 0
R 40,36864
v_fma_f32 v104, v79, v56, v92
v_mfma_f32_16x16x32_bf16 v[16:19], a[4:7], v[24:27], 0
v_fma_f32 v0, v0, v78, v104
v_fma_f32 v105, v79, v57, v93
v_mfma_f32_16x16x32_bf16 v[20:23], a[8:11], v[24:27], 0
D 85,3072
mav 12,12,28
s_add_u32 m0, s50, 4096
R 44,37888
v_fma_f32 v1, v1, v78, v105
mav 16,16,28
s_waitcnt vmcnt(4)
v_mul_f32_e64 v0, v0, |v0|
v_mul_f32_e64 v1, v1, |v1|
mav 20,20,28
s_waitcnt lgkmcnt(2)
v_cvt_pk_bf16_f32 v0, v0, v1
v_pk_max_i16 v68, v0, 0
mav 12,24,32
buffer_store_dword v76, v81, s[36:39], s52 offen sc1
mav 16,28,32
R 48,38912
v_fma_f32 v104, v79, v58, v94
mav 20,32,32
v_fma_f32 v2, v2, v78, v104
v_fma_f32 v105, v79, v59, v95
mav 12,36,36
R 52,39936
v_fma_f32 v3, v3, v78, v105
mav 16,40,36
D 86,0
mav 20,44,36
s_waitcnt lgkmcnt(2)
v_mul_f32_e64 v2, v2, |v2|
v_mul_f32_e64 v3, v3, |v3|
mav 12,48,40
R 24,40960
v_cvt_pk_bf16_f32 v2, v2, v3
mav 16,52,40
v_pk_max_i16 v69, v2, 0
v_fma_f32 v104, v79, v60, v96
mav 20,56,40
D 86,1024
mav 12,60,44
R 28,41984
v_fma_f32 v4, v4, v78, v104
mav 16,64,44
v_fma_f32 v105, v79, v61, v97
v_fma_f32 v5, v5, v78, v105
mav 20,68,44
s_waitcnt lgkmcnt(2)
v_mul_f32_e64 v4, v4, |v4|
v_mul_f32_e64 v5, v5, |v5|
mav 12,72,48
D 86,2048
mav 16,76,48
R 32,43008
v_cvt_pk_bf16_f32 v4, v4, v5
mav 20,80,48
v_pk_max_i16 v70, v4, 0
v_fma_f32 v104, v79, v62, v98
mav 12,84,52
R 36,44032
v_fma_f32 v6, v6, v78, v104
mav 16,88,52
D 86,3072
mav 20,92,52
s_waitcnt lgkmcnt(2)
v_fma_f32 v105, v79, v63, v99
v_fma_f32 v7, v7, v78, v105
mav 12,96,24
R 40,45056
v_mul_f32_e64 v6, v6, |v6|
mav 16,100,24
v_mul_f32_e64 v7, v7, |v7|
v_cvt_pk_bf16_f32 v6, v6, v7
mav 20,104,24
v_pk_max_i16 v71, v6, 0
v_fma_f32 v104, v79, v64, v100
mav 12,108,28
R 44,46080
v_fma_f32 v8, v8, v78, v104
mav 16,112,28
v_fma_f32 v105, v79, v65, v101
v_fma_f32 v9, v9, v78, v105
mav 20,116,28
s_waitcnt lgkmcnt(2)
v_mul_f32_e64 v8, v8, |v8|
v_mul_f32_e64 v9, v9, |v9|
mav 12,120,32
buffer_store_dwordx4 v[68:71], v83, s[36:39], s49 offen sc1
R 48,47104
v_cvt_pk_bf16_f32 v8, v8, v9
mav 16,124,32
v_pk_max_i16 v72, v8, 0
v_pk_max_i16 v74, v8, 0
mav 20,128,32
v_fma_f32 v104, v79, v66, v102
v_fma_f32 v10, v10, v78, v104
mav 12,132,36
R 52,48128
v_fma_f32 v105, v79, v67, v103
mav 16,136,36
v_fma_f32 v11, v11, v78, v105
v_mul_f32_e64 v10, v10, |v10|
mav 20,140,36
s_waitcnt lgkmcnt(2)
v_mul_f32_e64 v11, v11, |v11|
v_cvt_pk_bf16_f32 v10, v10, v11
mav 12,144,40
R 24,49152
v_pk_max_i16 v73, v10, 0
mav 16,148,40
v_pk_max_i16 v75, v10, 0
mav 20,152,40
v_permlane16_swap_b32 v72, v74
mav 12,156,44
R 28,50176
mav 16,160,44
v_permlane16_swap_b32 v73, v75
mav 20,164,44
s_waitcnt lgkmcnt(2)
mav 12,168,48
buffer_store_dwordx4 v[72:75], v84, s[36:39], s49 offen sc1
R 32,51200
mav 16,172,48
mav 20,176,48
mav 12,180,52
R 36,52224
mav 16,184,52
mav 20,188,52
s_waitcnt lgkmcnt(2)
mav 12,192,24
R 40,53248
mav 16,196,24
mav 20,200,24
mav 12,204,28
R 44,54272
mav 16,208,28
mav 20,212,28
s_waitcnt lgkmcnt(2)
mav 12,216,32
R 48,55296
mav 16,220,32
mav 20,224,32
mav 12,228,36
R 52,56320
mav 16,232,36
mav 20,236,36
s_waitcnt lgkmcnt(2)
mav 12,240,40
R 24,57344
mav 16,244,40
mav 20,248,40
mav 12,252,44
R 28,58368
mvv 16,128,44
mvv 20,132,44
s_waitcnt lgkmcnt(2)
mvv 12,136,48
R 32,59392
mvv 16,140,48
mvv 20,144,48
mvv 12,148,52
R 36,60416
mvv 16,152,52
mvv 20,156,52
s_waitcnt lgkmcnt(2)
s_add_u32 s60, s60, 1
mvv 12,160,24
s_cbranch_scc0 LF2_1
mvv 16,164,24
R 40,61440
mvv 20,168,24
mvv 12,172,28
R 44,62464
mvv 16,176,28
v_add_u32 v76, 1, v76
mvv 20,180,28
s_waitcnt lgkmcnt(2)
mvv 12,184,32
R 48,63488
mvv 16,188,32
mvv 20,192,32
mvv 12,196,36
R 52,64512
v_xor_b32 v80, 0x10000, v80
LsF11:
s_cbranch_execz LsF15
mvv 16,200,36
s_waitcnt vmcnt(2) lgkmcnt(2)
s_add_u32 s49, s49, 313344
mvv 20,204,36
s_barrier
mvv 12,208,40
s_add_u32 m0, s50, 32768
R 24,0
mvv 16,212,40
ds_read_b64 v[78:79], v89 offset:128
mvv 20,216,40
D 85,0
mvv 12,220,44
R 28,1024
mvv 16,224,44
mvv 20,228,44
s_waitcnt lgkmcnt(3)
mvv 12,232,48
D 85,1024
mvv 16,236,48
R 32,2048
mvv 20,240,48
mvv 12,244,52
R 36,3072
mvv 16,248,52
D 85,2048
mvv 20,252,52
s_waitcnt lgkmcnt(2)
v_mfma_f32_16x16x32_bf16 v[0:3], a[0:3], v[24:27], 0
R 40,4096
v_fma_f32 v104, v79, v56, v92
v_mfma_f32_16x16x32_bf16 v[4:7], a[4:7], v[24:27], 0
v_fma_f32 v12, v12, v78, v104
v_fma_f32 v105, v79, v57, v93
v_mfma_f32_16x16x32_bf16 v[8:11], a[8:11], v[24:27], 0
D 85,3072
mav 0,12,28
s_add_u32 m0, s50, 36864
R 44,5120
v_fma_f32 v13, v13, v78, v105
mav 4,16,28
s_waitcnt vmcnt(4)
v_mul_f32_e64 v12, v12, |v12|
v_mul_f32_e64 v13, v13, |v13|
mav 8,20,28
s_waitcnt lgkmcnt(2)
v_cvt_pk_bf16_f32 v12, v12, v13
v_pk_max_i16 v68, v12, 0
mav 0,24,32
buffer_store_dword v76, v81, s[36:39], s52 offen sc1
mav 4,28,32
R 48,6144
v_fma_f32 v104, v79, v58, v94
mav 8,32,32
v_fma_f32 v14, v14, v78, v104
v_fma_f32 v105, v79, v59, v95
mav 0,36,36
R 52,7168
v_fma_f32 v15, v15, v78, v105
mav 4,40,36
D 86,0
mav 8,44,36
s_waitcnt lgkmcnt(2)
v_mul_f32_e64 v14, v14, |v14|
v_mul_f32_e64 v15, v15, |v15|
mav 0,48,40
R 24,8192
v_cvt_pk_bf16_f32 v14, v14, v15
mav 4,52,40
v_pk_max_i16 v69, v14, 0
v_fma_f32 v104, v79, v60, v96
mav 8,56,40
D 86,1024
mav 0,60,44
R 28,9216
v_fma_f32 v16, v16, v78, v104
mav 4,64,44
v_fma_f32 v105, v79, v61, v97
v_fma_f32 v17, v17, v78, v105
mav 8,68,44
s_waitcnt lgkmcnt(2)
v_mul_f32_e64 v16, v16, |v16|
v_mul_f32_e64 v17, v17, |v17|
mav 0,72,48
D 86,2048
mav 4,76,48
R 32,10240
v_cvt_pk_bf16_f32 v16, v16, v17
mav 8,80,48
v_pk_max_i16 v70, v16, 0
v_fma_f32 v104, v79, v62, v98
mav 0,84,52
R 36,11264
v_fma_f32 v18, v18, v78, v104
mav 4,88,52
D 86,3072
mav 8,92,52
s_waitcnt lgkmcnt(2)
v_fma_f32 v105, v79, v63, v99
v_fma_f32 v19, v19, v78, v105
mav 0,96,24
R 40,12288
v_mul_f32_e64 v18, v18, |v18|
mav 4,100,24
v_mul_f32_e64 v19, v19, |v19|
v_cvt_pk_bf16_f32 v18, v18, v19
mav 8,104,24
v_pk_max_i16 v71, v18, 0
v_fma_f32 v104, v79, v64, v100
mav 0,108,28
R 44,13312
v_fma_f32 v20, v20, v78, v104
mav 4,112,28
v_fma_f32 v105, v79, v65, v101
v_fma_f32 v21, v21, v78, v105
mav 8,116,28
s_waitcnt lgkmcnt(2)
v_mul_f32_e64 v20, v20, |v20|
v_mul_f32_e64 v21, v21, |v21|
mav 0,120,32
buffer_store_dwordx4 v[68:71], v83, s[36:39], s49 offen sc1
R 48,14336
v_cvt_pk_bf16_f32 v20, v20, v21
mav 4,124,32
v_pk_max_i16 v72, v20, 0
v_pk_max_i16 v74, v20, 0
mav 8,128,32
v_fma_f32 v104, v79, v66, v102
v_fma_f32 v22, v22, v78, v104
mav 0,132,36
R 52,15360
v_fma_f32 v105, v79, v67, v103
mav 4,136,36
v_fma_f32 v23, v23, v78, v105
v_mul_f32_e64 v22, v22, |v22|
mav 8,140,36
s_waitcnt lgkmcnt(2)
v_mul_f32_e64 v23, v23, |v23|
v_cvt_pk_bf16_f32 v22, v22, v23
mav 0,144,40
R 24,16384
v_pk_max_i16 v73, v22, 0
mav 4,148,40
v_pk_max_i16 v75, v22, 0
mav 8,152,40
v_permlane16_swap_b32 v72, v74
mav 0,156,44
R 28,17408
mav 4,160,44
v_permlane16_swap_b32 v73, v75
mav 8,164,44
s_waitcnt lgkmcnt(2)
mav 0,168,48
buffer_store_dwordx4 v[72:75], v84, s[36:39], s49 offen sc1
R 32,18432
mav 4,172,48
mav 8,176,48
mav 0,180,52
R 36,19456
mav 4,184,52
mav 8,188,52
s_waitcnt lgkmcnt(2)
mav 0,192,24
R 40,20480
mav 4,196,24
mav 8,200,24
mav 0,204,28
R 44,21504
mav 4,208,28
mav 8,212,28
s_waitcnt lgkmcnt(2)
mav 0,216,32
R 48,22528
mav 4,220,32
mav 8,224,32
mav 0,228,36
R 52,23552
mav 4,232,36
mav 8,236,36
s_waitcnt lgkmcnt(2)
mav 0,240,40
R 24,24576
mav 4,244,40
mav 8,248,40
mav 0,252,44
R 28,25600
mvv 4,128,44
mvv 8,132,44
s_waitcnt lgkmcnt(2)
mvv 0,136,48
R 32,26624
mvv 4,140,48
mvv 8,144,48
mvv 0,148,52
R 36,27648
mvv 4,152,52
mvv 8,156,52
s_waitcnt lgkmcnt(2)
mvv 0,160,24
R 40,28672
mvv 4,164,24
mvv 8,168,24
mvv 0,172,28
R 44,29696
mvv 4,176,28
v_add_u32 v76, 1, v76
mvv 8,180,28
s_waitcnt lgkmcnt(2)
mvv 0,184,32
R 48,30720
mvv 4,188,32
mvv 8,192,32
mvv 0,196,36
R 52,31744
mvv 4,200,36
s_waitcnt vmcnt(2) lgkmcnt(2)
s_add_u32 s49, s49, 313344
mvv 8,204,36
s_barrier
mvv 0,208,40
R 24,32768
mvv 4,212,40
ds_read_b64 v[78:79], v89 offset:0
mvv 8,216,40
mvv 0,220,44
R 28,33792
mvv 4,224,44
mvv 8,228,44
s_waitcnt lgkmcnt(3)
mvv 0,232,48
R 32,34816
mvv 4,236,48
mvv 8,240,48
mvv 0,244,52
R 36,35840
mvv 4,248,52
mvv 8,252,52
s_waitcnt lgkmcnt(2)
v_mfma_f32_16x16x32_bf16 v[12:15], a[0:3], v[24:27], 0
R 40,36864
v_fma_f32 v104, v79, v56, v92
v_mfma_f32_16x16x32_bf16 v[16:19], a[4:7], v[24:27], 0
v_fma_f32 v0, v0, v78, v104
v_fma_f32 v105, v79, v57, v93
v_mfma_f32_16x16x32_bf16 v[20:23], a[8:11], v[24:27], 0
v_fma_f32 v1, v1, v78, v105
v_mul_f32_e64 v0, v0, |v0|
mav 12,12,28
R 44,37888
v_mul_f32_e64 v1, v1, |v1|
mav 16,16,28
s_waitcnt vmcnt(0)
v_cvt_pk_bf16_f32 v0, v0, v1
v_pk_max_i16 v68, v0, 0
mav 20,20,28
s_waitcnt lgkmcnt(2)
v_fma_f32 v104, v79, v58, v94
v_fma_f32 v2, v2, v78, v104
mav 12,24,32
buffer_store_dword v76, v81, s[36:39], s52 offen sc1
mav 16,28,32
R 48,38912
v_fma_f32 v105, v79, v59, v95
mav 20,32,32
v_fma_f32 v3, v3, v78, v105
v_mul_f32_e64 v2, v2, |v2|
mav 12,36,36
R 52,39936
v_mul_f32_e64 v3, v3, |v3|
mav 16,40,36
v_cvt_pk_bf16_f32 v2, v2, v3
v_pk_max_i16 v69, v2, 0
mav 20,44,36
s_waitcnt lgkmcnt(2)
v_fma_f32 v104, v79, v60, v96
v_fma_f32 v4, v4, v78, v104
mav 12,48,40
R 24,40960
v_fma_f32 v105, v79, v61, v97
mav 16,52,40
v_fma_f32 v5, v5, v78, v105
v_mul_f32_e64 v4, v4, |v4|
mav 20,56,40
v_mul_f32_e64 v5, v5, |v5|
v_cvt_pk_bf16_f32 v4, v4, v5
mav 12,60,44
R 28,41984
v_pk_max_i16 v70, v4, 0
mav 16,64,44
)"
R"(
v_fma_f32 v104, v79, v62, v98
v_fma_f32 v6, v6, v78, v104
mav 20,68,44
s_waitcnt lgkmcnt(2)
v_fma_f32 v105, v79, v63, v99
v_fma_f32 v7, v7, v78, v105
mav 12,72,48
R 32,43008
v_mul_f32_e64 v6, v6, |v6|
mav 16,76,48
v_mul_f32_e64 v7, v7, |v7|
v_cvt_pk_bf16_f32 v6, v6, v7
mav 20,80,48
v_pk_max_i16 v71, v6, 0
v_fma_f32 v104, v79, v64, v100
mav 12,84,52
R 36,44032
v_fma_f32 v8, v8, v78, v104
mav 16,88,52
buffer_store_dwordx4 v[68:71], v83, s[36:39], s49 offen sc1
v_fma_f32 v105, v79, v65, v101
v_fma_f32 v9, v9, v78, v105
v_mul_f32_e64 v8, v8, |v8|
mav 20,92,52
s_waitcnt lgkmcnt(2)
v_mul_f32_e64 v9, v9, |v9|
v_cvt_pk_bf16_f32 v8, v8, v9
mav 12,96,24
R 40,45056
v_pk_max_i16 v72, v8, 0
mav 16,100,24
v_pk_max_i16 v74, v8, 0
v_fma_f32 v104, v79, v66, v102
mav 20,104,24
v_fma_f32 v10, v10, v78, v104
v_fma_f32 v105, v79, v67, v103
mav 12,108,28
R 44,46080
v_fma_f32 v11, v11, v78, v105
mav 16,112,28
v_mul_f32_e64 v10, v10, |v10|
v_mul_f32_e64 v11, v11, |v11|
mav 20,116,28
s_waitcnt lgkmcnt(2)
v_cvt_pk_bf16_f32 v10, v10, v11
v_pk_max_i16 v73, v10, 0
mav 12,120,32
R 48,47104
v_pk_max_i16 v75, v10, 0
mav 16,124,32
v_permlane16_swap_b32 v72, v74
mav 20,128,32
v_permlane16_swap_b32 v73, v75
mav 12,132,36
R 52,48128
mav 16,136,36
buffer_store_dwordx4 v[72:75], v84, s[36:39], s49 offen sc1
mav 20,140,36
s_waitcnt lgkmcnt(2)
mav 12,144,40
R 24,49152
mav 16,148,40
mav 20,152,40
mav 12,156,44
R 28,50176
mav 16,160,44
mav 20,164,44
s_waitcnt lgkmcnt(2)
mav 12,168,48
R 32,51200
mav 16,172,48
mav 20,176,48
mav 12,180,52
R 36,52224
mav 16,184,52
mav 20,188,52
s_waitcnt lgkmcnt(2)
mav 12,192,24
R 40,53248
mav 16,196,24
mav 20,200,24
mav 12,204,28
R 44,54272
mav 16,208,28
mav 20,212,28
s_waitcnt lgkmcnt(2)
mav 12,216,32
R 48,55296
mav 16,220,32
mav 20,224,32
mav 12,228,36
R 52,56320
mav 16,232,36
mav 20,236,36
s_waitcnt lgkmcnt(2)
mav 12,240,40
R 24,57344
mav 16,244,40
mav 20,248,40
mav 12,252,44
R 28,58368
mvv 16,128,44
mvv 20,132,44
s_waitcnt lgkmcnt(2)
mvv 12,136,48
R 32,59392
mvv 16,140,48
mvv 20,144,48
mvv 12,148,52
R 36,60416
mvv 16,152,52
mvv 20,156,52
s_waitcnt lgkmcnt(2)
mvv 12,160,24
mvv 16,164,24
R 40,61440
mvv 20,168,24
mvv 12,172,28
R 44,62464
mvv 16,176,28
v_add_u32 v76, 1, v76
mvv 20,180,28
s_waitcnt lgkmcnt(2)
mvv 12,184,32
R 48,63488
mvv 16,188,32
mvv 20,192,32
mvv 12,196,36
R 52,64512
mvv 16,200,36
s_waitcnt lgkmcnt(2)
s_add_u32 s49, s49, 313344
mvv 20,204,36
s_barrier
mvv 12,208,40
mvv 16,212,40
ds_read_b64 v[78:79], v89 offset:128
mvv 20,216,40
mvv 12,220,44
mvv 16,224,44
mvv 20,228,44
s_waitcnt lgkmcnt(1)
mvv 12,232,48
mvv 16,236,48
mvv 20,240,48
mvv 12,244,52
mvv 16,248,52
mvv 20,252,52
s_waitcnt lgkmcnt(0)
v_fma_f32 v104, v79, v56, v92
s_nop 3
v_fma_f32 v12, v12, v78, v104
v_fma_f32 v105, v79, v57, v93
v_fma_f32 v13, v13, v78, v105
v_mul_f32_e64 v12, v12, |v12|
v_mul_f32_e64 v13, v13, |v13|
v_cvt_pk_bf16_f32 v12, v12, v13
v_pk_max_i16 v68, v12, 0
v_fma_f32 v104, v79, v58, v94
v_fma_f32 v14, v14, v78, v104
v_fma_f32 v105, v79, v59, v95
v_fma_f32 v15, v15, v78, v105
v_mul_f32_e64 v14, v14, |v14|
v_mul_f32_e64 v15, v15, |v15|
v_cvt_pk_bf16_f32 v14, v14, v15
v_pk_max_i16 v69, v14, 0
v_fma_f32 v104, v79, v60, v96
v_fma_f32 v16, v16, v78, v104
v_fma_f32 v105, v79, v61, v97
v_fma_f32 v17, v17, v78, v105
v_mul_f32_e64 v16, v16, |v16|
v_mul_f32_e64 v17, v17, |v17|
v_cvt_pk_bf16_f32 v16, v16, v17
v_pk_max_i16 v70, v16, 0
v_fma_f32 v104, v79, v62, v98
v_fma_f32 v18, v18, v78, v104
v_fma_f32 v105, v79, v63, v99
v_fma_f32 v19, v19, v78, v105
v_mul_f32_e64 v18, v18, |v18|
v_mul_f32_e64 v19, v19, |v19|
v_cvt_pk_bf16_f32 v18, v18, v19
v_pk_max_i16 v71, v18, 0
buffer_store_dwordx4 v[68:71], v83, s[36:39], s49 offen sc1
v_fma_f32 v104, v79, v64, v100
v_fma_f32 v20, v20, v78, v104
v_fma_f32 v105, v79, v65, v101
v_fma_f32 v21, v21, v78, v105
v_mul_f32_e64 v20, v20, |v20|
v_mul_f32_e64 v21, v21, |v21|
v_cvt_pk_bf16_f32 v20, v20, v21
v_pk_max_i16 v72, v20, 0
v_pk_max_i16 v74, v20, 0
v_fma_f32 v104, v79, v66, v102
v_fma_f32 v22, v22, v78, v104
v_fma_f32 v105, v79, v67, v103
v_fma_f32 v23, v23, v78, v105
v_mul_f32_e64 v22, v22, |v22|
v_mul_f32_e64 v23, v23, |v23|
v_cvt_pk_bf16_f32 v22, v22, v23
v_pk_max_i16 v73, v22, 0
v_pk_max_i16 v75, v22, 0
v_permlane16_swap_b32 v72, v74
s_nop 0
v_permlane16_swap_b32 v73, v75
buffer_store_dwordx4 v[72:75], v84, s[36:39], s49 offen sc1
s_waitcnt vmcnt(2)
buffer_store_dword v76, v81, s[36:39], s52 offen sc1
v_add_u32 v76, 37, v76
s_waitcnt vmcnt(1)
buffer_store_dword v76, v81, s[36:39], s52 offen sc1
s_endpgm
LvF3:
LsF12:
s_cbranch_execz LwF
s_add_u32 s49, s49, 313344
s_barrier
s_add_u32 m0, s50, 0
s_nop 0
D 85,0
D 85,1024
D 85,2048
D 85,3072
s_add_u32 m0, s50, 4096
s_nop 0
D 86,0
D 86,1024
D 86,2048
D 86,3072
s_waitcnt vmcnt(0)
s_add_u32 s49, s49, 313344
s_barrier
s_add_u32 m0, s50, 32768
R 24,0
D 85,0
R 28,1024
D 85,1024
R 32,2048
R 36,3072
D 85,2048
s_waitcnt lgkmcnt(2)
v_mfma_f32_16x16x32_bf16 v[0:3], a[0:3], v[24:27], 0
R 40,4096
v_mfma_f32_16x16x32_bf16 v[4:7], a[4:7], v[24:27], 0
v_mfma_f32_16x16x32_bf16 v[8:11], a[8:11], v[24:27], 0
mav 0,12,28
D 85,3072
mav 4,16,28
s_add_u32 m0, s50, 36864
R 44,5120
mav 8,20,28
s_waitcnt lgkmcnt(2)
mav 0,24,32
R 48,6144
mav 4,28,32
D 86,0
mav 8,32,32
LsF13:
s_cbranch_execz LwF
mav 0,36,36
R 52,7168
mav 4,40,36
mav 8,44,36
s_waitcnt lgkmcnt(2)
D 86,1024
mav 0,48,40
R 24,8192
mav 4,52,40
mav 8,56,40
mav 0,60,44
D 86,2048
mav 4,64,44
R 28,9216
mav 8,68,44
s_waitcnt lgkmcnt(2)
mav 0,72,48
R 32,10240
mav 4,76,48
D 86,3072
mav 8,80,48
mav 0,84,52
R 36,11264
mav 4,88,52
mav 8,92,52
s_waitcnt lgkmcnt(2)
mav 0,96,24
R 40,12288
mav 4,100,24
mav 8,104,24
mav 0,108,28
R 44,13312
mav 4,112,28
mav 8,116,28
s_waitcnt lgkmcnt(2)
mav 0,120,32
R 48,14336
mav 4,124,32
mav 8,128,32
mav 0,132,36
R 52,15360
mav 4,136,36
mav 8,140,36
s_waitcnt lgkmcnt(2)
mav 0,144,40
R 24,16384
mav 4,148,40
mav 8,152,40
mav 0,156,44
R 28,17408
mav 4,160,44
mav 8,164,44
s_waitcnt lgkmcnt(2)
mav 0,168,48
R 32,18432
mav 4,172,48
mav 8,176,48
mav 0,180,52
R 36,19456
mav 4,184,52
mav 8,188,52
s_waitcnt lgkmcnt(2)
mav 0,192,24
R 40,20480
mav 4,196,24
mav 8,200,24
mav 0,204,28
R 44,21504
mav 4,208,28
mav 8,212,28
s_waitcnt lgkmcnt(2)
mav 0,216,32
R 48,22528
mav 4,220,32
mav 8,224,32
mav 0,228,36
R 52,23552
mav 4,232,36
mav 8,236,36
s_waitcnt lgkmcnt(2)
mav 0,240,40
R 24,24576
mav 4,244,40
mav 8,248,40
mav 0,252,44
R 28,25600
mvv 4,128,44
mvv 8,132,44
s_waitcnt lgkmcnt(2)
mvv 0,136,48
R 32,26624
mvv 4,140,48
mvv 8,144,48
mvv 0,148,52
R 36,27648
mvv 4,152,52
mvv 8,156,52
s_waitcnt lgkmcnt(2)
mvv 0,160,24
R 40,28672
mvv 4,164,24
mvv 8,168,24
mvv 0,172,28
R 44,29696
mvv 4,176,28
mvv 8,180,28
s_waitcnt lgkmcnt(2)
mvv 0,184,32
s_xor_b32 s50, s50, 0x10000
R 48,30720
mvv 4,188,32
mvv 8,192,32
mvv 0,196,36
R 52,31744
mvv 4,200,36
s_waitcnt vmcnt(0) lgkmcnt(2)
s_add_u32 s49, s49, 313344
mvv 8,204,36
s_barrier
mvv 0,208,40
s_add_u32 m0, s50, 0
R 24,32768
mvv 4,212,40
ds_read_b64 v[78:79], v89 offset:0
mvv 8,216,40
mvv 0,220,44
D 85,0
mvv 4,224,44
R 28,33792
mvv 8,228,44
s_waitcnt lgkmcnt(3)
mvv 0,232,48
R 32,34816
mvv 4,236,48
D 85,1024
mvv 8,240,48
mvv 0,244,52
R 36,35840
mvv 4,248,52
mvv 8,252,52
s_waitcnt lgkmcnt(2)
D 85,2048
v_mfma_f32_16x16x32_bf16 v[12:15], a[0:3], v[24:27], 0
R 40,36864
v_fma_f32 v104, v79, v56, v92
v_mfma_f32_16x16x32_bf16 v[16:19], a[4:7], v[24:27], 0
v_fma_f32 v0, v0, v78, v104
v_fma_f32 v105, v79, v57, v93
v_mfma_f32_16x16x32_bf16 v[20:23], a[8:11], v[24:27], 0
v_fma_f32 v1, v1, v78, v105
v_mul_f32_e64 v0, v0, |v0|
mav 12,12,28
D 85,3072
mav 16,16,28
s_add_u32 m0, s50, 4096
R 44,37888
v_mul_f32_e64 v1, v1, |v1|
mav 20,20,28
s_waitcnt lgkmcnt(2)
v_cvt_pk_bf16_f32 v0, v0, v1
v_pk_max_i16 v68, v0, 0
mav 12,24,32
R 48,38912
v_fma_f32 v104, v79, v58, v94
mav 16,28,32
D 86,0
mav 20,32,32
v_fma_f32 v2, v2, v78, v104
v_fma_f32 v105, v79, v59, v95
mav 12,36,36
R 52,39936
v_fma_f32 v3, v3, v78, v105
mav 16,40,36
v_mul_f32_e64 v2, v2, |v2|
v_mul_f32_e64 v3, v3, |v3|
mav 20,44,36
s_waitcnt lgkmcnt(2)
D 86,1024
mav 12,48,40
R 24,40960
v_cvt_pk_bf16_f32 v2, v2, v3
mav 16,52,40
v_pk_max_i16 v69, v2, 0
v_fma_f32 v104, v79, v60, v96
mav 20,56,40
v_fma_f32 v4, v4, v78, v104
v_fma_f32 v105, v79, v61, v97
mav 12,60,44
D 86,2048
mav 16,64,44
R 28,41984
v_fma_f32 v5, v5, v78, v105
mav 20,68,44
s_waitcnt lgkmcnt(2)
v_mul_f32_e64 v4, v4, |v4|
v_mul_f32_e64 v5, v5, |v5|
mav 12,72,48
R 32,43008
v_cvt_pk_bf16_f32 v4, v4, v5
mav 16,76,48
D 86,3072
mav 20,80,48
v_pk_max_i16 v70, v4, 0
v_fma_f32 v104, v79, v62, v98
mav 12,84,52
R 36,44032
v_fma_f32 v6, v6, v78, v104
mav 16,88,52
v_fma_f32 v105, v79, v63, v99
v_fma_f32 v7, v7, v78, v105
mav 20,92,52
s_waitcnt lgkmcnt(2)
v_mul_f32_e64 v6, v6, |v6|
v_mul_f32_e64 v7, v7, |v7|
mav 12,96,24
R 40,45056
v_cvt_pk_bf16_f32 v6, v6, v7
mav 16,100,24
v_pk_max_i16 v71, v6, 0
v_fma_f32 v104, v79, v64, v100
mav 20,104,24
v_fma_f32 v8, v8, v78, v104
v_fma_f32 v105, v79, v65, v101
mav 12,108,28
buffer_store_dwordx4 v[68:71], v83, s[36:39], s49 offen sc1
R 44,46080
v_fma_f32 v9, v9, v78, v105
mav 16,112,28
v_mul_f32_e64 v8, v8, |v8|
v_mul_f32_e64 v9, v9, |v9|
mav 20,116,28
s_waitcnt lgkmcnt(2)
v_cvt_pk_bf16_f32 v8, v8, v9
v_pk_max_i16 v72, v8, 0
mav 12,120,32
R 48,47104
v_pk_max_i16 v74, v8, 0
mav 16,124,32
v_fma_f32 v104, v79, v66, v102
v_fma_f32 v10, v10, v78, v104
mav 20,128,32
v_fma_f32 v105, v79, v67, v103
v_fma_f32 v11, v11, v78, v105
mav 12,132,36
R 52,48128
v_mul_f32_e64 v10, v10, |v10|
mav 16,136,36
v_mul_f32_e64 v11, v11, |v11|
v_cvt_pk_bf16_f32 v10, v10, v11
mav 20,140,36
s_waitcnt lgkmcnt(2)
v_pk_max_i16 v73, v10, 0
v_pk_max_i16 v75, v10, 0
mav 12,144,40
R 24,49152
mav 16,148,40
v_permlane16_swap_b32 v72, v74
mav 20,152,40
v_permlane16_swap_b32 v73, v75
mav 12,156,44
buffer_store_dwordx4 v[72:75], v84, s[36:39], s49 offen sc1
R 28,50176
mav 16,160,44
mav 20,164,44
s_waitcnt lgkmcnt(2)
mav 12,168,48
R 32,51200
mav 16,172,48
mav 20,176,48
mav 12,180,52
R 36,52224
mav 16,184,52
mav 20,188,52
s_waitcnt lgkmcnt(2)
mav 12,192,24
R 40,53248
mav 16,196,24
mav 20,200,24
mav 12,204,28
R 44,54272
mav 16,208,28
mav 20,212,28
s_waitcnt lgkmcnt(2)
mav 12,216,32
R 48,55296
mav 16,220,32
mav 20,224,32
mav 12,228,36
R 52,56320
mav 16,232,36
mav 20,236,36
s_waitcnt lgkmcnt(2)
mav 12,240,40
R 24,57344
mav 16,244,40
mav 20,248,40
mav 12,252,44
R 28,58368
mvv 16,128,44
mvv 20,132,44
s_waitcnt lgkmcnt(2)
mvv 12,136,48
R 32,59392
mvv 16,140,48
mvv 20,144,48
mvv 12,148,52
R 36,60416
mvv 16,152,52
mvv 20,156,52
s_waitcnt lgkmcnt(2)
mvv 12,160,24
s_mov_b32 s60, -30
.p2align 6
LF3_1:
mvv 16,164,24
R 40,61440
mvv 20,168,24
mvv 12,172,28
R 44,62464
mvv 16,176,28
v_add_u32 v76, 1, v76
mvv 20,180,28
s_waitcnt lgkmcnt(2)
mvv 12,184,32
R 48,63488
mvv 16,188,32
mvv 20,192,32
mvv 12,196,36
R 52,64512
v_xor_b32 v80, 0x10000, v80
mvv 16,200,36
s_waitcnt vmcnt(2) lgkmcnt(2)
s_add_u32 s49, s49, 313344
mvv 20,204,36
s_barrier
mvv 12,208,40
s_add_u32 m0, s50, 32768
R 24,0
mvv 16,212,40
ds_read_b64 v[78:79], v89 offset:128
mvv 20,216,40
mvv 12,220,44
D 85,0
mvv 16,224,44
R 28,1024
mvv 20,228,44
s_waitcnt lgkmcnt(3)
mvv 12,232,48
R 32,2048
mvv 16,236,48
D 85,1024
mvv 20,240,48
mvv 12,244,52
R 36,3072
mvv 16,248,52
mvv 20,252,52
s_waitcnt lgkmcnt(2)
D 85,2048
v_mfma_f32_16x16x32_bf16 v[0:3], a[0:3], v[24:27], 0
R 40,4096
v_fma_f32 v104, v79, v56, v92
v_mfma_f32_16x16x32_bf16 v[4:7], a[4:7], v[24:27], 0
v_fma_f32 v12, v12, v78, v104
v_fma_f32 v105, v79, v57, v93
v_mfma_f32_16x16x32_bf16 v[8:11], a[8:11], v[24:27], 0
v_fma_f32 v13, v13, v78, v105
v_mul_f32_e64 v12, v12, |v12|
mav 0,12,28
D 85,3072
mav 4,16,28
s_waitcnt vmcnt(4)
R 44,5120
s_add_u32 m0, s50, 36864
mav 8,20,28
s_waitcnt lgkmcnt(2)
v_mul_f32_e64 v13, v13, |v13|
v_cvt_pk_bf16_f32 v12, v12, v13
mav 0,24,32
R 48,6144
v_pk_max_i16 v68, v12, 0
mav 4,28,32
buffer_store_dword v76, v81, s[36:39], s52 offen sc1
mav 8,32,32
v_fma_f32 v104, v79, v58, v94
v_fma_f32 v14, v14, v78, v104
mav 0,36,36
R 52,7168
v_fma_f32 v105, v79, v59, v95
mav 4,40,36
v_fma_f32 v15, v15, v78, v105
v_mul_f32_e64 v14, v14, |v14|
mav 8,44,36
s_waitcnt lgkmcnt(2)
D 86,0
mav 0,48,40
R 24,8192
v_mul_f32_e64 v15, v15, |v15|
mav 4,52,40
v_cvt_pk_bf16_f32 v14, v14, v15
v_pk_max_i16 v69, v14, 0
mav 8,56,40
v_fma_f32 v104, v79, v60, v96
v_fma_f32 v16, v16, v78, v104
mav 0,60,44
D 86,1024
mav 4,64,44
R 28,9216
v_fma_f32 v105, v79, v61, v97
mav 8,68,44
s_waitcnt lgkmcnt(2)
v_fma_f32 v17, v17, v78, v105
v_mul_f32_e64 v16, v16, |v16|
mav 0,72,48
R 32,10240
v_mul_f32_e64 v17, v17, |v17|
mav 4,76,48
D 86,2048
mav 8,80,48
v_cvt_pk_bf16_f32 v16, v16, v17
v_pk_max_i16 v70, v16, 0
mav 0,84,52
R 36,11264
v_fma_f32 v104, v79, v62, v98
mav 4,88,52
v_fma_f32 v18, v18, v78, v104
v_fma_f32 v105, v79, v63, v99
mav 8,92,52
s_waitcnt lgkmcnt(2)
D 86,3072
mav 0,96,24
R 40,12288
v_fma_f32 v19, v19, v78, v105
mav 4,100,24
v_mul_f32_e64 v18, v18, |v18|
v_mul_f32_e64 v19, v19, |v19|
mav 8,104,24
v_cvt_pk_bf16_f32 v18, v18, v19
v_pk_max_i16 v71, v18, 0
mav 0,108,28
buffer_store_dwordx4 v[68:71], v83, s[36:39], s49 offen sc1
R 44,13312
v_fma_f32 v104, v79, v64, v100
mav 4,112,28
v_fma_f32 v20, v20, v78, v104
v_fma_f32 v105, v79, v65, v101
mav 8,116,28
s_waitcnt lgkmcnt(2)
v_fma_f32 v21, v21, v78, v105
v_mul_f32_e64 v20, v20, |v20|
mav 0,120,32
R 48,14336
v_mul_f32_e64 v21, v21, |v21|
mav 4,124,32
v_cvt_pk_bf16_f32 v20, v20, v21
v_pk_max_i16 v72, v20, 0
mav 8,128,32
v_pk_max_i16 v74, v20, 0
v_fma_f32 v104, v79, v66, v102
mav 0,132,36
R 52,15360
v_fma_f32 v22, v22, v78, v104
mav 4,136,36
v_fma_f32 v105, v79, v67, v103
v_fma_f32 v23, v23, v78, v105
mav 8,140,36
s_waitcnt lgkmcnt(2)
v_mul_f32_e64 v22, v22, |v22|
v_mul_f32_e64 v23, v23, |v23|
mav 0,144,40
R 24,16384
v_cvt_pk_bf16_f32 v22, v22, v23
mav 4,148,40
v_pk_max_i16 v73, v22, 0
v_pk_max_i16 v75, v22, 0
mav 8,152,40
v_permlane16_swap_b32 v72, v74
mav 0,156,44
R 28,17408
mav 4,160,44
v_permlane16_swap_b32 v73, v75
mav 8,164,44
s_waitcnt lgkmcnt(2)
mav 0,168,48
R 32,18432
mav 4,172,48
buffer_store_dwordx4 v[72:75], v84, s[36:39], s49 offen sc1
mav 8,176,48
mav 0,180,52
R 36,19456
mav 4,184,52
mav 8,188,52
s_waitcnt lgkmcnt(2)
mav 0,192,24
R 40,20480
mav 4,196,24
mav 8,200,24
mav 0,204,28
R 44,21504
mav 4,208,28
mav 8,212,28
s_waitcnt lgkmcnt(2)
mav 0,216,32
R 48,22528
mav 4,220,32
mav 8,224,32
mav 0,228,36
R 52,23552
mav 4,232,36
mav 8,236,36
s_waitcnt lgkmcnt(2)
mav 0,240,40
R 24,24576
mav 4,244,40
mav 8,248,40
mav 0,252,44
R 28,25600
mvv 4,128,44
mvv 8,132,44
s_waitcnt lgkmcnt(2)
mvv 0,136,48
R 32,26624
mvv 4,140,48
mvv 8,144,48
mvv 0,148,52
R 36,27648
mvv 4,152,52
mvv 8,156,52
s_waitcnt lgkmcnt(2)
mvv 0,160,24
R 40,28672
mvv 4,164,24
mvv 8,168,24
mvv 0,172,28
LsF14:
s_cbranch_execz LwF
R 44,29696
mvv 4,176,28
v_add_u32 v76, 1, v76
mvv 8,180,28
s_waitcnt lgkmcnt(2)
mvv 0,184,32
s_xor_b32 s50, s50, 0x10000
R 48,30720
mvv 4,188,32
mvv 8,192,32
mvv 0,196,36
R 52,31744
mvv 4,200,36
s_waitcnt vmcnt(2) lgkmcnt(2)
s_add_u32 s49, s49, 313344
mvv 8,204,36
s_barrier
mvv 0,208,40
s_add_u32 m0, s50, 0
R 24,32768
mvv 4,212,40
ds_read_b64 v[78:79], v89 offset:0
mvv 8,216,40
mvv 0,220,44
D 85,0
mvv 4,224,44
R 28,33792
mvv 8,228,44
s_waitcnt lgkmcnt(3)
mvv 0,232,48
R 32,34816
mvv 4,236,48
D 85,1024
mvv 8,240,48
mvv 0,244,52
R 36,35840
mvv 4,248,52
mvv 8,252,52
s_waitcnt lgkmcnt(2)
D 85,2048
v_mfma_f32_16x16x32_bf16 v[12:15], a[0:3], v[24:27], 0
R 40,36864
v_fma_f32 v104, v79, v56, v92
v_mfma_f32_16x16x32_bf16 v[16:19], a[4:7], v[24:27], 0
v_fma_f32 v0, v0, v78, v104
v_fma_f32 v105, v79, v57, v93
v_mfma_f32_16x16x32_bf16 v[20:23], a[8:11], v[24:27], 0
v_fma_f32 v1, v1, v78, v105
v_mul_f32_e64 v0, v0, |v0|
mav 12,12,28
D 85,3072
mav 16,16,28
s_waitcnt vmcnt(4)
R 44,37888
s_add_u32 m0, s50, 4096
mav 20,20,28
s_waitcnt lgkmcnt(2)
v_mul_f32_e64 v1, v1, |v1|
v_cvt_pk_bf16_f32 v0, v0, v1
mav 12,24,32
R 48,38912
v_pk_max_i16 v68, v0, 0
mav 16,28,32
buffer_store_dword v76, v81, s[36:39], s52 offen sc1
mav 20,32,32
v_fma_f32 v104, v79, v58, v94
v_fma_f32 v2, v2, v78, v104
mav 12,36,36
R 52,39936
v_fma_f32 v105, v79, v59, v95
mav 16,40,36
v_fma_f32 v3, v3, v78, v105
v_mul_f32_e64 v2, v2, |v2|
mav 20,44,36
s_waitcnt lgkmcnt(2)
D 86,0
mav 12,48,40
R 24,40960
v_mul_f32_e64 v3, v3, |v3|
mav 16,52,40
v_cvt_pk_bf16_f32 v2, v2, v3
v_pk_max_i16 v69, v2, 0
mav 20,56,40
v_fma_f32 v104, v79, v60, v96
v_fma_f32 v4, v4, v78, v104
mav 12,60,44
D 86,1024
mav 16,64,44
R 28,41984
v_fma_f32 v105, v79, v61, v97
mav 20,68,44
s_waitcnt lgkmcnt(2)
v_fma_f32 v5, v5, v78, v105
v_mul_f32_e64 v4, v4, |v4|
mav 12,72,48
R 32,43008
v_mul_f32_e64 v5, v5, |v5|
mav 16,76,48
D 86,2048
mav 20,80,48
v_cvt_pk_bf16_f32 v4, v4, v5
v_pk_max_i16 v70, v4, 0
mav 12,84,52
R 36,44032
v_fma_f32 v104, v79, v62, v98
mav 16,88,52
v_fma_f32 v6, v6, v78, v104
v_fma_f32 v105, v79, v63, v99
mav 20,92,52
s_waitcnt lgkmcnt(2)
D 86,3072
mav 12,96,24
R 40,45056
v_fma_f32 v7, v7, v78, v105
mav 16,100,24
v_mul_f32_e64 v6, v6, |v6|
v_mul_f32_e64 v7, v7, |v7|
mav 20,104,24
v_cvt_pk_bf16_f32 v6, v6, v7
v_pk_max_i16 v71, v6, 0
mav 12,108,28
buffer_store_dwordx4 v[68:71], v83, s[36:39], s49 offen sc1
R 44,46080
v_fma_f32 v104, v79, v64, v100
mav 16,112,28
v_fma_f32 v8, v8, v78, v104
v_fma_f32 v105, v79, v65, v101
mav 20,116,28
s_waitcnt lgkmcnt(2)
v_fma_f32 v9, v9, v78, v105
v_mul_f32_e64 v8, v8, |v8|
mav 12,120,32
R 48,47104
v_mul_f32_e64 v9, v9, |v9|
mav 16,124,32
v_cvt_pk_bf16_f32 v8, v8, v9
v_pk_max_i16 v72, v8, 0
mav 20,128,32
v_pk_max_i16 v74, v8, 0
v_fma_f32 v104, v79, v66, v102
mav 12,132,36
R 52,48128
v_fma_f32 v10, v10, v78, v104
mav 16,136,36
v_fma_f32 v105, v79, v67, v103
v_fma_f32 v11, v11, v78, v105
mav 20,140,36
s_waitcnt lgkmcnt(2)
v_mul_f32_e64 v10, v10, |v10|
v_mul_f32_e64 v11, v11, |v11|
mav 12,144,40
R 24,49152
v_cvt_pk_bf16_f32 v10, v10, v11
mav 16,148,40
v_pk_max_i16 v73, v10, 0
v_pk_max_i16 v75, v10, 0
mav 20,152,40
v_permlane16_swap_b32 v72, v74
mav 12,156,44
R 28,50176
mav 16,160,44
v_permlane16_swap_b32 v73, v75
mav 20,164,44
s_waitcnt lgkmcnt(2)
mav 12,168,48
R 32,51200
mav 16,172,48
buffer_store_dwordx4 v[72:75], v84, s[36:39], s49 offen sc1
mav 20,176,48
mav 12,180,52
R 36,52224
mav 16,184,52
mav 20,188,52
s_waitcnt lgkmcnt(2)
mav 12,192,24
R 40,53248
mav 16,196,24
mav 20,200,24
mav 12,204,28
R 44,54272
mav 16,208,28
mav 20,212,28
s_waitcnt lgkmcnt(2)
mav 12,216,32
R 48,55296
mav 16,220,32
mav 20,224,32
mav 12,228,36
R 52,56320
mav 16,232,36
mav 20,236,36
s_waitcnt lgkmcnt(2)
mav 12,240,40
R 24,57344
mav 16,244,40
mav 20,248,40
mav 12,252,44
R 28,58368
mvv 16,128,44
mvv 20,132,44
s_waitcnt lgkmcnt(2)
mvv 12,136,48
R 32,59392
mvv 16,140,48
mvv 20,144,48
mvv 12,148,52
R 36,60416
mvv 16,152,52
mvv 20,156,52
s_waitcnt lgkmcnt(2)
s_add_u32 s60, s60, 1
mvv 12,160,24
s_cbranch_scc0 LF3_1
mvv 16,164,24
R 40,61440
mvv 20,168,24
mvv 12,172,28
R 44,62464
mvv 16,176,28
v_add_u32 v76, 1, v76
mvv 20,180,28
s_waitcnt lgkmcnt(2)
mvv 12,184,32
R 48,63488
mvv 16,188,32
mvv 20,192,32
mvv 12,196,36
R 52,64512
v_xor_b32 v80, 0x10000, v80
mvv 16,200,36
s_waitcnt vmcnt(2) lgkmcnt(2)
s_add_u32 s49, s49, 313344
mvv 20,204,36
s_barrier
mvv 12,208,40
s_add_u32 m0, s50, 32768
R 24,0
mvv 16,212,40
ds_read_b64 v[78:79], v89 offset:128
mvv 20,216,40
mvv 12,220,44
D 85,0
mvv 16,224,44
R 28,1024
mvv 20,228,44
s_waitcnt lgkmcnt(3)
mvv 12,232,48
R 32,2048
mvv 16,236,48
D 85,1024
mvv 20,240,48
mvv 12,244,52
R 36,3072
mvv 16,248,52
mvv 20,252,52
s_waitcnt lgkmcnt(2)
D 85,2048
v_mfma_f32_16x16x32_bf16 v[0:3], a[0:3], v[24:27], 0
R 40,4096
v_fma_f32 v104, v79, v56, v92
v_mfma_f32_16x16x32_bf16 v[4:7], a[4:7], v[24:27], 0
v_fma_f32 v12, v12, v78, v104
v_fma_f32 v105, v79, v57, v93
v_mfma_f32_16x16x32_bf16 v[8:11], a[8:11], v[24:27], 0
v_fma_f32 v13, v13, v78, v105
v_mul_f32_e64 v12, v12, |v12|
mav 0,12,28
D 85,3072
mav 4,16,28
s_waitcnt vmcnt(4)
R 44,5120
s_add_u32 m0, s50, 36864
mav 8,20,28
s_waitcnt lgkmcnt(2)
v_mul_f32_e64 v13, v13, |v13|
v_cvt_pk_bf16_f32 v12, v12, v13
mav 0,24,32
R 48,6144
v_pk_max_i16 v68, v12, 0
mav 4,28,32
buffer_store_dword v76, v81, s[36:39], s52 offen sc1
mav 8,32,32
v_fma_f32 v104, v79, v58, v94
v_fma_f32 v14, v14, v78, v104
mav 0,36,36
R 52,7168
v_fma_f32 v105, v79, v59, v95
mav 4,40,36
v_fma_f32 v15, v15, v78, v105
v_mul_f32_e64 v14, v14, |v14|
mav 8,44,36
s_waitcnt lgkmcnt(2)
D 86,0
mav 0,48,40
R 24,8192
v_mul_f32_e64 v15, v15, |v15|
mav 4,52,40
v_cvt_pk_bf16_f32 v14, v14, v15
v_pk_max_i16 v69, v14, 0
mav 8,56,40
v_fma_f32 v104, v79, v60, v96
v_fma_f32 v16, v16, v78, v104
mav 0,60,44
D 86,1024
mav 4,64,44
R 28,9216
v_fma_f32 v105, v79, v61, v97
mav 8,68,44
s_waitcnt lgkmcnt(2)
v_fma_f32 v17, v17, v78, v105
v_mul_f32_e64 v16, v16, |v16|
mav 0,72,48
R 32,10240
v_mul_f32_e64 v17, v17, |v17|
mav 4,76,48
D 86,2048
mav 8,80,48
v_cvt_pk_bf16_f32 v16, v16, v17
v_pk_max_i16 v70, v16, 0
mav 0,84,52
R 36,11264
v_fma_f32 v104, v79, v62, v98
mav 4,88,52
v_fma_f32 v18, v18, v78, v104
v_fma_f32 v105, v79, v63, v99
mav 8,92,52
s_waitcnt lgkmcnt(2)
D 86,3072
mav 0,96,24
R 40,12288
v_fma_f32 v19, v19, v78, v105
mav 4,100,24
v_mul_f32_e64 v18, v18, |v18|
v_mul_f32_e64 v19, v19, |v19|
mav 8,104,24
v_cvt_pk_bf16_f32 v18, v18, v19
v_pk_max_i16 v71, v18, 0
mav 0,108,28
buffer_store_dwordx4 v[68:71], v83, s[36:39], s49 offen sc1
R 44,13312
v_fma_f32 v104, v79, v64, v100
mav 4,112,28
v_fma_f32 v20, v20, v78, v104
v_fma_f32 v105, v79, v65, v101
mav 8,116,28
s_waitcnt lgkmcnt(2)
v_fma_f32 v21, v21, v78, v105
v_mul_f32_e64 v20, v20, |v20|
mav 0,120,32
R 48,14336
v_mul_f32_e64 v21, v21, |v21|
mav 4,124,32
v_cvt_pk_bf16_f32 v20, v20, v21
v_pk_max_i16 v72, v20, 0
mav 8,128,32
v_pk_max_i16 v74, v20, 0
v_fma_f32 v104, v79, v66, v102
mav 0,132,36
R 52,15360
v_fma_f32 v22, v22, v78, v104
mav 4,136,36
v_fma_f32 v105, v79, v67, v103
v_fma_f32 v23, v23, v78, v105
mav 8,140,36
s_waitcnt lgkmcnt(2)
v_mul_f32_e64 v22, v22, |v22|
v_mul_f32_e64 v23, v23, |v23|
mav 0,144,40
R 24,16384
v_cvt_pk_bf16_f32 v22, v22, v23
mav 4,148,40
v_pk_max_i16 v73, v22, 0
v_pk_max_i16 v75, v22, 0
mav 8,152,40
v_permlane16_swap_b32 v72, v74
mav 0,156,44
R 28,17408
mav 4,160,44
v_permlane16_swap_b32 v73, v75
mav 8,164,44
s_waitcnt lgkmcnt(2)
mav 0,168,48
R 32,18432
mav 4,172,48
buffer_store_dwordx4 v[72:75], v84, s[36:39], s49 offen sc1
mav 8,176,48
mav 0,180,52
R 36,19456
mav 4,184,52
mav 8,188,52
s_waitcnt lgkmcnt(2)
mav 0,192,24
R 40,20480
mav 4,196,24
mav 8,200,24
mav 0,204,28
R 44,21504
mav 4,208,28
mav 8,212,28
s_waitcnt lgkmcnt(2)
mav 0,216,32
R 48,22528
mav 4,220,32
mav 8,224,32
mav 0,228,36
R 52,23552
mav 4,232,36
mav 8,236,36
s_waitcnt lgkmcnt(2)
mav 0,240,40
R 24,24576
mav 4,244,40
mav 8,248,40
mav 0,252,44
R 28,25600
mvv 4,128,44
mvv 8,132,44
s_waitcnt lgkmcnt(2)
mvv 0,136,48
R 32,26624
mvv 4,140,48
mvv 8,144,48
mvv 0,148,52
R 36,27648
mvv 4,152,52
mvv 8,156,52
s_waitcnt lgkmcnt(2)
mvv 0,160,24
R 40,28672
mvv 4,164,24
mvv 8,168,24
mvv 0,172,28
R 44,29696
mvv 4,176,28
v_add_u32 v76, 1, v76
mvv 8,180,28
s_waitcnt lgkmcnt(2)
mvv 0,184,32
R 48,30720
mvv 4,188,32
mvv 8,192,32
mvv 0,196,36
R 52,31744
mvv 4,200,36
s_waitcnt vmcnt(2) lgkmcnt(2)
s_add_u32 s49, s49, 313344
mvv 8,204,36
s_barrier
mvv 0,208,40
R 24,32768
mvv 4,212,40
ds_read_b64 v[78:79], v89 offset:0
mvv 8,216,40
mvv 0,220,44
R 28,33792
mvv 4,224,44
mvv 8,228,44
s_waitcnt lgkmcnt(3)
mvv 0,232,48
R 32,34816
mvv 4,236,48
mvv 8,240,48
mvv 0,244,52
R 36,35840
mvv 4,248,52
mvv 8,252,52
s_waitcnt lgkmcnt(2)
v_mfma_f32_16x16x32_bf16 v[12:15], a[0:3], v[24:27], 0
R 40,36864
v_fma_f32 v104, v79, v56, v92
v_mfma_f32_16x16x32_bf16 v[16:19], a[4:7], v[24:27], 0
v_fma_f32 v0, v0, v78, v104
v_fma_f32 v105, v79, v57, v93
v_mfma_f32_16x16x32_bf16 v[20:23], a[8:11], v[24:27], 0
v_fma_f32 v1, v1, v78, v105
v_mul_f32_e64 v0, v0, |v0|
mav 12,12,28
R 44,37888
v_mul_f32_e64 v1, v1, |v1|
mav 16,16,28
s_waitcnt vmcnt(0)
v_cvt_pk_bf16_f32 v0, v0, v1
v_pk_max_i16 v68, v0, 0
mav 20,20,28
s_waitcnt lgkmcnt(2)
v_fma_f32 v104, v79, v58, v94
v_fma_f32 v2, v2, v78, v104
mav 12,24,32
R 48,38912
v_fma_f32 v105, v79, v59, v95
mav 16,28,32
buffer_store_dword v76, v81, s[36:39], s52 offen sc1
mav 20,32,32
v_fma_f32 v3, v3, v78, v105
v_mul_f32_e64 v2, v2, |v2|
mav 12,36,36
R 52,39936
v_mul_f32_e64 v3, v3, |v3|
mav 16,40,36
v_cvt_pk_bf16_f32 v2, v2, v3
v_pk_max_i16 v69, v2, 0
mav 20,44,36
s_waitcnt lgkmcnt(2)
v_fma_f32 v104, v79, v60, v96
v_fma_f32 v4, v4, v78, v104
mav 12,48,40
R 24,40960
v_fma_f32 v105, v79, v61, v97
mav 16,52,40
v_fma_f32 v5, v5, v78, v105
v_mul_f32_e64 v4, v4, |v4|
mav 20,56,40
v_mul_f32_e64 v5, v5, |v5|
v_cvt_pk_bf16_f32 v4, v4, v5
mav 12,60,44
R 28,41984
v_pk_max_i16 v70, v4, 0
mav 16,64,44
v_fma_f32 v104, v79, v62, v98
v_fma_f32 v6, v6, v78, v104
mav 20,68,44
s_waitcnt lgkmcnt(2)
v_fma_f32 v105, v79, v63, v99
v_fma_f32 v7, v7, v78, v105
mav 12,72,48
R 32,43008
v_mul_f32_e64 v6, v6, |v6|
mav 16,76,48
v_mul_f32_e64 v7, v7, |v7|
v_cvt_pk_bf16_f32 v6, v6, v7
mav 20,80,48
v_pk_max_i16 v71, v6, 0
v_fma_f32 v104, v79, v64, v100
mav 12,84,52
R 36,44032
v_fma_f32 v8, v8, v78, v104
mav 16,88,52
v_fma_f32 v105, v79, v65, v101
v_fma_f32 v9, v9, v78, v105
LsF15:
s_cbranch_execz LwF
mav 20,92,52
buffer_store_dwordx4 v[68:71], v83, s[36:39], s49 offen sc1
s_waitcnt lgkmcnt(2)
v_mul_f32_e64 v8, v8, |v8|
v_mul_f32_e64 v9, v9, |v9|
mav 12,96,24
R 40,45056
v_cvt_pk_bf16_f32 v8, v8, v9
mav 16,100,24
v_pk_max_i16 v72, v8, 0
v_pk_max_i16 v74, v8, 0
mav 20,104,24
v_fma_f32 v104, v79, v66, v102
v_fma_f32 v10, v10, v78, v104
mav 12,108,28
R 44,46080
v_fma_f32 v105, v79, v67, v103
mav 16,112,28
v_fma_f32 v11, v11, v78, v105
v_mul_f32_e64 v10, v10, |v10|
mav 20,116,28
s_waitcnt lgkmcnt(2)
v_mul_f32_e64 v11, v11, |v11|
v_cvt_pk_bf16_f32 v10, v10, v11
mav 12,120,32
R 48,47104
v_pk_max_i16 v73, v10, 0
mav 16,124,32
v_pk_max_i16 v75, v10, 0
mav 20,128,32
v_permlane16_swap_b32 v72, v74
mav 12,132,36
R 52,48128
mav 16,136,36
v_permlane16_swap_b32 v73, v75
mav 20,140,36
buffer_store_dwordx4 v[72:75], v84, s[36:39], s49 offen sc1
s_waitcnt lgkmcnt(2)
mav 12,144,40
R 24,49152
mav 16,148,40
mav 20,152,40
mav 12,156,44
R 28,50176
mav 16,160,44
mav 20,164,44
s_waitcnt lgkmcnt(2)
mav 12,168,48
R 32,51200
mav 16,172,48
mav 20,176,48
mav 12,180,52
R 36,52224
mav 16,184,52
mav 20,188,52
s_waitcnt lgkmcnt(2)
mav 12,192,24
R 40,53248
mav 16,196,24
mav 20,200,24
mav 12,204,28
R 44,54272
mav 16,208,28
mav 20,212,28
s_waitcnt lgkmcnt(2)
mav 12,216,32
R 48,55296
mav 16,220,32
mav 20,224,32
mav 12,228,36
R 52,56320
mav 16,232,36
mav 20,236,36
s_waitcnt lgkmcnt(2)
mav 12,240,40
R 24,57344
mav 16,244,40
mav 20,248,40
mav 12,252,44
R 28,58368
mvv 16,128,44
mvv 20,132,44
s_waitcnt lgkmcnt(2)
mvv 12,136,48
R 32,59392
mvv 16,140,48
mvv 20,144,48
mvv 12,148,52
R 36,60416
mvv 16,152,52
mvv 20,156,52
s_waitcnt lgkmcnt(2)
mvv 12,160,24
mvv 16,164,24
R 40,61440
mvv 20,168,24
mvv 12,172,28
R 44,62464
mvv 16,176,28
v_add_u32 v76, 1, v76
mvv 20,180,28
s_waitcnt lgkmcnt(2)
mvv 12,184,32
R 48,63488
mvv 16,188,32
mvv 20,192,32
mvv 12,196,36
R 52,64512
mvv 16,200,36
s_waitcnt lgkmcnt(2)
s_add_u32 s49, s49, 313344
mvv 20,204,36
s_barrier
mvv 12,208,40
mvv 16,212,40
ds_read_b64 v[78:79], v89 offset:128
mvv 20,216,40
mvv 12,220,44
mvv 16,224,44
mvv 20,228,44
s_waitcnt lgkmcnt(1)
mvv 12,232,48
mvv 16,236,48
mvv 20,240,48
mvv 12,244,52
mvv 16,248,52
mvv 20,252,52
s_waitcnt lgkmcnt(0)
v_fma_f32 v104, v79, v56, v92
s_nop 3
v_fma_f32 v12, v12, v78, v104
v_fma_f32 v105, v79, v57, v93
v_fma_f32 v13, v13, v78, v105
v_mul_f32_e64 v12, v12, |v12|
v_mul_f32_e64 v13, v13, |v13|
v_cvt_pk_bf16_f32 v12, v12, v13
v_pk_max_i16 v68, v12, 0
v_fma_f32 v104, v79, v58, v94
v_fma_f32 v14, v14, v78, v104
v_fma_f32 v105, v79, v59, v95
v_fma_f32 v15, v15, v78, v105
v_mul_f32_e64 v14, v14, |v14|
v_mul_f32_e64 v15, v15, |v15|
v_cvt_pk_bf16_f32 v14, v14, v15
v_pk_max_i16 v69, v14, 0
v_fma_f32 v104, v79, v60, v96
v_fma_f32 v16, v16, v78, v104
v_fma_f32 v105, v79, v61, v97
v_fma_f32 v17, v17, v78, v105
v_mul_f32_e64 v16, v16, |v16|
v_mul_f32_e64 v17, v17, |v17|
v_cvt_pk_bf16_f32 v16, v16, v17
v_pk_max_i16 v70, v16, 0
v_fma_f32 v104, v79, v62, v98
v_fma_f32 v18, v18, v78, v104
v_fma_f32 v105, v79, v63, v99
v_fma_f32 v19, v19, v78, v105
v_mul_f32_e64 v18, v18, |v18|
v_mul_f32_e64 v19, v19, |v19|
v_cvt_pk_bf16_f32 v18, v18, v19
v_pk_max_i16 v71, v18, 0
buffer_store_dwordx4 v[68:71], v83, s[36:39], s49 offen sc1
v_fma_f32 v104, v79, v64, v100
v_fma_f32 v20, v20, v78, v104
v_fma_f32 v105, v79, v65, v101
v_fma_f32 v21, v21, v78, v105
v_mul_f32_e64 v20, v20, |v20|
v_mul_f32_e64 v21, v21, |v21|
v_cvt_pk_bf16_f32 v20, v20, v21
v_pk_max_i16 v72, v20, 0
v_pk_max_i16 v74, v20, 0
v_fma_f32 v104, v79, v66, v102
v_fma_f32 v22, v22, v78, v104
v_fma_f32 v105, v79, v67, v103
v_fma_f32 v23, v23, v78, v105
v_mul_f32_e64 v22, v22, |v22|
v_mul_f32_e64 v23, v23, |v23|
v_cvt_pk_bf16_f32 v22, v22, v23
v_pk_max_i16 v73, v22, 0
v_pk_max_i16 v75, v22, 0
v_permlane16_swap_b32 v72, v74
s_nop 0
v_permlane16_swap_b32 v73, v75
buffer_store_dwordx4 v[72:75], v84, s[36:39], s49 offen sc1
s_waitcnt vmcnt(2)
buffer_store_dword v76, v81, s[36:39], s52 offen sc1
v_add_u32 v76, 37, v76
s_waitcnt vmcnt(1)
buffer_store_dword v76, v81, s[36:39], s52 offen sc1
s_endpgm
Lr4:
s_cmp_eq_u32 s60, 4
s_cbranch_scc0 Lr5
s_waitcnt vmcnt(0)
v_accvgpr_read_b32 v65, a255
v_mov_b32 v64, 0x100
v_add_u32 v65, 0x9e3779b9, v65
buffer_store_dwordx2 v[64:65], v81, s[36:39], s52 offen sc1
v_add_u32 v87, v87, v64
s_sleep 32
s_sleep 32
s_sleep 32
E 0,65536
E 1,69632
s_add_u32 s51, s51, 229376
E 0,73728
E 1,77824
s_add_u32 s51, s51, 229376
E 0,81920
E 1,86016
s_add_u32 s51, s51, 229376
E 0,90112
E 1,94208
s_add_u32 s51, s51, 229376
s_mov_b64 exec, 0
s_cmp_eq_u32 s53, 0
s_cbranch_scc1 LsE0
s_cmp_eq_u32 s53, 1024
s_cbranch_scc1 LsE1
s_cmp_eq_u32 s53, 2048
s_cbranch_scc1 LsE2
s_branch LsE3
LwE:
s_mov_b64 exec, -1
v_mov_b32 v16, s64
v_mov_b32 v17, s64
v_mov_b32 v18, s64
v_mov_b32 v19, s64
s_waitcnt vmcnt(4)
s_barrier
v_add_u32 v4, 65536, v4
v_add_u32 v5, 65536, v5
Ta 0,4,0
Ta 4,5,0
Ta 8,4,8192
Ta 12,5,8192
E 0,98304
E 1,102400
s_add_u32 s51, s51, 229376
E 0,106496
E 1,110592
s_add_u32 s51, s51, 229376
E 0,114688
E 1,118784
s_add_u32 s51, s51, 229376
E 0,122880
E 1,126976
s_add_u32 s51, s51, 229376
s_cmp_ge_u32 s51, s57
s_cselect_b32 s60, 7340032, 0
s_sub_u32 s51, s51, s60
E 0,131072
E 1,135168
s_add_u32 s51, s51, 229376
E 0,139264
E 1,143360
s_add_u32 s51, s51, 229376
E 0,147456
E 1,151552
s_add_u32 s51, s51, 229376
E 0,155648
E 1,159744
s_add_u32 s51, s51, 229376
s_waitcnt lgkmcnt(0)
s_waitcnt vmcnt(16)
s_barrier
E 0,65536
Ta 16,4,16384
v_mfma_f32_16x16x32_bf16 v[48:51], a[0:3], v[16:19], 0
Ta 20,5,16384
E 1,69632
s_add_u32 s51, s51, 229376
v_mfma_f32_16x16x32_bf16 v[52:55], a[4:7], v[16:19], 0
Ta 24,4,24576
Ta 28,5,24576
E 0,73728
mav 48,8,16
E 1,77824
s_add_u32 s51, s51, 229376
mav 52,12,16
s_waitcnt lgkmcnt(0)
s_waitcnt vmcnt(16)
s_barrier
E 0,81920
Ta 32,4,32768
mav 48,16,16
Ta 36,5,32768
E 1,86016
s_add_u32 s51, s51, 229376
mav 52,20,16
Ta 40,4,40960
Ta 44,5,40960
E 0,90112
mav 48,24,16
E 1,94208
s_add_u32 s51, s51, 229376
s_cmp_ge_u32 s51, s57
s_cselect_b32 s60, 7340032, 0
s_sub_u32 s51, s51, s60
mav 52,28,16
s_waitcnt lgkmcnt(0)
s_waitcnt vmcnt(16)
s_barrier
E 0,98304
Ta 48,4,49152
mav 48,32,16
Ta 52,5,49152
E 1,102400
s_add_u32 s51, s51, 229376
mav 52,36,16
Ta 56,4,57344
Ta 60,5,57344
E 0,106496
mav 48,40,16
E 1,110592
s_add_u32 s51, s51, 229376
mav 52,44,16
s_waitcnt lgkmcnt(0)
s_waitcnt vmcnt(16)
s_barrier
v_add_u32 v4, 65536, v4
v_add_u32 v5, 65536, v5
E 0,114688
Ta 64,4,0
mav 48,48,16
Ta 68,5,0
E 1,118784
s_add_u32 s51, s51, 229376
mav 52,52,16
Ta 72,4,8192
Ta 76,5,8192
E 0,122880
mav 48,56,16
E 1,126976
s_add_u32 s51, s51, 229376
mav 52,60,16
s_waitcnt lgkmcnt(0)
s_waitcnt vmcnt(16)
s_barrier
E 0,131072
Ta 80,4,16384
mav 48,64,16
Ta 84,5,16384
E 1,135168
s_add_u32 s51, s51, 229376
mav 52,68,16
Ta 88,4,24576
Ta 92,5,24576
E 0,139264
mav 48,72,16
E 1,143360
s_add_u32 s51, s51, 229376
mav 52,76,16
s_waitcnt lgkmcnt(0)
s_waitcnt vmcnt(16)
s_barrier
v_add_u32 v4, -65536, v4
v_add_u32 v5, -65536, v5
E 0,147456
Ta 96,4,0
mav 48,80,16
Ta 100,5,0
E 1,151552
s_add_u32 s51, s51, 229376
mav 52,84,16
Ta 104,4,8192
Ta 108,5,8192
E 0,155648
mav 48,88,16
E 1,159744
s_add_u32 s51, s51, 229376
s_cmp_ge_u32 s51, s57
s_cselect_b32 s60, 7340032, 0
s_sub_u32 s51, s51, s60
mav 52,92,16
s_waitcnt lgkmcnt(0)
s_waitcnt vmcnt(16)
s_barrier
E 0,65536
Ta 112,4,16384
mav 48,96,16
Ta 116,5,16384
E 1,69632
s_add_u32 s51, s51, 229376
mav 52,100,16
Ta 120,4,24576
Ta 124,5,24576
E 0,73728
mav 48,104,16
E 1,77824
s_add_u32 s51, s51, 229376
mav 52,108,16
s_waitcnt lgkmcnt(0)
s_waitcnt vmcnt(16)
s_barrier
E 0,81920
Ta 128,4,32768
mav 48,112,16
Ta 132,5,32768
E 1,86016
s_add_u32 s51, s51, 229376
mav 52,116,16
Ta 136,4,40960
Ta 140,5,40960
E 0,90112
mav 48,120,16
E 1,94208
s_add_u32 s51, s51, 229376
mav 52,124,16
s_waitcnt lgkmcnt(0)
s_waitcnt vmcnt(16)
s_barrier
E 0,98304
Ta 144,4,49152
mav 48,128,16
Ta 148,5,49152
E 1,102400
s_add_u32 s51, s51, 229376
mav 52,132,16
Ta 152,4,57344
Ta 156,5,57344
E 0,106496
mav 48,136,16
E 1,110592
LsE0:
s_cbranch_execz LsE4
s_add_u32 s51, s51, 229376
mav 52,140,16
s_waitcnt lgkmcnt(0)
s_waitcnt vmcnt(16)
s_barrier
v_add_u32 v4, 65536, v4
v_add_u32 v5, 65536, v5
E 0,114688
Ta 160,4,0
mav 48,144,16
Ta 164,5,0
E 1,118784
s_add_u32 s51, s51, 229376
mav 52,148,16
Ta 168,4,8192
Ta 172,5,8192
E 0,122880
mav 48,152,16
E 1,126976
s_add_u32 s51, s51, 229376
s_cmp_ge_u32 s51, s57
s_cselect_b32 s60, 7340032, 0
s_sub_u32 s51, s51, s60
mav 52,156,16
s_waitcnt lgkmcnt(0)
s_waitcnt vmcnt(16)
s_barrier
Ta 176,4,16384
mav 48,160,16
Ta 180,5,16384
mav 52,164,16
Ta 184,4,24576
Ta 188,5,24576
mav 48,168,16
mav 52,172,16
s_waitcnt lgkmcnt(0)
s_waitcnt vmcnt(12)
s_barrier
v_add_u32 v4, -65536, v4
v_add_u32 v5, -65536, v5
Ta 192,4,0
mav 48,176,16
Ta 196,5,0
mav 52,180,16
Ta 200,4,8192
Ta 204,5,8192
mav 48,184,16
mav 52,188,16
s_waitcnt lgkmcnt(0)
s_waitcnt vmcnt(8)
s_barrier
Ta 208,4,16384
mav 48,192,16
Ta 212,5,16384
mav 52,196,16
Ta 216,4,24576
Ta 220,5,24576
mav 48,200,16
mav 52,204,16
s_waitcnt lgkmcnt(0)
s_waitcnt vmcnt(4)
s_barrier
Ta 224,4,32768
mav 48,208,16
Ta 228,5,32768
mav 52,212,16
Ta 232,4,40960
Ta 236,5,40960
mav 48,216,16
mav 52,220,16
s_waitcnt lgkmcnt(0)
s_waitcnt vmcnt(0)
s_barrier
Ta 240,4,49152
mav 48,224,16
Ta 244,5,49152
mav 52,228,16
Ta 248,4,57344
Ta 252,5,57344
mav 48,232,16
mav 52,236,16
s_waitcnt lgkmcnt(0)
mav 48,240,16
mav 52,244,16
mav 48,248,16
mav 52,252,16
s_waitcnt vmcnt(0)
s_barrier
s_nop 7
s_and_b32 s60, s48, 3
s_cmp_eq_u32 s60, 1
s_cbranch_scc1 LvE1
s_cmp_eq_u32 s60, 2
s_cbranch_scc1 LvE2
s_cmp_eq_u32 s60, 3
s_cbranch_scc1 LvE3
LvE0:
LsE1:
s_cbranch_execz LsE5
buffer_load_dwordx2 v[70:71], v86, s[36:39], 0 offen sc1
s_waitcnt vmcnt(0)
ds_write_b32 v82, v70
v_sub_u32 v70, v70, v87
v_xor_b32 v71, v71, v65
v_cmp_lt_u64 vcc, s[62:63], v[70:71]
s_waitcnt lgkmcnt(0)
s_cbranch_vccnz CE0_1
BE0_1:
s_add_u32 s49, s49, 313344
s_barrier
s_add_u32 m0, s50, 0
s_nop 0
D 84,0
D 84,1024
D 84,2048
D 84,3072
s_add_u32 m0, s50, 4096
s_nop 0
D 85,0
D 85,1024
D 85,2048
D 85,3072
s_mov_b32 m0, 144384
s_nop 0
buffer_load_dword v86, s[36:39], 0 offen sc1 lds
v_add_u32 v87, 1, v87
ds_read_b32 v70, v82 offset:0
s_waitcnt lgkmcnt(0)
v_cmp_gt_i32 vcc, v87, v70
s_waitcnt vmcnt(1)
s_add_u32 s49, s49, 313344
s_cbranch_vccnz CE0_2
BE0_2:
s_barrier
s_add_u32 m0, s50, 32768
R 16,0
D 84,0
R 20,1024
R 24,2048
D 84,1024
R 28,3072
R 32,4096
D 84,2048
s_waitcnt lgkmcnt(3)
v_mfma_f32_16x16x32_bf16 v[0:3], a[0:3], v[16:19], 0
R 36,5120
v_mfma_f32_16x16x32_bf16 v[4:7], a[4:7], v[16:19], 0
mav 0,8,20
R 40,6144
mav 4,12,20
s_waitcnt lgkmcnt(3)
D 84,3072
mav 0,16,24
R 44,7168
mav 4,20,24
s_add_u32 m0, s50, 36864
mav 0,24,28
R 16,8192
mav 4,28,28
s_waitcnt lgkmcnt(3)
D 85,0
mav 0,32,32
R 20,9216
mav 4,36,32
mav 0,40,36
R 24,10240
mav 4,44,36
s_waitcnt lgkmcnt(3)
D 85,1024
mav 0,48,40
R 28,11264
mav 4,52,40
mav 0,56,44
R 32,12288
mav 4,60,44
s_waitcnt lgkmcnt(3)
D 85,2048
mav 0,64,16
R 36,13312
mav 4,68,16
mav 0,72,20
R 40,14336
mav 4,76,20
s_waitcnt lgkmcnt(3)
D 85,3072
mav 0,80,24
R 44,15360
mav 4,84,24
mav 0,88,28
R 16,16384
mav 4,92,28
s_waitcnt lgkmcnt(3)
mav 0,96,32
R 20,17408
mav 4,100,32
mav 0,104,36
R 24,18432
mav 4,108,36
s_waitcnt lgkmcnt(3)
mav 0,112,40
s_mov_b32 m0, 144384
R 28,19456
mav 4,116,40
mav 0,120,44
R 32,20480
mav 4,124,44
s_waitcnt lgkmcnt(3)
buffer_load_dword v86, s[36:39], 0 offen sc1 lds
mav 0,128,16
R 36,21504
mav 4,132,16
mav 0,136,20
R 40,22528
mav 4,140,20
s_waitcnt lgkmcnt(3)
mav 0,144,24
R 44,23552
mav 4,148,24
mav 0,152,28
R 16,24576
mav 4,156,28
s_waitcnt lgkmcnt(3)
mav 0,160,32
R 20,25600
v_add_u32 v87, 1, v87
mav 4,164,32
mav 0,168,36
R 24,26624
mav 4,172,36
s_waitcnt lgkmcnt(3)
mav 0,176,40
R 28,27648
mav 4,180,40
mav 0,184,44
R 32,28672
mav 4,188,44
s_waitcnt lgkmcnt(3)
mav 0,192,16
R 36,29696
mav 4,196,16
)"
R"(
mav 0,200,20
R 40,30720
mav 4,204,20
s_waitcnt lgkmcnt(3)
mav 0,208,24
R 44,31744
mav 4,212,24
mav 0,216,28
mav 4,220,28
s_waitcnt lgkmcnt(2)
mav 0,224,32
mav 4,228,32
mav 0,232,36
mav 4,236,36
s_waitcnt lgkmcnt(0)
mav 0,240,40
mav 4,244,40
mav 0,248,44
mav 4,252,44
ds_read_b32 v70, v82 offset:0
s_xor_b32 s50, s50, 0x10000
s_waitcnt lgkmcnt(0)
v_cmp_gt_i32 vcc, v87, v70
s_waitcnt vmcnt(1)
s_add_u32 s49, s49, 313344
s_cbranch_vccnz CE0_3
BE0_3:
s_barrier
s_add_u32 m0, s50, 0
R 16,32768
D 84,0
ds_read_b64 v[66:67], v88 offset:0
R 20,33792
R 24,34816
D 84,1024
R 28,35840
R 32,36864
D 84,2048
s_waitcnt lgkmcnt(3)
v_mfma_f32_16x16x32_bf16 v[8:11], a[0:3], v[16:19], 0
R 36,37888
v_fma_f32 v68, v67, v48, v90
v_mfma_f32_16x16x32_bf16 v[12:15], a[4:7], v[16:19], 0
v_fma_f32 v0, v0, v66, v68
v_fma_f32 v69, v67, v49, v91
mav 8,8,20
R 40,38912
v_fma_f32 v1, v1, v66, v69
mav 12,12,20
s_waitcnt lgkmcnt(3)
D 84,3072
mav 8,16,24
R 44,39936
v_mul_f32_e64 v0, v0, |v0|
mav 12,20,24
s_add_u32 m0, s50, 4096
v_mul_f32_e64 v1, v1, |v1|
v_cvt_pk_bf16_f32 v0, v0, v1
mav 8,24,28
R 16,40960
v_pk_max_i16 v56, v0, 0
mav 12,28,28
s_waitcnt lgkmcnt(3)
D 85,0
mav 8,32,32
R 20,41984
v_fma_f32 v68, v67, v50, v92
mav 12,36,32
v_fma_f32 v2, v2, v66, v68
v_fma_f32 v69, v67, v51, v93
mav 8,40,36
R 24,43008
v_fma_f32 v3, v3, v66, v69
mav 12,44,36
s_waitcnt lgkmcnt(3)
D 85,1024
mav 8,48,40
R 28,44032
v_mul_f32_e64 v2, v2, |v2|
mav 12,52,40
v_mul_f32_e64 v3, v3, |v3|
v_cvt_pk_bf16_f32 v2, v2, v3
mav 8,56,44
R 32,45056
v_pk_max_i16 v57, v2, 0
mav 12,60,44
s_waitcnt lgkmcnt(3)
D 85,2048
mav 8,64,16
R 36,46080
v_fma_f32 v68, v67, v52, v94
mav 12,68,16
v_fma_f32 v4, v4, v66, v68
v_fma_f32 v69, v67, v53, v95
mav 8,72,20
R 40,47104
v_fma_f32 v5, v5, v66, v69
mav 12,76,20
s_waitcnt lgkmcnt(3)
D 85,3072
mav 8,80,24
R 44,48128
v_mul_f32_e64 v4, v4, |v4|
mav 12,84,24
v_mul_f32_e64 v5, v5, |v5|
v_cvt_pk_bf16_f32 v4, v4, v5
mav 8,88,28
R 16,49152
v_pk_max_i16 v58, v4, 0
mav 12,92,28
s_waitcnt lgkmcnt(3)
v_fma_f32 v68, v67, v54, v96
v_fma_f32 v6, v6, v66, v68
mav 8,96,32
R 20,50176
v_fma_f32 v69, v67, v55, v97
mav 12,100,32
v_fma_f32 v7, v7, v66, v69
v_mul_f32_e64 v6, v6, |v6|
mav 8,104,36
R 24,51200
v_mul_f32_e64 v7, v7, |v7|
mav 12,108,36
s_waitcnt lgkmcnt(3)
v_cvt_pk_bf16_f32 v6, v6, v7
v_pk_max_i16 v59, v6, 0
mav 8,112,40
s_mov_b32 m0, 144384
R 28,52224
mav 12,116,40
mav 8,120,44
R 32,53248
mav 12,124,44
buffer_store_dwordx4 v[56:59], v83, s[36:39], s49 offen sc1
s_waitcnt lgkmcnt(3)
mav 8,128,16
R 36,54272
mav 12,132,16
mav 8,136,20
R 40,55296
mav 12,140,20
s_waitcnt lgkmcnt(3)
buffer_load_dword v86, s[36:39], 0 offen sc1 lds
mav 8,144,24
R 44,56320
mav 12,148,24
mav 8,152,28
R 16,57344
mav 12,156,28
s_waitcnt lgkmcnt(3)
mav 8,160,32
R 20,58368
v_add_u32 v87, 1, v87
mav 12,164,32
mav 8,168,36
R 24,59392
mav 12,172,36
s_waitcnt lgkmcnt(3)
mav 8,176,40
R 28,60416
mav 12,180,40
mav 8,184,44
R 32,61440
mav 12,188,44
s_waitcnt lgkmcnt(3)
mav 8,192,16
R 36,62464
mav 12,196,16
mav 8,200,20
R 40,63488
mav 12,204,20
s_waitcnt lgkmcnt(3)
mav 8,208,24
R 44,64512
mav 12,212,24
mav 8,216,28
mav 12,220,28
s_waitcnt lgkmcnt(2)
mav 8,224,32
v_add_u32 v64, 1, v64
mav 12,228,32
mav 8,232,36
s_mov_b32 s60, -30
.p2align 6
LE0_4:
mav 12,236,36
s_waitcnt lgkmcnt(0)
mav 8,240,40
mav 12,244,40
mav 8,248,44
mav 12,252,44
ds_read_b32 v70, v82 offset:0
s_waitcnt lgkmcnt(0)
v_cmp_gt_i32 vcc, v87, v70
v_xor_b32 v80, 0x10000, v80
s_waitcnt vmcnt(2)
s_add_u32 s49, s49, 313344
s_cbranch_vccnz CE0_5
BE0_5:
s_barrier
s_add_u32 m0, s50, 32768
R 16,0
D 84,0
ds_read_b64 v[66:67], v88 offset:128
R 20,1024
R 24,2048
D 84,1024
R 28,3072
s_waitcnt vmcnt(3)
buffer_store_dword v64, v81, s[36:39], s52 offen sc1
R 32,4096
D 84,2048
s_waitcnt lgkmcnt(3)
v_mfma_f32_16x16x32_bf16 v[0:3], a[0:3], v[16:19], 0
R 36,5120
v_fma_f32 v68, v67, v48, v90
v_mfma_f32_16x16x32_bf16 v[4:7], a[4:7], v[16:19], 0
v_fma_f32 v8, v8, v66, v68
v_fma_f32 v69, v67, v49, v91
mav 0,8,20
R 40,6144
v_fma_f32 v9, v9, v66, v69
mav 4,12,20
s_waitcnt lgkmcnt(3)
D 84,3072
mav 0,16,24
R 44,7168
v_mul_f32_e64 v8, v8, |v8|
mav 4,20,24
s_add_u32 m0, s50, 36864
v_mul_f32_e64 v9, v9, |v9|
v_cvt_pk_bf16_f32 v8, v8, v9
mav 0,24,28
R 16,8192
v_pk_max_i16 v56, v8, 0
mav 4,28,28
s_waitcnt lgkmcnt(3)
D 85,0
mav 0,32,32
R 20,9216
v_fma_f32 v68, v67, v50, v92
mav 4,36,32
v_fma_f32 v10, v10, v66, v68
v_fma_f32 v69, v67, v51, v93
mav 0,40,36
R 24,10240
v_fma_f32 v11, v11, v66, v69
mav 4,44,36
s_waitcnt lgkmcnt(3)
D 85,1024
mav 0,48,40
R 28,11264
v_mul_f32_e64 v10, v10, |v10|
mav 4,52,40
v_mul_f32_e64 v11, v11, |v11|
v_cvt_pk_bf16_f32 v10, v10, v11
mav 0,56,44
R 32,12288
v_pk_max_i16 v57, v10, 0
mav 4,60,44
s_waitcnt lgkmcnt(3)
D 85,2048
mav 0,64,16
R 36,13312
v_fma_f32 v68, v67, v52, v94
mav 4,68,16
v_fma_f32 v12, v12, v66, v68
v_fma_f32 v69, v67, v53, v95
mav 0,72,20
R 40,14336
v_fma_f32 v13, v13, v66, v69
mav 4,76,20
s_waitcnt lgkmcnt(3)
D 85,3072
mav 0,80,24
R 44,15360
v_mul_f32_e64 v12, v12, |v12|
mav 4,84,24
v_mul_f32_e64 v13, v13, |v13|
v_cvt_pk_bf16_f32 v12, v12, v13
mav 0,88,28
R 16,16384
v_pk_max_i16 v58, v12, 0
mav 4,92,28
s_waitcnt lgkmcnt(3)
v_fma_f32 v68, v67, v54, v96
v_fma_f32 v14, v14, v66, v68
mav 0,96,32
R 20,17408
v_fma_f32 v69, v67, v55, v97
mav 4,100,32
v_fma_f32 v15, v15, v66, v69
v_mul_f32_e64 v14, v14, |v14|
mav 0,104,36
R 24,18432
v_mul_f32_e64 v15, v15, |v15|
mav 4,108,36
LsE2:
s_cbranch_execz LsE6
s_waitcnt lgkmcnt(3)
v_cvt_pk_bf16_f32 v14, v14, v15
v_pk_max_i16 v59, v14, 0
mav 0,112,40
s_mov_b32 m0, 144384
R 28,19456
mav 4,116,40
mav 0,120,44
R 32,20480
mav 4,124,44
buffer_store_dwordx4 v[56:59], v83, s[36:39], s49 offen sc1
s_waitcnt lgkmcnt(3)
mav 0,128,16
R 36,21504
mav 4,132,16
mav 0,136,20
R 40,22528
mav 4,140,20
s_waitcnt lgkmcnt(3)
buffer_load_dword v86, s[36:39], 0 offen sc1 lds
mav 0,144,24
R 44,23552
mav 4,148,24
mav 0,152,28
R 16,24576
mav 4,156,28
s_waitcnt lgkmcnt(3)
mav 0,160,32
R 20,25600
v_add_u32 v87, 1, v87
mav 4,164,32
mav 0,168,36
R 24,26624
mav 4,172,36
s_waitcnt lgkmcnt(3)
mav 0,176,40
R 28,27648
mav 4,180,40
mav 0,184,44
R 32,28672
mav 4,188,44
s_waitcnt lgkmcnt(3)
mav 0,192,16
R 36,29696
mav 4,196,16
mav 0,200,20
R 40,30720
mav 4,204,20
s_waitcnt lgkmcnt(3)
mav 0,208,24
R 44,31744
mav 4,212,24
mav 0,216,28
mav 4,220,28
s_waitcnt lgkmcnt(2)
mav 0,224,32
v_add_u32 v64, 1, v64
mav 4,228,32
mav 0,232,36
mav 4,236,36
s_waitcnt lgkmcnt(0)
mav 0,240,40
mav 4,244,40
mav 0,248,44
mav 4,252,44
ds_read_b32 v70, v82 offset:0
s_xor_b32 s50, s50, 0x10000
s_waitcnt lgkmcnt(0)
v_cmp_gt_i32 vcc, v87, v70
s_waitcnt vmcnt(2)
s_add_u32 s49, s49, 313344
s_cbranch_vccnz CE0_6
BE0_6:
s_barrier
s_add_u32 m0, s50, 0
R 16,32768
D 84,0
ds_read_b64 v[66:67], v88 offset:0
R 20,33792
R 24,34816
D 84,1024
R 28,35840
s_waitcnt vmcnt(3)
buffer_store_dword v64, v81, s[36:39], s52 offen sc1
R 32,36864
D 84,2048
s_waitcnt lgkmcnt(3)
v_mfma_f32_16x16x32_bf16 v[8:11], a[0:3], v[16:19], 0
R 36,37888
v_fma_f32 v68, v67, v48, v90
v_mfma_f32_16x16x32_bf16 v[12:15], a[4:7], v[16:19], 0
v_fma_f32 v0, v0, v66, v68
v_fma_f32 v69, v67, v49, v91
mav 8,8,20
R 40,38912
v_fma_f32 v1, v1, v66, v69
mav 12,12,20
s_waitcnt lgkmcnt(3)
D 84,3072
mav 8,16,24
R 44,39936
v_mul_f32_e64 v0, v0, |v0|
mav 12,20,24
s_add_u32 m0, s50, 4096
v_mul_f32_e64 v1, v1, |v1|
v_cvt_pk_bf16_f32 v0, v0, v1
mav 8,24,28
R 16,40960
v_pk_max_i16 v56, v0, 0
mav 12,28,28
s_waitcnt lgkmcnt(3)
D 85,0
mav 8,32,32
R 20,41984
v_fma_f32 v68, v67, v50, v92
mav 12,36,32
v_fma_f32 v2, v2, v66, v68
v_fma_f32 v69, v67, v51, v93
mav 8,40,36
R 24,43008
v_fma_f32 v3, v3, v66, v69
mav 12,44,36
s_waitcnt lgkmcnt(3)
D 85,1024
mav 8,48,40
R 28,44032
v_mul_f32_e64 v2, v2, |v2|
mav 12,52,40
v_mul_f32_e64 v3, v3, |v3|
v_cvt_pk_bf16_f32 v2, v2, v3
mav 8,56,44
R 32,45056
v_pk_max_i16 v57, v2, 0
mav 12,60,44
s_waitcnt lgkmcnt(3)
D 85,2048
mav 8,64,16
R 36,46080
v_fma_f32 v68, v67, v52, v94
mav 12,68,16
v_fma_f32 v4, v4, v66, v68
v_fma_f32 v69, v67, v53, v95
mav 8,72,20
R 40,47104
v_fma_f32 v5, v5, v66, v69
mav 12,76,20
s_waitcnt lgkmcnt(3)
D 85,3072
mav 8,80,24
R 44,48128
v_mul_f32_e64 v4, v4, |v4|
mav 12,84,24
v_mul_f32_e64 v5, v5, |v5|
v_cvt_pk_bf16_f32 v4, v4, v5
mav 8,88,28
R 16,49152
v_pk_max_i16 v58, v4, 0
mav 12,92,28
s_waitcnt lgkmcnt(3)
v_fma_f32 v68, v67, v54, v96
v_fma_f32 v6, v6, v66, v68
mav 8,96,32
R 20,50176
v_fma_f32 v69, v67, v55, v97
mav 12,100,32
v_fma_f32 v7, v7, v66, v69
v_mul_f32_e64 v6, v6, |v6|
mav 8,104,36
R 24,51200
v_mul_f32_e64 v7, v7, |v7|
mav 12,108,36
s_waitcnt lgkmcnt(3)
v_cvt_pk_bf16_f32 v6, v6, v7
v_pk_max_i16 v59, v6, 0
mav 8,112,40
s_mov_b32 m0, 144384
R 28,52224
mav 12,116,40
mav 8,120,44
R 32,53248
mav 12,124,44
buffer_store_dwordx4 v[56:59], v83, s[36:39], s49 offen sc1
s_waitcnt lgkmcnt(3)
mav 8,128,16
R 36,54272
mav 12,132,16
mav 8,136,20
R 40,55296
mav 12,140,20
s_waitcnt lgkmcnt(3)
buffer_load_dword v86, s[36:39], 0 offen sc1 lds
mav 8,144,24
R 44,56320
mav 12,148,24
mav 8,152,28
R 16,57344
mav 12,156,28
s_waitcnt lgkmcnt(3)
mav 8,160,32
R 20,58368
v_add_u32 v87, 1, v87
mav 12,164,32
mav 8,168,36
R 24,59392
mav 12,172,36
s_waitcnt lgkmcnt(3)
mav 8,176,40
R 28,60416
mav 12,180,40
mav 8,184,44
R 32,61440
mav 12,188,44
s_waitcnt lgkmcnt(3)
mav 8,192,16
R 36,62464
mav 12,196,16
mav 8,200,20
R 40,63488
mav 12,204,20
s_waitcnt lgkmcnt(3)
mav 8,208,24
R 44,64512
mav 12,212,24
mav 8,216,28
mav 12,220,28
s_waitcnt lgkmcnt(2)
mav 8,224,32
v_add_u32 v64, 1, v64
mav 12,228,32
s_add_u32 s60, s60, 1
mav 8,232,36
s_cbranch_scc0 LE0_4
mav 12,236,36
s_waitcnt lgkmcnt(0)
mav 8,240,40
mav 12,244,40
mav 8,248,44
mav 12,252,44
ds_read_b32 v70, v82 offset:0
s_waitcnt lgkmcnt(0)
v_cmp_gt_i32 vcc, v87, v70
v_xor_b32 v80, 0x10000, v80
s_waitcnt vmcnt(2)
s_add_u32 s49, s49, 313344
s_cbranch_vccnz CE0_7
BE0_7:
s_barrier
s_add_u32 m0, s50, 32768
R 16,0
D 84,0
ds_read_b64 v[66:67], v88 offset:128
R 20,1024
R 24,2048
D 84,1024
R 28,3072
s_waitcnt vmcnt(3)
buffer_store_dword v64, v81, s[36:39], s52 offen sc1
R 32,4096
D 84,2048
s_waitcnt lgkmcnt(3)
v_mfma_f32_16x16x32_bf16 v[0:3], a[0:3], v[16:19], 0
R 36,5120
v_fma_f32 v68, v67, v48, v90
v_mfma_f32_16x16x32_bf16 v[4:7], a[4:7], v[16:19], 0
v_fma_f32 v8, v8, v66, v68
v_fma_f32 v69, v67, v49, v91
mav 0,8,20
R 40,6144
v_fma_f32 v9, v9, v66, v69
mav 4,12,20
s_waitcnt lgkmcnt(3)
D 84,3072
mav 0,16,24
R 44,7168
v_mul_f32_e64 v8, v8, |v8|
mav 4,20,24
s_add_u32 m0, s50, 36864
v_mul_f32_e64 v9, v9, |v9|
v_cvt_pk_bf16_f32 v8, v8, v9
mav 0,24,28
R 16,8192
v_pk_max_i16 v56, v8, 0
mav 4,28,28
s_waitcnt lgkmcnt(3)
D 85,0
mav 0,32,32
R 20,9216
v_fma_f32 v68, v67, v50, v92
mav 4,36,32
v_fma_f32 v10, v10, v66, v68
v_fma_f32 v69, v67, v51, v93
mav 0,40,36
R 24,10240
v_fma_f32 v11, v11, v66, v69
mav 4,44,36
s_waitcnt lgkmcnt(3)
D 85,1024
mav 0,48,40
R 28,11264
v_mul_f32_e64 v10, v10, |v10|
mav 4,52,40
v_mul_f32_e64 v11, v11, |v11|
v_cvt_pk_bf16_f32 v10, v10, v11
mav 0,56,44
R 32,12288
v_pk_max_i16 v57, v10, 0
mav 4,60,44
s_waitcnt lgkmcnt(3)
D 85,2048
mav 0,64,16
R 36,13312
v_fma_f32 v68, v67, v52, v94
mav 4,68,16
v_fma_f32 v12, v12, v66, v68
v_fma_f32 v69, v67, v53, v95
mav 0,72,20
R 40,14336
v_fma_f32 v13, v13, v66, v69
mav 4,76,20
s_waitcnt lgkmcnt(3)
D 85,3072
mav 0,80,24
R 44,15360
v_mul_f32_e64 v12, v12, |v12|
mav 4,84,24
v_mul_f32_e64 v13, v13, |v13|
v_cvt_pk_bf16_f32 v12, v12, v13
mav 0,88,28
R 16,16384
v_pk_max_i16 v58, v12, 0
mav 4,92,28
s_waitcnt lgkmcnt(3)
v_fma_f32 v68, v67, v54, v96
v_fma_f32 v14, v14, v66, v68
mav 0,96,32
R 20,17408
v_fma_f32 v69, v67, v55, v97
mav 4,100,32
v_fma_f32 v15, v15, v66, v69
v_mul_f32_e64 v14, v14, |v14|
mav 0,104,36
R 24,18432
v_mul_f32_e64 v15, v15, |v15|
mav 4,108,36
s_waitcnt lgkmcnt(3)
v_cvt_pk_bf16_f32 v14, v14, v15
v_pk_max_i16 v59, v14, 0
mav 0,112,40
s_mov_b32 m0, 144384
R 28,19456
mav 4,116,40
mav 0,120,44
R 32,20480
mav 4,124,44
buffer_store_dwordx4 v[56:59], v83, s[36:39], s49 offen sc1
s_waitcnt lgkmcnt(3)
mav 0,128,16
R 36,21504
mav 4,132,16
mav 0,136,20
R 40,22528
mav 4,140,20
s_waitcnt lgkmcnt(3)
buffer_load_dword v86, s[36:39], 0 offen sc1 lds
mav 0,144,24
R 44,23552
mav 4,148,24
mav 0,152,28
R 16,24576
mav 4,156,28
s_waitcnt lgkmcnt(3)
mav 0,160,32
R 20,25600
v_add_u32 v87, 1, v87
mav 4,164,32
mav 0,168,36
R 24,26624
mav 4,172,36
s_waitcnt lgkmcnt(3)
mav 0,176,40
R 28,27648
mav 4,180,40
mav 0,184,44
R 32,28672
mav 4,188,44
s_waitcnt lgkmcnt(3)
mav 0,192,16
R 36,29696
mav 4,196,16
mav 0,200,20
R 40,30720
mav 4,204,20
s_waitcnt lgkmcnt(3)
mav 0,208,24
R 44,31744
mav 4,212,24
mav 0,216,28
mav 4,220,28
s_waitcnt lgkmcnt(2)
mav 0,224,32
v_add_u32 v64, 1, v64
mav 4,228,32
mav 0,232,36
mav 4,236,36
s_waitcnt lgkmcnt(0)
mav 0,240,40
mav 4,244,40
mav 0,248,44
mav 4,252,44
ds_read_b32 v70, v82 offset:0
s_waitcnt lgkmcnt(0)
v_cmp_gt_i32 vcc, v87, v70
s_waitcnt vmcnt(2)
s_add_u32 s49, s49, 313344
s_cbranch_vccnz CE0_8
BE0_8:
s_barrier
R 16,32768
ds_read_b64 v[66:67], v88 offset:0
R 20,33792
R 24,34816
R 28,35840
s_waitcnt vmcnt(1)
buffer_store_dword v64, v81, s[36:39], s52 offen sc1
R 32,36864
s_waitcnt lgkmcnt(3)
v_mfma_f32_16x16x32_bf16 v[8:11], a[0:3], v[16:19], 0
R 36,37888
v_fma_f32 v68, v67, v48, v90
v_mfma_f32_16x16x32_bf16 v[12:15], a[4:7], v[16:19], 0
v_fma_f32 v0, v0, v66, v68
v_fma_f32 v69, v67, v49, v91
mav 8,8,20
R 40,38912
v_fma_f32 v1, v1, v66, v69
mav 12,12,20
s_waitcnt lgkmcnt(3)
v_mul_f32_e64 v0, v0, |v0|
v_mul_f32_e64 v1, v1, |v1|
mav 8,16,24
R 44,39936
v_cvt_pk_bf16_f32 v0, v0, v1
mav 12,20,24
v_pk_max_i16 v56, v0, 0
v_fma_f32 v68, v67, v50, v92
mav 8,24,28
R 16,40960
v_fma_f32 v2, v2, v66, v68
mav 12,28,28
s_waitcnt lgkmcnt(3)
v_fma_f32 v69, v67, v51, v93
v_fma_f32 v3, v3, v66, v69
mav 8,32,32
R 20,41984
v_mul_f32_e64 v2, v2, |v2|
mav 12,36,32
v_mul_f32_e64 v3, v3, |v3|
v_cvt_pk_bf16_f32 v2, v2, v3
mav 8,40,36
R 24,43008
v_pk_max_i16 v57, v2, 0
mav 12,44,36
s_waitcnt lgkmcnt(3)
v_fma_f32 v68, v67, v52, v94
v_fma_f32 v4, v4, v66, v68
mav 8,48,40
R 28,44032
v_fma_f32 v69, v67, v53, v95
mav 12,52,40
v_fma_f32 v5, v5, v66, v69
v_mul_f32_e64 v4, v4, |v4|
mav 8,56,44
R 32,45056
v_mul_f32_e64 v5, v5, |v5|
mav 12,60,44
s_waitcnt lgkmcnt(3)
v_cvt_pk_bf16_f32 v4, v4, v5
v_pk_max_i16 v58, v4, 0
mav 8,64,16
R 36,46080
v_fma_f32 v68, v67, v54, v96
mav 12,68,16
v_fma_f32 v6, v6, v66, v68
v_fma_f32 v69, v67, v55, v97
mav 8,72,20
R 40,47104
v_fma_f32 v7, v7, v66, v69
mav 12,76,20
s_waitcnt lgkmcnt(3)
v_mul_f32_e64 v6, v6, |v6|
v_mul_f32_e64 v7, v7, |v7|
mav 8,80,24
R 44,48128
v_cvt_pk_bf16_f32 v6, v6, v7
mav 12,84,24
v_pk_max_i16 v59, v6, 0
mav 8,88,28
R 16,49152
mav 12,92,28
buffer_store_dwordx4 v[56:59], v83, s[36:39], s49 offen sc1
s_waitcnt lgkmcnt(3)
mav 8,96,32
R 20,50176
mav 12,100,32
mav 8,104,36
R 24,51200
mav 12,108,36
s_waitcnt lgkmcnt(3)
mav 8,112,40
R 28,52224
mav 12,116,40
mav 8,120,44
R 32,53248
mav 12,124,44
s_waitcnt lgkmcnt(3)
mav 8,128,16
R 36,54272
mav 12,132,16
mav 8,136,20
R 40,55296
mav 12,140,20
s_waitcnt lgkmcnt(3)
mav 8,144,24
R 44,56320
mav 12,148,24
mav 8,152,28
R 16,57344
mav 12,156,28
s_waitcnt lgkmcnt(3)
mav 8,160,32
R 20,58368
mav 12,164,32
mav 8,168,36
R 24,59392
mav 12,172,36
s_waitcnt lgkmcnt(3)
mav 8,176,40
R 28,60416
mav 12,180,40
mav 8,184,44
R 32,61440
mav 12,188,44
s_waitcnt lgkmcnt(3)
mav 8,192,16
R 36,62464
mav 12,196,16
mav 8,200,20
R 40,63488
mav 12,204,20
s_waitcnt lgkmcnt(3)
mav 8,208,24
R 44,64512
mav 12,212,24
mav 8,216,28
mav 12,220,28
s_waitcnt lgkmcnt(2)
LsE3:
s_cbranch_execz LsE7
mav 8,224,32
v_add_u32 v64, 1, v64
mav 12,228,32
mav 8,232,36
mav 12,236,36
s_waitcnt lgkmcnt(0)
mav 8,240,40
mav 12,244,40
mav 8,248,44
mav 12,252,44
s_add_u32 s49, s49, 313344
s_barrier
ds_read_b64 v[66:67], v88 offset:128
s_waitcnt vmcnt(0)
buffer_store_dword v64, v81, s[36:39], s52 offen sc1
s_waitcnt lgkmcnt(0)
v_fma_f32 v68, v67, v48, v90
v_fma_f32 v8, v8, v66, v68
v_fma_f32 v69, v67, v49, v91
v_fma_f32 v9, v9, v66, v69
v_mul_f32_e64 v8, v8, |v8|
v_mul_f32_e64 v9, v9, |v9|
v_cvt_pk_bf16_f32 v8, v8, v9
v_pk_max_i16 v56, v8, 0
v_fma_f32 v68, v67, v50, v92
v_fma_f32 v10, v10, v66, v68
v_fma_f32 v69, v67, v51, v93
v_fma_f32 v11, v11, v66, v69
v_mul_f32_e64 v10, v10, |v10|
v_mul_f32_e64 v11, v11, |v11|
v_cvt_pk_bf16_f32 v10, v10, v11
v_pk_max_i16 v57, v10, 0
v_fma_f32 v68, v67, v52, v94
v_fma_f32 v12, v12, v66, v68
v_fma_f32 v69, v67, v53, v95
v_fma_f32 v13, v13, v66, v69
v_mul_f32_e64 v12, v12, |v12|
v_mul_f32_e64 v13, v13, |v13|
v_cvt_pk_bf16_f32 v12, v12, v13
v_pk_max_i16 v58, v12, 0
v_fma_f32 v68, v67, v54, v96
v_fma_f32 v14, v14, v66, v68
v_fma_f32 v69, v67, v55, v97
v_fma_f32 v15, v15, v66, v69
v_mul_f32_e64 v14, v14, |v14|
v_mul_f32_e64 v15, v15, |v15|
v_cvt_pk_bf16_f32 v14, v14, v15
v_pk_max_i16 v59, v14, 0
buffer_store_dwordx4 v[56:59], v83, s[36:39], s49 offen sc1
v_add_u32 v64, 37, v64
s_waitcnt vmcnt(0)
buffer_store_dword v64, v81, s[36:39], s52 offen sc1
s_endpgm
CE0_1:
s_cmp_eq_u32 s61, 0
s_cbranch_scc1 BE0_1
s_sub_u32 s61, s61, 1
s_sleep 8
buffer_load_dwordx2 v[70:71], v86, s[36:39], 0 offen sc1
s_waitcnt vmcnt(0)
ds_write_b32 v82, v70
v_sub_u32 v70, v70, v87
v_xor_b32 v71, v71, v65
v_cmp_lt_u64 vcc, s[62:63], v[70:71]
s_waitcnt lgkmcnt(0)
s_cbranch_vccnz CE0_1
s_branch BE0_1
CE0_2:
s_mov_b32 s58, m0
s_mov_b32 m0, 144384
DE0_2:
s_cmp_eq_u32 s61, 0
s_cbranch_scc1 EE0_2
s_sub_u32 s61, s61, 1
buffer_load_dword v86, s[36:39], 0 offen sc1 lds
s_sleep 1
ds_read_b32 v70, v82
s_waitcnt lgkmcnt(0)
v_cmp_gt_i32 vcc, v87, v70
s_cbranch_vccnz DE0_2
EE0_2:
s_mov_b32 m0, s58
s_branch BE0_2
CE0_3:
s_mov_b32 s58, m0
s_mov_b32 m0, 144384
DE0_3:
s_cmp_eq_u32 s61, 0
s_cbranch_scc1 EE0_3
s_sub_u32 s61, s61, 1
buffer_load_dword v86, s[36:39], 0 offen sc1 lds
s_sleep 1
ds_read_b32 v70, v82
s_waitcnt lgkmcnt(0)
v_cmp_gt_i32 vcc, v87, v70
s_cbranch_vccnz DE0_3
EE0_3:
s_mov_b32 m0, s58
s_branch BE0_3
CE0_5:
s_mov_b32 s58, m0
s_mov_b32 m0, 144384
DE0_5:
s_cmp_eq_u32 s61, 0
s_cbranch_scc1 EE0_5
s_sub_u32 s61, s61, 1
buffer_load_dword v86, s[36:39], 0 offen sc1 lds
s_sleep 1
ds_read_b32 v70, v82
s_waitcnt lgkmcnt(0)
v_cmp_gt_i32 vcc, v87, v70
s_cbranch_vccnz DE0_5
EE0_5:
s_mov_b32 m0, s58
s_branch BE0_5
CE0_6:
s_mov_b32 s58, m0
s_mov_b32 m0, 144384
DE0_6:
s_cmp_eq_u32 s61, 0
s_cbranch_scc1 EE0_6
s_sub_u32 s61, s61, 1
buffer_load_dword v86, s[36:39], 0 offen sc1 lds
s_sleep 1
ds_read_b32 v70, v82
s_waitcnt lgkmcnt(0)
v_cmp_gt_i32 vcc, v87, v70
s_cbranch_vccnz DE0_6
EE0_6:
s_mov_b32 m0, s58
s_branch BE0_6
CE0_7:
s_mov_b32 s58, m0
s_mov_b32 m0, 144384
DE0_7:
s_cmp_eq_u32 s61, 0
s_cbranch_scc1 EE0_7
s_sub_u32 s61, s61, 1
buffer_load_dword v86, s[36:39], 0 offen sc1 lds
s_sleep 1
ds_read_b32 v70, v82
s_waitcnt lgkmcnt(0)
v_cmp_gt_i32 vcc, v87, v70
s_cbranch_vccnz DE0_7
EE0_7:
s_mov_b32 m0, s58
s_branch BE0_7
CE0_8:
s_mov_b32 s58, m0
s_mov_b32 m0, 144384
DE0_8:
s_cmp_eq_u32 s61, 0
s_cbranch_scc1 EE0_8
s_sub_u32 s61, s61, 1
buffer_load_dword v86, s[36:39], 0 offen sc1 lds
s_sleep 1
ds_read_b32 v70, v82
s_waitcnt lgkmcnt(0)
v_cmp_gt_i32 vcc, v87, v70
s_cbranch_vccnz DE0_8
EE0_8:
s_mov_b32 m0, s58
s_branch BE0_8
LvE1:
LsE4:
s_cbranch_execz LsE8
s_add_u32 s49, s49, 313344
s_barrier
s_add_u32 m0, s50, 0
s_nop 0
D 84,0
D 84,1024
D 84,2048
D 84,3072
s_add_u32 m0, s50, 4096
s_nop 0
D 85,0
D 85,1024
D 85,2048
D 85,3072
s_waitcnt vmcnt(0)
s_add_u32 s49, s49, 313344
s_barrier
s_add_u32 m0, s50, 32768
R 16,0
buffer_load_dwordx2 v[72:73], v89, s[36:39], s49 offen sc1
buffer_load_dwordx2 v[74:75], v89, s[36:39], s49 offen offset:512 sc1
D 84,0
R 20,1024
R 24,2048
D 84,1024
R 28,3072
R 32,4096
D 84,2048
s_waitcnt lgkmcnt(3)
v_mfma_f32_16x16x32_bf16 v[0:3], a[0:3], v[16:19], 0
R 36,5120
v_mfma_f32_16x16x32_bf16 v[4:7], a[4:7], v[16:19], 0
mav 0,8,20
R 40,6144
mav 4,12,20
s_waitcnt lgkmcnt(3)
mav 0,16,24
R 44,7168
mav 4,20,24
mav 0,24,28
D 84,3072
mav 4,28,28
s_waitcnt lgkmcnt(2)
R 16,8192
s_add_u32 m0, s50, 36864
mav 0,32,32
R 20,9216
mav 4,36,32
mav 0,40,36
D 85,0
mav 4,44,36
s_waitcnt lgkmcnt(2)
R 24,10240
mav 0,48,40
R 28,11264
mav 4,52,40
mav 0,56,44
D 85,1024
mav 4,60,44
s_waitcnt lgkmcnt(2)
R 32,12288
mav 0,64,16
R 36,13312
mav 4,68,16
mav 0,72,20
D 85,2048
mav 4,76,20
s_waitcnt lgkmcnt(2)
R 40,14336
mav 0,80,24
R 44,15360
mav 4,84,24
mav 0,88,28
D 85,3072
mav 4,92,28
s_waitcnt lgkmcnt(2)
R 16,16384
mav 0,96,32
R 20,17408
mav 4,100,32
mav 0,104,36
R 24,18432
mav 4,108,36
s_waitcnt lgkmcnt(3)
mav 0,112,40
R 28,19456
mav 4,116,40
mav 0,120,44
R 32,20480
mav 4,124,44
s_waitcnt lgkmcnt(3)
mav 0,128,16
R 36,21504
mav 4,132,16
mav 0,136,20
R 40,22528
mav 4,140,20
s_waitcnt vmcnt(8) lgkmcnt(3)
v_add_f32 v72, v72, v74
v_add_f32 v73, v73, v75
mav 0,144,24
R 44,23552
mav 4,148,24
v_permlane32_swap_b32 v72, v73
mav 0,152,28
R 16,24576
v_add_f32 v74, v72, v73
mav 4,156,28
s_waitcnt lgkmcnt(3)
v_add_f32 v75, v72, v73
mav 0,160,32
R 20,25600
mav 4,164,32
v_permlane16_swap_b32 v74, v75
mav 0,168,36
R 24,26624
v_add_f32 v72, v74, v75
mav 4,172,36
s_waitcnt lgkmcnt(3)
v_add_f32 v73, v74, v75
mav 0,176,40
R 28,27648
mav 4,180,40
v_permlane32_swap_b32 v72, v73
mav 0,184,44
R 32,28672
v_mul_f32 v74, 0x3a800000, v72
mav 4,188,44
s_waitcnt lgkmcnt(3)
v_mul_f32 v75, 0x3a800000, v73
v_fma_f32 v75, -v74, v74, v75
mav 0,192,16
R 36,29696
v_max_f32 v75, 0, v75
mav 4,196,16
v_add_f32 v75, 0x3727c5ac, v75
mav 0,200,20
R 40,30720
mav 4,204,20
s_waitcnt lgkmcnt(3)
v_rsq_f32 v72, v75
mav 0,208,24
R 44,31744
v_mul_f32_e64 v73, -v74, v72
mav 4,212,24
mav 0,216,28
mav 4,220,28
s_waitcnt lgkmcnt(2)
mav 0,224,32
mav 4,228,32
mav 0,232,36
mav 4,236,36
s_waitcnt lgkmcnt(0)
mav 0,240,40
mav 4,244,40
mav 0,248,44
mav 4,252,44
ds_write_b64 v88, v[72:73] offset:0
s_xor_b32 s50, s50, 0x10000
s_waitcnt vmcnt(0) lgkmcnt(0)
s_add_u32 s49, s49, 313344
s_barrier
s_add_u32 m0, s50, 0
R 16,32768
buffer_load_dwordx2 v[72:73], v89, s[36:39], s49 offen sc1
buffer_load_dwordx2 v[74:75], v89, s[36:39], s49 offen offset:512 sc1
D 84,0
ds_read_b64 v[66:67], v88 offset:0
R 20,33792
R 24,34816
D 84,1024
R 28,35840
R 32,36864
D 84,2048
s_waitcnt lgkmcnt(3)
v_mfma_f32_16x16x32_bf16 v[8:11], a[0:3], v[16:19], 0
R 36,37888
v_fma_f32 v68, v67, v48, v90
v_mfma_f32_16x16x32_bf16 v[12:15], a[4:7], v[16:19], 0
v_fma_f32 v0, v0, v66, v68
v_fma_f32 v69, v67, v49, v91
mav 8,8,20
R 40,38912
v_fma_f32 v1, v1, v66, v69
mav 12,12,20
s_waitcnt lgkmcnt(3)
v_mul_f32_e64 v0, v0, |v0|
v_mul_f32_e64 v1, v1, |v1|
mav 8,16,24
R 44,39936
v_cvt_pk_bf16_f32 v0, v0, v1
mav 12,20,24
v_pk_max_i16 v56, v0, 0
v_fma_f32 v68, v67, v50, v92
mav 8,24,28
D 84,3072
mav 12,28,28
s_waitcnt lgkmcnt(2)
R 16,40960
s_add_u32 m0, s50, 4096
mav 8,32,32
R 20,41984
v_fma_f32 v2, v2, v66, v68
mav 12,36,32
v_fma_f32 v69, v67, v51, v93
v_fma_f32 v3, v3, v66, v69
mav 8,40,36
D 85,0
mav 12,44,36
s_waitcnt lgkmcnt(2)
R 24,43008
v_mul_f32_e64 v2, v2, |v2|
mav 8,48,40
R 28,44032
v_mul_f32_e64 v3, v3, |v3|
mav 12,52,40
v_cvt_pk_bf16_f32 v2, v2, v3
v_pk_max_i16 v57, v2, 0
mav 8,56,44
D 85,1024
mav 12,60,44
s_waitcnt lgkmcnt(2)
R 32,45056
v_fma_f32 v68, v67, v52, v94
mav 8,64,16
R 36,46080
v_fma_f32 v4, v4, v66, v68
mav 12,68,16
v_fma_f32 v69, v67, v53, v95
v_fma_f32 v5, v5, v66, v69
mav 8,72,20
D 85,2048
mav 12,76,20
s_waitcnt lgkmcnt(2)
R 40,47104
v_mul_f32_e64 v4, v4, |v4|
mav 8,80,24
R 44,48128
v_mul_f32_e64 v5, v5, |v5|
mav 12,84,24
v_cvt_pk_bf16_f32 v4, v4, v5
v_pk_max_i16 v58, v4, 0
mav 8,88,28
D 85,3072
mav 12,92,28
s_waitcnt lgkmcnt(2)
R 16,49152
v_fma_f32 v68, v67, v54, v96
mav 8,96,32
R 20,50176
v_fma_f32 v6, v6, v66, v68
mav 12,100,32
v_fma_f32 v69, v67, v55, v97
v_fma_f32 v7, v7, v66, v69
mav 8,104,36
R 24,51200
v_mul_f32_e64 v6, v6, |v6|
mav 12,108,36
s_waitcnt lgkmcnt(3)
v_mul_f32_e64 v7, v7, |v7|
v_cvt_pk_bf16_f32 v6, v6, v7
mav 8,112,40
R 28,52224
v_pk_max_i16 v59, v6, 0
mav 12,116,40
mav 8,120,44
buffer_store_dwordx4 v[56:59], v83, s[36:39], s49 offen sc1
R 32,53248
mav 12,124,44
s_waitcnt lgkmcnt(3)
mav 8,128,16
R 36,54272
mav 12,132,16
mav 8,136,20
R 40,55296
mav 12,140,20
s_waitcnt vmcnt(9) lgkmcnt(3)
v_add_f32 v72, v72, v74
v_add_f32 v73, v73, v75
mav 8,144,24
R 44,56320
mav 12,148,24
v_permlane32_swap_b32 v72, v73
mav 8,152,28
R 16,57344
v_add_f32 v74, v72, v73
mav 12,156,28
s_waitcnt lgkmcnt(3)
v_add_f32 v75, v72, v73
mav 8,160,32
R 20,58368
mav 12,164,32
v_permlane16_swap_b32 v74, v75
mav 8,168,36
R 24,59392
v_add_f32 v72, v74, v75
mav 12,172,36
s_waitcnt lgkmcnt(3)
v_add_f32 v73, v74, v75
mav 8,176,40
R 28,60416
mav 12,180,40
v_permlane32_swap_b32 v72, v73
mav 8,184,44
R 32,61440
v_mul_f32 v74, 0x3a800000, v72
mav 12,188,44
s_waitcnt lgkmcnt(3)
v_mul_f32 v75, 0x3a800000, v73
v_fma_f32 v75, -v74, v74, v75
mav 8,192,16
R 36,62464
v_max_f32 v75, 0, v75
mav 12,196,16
v_add_f32 v75, 0x3727c5ac, v75
mav 8,200,20
R 40,63488
mav 12,204,20
s_waitcnt lgkmcnt(3)
v_rsq_f32 v72, v75
mav 8,208,24
R 44,64512
v_mul_f32_e64 v73, -v74, v72
mav 12,212,24
mav 8,216,28
mav 12,220,28
s_waitcnt lgkmcnt(2)
mav 8,224,32
v_add_u32 v64, 1, v64
mav 12,228,32
mav 8,232,36
s_mov_b32 s60, -30
.p2align 6
LE1_1:
mav 12,236,36
s_waitcnt lgkmcnt(0)
mav 8,240,40
mav 12,244,40
mav 8,248,44
mav 12,252,44
ds_write_b64 v88, v[72:73] offset:128
v_xor_b32 v80, 0x10000, v80
s_waitcnt vmcnt(1) lgkmcnt(0)
s_add_u32 s49, s49, 313344
s_barrier
s_add_u32 m0, s50, 32768
R 16,0
buffer_load_dwordx2 v[72:73], v89, s[36:39], s49 offen sc1
buffer_load_dwordx2 v[74:75], v89, s[36:39], s49 offen offset:512 sc1
D 84,0
ds_read_b64 v[66:67], v88 offset:128
R 20,1024
R 24,2048
D 84,1024
R 28,3072
s_waitcnt vmcnt(4)
buffer_store_dword v64, v81, s[36:39], s52 offen sc1
R 32,4096
D 84,2048
s_waitcnt lgkmcnt(3)
v_mfma_f32_16x16x32_bf16 v[0:3], a[0:3], v[16:19], 0
R 36,5120
v_fma_f32 v68, v67, v48, v90
v_mfma_f32_16x16x32_bf16 v[4:7], a[4:7], v[16:19], 0
v_fma_f32 v8, v8, v66, v68
v_fma_f32 v69, v67, v49, v91
mav 0,8,20
R 40,6144
v_fma_f32 v9, v9, v66, v69
mav 4,12,20
s_waitcnt lgkmcnt(3)
v_mul_f32_e64 v8, v8, |v8|
v_mul_f32_e64 v9, v9, |v9|
mav 0,16,24
R 44,7168
v_cvt_pk_bf16_f32 v8, v8, v9
mav 4,20,24
v_pk_max_i16 v56, v8, 0
v_fma_f32 v68, v67, v50, v92
mav 0,24,28
D 84,3072
mav 4,28,28
s_waitcnt lgkmcnt(2)
R 16,8192
s_add_u32 m0, s50, 36864
mav 0,32,32
R 20,9216
v_fma_f32 v10, v10, v66, v68
mav 4,36,32
v_fma_f32 v69, v67, v51, v93
v_fma_f32 v11, v11, v66, v69
mav 0,40,36
D 85,0
mav 4,44,36
s_waitcnt lgkmcnt(2)
R 24,10240
v_mul_f32_e64 v10, v10, |v10|
mav 0,48,40
R 28,11264
v_mul_f32_e64 v11, v11, |v11|
mav 4,52,40
v_cvt_pk_bf16_f32 v10, v10, v11
v_pk_max_i16 v57, v10, 0
mav 0,56,44
D 85,1024
mav 4,60,44
s_waitcnt lgkmcnt(2)
R 32,12288
v_fma_f32 v68, v67, v52, v94
mav 0,64,16
R 36,13312
v_fma_f32 v12, v12, v66, v68
mav 4,68,16
v_fma_f32 v69, v67, v53, v95
v_fma_f32 v13, v13, v66, v69
mav 0,72,20
D 85,2048
mav 4,76,20
s_waitcnt lgkmcnt(2)
R 40,14336
v_mul_f32_e64 v12, v12, |v12|
mav 0,80,24
R 44,15360
v_mul_f32_e64 v13, v13, |v13|
mav 4,84,24
v_cvt_pk_bf16_f32 v12, v12, v13
v_pk_max_i16 v58, v12, 0
mav 0,88,28
D 85,3072
mav 4,92,28
s_waitcnt lgkmcnt(2)
R 16,16384
v_fma_f32 v68, v67, v54, v96
mav 0,96,32
R 20,17408
v_fma_f32 v14, v14, v66, v68
mav 4,100,32
v_fma_f32 v69, v67, v55, v97
v_fma_f32 v15, v15, v66, v69
mav 0,104,36
R 24,18432
v_mul_f32_e64 v14, v14, |v14|
mav 4,108,36
s_waitcnt lgkmcnt(3)
LsE5:
s_cbranch_execz LsE9
v_mul_f32_e64 v15, v15, |v15|
v_cvt_pk_bf16_f32 v14, v14, v15
mav 0,112,40
R 28,19456
v_pk_max_i16 v59, v14, 0
mav 4,116,40
mav 0,120,44
buffer_store_dwordx4 v[56:59], v83, s[36:39], s49 offen sc1
R 32,20480
mav 4,124,44
s_waitcnt lgkmcnt(3)
mav 0,128,16
R 36,21504
mav 4,132,16
mav 0,136,20
R 40,22528
mav 4,140,20
s_waitcnt vmcnt(10) lgkmcnt(3)
v_add_f32 v72, v72, v74
v_add_f32 v73, v73, v75
mav 0,144,24
R 44,23552
mav 4,148,24
v_permlane32_swap_b32 v72, v73
mav 0,152,28
R 16,24576
v_add_f32 v74, v72, v73
mav 4,156,28
s_waitcnt lgkmcnt(3)
v_add_f32 v75, v72, v73
mav 0,160,32
R 20,25600
mav 4,164,32
v_permlane16_swap_b32 v74, v75
mav 0,168,36
R 24,26624
v_add_f32 v72, v74, v75
mav 4,172,36
s_waitcnt lgkmcnt(3)
v_add_f32 v73, v74, v75
mav 0,176,40
R 28,27648
mav 4,180,40
v_permlane32_swap_b32 v72, v73
mav 0,184,44
R 32,28672
v_mul_f32 v74, 0x3a800000, v72
mav 4,188,44
s_waitcnt lgkmcnt(3)
v_mul_f32 v75, 0x3a800000, v73
v_fma_f32 v75, -v74, v74, v75
mav 0,192,16
R 36,29696
v_max_f32 v75, 0, v75
mav 4,196,16
v_add_f32 v75, 0x3727c5ac, v75
mav 0,200,20
R 40,30720
mav 4,204,20
s_waitcnt lgkmcnt(3)
v_rsq_f32 v72, v75
mav 0,208,24
R 44,31744
v_mul_f32_e64 v73, -v74, v72
mav 4,212,24
mav 0,216,28
mav 4,220,28
s_waitcnt lgkmcnt(2)
mav 0,224,32
v_add_u32 v64, 1, v64
mav 4,228,32
mav 0,232,36
mav 4,236,36
s_waitcnt lgkmcnt(0)
mav 0,240,40
mav 4,244,40
mav 0,248,44
mav 4,252,44
ds_write_b64 v88, v[72:73] offset:0
s_xor_b32 s50, s50, 0x10000
s_waitcnt vmcnt(1) lgkmcnt(0)
s_add_u32 s49, s49, 313344
s_barrier
s_add_u32 m0, s50, 0
R 16,32768
buffer_load_dwordx2 v[72:73], v89, s[36:39], s49 offen sc1
buffer_load_dwordx2 v[74:75], v89, s[36:39], s49 offen offset:512 sc1
D 84,0
ds_read_b64 v[66:67], v88 offset:0
R 20,33792
R 24,34816
D 84,1024
R 28,35840
s_waitcnt vmcnt(4)
buffer_store_dword v64, v81, s[36:39], s52 offen sc1
R 32,36864
D 84,2048
s_waitcnt lgkmcnt(3)
v_mfma_f32_16x16x32_bf16 v[8:11], a[0:3], v[16:19], 0
R 36,37888
v_fma_f32 v68, v67, v48, v90
v_mfma_f32_16x16x32_bf16 v[12:15], a[4:7], v[16:19], 0
v_fma_f32 v0, v0, v66, v68
v_fma_f32 v69, v67, v49, v91
mav 8,8,20
R 40,38912
v_fma_f32 v1, v1, v66, v69
mav 12,12,20
s_waitcnt lgkmcnt(3)
v_mul_f32_e64 v0, v0, |v0|
v_mul_f32_e64 v1, v1, |v1|
mav 8,16,24
R 44,39936
v_cvt_pk_bf16_f32 v0, v0, v1
mav 12,20,24
v_pk_max_i16 v56, v0, 0
v_fma_f32 v68, v67, v50, v92
mav 8,24,28
D 84,3072
mav 12,28,28
s_waitcnt lgkmcnt(2)
R 16,40960
s_add_u32 m0, s50, 4096
mav 8,32,32
R 20,41984
v_fma_f32 v2, v2, v66, v68
mav 12,36,32
v_fma_f32 v69, v67, v51, v93
v_fma_f32 v3, v3, v66, v69
mav 8,40,36
D 85,0
mav 12,44,36
s_waitcnt lgkmcnt(2)
R 24,43008
v_mul_f32_e64 v2, v2, |v2|
mav 8,48,40
R 28,44032
v_mul_f32_e64 v3, v3, |v3|
mav 12,52,40
v_cvt_pk_bf16_f32 v2, v2, v3
v_pk_max_i16 v57, v2, 0
mav 8,56,44
D 85,1024
mav 12,60,44
s_waitcnt lgkmcnt(2)
R 32,45056
v_fma_f32 v68, v67, v52, v94
mav 8,64,16
R 36,46080
v_fma_f32 v4, v4, v66, v68
mav 12,68,16
v_fma_f32 v69, v67, v53, v95
v_fma_f32 v5, v5, v66, v69
mav 8,72,20
D 85,2048
mav 12,76,20
s_waitcnt lgkmcnt(2)
R 40,47104
v_mul_f32_e64 v4, v4, |v4|
mav 8,80,24
R 44,48128
v_mul_f32_e64 v5, v5, |v5|
mav 12,84,24
v_cvt_pk_bf16_f32 v4, v4, v5
v_pk_max_i16 v58, v4, 0
mav 8,88,28
D 85,3072
mav 12,92,28
s_waitcnt lgkmcnt(2)
R 16,49152
v_fma_f32 v68, v67, v54, v96
mav 8,96,32
R 20,50176
v_fma_f32 v6, v6, v66, v68
mav 12,100,32
v_fma_f32 v69, v67, v55, v97
v_fma_f32 v7, v7, v66, v69
mav 8,104,36
R 24,51200
v_mul_f32_e64 v6, v6, |v6|
mav 12,108,36
s_waitcnt lgkmcnt(3)
v_mul_f32_e64 v7, v7, |v7|
v_cvt_pk_bf16_f32 v6, v6, v7
mav 8,112,40
R 28,52224
v_pk_max_i16 v59, v6, 0
mav 12,116,40
mav 8,120,44
buffer_store_dwordx4 v[56:59], v83, s[36:39], s49 offen sc1
R 32,53248
mav 12,124,44
s_waitcnt lgkmcnt(3)
mav 8,128,16
R 36,54272
mav 12,132,16
mav 8,136,20
R 40,55296
mav 12,140,20
s_waitcnt vmcnt(10) lgkmcnt(3)
v_add_f32 v72, v72, v74
v_add_f32 v73, v73, v75
mav 8,144,24
R 44,56320
mav 12,148,24
v_permlane32_swap_b32 v72, v73
mav 8,152,28
R 16,57344
v_add_f32 v74, v72, v73
mav 12,156,28
s_waitcnt lgkmcnt(3)
v_add_f32 v75, v72, v73
mav 8,160,32
R 20,58368
mav 12,164,32
v_permlane16_swap_b32 v74, v75
mav 8,168,36
R 24,59392
v_add_f32 v72, v74, v75
mav 12,172,36
s_waitcnt lgkmcnt(3)
v_add_f32 v73, v74, v75
mav 8,176,40
R 28,60416
mav 12,180,40
v_permlane32_swap_b32 v72, v73
mav 8,184,44
R 32,61440
v_mul_f32 v74, 0x3a800000, v72
mav 12,188,44
s_waitcnt lgkmcnt(3)
v_mul_f32 v75, 0x3a800000, v73
v_fma_f32 v75, -v74, v74, v75
mav 8,192,16
R 36,62464
v_max_f32 v75, 0, v75
mav 12,196,16
v_add_f32 v75, 0x3727c5ac, v75
mav 8,200,20
R 40,63488
mav 12,204,20
s_waitcnt lgkmcnt(3)
v_rsq_f32 v72, v75
mav 8,208,24
R 44,64512
v_mul_f32_e64 v73, -v74, v72
mav 12,212,24
mav 8,216,28
mav 12,220,28
s_waitcnt lgkmcnt(2)
mav 8,224,32
v_add_u32 v64, 1, v64
mav 12,228,32
s_add_u32 s60, s60, 1
mav 8,232,36
s_cbranch_scc0 LE1_1
mav 12,236,36
s_waitcnt lgkmcnt(0)
mav 8,240,40
mav 12,244,40
mav 8,248,44
mav 12,252,44
ds_write_b64 v88, v[72:73] offset:128
v_xor_b32 v80, 0x10000, v80
s_waitcnt vmcnt(1) lgkmcnt(0)
s_add_u32 s49, s49, 313344
s_barrier
s_add_u32 m0, s50, 32768
R 16,0
buffer_load_dwordx2 v[72:73], v89, s[36:39], s49 offen sc1
buffer_load_dwordx2 v[74:75], v89, s[36:39], s49 offen offset:512 sc1
D 84,0
ds_read_b64 v[66:67], v88 offset:128
R 20,1024
R 24,2048
D 84,1024
R 28,3072
s_waitcnt vmcnt(4)
buffer_store_dword v64, v81, s[36:39], s52 offen sc1
R 32,4096
D 84,2048
s_waitcnt lgkmcnt(3)
v_mfma_f32_16x16x32_bf16 v[0:3], a[0:3], v[16:19], 0
R 36,5120
v_fma_f32 v68, v67, v48, v90
v_mfma_f32_16x16x32_bf16 v[4:7], a[4:7], v[16:19], 0
v_fma_f32 v8, v8, v66, v68
v_fma_f32 v69, v67, v49, v91
mav 0,8,20
R 40,6144
v_fma_f32 v9, v9, v66, v69
mav 4,12,20
s_waitcnt lgkmcnt(3)
v_mul_f32_e64 v8, v8, |v8|
v_mul_f32_e64 v9, v9, |v9|
mav 0,16,24
R 44,7168
v_cvt_pk_bf16_f32 v8, v8, v9
mav 4,20,24
v_pk_max_i16 v56, v8, 0
v_fma_f32 v68, v67, v50, v92
mav 0,24,28
D 84,3072
mav 4,28,28
s_waitcnt lgkmcnt(2)
R 16,8192
s_add_u32 m0, s50, 36864
mav 0,32,32
R 20,9216
v_fma_f32 v10, v10, v66, v68
mav 4,36,32
v_fma_f32 v69, v67, v51, v93
v_fma_f32 v11, v11, v66, v69
mav 0,40,36
D 85,0
mav 4,44,36
s_waitcnt lgkmcnt(2)
R 24,10240
v_mul_f32_e64 v10, v10, |v10|
mav 0,48,40
R 28,11264
v_mul_f32_e64 v11, v11, |v11|
mav 4,52,40
v_cvt_pk_bf16_f32 v10, v10, v11
v_pk_max_i16 v57, v10, 0
mav 0,56,44
D 85,1024
mav 4,60,44
s_waitcnt lgkmcnt(2)
R 32,12288
v_fma_f32 v68, v67, v52, v94
mav 0,64,16
R 36,13312
v_fma_f32 v12, v12, v66, v68
mav 4,68,16
v_fma_f32 v69, v67, v53, v95
v_fma_f32 v13, v13, v66, v69
mav 0,72,20
D 85,2048
mav 4,76,20
s_waitcnt lgkmcnt(2)
R 40,14336
v_mul_f32_e64 v12, v12, |v12|
mav 0,80,24
R 44,15360
v_mul_f32_e64 v13, v13, |v13|
mav 4,84,24
v_cvt_pk_bf16_f32 v12, v12, v13
v_pk_max_i16 v58, v12, 0
mav 0,88,28
D 85,3072
mav 4,92,28
s_waitcnt lgkmcnt(2)
R 16,16384
v_fma_f32 v68, v67, v54, v96
mav 0,96,32
R 20,17408
v_fma_f32 v14, v14, v66, v68
mav 4,100,32
v_fma_f32 v69, v67, v55, v97
v_fma_f32 v15, v15, v66, v69
mav 0,104,36
R 24,18432
v_mul_f32_e64 v14, v14, |v14|
mav 4,108,36
s_waitcnt lgkmcnt(3)
v_mul_f32_e64 v15, v15, |v15|
v_cvt_pk_bf16_f32 v14, v14, v15
mav 0,112,40
R 28,19456
v_pk_max_i16 v59, v14, 0
mav 4,116,40
mav 0,120,44
buffer_store_dwordx4 v[56:59], v83, s[36:39], s49 offen sc1
R 32,20480
mav 4,124,44
s_waitcnt lgkmcnt(3)
mav 0,128,16
R 36,21504
mav 4,132,16
mav 0,136,20
R 40,22528
mav 4,140,20
s_waitcnt vmcnt(10) lgkmcnt(3)
v_add_f32 v72, v72, v74
v_add_f32 v73, v73, v75
mav 0,144,24
R 44,23552
mav 4,148,24
v_permlane32_swap_b32 v72, v73
mav 0,152,28
R 16,24576
v_add_f32 v74, v72, v73
mav 4,156,28
s_waitcnt lgkmcnt(3)
v_add_f32 v75, v72, v73
mav 0,160,32
R 20,25600
mav 4,164,32
v_permlane16_swap_b32 v74, v75
mav 0,168,36
R 24,26624
v_add_f32 v72, v74, v75
mav 4,172,36
s_waitcnt lgkmcnt(3)
v_add_f32 v73, v74, v75
mav 0,176,40
R 28,27648
mav 4,180,40
v_permlane32_swap_b32 v72, v73
mav 0,184,44
R 32,28672
v_mul_f32 v74, 0x3a800000, v72
mav 4,188,44
s_waitcnt lgkmcnt(3)
v_mul_f32 v75, 0x3a800000, v73
v_fma_f32 v75, -v74, v74, v75
mav 0,192,16
R 36,29696
v_max_f32 v75, 0, v75
mav 4,196,16
v_add_f32 v75, 0x3727c5ac, v75
mav 0,200,20
R 40,30720
mav 4,204,20
s_waitcnt lgkmcnt(3)
v_rsq_f32 v72, v75
mav 0,208,24
R 44,31744
v_mul_f32_e64 v73, -v74, v72
mav 4,212,24
mav 0,216,28
mav 4,220,28
s_waitcnt lgkmcnt(2)
mav 0,224,32
v_add_u32 v64, 1, v64
mav 4,228,32
mav 0,232,36
mav 4,236,36
s_waitcnt lgkmcnt(0)
mav 0,240,40
mav 4,244,40
mav 0,248,44
mav 4,252,44
ds_write_b64 v88, v[72:73] offset:0
s_waitcnt vmcnt(1) lgkmcnt(0)
s_add_u32 s49, s49, 313344
s_barrier
R 16,32768
buffer_load_dwordx2 v[72:73], v89, s[36:39], s49 offen sc1
buffer_load_dwordx2 v[74:75], v89, s[36:39], s49 offen offset:512 sc1
ds_read_b64 v[66:67], v88 offset:0
R 20,33792
R 24,34816
R 28,35840
s_waitcnt vmcnt(2)
buffer_store_dword v64, v81, s[36:39], s52 offen sc1
R 32,36864
s_waitcnt lgkmcnt(3)
v_mfma_f32_16x16x32_bf16 v[8:11], a[0:3], v[16:19], 0
R 36,37888
v_fma_f32 v68, v67, v48, v90
v_mfma_f32_16x16x32_bf16 v[12:15], a[4:7], v[16:19], 0
v_fma_f32 v0, v0, v66, v68
v_fma_f32 v69, v67, v49, v91
mav 8,8,20
R 40,38912
v_fma_f32 v1, v1, v66, v69
mav 12,12,20
s_waitcnt lgkmcnt(3)
v_mul_f32_e64 v0, v0, |v0|
v_mul_f32_e64 v1, v1, |v1|
mav 8,16,24
R 44,39936
v_cvt_pk_bf16_f32 v0, v0, v1
mav 12,20,24
v_pk_max_i16 v56, v0, 0
v_fma_f32 v68, v67, v50, v92
mav 8,24,28
R 16,40960
v_fma_f32 v2, v2, v66, v68
mav 12,28,28
s_waitcnt lgkmcnt(3)
v_fma_f32 v69, v67, v51, v93
v_fma_f32 v3, v3, v66, v69
mav 8,32,32
R 20,41984
v_mul_f32_e64 v2, v2, |v2|
mav 12,36,32
v_mul_f32_e64 v3, v3, |v3|
v_cvt_pk_bf16_f32 v2, v2, v3
mav 8,40,36
R 24,43008
v_pk_max_i16 v57, v2, 0
mav 12,44,36
s_waitcnt lgkmcnt(3)
v_fma_f32 v68, v67, v52, v94
v_fma_f32 v4, v4, v66, v68
mav 8,48,40
R 28,44032
v_fma_f32 v69, v67, v53, v95
mav 12,52,40
v_fma_f32 v5, v5, v66, v69
v_mul_f32_e64 v4, v4, |v4|
mav 8,56,44
R 32,45056
v_mul_f32_e64 v5, v5, |v5|
mav 12,60,44
s_waitcnt lgkmcnt(3)
v_cvt_pk_bf16_f32 v4, v4, v5
v_pk_max_i16 v58, v4, 0
mav 8,64,16
R 36,46080
v_fma_f32 v68, v67, v54, v96
mav 12,68,16
v_fma_f32 v6, v6, v66, v68
v_fma_f32 v69, v67, v55, v97
mav 8,72,20
R 40,47104
v_fma_f32 v7, v7, v66, v69
mav 12,76,20
s_waitcnt lgkmcnt(3)
v_mul_f32_e64 v6, v6, |v6|
v_mul_f32_e64 v7, v7, |v7|
mav 8,80,24
R 44,48128
v_cvt_pk_bf16_f32 v6, v6, v7
mav 12,84,24
v_pk_max_i16 v59, v6, 0
mav 8,88,28
buffer_store_dwordx4 v[56:59], v83, s[36:39], s49 offen sc1
)"
R"(
R 16,49152
mav 12,92,28
s_waitcnt lgkmcnt(3)
mav 8,96,32
R 20,50176
mav 12,100,32
mav 8,104,36
R 24,51200
mav 12,108,36
s_waitcnt lgkmcnt(3)
mav 8,112,40
R 28,52224
mav 12,116,40
mav 8,120,44
R 32,53248
mav 12,124,44
s_waitcnt lgkmcnt(3)
mav 8,128,16
R 36,54272
mav 12,132,16
mav 8,136,20
R 40,55296
mav 12,140,20
s_waitcnt vmcnt(2) lgkmcnt(3)
v_add_f32 v72, v72, v74
v_add_f32 v73, v73, v75
mav 8,144,24
R 44,56320
mav 12,148,24
v_permlane32_swap_b32 v72, v73
mav 8,152,28
LsE6:
s_cbranch_execz LsE10
R 16,57344
v_add_f32 v74, v72, v73
mav 12,156,28
s_waitcnt lgkmcnt(3)
v_add_f32 v75, v72, v73
mav 8,160,32
R 20,58368
mav 12,164,32
v_permlane16_swap_b32 v74, v75
mav 8,168,36
R 24,59392
v_add_f32 v72, v74, v75
mav 12,172,36
s_waitcnt lgkmcnt(3)
v_add_f32 v73, v74, v75
mav 8,176,40
R 28,60416
mav 12,180,40
v_permlane32_swap_b32 v72, v73
mav 8,184,44
R 32,61440
v_mul_f32 v74, 0x3a800000, v72
mav 12,188,44
s_waitcnt lgkmcnt(3)
v_mul_f32 v75, 0x3a800000, v73
v_fma_f32 v75, -v74, v74, v75
mav 8,192,16
R 36,62464
v_max_f32 v75, 0, v75
mav 12,196,16
v_add_f32 v75, 0x3727c5ac, v75
mav 8,200,20
R 40,63488
mav 12,204,20
s_waitcnt lgkmcnt(3)
v_rsq_f32 v72, v75
mav 8,208,24
R 44,64512
v_mul_f32_e64 v73, -v74, v72
mav 12,212,24
mav 8,216,28
mav 12,220,28
s_waitcnt lgkmcnt(2)
mav 8,224,32
v_add_u32 v64, 1, v64
mav 12,228,32
mav 8,232,36
mav 12,236,36
s_waitcnt lgkmcnt(0)
mav 8,240,40
mav 12,244,40
mav 8,248,44
mav 12,252,44
ds_write_b64 v88, v[72:73] offset:128
s_waitcnt lgkmcnt(0)
s_add_u32 s49, s49, 313344
s_barrier
ds_read_b64 v[66:67], v88 offset:128
s_waitcnt vmcnt(0)
buffer_store_dword v64, v81, s[36:39], s52 offen sc1
s_waitcnt lgkmcnt(0)
v_fma_f32 v68, v67, v48, v90
v_fma_f32 v8, v8, v66, v68
v_fma_f32 v69, v67, v49, v91
v_fma_f32 v9, v9, v66, v69
v_mul_f32_e64 v8, v8, |v8|
v_mul_f32_e64 v9, v9, |v9|
v_cvt_pk_bf16_f32 v8, v8, v9
v_pk_max_i16 v56, v8, 0
v_fma_f32 v68, v67, v50, v92
v_fma_f32 v10, v10, v66, v68
v_fma_f32 v69, v67, v51, v93
v_fma_f32 v11, v11, v66, v69
v_mul_f32_e64 v10, v10, |v10|
v_mul_f32_e64 v11, v11, |v11|
v_cvt_pk_bf16_f32 v10, v10, v11
v_pk_max_i16 v57, v10, 0
v_fma_f32 v68, v67, v52, v94
v_fma_f32 v12, v12, v66, v68
v_fma_f32 v69, v67, v53, v95
v_fma_f32 v13, v13, v66, v69
v_mul_f32_e64 v12, v12, |v12|
v_mul_f32_e64 v13, v13, |v13|
v_cvt_pk_bf16_f32 v12, v12, v13
v_pk_max_i16 v58, v12, 0
v_fma_f32 v68, v67, v54, v96
v_fma_f32 v14, v14, v66, v68
v_fma_f32 v69, v67, v55, v97
v_fma_f32 v15, v15, v66, v69
v_mul_f32_e64 v14, v14, |v14|
v_mul_f32_e64 v15, v15, |v15|
v_cvt_pk_bf16_f32 v14, v14, v15
v_pk_max_i16 v59, v14, 0
buffer_store_dwordx4 v[56:59], v83, s[36:39], s49 offen sc1
v_add_u32 v64, 37, v64
s_waitcnt vmcnt(0)
buffer_store_dword v64, v81, s[36:39], s52 offen sc1
s_endpgm
LvE2:
LsE7:
s_cbranch_execz LsE11
s_add_u32 s49, s49, 313344
s_barrier
s_add_u32 m0, s50, 0
s_nop 0
D 84,0
D 84,1024
D 84,2048
D 84,3072
s_add_u32 m0, s50, 4096
s_nop 0
D 85,0
D 85,1024
D 85,2048
D 85,3072
s_waitcnt vmcnt(0)
s_add_u32 s49, s49, 313344
s_barrier
s_add_u32 m0, s50, 32768
R 16,0
D 84,0
R 20,1024
R 24,2048
D 84,1024
R 28,3072
R 32,4096
D 84,2048
s_waitcnt lgkmcnt(3)
v_mfma_f32_16x16x32_bf16 v[0:3], a[0:3], v[16:19], 0
R 36,5120
v_mfma_f32_16x16x32_bf16 v[4:7], a[4:7], v[16:19], 0
mav 0,8,20
R 40,6144
mav 4,12,20
s_waitcnt lgkmcnt(3)
mav 0,16,24
D 84,3072
mav 4,20,24
s_add_u32 m0, s50, 36864
R 44,7168
mav 0,24,28
R 16,8192
mav 4,28,28
s_waitcnt lgkmcnt(3)
mav 0,32,32
D 85,0
mav 4,36,32
R 20,9216
mav 0,40,36
R 24,10240
mav 4,44,36
s_waitcnt lgkmcnt(3)
mav 0,48,40
D 85,1024
mav 4,52,40
R 28,11264
mav 0,56,44
R 32,12288
mav 4,60,44
s_waitcnt lgkmcnt(3)
mav 0,64,16
D 85,2048
mav 4,68,16
R 36,13312
mav 0,72,20
R 40,14336
mav 4,76,20
s_waitcnt lgkmcnt(3)
mav 0,80,24
D 85,3072
mav 4,84,24
R 44,15360
mav 0,88,28
R 16,16384
mav 4,92,28
s_waitcnt lgkmcnt(3)
mav 0,96,32
R 20,17408
mav 4,100,32
mav 0,104,36
R 24,18432
mav 4,108,36
s_waitcnt lgkmcnt(3)
mav 0,112,40
R 28,19456
mav 4,116,40
mav 0,120,44
R 32,20480
mav 4,124,44
s_waitcnt lgkmcnt(3)
mav 0,128,16
R 36,21504
mav 4,132,16
mav 0,136,20
R 40,22528
mav 4,140,20
s_waitcnt lgkmcnt(3)
mav 0,144,24
R 44,23552
mav 4,148,24
mav 0,152,28
R 16,24576
mav 4,156,28
s_waitcnt lgkmcnt(3)
mav 0,160,32
R 20,25600
mav 4,164,32
mav 0,168,36
R 24,26624
mav 4,172,36
s_waitcnt lgkmcnt(3)
mav 0,176,40
R 28,27648
mav 4,180,40
mav 0,184,44
R 32,28672
mav 4,188,44
s_waitcnt lgkmcnt(3)
mav 0,192,16
R 36,29696
mav 4,196,16
mav 0,200,20
R 40,30720
mav 4,204,20
s_waitcnt lgkmcnt(3)
mav 0,208,24
R 44,31744
mav 4,212,24
mav 0,216,28
mav 4,220,28
s_waitcnt lgkmcnt(2)
mav 0,224,32
mav 4,228,32
mav 0,232,36
mav 4,236,36
s_waitcnt lgkmcnt(0)
mav 0,240,40
mav 4,244,40
mav 0,248,44
mav 4,252,44
s_xor_b32 s50, s50, 0x10000
s_waitcnt vmcnt(0)
s_add_u32 s49, s49, 313344
s_barrier
s_add_u32 m0, s50, 0
R 16,32768
D 84,0
ds_read_b64 v[66:67], v88 offset:0
R 20,33792
R 24,34816
D 84,1024
R 28,35840
R 32,36864
D 84,2048
s_waitcnt lgkmcnt(3)
v_mfma_f32_16x16x32_bf16 v[8:11], a[0:3], v[16:19], 0
R 36,37888
v_fma_f32 v68, v67, v48, v90
v_mfma_f32_16x16x32_bf16 v[12:15], a[4:7], v[16:19], 0
v_fma_f32 v0, v0, v66, v68
v_fma_f32 v69, v67, v49, v91
mav 8,8,20
R 40,38912
v_fma_f32 v1, v1, v66, v69
mav 12,12,20
s_waitcnt lgkmcnt(3)
v_mul_f32_e64 v0, v0, |v0|
v_mul_f32_e64 v1, v1, |v1|
mav 8,16,24
D 84,3072
mav 12,20,24
s_add_u32 m0, s50, 4096
R 44,39936
v_cvt_pk_bf16_f32 v0, v0, v1
mav 8,24,28
R 16,40960
v_pk_max_i16 v56, v0, 0
mav 12,28,28
s_waitcnt lgkmcnt(3)
v_fma_f32 v68, v67, v50, v92
v_fma_f32 v2, v2, v66, v68
mav 8,32,32
D 85,0
mav 12,36,32
R 20,41984
v_fma_f32 v69, v67, v51, v93
mav 8,40,36
R 24,43008
v_fma_f32 v3, v3, v66, v69
mav 12,44,36
s_waitcnt lgkmcnt(3)
v_mul_f32_e64 v2, v2, |v2|
v_mul_f32_e64 v3, v3, |v3|
mav 8,48,40
D 85,1024
mav 12,52,40
R 28,44032
v_cvt_pk_bf16_f32 v2, v2, v3
mav 8,56,44
R 32,45056
v_pk_max_i16 v57, v2, 0
mav 12,60,44
s_waitcnt lgkmcnt(3)
v_fma_f32 v68, v67, v52, v94
v_fma_f32 v4, v4, v66, v68
mav 8,64,16
D 85,2048
mav 12,68,16
R 36,46080
v_fma_f32 v69, v67, v53, v95
mav 8,72,20
R 40,47104
v_fma_f32 v5, v5, v66, v69
mav 12,76,20
s_waitcnt lgkmcnt(3)
v_mul_f32_e64 v4, v4, |v4|
v_mul_f32_e64 v5, v5, |v5|
mav 8,80,24
D 85,3072
mav 12,84,24
R 44,48128
v_cvt_pk_bf16_f32 v4, v4, v5
mav 8,88,28
R 16,49152
v_pk_max_i16 v58, v4, 0
mav 12,92,28
s_waitcnt lgkmcnt(3)
v_fma_f32 v68, v67, v54, v96
v_fma_f32 v6, v6, v66, v68
mav 8,96,32
R 20,50176
v_fma_f32 v69, v67, v55, v97
mav 12,100,32
v_fma_f32 v7, v7, v66, v69
v_mul_f32_e64 v6, v6, |v6|
mav 8,104,36
R 24,51200
v_mul_f32_e64 v7, v7, |v7|
mav 12,108,36
s_waitcnt lgkmcnt(3)
v_cvt_pk_bf16_f32 v6, v6, v7
v_pk_max_i16 v59, v6, 0
mav 8,112,40
buffer_store_dwordx4 v[56:59], v83, s[36:39], s49 offen sc1
R 28,52224
mav 12,116,40
mav 8,120,44
R 32,53248
mav 12,124,44
s_waitcnt lgkmcnt(3)
mav 8,128,16
R 36,54272
mav 12,132,16
mav 8,136,20
R 40,55296
mav 12,140,20
s_waitcnt lgkmcnt(3)
mav 8,144,24
R 44,56320
mav 12,148,24
mav 8,152,28
R 16,57344
mav 12,156,28
s_waitcnt lgkmcnt(3)
mav 8,160,32
R 20,58368
mav 12,164,32
mav 8,168,36
R 24,59392
mav 12,172,36
s_waitcnt lgkmcnt(3)
mav 8,176,40
R 28,60416
mav 12,180,40
mav 8,184,44
R 32,61440
mav 12,188,44
s_waitcnt lgkmcnt(3)
mav 8,192,16
R 36,62464
mav 12,196,16
mav 8,200,20
R 40,63488
mav 12,204,20
s_waitcnt lgkmcnt(3)
mav 8,208,24
R 44,64512
mav 12,212,24
mav 8,216,28
mav 12,220,28
s_waitcnt lgkmcnt(2)
mav 8,224,32
v_add_u32 v64, 1, v64
mav 12,228,32
mav 8,232,36
s_mov_b32 s60, -30
.p2align 6
LE2_1:
mav 12,236,36
s_waitcnt lgkmcnt(0)
mav 8,240,40
mav 12,244,40
mav 8,248,44
mav 12,252,44
v_xor_b32 v80, 0x10000, v80
s_waitcnt vmcnt(1)
s_add_u32 s49, s49, 313344
s_barrier
s_add_u32 m0, s50, 32768
R 16,0
D 84,0
ds_read_b64 v[66:67], v88 offset:128
R 20,1024
R 24,2048
D 84,1024
R 28,3072
s_waitcnt vmcnt(2)
buffer_store_dword v64, v81, s[36:39], s52 offen sc1
R 32,4096
D 84,2048
s_waitcnt lgkmcnt(3)
v_mfma_f32_16x16x32_bf16 v[0:3], a[0:3], v[16:19], 0
R 36,5120
v_fma_f32 v68, v67, v48, v90
v_mfma_f32_16x16x32_bf16 v[4:7], a[4:7], v[16:19], 0
v_fma_f32 v8, v8, v66, v68
v_fma_f32 v69, v67, v49, v91
mav 0,8,20
R 40,6144
v_fma_f32 v9, v9, v66, v69
mav 4,12,20
s_waitcnt lgkmcnt(3)
v_mul_f32_e64 v8, v8, |v8|
v_mul_f32_e64 v9, v9, |v9|
mav 0,16,24
D 84,3072
mav 4,20,24
s_add_u32 m0, s50, 36864
R 44,7168
v_cvt_pk_bf16_f32 v8, v8, v9
mav 0,24,28
R 16,8192
v_pk_max_i16 v56, v8, 0
mav 4,28,28
s_waitcnt lgkmcnt(3)
v_fma_f32 v68, v67, v50, v92
v_fma_f32 v10, v10, v66, v68
mav 0,32,32
D 85,0
mav 4,36,32
R 20,9216
v_fma_f32 v69, v67, v51, v93
mav 0,40,36
R 24,10240
v_fma_f32 v11, v11, v66, v69
mav 4,44,36
s_waitcnt lgkmcnt(3)
v_mul_f32_e64 v10, v10, |v10|
v_mul_f32_e64 v11, v11, |v11|
mav 0,48,40
D 85,1024
mav 4,52,40
R 28,11264
v_cvt_pk_bf16_f32 v10, v10, v11
mav 0,56,44
R 32,12288
v_pk_max_i16 v57, v10, 0
mav 4,60,44
s_waitcnt lgkmcnt(3)
v_fma_f32 v68, v67, v52, v94
v_fma_f32 v12, v12, v66, v68
mav 0,64,16
D 85,2048
mav 4,68,16
R 36,13312
v_fma_f32 v69, v67, v53, v95
mav 0,72,20
R 40,14336
v_fma_f32 v13, v13, v66, v69
mav 4,76,20
s_waitcnt lgkmcnt(3)
v_mul_f32_e64 v12, v12, |v12|
v_mul_f32_e64 v13, v13, |v13|
mav 0,80,24
D 85,3072
mav 4,84,24
R 44,15360
v_cvt_pk_bf16_f32 v12, v12, v13
mav 0,88,28
R 16,16384
v_pk_max_i16 v58, v12, 0
mav 4,92,28
s_waitcnt lgkmcnt(3)
v_fma_f32 v68, v67, v54, v96
v_fma_f32 v14, v14, v66, v68
mav 0,96,32
R 20,17408
v_fma_f32 v69, v67, v55, v97
mav 4,100,32
v_fma_f32 v15, v15, v66, v69
v_mul_f32_e64 v14, v14, |v14|
mav 0,104,36
R 24,18432
v_mul_f32_e64 v15, v15, |v15|
mav 4,108,36
s_waitcnt lgkmcnt(3)
v_cvt_pk_bf16_f32 v14, v14, v15
v_pk_max_i16 v59, v14, 0
mav 0,112,40
buffer_store_dwordx4 v[56:59], v83, s[36:39], s49 offen sc1
R 28,19456
mav 4,116,40
mav 0,120,44
R 32,20480
mav 4,124,44
s_waitcnt lgkmcnt(3)
mav 0,128,16
R 36,21504
mav 4,132,16
mav 0,136,20
R 40,22528
mav 4,140,20
s_waitcnt lgkmcnt(3)
mav 0,144,24
R 44,23552
mav 4,148,24
mav 0,152,28
R 16,24576
mav 4,156,28
s_waitcnt lgkmcnt(3)
mav 0,160,32
R 20,25600
mav 4,164,32
mav 0,168,36
R 24,26624
mav 4,172,36
s_waitcnt lgkmcnt(3)
mav 0,176,40
R 28,27648
mav 4,180,40
mav 0,184,44
R 32,28672
mav 4,188,44
s_waitcnt lgkmcnt(3)
mav 0,192,16
R 36,29696
mav 4,196,16
mav 0,200,20
R 40,30720
mav 4,204,20
s_waitcnt lgkmcnt(3)
LsE8:
s_cbranch_execz LwE
mav 0,208,24
R 44,31744
mav 4,212,24
mav 0,216,28
mav 4,220,28
s_waitcnt lgkmcnt(2)
mav 0,224,32
v_add_u32 v64, 1, v64
mav 4,228,32
mav 0,232,36
mav 4,236,36
s_waitcnt lgkmcnt(0)
mav 0,240,40
mav 4,244,40
mav 0,248,44
mav 4,252,44
s_xor_b32 s50, s50, 0x10000
s_waitcnt vmcnt(1)
s_add_u32 s49, s49, 313344
s_barrier
s_add_u32 m0, s50, 0
R 16,32768
D 84,0
ds_read_b64 v[66:67], v88 offset:0
R 20,33792
R 24,34816
D 84,1024
R 28,35840
s_waitcnt vmcnt(2)
buffer_store_dword v64, v81, s[36:39], s52 offen sc1
R 32,36864
D 84,2048
s_waitcnt lgkmcnt(3)
v_mfma_f32_16x16x32_bf16 v[8:11], a[0:3], v[16:19], 0
R 36,37888
v_fma_f32 v68, v67, v48, v90
v_mfma_f32_16x16x32_bf16 v[12:15], a[4:7], v[16:19], 0
v_fma_f32 v0, v0, v66, v68
v_fma_f32 v69, v67, v49, v91
mav 8,8,20
R 40,38912
v_fma_f32 v1, v1, v66, v69
mav 12,12,20
s_waitcnt lgkmcnt(3)
v_mul_f32_e64 v0, v0, |v0|
v_mul_f32_e64 v1, v1, |v1|
mav 8,16,24
D 84,3072
mav 12,20,24
s_add_u32 m0, s50, 4096
R 44,39936
v_cvt_pk_bf16_f32 v0, v0, v1
mav 8,24,28
R 16,40960
v_pk_max_i16 v56, v0, 0
mav 12,28,28
s_waitcnt lgkmcnt(3)
v_fma_f32 v68, v67, v50, v92
v_fma_f32 v2, v2, v66, v68
mav 8,32,32
D 85,0
mav 12,36,32
R 20,41984
v_fma_f32 v69, v67, v51, v93
mav 8,40,36
R 24,43008
v_fma_f32 v3, v3, v66, v69
mav 12,44,36
s_waitcnt lgkmcnt(3)
v_mul_f32_e64 v2, v2, |v2|
v_mul_f32_e64 v3, v3, |v3|
mav 8,48,40
D 85,1024
mav 12,52,40
R 28,44032
v_cvt_pk_bf16_f32 v2, v2, v3
mav 8,56,44
R 32,45056
v_pk_max_i16 v57, v2, 0
mav 12,60,44
s_waitcnt lgkmcnt(3)
v_fma_f32 v68, v67, v52, v94
v_fma_f32 v4, v4, v66, v68
mav 8,64,16
D 85,2048
mav 12,68,16
R 36,46080
v_fma_f32 v69, v67, v53, v95
mav 8,72,20
R 40,47104
v_fma_f32 v5, v5, v66, v69
mav 12,76,20
s_waitcnt lgkmcnt(3)
v_mul_f32_e64 v4, v4, |v4|
v_mul_f32_e64 v5, v5, |v5|
mav 8,80,24
D 85,3072
mav 12,84,24
R 44,48128
v_cvt_pk_bf16_f32 v4, v4, v5
mav 8,88,28
R 16,49152
v_pk_max_i16 v58, v4, 0
mav 12,92,28
s_waitcnt lgkmcnt(3)
v_fma_f32 v68, v67, v54, v96
v_fma_f32 v6, v6, v66, v68
mav 8,96,32
R 20,50176
v_fma_f32 v69, v67, v55, v97
mav 12,100,32
v_fma_f32 v7, v7, v66, v69
v_mul_f32_e64 v6, v6, |v6|
mav 8,104,36
R 24,51200
v_mul_f32_e64 v7, v7, |v7|
mav 12,108,36
s_waitcnt lgkmcnt(3)
v_cvt_pk_bf16_f32 v6, v6, v7
v_pk_max_i16 v59, v6, 0
mav 8,112,40
buffer_store_dwordx4 v[56:59], v83, s[36:39], s49 offen sc1
R 28,52224
mav 12,116,40
mav 8,120,44
R 32,53248
mav 12,124,44
s_waitcnt lgkmcnt(3)
mav 8,128,16
R 36,54272
mav 12,132,16
mav 8,136,20
R 40,55296
mav 12,140,20
s_waitcnt lgkmcnt(3)
mav 8,144,24
R 44,56320
mav 12,148,24
mav 8,152,28
R 16,57344
mav 12,156,28
s_waitcnt lgkmcnt(3)
mav 8,160,32
R 20,58368
mav 12,164,32
mav 8,168,36
R 24,59392
mav 12,172,36
s_waitcnt lgkmcnt(3)
mav 8,176,40
R 28,60416
mav 12,180,40
mav 8,184,44
R 32,61440
mav 12,188,44
s_waitcnt lgkmcnt(3)
mav 8,192,16
R 36,62464
mav 12,196,16
mav 8,200,20
R 40,63488
mav 12,204,20
s_waitcnt lgkmcnt(3)
mav 8,208,24
R 44,64512
mav 12,212,24
mav 8,216,28
mav 12,220,28
s_waitcnt lgkmcnt(2)
mav 8,224,32
v_add_u32 v64, 1, v64
mav 12,228,32
s_add_u32 s60, s60, 1
mav 8,232,36
s_cbranch_scc0 LE2_1
mav 12,236,36
s_waitcnt lgkmcnt(0)
mav 8,240,40
mav 12,244,40
mav 8,248,44
mav 12,252,44
v_xor_b32 v80, 0x10000, v80
s_waitcnt vmcnt(1)
s_add_u32 s49, s49, 313344
s_barrier
s_add_u32 m0, s50, 32768
R 16,0
D 84,0
ds_read_b64 v[66:67], v88 offset:128
R 20,1024
R 24,2048
D 84,1024
R 28,3072
s_waitcnt vmcnt(2)
buffer_store_dword v64, v81, s[36:39], s52 offen sc1
R 32,4096
D 84,2048
s_waitcnt lgkmcnt(3)
v_mfma_f32_16x16x32_bf16 v[0:3], a[0:3], v[16:19], 0
R 36,5120
v_fma_f32 v68, v67, v48, v90
v_mfma_f32_16x16x32_bf16 v[4:7], a[4:7], v[16:19], 0
v_fma_f32 v8, v8, v66, v68
v_fma_f32 v69, v67, v49, v91
mav 0,8,20
R 40,6144
v_fma_f32 v9, v9, v66, v69
mav 4,12,20
s_waitcnt lgkmcnt(3)
v_mul_f32_e64 v8, v8, |v8|
v_mul_f32_e64 v9, v9, |v9|
mav 0,16,24
D 84,3072
mav 4,20,24
s_add_u32 m0, s50, 36864
R 44,7168
v_cvt_pk_bf16_f32 v8, v8, v9
mav 0,24,28
R 16,8192
v_pk_max_i16 v56, v8, 0
mav 4,28,28
s_waitcnt lgkmcnt(3)
v_fma_f32 v68, v67, v50, v92
v_fma_f32 v10, v10, v66, v68
mav 0,32,32
D 85,0
mav 4,36,32
R 20,9216
v_fma_f32 v69, v67, v51, v93
mav 0,40,36
R 24,10240
v_fma_f32 v11, v11, v66, v69
mav 4,44,36
s_waitcnt lgkmcnt(3)
v_mul_f32_e64 v10, v10, |v10|
v_mul_f32_e64 v11, v11, |v11|
mav 0,48,40
D 85,1024
mav 4,52,40
R 28,11264
v_cvt_pk_bf16_f32 v10, v10, v11
mav 0,56,44
R 32,12288
v_pk_max_i16 v57, v10, 0
mav 4,60,44
s_waitcnt lgkmcnt(3)
v_fma_f32 v68, v67, v52, v94
v_fma_f32 v12, v12, v66, v68
mav 0,64,16
D 85,2048
mav 4,68,16
R 36,13312
v_fma_f32 v69, v67, v53, v95
mav 0,72,20
R 40,14336
v_fma_f32 v13, v13, v66, v69
mav 4,76,20
s_waitcnt lgkmcnt(3)
v_mul_f32_e64 v12, v12, |v12|
v_mul_f32_e64 v13, v13, |v13|
mav 0,80,24
D 85,3072
mav 4,84,24
R 44,15360
v_cvt_pk_bf16_f32 v12, v12, v13
mav 0,88,28
R 16,16384
v_pk_max_i16 v58, v12, 0
mav 4,92,28
s_waitcnt lgkmcnt(3)
v_fma_f32 v68, v67, v54, v96
v_fma_f32 v14, v14, v66, v68
mav 0,96,32
R 20,17408
v_fma_f32 v69, v67, v55, v97
mav 4,100,32
v_fma_f32 v15, v15, v66, v69
v_mul_f32_e64 v14, v14, |v14|
mav 0,104,36
R 24,18432
v_mul_f32_e64 v15, v15, |v15|
mav 4,108,36
s_waitcnt lgkmcnt(3)
v_cvt_pk_bf16_f32 v14, v14, v15
v_pk_max_i16 v59, v14, 0
mav 0,112,40
buffer_store_dwordx4 v[56:59], v83, s[36:39], s49 offen sc1
R 28,19456
mav 4,116,40
mav 0,120,44
R 32,20480
mav 4,124,44
s_waitcnt lgkmcnt(3)
mav 0,128,16
R 36,21504
mav 4,132,16
mav 0,136,20
R 40,22528
mav 4,140,20
s_waitcnt lgkmcnt(3)
mav 0,144,24
R 44,23552
mav 4,148,24
mav 0,152,28
R 16,24576
mav 4,156,28
s_waitcnt lgkmcnt(3)
mav 0,160,32
R 20,25600
mav 4,164,32
mav 0,168,36
R 24,26624
mav 4,172,36
s_waitcnt lgkmcnt(3)
mav 0,176,40
R 28,27648
mav 4,180,40
mav 0,184,44
R 32,28672
mav 4,188,44
s_waitcnt lgkmcnt(3)
mav 0,192,16
R 36,29696
mav 4,196,16
mav 0,200,20
R 40,30720
mav 4,204,20
s_waitcnt lgkmcnt(3)
mav 0,208,24
R 44,31744
mav 4,212,24
mav 0,216,28
mav 4,220,28
s_waitcnt lgkmcnt(2)
mav 0,224,32
v_add_u32 v64, 1, v64
mav 4,228,32
mav 0,232,36
mav 4,236,36
s_waitcnt lgkmcnt(0)
mav 0,240,40
mav 4,244,40
mav 0,248,44
mav 4,252,44
s_waitcnt vmcnt(1)
s_add_u32 s49, s49, 313344
s_barrier
R 16,32768
ds_read_b64 v[66:67], v88 offset:0
R 20,33792
R 24,34816
R 28,35840
s_waitcnt vmcnt(0)
buffer_store_dword v64, v81, s[36:39], s52 offen sc1
R 32,36864
s_waitcnt lgkmcnt(3)
v_mfma_f32_16x16x32_bf16 v[8:11], a[0:3], v[16:19], 0
R 36,37888
v_fma_f32 v68, v67, v48, v90
v_mfma_f32_16x16x32_bf16 v[12:15], a[4:7], v[16:19], 0
v_fma_f32 v0, v0, v66, v68
v_fma_f32 v69, v67, v49, v91
mav 8,8,20
R 40,38912
v_fma_f32 v1, v1, v66, v69
mav 12,12,20
s_waitcnt lgkmcnt(3)
v_mul_f32_e64 v0, v0, |v0|
v_mul_f32_e64 v1, v1, |v1|
mav 8,16,24
R 44,39936
v_cvt_pk_bf16_f32 v0, v0, v1
mav 12,20,24
v_pk_max_i16 v56, v0, 0
v_fma_f32 v68, v67, v50, v92
mav 8,24,28
R 16,40960
v_fma_f32 v2, v2, v66, v68
mav 12,28,28
s_waitcnt lgkmcnt(3)
v_fma_f32 v69, v67, v51, v93
v_fma_f32 v3, v3, v66, v69
mav 8,32,32
R 20,41984
v_mul_f32_e64 v2, v2, |v2|
mav 12,36,32
v_mul_f32_e64 v3, v3, |v3|
v_cvt_pk_bf16_f32 v2, v2, v3
mav 8,40,36
R 24,43008
v_pk_max_i16 v57, v2, 0
mav 12,44,36
s_waitcnt lgkmcnt(3)
v_fma_f32 v68, v67, v52, v94
v_fma_f32 v4, v4, v66, v68
mav 8,48,40
R 28,44032
v_fma_f32 v69, v67, v53, v95
mav 12,52,40
v_fma_f32 v5, v5, v66, v69
v_mul_f32_e64 v4, v4, |v4|
mav 8,56,44
R 32,45056
v_mul_f32_e64 v5, v5, |v5|
mav 12,60,44
s_waitcnt lgkmcnt(3)
v_cvt_pk_bf16_f32 v4, v4, v5
v_pk_max_i16 v58, v4, 0
mav 8,64,16
R 36,46080
v_fma_f32 v68, v67, v54, v96
mav 12,68,16
v_fma_f32 v6, v6, v66, v68
v_fma_f32 v69, v67, v55, v97
mav 8,72,20
R 40,47104
v_fma_f32 v7, v7, v66, v69
mav 12,76,20
s_waitcnt lgkmcnt(3)
v_mul_f32_e64 v6, v6, |v6|
v_mul_f32_e64 v7, v7, |v7|
mav 8,80,24
R 44,48128
v_cvt_pk_bf16_f32 v6, v6, v7
mav 12,84,24
v_pk_max_i16 v59, v6, 0
mav 8,88,28
R 16,49152
mav 12,92,28
s_waitcnt lgkmcnt(3)
mav 8,96,32
buffer_store_dwordx4 v[56:59], v83, s[36:39], s49 offen sc1
R 20,50176
mav 12,100,32
mav 8,104,36
R 24,51200
mav 12,108,36
s_waitcnt lgkmcnt(3)
mav 8,112,40
R 28,52224
mav 12,116,40
mav 8,120,44
R 32,53248
mav 12,124,44
s_waitcnt lgkmcnt(3)
mav 8,128,16
R 36,54272
mav 12,132,16
mav 8,136,20
R 40,55296
mav 12,140,20
s_waitcnt lgkmcnt(3)
mav 8,144,24
R 44,56320
mav 12,148,24
mav 8,152,28
R 16,57344
mav 12,156,28
s_waitcnt lgkmcnt(3)
mav 8,160,32
R 20,58368
mav 12,164,32
mav 8,168,36
R 24,59392
mav 12,172,36
s_waitcnt lgkmcnt(3)
mav 8,176,40
R 28,60416
mav 12,180,40
mav 8,184,44
R 32,61440
mav 12,188,44
s_waitcnt lgkmcnt(3)
mav 8,192,16
R 36,62464
mav 12,196,16
mav 8,200,20
R 40,63488
mav 12,204,20
s_waitcnt lgkmcnt(3)
mav 8,208,24
R 44,64512
mav 12,212,24
mav 8,216,28
mav 12,220,28
s_waitcnt lgkmcnt(2)
mav 8,224,32
v_add_u32 v64, 1, v64
mav 12,228,32
mav 8,232,36
mav 12,236,36
s_waitcnt lgkmcnt(0)
mav 8,240,40
mav 12,244,40
mav 8,248,44
mav 12,252,44
s_add_u32 s49, s49, 313344
s_barrier
ds_read_b64 v[66:67], v88 offset:128
s_waitcnt vmcnt(0)
buffer_store_dword v64, v81, s[36:39], s52 offen sc1
s_waitcnt lgkmcnt(0)
v_fma_f32 v68, v67, v48, v90
v_fma_f32 v8, v8, v66, v68
v_fma_f32 v69, v67, v49, v91
v_fma_f32 v9, v9, v66, v69
v_mul_f32_e64 v8, v8, |v8|
v_mul_f32_e64 v9, v9, |v9|
v_cvt_pk_bf16_f32 v8, v8, v9
v_pk_max_i16 v56, v8, 0
v_fma_f32 v68, v67, v50, v92
v_fma_f32 v10, v10, v66, v68
v_fma_f32 v69, v67, v51, v93
v_fma_f32 v11, v11, v66, v69
v_mul_f32_e64 v10, v10, |v10|
v_mul_f32_e64 v11, v11, |v11|
v_cvt_pk_bf16_f32 v10, v10, v11
v_pk_max_i16 v57, v10, 0
v_fma_f32 v68, v67, v52, v94
v_fma_f32 v12, v12, v66, v68
v_fma_f32 v69, v67, v53, v95
v_fma_f32 v13, v13, v66, v69
v_mul_f32_e64 v12, v12, |v12|
v_mul_f32_e64 v13, v13, |v13|
v_cvt_pk_bf16_f32 v12, v12, v13
v_pk_max_i16 v58, v12, 0
v_fma_f32 v68, v67, v54, v96
v_fma_f32 v14, v14, v66, v68
v_fma_f32 v69, v67, v55, v97
v_fma_f32 v15, v15, v66, v69
v_mul_f32_e64 v14, v14, |v14|
v_mul_f32_e64 v15, v15, |v15|
v_cvt_pk_bf16_f32 v14, v14, v15
v_pk_max_i16 v59, v14, 0
buffer_store_dwordx4 v[56:59], v83, s[36:39], s49 offen sc1
v_add_u32 v64, 37, v64
s_waitcnt vmcnt(0)
buffer_store_dword v64, v81, s[36:39], s52 offen sc1
s_endpgm
LvE3:
LsE9:
s_cbranch_execz LwE
s_add_u32 s49, s49, 313344
s_barrier
s_add_u32 m0, s50, 0
s_nop 0
D 84,0
D 84,1024
D 84,2048
D 84,3072
s_add_u32 m0, s50, 4096
s_nop 0
LsE10:
s_cbranch_execz LwE
D 85,0
D 85,1024
D 85,2048
D 85,3072
s_waitcnt vmcnt(0)
s_add_u32 s49, s49, 313344
s_barrier
s_add_u32 m0, s50, 32768
R 16,0
D 84,0
R 20,1024
R 24,2048
D 84,1024
R 28,3072
R 32,4096
D 84,2048
s_waitcnt lgkmcnt(3)
v_mfma_f32_16x16x32_bf16 v[0:3], a[0:3], v[16:19], 0
R 36,5120
v_mfma_f32_16x16x32_bf16 v[4:7], a[4:7], v[16:19], 0
mav 0,8,20
R 40,6144
mav 4,12,20
s_waitcnt lgkmcnt(3)
mav 0,16,24
R 44,7168
mav 4,20,24
D 84,3072
mav 0,24,28
s_add_u32 m0, s50, 36864
R 16,8192
mav 4,28,28
s_waitcnt lgkmcnt(3)
mav 0,32,32
R 20,9216
mav 4,36,32
D 85,0
mav 0,40,36
R 24,10240
mav 4,44,36
s_waitcnt lgkmcnt(3)
mav 0,48,40
R 28,11264
mav 4,52,40
D 85,1024
mav 0,56,44
R 32,12288
mav 4,60,44
s_waitcnt lgkmcnt(3)
mav 0,64,16
R 36,13312
mav 4,68,16
D 85,2048
mav 0,72,20
R 40,14336
mav 4,76,20
s_waitcnt lgkmcnt(3)
mav 0,80,24
R 44,15360
mav 4,84,24
D 85,3072
mav 0,88,28
R 16,16384
mav 4,92,28
s_waitcnt lgkmcnt(3)
mav 0,96,32
R 20,17408
mav 4,100,32
mav 0,104,36
R 24,18432
mav 4,108,36
s_waitcnt lgkmcnt(3)
mav 0,112,40
R 28,19456
mav 4,116,40
mav 0,120,44
R 32,20480
mav 4,124,44
s_waitcnt lgkmcnt(3)
mav 0,128,16
R 36,21504
mav 4,132,16
mav 0,136,20
R 40,22528
mav 4,140,20
s_waitcnt lgkmcnt(3)
mav 0,144,24
R 44,23552
mav 4,148,24
mav 0,152,28
R 16,24576
mav 4,156,28
s_waitcnt lgkmcnt(3)
mav 0,160,32
R 20,25600
mav 4,164,32
mav 0,168,36
R 24,26624
mav 4,172,36
s_waitcnt lgkmcnt(3)
mav 0,176,40
R 28,27648
mav 4,180,40
mav 0,184,44
R 32,28672
mav 4,188,44
s_waitcnt lgkmcnt(3)
mav 0,192,16
R 36,29696
mav 4,196,16
mav 0,200,20
R 40,30720
mav 4,204,20
s_waitcnt lgkmcnt(3)
mav 0,208,24
R 44,31744
mav 4,212,24
mav 0,216,28
mav 4,220,28
s_waitcnt lgkmcnt(2)
mav 0,224,32
mav 4,228,32
mav 0,232,36
mav 4,236,36
s_waitcnt lgkmcnt(0)
mav 0,240,40
mav 4,244,40
mav 0,248,44
mav 4,252,44
s_xor_b32 s50, s50, 0x10000
s_waitcnt vmcnt(0)
s_add_u32 s49, s49, 313344
s_barrier
s_add_u32 m0, s50, 0
R 16,32768
D 84,0
ds_read_b64 v[66:67], v88 offset:0
R 20,33792
R 24,34816
D 84,1024
R 28,35840
R 32,36864
D 84,2048
s_waitcnt lgkmcnt(3)
v_mfma_f32_16x16x32_bf16 v[8:11], a[0:3], v[16:19], 0
R 36,37888
v_fma_f32 v68, v67, v48, v90
v_mfma_f32_16x16x32_bf16 v[12:15], a[4:7], v[16:19], 0
v_fma_f32 v0, v0, v66, v68
v_fma_f32 v69, v67, v49, v91
mav 8,8,20
R 40,38912
v_fma_f32 v1, v1, v66, v69
mav 12,12,20
s_waitcnt lgkmcnt(3)
v_mul_f32_e64 v0, v0, |v0|
v_mul_f32_e64 v1, v1, |v1|
mav 8,16,24
R 44,39936
v_cvt_pk_bf16_f32 v0, v0, v1
mav 12,20,24
D 84,3072
mav 8,24,28
s_add_u32 m0, s50, 4096
R 16,40960
v_pk_max_i16 v56, v0, 0
mav 12,28,28
s_waitcnt lgkmcnt(3)
v_fma_f32 v68, v67, v50, v92
v_fma_f32 v2, v2, v66, v68
mav 8,32,32
R 20,41984
v_fma_f32 v69, v67, v51, v93
mav 12,36,32
D 85,0
mav 8,40,36
R 24,43008
v_fma_f32 v3, v3, v66, v69
mav 12,44,36
s_waitcnt lgkmcnt(3)
v_mul_f32_e64 v2, v2, |v2|
v_mul_f32_e64 v3, v3, |v3|
mav 8,48,40
R 28,44032
v_cvt_pk_bf16_f32 v2, v2, v3
mav 12,52,40
D 85,1024
mav 8,56,44
R 32,45056
v_pk_max_i16 v57, v2, 0
mav 12,60,44
s_waitcnt lgkmcnt(3)
v_fma_f32 v68, v67, v52, v94
v_fma_f32 v4, v4, v66, v68
mav 8,64,16
R 36,46080
v_fma_f32 v69, v67, v53, v95
mav 12,68,16
D 85,2048
mav 8,72,20
R 40,47104
v_fma_f32 v5, v5, v66, v69
mav 12,76,20
s_waitcnt lgkmcnt(3)
v_mul_f32_e64 v4, v4, |v4|
v_mul_f32_e64 v5, v5, |v5|
mav 8,80,24
R 44,48128
v_cvt_pk_bf16_f32 v4, v4, v5
mav 12,84,24
D 85,3072
mav 8,88,28
R 16,49152
v_pk_max_i16 v58, v4, 0
mav 12,92,28
s_waitcnt lgkmcnt(3)
v_fma_f32 v68, v67, v54, v96
v_fma_f32 v6, v6, v66, v68
mav 8,96,32
R 20,50176
v_fma_f32 v69, v67, v55, v97
mav 12,100,32
v_fma_f32 v7, v7, v66, v69
v_mul_f32_e64 v6, v6, |v6|
mav 8,104,36
R 24,51200
v_mul_f32_e64 v7, v7, |v7|
mav 12,108,36
s_waitcnt lgkmcnt(3)
v_cvt_pk_bf16_f32 v6, v6, v7
v_pk_max_i16 v59, v6, 0
mav 8,112,40
R 28,52224
mav 12,116,40
buffer_store_dwordx4 v[56:59], v83, s[36:39], s49 offen sc1
mav 8,120,44
R 32,53248
mav 12,124,44
s_waitcnt lgkmcnt(3)
mav 8,128,16
R 36,54272
mav 12,132,16
mav 8,136,20
R 40,55296
mav 12,140,20
s_waitcnt lgkmcnt(3)
mav 8,144,24
R 44,56320
mav 12,148,24
mav 8,152,28
R 16,57344
mav 12,156,28
s_waitcnt lgkmcnt(3)
mav 8,160,32
R 20,58368
mav 12,164,32
mav 8,168,36
R 24,59392
mav 12,172,36
s_waitcnt lgkmcnt(3)
mav 8,176,40
R 28,60416
mav 12,180,40
mav 8,184,44
R 32,61440
mav 12,188,44
s_waitcnt lgkmcnt(3)
mav 8,192,16
R 36,62464
mav 12,196,16
mav 8,200,20
R 40,63488
mav 12,204,20
s_waitcnt lgkmcnt(3)
mav 8,208,24
R 44,64512
mav 12,212,24
mav 8,216,28
mav 12,220,28
s_waitcnt lgkmcnt(2)
mav 8,224,32
v_add_u32 v64, 1, v64
mav 12,228,32
mav 8,232,36
s_mov_b32 s60, -30
.p2align 6
LE3_1:
mav 12,236,36
s_waitcnt lgkmcnt(0)
mav 8,240,40
mav 12,244,40
mav 8,248,44
mav 12,252,44
v_xor_b32 v80, 0x10000, v80
s_waitcnt vmcnt(1)
s_add_u32 s49, s49, 313344
s_barrier
s_add_u32 m0, s50, 32768
R 16,0
D 84,0
ds_read_b64 v[66:67], v88 offset:128
R 20,1024
R 24,2048
D 84,1024
R 28,3072
s_waitcnt vmcnt(2)
buffer_store_dword v64, v81, s[36:39], s52 offen sc1
R 32,4096
D 84,2048
s_waitcnt lgkmcnt(3)
v_mfma_f32_16x16x32_bf16 v[0:3], a[0:3], v[16:19], 0
R 36,5120
v_fma_f32 v68, v67, v48, v90
v_mfma_f32_16x16x32_bf16 v[4:7], a[4:7], v[16:19], 0
v_fma_f32 v8, v8, v66, v68
v_fma_f32 v69, v67, v49, v91
mav 0,8,20
R 40,6144
v_fma_f32 v9, v9, v66, v69
mav 4,12,20
s_waitcnt lgkmcnt(3)
v_mul_f32_e64 v8, v8, |v8|
v_mul_f32_e64 v9, v9, |v9|
mav 0,16,24
R 44,7168
v_cvt_pk_bf16_f32 v8, v8, v9
mav 4,20,24
D 84,3072
mav 0,24,28
s_add_u32 m0, s50, 36864
R 16,8192
v_pk_max_i16 v56, v8, 0
mav 4,28,28
s_waitcnt lgkmcnt(3)
v_fma_f32 v68, v67, v50, v92
v_fma_f32 v10, v10, v66, v68
mav 0,32,32
R 20,9216
v_fma_f32 v69, v67, v51, v93
mav 4,36,32
D 85,0
mav 0,40,36
R 24,10240
v_fma_f32 v11, v11, v66, v69
mav 4,44,36
s_waitcnt lgkmcnt(3)
v_mul_f32_e64 v10, v10, |v10|
v_mul_f32_e64 v11, v11, |v11|
mav 0,48,40
R 28,11264
v_cvt_pk_bf16_f32 v10, v10, v11
mav 4,52,40
D 85,1024
mav 0,56,44
R 32,12288
v_pk_max_i16 v57, v10, 0
mav 4,60,44
s_waitcnt lgkmcnt(3)
v_fma_f32 v68, v67, v52, v94
v_fma_f32 v12, v12, v66, v68
mav 0,64,16
R 36,13312
v_fma_f32 v69, v67, v53, v95
mav 4,68,16
D 85,2048
mav 0,72,20
R 40,14336
v_fma_f32 v13, v13, v66, v69
mav 4,76,20
s_waitcnt lgkmcnt(3)
v_mul_f32_e64 v12, v12, |v12|
v_mul_f32_e64 v13, v13, |v13|
mav 0,80,24
R 44,15360
v_cvt_pk_bf16_f32 v12, v12, v13
mav 4,84,24
D 85,3072
mav 0,88,28
R 16,16384
v_pk_max_i16 v58, v12, 0
mav 4,92,28
s_waitcnt lgkmcnt(3)
v_fma_f32 v68, v67, v54, v96
v_fma_f32 v14, v14, v66, v68
mav 0,96,32
R 20,17408
v_fma_f32 v69, v67, v55, v97
mav 4,100,32
v_fma_f32 v15, v15, v66, v69
v_mul_f32_e64 v14, v14, |v14|
mav 0,104,36
R 24,18432
v_mul_f32_e64 v15, v15, |v15|
mav 4,108,36
s_waitcnt lgkmcnt(3)
v_cvt_pk_bf16_f32 v14, v14, v15
v_pk_max_i16 v59, v14, 0
mav 0,112,40
R 28,19456
mav 4,116,40
buffer_store_dwordx4 v[56:59], v83, s[36:39], s49 offen sc1
mav 0,120,44
R 32,20480
mav 4,124,44
s_waitcnt lgkmcnt(3)
mav 0,128,16
R 36,21504
mav 4,132,16
mav 0,136,20
R 40,22528
mav 4,140,20
s_waitcnt lgkmcnt(3)
mav 0,144,24
R 44,23552
mav 4,148,24
mav 0,152,28
R 16,24576
mav 4,156,28
s_waitcnt lgkmcnt(3)
mav 0,160,32
R 20,25600
mav 4,164,32
mav 0,168,36
R 24,26624
mav 4,172,36
s_waitcnt lgkmcnt(3)
mav 0,176,40
R 28,27648
mav 4,180,40
mav 0,184,44
R 32,28672
mav 4,188,44
s_waitcnt lgkmcnt(3)
mav 0,192,16
R 36,29696
mav 4,196,16
mav 0,200,20
R 40,30720
mav 4,204,20
s_waitcnt lgkmcnt(3)
mav 0,208,24
R 44,31744
mav 4,212,24
mav 0,216,28
mav 4,220,28
s_waitcnt lgkmcnt(2)
mav 0,224,32
v_add_u32 v64, 1, v64
mav 4,228,32
mav 0,232,36
mav 4,236,36
s_waitcnt lgkmcnt(0)
mav 0,240,40
mav 4,244,40
mav 0,248,44
mav 4,252,44
s_xor_b32 s50, s50, 0x10000
s_waitcnt vmcnt(1)
s_add_u32 s49, s49, 313344
s_barrier
s_add_u32 m0, s50, 0
R 16,32768
D 84,0
ds_read_b64 v[66:67], v88 offset:0
R 20,33792
R 24,34816
D 84,1024
R 28,35840
s_waitcnt vmcnt(2)
buffer_store_dword v64, v81, s[36:39], s52 offen sc1
R 32,36864
D 84,2048
s_waitcnt lgkmcnt(3)
v_mfma_f32_16x16x32_bf16 v[8:11], a[0:3], v[16:19], 0
R 36,37888
v_fma_f32 v68, v67, v48, v90
v_mfma_f32_16x16x32_bf16 v[12:15], a[4:7], v[16:19], 0
v_fma_f32 v0, v0, v66, v68
v_fma_f32 v69, v67, v49, v91
mav 8,8,20
R 40,38912
v_fma_f32 v1, v1, v66, v69
mav 12,12,20
s_waitcnt lgkmcnt(3)
v_mul_f32_e64 v0, v0, |v0|
v_mul_f32_e64 v1, v1, |v1|
mav 8,16,24
R 44,39936
v_cvt_pk_bf16_f32 v0, v0, v1
mav 12,20,24
D 84,3072
mav 8,24,28
s_add_u32 m0, s50, 4096
R 16,40960
v_pk_max_i16 v56, v0, 0
mav 12,28,28
s_waitcnt lgkmcnt(3)
v_fma_f32 v68, v67, v50, v92
v_fma_f32 v2, v2, v66, v68
mav 8,32,32
R 20,41984
v_fma_f32 v69, v67, v51, v93
mav 12,36,32
D 85,0
mav 8,40,36
R 24,43008
v_fma_f32 v3, v3, v66, v69
mav 12,44,36
s_waitcnt lgkmcnt(3)
v_mul_f32_e64 v2, v2, |v2|
v_mul_f32_e64 v3, v3, |v3|
mav 8,48,40
R 28,44032
v_cvt_pk_bf16_f32 v2, v2, v3
mav 12,52,40
D 85,1024
mav 8,56,44
R 32,45056
v_pk_max_i16 v57, v2, 0
mav 12,60,44
s_waitcnt lgkmcnt(3)
v_fma_f32 v68, v67, v52, v94
v_fma_f32 v4, v4, v66, v68
mav 8,64,16
R 36,46080
v_fma_f32 v69, v67, v53, v95
mav 12,68,16
D 85,2048
mav 8,72,20
R 40,47104
v_fma_f32 v5, v5, v66, v69
mav 12,76,20
s_waitcnt lgkmcnt(3)
v_mul_f32_e64 v4, v4, |v4|
v_mul_f32_e64 v5, v5, |v5|
mav 8,80,24
R 44,48128
v_cvt_pk_bf16_f32 v4, v4, v5
mav 12,84,24
D 85,3072
mav 8,88,28
R 16,49152
v_pk_max_i16 v58, v4, 0
LsE11:
s_cbranch_execz LwE
mav 12,92,28
s_waitcnt lgkmcnt(3)
v_fma_f32 v68, v67, v54, v96
v_fma_f32 v6, v6, v66, v68
mav 8,96,32
R 20,50176
v_fma_f32 v69, v67, v55, v97
mav 12,100,32
v_fma_f32 v7, v7, v66, v69
v_mul_f32_e64 v6, v6, |v6|
mav 8,104,36
R 24,51200
v_mul_f32_e64 v7, v7, |v7|
mav 12,108,36
s_waitcnt lgkmcnt(3)
v_cvt_pk_bf16_f32 v6, v6, v7
v_pk_max_i16 v59, v6, 0
mav 8,112,40
R 28,52224
mav 12,116,40
buffer_store_dwordx4 v[56:59], v83, s[36:39], s49 offen sc1
mav 8,120,44
R 32,53248
mav 12,124,44
s_waitcnt lgkmcnt(3)
mav 8,128,16
R 36,54272
mav 12,132,16
mav 8,136,20
R 40,55296
mav 12,140,20
s_waitcnt lgkmcnt(3)
mav 8,144,24
R 44,56320
mav 12,148,24
mav 8,152,28
R 16,57344
mav 12,156,28
s_waitcnt lgkmcnt(3)
mav 8,160,32
R 20,58368
mav 12,164,32
mav 8,168,36
R 24,59392
mav 12,172,36
s_waitcnt lgkmcnt(3)
mav 8,176,40
R 28,60416
mav 12,180,40
mav 8,184,44
R 32,61440
mav 12,188,44
s_waitcnt lgkmcnt(3)
mav 8,192,16
R 36,62464
mav 12,196,16
mav 8,200,20
R 40,63488
mav 12,204,20
s_waitcnt lgkmcnt(3)
mav 8,208,24
R 44,64512
mav 12,212,24
mav 8,216,28
mav 12,220,28
s_waitcnt lgkmcnt(2)
mav 8,224,32
v_add_u32 v64, 1, v64
mav 12,228,32
s_add_u32 s60, s60, 1
mav 8,232,36
s_cbranch_scc0 LE3_1
mav 12,236,36
s_waitcnt lgkmcnt(0)
mav 8,240,40
mav 12,244,40
mav 8,248,44
mav 12,252,44
v_xor_b32 v80, 0x10000, v80
s_waitcnt vmcnt(1)
s_add_u32 s49, s49, 313344
s_barrier
s_add_u32 m0, s50, 32768
R 16,0
D 84,0
ds_read_b64 v[66:67], v88 offset:128
R 20,1024
R 24,2048
D 84,1024
R 28,3072
s_waitcnt vmcnt(2)
buffer_store_dword v64, v81, s[36:39], s52 offen sc1
R 32,4096
D 84,2048
s_waitcnt lgkmcnt(3)
v_mfma_f32_16x16x32_bf16 v[0:3], a[0:3], v[16:19], 0
R 36,5120
v_fma_f32 v68, v67, v48, v90
v_mfma_f32_16x16x32_bf16 v[4:7], a[4:7], v[16:19], 0
v_fma_f32 v8, v8, v66, v68
v_fma_f32 v69, v67, v49, v91
mav 0,8,20
R 40,6144
v_fma_f32 v9, v9, v66, v69
mav 4,12,20
s_waitcnt lgkmcnt(3)
v_mul_f32_e64 v8, v8, |v8|
v_mul_f32_e64 v9, v9, |v9|
mav 0,16,24
R 44,7168
v_cvt_pk_bf16_f32 v8, v8, v9
mav 4,20,24
D 84,3072
mav 0,24,28
s_add_u32 m0, s50, 36864
R 16,8192
v_pk_max_i16 v56, v8, 0
mav 4,28,28
s_waitcnt lgkmcnt(3)
v_fma_f32 v68, v67, v50, v92
v_fma_f32 v10, v10, v66, v68
mav 0,32,32
R 20,9216
v_fma_f32 v69, v67, v51, v93
mav 4,36,32
D 85,0
mav 0,40,36
R 24,10240
v_fma_f32 v11, v11, v66, v69
mav 4,44,36
s_waitcnt lgkmcnt(3)
v_mul_f32_e64 v10, v10, |v10|
v_mul_f32_e64 v11, v11, |v11|
mav 0,48,40
R 28,11264
v_cvt_pk_bf16_f32 v10, v10, v11
mav 4,52,40
D 85,1024
mav 0,56,44
R 32,12288
v_pk_max_i16 v57, v10, 0
mav 4,60,44
s_waitcnt lgkmcnt(3)
v_fma_f32 v68, v67, v52, v94
v_fma_f32 v12, v12, v66, v68
mav 0,64,16
R 36,13312
v_fma_f32 v69, v67, v53, v95
mav 4,68,16
D 85,2048
mav 0,72,20
R 40,14336
v_fma_f32 v13, v13, v66, v69
mav 4,76,20
s_waitcnt lgkmcnt(3)
v_mul_f32_e64 v12, v12, |v12|
v_mul_f32_e64 v13, v13, |v13|
mav 0,80,24
R 44,15360
v_cvt_pk_bf16_f32 v12, v12, v13
mav 4,84,24
D 85,3072
mav 0,88,28
R 16,16384
v_pk_max_i16 v58, v12, 0
mav 4,92,28
s_waitcnt lgkmcnt(3)
v_fma_f32 v68, v67, v54, v96
v_fma_f32 v14, v14, v66, v68
mav 0,96,32
R 20,17408
v_fma_f32 v69, v67, v55, v97
mav 4,100,32
v_fma_f32 v15, v15, v66, v69
v_mul_f32_e64 v14, v14, |v14|
mav 0,104,36
R 24,18432
v_mul_f32_e64 v15, v15, |v15|
mav 4,108,36
s_waitcnt lgkmcnt(3)
v_cvt_pk_bf16_f32 v14, v14, v15
v_pk_max_i16 v59, v14, 0
mav 0,112,40
R 28,19456
mav 4,116,40
buffer_store_dwordx4 v[56:59], v83, s[36:39], s49 offen sc1
mav 0,120,44
R 32,20480
mav 4,124,44
s_waitcnt lgkmcnt(3)
mav 0,128,16
R 36,21504
mav 4,132,16
mav 0,136,20
R 40,22528
mav 4,140,20
s_waitcnt lgkmcnt(3)
mav 0,144,24
R 44,23552
mav 4,148,24
mav 0,152,28
R 16,24576
mav 4,156,28
s_waitcnt lgkmcnt(3)
mav 0,160,32
R 20,25600
mav 4,164,32
mav 0,168,36
R 24,26624
mav 4,172,36
s_waitcnt lgkmcnt(3)
mav 0,176,40
R 28,27648
mav 4,180,40
mav 0,184,44
R 32,28672
mav 4,188,44
s_waitcnt lgkmcnt(3)
mav 0,192,16
R 36,29696
mav 4,196,16
mav 0,200,20
R 40,30720
mav 4,204,20
s_waitcnt lgkmcnt(3)
mav 0,208,24
R 44,31744
mav 4,212,24
mav 0,216,28
mav 4,220,28
s_waitcnt lgkmcnt(2)
mav 0,224,32
v_add_u32 v64, 1, v64
mav 4,228,32
mav 0,232,36
mav 4,236,36
s_waitcnt lgkmcnt(0)
mav 0,240,40
mav 4,244,40
mav 0,248,44
mav 4,252,44
s_waitcnt vmcnt(1)
s_add_u32 s49, s49, 313344
s_barrier
R 16,32768
ds_read_b64 v[66:67], v88 offset:0
R 20,33792
R 24,34816
R 28,35840
s_waitcnt vmcnt(0)
buffer_store_dword v64, v81, s[36:39], s52 offen sc1
R 32,36864
s_waitcnt lgkmcnt(3)
v_mfma_f32_16x16x32_bf16 v[8:11], a[0:3], v[16:19], 0
R 36,37888
v_fma_f32 v68, v67, v48, v90
v_mfma_f32_16x16x32_bf16 v[12:15], a[4:7], v[16:19], 0
v_fma_f32 v0, v0, v66, v68
v_fma_f32 v69, v67, v49, v91
mav 8,8,20
R 40,38912
v_fma_f32 v1, v1, v66, v69
mav 12,12,20
s_waitcnt lgkmcnt(3)
v_mul_f32_e64 v0, v0, |v0|
v_mul_f32_e64 v1, v1, |v1|
mav 8,16,24
R 44,39936
v_cvt_pk_bf16_f32 v0, v0, v1
mav 12,20,24
v_pk_max_i16 v56, v0, 0
v_fma_f32 v68, v67, v50, v92
mav 8,24,28
R 16,40960
v_fma_f32 v2, v2, v66, v68
mav 12,28,28
s_waitcnt lgkmcnt(3)
v_fma_f32 v69, v67, v51, v93
v_fma_f32 v3, v3, v66, v69
mav 8,32,32
R 20,41984
v_mul_f32_e64 v2, v2, |v2|
mav 12,36,32
v_mul_f32_e64 v3, v3, |v3|
v_cvt_pk_bf16_f32 v2, v2, v3
mav 8,40,36
R 24,43008
v_pk_max_i16 v57, v2, 0
mav 12,44,36
s_waitcnt lgkmcnt(3)
v_fma_f32 v68, v67, v52, v94
v_fma_f32 v4, v4, v66, v68
mav 8,48,40
R 28,44032
v_fma_f32 v69, v67, v53, v95
mav 12,52,40
v_fma_f32 v5, v5, v66, v69
v_mul_f32_e64 v4, v4, |v4|
mav 8,56,44
R 32,45056
v_mul_f32_e64 v5, v5, |v5|
mav 12,60,44
s_waitcnt lgkmcnt(3)
v_cvt_pk_bf16_f32 v4, v4, v5
v_pk_max_i16 v58, v4, 0
mav 8,64,16
R 36,46080
v_fma_f32 v68, v67, v54, v96
mav 12,68,16
v_fma_f32 v6, v6, v66, v68
v_fma_f32 v69, v67, v55, v97
mav 8,72,20
R 40,47104
v_fma_f32 v7, v7, v66, v69
mav 12,76,20
)"
R"(
s_waitcnt lgkmcnt(3)
v_mul_f32_e64 v6, v6, |v6|
v_mul_f32_e64 v7, v7, |v7|
mav 8,80,24
R 44,48128
v_cvt_pk_bf16_f32 v6, v6, v7
mav 12,84,24
v_pk_max_i16 v59, v6, 0
mav 8,88,28
R 16,49152
mav 12,92,28
s_waitcnt lgkmcnt(3)
mav 8,96,32
R 20,50176
mav 12,100,32
buffer_store_dwordx4 v[56:59], v83, s[36:39], s49 offen sc1
mav 8,104,36
R 24,51200
mav 12,108,36
s_waitcnt lgkmcnt(3)
mav 8,112,40
R 28,52224
mav 12,116,40
mav 8,120,44
R 32,53248
mav 12,124,44
s_waitcnt lgkmcnt(3)
mav 8,128,16
R 36,54272
mav 12,132,16
mav 8,136,20
R 40,55296
mav 12,140,20
s_waitcnt lgkmcnt(3)
mav 8,144,24
R 44,56320
mav 12,148,24
mav 8,152,28
R 16,57344
mav 12,156,28
s_waitcnt lgkmcnt(3)
mav 8,160,32
R 20,58368
mav 12,164,32
mav 8,168,36
R 24,59392
mav 12,172,36
s_waitcnt lgkmcnt(3)
mav 8,176,40
R 28,60416
mav 12,180,40
mav 8,184,44
R 32,61440
mav 12,188,44
s_waitcnt lgkmcnt(3)
mav 8,192,16
R 36,62464
mav 12,196,16
mav 8,200,20
R 40,63488
mav 12,204,20
s_waitcnt lgkmcnt(3)
mav 8,208,24
R 44,64512
mav 12,212,24
mav 8,216,28
mav 12,220,28
s_waitcnt lgkmcnt(2)
mav 8,224,32
v_add_u32 v64, 1, v64
mav 12,228,32
mav 8,232,36
mav 12,236,36
s_waitcnt lgkmcnt(0)
mav 8,240,40
mav 12,244,40
mav 8,248,44
mav 12,252,44
s_add_u32 s49, s49, 313344
s_barrier
ds_read_b64 v[66:67], v88 offset:128
s_waitcnt vmcnt(0)
buffer_store_dword v64, v81, s[36:39], s52 offen sc1
s_waitcnt lgkmcnt(0)
v_fma_f32 v68, v67, v48, v90
v_fma_f32 v8, v8, v66, v68
v_fma_f32 v69, v67, v49, v91
v_fma_f32 v9, v9, v66, v69
v_mul_f32_e64 v8, v8, |v8|
v_mul_f32_e64 v9, v9, |v9|
v_cvt_pk_bf16_f32 v8, v8, v9
v_pk_max_i16 v56, v8, 0
v_fma_f32 v68, v67, v50, v92
v_fma_f32 v10, v10, v66, v68
v_fma_f32 v69, v67, v51, v93
v_fma_f32 v11, v11, v66, v69
v_mul_f32_e64 v10, v10, |v10|
v_mul_f32_e64 v11, v11, |v11|
v_cvt_pk_bf16_f32 v10, v10, v11
v_pk_max_i16 v57, v10, 0
v_fma_f32 v68, v67, v52, v94
v_fma_f32 v12, v12, v66, v68
v_fma_f32 v69, v67, v53, v95
v_fma_f32 v13, v13, v66, v69
v_mul_f32_e64 v12, v12, |v12|
v_mul_f32_e64 v13, v13, |v13|
v_cvt_pk_bf16_f32 v12, v12, v13
v_pk_max_i16 v58, v12, 0
v_fma_f32 v68, v67, v54, v96
v_fma_f32 v14, v14, v66, v68
v_fma_f32 v69, v67, v55, v97
v_fma_f32 v15, v15, v66, v69
v_mul_f32_e64 v14, v14, |v14|
v_mul_f32_e64 v15, v15, |v15|
v_cvt_pk_bf16_f32 v14, v14, v15
v_pk_max_i16 v59, v14, 0
buffer_store_dwordx4 v[56:59], v83, s[36:39], s49 offen sc1
v_add_u32 v64, 37, v64
s_waitcnt vmcnt(0)
buffer_store_dword v64, v81, s[36:39], s52 offen sc1
s_endpgm
Lr5:
s_cmp_eq_u32 s60, 5
s_cbranch_scc0 Lr6
s_waitcnt vmcnt(0)
v_accvgpr_read_b32 v73, a255
v_mov_b32 v72, 0x100
v_add_u32 v73, 0x9e3779b9, v73
buffer_store_dwordx2 v[72:73], v81, s[36:39], s52 offen sc1
v_add_u32 v86, v86, v72
s_sleep 32
s_sleep 32
s_sleep 32
s_sleep 32
s_sleep 32
s_sleep 32
E 0,65536
E 1,69632
E 2,73728
E 3,77824
s_add_u32 s51, s51, 65536
E 0,81920
E 1,86016
E 2,90112
E 3,94208
s_add_u32 s51, s51, 65536
E 0,98304
E 1,102400
E 2,106496
E 3,110592
s_add_u32 s51, s51, 65536
E 0,114688
E 1,118784
E 2,122880
E 3,126976
s_add_u32 s51, s51, 65536
s_mov_b64 exec, 0
s_cmp_eq_u32 s53, 0
s_cbranch_scc1 LsG0
s_cmp_eq_u32 s53, 1024
s_cbranch_scc1 LsG1
s_cmp_eq_u32 s53, 2048
s_cbranch_scc1 LsG2
s_branch LsG3
LwG:
s_mov_b64 exec, -1
s_waitcnt vmcnt(8)
s_barrier
v_add_u32 v4, 65536, v4
v_add_u32 v5, 65536, v5
v_add_u32 v6, 65536, v6
v_add_u32 v7, 65536, v7
Ta 0,4,0
Ta 4,5,0
Ta 8,6,0
Ta 12,7,0
Ta 16,4,16384
Ta 20,5,16384
Ta 24,6,16384
Ta 28,7,16384
E 0,131072
E 1,135168
E 2,139264
E 3,143360
s_add_u32 s51, s51, 65536
E 0,147456
E 1,151552
E 2,155648
E 3,159744
s_add_u32 s51, s51, 65536
s_cmp_ge_u32 s51, s57
s_cselect_b32 s60, 1572864, 0
s_sub_u32 s51, s51, s60
s_waitcnt lgkmcnt(0)
s_waitcnt vmcnt(8)
LsG0:
s_cbranch_execz LsG4
s_barrier
E 0,65536
Ta 32,4,32768
Ta 36,5,32768
E 1,69632
Ta 40,6,32768
Ta 44,7,32768
E 2,73728
Ta 48,4,49152
E 3,77824
s_add_u32 s51, s51, 65536
Ta 52,5,49152
Ta 56,6,49152
E 0,81920
Ta 60,7,49152
E 1,86016
E 2,90112
E 3,94208
s_add_u32 s51, s51, 65536
s_waitcnt lgkmcnt(0)
s_waitcnt vmcnt(8)
s_barrier
v_add_u32 v4, 65536, v4
v_add_u32 v5, 65536, v5
v_add_u32 v6, 65536, v6
v_add_u32 v7, 65536, v7
E 0,98304
Ta 64,4,0
Ta 68,5,0
E 1,102400
Ta 72,6,0
Ta 76,7,0
E 2,106496
Ta 80,4,16384
E 3,110592
s_add_u32 s51, s51, 65536
Ta 84,5,16384
Ta 88,6,16384
E 0,114688
Ta 92,7,16384
E 1,118784
E 2,122880
E 3,126976
s_add_u32 s51, s51, 65536
s_waitcnt lgkmcnt(0)
s_waitcnt vmcnt(8)
s_barrier
v_add_u32 v4, -65536, v4
v_add_u32 v5, -65536, v5
v_add_u32 v6, -65536, v6
v_add_u32 v7, -65536, v7
E 0,131072
Ta 96,4,0
Ta 100,5,0
E 1,135168
Ta 104,6,0
Ta 108,7,0
E 2,139264
Ta 112,4,16384
E 3,143360
s_add_u32 s51, s51, 65536
Ta 116,5,16384
Ta 120,6,16384
E 0,147456
Ta 124,7,16384
E 1,151552
E 2,155648
E 3,159744
s_add_u32 s51, s51, 65536
s_cmp_ge_u32 s51, s57
s_cselect_b32 s60, 1572864, 0
s_sub_u32 s51, s51, s60
s_waitcnt lgkmcnt(0)
s_waitcnt vmcnt(8)
s_barrier
E 0,65536
Ta 128,4,32768
Ta 132,5,32768
E 1,69632
Ta 136,6,32768
Ta 140,7,32768
E 2,73728
Ta 144,4,49152
E 3,77824
s_add_u32 s51, s51, 65536
Ta 148,5,49152
Ta 152,6,49152
E 0,81920
Ta 156,7,49152
E 1,86016
E 2,90112
E 3,94208
s_add_u32 s51, s51, 65536
s_waitcnt lgkmcnt(0)
s_waitcnt vmcnt(8)
s_barrier
v_add_u32 v4, 65536, v4
v_add_u32 v5, 65536, v5
v_add_u32 v6, 65536, v6
v_add_u32 v7, 65536, v7
E 0,98304
Ta 160,4,0
Ta 164,5,0
E 1,102400
Ta 168,6,0
Ta 172,7,0
E 2,106496
Ta 176,4,16384
E 3,110592
s_add_u32 s51, s51, 65536
Ta 180,5,16384
Ta 184,6,16384
E 0,114688
Ta 188,7,16384
E 1,118784
E 2,122880
E 3,126976
s_add_u32 s51, s51, 65536
s_waitcnt lgkmcnt(0)
s_waitcnt vmcnt(8)
s_barrier
v_add_u32 v4, -65536, v4
v_add_u32 v5, -65536, v5
v_add_u32 v6, -65536, v6
v_add_u32 v7, -65536, v7
E 0,131072
Ta 192,4,0
Ta 196,5,0
E 1,135168
Ta 200,6,0
Ta 204,7,0
E 2,139264
Ta 208,4,16384
E 3,143360
s_add_u32 s51, s51, 65536
Ta 212,5,16384
Ta 216,6,16384
E 0,147456
Ta 220,7,16384
E 1,151552
E 2,155648
E 3,159744
s_add_u32 s51, s51, 65536
s_cmp_ge_u32 s51, s57
s_cselect_b32 s60, 1572864, 0
s_sub_u32 s51, s51, s60
s_waitcnt lgkmcnt(0)
s_waitcnt vmcnt(8)
s_barrier
E 0,65536
Ta 224,4,32768
Ta 228,5,32768
E 1,69632
Ta 232,6,32768
Ta 236,7,32768
E 2,73728
Ta 240,4,49152
E 3,77824
s_add_u32 s51, s51, 65536
Ta 244,5,49152
Ta 248,6,49152
E 0,81920
Ta 252,7,49152
E 1,86016
E 2,90112
E 3,94208
s_add_u32 s51, s51, 65536
s_waitcnt lgkmcnt(0)
s_waitcnt vmcnt(8)
s_barrier
v_add_u32 v4, 65536, v4
v_add_u32 v5, 65536, v5
v_add_u32 v6, 65536, v6
v_add_u32 v7, 65536, v7
E 0,98304
Tv 128,4,0
Tv 132,5,0
E 1,102400
Tv 136,6,0
Tv 140,7,0
E 2,106496
Tv 144,4,16384
E 3,110592
s_add_u32 s51, s51, 65536
Tv 148,5,16384
Tv 152,6,16384
E 0,114688
Tv 156,7,16384
E 1,118784
E 2,122880
E 3,126976
s_add_u32 s51, s51, 65536
s_waitcnt lgkmcnt(0)
s_waitcnt vmcnt(8)
s_barrier
v_add_u32 v4, -65536, v4
v_add_u32 v5, -65536, v5
v_add_u32 v6, -65536, v6
v_add_u32 v7, -65536, v7
E 0,131072
Tv 160,4,0
Tv 164,5,0
E 1,135168
Tv 168,6,0
Tv 172,7,0
E 2,139264
Tv 176,4,16384
E 3,143360
s_add_u32 s51, s51, 65536
Tv 180,5,16384
Tv 184,6,16384
E 0,147456
Tv 188,7,16384
E 1,151552
E 2,155648
E 3,159744
s_add_u32 s51, s51, 65536
s_cmp_ge_u32 s51, s57
s_cselect_b32 s60, 1572864, 0
s_sub_u32 s51, s51, s60
s_waitcnt lgkmcnt(0)
s_waitcnt vmcnt(8)
s_barrier
Tv 192,4,32768
Tv 196,5,32768
Tv 200,6,32768
Tv 204,7,32768
Tv 208,4,49152
Tv 212,5,49152
Tv 216,6,49152
Tv 220,7,49152
s_waitcnt lgkmcnt(0)
s_waitcnt vmcnt(0)
s_barrier
v_add_u32 v4, 65536, v4
v_add_u32 v5, 65536, v5
v_add_u32 v6, 65536, v6
v_add_u32 v7, 65536, v7
Tv 224,4,0
Tv 228,5,0
Tv 232,6,0
Tv 236,7,0
Tv 240,4,16384
Tv 244,5,16384
Tv 248,6,16384
Tv 252,7,16384
s_waitcnt lgkmcnt(0)
s_waitcnt vmcnt(0)
s_barrier
s_nop 7
s_and_b32 s60, s48, 3
s_cmp_eq_u32 s60, 1
s_cbranch_scc1 LvG1
s_cmp_eq_u32 s60, 2
s_cbranch_scc1 LvG2
s_cmp_eq_u32 s60, 3
s_cbranch_scc1 LvG3
LvG0:
LsG1:
s_cbranch_execz LsG5
v_add_u32 v72, 1, v72
buffer_load_dwordx2 v[74:75], v85, s[36:39], 0 offen sc1
s_waitcnt vmcnt(0)
ds_write_b32 v82, v74
v_sub_u32 v74, v74, v86
v_xor_b32 v75, v75, v73
v_cmp_lt_u64 vcc, s[62:63], v[74:75]
s_waitcnt lgkmcnt(0)
s_cbranch_vccnz CG0_1
BG0_1:
s_add_u32 s49, s49, 313344
s_barrier
s_add_u32 m0, s50, 0
buffer_store_dword v72, v81, s[36:39], s52 offen sc1
D 83,0
D 83,1024
D 83,2048
D 83,3072
s_add_u32 m0, s50, 4096
s_nop 0
D 84,0
D 84,1024
s_mov_b32 m0, 144384
s_nop 0
buffer_load_dword v85, s[36:39], 0 offen sc1 lds
v_add_u32 v86, 1, v86
v_add_u32 v72, 1, v72
ds_read_b32 v74, v82 offset:0
s_waitcnt lgkmcnt(0)
v_cmp_gt_i32 vcc, v86, v74
s_waitcnt vmcnt(1)
s_add_u32 s49, s49, 313344
s_cbranch_vccnz CG0_2
BG0_2:
s_barrier
s_add_u32 m0, s50, 32768
R 32,0
buffer_store_dword v72, v81, s[36:39], s52 offen sc1
D 83,0
R 36,1024
D 83,1024
R 40,2048
D 83,2048
s_waitcnt lgkmcnt(1)
v_mfma_f32_16x16x32_bf16 v[0:3], a[0:3], v[32:35], 0
R 44,3072
v_mfma_f32_16x16x32_bf16 v[4:7], a[4:7], v[32:35], 0
D 83,3072
v_mfma_f32_16x16x32_bf16 v[8:11], a[8:11], v[32:35], 0
v_mfma_f32_16x16x32_bf16 v[12:15], a[12:15], v[32:35], 0
s_add_u32 m0, s50, 36864
mav 0,16,36
R 48,4096
mav 4,20,36
D 84,0
mav 8,24,36
mav 12,28,36
s_waitcnt lgkmcnt(1)
mav 0,32,40
R 52,5120
mav 4,36,40
D 84,1024
mav 8,40,40
mav 12,44,40
mav 0,48,44
R 56,6144
mav 4,52,44
mav 8,56,44
mav 12,60,44
s_waitcnt lgkmcnt(1)
mav 0,64,48
R 60,7168
mav 4,68,48
mav 8,72,48
mav 12,76,48
mav 0,80,52
R 32,8192
mav 4,84,52
mav 8,88,52
mav 12,92,52
s_waitcnt lgkmcnt(1)
mav 0,96,56
R 36,9216
mav 4,100,56
mav 8,104,56
mav 12,108,56
mav 0,112,60
R 40,10240
mav 4,116,60
mav 8,120,60
mav 12,124,60
s_waitcnt lgkmcnt(1)
mav 0,128,32
R 44,11264
mav 4,132,32
mav 8,136,32
LsG2:
s_cbranch_execz LsG6
s_mov_b32 m0, 144384
mav 12,140,32
mav 0,144,36
R 48,12288
mav 4,148,36
buffer_load_dword v85, s[36:39], 0 offen sc1 lds
mav 8,152,36
mav 12,156,36
s_waitcnt lgkmcnt(1)
mav 0,160,40
R 52,13312
v_add_u32 v86, 1, v86
mav 4,164,40
mav 8,168,40
mav 12,172,40
mav 0,176,44
R 56,14336
mav 4,180,44
mav 8,184,44
mav 12,188,44
s_waitcnt lgkmcnt(1)
mav 0,192,48
R 60,15360
mav 4,196,48
mav 8,200,48
mav 12,204,48
mav 0,208,52
R 32,16384
mav 4,212,52
mav 8,216,52
mav 12,220,52
s_waitcnt lgkmcnt(1)
mav 0,224,56
R 36,17408
mav 4,228,56
mav 8,232,56
mav 12,236,56
mav 0,240,60
R 40,18432
mav 4,244,60
mav 8,248,60
mav 12,252,60
s_waitcnt lgkmcnt(1)
mvv 0,128,32
R 44,19456
mvv 4,132,32
mvv 8,136,32
mvv 12,140,32
mvv 0,144,36
R 48,20480
mvv 4,148,36
mvv 8,152,36
mvv 12,156,36
s_waitcnt lgkmcnt(1)
mvv 0,160,40
R 52,21504
mvv 4,164,40
ds_read_b32 v74, v82 offset:0
mvv 8,168,40
mvv 12,172,40
mvv 0,176,44
R 56,22528
mvv 4,180,44
mvv 8,184,44
s_xor_b32 s50, s50, 0x10000
mvv 12,188,44
s_waitcnt lgkmcnt(1)
mvv 0,192,48
R 60,23552
v_cmp_gt_i32 vcc, v86, v74
mvv 4,196,48
mvv 8,200,48
s_waitcnt vmcnt(1)
s_add_u32 s49, s49, 313344
mvv 12,204,48
s_cbranch_vccnz CG0_3
BG0_3:
s_barrier
mvv 0,208,52
s_add_u32 m0, s50, 0
R 32,32768
mvv 4,212,52
D 83,0
mvv 8,216,52
mvv 12,220,52
s_waitcnt lgkmcnt(1)
mvv 0,224,56
R 36,33792
mvv 4,228,56
D 83,1024
mvv 8,232,56
mvv 12,236,56
mvv 0,240,60
R 40,34816
mvv 4,244,60
D 83,2048
mvv 8,248,60
mvv 12,252,60
s_waitcnt lgkmcnt(1)
v_mfma_f32_16x16x32_bf16 v[16:19], a[0:3], v[32:35], 0
R 44,35840
v_cvt_pk_bf16_f32 v64, v0, v1
v_mfma_f32_16x16x32_bf16 v[20:23], a[4:7], v[32:35], 0
D 83,3072
v_mfma_f32_16x16x32_bf16 v[24:27], a[8:11], v[32:35], 0
v_cvt_pk_bf16_f32 v65, v2, v3
v_cvt_pk_bf16_f32 v66, v4, v5
v_mfma_f32_16x16x32_bf16 v[28:31], a[12:15], v[32:35], 0
s_add_u32 m0, s50, 4096
v_cvt_pk_bf16_f32 v67, v6, v7
v_cvt_pk_bf16_f32 v68, v8, v9
mav 16,16,36
R 48,36864
v_cvt_pk_bf16_f32 v69, v10, v11
mav 20,20,36
buffer_store_dwordx4 v[64:67], v87, s[36:39], s49 offen sc1
D 84,0
mav 24,24,36
v_cvt_pk_bf16_f32 v70, v12, v13
v_cvt_pk_bf16_f32 v71, v14, v15
mav 28,28,36
s_waitcnt lgkmcnt(1)
mav 16,32,40
R 52,37888
mav 20,36,40
buffer_store_dwordx4 v[68:71], v87, s[36:39], s49 offen offset:1024 sc1
D 84,1024
mav 24,40,40
mav 28,44,40
mav 16,48,44
R 56,38912
mav 20,52,44
mav 24,56,44
mav 28,60,44
s_waitcnt lgkmcnt(1)
mav 16,64,48
R 60,39936
mav 20,68,48
mav 24,72,48
mav 28,76,48
mav 16,80,52
R 32,40960
mav 20,84,52
mav 24,88,52
mav 28,92,52
s_waitcnt lgkmcnt(1)
mav 16,96,56
R 36,41984
mav 20,100,56
mav 24,104,56
mav 28,108,56
mav 16,112,60
R 40,43008
mav 20,116,60
mav 24,120,60
mav 28,124,60
s_waitcnt lgkmcnt(1)
mav 16,128,32
R 44,44032
mav 20,132,32
mav 24,136,32
s_mov_b32 m0, 144384
mav 28,140,32
mav 16,144,36
R 48,45056
v_add_u32 v72, 1, v72
mav 20,148,36
buffer_load_dword v85, s[36:39], 0 offen sc1 lds
mav 24,152,36
mav 28,156,36
s_waitcnt lgkmcnt(1)
mav 16,160,40
R 52,46080
v_add_u32 v86, 1, v86
mav 20,164,40
mav 24,168,40
mav 28,172,40
mav 16,176,44
R 56,47104
mav 20,180,44
mav 24,184,44
mav 28,188,44
s_waitcnt lgkmcnt(1)
mav 16,192,48
R 60,48128
mav 20,196,48
mav 24,200,48
s_mov_b32 s60, -30
.p2align 6
LG0_4:
mav 28,204,48
mav 16,208,52
R 32,49152
mav 20,212,52
mav 24,216,52
mav 28,220,52
s_waitcnt lgkmcnt(1)
mav 16,224,56
R 36,50176
mav 20,228,56
mav 24,232,56
mav 28,236,56
mav 16,240,60
s_waitcnt vmcnt(2)
R 40,51200
mav 20,244,60
buffer_store_dword v72, v81, s[36:39], s52 offen sc1
mav 24,248,60
mav 28,252,60
s_waitcnt lgkmcnt(1)
mvv 16,128,32
R 44,52224
mvv 20,132,32
mvv 24,136,32
mvv 28,140,32
mvv 16,144,36
R 48,53248
mvv 20,148,36
mvv 24,152,36
mvv 28,156,36
s_waitcnt lgkmcnt(1)
mvv 16,160,40
R 52,54272
mvv 20,164,40
ds_read_b32 v74, v82 offset:0
mvv 24,168,40
mvv 28,172,40
mvv 16,176,44
R 56,55296
mvv 20,180,44
mvv 24,184,44
mvv 28,188,44
s_waitcnt lgkmcnt(1)
mvv 16,192,48
R 60,56320
v_cmp_gt_i32 vcc, v86, v74
mvv 20,196,48
v_xor_b32 v80, 0x10000, v80
mvv 24,200,48
s_waitcnt vmcnt(2)
s_add_u32 s49, s49, 313344
mvv 28,204,48
s_cbranch_vccnz CG0_5
BG0_5:
s_barrier
mvv 16,208,52
s_add_u32 m0, s50, 32768
R 32,0
mvv 20,212,52
D 83,0
mvv 24,216,52
mvv 28,220,52
s_waitcnt lgkmcnt(1)
mvv 16,224,56
R 36,1024
mvv 20,228,56
D 83,1024
mvv 24,232,56
mvv 28,236,56
mvv 16,240,60
R 40,2048
mvv 20,244,60
D 83,2048
mvv 24,248,60
mvv 28,252,60
s_waitcnt lgkmcnt(1)
v_mfma_f32_16x16x32_bf16 v[0:3], a[0:3], v[32:35], 0
R 44,3072
v_cvt_pk_bf16_f32 v64, v16, v17
v_mfma_f32_16x16x32_bf16 v[4:7], a[4:7], v[32:35], 0
D 83,3072
v_mfma_f32_16x16x32_bf16 v[8:11], a[8:11], v[32:35], 0
v_cvt_pk_bf16_f32 v65, v18, v19
v_cvt_pk_bf16_f32 v66, v20, v21
v_mfma_f32_16x16x32_bf16 v[12:15], a[12:15], v[32:35], 0
s_add_u32 m0, s50, 36864
v_cvt_pk_bf16_f32 v67, v22, v23
v_cvt_pk_bf16_f32 v68, v24, v25
mav 0,16,36
R 48,4096
v_cvt_pk_bf16_f32 v69, v26, v27
mav 4,20,36
buffer_store_dwordx4 v[64:67], v87, s[36:39], s49 offen sc1
D 84,0
mav 8,24,36
v_cvt_pk_bf16_f32 v70, v28, v29
v_cvt_pk_bf16_f32 v71, v30, v31
mav 12,28,36
s_waitcnt lgkmcnt(1)
mav 0,32,40
R 52,5120
mav 4,36,40
buffer_store_dwordx4 v[68:71], v87, s[36:39], s49 offen offset:1024 sc1
D 84,1024
mav 8,40,40
mav 12,44,40
mav 0,48,44
R 56,6144
mav 4,52,44
mav 8,56,44
mav 12,60,44
s_waitcnt lgkmcnt(1)
mav 0,64,48
R 60,7168
mav 4,68,48
mav 8,72,48
mav 12,76,48
mav 0,80,52
R 32,8192
mav 4,84,52
mav 8,88,52
mav 12,92,52
s_waitcnt lgkmcnt(1)
mav 0,96,56
R 36,9216
mav 4,100,56
mav 8,104,56
mav 12,108,56
mav 0,112,60
R 40,10240
mav 4,116,60
mav 8,120,60
mav 12,124,60
s_waitcnt lgkmcnt(1)
mav 0,128,32
R 44,11264
mav 4,132,32
mav 8,136,32
s_mov_b32 m0, 144384
mav 12,140,32
mav 0,144,36
R 48,12288
v_add_u32 v72, 1, v72
mav 4,148,36
buffer_load_dword v85, s[36:39], 0 offen sc1 lds
mav 8,152,36
mav 12,156,36
s_waitcnt lgkmcnt(1)
mav 0,160,40
R 52,13312
v_add_u32 v86, 1, v86
mav 4,164,40
mav 8,168,40
mav 12,172,40
mav 0,176,44
R 56,14336
mav 4,180,44
mav 8,184,44
mav 12,188,44
s_waitcnt lgkmcnt(1)
mav 0,192,48
R 60,15360
mav 4,196,48
mav 8,200,48
mav 12,204,48
mav 0,208,52
R 32,16384
mav 4,212,52
mav 8,216,52
mav 12,220,52
s_waitcnt lgkmcnt(1)
mav 0,224,56
R 36,17408
mav 4,228,56
mav 8,232,56
mav 12,236,56
mav 0,240,60
s_waitcnt vmcnt(2)
R 40,18432
mav 4,244,60
buffer_store_dword v72, v81, s[36:39], s52 offen sc1
mav 8,248,60
mav 12,252,60
s_waitcnt lgkmcnt(1)
mvv 0,128,32
R 44,19456
mvv 4,132,32
mvv 8,136,32
mvv 12,140,32
mvv 0,144,36
R 48,20480
mvv 4,148,36
mvv 8,152,36
mvv 12,156,36
s_waitcnt lgkmcnt(1)
mvv 0,160,40
R 52,21504
mvv 4,164,40
ds_read_b32 v74, v82 offset:0
mvv 8,168,40
mvv 12,172,40
mvv 0,176,44
R 56,22528
mvv 4,180,44
mvv 8,184,44
s_xor_b32 s50, s50, 0x10000
mvv 12,188,44
s_waitcnt lgkmcnt(1)
mvv 0,192,48
R 60,23552
v_cmp_gt_i32 vcc, v86, v74
mvv 4,196,48
mvv 8,200,48
s_waitcnt vmcnt(2)
s_add_u32 s49, s49, 313344
mvv 12,204,48
s_cbranch_vccnz CG0_6
BG0_6:
s_barrier
mvv 0,208,52
s_add_u32 m0, s50, 0
R 32,32768
mvv 4,212,52
D 83,0
mvv 8,216,52
mvv 12,220,52
s_waitcnt lgkmcnt(1)
mvv 0,224,56
R 36,33792
mvv 4,228,56
D 83,1024
mvv 8,232,56
mvv 12,236,56
mvv 0,240,60
R 40,34816
mvv 4,244,60
D 83,2048
mvv 8,248,60
mvv 12,252,60
s_waitcnt lgkmcnt(1)
v_mfma_f32_16x16x32_bf16 v[16:19], a[0:3], v[32:35], 0
R 44,35840
v_cvt_pk_bf16_f32 v64, v0, v1
v_mfma_f32_16x16x32_bf16 v[20:23], a[4:7], v[32:35], 0
D 83,3072
v_mfma_f32_16x16x32_bf16 v[24:27], a[8:11], v[32:35], 0
v_cvt_pk_bf16_f32 v65, v2, v3
v_cvt_pk_bf16_f32 v66, v4, v5
v_mfma_f32_16x16x32_bf16 v[28:31], a[12:15], v[32:35], 0
s_add_u32 m0, s50, 4096
v_cvt_pk_bf16_f32 v67, v6, v7
v_cvt_pk_bf16_f32 v68, v8, v9
mav 16,16,36
R 48,36864
v_cvt_pk_bf16_f32 v69, v10, v11
mav 20,20,36
buffer_store_dwordx4 v[64:67], v87, s[36:39], s49 offen sc1
D 84,0
mav 24,24,36
v_cvt_pk_bf16_f32 v70, v12, v13
v_cvt_pk_bf16_f32 v71, v14, v15
mav 28,28,36
s_waitcnt lgkmcnt(1)
mav 16,32,40
R 52,37888
mav 20,36,40
buffer_store_dwordx4 v[68:71], v87, s[36:39], s49 offen offset:1024 sc1
D 84,1024
mav 24,40,40
mav 28,44,40
mav 16,48,44
R 56,38912
mav 20,52,44
mav 24,56,44
mav 28,60,44
s_waitcnt lgkmcnt(1)
mav 16,64,48
R 60,39936
mav 20,68,48
mav 24,72,48
mav 28,76,48
mav 16,80,52
R 32,40960
mav 20,84,52
mav 24,88,52
mav 28,92,52
s_waitcnt lgkmcnt(1)
mav 16,96,56
R 36,41984
mav 20,100,56
mav 24,104,56
mav 28,108,56
mav 16,112,60
R 40,43008
mav 20,116,60
mav 24,120,60
mav 28,124,60
s_waitcnt lgkmcnt(1)
mav 16,128,32
R 44,44032
mav 20,132,32
mav 24,136,32
s_mov_b32 m0, 144384
mav 28,140,32
mav 16,144,36
R 48,45056
v_add_u32 v72, 1, v72
mav 20,148,36
buffer_load_dword v85, s[36:39], 0 offen sc1 lds
mav 24,152,36
mav 28,156,36
s_waitcnt lgkmcnt(1)
mav 16,160,40
R 52,46080
v_add_u32 v86, 1, v86
mav 20,164,40
mav 24,168,40
mav 28,172,40
mav 16,176,44
R 56,47104
mav 20,180,44
mav 24,184,44
mav 28,188,44
s_waitcnt lgkmcnt(1)
mav 16,192,48
R 60,48128
mav 20,196,48
s_add_u32 s60, s60, 1
mav 24,200,48
s_cbranch_scc0 LG0_4
mav 28,204,48
mav 16,208,52
R 32,49152
mav 20,212,52
mav 24,216,52
mav 28,220,52
s_waitcnt lgkmcnt(1)
mav 16,224,56
R 36,50176
mav 20,228,56
mav 24,232,56
mav 28,236,56
mav 16,240,60
s_waitcnt vmcnt(2)
R 40,51200
mav 20,244,60
buffer_store_dword v72, v81, s[36:39], s52 offen sc1
mav 24,248,60
mav 28,252,60
s_waitcnt lgkmcnt(1)
mvv 16,128,32
R 44,52224
LsG3:
s_cbranch_execz LsG7
mvv 20,132,32
mvv 24,136,32
mvv 28,140,32
mvv 16,144,36
R 48,53248
mvv 20,148,36
mvv 24,152,36
mvv 28,156,36
s_waitcnt lgkmcnt(1)
mvv 16,160,40
R 52,54272
mvv 20,164,40
ds_read_b32 v74, v82 offset:0
mvv 24,168,40
mvv 28,172,40
mvv 16,176,44
R 56,55296
mvv 20,180,44
mvv 24,184,44
mvv 28,188,44
s_waitcnt lgkmcnt(1)
mvv 16,192,48
R 60,56320
v_cmp_gt_i32 vcc, v86, v74
mvv 20,196,48
v_xor_b32 v80, 0x10000, v80
mvv 24,200,48
s_waitcnt vmcnt(2)
s_add_u32 s49, s49, 313344
mvv 28,204,48
s_cbranch_vccnz CG0_7
BG0_7:
s_barrier
mvv 16,208,52
s_add_u32 m0, s50, 32768
R 32,0
mvv 20,212,52
D 83,0
mvv 24,216,52
mvv 28,220,52
s_waitcnt lgkmcnt(1)
mvv 16,224,56
R 36,1024
mvv 20,228,56
D 83,1024
mvv 24,232,56
mvv 28,236,56
mvv 16,240,60
R 40,2048
mvv 20,244,60
D 83,2048
mvv 24,248,60
mvv 28,252,60
s_waitcnt lgkmcnt(1)
v_mfma_f32_16x16x32_bf16 v[0:3], a[0:3], v[32:35], 0
R 44,3072
v_cvt_pk_bf16_f32 v64, v16, v17
v_mfma_f32_16x16x32_bf16 v[4:7], a[4:7], v[32:35], 0
D 83,3072
v_mfma_f32_16x16x32_bf16 v[8:11], a[8:11], v[32:35], 0
v_cvt_pk_bf16_f32 v65, v18, v19
v_cvt_pk_bf16_f32 v66, v20, v21
v_mfma_f32_16x16x32_bf16 v[12:15], a[12:15], v[32:35], 0
s_add_u32 m0, s50, 36864
v_cvt_pk_bf16_f32 v67, v22, v23
v_cvt_pk_bf16_f32 v68, v24, v25
mav 0,16,36
R 48,4096
v_cvt_pk_bf16_f32 v69, v26, v27
mav 4,20,36
buffer_store_dwordx4 v[64:67], v87, s[36:39], s49 offen sc1
D 84,0
mav 8,24,36
v_cvt_pk_bf16_f32 v70, v28, v29
v_cvt_pk_bf16_f32 v71, v30, v31
mav 12,28,36
s_waitcnt lgkmcnt(1)
mav 0,32,40
R 52,5120
mav 4,36,40
buffer_store_dwordx4 v[68:71], v87, s[36:39], s49 offen offset:1024 sc1
D 84,1024
mav 8,40,40
mav 12,44,40
mav 0,48,44
R 56,6144
mav 4,52,44
mav 8,56,44
mav 12,60,44
s_waitcnt lgkmcnt(1)
mav 0,64,48
R 60,7168
mav 4,68,48
mav 8,72,48
mav 12,76,48
mav 0,80,52
R 32,8192
mav 4,84,52
mav 8,88,52
mav 12,92,52
s_waitcnt lgkmcnt(1)
mav 0,96,56
R 36,9216
mav 4,100,56
mav 8,104,56
mav 12,108,56
mav 0,112,60
R 40,10240
mav 4,116,60
mav 8,120,60
mav 12,124,60
s_waitcnt lgkmcnt(1)
mav 0,128,32
R 44,11264
mav 4,132,32
mav 8,136,32
mav 12,140,32
mav 0,144,36
R 48,12288
v_add_u32 v72, 1, v72
mav 4,148,36
mav 8,152,36
mav 12,156,36
s_waitcnt lgkmcnt(1)
mav 0,160,40
R 52,13312
mav 4,164,40
mav 8,168,40
mav 12,172,40
mav 0,176,44
R 56,14336
mav 4,180,44
mav 8,184,44
mav 12,188,44
s_waitcnt lgkmcnt(1)
mav 0,192,48
R 60,15360
mav 4,196,48
mav 8,200,48
mav 12,204,48
mav 0,208,52
R 32,16384
mav 4,212,52
mav 8,216,52
mav 12,220,52
s_waitcnt lgkmcnt(1)
mav 0,224,56
R 36,17408
mav 4,228,56
mav 8,232,56
mav 12,236,56
mav 0,240,60
s_waitcnt vmcnt(1)
R 40,18432
mav 4,244,60
buffer_store_dword v72, v81, s[36:39], s52 offen sc1
mav 8,248,60
mav 12,252,60
s_waitcnt lgkmcnt(1)
mvv 0,128,32
R 44,19456
mvv 4,132,32
mvv 8,136,32
mvv 12,140,32
mvv 0,144,36
R 48,20480
mvv 4,148,36
mvv 8,152,36
mvv 12,156,36
s_waitcnt lgkmcnt(1)
mvv 0,160,40
R 52,21504
mvv 4,164,40
mvv 8,168,40
mvv 12,172,40
mvv 0,176,44
R 56,22528
mvv 4,180,44
mvv 8,184,44
mvv 12,188,44
s_waitcnt lgkmcnt(1)
mvv 0,192,48
R 60,23552
mvv 4,196,48
mvv 8,200,48
s_waitcnt vmcnt(1)
s_add_u32 s49, s49, 313344
mvv 12,204,48
s_barrier
mvv 0,208,52
R 32,32768
mvv 4,212,52
mvv 8,216,52
mvv 12,220,52
s_waitcnt lgkmcnt(1)
mvv 0,224,56
R 36,33792
mvv 4,228,56
mvv 8,232,56
mvv 12,236,56
mvv 0,240,60
R 40,34816
mvv 4,244,60
mvv 8,248,60
mvv 12,252,60
s_waitcnt lgkmcnt(1)
v_mfma_f32_16x16x32_bf16 v[16:19], a[0:3], v[32:35], 0
R 44,35840
v_mfma_f32_16x16x32_bf16 v[20:23], a[4:7], v[32:35], 0
v_cvt_pk_bf16_f32 v64, v0, v1
v_cvt_pk_bf16_f32 v65, v2, v3
v_mfma_f32_16x16x32_bf16 v[24:27], a[8:11], v[32:35], 0
v_cvt_pk_bf16_f32 v66, v4, v5
v_cvt_pk_bf16_f32 v67, v6, v7
v_mfma_f32_16x16x32_bf16 v[28:31], a[12:15], v[32:35], 0
v_cvt_pk_bf16_f32 v68, v8, v9
v_cvt_pk_bf16_f32 v69, v10, v11
mav 16,16,36
R 48,36864
v_cvt_pk_bf16_f32 v70, v12, v13
mav 20,20,36
buffer_store_dwordx4 v[64:67], v87, s[36:39], s49 offen sc1
v_cvt_pk_bf16_f32 v71, v14, v15
mav 24,24,36
mav 28,28,36
s_waitcnt lgkmcnt(1)
mav 16,32,40
R 52,37888
mav 20,36,40
buffer_store_dwordx4 v[68:71], v87, s[36:39], s49 offen offset:1024 sc1
mav 24,40,40
mav 28,44,40
mav 16,48,44
R 56,38912
mav 20,52,44
mav 24,56,44
mav 28,60,44
s_waitcnt lgkmcnt(1)
mav 16,64,48
R 60,39936
mav 20,68,48
mav 24,72,48
mav 28,76,48
mav 16,80,52
R 32,40960
mav 20,84,52
mav 24,88,52
mav 28,92,52
s_waitcnt lgkmcnt(1)
mav 16,96,56
R 36,41984
mav 20,100,56
mav 24,104,56
mav 28,108,56
mav 16,112,60
R 40,43008
mav 20,116,60
mav 24,120,60
mav 28,124,60
s_waitcnt lgkmcnt(1)
mav 16,128,32
R 44,44032
mav 20,132,32
mav 24,136,32
mav 28,140,32
mav 16,144,36
R 48,45056
v_add_u32 v72, 1, v72
mav 20,148,36
mav 24,152,36
mav 28,156,36
s_waitcnt lgkmcnt(1)
mav 16,160,40
R 52,46080
mav 20,164,40
mav 24,168,40
mav 28,172,40
mav 16,176,44
R 56,47104
mav 20,180,44
mav 24,184,44
mav 28,188,44
s_waitcnt lgkmcnt(1)
mav 16,192,48
R 60,48128
mav 20,196,48
mav 24,200,48
mav 28,204,48
mav 16,208,52
R 32,49152
mav 20,212,52
mav 24,216,52
mav 28,220,52
s_waitcnt lgkmcnt(1)
mav 16,224,56
R 36,50176
mav 20,228,56
mav 24,232,56
mav 28,236,56
mav 16,240,60
s_waitcnt vmcnt(0)
R 40,51200
mav 20,244,60
buffer_store_dword v72, v81, s[36:39], s52 offen sc1
mav 24,248,60
mav 28,252,60
s_waitcnt lgkmcnt(1)
mvv 16,128,32
R 44,52224
mvv 20,132,32
mvv 24,136,32
mvv 28,140,32
mvv 16,144,36
R 48,53248
mvv 20,148,36
mvv 24,152,36
mvv 28,156,36
s_waitcnt lgkmcnt(1)
mvv 16,160,40
R 52,54272
mvv 20,164,40
mvv 24,168,40
mvv 28,172,40
mvv 16,176,44
R 56,55296
mvv 20,180,44
mvv 24,184,44
mvv 28,188,44
s_waitcnt lgkmcnt(1)
mvv 16,192,48
R 60,56320
mvv 20,196,48
mvv 24,200,48
s_add_u32 s49, s49, 313344
mvv 28,204,48
mvv 16,208,52
mvv 20,212,52
mvv 24,216,52
mvv 28,220,52
s_waitcnt lgkmcnt(0)
mvv 16,224,56
mvv 20,228,56
mvv 24,232,56
mvv 28,236,56
mvv 16,240,60
mvv 20,244,60
mvv 24,248,60
mvv 28,252,60
s_nop 4
v_cvt_pk_bf16_f32 v64, v16, v17
v_cvt_pk_bf16_f32 v65, v18, v19
v_cvt_pk_bf16_f32 v66, v20, v21
v_cvt_pk_bf16_f32 v67, v22, v23
buffer_store_dwordx4 v[64:67], v87, s[36:39], s49 offen sc1
v_cvt_pk_bf16_f32 v68, v24, v25
v_cvt_pk_bf16_f32 v69, v26, v27
v_cvt_pk_bf16_f32 v70, v28, v29
v_cvt_pk_bf16_f32 v71, v30, v31
buffer_store_dwordx4 v[68:71], v87, s[36:39], s49 offen offset:1024 sc1
v_add_u32 v72, 35, v72
s_waitcnt vmcnt(0)
buffer_store_dword v72, v81, s[36:39], s52 offen sc1
s_endpgm
CG0_1:
s_cmp_eq_u32 s61, 0
s_cbranch_scc1 BG0_1
s_sub_u32 s61, s61, 1
s_sleep 8
buffer_load_dwordx2 v[74:75], v85, s[36:39], 0 offen sc1
s_waitcnt vmcnt(0)
ds_write_b32 v82, v74
v_sub_u32 v74, v74, v86
v_xor_b32 v75, v75, v73
v_cmp_lt_u64 vcc, s[62:63], v[74:75]
s_waitcnt lgkmcnt(0)
s_cbranch_vccnz CG0_1
s_branch BG0_1
CG0_2:
s_mov_b32 s58, m0
s_mov_b32 m0, 144384
DG0_2:
s_cmp_eq_u32 s61, 0
s_cbranch_scc1 EG0_2
s_sub_u32 s61, s61, 1
buffer_load_dword v85, s[36:39], 0 offen sc1 lds
s_sleep 1
ds_read_b32 v74, v82
s_waitcnt lgkmcnt(0)
v_cmp_gt_i32 vcc, v86, v74
s_cbranch_vccnz DG0_2
EG0_2:
s_mov_b32 m0, s58
s_branch BG0_2
CG0_3:
s_mov_b32 s58, m0
s_mov_b32 m0, 144384
DG0_3:
s_cmp_eq_u32 s61, 0
s_cbranch_scc1 EG0_3
s_sub_u32 s61, s61, 1
buffer_load_dword v85, s[36:39], 0 offen sc1 lds
s_sleep 1
ds_read_b32 v74, v82
s_waitcnt lgkmcnt(0)
v_cmp_gt_i32 vcc, v86, v74
s_cbranch_vccnz DG0_3
EG0_3:
s_mov_b32 m0, s58
s_branch BG0_3
CG0_5:
s_mov_b32 s58, m0
s_mov_b32 m0, 144384
DG0_5:
s_cmp_eq_u32 s61, 0
s_cbranch_scc1 EG0_5
s_sub_u32 s61, s61, 1
buffer_load_dword v85, s[36:39], 0 offen sc1 lds
s_sleep 1
ds_read_b32 v74, v82
s_waitcnt lgkmcnt(0)
v_cmp_gt_i32 vcc, v86, v74
s_cbranch_vccnz DG0_5
EG0_5:
s_mov_b32 m0, s58
s_branch BG0_5
CG0_6:
s_mov_b32 s58, m0
s_mov_b32 m0, 144384
DG0_6:
s_cmp_eq_u32 s61, 0
s_cbranch_scc1 EG0_6
s_sub_u32 s61, s61, 1
buffer_load_dword v85, s[36:39], 0 offen sc1 lds
s_sleep 1
ds_read_b32 v74, v82
s_waitcnt lgkmcnt(0)
v_cmp_gt_i32 vcc, v86, v74
s_cbranch_vccnz DG0_6
EG0_6:
s_mov_b32 m0, s58
s_branch BG0_6
CG0_7:
s_mov_b32 s58, m0
s_mov_b32 m0, 144384
DG0_7:
s_cmp_eq_u32 s61, 0
s_cbranch_scc1 EG0_7
s_sub_u32 s61, s61, 1
buffer_load_dword v85, s[36:39], 0 offen sc1 lds
s_sleep 1
ds_read_b32 v74, v82
s_waitcnt lgkmcnt(0)
v_cmp_gt_i32 vcc, v86, v74
s_cbranch_vccnz DG0_7
EG0_7:
s_mov_b32 m0, s58
s_branch BG0_7
LvG1:
LsG4:
s_cbranch_execz LsG8
v_add_u32 v72, 1, v72
s_add_u32 s49, s49, 313344
s_barrier
s_add_u32 m0, s50, 0
buffer_store_dword v72, v81, s[36:39], s52 offen sc1
D 83,0
D 83,1024
D 83,2048
D 83,3072
s_add_u32 m0, s50, 4096
s_nop 0
D 84,0
D 84,1024
v_add_u32 v72, 1, v72
s_waitcnt vmcnt(0)
s_add_u32 s49, s49, 313344
s_barrier
s_add_u32 m0, s50, 32768
R 32,0
buffer_store_dword v72, v81, s[36:39], s52 offen sc1
D 83,0
R 36,1024
D 83,1024
R 40,2048
D 83,2048
s_waitcnt lgkmcnt(1)
v_mfma_f32_16x16x32_bf16 v[0:3], a[0:3], v[32:35], 0
R 44,3072
v_mfma_f32_16x16x32_bf16 v[4:7], a[4:7], v[32:35], 0
v_mfma_f32_16x16x32_bf16 v[8:11], a[8:11], v[32:35], 0
v_mfma_f32_16x16x32_bf16 v[12:15], a[12:15], v[32:35], 0
mav 0,16,36
D 83,3072
mav 4,20,36
s_add_u32 m0, s50, 36864
R 48,4096
mav 8,24,36
mav 12,28,36
s_waitcnt lgkmcnt(1)
mav 0,32,40
D 84,0
mav 4,36,40
R 52,5120
mav 8,40,40
mav 12,44,40
mav 0,48,44
D 84,1024
mav 4,52,44
R 56,6144
mav 8,56,44
mav 12,60,44
s_waitcnt lgkmcnt(1)
mav 0,64,48
R 60,7168
mav 4,68,48
mav 8,72,48
mav 12,76,48
mav 0,80,52
R 32,8192
mav 4,84,52
mav 8,88,52
mav 12,92,52
s_waitcnt lgkmcnt(1)
mav 0,96,56
R 36,9216
mav 4,100,56
mav 8,104,56
mav 12,108,56
mav 0,112,60
R 40,10240
mav 4,116,60
mav 8,120,60
mav 12,124,60
s_waitcnt lgkmcnt(1)
mav 0,128,32
R 44,11264
mav 4,132,32
mav 8,136,32
mav 12,140,32
mav 0,144,36
R 48,12288
mav 4,148,36
mav 8,152,36
mav 12,156,36
s_waitcnt lgkmcnt(1)
mav 0,160,40
R 52,13312
mav 4,164,40
mav 8,168,40
mav 12,172,40
mav 0,176,44
R 56,14336
mav 4,180,44
mav 8,184,44
mav 12,188,44
s_waitcnt lgkmcnt(1)
mav 0,192,48
R 60,15360
mav 4,196,48
mav 8,200,48
mav 12,204,48
mav 0,208,52
R 32,16384
mav 4,212,52
mav 8,216,52
mav 12,220,52
s_waitcnt lgkmcnt(1)
mav 0,224,56
R 36,17408
mav 4,228,56
mav 8,232,56
mav 12,236,56
mav 0,240,60
R 40,18432
mav 4,244,60
mav 8,248,60
mav 12,252,60
s_waitcnt lgkmcnt(1)
mvv 0,128,32
R 44,19456
mvv 4,132,32
mvv 8,136,32
mvv 12,140,32
mvv 0,144,36
R 48,20480
mvv 4,148,36
mvv 8,152,36
mvv 12,156,36
s_waitcnt lgkmcnt(1)
mvv 0,160,40
R 52,21504
LsG5:
s_cbranch_execz LsG9
mvv 4,164,40
mvv 8,168,40
mvv 12,172,40
mvv 0,176,44
R 56,22528
mvv 4,180,44
mvv 8,184,44
s_xor_b32 s50, s50, 0x10000
mvv 12,188,44
s_waitcnt lgkmcnt(1)
mvv 0,192,48
R 60,23552
mvv 4,196,48
mvv 8,200,48
s_waitcnt vmcnt(0)
s_add_u32 s49, s49, 313344
mvv 12,204,48
s_barrier
mvv 0,208,52
s_add_u32 m0, s50, 0
R 32,32768
mvv 4,212,52
mvv 8,216,52
mvv 12,220,52
s_waitcnt lgkmcnt(1)
mvv 0,224,56
D 83,0
mvv 4,228,56
R 36,33792
mvv 8,232,56
mvv 12,236,56
mvv 0,240,60
D 83,1024
mvv 4,244,60
R 40,34816
mvv 8,248,60
mvv 12,252,60
s_waitcnt lgkmcnt(1)
v_mfma_f32_16x16x32_bf16 v[16:19], a[0:3], v[32:35], 0
D 83,2048
v_mfma_f32_16x16x32_bf16 v[20:23], a[4:7], v[32:35], 0
R 44,35840
v_cvt_pk_bf16_f32 v64, v0, v1
v_mfma_f32_16x16x32_bf16 v[24:27], a[8:11], v[32:35], 0
v_cvt_pk_bf16_f32 v65, v2, v3
v_cvt_pk_bf16_f32 v66, v4, v5
v_mfma_f32_16x16x32_bf16 v[28:31], a[12:15], v[32:35], 0
v_cvt_pk_bf16_f32 v67, v6, v7
v_cvt_pk_bf16_f32 v68, v8, v9
mav 16,16,36
buffer_store_dwordx4 v[64:67], v87, s[36:39], s49 offen sc1
D 83,3072
mav 20,20,36
s_add_u32 m0, s50, 4096
R 48,36864
v_cvt_pk_bf16_f32 v69, v10, v11
mav 24,24,36
v_cvt_pk_bf16_f32 v70, v12, v13
v_cvt_pk_bf16_f32 v71, v14, v15
mav 28,28,36
s_waitcnt lgkmcnt(1)
mav 16,32,40
buffer_store_dwordx4 v[68:71], v87, s[36:39], s49 offen offset:1024 sc1
D 84,0
mav 20,36,40
R 52,37888
mav 24,40,40
mav 28,44,40
mav 16,48,44
D 84,1024
mav 20,52,44
R 56,38912
mav 24,56,44
mav 28,60,44
s_waitcnt lgkmcnt(1)
mav 16,64,48
R 60,39936
mav 20,68,48
mav 24,72,48
mav 28,76,48
mav 16,80,52
R 32,40960
mav 20,84,52
mav 24,88,52
mav 28,92,52
s_waitcnt lgkmcnt(1)
mav 16,96,56
R 36,41984
mav 20,100,56
mav 24,104,56
mav 28,108,56
mav 16,112,60
R 40,43008
mav 20,116,60
mav 24,120,60
mav 28,124,60
s_waitcnt lgkmcnt(1)
mav 16,128,32
R 44,44032
mav 20,132,32
mav 24,136,32
mav 28,140,32
mav 16,144,36
R 48,45056
v_add_u32 v72, 1, v72
mav 20,148,36
mav 24,152,36
mav 28,156,36
s_waitcnt lgkmcnt(1)
mav 16,160,40
R 52,46080
mav 20,164,40
mav 24,168,40
mav 28,172,40
mav 16,176,44
R 56,47104
mav 20,180,44
mav 24,184,44
mav 28,188,44
s_waitcnt lgkmcnt(1)
mav 16,192,48
R 60,48128
mav 20,196,48
mav 24,200,48
s_mov_b32 s60, -30
.p2align 6
LG1_1:
mav 28,204,48
mav 16,208,52
R 32,49152
mav 20,212,52
mav 24,216,52
mav 28,220,52
s_waitcnt lgkmcnt(1)
mav 16,224,56
R 36,50176
mav 20,228,56
mav 24,232,56
mav 28,236,56
mav 16,240,60
s_waitcnt vmcnt(2)
buffer_store_dword v72, v81, s[36:39], s52 offen sc1
mav 20,244,60
R 40,51200
mav 24,248,60
mav 28,252,60
s_waitcnt lgkmcnt(1)
mvv 16,128,32
R 44,52224
mvv 20,132,32
mvv 24,136,32
mvv 28,140,32
mvv 16,144,36
R 48,53248
mvv 20,148,36
mvv 24,152,36
mvv 28,156,36
s_waitcnt lgkmcnt(1)
mvv 16,160,40
R 52,54272
mvv 20,164,40
mvv 24,168,40
mvv 28,172,40
mvv 16,176,44
R 56,55296
mvv 20,180,44
mvv 24,184,44
mvv 28,188,44
s_waitcnt lgkmcnt(1)
mvv 16,192,48
R 60,56320
mvv 20,196,48
v_xor_b32 v80, 0x10000, v80
mvv 24,200,48
s_waitcnt vmcnt(1)
s_add_u32 s49, s49, 313344
mvv 28,204,48
s_barrier
mvv 16,208,52
s_add_u32 m0, s50, 32768
R 32,0
mvv 20,212,52
mvv 24,216,52
mvv 28,220,52
s_waitcnt lgkmcnt(1)
mvv 16,224,56
D 83,0
mvv 20,228,56
R 36,1024
mvv 24,232,56
mvv 28,236,56
mvv 16,240,60
D 83,1024
mvv 20,244,60
R 40,2048
mvv 24,248,60
mvv 28,252,60
s_waitcnt lgkmcnt(1)
v_mfma_f32_16x16x32_bf16 v[0:3], a[0:3], v[32:35], 0
D 83,2048
v_mfma_f32_16x16x32_bf16 v[4:7], a[4:7], v[32:35], 0
R 44,3072
v_cvt_pk_bf16_f32 v64, v16, v17
v_mfma_f32_16x16x32_bf16 v[8:11], a[8:11], v[32:35], 0
v_cvt_pk_bf16_f32 v65, v18, v19
v_cvt_pk_bf16_f32 v66, v20, v21
v_mfma_f32_16x16x32_bf16 v[12:15], a[12:15], v[32:35], 0
v_cvt_pk_bf16_f32 v67, v22, v23
v_cvt_pk_bf16_f32 v68, v24, v25
mav 0,16,36
buffer_store_dwordx4 v[64:67], v87, s[36:39], s49 offen sc1
D 83,3072
mav 4,20,36
s_add_u32 m0, s50, 36864
R 48,4096
v_cvt_pk_bf16_f32 v69, v26, v27
mav 8,24,36
v_cvt_pk_bf16_f32 v70, v28, v29
v_cvt_pk_bf16_f32 v71, v30, v31
mav 12,28,36
s_waitcnt lgkmcnt(1)
mav 0,32,40
buffer_store_dwordx4 v[68:71], v87, s[36:39], s49 offen offset:1024 sc1
D 84,0
mav 4,36,40
R 52,5120
mav 8,40,40
mav 12,44,40
mav 0,48,44
D 84,1024
mav 4,52,44
R 56,6144
mav 8,56,44
mav 12,60,44
s_waitcnt lgkmcnt(1)
mav 0,64,48
R 60,7168
mav 4,68,48
mav 8,72,48
mav 12,76,48
mav 0,80,52
R 32,8192
mav 4,84,52
mav 8,88,52
mav 12,92,52
s_waitcnt lgkmcnt(1)
mav 0,96,56
R 36,9216
mav 4,100,56
mav 8,104,56
mav 12,108,56
mav 0,112,60
R 40,10240
mav 4,116,60
mav 8,120,60
mav 12,124,60
s_waitcnt lgkmcnt(1)
mav 0,128,32
R 44,11264
mav 4,132,32
mav 8,136,32
mav 12,140,32
mav 0,144,36
R 48,12288
v_add_u32 v72, 1, v72
mav 4,148,36
mav 8,152,36
mav 12,156,36
s_waitcnt lgkmcnt(1)
mav 0,160,40
R 52,13312
mav 4,164,40
mav 8,168,40
mav 12,172,40
mav 0,176,44
R 56,14336
mav 4,180,44
mav 8,184,44
mav 12,188,44
s_waitcnt lgkmcnt(1)
mav 0,192,48
R 60,15360
mav 4,196,48
mav 8,200,48
mav 12,204,48
mav 0,208,52
R 32,16384
mav 4,212,52
mav 8,216,52
mav 12,220,52
s_waitcnt lgkmcnt(1)
mav 0,224,56
R 36,17408
mav 4,228,56
mav 8,232,56
mav 12,236,56
mav 0,240,60
s_waitcnt vmcnt(2)
buffer_store_dword v72, v81, s[36:39], s52 offen sc1
mav 4,244,60
R 40,18432
mav 8,248,60
mav 12,252,60
s_waitcnt lgkmcnt(1)
mvv 0,128,32
R 44,19456
mvv 4,132,32
mvv 8,136,32
)"
R"(
mvv 12,140,32
mvv 0,144,36
R 48,20480
mvv 4,148,36
mvv 8,152,36
mvv 12,156,36
s_waitcnt lgkmcnt(1)
mvv 0,160,40
R 52,21504
mvv 4,164,40
mvv 8,168,40
mvv 12,172,40
mvv 0,176,44
R 56,22528
mvv 4,180,44
mvv 8,184,44
s_xor_b32 s50, s50, 0x10000
mvv 12,188,44
s_waitcnt lgkmcnt(1)
mvv 0,192,48
R 60,23552
mvv 4,196,48
mvv 8,200,48
s_waitcnt vmcnt(1)
s_add_u32 s49, s49, 313344
mvv 12,204,48
s_barrier
mvv 0,208,52
s_add_u32 m0, s50, 0
R 32,32768
mvv 4,212,52
mvv 8,216,52
mvv 12,220,52
s_waitcnt lgkmcnt(1)
mvv 0,224,56
D 83,0
mvv 4,228,56
R 36,33792
mvv 8,232,56
mvv 12,236,56
mvv 0,240,60
D 83,1024
mvv 4,244,60
R 40,34816
mvv 8,248,60
mvv 12,252,60
s_waitcnt lgkmcnt(1)
v_mfma_f32_16x16x32_bf16 v[16:19], a[0:3], v[32:35], 0
D 83,2048
v_mfma_f32_16x16x32_bf16 v[20:23], a[4:7], v[32:35], 0
R 44,35840
v_cvt_pk_bf16_f32 v64, v0, v1
v_mfma_f32_16x16x32_bf16 v[24:27], a[8:11], v[32:35], 0
v_cvt_pk_bf16_f32 v65, v2, v3
v_cvt_pk_bf16_f32 v66, v4, v5
v_mfma_f32_16x16x32_bf16 v[28:31], a[12:15], v[32:35], 0
v_cvt_pk_bf16_f32 v67, v6, v7
v_cvt_pk_bf16_f32 v68, v8, v9
mav 16,16,36
buffer_store_dwordx4 v[64:67], v87, s[36:39], s49 offen sc1
D 83,3072
mav 20,20,36
s_add_u32 m0, s50, 4096
R 48,36864
v_cvt_pk_bf16_f32 v69, v10, v11
mav 24,24,36
v_cvt_pk_bf16_f32 v70, v12, v13
v_cvt_pk_bf16_f32 v71, v14, v15
mav 28,28,36
s_waitcnt lgkmcnt(1)
mav 16,32,40
buffer_store_dwordx4 v[68:71], v87, s[36:39], s49 offen offset:1024 sc1
D 84,0
mav 20,36,40
R 52,37888
mav 24,40,40
mav 28,44,40
mav 16,48,44
D 84,1024
mav 20,52,44
R 56,38912
mav 24,56,44
mav 28,60,44
s_waitcnt lgkmcnt(1)
mav 16,64,48
R 60,39936
mav 20,68,48
mav 24,72,48
mav 28,76,48
mav 16,80,52
R 32,40960
mav 20,84,52
mav 24,88,52
mav 28,92,52
s_waitcnt lgkmcnt(1)
mav 16,96,56
R 36,41984
mav 20,100,56
mav 24,104,56
mav 28,108,56
mav 16,112,60
R 40,43008
mav 20,116,60
mav 24,120,60
mav 28,124,60
s_waitcnt lgkmcnt(1)
mav 16,128,32
R 44,44032
mav 20,132,32
mav 24,136,32
mav 28,140,32
mav 16,144,36
R 48,45056
v_add_u32 v72, 1, v72
mav 20,148,36
mav 24,152,36
mav 28,156,36
s_waitcnt lgkmcnt(1)
mav 16,160,40
R 52,46080
mav 20,164,40
mav 24,168,40
mav 28,172,40
mav 16,176,44
R 56,47104
mav 20,180,44
mav 24,184,44
mav 28,188,44
s_waitcnt lgkmcnt(1)
mav 16,192,48
R 60,48128
mav 20,196,48
s_add_u32 s60, s60, 1
mav 24,200,48
s_cbranch_scc0 LG1_1
mav 28,204,48
mav 16,208,52
R 32,49152
mav 20,212,52
mav 24,216,52
mav 28,220,52
s_waitcnt lgkmcnt(1)
mav 16,224,56
R 36,50176
mav 20,228,56
mav 24,232,56
mav 28,236,56
mav 16,240,60
s_waitcnt vmcnt(2)
buffer_store_dword v72, v81, s[36:39], s52 offen sc1
mav 20,244,60
R 40,51200
mav 24,248,60
mav 28,252,60
s_waitcnt lgkmcnt(1)
mvv 16,128,32
R 44,52224
mvv 20,132,32
mvv 24,136,32
mvv 28,140,32
mvv 16,144,36
R 48,53248
mvv 20,148,36
mvv 24,152,36
mvv 28,156,36
s_waitcnt lgkmcnt(1)
mvv 16,160,40
R 52,54272
mvv 20,164,40
mvv 24,168,40
mvv 28,172,40
mvv 16,176,44
R 56,55296
mvv 20,180,44
mvv 24,184,44
mvv 28,188,44
s_waitcnt lgkmcnt(1)
mvv 16,192,48
R 60,56320
mvv 20,196,48
v_xor_b32 v80, 0x10000, v80
mvv 24,200,48
s_waitcnt vmcnt(1)
s_add_u32 s49, s49, 313344
mvv 28,204,48
s_barrier
mvv 16,208,52
s_add_u32 m0, s50, 32768
R 32,0
mvv 20,212,52
mvv 24,216,52
mvv 28,220,52
s_waitcnt lgkmcnt(1)
mvv 16,224,56
D 83,0
mvv 20,228,56
R 36,1024
mvv 24,232,56
mvv 28,236,56
mvv 16,240,60
D 83,1024
mvv 20,244,60
R 40,2048
mvv 24,248,60
mvv 28,252,60
s_waitcnt lgkmcnt(1)
v_mfma_f32_16x16x32_bf16 v[0:3], a[0:3], v[32:35], 0
D 83,2048
v_mfma_f32_16x16x32_bf16 v[4:7], a[4:7], v[32:35], 0
R 44,3072
v_cvt_pk_bf16_f32 v64, v16, v17
v_mfma_f32_16x16x32_bf16 v[8:11], a[8:11], v[32:35], 0
v_cvt_pk_bf16_f32 v65, v18, v19
v_cvt_pk_bf16_f32 v66, v20, v21
v_mfma_f32_16x16x32_bf16 v[12:15], a[12:15], v[32:35], 0
v_cvt_pk_bf16_f32 v67, v22, v23
v_cvt_pk_bf16_f32 v68, v24, v25
mav 0,16,36
buffer_store_dwordx4 v[64:67], v87, s[36:39], s49 offen sc1
D 83,3072
mav 4,20,36
s_add_u32 m0, s50, 36864
R 48,4096
v_cvt_pk_bf16_f32 v69, v26, v27
mav 8,24,36
v_cvt_pk_bf16_f32 v70, v28, v29
v_cvt_pk_bf16_f32 v71, v30, v31
mav 12,28,36
s_waitcnt lgkmcnt(1)
mav 0,32,40
buffer_store_dwordx4 v[68:71], v87, s[36:39], s49 offen offset:1024 sc1
D 84,0
mav 4,36,40
R 52,5120
LsG6:
s_cbranch_execz LsG10
mav 8,40,40
mav 12,44,40
mav 0,48,44
D 84,1024
mav 4,52,44
R 56,6144
mav 8,56,44
mav 12,60,44
s_waitcnt lgkmcnt(1)
mav 0,64,48
R 60,7168
mav 4,68,48
mav 8,72,48
mav 12,76,48
mav 0,80,52
R 32,8192
mav 4,84,52
mav 8,88,52
mav 12,92,52
s_waitcnt lgkmcnt(1)
mav 0,96,56
R 36,9216
mav 4,100,56
mav 8,104,56
mav 12,108,56
mav 0,112,60
R 40,10240
mav 4,116,60
mav 8,120,60
mav 12,124,60
s_waitcnt lgkmcnt(1)
mav 0,128,32
R 44,11264
mav 4,132,32
mav 8,136,32
mav 12,140,32
mav 0,144,36
R 48,12288
v_add_u32 v72, 1, v72
mav 4,148,36
mav 8,152,36
mav 12,156,36
s_waitcnt lgkmcnt(1)
mav 0,160,40
R 52,13312
mav 4,164,40
mav 8,168,40
mav 12,172,40
mav 0,176,44
R 56,14336
mav 4,180,44
mav 8,184,44
mav 12,188,44
s_waitcnt lgkmcnt(1)
mav 0,192,48
R 60,15360
mav 4,196,48
mav 8,200,48
mav 12,204,48
mav 0,208,52
R 32,16384
mav 4,212,52
mav 8,216,52
mav 12,220,52
s_waitcnt lgkmcnt(1)
mav 0,224,56
R 36,17408
mav 4,228,56
mav 8,232,56
mav 12,236,56
mav 0,240,60
s_waitcnt vmcnt(2)
buffer_store_dword v72, v81, s[36:39], s52 offen sc1
mav 4,244,60
R 40,18432
mav 8,248,60
mav 12,252,60
s_waitcnt lgkmcnt(1)
mvv 0,128,32
R 44,19456
mvv 4,132,32
mvv 8,136,32
mvv 12,140,32
mvv 0,144,36
R 48,20480
mvv 4,148,36
mvv 8,152,36
mvv 12,156,36
s_waitcnt lgkmcnt(1)
mvv 0,160,40
R 52,21504
mvv 4,164,40
mvv 8,168,40
mvv 12,172,40
mvv 0,176,44
R 56,22528
mvv 4,180,44
mvv 8,184,44
mvv 12,188,44
s_waitcnt lgkmcnt(1)
mvv 0,192,48
R 60,23552
mvv 4,196,48
mvv 8,200,48
s_waitcnt vmcnt(1)
s_add_u32 s49, s49, 313344
mvv 12,204,48
s_barrier
mvv 0,208,52
R 32,32768
mvv 4,212,52
mvv 8,216,52
mvv 12,220,52
s_waitcnt lgkmcnt(1)
mvv 0,224,56
R 36,33792
mvv 4,228,56
mvv 8,232,56
mvv 12,236,56
mvv 0,240,60
R 40,34816
mvv 4,244,60
mvv 8,248,60
mvv 12,252,60
s_waitcnt lgkmcnt(1)
v_mfma_f32_16x16x32_bf16 v[16:19], a[0:3], v[32:35], 0
R 44,35840
v_mfma_f32_16x16x32_bf16 v[20:23], a[4:7], v[32:35], 0
v_cvt_pk_bf16_f32 v64, v0, v1
v_cvt_pk_bf16_f32 v65, v2, v3
v_mfma_f32_16x16x32_bf16 v[24:27], a[8:11], v[32:35], 0
v_cvt_pk_bf16_f32 v66, v4, v5
v_cvt_pk_bf16_f32 v67, v6, v7
v_mfma_f32_16x16x32_bf16 v[28:31], a[12:15], v[32:35], 0
v_cvt_pk_bf16_f32 v68, v8, v9
v_cvt_pk_bf16_f32 v69, v10, v11
mav 16,16,36
buffer_store_dwordx4 v[64:67], v87, s[36:39], s49 offen sc1
R 48,36864
v_cvt_pk_bf16_f32 v70, v12, v13
mav 20,20,36
v_cvt_pk_bf16_f32 v71, v14, v15
mav 24,24,36
mav 28,28,36
s_waitcnt lgkmcnt(1)
mav 16,32,40
buffer_store_dwordx4 v[68:71], v87, s[36:39], s49 offen offset:1024 sc1
R 52,37888
mav 20,36,40
mav 24,40,40
mav 28,44,40
mav 16,48,44
R 56,38912
mav 20,52,44
mav 24,56,44
mav 28,60,44
s_waitcnt lgkmcnt(1)
mav 16,64,48
R 60,39936
mav 20,68,48
mav 24,72,48
mav 28,76,48
mav 16,80,52
R 32,40960
mav 20,84,52
mav 24,88,52
mav 28,92,52
s_waitcnt lgkmcnt(1)
mav 16,96,56
R 36,41984
mav 20,100,56
mav 24,104,56
mav 28,108,56
mav 16,112,60
R 40,43008
mav 20,116,60
mav 24,120,60
mav 28,124,60
s_waitcnt lgkmcnt(1)
mav 16,128,32
R 44,44032
mav 20,132,32
mav 24,136,32
mav 28,140,32
mav 16,144,36
R 48,45056
v_add_u32 v72, 1, v72
mav 20,148,36
mav 24,152,36
mav 28,156,36
s_waitcnt lgkmcnt(1)
mav 16,160,40
R 52,46080
mav 20,164,40
mav 24,168,40
mav 28,172,40
mav 16,176,44
R 56,47104
mav 20,180,44
mav 24,184,44
mav 28,188,44
s_waitcnt lgkmcnt(1)
mav 16,192,48
R 60,48128
mav 20,196,48
mav 24,200,48
mav 28,204,48
mav 16,208,52
R 32,49152
mav 20,212,52
mav 24,216,52
mav 28,220,52
s_waitcnt lgkmcnt(1)
mav 16,224,56
R 36,50176
mav 20,228,56
mav 24,232,56
mav 28,236,56
mav 16,240,60
s_waitcnt vmcnt(0)
buffer_store_dword v72, v81, s[36:39], s52 offen sc1
mav 20,244,60
R 40,51200
mav 24,248,60
mav 28,252,60
s_waitcnt lgkmcnt(1)
mvv 16,128,32
R 44,52224
mvv 20,132,32
mvv 24,136,32
mvv 28,140,32
mvv 16,144,36
R 48,53248
mvv 20,148,36
mvv 24,152,36
mvv 28,156,36
s_waitcnt lgkmcnt(1)
mvv 16,160,40
R 52,54272
mvv 20,164,40
mvv 24,168,40
mvv 28,172,40
mvv 16,176,44
R 56,55296
mvv 20,180,44
mvv 24,184,44
mvv 28,188,44
s_waitcnt lgkmcnt(1)
mvv 16,192,48
R 60,56320
mvv 20,196,48
mvv 24,200,48
s_add_u32 s49, s49, 313344
mvv 28,204,48
mvv 16,208,52
mvv 20,212,52
mvv 24,216,52
mvv 28,220,52
s_waitcnt lgkmcnt(0)
mvv 16,224,56
mvv 20,228,56
mvv 24,232,56
mvv 28,236,56
mvv 16,240,60
mvv 20,244,60
mvv 24,248,60
mvv 28,252,60
s_nop 4
v_cvt_pk_bf16_f32 v64, v16, v17
v_cvt_pk_bf16_f32 v65, v18, v19
v_cvt_pk_bf16_f32 v66, v20, v21
v_cvt_pk_bf16_f32 v67, v22, v23
buffer_store_dwordx4 v[64:67], v87, s[36:39], s49 offen sc1
v_cvt_pk_bf16_f32 v68, v24, v25
v_cvt_pk_bf16_f32 v69, v26, v27
v_cvt_pk_bf16_f32 v70, v28, v29
v_cvt_pk_bf16_f32 v71, v30, v31
buffer_store_dwordx4 v[68:71], v87, s[36:39], s49 offen offset:1024 sc1
v_add_u32 v72, 35, v72
s_waitcnt vmcnt(0)
buffer_store_dword v72, v81, s[36:39], s52 offen sc1
s_endpgm
LvG2:
LsG7:
s_cbranch_execz LsG11
v_add_u32 v72, 1, v72
s_add_u32 s49, s49, 313344
s_barrier
s_add_u32 m0, s50, 0
buffer_store_dword v72, v81, s[36:39], s52 offen sc1
D 83,0
D 83,1024
D 83,2048
D 83,3072
s_add_u32 m0, s50, 4096
s_nop 0
D 84,0
D 84,1024
v_add_u32 v72, 1, v72
s_waitcnt vmcnt(0)
s_add_u32 s49, s49, 313344
s_barrier
s_add_u32 m0, s50, 32768
R 32,0
buffer_store_dword v72, v81, s[36:39], s52 offen sc1
D 83,0
R 36,1024
D 83,1024
R 40,2048
D 83,2048
s_waitcnt lgkmcnt(1)
v_mfma_f32_16x16x32_bf16 v[0:3], a[0:3], v[32:35], 0
R 44,3072
v_mfma_f32_16x16x32_bf16 v[4:7], a[4:7], v[32:35], 0
v_mfma_f32_16x16x32_bf16 v[8:11], a[8:11], v[32:35], 0
D 83,3072
v_mfma_f32_16x16x32_bf16 v[12:15], a[12:15], v[32:35], 0
s_add_u32 m0, s50, 36864
mav 0,16,36
R 48,4096
mav 4,20,36
mav 8,24,36
D 84,0
mav 12,28,36
s_waitcnt lgkmcnt(1)
mav 0,32,40
R 52,5120
mav 4,36,40
mav 8,40,40
D 84,1024
mav 12,44,40
mav 0,48,44
R 56,6144
mav 4,52,44
mav 8,56,44
mav 12,60,44
s_waitcnt lgkmcnt(1)
mav 0,64,48
R 60,7168
mav 4,68,48
mav 8,72,48
mav 12,76,48
mav 0,80,52
R 32,8192
mav 4,84,52
mav 8,88,52
mav 12,92,52
s_waitcnt lgkmcnt(1)
mav 0,96,56
R 36,9216
mav 4,100,56
mav 8,104,56
mav 12,108,56
mav 0,112,60
R 40,10240
mav 4,116,60
mav 8,120,60
mav 12,124,60
s_waitcnt lgkmcnt(1)
mav 0,128,32
R 44,11264
mav 4,132,32
mav 8,136,32
mav 12,140,32
mav 0,144,36
R 48,12288
mav 4,148,36
mav 8,152,36
mav 12,156,36
s_waitcnt lgkmcnt(1)
mav 0,160,40
R 52,13312
mav 4,164,40
mav 8,168,40
mav 12,172,40
mav 0,176,44
R 56,14336
mav 4,180,44
mav 8,184,44
mav 12,188,44
s_waitcnt lgkmcnt(1)
mav 0,192,48
R 60,15360
mav 4,196,48
mav 8,200,48
mav 12,204,48
mav 0,208,52
R 32,16384
mav 4,212,52
mav 8,216,52
mav 12,220,52
s_waitcnt lgkmcnt(1)
mav 0,224,56
R 36,17408
mav 4,228,56
mav 8,232,56
mav 12,236,56
mav 0,240,60
R 40,18432
mav 4,244,60
mav 8,248,60
mav 12,252,60
s_waitcnt lgkmcnt(1)
mvv 0,128,32
R 44,19456
mvv 4,132,32
mvv 8,136,32
mvv 12,140,32
mvv 0,144,36
R 48,20480
mvv 4,148,36
mvv 8,152,36
mvv 12,156,36
s_waitcnt lgkmcnt(1)
mvv 0,160,40
R 52,21504
mvv 4,164,40
mvv 8,168,40
mvv 12,172,40
mvv 0,176,44
R 56,22528
mvv 4,180,44
mvv 8,184,44
s_xor_b32 s50, s50, 0x10000
mvv 12,188,44
s_waitcnt lgkmcnt(1)
mvv 0,192,48
R 60,23552
mvv 4,196,48
mvv 8,200,48
s_waitcnt vmcnt(0)
s_add_u32 s49, s49, 313344
mvv 12,204,48
s_barrier
mvv 0,208,52
s_add_u32 m0, s50, 0
R 32,32768
mvv 4,212,52
mvv 8,216,52
D 83,0
mvv 12,220,52
s_waitcnt lgkmcnt(1)
mvv 0,224,56
R 36,33792
mvv 4,228,56
mvv 8,232,56
D 83,1024
mvv 12,236,56
mvv 0,240,60
R 40,34816
mvv 4,244,60
mvv 8,248,60
D 83,2048
mvv 12,252,60
s_waitcnt lgkmcnt(1)
v_mfma_f32_16x16x32_bf16 v[16:19], a[0:3], v[32:35], 0
R 44,35840
v_cvt_pk_bf16_f32 v64, v0, v1
v_mfma_f32_16x16x32_bf16 v[20:23], a[4:7], v[32:35], 0
v_cvt_pk_bf16_f32 v65, v2, v3
v_cvt_pk_bf16_f32 v66, v4, v5
v_mfma_f32_16x16x32_bf16 v[24:27], a[8:11], v[32:35], 0
D 83,3072
v_mfma_f32_16x16x32_bf16 v[28:31], a[12:15], v[32:35], 0
s_add_u32 m0, s50, 4096
v_cvt_pk_bf16_f32 v67, v6, v7
v_cvt_pk_bf16_f32 v68, v8, v9
mav 16,16,36
R 48,36864
v_cvt_pk_bf16_f32 v69, v10, v11
mav 20,20,36
v_cvt_pk_bf16_f32 v70, v12, v13
v_cvt_pk_bf16_f32 v71, v14, v15
mav 24,24,36
buffer_store_dwordx4 v[64:67], v87, s[36:39], s49 offen sc1
D 84,0
mav 28,28,36
s_waitcnt lgkmcnt(1)
mav 16,32,40
R 52,37888
mav 20,36,40
mav 24,40,40
buffer_store_dwordx4 v[68:71], v87, s[36:39], s49 offen offset:1024 sc1
D 84,1024
mav 28,44,40
mav 16,48,44
R 56,38912
mav 20,52,44
mav 24,56,44
mav 28,60,44
s_waitcnt lgkmcnt(1)
mav 16,64,48
R 60,39936
mav 20,68,48
mav 24,72,48
mav 28,76,48
mav 16,80,52
R 32,40960
mav 20,84,52
mav 24,88,52
mav 28,92,52
s_waitcnt lgkmcnt(1)
mav 16,96,56
R 36,41984
mav 20,100,56
mav 24,104,56
mav 28,108,56
mav 16,112,60
R 40,43008
mav 20,116,60
mav 24,120,60
mav 28,124,60
s_waitcnt lgkmcnt(1)
mav 16,128,32
R 44,44032
mav 20,132,32
mav 24,136,32
mav 28,140,32
mav 16,144,36
R 48,45056
v_add_u32 v72, 1, v72
mav 20,148,36
mav 24,152,36
mav 28,156,36
s_waitcnt lgkmcnt(1)
mav 16,160,40
R 52,46080
mav 20,164,40
mav 24,168,40
mav 28,172,40
mav 16,176,44
R 56,47104
mav 20,180,44
mav 24,184,44
mav 28,188,44
s_waitcnt lgkmcnt(1)
mav 16,192,48
R 60,48128
mav 20,196,48
mav 24,200,48
s_mov_b32 s60, -30
.p2align 6
LG2_1:
LsG8:
s_cbranch_execz LsG12
mav 28,204,48
mav 16,208,52
R 32,49152
mav 20,212,52
mav 24,216,52
mav 28,220,52
s_waitcnt lgkmcnt(1)
mav 16,224,56
R 36,50176
mav 20,228,56
mav 24,232,56
mav 28,236,56
mav 16,240,60
s_waitcnt vmcnt(1)
R 40,51200
mav 20,244,60
mav 24,248,60
buffer_store_dword v72, v81, s[36:39], s52 offen sc1
mav 28,252,60
s_waitcnt lgkmcnt(1)
mvv 16,128,32
R 44,52224
mvv 20,132,32
mvv 24,136,32
mvv 28,140,32
mvv 16,144,36
R 48,53248
mvv 20,148,36
mvv 24,152,36
mvv 28,156,36
s_waitcnt lgkmcnt(1)
mvv 16,160,40
R 52,54272
mvv 20,164,40
mvv 24,168,40
mvv 28,172,40
mvv 16,176,44
R 56,55296
mvv 20,180,44
mvv 24,184,44
mvv 28,188,44
s_waitcnt lgkmcnt(1)
mvv 16,192,48
R 60,56320
mvv 20,196,48
v_xor_b32 v80, 0x10000, v80
mvv 24,200,48
s_waitcnt vmcnt(1)
s_add_u32 s49, s49, 313344
mvv 28,204,48
s_barrier
mvv 16,208,52
s_add_u32 m0, s50, 32768
R 32,0
mvv 20,212,52
mvv 24,216,52
D 83,0
mvv 28,220,52
s_waitcnt lgkmcnt(1)
mvv 16,224,56
R 36,1024
mvv 20,228,56
mvv 24,232,56
D 83,1024
mvv 28,236,56
mvv 16,240,60
R 40,2048
mvv 20,244,60
mvv 24,248,60
D 83,2048
mvv 28,252,60
s_waitcnt lgkmcnt(1)
v_mfma_f32_16x16x32_bf16 v[0:3], a[0:3], v[32:35], 0
R 44,3072
v_cvt_pk_bf16_f32 v64, v16, v17
v_mfma_f32_16x16x32_bf16 v[4:7], a[4:7], v[32:35], 0
v_cvt_pk_bf16_f32 v65, v18, v19
v_cvt_pk_bf16_f32 v66, v20, v21
v_mfma_f32_16x16x32_bf16 v[8:11], a[8:11], v[32:35], 0
D 83,3072
v_mfma_f32_16x16x32_bf16 v[12:15], a[12:15], v[32:35], 0
s_add_u32 m0, s50, 36864
v_cvt_pk_bf16_f32 v67, v22, v23
v_cvt_pk_bf16_f32 v68, v24, v25
mav 0,16,36
R 48,4096
v_cvt_pk_bf16_f32 v69, v26, v27
mav 4,20,36
v_cvt_pk_bf16_f32 v70, v28, v29
v_cvt_pk_bf16_f32 v71, v30, v31
mav 8,24,36
buffer_store_dwordx4 v[64:67], v87, s[36:39], s49 offen sc1
D 84,0
mav 12,28,36
s_waitcnt lgkmcnt(1)
mav 0,32,40
R 52,5120
mav 4,36,40
mav 8,40,40
buffer_store_dwordx4 v[68:71], v87, s[36:39], s49 offen offset:1024 sc1
D 84,1024
mav 12,44,40
mav 0,48,44
R 56,6144
mav 4,52,44
mav 8,56,44
mav 12,60,44
s_waitcnt lgkmcnt(1)
mav 0,64,48
R 60,7168
mav 4,68,48
mav 8,72,48
mav 12,76,48
mav 0,80,52
R 32,8192
mav 4,84,52
mav 8,88,52
mav 12,92,52
s_waitcnt lgkmcnt(1)
mav 0,96,56
R 36,9216
mav 4,100,56
mav 8,104,56
mav 12,108,56
mav 0,112,60
R 40,10240
mav 4,116,60
mav 8,120,60
mav 12,124,60
s_waitcnt lgkmcnt(1)
mav 0,128,32
R 44,11264
mav 4,132,32
mav 8,136,32
mav 12,140,32
mav 0,144,36
R 48,12288
v_add_u32 v72, 1, v72
mav 4,148,36
mav 8,152,36
mav 12,156,36
s_waitcnt lgkmcnt(1)
mav 0,160,40
R 52,13312
mav 4,164,40
mav 8,168,40
mav 12,172,40
mav 0,176,44
R 56,14336
mav 4,180,44
mav 8,184,44
mav 12,188,44
s_waitcnt lgkmcnt(1)
mav 0,192,48
R 60,15360
mav 4,196,48
mav 8,200,48
mav 12,204,48
mav 0,208,52
R 32,16384
mav 4,212,52
mav 8,216,52
mav 12,220,52
s_waitcnt lgkmcnt(1)
mav 0,224,56
R 36,17408
mav 4,228,56
mav 8,232,56
mav 12,236,56
mav 0,240,60
s_waitcnt vmcnt(1)
R 40,18432
mav 4,244,60
mav 8,248,60
buffer_store_dword v72, v81, s[36:39], s52 offen sc1
mav 12,252,60
s_waitcnt lgkmcnt(1)
mvv 0,128,32
R 44,19456
mvv 4,132,32
mvv 8,136,32
mvv 12,140,32
mvv 0,144,36
R 48,20480
mvv 4,148,36
mvv 8,152,36
mvv 12,156,36
s_waitcnt lgkmcnt(1)
mvv 0,160,40
R 52,21504
mvv 4,164,40
mvv 8,168,40
mvv 12,172,40
mvv 0,176,44
R 56,22528
mvv 4,180,44
mvv 8,184,44
s_xor_b32 s50, s50, 0x10000
mvv 12,188,44
s_waitcnt lgkmcnt(1)
mvv 0,192,48
R 60,23552
mvv 4,196,48
mvv 8,200,48
s_waitcnt vmcnt(1)
s_add_u32 s49, s49, 313344
mvv 12,204,48
s_barrier
mvv 0,208,52
s_add_u32 m0, s50, 0
R 32,32768
mvv 4,212,52
mvv 8,216,52
D 83,0
mvv 12,220,52
s_waitcnt lgkmcnt(1)
mvv 0,224,56
R 36,33792
mvv 4,228,56
mvv 8,232,56
D 83,1024
mvv 12,236,56
mvv 0,240,60
R 40,34816
mvv 4,244,60
mvv 8,248,60
D 83,2048
mvv 12,252,60
s_waitcnt lgkmcnt(1)
v_mfma_f32_16x16x32_bf16 v[16:19], a[0:3], v[32:35], 0
R 44,35840
v_cvt_pk_bf16_f32 v64, v0, v1
v_mfma_f32_16x16x32_bf16 v[20:23], a[4:7], v[32:35], 0
v_cvt_pk_bf16_f32 v65, v2, v3
v_cvt_pk_bf16_f32 v66, v4, v5
v_mfma_f32_16x16x32_bf16 v[24:27], a[8:11], v[32:35], 0
D 83,3072
v_mfma_f32_16x16x32_bf16 v[28:31], a[12:15], v[32:35], 0
s_add_u32 m0, s50, 4096
v_cvt_pk_bf16_f32 v67, v6, v7
v_cvt_pk_bf16_f32 v68, v8, v9
mav 16,16,36
R 48,36864
v_cvt_pk_bf16_f32 v69, v10, v11
mav 20,20,36
v_cvt_pk_bf16_f32 v70, v12, v13
v_cvt_pk_bf16_f32 v71, v14, v15
mav 24,24,36
buffer_store_dwordx4 v[64:67], v87, s[36:39], s49 offen sc1
D 84,0
mav 28,28,36
s_waitcnt lgkmcnt(1)
mav 16,32,40
R 52,37888
mav 20,36,40
mav 24,40,40
buffer_store_dwordx4 v[68:71], v87, s[36:39], s49 offen offset:1024 sc1
D 84,1024
mav 28,44,40
mav 16,48,44
R 56,38912
mav 20,52,44
mav 24,56,44
mav 28,60,44
s_waitcnt lgkmcnt(1)
mav 16,64,48
R 60,39936
mav 20,68,48
mav 24,72,48
mav 28,76,48
mav 16,80,52
R 32,40960
mav 20,84,52
mav 24,88,52
mav 28,92,52
s_waitcnt lgkmcnt(1)
mav 16,96,56
R 36,41984
mav 20,100,56
mav 24,104,56
mav 28,108,56
mav 16,112,60
R 40,43008
mav 20,116,60
mav 24,120,60
mav 28,124,60
s_waitcnt lgkmcnt(1)
mav 16,128,32
R 44,44032
mav 20,132,32
mav 24,136,32
mav 28,140,32
mav 16,144,36
R 48,45056
v_add_u32 v72, 1, v72
mav 20,148,36
mav 24,152,36
mav 28,156,36
s_waitcnt lgkmcnt(1)
mav 16,160,40
R 52,46080
mav 20,164,40
mav 24,168,40
mav 28,172,40
mav 16,176,44
R 56,47104
mav 20,180,44
mav 24,184,44
mav 28,188,44
s_waitcnt lgkmcnt(1)
mav 16,192,48
R 60,48128
mav 20,196,48
s_add_u32 s60, s60, 1
mav 24,200,48
s_cbranch_scc0 LG2_1
mav 28,204,48
mav 16,208,52
R 32,49152
mav 20,212,52
mav 24,216,52
mav 28,220,52
s_waitcnt lgkmcnt(1)
mav 16,224,56
R 36,50176
mav 20,228,56
mav 24,232,56
mav 28,236,56
mav 16,240,60
s_waitcnt vmcnt(1)
R 40,51200
mav 20,244,60
mav 24,248,60
buffer_store_dword v72, v81, s[36:39], s52 offen sc1
mav 28,252,60
s_waitcnt lgkmcnt(1)
mvv 16,128,32
R 44,52224
mvv 20,132,32
mvv 24,136,32
mvv 28,140,32
mvv 16,144,36
R 48,53248
mvv 20,148,36
mvv 24,152,36
mvv 28,156,36
s_waitcnt lgkmcnt(1)
mvv 16,160,40
R 52,54272
mvv 20,164,40
mvv 24,168,40
mvv 28,172,40
mvv 16,176,44
R 56,55296
mvv 20,180,44
mvv 24,184,44
mvv 28,188,44
s_waitcnt lgkmcnt(1)
mvv 16,192,48
R 60,56320
mvv 20,196,48
v_xor_b32 v80, 0x10000, v80
mvv 24,200,48
s_waitcnt vmcnt(1)
s_add_u32 s49, s49, 313344
mvv 28,204,48
s_barrier
mvv 16,208,52
s_add_u32 m0, s50, 32768
R 32,0
mvv 20,212,52
mvv 24,216,52
D 83,0
mvv 28,220,52
s_waitcnt lgkmcnt(1)
mvv 16,224,56
R 36,1024
mvv 20,228,56
mvv 24,232,56
D 83,1024
mvv 28,236,56
mvv 16,240,60
R 40,2048
mvv 20,244,60
mvv 24,248,60
D 83,2048
mvv 28,252,60
s_waitcnt lgkmcnt(1)
v_mfma_f32_16x16x32_bf16 v[0:3], a[0:3], v[32:35], 0
R 44,3072
v_cvt_pk_bf16_f32 v64, v16, v17
v_mfma_f32_16x16x32_bf16 v[4:7], a[4:7], v[32:35], 0
v_cvt_pk_bf16_f32 v65, v18, v19
v_cvt_pk_bf16_f32 v66, v20, v21
v_mfma_f32_16x16x32_bf16 v[8:11], a[8:11], v[32:35], 0
D 83,3072
v_mfma_f32_16x16x32_bf16 v[12:15], a[12:15], v[32:35], 0
s_add_u32 m0, s50, 36864
v_cvt_pk_bf16_f32 v67, v22, v23
v_cvt_pk_bf16_f32 v68, v24, v25
mav 0,16,36
R 48,4096
v_cvt_pk_bf16_f32 v69, v26, v27
mav 4,20,36
v_cvt_pk_bf16_f32 v70, v28, v29
v_cvt_pk_bf16_f32 v71, v30, v31
mav 8,24,36
buffer_store_dwordx4 v[64:67], v87, s[36:39], s49 offen sc1
D 84,0
mav 12,28,36
s_waitcnt lgkmcnt(1)
mav 0,32,40
R 52,5120
mav 4,36,40
mav 8,40,40
buffer_store_dwordx4 v[68:71], v87, s[36:39], s49 offen offset:1024 sc1
D 84,1024
mav 12,44,40
mav 0,48,44
R 56,6144
mav 4,52,44
mav 8,56,44
mav 12,60,44
s_waitcnt lgkmcnt(1)
mav 0,64,48
R 60,7168
mav 4,68,48
mav 8,72,48
mav 12,76,48
mav 0,80,52
R 32,8192
mav 4,84,52
mav 8,88,52
mav 12,92,52
s_waitcnt lgkmcnt(1)
mav 0,96,56
R 36,9216
mav 4,100,56
mav 8,104,56
mav 12,108,56
mav 0,112,60
R 40,10240
mav 4,116,60
mav 8,120,60
mav 12,124,60
s_waitcnt lgkmcnt(1)
mav 0,128,32
R 44,11264
mav 4,132,32
mav 8,136,32
mav 12,140,32
mav 0,144,36
R 48,12288
v_add_u32 v72, 1, v72
mav 4,148,36
mav 8,152,36
mav 12,156,36
s_waitcnt lgkmcnt(1)
mav 0,160,40
R 52,13312
mav 4,164,40
mav 8,168,40
mav 12,172,40
mav 0,176,44
R 56,14336
mav 4,180,44
mav 8,184,44
mav 12,188,44
s_waitcnt lgkmcnt(1)
mav 0,192,48
R 60,15360
mav 4,196,48
mav 8,200,48
mav 12,204,48
mav 0,208,52
R 32,16384
mav 4,212,52
mav 8,216,52
mav 12,220,52
s_waitcnt lgkmcnt(1)
mav 0,224,56
R 36,17408
mav 4,228,56
mav 8,232,56
mav 12,236,56
mav 0,240,60
s_waitcnt vmcnt(1)
R 40,18432
mav 4,244,60
mav 8,248,60
buffer_store_dword v72, v81, s[36:39], s52 offen sc1
mav 12,252,60
s_waitcnt lgkmcnt(1)
mvv 0,128,32
R 44,19456
mvv 4,132,32
mvv 8,136,32
mvv 12,140,32
mvv 0,144,36
R 48,20480
mvv 4,148,36
mvv 8,152,36
mvv 12,156,36
s_waitcnt lgkmcnt(1)
mvv 0,160,40
R 52,21504
mvv 4,164,40
mvv 8,168,40
mvv 12,172,40
mvv 0,176,44
R 56,22528
mvv 4,180,44
mvv 8,184,44
mvv 12,188,44
s_waitcnt lgkmcnt(1)
mvv 0,192,48
R 60,23552
mvv 4,196,48
mvv 8,200,48
s_waitcnt vmcnt(1)
s_add_u32 s49, s49, 313344
mvv 12,204,48
s_barrier
mvv 0,208,52
R 32,32768
mvv 4,212,52
mvv 8,216,52
mvv 12,220,52
s_waitcnt lgkmcnt(1)
mvv 0,224,56
R 36,33792
mvv 4,228,56
mvv 8,232,56
mvv 12,236,56
mvv 0,240,60
R 40,34816
mvv 4,244,60
mvv 8,248,60
mvv 12,252,60
s_waitcnt lgkmcnt(1)
v_mfma_f32_16x16x32_bf16 v[16:19], a[0:3], v[32:35], 0
R 44,35840
LsG9:
s_cbranch_execz LwG
v_mfma_f32_16x16x32_bf16 v[20:23], a[4:7], v[32:35], 0
v_cvt_pk_bf16_f32 v64, v0, v1
v_cvt_pk_bf16_f32 v65, v2, v3
v_mfma_f32_16x16x32_bf16 v[24:27], a[8:11], v[32:35], 0
v_cvt_pk_bf16_f32 v66, v4, v5
v_cvt_pk_bf16_f32 v67, v6, v7
v_mfma_f32_16x16x32_bf16 v[28:31], a[12:15], v[32:35], 0
v_cvt_pk_bf16_f32 v68, v8, v9
v_cvt_pk_bf16_f32 v69, v10, v11
mav 16,16,36
R 48,36864
v_cvt_pk_bf16_f32 v70, v12, v13
mav 20,20,36
v_cvt_pk_bf16_f32 v71, v14, v15
mav 24,24,36
buffer_store_dwordx4 v[64:67], v87, s[36:39], s49 offen sc1
mav 28,28,36
s_waitcnt lgkmcnt(1)
mav 16,32,40
R 52,37888
mav 20,36,40
mav 24,40,40
buffer_store_dwordx4 v[68:71], v87, s[36:39], s49 offen offset:1024 sc1
mav 28,44,40
mav 16,48,44
R 56,38912
mav 20,52,44
mav 24,56,44
mav 28,60,44
s_waitcnt lgkmcnt(1)
mav 16,64,48
R 60,39936
mav 20,68,48
mav 24,72,48
mav 28,76,48
mav 16,80,52
R 32,40960
mav 20,84,52
mav 24,88,52
mav 28,92,52
s_waitcnt lgkmcnt(1)
mav 16,96,56
R 36,41984
mav 20,100,56
mav 24,104,56
mav 28,108,56
mav 16,112,60
R 40,43008
mav 20,116,60
mav 24,120,60
mav 28,124,60
s_waitcnt lgkmcnt(1)
mav 16,128,32
R 44,44032
mav 20,132,32
mav 24,136,32
mav 28,140,32
mav 16,144,36
R 48,45056
v_add_u32 v72, 1, v72
mav 20,148,36
mav 24,152,36
mav 28,156,36
s_waitcnt lgkmcnt(1)
mav 16,160,40
R 52,46080
mav 20,164,40
mav 24,168,40
mav 28,172,40
mav 16,176,44
R 56,47104
mav 20,180,44
mav 24,184,44
mav 28,188,44
s_waitcnt lgkmcnt(1)
mav 16,192,48
R 60,48128
mav 20,196,48
mav 24,200,48
mav 28,204,48
mav 16,208,52
R 32,49152
mav 20,212,52
mav 24,216,52
mav 28,220,52
s_waitcnt lgkmcnt(1)
mav 16,224,56
R 36,50176
mav 20,228,56
mav 24,232,56
mav 28,236,56
mav 16,240,60
s_waitcnt vmcnt(0)
R 40,51200
mav 20,244,60
mav 24,248,60
buffer_store_dword v72, v81, s[36:39], s52 offen sc1
mav 28,252,60
s_waitcnt lgkmcnt(1)
mvv 16,128,32
R 44,52224
mvv 20,132,32
mvv 24,136,32
mvv 28,140,32
mvv 16,144,36
R 48,53248
mvv 20,148,36
mvv 24,152,36
mvv 28,156,36
s_waitcnt lgkmcnt(1)
mvv 16,160,40
R 52,54272
mvv 20,164,40
mvv 24,168,40
mvv 28,172,40
mvv 16,176,44
R 56,55296
mvv 20,180,44
mvv 24,184,44
mvv 28,188,44
s_waitcnt lgkmcnt(1)
mvv 16,192,48
R 60,56320
mvv 20,196,48
mvv 24,200,48
s_add_u32 s49, s49, 313344
mvv 28,204,48
mvv 16,208,52
mvv 20,212,52
mvv 24,216,52
mvv 28,220,52
s_waitcnt lgkmcnt(0)
mvv 16,224,56
mvv 20,228,56
mvv 24,232,56
mvv 28,236,56
mvv 16,240,60
mvv 20,244,60
mvv 24,248,60
mvv 28,252,60
s_nop 4
v_cvt_pk_bf16_f32 v64, v16, v17
v_cvt_pk_bf16_f32 v65, v18, v19
v_cvt_pk_bf16_f32 v66, v20, v21
v_cvt_pk_bf16_f32 v67, v22, v23
buffer_store_dwordx4 v[64:67], v87, s[36:39], s49 offen sc1
v_cvt_pk_bf16_f32 v68, v24, v25
v_cvt_pk_bf16_f32 v69, v26, v27
v_cvt_pk_bf16_f32 v70, v28, v29
v_cvt_pk_bf16_f32 v71, v30, v31
buffer_store_dwordx4 v[68:71], v87, s[36:39], s49 offen offset:1024 sc1
v_add_u32 v72, 35, v72
s_waitcnt vmcnt(0)
buffer_store_dword v72, v81, s[36:39], s52 offen sc1
s_endpgm
LvG3:
LsG10:
s_cbranch_execz LwG
v_add_u32 v72, 1, v72
s_add_u32 s49, s49, 313344
s_barrier
s_add_u32 m0, s50, 0
buffer_store_dword v72, v81, s[36:39], s52 offen sc1
D 83,0
D 83,1024
D 83,2048
D 83,3072
s_add_u32 m0, s50, 4096
s_nop 0
D 84,0
D 84,1024
v_add_u32 v72, 1, v72
s_waitcnt vmcnt(0)
s_add_u32 s49, s49, 313344
s_barrier
s_add_u32 m0, s50, 32768
R 32,0
buffer_store_dword v72, v81, s[36:39], s52 offen sc1
D 83,0
R 36,1024
D 83,1024
R 40,2048
D 83,2048
s_waitcnt lgkmcnt(1)
v_mfma_f32_16x16x32_bf16 v[0:3], a[0:3], v[32:35], 0
R 44,3072
v_mfma_f32_16x16x32_bf16 v[4:7], a[4:7], v[32:35], 0
v_mfma_f32_16x16x32_bf16 v[8:11], a[8:11], v[32:35], 0
v_mfma_f32_16x16x32_bf16 v[12:15], a[12:15], v[32:35], 0
D 83,3072
mav 0,16,36
s_add_u32 m0, s50, 36864
R 48,4096
mav 4,20,36
mav 8,24,36
mav 12,28,36
s_waitcnt lgkmcnt(1)
D 84,0
mav 0,32,40
R 52,5120
mav 4,36,40
mav 8,40,40
mav 12,44,40
D 84,1024
mav 0,48,44
R 56,6144
mav 4,52,44
mav 8,56,44
mav 12,60,44
s_waitcnt lgkmcnt(1)
mav 0,64,48
R 60,7168
mav 4,68,48
mav 8,72,48
mav 12,76,48
mav 0,80,52
R 32,8192
mav 4,84,52
mav 8,88,52
mav 12,92,52
s_waitcnt lgkmcnt(1)
mav 0,96,56
R 36,9216
mav 4,100,56
mav 8,104,56
mav 12,108,56
mav 0,112,60
R 40,10240
mav 4,116,60
mav 8,120,60
mav 12,124,60
s_waitcnt lgkmcnt(1)
mav 0,128,32
R 44,11264
mav 4,132,32
mav 8,136,32
mav 12,140,32
mav 0,144,36
R 48,12288
mav 4,148,36
mav 8,152,36
mav 12,156,36
s_waitcnt lgkmcnt(1)
mav 0,160,40
R 52,13312
mav 4,164,40
mav 8,168,40
mav 12,172,40
mav 0,176,44
R 56,14336
mav 4,180,44
mav 8,184,44
mav 12,188,44
s_waitcnt lgkmcnt(1)
mav 0,192,48
R 60,15360
mav 4,196,48
mav 8,200,48
mav 12,204,48
mav 0,208,52
R 32,16384
mav 4,212,52
mav 8,216,52
mav 12,220,52
s_waitcnt lgkmcnt(1)
mav 0,224,56
R 36,17408
mav 4,228,56
mav 8,232,56
mav 12,236,56
mav 0,240,60
R 40,18432
mav 4,244,60
mav 8,248,60
mav 12,252,60
s_waitcnt lgkmcnt(1)
mvv 0,128,32
R 44,19456
mvv 4,132,32
mvv 8,136,32
mvv 12,140,32
mvv 0,144,36
R 48,20480
mvv 4,148,36
mvv 8,152,36
mvv 12,156,36
s_waitcnt lgkmcnt(1)
mvv 0,160,40
R 52,21504
mvv 4,164,40
mvv 8,168,40
mvv 12,172,40
mvv 0,176,44
R 56,22528
mvv 4,180,44
mvv 8,184,44
s_xor_b32 s50, s50, 0x10000
mvv 12,188,44
s_waitcnt lgkmcnt(1)
mvv 0,192,48
R 60,23552
mvv 4,196,48
mvv 8,200,48
s_waitcnt vmcnt(0)
s_add_u32 s49, s49, 313344
mvv 12,204,48
s_barrier
mvv 0,208,52
s_add_u32 m0, s50, 0
R 32,32768
mvv 4,212,52
mvv 8,216,52
mvv 12,220,52
s_waitcnt lgkmcnt(1)
D 83,0
mvv 0,224,56
R 36,33792
mvv 4,228,56
mvv 8,232,56
mvv 12,236,56
D 83,1024
mvv 0,240,60
R 40,34816
mvv 4,244,60
mvv 8,248,60
mvv 12,252,60
s_waitcnt lgkmcnt(1)
D 83,2048
v_mfma_f32_16x16x32_bf16 v[16:19], a[0:3], v[32:35], 0
R 44,35840
v_cvt_pk_bf16_f32 v64, v0, v1
v_mfma_f32_16x16x32_bf16 v[20:23], a[4:7], v[32:35], 0
v_cvt_pk_bf16_f32 v65, v2, v3
v_cvt_pk_bf16_f32 v66, v4, v5
v_mfma_f32_16x16x32_bf16 v[24:27], a[8:11], v[32:35], 0
v_cvt_pk_bf16_f32 v67, v6, v7
v_cvt_pk_bf16_f32 v68, v8, v9
v_mfma_f32_16x16x32_bf16 v[28:31], a[12:15], v[32:35], 0
buffer_store_dwordx4 v[64:67], v87, s[36:39], s49 offen sc1
D 83,3072
mav 16,16,36
s_add_u32 m0, s50, 4096
R 48,36864
v_cvt_pk_bf16_f32 v69, v10, v11
mav 20,20,36
v_cvt_pk_bf16_f32 v70, v12, v13
v_cvt_pk_bf16_f32 v71, v14, v15
mav 24,24,36
mav 28,28,36
buffer_store_dwordx4 v[68:71], v87, s[36:39], s49 offen offset:1024 sc1
s_waitcnt lgkmcnt(1)
mav 16,32,40
R 52,37888
mav 20,36,40
mav 24,40,40
mav 28,44,40
D 84,0
mav 16,48,44
R 56,38912
mav 20,52,44
mav 24,56,44
mav 28,60,44
s_waitcnt lgkmcnt(1)
D 84,1024
mav 16,64,48
R 60,39936
mav 20,68,48
mav 24,72,48
mav 28,76,48
mav 16,80,52
R 32,40960
mav 20,84,52
mav 24,88,52
mav 28,92,52
s_waitcnt lgkmcnt(1)
mav 16,96,56
R 36,41984
mav 20,100,56
mav 24,104,56
mav 28,108,56
mav 16,112,60
R 40,43008
mav 20,116,60
mav 24,120,60
mav 28,124,60
s_waitcnt lgkmcnt(1)
mav 16,128,32
R 44,44032
mav 20,132,32
mav 24,136,32
mav 28,140,32
mav 16,144,36
R 48,45056
v_add_u32 v72, 1, v72
mav 20,148,36
mav 24,152,36
mav 28,156,36
s_waitcnt lgkmcnt(1)
mav 16,160,40
R 52,46080
mav 20,164,40
mav 24,168,40
mav 28,172,40
mav 16,176,44
R 56,47104
mav 20,180,44
mav 24,184,44
mav 28,188,44
s_waitcnt lgkmcnt(1)
mav 16,192,48
R 60,48128
mav 20,196,48
mav 24,200,48
s_mov_b32 s60, -30
.p2align 6
LG3_1:
mav 28,204,48
mav 16,208,52
R 32,49152
mav 20,212,52
mav 24,216,52
mav 28,220,52
s_waitcnt lgkmcnt(1)
mav 16,224,56
R 36,50176
mav 20,228,56
mav 24,232,56
mav 28,236,56
mav 16,240,60
s_waitcnt vmcnt(2)
R 40,51200
mav 20,244,60
mav 24,248,60
mav 28,252,60
s_waitcnt lgkmcnt(1)
buffer_store_dword v72, v81, s[36:39], s52 offen sc1
mvv 16,128,32
R 44,52224
mvv 20,132,32
mvv 24,136,32
mvv 28,140,32
mvv 16,144,36
R 48,53248
mvv 20,148,36
mvv 24,152,36
mvv 28,156,36
s_waitcnt lgkmcnt(1)
mvv 16,160,40
R 52,54272
mvv 20,164,40
mvv 24,168,40
mvv 28,172,40
mvv 16,176,44
R 56,55296
mvv 20,180,44
mvv 24,184,44
mvv 28,188,44
s_waitcnt lgkmcnt(1)
mvv 16,192,48
R 60,56320
mvv 20,196,48
v_xor_b32 v80, 0x10000, v80
mvv 24,200,48
s_waitcnt vmcnt(1)
s_add_u32 s49, s49, 313344
mvv 28,204,48
s_barrier
mvv 16,208,52
s_add_u32 m0, s50, 32768
R 32,0
mvv 20,212,52
mvv 24,216,52
mvv 28,220,52
s_waitcnt lgkmcnt(1)
D 83,0
mvv 16,224,56
R 36,1024
mvv 20,228,56
mvv 24,232,56
mvv 28,236,56
D 83,1024
mvv 16,240,60
R 40,2048
mvv 20,244,60
mvv 24,248,60
mvv 28,252,60
s_waitcnt lgkmcnt(1)
D 83,2048
v_mfma_f32_16x16x32_bf16 v[0:3], a[0:3], v[32:35], 0
R 44,3072
v_cvt_pk_bf16_f32 v64, v16, v17
v_mfma_f32_16x16x32_bf16 v[4:7], a[4:7], v[32:35], 0
v_cvt_pk_bf16_f32 v65, v18, v19
v_cvt_pk_bf16_f32 v66, v20, v21
v_mfma_f32_16x16x32_bf16 v[8:11], a[8:11], v[32:35], 0
v_cvt_pk_bf16_f32 v67, v22, v23
v_cvt_pk_bf16_f32 v68, v24, v25
v_mfma_f32_16x16x32_bf16 v[12:15], a[12:15], v[32:35], 0
buffer_store_dwordx4 v[64:67], v87, s[36:39], s49 offen sc1
D 83,3072
mav 0,16,36
s_add_u32 m0, s50, 36864
R 48,4096
v_cvt_pk_bf16_f32 v69, v26, v27
mav 4,20,36
v_cvt_pk_bf16_f32 v70, v28, v29
v_cvt_pk_bf16_f32 v71, v30, v31
mav 8,24,36
mav 12,28,36
buffer_store_dwordx4 v[68:71], v87, s[36:39], s49 offen offset:1024 sc1
s_waitcnt lgkmcnt(1)
mav 0,32,40
R 52,5120
mav 4,36,40
mav 8,40,40
mav 12,44,40
D 84,0
mav 0,48,44
R 56,6144
mav 4,52,44
mav 8,56,44
mav 12,60,44
s_waitcnt lgkmcnt(1)
D 84,1024
mav 0,64,48
R 60,7168
mav 4,68,48
mav 8,72,48
mav 12,76,48
mav 0,80,52
R 32,8192
mav 4,84,52
mav 8,88,52
mav 12,92,52
s_waitcnt lgkmcnt(1)
mav 0,96,56
R 36,9216
mav 4,100,56
mav 8,104,56
mav 12,108,56
mav 0,112,60
R 40,10240
LsG11:
s_cbranch_execz LwG
mav 4,116,60
mav 8,120,60
mav 12,124,60
s_waitcnt lgkmcnt(1)
mav 0,128,32
R 44,11264
mav 4,132,32
mav 8,136,32
mav 12,140,32
mav 0,144,36
R 48,12288
v_add_u32 v72, 1, v72
mav 4,148,36
mav 8,152,36
mav 12,156,36
s_waitcnt lgkmcnt(1)
mav 0,160,40
R 52,13312
mav 4,164,40
mav 8,168,40
mav 12,172,40
mav 0,176,44
R 56,14336
mav 4,180,44
mav 8,184,44
mav 12,188,44
s_waitcnt lgkmcnt(1)
mav 0,192,48
R 60,15360
mav 4,196,48
mav 8,200,48
mav 12,204,48
mav 0,208,52
R 32,16384
mav 4,212,52
mav 8,216,52
mav 12,220,52
s_waitcnt lgkmcnt(1)
mav 0,224,56
R 36,17408
mav 4,228,56
mav 8,232,56
mav 12,236,56
mav 0,240,60
s_waitcnt vmcnt(2)
R 40,18432
mav 4,244,60
mav 8,248,60
mav 12,252,60
s_waitcnt lgkmcnt(1)
buffer_store_dword v72, v81, s[36:39], s52 offen sc1
mvv 0,128,32
R 44,19456
mvv 4,132,32
mvv 8,136,32
mvv 12,140,32
mvv 0,144,36
R 48,20480
mvv 4,148,36
mvv 8,152,36
mvv 12,156,36
s_waitcnt lgkmcnt(1)
mvv 0,160,40
R 52,21504
mvv 4,164,40
mvv 8,168,40
mvv 12,172,40
mvv 0,176,44
R 56,22528
mvv 4,180,44
mvv 8,184,44
s_xor_b32 s50, s50, 0x10000
mvv 12,188,44
s_waitcnt lgkmcnt(1)
mvv 0,192,48
R 60,23552
mvv 4,196,48
mvv 8,200,48
s_waitcnt vmcnt(1)
s_add_u32 s49, s49, 313344
mvv 12,204,48
s_barrier
mvv 0,208,52
s_add_u32 m0, s50, 0
R 32,32768
mvv 4,212,52
mvv 8,216,52
mvv 12,220,52
s_waitcnt lgkmcnt(1)
D 83,0
mvv 0,224,56
R 36,33792
mvv 4,228,56
mvv 8,232,56
mvv 12,236,56
D 83,1024
mvv 0,240,60
R 40,34816
mvv 4,244,60
mvv 8,248,60
mvv 12,252,60
s_waitcnt lgkmcnt(1)
D 83,2048
v_mfma_f32_16x16x32_bf16 v[16:19], a[0:3], v[32:35], 0
R 44,35840
v_cvt_pk_bf16_f32 v64, v0, v1
v_mfma_f32_16x16x32_bf16 v[20:23], a[4:7], v[32:35], 0
v_cvt_pk_bf16_f32 v65, v2, v3
v_cvt_pk_bf16_f32 v66, v4, v5
v_mfma_f32_16x16x32_bf16 v[24:27], a[8:11], v[32:35], 0
v_cvt_pk_bf16_f32 v67, v6, v7
v_cvt_pk_bf16_f32 v68, v8, v9
v_mfma_f32_16x16x32_bf16 v[28:31], a[12:15], v[32:35], 0
buffer_store_dwordx4 v[64:67], v87, s[36:39], s49 offen sc1
D 83,3072
mav 16,16,36
s_add_u32 m0, s50, 4096
R 48,36864
v_cvt_pk_bf16_f32 v69, v10, v11
mav 20,20,36
v_cvt_pk_bf16_f32 v70, v12, v13
v_cvt_pk_bf16_f32 v71, v14, v15
mav 24,24,36
mav 28,28,36
buffer_store_dwordx4 v[68:71], v87, s[36:39], s49 offen offset:1024 sc1
s_waitcnt lgkmcnt(1)
mav 16,32,40
R 52,37888
mav 20,36,40
mav 24,40,40
mav 28,44,40
)"
R"(
D 84,0
mav 16,48,44
R 56,38912
mav 20,52,44
mav 24,56,44
mav 28,60,44
s_waitcnt lgkmcnt(1)
D 84,1024
mav 16,64,48
R 60,39936
mav 20,68,48
mav 24,72,48
mav 28,76,48
mav 16,80,52
R 32,40960
mav 20,84,52
mav 24,88,52
mav 28,92,52
s_waitcnt lgkmcnt(1)
mav 16,96,56
R 36,41984
mav 20,100,56
mav 24,104,56
mav 28,108,56
mav 16,112,60
R 40,43008
mav 20,116,60
mav 24,120,60
mav 28,124,60
s_waitcnt lgkmcnt(1)
mav 16,128,32
R 44,44032
mav 20,132,32
mav 24,136,32
mav 28,140,32
mav 16,144,36
R 48,45056
v_add_u32 v72, 1, v72
mav 20,148,36
mav 24,152,36
mav 28,156,36
s_waitcnt lgkmcnt(1)
mav 16,160,40
R 52,46080
mav 20,164,40
mav 24,168,40
mav 28,172,40
mav 16,176,44
R 56,47104
mav 20,180,44
mav 24,184,44
mav 28,188,44
s_waitcnt lgkmcnt(1)
mav 16,192,48
R 60,48128
mav 20,196,48
s_add_u32 s60, s60, 1
mav 24,200,48
s_cbranch_scc0 LG3_1
mav 28,204,48
mav 16,208,52
R 32,49152
mav 20,212,52
mav 24,216,52
mav 28,220,52
s_waitcnt lgkmcnt(1)
mav 16,224,56
R 36,50176
mav 20,228,56
mav 24,232,56
mav 28,236,56
mav 16,240,60
s_waitcnt vmcnt(2)
R 40,51200
mav 20,244,60
mav 24,248,60
mav 28,252,60
s_waitcnt lgkmcnt(1)
buffer_store_dword v72, v81, s[36:39], s52 offen sc1
mvv 16,128,32
R 44,52224
mvv 20,132,32
mvv 24,136,32
mvv 28,140,32
mvv 16,144,36
R 48,53248
mvv 20,148,36
mvv 24,152,36
mvv 28,156,36
s_waitcnt lgkmcnt(1)
mvv 16,160,40
R 52,54272
mvv 20,164,40
mvv 24,168,40
mvv 28,172,40
mvv 16,176,44
R 56,55296
mvv 20,180,44
mvv 24,184,44
mvv 28,188,44
s_waitcnt lgkmcnt(1)
mvv 16,192,48
R 60,56320
mvv 20,196,48
v_xor_b32 v80, 0x10000, v80
mvv 24,200,48
s_waitcnt vmcnt(1)
s_add_u32 s49, s49, 313344
mvv 28,204,48
s_barrier
mvv 16,208,52
s_add_u32 m0, s50, 32768
R 32,0
mvv 20,212,52
mvv 24,216,52
mvv 28,220,52
s_waitcnt lgkmcnt(1)
D 83,0
mvv 16,224,56
R 36,1024
mvv 20,228,56
mvv 24,232,56
mvv 28,236,56
D 83,1024
mvv 16,240,60
R 40,2048
mvv 20,244,60
mvv 24,248,60
mvv 28,252,60
s_waitcnt lgkmcnt(1)
D 83,2048
v_mfma_f32_16x16x32_bf16 v[0:3], a[0:3], v[32:35], 0
R 44,3072
v_cvt_pk_bf16_f32 v64, v16, v17
v_mfma_f32_16x16x32_bf16 v[4:7], a[4:7], v[32:35], 0
v_cvt_pk_bf16_f32 v65, v18, v19
v_cvt_pk_bf16_f32 v66, v20, v21
v_mfma_f32_16x16x32_bf16 v[8:11], a[8:11], v[32:35], 0
v_cvt_pk_bf16_f32 v67, v22, v23
v_cvt_pk_bf16_f32 v68, v24, v25
v_mfma_f32_16x16x32_bf16 v[12:15], a[12:15], v[32:35], 0
buffer_store_dwordx4 v[64:67], v87, s[36:39], s49 offen sc1
D 83,3072
mav 0,16,36
s_add_u32 m0, s50, 36864
R 48,4096
v_cvt_pk_bf16_f32 v69, v26, v27
mav 4,20,36
v_cvt_pk_bf16_f32 v70, v28, v29
v_cvt_pk_bf16_f32 v71, v30, v31
mav 8,24,36
mav 12,28,36
buffer_store_dwordx4 v[68:71], v87, s[36:39], s49 offen offset:1024 sc1
s_waitcnt lgkmcnt(1)
mav 0,32,40
R 52,5120
mav 4,36,40
mav 8,40,40
mav 12,44,40
D 84,0
mav 0,48,44
R 56,6144
mav 4,52,44
mav 8,56,44
mav 12,60,44
s_waitcnt lgkmcnt(1)
D 84,1024
mav 0,64,48
R 60,7168
mav 4,68,48
mav 8,72,48
mav 12,76,48
mav 0,80,52
R 32,8192
mav 4,84,52
mav 8,88,52
mav 12,92,52
s_waitcnt lgkmcnt(1)
mav 0,96,56
R 36,9216
mav 4,100,56
mav 8,104,56
mav 12,108,56
mav 0,112,60
R 40,10240
mav 4,116,60
mav 8,120,60
mav 12,124,60
s_waitcnt lgkmcnt(1)
mav 0,128,32
R 44,11264
mav 4,132,32
mav 8,136,32
mav 12,140,32
mav 0,144,36
R 48,12288
v_add_u32 v72, 1, v72
mav 4,148,36
mav 8,152,36
mav 12,156,36
s_waitcnt lgkmcnt(1)
mav 0,160,40
R 52,13312
mav 4,164,40
mav 8,168,40
mav 12,172,40
mav 0,176,44
R 56,14336
mav 4,180,44
mav 8,184,44
mav 12,188,44
s_waitcnt lgkmcnt(1)
mav 0,192,48
R 60,15360
mav 4,196,48
mav 8,200,48
mav 12,204,48
mav 0,208,52
R 32,16384
mav 4,212,52
mav 8,216,52
mav 12,220,52
s_waitcnt lgkmcnt(1)
mav 0,224,56
R 36,17408
mav 4,228,56
mav 8,232,56
mav 12,236,56
mav 0,240,60
s_waitcnt vmcnt(2)
R 40,18432
mav 4,244,60
mav 8,248,60
mav 12,252,60
s_waitcnt lgkmcnt(1)
buffer_store_dword v72, v81, s[36:39], s52 offen sc1
mvv 0,128,32
R 44,19456
mvv 4,132,32
mvv 8,136,32
mvv 12,140,32
mvv 0,144,36
R 48,20480
mvv 4,148,36
mvv 8,152,36
mvv 12,156,36
s_waitcnt lgkmcnt(1)
mvv 0,160,40
R 52,21504
mvv 4,164,40
mvv 8,168,40
mvv 12,172,40
mvv 0,176,44
R 56,22528
mvv 4,180,44
mvv 8,184,44
mvv 12,188,44
s_waitcnt lgkmcnt(1)
mvv 0,192,48
R 60,23552
mvv 4,196,48
mvv 8,200,48
s_waitcnt vmcnt(1)
s_add_u32 s49, s49, 313344
mvv 12,204,48
s_barrier
mvv 0,208,52
R 32,32768
mvv 4,212,52
mvv 8,216,52
mvv 12,220,52
s_waitcnt lgkmcnt(1)
mvv 0,224,56
R 36,33792
mvv 4,228,56
mvv 8,232,56
mvv 12,236,56
mvv 0,240,60
R 40,34816
mvv 4,244,60
mvv 8,248,60
mvv 12,252,60
s_waitcnt lgkmcnt(1)
v_mfma_f32_16x16x32_bf16 v[16:19], a[0:3], v[32:35], 0
R 44,35840
v_mfma_f32_16x16x32_bf16 v[20:23], a[4:7], v[32:35], 0
v_cvt_pk_bf16_f32 v64, v0, v1
v_cvt_pk_bf16_f32 v65, v2, v3
v_mfma_f32_16x16x32_bf16 v[24:27], a[8:11], v[32:35], 0
v_cvt_pk_bf16_f32 v66, v4, v5
v_cvt_pk_bf16_f32 v67, v6, v7
v_mfma_f32_16x16x32_bf16 v[28:31], a[12:15], v[32:35], 0
buffer_store_dwordx4 v[64:67], v87, s[36:39], s49 offen sc1
v_cvt_pk_bf16_f32 v68, v8, v9
v_cvt_pk_bf16_f32 v69, v10, v11
v_cvt_pk_bf16_f32 v70, v12, v13
mav 16,16,36
R 48,36864
v_cvt_pk_bf16_f32 v71, v14, v15
mav 20,20,36
mav 24,24,36
mav 28,28,36
buffer_store_dwordx4 v[68:71], v87, s[36:39], s49 offen offset:1024 sc1
s_waitcnt lgkmcnt(1)
mav 16,32,40
R 52,37888
mav 20,36,40
mav 24,40,40
mav 28,44,40
mav 16,48,44
R 56,38912
mav 20,52,44
mav 24,56,44
mav 28,60,44
s_waitcnt lgkmcnt(1)
mav 16,64,48
R 60,39936
mav 20,68,48
mav 24,72,48
mav 28,76,48
mav 16,80,52
R 32,40960
mav 20,84,52
mav 24,88,52
mav 28,92,52
s_waitcnt lgkmcnt(1)
mav 16,96,56
R 36,41984
mav 20,100,56
mav 24,104,56
mav 28,108,56
mav 16,112,60
R 40,43008
mav 20,116,60
mav 24,120,60
mav 28,124,60
s_waitcnt lgkmcnt(1)
mav 16,128,32
R 44,44032
mav 20,132,32
mav 24,136,32
mav 28,140,32
mav 16,144,36
R 48,45056
v_add_u32 v72, 1, v72
mav 20,148,36
mav 24,152,36
mav 28,156,36
s_waitcnt lgkmcnt(1)
mav 16,160,40
R 52,46080
mav 20,164,40
mav 24,168,40
mav 28,172,40
mav 16,176,44
R 56,47104
mav 20,180,44
mav 24,184,44
mav 28,188,44
s_waitcnt lgkmcnt(1)
mav 16,192,48
R 60,48128
mav 20,196,48
mav 24,200,48
mav 28,204,48
mav 16,208,52
R 32,49152
mav 20,212,52
mav 24,216,52
mav 28,220,52
s_waitcnt lgkmcnt(1)
mav 16,224,56
R 36,50176
mav 20,228,56
mav 24,232,56
mav 28,236,56
mav 16,240,60
s_waitcnt vmcnt(0)
R 40,51200
mav 20,244,60
mav 24,248,60
mav 28,252,60
s_waitcnt lgkmcnt(1)
buffer_store_dword v72, v81, s[36:39], s52 offen sc1
mvv 16,128,32
R 44,52224
mvv 20,132,32
mvv 24,136,32
mvv 28,140,32
mvv 16,144,36
R 48,53248
mvv 20,148,36
mvv 24,152,36
mvv 28,156,36
s_waitcnt lgkmcnt(1)
mvv 16,160,40
R 52,54272
mvv 20,164,40
mvv 24,168,40
mvv 28,172,40
mvv 16,176,44
R 56,55296
mvv 20,180,44
mvv 24,184,44
mvv 28,188,44
s_waitcnt lgkmcnt(1)
mvv 16,192,48
R 60,56320
mvv 20,196,48
mvv 24,200,48
s_add_u32 s49, s49, 313344
LsG12:
s_cbranch_execz LwG
mvv 28,204,48
mvv 16,208,52
mvv 20,212,52
mvv 24,216,52
mvv 28,220,52
s_waitcnt lgkmcnt(0)
mvv 16,224,56
mvv 20,228,56
mvv 24,232,56
mvv 28,236,56
mvv 16,240,60
mvv 20,244,60
mvv 24,248,60
mvv 28,252,60
s_nop 4
v_cvt_pk_bf16_f32 v64, v16, v17
v_cvt_pk_bf16_f32 v65, v18, v19
v_cvt_pk_bf16_f32 v66, v20, v21
v_cvt_pk_bf16_f32 v67, v22, v23
buffer_store_dwordx4 v[64:67], v87, s[36:39], s49 offen sc1
v_cvt_pk_bf16_f32 v68, v24, v25
v_cvt_pk_bf16_f32 v69, v26, v27
v_cvt_pk_bf16_f32 v70, v28, v29
v_cvt_pk_bf16_f32 v71, v30, v31
buffer_store_dwordx4 v[68:71], v87, s[36:39], s49 offen offset:1024 sc1
v_add_u32 v72, 35, v72
s_waitcnt vmcnt(0)
buffer_store_dword v72, v81, s[36:39], s52 offen sc1
s_endpgm
Lr6:
s_waitcnt vmcnt(0)
v_accvgpr_read_b32 v73, a255
v_mov_b32 v72, 0x100
v_add_u32 v73, 0x9e3779b9, v73
buffer_store_dwordx2 v[72:73], v81, s[36:39], s52 offen sc1
v_add_u32 v85, v85, v72
s_sleep 32
s_sleep 32
s_sleep 32
s_sleep 32
E 0,65536
E 1,69632
E 2,73728
E 3,77824
s_add_u32 s51, s51, 65536
E 0,81920
E 1,86016
E 2,90112
E 3,94208
s_add_u32 s51, s51, 65536
E 0,98304
E 1,102400
E 2,106496
E 3,110592
s_add_u32 s51, s51, 65536
E 0,114688
E 1,118784
E 2,122880
E 3,126976
s_add_u32 s51, s51, 65536
s_cmp_ge_u32 s51, s57
s_cselect_b32 s60, 1048576, 0
s_sub_u32 s51, s51, s60
s_mov_b64 exec, 0
s_cmp_eq_u32 s53, 0
s_cbranch_scc1 LsR0
s_cmp_eq_u32 s53, 1024
s_cbranch_scc1 LsR1
s_cmp_eq_u32 s53, 2048
s_cbranch_scc1 LsR2
s_branch LsR3
LwR:
s_mov_b64 exec, -1
s_waitcnt vmcnt(8)
s_barrier
v_add_u32 v4, 65536, v4
v_add_u32 v5, 65536, v5
v_add_u32 v6, 65536, v6
v_add_u32 v7, 65536, v7
Ta 0,4,0
Ta 4,5,0
Ta 8,6,0
Ta 12,7,0
Ta 16,4,16384
Ta 20,5,16384
Ta 24,6,16384
Ta 28,7,16384
E 0,131072
E 1,135168
E 2,139264
E 3,143360
s_add_u32 s51, s51, 65536
E 0,147456
E 1,151552
E 2,155648
E 3,159744
s_add_u32 s51, s51, 65536
s_waitcnt lgkmcnt(0)
s_waitcnt vmcnt(8)
s_barrier
E 0,65536
Ta 32,4,32768
Ta 36,5,32768
E 1,69632
Ta 40,6,32768
Ta 44,7,32768
E 2,73728
Ta 48,4,49152
E 3,77824
s_add_u32 s51, s51, 65536
Ta 52,5,49152
Ta 56,6,49152
E 0,81920
Ta 60,7,49152
E 1,86016
E 2,90112
E 3,94208
s_add_u32 s51, s51, 65536
s_cmp_ge_u32 s51, s57
s_cselect_b32 s60, 1048576, 0
s_sub_u32 s51, s51, s60
s_waitcnt lgkmcnt(0)
s_waitcnt vmcnt(8)
s_barrier
v_add_u32 v4, 65536, v4
v_add_u32 v5, 65536, v5
v_add_u32 v6, 65536, v6
v_add_u32 v7, 65536, v7
E 0,98304
Ta 64,4,0
Ta 68,5,0
E 1,102400
Ta 72,6,0
Ta 76,7,0
E 2,106496
Ta 80,4,16384
E 3,110592
s_add_u32 s51, s51, 65536
Ta 84,5,16384
Ta 88,6,16384
E 0,114688
Ta 92,7,16384
E 1,118784
E 2,122880
E 3,126976
s_add_u32 s51, s51, 65536
s_waitcnt lgkmcnt(0)
s_waitcnt vmcnt(8)
s_barrier
v_add_u32 v4, -65536, v4
v_add_u32 v5, -65536, v5
v_add_u32 v6, -65536, v6
v_add_u32 v7, -65536, v7
E 0,131072
Ta 96,4,0
Ta 100,5,0
E 1,135168
Ta 104,6,0
Ta 108,7,0
E 2,139264
Ta 112,4,16384
E 3,143360
s_add_u32 s51, s51, 65536
Ta 116,5,16384
Ta 120,6,16384
E 0,147456
Ta 124,7,16384
E 1,151552
E 2,155648
E 3,159744
s_add_u32 s51, s51, 65536
s_cmp_ge_u32 s51, s57
s_cselect_b32 s60, 1048576, 0
s_sub_u32 s51, s51, s60
s_waitcnt lgkmcnt(0)
s_waitcnt vmcnt(8)
s_barrier
E 0,65536
Ta 128,4,32768
Ta 132,5,32768
E 1,69632
Ta 136,6,32768
Ta 140,7,32768
E 2,73728
Ta 144,4,49152
E 3,77824
s_add_u32 s51, s51, 65536
Ta 148,5,49152
Ta 152,6,49152
E 0,81920
Ta 156,7,49152
E 1,86016
E 2,90112
E 3,94208
s_add_u32 s51, s51, 65536
s_waitcnt lgkmcnt(0)
s_waitcnt vmcnt(8)
s_barrier
v_add_u32 v4, 65536, v4
v_add_u32 v5, 65536, v5
v_add_u32 v6, 65536, v6
v_add_u32 v7, 65536, v7
E 0,98304
Ta 160,4,0
Ta 164,5,0
E 1,102400
Ta 168,6,0
Ta 172,7,0
E 2,106496
Ta 176,4,16384
E 3,110592
s_add_u32 s51, s51, 65536
Ta 180,5,16384
Ta 184,6,16384
E 0,114688
Ta 188,7,16384
E 1,118784
E 2,122880
E 3,126976
s_add_u32 s51, s51, 65536
s_cmp_ge_u32 s51, s57
s_cselect_b32 s60, 1048576, 0
s_sub_u32 s51, s51, s60
s_waitcnt lgkmcnt(0)
s_waitcnt vmcnt(8)
s_barrier
v_add_u32 v4, -65536, v4
v_add_u32 v5, -65536, v5
v_add_u32 v6, -65536, v6
v_add_u32 v7, -65536, v7
Ta 192,4,0
Ta 196,5,0
Ta 200,6,0
Ta 204,7,0
Ta 208,4,16384
Ta 212,5,16384
Ta 216,6,16384
Ta 220,7,16384
s_waitcnt lgkmcnt(0)
s_waitcnt vmcnt(0)
s_barrier
Ta 224,4,32768
Ta 228,5,32768
Ta 232,6,32768
Ta 236,7,32768
Ta 240,4,49152
Ta 244,5,49152
Ta 248,6,49152
Ta 252,7,49152
s_waitcnt lgkmcnt(0)
s_waitcnt vmcnt(0)
s_barrier
s_nop 7
s_cmp_eq_u32 s52, 36760
s_cbranch_scc1 Ct
Bt:
s_and_b32 s60, s48, 3
s_cmp_eq_u32 s60, 1
s_cbranch_scc1 LvR1
s_cmp_eq_u32 s60, 2
s_cbranch_scc1 LvR2
s_cmp_eq_u32 s60, 3
s_cbranch_scc1 LvR3
LvR0:
LsR0:
s_cbranch_execz LsR4
buffer_load_dwordx2 v[76:77], v84, s[36:39], 0 offen sc1
s_waitcnt vmcnt(0)
ds_write_b32 v82, v76
v_sub_u32 v76, v76, v85
v_xor_b32 v77, v77, v73
v_cmp_lt_u64 vcc, s[62:63], v[76:77]
s_waitcnt lgkmcnt(0)
s_cbranch_vccnz CR0_1
BR0_1:
s_add_u32 s49, s49, 313344
s_barrier
s_mov_b32 m0, 144384
s_nop 0
buffer_load_dword v84, s[36:39], 0 offen sc1 lds
s_add_u32 m0, s50, 0
s_nop 0
D 83,0
D 83,1024
v_add_u32 v85, 1, v85
D 83,2048
D 83,3072
ds_read_b32 v76, v82 offset:0
s_waitcnt lgkmcnt(0)
v_cmp_gt_i32 vcc, v85, v76
s_add_u32 s49, s49, 313344
s_cbranch_vccnz CR0_2
BR0_2:
s_barrier
s_mov_b32 m0, 144384
s_nop 0
buffer_load_dword v84, s[36:39], 0 offen sc1 lds
s_add_u32 m0, s50, 32768
s_nop 0
D 83,0
D 83,1024
v_add_u32 v85, 1, v85
D 83,2048
D 83,3072
buffer_load_dwordx4 v[124:127], v90, s[36:39], s49 offen sc1
ds_read_b32 v76, v82 offset:0
buffer_load_dwordx4 v[128:131], v90, s[36:39], s49 offen offset:1024 sc1
s_xor_b32 s50, s50, 0x10000
s_waitcnt lgkmcnt(0)
v_cmp_gt_i32 vcc, v85, v76
s_waitcnt vmcnt(7)
s_add_u32 s49, s49, 313344
s_cbranch_vccnz CR0_3
BR0_3:
s_barrier
R 32,0
buffer_load_dwordx4 v[132:135], v86, s[36:39], s49 offen sc1
R 36,1024
buffer_load_dwordx4 v[136:139], v86, s[36:39], s49 offen offset:1024 sc1
R 40,2048
buffer_load_dwordx4 v[140:143], v87, s[36:39], s49 offen sc1
s_waitcnt lgkmcnt(1)
v_mfma_f32_16x16x32_bf16 v[0:3], a[0:3], v[32:35], v[100:103]
R 44,3072
v_mfma_f32_16x16x32_bf16 v[4:7], a[4:7], v[32:35], v[104:107]
buffer_load_dwordx4 v[144:147], v87, s[36:39], s49 offen offset:1024 sc1
v_mfma_f32_16x16x32_bf16 v[8:11], a[8:11], v[32:35], v[108:111]
v_mfma_f32_16x16x32_bf16 v[12:15], a[12:15], v[32:35], v[112:115]
mav 0,16,36
R 48,4096
mav 4,20,36
buffer_load_dwordx4 v[148:151], v88, s[36:39], s49 offen sc1
mav 8,24,36
mav 12,28,36
s_waitcnt lgkmcnt(1)
mav 0,32,40
R 52,5120
mav 4,36,40
buffer_load_dwordx4 v[152:155], v88, s[36:39], s49 offen offset:1024 sc1
mav 8,40,40
mav 12,44,40
mav 0,48,44
R 56,6144
mav 4,52,44
buffer_load_dwordx4 v[156:159], v89, s[36:39], s49 offen sc1
mav 8,56,44
mav 12,60,44
s_waitcnt lgkmcnt(1)
mav 0,64,48
R 60,7168
mav 4,68,48
buffer_load_dwordx4 v[160:163], v89, s[36:39], s49 offen offset:1024 sc1
mav 8,72,48
mav 12,76,48
s_mov_b32 m0, 144384
mav 0,80,52
R 32,8192
mav 4,84,52
mav 8,88,52
buffer_load_dword v84, s[36:39], 0 offen sc1 lds
mav 12,92,52
s_waitcnt lgkmcnt(1)
s_add_u32 m0, s50, 0
mav 0,96,56
R 36,9216
mav 4,100,56
D 83,0
mav 8,104,56
mav 12,108,56
mav 0,112,60
R 40,10240
mav 4,116,60
D 83,1024
mav 8,120,60
mav 12,124,60
s_waitcnt lgkmcnt(1)
mav 0,128,32
R 44,11264
mav 4,132,32
mav 8,136,32
mav 12,140,32
mav 0,144,36
R 48,12288
mav 4,148,36
mav 8,152,36
mav 12,156,36
s_waitcnt lgkmcnt(1)
mav 0,160,40
R 52,13312
v_add_u32 v85, 1, v85
mav 4,164,40
mav 8,168,40
mav 12,172,40
mav 0,176,44
R 56,14336
mav 4,180,44
D 83,2048
mav 8,184,44
mav 12,188,44
s_waitcnt lgkmcnt(1)
mav 0,192,48
R 60,15360
mav 4,196,48
D 83,3072
mav 8,200,48
mav 12,204,48
LsR1:
s_cbranch_execz LsR5
mav 0,208,52
mav 4,212,52
mav 8,216,52
mav 12,220,52
s_waitcnt lgkmcnt(0)
mav 0,224,56
mav 4,228,56
mav 8,232,56
mav 12,236,56
mav 0,240,60
mav 4,244,60
buffer_load_dwordx4 v[164:167], v90, s[36:39], s49 offen sc1
mav 8,248,60
ds_read_b32 v76, v82 offset:0
mav 12,252,60
s_waitcnt vmcnt(15)
mvv 0,116,124
mvv 4,120,124
s_waitcnt vmcnt(14)
buffer_load_dwordx4 v[168:171], v90, s[36:39], s49 offen offset:1024 sc1
mvv 8,116,128
mvv 12,120,128
s_waitcnt vmcnt(14)
mvv 0,116,132
mvv 4,120,132
s_waitcnt vmcnt(13) lgkmcnt(0)
v_cmp_gt_i32 vcc, v85, v76
mvv 8,116,136
s_waitcnt vmcnt(12)
s_add_u32 s49, s49, 313344
mvv 12,120,136
s_cbranch_vccnz CR0_4
BR0_4:
s_barrier
mvv 0,116,140
R 32,32768
mvv 4,120,140
s_waitcnt vmcnt(11)
buffer_load_dwordx4 v[172:175], v86, s[36:39], s49 offen sc1
mvv 8,116,144
mvv 12,120,144
s_waitcnt vmcnt(11)
mvv 0,116,148
R 36,33792
mvv 4,120,148
s_waitcnt vmcnt(10)
buffer_load_dwordx4 v[176:179], v86, s[36:39], s49 offen offset:1024 sc1
mvv 8,116,152
mvv 12,120,152
s_waitcnt vmcnt(10)
mvv 0,116,156
R 40,34816
mvv 4,120,156
s_waitcnt vmcnt(9)
buffer_load_dwordx4 v[180:183], v87, s[36:39], s49 offen sc1
mvv 8,116,160
mvv 12,120,160
s_waitcnt lgkmcnt(1)
v_mfma_f32_16x16x32_bf16 v[16:19], a[0:3], v[32:35], v[100:103]
R 44,35840
v_cvt_pk_bf16_f32 v64, v0, v1
v_mfma_f32_16x16x32_bf16 v[20:23], a[4:7], v[32:35], v[104:107]
buffer_load_dwordx4 v[184:187], v87, s[36:39], s49 offen offset:1024 sc1
v_mfma_f32_16x16x32_bf16 v[24:27], a[8:11], v[32:35], v[108:111]
v_cvt_pk_bf16_f32 v65, v2, v3
v_cvt_pk_bf16_f32 v66, v4, v5
v_mfma_f32_16x16x32_bf16 v[28:31], a[12:15], v[32:35], v[112:115]
v_cvt_pk_bf16_f32 v67, v6, v7
v_cvt_pk_bf16_f32 v68, v8, v9
mav 16,16,36
R 48,36864
v_cvt_pk_bf16_f32 v69, v10, v11
mav 20,20,36
buffer_load_dwordx4 v[188:191], v88, s[36:39], s49 offen sc1
mav 24,24,36
v_cvt_pk_bf16_f32 v70, v12, v13
v_cvt_pk_bf16_f32 v71, v14, v15
mav 28,28,36
s_waitcnt lgkmcnt(1)
v_mul_f32 v75, v0, v0
v_add_f32 v74, v1, v0
mav 16,32,40
R 52,37888
v_fmac_f32 v75, v1, v1
mav 20,36,40
buffer_load_dwordx4 v[192:195], v88, s[36:39], s49 offen offset:1024 sc1
mav 24,40,40
v_add_f32 v74, v2, v74
v_fmac_f32 v75, v2, v2
mav 28,44,40
v_add_f32 v74, v3, v74
v_fmac_f32 v75, v3, v3
mav 16,48,44
R 56,38912
v_add_f32 v74, v4, v74
mav 20,52,44
buffer_load_dwordx4 v[196:199], v89, s[36:39], s49 offen sc1
mav 24,56,44
v_fmac_f32 v75, v4, v4
v_add_f32 v74, v5, v74
mav 28,60,44
s_waitcnt lgkmcnt(1)
v_fmac_f32 v75, v5, v5
v_add_f32 v74, v6, v74
mav 16,64,48
R 60,39936
v_fmac_f32 v75, v6, v6
mav 20,68,48
buffer_load_dwordx4 v[200:203], v89, s[36:39], s49 offen offset:1024 sc1
mav 24,72,48
v_add_f32 v74, v7, v74
v_fmac_f32 v75, v7, v7
mav 28,76,48
ds_write_b128 v94, v[64:67] offset:0
mav 16,80,52
s_mov_b32 m0, 144384
R 32,40960
v_add_f32 v74, v8, v74
mav 20,84,52
ds_write_b128 v95, v[68:71] offset:0
mav 24,88,52
buffer_load_dword v84, s[36:39], 0 offen sc1 lds
mav 28,92,52
s_waitcnt lgkmcnt(3)
ds_read_b128 v[64:67], v96 offset:0
s_add_u32 m0, s50, 32768
mav 16,96,56
R 36,41984
v_fmac_f32 v75, v8, v8
mav 20,100,56
D 83,0
mav 24,104,56
ds_read_b128 v[68:71], v96 offset:1024
v_add_f32 v74, v9, v74
mav 28,108,56
v_fmac_f32 v75, v9, v9
v_add_f32 v74, v10, v74
mav 16,112,60
R 40,43008
v_fmac_f32 v75, v10, v10
mav 20,116,60
D 83,1024
mav 24,120,60
v_add_f32 v74, v11, v74
v_fmac_f32 v75, v11, v11
mav 28,124,60
s_waitcnt lgkmcnt(2)
v_add_f32 v74, v12, v74
v_fmac_f32 v75, v12, v12
mav 16,128,32
R 44,44032
v_add_f32 v74, v13, v74
mav 20,132,32
v_fmac_f32 v75, v13, v13
v_add_f32 v74, v14, v74
mav 24,136,32
v_fmac_f32 v75, v14, v14
v_add_f32 v74, v15, v74
mav 28,140,32
v_fmac_f32 v75, v15, v15
mav 16,144,36
R 48,45056
mav 20,148,36
v_permlane32_swap_b32 v74, v75
mav 24,152,36
v_add_f32 v74, v74, v75
mav 28,156,36
s_waitcnt lgkmcnt(1)
mav 16,160,40
mav 20,164,40
R 52,46080
v_add_u32 v85, 1, v85
mav 24,168,40
mav 28,172,40
mav 16,176,44
R 56,47104
mav 20,180,44
D 83,2048
mav 24,184,44
mav 28,188,44
s_waitcnt lgkmcnt(1)
mav 16,192,48
R 60,48128
mav 20,196,48
D 83,3072
mav 24,200,48
mav 28,204,48
mav 16,208,52
mav 20,212,52
buffer_store_dwordx4 v[64:67], v91, s[36:39], s54 offen sc1
mav 24,216,52
mav 28,220,52
s_waitcnt lgkmcnt(0)
mav 16,224,56
mav 20,228,56
buffer_store_dwordx4 v[68:71], v92, s[36:39], s54 offen sc1
mav 24,232,56
mav 28,236,56
mav 16,240,60
mav 20,244,60
buffer_load_dwordx4 v[124:127], v90, s[36:39], s49 offen sc1
mav 24,248,60
ds_read_b32 v76, v82 offset:0
mav 28,252,60
s_waitcnt vmcnt(17)
mvv 16,116,164
mvv 20,120,164
s_waitcnt vmcnt(16)
buffer_load_dwordx4 v[128:131], v90, s[36:39], s49 offen offset:1024 sc1
mvv 24,116,168
s_xor_b32 s50, s50, 0x10000
s_sub_u32 s54, s54, 32768
mvv 28,120,168
s_waitcnt vmcnt(16)
mvv 16,116,172
mvv 20,120,172
s_waitcnt vmcnt(15) lgkmcnt(0)
v_cmp_gt_i32 vcc, v85, v76
v_xor_b32 v80, 0x10000, v80
mvv 24,116,176
s_waitcnt vmcnt(14)
s_add_u32 s49, s49, 313344
mvv 28,120,176
s_cbranch_vccnz CR0_5
BR0_5:
s_barrier
mvv 16,116,180
R 32,0
mvv 20,120,180
s_waitcnt vmcnt(13)
buffer_load_dwordx4 v[132:135], v86, s[36:39], s49 offen sc1
mvv 24,116,184
mvv 28,120,184
s_waitcnt vmcnt(13)
mvv 16,116,188
R 36,1024
mvv 20,120,188
s_waitcnt vmcnt(12)
buffer_load_dwordx4 v[136:139], v86, s[36:39], s49 offen offset:1024 sc1
mvv 24,116,192
mvv 28,120,192
s_waitcnt vmcnt(12)
mvv 16,116,196
R 40,2048
mvv 20,120,196
s_waitcnt vmcnt(11)
buffer_load_dwordx4 v[140:143], v87, s[36:39], s49 offen sc1
mvv 24,116,200
mvv 28,120,200
s_waitcnt lgkmcnt(1)
v_mfma_f32_16x16x32_bf16 v[0:3], a[0:3], v[32:35], v[100:103]
R 44,3072
v_cvt_pk_bf16_f32 v64, v16, v17
v_mfma_f32_16x16x32_bf16 v[4:7], a[4:7], v[32:35], v[104:107]
buffer_load_dwordx4 v[144:147], v87, s[36:39], s49 offen offset:1024 sc1
v_mfma_f32_16x16x32_bf16 v[8:11], a[8:11], v[32:35], v[108:111]
v_cvt_pk_bf16_f32 v65, v18, v19
v_cvt_pk_bf16_f32 v66, v20, v21
v_mfma_f32_16x16x32_bf16 v[12:15], a[12:15], v[32:35], v[112:115]
v_cvt_pk_bf16_f32 v67, v22, v23
v_cvt_pk_bf16_f32 v68, v24, v25
mav 0,16,36
R 48,4096
v_cvt_pk_bf16_f32 v69, v26, v27
mav 4,20,36
buffer_load_dwordx4 v[148:151], v88, s[36:39], s49 offen sc1
mav 8,24,36
v_cvt_pk_bf16_f32 v70, v28, v29
v_cvt_pk_bf16_f32 v71, v30, v31
mav 12,28,36
s_waitcnt lgkmcnt(1)
v_mul_f32 v75, v16, v16
v_fmac_f32 v75, v17, v17
mav 0,32,40
R 52,5120
v_fmac_f32 v75, v18, v18
mav 4,36,40
buffer_load_dwordx4 v[152:155], v88, s[36:39], s49 offen offset:1024 sc1
mav 8,40,40
v_fmac_f32 v75, v19, v19
v_fmac_f32 v75, v20, v20
mav 12,44,40
v_fmac_f32 v75, v21, v21
v_fmac_f32 v75, v22, v22
mav 0,48,44
R 56,6144
v_fmac_f32 v75, v23, v23
mav 4,52,44
buffer_load_dwordx4 v[156:159], v89, s[36:39], s49 offen sc1
mav 8,56,44
v_fmac_f32 v75, v24, v24
v_fmac_f32 v75, v25, v25
mav 12,60,44
s_waitcnt lgkmcnt(1)
v_fmac_f32 v75, v26, v26
v_fmac_f32 v75, v27, v27
mav 0,64,48
R 60,7168
v_fmac_f32 v75, v28, v28
mav 4,68,48
buffer_load_dwordx4 v[160:163], v89, s[36:39], s49 offen offset:1024 sc1
mav 8,72,48
ds_write_b32 v97, v74 offset:0
mav 12,76,48
ds_write_b128 v94, v[64:67] offset:0
mav 0,80,52
s_mov_b32 m0, 144384
R 32,8192
v_add_f32 v74, v17, v16
mav 4,84,52
ds_write_b128 v95, v[68:71] offset:0
mav 8,88,52
buffer_load_dword v84, s[36:39], 0 offen sc1 lds
mav 12,92,52
s_waitcnt lgkmcnt(4)
ds_read_b128 v[64:67], v96 offset:0
s_add_u32 m0, s50, 0
mav 0,96,56
R 36,9216
v_add_f32 v74, v18, v74
mav 4,100,56
D 83,0
mav 8,104,56
ds_read_b128 v[68:71], v96 offset:1024
v_add_f32 v74, v19, v74
mav 12,108,56
v_add_f32 v74, v20, v74
v_add_f32 v74, v21, v74
mav 0,112,60
R 40,10240
v_add_f32 v74, v22, v74
mav 4,116,60
D 83,1024
mav 8,120,60
v_add_f32 v74, v23, v74
v_add_f32 v74, v24, v74
mav 12,124,60
s_waitcnt lgkmcnt(2)
v_add_f32 v74, v25, v74
v_add_f32 v74, v26, v74
mav 0,128,32
R 44,11264
v_add_f32 v74, v27, v74
mav 4,132,32
v_add_f32 v74, v28, v74
v_add_f32 v74, v29, v74
mav 8,136,32
v_fmac_f32 v75, v29, v29
v_add_f32 v74, v30, v74
mav 12,140,32
v_fmac_f32 v75, v30, v30
v_add_f32 v74, v31, v74
mav 0,144,36
R 48,12288
v_fmac_f32 v75, v31, v31
mav 4,148,36
mav 8,152,36
v_permlane32_swap_b32 v74, v75
mav 12,156,36
s_waitcnt lgkmcnt(1)
v_add_f32 v74, v74, v75
mav 0,160,40
R 52,13312
v_add_u32 v85, 1, v85
mav 4,164,40
v_add_u32 v72, 1, v72
mav 8,168,40
mav 12,172,40
mav 0,176,44
R 56,14336
mav 4,180,44
D 83,2048
mav 8,184,44
mav 12,188,44
s_waitcnt lgkmcnt(1)
mav 0,192,48
R 60,15360
mav 4,196,48
D 83,3072
mav 8,200,48
mav 12,204,48
mav 0,208,52
mav 4,212,52
buffer_store_dwordx4 v[64:67], v91, s[36:39], s54 offen sc1
mav 8,216,52
mav 12,220,52
s_waitcnt lgkmcnt(0)
mav 0,224,56
mav 4,228,56
buffer_store_dwordx4 v[68:71], v92, s[36:39], s54 offen sc1
mav 8,232,56
mav 12,236,56
mav 0,240,60
mav 4,244,60
buffer_load_dwordx4 v[164:167], v90, s[36:39], s49 offen sc1
mav 8,248,60
ds_read_b32 v76, v82 offset:0
mav 12,252,60
s_waitcnt vmcnt(17)
mvv 0,116,124
buffer_store_dword v72, v81, s[36:39], s52 offen sc1
mvv 4,120,124
s_waitcnt vmcnt(17)
buffer_load_dwordx4 v[168:171], v90, s[36:39], s49 offen offset:1024 sc1
mvv 8,116,128
s_sub_u32 s54, s54, 32768
mvv 12,120,128
s_waitcnt vmcnt(17)
mvv 0,116,132
mvv 4,120,132
s_waitcnt vmcnt(16) lgkmcnt(0)
v_cmp_gt_i32 vcc, v85, v76
mvv 8,116,136
s_waitcnt vmcnt(15)
s_add_u32 s49, s49, 313344
mvv 12,120,136
s_cbranch_vccnz CR0_6
BR0_6:
s_barrier
mvv 0,116,140
R 32,32768
mvv 4,120,140
s_waitcnt vmcnt(14)
buffer_load_dwordx4 v[172:175], v86, s[36:39], s49 offen sc1
mvv 8,116,144
mvv 12,120,144
s_waitcnt vmcnt(14)
mvv 0,116,148
R 36,33792
mvv 4,120,148
s_waitcnt vmcnt(13)
buffer_load_dwordx4 v[176:179], v86, s[36:39], s49 offen offset:1024 sc1
mvv 8,116,152
mvv 12,120,152
s_waitcnt vmcnt(13)
mvv 0,116,156
R 40,34816
mvv 4,120,156
s_waitcnt vmcnt(12)
buffer_load_dwordx4 v[180:183], v87, s[36:39], s49 offen sc1
mvv 8,116,160
mvv 12,120,160
s_waitcnt lgkmcnt(1)
v_mfma_f32_16x16x32_bf16 v[16:19], a[0:3], v[32:35], v[100:103]
R 44,35840
v_cvt_pk_bf16_f32 v64, v0, v1
v_mfma_f32_16x16x32_bf16 v[20:23], a[4:7], v[32:35], v[104:107]
buffer_load_dwordx4 v[184:187], v87, s[36:39], s49 offen offset:1024 sc1
v_mfma_f32_16x16x32_bf16 v[24:27], a[8:11], v[32:35], v[108:111]
v_cvt_pk_bf16_f32 v65, v2, v3
v_cvt_pk_bf16_f32 v66, v4, v5
v_mfma_f32_16x16x32_bf16 v[28:31], a[12:15], v[32:35], v[112:115]
v_cvt_pk_bf16_f32 v67, v6, v7
v_cvt_pk_bf16_f32 v68, v8, v9
mav 16,16,36
R 48,36864
v_cvt_pk_bf16_f32 v69, v10, v11
mav 20,20,36
buffer_load_dwordx4 v[188:191], v88, s[36:39], s49 offen sc1
mav 24,24,36
v_cvt_pk_bf16_f32 v70, v12, v13
v_cvt_pk_bf16_f32 v71, v14, v15
mav 28,28,36
s_waitcnt lgkmcnt(1)
v_mul_f32 v75, v0, v0
v_fmac_f32 v75, v1, v1
mav 16,32,40
R 52,37888
v_fmac_f32 v75, v2, v2
mav 20,36,40
buffer_load_dwordx4 v[192:195], v88, s[36:39], s49 offen offset:1024 sc1
mav 24,40,40
v_fmac_f32 v75, v3, v3
v_fmac_f32 v75, v4, v4
mav 28,44,40
v_fmac_f32 v75, v5, v5
v_fmac_f32 v75, v6, v6
mav 16,48,44
R 56,38912
v_fmac_f32 v75, v7, v7
mav 20,52,44
buffer_load_dwordx4 v[196:199], v89, s[36:39], s49 offen sc1
mav 24,56,44
v_fmac_f32 v75, v8, v8
v_fmac_f32 v75, v9, v9
mav 28,60,44
s_waitcnt lgkmcnt(1)
v_fmac_f32 v75, v10, v10
v_fmac_f32 v75, v11, v11
mav 16,64,48
R 60,39936
v_fmac_f32 v75, v12, v12
mav 20,68,48
buffer_load_dwordx4 v[200:203], v89, s[36:39], s49 offen offset:1024 sc1
mav 24,72,48
ds_write_b32 v97, v74 offset:1024
mav 28,76,48
ds_write_b128 v94, v[64:67] offset:0
mav 16,80,52
s_mov_b32 m0, 144384
R 32,40960
v_add_f32 v74, v1, v0
mav 20,84,52
ds_write_b128 v95, v[68:71] offset:0
mav 24,88,52
buffer_load_dword v84, s[36:39], 0 offen sc1 lds
mav 28,92,52
s_waitcnt lgkmcnt(4)
ds_read_b128 v[64:67], v96 offset:0
s_add_u32 m0, s50, 32768
mav 16,96,56
R 36,41984
v_add_f32 v74, v2, v74
mav 20,100,56
D 83,0
mav 24,104,56
ds_read_b128 v[68:71], v96 offset:1024
v_add_f32 v74, v3, v74
mav 28,108,56
v_add_f32 v74, v4, v74
v_add_f32 v74, v5, v74
mav 16,112,60
R 40,43008
v_add_f32 v74, v6, v74
mav 20,116,60
D 83,1024
mav 24,120,60
v_add_f32 v74, v7, v74
v_add_f32 v74, v8, v74
mav 28,124,60
s_waitcnt lgkmcnt(2)
v_add_f32 v74, v9, v74
v_add_f32 v74, v10, v74
mav 16,128,32
R 44,44032
v_add_f32 v74, v11, v74
mav 20,132,32
v_add_f32 v74, v12, v74
v_add_f32 v74, v13, v74
mav 24,136,32
v_fmac_f32 v75, v13, v13
v_add_f32 v74, v14, v74
mav 28,140,32
v_fmac_f32 v75, v14, v14
v_add_f32 v74, v15, v74
mav 16,144,36
R 48,45056
v_fmac_f32 v75, v15, v15
mav 20,148,36
mav 24,152,36
v_permlane32_swap_b32 v74, v75
mav 28,156,36
s_waitcnt lgkmcnt(1)
v_add_f32 v74, v74, v75
mav 16,160,40
s_mov_b32 s60, -14
.p2align 6
LR0_7:
mav 20,164,40
R 52,46080
v_add_u32 v85, 1, v85
mav 24,168,40
v_add_u32 v72, 1, v72
mav 28,172,40
mav 16,176,44
R 56,47104
mav 20,180,44
D 83,2048
mav 24,184,44
mav 28,188,44
s_waitcnt lgkmcnt(1)
mav 16,192,48
R 60,48128
mav 20,196,48
D 83,3072
mav 24,200,48
mav 28,204,48
mav 16,208,52
mav 20,212,52
buffer_store_dwordx4 v[64:67], v91, s[36:39], s54 offen sc1
mav 24,216,52
mav 28,220,52
s_waitcnt lgkmcnt(0)
mav 16,224,56
mav 20,228,56
buffer_store_dwordx4 v[68:71], v92, s[36:39], s54 offen sc1
mav 24,232,56
mav 28,236,56
mav 16,240,60
mav 20,244,60
buffer_load_dwordx4 v[124:127], v90, s[36:39], s49 offen sc1
mav 24,248,60
ds_read_b32 v76, v82 offset:0
mav 28,252,60
s_waitcnt vmcnt(18)
mvv 16,116,164
buffer_store_dword v72, v81, s[36:39], s52 offen sc1
mvv 20,120,164
s_waitcnt vmcnt(17)
buffer_load_dwordx4 v[128:131], v90, s[36:39], s49 offen offset:1024 sc1
mvv 24,116,168
LsR2:
s_cbranch_execz LsR6
s_xor_b32 s50, s50, 0x10000
s_sub_u32 s54, s54, 32768
mvv 28,120,168
s_waitcnt vmcnt(17)
mvv 16,116,172
mvv 20,120,172
s_waitcnt vmcnt(16) lgkmcnt(0)
v_cmp_gt_i32 vcc, v85, v76
v_xor_b32 v80, 0x10000, v80
mvv 24,116,176
s_waitcnt vmcnt(15)
s_add_u32 s49, s49, 313344
mvv 28,120,176
s_cbranch_vccnz CR0_8
BR0_8:
s_barrier
mvv 16,116,180
R 32,0
mvv 20,120,180
s_waitcnt vmcnt(14)
buffer_load_dwordx4 v[132:135], v86, s[36:39], s49 offen sc1
mvv 24,116,184
mvv 28,120,184
s_waitcnt vmcnt(14)
mvv 16,116,188
R 36,1024
mvv 20,120,188
s_waitcnt vmcnt(13)
buffer_load_dwordx4 v[136:139], v86, s[36:39], s49 offen offset:1024 sc1
mvv 24,116,192
mvv 28,120,192
s_waitcnt vmcnt(13)
mvv 16,116,196
R 40,2048
mvv 20,120,196
s_waitcnt vmcnt(12)
buffer_load_dwordx4 v[140:143], v87, s[36:39], s49 offen sc1
mvv 24,116,200
mvv 28,120,200
s_waitcnt lgkmcnt(1)
v_mfma_f32_16x16x32_bf16 v[0:3], a[0:3], v[32:35], v[100:103]
R 44,3072
v_cvt_pk_bf16_f32 v64, v16, v17
v_mfma_f32_16x16x32_bf16 v[4:7], a[4:7], v[32:35], v[104:107]
buffer_load_dwordx4 v[144:147], v87, s[36:39], s49 offen offset:1024 sc1
v_mfma_f32_16x16x32_bf16 v[8:11], a[8:11], v[32:35], v[108:111]
v_cvt_pk_bf16_f32 v65, v18, v19
v_cvt_pk_bf16_f32 v66, v20, v21
v_mfma_f32_16x16x32_bf16 v[12:15], a[12:15], v[32:35], v[112:115]
v_cvt_pk_bf16_f32 v67, v22, v23
v_cvt_pk_bf16_f32 v68, v24, v25
mav 0,16,36
R 48,4096
v_cvt_pk_bf16_f32 v69, v26, v27
mav 4,20,36
buffer_load_dwordx4 v[148:151], v88, s[36:39], s49 offen sc1
mav 8,24,36
v_cvt_pk_bf16_f32 v70, v28, v29
v_cvt_pk_bf16_f32 v71, v30, v31
mav 12,28,36
s_waitcnt lgkmcnt(1)
v_mul_f32 v75, v16, v16
v_fmac_f32 v75, v17, v17
mav 0,32,40
R 52,5120
v_fmac_f32 v75, v18, v18
mav 4,36,40
buffer_load_dwordx4 v[152:155], v88, s[36:39], s49 offen offset:1024 sc1
mav 8,40,40
v_fmac_f32 v75, v19, v19
v_fmac_f32 v75, v20, v20
mav 12,44,40
v_fmac_f32 v75, v21, v21
v_fmac_f32 v75, v22, v22
mav 0,48,44
R 56,6144
v_fmac_f32 v75, v23, v23
mav 4,52,44
buffer_load_dwordx4 v[156:159], v89, s[36:39], s49 offen sc1
mav 8,56,44
v_fmac_f32 v75, v24, v24
v_fmac_f32 v75, v25, v25
mav 12,60,44
s_waitcnt lgkmcnt(1)
v_fmac_f32 v75, v26, v26
v_fmac_f32 v75, v27, v27
mav 0,64,48
R 60,7168
v_fmac_f32 v75, v28, v28
mav 4,68,48
buffer_load_dwordx4 v[160:163], v89, s[36:39], s49 offen offset:1024 sc1
mav 8,72,48
ds_write_b32 v97, v74 offset:0
mav 12,76,48
ds_write_b128 v94, v[64:67] offset:0
mav 0,80,52
s_mov_b32 m0, 144384
R 32,8192
v_add_f32 v74, v17, v16
mav 4,84,52
ds_write_b128 v95, v[68:71] offset:0
mav 8,88,52
buffer_load_dword v84, s[36:39], 0 offen sc1 lds
mav 12,92,52
s_waitcnt lgkmcnt(4)
ds_read_b128 v[64:67], v96 offset:0
s_add_u32 m0, s50, 0
mav 0,96,56
R 36,9216
v_add_f32 v74, v18, v74
mav 4,100,56
D 83,0
mav 8,104,56
ds_read_b128 v[68:71], v96 offset:1024
v_add_f32 v74, v19, v74
mav 12,108,56
v_add_f32 v74, v20, v74
v_add_f32 v74, v21, v74
mav 0,112,60
R 40,10240
v_add_f32 v74, v22, v74
mav 4,116,60
D 83,1024
mav 8,120,60
v_add_f32 v74, v23, v74
v_add_f32 v74, v24, v74
mav 12,124,60
s_waitcnt lgkmcnt(2)
v_add_f32 v74, v25, v74
v_add_f32 v74, v26, v74
mav 0,128,32
R 44,11264
v_add_f32 v74, v27, v74
mav 4,132,32
v_add_f32 v74, v28, v74
v_add_f32 v74, v29, v74
mav 8,136,32
v_fmac_f32 v75, v29, v29
v_add_f32 v74, v30, v74
mav 12,140,32
v_fmac_f32 v75, v30, v30
v_add_f32 v74, v31, v74
mav 0,144,36
R 48,12288
v_fmac_f32 v75, v31, v31
mav 4,148,36
mav 8,152,36
v_permlane32_swap_b32 v74, v75
mav 12,156,36
s_waitcnt lgkmcnt(1)
v_add_f32 v74, v74, v75
mav 0,160,40
R 52,13312
v_add_u32 v85, 1, v85
mav 4,164,40
v_add_u32 v72, 1, v72
mav 8,168,40
mav 12,172,40
mav 0,176,44
R 56,14336
mav 4,180,44
D 83,2048
mav 8,184,44
mav 12,188,44
s_waitcnt lgkmcnt(1)
mav 0,192,48
R 60,15360
mav 4,196,48
D 83,3072
mav 8,200,48
mav 12,204,48
mav 0,208,52
mav 4,212,52
buffer_store_dwordx4 v[64:67], v91, s[36:39], s54 offen sc1
mav 8,216,52
mav 12,220,52
s_waitcnt lgkmcnt(0)
mav 0,224,56
mav 4,228,56
buffer_store_dwordx4 v[68:71], v92, s[36:39], s54 offen sc1
mav 8,232,56
mav 12,236,56
mav 0,240,60
mav 4,244,60
buffer_load_dwordx4 v[164:167], v90, s[36:39], s49 offen sc1
mav 8,248,60
ds_read_b32 v76, v82 offset:0
mav 12,252,60
s_waitcnt vmcnt(18)
mvv 0,116,124
buffer_store_dword v72, v81, s[36:39], s52 offen sc1
mvv 4,120,124
s_waitcnt vmcnt(17)
buffer_load_dwordx4 v[168:171], v90, s[36:39], s49 offen offset:1024 sc1
mvv 8,116,128
s_sub_u32 s54, s54, 32768
mvv 12,120,128
s_waitcnt vmcnt(17)
mvv 0,116,132
mvv 4,120,132
s_waitcnt vmcnt(16) lgkmcnt(0)
v_cmp_gt_i32 vcc, v85, v76
mvv 8,116,136
s_waitcnt vmcnt(15)
s_add_u32 s49, s49, 313344
mvv 12,120,136
s_cbranch_vccnz CR0_9
BR0_9:
s_barrier
mvv 0,116,140
R 32,32768
mvv 4,120,140
s_waitcnt vmcnt(14)
buffer_load_dwordx4 v[172:175], v86, s[36:39], s49 offen sc1
mvv 8,116,144
mvv 12,120,144
s_waitcnt vmcnt(14)
mvv 0,116,148
R 36,33792
mvv 4,120,148
s_waitcnt vmcnt(13)
buffer_load_dwordx4 v[176:179], v86, s[36:39], s49 offen offset:1024 sc1
mvv 8,116,152
mvv 12,120,152
s_waitcnt vmcnt(13)
mvv 0,116,156
R 40,34816
mvv 4,120,156
s_waitcnt vmcnt(12)
buffer_load_dwordx4 v[180:183], v87, s[36:39], s49 offen sc1
mvv 8,116,160
mvv 12,120,160
s_waitcnt lgkmcnt(1)
v_mfma_f32_16x16x32_bf16 v[16:19], a[0:3], v[32:35], v[100:103]
R 44,35840
v_cvt_pk_bf16_f32 v64, v0, v1
v_mfma_f32_16x16x32_bf16 v[20:23], a[4:7], v[32:35], v[104:107]
buffer_load_dwordx4 v[184:187], v87, s[36:39], s49 offen offset:1024 sc1
v_mfma_f32_16x16x32_bf16 v[24:27], a[8:11], v[32:35], v[108:111]
v_cvt_pk_bf16_f32 v65, v2, v3
v_cvt_pk_bf16_f32 v66, v4, v5
v_mfma_f32_16x16x32_bf16 v[28:31], a[12:15], v[32:35], v[112:115]
v_cvt_pk_bf16_f32 v67, v6, v7
v_cvt_pk_bf16_f32 v68, v8, v9
mav 16,16,36
R 48,36864
v_cvt_pk_bf16_f32 v69, v10, v11
mav 20,20,36
buffer_load_dwordx4 v[188:191], v88, s[36:39], s49 offen sc1
mav 24,24,36
v_cvt_pk_bf16_f32 v70, v12, v13
v_cvt_pk_bf16_f32 v71, v14, v15
mav 28,28,36
s_waitcnt lgkmcnt(1)
v_mul_f32 v75, v0, v0
v_fmac_f32 v75, v1, v1
mav 16,32,40
R 52,37888
v_fmac_f32 v75, v2, v2
mav 20,36,40
buffer_load_dwordx4 v[192:195], v88, s[36:39], s49 offen offset:1024 sc1
mav 24,40,40
v_fmac_f32 v75, v3, v3
v_fmac_f32 v75, v4, v4
mav 28,44,40
v_fmac_f32 v75, v5, v5
v_fmac_f32 v75, v6, v6
mav 16,48,44
R 56,38912
v_fmac_f32 v75, v7, v7
mav 20,52,44
buffer_load_dwordx4 v[196:199], v89, s[36:39], s49 offen sc1
mav 24,56,44
v_fmac_f32 v75, v8, v8
v_fmac_f32 v75, v9, v9
mav 28,60,44
s_waitcnt lgkmcnt(1)
v_fmac_f32 v75, v10, v10
v_fmac_f32 v75, v11, v11
mav 16,64,48
R 60,39936
v_fmac_f32 v75, v12, v12
mav 20,68,48
buffer_load_dwordx4 v[200:203], v89, s[36:39], s49 offen offset:1024 sc1
mav 24,72,48
ds_write_b32 v97, v74 offset:1024
mav 28,76,48
ds_write_b128 v94, v[64:67] offset:0
mav 16,80,52
s_mov_b32 m0, 144384
R 32,40960
v_add_f32 v74, v1, v0
mav 20,84,52
ds_write_b128 v95, v[68:71] offset:0
mav 24,88,52
buffer_load_dword v84, s[36:39], 0 offen sc1 lds
mav 28,92,52
s_waitcnt lgkmcnt(4)
ds_read_b128 v[64:67], v96 offset:0
s_add_u32 m0, s50, 32768
mav 16,96,56
R 36,41984
v_add_f32 v74, v2, v74
mav 20,100,56
D 83,0
mav 24,104,56
ds_read_b128 v[68:71], v96 offset:1024
v_add_f32 v74, v3, v74
mav 28,108,56
v_add_f32 v74, v4, v74
v_add_f32 v74, v5, v74
mav 16,112,60
R 40,43008
v_add_f32 v74, v6, v74
mav 20,116,60
D 83,1024
mav 24,120,60
v_add_f32 v74, v7, v74
v_add_f32 v74, v8, v74
mav 28,124,60
s_waitcnt lgkmcnt(2)
v_add_f32 v74, v9, v74
v_add_f32 v74, v10, v74
mav 16,128,32
R 44,44032
v_add_f32 v74, v11, v74
mav 20,132,32
v_add_f32 v74, v12, v74
v_add_f32 v74, v13, v74
mav 24,136,32
v_fmac_f32 v75, v13, v13
v_add_f32 v74, v14, v74
mav 28,140,32
v_fmac_f32 v75, v14, v14
v_add_f32 v74, v15, v74
mav 16,144,36
R 48,45056
v_fmac_f32 v75, v15, v15
mav 20,148,36
mav 24,152,36
v_permlane32_swap_b32 v74, v75
mav 28,156,36
s_waitcnt lgkmcnt(1)
s_add_u32 s60, s60, 1
v_add_f32 v74, v74, v75
mav 16,160,40
s_cbranch_scc0 LR0_7
mav 20,164,40
R 52,46080
v_add_u32 v85, 1, v85
mav 24,168,40
v_add_u32 v72, 1, v72
mav 28,172,40
mav 16,176,44
R 56,47104
mav 20,180,44
D 83,2048
mav 24,184,44
mav 28,188,44
s_waitcnt lgkmcnt(1)
mav 16,192,48
R 60,48128
mav 20,196,48
D 83,3072
mav 24,200,48
mav 28,204,48
mav 16,208,52
mav 20,212,52
buffer_store_dwordx4 v[64:67], v91, s[36:39], s54 offen sc1
mav 24,216,52
mav 28,220,52
s_waitcnt lgkmcnt(0)
mav 16,224,56
mav 20,228,56
buffer_store_dwordx4 v[68:71], v92, s[36:39], s54 offen sc1
mav 24,232,56
mav 28,236,56
mav 16,240,60
mav 20,244,60
buffer_load_dwordx4 v[124:127], v90, s[36:39], s49 offen sc1
mav 24,248,60
ds_read_b32 v76, v82 offset:0
mav 28,252,60
s_waitcnt vmcnt(18)
mvv 16,116,164
buffer_store_dword v72, v81, s[36:39], s52 offen sc1
mvv 20,120,164
s_waitcnt vmcnt(17)
buffer_load_dwordx4 v[128:131], v90, s[36:39], s49 offen offset:1024 sc1
mvv 24,116,168
s_xor_b32 s50, s50, 0x10000
s_sub_u32 s54, s54, 32768
mvv 28,120,168
s_waitcnt vmcnt(17)
mvv 16,116,172
mvv 20,120,172
s_waitcnt vmcnt(16) lgkmcnt(0)
v_cmp_gt_i32 vcc, v85, v76
v_xor_b32 v80, 0x10000, v80
mvv 24,116,176
s_waitcnt vmcnt(15)
s_add_u32 s49, s49, 313344
mvv 28,120,176
s_cbranch_vccnz CR0_10
BR0_10:
s_barrier
mvv 16,116,180
R 32,0
mvv 20,120,180
s_waitcnt vmcnt(14)
buffer_load_dwordx4 v[132:135], v86, s[36:39], s49 offen sc1
mvv 24,116,184
mvv 28,120,184
s_waitcnt vmcnt(14)
mvv 16,116,188
R 36,1024
mvv 20,120,188
s_waitcnt vmcnt(13)
buffer_load_dwordx4 v[136:139], v86, s[36:39], s49 offen offset:1024 sc1
mvv 24,116,192
mvv 28,120,192
s_waitcnt vmcnt(13)
mvv 16,116,196
R 40,2048
mvv 20,120,196
s_waitcnt vmcnt(12)
buffer_load_dwordx4 v[140:143], v87, s[36:39], s49 offen sc1
mvv 24,116,200
mvv 28,120,200
s_waitcnt lgkmcnt(1)
v_mfma_f32_16x16x32_bf16 v[0:3], a[0:3], v[32:35], v[100:103]
R 44,3072
v_cvt_pk_bf16_f32 v64, v16, v17
v_mfma_f32_16x16x32_bf16 v[4:7], a[4:7], v[32:35], v[104:107]
buffer_load_dwordx4 v[144:147], v87, s[36:39], s49 offen offset:1024 sc1
v_mfma_f32_16x16x32_bf16 v[8:11], a[8:11], v[32:35], v[108:111]
v_cvt_pk_bf16_f32 v65, v18, v19
v_cvt_pk_bf16_f32 v66, v20, v21
v_mfma_f32_16x16x32_bf16 v[12:15], a[12:15], v[32:35], v[112:115]
v_cvt_pk_bf16_f32 v67, v22, v23
v_cvt_pk_bf16_f32 v68, v24, v25
mav 0,16,36
R 48,4096
v_cvt_pk_bf16_f32 v69, v26, v27
mav 4,20,36
buffer_load_dwordx4 v[148:151], v88, s[36:39], s49 offen sc1
mav 8,24,36
v_cvt_pk_bf16_f32 v70, v28, v29
v_cvt_pk_bf16_f32 v71, v30, v31
mav 12,28,36
s_waitcnt lgkmcnt(1)
v_mul_f32 v75, v16, v16
v_fmac_f32 v75, v17, v17
mav 0,32,40
R 52,5120
v_fmac_f32 v75, v18, v18
mav 4,36,40
buffer_load_dwordx4 v[152:155], v88, s[36:39], s49 offen offset:1024 sc1
mav 8,40,40
v_fmac_f32 v75, v19, v19
v_fmac_f32 v75, v20, v20
mav 12,44,40
v_fmac_f32 v75, v21, v21
v_fmac_f32 v75, v22, v22
mav 0,48,44
R 56,6144
v_fmac_f32 v75, v23, v23
mav 4,52,44
buffer_load_dwordx4 v[156:159], v89, s[36:39], s49 offen sc1
mav 8,56,44
v_fmac_f32 v75, v24, v24
v_fmac_f32 v75, v25, v25
mav 12,60,44
s_waitcnt lgkmcnt(1)
v_fmac_f32 v75, v26, v26
v_fmac_f32 v75, v27, v27
mav 0,64,48
R 60,7168
v_fmac_f32 v75, v28, v28
mav 4,68,48
buffer_load_dwordx4 v[160:163], v89, s[36:39], s49 offen offset:1024 sc1
mav 8,72,48
ds_write_b32 v97, v74 offset:0
mav 12,76,48
ds_write_b128 v94, v[64:67] offset:0
mav 0,80,52
s_mov_b32 m0, 144384
R 32,8192
v_add_f32 v74, v17, v16
mav 4,84,52
ds_write_b128 v95, v[68:71] offset:0
mav 8,88,52
buffer_load_dword v84, s[36:39], 0 offen sc1 lds
mav 12,92,52
s_waitcnt lgkmcnt(4)
ds_read_b128 v[64:67], v96 offset:0
s_add_u32 m0, s50, 0
mav 0,96,56
R 36,9216
v_add_f32 v74, v18, v74
mav 4,100,56
D 83,0
mav 8,104,56
ds_read_b128 v[68:71], v96 offset:1024
v_add_f32 v74, v19, v74
mav 12,108,56
v_add_f32 v74, v20, v74
v_add_f32 v74, v21, v74
mav 0,112,60
R 40,10240
v_add_f32 v74, v22, v74
mav 4,116,60
D 83,1024
mav 8,120,60
v_add_f32 v74, v23, v74
v_add_f32 v74, v24, v74
mav 12,124,60
s_waitcnt lgkmcnt(2)
v_add_f32 v74, v25, v74
v_add_f32 v74, v26, v74
mav 0,128,32
R 44,11264
v_add_f32 v74, v27, v74
mav 4,132,32
v_add_f32 v74, v28, v74
v_add_f32 v74, v29, v74
mav 8,136,32
v_fmac_f32 v75, v29, v29
v_add_f32 v74, v30, v74
mav 12,140,32
v_fmac_f32 v75, v30, v30
v_add_f32 v74, v31, v74
mav 0,144,36
R 48,12288
v_fmac_f32 v75, v31, v31
mav 4,148,36
mav 8,152,36
v_permlane32_swap_b32 v74, v75
mav 12,156,36
s_waitcnt lgkmcnt(1)
v_add_f32 v74, v74, v75
mav 0,160,40
R 52,13312
v_add_u32 v85, 1, v85
mav 4,164,40
v_add_u32 v72, 1, v72
mav 8,168,40
mav 12,172,40
mav 0,176,44
R 56,14336
mav 4,180,44
D 83,2048
mav 8,184,44
mav 12,188,44
s_waitcnt lgkmcnt(1)
mav 0,192,48
R 60,15360
mav 4,196,48
D 83,3072
mav 8,200,48
mav 12,204,48
mav 0,208,52
mav 4,212,52
buffer_store_dwordx4 v[64:67], v91, s[36:39], s54 offen sc1
mav 8,216,52
mav 12,220,52
s_waitcnt lgkmcnt(0)
mav 0,224,56
mav 4,228,56
buffer_store_dwordx4 v[68:71], v92, s[36:39], s54 offen sc1
mav 8,232,56
mav 12,236,56
mav 0,240,60
mav 4,244,60
buffer_load_dwordx4 v[164:167], v90, s[36:39], s49 offen sc1
mav 8,248,60
)"
R"(
ds_read_b32 v76, v82 offset:0
mav 12,252,60
s_waitcnt vmcnt(18)
mvv 0,116,124
buffer_store_dword v72, v81, s[36:39], s52 offen sc1
mvv 4,120,124
s_waitcnt vmcnt(17)
buffer_load_dwordx4 v[168:171], v90, s[36:39], s49 offen offset:1024 sc1
mvv 8,116,128
s_sub_u32 s54, s54, 3178496
mvv 12,120,128
s_waitcnt vmcnt(17)
mvv 0,116,132
mvv 4,120,132
s_waitcnt vmcnt(16) lgkmcnt(0)
v_cmp_gt_i32 vcc, v85, v76
mvv 8,116,136
s_waitcnt vmcnt(15)
s_add_u32 s49, s49, 313344
mvv 12,120,136
s_cbranch_vccnz CR0_11
BR0_11:
s_barrier
mvv 0,116,140
R 32,32768
mvv 4,120,140
s_waitcnt vmcnt(14)
buffer_load_dwordx4 v[172:175], v86, s[36:39], s49 offen sc1
mvv 8,116,144
LsR3:
s_cbranch_execz LsR7
mvv 12,120,144
s_waitcnt vmcnt(14)
mvv 0,116,148
R 36,33792
mvv 4,120,148
s_waitcnt vmcnt(13)
buffer_load_dwordx4 v[176:179], v86, s[36:39], s49 offen offset:1024 sc1
mvv 8,116,152
mvv 12,120,152
s_waitcnt vmcnt(13)
mvv 0,116,156
R 40,34816
mvv 4,120,156
s_waitcnt vmcnt(12)
buffer_load_dwordx4 v[180:183], v87, s[36:39], s49 offen sc1
mvv 8,116,160
mvv 12,120,160
s_waitcnt lgkmcnt(1)
v_mfma_f32_16x16x32_bf16 v[16:19], a[0:3], v[32:35], v[100:103]
R 44,35840
v_cvt_pk_bf16_f32 v64, v0, v1
v_mfma_f32_16x16x32_bf16 v[20:23], a[4:7], v[32:35], v[104:107]
buffer_load_dwordx4 v[184:187], v87, s[36:39], s49 offen offset:1024 sc1
v_mfma_f32_16x16x32_bf16 v[24:27], a[8:11], v[32:35], v[108:111]
v_cvt_pk_bf16_f32 v65, v2, v3
v_cvt_pk_bf16_f32 v66, v4, v5
v_mfma_f32_16x16x32_bf16 v[28:31], a[12:15], v[32:35], v[112:115]
v_cvt_pk_bf16_f32 v67, v6, v7
v_cvt_pk_bf16_f32 v68, v8, v9
mav 16,16,36
R 48,36864
v_cvt_pk_bf16_f32 v69, v10, v11
mav 20,20,36
buffer_load_dwordx4 v[188:191], v88, s[36:39], s49 offen sc1
mav 24,24,36
v_cvt_pk_bf16_f32 v70, v12, v13
v_cvt_pk_bf16_f32 v71, v14, v15
mav 28,28,36
s_waitcnt lgkmcnt(1)
mav 16,32,40
R 52,37888
mav 20,36,40
buffer_load_dwordx4 v[192:195], v88, s[36:39], s49 offen offset:1024 sc1
mav 24,40,40
mav 28,44,40
mav 16,48,44
R 56,38912
mav 20,52,44
buffer_load_dwordx4 v[196:199], v89, s[36:39], s49 offen sc1
mav 24,56,44
mav 28,60,44
s_waitcnt lgkmcnt(1)
mav 16,64,48
R 60,39936
mav 20,68,48
buffer_load_dwordx4 v[200:203], v89, s[36:39], s49 offen offset:1024 sc1
mav 24,72,48
ds_write_b32 v97, v74 offset:1024
mav 28,76,48
ds_write_b128 v94, v[64:67] offset:0
mav 16,80,52
s_mov_b32 m0, 144384
R 32,40960
mav 20,84,52
ds_write_b128 v95, v[68:71] offset:0
mav 24,88,52
buffer_load_dword v84, s[36:39], 0 offen sc1 lds
mav 28,92,52
s_waitcnt lgkmcnt(4)
ds_read_b128 v[64:67], v96 offset:0
s_add_u32 m0, s50, 32768
mav 16,96,56
R 36,41984
mav 20,100,56
D 83,0
mav 24,104,56
ds_read_b128 v[68:71], v96 offset:1024
mav 28,108,56
mav 16,112,60
R 40,43008
mav 20,116,60
D 83,1024
mav 24,120,60
mav 28,124,60
s_waitcnt lgkmcnt(2)
mav 16,128,32
R 44,44032
mav 20,132,32
mav 24,136,32
mav 28,140,32
mav 16,144,36
R 48,45056
mav 20,148,36
mav 24,152,36
mav 28,156,36
s_waitcnt lgkmcnt(1)
mav 16,160,40
mav 20,164,40
R 52,46080
v_add_u32 v85, 1, v85
mav 24,168,40
v_add_u32 v72, 69, v72
mav 28,172,40
mav 16,176,44
R 56,47104
mav 20,180,44
D 83,2048
mav 24,184,44
mav 28,188,44
s_waitcnt lgkmcnt(1)
mav 16,192,48
R 60,48128
mav 20,196,48
D 83,3072
mav 24,200,48
mav 28,204,48
mav 16,208,52
mav 20,212,52
buffer_store_dwordx4 v[64:67], v91, s[44:47], s54 offen
mav 24,216,52
mav 28,220,52
s_waitcnt lgkmcnt(0)
mav 16,224,56
mav 20,228,56
buffer_store_dwordx4 v[68:71], v92, s[44:47], s54 offen
mav 24,232,56
mav 28,236,56
mav 16,240,60
mav 20,244,60
buffer_load_dwordx4 v[124:127], v90, s[36:39], s49 offen sc1
mav 24,248,60
ds_read_b32 v76, v82 offset:0
mav 28,252,60
s_waitcnt vmcnt(18)
mvv 16,116,164
buffer_store_dword v72, v81, s[36:39], s52 offen sc1
mvv 20,120,164
s_waitcnt vmcnt(17)
buffer_load_dwordx4 v[128:131], v90, s[36:39], s49 offen offset:1024 sc1
mvv 24,116,168
s_xor_b32 s50, s50, 0x10000
s_sub_u32 s54, s54, 32768
mvv 28,120,168
s_waitcnt vmcnt(17)
mvv 16,116,172
mvv 20,120,172
s_waitcnt vmcnt(16) lgkmcnt(0)
v_cmp_gt_i32 vcc, v85, v76
v_xor_b32 v80, 0x10000, v80
mvv 24,116,176
s_waitcnt vmcnt(15)
s_add_u32 s49, s49, 313344
mvv 28,120,176
s_cbranch_vccnz CR0_12
BR0_12:
s_barrier
mvv 16,116,180
R 32,0
mvv 20,120,180
s_waitcnt vmcnt(14)
buffer_load_dwordx4 v[132:135], v86, s[36:39], s49 offen sc1
mvv 24,116,184
mvv 28,120,184
s_waitcnt vmcnt(14)
mvv 16,116,188
R 36,1024
mvv 20,120,188
s_waitcnt vmcnt(13)
buffer_load_dwordx4 v[136:139], v86, s[36:39], s49 offen offset:1024 sc1
mvv 24,116,192
mvv 28,120,192
s_waitcnt vmcnt(13)
mvv 16,116,196
R 40,2048
mvv 20,120,196
s_waitcnt vmcnt(12)
buffer_load_dwordx4 v[140:143], v87, s[36:39], s49 offen sc1
mvv 24,116,200
mvv 28,120,200
s_waitcnt lgkmcnt(1)
v_mfma_f32_16x16x32_bf16 v[0:3], a[0:3], v[32:35], v[100:103]
R 44,3072
v_cvt_pk_bf16_f32 v64, v16, v17
v_mfma_f32_16x16x32_bf16 v[4:7], a[4:7], v[32:35], v[104:107]
buffer_load_dwordx4 v[144:147], v87, s[36:39], s49 offen offset:1024 sc1
v_mfma_f32_16x16x32_bf16 v[8:11], a[8:11], v[32:35], v[108:111]
v_cvt_pk_bf16_f32 v65, v18, v19
v_cvt_pk_bf16_f32 v66, v20, v21
v_mfma_f32_16x16x32_bf16 v[12:15], a[12:15], v[32:35], v[112:115]
v_cvt_pk_bf16_f32 v67, v22, v23
v_cvt_pk_bf16_f32 v68, v24, v25
mav 0,16,36
R 48,4096
v_cvt_pk_bf16_f32 v69, v26, v27
mav 4,20,36
buffer_load_dwordx4 v[148:151], v88, s[36:39], s49 offen sc1
mav 8,24,36
v_cvt_pk_bf16_f32 v70, v28, v29
v_cvt_pk_bf16_f32 v71, v30, v31
mav 12,28,36
s_waitcnt lgkmcnt(1)
mav 0,32,40
R 52,5120
mav 4,36,40
buffer_load_dwordx4 v[152:155], v88, s[36:39], s49 offen offset:1024 sc1
mav 8,40,40
mav 12,44,40
mav 0,48,44
R 56,6144
mav 4,52,44
buffer_load_dwordx4 v[156:159], v89, s[36:39], s49 offen sc1
mav 8,56,44
mav 12,60,44
s_waitcnt lgkmcnt(1)
mav 0,64,48
R 60,7168
mav 4,68,48
buffer_load_dwordx4 v[160:163], v89, s[36:39], s49 offen offset:1024 sc1
mav 8,72,48
mav 12,76,48
ds_write_b128 v94, v[64:67] offset:0
mav 0,80,52
s_mov_b32 m0, 144384
R 32,8192
mav 4,84,52
ds_write_b128 v95, v[68:71] offset:0
mav 8,88,52
buffer_load_dword v84, s[36:39], 0 offen sc1 lds
mav 12,92,52
s_waitcnt lgkmcnt(3)
ds_read_b128 v[64:67], v96 offset:0
s_add_u32 m0, s50, 0
mav 0,96,56
R 36,9216
mav 4,100,56
D 83,0
mav 8,104,56
ds_read_b128 v[68:71], v96 offset:1024
mav 12,108,56
mav 0,112,60
R 40,10240
mav 4,116,60
D 83,1024
mav 8,120,60
mav 12,124,60
s_waitcnt lgkmcnt(2)
mav 0,128,32
R 44,11264
mav 4,132,32
mav 8,136,32
mav 12,140,32
mav 0,144,36
R 48,12288
mav 4,148,36
mav 8,152,36
mav 12,156,36
s_waitcnt lgkmcnt(1)
mav 0,160,40
R 52,13312
v_add_u32 v85, 1, v85
mav 4,164,40
mav 8,168,40
mav 12,172,40
mav 0,176,44
R 56,14336
mav 4,180,44
D 83,2048
mav 8,184,44
mav 12,188,44
s_waitcnt lgkmcnt(1)
mav 0,192,48
R 60,15360
mav 4,196,48
D 83,3072
mav 8,200,48
mav 12,204,48
mav 0,208,52
mav 4,212,52
buffer_store_dwordx4 v[64:67], v91, s[44:47], s54 offen
mav 8,216,52
mav 12,220,52
s_waitcnt lgkmcnt(0)
mav 0,224,56
mav 4,228,56
buffer_store_dwordx4 v[68:71], v92, s[44:47], s54 offen
mav 8,232,56
mav 12,236,56
mav 0,240,60
mav 4,244,60
buffer_load_dwordx4 v[164:167], v90, s[36:39], s49 offen sc1
mav 8,248,60
ds_read_b32 v76, v82 offset:0
mav 12,252,60
s_waitcnt vmcnt(18)
mvv 0,116,124
mvv 4,120,124
s_waitcnt vmcnt(16)
buffer_load_dwordx4 v[168:171], v90, s[36:39], s49 offen offset:1024 sc1
mvv 8,116,128
s_sub_u32 s54, s54, 32768
mvv 12,120,128
s_waitcnt vmcnt(16)
mvv 0,116,132
mvv 4,120,132
s_waitcnt vmcnt(15) lgkmcnt(0)
v_cmp_gt_i32 vcc, v85, v76
mvv 8,116,136
s_waitcnt vmcnt(14)
s_add_u32 s49, s49, 313344
mvv 12,120,136
s_cbranch_vccnz CR0_13
BR0_13:
s_barrier
mvv 0,116,140
R 32,32768
mvv 4,120,140
s_waitcnt vmcnt(13)
buffer_load_dwordx4 v[172:175], v86, s[36:39], s49 offen sc1
mvv 8,116,144
mvv 12,120,144
s_waitcnt vmcnt(13)
mvv 0,116,148
R 36,33792
mvv 4,120,148
s_waitcnt vmcnt(12)
buffer_load_dwordx4 v[176:179], v86, s[36:39], s49 offen offset:1024 sc1
mvv 8,116,152
mvv 12,120,152
s_waitcnt vmcnt(12)
mvv 0,116,156
R 40,34816
mvv 4,120,156
s_waitcnt vmcnt(11)
buffer_load_dwordx4 v[180:183], v87, s[36:39], s49 offen sc1
mvv 8,116,160
mvv 12,120,160
s_waitcnt lgkmcnt(1)
v_mfma_f32_16x16x32_bf16 v[16:19], a[0:3], v[32:35], v[100:103]
R 44,35840
v_cvt_pk_bf16_f32 v64, v0, v1
v_mfma_f32_16x16x32_bf16 v[20:23], a[4:7], v[32:35], v[104:107]
buffer_load_dwordx4 v[184:187], v87, s[36:39], s49 offen offset:1024 sc1
v_mfma_f32_16x16x32_bf16 v[24:27], a[8:11], v[32:35], v[108:111]
v_cvt_pk_bf16_f32 v65, v2, v3
v_cvt_pk_bf16_f32 v66, v4, v5
v_mfma_f32_16x16x32_bf16 v[28:31], a[12:15], v[32:35], v[112:115]
v_cvt_pk_bf16_f32 v67, v6, v7
v_cvt_pk_bf16_f32 v68, v8, v9
mav 16,16,36
R 48,36864
v_cvt_pk_bf16_f32 v69, v10, v11
mav 20,20,36
buffer_load_dwordx4 v[188:191], v88, s[36:39], s49 offen sc1
mav 24,24,36
v_cvt_pk_bf16_f32 v70, v12, v13
v_cvt_pk_bf16_f32 v71, v14, v15
mav 28,28,36
s_waitcnt lgkmcnt(1)
mav 16,32,40
R 52,37888
mav 20,36,40
buffer_load_dwordx4 v[192:195], v88, s[36:39], s49 offen offset:1024 sc1
mav 24,40,40
mav 28,44,40
mav 16,48,44
R 56,38912
mav 20,52,44
buffer_load_dwordx4 v[196:199], v89, s[36:39], s49 offen sc1
mav 24,56,44
mav 28,60,44
s_waitcnt lgkmcnt(1)
mav 16,64,48
R 60,39936
mav 20,68,48
buffer_load_dwordx4 v[200:203], v89, s[36:39], s49 offen offset:1024 sc1
mav 24,72,48
mav 28,76,48
ds_write_b128 v94, v[64:67] offset:0
mav 16,80,52
s_mov_b32 m0, 144384
R 32,40960
mav 20,84,52
ds_write_b128 v95, v[68:71] offset:0
mav 24,88,52
buffer_load_dword v84, s[36:39], 0 offen sc1 lds
mav 28,92,52
s_waitcnt lgkmcnt(3)
ds_read_b128 v[64:67], v96 offset:0
s_add_u32 m0, s50, 32768
mav 16,96,56
R 36,41984
mav 20,100,56
D 83,0
mav 24,104,56
ds_read_b128 v[68:71], v96 offset:1024
mav 28,108,56
mav 16,112,60
R 40,43008
mav 20,116,60
D 83,1024
mav 24,120,60
mav 28,124,60
s_waitcnt lgkmcnt(2)
mav 16,128,32
R 44,44032
mav 20,132,32
mav 24,136,32
mav 28,140,32
mav 16,144,36
R 48,45056
mav 20,148,36
mav 24,152,36
mav 28,156,36
s_waitcnt lgkmcnt(1)
mav 16,160,40
s_mov_b32 s60, -13
.p2align 6
LR0_14:
mav 20,164,40
R 52,46080
v_add_u32 v85, 1, v85
mav 24,168,40
mav 28,172,40
mav 16,176,44
R 56,47104
mav 20,180,44
D 83,2048
mav 24,184,44
mav 28,188,44
s_waitcnt lgkmcnt(1)
mav 16,192,48
R 60,48128
mav 20,196,48
D 83,3072
mav 24,200,48
mav 28,204,48
mav 16,208,52
mav 20,212,52
buffer_store_dwordx4 v[64:67], v91, s[44:47], s54 offen
mav 24,216,52
mav 28,220,52
s_waitcnt lgkmcnt(0)
mav 16,224,56
mav 20,228,56
buffer_store_dwordx4 v[68:71], v92, s[44:47], s54 offen
mav 24,232,56
mav 28,236,56
mav 16,240,60
mav 20,244,60
buffer_load_dwordx4 v[124:127], v90, s[36:39], s49 offen sc1
mav 24,248,60
ds_read_b32 v76, v82 offset:0
mav 28,252,60
s_waitcnt vmcnt(17)
mvv 16,116,164
mvv 20,120,164
s_waitcnt vmcnt(16)
buffer_load_dwordx4 v[128:131], v90, s[36:39], s49 offen offset:1024 sc1
mvv 24,116,168
s_xor_b32 s50, s50, 0x10000
s_sub_u32 s54, s54, 32768
mvv 28,120,168
s_waitcnt vmcnt(16)
mvv 16,116,172
mvv 20,120,172
s_waitcnt vmcnt(15) lgkmcnt(0)
v_cmp_gt_i32 vcc, v85, v76
v_xor_b32 v80, 0x10000, v80
mvv 24,116,176
s_waitcnt vmcnt(14)
s_add_u32 s49, s49, 313344
mvv 28,120,176
s_cbranch_vccnz CR0_15
BR0_15:
s_barrier
mvv 16,116,180
R 32,0
mvv 20,120,180
s_waitcnt vmcnt(13)
buffer_load_dwordx4 v[132:135], v86, s[36:39], s49 offen sc1
mvv 24,116,184
mvv 28,120,184
s_waitcnt vmcnt(13)
mvv 16,116,188
R 36,1024
mvv 20,120,188
s_waitcnt vmcnt(12)
buffer_load_dwordx4 v[136:139], v86, s[36:39], s49 offen offset:1024 sc1
mvv 24,116,192
mvv 28,120,192
s_waitcnt vmcnt(12)
mvv 16,116,196
R 40,2048
mvv 20,120,196
s_waitcnt vmcnt(11)
buffer_load_dwordx4 v[140:143], v87, s[36:39], s49 offen sc1
mvv 24,116,200
mvv 28,120,200
s_waitcnt lgkmcnt(1)
v_mfma_f32_16x16x32_bf16 v[0:3], a[0:3], v[32:35], v[100:103]
R 44,3072
v_cvt_pk_bf16_f32 v64, v16, v17
v_mfma_f32_16x16x32_bf16 v[4:7], a[4:7], v[32:35], v[104:107]
buffer_load_dwordx4 v[144:147], v87, s[36:39], s49 offen offset:1024 sc1
v_mfma_f32_16x16x32_bf16 v[8:11], a[8:11], v[32:35], v[108:111]
v_cvt_pk_bf16_f32 v65, v18, v19
v_cvt_pk_bf16_f32 v66, v20, v21
v_mfma_f32_16x16x32_bf16 v[12:15], a[12:15], v[32:35], v[112:115]
v_cvt_pk_bf16_f32 v67, v22, v23
v_cvt_pk_bf16_f32 v68, v24, v25
mav 0,16,36
R 48,4096
v_cvt_pk_bf16_f32 v69, v26, v27
mav 4,20,36
buffer_load_dwordx4 v[148:151], v88, s[36:39], s49 offen sc1
mav 8,24,36
v_cvt_pk_bf16_f32 v70, v28, v29
v_cvt_pk_bf16_f32 v71, v30, v31
mav 12,28,36
s_waitcnt lgkmcnt(1)
mav 0,32,40
R 52,5120
mav 4,36,40
buffer_load_dwordx4 v[152:155], v88, s[36:39], s49 offen offset:1024 sc1
mav 8,40,40
mav 12,44,40
mav 0,48,44
R 56,6144
mav 4,52,44
buffer_load_dwordx4 v[156:159], v89, s[36:39], s49 offen sc1
mav 8,56,44
mav 12,60,44
s_waitcnt lgkmcnt(1)
mav 0,64,48
R 60,7168
mav 4,68,48
buffer_load_dwordx4 v[160:163], v89, s[36:39], s49 offen offset:1024 sc1
mav 8,72,48
mav 12,76,48
ds_write_b128 v94, v[64:67] offset:0
mav 0,80,52
s_mov_b32 m0, 144384
R 32,8192
mav 4,84,52
ds_write_b128 v95, v[68:71] offset:0
mav 8,88,52
buffer_load_dword v84, s[36:39], 0 offen sc1 lds
mav 12,92,52
s_waitcnt lgkmcnt(3)
LsR4:
s_cbranch_execz LsR8
ds_read_b128 v[64:67], v96 offset:0
s_add_u32 m0, s50, 0
mav 0,96,56
R 36,9216
mav 4,100,56
D 83,0
mav 8,104,56
ds_read_b128 v[68:71], v96 offset:1024
mav 12,108,56
mav 0,112,60
R 40,10240
mav 4,116,60
D 83,1024
mav 8,120,60
mav 12,124,60
s_waitcnt lgkmcnt(2)
mav 0,128,32
R 44,11264
mav 4,132,32
mav 8,136,32
mav 12,140,32
mav 0,144,36
R 48,12288
mav 4,148,36
mav 8,152,36
mav 12,156,36
s_waitcnt lgkmcnt(1)
mav 0,160,40
R 52,13312
v_add_u32 v85, 1, v85
mav 4,164,40
mav 8,168,40
mav 12,172,40
mav 0,176,44
R 56,14336
mav 4,180,44
D 83,2048
mav 8,184,44
mav 12,188,44
s_waitcnt lgkmcnt(1)
mav 0,192,48
R 60,15360
mav 4,196,48
D 83,3072
mav 8,200,48
mav 12,204,48
mav 0,208,52
mav 4,212,52
buffer_store_dwordx4 v[64:67], v91, s[44:47], s54 offen
mav 8,216,52
mav 12,220,52
s_waitcnt lgkmcnt(0)
mav 0,224,56
mav 4,228,56
buffer_store_dwordx4 v[68:71], v92, s[44:47], s54 offen
mav 8,232,56
mav 12,236,56
mav 0,240,60
mav 4,244,60
buffer_load_dwordx4 v[164:167], v90, s[36:39], s49 offen sc1
mav 8,248,60
ds_read_b32 v76, v82 offset:0
mav 12,252,60
s_waitcnt vmcnt(17)
mvv 0,116,124
mvv 4,120,124
s_waitcnt vmcnt(16)
buffer_load_dwordx4 v[168:171], v90, s[36:39], s49 offen offset:1024 sc1
mvv 8,116,128
s_sub_u32 s54, s54, 32768
mvv 12,120,128
s_waitcnt vmcnt(16)
mvv 0,116,132
mvv 4,120,132
s_waitcnt vmcnt(15) lgkmcnt(0)
v_cmp_gt_i32 vcc, v85, v76
mvv 8,116,136
s_waitcnt vmcnt(14)
s_add_u32 s49, s49, 313344
mvv 12,120,136
s_cbranch_vccnz CR0_16
BR0_16:
s_barrier
mvv 0,116,140
R 32,32768
mvv 4,120,140
s_waitcnt vmcnt(13)
buffer_load_dwordx4 v[172:175], v86, s[36:39], s49 offen sc1
mvv 8,116,144
mvv 12,120,144
s_waitcnt vmcnt(13)
mvv 0,116,148
R 36,33792
mvv 4,120,148
s_waitcnt vmcnt(12)
buffer_load_dwordx4 v[176:179], v86, s[36:39], s49 offen offset:1024 sc1
mvv 8,116,152
mvv 12,120,152
s_waitcnt vmcnt(12)
mvv 0,116,156
R 40,34816
mvv 4,120,156
s_waitcnt vmcnt(11)
buffer_load_dwordx4 v[180:183], v87, s[36:39], s49 offen sc1
mvv 8,116,160
mvv 12,120,160
s_waitcnt lgkmcnt(1)
v_mfma_f32_16x16x32_bf16 v[16:19], a[0:3], v[32:35], v[100:103]
R 44,35840
v_cvt_pk_bf16_f32 v64, v0, v1
v_mfma_f32_16x16x32_bf16 v[20:23], a[4:7], v[32:35], v[104:107]
buffer_load_dwordx4 v[184:187], v87, s[36:39], s49 offen offset:1024 sc1
v_mfma_f32_16x16x32_bf16 v[24:27], a[8:11], v[32:35], v[108:111]
v_cvt_pk_bf16_f32 v65, v2, v3
v_cvt_pk_bf16_f32 v66, v4, v5
v_mfma_f32_16x16x32_bf16 v[28:31], a[12:15], v[32:35], v[112:115]
v_cvt_pk_bf16_f32 v67, v6, v7
v_cvt_pk_bf16_f32 v68, v8, v9
mav 16,16,36
R 48,36864
v_cvt_pk_bf16_f32 v69, v10, v11
mav 20,20,36
buffer_load_dwordx4 v[188:191], v88, s[36:39], s49 offen sc1
mav 24,24,36
v_cvt_pk_bf16_f32 v70, v12, v13
v_cvt_pk_bf16_f32 v71, v14, v15
mav 28,28,36
s_waitcnt lgkmcnt(1)
mav 16,32,40
R 52,37888
mav 20,36,40
buffer_load_dwordx4 v[192:195], v88, s[36:39], s49 offen offset:1024 sc1
mav 24,40,40
mav 28,44,40
mav 16,48,44
R 56,38912
mav 20,52,44
buffer_load_dwordx4 v[196:199], v89, s[36:39], s49 offen sc1
mav 24,56,44
mav 28,60,44
s_waitcnt lgkmcnt(1)
mav 16,64,48
R 60,39936
mav 20,68,48
buffer_load_dwordx4 v[200:203], v89, s[36:39], s49 offen offset:1024 sc1
mav 24,72,48
mav 28,76,48
ds_write_b128 v94, v[64:67] offset:0
mav 16,80,52
s_mov_b32 m0, 144384
R 32,40960
mav 20,84,52
ds_write_b128 v95, v[68:71] offset:0
mav 24,88,52
buffer_load_dword v84, s[36:39], 0 offen sc1 lds
mav 28,92,52
s_waitcnt lgkmcnt(3)
ds_read_b128 v[64:67], v96 offset:0
s_add_u32 m0, s50, 32768
mav 16,96,56
R 36,41984
mav 20,100,56
D 83,0
mav 24,104,56
ds_read_b128 v[68:71], v96 offset:1024
mav 28,108,56
mav 16,112,60
R 40,43008
mav 20,116,60
D 83,1024
mav 24,120,60
mav 28,124,60
s_waitcnt lgkmcnt(2)
mav 16,128,32
R 44,44032
mav 20,132,32
mav 24,136,32
mav 28,140,32
mav 16,144,36
R 48,45056
mav 20,148,36
mav 24,152,36
mav 28,156,36
s_waitcnt lgkmcnt(1)
s_add_u32 s60, s60, 1
mav 16,160,40
s_cbranch_scc0 LR0_14
mav 20,164,40
R 52,46080
v_add_u32 v85, 1, v85
mav 24,168,40
mav 28,172,40
mav 16,176,44
R 56,47104
mav 20,180,44
D 83,2048
mav 24,184,44
mav 28,188,44
s_waitcnt lgkmcnt(1)
mav 16,192,48
R 60,48128
mav 20,196,48
D 83,3072
mav 24,200,48
mav 28,204,48
mav 16,208,52
mav 20,212,52
buffer_store_dwordx4 v[64:67], v91, s[44:47], s54 offen
mav 24,216,52
mav 28,220,52
s_waitcnt lgkmcnt(0)
mav 16,224,56
mav 20,228,56
buffer_store_dwordx4 v[68:71], v92, s[44:47], s54 offen
mav 24,232,56
mav 28,236,56
mav 16,240,60
mav 20,244,60
buffer_load_dwordx4 v[124:127], v90, s[36:39], s49 offen sc1
mav 24,248,60
ds_read_b32 v76, v82 offset:0
mav 28,252,60
s_waitcnt vmcnt(17)
mvv 16,116,164
mvv 20,120,164
s_waitcnt vmcnt(16)
buffer_load_dwordx4 v[128:131], v90, s[36:39], s49 offen offset:1024 sc1
mvv 24,116,168
s_sub_u32 s54, s54, 32768
mvv 28,120,168
s_waitcnt vmcnt(16)
mvv 16,116,172
mvv 20,120,172
s_waitcnt vmcnt(15) lgkmcnt(0)
v_cmp_gt_i32 vcc, v85, v76
v_xor_b32 v80, 0x10000, v80
mvv 24,116,176
s_waitcnt vmcnt(14)
s_add_u32 s49, s49, 313344
mvv 28,120,176
s_cbranch_vccnz CR0_17
BR0_17:
s_barrier
mvv 16,116,180
R 32,0
mvv 20,120,180
s_waitcnt vmcnt(13)
buffer_load_dwordx4 v[132:135], v86, s[36:39], s49 offen sc1
mvv 24,116,184
mvv 28,120,184
s_waitcnt vmcnt(13)
mvv 16,116,188
R 36,1024
mvv 20,120,188
s_waitcnt vmcnt(12)
buffer_load_dwordx4 v[136:139], v86, s[36:39], s49 offen offset:1024 sc1
mvv 24,116,192
mvv 28,120,192
s_waitcnt vmcnt(12)
mvv 16,116,196
R 40,2048
mvv 20,120,196
s_waitcnt vmcnt(11)
buffer_load_dwordx4 v[140:143], v87, s[36:39], s49 offen sc1
mvv 24,116,200
mvv 28,120,200
s_waitcnt lgkmcnt(1)
v_mfma_f32_16x16x32_bf16 v[0:3], a[0:3], v[32:35], v[100:103]
R 44,3072
v_cvt_pk_bf16_f32 v64, v16, v17
v_mfma_f32_16x16x32_bf16 v[4:7], a[4:7], v[32:35], v[104:107]
buffer_load_dwordx4 v[144:147], v87, s[36:39], s49 offen offset:1024 sc1
v_mfma_f32_16x16x32_bf16 v[8:11], a[8:11], v[32:35], v[108:111]
v_cvt_pk_bf16_f32 v65, v18, v19
v_cvt_pk_bf16_f32 v66, v20, v21
v_mfma_f32_16x16x32_bf16 v[12:15], a[12:15], v[32:35], v[112:115]
v_cvt_pk_bf16_f32 v67, v22, v23
v_cvt_pk_bf16_f32 v68, v24, v25
mav 0,16,36
R 48,4096
v_cvt_pk_bf16_f32 v69, v26, v27
mav 4,20,36
buffer_load_dwordx4 v[148:151], v88, s[36:39], s49 offen sc1
mav 8,24,36
v_cvt_pk_bf16_f32 v70, v28, v29
v_cvt_pk_bf16_f32 v71, v30, v31
mav 12,28,36
s_waitcnt lgkmcnt(1)
mav 0,32,40
R 52,5120
mav 4,36,40
buffer_load_dwordx4 v[152:155], v88, s[36:39], s49 offen offset:1024 sc1
mav 8,40,40
mav 12,44,40
mav 0,48,44
R 56,6144
mav 4,52,44
buffer_load_dwordx4 v[156:159], v89, s[36:39], s49 offen sc1
mav 8,56,44
mav 12,60,44
s_waitcnt lgkmcnt(1)
mav 0,64,48
R 60,7168
mav 4,68,48
buffer_load_dwordx4 v[160:163], v89, s[36:39], s49 offen offset:1024 sc1
mav 8,72,48
mav 12,76,48
ds_write_b128 v94, v[64:67] offset:0
mav 0,80,52
s_mov_b32 m0, 144384
R 32,8192
mav 4,84,52
ds_write_b128 v95, v[68:71] offset:0
mav 8,88,52
buffer_load_dword v84, s[36:39], 0 offen sc1 lds
mav 12,92,52
s_waitcnt lgkmcnt(3)
ds_read_b128 v[64:67], v96 offset:0
mav 0,96,56
R 36,9216
mav 4,100,56
ds_read_b128 v[68:71], v96 offset:1024
mav 8,104,56
mav 12,108,56
mav 0,112,60
R 40,10240
mav 4,116,60
mav 8,120,60
mav 12,124,60
s_waitcnt lgkmcnt(1)
mav 0,128,32
R 44,11264
mav 4,132,32
mav 8,136,32
mav 12,140,32
mav 0,144,36
R 48,12288
mav 4,148,36
mav 8,152,36
mav 12,156,36
s_waitcnt lgkmcnt(1)
mav 0,160,40
R 52,13312
v_add_u32 v85, 1, v85
mav 4,164,40
mav 8,168,40
mav 12,172,40
mav 0,176,44
R 56,14336
mav 4,180,44
mav 8,184,44
mav 12,188,44
s_waitcnt lgkmcnt(1)
mav 0,192,48
R 60,15360
mav 4,196,48
mav 8,200,48
mav 12,204,48
mav 0,208,52
mav 4,212,52
buffer_store_dwordx4 v[64:67], v91, s[44:47], s54 offen
mav 8,216,52
mav 12,220,52
s_waitcnt lgkmcnt(0)
mav 0,224,56
mav 4,228,56
buffer_store_dwordx4 v[68:71], v92, s[44:47], s54 offen
mav 8,232,56
mav 12,236,56
mav 0,240,60
mav 4,244,60
buffer_load_dwordx4 v[164:167], v90, s[36:39], s49 offen sc1
mav 8,248,60
ds_read_b32 v76, v82 offset:0
mav 12,252,60
s_waitcnt vmcnt(13)
mvv 0,116,124
mvv 4,120,124
s_waitcnt vmcnt(12)
buffer_load_dwordx4 v[168:171], v90, s[36:39], s49 offen offset:1024 sc1
mvv 8,116,128
s_sub_u32 s54, s54, 32768
mvv 12,120,128
s_waitcnt vmcnt(12)
mvv 0,116,132
mvv 4,120,132
s_waitcnt vmcnt(11) lgkmcnt(0)
v_cmp_gt_i32 vcc, v85, v76
mvv 8,116,136
s_waitcnt vmcnt(10)
s_add_u32 s49, s49, 313344
mvv 12,120,136
s_cbranch_vccnz CR0_18
BR0_18:
s_barrier
mvv 0,116,140
R 32,32768
mvv 4,120,140
s_waitcnt vmcnt(9)
buffer_load_dwordx4 v[172:175], v86, s[36:39], s49 offen sc1
mvv 8,116,144
mvv 12,120,144
s_waitcnt vmcnt(9)
mvv 0,116,148
R 36,33792
mvv 4,120,148
s_waitcnt vmcnt(8)
buffer_load_dwordx4 v[176:179], v86, s[36:39], s49 offen offset:1024 sc1
mvv 8,116,152
mvv 12,120,152
s_waitcnt vmcnt(8)
mvv 0,116,156
R 40,34816
mvv 4,120,156
s_waitcnt vmcnt(7)
buffer_load_dwordx4 v[180:183], v87, s[36:39], s49 offen sc1
mvv 8,116,160
mvv 12,120,160
s_waitcnt lgkmcnt(1)
v_mfma_f32_16x16x32_bf16 v[16:19], a[0:3], v[32:35], v[100:103]
R 44,35840
v_cvt_pk_bf16_f32 v64, v0, v1
v_mfma_f32_16x16x32_bf16 v[20:23], a[4:7], v[32:35], v[104:107]
buffer_load_dwordx4 v[184:187], v87, s[36:39], s49 offen offset:1024 sc1
v_mfma_f32_16x16x32_bf16 v[24:27], a[8:11], v[32:35], v[108:111]
v_cvt_pk_bf16_f32 v65, v2, v3
v_cvt_pk_bf16_f32 v66, v4, v5
v_mfma_f32_16x16x32_bf16 v[28:31], a[12:15], v[32:35], v[112:115]
v_cvt_pk_bf16_f32 v67, v6, v7
v_cvt_pk_bf16_f32 v68, v8, v9
mav 16,16,36
R 48,36864
v_cvt_pk_bf16_f32 v69, v10, v11
mav 20,20,36
buffer_load_dwordx4 v[188:191], v88, s[36:39], s49 offen sc1
mav 24,24,36
ds_write_b128 v94, v[64:67] offset:0
mav 28,28,36
s_waitcnt lgkmcnt(2)
v_cvt_pk_bf16_f32 v70, v12, v13
v_cvt_pk_bf16_f32 v71, v14, v15
mav 16,32,40
R 52,37888
mav 20,36,40
buffer_load_dwordx4 v[192:195], v88, s[36:39], s49 offen offset:1024 sc1
mav 24,40,40
ds_write_b128 v95, v[68:71] offset:0
mav 28,44,40
ds_read_b128 v[64:67], v96 offset:0
mav 16,48,44
R 56,38912
mav 20,52,44
buffer_load_dwordx4 v[196:199], v89, s[36:39], s49 offen sc1
mav 24,56,44
ds_read_b128 v[68:71], v96 offset:1024
mav 28,60,44
s_waitcnt lgkmcnt(4)
mav 16,64,48
R 60,39936
mav 20,68,48
buffer_load_dwordx4 v[200:203], v89, s[36:39], s49 offen offset:1024 sc1
mav 24,72,48
s_waitcnt lgkmcnt(2)
mav 28,76,48
mav 16,80,52
R 32,40960
mav 20,84,52
buffer_store_dwordx4 v[64:67], v91, s[44:47], s54 offen
s_waitcnt lgkmcnt(2)
mav 24,88,52
mav 28,92,52
mav 16,96,56
R 36,41984
mav 20,100,56
buffer_store_dwordx4 v[68:71], v92, s[44:47], s54 offen
mav 24,104,56
mav 28,108,56
s_waitcnt lgkmcnt(1)
mav 16,112,60
R 40,43008
mav 20,116,60
mav 24,120,60
mav 28,124,60
mav 16,128,32
R 44,44032
mav 20,132,32
mav 24,136,32
mav 28,140,32
s_waitcnt lgkmcnt(1)
mav 16,144,36
R 48,45056
mav 20,148,36
mav 24,152,36
mav 28,156,36
mav 16,160,40
mav 20,164,40
R 52,46080
mav 24,168,40
mav 28,172,40
s_waitcnt lgkmcnt(1)
mav 16,176,44
R 56,47104
mav 20,180,44
mav 24,184,44
mav 28,188,44
mav 16,192,48
R 60,48128
mav 20,196,48
mav 24,200,48
mav 28,204,48
s_waitcnt lgkmcnt(1)
mav 16,208,52
mav 20,212,52
mav 24,216,52
mav 28,220,52
mav 16,224,56
mav 20,228,56
mav 24,232,56
mav 28,236,56
s_waitcnt lgkmcnt(0)
mav 16,240,60
mav 20,244,60
mav 24,248,60
mav 28,252,60
s_waitcnt vmcnt(11)
mvv 16,116,164
mvv 20,120,164
s_waitcnt vmcnt(10)
mvv 24,116,168
s_sub_u32 s54, s54, 32768
mvv 28,120,168
s_waitcnt vmcnt(9)
mvv 16,116,172
mvv 20,120,172
s_waitcnt vmcnt(8)
mvv 24,116,176
mvv 28,120,176
s_waitcnt vmcnt(7)
mvv 16,116,180
mvv 20,120,180
s_waitcnt vmcnt(6)
mvv 24,116,184
mvv 28,120,184
s_waitcnt vmcnt(5)
mvv 16,116,188
mvv 20,120,188
s_waitcnt vmcnt(4)
mvv 24,116,192
mvv 28,120,192
s_waitcnt vmcnt(3)
mvv 16,116,196
mvv 20,120,196
s_waitcnt vmcnt(2)
mvv 24,116,200
mvv 28,120,200
s_nop 3
v_cvt_pk_bf16_f32 v64, v16, v17
v_cvt_pk_bf16_f32 v65, v18, v19
LsR5:
s_cbranch_execz LsR9
v_cvt_pk_bf16_f32 v66, v20, v21
v_cvt_pk_bf16_f32 v67, v22, v23
ds_write_b128 v94, v[64:67] offset:0
v_cvt_pk_bf16_f32 v68, v24, v25
v_cvt_pk_bf16_f32 v69, v26, v27
v_cvt_pk_bf16_f32 v70, v28, v29
v_cvt_pk_bf16_f32 v71, v30, v31
ds_write_b128 v95, v[68:71] offset:0
ds_read_b128 v[64:67], v96 offset:0
ds_read_b128 v[68:71], v96 offset:1024
s_waitcnt lgkmcnt(0)
buffer_store_dwordx4 v[64:67], v91, s[44:47], s54 offen
buffer_store_dwordx4 v[68:71], v92, s[44:47], s54 offen
s_endpgm
CR0_1:
s_cmp_eq_u32 s61, 0
s_cbranch_scc1 BR0_1
s_sub_u32 s61, s61, 1
s_sleep 8
buffer_load_dwordx2 v[76:77], v84, s[36:39], 0 offen sc1
s_waitcnt vmcnt(0)
ds_write_b32 v82, v76
v_sub_u32 v76, v76, v85
v_xor_b32 v77, v77, v73
v_cmp_lt_u64 vcc, s[62:63], v[76:77]
s_waitcnt lgkmcnt(0)
s_cbranch_vccnz CR0_1
s_branch BR0_1
CR0_2:
s_mov_b32 s58, m0
s_mov_b32 m0, 144384
DR0_2:
s_cmp_eq_u32 s61, 0
s_cbranch_scc1 ER0_2
s_sub_u32 s61, s61, 1
buffer_load_dword v84, s[36:39], 0 offen sc1 lds
s_sleep 1
ds_read_b32 v76, v82
s_waitcnt lgkmcnt(0)
v_cmp_gt_i32 vcc, v85, v76
s_cbranch_vccnz DR0_2
ER0_2:
s_mov_b32 m0, s58
s_branch BR0_2
CR0_3:
s_mov_b32 s58, m0
s_mov_b32 m0, 144384
DR0_3:
s_cmp_eq_u32 s61, 0
s_cbranch_scc1 ER0_3
s_sub_u32 s61, s61, 1
buffer_load_dword v84, s[36:39], 0 offen sc1 lds
s_sleep 1
ds_read_b32 v76, v82
s_waitcnt lgkmcnt(0)
v_cmp_gt_i32 vcc, v85, v76
s_cbranch_vccnz DR0_3
ER0_3:
s_mov_b32 m0, s58
s_branch BR0_3
CR0_4:
s_mov_b32 s58, m0
s_mov_b32 m0, 144384
DR0_4:
s_cmp_eq_u32 s61, 0
s_cbranch_scc1 ER0_4
s_sub_u32 s61, s61, 1
buffer_load_dword v84, s[36:39], 0 offen sc1 lds
s_sleep 1
ds_read_b32 v76, v82
s_waitcnt lgkmcnt(0)
v_cmp_gt_i32 vcc, v85, v76
s_cbranch_vccnz DR0_4
ER0_4:
s_mov_b32 m0, s58
s_branch BR0_4
CR0_5:
s_mov_b32 s58, m0
s_mov_b32 m0, 144384
DR0_5:
s_cmp_eq_u32 s61, 0
s_cbranch_scc1 ER0_5
s_sub_u32 s61, s61, 1
buffer_load_dword v84, s[36:39], 0 offen sc1 lds
s_sleep 1
ds_read_b32 v76, v82
s_waitcnt lgkmcnt(0)
v_cmp_gt_i32 vcc, v85, v76
s_cbranch_vccnz DR0_5
ER0_5:
s_mov_b32 m0, s58
s_branch BR0_5
CR0_6:
s_mov_b32 s58, m0
s_mov_b32 m0, 144384
DR0_6:
s_cmp_eq_u32 s61, 0
s_cbranch_scc1 ER0_6
s_sub_u32 s61, s61, 1
buffer_load_dword v84, s[36:39], 0 offen sc1 lds
s_sleep 1
ds_read_b32 v76, v82
s_waitcnt lgkmcnt(0)
v_cmp_gt_i32 vcc, v85, v76
s_cbranch_vccnz DR0_6
ER0_6:
s_mov_b32 m0, s58
s_branch BR0_6
CR0_8:
s_mov_b32 s58, m0
s_mov_b32 m0, 144384
DR0_8:
s_cmp_eq_u32 s61, 0
s_cbranch_scc1 ER0_8
s_sub_u32 s61, s61, 1
buffer_load_dword v84, s[36:39], 0 offen sc1 lds
s_sleep 1
ds_read_b32 v76, v82
s_waitcnt lgkmcnt(0)
v_cmp_gt_i32 vcc, v85, v76
s_cbranch_vccnz DR0_8
ER0_8:
s_mov_b32 m0, s58
s_branch BR0_8
CR0_9:
s_mov_b32 s58, m0
s_mov_b32 m0, 144384
DR0_9:
s_cmp_eq_u32 s61, 0
s_cbranch_scc1 ER0_9
s_sub_u32 s61, s61, 1
buffer_load_dword v84, s[36:39], 0 offen sc1 lds
s_sleep 1
ds_read_b32 v76, v82
s_waitcnt lgkmcnt(0)
v_cmp_gt_i32 vcc, v85, v76
s_cbranch_vccnz DR0_9
ER0_9:
s_mov_b32 m0, s58
s_branch BR0_9
CR0_10:
s_mov_b32 s58, m0
s_mov_b32 m0, 144384
DR0_10:
s_cmp_eq_u32 s61, 0
s_cbranch_scc1 ER0_10
s_sub_u32 s61, s61, 1
buffer_load_dword v84, s[36:39], 0 offen sc1 lds
s_sleep 1
ds_read_b32 v76, v82
s_waitcnt lgkmcnt(0)
v_cmp_gt_i32 vcc, v85, v76
s_cbranch_vccnz DR0_10
ER0_10:
s_mov_b32 m0, s58
s_branch BR0_10
CR0_11:
s_mov_b32 s58, m0
s_mov_b32 m0, 144384
DR0_11:
s_cmp_eq_u32 s61, 0
s_cbranch_scc1 ER0_11
s_sub_u32 s61, s61, 1
buffer_load_dword v84, s[36:39], 0 offen sc1 lds
s_sleep 1
ds_read_b32 v76, v82
s_waitcnt lgkmcnt(0)
v_cmp_gt_i32 vcc, v85, v76
s_cbranch_vccnz DR0_11
ER0_11:
s_mov_b32 m0, s58
s_branch BR0_11
CR0_12:
s_mov_b32 s58, m0
s_mov_b32 m0, 144384
DR0_12:
s_cmp_eq_u32 s61, 0
s_cbranch_scc1 ER0_12
s_sub_u32 s61, s61, 1
buffer_load_dword v84, s[36:39], 0 offen sc1 lds
s_sleep 1
ds_read_b32 v76, v82
s_waitcnt lgkmcnt(0)
v_cmp_gt_i32 vcc, v85, v76
s_cbranch_vccnz DR0_12
ER0_12:
s_mov_b32 m0, s58
s_branch BR0_12
CR0_13:
s_mov_b32 s58, m0
s_mov_b32 m0, 144384
DR0_13:
s_cmp_eq_u32 s61, 0
s_cbranch_scc1 ER0_13
s_sub_u32 s61, s61, 1
buffer_load_dword v84, s[36:39], 0 offen sc1 lds
s_sleep 1
ds_read_b32 v76, v82
s_waitcnt lgkmcnt(0)
v_cmp_gt_i32 vcc, v85, v76
s_cbranch_vccnz DR0_13
ER0_13:
s_mov_b32 m0, s58
s_branch BR0_13
CR0_15:
s_mov_b32 s58, m0
s_mov_b32 m0, 144384
DR0_15:
s_cmp_eq_u32 s61, 0
s_cbranch_scc1 ER0_15
s_sub_u32 s61, s61, 1
buffer_load_dword v84, s[36:39], 0 offen sc1 lds
s_sleep 1
ds_read_b32 v76, v82
s_waitcnt lgkmcnt(0)
v_cmp_gt_i32 vcc, v85, v76
s_cbranch_vccnz DR0_15
ER0_15:
s_mov_b32 m0, s58
s_branch BR0_15
CR0_16:
s_mov_b32 s58, m0
s_mov_b32 m0, 144384
DR0_16:
s_cmp_eq_u32 s61, 0
s_cbranch_scc1 ER0_16
s_sub_u32 s61, s61, 1
buffer_load_dword v84, s[36:39], 0 offen sc1 lds
s_sleep 1
ds_read_b32 v76, v82
s_waitcnt lgkmcnt(0)
v_cmp_gt_i32 vcc, v85, v76
s_cbranch_vccnz DR0_16
ER0_16:
s_mov_b32 m0, s58
s_branch BR0_16
CR0_17:
s_mov_b32 s58, m0
s_mov_b32 m0, 144384
DR0_17:
s_cmp_eq_u32 s61, 0
s_cbranch_scc1 ER0_17
s_sub_u32 s61, s61, 1
buffer_load_dword v84, s[36:39], 0 offen sc1 lds
s_sleep 1
ds_read_b32 v76, v82
s_waitcnt lgkmcnt(0)
v_cmp_gt_i32 vcc, v85, v76
s_cbranch_vccnz DR0_17
ER0_17:
s_mov_b32 m0, s58
s_branch BR0_17
CR0_18:
s_mov_b32 s58, m0
s_mov_b32 m0, 144384
DR0_18:
s_cmp_eq_u32 s61, 0
s_cbranch_scc1 ER0_18
s_sub_u32 s61, s61, 1
buffer_load_dword v84, s[36:39], 0 offen sc1 lds
s_sleep 1
ds_read_b32 v76, v82
s_waitcnt lgkmcnt(0)
v_cmp_gt_i32 vcc, v85, v76
s_cbranch_vccnz DR0_18
ER0_18:
s_mov_b32 m0, s58
s_branch BR0_18
LvR1:
LsR6:
s_cbranch_execz LsR10
s_add_u32 s49, s49, 313344
s_barrier
s_add_u32 m0, s50, 0
s_nop 0
D 83,0
D 83,1024
D 83,2048
D 83,3072
s_add_u32 s49, s49, 313344
s_barrier
s_add_u32 m0, s50, 32768
s_nop 0
D 83,0
D 83,1024
D 83,2048
D 83,3072
buffer_load_dwordx4 v[124:127], v90, s[36:39], s49 offen sc1
buffer_load_dwordx4 v[128:131], v90, s[36:39], s49 offen offset:1024 sc1
s_xor_b32 s50, s50, 0x10000
s_waitcnt vmcnt(6)
s_add_u32 s49, s49, 313344
s_barrier
R 32,0
buffer_load_dwordx4 v[132:135], v86, s[36:39], s49 offen sc1
R 36,1024
buffer_load_dwordx4 v[136:139], v86, s[36:39], s49 offen offset:1024 sc1
R 40,2048
buffer_load_dwordx4 v[140:143], v87, s[36:39], s49 offen sc1
s_waitcnt lgkmcnt(1)
v_mfma_f32_16x16x32_bf16 v[0:3], a[0:3], v[32:35], v[100:103]
R 44,3072
v_mfma_f32_16x16x32_bf16 v[4:7], a[4:7], v[32:35], v[104:107]
v_mfma_f32_16x16x32_bf16 v[8:11], a[8:11], v[32:35], v[108:111]
v_mfma_f32_16x16x32_bf16 v[12:15], a[12:15], v[32:35], v[112:115]
mav 0,16,36
buffer_load_dwordx4 v[144:147], v87, s[36:39], s49 offen offset:1024 sc1
mav 4,20,36
R 48,4096
mav 8,24,36
mav 12,28,36
s_waitcnt lgkmcnt(1)
mav 0,32,40
buffer_load_dwordx4 v[148:151], v88, s[36:39], s49 offen sc1
mav 4,36,40
R 52,5120
mav 8,40,40
mav 12,44,40
mav 0,48,44
buffer_load_dwordx4 v[152:155], v88, s[36:39], s49 offen offset:1024 sc1
mav 4,52,44
R 56,6144
mav 8,56,44
mav 12,60,44
s_waitcnt lgkmcnt(1)
mav 0,64,48
buffer_load_dwordx4 v[156:159], v89, s[36:39], s49 offen sc1
mav 4,68,48
R 60,7168
mav 8,72,48
mav 12,76,48
mav 0,80,52
buffer_load_dwordx4 v[160:163], v89, s[36:39], s49 offen offset:1024 sc1
mav 4,84,52
R 32,8192
mav 8,88,52
s_add_u32 m0, s50, 0
mav 12,92,52
s_waitcnt lgkmcnt(1)
mav 0,96,56
D 83,0
mav 4,100,56
R 36,9216
mav 8,104,56
mav 12,108,56
mav 0,112,60
D 83,1024
mav 4,116,60
R 40,10240
mav 8,120,60
mav 12,124,60
s_waitcnt lgkmcnt(1)
mav 0,128,32
R 44,11264
mav 4,132,32
mav 8,136,32
mav 12,140,32
mav 0,144,36
R 48,12288
mav 4,148,36
mav 8,152,36
mav 12,156,36
s_waitcnt lgkmcnt(1)
mav 0,160,40
R 52,13312
mav 4,164,40
mav 8,168,40
mav 12,172,40
mav 0,176,44
D 83,2048
mav 4,180,44
R 56,14336
mav 8,184,44
mav 12,188,44
s_waitcnt lgkmcnt(1)
mav 0,192,48
D 83,3072
mav 4,196,48
R 60,15360
mav 8,200,48
mav 12,204,48
mav 0,208,52
mav 4,212,52
mav 8,216,52
mav 12,220,52
s_waitcnt lgkmcnt(0)
mav 0,224,56
mav 4,228,56
mav 8,232,56
mav 12,236,56
mav 0,240,60
buffer_load_dwordx4 v[164:167], v90, s[36:39], s49 offen sc1
mav 4,244,60
mav 8,248,60
mav 12,252,60
s_waitcnt vmcnt(14)
mvv 0,116,124
buffer_load_dwordx4 v[168:171], v90, s[36:39], s49 offen offset:1024 sc1
mvv 4,120,124
s_waitcnt vmcnt(14)
mvv 8,116,128
mvv 12,120,128
s_waitcnt vmcnt(13)
mvv 0,116,132
mvv 4,120,132
s_waitcnt vmcnt(12)
mvv 8,116,136
s_waitcnt vmcnt(11)
s_add_u32 s49, s49, 313344
mvv 12,120,136
s_barrier
mvv 0,116,140
R 32,32768
mvv 4,120,140
s_waitcnt vmcnt(10)
mvv 8,116,144
mvv 12,120,144
s_waitcnt vmcnt(9)
mvv 0,116,148
buffer_load_dwordx4 v[172:175], v86, s[36:39], s49 offen sc1
mvv 4,120,148
s_waitcnt vmcnt(9)
R 36,33792
mvv 8,116,152
mvv 12,120,152
s_waitcnt vmcnt(8)
mvv 0,116,156
buffer_load_dwordx4 v[176:179], v86, s[36:39], s49 offen offset:1024 sc1
mvv 4,120,156
s_waitcnt vmcnt(8)
R 40,34816
mvv 8,116,160
mvv 12,120,160
s_waitcnt lgkmcnt(1)
v_mfma_f32_16x16x32_bf16 v[16:19], a[0:3], v[32:35], v[100:103]
buffer_load_dwordx4 v[180:183], v87, s[36:39], s49 offen sc1
v_mfma_f32_16x16x32_bf16 v[20:23], a[4:7], v[32:35], v[104:107]
R 44,35840
v_cvt_pk_bf16_f32 v64, v0, v1
v_mfma_f32_16x16x32_bf16 v[24:27], a[8:11], v[32:35], v[108:111]
v_cvt_pk_bf16_f32 v65, v2, v3
v_cvt_pk_bf16_f32 v66, v4, v5
v_mfma_f32_16x16x32_bf16 v[28:31], a[12:15], v[32:35], v[112:115]
v_cvt_pk_bf16_f32 v67, v6, v7
v_cvt_pk_bf16_f32 v68, v8, v9
mav 16,16,36
buffer_load_dwordx4 v[184:187], v87, s[36:39], s49 offen offset:1024 sc1
mav 20,20,36
R 48,36864
v_cvt_pk_bf16_f32 v69, v10, v11
mav 24,24,36
v_cvt_pk_bf16_f32 v70, v12, v13
v_cvt_pk_bf16_f32 v71, v14, v15
mav 28,28,36
s_waitcnt lgkmcnt(1)
v_mul_f32 v75, v0, v0
v_add_f32 v74, v1, v0
mav 16,32,40
buffer_load_dwordx4 v[188:191], v88, s[36:39], s49 offen sc1
mav 20,36,40
R 52,37888
v_fmac_f32 v75, v1, v1
mav 24,40,40
v_add_f32 v74, v2, v74
v_fmac_f32 v75, v2, v2
mav 28,44,40
v_add_f32 v74, v3, v74
v_fmac_f32 v75, v3, v3
mav 16,48,44
buffer_load_dwordx4 v[192:195], v88, s[36:39], s49 offen offset:1024 sc1
mav 20,52,44
R 56,38912
v_add_f32 v74, v4, v74
mav 24,56,44
v_fmac_f32 v75, v4, v4
v_add_f32 v74, v5, v74
mav 28,60,44
s_waitcnt lgkmcnt(1)
v_fmac_f32 v75, v5, v5
v_add_f32 v74, v6, v74
mav 16,64,48
buffer_load_dwordx4 v[196:199], v89, s[36:39], s49 offen sc1
mav 20,68,48
R 60,39936
v_fmac_f32 v75, v6, v6
mav 24,72,48
v_add_f32 v74, v7, v74
v_fmac_f32 v75, v7, v7
mav 28,76,48
v_add_f32 v74, v8, v74
v_fmac_f32 v75, v8, v8
mav 16,80,52
buffer_load_dwordx4 v[200:203], v89, s[36:39], s49 offen offset:1024 sc1
mav 20,84,52
R 32,40960
v_add_f32 v74, v9, v74
mav 24,88,52
s_add_u32 m0, s50, 32768
v_fmac_f32 v75, v9, v9
v_add_f32 v74, v10, v74
mav 28,92,52
s_waitcnt lgkmcnt(1)
ds_write_b128 v94, v[64:67] offset:0
mav 16,96,56
D 83,0
mav 20,100,56
R 36,41984
v_fmac_f32 v75, v10, v10
mav 24,104,56
ds_write_b128 v95, v[68:71] offset:0
mav 28,108,56
ds_read_b128 v[64:67], v96 offset:0
v_add_f32 v74, v11, v74
mav 16,112,60
D 83,1024
mav 20,116,60
R 40,43008
v_fmac_f32 v75, v11, v11
mav 24,120,60
ds_read_b128 v[68:71], v96 offset:1024
v_add_f32 v74, v12, v74
mav 28,124,60
s_waitcnt lgkmcnt(4)
v_fmac_f32 v75, v12, v12
v_add_f32 v74, v13, v74
mav 16,128,32
R 44,44032
v_fmac_f32 v75, v13, v13
mav 20,132,32
v_add_f32 v74, v14, v74
v_fmac_f32 v75, v14, v14
mav 24,136,32
v_add_f32 v74, v15, v74
v_fmac_f32 v75, v15, v15
mav 28,140,32
mav 16,144,36
R 48,45056
mav 20,148,36
v_permlane32_swap_b32 v74, v75
mav 24,152,36
v_add_f32 v74, v74, v75
mav 28,156,36
s_waitcnt lgkmcnt(1)
mav 16,160,40
mav 20,164,40
R 52,46080
mav 24,168,40
mav 28,172,40
mav 16,176,44
D 83,2048
mav 20,180,44
R 56,47104
mav 24,184,44
mav 28,188,44
s_waitcnt lgkmcnt(1)
mav 16,192,48
D 83,3072
mav 20,196,48
R 60,48128
mav 24,200,48
mav 28,204,48
mav 16,208,52
buffer_store_dwordx4 v[64:67], v91, s[36:39], s54 offen sc1
mav 20,212,52
mav 24,216,52
mav 28,220,52
s_waitcnt lgkmcnt(0)
mav 16,224,56
buffer_store_dwordx4 v[68:71], v92, s[36:39], s54 offen sc1
mav 20,228,56
mav 24,232,56
mav 28,236,56
mav 16,240,60
buffer_load_dwordx4 v[124:127], v90, s[36:39], s49 offen sc1
mav 20,244,60
mav 24,248,60
mav 28,252,60
s_waitcnt vmcnt(16)
mvv 16,116,164
buffer_load_dwordx4 v[128:131], v90, s[36:39], s49 offen offset:1024 sc1
mvv 20,120,164
s_waitcnt vmcnt(16)
mvv 24,116,168
s_xor_b32 s50, s50, 0x10000
s_sub_u32 s54, s54, 32768
mvv 28,120,168
s_waitcnt vmcnt(15)
mvv 16,116,172
mvv 20,120,172
s_waitcnt vmcnt(14)
v_xor_b32 v80, 0x10000, v80
mvv 24,116,176
s_waitcnt vmcnt(13)
s_add_u32 s49, s49, 313344
mvv 28,120,176
s_barrier
mvv 16,116,180
R 32,0
mvv 20,120,180
s_waitcnt vmcnt(12)
mvv 24,116,184
mvv 28,120,184
s_waitcnt vmcnt(11)
mvv 16,116,188
buffer_load_dwordx4 v[132:135], v86, s[36:39], s49 offen sc1
mvv 20,120,188
s_waitcnt vmcnt(11)
R 36,1024
mvv 24,116,192
mvv 28,120,192
s_waitcnt vmcnt(10)
mvv 16,116,196
buffer_load_dwordx4 v[136:139], v86, s[36:39], s49 offen offset:1024 sc1
mvv 20,120,196
s_waitcnt vmcnt(10)
R 40,2048
mvv 24,116,200
mvv 28,120,200
s_waitcnt lgkmcnt(1)
v_mfma_f32_16x16x32_bf16 v[0:3], a[0:3], v[32:35], v[100:103]
buffer_load_dwordx4 v[140:143], v87, s[36:39], s49 offen sc1
v_mfma_f32_16x16x32_bf16 v[4:7], a[4:7], v[32:35], v[104:107]
R 44,3072
v_cvt_pk_bf16_f32 v64, v16, v17
v_mfma_f32_16x16x32_bf16 v[8:11], a[8:11], v[32:35], v[108:111]
v_cvt_pk_bf16_f32 v65, v18, v19
v_cvt_pk_bf16_f32 v66, v20, v21
v_mfma_f32_16x16x32_bf16 v[12:15], a[12:15], v[32:35], v[112:115]
v_cvt_pk_bf16_f32 v67, v22, v23
v_cvt_pk_bf16_f32 v68, v24, v25
mav 0,16,36
buffer_load_dwordx4 v[144:147], v87, s[36:39], s49 offen offset:1024 sc1
mav 4,20,36
R 48,4096
v_cvt_pk_bf16_f32 v69, v26, v27
mav 8,24,36
v_cvt_pk_bf16_f32 v70, v28, v29
v_cvt_pk_bf16_f32 v71, v30, v31
mav 12,28,36
s_waitcnt lgkmcnt(1)
v_mul_f32 v75, v16, v16
v_fmac_f32 v75, v17, v17
mav 0,32,40
buffer_load_dwordx4 v[148:151], v88, s[36:39], s49 offen sc1
mav 4,36,40
R 52,5120
v_fmac_f32 v75, v18, v18
mav 8,40,40
v_fmac_f32 v75, v19, v19
v_fmac_f32 v75, v20, v20
mav 12,44,40
v_fmac_f32 v75, v21, v21
v_fmac_f32 v75, v22, v22
mav 0,48,44
buffer_load_dwordx4 v[152:155], v88, s[36:39], s49 offen offset:1024 sc1
mav 4,52,44
R 56,6144
v_fmac_f32 v75, v23, v23
mav 8,56,44
v_fmac_f32 v75, v24, v24
v_fmac_f32 v75, v25, v25
mav 12,60,44
s_waitcnt lgkmcnt(1)
v_fmac_f32 v75, v26, v26
v_fmac_f32 v75, v27, v27
mav 0,64,48
buffer_load_dwordx4 v[156:159], v89, s[36:39], s49 offen sc1
mav 4,68,48
R 60,7168
v_fmac_f32 v75, v28, v28
mav 8,72,48
v_fmac_f32 v75, v29, v29
v_fmac_f32 v75, v30, v30
mav 12,76,48
v_fmac_f32 v75, v31, v31
mav 0,80,52
buffer_load_dwordx4 v[160:163], v89, s[36:39], s49 offen offset:1024 sc1
mav 4,84,52
R 32,8192
mav 8,88,52
ds_write_b32 v97, v74 offset:0
mav 12,92,52
s_waitcnt lgkmcnt(2)
ds_write_b128 v94, v[64:67] offset:0
mav 0,96,56
s_add_u32 m0, s50, 0
R 36,9216
v_add_f32 v74, v17, v16
mav 4,100,56
ds_write_b128 v95, v[68:71] offset:0
mav 8,104,56
ds_read_b128 v[64:67], v96 offset:0
LsR7:
s_cbranch_execz LsR11
v_add_f32 v74, v18, v74
mav 12,108,56
ds_read_b128 v[68:71], v96 offset:1024
v_add_f32 v74, v19, v74
mav 0,112,60
D 83,0
mav 4,116,60
R 40,10240
v_add_f32 v74, v20, v74
mav 8,120,60
v_add_f32 v74, v21, v74
v_add_f32 v74, v22, v74
mav 12,124,60
s_waitcnt lgkmcnt(3)
v_add_f32 v74, v23, v74
v_add_f32 v74, v24, v74
mav 0,128,32
D 83,1024
mav 4,132,32
R 44,11264
v_add_f32 v74, v25, v74
mav 8,136,32
v_add_f32 v74, v26, v74
v_add_f32 v74, v27, v74
mav 12,140,32
v_add_f32 v74, v28, v74
v_add_f32 v74, v29, v74
mav 0,144,36
R 48,12288
v_add_f32 v74, v30, v74
mav 4,148,36
v_add_f32 v74, v31, v74
mav 8,152,36
mav 12,156,36
s_waitcnt lgkmcnt(1)
v_permlane32_swap_b32 v74, v75
mav 0,160,40
R 52,13312
v_add_f32 v74, v74, v75
mav 4,164,40
v_add_u32 v72, 1, v72
mav 8,168,40
mav 12,172,40
mav 0,176,44
D 83,2048
mav 4,180,44
R 56,14336
mav 8,184,44
mav 12,188,44
s_waitcnt lgkmcnt(1)
mav 0,192,48
D 83,3072
mav 4,196,48
R 60,15360
mav 8,200,48
mav 12,204,48
mav 0,208,52
buffer_store_dwordx4 v[64:67], v91, s[36:39], s54 offen sc1
mav 4,212,52
mav 8,216,52
mav 12,220,52
s_waitcnt lgkmcnt(0)
mav 0,224,56
buffer_store_dwordx4 v[68:71], v92, s[36:39], s54 offen sc1
mav 4,228,56
mav 8,232,56
mav 12,236,56
mav 0,240,60
buffer_load_dwordx4 v[164:167], v90, s[36:39], s49 offen sc1
mav 4,244,60
mav 8,248,60
mav 12,252,60
s_waitcnt vmcnt(16)
mvv 0,116,124
buffer_load_dwordx4 v[168:171], v90, s[36:39], s49 offen offset:1024 sc1
mvv 4,120,124
s_waitcnt vmcnt(16)
buffer_store_dword v72, v81, s[36:39], s52 offen sc1
mvv 8,116,128
s_sub_u32 s54, s54, 32768
mvv 12,120,128
s_waitcnt vmcnt(16)
mvv 0,116,132
mvv 4,120,132
s_waitcnt vmcnt(15)
mvv 8,116,136
s_waitcnt vmcnt(14)
s_add_u32 s49, s49, 313344
mvv 12,120,136
s_barrier
mvv 0,116,140
R 32,32768
mvv 4,120,140
s_waitcnt vmcnt(13)
ds_read_b128 v[204:207], v98 offset:0
mvv 8,116,144
mvv 12,120,144
s_waitcnt vmcnt(12)
mvv 0,116,148
buffer_load_dwordx4 v[172:175], v86, s[36:39], s49 offen sc1
mvv 4,120,148
s_waitcnt vmcnt(12)
R 36,33792
mvv 8,116,152
mvv 12,120,152
s_waitcnt vmcnt(11)
mvv 0,116,156
s_waitcnt lgkmcnt(1)
buffer_load_dwordx4 v[176:179], v86, s[36:39], s49 offen offset:1024 sc1
mvv 4,120,156
s_waitcnt vmcnt(11)
R 40,34816
v_add_f32 v204, v204, v205
mvv 8,116,160
v_add_f32 v206, v206, v207
v_add_f32 v204, v204, v206
mvv 12,120,160
v_mfma_f32_16x16x32_bf16 v[16:19], a[0:3], v[32:35], v[100:103]
buffer_load_dwordx4 v[180:183], v87, s[36:39], s49 offen sc1
v_mfma_f32_16x16x32_bf16 v[20:23], a[4:7], v[32:35], v[104:107]
R 44,35840
v_add_f32_dpp v205, v204, v204 quad_perm:[1,0,3,2] row_mask:0xf bank_mask:0xf
v_mfma_f32_16x16x32_bf16 v[24:27], a[8:11], v[32:35], v[108:111]
buffer_store_dword v205, v99, s[36:39], s49 offen sc1
v_mfma_f32_16x16x32_bf16 v[28:31], a[12:15], v[32:35], v[112:115]
s_waitcnt lgkmcnt(1)
v_cvt_pk_bf16_f32 v64, v0, v1
v_cvt_pk_bf16_f32 v65, v2, v3
mav 16,16,36
buffer_load_dwordx4 v[184:187], v87, s[36:39], s49 offen offset:1024 sc1
mav 20,20,36
R 48,36864
v_cvt_pk_bf16_f32 v66, v4, v5
mav 24,24,36
v_cvt_pk_bf16_f32 v67, v6, v7
v_cvt_pk_bf16_f32 v68, v8, v9
mav 28,28,36
v_cvt_pk_bf16_f32 v69, v10, v11
v_cvt_pk_bf16_f32 v70, v12, v13
mav 16,32,40
buffer_load_dwordx4 v[188:191], v88, s[36:39], s49 offen sc1
mav 20,36,40
R 52,37888
v_cvt_pk_bf16_f32 v71, v14, v15
mav 24,40,40
v_mul_f32 v75, v0, v0
v_fmac_f32 v75, v1, v1
mav 28,44,40
s_waitcnt lgkmcnt(1)
v_fmac_f32 v75, v2, v2
v_fmac_f32 v75, v3, v3
mav 16,48,44
buffer_load_dwordx4 v[192:195], v88, s[36:39], s49 offen offset:1024 sc1
mav 20,52,44
R 56,38912
v_fmac_f32 v75, v4, v4
mav 24,56,44
v_fmac_f32 v75, v5, v5
v_fmac_f32 v75, v6, v6
mav 28,60,44
v_fmac_f32 v75, v7, v7
v_fmac_f32 v75, v8, v8
mav 16,64,48
buffer_load_dwordx4 v[196:199], v89, s[36:39], s49 offen sc1
)"
R"(
mav 20,68,48
R 60,39936
v_fmac_f32 v75, v9, v9
mav 24,72,48
v_fmac_f32 v75, v10, v10
v_fmac_f32 v75, v11, v11
mav 28,76,48
s_waitcnt lgkmcnt(1)
v_fmac_f32 v75, v12, v12
v_fmac_f32 v75, v13, v13
mav 16,80,52
buffer_load_dwordx4 v[200:203], v89, s[36:39], s49 offen offset:1024 sc1
mav 20,84,52
R 32,40960
v_fmac_f32 v75, v14, v14
mav 24,88,52
ds_write_b32 v97, v74 offset:1024
mav 28,92,52
ds_write_b128 v94, v[64:67] offset:0
mav 16,96,56
s_add_u32 m0, s50, 32768
R 36,41984
v_add_f32 v74, v1, v0
mav 20,100,56
ds_write_b128 v95, v[68:71] offset:0
mav 24,104,56
ds_read_b128 v[64:67], v96 offset:0
v_add_f32 v74, v2, v74
mav 28,108,56
s_waitcnt lgkmcnt(5)
ds_read_b128 v[68:71], v96 offset:1024
v_add_f32 v74, v3, v74
mav 16,112,60
D 83,0
mav 20,116,60
R 40,43008
v_add_f32 v74, v4, v74
mav 24,120,60
v_add_f32 v74, v5, v74
v_add_f32 v74, v6, v74
mav 28,124,60
v_add_f32 v74, v7, v74
v_add_f32 v74, v8, v74
mav 16,128,32
D 83,1024
mav 20,132,32
R 44,44032
v_add_f32 v74, v9, v74
mav 24,136,32
v_add_f32 v74, v10, v74
v_add_f32 v74, v11, v74
mav 28,140,32
s_waitcnt lgkmcnt(1)
v_add_f32 v74, v12, v74
v_add_f32 v74, v13, v74
mav 16,144,36
R 48,45056
v_add_f32 v74, v14, v74
mav 20,148,36
v_add_f32 v74, v15, v74
v_fmac_f32 v75, v15, v15
mav 24,152,36
mav 28,156,36
v_permlane32_swap_b32 v74, v75
mav 16,160,40
s_mov_b32 s60, -14
.p2align 6
LR1_1:
mav 20,164,40
R 52,46080
v_add_f32 v74, v74, v75
mav 24,168,40
v_add_u32 v72, 1, v72
mav 28,172,40
s_waitcnt lgkmcnt(1)
mav 16,176,44
D 83,2048
mav 20,180,44
R 56,47104
mav 24,184,44
mav 28,188,44
mav 16,192,48
D 83,3072
mav 20,196,48
R 60,48128
mav 24,200,48
mav 28,204,48
s_waitcnt lgkmcnt(1)
mav 16,208,52
buffer_store_dwordx4 v[64:67], v91, s[36:39], s54 offen sc1
mav 20,212,52
mav 24,216,52
mav 28,220,52
mav 16,224,56
buffer_store_dwordx4 v[68:71], v92, s[36:39], s54 offen sc1
mav 20,228,56
mav 24,232,56
mav 28,236,56
s_waitcnt lgkmcnt(0)
mav 16,240,60
buffer_load_dwordx4 v[124:127], v90, s[36:39], s49 offen sc1
mav 20,244,60
mav 24,248,60
mav 28,252,60
s_waitcnt vmcnt(18)
mvv 16,116,164
s_waitcnt vmcnt(12)
buffer_load_dwordx4 v[128:131], v90, s[36:39], s49 offen offset:1024 sc1
mvv 20,120,164
buffer_store_dword v72, v81, s[36:39], s52 offen sc1
mvv 24,116,168
s_xor_b32 s50, s50, 0x10000
s_sub_u32 s54, s54, 32768
mvv 28,120,168
mvv 16,116,172
mvv 20,120,172
v_xor_b32 v80, 0x10000, v80
mvv 24,116,176
s_add_u32 s49, s49, 313344
mvv 28,120,176
s_barrier
mvv 16,116,180
R 32,0
mvv 20,120,180
s_waitcnt vmcnt(13)
ds_read_b128 v[204:207], v98 offset:1024
mvv 24,116,184
mvv 28,120,184
s_waitcnt vmcnt(12)
mvv 16,116,188
buffer_load_dwordx4 v[132:135], v86, s[36:39], s49 offen sc1
mvv 20,120,188
s_waitcnt vmcnt(12)
R 36,1024
mvv 24,116,192
mvv 28,120,192
s_waitcnt vmcnt(11)
mvv 16,116,196
s_waitcnt lgkmcnt(1)
buffer_load_dwordx4 v[136:139], v86, s[36:39], s49 offen offset:1024 sc1
mvv 20,120,196
s_waitcnt vmcnt(11)
R 40,2048
v_add_f32 v204, v204, v205
mvv 24,116,200
v_add_f32 v206, v206, v207
v_add_f32 v204, v204, v206
mvv 28,120,200
v_mfma_f32_16x16x32_bf16 v[0:3], a[0:3], v[32:35], v[100:103]
buffer_load_dwordx4 v[140:143], v87, s[36:39], s49 offen sc1
v_mfma_f32_16x16x32_bf16 v[4:7], a[4:7], v[32:35], v[104:107]
R 44,3072
v_add_f32_dpp v205, v204, v204 quad_perm:[1,0,3,2] row_mask:0xf bank_mask:0xf
v_mfma_f32_16x16x32_bf16 v[8:11], a[8:11], v[32:35], v[108:111]
buffer_store_dword v205, v99, s[36:39], s49 offen sc1
v_mfma_f32_16x16x32_bf16 v[12:15], a[12:15], v[32:35], v[112:115]
s_waitcnt lgkmcnt(1)
v_cvt_pk_bf16_f32 v64, v16, v17
v_cvt_pk_bf16_f32 v65, v18, v19
mav 0,16,36
buffer_load_dwordx4 v[144:147], v87, s[36:39], s49 offen offset:1024 sc1
mav 4,20,36
R 48,4096
v_cvt_pk_bf16_f32 v66, v20, v21
mav 8,24,36
v_cvt_pk_bf16_f32 v67, v22, v23
v_cvt_pk_bf16_f32 v68, v24, v25
mav 12,28,36
v_cvt_pk_bf16_f32 v69, v26, v27
v_cvt_pk_bf16_f32 v70, v28, v29
mav 0,32,40
buffer_load_dwordx4 v[148:151], v88, s[36:39], s49 offen sc1
mav 4,36,40
R 52,5120
v_cvt_pk_bf16_f32 v71, v30, v31
mav 8,40,40
v_mul_f32 v75, v16, v16
v_fmac_f32 v75, v17, v17
mav 12,44,40
s_waitcnt lgkmcnt(1)
v_fmac_f32 v75, v18, v18
v_fmac_f32 v75, v19, v19
mav 0,48,44
buffer_load_dwordx4 v[152:155], v88, s[36:39], s49 offen offset:1024 sc1
mav 4,52,44
R 56,6144
v_fmac_f32 v75, v20, v20
mav 8,56,44
v_fmac_f32 v75, v21, v21
v_fmac_f32 v75, v22, v22
mav 12,60,44
v_fmac_f32 v75, v23, v23
v_fmac_f32 v75, v24, v24
mav 0,64,48
buffer_load_dwordx4 v[156:159], v89, s[36:39], s49 offen sc1
mav 4,68,48
R 60,7168
v_fmac_f32 v75, v25, v25
mav 8,72,48
v_fmac_f32 v75, v26, v26
v_fmac_f32 v75, v27, v27
mav 12,76,48
s_waitcnt lgkmcnt(1)
v_fmac_f32 v75, v28, v28
v_fmac_f32 v75, v29, v29
mav 0,80,52
buffer_load_dwordx4 v[160:163], v89, s[36:39], s49 offen offset:1024 sc1
mav 4,84,52
R 32,8192
v_fmac_f32 v75, v30, v30
mav 8,88,52
ds_write_b32 v97, v74 offset:0
mav 12,92,52
ds_write_b128 v94, v[64:67] offset:0
mav 0,96,56
s_add_u32 m0, s50, 0
R 36,9216
v_add_f32 v74, v17, v16
mav 4,100,56
ds_write_b128 v95, v[68:71] offset:0
mav 8,104,56
ds_read_b128 v[64:67], v96 offset:0
v_add_f32 v74, v18, v74
mav 12,108,56
s_waitcnt lgkmcnt(5)
ds_read_b128 v[68:71], v96 offset:1024
v_add_f32 v74, v19, v74
mav 0,112,60
D 83,0
mav 4,116,60
R 40,10240
v_add_f32 v74, v20, v74
mav 8,120,60
v_add_f32 v74, v21, v74
v_add_f32 v74, v22, v74
mav 12,124,60
v_add_f32 v74, v23, v74
v_add_f32 v74, v24, v74
mav 0,128,32
D 83,1024
mav 4,132,32
R 44,11264
v_add_f32 v74, v25, v74
mav 8,136,32
v_add_f32 v74, v26, v74
v_add_f32 v74, v27, v74
mav 12,140,32
s_waitcnt lgkmcnt(1)
v_add_f32 v74, v28, v74
v_add_f32 v74, v29, v74
mav 0,144,36
R 48,12288
v_add_f32 v74, v30, v74
mav 4,148,36
v_add_f32 v74, v31, v74
v_fmac_f32 v75, v31, v31
mav 8,152,36
mav 12,156,36
v_permlane32_swap_b32 v74, v75
mav 0,160,40
R 52,13312
v_add_f32 v74, v74, v75
mav 4,164,40
v_add_u32 v72, 1, v72
mav 8,168,40
mav 12,172,40
s_waitcnt lgkmcnt(1)
mav 0,176,44
D 83,2048
mav 4,180,44
R 56,14336
mav 8,184,44
mav 12,188,44
mav 0,192,48
D 83,3072
mav 4,196,48
R 60,15360
mav 8,200,48
mav 12,204,48
s_waitcnt lgkmcnt(1)
mav 0,208,52
buffer_store_dwordx4 v[64:67], v91, s[36:39], s54 offen sc1
mav 4,212,52
mav 8,216,52
mav 12,220,52
mav 0,224,56
buffer_store_dwordx4 v[68:71], v92, s[36:39], s54 offen sc1
mav 4,228,56
mav 8,232,56
mav 12,236,56
s_waitcnt lgkmcnt(0)
mav 0,240,60
buffer_load_dwordx4 v[164:167], v90, s[36:39], s49 offen sc1
mav 4,244,60
mav 8,248,60
mav 12,252,60
s_waitcnt vmcnt(18)
mvv 0,116,124
s_waitcnt vmcnt(12)
buffer_load_dwordx4 v[168:171], v90, s[36:39], s49 offen offset:1024 sc1
mvv 4,120,124
buffer_store_dword v72, v81, s[36:39], s52 offen sc1
mvv 8,116,128
s_sub_u32 s54, s54, 32768
mvv 12,120,128
mvv 0,116,132
mvv 4,120,132
mvv 8,116,136
s_add_u32 s49, s49, 313344
mvv 12,120,136
s_barrier
mvv 0,116,140
R 32,32768
mvv 4,120,140
s_waitcnt vmcnt(13)
ds_read_b128 v[204:207], v98 offset:0
mvv 8,116,144
mvv 12,120,144
s_waitcnt vmcnt(12)
mvv 0,116,148
buffer_load_dwordx4 v[172:175], v86, s[36:39], s49 offen sc1
mvv 4,120,148
s_waitcnt vmcnt(12)
R 36,33792
mvv 8,116,152
mvv 12,120,152
s_waitcnt vmcnt(11)
mvv 0,116,156
s_waitcnt lgkmcnt(1)
buffer_load_dwordx4 v[176:179], v86, s[36:39], s49 offen offset:1024 sc1
mvv 4,120,156
s_waitcnt vmcnt(11)
R 40,34816
v_add_f32 v204, v204, v205
mvv 8,116,160
v_add_f32 v206, v206, v207
v_add_f32 v204, v204, v206
mvv 12,120,160
v_mfma_f32_16x16x32_bf16 v[16:19], a[0:3], v[32:35], v[100:103]
buffer_load_dwordx4 v[180:183], v87, s[36:39], s49 offen sc1
v_mfma_f32_16x16x32_bf16 v[20:23], a[4:7], v[32:35], v[104:107]
R 44,35840
v_add_f32_dpp v205, v204, v204 quad_perm:[1,0,3,2] row_mask:0xf bank_mask:0xf
v_mfma_f32_16x16x32_bf16 v[24:27], a[8:11], v[32:35], v[108:111]
buffer_store_dword v205, v99, s[36:39], s49 offen sc1
v_mfma_f32_16x16x32_bf16 v[28:31], a[12:15], v[32:35], v[112:115]
s_waitcnt lgkmcnt(1)
v_cvt_pk_bf16_f32 v64, v0, v1
v_cvt_pk_bf16_f32 v65, v2, v3
mav 16,16,36
buffer_load_dwordx4 v[184:187], v87, s[36:39], s49 offen offset:1024 sc1
mav 20,20,36
R 48,36864
v_cvt_pk_bf16_f32 v66, v4, v5
mav 24,24,36
v_cvt_pk_bf16_f32 v67, v6, v7
v_cvt_pk_bf16_f32 v68, v8, v9
mav 28,28,36
v_cvt_pk_bf16_f32 v69, v10, v11
v_cvt_pk_bf16_f32 v70, v12, v13
mav 16,32,40
buffer_load_dwordx4 v[188:191], v88, s[36:39], s49 offen sc1
mav 20,36,40
R 52,37888
v_cvt_pk_bf16_f32 v71, v14, v15
mav 24,40,40
v_mul_f32 v75, v0, v0
v_fmac_f32 v75, v1, v1
mav 28,44,40
s_waitcnt lgkmcnt(1)
v_fmac_f32 v75, v2, v2
v_fmac_f32 v75, v3, v3
mav 16,48,44
buffer_load_dwordx4 v[192:195], v88, s[36:39], s49 offen offset:1024 sc1
mav 20,52,44
R 56,38912
v_fmac_f32 v75, v4, v4
mav 24,56,44
v_fmac_f32 v75, v5, v5
v_fmac_f32 v75, v6, v6
mav 28,60,44
v_fmac_f32 v75, v7, v7
v_fmac_f32 v75, v8, v8
mav 16,64,48
buffer_load_dwordx4 v[196:199], v89, s[36:39], s49 offen sc1
mav 20,68,48
R 60,39936
v_fmac_f32 v75, v9, v9
mav 24,72,48
v_fmac_f32 v75, v10, v10
v_fmac_f32 v75, v11, v11
mav 28,76,48
s_waitcnt lgkmcnt(1)
v_fmac_f32 v75, v12, v12
v_fmac_f32 v75, v13, v13
mav 16,80,52
buffer_load_dwordx4 v[200:203], v89, s[36:39], s49 offen offset:1024 sc1
mav 20,84,52
R 32,40960
v_fmac_f32 v75, v14, v14
mav 24,88,52
ds_write_b32 v97, v74 offset:1024
mav 28,92,52
ds_write_b128 v94, v[64:67] offset:0
mav 16,96,56
s_add_u32 m0, s50, 32768
R 36,41984
v_add_f32 v74, v1, v0
mav 20,100,56
ds_write_b128 v95, v[68:71] offset:0
mav 24,104,56
ds_read_b128 v[64:67], v96 offset:0
v_add_f32 v74, v2, v74
mav 28,108,56
s_waitcnt lgkmcnt(5)
ds_read_b128 v[68:71], v96 offset:1024
v_add_f32 v74, v3, v74
mav 16,112,60
D 83,0
mav 20,116,60
R 40,43008
v_add_f32 v74, v4, v74
mav 24,120,60
v_add_f32 v74, v5, v74
v_add_f32 v74, v6, v74
mav 28,124,60
v_add_f32 v74, v7, v74
v_add_f32 v74, v8, v74
mav 16,128,32
D 83,1024
LsR8:
s_cbranch_execz LsR12
mav 20,132,32
R 44,44032
v_add_f32 v74, v9, v74
mav 24,136,32
v_add_f32 v74, v10, v74
v_add_f32 v74, v11, v74
mav 28,140,32
s_waitcnt lgkmcnt(1)
v_add_f32 v74, v12, v74
v_add_f32 v74, v13, v74
mav 16,144,36
R 48,45056
v_add_f32 v74, v14, v74
mav 20,148,36
v_add_f32 v74, v15, v74
v_fmac_f32 v75, v15, v15
mav 24,152,36
mav 28,156,36
s_add_u32 s60, s60, 1
v_permlane32_swap_b32 v74, v75
mav 16,160,40
s_cbranch_scc0 LR1_1
mav 20,164,40
R 52,46080
v_add_f32 v74, v74, v75
mav 24,168,40
v_add_u32 v72, 1, v72
mav 28,172,40
s_waitcnt lgkmcnt(1)
mav 16,176,44
D 83,2048
mav 20,180,44
R 56,47104
mav 24,184,44
mav 28,188,44
mav 16,192,48
D 83,3072
mav 20,196,48
R 60,48128
mav 24,200,48
mav 28,204,48
s_waitcnt lgkmcnt(1)
mav 16,208,52
buffer_store_dwordx4 v[64:67], v91, s[36:39], s54 offen sc1
mav 20,212,52
mav 24,216,52
mav 28,220,52
mav 16,224,56
buffer_store_dwordx4 v[68:71], v92, s[36:39], s54 offen sc1
mav 20,228,56
mav 24,232,56
mav 28,236,56
s_waitcnt lgkmcnt(0)
mav 16,240,60
buffer_load_dwordx4 v[124:127], v90, s[36:39], s49 offen sc1
mav 20,244,60
mav 24,248,60
mav 28,252,60
s_waitcnt vmcnt(18)
mvv 16,116,164
s_waitcnt vmcnt(12)
buffer_load_dwordx4 v[128:131], v90, s[36:39], s49 offen offset:1024 sc1
mvv 20,120,164
buffer_store_dword v72, v81, s[36:39], s52 offen sc1
mvv 24,116,168
s_xor_b32 s50, s50, 0x10000
s_sub_u32 s54, s54, 32768
mvv 28,120,168
mvv 16,116,172
mvv 20,120,172
v_xor_b32 v80, 0x10000, v80
mvv 24,116,176
s_add_u32 s49, s49, 313344
mvv 28,120,176
s_barrier
mvv 16,116,180
R 32,0
mvv 20,120,180
s_waitcnt vmcnt(13)
ds_read_b128 v[204:207], v98 offset:1024
mvv 24,116,184
mvv 28,120,184
s_waitcnt vmcnt(12)
mvv 16,116,188
buffer_load_dwordx4 v[132:135], v86, s[36:39], s49 offen sc1
mvv 20,120,188
s_waitcnt vmcnt(12)
R 36,1024
mvv 24,116,192
mvv 28,120,192
s_waitcnt vmcnt(11)
mvv 16,116,196
s_waitcnt lgkmcnt(1)
buffer_load_dwordx4 v[136:139], v86, s[36:39], s49 offen offset:1024 sc1
mvv 20,120,196
s_waitcnt vmcnt(11)
R 40,2048
v_add_f32 v204, v204, v205
mvv 24,116,200
v_add_f32 v206, v206, v207
v_add_f32 v204, v204, v206
mvv 28,120,200
v_mfma_f32_16x16x32_bf16 v[0:3], a[0:3], v[32:35], v[100:103]
buffer_load_dwordx4 v[140:143], v87, s[36:39], s49 offen sc1
v_mfma_f32_16x16x32_bf16 v[4:7], a[4:7], v[32:35], v[104:107]
R 44,3072
v_add_f32_dpp v205, v204, v204 quad_perm:[1,0,3,2] row_mask:0xf bank_mask:0xf
v_mfma_f32_16x16x32_bf16 v[8:11], a[8:11], v[32:35], v[108:111]
buffer_store_dword v205, v99, s[36:39], s49 offen sc1
v_mfma_f32_16x16x32_bf16 v[12:15], a[12:15], v[32:35], v[112:115]
s_waitcnt lgkmcnt(1)
v_cvt_pk_bf16_f32 v64, v16, v17
v_cvt_pk_bf16_f32 v65, v18, v19
mav 0,16,36
buffer_load_dwordx4 v[144:147], v87, s[36:39], s49 offen offset:1024 sc1
mav 4,20,36
R 48,4096
v_cvt_pk_bf16_f32 v66, v20, v21
mav 8,24,36
v_cvt_pk_bf16_f32 v67, v22, v23
v_cvt_pk_bf16_f32 v68, v24, v25
mav 12,28,36
v_cvt_pk_bf16_f32 v69, v26, v27
v_cvt_pk_bf16_f32 v70, v28, v29
mav 0,32,40
buffer_load_dwordx4 v[148:151], v88, s[36:39], s49 offen sc1
mav 4,36,40
R 52,5120
v_cvt_pk_bf16_f32 v71, v30, v31
mav 8,40,40
v_mul_f32 v75, v16, v16
v_fmac_f32 v75, v17, v17
mav 12,44,40
s_waitcnt lgkmcnt(1)
v_fmac_f32 v75, v18, v18
v_fmac_f32 v75, v19, v19
mav 0,48,44
buffer_load_dwordx4 v[152:155], v88, s[36:39], s49 offen offset:1024 sc1
mav 4,52,44
R 56,6144
v_fmac_f32 v75, v20, v20
mav 8,56,44
v_fmac_f32 v75, v21, v21
v_fmac_f32 v75, v22, v22
mav 12,60,44
v_fmac_f32 v75, v23, v23
v_fmac_f32 v75, v24, v24
mav 0,64,48
buffer_load_dwordx4 v[156:159], v89, s[36:39], s49 offen sc1
mav 4,68,48
R 60,7168
v_fmac_f32 v75, v25, v25
mav 8,72,48
v_fmac_f32 v75, v26, v26
v_fmac_f32 v75, v27, v27
mav 12,76,48
s_waitcnt lgkmcnt(1)
v_fmac_f32 v75, v28, v28
v_fmac_f32 v75, v29, v29
mav 0,80,52
buffer_load_dwordx4 v[160:163], v89, s[36:39], s49 offen offset:1024 sc1
mav 4,84,52
R 32,8192
v_fmac_f32 v75, v30, v30
mav 8,88,52
ds_write_b32 v97, v74 offset:0
mav 12,92,52
ds_write_b128 v94, v[64:67] offset:0
mav 0,96,56
s_add_u32 m0, s50, 0
R 36,9216
v_add_f32 v74, v17, v16
mav 4,100,56
ds_write_b128 v95, v[68:71] offset:0
mav 8,104,56
ds_read_b128 v[64:67], v96 offset:0
v_add_f32 v74, v18, v74
mav 12,108,56
s_waitcnt lgkmcnt(5)
ds_read_b128 v[68:71], v96 offset:1024
v_add_f32 v74, v19, v74
mav 0,112,60
D 83,0
mav 4,116,60
R 40,10240
v_add_f32 v74, v20, v74
mav 8,120,60
v_add_f32 v74, v21, v74
v_add_f32 v74, v22, v74
mav 12,124,60
v_add_f32 v74, v23, v74
v_add_f32 v74, v24, v74
mav 0,128,32
D 83,1024
mav 4,132,32
R 44,11264
v_add_f32 v74, v25, v74
mav 8,136,32
v_add_f32 v74, v26, v74
v_add_f32 v74, v27, v74
mav 12,140,32
s_waitcnt lgkmcnt(1)
v_add_f32 v74, v28, v74
v_add_f32 v74, v29, v74
mav 0,144,36
R 48,12288
v_add_f32 v74, v30, v74
mav 4,148,36
v_add_f32 v74, v31, v74
v_fmac_f32 v75, v31, v31
mav 8,152,36
mav 12,156,36
v_permlane32_swap_b32 v74, v75
mav 0,160,40
R 52,13312
v_add_f32 v74, v74, v75
mav 4,164,40
v_add_u32 v72, 1, v72
mav 8,168,40
mav 12,172,40
s_waitcnt lgkmcnt(1)
mav 0,176,44
D 83,2048
mav 4,180,44
R 56,14336
mav 8,184,44
mav 12,188,44
mav 0,192,48
D 83,3072
mav 4,196,48
R 60,15360
mav 8,200,48
mav 12,204,48
s_waitcnt lgkmcnt(1)
mav 0,208,52
buffer_store_dwordx4 v[64:67], v91, s[36:39], s54 offen sc1
mav 4,212,52
mav 8,216,52
mav 12,220,52
mav 0,224,56
buffer_store_dwordx4 v[68:71], v92, s[36:39], s54 offen sc1
mav 4,228,56
mav 8,232,56
mav 12,236,56
s_waitcnt lgkmcnt(0)
mav 0,240,60
buffer_load_dwordx4 v[164:167], v90, s[36:39], s49 offen sc1
mav 4,244,60
mav 8,248,60
mav 12,252,60
s_waitcnt vmcnt(18)
mvv 0,116,124
s_waitcnt vmcnt(12)
buffer_load_dwordx4 v[168:171], v90, s[36:39], s49 offen offset:1024 sc1
mvv 4,120,124
buffer_store_dword v72, v81, s[36:39], s52 offen sc1
mvv 8,116,128
s_sub_u32 s54, s54, 3178496
mvv 12,120,128
mvv 0,116,132
mvv 4,120,132
mvv 8,116,136
s_add_u32 s49, s49, 313344
mvv 12,120,136
s_barrier
mvv 0,116,140
R 32,32768
mvv 4,120,140
s_waitcnt vmcnt(13)
ds_read_b128 v[204:207], v98 offset:0
mvv 8,116,144
mvv 12,120,144
s_waitcnt vmcnt(12)
mvv 0,116,148
buffer_load_dwordx4 v[172:175], v86, s[36:39], s49 offen sc1
mvv 4,120,148
s_waitcnt vmcnt(12)
R 36,33792
mvv 8,116,152
mvv 12,120,152
s_waitcnt vmcnt(11)
mvv 0,116,156
s_waitcnt lgkmcnt(1)
buffer_load_dwordx4 v[176:179], v86, s[36:39], s49 offen offset:1024 sc1
mvv 4,120,156
s_waitcnt vmcnt(11)
R 40,34816
v_add_f32 v204, v204, v205
mvv 8,116,160
v_add_f32 v206, v206, v207
v_add_f32 v204, v204, v206
mvv 12,120,160
v_mfma_f32_16x16x32_bf16 v[16:19], a[0:3], v[32:35], v[100:103]
buffer_load_dwordx4 v[180:183], v87, s[36:39], s49 offen sc1
v_mfma_f32_16x16x32_bf16 v[20:23], a[4:7], v[32:35], v[104:107]
R 44,35840
v_add_f32_dpp v205, v204, v204 quad_perm:[1,0,3,2] row_mask:0xf bank_mask:0xf
v_mfma_f32_16x16x32_bf16 v[24:27], a[8:11], v[32:35], v[108:111]
buffer_store_dword v205, v99, s[36:39], s49 offen sc1
v_mfma_f32_16x16x32_bf16 v[28:31], a[12:15], v[32:35], v[112:115]
s_waitcnt lgkmcnt(1)
v_cvt_pk_bf16_f32 v64, v0, v1
v_cvt_pk_bf16_f32 v65, v2, v3
mav 16,16,36
buffer_load_dwordx4 v[184:187], v87, s[36:39], s49 offen offset:1024 sc1
mav 20,20,36
R 48,36864
v_cvt_pk_bf16_f32 v66, v4, v5
mav 24,24,36
v_cvt_pk_bf16_f32 v67, v6, v7
v_cvt_pk_bf16_f32 v68, v8, v9
mav 28,28,36
v_cvt_pk_bf16_f32 v69, v10, v11
v_cvt_pk_bf16_f32 v70, v12, v13
mav 16,32,40
buffer_load_dwordx4 v[188:191], v88, s[36:39], s49 offen sc1
mav 20,36,40
R 52,37888
v_cvt_pk_bf16_f32 v71, v14, v15
mav 24,40,40
mav 28,44,40
s_waitcnt lgkmcnt(1)
mav 16,48,44
buffer_load_dwordx4 v[192:195], v88, s[36:39], s49 offen offset:1024 sc1
mav 20,52,44
R 56,38912
mav 24,56,44
mav 28,60,44
mav 16,64,48
buffer_load_dwordx4 v[196:199], v89, s[36:39], s49 offen sc1
mav 20,68,48
R 60,39936
mav 24,72,48
mav 28,76,48
s_waitcnt lgkmcnt(1)
mav 16,80,52
buffer_load_dwordx4 v[200:203], v89, s[36:39], s49 offen offset:1024 sc1
mav 20,84,52
R 32,40960
mav 24,88,52
ds_write_b32 v97, v74 offset:1024
mav 28,92,52
ds_write_b128 v94, v[64:67] offset:0
mav 16,96,56
s_add_u32 m0, s50, 32768
R 36,41984
mav 20,100,56
ds_write_b128 v95, v[68:71] offset:0
mav 24,104,56
ds_read_b128 v[64:67], v96 offset:0
mav 28,108,56
s_waitcnt lgkmcnt(5)
ds_read_b128 v[68:71], v96 offset:1024
mav 16,112,60
D 83,0
mav 20,116,60
R 40,43008
mav 24,120,60
mav 28,124,60
mav 16,128,32
D 83,1024
mav 20,132,32
R 44,44032
mav 24,136,32
mav 28,140,32
s_waitcnt lgkmcnt(1)
mav 16,144,36
R 48,45056
mav 20,148,36
mav 24,152,36
mav 28,156,36
mav 16,160,40
mav 20,164,40
R 52,46080
v_add_u32 v72, 1, v72
mav 24,168,40
mav 28,172,40
s_waitcnt lgkmcnt(1)
mav 16,176,44
D 83,2048
mav 20,180,44
R 56,47104
mav 24,184,44
mav 28,188,44
mav 16,192,48
D 83,3072
mav 20,196,48
R 60,48128
mav 24,200,48
mav 28,204,48
s_waitcnt lgkmcnt(1)
mav 16,208,52
buffer_store_dwordx4 v[64:67], v91, s[44:47], s54 offen
mav 20,212,52
mav 24,216,52
mav 28,220,52
mav 16,224,56
buffer_store_dwordx4 v[68:71], v92, s[44:47], s54 offen
mav 20,228,56
mav 24,232,56
mav 28,236,56
s_waitcnt lgkmcnt(0)
mav 16,240,60
buffer_load_dwordx4 v[124:127], v90, s[36:39], s49 offen sc1
mav 20,244,60
mav 24,248,60
mav 28,252,60
s_waitcnt vmcnt(18)
mvv 16,116,164
s_waitcnt vmcnt(12)
buffer_load_dwordx4 v[128:131], v90, s[36:39], s49 offen offset:1024 sc1
mvv 20,120,164
buffer_store_dword v72, v81, s[36:39], s52 offen sc1
mvv 24,116,168
s_xor_b32 s50, s50, 0x10000
s_sub_u32 s54, s54, 32768
mvv 28,120,168
mvv 16,116,172
mvv 20,120,172
v_xor_b32 v80, 0x10000, v80
mvv 24,116,176
s_add_u32 s49, s49, 313344
mvv 28,120,176
s_barrier
mvv 16,116,180
R 32,0
mvv 20,120,180
s_waitcnt vmcnt(13)
ds_read_b128 v[204:207], v98 offset:1024
mvv 24,116,184
mvv 28,120,184
s_waitcnt vmcnt(12)
mvv 16,116,188
buffer_load_dwordx4 v[132:135], v86, s[36:39], s49 offen sc1
mvv 20,120,188
s_waitcnt vmcnt(12)
R 36,1024
mvv 24,116,192
mvv 28,120,192
s_waitcnt vmcnt(11)
mvv 16,116,196
s_waitcnt lgkmcnt(1)
buffer_load_dwordx4 v[136:139], v86, s[36:39], s49 offen offset:1024 sc1
mvv 20,120,196
s_waitcnt vmcnt(11)
R 40,2048
v_add_f32 v204, v204, v205
mvv 24,116,200
v_add_f32 v206, v206, v207
v_add_f32 v204, v204, v206
mvv 28,120,200
v_mfma_f32_16x16x32_bf16 v[0:3], a[0:3], v[32:35], v[100:103]
buffer_load_dwordx4 v[140:143], v87, s[36:39], s49 offen sc1
v_mfma_f32_16x16x32_bf16 v[4:7], a[4:7], v[32:35], v[104:107]
R 44,3072
v_add_f32_dpp v205, v204, v204 quad_perm:[1,0,3,2] row_mask:0xf bank_mask:0xf
v_mfma_f32_16x16x32_bf16 v[8:11], a[8:11], v[32:35], v[108:111]
buffer_store_dword v205, v99, s[36:39], s49 offen sc1
v_mfma_f32_16x16x32_bf16 v[12:15], a[12:15], v[32:35], v[112:115]
s_waitcnt lgkmcnt(1)
v_cvt_pk_bf16_f32 v64, v16, v17
v_cvt_pk_bf16_f32 v65, v18, v19
mav 0,16,36
buffer_load_dwordx4 v[144:147], v87, s[36:39], s49 offen offset:1024 sc1
mav 4,20,36
R 48,4096
v_cvt_pk_bf16_f32 v66, v20, v21
mav 8,24,36
v_cvt_pk_bf16_f32 v67, v22, v23
v_cvt_pk_bf16_f32 v68, v24, v25
mav 12,28,36
v_cvt_pk_bf16_f32 v69, v26, v27
v_cvt_pk_bf16_f32 v70, v28, v29
mav 0,32,40
buffer_load_dwordx4 v[148:151], v88, s[36:39], s49 offen sc1
mav 4,36,40
R 52,5120
v_cvt_pk_bf16_f32 v71, v30, v31
mav 8,40,40
mav 12,44,40
s_waitcnt lgkmcnt(1)
mav 0,48,44
buffer_load_dwordx4 v[152:155], v88, s[36:39], s49 offen offset:1024 sc1
mav 4,52,44
R 56,6144
mav 8,56,44
mav 12,60,44
mav 0,64,48
buffer_load_dwordx4 v[156:159], v89, s[36:39], s49 offen sc1
mav 4,68,48
R 60,7168
mav 8,72,48
mav 12,76,48
s_waitcnt lgkmcnt(1)
mav 0,80,52
buffer_load_dwordx4 v[160:163], v89, s[36:39], s49 offen offset:1024 sc1
mav 4,84,52
R 32,8192
mav 8,88,52
s_add_u32 m0, s50, 0
mav 12,92,52
ds_write_b128 v94, v[64:67] offset:0
mav 0,96,56
D 83,0
mav 4,100,56
R 36,9216
mav 8,104,56
ds_write_b128 v95, v[68:71] offset:0
mav 12,108,56
s_waitcnt lgkmcnt(3)
ds_read_b128 v[64:67], v96 offset:0
mav 0,112,60
D 83,1024
mav 4,116,60
R 40,10240
mav 8,120,60
ds_read_b128 v[68:71], v96 offset:1024
mav 12,124,60
mav 0,128,32
R 44,11264
mav 4,132,32
mav 8,136,32
mav 12,140,32
s_waitcnt lgkmcnt(2)
mav 0,144,36
R 48,12288
mav 4,148,36
mav 8,152,36
mav 12,156,36
mav 0,160,40
R 52,13312
v_add_u32 v72, 68, v72
mav 4,164,40
mav 8,168,40
mav 12,172,40
s_waitcnt lgkmcnt(1)
mav 0,176,44
D 83,2048
mav 4,180,44
R 56,14336
mav 8,184,44
mav 12,188,44
mav 0,192,48
D 83,3072
mav 4,196,48
R 60,15360
mav 8,200,48
mav 12,204,48
s_waitcnt lgkmcnt(1)
mav 0,208,52
buffer_store_dwordx4 v[64:67], v91, s[44:47], s54 offen
mav 4,212,52
mav 8,216,52
mav 12,220,52
mav 0,224,56
buffer_store_dwordx4 v[68:71], v92, s[44:47], s54 offen
mav 4,228,56
mav 8,232,56
mav 12,236,56
s_waitcnt lgkmcnt(0)
mav 0,240,60
buffer_load_dwordx4 v[164:167], v90, s[36:39], s49 offen sc1
mav 4,244,60
mav 8,248,60
mav 12,252,60
s_waitcnt vmcnt(18)
mvv 0,116,124
s_waitcnt vmcnt(12)
buffer_load_dwordx4 v[168:171], v90, s[36:39], s49 offen offset:1024 sc1
LsR9:
s_cbranch_execz LsR13
mvv 4,120,124
buffer_store_dword v72, v81, s[36:39], s52 offen sc1
mvv 8,116,128
s_sub_u32 s54, s54, 32768
mvv 12,120,128
mvv 0,116,132
mvv 4,120,132
mvv 8,116,136
s_add_u32 s49, s49, 313344
mvv 12,120,136
s_barrier
mvv 0,116,140
R 32,32768
mvv 4,120,140
s_waitcnt vmcnt(13)
mvv 8,116,144
mvv 12,120,144
s_waitcnt vmcnt(12)
mvv 0,116,148
buffer_load_dwordx4 v[172:175], v86, s[36:39], s49 offen sc1
mvv 4,120,148
s_waitcnt vmcnt(12)
R 36,33792
mvv 8,116,152
mvv 12,120,152
s_waitcnt vmcnt(11)
mvv 0,116,156
buffer_load_dwordx4 v[176:179], v86, s[36:39], s49 offen offset:1024 sc1
mvv 4,120,156
s_waitcnt vmcnt(11)
R 40,34816
mvv 8,116,160
mvv 12,120,160
s_waitcnt lgkmcnt(1)
v_mfma_f32_16x16x32_bf16 v[16:19], a[0:3], v[32:35], v[100:103]
buffer_load_dwordx4 v[180:183], v87, s[36:39], s49 offen sc1
v_mfma_f32_16x16x32_bf16 v[20:23], a[4:7], v[32:35], v[104:107]
R 44,35840
v_cvt_pk_bf16_f32 v64, v0, v1
v_mfma_f32_16x16x32_bf16 v[24:27], a[8:11], v[32:35], v[108:111]
v_cvt_pk_bf16_f32 v65, v2, v3
v_cvt_pk_bf16_f32 v66, v4, v5
v_mfma_f32_16x16x32_bf16 v[28:31], a[12:15], v[32:35], v[112:115]
v_cvt_pk_bf16_f32 v67, v6, v7
v_cvt_pk_bf16_f32 v68, v8, v9
mav 16,16,36
buffer_load_dwordx4 v[184:187], v87, s[36:39], s49 offen offset:1024 sc1
mav 20,20,36
R 48,36864
v_cvt_pk_bf16_f32 v69, v10, v11
mav 24,24,36
v_cvt_pk_bf16_f32 v70, v12, v13
v_cvt_pk_bf16_f32 v71, v14, v15
mav 28,28,36
s_waitcnt lgkmcnt(1)
mav 16,32,40
buffer_load_dwordx4 v[188:191], v88, s[36:39], s49 offen sc1
mav 20,36,40
R 52,37888
mav 24,40,40
mav 28,44,40
mav 16,48,44
buffer_load_dwordx4 v[192:195], v88, s[36:39], s49 offen offset:1024 sc1
mav 20,52,44
R 56,38912
mav 24,56,44
mav 28,60,44
s_waitcnt lgkmcnt(1)
mav 16,64,48
buffer_load_dwordx4 v[196:199], v89, s[36:39], s49 offen sc1
mav 20,68,48
R 60,39936
mav 24,72,48
mav 28,76,48
mav 16,80,52
buffer_load_dwordx4 v[200:203], v89, s[36:39], s49 offen offset:1024 sc1
mav 20,84,52
R 32,40960
mav 24,88,52
s_add_u32 m0, s50, 32768
mav 28,92,52
s_waitcnt lgkmcnt(1)
ds_write_b128 v94, v[64:67] offset:0
mav 16,96,56
D 83,0
mav 20,100,56
R 36,41984
mav 24,104,56
ds_write_b128 v95, v[68:71] offset:0
mav 28,108,56
ds_read_b128 v[64:67], v96 offset:0
mav 16,112,60
D 83,1024
mav 20,116,60
R 40,43008
mav 24,120,60
ds_read_b128 v[68:71], v96 offset:1024
mav 28,124,60
s_waitcnt lgkmcnt(4)
mav 16,128,32
R 44,44032
mav 20,132,32
mav 24,136,32
mav 28,140,32
mav 16,144,36
R 48,45056
mav 20,148,36
mav 24,152,36
mav 28,156,36
s_waitcnt lgkmcnt(1)
mav 16,160,40
mav 20,164,40
R 52,46080
mav 24,168,40
mav 28,172,40
mav 16,176,44
D 83,2048
mav 20,180,44
R 56,47104
mav 24,184,44
mav 28,188,44
s_waitcnt lgkmcnt(1)
mav 16,192,48
D 83,3072
mav 20,196,48
R 60,48128
mav 24,200,48
mav 28,204,48
mav 16,208,52
buffer_store_dwordx4 v[64:67], v91, s[44:47], s54 offen
mav 20,212,52
mav 24,216,52
mav 28,220,52
s_waitcnt lgkmcnt(0)
mav 16,224,56
buffer_store_dwordx4 v[68:71], v92, s[44:47], s54 offen
mav 20,228,56
mav 24,232,56
mav 28,236,56
mav 16,240,60
buffer_load_dwordx4 v[124:127], v90, s[36:39], s49 offen sc1
mav 20,244,60
mav 24,248,60
mav 28,252,60
s_waitcnt vmcnt(17)
mvv 16,116,164
buffer_load_dwordx4 v[128:131], v90, s[36:39], s49 offen offset:1024 sc1
mvv 20,120,164
s_waitcnt vmcnt(17)
mvv 24,116,168
s_xor_b32 s50, s50, 0x10000
s_sub_u32 s54, s54, 32768
mvv 28,120,168
s_waitcnt vmcnt(15)
mvv 16,116,172
mvv 20,120,172
s_waitcnt vmcnt(14)
v_xor_b32 v80, 0x10000, v80
mvv 24,116,176
s_waitcnt vmcnt(13)
s_add_u32 s49, s49, 313344
mvv 28,120,176
s_barrier
mvv 16,116,180
R 32,0
mvv 20,120,180
s_waitcnt vmcnt(12)
mvv 24,116,184
mvv 28,120,184
s_waitcnt vmcnt(11)
mvv 16,116,188
buffer_load_dwordx4 v[132:135], v86, s[36:39], s49 offen sc1
mvv 20,120,188
s_waitcnt vmcnt(11)
R 36,1024
mvv 24,116,192
mvv 28,120,192
s_waitcnt vmcnt(10)
mvv 16,116,196
buffer_load_dwordx4 v[136:139], v86, s[36:39], s49 offen offset:1024 sc1
mvv 20,120,196
s_waitcnt vmcnt(10)
R 40,2048
mvv 24,116,200
mvv 28,120,200
s_waitcnt lgkmcnt(1)
v_mfma_f32_16x16x32_bf16 v[0:3], a[0:3], v[32:35], v[100:103]
buffer_load_dwordx4 v[140:143], v87, s[36:39], s49 offen sc1
v_mfma_f32_16x16x32_bf16 v[4:7], a[4:7], v[32:35], v[104:107]
R 44,3072
v_cvt_pk_bf16_f32 v64, v16, v17
v_mfma_f32_16x16x32_bf16 v[8:11], a[8:11], v[32:35], v[108:111]
v_cvt_pk_bf16_f32 v65, v18, v19
v_cvt_pk_bf16_f32 v66, v20, v21
v_mfma_f32_16x16x32_bf16 v[12:15], a[12:15], v[32:35], v[112:115]
v_cvt_pk_bf16_f32 v67, v22, v23
v_cvt_pk_bf16_f32 v68, v24, v25
mav 0,16,36
buffer_load_dwordx4 v[144:147], v87, s[36:39], s49 offen offset:1024 sc1
mav 4,20,36
R 48,4096
v_cvt_pk_bf16_f32 v69, v26, v27
mav 8,24,36
v_cvt_pk_bf16_f32 v70, v28, v29
v_cvt_pk_bf16_f32 v71, v30, v31
mav 12,28,36
s_waitcnt lgkmcnt(1)
mav 0,32,40
buffer_load_dwordx4 v[148:151], v88, s[36:39], s49 offen sc1
mav 4,36,40
R 52,5120
mav 8,40,40
mav 12,44,40
mav 0,48,44
buffer_load_dwordx4 v[152:155], v88, s[36:39], s49 offen offset:1024 sc1
mav 4,52,44
R 56,6144
mav 8,56,44
mav 12,60,44
s_waitcnt lgkmcnt(1)
mav 0,64,48
buffer_load_dwordx4 v[156:159], v89, s[36:39], s49 offen sc1
mav 4,68,48
R 60,7168
mav 8,72,48
mav 12,76,48
mav 0,80,52
buffer_load_dwordx4 v[160:163], v89, s[36:39], s49 offen offset:1024 sc1
mav 4,84,52
R 32,8192
mav 8,88,52
s_add_u32 m0, s50, 0
mav 12,92,52
s_waitcnt lgkmcnt(1)
ds_write_b128 v94, v[64:67] offset:0
mav 0,96,56
D 83,0
mav 4,100,56
R 36,9216
mav 8,104,56
ds_write_b128 v95, v[68:71] offset:0
mav 12,108,56
ds_read_b128 v[64:67], v96 offset:0
mav 0,112,60
D 83,1024
mav 4,116,60
R 40,10240
mav 8,120,60
ds_read_b128 v[68:71], v96 offset:1024
mav 12,124,60
s_waitcnt lgkmcnt(4)
mav 0,128,32
R 44,11264
mav 4,132,32
mav 8,136,32
mav 12,140,32
mav 0,144,36
R 48,12288
mav 4,148,36
mav 8,152,36
mav 12,156,36
s_waitcnt lgkmcnt(1)
mav 0,160,40
R 52,13312
mav 4,164,40
mav 8,168,40
mav 12,172,40
mav 0,176,44
D 83,2048
mav 4,180,44
R 56,14336
mav 8,184,44
mav 12,188,44
s_waitcnt lgkmcnt(1)
mav 0,192,48
D 83,3072
mav 4,196,48
R 60,15360
mav 8,200,48
mav 12,204,48
mav 0,208,52
buffer_store_dwordx4 v[64:67], v91, s[44:47], s54 offen
mav 4,212,52
mav 8,216,52
mav 12,220,52
s_waitcnt lgkmcnt(0)
mav 0,224,56
buffer_store_dwordx4 v[68:71], v92, s[44:47], s54 offen
mav 4,228,56
mav 8,232,56
mav 12,236,56
mav 0,240,60
buffer_load_dwordx4 v[164:167], v90, s[36:39], s49 offen sc1
mav 4,244,60
mav 8,248,60
mav 12,252,60
s_waitcnt vmcnt(16)
mvv 0,116,124
buffer_load_dwordx4 v[168:171], v90, s[36:39], s49 offen offset:1024 sc1
mvv 4,120,124
s_waitcnt vmcnt(16)
mvv 8,116,128
s_sub_u32 s54, s54, 32768
mvv 12,120,128
s_waitcnt vmcnt(15)
mvv 0,116,132
mvv 4,120,132
s_waitcnt vmcnt(14)
mvv 8,116,136
s_waitcnt vmcnt(13)
s_add_u32 s49, s49, 313344
mvv 12,120,136
s_barrier
mvv 0,116,140
R 32,32768
mvv 4,120,140
s_waitcnt vmcnt(12)
mvv 8,116,144
mvv 12,120,144
s_waitcnt vmcnt(11)
mvv 0,116,148
buffer_load_dwordx4 v[172:175], v86, s[36:39], s49 offen sc1
mvv 4,120,148
s_waitcnt vmcnt(11)
R 36,33792
mvv 8,116,152
mvv 12,120,152
s_waitcnt vmcnt(10)
mvv 0,116,156
buffer_load_dwordx4 v[176:179], v86, s[36:39], s49 offen offset:1024 sc1
mvv 4,120,156
s_waitcnt vmcnt(10)
R 40,34816
mvv 8,116,160
mvv 12,120,160
s_waitcnt lgkmcnt(1)
v_mfma_f32_16x16x32_bf16 v[16:19], a[0:3], v[32:35], v[100:103]
buffer_load_dwordx4 v[180:183], v87, s[36:39], s49 offen sc1
v_mfma_f32_16x16x32_bf16 v[20:23], a[4:7], v[32:35], v[104:107]
R 44,35840
v_cvt_pk_bf16_f32 v64, v0, v1
v_mfma_f32_16x16x32_bf16 v[24:27], a[8:11], v[32:35], v[108:111]
v_cvt_pk_bf16_f32 v65, v2, v3
v_cvt_pk_bf16_f32 v66, v4, v5
v_mfma_f32_16x16x32_bf16 v[28:31], a[12:15], v[32:35], v[112:115]
v_cvt_pk_bf16_f32 v67, v6, v7
v_cvt_pk_bf16_f32 v68, v8, v9
mav 16,16,36
buffer_load_dwordx4 v[184:187], v87, s[36:39], s49 offen offset:1024 sc1
mav 20,20,36
R 48,36864
v_cvt_pk_bf16_f32 v69, v10, v11
mav 24,24,36
v_cvt_pk_bf16_f32 v70, v12, v13
v_cvt_pk_bf16_f32 v71, v14, v15
mav 28,28,36
s_waitcnt lgkmcnt(1)
mav 16,32,40
buffer_load_dwordx4 v[188:191], v88, s[36:39], s49 offen sc1
mav 20,36,40
R 52,37888
mav 24,40,40
mav 28,44,40
mav 16,48,44
buffer_load_dwordx4 v[192:195], v88, s[36:39], s49 offen offset:1024 sc1
mav 20,52,44
R 56,38912
mav 24,56,44
mav 28,60,44
s_waitcnt lgkmcnt(1)
mav 16,64,48
buffer_load_dwordx4 v[196:199], v89, s[36:39], s49 offen sc1
mav 20,68,48
R 60,39936
mav 24,72,48
mav 28,76,48
mav 16,80,52
buffer_load_dwordx4 v[200:203], v89, s[36:39], s49 offen offset:1024 sc1
mav 20,84,52
R 32,40960
mav 24,88,52
s_add_u32 m0, s50, 32768
mav 28,92,52
s_waitcnt lgkmcnt(1)
ds_write_b128 v94, v[64:67] offset:0
mav 16,96,56
D 83,0
mav 20,100,56
R 36,41984
mav 24,104,56
ds_write_b128 v95, v[68:71] offset:0
mav 28,108,56
ds_read_b128 v[64:67], v96 offset:0
mav 16,112,60
D 83,1024
mav 20,116,60
R 40,43008
mav 24,120,60
ds_read_b128 v[68:71], v96 offset:1024
mav 28,124,60
s_waitcnt lgkmcnt(4)
mav 16,128,32
R 44,44032
mav 20,132,32
mav 24,136,32
mav 28,140,32
mav 16,144,36
R 48,45056
mav 20,148,36
mav 24,152,36
mav 28,156,36
s_waitcnt lgkmcnt(1)
mav 16,160,40
s_mov_b32 s60, -12
.p2align 6
LR1_2:
mav 20,164,40
R 52,46080
mav 24,168,40
mav 28,172,40
mav 16,176,44
D 83,2048
mav 20,180,44
R 56,47104
mav 24,184,44
mav 28,188,44
s_waitcnt lgkmcnt(1)
mav 16,192,48
D 83,3072
mav 20,196,48
R 60,48128
mav 24,200,48
mav 28,204,48
mav 16,208,52
buffer_store_dwordx4 v[64:67], v91, s[44:47], s54 offen
mav 20,212,52
mav 24,216,52
mav 28,220,52
s_waitcnt lgkmcnt(0)
mav 16,224,56
buffer_store_dwordx4 v[68:71], v92, s[44:47], s54 offen
mav 20,228,56
mav 24,232,56
mav 28,236,56
mav 16,240,60
buffer_load_dwordx4 v[124:127], v90, s[36:39], s49 offen sc1
mav 20,244,60
mav 24,248,60
mav 28,252,60
s_waitcnt vmcnt(16)
mvv 16,116,164
buffer_load_dwordx4 v[128:131], v90, s[36:39], s49 offen offset:1024 sc1
mvv 20,120,164
s_waitcnt vmcnt(16)
mvv 24,116,168
s_xor_b32 s50, s50, 0x10000
s_sub_u32 s54, s54, 32768
mvv 28,120,168
s_waitcnt vmcnt(15)
mvv 16,116,172
mvv 20,120,172
s_waitcnt vmcnt(14)
v_xor_b32 v80, 0x10000, v80
mvv 24,116,176
s_waitcnt vmcnt(13)
s_add_u32 s49, s49, 313344
mvv 28,120,176
s_barrier
mvv 16,116,180
R 32,0
mvv 20,120,180
s_waitcnt vmcnt(12)
mvv 24,116,184
mvv 28,120,184
s_waitcnt vmcnt(11)
mvv 16,116,188
buffer_load_dwordx4 v[132:135], v86, s[36:39], s49 offen sc1
mvv 20,120,188
s_waitcnt vmcnt(11)
R 36,1024
mvv 24,116,192
mvv 28,120,192
s_waitcnt vmcnt(10)
mvv 16,116,196
buffer_load_dwordx4 v[136:139], v86, s[36:39], s49 offen offset:1024 sc1
mvv 20,120,196
s_waitcnt vmcnt(10)
R 40,2048
mvv 24,116,200
mvv 28,120,200
s_waitcnt lgkmcnt(1)
v_mfma_f32_16x16x32_bf16 v[0:3], a[0:3], v[32:35], v[100:103]
buffer_load_dwordx4 v[140:143], v87, s[36:39], s49 offen sc1
v_mfma_f32_16x16x32_bf16 v[4:7], a[4:7], v[32:35], v[104:107]
R 44,3072
v_cvt_pk_bf16_f32 v64, v16, v17
v_mfma_f32_16x16x32_bf16 v[8:11], a[8:11], v[32:35], v[108:111]
v_cvt_pk_bf16_f32 v65, v18, v19
v_cvt_pk_bf16_f32 v66, v20, v21
v_mfma_f32_16x16x32_bf16 v[12:15], a[12:15], v[32:35], v[112:115]
v_cvt_pk_bf16_f32 v67, v22, v23
v_cvt_pk_bf16_f32 v68, v24, v25
mav 0,16,36
buffer_load_dwordx4 v[144:147], v87, s[36:39], s49 offen offset:1024 sc1
mav 4,20,36
R 48,4096
v_cvt_pk_bf16_f32 v69, v26, v27
mav 8,24,36
v_cvt_pk_bf16_f32 v70, v28, v29
v_cvt_pk_bf16_f32 v71, v30, v31
mav 12,28,36
s_waitcnt lgkmcnt(1)
mav 0,32,40
buffer_load_dwordx4 v[148:151], v88, s[36:39], s49 offen sc1
mav 4,36,40
R 52,5120
mav 8,40,40
mav 12,44,40
mav 0,48,44
buffer_load_dwordx4 v[152:155], v88, s[36:39], s49 offen offset:1024 sc1
mav 4,52,44
R 56,6144
mav 8,56,44
mav 12,60,44
s_waitcnt lgkmcnt(1)
mav 0,64,48
buffer_load_dwordx4 v[156:159], v89, s[36:39], s49 offen sc1
mav 4,68,48
R 60,7168
mav 8,72,48
mav 12,76,48
mav 0,80,52
buffer_load_dwordx4 v[160:163], v89, s[36:39], s49 offen offset:1024 sc1
mav 4,84,52
R 32,8192
mav 8,88,52
s_add_u32 m0, s50, 0
mav 12,92,52
s_waitcnt lgkmcnt(1)
ds_write_b128 v94, v[64:67] offset:0
mav 0,96,56
D 83,0
mav 4,100,56
R 36,9216
mav 8,104,56
LsR10:
s_cbranch_execz LsR14
ds_write_b128 v95, v[68:71] offset:0
mav 12,108,56
ds_read_b128 v[64:67], v96 offset:0
mav 0,112,60
D 83,1024
mav 4,116,60
R 40,10240
mav 8,120,60
ds_read_b128 v[68:71], v96 offset:1024
mav 12,124,60
s_waitcnt lgkmcnt(4)
mav 0,128,32
R 44,11264
mav 4,132,32
mav 8,136,32
mav 12,140,32
mav 0,144,36
R 48,12288
mav 4,148,36
mav 8,152,36
mav 12,156,36
s_waitcnt lgkmcnt(1)
mav 0,160,40
R 52,13312
mav 4,164,40
mav 8,168,40
mav 12,172,40
mav 0,176,44
D 83,2048
mav 4,180,44
R 56,14336
mav 8,184,44
mav 12,188,44
s_waitcnt lgkmcnt(1)
mav 0,192,48
D 83,3072
mav 4,196,48
R 60,15360
mav 8,200,48
mav 12,204,48
mav 0,208,52
buffer_store_dwordx4 v[64:67], v91, s[44:47], s54 offen
mav 4,212,52
mav 8,216,52
mav 12,220,52
s_waitcnt lgkmcnt(0)
mav 0,224,56
buffer_store_dwordx4 v[68:71], v92, s[44:47], s54 offen
mav 4,228,56
mav 8,232,56
mav 12,236,56
mav 0,240,60
buffer_load_dwordx4 v[164:167], v90, s[36:39], s49 offen sc1
mav 4,244,60
mav 8,248,60
mav 12,252,60
s_waitcnt vmcnt(16)
mvv 0,116,124
buffer_load_dwordx4 v[168:171], v90, s[36:39], s49 offen offset:1024 sc1
mvv 4,120,124
s_waitcnt vmcnt(16)
mvv 8,116,128
s_sub_u32 s54, s54, 32768
mvv 12,120,128
s_waitcnt vmcnt(15)
mvv 0,116,132
mvv 4,120,132
s_waitcnt vmcnt(14)
mvv 8,116,136
s_waitcnt vmcnt(13)
s_add_u32 s49, s49, 313344
mvv 12,120,136
s_barrier
mvv 0,116,140
R 32,32768
mvv 4,120,140
s_waitcnt vmcnt(12)
mvv 8,116,144
mvv 12,120,144
s_waitcnt vmcnt(11)
mvv 0,116,148
buffer_load_dwordx4 v[172:175], v86, s[36:39], s49 offen sc1
mvv 4,120,148
s_waitcnt vmcnt(11)
R 36,33792
mvv 8,116,152
mvv 12,120,152
s_waitcnt vmcnt(10)
mvv 0,116,156
buffer_load_dwordx4 v[176:179], v86, s[36:39], s49 offen offset:1024 sc1
mvv 4,120,156
s_waitcnt vmcnt(10)
R 40,34816
mvv 8,116,160
mvv 12,120,160
s_waitcnt lgkmcnt(1)
v_mfma_f32_16x16x32_bf16 v[16:19], a[0:3], v[32:35], v[100:103]
buffer_load_dwordx4 v[180:183], v87, s[36:39], s49 offen sc1
v_mfma_f32_16x16x32_bf16 v[20:23], a[4:7], v[32:35], v[104:107]
R 44,35840
v_cvt_pk_bf16_f32 v64, v0, v1
v_mfma_f32_16x16x32_bf16 v[24:27], a[8:11], v[32:35], v[108:111]
v_cvt_pk_bf16_f32 v65, v2, v3
v_cvt_pk_bf16_f32 v66, v4, v5
v_mfma_f32_16x16x32_bf16 v[28:31], a[12:15], v[32:35], v[112:115]
v_cvt_pk_bf16_f32 v67, v6, v7
v_cvt_pk_bf16_f32 v68, v8, v9
mav 16,16,36
buffer_load_dwordx4 v[184:187], v87, s[36:39], s49 offen offset:1024 sc1
mav 20,20,36
R 48,36864
v_cvt_pk_bf16_f32 v69, v10, v11
mav 24,24,36
v_cvt_pk_bf16_f32 v70, v12, v13
v_cvt_pk_bf16_f32 v71, v14, v15
mav 28,28,36
s_waitcnt lgkmcnt(1)
mav 16,32,40
buffer_load_dwordx4 v[188:191], v88, s[36:39], s49 offen sc1
mav 20,36,40
R 52,37888
mav 24,40,40
mav 28,44,40
mav 16,48,44
buffer_load_dwordx4 v[192:195], v88, s[36:39], s49 offen offset:1024 sc1
mav 20,52,44
R 56,38912
mav 24,56,44
mav 28,60,44
s_waitcnt lgkmcnt(1)
mav 16,64,48
buffer_load_dwordx4 v[196:199], v89, s[36:39], s49 offen sc1
mav 20,68,48
R 60,39936
mav 24,72,48
mav 28,76,48
mav 16,80,52
buffer_load_dwordx4 v[200:203], v89, s[36:39], s49 offen offset:1024 sc1
mav 20,84,52
R 32,40960
mav 24,88,52
s_add_u32 m0, s50, 32768
mav 28,92,52
s_waitcnt lgkmcnt(1)
ds_write_b128 v94, v[64:67] offset:0
mav 16,96,56
D 83,0
mav 20,100,56
R 36,41984
mav 24,104,56
ds_write_b128 v95, v[68:71] offset:0
mav 28,108,56
ds_read_b128 v[64:67], v96 offset:0
mav 16,112,60
D 83,1024
mav 20,116,60
R 40,43008
mav 24,120,60
ds_read_b128 v[68:71], v96 offset:1024
mav 28,124,60
s_waitcnt lgkmcnt(4)
mav 16,128,32
R 44,44032
mav 20,132,32
mav 24,136,32
mav 28,140,32
mav 16,144,36
R 48,45056
mav 20,148,36
mav 24,152,36
mav 28,156,36
s_waitcnt lgkmcnt(1)
s_add_u32 s60, s60, 1
mav 16,160,40
s_cbranch_scc0 LR1_2
mav 20,164,40
R 52,46080
mav 24,168,40
mav 28,172,40
mav 16,176,44
D 83,2048
mav 20,180,44
R 56,47104
mav 24,184,44
mav 28,188,44
s_waitcnt lgkmcnt(1)
mav 16,192,48
D 83,3072
mav 20,196,48
R 60,48128
mav 24,200,48
mav 28,204,48
mav 16,208,52
buffer_store_dwordx4 v[64:67], v91, s[44:47], s54 offen
mav 20,212,52
mav 24,216,52
mav 28,220,52
s_waitcnt lgkmcnt(0)
mav 16,224,56
buffer_store_dwordx4 v[68:71], v92, s[44:47], s54 offen
mav 20,228,56
mav 24,232,56
mav 28,236,56
mav 16,240,60
buffer_load_dwordx4 v[124:127], v90, s[36:39], s49 offen sc1
mav 20,244,60
mav 24,248,60
mav 28,252,60
s_waitcnt vmcnt(16)
mvv 16,116,164
buffer_load_dwordx4 v[128:131], v90, s[36:39], s49 offen offset:1024 sc1
mvv 20,120,164
s_waitcnt vmcnt(16)
mvv 24,116,168
s_sub_u32 s54, s54, 32768
mvv 28,120,168
s_waitcnt vmcnt(15)
mvv 16,116,172
mvv 20,120,172
s_waitcnt vmcnt(14)
v_xor_b32 v80, 0x10000, v80
mvv 24,116,176
s_waitcnt vmcnt(13)
s_add_u32 s49, s49, 313344
mvv 28,120,176
s_barrier
mvv 16,116,180
R 32,0
mvv 20,120,180
s_waitcnt vmcnt(12)
mvv 24,116,184
mvv 28,120,184
s_waitcnt vmcnt(11)
mvv 16,116,188
buffer_load_dwordx4 v[132:135], v86, s[36:39], s49 offen sc1
mvv 20,120,188
s_waitcnt vmcnt(11)
R 36,1024
mvv 24,116,192
mvv 28,120,192
s_waitcnt vmcnt(10)
mvv 16,116,196
buffer_load_dwordx4 v[136:139], v86, s[36:39], s49 offen offset:1024 sc1
mvv 20,120,196
s_waitcnt vmcnt(10)
R 40,2048
mvv 24,116,200
mvv 28,120,200
s_waitcnt lgkmcnt(1)
v_mfma_f32_16x16x32_bf16 v[0:3], a[0:3], v[32:35], v[100:103]
buffer_load_dwordx4 v[140:143], v87, s[36:39], s49 offen sc1
v_mfma_f32_16x16x32_bf16 v[4:7], a[4:7], v[32:35], v[104:107]
R 44,3072
v_cvt_pk_bf16_f32 v64, v16, v17
v_mfma_f32_16x16x32_bf16 v[8:11], a[8:11], v[32:35], v[108:111]
v_cvt_pk_bf16_f32 v65, v18, v19
v_cvt_pk_bf16_f32 v66, v20, v21
v_mfma_f32_16x16x32_bf16 v[12:15], a[12:15], v[32:35], v[112:115]
v_cvt_pk_bf16_f32 v67, v22, v23
v_cvt_pk_bf16_f32 v68, v24, v25
mav 0,16,36
buffer_load_dwordx4 v[144:147], v87, s[36:39], s49 offen offset:1024 sc1
mav 4,20,36
R 48,4096
v_cvt_pk_bf16_f32 v69, v26, v27
mav 8,24,36
v_cvt_pk_bf16_f32 v70, v28, v29
v_cvt_pk_bf16_f32 v71, v30, v31
mav 12,28,36
s_waitcnt lgkmcnt(1)
mav 0,32,40
buffer_load_dwordx4 v[148:151], v88, s[36:39], s49 offen sc1
mav 4,36,40
R 52,5120
mav 8,40,40
mav 12,44,40
mav 0,48,44
buffer_load_dwordx4 v[152:155], v88, s[36:39], s49 offen offset:1024 sc1
mav 4,52,44
R 56,6144
mav 8,56,44
mav 12,60,44
s_waitcnt lgkmcnt(1)
mav 0,64,48
buffer_load_dwordx4 v[156:159], v89, s[36:39], s49 offen sc1
mav 4,68,48
R 60,7168
mav 8,72,48
mav 12,76,48
mav 0,80,52
buffer_load_dwordx4 v[160:163], v89, s[36:39], s49 offen offset:1024 sc1
mav 4,84,52
R 32,8192
mav 8,88,52
mav 12,92,52
s_waitcnt lgkmcnt(1)
ds_write_b128 v94, v[64:67] offset:0
mav 0,96,56
R 36,9216
mav 4,100,56
ds_write_b128 v95, v[68:71] offset:0
mav 8,104,56
ds_read_b128 v[64:67], v96 offset:0
mav 12,108,56
ds_read_b128 v[68:71], v96 offset:1024
mav 0,112,60
R 40,10240
mav 4,116,60
mav 8,120,60
mav 12,124,60
s_waitcnt lgkmcnt(3)
mav 0,128,32
R 44,11264
mav 4,132,32
mav 8,136,32
mav 12,140,32
mav 0,144,36
R 48,12288
mav 4,148,36
mav 8,152,36
mav 12,156,36
s_waitcnt lgkmcnt(1)
mav 0,160,40
R 52,13312
mav 4,164,40
mav 8,168,40
mav 12,172,40
mav 0,176,44
R 56,14336
mav 4,180,44
mav 8,184,44
mav 12,188,44
s_waitcnt lgkmcnt(1)
mav 0,192,48
R 60,15360
mav 4,196,48
mav 8,200,48
mav 12,204,48
mav 0,208,52
buffer_store_dwordx4 v[64:67], v91, s[44:47], s54 offen
mav 4,212,52
mav 8,216,52
mav 12,220,52
s_waitcnt lgkmcnt(0)
mav 0,224,56
buffer_store_dwordx4 v[68:71], v92, s[44:47], s54 offen
mav 4,228,56
mav 8,232,56
mav 12,236,56
mav 0,240,60
buffer_load_dwordx4 v[164:167], v90, s[36:39], s49 offen sc1
mav 4,244,60
mav 8,248,60
mav 12,252,60
s_waitcnt vmcnt(12)
mvv 0,116,124
buffer_load_dwordx4 v[168:171], v90, s[36:39], s49 offen offset:1024 sc1
mvv 4,120,124
s_waitcnt vmcnt(12)
mvv 8,116,128
s_sub_u32 s54, s54, 32768
mvv 12,120,128
s_waitcnt vmcnt(11)
mvv 0,116,132
mvv 4,120,132
s_waitcnt vmcnt(10)
mvv 8,116,136
s_waitcnt vmcnt(9)
s_add_u32 s49, s49, 313344
mvv 12,120,136
s_barrier
mvv 0,116,140
R 32,32768
mvv 4,120,140
s_waitcnt vmcnt(8)
mvv 8,116,144
mvv 12,120,144
s_waitcnt vmcnt(7)
mvv 0,116,148
buffer_load_dwordx4 v[172:175], v86, s[36:39], s49 offen sc1
mvv 4,120,148
s_waitcnt vmcnt(7)
R 36,33792
mvv 8,116,152
mvv 12,120,152
s_waitcnt vmcnt(6)
mvv 0,116,156
buffer_load_dwordx4 v[176:179], v86, s[36:39], s49 offen offset:1024 sc1
mvv 4,120,156
s_waitcnt vmcnt(6)
R 40,34816
mvv 8,116,160
mvv 12,120,160
s_waitcnt lgkmcnt(1)
v_mfma_f32_16x16x32_bf16 v[16:19], a[0:3], v[32:35], v[100:103]
buffer_load_dwordx4 v[180:183], v87, s[36:39], s49 offen sc1
v_mfma_f32_16x16x32_bf16 v[20:23], a[4:7], v[32:35], v[104:107]
R 44,35840
v_cvt_pk_bf16_f32 v64, v0, v1
v_mfma_f32_16x16x32_bf16 v[24:27], a[8:11], v[32:35], v[108:111]
v_cvt_pk_bf16_f32 v65, v2, v3
v_cvt_pk_bf16_f32 v66, v4, v5
v_mfma_f32_16x16x32_bf16 v[28:31], a[12:15], v[32:35], v[112:115]
v_cvt_pk_bf16_f32 v67, v6, v7
v_cvt_pk_bf16_f32 v68, v8, v9
mav 16,16,36
buffer_load_dwordx4 v[184:187], v87, s[36:39], s49 offen offset:1024 sc1
mav 20,20,36
R 48,36864
v_cvt_pk_bf16_f32 v69, v10, v11
mav 24,24,36
ds_write_b128 v94, v[64:67] offset:0
mav 28,28,36
s_waitcnt lgkmcnt(2)
v_cvt_pk_bf16_f32 v70, v12, v13
v_cvt_pk_bf16_f32 v71, v14, v15
mav 16,32,40
buffer_load_dwordx4 v[188:191], v88, s[36:39], s49 offen sc1
mav 20,36,40
R 52,37888
mav 24,40,40
ds_write_b128 v95, v[68:71] offset:0
mav 28,44,40
ds_read_b128 v[64:67], v96 offset:0
mav 16,48,44
buffer_load_dwordx4 v[192:195], v88, s[36:39], s49 offen offset:1024 sc1
mav 20,52,44
R 56,38912
mav 24,56,44
ds_read_b128 v[68:71], v96 offset:1024
mav 28,60,44
s_waitcnt lgkmcnt(4)
mav 16,64,48
buffer_load_dwordx4 v[196:199], v89, s[36:39], s49 offen sc1
mav 20,68,48
R 60,39936
mav 24,72,48
s_waitcnt lgkmcnt(3)
mav 28,76,48
mav 16,80,52
buffer_store_dwordx4 v[64:67], v91, s[44:47], s54 offen
buffer_load_dwordx4 v[200:203], v89, s[36:39], s49 offen offset:1024 sc1
mav 20,84,52
s_waitcnt lgkmcnt(1)
R 32,40960
mav 24,88,52
mav 28,92,52
mav 16,96,56
buffer_store_dwordx4 v[68:71], v92, s[44:47], s54 offen
R 36,41984
)"
R"(
mav 20,100,56
mav 24,104,56
mav 28,108,56
s_waitcnt lgkmcnt(1)
mav 16,112,60
R 40,43008
mav 20,116,60
mav 24,120,60
mav 28,124,60
mav 16,128,32
R 44,44032
mav 20,132,32
mav 24,136,32
mav 28,140,32
s_waitcnt lgkmcnt(1)
mav 16,144,36
R 48,45056
mav 20,148,36
mav 24,152,36
mav 28,156,36
mav 16,160,40
mav 20,164,40
R 52,46080
mav 24,168,40
mav 28,172,40
s_waitcnt lgkmcnt(1)
mav 16,176,44
R 56,47104
mav 20,180,44
mav 24,184,44
mav 28,188,44
mav 16,192,48
R 60,48128
mav 20,196,48
mav 24,200,48
mav 28,204,48
s_waitcnt lgkmcnt(1)
mav 16,208,52
mav 20,212,52
mav 24,216,52
mav 28,220,52
mav 16,224,56
mav 20,228,56
mav 24,232,56
mav 28,236,56
s_waitcnt lgkmcnt(0)
mav 16,240,60
mav 20,244,60
mav 24,248,60
mav 28,252,60
s_waitcnt vmcnt(11)
mvv 16,116,164
mvv 20,120,164
s_waitcnt vmcnt(10)
mvv 24,116,168
s_sub_u32 s54, s54, 32768
mvv 28,120,168
s_waitcnt vmcnt(9)
mvv 16,116,172
mvv 20,120,172
s_waitcnt vmcnt(8)
mvv 24,116,176
mvv 28,120,176
s_waitcnt vmcnt(7)
mvv 16,116,180
mvv 20,120,180
s_waitcnt vmcnt(6)
mvv 24,116,184
mvv 28,120,184
s_waitcnt vmcnt(5)
mvv 16,116,188
mvv 20,120,188
s_waitcnt vmcnt(4)
mvv 24,116,192
mvv 28,120,192
s_waitcnt vmcnt(3)
mvv 16,116,196
mvv 20,120,196
s_waitcnt vmcnt(1)
mvv 24,116,200
mvv 28,120,200
s_nop 3
v_cvt_pk_bf16_f32 v64, v16, v17
v_cvt_pk_bf16_f32 v65, v18, v19
v_cvt_pk_bf16_f32 v66, v20, v21
v_cvt_pk_bf16_f32 v67, v22, v23
ds_write_b128 v94, v[64:67] offset:0
v_cvt_pk_bf16_f32 v68, v24, v25
v_cvt_pk_bf16_f32 v69, v26, v27
v_cvt_pk_bf16_f32 v70, v28, v29
v_cvt_pk_bf16_f32 v71, v30, v31
ds_write_b128 v95, v[68:71] offset:0
ds_read_b128 v[64:67], v96 offset:0
ds_read_b128 v[68:71], v96 offset:1024
s_waitcnt lgkmcnt(0)
buffer_store_dwordx4 v[64:67], v91, s[44:47], s54 offen
buffer_store_dwordx4 v[68:71], v92, s[44:47], s54 offen
s_endpgm
LvR2:
LsR11:
s_cbranch_execz LsR15
s_add_u32 s49, s49, 313344
s_barrier
s_add_u32 m0, s50, 0
LsR12:
s_cbranch_execz LsR16
s_nop 0
D 83,0
D 83,1024
D 83,2048
D 83,3072
s_add_u32 s49, s49, 313344
s_barrier
s_add_u32 m0, s50, 32768
s_nop 0
D 83,0
D 83,1024
D 83,2048
D 83,3072
buffer_load_dwordx4 v[124:127], v90, s[36:39], s49 offen sc1
buffer_load_dwordx4 v[128:131], v90, s[36:39], s49 offen offset:1024 sc1
s_xor_b32 s50, s50, 0x10000
s_waitcnt vmcnt(6)
s_add_u32 s49, s49, 313344
s_barrier
R 32,0
buffer_load_dwordx4 v[132:135], v86, s[36:39], s49 offen sc1
R 36,1024
buffer_load_dwordx4 v[136:139], v86, s[36:39], s49 offen offset:1024 sc1
R 40,2048
buffer_load_dwordx4 v[140:143], v87, s[36:39], s49 offen sc1
s_waitcnt lgkmcnt(1)
v_mfma_f32_16x16x32_bf16 v[0:3], a[0:3], v[32:35], v[100:103]
R 44,3072
v_mfma_f32_16x16x32_bf16 v[4:7], a[4:7], v[32:35], v[104:107]
v_mfma_f32_16x16x32_bf16 v[8:11], a[8:11], v[32:35], v[108:111]
buffer_load_dwordx4 v[144:147], v87, s[36:39], s49 offen offset:1024 sc1
v_mfma_f32_16x16x32_bf16 v[12:15], a[12:15], v[32:35], v[112:115]
mav 0,16,36
R 48,4096
mav 4,20,36
mav 8,24,36
buffer_load_dwordx4 v[148:151], v88, s[36:39], s49 offen sc1
mav 12,28,36
s_waitcnt lgkmcnt(1)
mav 0,32,40
R 52,5120
mav 4,36,40
mav 8,40,40
buffer_load_dwordx4 v[152:155], v88, s[36:39], s49 offen offset:1024 sc1
mav 12,44,40
mav 0,48,44
R 56,6144
mav 4,52,44
mav 8,56,44
buffer_load_dwordx4 v[156:159], v89, s[36:39], s49 offen sc1
mav 12,60,44
s_waitcnt lgkmcnt(1)
mav 0,64,48
R 60,7168
mav 4,68,48
mav 8,72,48
buffer_load_dwordx4 v[160:163], v89, s[36:39], s49 offen offset:1024 sc1
mav 12,76,48
mav 0,80,52
R 32,8192
mav 4,84,52
mav 8,88,52
s_add_u32 m0, s50, 0
mav 12,92,52
s_waitcnt lgkmcnt(1)
mav 0,96,56
R 36,9216
mav 4,100,56
mav 8,104,56
D 83,0
mav 12,108,56
mav 0,112,60
R 40,10240
mav 4,116,60
mav 8,120,60
D 83,1024
mav 12,124,60
s_waitcnt lgkmcnt(1)
mav 0,128,32
R 44,11264
mav 4,132,32
mav 8,136,32
mav 12,140,32
mav 0,144,36
R 48,12288
mav 4,148,36
mav 8,152,36
mav 12,156,36
s_waitcnt lgkmcnt(1)
mav 0,160,40
R 52,13312
mav 4,164,40
mav 8,168,40
mav 12,172,40
mav 0,176,44
R 56,14336
mav 4,180,44
mav 8,184,44
D 83,2048
mav 12,188,44
s_waitcnt lgkmcnt(1)
mav 0,192,48
R 60,15360
mav 4,196,48
mav 8,200,48
D 83,3072
mav 12,204,48
mav 0,208,52
mav 4,212,52
mav 8,216,52
mav 12,220,52
s_waitcnt lgkmcnt(0)
mav 0,224,56
mav 4,228,56
mav 8,232,56
mav 12,236,56
mav 0,240,60
mav 4,244,60
mav 8,248,60
buffer_load_dwordx4 v[164:167], v90, s[36:39], s49 offen sc1
mav 12,252,60
s_waitcnt vmcnt(14)
mvv 0,116,124
mvv 4,120,124
s_waitcnt vmcnt(13)
mvv 8,116,128
buffer_load_dwordx4 v[168:171], v90, s[36:39], s49 offen offset:1024 sc1
mvv 12,120,128
s_waitcnt vmcnt(13)
mvv 0,116,132
mvv 4,120,132
s_waitcnt vmcnt(12)
mvv 8,116,136
s_waitcnt vmcnt(11)
s_add_u32 s49, s49, 313344
mvv 12,120,136
s_barrier
mvv 0,116,140
R 32,32768
mvv 4,120,140
s_waitcnt vmcnt(10)
mvv 8,116,144
buffer_load_dwordx4 v[172:175], v86, s[36:39], s49 offen sc1
mvv 12,120,144
s_waitcnt vmcnt(10)
mvv 0,116,148
R 36,33792
mvv 4,120,148
s_waitcnt vmcnt(9)
mvv 8,116,152
buffer_load_dwordx4 v[176:179], v86, s[36:39], s49 offen offset:1024 sc1
mvv 12,120,152
s_waitcnt vmcnt(9)
mvv 0,116,156
R 40,34816
mvv 4,120,156
s_waitcnt vmcnt(8)
mvv 8,116,160
buffer_load_dwordx4 v[180:183], v87, s[36:39], s49 offen sc1
mvv 12,120,160
s_waitcnt lgkmcnt(1)
v_mfma_f32_16x16x32_bf16 v[16:19], a[0:3], v[32:35], v[100:103]
R 44,35840
v_cvt_pk_bf16_f32 v64, v0, v1
v_mfma_f32_16x16x32_bf16 v[20:23], a[4:7], v[32:35], v[104:107]
v_cvt_pk_bf16_f32 v65, v2, v3
v_cvt_pk_bf16_f32 v66, v4, v5
v_mfma_f32_16x16x32_bf16 v[24:27], a[8:11], v[32:35], v[108:111]
buffer_load_dwordx4 v[184:187], v87, s[36:39], s49 offen offset:1024 sc1
v_mfma_f32_16x16x32_bf16 v[28:31], a[12:15], v[32:35], v[112:115]
v_cvt_pk_bf16_f32 v67, v6, v7
v_cvt_pk_bf16_f32 v68, v8, v9
mav 16,16,36
R 48,36864
v_cvt_pk_bf16_f32 v69, v10, v11
mav 20,20,36
v_cvt_pk_bf16_f32 v70, v12, v13
v_cvt_pk_bf16_f32 v71, v14, v15
mav 24,24,36
buffer_load_dwordx4 v[188:191], v88, s[36:39], s49 offen sc1
mav 28,28,36
s_waitcnt lgkmcnt(1)
v_mul_f32 v75, v0, v0
v_add_f32 v74, v1, v0
mav 16,32,40
R 52,37888
v_fmac_f32 v75, v1, v1
mav 20,36,40
v_add_f32 v74, v2, v74
v_fmac_f32 v75, v2, v2
mav 24,40,40
buffer_load_dwordx4 v[192:195], v88, s[36:39], s49 offen offset:1024 sc1
mav 28,44,40
v_add_f32 v74, v3, v74
v_fmac_f32 v75, v3, v3
mav 16,48,44
R 56,38912
v_add_f32 v74, v4, v74
mav 20,52,44
v_fmac_f32 v75, v4, v4
v_add_f32 v74, v5, v74
mav 24,56,44
buffer_load_dwordx4 v[196:199], v89, s[36:39], s49 offen sc1
mav 28,60,44
s_waitcnt lgkmcnt(1)
v_fmac_f32 v75, v5, v5
v_add_f32 v74, v6, v74
mav 16,64,48
R 60,39936
v_fmac_f32 v75, v6, v6
mav 20,68,48
v_add_f32 v74, v7, v74
v_fmac_f32 v75, v7, v7
mav 24,72,48
buffer_load_dwordx4 v[200:203], v89, s[36:39], s49 offen offset:1024 sc1
mav 28,76,48
v_add_f32 v74, v8, v74
v_fmac_f32 v75, v8, v8
mav 16,80,52
R 32,40960
v_add_f32 v74, v9, v74
mav 20,84,52
v_fmac_f32 v75, v9, v9
v_add_f32 v74, v10, v74
mav 24,88,52
s_add_u32 m0, s50, 32768
v_fmac_f32 v75, v10, v10
v_add_f32 v74, v11, v74
mav 28,92,52
s_waitcnt lgkmcnt(1)
v_fmac_f32 v75, v11, v11
v_add_f32 v74, v12, v74
mav 16,96,56
R 36,41984
v_fmac_f32 v75, v12, v12
mav 20,100,56
v_add_f32 v74, v13, v74
v_fmac_f32 v75, v13, v13
mav 24,104,56
D 83,0
mav 28,108,56
ds_write_b128 v94, v[64:67] offset:0
mav 16,112,60
R 40,43008
v_add_f32 v74, v14, v74
mav 20,116,60
ds_write_b128 v95, v[68:71] offset:0
mav 24,120,60
D 83,1024
mav 28,124,60
s_waitcnt lgkmcnt(3)
ds_read_b128 v[64:67], v96 offset:0
v_fmac_f32 v75, v14, v14
mav 16,128,32
R 44,44032
v_add_f32 v74, v15, v74
mav 20,132,32
ds_read_b128 v[68:71], v96 offset:1024
v_fmac_f32 v75, v15, v15
mav 24,136,32
mav 28,140,32
v_permlane32_swap_b32 v74, v75
mav 16,144,36
R 48,45056
v_add_f32 v74, v74, v75
mav 20,148,36
mav 24,152,36
mav 28,156,36
s_waitcnt lgkmcnt(1)
mav 16,160,40
mav 20,164,40
R 52,46080
mav 24,168,40
mav 28,172,40
mav 16,176,44
R 56,47104
mav 20,180,44
mav 24,184,44
D 83,2048
mav 28,188,44
s_waitcnt lgkmcnt(1)
mav 16,192,48
R 60,48128
mav 20,196,48
mav 24,200,48
D 83,3072
mav 28,204,48
mav 16,208,52
mav 20,212,52
mav 24,216,52
buffer_store_dwordx4 v[64:67], v91, s[36:39], s54 offen sc1
mav 28,220,52
s_waitcnt lgkmcnt(0)
mav 16,224,56
mav 20,228,56
mav 24,232,56
buffer_store_dwordx4 v[68:71], v92, s[36:39], s54 offen sc1
mav 28,236,56
mav 16,240,60
mav 20,244,60
mav 24,248,60
buffer_load_dwordx4 v[124:127], v90, s[36:39], s49 offen sc1
mav 28,252,60
s_waitcnt vmcnt(16)
mvv 16,116,164
mvv 20,120,164
s_waitcnt vmcnt(15)
mvv 24,116,168
buffer_load_dwordx4 v[128:131], v90, s[36:39], s49 offen offset:1024 sc1
mvv 28,120,168
s_waitcnt vmcnt(15)
s_xor_b32 s50, s50, 0x10000
s_sub_u32 s54, s54, 32768
mvv 16,116,172
mvv 20,120,172
s_waitcnt vmcnt(14)
v_xor_b32 v80, 0x10000, v80
mvv 24,116,176
s_waitcnt vmcnt(13)
s_add_u32 s49, s49, 313344
mvv 28,120,176
s_barrier
mvv 16,116,180
R 32,0
mvv 20,120,180
s_waitcnt vmcnt(12)
mvv 24,116,184
buffer_load_dwordx4 v[132:135], v86, s[36:39], s49 offen sc1
mvv 28,120,184
s_waitcnt vmcnt(12)
mvv 16,116,188
R 36,1024
mvv 20,120,188
s_waitcnt vmcnt(11)
mvv 24,116,192
buffer_load_dwordx4 v[136:139], v86, s[36:39], s49 offen offset:1024 sc1
mvv 28,120,192
s_waitcnt vmcnt(11)
mvv 16,116,196
R 40,2048
mvv 20,120,196
s_waitcnt vmcnt(10)
mvv 24,116,200
buffer_load_dwordx4 v[140:143], v87, s[36:39], s49 offen sc1
mvv 28,120,200
s_waitcnt lgkmcnt(1)
v_mfma_f32_16x16x32_bf16 v[0:3], a[0:3], v[32:35], v[100:103]
R 44,3072
v_cvt_pk_bf16_f32 v64, v16, v17
v_mfma_f32_16x16x32_bf16 v[4:7], a[4:7], v[32:35], v[104:107]
v_cvt_pk_bf16_f32 v65, v18, v19
v_cvt_pk_bf16_f32 v66, v20, v21
v_mfma_f32_16x16x32_bf16 v[8:11], a[8:11], v[32:35], v[108:111]
buffer_load_dwordx4 v[144:147], v87, s[36:39], s49 offen offset:1024 sc1
v_mfma_f32_16x16x32_bf16 v[12:15], a[12:15], v[32:35], v[112:115]
v_cvt_pk_bf16_f32 v67, v22, v23
v_cvt_pk_bf16_f32 v68, v24, v25
mav 0,16,36
R 48,4096
v_cvt_pk_bf16_f32 v69, v26, v27
mav 4,20,36
v_cvt_pk_bf16_f32 v70, v28, v29
v_cvt_pk_bf16_f32 v71, v30, v31
mav 8,24,36
buffer_load_dwordx4 v[148:151], v88, s[36:39], s49 offen sc1
mav 12,28,36
s_waitcnt lgkmcnt(1)
v_mul_f32 v75, v16, v16
v_fmac_f32 v75, v17, v17
mav 0,32,40
R 52,5120
v_fmac_f32 v75, v18, v18
mav 4,36,40
v_fmac_f32 v75, v19, v19
v_fmac_f32 v75, v20, v20
mav 8,40,40
buffer_load_dwordx4 v[152:155], v88, s[36:39], s49 offen offset:1024 sc1
mav 12,44,40
v_fmac_f32 v75, v21, v21
v_fmac_f32 v75, v22, v22
mav 0,48,44
R 56,6144
v_fmac_f32 v75, v23, v23
mav 4,52,44
v_fmac_f32 v75, v24, v24
v_fmac_f32 v75, v25, v25
mav 8,56,44
buffer_load_dwordx4 v[156:159], v89, s[36:39], s49 offen sc1
mav 12,60,44
s_waitcnt lgkmcnt(1)
v_fmac_f32 v75, v26, v26
v_fmac_f32 v75, v27, v27
mav 0,64,48
R 60,7168
v_fmac_f32 v75, v28, v28
mav 4,68,48
v_fmac_f32 v75, v29, v29
v_fmac_f32 v75, v30, v30
mav 8,72,48
buffer_load_dwordx4 v[160:163], v89, s[36:39], s49 offen offset:1024 sc1
mav 12,76,48
v_fmac_f32 v75, v31, v31
mav 0,80,52
R 32,8192
mav 4,84,52
mav 8,88,52
s_add_u32 m0, s50, 0
mav 12,92,52
s_waitcnt lgkmcnt(1)
mav 0,96,56
R 36,9216
mav 4,100,56
mav 8,104,56
D 83,0
mav 12,108,56
ds_write_b32 v97, v74 offset:0
mav 0,112,60
R 40,10240
v_add_f32 v74, v17, v16
mav 4,116,60
ds_write_b128 v94, v[64:67] offset:0
mav 8,120,60
D 83,1024
mav 12,124,60
s_waitcnt lgkmcnt(3)
ds_write_b128 v95, v[68:71] offset:0
mav 0,128,32
R 44,11264
v_add_f32 v74, v18, v74
mav 4,132,32
ds_read_b128 v[64:67], v96 offset:0
v_add_f32 v74, v19, v74
mav 8,136,32
ds_read_b128 v[68:71], v96 offset:1024
v_add_f32 v74, v20, v74
mav 12,140,32
v_add_f32 v74, v21, v74
v_add_f32 v74, v22, v74
mav 0,144,36
R 48,12288
v_add_f32 v74, v23, v74
mav 4,148,36
v_add_f32 v74, v24, v74
v_add_f32 v74, v25, v74
mav 8,152,36
v_add_f32 v74, v26, v74
v_add_f32 v74, v27, v74
mav 12,156,36
s_waitcnt lgkmcnt(2)
v_add_f32 v74, v28, v74
v_add_f32 v74, v29, v74
mav 0,160,40
R 52,13312
v_add_f32 v74, v30, v74
mav 4,164,40
v_add_f32 v74, v31, v74
v_add_u32 v72, 1, v72
mav 8,168,40
v_permlane32_swap_b32 v74, v75
mav 12,172,40
v_add_f32 v74, v74, v75
mav 0,176,44
R 56,14336
mav 4,180,44
mav 8,184,44
D 83,2048
mav 12,188,44
s_waitcnt lgkmcnt(1)
mav 0,192,48
R 60,15360
mav 4,196,48
mav 8,200,48
D 83,3072
mav 12,204,48
mav 0,208,52
mav 4,212,52
mav 8,216,52
buffer_store_dwordx4 v[64:67], v91, s[36:39], s54 offen sc1
mav 12,220,52
s_waitcnt lgkmcnt(0)
mav 0,224,56
mav 4,228,56
mav 8,232,56
buffer_store_dwordx4 v[68:71], v92, s[36:39], s54 offen sc1
mav 12,236,56
mav 0,240,60
mav 4,244,60
mav 8,248,60
buffer_load_dwordx4 v[164:167], v90, s[36:39], s49 offen sc1
mav 12,252,60
s_waitcnt vmcnt(16)
mvv 0,116,124
buffer_store_dword v72, v81, s[36:39], s52 offen sc1
mvv 4,120,124
s_waitcnt vmcnt(16)
mvv 8,116,128
buffer_load_dwordx4 v[168:171], v90, s[36:39], s49 offen offset:1024 sc1
mvv 12,120,128
s_waitcnt vmcnt(16)
s_sub_u32 s54, s54, 32768
mvv 0,116,132
mvv 4,120,132
s_waitcnt vmcnt(15)
mvv 8,116,136
s_waitcnt vmcnt(14)
s_add_u32 s49, s49, 313344
mvv 12,120,136
s_barrier
mvv 0,116,140
R 32,32768
mvv 4,120,140
s_waitcnt vmcnt(13)
mvv 8,116,144
buffer_load_dwordx4 v[172:175], v86, s[36:39], s49 offen sc1
mvv 12,120,144
s_waitcnt vmcnt(13)
mvv 0,116,148
R 36,33792
mvv 4,120,148
s_waitcnt vmcnt(12)
mvv 8,116,152
buffer_load_dwordx4 v[176:179], v86, s[36:39], s49 offen offset:1024 sc1
mvv 12,120,152
s_waitcnt vmcnt(12)
mvv 0,116,156
R 40,34816
mvv 4,120,156
s_waitcnt vmcnt(11)
mvv 8,116,160
buffer_load_dwordx4 v[180:183], v87, s[36:39], s49 offen sc1
mvv 12,120,160
s_waitcnt lgkmcnt(1)
v_mfma_f32_16x16x32_bf16 v[16:19], a[0:3], v[32:35], v[100:103]
R 44,35840
v_cvt_pk_bf16_f32 v64, v0, v1
v_mfma_f32_16x16x32_bf16 v[20:23], a[4:7], v[32:35], v[104:107]
v_cvt_pk_bf16_f32 v65, v2, v3
v_cvt_pk_bf16_f32 v66, v4, v5
v_mfma_f32_16x16x32_bf16 v[24:27], a[8:11], v[32:35], v[108:111]
buffer_load_dwordx4 v[184:187], v87, s[36:39], s49 offen offset:1024 sc1
v_mfma_f32_16x16x32_bf16 v[28:31], a[12:15], v[32:35], v[112:115]
v_cvt_pk_bf16_f32 v67, v6, v7
v_cvt_pk_bf16_f32 v68, v8, v9
mav 16,16,36
R 48,36864
v_cvt_pk_bf16_f32 v69, v10, v11
mav 20,20,36
v_cvt_pk_bf16_f32 v70, v12, v13
v_cvt_pk_bf16_f32 v71, v14, v15
mav 24,24,36
buffer_load_dwordx4 v[188:191], v88, s[36:39], s49 offen sc1
mav 28,28,36
s_waitcnt lgkmcnt(1)
v_mul_f32 v75, v0, v0
v_fmac_f32 v75, v1, v1
mav 16,32,40
R 52,37888
v_fmac_f32 v75, v2, v2
mav 20,36,40
v_fmac_f32 v75, v3, v3
v_fmac_f32 v75, v4, v4
mav 24,40,40
buffer_load_dwordx4 v[192:195], v88, s[36:39], s49 offen offset:1024 sc1
mav 28,44,40
v_fmac_f32 v75, v5, v5
v_fmac_f32 v75, v6, v6
mav 16,48,44
R 56,38912
v_fmac_f32 v75, v7, v7
mav 20,52,44
v_fmac_f32 v75, v8, v8
v_fmac_f32 v75, v9, v9
mav 24,56,44
buffer_load_dwordx4 v[196:199], v89, s[36:39], s49 offen sc1
mav 28,60,44
s_waitcnt lgkmcnt(1)
v_fmac_f32 v75, v10, v10
v_fmac_f32 v75, v11, v11
mav 16,64,48
R 60,39936
v_fmac_f32 v75, v12, v12
mav 20,68,48
v_fmac_f32 v75, v13, v13
v_fmac_f32 v75, v14, v14
mav 24,72,48
buffer_load_dwordx4 v[200:203], v89, s[36:39], s49 offen offset:1024 sc1
LsR13:
s_cbranch_execz LsR17
mav 28,76,48
v_fmac_f32 v75, v15, v15
mav 16,80,52
R 32,40960
mav 20,84,52
mav 24,88,52
s_add_u32 m0, s50, 32768
mav 28,92,52
s_waitcnt lgkmcnt(1)
mav 16,96,56
R 36,41984
mav 20,100,56
mav 24,104,56
D 83,0
mav 28,108,56
ds_write_b32 v97, v74 offset:1024
mav 16,112,60
R 40,43008
v_add_f32 v74, v1, v0
mav 20,116,60
ds_write_b128 v94, v[64:67] offset:0
mav 24,120,60
D 83,1024
mav 28,124,60
s_waitcnt lgkmcnt(3)
ds_write_b128 v95, v[68:71] offset:0
mav 16,128,32
R 44,44032
v_add_f32 v74, v2, v74
mav 20,132,32
ds_read_b128 v[64:67], v96 offset:0
v_add_f32 v74, v3, v74
mav 24,136,32
ds_read_b128 v[68:71], v96 offset:1024
v_add_f32 v74, v4, v74
mav 28,140,32
v_add_f32 v74, v5, v74
v_add_f32 v74, v6, v74
mav 16,144,36
R 48,45056
v_add_f32 v74, v7, v74
mav 20,148,36
v_add_f32 v74, v8, v74
v_add_f32 v74, v9, v74
mav 24,152,36
v_add_f32 v74, v10, v74
v_add_f32 v74, v11, v74
mav 28,156,36
s_waitcnt lgkmcnt(2)
v_add_f32 v74, v12, v74
mav 16,160,40
s_mov_b32 s60, -14
.p2align 6
LR2_1:
mav 20,164,40
R 52,46080
v_add_f32 v74, v13, v74
mav 24,168,40
v_add_f32 v74, v14, v74
v_add_f32 v74, v15, v74
mav 28,172,40
v_add_u32 v72, 1, v72
mav 16,176,44
R 56,47104
mav 20,180,44
v_permlane32_swap_b32 v74, v75
mav 24,184,44
D 83,2048
mav 28,188,44
s_waitcnt lgkmcnt(1)
v_add_f32 v74, v74, v75
mav 16,192,48
R 60,48128
mav 20,196,48
mav 24,200,48
D 83,3072
mav 28,204,48
mav 16,208,52
mav 20,212,52
mav 24,216,52
buffer_store_dwordx4 v[64:67], v91, s[36:39], s54 offen sc1
mav 28,220,52
s_waitcnt lgkmcnt(0)
mav 16,224,56
mav 20,228,56
mav 24,232,56
buffer_store_dwordx4 v[68:71], v92, s[36:39], s54 offen sc1
mav 28,236,56
mav 16,240,60
mav 20,244,60
mav 24,248,60
buffer_load_dwordx4 v[124:127], v90, s[36:39], s49 offen sc1
mav 28,252,60
s_waitcnt vmcnt(17)
mvv 16,116,164
buffer_store_dword v72, v81, s[36:39], s52 offen sc1
mvv 20,120,164
s_waitcnt vmcnt(16)
mvv 24,116,168
buffer_load_dwordx4 v[128:131], v90, s[36:39], s49 offen offset:1024 sc1
mvv 28,120,168
s_waitcnt vmcnt(16)
s_xor_b32 s50, s50, 0x10000
s_sub_u32 s54, s54, 32768
mvv 16,116,172
mvv 20,120,172
s_waitcnt vmcnt(15)
v_xor_b32 v80, 0x10000, v80
mvv 24,116,176
s_waitcnt vmcnt(14)
s_add_u32 s49, s49, 313344
mvv 28,120,176
s_barrier
mvv 16,116,180
R 32,0
mvv 20,120,180
s_waitcnt vmcnt(13)
mvv 24,116,184
buffer_load_dwordx4 v[132:135], v86, s[36:39], s49 offen sc1
mvv 28,120,184
s_waitcnt vmcnt(13)
mvv 16,116,188
R 36,1024
mvv 20,120,188
s_waitcnt vmcnt(12)
mvv 24,116,192
buffer_load_dwordx4 v[136:139], v86, s[36:39], s49 offen offset:1024 sc1
mvv 28,120,192
s_waitcnt vmcnt(12)
mvv 16,116,196
R 40,2048
mvv 20,120,196
s_waitcnt vmcnt(11)
mvv 24,116,200
buffer_load_dwordx4 v[140:143], v87, s[36:39], s49 offen sc1
mvv 28,120,200
s_waitcnt lgkmcnt(1)
v_mfma_f32_16x16x32_bf16 v[0:3], a[0:3], v[32:35], v[100:103]
R 44,3072
v_cvt_pk_bf16_f32 v64, v16, v17
v_mfma_f32_16x16x32_bf16 v[4:7], a[4:7], v[32:35], v[104:107]
v_cvt_pk_bf16_f32 v65, v18, v19
v_cvt_pk_bf16_f32 v66, v20, v21
v_mfma_f32_16x16x32_bf16 v[8:11], a[8:11], v[32:35], v[108:111]
buffer_load_dwordx4 v[144:147], v87, s[36:39], s49 offen offset:1024 sc1
v_mfma_f32_16x16x32_bf16 v[12:15], a[12:15], v[32:35], v[112:115]
v_cvt_pk_bf16_f32 v67, v22, v23
v_cvt_pk_bf16_f32 v68, v24, v25
mav 0,16,36
R 48,4096
v_cvt_pk_bf16_f32 v69, v26, v27
mav 4,20,36
v_cvt_pk_bf16_f32 v70, v28, v29
v_cvt_pk_bf16_f32 v71, v30, v31
mav 8,24,36
buffer_load_dwordx4 v[148:151], v88, s[36:39], s49 offen sc1
mav 12,28,36
s_waitcnt lgkmcnt(1)
v_mul_f32 v75, v16, v16
v_fmac_f32 v75, v17, v17
mav 0,32,40
R 52,5120
v_fmac_f32 v75, v18, v18
mav 4,36,40
v_fmac_f32 v75, v19, v19
v_fmac_f32 v75, v20, v20
mav 8,40,40
buffer_load_dwordx4 v[152:155], v88, s[36:39], s49 offen offset:1024 sc1
mav 12,44,40
v_fmac_f32 v75, v21, v21
v_fmac_f32 v75, v22, v22
mav 0,48,44
R 56,6144
v_fmac_f32 v75, v23, v23
mav 4,52,44
v_fmac_f32 v75, v24, v24
v_fmac_f32 v75, v25, v25
mav 8,56,44
buffer_load_dwordx4 v[156:159], v89, s[36:39], s49 offen sc1
mav 12,60,44
s_waitcnt lgkmcnt(1)
v_fmac_f32 v75, v26, v26
v_fmac_f32 v75, v27, v27
mav 0,64,48
R 60,7168
v_fmac_f32 v75, v28, v28
mav 4,68,48
v_fmac_f32 v75, v29, v29
v_fmac_f32 v75, v30, v30
mav 8,72,48
buffer_load_dwordx4 v[160:163], v89, s[36:39], s49 offen offset:1024 sc1
mav 12,76,48
v_fmac_f32 v75, v31, v31
mav 0,80,52
R 32,8192
mav 4,84,52
mav 8,88,52
s_add_u32 m0, s50, 0
mav 12,92,52
s_waitcnt lgkmcnt(1)
mav 0,96,56
R 36,9216
mav 4,100,56
mav 8,104,56
D 83,0
mav 12,108,56
ds_write_b32 v97, v74 offset:0
mav 0,112,60
R 40,10240
v_add_f32 v74, v17, v16
mav 4,116,60
ds_write_b128 v94, v[64:67] offset:0
mav 8,120,60
D 83,1024
mav 12,124,60
s_waitcnt lgkmcnt(3)
ds_write_b128 v95, v[68:71] offset:0
mav 0,128,32
R 44,11264
v_add_f32 v74, v18, v74
mav 4,132,32
ds_read_b128 v[64:67], v96 offset:0
v_add_f32 v74, v19, v74
mav 8,136,32
ds_read_b128 v[68:71], v96 offset:1024
v_add_f32 v74, v20, v74
mav 12,140,32
v_add_f32 v74, v21, v74
v_add_f32 v74, v22, v74
mav 0,144,36
R 48,12288
v_add_f32 v74, v23, v74
mav 4,148,36
v_add_f32 v74, v24, v74
v_add_f32 v74, v25, v74
mav 8,152,36
v_add_f32 v74, v26, v74
v_add_f32 v74, v27, v74
mav 12,156,36
s_waitcnt lgkmcnt(2)
v_add_f32 v74, v28, v74
v_add_f32 v74, v29, v74
mav 0,160,40
R 52,13312
v_add_f32 v74, v30, v74
mav 4,164,40
v_add_f32 v74, v31, v74
v_add_u32 v72, 1, v72
mav 8,168,40
v_permlane32_swap_b32 v74, v75
mav 12,172,40
v_add_f32 v74, v74, v75
mav 0,176,44
R 56,14336
mav 4,180,44
mav 8,184,44
D 83,2048
mav 12,188,44
s_waitcnt lgkmcnt(1)
mav 0,192,48
R 60,15360
mav 4,196,48
mav 8,200,48
D 83,3072
mav 12,204,48
mav 0,208,52
mav 4,212,52
mav 8,216,52
buffer_store_dwordx4 v[64:67], v91, s[36:39], s54 offen sc1
mav 12,220,52
s_waitcnt lgkmcnt(0)
mav 0,224,56
mav 4,228,56
mav 8,232,56
buffer_store_dwordx4 v[68:71], v92, s[36:39], s54 offen sc1
mav 12,236,56
mav 0,240,60
mav 4,244,60
mav 8,248,60
buffer_load_dwordx4 v[164:167], v90, s[36:39], s49 offen sc1
mav 12,252,60
s_waitcnt vmcnt(17)
mvv 0,116,124
buffer_store_dword v72, v81, s[36:39], s52 offen sc1
mvv 4,120,124
s_waitcnt vmcnt(16)
mvv 8,116,128
buffer_load_dwordx4 v[168:171], v90, s[36:39], s49 offen offset:1024 sc1
mvv 12,120,128
s_waitcnt vmcnt(16)
s_sub_u32 s54, s54, 32768
mvv 0,116,132
mvv 4,120,132
s_waitcnt vmcnt(15)
mvv 8,116,136
s_waitcnt vmcnt(14)
s_add_u32 s49, s49, 313344
mvv 12,120,136
s_barrier
mvv 0,116,140
R 32,32768
mvv 4,120,140
s_waitcnt vmcnt(13)
mvv 8,116,144
buffer_load_dwordx4 v[172:175], v86, s[36:39], s49 offen sc1
mvv 12,120,144
s_waitcnt vmcnt(13)
mvv 0,116,148
R 36,33792
mvv 4,120,148
s_waitcnt vmcnt(12)
mvv 8,116,152
buffer_load_dwordx4 v[176:179], v86, s[36:39], s49 offen offset:1024 sc1
mvv 12,120,152
s_waitcnt vmcnt(12)
mvv 0,116,156
R 40,34816
mvv 4,120,156
s_waitcnt vmcnt(11)
mvv 8,116,160
buffer_load_dwordx4 v[180:183], v87, s[36:39], s49 offen sc1
mvv 12,120,160
s_waitcnt lgkmcnt(1)
v_mfma_f32_16x16x32_bf16 v[16:19], a[0:3], v[32:35], v[100:103]
R 44,35840
v_cvt_pk_bf16_f32 v64, v0, v1
v_mfma_f32_16x16x32_bf16 v[20:23], a[4:7], v[32:35], v[104:107]
v_cvt_pk_bf16_f32 v65, v2, v3
v_cvt_pk_bf16_f32 v66, v4, v5
v_mfma_f32_16x16x32_bf16 v[24:27], a[8:11], v[32:35], v[108:111]
buffer_load_dwordx4 v[184:187], v87, s[36:39], s49 offen offset:1024 sc1
v_mfma_f32_16x16x32_bf16 v[28:31], a[12:15], v[32:35], v[112:115]
v_cvt_pk_bf16_f32 v67, v6, v7
v_cvt_pk_bf16_f32 v68, v8, v9
mav 16,16,36
R 48,36864
v_cvt_pk_bf16_f32 v69, v10, v11
mav 20,20,36
v_cvt_pk_bf16_f32 v70, v12, v13
v_cvt_pk_bf16_f32 v71, v14, v15
mav 24,24,36
buffer_load_dwordx4 v[188:191], v88, s[36:39], s49 offen sc1
mav 28,28,36
s_waitcnt lgkmcnt(1)
v_mul_f32 v75, v0, v0
v_fmac_f32 v75, v1, v1
mav 16,32,40
R 52,37888
v_fmac_f32 v75, v2, v2
mav 20,36,40
v_fmac_f32 v75, v3, v3
v_fmac_f32 v75, v4, v4
mav 24,40,40
buffer_load_dwordx4 v[192:195], v88, s[36:39], s49 offen offset:1024 sc1
mav 28,44,40
v_fmac_f32 v75, v5, v5
v_fmac_f32 v75, v6, v6
mav 16,48,44
R 56,38912
v_fmac_f32 v75, v7, v7
mav 20,52,44
v_fmac_f32 v75, v8, v8
v_fmac_f32 v75, v9, v9
mav 24,56,44
buffer_load_dwordx4 v[196:199], v89, s[36:39], s49 offen sc1
mav 28,60,44
s_waitcnt lgkmcnt(1)
v_fmac_f32 v75, v10, v10
v_fmac_f32 v75, v11, v11
mav 16,64,48
R 60,39936
v_fmac_f32 v75, v12, v12
mav 20,68,48
v_fmac_f32 v75, v13, v13
v_fmac_f32 v75, v14, v14
mav 24,72,48
buffer_load_dwordx4 v[200:203], v89, s[36:39], s49 offen offset:1024 sc1
mav 28,76,48
v_fmac_f32 v75, v15, v15
mav 16,80,52
R 32,40960
mav 20,84,52
mav 24,88,52
s_add_u32 m0, s50, 32768
mav 28,92,52
s_waitcnt lgkmcnt(1)
mav 16,96,56
R 36,41984
mav 20,100,56
mav 24,104,56
D 83,0
mav 28,108,56
ds_write_b32 v97, v74 offset:1024
mav 16,112,60
R 40,43008
v_add_f32 v74, v1, v0
mav 20,116,60
ds_write_b128 v94, v[64:67] offset:0
mav 24,120,60
D 83,1024
mav 28,124,60
s_waitcnt lgkmcnt(3)
ds_write_b128 v95, v[68:71] offset:0
mav 16,128,32
R 44,44032
v_add_f32 v74, v2, v74
mav 20,132,32
ds_read_b128 v[64:67], v96 offset:0
v_add_f32 v74, v3, v74
mav 24,136,32
ds_read_b128 v[68:71], v96 offset:1024
v_add_f32 v74, v4, v74
mav 28,140,32
v_add_f32 v74, v5, v74
v_add_f32 v74, v6, v74
mav 16,144,36
R 48,45056
v_add_f32 v74, v7, v74
mav 20,148,36
v_add_f32 v74, v8, v74
v_add_f32 v74, v9, v74
mav 24,152,36
v_add_f32 v74, v10, v74
v_add_f32 v74, v11, v74
mav 28,156,36
s_waitcnt lgkmcnt(2)
s_add_u32 s60, s60, 1
v_add_f32 v74, v12, v74
mav 16,160,40
s_cbranch_scc0 LR2_1
mav 20,164,40
R 52,46080
v_add_f32 v74, v13, v74
mav 24,168,40
v_add_f32 v74, v14, v74
v_add_f32 v74, v15, v74
mav 28,172,40
v_add_u32 v72, 1, v72
mav 16,176,44
R 56,47104
mav 20,180,44
v_permlane32_swap_b32 v74, v75
mav 24,184,44
D 83,2048
mav 28,188,44
s_waitcnt lgkmcnt(1)
v_add_f32 v74, v74, v75
mav 16,192,48
R 60,48128
mav 20,196,48
mav 24,200,48
D 83,3072
mav 28,204,48
mav 16,208,52
mav 20,212,52
mav 24,216,52
buffer_store_dwordx4 v[64:67], v91, s[36:39], s54 offen sc1
mav 28,220,52
s_waitcnt lgkmcnt(0)
mav 16,224,56
mav 20,228,56
mav 24,232,56
buffer_store_dwordx4 v[68:71], v92, s[36:39], s54 offen sc1
mav 28,236,56
mav 16,240,60
mav 20,244,60
mav 24,248,60
buffer_load_dwordx4 v[124:127], v90, s[36:39], s49 offen sc1
mav 28,252,60
s_waitcnt vmcnt(17)
mvv 16,116,164
buffer_store_dword v72, v81, s[36:39], s52 offen sc1
mvv 20,120,164
s_waitcnt vmcnt(16)
mvv 24,116,168
buffer_load_dwordx4 v[128:131], v90, s[36:39], s49 offen offset:1024 sc1
mvv 28,120,168
s_waitcnt vmcnt(16)
s_xor_b32 s50, s50, 0x10000
s_sub_u32 s54, s54, 32768
mvv 16,116,172
mvv 20,120,172
s_waitcnt vmcnt(15)
v_xor_b32 v80, 0x10000, v80
mvv 24,116,176
s_waitcnt vmcnt(14)
s_add_u32 s49, s49, 313344
mvv 28,120,176
s_barrier
mvv 16,116,180
R 32,0
mvv 20,120,180
s_waitcnt vmcnt(13)
mvv 24,116,184
buffer_load_dwordx4 v[132:135], v86, s[36:39], s49 offen sc1
mvv 28,120,184
s_waitcnt vmcnt(13)
mvv 16,116,188
R 36,1024
mvv 20,120,188
s_waitcnt vmcnt(12)
mvv 24,116,192
buffer_load_dwordx4 v[136:139], v86, s[36:39], s49 offen offset:1024 sc1
mvv 28,120,192
s_waitcnt vmcnt(12)
mvv 16,116,196
R 40,2048
mvv 20,120,196
s_waitcnt vmcnt(11)
mvv 24,116,200
buffer_load_dwordx4 v[140:143], v87, s[36:39], s49 offen sc1
mvv 28,120,200
s_waitcnt lgkmcnt(1)
v_mfma_f32_16x16x32_bf16 v[0:3], a[0:3], v[32:35], v[100:103]
R 44,3072
v_cvt_pk_bf16_f32 v64, v16, v17
v_mfma_f32_16x16x32_bf16 v[4:7], a[4:7], v[32:35], v[104:107]
v_cvt_pk_bf16_f32 v65, v18, v19
v_cvt_pk_bf16_f32 v66, v20, v21
v_mfma_f32_16x16x32_bf16 v[8:11], a[8:11], v[32:35], v[108:111]
buffer_load_dwordx4 v[144:147], v87, s[36:39], s49 offen offset:1024 sc1
v_mfma_f32_16x16x32_bf16 v[12:15], a[12:15], v[32:35], v[112:115]
v_cvt_pk_bf16_f32 v67, v22, v23
v_cvt_pk_bf16_f32 v68, v24, v25
mav 0,16,36
R 48,4096
v_cvt_pk_bf16_f32 v69, v26, v27
mav 4,20,36
v_cvt_pk_bf16_f32 v70, v28, v29
v_cvt_pk_bf16_f32 v71, v30, v31
mav 8,24,36
buffer_load_dwordx4 v[148:151], v88, s[36:39], s49 offen sc1
mav 12,28,36
s_waitcnt lgkmcnt(1)
v_mul_f32 v75, v16, v16
v_fmac_f32 v75, v17, v17
mav 0,32,40
R 52,5120
v_fmac_f32 v75, v18, v18
mav 4,36,40
v_fmac_f32 v75, v19, v19
v_fmac_f32 v75, v20, v20
mav 8,40,40
buffer_load_dwordx4 v[152:155], v88, s[36:39], s49 offen offset:1024 sc1
mav 12,44,40
v_fmac_f32 v75, v21, v21
v_fmac_f32 v75, v22, v22
mav 0,48,44
R 56,6144
v_fmac_f32 v75, v23, v23
mav 4,52,44
v_fmac_f32 v75, v24, v24
v_fmac_f32 v75, v25, v25
mav 8,56,44
buffer_load_dwordx4 v[156:159], v89, s[36:39], s49 offen sc1
mav 12,60,44
s_waitcnt lgkmcnt(1)
v_fmac_f32 v75, v26, v26
v_fmac_f32 v75, v27, v27
mav 0,64,48
R 60,7168
v_fmac_f32 v75, v28, v28
mav 4,68,48
v_fmac_f32 v75, v29, v29
v_fmac_f32 v75, v30, v30
mav 8,72,48
buffer_load_dwordx4 v[160:163], v89, s[36:39], s49 offen offset:1024 sc1
mav 12,76,48
v_fmac_f32 v75, v31, v31
mav 0,80,52
R 32,8192
mav 4,84,52
mav 8,88,52
s_add_u32 m0, s50, 0
mav 12,92,52
s_waitcnt lgkmcnt(1)
mav 0,96,56
R 36,9216
mav 4,100,56
mav 8,104,56
D 83,0
mav 12,108,56
ds_write_b32 v97, v74 offset:0
mav 0,112,60
R 40,10240
v_add_f32 v74, v17, v16
mav 4,116,60
ds_write_b128 v94, v[64:67] offset:0
mav 8,120,60
D 83,1024
mav 12,124,60
s_waitcnt lgkmcnt(3)
ds_write_b128 v95, v[68:71] offset:0
mav 0,128,32
LsR14:
s_cbranch_execz LsR18
R 44,11264
v_add_f32 v74, v18, v74
mav 4,132,32
ds_read_b128 v[64:67], v96 offset:0
v_add_f32 v74, v19, v74
mav 8,136,32
ds_read_b128 v[68:71], v96 offset:1024
v_add_f32 v74, v20, v74
mav 12,140,32
v_add_f32 v74, v21, v74
v_add_f32 v74, v22, v74
mav 0,144,36
R 48,12288
v_add_f32 v74, v23, v74
mav 4,148,36
v_add_f32 v74, v24, v74
v_add_f32 v74, v25, v74
mav 8,152,36
v_add_f32 v74, v26, v74
v_add_f32 v74, v27, v74
mav 12,156,36
s_waitcnt lgkmcnt(2)
v_add_f32 v74, v28, v74
v_add_f32 v74, v29, v74
mav 0,160,40
R 52,13312
v_add_f32 v74, v30, v74
mav 4,164,40
v_add_f32 v74, v31, v74
v_add_u32 v72, 1, v72
mav 8,168,40
v_permlane32_swap_b32 v74, v75
mav 12,172,40
v_add_f32 v74, v74, v75
mav 0,176,44
R 56,14336
mav 4,180,44
mav 8,184,44
D 83,2048
mav 12,188,44
s_waitcnt lgkmcnt(1)
mav 0,192,48
R 60,15360
mav 4,196,48
mav 8,200,48
D 83,3072
mav 12,204,48
mav 0,208,52
mav 4,212,52
mav 8,216,52
buffer_store_dwordx4 v[64:67], v91, s[36:39], s54 offen sc1
mav 12,220,52
s_waitcnt lgkmcnt(0)
mav 0,224,56
mav 4,228,56
mav 8,232,56
buffer_store_dwordx4 v[68:71], v92, s[36:39], s54 offen sc1
mav 12,236,56
mav 0,240,60
mav 4,244,60
mav 8,248,60
buffer_load_dwordx4 v[164:167], v90, s[36:39], s49 offen sc1
mav 12,252,60
s_waitcnt vmcnt(17)
mvv 0,116,124
buffer_store_dword v72, v81, s[36:39], s52 offen sc1
mvv 4,120,124
s_waitcnt vmcnt(16)
mvv 8,116,128
buffer_load_dwordx4 v[168:171], v90, s[36:39], s49 offen offset:1024 sc1
mvv 12,120,128
s_waitcnt vmcnt(16)
s_sub_u32 s54, s54, 3178496
mvv 0,116,132
mvv 4,120,132
s_waitcnt vmcnt(15)
mvv 8,116,136
s_waitcnt vmcnt(14)
s_add_u32 s49, s49, 313344
mvv 12,120,136
s_barrier
mvv 0,116,140
R 32,32768
mvv 4,120,140
s_waitcnt vmcnt(13)
mvv 8,116,144
buffer_load_dwordx4 v[172:175], v86, s[36:39], s49 offen sc1
mvv 12,120,144
s_waitcnt vmcnt(13)
mvv 0,116,148
R 36,33792
mvv 4,120,148
s_waitcnt vmcnt(12)
mvv 8,116,152
buffer_load_dwordx4 v[176:179], v86, s[36:39], s49 offen offset:1024 sc1
mvv 12,120,152
s_waitcnt vmcnt(12)
mvv 0,116,156
R 40,34816
mvv 4,120,156
s_waitcnt vmcnt(11)
mvv 8,116,160
buffer_load_dwordx4 v[180:183], v87, s[36:39], s49 offen sc1
mvv 12,120,160
s_waitcnt lgkmcnt(1)
v_mfma_f32_16x16x32_bf16 v[16:19], a[0:3], v[32:35], v[100:103]
R 44,35840
v_cvt_pk_bf16_f32 v64, v0, v1
v_mfma_f32_16x16x32_bf16 v[20:23], a[4:7], v[32:35], v[104:107]
v_cvt_pk_bf16_f32 v65, v2, v3
v_cvt_pk_bf16_f32 v66, v4, v5
v_mfma_f32_16x16x32_bf16 v[24:27], a[8:11], v[32:35], v[108:111]
buffer_load_dwordx4 v[184:187], v87, s[36:39], s49 offen offset:1024 sc1
v_mfma_f32_16x16x32_bf16 v[28:31], a[12:15], v[32:35], v[112:115]
v_cvt_pk_bf16_f32 v67, v6, v7
v_cvt_pk_bf16_f32 v68, v8, v9
mav 16,16,36
R 48,36864
v_cvt_pk_bf16_f32 v69, v10, v11
mav 20,20,36
v_cvt_pk_bf16_f32 v70, v12, v13
v_cvt_pk_bf16_f32 v71, v14, v15
mav 24,24,36
buffer_load_dwordx4 v[188:191], v88, s[36:39], s49 offen sc1
mav 28,28,36
s_waitcnt lgkmcnt(1)
mav 16,32,40
R 52,37888
mav 20,36,40
mav 24,40,40
buffer_load_dwordx4 v[192:195], v88, s[36:39], s49 offen offset:1024 sc1
mav 28,44,40
mav 16,48,44
R 56,38912
mav 20,52,44
mav 24,56,44
buffer_load_dwordx4 v[196:199], v89, s[36:39], s49 offen sc1
mav 28,60,44
s_waitcnt lgkmcnt(1)
mav 16,64,48
R 60,39936
mav 20,68,48
mav 24,72,48
buffer_load_dwordx4 v[200:203], v89, s[36:39], s49 offen offset:1024 sc1
mav 28,76,48
mav 16,80,52
R 32,40960
mav 20,84,52
mav 24,88,52
s_add_u32 m0, s50, 32768
mav 28,92,52
s_waitcnt lgkmcnt(1)
mav 16,96,56
R 36,41984
mav 20,100,56
mav 24,104,56
D 83,0
mav 28,108,56
ds_write_b32 v97, v74 offset:1024
mav 16,112,60
R 40,43008
mav 20,116,60
ds_write_b128 v94, v[64:67] offset:0
mav 24,120,60
D 83,1024
mav 28,124,60
s_waitcnt lgkmcnt(3)
ds_write_b128 v95, v[68:71] offset:0
mav 16,128,32
R 44,44032
mav 20,132,32
ds_read_b128 v[64:67], v96 offset:0
mav 24,136,32
ds_read_b128 v[68:71], v96 offset:1024
mav 28,140,32
mav 16,144,36
R 48,45056
mav 20,148,36
mav 24,152,36
mav 28,156,36
s_waitcnt lgkmcnt(2)
mav 16,160,40
mav 20,164,40
R 52,46080
v_add_u32 v72, 69, v72
mav 24,168,40
mav 28,172,40
mav 16,176,44
R 56,47104
mav 20,180,44
mav 24,184,44
D 83,2048
mav 28,188,44
s_waitcnt lgkmcnt(1)
mav 16,192,48
R 60,48128
mav 20,196,48
mav 24,200,48
D 83,3072
mav 28,204,48
mav 16,208,52
mav 20,212,52
mav 24,216,52
buffer_store_dwordx4 v[64:67], v91, s[44:47], s54 offen
mav 28,220,52
s_waitcnt lgkmcnt(0)
mav 16,224,56
mav 20,228,56
mav 24,232,56
buffer_store_dwordx4 v[68:71], v92, s[44:47], s54 offen
mav 28,236,56
mav 16,240,60
mav 20,244,60
mav 24,248,60
buffer_load_dwordx4 v[124:127], v90, s[36:39], s49 offen sc1
mav 28,252,60
s_waitcnt vmcnt(17)
mvv 16,116,164
buffer_store_dword v72, v81, s[36:39], s52 offen sc1
mvv 20,120,164
s_waitcnt vmcnt(16)
mvv 24,116,168
buffer_load_dwordx4 v[128:131], v90, s[36:39], s49 offen offset:1024 sc1
mvv 28,120,168
s_waitcnt vmcnt(16)
s_xor_b32 s50, s50, 0x10000
s_sub_u32 s54, s54, 32768
mvv 16,116,172
mvv 20,120,172
s_waitcnt vmcnt(15)
v_xor_b32 v80, 0x10000, v80
mvv 24,116,176
s_waitcnt vmcnt(14)
s_add_u32 s49, s49, 313344
mvv 28,120,176
s_barrier
mvv 16,116,180
R 32,0
mvv 20,120,180
s_waitcnt vmcnt(13)
mvv 24,116,184
buffer_load_dwordx4 v[132:135], v86, s[36:39], s49 offen sc1
mvv 28,120,184
s_waitcnt vmcnt(13)
mvv 16,116,188
R 36,1024
mvv 20,120,188
s_waitcnt vmcnt(12)
mvv 24,116,192
buffer_load_dwordx4 v[136:139], v86, s[36:39], s49 offen offset:1024 sc1
mvv 28,120,192
s_waitcnt vmcnt(12)
mvv 16,116,196
R 40,2048
mvv 20,120,196
s_waitcnt vmcnt(11)
mvv 24,116,200
buffer_load_dwordx4 v[140:143], v87, s[36:39], s49 offen sc1
mvv 28,120,200
s_waitcnt lgkmcnt(1)
v_mfma_f32_16x16x32_bf16 v[0:3], a[0:3], v[32:35], v[100:103]
R 44,3072
v_cvt_pk_bf16_f32 v64, v16, v17
v_mfma_f32_16x16x32_bf16 v[4:7], a[4:7], v[32:35], v[104:107]
v_cvt_pk_bf16_f32 v65, v18, v19
v_cvt_pk_bf16_f32 v66, v20, v21
v_mfma_f32_16x16x32_bf16 v[8:11], a[8:11], v[32:35], v[108:111]
buffer_load_dwordx4 v[144:147], v87, s[36:39], s49 offen offset:1024 sc1
v_mfma_f32_16x16x32_bf16 v[12:15], a[12:15], v[32:35], v[112:115]
v_cvt_pk_bf16_f32 v67, v22, v23
v_cvt_pk_bf16_f32 v68, v24, v25
mav 0,16,36
R 48,4096
v_cvt_pk_bf16_f32 v69, v26, v27
mav 4,20,36
v_cvt_pk_bf16_f32 v70, v28, v29
v_cvt_pk_bf16_f32 v71, v30, v31
mav 8,24,36
buffer_load_dwordx4 v[148:151], v88, s[36:39], s49 offen sc1
mav 12,28,36
s_waitcnt lgkmcnt(1)
mav 0,32,40
R 52,5120
mav 4,36,40
mav 8,40,40
buffer_load_dwordx4 v[152:155], v88, s[36:39], s49 offen offset:1024 sc1
mav 12,44,40
mav 0,48,44
R 56,6144
mav 4,52,44
mav 8,56,44
buffer_load_dwordx4 v[156:159], v89, s[36:39], s49 offen sc1
mav 12,60,44
s_waitcnt lgkmcnt(1)
mav 0,64,48
R 60,7168
mav 4,68,48
mav 8,72,48
buffer_load_dwordx4 v[160:163], v89, s[36:39], s49 offen offset:1024 sc1
mav 12,76,48
mav 0,80,52
R 32,8192
mav 4,84,52
mav 8,88,52
s_add_u32 m0, s50, 0
mav 12,92,52
s_waitcnt lgkmcnt(1)
mav 0,96,56
R 36,9216
mav 4,100,56
mav 8,104,56
D 83,0
mav 12,108,56
ds_write_b128 v94, v[64:67] offset:0
mav 0,112,60
R 40,10240
mav 4,116,60
ds_write_b128 v95, v[68:71] offset:0
mav 8,120,60
D 83,1024
mav 12,124,60
s_waitcnt lgkmcnt(3)
ds_read_b128 v[64:67], v96 offset:0
mav 0,128,32
R 44,11264
mav 4,132,32
ds_read_b128 v[68:71], v96 offset:1024
mav 8,136,32
mav 12,140,32
mav 0,144,36
R 48,12288
mav 4,148,36
mav 8,152,36
mav 12,156,36
s_waitcnt lgkmcnt(1)
mav 0,160,40
R 52,13312
mav 4,164,40
mav 8,168,40
mav 12,172,40
mav 0,176,44
R 56,14336
mav 4,180,44
mav 8,184,44
D 83,2048
mav 12,188,44
s_waitcnt lgkmcnt(1)
mav 0,192,48
R 60,15360
mav 4,196,48
mav 8,200,48
D 83,3072
mav 12,204,48
mav 0,208,52
mav 4,212,52
mav 8,216,52
buffer_store_dwordx4 v[64:67], v91, s[44:47], s54 offen
mav 12,220,52
s_waitcnt lgkmcnt(0)
mav 0,224,56
mav 4,228,56
mav 8,232,56
buffer_store_dwordx4 v[68:71], v92, s[44:47], s54 offen
mav 12,236,56
mav 0,240,60
mav 4,244,60
mav 8,248,60
buffer_load_dwordx4 v[164:167], v90, s[36:39], s49 offen sc1
mav 12,252,60
s_waitcnt vmcnt(17)
mvv 0,116,124
mvv 4,120,124
s_waitcnt vmcnt(15)
mvv 8,116,128
buffer_load_dwordx4 v[168:171], v90, s[36:39], s49 offen offset:1024 sc1
mvv 12,120,128
s_waitcnt vmcnt(15)
s_sub_u32 s54, s54, 32768
mvv 0,116,132
mvv 4,120,132
s_waitcnt vmcnt(14)
mvv 8,116,136
s_waitcnt vmcnt(13)
s_add_u32 s49, s49, 313344
mvv 12,120,136
s_barrier
mvv 0,116,140
R 32,32768
mvv 4,120,140
s_waitcnt vmcnt(12)
mvv 8,116,144
buffer_load_dwordx4 v[172:175], v86, s[36:39], s49 offen sc1
mvv 12,120,144
s_waitcnt vmcnt(12)
mvv 0,116,148
R 36,33792
mvv 4,120,148
s_waitcnt vmcnt(11)
mvv 8,116,152
buffer_load_dwordx4 v[176:179], v86, s[36:39], s49 offen offset:1024 sc1
mvv 12,120,152
s_waitcnt vmcnt(11)
mvv 0,116,156
R 40,34816
mvv 4,120,156
s_waitcnt vmcnt(10)
mvv 8,116,160
buffer_load_dwordx4 v[180:183], v87, s[36:39], s49 offen sc1
mvv 12,120,160
s_waitcnt lgkmcnt(1)
v_mfma_f32_16x16x32_bf16 v[16:19], a[0:3], v[32:35], v[100:103]
R 44,35840
v_cvt_pk_bf16_f32 v64, v0, v1
v_mfma_f32_16x16x32_bf16 v[20:23], a[4:7], v[32:35], v[104:107]
v_cvt_pk_bf16_f32 v65, v2, v3
v_cvt_pk_bf16_f32 v66, v4, v5
v_mfma_f32_16x16x32_bf16 v[24:27], a[8:11], v[32:35], v[108:111]
buffer_load_dwordx4 v[184:187], v87, s[36:39], s49 offen offset:1024 sc1
v_mfma_f32_16x16x32_bf16 v[28:31], a[12:15], v[32:35], v[112:115]
v_cvt_pk_bf16_f32 v67, v6, v7
v_cvt_pk_bf16_f32 v68, v8, v9
mav 16,16,36
R 48,36864
v_cvt_pk_bf16_f32 v69, v10, v11
mav 20,20,36
v_cvt_pk_bf16_f32 v70, v12, v13
v_cvt_pk_bf16_f32 v71, v14, v15
mav 24,24,36
buffer_load_dwordx4 v[188:191], v88, s[36:39], s49 offen sc1
mav 28,28,36
s_waitcnt lgkmcnt(1)
mav 16,32,40
R 52,37888
mav 20,36,40
mav 24,40,40
buffer_load_dwordx4 v[192:195], v88, s[36:39], s49 offen offset:1024 sc1
mav 28,44,40
mav 16,48,44
R 56,38912
mav 20,52,44
mav 24,56,44
buffer_load_dwordx4 v[196:199], v89, s[36:39], s49 offen sc1
mav 28,60,44
s_waitcnt lgkmcnt(1)
mav 16,64,48
R 60,39936
mav 20,68,48
mav 24,72,48
buffer_load_dwordx4 v[200:203], v89, s[36:39], s49 offen offset:1024 sc1
mav 28,76,48
mav 16,80,52
R 32,40960
mav 20,84,52
mav 24,88,52
s_add_u32 m0, s50, 32768
mav 28,92,52
s_waitcnt lgkmcnt(1)
mav 16,96,56
R 36,41984
mav 20,100,56
mav 24,104,56
D 83,0
mav 28,108,56
ds_write_b128 v94, v[64:67] offset:0
mav 16,112,60
R 40,43008
mav 20,116,60
ds_write_b128 v95, v[68:71] offset:0
mav 24,120,60
D 83,1024
mav 28,124,60
s_waitcnt lgkmcnt(3)
ds_read_b128 v[64:67], v96 offset:0
mav 16,128,32
R 44,44032
mav 20,132,32
ds_read_b128 v[68:71], v96 offset:1024
mav 24,136,32
mav 28,140,32
mav 16,144,36
R 48,45056
mav 20,148,36
mav 24,152,36
mav 28,156,36
s_waitcnt lgkmcnt(1)
mav 16,160,40
s_mov_b32 s60, -13
.p2align 6
LR2_2:
mav 20,164,40
R 52,46080
mav 24,168,40
mav 28,172,40
mav 16,176,44
R 56,47104
mav 20,180,44
mav 24,184,44
D 83,2048
mav 28,188,44
s_waitcnt lgkmcnt(1)
mav 16,192,48
R 60,48128
mav 20,196,48
mav 24,200,48
D 83,3072
mav 28,204,48
mav 16,208,52
mav 20,212,52
mav 24,216,52
buffer_store_dwordx4 v[64:67], v91, s[44:47], s54 offen
mav 28,220,52
s_waitcnt lgkmcnt(0)
mav 16,224,56
mav 20,228,56
mav 24,232,56
buffer_store_dwordx4 v[68:71], v92, s[44:47], s54 offen
mav 28,236,56
mav 16,240,60
mav 20,244,60
mav 24,248,60
buffer_load_dwordx4 v[124:127], v90, s[36:39], s49 offen sc1
mav 28,252,60
s_waitcnt vmcnt(16)
mvv 16,116,164
mvv 20,120,164
s_waitcnt vmcnt(15)
mvv 24,116,168
buffer_load_dwordx4 v[128:131], v90, s[36:39], s49 offen offset:1024 sc1
mvv 28,120,168
s_waitcnt vmcnt(15)
s_xor_b32 s50, s50, 0x10000
s_sub_u32 s54, s54, 32768
mvv 16,116,172
mvv 20,120,172
s_waitcnt vmcnt(14)
v_xor_b32 v80, 0x10000, v80
mvv 24,116,176
s_waitcnt vmcnt(13)
s_add_u32 s49, s49, 313344
mvv 28,120,176
s_barrier
mvv 16,116,180
R 32,0
mvv 20,120,180
s_waitcnt vmcnt(12)
mvv 24,116,184
buffer_load_dwordx4 v[132:135], v86, s[36:39], s49 offen sc1
mvv 28,120,184
s_waitcnt vmcnt(12)
mvv 16,116,188
LsR15:
s_cbranch_execz LsR19
R 36,1024
mvv 20,120,188
s_waitcnt vmcnt(11)
mvv 24,116,192
buffer_load_dwordx4 v[136:139], v86, s[36:39], s49 offen offset:1024 sc1
mvv 28,120,192
s_waitcnt vmcnt(11)
mvv 16,116,196
R 40,2048
mvv 20,120,196
s_waitcnt vmcnt(10)
mvv 24,116,200
buffer_load_dwordx4 v[140:143], v87, s[36:39], s49 offen sc1
mvv 28,120,200
s_waitcnt lgkmcnt(1)
v_mfma_f32_16x16x32_bf16 v[0:3], a[0:3], v[32:35], v[100:103]
R 44,3072
v_cvt_pk_bf16_f32 v64, v16, v17
v_mfma_f32_16x16x32_bf16 v[4:7], a[4:7], v[32:35], v[104:107]
v_cvt_pk_bf16_f32 v65, v18, v19
v_cvt_pk_bf16_f32 v66, v20, v21
v_mfma_f32_16x16x32_bf16 v[8:11], a[8:11], v[32:35], v[108:111]
buffer_load_dwordx4 v[144:147], v87, s[36:39], s49 offen offset:1024 sc1
v_mfma_f32_16x16x32_bf16 v[12:15], a[12:15], v[32:35], v[112:115]
v_cvt_pk_bf16_f32 v67, v22, v23
v_cvt_pk_bf16_f32 v68, v24, v25
mav 0,16,36
R 48,4096
v_cvt_pk_bf16_f32 v69, v26, v27
mav 4,20,36
v_cvt_pk_bf16_f32 v70, v28, v29
v_cvt_pk_bf16_f32 v71, v30, v31
mav 8,24,36
buffer_load_dwordx4 v[148:151], v88, s[36:39], s49 offen sc1
mav 12,28,36
s_waitcnt lgkmcnt(1)
mav 0,32,40
R 52,5120
mav 4,36,40
mav 8,40,40
buffer_load_dwordx4 v[152:155], v88, s[36:39], s49 offen offset:1024 sc1
mav 12,44,40
mav 0,48,44
R 56,6144
mav 4,52,44
mav 8,56,44
buffer_load_dwordx4 v[156:159], v89, s[36:39], s49 offen sc1
mav 12,60,44
s_waitcnt lgkmcnt(1)
mav 0,64,48
R 60,7168
mav 4,68,48
mav 8,72,48
buffer_load_dwordx4 v[160:163], v89, s[36:39], s49 offen offset:1024 sc1
mav 12,76,48
mav 0,80,52
R 32,8192
mav 4,84,52
mav 8,88,52
s_add_u32 m0, s50, 0
mav 12,92,52
s_waitcnt lgkmcnt(1)
mav 0,96,56
R 36,9216
mav 4,100,56
mav 8,104,56
D 83,0
mav 12,108,56
ds_write_b128 v94, v[64:67] offset:0
mav 0,112,60
R 40,10240
mav 4,116,60
ds_write_b128 v95, v[68:71] offset:0
mav 8,120,60
D 83,1024
mav 12,124,60
s_waitcnt lgkmcnt(3)
ds_read_b128 v[64:67], v96 offset:0
mav 0,128,32
R 44,11264
mav 4,132,32
ds_read_b128 v[68:71], v96 offset:1024
mav 8,136,32
mav 12,140,32
mav 0,144,36
R 48,12288
mav 4,148,36
mav 8,152,36
mav 12,156,36
s_waitcnt lgkmcnt(1)
mav 0,160,40
R 52,13312
mav 4,164,40
mav 8,168,40
mav 12,172,40
mav 0,176,44
R 56,14336
mav 4,180,44
mav 8,184,44
D 83,2048
mav 12,188,44
s_waitcnt lgkmcnt(1)
mav 0,192,48
R 60,15360
mav 4,196,48
mav 8,200,48
D 83,3072
mav 12,204,48
mav 0,208,52
mav 4,212,52
mav 8,216,52
buffer_store_dwordx4 v[64:67], v91, s[44:47], s54 offen
mav 12,220,52
s_waitcnt lgkmcnt(0)
mav 0,224,56
mav 4,228,56
mav 8,232,56
buffer_store_dwordx4 v[68:71], v92, s[44:47], s54 offen
mav 12,236,56
mav 0,240,60
mav 4,244,60
mav 8,248,60
buffer_load_dwordx4 v[164:167], v90, s[36:39], s49 offen sc1
mav 12,252,60
s_waitcnt vmcnt(16)
mvv 0,116,124
mvv 4,120,124
s_waitcnt vmcnt(15)
mvv 8,116,128
buffer_load_dwordx4 v[168:171], v90, s[36:39], s49 offen offset:1024 sc1
mvv 12,120,128
s_waitcnt vmcnt(15)
s_sub_u32 s54, s54, 32768
mvv 0,116,132
mvv 4,120,132
s_waitcnt vmcnt(14)
mvv 8,116,136
s_waitcnt vmcnt(13)
s_add_u32 s49, s49, 313344
mvv 12,120,136
s_barrier
mvv 0,116,140
R 32,32768
mvv 4,120,140
s_waitcnt vmcnt(12)
mvv 8,116,144
buffer_load_dwordx4 v[172:175], v86, s[36:39], s49 offen sc1
mvv 12,120,144
s_waitcnt vmcnt(12)
mvv 0,116,148
R 36,33792
mvv 4,120,148
s_waitcnt vmcnt(11)
mvv 8,116,152
buffer_load_dwordx4 v[176:179], v86, s[36:39], s49 offen offset:1024 sc1
mvv 12,120,152
s_waitcnt vmcnt(11)
mvv 0,116,156
)"
R"(
R 40,34816
mvv 4,120,156
s_waitcnt vmcnt(10)
mvv 8,116,160
buffer_load_dwordx4 v[180:183], v87, s[36:39], s49 offen sc1
mvv 12,120,160
s_waitcnt lgkmcnt(1)
v_mfma_f32_16x16x32_bf16 v[16:19], a[0:3], v[32:35], v[100:103]
R 44,35840
v_cvt_pk_bf16_f32 v64, v0, v1
v_mfma_f32_16x16x32_bf16 v[20:23], a[4:7], v[32:35], v[104:107]
v_cvt_pk_bf16_f32 v65, v2, v3
v_cvt_pk_bf16_f32 v66, v4, v5
v_mfma_f32_16x16x32_bf16 v[24:27], a[8:11], v[32:35], v[108:111]
buffer_load_dwordx4 v[184:187], v87, s[36:39], s49 offen offset:1024 sc1
v_mfma_f32_16x16x32_bf16 v[28:31], a[12:15], v[32:35], v[112:115]
v_cvt_pk_bf16_f32 v67, v6, v7
v_cvt_pk_bf16_f32 v68, v8, v9
mav 16,16,36
R 48,36864
v_cvt_pk_bf16_f32 v69, v10, v11
mav 20,20,36
v_cvt_pk_bf16_f32 v70, v12, v13
v_cvt_pk_bf16_f32 v71, v14, v15
mav 24,24,36
buffer_load_dwordx4 v[188:191], v88, s[36:39], s49 offen sc1
mav 28,28,36
s_waitcnt lgkmcnt(1)
mav 16,32,40
R 52,37888
mav 20,36,40
mav 24,40,40
buffer_load_dwordx4 v[192:195], v88, s[36:39], s49 offen offset:1024 sc1
mav 28,44,40
mav 16,48,44
R 56,38912
mav 20,52,44
mav 24,56,44
buffer_load_dwordx4 v[196:199], v89, s[36:39], s49 offen sc1
mav 28,60,44
s_waitcnt lgkmcnt(1)
mav 16,64,48
R 60,39936
mav 20,68,48
mav 24,72,48
buffer_load_dwordx4 v[200:203], v89, s[36:39], s49 offen offset:1024 sc1
mav 28,76,48
mav 16,80,52
R 32,40960
mav 20,84,52
mav 24,88,52
s_add_u32 m0, s50, 32768
mav 28,92,52
s_waitcnt lgkmcnt(1)
mav 16,96,56
R 36,41984
mav 20,100,56
mav 24,104,56
D 83,0
mav 28,108,56
ds_write_b128 v94, v[64:67] offset:0
mav 16,112,60
R 40,43008
mav 20,116,60
ds_write_b128 v95, v[68:71] offset:0
mav 24,120,60
D 83,1024
mav 28,124,60
s_waitcnt lgkmcnt(3)
ds_read_b128 v[64:67], v96 offset:0
mav 16,128,32
R 44,44032
mav 20,132,32
ds_read_b128 v[68:71], v96 offset:1024
mav 24,136,32
mav 28,140,32
mav 16,144,36
R 48,45056
mav 20,148,36
mav 24,152,36
mav 28,156,36
s_waitcnt lgkmcnt(1)
s_add_u32 s60, s60, 1
mav 16,160,40
s_cbranch_scc0 LR2_2
mav 20,164,40
R 52,46080
mav 24,168,40
mav 28,172,40
mav 16,176,44
R 56,47104
mav 20,180,44
mav 24,184,44
D 83,2048
mav 28,188,44
s_waitcnt lgkmcnt(1)
mav 16,192,48
R 60,48128
mav 20,196,48
mav 24,200,48
D 83,3072
mav 28,204,48
mav 16,208,52
mav 20,212,52
mav 24,216,52
buffer_store_dwordx4 v[64:67], v91, s[44:47], s54 offen
mav 28,220,52
s_waitcnt lgkmcnt(0)
mav 16,224,56
mav 20,228,56
mav 24,232,56
buffer_store_dwordx4 v[68:71], v92, s[44:47], s54 offen
mav 28,236,56
mav 16,240,60
mav 20,244,60
mav 24,248,60
buffer_load_dwordx4 v[124:127], v90, s[36:39], s49 offen sc1
mav 28,252,60
s_waitcnt vmcnt(16)
mvv 16,116,164
mvv 20,120,164
s_waitcnt vmcnt(15)
mvv 24,116,168
buffer_load_dwordx4 v[128:131], v90, s[36:39], s49 offen offset:1024 sc1
mvv 28,120,168
s_waitcnt vmcnt(15)
s_sub_u32 s54, s54, 32768
mvv 16,116,172
mvv 20,120,172
s_waitcnt vmcnt(14)
v_xor_b32 v80, 0x10000, v80
mvv 24,116,176
s_waitcnt vmcnt(13)
s_add_u32 s49, s49, 313344
mvv 28,120,176
s_barrier
mvv 16,116,180
R 32,0
mvv 20,120,180
s_waitcnt vmcnt(12)
mvv 24,116,184
buffer_load_dwordx4 v[132:135], v86, s[36:39], s49 offen sc1
mvv 28,120,184
s_waitcnt vmcnt(12)
mvv 16,116,188
R 36,1024
mvv 20,120,188
s_waitcnt vmcnt(11)
mvv 24,116,192
buffer_load_dwordx4 v[136:139], v86, s[36:39], s49 offen offset:1024 sc1
mvv 28,120,192
s_waitcnt vmcnt(11)
mvv 16,116,196
R 40,2048
mvv 20,120,196
s_waitcnt vmcnt(10)
mvv 24,116,200
buffer_load_dwordx4 v[140:143], v87, s[36:39], s49 offen sc1
mvv 28,120,200
s_waitcnt lgkmcnt(1)
v_mfma_f32_16x16x32_bf16 v[0:3], a[0:3], v[32:35], v[100:103]
R 44,3072
v_cvt_pk_bf16_f32 v64, v16, v17
v_mfma_f32_16x16x32_bf16 v[4:7], a[4:7], v[32:35], v[104:107]
v_cvt_pk_bf16_f32 v65, v18, v19
v_cvt_pk_bf16_f32 v66, v20, v21
v_mfma_f32_16x16x32_bf16 v[8:11], a[8:11], v[32:35], v[108:111]
buffer_load_dwordx4 v[144:147], v87, s[36:39], s49 offen offset:1024 sc1
v_mfma_f32_16x16x32_bf16 v[12:15], a[12:15], v[32:35], v[112:115]
v_cvt_pk_bf16_f32 v67, v22, v23
v_cvt_pk_bf16_f32 v68, v24, v25
mav 0,16,36
R 48,4096
v_cvt_pk_bf16_f32 v69, v26, v27
mav 4,20,36
v_cvt_pk_bf16_f32 v70, v28, v29
v_cvt_pk_bf16_f32 v71, v30, v31
mav 8,24,36
buffer_load_dwordx4 v[148:151], v88, s[36:39], s49 offen sc1
mav 12,28,36
s_waitcnt lgkmcnt(1)
mav 0,32,40
R 52,5120
mav 4,36,40
mav 8,40,40
buffer_load_dwordx4 v[152:155], v88, s[36:39], s49 offen offset:1024 sc1
mav 12,44,40
mav 0,48,44
R 56,6144
mav 4,52,44
mav 8,56,44
buffer_load_dwordx4 v[156:159], v89, s[36:39], s49 offen sc1
mav 12,60,44
s_waitcnt lgkmcnt(1)
mav 0,64,48
R 60,7168
mav 4,68,48
mav 8,72,48
buffer_load_dwordx4 v[160:163], v89, s[36:39], s49 offen offset:1024 sc1
mav 12,76,48
mav 0,80,52
R 32,8192
mav 4,84,52
mav 8,88,52
mav 12,92,52
s_waitcnt lgkmcnt(1)
mav 0,96,56
R 36,9216
mav 4,100,56
mav 8,104,56
mav 12,108,56
ds_write_b128 v94, v[64:67] offset:0
mav 0,112,60
R 40,10240
mav 4,116,60
ds_write_b128 v95, v[68:71] offset:0
mav 8,120,60
ds_read_b128 v[64:67], v96 offset:0
mav 12,124,60
s_waitcnt lgkmcnt(4)
ds_read_b128 v[68:71], v96 offset:1024
mav 0,128,32
R 44,11264
mav 4,132,32
mav 8,136,32
mav 12,140,32
mav 0,144,36
R 48,12288
mav 4,148,36
mav 8,152,36
mav 12,156,36
s_waitcnt lgkmcnt(1)
mav 0,160,40
R 52,13312
mav 4,164,40
mav 8,168,40
mav 12,172,40
mav 0,176,44
R 56,14336
mav 4,180,44
mav 8,184,44
mav 12,188,44
s_waitcnt lgkmcnt(1)
mav 0,192,48
R 60,15360
mav 4,196,48
mav 8,200,48
mav 12,204,48
mav 0,208,52
mav 4,212,52
mav 8,216,52
buffer_store_dwordx4 v[64:67], v91, s[44:47], s54 offen
mav 12,220,52
s_waitcnt lgkmcnt(0)
mav 0,224,56
mav 4,228,56
mav 8,232,56
buffer_store_dwordx4 v[68:71], v92, s[44:47], s54 offen
mav 12,236,56
mav 0,240,60
mav 4,244,60
mav 8,248,60
buffer_load_dwordx4 v[164:167], v90, s[36:39], s49 offen sc1
mav 12,252,60
s_waitcnt vmcnt(12)
mvv 0,116,124
mvv 4,120,124
s_waitcnt vmcnt(11)
mvv 8,116,128
buffer_load_dwordx4 v[168:171], v90, s[36:39], s49 offen offset:1024 sc1
mvv 12,120,128
s_waitcnt vmcnt(11)
s_sub_u32 s54, s54, 32768
mvv 0,116,132
mvv 4,120,132
s_waitcnt vmcnt(10)
mvv 8,116,136
s_waitcnt vmcnt(9)
s_add_u32 s49, s49, 313344
mvv 12,120,136
s_barrier
mvv 0,116,140
R 32,32768
mvv 4,120,140
s_waitcnt vmcnt(8)
mvv 8,116,144
buffer_load_dwordx4 v[172:175], v86, s[36:39], s49 offen sc1
mvv 12,120,144
s_waitcnt vmcnt(8)
mvv 0,116,148
R 36,33792
mvv 4,120,148
s_waitcnt vmcnt(7)
mvv 8,116,152
buffer_load_dwordx4 v[176:179], v86, s[36:39], s49 offen offset:1024 sc1
mvv 12,120,152
s_waitcnt vmcnt(7)
mvv 0,116,156
R 40,34816
mvv 4,120,156
s_waitcnt vmcnt(6)
mvv 8,116,160
buffer_load_dwordx4 v[180:183], v87, s[36:39], s49 offen sc1
mvv 12,120,160
s_waitcnt lgkmcnt(1)
v_mfma_f32_16x16x32_bf16 v[16:19], a[0:3], v[32:35], v[100:103]
R 44,35840
v_cvt_pk_bf16_f32 v64, v0, v1
v_mfma_f32_16x16x32_bf16 v[20:23], a[4:7], v[32:35], v[104:107]
v_cvt_pk_bf16_f32 v65, v2, v3
v_cvt_pk_bf16_f32 v66, v4, v5
v_mfma_f32_16x16x32_bf16 v[24:27], a[8:11], v[32:35], v[108:111]
buffer_load_dwordx4 v[184:187], v87, s[36:39], s49 offen offset:1024 sc1
v_mfma_f32_16x16x32_bf16 v[28:31], a[12:15], v[32:35], v[112:115]
v_cvt_pk_bf16_f32 v67, v6, v7
v_cvt_pk_bf16_f32 v68, v8, v9
mav 16,16,36
R 48,36864
v_cvt_pk_bf16_f32 v69, v10, v11
mav 20,20,36
ds_write_b128 v94, v[64:67] offset:0
mav 24,24,36
buffer_load_dwordx4 v[188:191], v88, s[36:39], s49 offen sc1
mav 28,28,36
s_waitcnt lgkmcnt(2)
v_cvt_pk_bf16_f32 v70, v12, v13
v_cvt_pk_bf16_f32 v71, v14, v15
mav 16,32,40
R 52,37888
mav 20,36,40
ds_write_b128 v95, v[68:71] offset:0
mav 24,40,40
buffer_load_dwordx4 v[192:195], v88, s[36:39], s49 offen offset:1024 sc1
mav 28,44,40
ds_read_b128 v[64:67], v96 offset:0
mav 16,48,44
R 56,38912
mav 20,52,44
ds_read_b128 v[68:71], v96 offset:1024
mav 24,56,44
buffer_load_dwordx4 v[196:199], v89, s[36:39], s49 offen sc1
mav 28,60,44
s_waitcnt lgkmcnt(3)
mav 16,64,48
R 60,39936
mav 20,68,48
mav 24,72,48
s_waitcnt lgkmcnt(2)
buffer_load_dwordx4 v[200:203], v89, s[36:39], s49 offen offset:1024 sc1
mav 28,76,48
mav 16,80,52
s_waitcnt lgkmcnt(1)
R 32,40960
mav 20,84,52
mav 24,88,52
buffer_store_dwordx4 v[64:67], v91, s[44:47], s54 offen
mav 28,92,52
mav 16,96,56
R 36,41984
mav 20,100,56
mav 24,104,56
buffer_store_dwordx4 v[68:71], v92, s[44:47], s54 offen
mav 28,108,56
s_waitcnt lgkmcnt(1)
mav 16,112,60
R 40,43008
mav 20,116,60
mav 24,120,60
mav 28,124,60
mav 16,128,32
R 44,44032
mav 20,132,32
mav 24,136,32
mav 28,140,32
s_waitcnt lgkmcnt(1)
mav 16,144,36
R 48,45056
mav 20,148,36
mav 24,152,36
mav 28,156,36
mav 16,160,40
mav 20,164,40
R 52,46080
mav 24,168,40
mav 28,172,40
s_waitcnt lgkmcnt(1)
mav 16,176,44
R 56,47104
mav 20,180,44
LsR16:
s_cbranch_execz LsR20
mav 24,184,44
mav 28,188,44
mav 16,192,48
R 60,48128
mav 20,196,48
mav 24,200,48
mav 28,204,48
s_waitcnt lgkmcnt(1)
mav 16,208,52
mav 20,212,52
mav 24,216,52
mav 28,220,52
mav 16,224,56
mav 20,228,56
mav 24,232,56
mav 28,236,56
s_waitcnt lgkmcnt(0)
mav 16,240,60
mav 20,244,60
mav 24,248,60
mav 28,252,60
s_waitcnt vmcnt(11)
mvv 16,116,164
mvv 20,120,164
s_waitcnt vmcnt(10)
mvv 24,116,168
s_sub_u32 s54, s54, 32768
mvv 28,120,168
s_waitcnt vmcnt(9)
mvv 16,116,172
mvv 20,120,172
s_waitcnt vmcnt(8)
mvv 24,116,176
mvv 28,120,176
s_waitcnt vmcnt(7)
mvv 16,116,180
mvv 20,120,180
s_waitcnt vmcnt(6)
mvv 24,116,184
mvv 28,120,184
s_waitcnt vmcnt(5)
mvv 16,116,188
mvv 20,120,188
s_waitcnt vmcnt(4)
mvv 24,116,192
mvv 28,120,192
s_waitcnt vmcnt(3)
mvv 16,116,196
mvv 20,120,196
s_waitcnt vmcnt(2)
mvv 24,116,200
mvv 28,120,200
s_nop 3
v_cvt_pk_bf16_f32 v64, v16, v17
v_cvt_pk_bf16_f32 v65, v18, v19
v_cvt_pk_bf16_f32 v66, v20, v21
v_cvt_pk_bf16_f32 v67, v22, v23
ds_write_b128 v94, v[64:67] offset:0
v_cvt_pk_bf16_f32 v68, v24, v25
v_cvt_pk_bf16_f32 v69, v26, v27
v_cvt_pk_bf16_f32 v70, v28, v29
v_cvt_pk_bf16_f32 v71, v30, v31
ds_write_b128 v95, v[68:71] offset:0
ds_read_b128 v[64:67], v96 offset:0
ds_read_b128 v[68:71], v96 offset:1024
s_waitcnt lgkmcnt(0)
buffer_store_dwordx4 v[64:67], v91, s[44:47], s54 offen
buffer_store_dwordx4 v[68:71], v92, s[44:47], s54 offen
s_endpgm
LvR3:
LsR17:
s_cbranch_execz LsR21
s_add_u32 s49, s49, 313344
s_barrier
s_add_u32 m0, s50, 0
s_nop 0
D 83,0
D 83,1024
D 83,2048
D 83,3072
s_add_u32 s49, s49, 313344
s_barrier
s_add_u32 m0, s50, 32768
s_nop 0
D 83,0
D 83,1024
D 83,2048
D 83,3072
buffer_load_dwordx4 v[124:127], v90, s[36:39], s49 offen sc1
buffer_load_dwordx4 v[128:131], v90, s[36:39], s49 offen offset:1024 sc1
s_xor_b32 s50, s50, 0x10000
s_waitcnt vmcnt(6)
s_add_u32 s49, s49, 313344
s_barrier
R 32,0
buffer_load_dwordx4 v[132:135], v86, s[36:39], s49 offen sc1
R 36,1024
buffer_load_dwordx4 v[136:139], v86, s[36:39], s49 offen offset:1024 sc1
R 40,2048
buffer_load_dwordx4 v[140:143], v87, s[36:39], s49 offen sc1
s_waitcnt lgkmcnt(1)
v_mfma_f32_16x16x32_bf16 v[0:3], a[0:3], v[32:35], v[100:103]
R 44,3072
v_mfma_f32_16x16x32_bf16 v[4:7], a[4:7], v[32:35], v[104:107]
v_mfma_f32_16x16x32_bf16 v[8:11], a[8:11], v[32:35], v[108:111]
v_mfma_f32_16x16x32_bf16 v[12:15], a[12:15], v[32:35], v[112:115]
buffer_load_dwordx4 v[144:147], v87, s[36:39], s49 offen offset:1024 sc1
mav 0,16,36
R 48,4096
mav 4,20,36
mav 8,24,36
mav 12,28,36
s_waitcnt lgkmcnt(1)
buffer_load_dwordx4 v[148:151], v88, s[36:39], s49 offen sc1
mav 0,32,40
R 52,5120
mav 4,36,40
mav 8,40,40
mav 12,44,40
buffer_load_dwordx4 v[152:155], v88, s[36:39], s49 offen offset:1024 sc1
mav 0,48,44
R 56,6144
mav 4,52,44
mav 8,56,44
mav 12,60,44
s_waitcnt lgkmcnt(1)
buffer_load_dwordx4 v[156:159], v89, s[36:39], s49 offen sc1
mav 0,64,48
R 60,7168
mav 4,68,48
mav 8,72,48
mav 12,76,48
buffer_load_dwordx4 v[160:163], v89, s[36:39], s49 offen offset:1024 sc1
mav 0,80,52
R 32,8192
mav 4,84,52
mav 8,88,52
s_add_u32 m0, s50, 0
mav 12,92,52
s_waitcnt lgkmcnt(1)
mav 0,96,56
R 36,9216
mav 4,100,56
mav 8,104,56
mav 12,108,56
D 83,0
mav 0,112,60
R 40,10240
mav 4,116,60
mav 8,120,60
mav 12,124,60
s_waitcnt lgkmcnt(1)
D 83,1024
mav 0,128,32
R 44,11264
mav 4,132,32
mav 8,136,32
mav 12,140,32
mav 0,144,36
R 48,12288
mav 4,148,36
mav 8,152,36
mav 12,156,36
s_waitcnt lgkmcnt(1)
mav 0,160,40
R 52,13312
mav 4,164,40
mav 8,168,40
mav 12,172,40
mav 0,176,44
R 56,14336
mav 4,180,44
mav 8,184,44
mav 12,188,44
s_waitcnt lgkmcnt(1)
D 83,2048
mav 0,192,48
R 60,15360
mav 4,196,48
mav 8,200,48
mav 12,204,48
D 83,3072
mav 0,208,52
mav 4,212,52
mav 8,216,52
mav 12,220,52
s_waitcnt lgkmcnt(0)
mav 0,224,56
mav 4,228,56
mav 8,232,56
mav 12,236,56
mav 0,240,60
mav 4,244,60
mav 8,248,60
mav 12,252,60
s_waitcnt vmcnt(13)
buffer_load_dwordx4 v[164:167], v90, s[36:39], s49 offen sc1
mvv 0,116,124
mvv 4,120,124
s_waitcnt vmcnt(13)
mvv 8,116,128
mvv 12,120,128
s_waitcnt vmcnt(12)
buffer_load_dwordx4 v[168:171], v90, s[36:39], s49 offen offset:1024 sc1
mvv 0,116,132
mvv 4,120,132
s_waitcnt vmcnt(12)
mvv 8,116,136
s_waitcnt vmcnt(11)
s_add_u32 s49, s49, 313344
mvv 12,120,136
s_barrier
mvv 0,116,140
R 32,32768
mvv 4,120,140
s_waitcnt vmcnt(10)
mvv 8,116,144
mvv 12,120,144
s_waitcnt vmcnt(9)
buffer_load_dwordx4 v[172:175], v86, s[36:39], s49 offen sc1
mvv 0,116,148
R 36,33792
mvv 4,120,148
s_waitcnt vmcnt(9)
mvv 8,116,152
mvv 12,120,152
s_waitcnt vmcnt(8)
buffer_load_dwordx4 v[176:179], v86, s[36:39], s49 offen offset:1024 sc1
mvv 0,116,156
R 40,34816
mvv 4,120,156
s_waitcnt vmcnt(8)
mvv 8,116,160
mvv 12,120,160
s_waitcnt lgkmcnt(1)
buffer_load_dwordx4 v[180:183], v87, s[36:39], s49 offen sc1
v_mfma_f32_16x16x32_bf16 v[16:19], a[0:3], v[32:35], v[100:103]
R 44,35840
v_cvt_pk_bf16_f32 v64, v0, v1
v_mfma_f32_16x16x32_bf16 v[20:23], a[4:7], v[32:35], v[104:107]
v_cvt_pk_bf16_f32 v65, v2, v3
v_cvt_pk_bf16_f32 v66, v4, v5
v_mfma_f32_16x16x32_bf16 v[24:27], a[8:11], v[32:35], v[108:111]
v_cvt_pk_bf16_f32 v67, v6, v7
v_cvt_pk_bf16_f32 v68, v8, v9
v_mfma_f32_16x16x32_bf16 v[28:31], a[12:15], v[32:35], v[112:115]
buffer_load_dwordx4 v[184:187], v87, s[36:39], s49 offen offset:1024 sc1
mav 16,16,36
R 48,36864
v_cvt_pk_bf16_f32 v69, v10, v11
mav 20,20,36
v_cvt_pk_bf16_f32 v70, v12, v13
v_cvt_pk_bf16_f32 v71, v14, v15
mav 24,24,36
v_mul_f32 v75, v0, v0
v_add_f32 v74, v1, v0
mav 28,28,36
s_waitcnt lgkmcnt(1)
buffer_load_dwordx4 v[188:191], v88, s[36:39], s49 offen sc1
mav 16,32,40
R 52,37888
v_fmac_f32 v75, v1, v1
mav 20,36,40
v_add_f32 v74, v2, v74
v_fmac_f32 v75, v2, v2
mav 24,40,40
v_add_f32 v74, v3, v74
v_fmac_f32 v75, v3, v3
mav 28,44,40
buffer_load_dwordx4 v[192:195], v88, s[36:39], s49 offen offset:1024 sc1
mav 16,48,44
R 56,38912
v_add_f32 v74, v4, v74
mav 20,52,44
v_fmac_f32 v75, v4, v4
v_add_f32 v74, v5, v74
mav 24,56,44
v_fmac_f32 v75, v5, v5
v_add_f32 v74, v6, v74
mav 28,60,44
s_waitcnt lgkmcnt(1)
buffer_load_dwordx4 v[196:199], v89, s[36:39], s49 offen sc1
mav 16,64,48
R 60,39936
v_fmac_f32 v75, v6, v6
mav 20,68,48
v_add_f32 v74, v7, v74
v_fmac_f32 v75, v7, v7
mav 24,72,48
v_add_f32 v74, v8, v74
v_fmac_f32 v75, v8, v8
mav 28,76,48
buffer_load_dwordx4 v[200:203], v89, s[36:39], s49 offen offset:1024 sc1
mav 16,80,52
R 32,40960
v_add_f32 v74, v9, v74
mav 20,84,52
v_fmac_f32 v75, v9, v9
v_add_f32 v74, v10, v74
mav 24,88,52
s_add_u32 m0, s50, 32768
v_fmac_f32 v75, v10, v10
v_add_f32 v74, v11, v74
mav 28,92,52
s_waitcnt lgkmcnt(1)
v_fmac_f32 v75, v11, v11
v_add_f32 v74, v12, v74
mav 16,96,56
R 36,41984
v_fmac_f32 v75, v12, v12
mav 20,100,56
v_add_f32 v74, v13, v74
v_fmac_f32 v75, v13, v13
mav 24,104,56
v_add_f32 v74, v14, v74
v_fmac_f32 v75, v14, v14
mav 28,108,56
D 83,0
mav 16,112,60
R 40,43008
v_add_f32 v74, v15, v74
mav 20,116,60
v_fmac_f32 v75, v15, v15
mav 24,120,60
mav 28,124,60
s_waitcnt lgkmcnt(1)
D 83,1024
mav 16,128,32
R 44,44032
mav 20,132,32
ds_write_b128 v94, v[64:67] offset:0
mav 24,136,32
ds_write_b128 v95, v[68:71] offset:0
mav 28,140,32
ds_read_b128 v[64:67], v96 offset:0
mav 16,144,36
R 48,45056
mav 20,148,36
ds_read_b128 v[68:71], v96 offset:1024
mav 24,152,36
v_permlane32_swap_b32 v74, v75
mav 28,156,36
s_waitcnt lgkmcnt(4)
v_add_f32 v74, v74, v75
mav 16,160,40
mav 20,164,40
R 52,46080
mav 24,168,40
mav 28,172,40
mav 16,176,44
R 56,47104
mav 20,180,44
mav 24,184,44
mav 28,188,44
s_waitcnt lgkmcnt(1)
D 83,2048
mav 16,192,48
R 60,48128
mav 20,196,48
mav 24,200,48
mav 28,204,48
D 83,3072
mav 16,208,52
mav 20,212,52
mav 24,216,52
mav 28,220,52
buffer_store_dwordx4 v[64:67], v91, s[36:39], s54 offen sc1
s_waitcnt lgkmcnt(0)
mav 16,224,56
mav 20,228,56
mav 24,232,56
mav 28,236,56
buffer_store_dwordx4 v[68:71], v92, s[36:39], s54 offen sc1
mav 16,240,60
mav 20,244,60
mav 24,248,60
mav 28,252,60
s_waitcnt vmcnt(15)
buffer_load_dwordx4 v[124:127], v90, s[36:39], s49 offen sc1
mvv 16,116,164
mvv 20,120,164
s_waitcnt vmcnt(15)
mvv 24,116,168
s_xor_b32 s50, s50, 0x10000
s_sub_u32 s54, s54, 32768
mvv 28,120,168
s_waitcnt vmcnt(14)
buffer_load_dwordx4 v[128:131], v90, s[36:39], s49 offen offset:1024 sc1
mvv 16,116,172
mvv 20,120,172
s_waitcnt vmcnt(14)
v_xor_b32 v80, 0x10000, v80
mvv 24,116,176
s_waitcnt vmcnt(13)
s_add_u32 s49, s49, 313344
mvv 28,120,176
s_barrier
mvv 16,116,180
R 32,0
mvv 20,120,180
s_waitcnt vmcnt(12)
mvv 24,116,184
mvv 28,120,184
s_waitcnt vmcnt(11)
buffer_load_dwordx4 v[132:135], v86, s[36:39], s49 offen sc1
mvv 16,116,188
R 36,1024
mvv 20,120,188
s_waitcnt vmcnt(11)
mvv 24,116,192
mvv 28,120,192
s_waitcnt vmcnt(10)
buffer_load_dwordx4 v[136:139], v86, s[36:39], s49 offen offset:1024 sc1
mvv 16,116,196
R 40,2048
mvv 20,120,196
s_waitcnt vmcnt(10)
mvv 24,116,200
mvv 28,120,200
s_waitcnt lgkmcnt(1)
buffer_load_dwordx4 v[140:143], v87, s[36:39], s49 offen sc1
v_mfma_f32_16x16x32_bf16 v[0:3], a[0:3], v[32:35], v[100:103]
R 44,3072
v_cvt_pk_bf16_f32 v64, v16, v17
v_mfma_f32_16x16x32_bf16 v[4:7], a[4:7], v[32:35], v[104:107]
v_cvt_pk_bf16_f32 v65, v18, v19
v_cvt_pk_bf16_f32 v66, v20, v21
v_mfma_f32_16x16x32_bf16 v[8:11], a[8:11], v[32:35], v[108:111]
v_cvt_pk_bf16_f32 v67, v22, v23
v_cvt_pk_bf16_f32 v68, v24, v25
v_mfma_f32_16x16x32_bf16 v[12:15], a[12:15], v[32:35], v[112:115]
buffer_load_dwordx4 v[144:147], v87, s[36:39], s49 offen offset:1024 sc1
mav 0,16,36
R 48,4096
v_cvt_pk_bf16_f32 v69, v26, v27
mav 4,20,36
v_cvt_pk_bf16_f32 v70, v28, v29
v_cvt_pk_bf16_f32 v71, v30, v31
mav 8,24,36
v_mul_f32 v75, v16, v16
v_fmac_f32 v75, v17, v17
mav 12,28,36
s_waitcnt lgkmcnt(1)
buffer_load_dwordx4 v[148:151], v88, s[36:39], s49 offen sc1
mav 0,32,40
R 52,5120
v_fmac_f32 v75, v18, v18
mav 4,36,40
v_fmac_f32 v75, v19, v19
v_fmac_f32 v75, v20, v20
mav 8,40,40
v_fmac_f32 v75, v21, v21
v_fmac_f32 v75, v22, v22
mav 12,44,40
buffer_load_dwordx4 v[152:155], v88, s[36:39], s49 offen offset:1024 sc1
mav 0,48,44
R 56,6144
v_fmac_f32 v75, v23, v23
mav 4,52,44
v_fmac_f32 v75, v24, v24
v_fmac_f32 v75, v25, v25
mav 8,56,44
v_fmac_f32 v75, v26, v26
v_fmac_f32 v75, v27, v27
mav 12,60,44
s_waitcnt lgkmcnt(1)
buffer_load_dwordx4 v[156:159], v89, s[36:39], s49 offen sc1
mav 0,64,48
R 60,7168
v_fmac_f32 v75, v28, v28
mav 4,68,48
v_fmac_f32 v75, v29, v29
v_fmac_f32 v75, v30, v30
mav 8,72,48
v_fmac_f32 v75, v31, v31
mav 12,76,48
buffer_load_dwordx4 v[160:163], v89, s[36:39], s49 offen offset:1024 sc1
mav 0,80,52
R 32,8192
mav 4,84,52
mav 8,88,52
s_add_u32 m0, s50, 0
mav 12,92,52
s_waitcnt lgkmcnt(1)
mav 0,96,56
R 36,9216
mav 4,100,56
mav 8,104,56
mav 12,108,56
D 83,0
mav 0,112,60
R 40,10240
mav 4,116,60
mav 8,120,60
ds_write_b32 v97, v74 offset:0
mav 12,124,60
s_waitcnt lgkmcnt(2)
D 83,1024
mav 0,128,32
R 44,11264
v_add_f32 v74, v17, v16
mav 4,132,32
ds_write_b128 v94, v[64:67] offset:0
mav 8,136,32
ds_write_b128 v95, v[68:71] offset:0
mav 12,140,32
ds_read_b128 v[64:67], v96 offset:0
v_add_f32 v74, v18, v74
mav 0,144,36
R 48,12288
v_add_f32 v74, v19, v74
mav 4,148,36
ds_read_b128 v[68:71], v96 offset:1024
v_add_f32 v74, v20, v74
mav 8,152,36
v_add_f32 v74, v21, v74
v_add_f32 v74, v22, v74
mav 12,156,36
s_waitcnt lgkmcnt(4)
v_add_f32 v74, v23, v74
v_add_f32 v74, v24, v74
mav 0,160,40
R 52,13312
v_add_f32 v74, v25, v74
mav 4,164,40
v_add_f32 v74, v26, v74
v_add_f32 v74, v27, v74
mav 8,168,40
v_add_f32 v74, v28, v74
v_add_f32 v74, v29, v74
mav 12,172,40
v_add_f32 v74, v30, v74
v_add_f32 v74, v31, v74
mav 0,176,44
R 56,14336
v_add_u32 v72, 1, v72
mav 4,180,44
v_permlane32_swap_b32 v74, v75
mav 8,184,44
v_add_f32 v74, v74, v75
mav 12,188,44
s_waitcnt lgkmcnt(1)
D 83,2048
mav 0,192,48
R 60,15360
mav 4,196,48
mav 8,200,48
mav 12,204,48
D 83,3072
mav 0,208,52
mav 4,212,52
mav 8,216,52
mav 12,220,52
buffer_store_dwordx4 v[64:67], v91, s[36:39], s54 offen sc1
s_waitcnt lgkmcnt(0)
mav 0,224,56
mav 4,228,56
mav 8,232,56
mav 12,236,56
buffer_store_dwordx4 v[68:71], v92, s[36:39], s54 offen sc1
mav 0,240,60
mav 4,244,60
mav 8,248,60
mav 12,252,60
s_waitcnt vmcnt(15)
buffer_load_dwordx4 v[164:167], v90, s[36:39], s49 offen sc1
mvv 0,116,124
buffer_store_dword v72, v81, s[36:39], s52 offen sc1
mvv 4,120,124
s_waitcnt vmcnt(16)
mvv 8,116,128
s_sub_u32 s54, s54, 32768
mvv 12,120,128
s_waitcnt vmcnt(15)
buffer_load_dwordx4 v[168:171], v90, s[36:39], s49 offen offset:1024 sc1
mvv 0,116,132
mvv 4,120,132
s_waitcnt vmcnt(15)
mvv 8,116,136
s_waitcnt vmcnt(14)
s_add_u32 s49, s49, 313344
mvv 12,120,136
s_barrier
mvv 0,116,140
R 32,32768
mvv 4,120,140
LsR18:
s_cbranch_execz LwR
s_waitcnt vmcnt(13)
mvv 8,116,144
mvv 12,120,144
s_waitcnt vmcnt(12)
buffer_load_dwordx4 v[172:175], v86, s[36:39], s49 offen sc1
mvv 0,116,148
R 36,33792
mvv 4,120,148
s_waitcnt vmcnt(12)
mvv 8,116,152
mvv 12,120,152
s_waitcnt vmcnt(11)
buffer_load_dwordx4 v[176:179], v86, s[36:39], s49 offen offset:1024 sc1
mvv 0,116,156
R 40,34816
mvv 4,120,156
s_waitcnt vmcnt(11)
mvv 8,116,160
mvv 12,120,160
s_waitcnt lgkmcnt(1)
buffer_load_dwordx4 v[180:183], v87, s[36:39], s49 offen sc1
v_mfma_f32_16x16x32_bf16 v[16:19], a[0:3], v[32:35], v[100:103]
R 44,35840
v_cvt_pk_bf16_f32 v64, v0, v1
v_mfma_f32_16x16x32_bf16 v[20:23], a[4:7], v[32:35], v[104:107]
v_cvt_pk_bf16_f32 v65, v2, v3
v_cvt_pk_bf16_f32 v66, v4, v5
v_mfma_f32_16x16x32_bf16 v[24:27], a[8:11], v[32:35], v[108:111]
v_cvt_pk_bf16_f32 v67, v6, v7
v_cvt_pk_bf16_f32 v68, v8, v9
v_mfma_f32_16x16x32_bf16 v[28:31], a[12:15], v[32:35], v[112:115]
buffer_load_dwordx4 v[184:187], v87, s[36:39], s49 offen offset:1024 sc1
mav 16,16,36
R 48,36864
v_cvt_pk_bf16_f32 v69, v10, v11
mav 20,20,36
v_cvt_pk_bf16_f32 v70, v12, v13
v_cvt_pk_bf16_f32 v71, v14, v15
mav 24,24,36
v_mul_f32 v75, v0, v0
v_fmac_f32 v75, v1, v1
mav 28,28,36
s_waitcnt lgkmcnt(1)
buffer_load_dwordx4 v[188:191], v88, s[36:39], s49 offen sc1
mav 16,32,40
R 52,37888
v_fmac_f32 v75, v2, v2
mav 20,36,40
v_fmac_f32 v75, v3, v3
v_fmac_f32 v75, v4, v4
mav 24,40,40
v_fmac_f32 v75, v5, v5
v_fmac_f32 v75, v6, v6
mav 28,44,40
buffer_load_dwordx4 v[192:195], v88, s[36:39], s49 offen offset:1024 sc1
mav 16,48,44
R 56,38912
v_fmac_f32 v75, v7, v7
mav 20,52,44
v_fmac_f32 v75, v8, v8
v_fmac_f32 v75, v9, v9
mav 24,56,44
v_fmac_f32 v75, v10, v10
v_fmac_f32 v75, v11, v11
mav 28,60,44
s_waitcnt lgkmcnt(1)
buffer_load_dwordx4 v[196:199], v89, s[36:39], s49 offen sc1
mav 16,64,48
R 60,39936
v_fmac_f32 v75, v12, v12
mav 20,68,48
v_fmac_f32 v75, v13, v13
v_fmac_f32 v75, v14, v14
mav 24,72,48
v_fmac_f32 v75, v15, v15
mav 28,76,48
buffer_load_dwordx4 v[200:203], v89, s[36:39], s49 offen offset:1024 sc1
mav 16,80,52
R 32,40960
mav 20,84,52
mav 24,88,52
s_add_u32 m0, s50, 32768
mav 28,92,52
s_waitcnt lgkmcnt(1)
mav 16,96,56
R 36,41984
mav 20,100,56
mav 24,104,56
mav 28,108,56
D 83,0
mav 16,112,60
R 40,43008
mav 20,116,60
mav 24,120,60
ds_write_b32 v97, v74 offset:1024
mav 28,124,60
s_waitcnt lgkmcnt(2)
D 83,1024
mav 16,128,32
R 44,44032
v_add_f32 v74, v1, v0
mav 20,132,32
ds_write_b128 v94, v[64:67] offset:0
mav 24,136,32
ds_write_b128 v95, v[68:71] offset:0
mav 28,140,32
ds_read_b128 v[64:67], v96 offset:0
v_add_f32 v74, v2, v74
mav 16,144,36
R 48,45056
v_add_f32 v74, v3, v74
mav 20,148,36
ds_read_b128 v[68:71], v96 offset:1024
v_add_f32 v74, v4, v74
mav 24,152,36
v_add_f32 v74, v5, v74
v_add_f32 v74, v6, v74
mav 28,156,36
s_waitcnt lgkmcnt(4)
v_add_f32 v74, v7, v74
mav 16,160,40
s_mov_b32 s60, -14
.p2align 6
LR3_1:
mav 20,164,40
R 52,46080
v_add_f32 v74, v8, v74
mav 24,168,40
v_add_f32 v74, v9, v74
v_add_f32 v74, v10, v74
mav 28,172,40
v_add_f32 v74, v11, v74
v_add_f32 v74, v12, v74
mav 16,176,44
R 56,47104
v_add_f32 v74, v13, v74
mav 20,180,44
v_add_f32 v74, v14, v74
v_add_f32 v74, v15, v74
mav 24,184,44
v_add_u32 v72, 1, v72
mav 28,188,44
s_waitcnt lgkmcnt(1)
D 83,2048
mav 16,192,48
R 60,48128
mav 20,196,48
v_permlane32_swap_b32 v74, v75
mav 24,200,48
v_add_f32 v74, v74, v75
mav 28,204,48
D 83,3072
mav 16,208,52
mav 20,212,52
mav 24,216,52
mav 28,220,52
buffer_store_dwordx4 v[64:67], v91, s[36:39], s54 offen sc1
s_waitcnt lgkmcnt(0)
mav 16,224,56
mav 20,228,56
mav 24,232,56
mav 28,236,56
buffer_store_dwordx4 v[68:71], v92, s[36:39], s54 offen sc1
mav 16,240,60
mav 20,244,60
mav 24,248,60
mav 28,252,60
s_waitcnt vmcnt(16)
buffer_load_dwordx4 v[124:127], v90, s[36:39], s49 offen sc1
mvv 16,116,164
buffer_store_dword v72, v81, s[36:39], s52 offen sc1
mvv 20,120,164
s_waitcnt vmcnt(16)
mvv 24,116,168
s_xor_b32 s50, s50, 0x10000
s_sub_u32 s54, s54, 32768
mvv 28,120,168
s_waitcnt vmcnt(15)
buffer_load_dwordx4 v[128:131], v90, s[36:39], s49 offen offset:1024 sc1
mvv 16,116,172
mvv 20,120,172
s_waitcnt vmcnt(15)
v_xor_b32 v80, 0x10000, v80
mvv 24,116,176
s_waitcnt vmcnt(14)
s_add_u32 s49, s49, 313344
mvv 28,120,176
s_barrier
mvv 16,116,180
R 32,0
mvv 20,120,180
s_waitcnt vmcnt(13)
mvv 24,116,184
mvv 28,120,184
s_waitcnt vmcnt(12)
buffer_load_dwordx4 v[132:135], v86, s[36:39], s49 offen sc1
mvv 16,116,188
R 36,1024
mvv 20,120,188
s_waitcnt vmcnt(12)
mvv 24,116,192
mvv 28,120,192
s_waitcnt vmcnt(11)
buffer_load_dwordx4 v[136:139], v86, s[36:39], s49 offen offset:1024 sc1
mvv 16,116,196
R 40,2048
mvv 20,120,196
s_waitcnt vmcnt(11)
mvv 24,116,200
mvv 28,120,200
s_waitcnt lgkmcnt(1)
buffer_load_dwordx4 v[140:143], v87, s[36:39], s49 offen sc1
v_mfma_f32_16x16x32_bf16 v[0:3], a[0:3], v[32:35], v[100:103]
R 44,3072
v_cvt_pk_bf16_f32 v64, v16, v17
v_mfma_f32_16x16x32_bf16 v[4:7], a[4:7], v[32:35], v[104:107]
v_cvt_pk_bf16_f32 v65, v18, v19
v_cvt_pk_bf16_f32 v66, v20, v21
v_mfma_f32_16x16x32_bf16 v[8:11], a[8:11], v[32:35], v[108:111]
v_cvt_pk_bf16_f32 v67, v22, v23
v_cvt_pk_bf16_f32 v68, v24, v25
v_mfma_f32_16x16x32_bf16 v[12:15], a[12:15], v[32:35], v[112:115]
buffer_load_dwordx4 v[144:147], v87, s[36:39], s49 offen offset:1024 sc1
mav 0,16,36
R 48,4096
v_cvt_pk_bf16_f32 v69, v26, v27
mav 4,20,36
v_cvt_pk_bf16_f32 v70, v28, v29
v_cvt_pk_bf16_f32 v71, v30, v31
mav 8,24,36
v_mul_f32 v75, v16, v16
v_fmac_f32 v75, v17, v17
mav 12,28,36
s_waitcnt lgkmcnt(1)
buffer_load_dwordx4 v[148:151], v88, s[36:39], s49 offen sc1
mav 0,32,40
R 52,5120
v_fmac_f32 v75, v18, v18
mav 4,36,40
v_fmac_f32 v75, v19, v19
v_fmac_f32 v75, v20, v20
mav 8,40,40
v_fmac_f32 v75, v21, v21
v_fmac_f32 v75, v22, v22
mav 12,44,40
buffer_load_dwordx4 v[152:155], v88, s[36:39], s49 offen offset:1024 sc1
mav 0,48,44
R 56,6144
v_fmac_f32 v75, v23, v23
mav 4,52,44
v_fmac_f32 v75, v24, v24
v_fmac_f32 v75, v25, v25
mav 8,56,44
v_fmac_f32 v75, v26, v26
v_fmac_f32 v75, v27, v27
mav 12,60,44
s_waitcnt lgkmcnt(1)
buffer_load_dwordx4 v[156:159], v89, s[36:39], s49 offen sc1
mav 0,64,48
R 60,7168
v_fmac_f32 v75, v28, v28
mav 4,68,48
v_fmac_f32 v75, v29, v29
v_fmac_f32 v75, v30, v30
mav 8,72,48
v_fmac_f32 v75, v31, v31
mav 12,76,48
buffer_load_dwordx4 v[160:163], v89, s[36:39], s49 offen offset:1024 sc1
mav 0,80,52
R 32,8192
mav 4,84,52
mav 8,88,52
s_add_u32 m0, s50, 0
mav 12,92,52
s_waitcnt lgkmcnt(1)
mav 0,96,56
R 36,9216
mav 4,100,56
mav 8,104,56
mav 12,108,56
D 83,0
mav 0,112,60
R 40,10240
mav 4,116,60
mav 8,120,60
ds_write_b32 v97, v74 offset:0
mav 12,124,60
s_waitcnt lgkmcnt(2)
D 83,1024
mav 0,128,32
R 44,11264
v_add_f32 v74, v17, v16
mav 4,132,32
ds_write_b128 v94, v[64:67] offset:0
mav 8,136,32
ds_write_b128 v95, v[68:71] offset:0
mav 12,140,32
ds_read_b128 v[64:67], v96 offset:0
v_add_f32 v74, v18, v74
mav 0,144,36
R 48,12288
v_add_f32 v74, v19, v74
mav 4,148,36
ds_read_b128 v[68:71], v96 offset:1024
v_add_f32 v74, v20, v74
mav 8,152,36
v_add_f32 v74, v21, v74
v_add_f32 v74, v22, v74
mav 12,156,36
s_waitcnt lgkmcnt(4)
v_add_f32 v74, v23, v74
v_add_f32 v74, v24, v74
mav 0,160,40
R 52,13312
v_add_f32 v74, v25, v74
mav 4,164,40
v_add_f32 v74, v26, v74
v_add_f32 v74, v27, v74
mav 8,168,40
v_add_f32 v74, v28, v74
v_add_f32 v74, v29, v74
mav 12,172,40
v_add_f32 v74, v30, v74
v_add_f32 v74, v31, v74
mav 0,176,44
R 56,14336
v_add_u32 v72, 1, v72
mav 4,180,44
v_permlane32_swap_b32 v74, v75
mav 8,184,44
v_add_f32 v74, v74, v75
mav 12,188,44
s_waitcnt lgkmcnt(1)
D 83,2048
mav 0,192,48
R 60,15360
mav 4,196,48
mav 8,200,48
mav 12,204,48
D 83,3072
mav 0,208,52
mav 4,212,52
mav 8,216,52
mav 12,220,52
buffer_store_dwordx4 v[64:67], v91, s[36:39], s54 offen sc1
s_waitcnt lgkmcnt(0)
mav 0,224,56
mav 4,228,56
mav 8,232,56
mav 12,236,56
buffer_store_dwordx4 v[68:71], v92, s[36:39], s54 offen sc1
mav 0,240,60
mav 4,244,60
mav 8,248,60
mav 12,252,60
s_waitcnt vmcnt(16)
buffer_load_dwordx4 v[164:167], v90, s[36:39], s49 offen sc1
mvv 0,116,124
buffer_store_dword v72, v81, s[36:39], s52 offen sc1
mvv 4,120,124
s_waitcnt vmcnt(16)
mvv 8,116,128
s_sub_u32 s54, s54, 32768
mvv 12,120,128
s_waitcnt vmcnt(15)
buffer_load_dwordx4 v[168:171], v90, s[36:39], s49 offen offset:1024 sc1
mvv 0,116,132
mvv 4,120,132
s_waitcnt vmcnt(15)
mvv 8,116,136
s_waitcnt vmcnt(14)
s_add_u32 s49, s49, 313344
mvv 12,120,136
s_barrier
mvv 0,116,140
R 32,32768
mvv 4,120,140
s_waitcnt vmcnt(13)
mvv 8,116,144
mvv 12,120,144
s_waitcnt vmcnt(12)
buffer_load_dwordx4 v[172:175], v86, s[36:39], s49 offen sc1
mvv 0,116,148
R 36,33792
mvv 4,120,148
s_waitcnt vmcnt(12)
mvv 8,116,152
mvv 12,120,152
s_waitcnt vmcnt(11)
buffer_load_dwordx4 v[176:179], v86, s[36:39], s49 offen offset:1024 sc1
mvv 0,116,156
R 40,34816
mvv 4,120,156
s_waitcnt vmcnt(11)
mvv 8,116,160
mvv 12,120,160
s_waitcnt lgkmcnt(1)
buffer_load_dwordx4 v[180:183], v87, s[36:39], s49 offen sc1
v_mfma_f32_16x16x32_bf16 v[16:19], a[0:3], v[32:35], v[100:103]
R 44,35840
v_cvt_pk_bf16_f32 v64, v0, v1
v_mfma_f32_16x16x32_bf16 v[20:23], a[4:7], v[32:35], v[104:107]
v_cvt_pk_bf16_f32 v65, v2, v3
v_cvt_pk_bf16_f32 v66, v4, v5
v_mfma_f32_16x16x32_bf16 v[24:27], a[8:11], v[32:35], v[108:111]
v_cvt_pk_bf16_f32 v67, v6, v7
v_cvt_pk_bf16_f32 v68, v8, v9
v_mfma_f32_16x16x32_bf16 v[28:31], a[12:15], v[32:35], v[112:115]
buffer_load_dwordx4 v[184:187], v87, s[36:39], s49 offen offset:1024 sc1
mav 16,16,36
R 48,36864
v_cvt_pk_bf16_f32 v69, v10, v11
mav 20,20,36
v_cvt_pk_bf16_f32 v70, v12, v13
v_cvt_pk_bf16_f32 v71, v14, v15
mav 24,24,36
v_mul_f32 v75, v0, v0
v_fmac_f32 v75, v1, v1
mav 28,28,36
s_waitcnt lgkmcnt(1)
buffer_load_dwordx4 v[188:191], v88, s[36:39], s49 offen sc1
mav 16,32,40
R 52,37888
v_fmac_f32 v75, v2, v2
mav 20,36,40
v_fmac_f32 v75, v3, v3
v_fmac_f32 v75, v4, v4
mav 24,40,40
v_fmac_f32 v75, v5, v5
v_fmac_f32 v75, v6, v6
mav 28,44,40
buffer_load_dwordx4 v[192:195], v88, s[36:39], s49 offen offset:1024 sc1
mav 16,48,44
R 56,38912
v_fmac_f32 v75, v7, v7
mav 20,52,44
v_fmac_f32 v75, v8, v8
v_fmac_f32 v75, v9, v9
mav 24,56,44
v_fmac_f32 v75, v10, v10
v_fmac_f32 v75, v11, v11
mav 28,60,44
s_waitcnt lgkmcnt(1)
buffer_load_dwordx4 v[196:199], v89, s[36:39], s49 offen sc1
mav 16,64,48
R 60,39936
v_fmac_f32 v75, v12, v12
mav 20,68,48
v_fmac_f32 v75, v13, v13
v_fmac_f32 v75, v14, v14
mav 24,72,48
v_fmac_f32 v75, v15, v15
mav 28,76,48
buffer_load_dwordx4 v[200:203], v89, s[36:39], s49 offen offset:1024 sc1
mav 16,80,52
R 32,40960
mav 20,84,52
mav 24,88,52
s_add_u32 m0, s50, 32768
mav 28,92,52
s_waitcnt lgkmcnt(1)
mav 16,96,56
R 36,41984
mav 20,100,56
mav 24,104,56
mav 28,108,56
D 83,0
mav 16,112,60
R 40,43008
mav 20,116,60
mav 24,120,60
ds_write_b32 v97, v74 offset:1024
mav 28,124,60
s_waitcnt lgkmcnt(2)
D 83,1024
mav 16,128,32
R 44,44032
v_add_f32 v74, v1, v0
mav 20,132,32
ds_write_b128 v94, v[64:67] offset:0
mav 24,136,32
ds_write_b128 v95, v[68:71] offset:0
mav 28,140,32
ds_read_b128 v[64:67], v96 offset:0
v_add_f32 v74, v2, v74
mav 16,144,36
R 48,45056
v_add_f32 v74, v3, v74
mav 20,148,36
ds_read_b128 v[68:71], v96 offset:1024
v_add_f32 v74, v4, v74
mav 24,152,36
v_add_f32 v74, v5, v74
v_add_f32 v74, v6, v74
mav 28,156,36
s_waitcnt lgkmcnt(4)
s_add_u32 s60, s60, 1
v_add_f32 v74, v7, v74
mav 16,160,40
s_cbranch_scc0 LR3_1
mav 20,164,40
R 52,46080
v_add_f32 v74, v8, v74
mav 24,168,40
v_add_f32 v74, v9, v74
v_add_f32 v74, v10, v74
mav 28,172,40
v_add_f32 v74, v11, v74
v_add_f32 v74, v12, v74
mav 16,176,44
R 56,47104
v_add_f32 v74, v13, v74
mav 20,180,44
v_add_f32 v74, v14, v74
v_add_f32 v74, v15, v74
mav 24,184,44
v_add_u32 v72, 1, v72
mav 28,188,44
s_waitcnt lgkmcnt(1)
D 83,2048
mav 16,192,48
R 60,48128
mav 20,196,48
v_permlane32_swap_b32 v74, v75
mav 24,200,48
v_add_f32 v74, v74, v75
mav 28,204,48
D 83,3072
mav 16,208,52
mav 20,212,52
mav 24,216,52
mav 28,220,52
buffer_store_dwordx4 v[64:67], v91, s[36:39], s54 offen sc1
s_waitcnt lgkmcnt(0)
mav 16,224,56
mav 20,228,56
mav 24,232,56
mav 28,236,56
buffer_store_dwordx4 v[68:71], v92, s[36:39], s54 offen sc1
mav 16,240,60
mav 20,244,60
mav 24,248,60
mav 28,252,60
s_waitcnt vmcnt(16)
buffer_load_dwordx4 v[124:127], v90, s[36:39], s49 offen sc1
mvv 16,116,164
buffer_store_dword v72, v81, s[36:39], s52 offen sc1
mvv 20,120,164
s_waitcnt vmcnt(16)
mvv 24,116,168
s_xor_b32 s50, s50, 0x10000
s_sub_u32 s54, s54, 32768
mvv 28,120,168
s_waitcnt vmcnt(15)
buffer_load_dwordx4 v[128:131], v90, s[36:39], s49 offen offset:1024 sc1
mvv 16,116,172
mvv 20,120,172
s_waitcnt vmcnt(15)
v_xor_b32 v80, 0x10000, v80
mvv 24,116,176
s_waitcnt vmcnt(14)
s_add_u32 s49, s49, 313344
mvv 28,120,176
s_barrier
mvv 16,116,180
R 32,0
mvv 20,120,180
s_waitcnt vmcnt(13)
mvv 24,116,184
mvv 28,120,184
s_waitcnt vmcnt(12)
buffer_load_dwordx4 v[132:135], v86, s[36:39], s49 offen sc1
mvv 16,116,188
R 36,1024
mvv 20,120,188
s_waitcnt vmcnt(12)
mvv 24,116,192
mvv 28,120,192
s_waitcnt vmcnt(11)
buffer_load_dwordx4 v[136:139], v86, s[36:39], s49 offen offset:1024 sc1
mvv 16,116,196
R 40,2048
mvv 20,120,196
s_waitcnt vmcnt(11)
mvv 24,116,200
mvv 28,120,200
s_waitcnt lgkmcnt(1)
buffer_load_dwordx4 v[140:143], v87, s[36:39], s49 offen sc1
v_mfma_f32_16x16x32_bf16 v[0:3], a[0:3], v[32:35], v[100:103]
R 44,3072
v_cvt_pk_bf16_f32 v64, v16, v17
LsR19:
s_cbranch_execz LwR
v_mfma_f32_16x16x32_bf16 v[4:7], a[4:7], v[32:35], v[104:107]
v_cvt_pk_bf16_f32 v65, v18, v19
v_cvt_pk_bf16_f32 v66, v20, v21
v_mfma_f32_16x16x32_bf16 v[8:11], a[8:11], v[32:35], v[108:111]
v_cvt_pk_bf16_f32 v67, v22, v23
v_cvt_pk_bf16_f32 v68, v24, v25
v_mfma_f32_16x16x32_bf16 v[12:15], a[12:15], v[32:35], v[112:115]
buffer_load_dwordx4 v[144:147], v87, s[36:39], s49 offen offset:1024 sc1
mav 0,16,36
R 48,4096
v_cvt_pk_bf16_f32 v69, v26, v27
mav 4,20,36
v_cvt_pk_bf16_f32 v70, v28, v29
v_cvt_pk_bf16_f32 v71, v30, v31
mav 8,24,36
v_mul_f32 v75, v16, v16
v_fmac_f32 v75, v17, v17
mav 12,28,36
s_waitcnt lgkmcnt(1)
buffer_load_dwordx4 v[148:151], v88, s[36:39], s49 offen sc1
mav 0,32,40
R 52,5120
v_fmac_f32 v75, v18, v18
mav 4,36,40
v_fmac_f32 v75, v19, v19
v_fmac_f32 v75, v20, v20
mav 8,40,40
v_fmac_f32 v75, v21, v21
v_fmac_f32 v75, v22, v22
mav 12,44,40
buffer_load_dwordx4 v[152:155], v88, s[36:39], s49 offen offset:1024 sc1
mav 0,48,44
R 56,6144
v_fmac_f32 v75, v23, v23
mav 4,52,44
v_fmac_f32 v75, v24, v24
v_fmac_f32 v75, v25, v25
mav 8,56,44
v_fmac_f32 v75, v26, v26
v_fmac_f32 v75, v27, v27
mav 12,60,44
s_waitcnt lgkmcnt(1)
buffer_load_dwordx4 v[156:159], v89, s[36:39], s49 offen sc1
mav 0,64,48
R 60,7168
v_fmac_f32 v75, v28, v28
mav 4,68,48
v_fmac_f32 v75, v29, v29
v_fmac_f32 v75, v30, v30
mav 8,72,48
v_fmac_f32 v75, v31, v31
mav 12,76,48
buffer_load_dwordx4 v[160:163], v89, s[36:39], s49 offen offset:1024 sc1
mav 0,80,52
R 32,8192
mav 4,84,52
mav 8,88,52
s_add_u32 m0, s50, 0
mav 12,92,52
s_waitcnt lgkmcnt(1)
mav 0,96,56
R 36,9216
mav 4,100,56
mav 8,104,56
mav 12,108,56
D 83,0
mav 0,112,60
R 40,10240
mav 4,116,60
mav 8,120,60
ds_write_b32 v97, v74 offset:0
mav 12,124,60
s_waitcnt lgkmcnt(2)
D 83,1024
mav 0,128,32
R 44,11264
v_add_f32 v74, v17, v16
mav 4,132,32
ds_write_b128 v94, v[64:67] offset:0
mav 8,136,32
ds_write_b128 v95, v[68:71] offset:0
mav 12,140,32
ds_read_b128 v[64:67], v96 offset:0
v_add_f32 v74, v18, v74
mav 0,144,36
R 48,12288
v_add_f32 v74, v19, v74
mav 4,148,36
ds_read_b128 v[68:71], v96 offset:1024
v_add_f32 v74, v20, v74
mav 8,152,36
v_add_f32 v74, v21, v74
v_add_f32 v74, v22, v74
mav 12,156,36
s_waitcnt lgkmcnt(4)
v_add_f32 v74, v23, v74
v_add_f32 v74, v24, v74
mav 0,160,40
R 52,13312
v_add_f32 v74, v25, v74
mav 4,164,40
v_add_f32 v74, v26, v74
v_add_f32 v74, v27, v74
mav 8,168,40
v_add_f32 v74, v28, v74
v_add_f32 v74, v29, v74
mav 12,172,40
v_add_f32 v74, v30, v74
v_add_f32 v74, v31, v74
mav 0,176,44
R 56,14336
v_add_u32 v72, 1, v72
mav 4,180,44
v_permlane32_swap_b32 v74, v75
mav 8,184,44
v_add_f32 v74, v74, v75
mav 12,188,44
s_waitcnt lgkmcnt(1)
D 83,2048
mav 0,192,48
R 60,15360
mav 4,196,48
mav 8,200,48
mav 12,204,48
D 83,3072
mav 0,208,52
mav 4,212,52
mav 8,216,52
mav 12,220,52
buffer_store_dwordx4 v[64:67], v91, s[36:39], s54 offen sc1
s_waitcnt lgkmcnt(0)
mav 0,224,56
mav 4,228,56
mav 8,232,56
mav 12,236,56
buffer_store_dwordx4 v[68:71], v92, s[36:39], s54 offen sc1
mav 0,240,60
mav 4,244,60
mav 8,248,60
mav 12,252,60
s_waitcnt vmcnt(16)
buffer_load_dwordx4 v[164:167], v90, s[36:39], s49 offen sc1
mvv 0,116,124
buffer_store_dword v72, v81, s[36:39], s52 offen sc1
mvv 4,120,124
s_waitcnt vmcnt(16)
mvv 8,116,128
s_sub_u32 s54, s54, 3178496
mvv 12,120,128
s_waitcnt vmcnt(15)
buffer_load_dwordx4 v[168:171], v90, s[36:39], s49 offen offset:1024 sc1
mvv 0,116,132
mvv 4,120,132
s_waitcnt vmcnt(15)
mvv 8,116,136
s_waitcnt vmcnt(14)
s_add_u32 s49, s49, 313344
mvv 12,120,136
s_barrier
mvv 0,116,140
R 32,32768
mvv 4,120,140
s_waitcnt vmcnt(13)
mvv 8,116,144
mvv 12,120,144
s_waitcnt vmcnt(12)
buffer_load_dwordx4 v[172:175], v86, s[36:39], s49 offen sc1
mvv 0,116,148
R 36,33792
mvv 4,120,148
s_waitcnt vmcnt(12)
mvv 8,116,152
mvv 12,120,152
s_waitcnt vmcnt(11)
buffer_load_dwordx4 v[176:179], v86, s[36:39], s49 offen offset:1024 sc1
mvv 0,116,156
R 40,34816
mvv 4,120,156
s_waitcnt vmcnt(11)
mvv 8,116,160
mvv 12,120,160
s_waitcnt lgkmcnt(1)
buffer_load_dwordx4 v[180:183], v87, s[36:39], s49 offen sc1
v_mfma_f32_16x16x32_bf16 v[16:19], a[0:3], v[32:35], v[100:103]
R 44,35840
v_cvt_pk_bf16_f32 v64, v0, v1
v_mfma_f32_16x16x32_bf16 v[20:23], a[4:7], v[32:35], v[104:107]
v_cvt_pk_bf16_f32 v65, v2, v3
v_cvt_pk_bf16_f32 v66, v4, v5
v_mfma_f32_16x16x32_bf16 v[24:27], a[8:11], v[32:35], v[108:111]
v_cvt_pk_bf16_f32 v67, v6, v7
v_cvt_pk_bf16_f32 v68, v8, v9
v_mfma_f32_16x16x32_bf16 v[28:31], a[12:15], v[32:35], v[112:115]
buffer_load_dwordx4 v[184:187], v87, s[36:39], s49 offen offset:1024 sc1
mav 16,16,36
R 48,36864
v_cvt_pk_bf16_f32 v69, v10, v11
mav 20,20,36
v_cvt_pk_bf16_f32 v70, v12, v13
v_cvt_pk_bf16_f32 v71, v14, v15
mav 24,24,36
mav 28,28,36
s_waitcnt lgkmcnt(1)
buffer_load_dwordx4 v[188:191], v88, s[36:39], s49 offen sc1
mav 16,32,40
R 52,37888
mav 20,36,40
mav 24,40,40
mav 28,44,40
buffer_load_dwordx4 v[192:195], v88, s[36:39], s49 offen offset:1024 sc1
mav 16,48,44
R 56,38912
mav 20,52,44
mav 24,56,44
mav 28,60,44
s_waitcnt lgkmcnt(1)
buffer_load_dwordx4 v[196:199], v89, s[36:39], s49 offen sc1
mav 16,64,48
R 60,39936
mav 20,68,48
mav 24,72,48
mav 28,76,48
buffer_load_dwordx4 v[200:203], v89, s[36:39], s49 offen offset:1024 sc1
mav 16,80,52
R 32,40960
mav 20,84,52
mav 24,88,52
s_add_u32 m0, s50, 32768
mav 28,92,52
s_waitcnt lgkmcnt(1)
mav 16,96,56
R 36,41984
mav 20,100,56
mav 24,104,56
mav 28,108,56
D 83,0
mav 16,112,60
R 40,43008
mav 20,116,60
mav 24,120,60
ds_write_b32 v97, v74 offset:1024
mav 28,124,60
s_waitcnt lgkmcnt(2)
D 83,1024
mav 16,128,32
R 44,44032
mav 20,132,32
ds_write_b128 v94, v[64:67] offset:0
mav 24,136,32
ds_write_b128 v95, v[68:71] offset:0
mav 28,140,32
ds_read_b128 v[64:67], v96 offset:0
mav 16,144,36
R 48,45056
mav 20,148,36
ds_read_b128 v[68:71], v96 offset:1024
mav 24,152,36
mav 28,156,36
s_waitcnt lgkmcnt(4)
mav 16,160,40
mav 20,164,40
R 52,46080
v_add_u32 v72, 69, v72
mav 24,168,40
mav 28,172,40
mav 16,176,44
R 56,47104
mav 20,180,44
mav 24,184,44
mav 28,188,44
s_waitcnt lgkmcnt(1)
D 83,2048
mav 16,192,48
R 60,48128
mav 20,196,48
mav 24,200,48
mav 28,204,48
D 83,3072
mav 16,208,52
mav 20,212,52
mav 24,216,52
mav 28,220,52
buffer_store_dwordx4 v[64:67], v91, s[44:47], s54 offen
s_waitcnt lgkmcnt(0)
mav 16,224,56
mav 20,228,56
mav 24,232,56
mav 28,236,56
buffer_store_dwordx4 v[68:71], v92, s[44:47], s54 offen
mav 16,240,60
mav 20,244,60
mav 24,248,60
mav 28,252,60
s_waitcnt vmcnt(16)
buffer_load_dwordx4 v[124:127], v90, s[36:39], s49 offen sc1
mvv 16,116,164
buffer_store_dword v72, v81, s[36:39], s52 offen sc1
mvv 20,120,164
s_waitcnt vmcnt(16)
mvv 24,116,168
s_xor_b32 s50, s50, 0x10000
s_sub_u32 s54, s54, 32768
mvv 28,120,168
s_waitcnt vmcnt(15)
buffer_load_dwordx4 v[128:131], v90, s[36:39], s49 offen offset:1024 sc1
mvv 16,116,172
mvv 20,120,172
s_waitcnt vmcnt(15)
v_xor_b32 v80, 0x10000, v80
mvv 24,116,176
s_waitcnt vmcnt(14)
s_add_u32 s49, s49, 313344
mvv 28,120,176
s_barrier
mvv 16,116,180
R 32,0
mvv 20,120,180
s_waitcnt vmcnt(13)
mvv 24,116,184
mvv 28,120,184
s_waitcnt vmcnt(12)
buffer_load_dwordx4 v[132:135], v86, s[36:39], s49 offen sc1
mvv 16,116,188
R 36,1024
mvv 20,120,188
s_waitcnt vmcnt(12)
mvv 24,116,192
mvv 28,120,192
s_waitcnt vmcnt(11)
buffer_load_dwordx4 v[136:139], v86, s[36:39], s49 offen offset:1024 sc1
mvv 16,116,196
R 40,2048
mvv 20,120,196
s_waitcnt vmcnt(11)
mvv 24,116,200
mvv 28,120,200
s_waitcnt lgkmcnt(1)
buffer_load_dwordx4 v[140:143], v87, s[36:39], s49 offen sc1
v_mfma_f32_16x16x32_bf16 v[0:3], a[0:3], v[32:35], v[100:103]
R 44,3072
v_cvt_pk_bf16_f32 v64, v16, v17
v_mfma_f32_16x16x32_bf16 v[4:7], a[4:7], v[32:35], v[104:107]
v_cvt_pk_bf16_f32 v65, v18, v19
v_cvt_pk_bf16_f32 v66, v20, v21
v_mfma_f32_16x16x32_bf16 v[8:11], a[8:11], v[32:35], v[108:111]
v_cvt_pk_bf16_f32 v67, v22, v23
v_cvt_pk_bf16_f32 v68, v24, v25
v_mfma_f32_16x16x32_bf16 v[12:15], a[12:15], v[32:35], v[112:115]
buffer_load_dwordx4 v[144:147], v87, s[36:39], s49 offen offset:1024 sc1
mav 0,16,36
R 48,4096
v_cvt_pk_bf16_f32 v69, v26, v27
mav 4,20,36
v_cvt_pk_bf16_f32 v70, v28, v29
v_cvt_pk_bf16_f32 v71, v30, v31
mav 8,24,36
mav 12,28,36
s_waitcnt lgkmcnt(1)
buffer_load_dwordx4 v[148:151], v88, s[36:39], s49 offen sc1
mav 0,32,40
R 52,5120
mav 4,36,40
mav 8,40,40
mav 12,44,40
buffer_load_dwordx4 v[152:155], v88, s[36:39], s49 offen offset:1024 sc1
mav 0,48,44
R 56,6144
mav 4,52,44
mav 8,56,44
mav 12,60,44
s_waitcnt lgkmcnt(1)
buffer_load_dwordx4 v[156:159], v89, s[36:39], s49 offen sc1
mav 0,64,48
R 60,7168
mav 4,68,48
mav 8,72,48
mav 12,76,48
buffer_load_dwordx4 v[160:163], v89, s[36:39], s49 offen offset:1024 sc1
mav 0,80,52
R 32,8192
mav 4,84,52
mav 8,88,52
s_add_u32 m0, s50, 0
mav 12,92,52
s_waitcnt lgkmcnt(1)
mav 0,96,56
R 36,9216
mav 4,100,56
mav 8,104,56
mav 12,108,56
D 83,0
mav 0,112,60
R 40,10240
mav 4,116,60
mav 8,120,60
mav 12,124,60
s_waitcnt lgkmcnt(1)
D 83,1024
mav 0,128,32
R 44,11264
mav 4,132,32
ds_write_b128 v94, v[64:67] offset:0
mav 8,136,32
ds_write_b128 v95, v[68:71] offset:0
mav 12,140,32
ds_read_b128 v[64:67], v96 offset:0
mav 0,144,36
R 48,12288
mav 4,148,36
ds_read_b128 v[68:71], v96 offset:1024
mav 8,152,36
mav 12,156,36
s_waitcnt lgkmcnt(4)
mav 0,160,40
R 52,13312
mav 4,164,40
mav 8,168,40
mav 12,172,40
mav 0,176,44
R 56,14336
mav 4,180,44
mav 8,184,44
mav 12,188,44
s_waitcnt lgkmcnt(1)
D 83,2048
mav 0,192,48
R 60,15360
mav 4,196,48
)"
R"(
mav 8,200,48
mav 12,204,48
D 83,3072
mav 0,208,52
mav 4,212,52
mav 8,216,52
mav 12,220,52
buffer_store_dwordx4 v[64:67], v91, s[44:47], s54 offen
s_waitcnt lgkmcnt(0)
mav 0,224,56
mav 4,228,56
mav 8,232,56
mav 12,236,56
buffer_store_dwordx4 v[68:71], v92, s[44:47], s54 offen
mav 0,240,60
mav 4,244,60
mav 8,248,60
mav 12,252,60
s_waitcnt vmcnt(16)
buffer_load_dwordx4 v[164:167], v90, s[36:39], s49 offen sc1
mvv 0,116,124
mvv 4,120,124
s_waitcnt vmcnt(15)
mvv 8,116,128
s_sub_u32 s54, s54, 32768
mvv 12,120,128
s_waitcnt vmcnt(14)
buffer_load_dwordx4 v[168:171], v90, s[36:39], s49 offen offset:1024 sc1
mvv 0,116,132
mvv 4,120,132
s_waitcnt vmcnt(14)
mvv 8,116,136
s_waitcnt vmcnt(13)
s_add_u32 s49, s49, 313344
mvv 12,120,136
s_barrier
mvv 0,116,140
R 32,32768
mvv 4,120,140
s_waitcnt vmcnt(12)
mvv 8,116,144
mvv 12,120,144
s_waitcnt vmcnt(11)
buffer_load_dwordx4 v[172:175], v86, s[36:39], s49 offen sc1
mvv 0,116,148
R 36,33792
mvv 4,120,148
s_waitcnt vmcnt(11)
mvv 8,116,152
mvv 12,120,152
s_waitcnt vmcnt(10)
buffer_load_dwordx4 v[176:179], v86, s[36:39], s49 offen offset:1024 sc1
mvv 0,116,156
R 40,34816
mvv 4,120,156
s_waitcnt vmcnt(10)
mvv 8,116,160
mvv 12,120,160
s_waitcnt lgkmcnt(1)
buffer_load_dwordx4 v[180:183], v87, s[36:39], s49 offen sc1
v_mfma_f32_16x16x32_bf16 v[16:19], a[0:3], v[32:35], v[100:103]
R 44,35840
v_cvt_pk_bf16_f32 v64, v0, v1
v_mfma_f32_16x16x32_bf16 v[20:23], a[4:7], v[32:35], v[104:107]
v_cvt_pk_bf16_f32 v65, v2, v3
v_cvt_pk_bf16_f32 v66, v4, v5
v_mfma_f32_16x16x32_bf16 v[24:27], a[8:11], v[32:35], v[108:111]
v_cvt_pk_bf16_f32 v67, v6, v7
v_cvt_pk_bf16_f32 v68, v8, v9
v_mfma_f32_16x16x32_bf16 v[28:31], a[12:15], v[32:35], v[112:115]
buffer_load_dwordx4 v[184:187], v87, s[36:39], s49 offen offset:1024 sc1
mav 16,16,36
R 48,36864
v_cvt_pk_bf16_f32 v69, v10, v11
mav 20,20,36
v_cvt_pk_bf16_f32 v70, v12, v13
v_cvt_pk_bf16_f32 v71, v14, v15
mav 24,24,36
mav 28,28,36
s_waitcnt lgkmcnt(1)
buffer_load_dwordx4 v[188:191], v88, s[36:39], s49 offen sc1
mav 16,32,40
R 52,37888
mav 20,36,40
mav 24,40,40
mav 28,44,40
buffer_load_dwordx4 v[192:195], v88, s[36:39], s49 offen offset:1024 sc1
mav 16,48,44
R 56,38912
mav 20,52,44
mav 24,56,44
mav 28,60,44
s_waitcnt lgkmcnt(1)
buffer_load_dwordx4 v[196:199], v89, s[36:39], s49 offen sc1
mav 16,64,48
R 60,39936
mav 20,68,48
mav 24,72,48
mav 28,76,48
buffer_load_dwordx4 v[200:203], v89, s[36:39], s49 offen offset:1024 sc1
mav 16,80,52
R 32,40960
mav 20,84,52
mav 24,88,52
s_add_u32 m0, s50, 32768
mav 28,92,52
s_waitcnt lgkmcnt(1)
mav 16,96,56
R 36,41984
mav 20,100,56
mav 24,104,56
mav 28,108,56
D 83,0
mav 16,112,60
R 40,43008
mav 20,116,60
mav 24,120,60
mav 28,124,60
s_waitcnt lgkmcnt(1)
D 83,1024
mav 16,128,32
R 44,44032
mav 20,132,32
ds_write_b128 v94, v[64:67] offset:0
mav 24,136,32
ds_write_b128 v95, v[68:71] offset:0
mav 28,140,32
ds_read_b128 v[64:67], v96 offset:0
mav 16,144,36
R 48,45056
mav 20,148,36
ds_read_b128 v[68:71], v96 offset:1024
LsR20:
s_cbranch_execz LwR
mav 24,152,36
mav 28,156,36
s_waitcnt lgkmcnt(4)
mav 16,160,40
s_mov_b32 s60, -13
.p2align 6
LR3_2:
mav 20,164,40
R 52,46080
mav 24,168,40
mav 28,172,40
mav 16,176,44
R 56,47104
mav 20,180,44
mav 24,184,44
mav 28,188,44
s_waitcnt lgkmcnt(1)
D 83,2048
mav 16,192,48
R 60,48128
mav 20,196,48
mav 24,200,48
mav 28,204,48
D 83,3072
mav 16,208,52
mav 20,212,52
mav 24,216,52
mav 28,220,52
buffer_store_dwordx4 v[64:67], v91, s[44:47], s54 offen
s_waitcnt lgkmcnt(0)
mav 16,224,56
mav 20,228,56
mav 24,232,56
mav 28,236,56
buffer_store_dwordx4 v[68:71], v92, s[44:47], s54 offen
mav 16,240,60
mav 20,244,60
mav 24,248,60
mav 28,252,60
s_waitcnt vmcnt(15)
buffer_load_dwordx4 v[124:127], v90, s[36:39], s49 offen sc1
mvv 16,116,164
mvv 20,120,164
s_waitcnt vmcnt(15)
mvv 24,116,168
s_xor_b32 s50, s50, 0x10000
s_sub_u32 s54, s54, 32768
mvv 28,120,168
s_waitcnt vmcnt(14)
buffer_load_dwordx4 v[128:131], v90, s[36:39], s49 offen offset:1024 sc1
mvv 16,116,172
mvv 20,120,172
s_waitcnt vmcnt(14)
v_xor_b32 v80, 0x10000, v80
mvv 24,116,176
s_waitcnt vmcnt(13)
s_add_u32 s49, s49, 313344
mvv 28,120,176
s_barrier
mvv 16,116,180
R 32,0
mvv 20,120,180
s_waitcnt vmcnt(12)
mvv 24,116,184
mvv 28,120,184
s_waitcnt vmcnt(11)
buffer_load_dwordx4 v[132:135], v86, s[36:39], s49 offen sc1
mvv 16,116,188
R 36,1024
mvv 20,120,188
s_waitcnt vmcnt(11)
mvv 24,116,192
mvv 28,120,192
s_waitcnt vmcnt(10)
buffer_load_dwordx4 v[136:139], v86, s[36:39], s49 offen offset:1024 sc1
mvv 16,116,196
R 40,2048
mvv 20,120,196
s_waitcnt vmcnt(10)
mvv 24,116,200
mvv 28,120,200
s_waitcnt lgkmcnt(1)
buffer_load_dwordx4 v[140:143], v87, s[36:39], s49 offen sc1
v_mfma_f32_16x16x32_bf16 v[0:3], a[0:3], v[32:35], v[100:103]
R 44,3072
v_cvt_pk_bf16_f32 v64, v16, v17
v_mfma_f32_16x16x32_bf16 v[4:7], a[4:7], v[32:35], v[104:107]
v_cvt_pk_bf16_f32 v65, v18, v19
v_cvt_pk_bf16_f32 v66, v20, v21
v_mfma_f32_16x16x32_bf16 v[8:11], a[8:11], v[32:35], v[108:111]
v_cvt_pk_bf16_f32 v67, v22, v23
v_cvt_pk_bf16_f32 v68, v24, v25
v_mfma_f32_16x16x32_bf16 v[12:15], a[12:15], v[32:35], v[112:115]
buffer_load_dwordx4 v[144:147], v87, s[36:39], s49 offen offset:1024 sc1
mav 0,16,36
R 48,4096
v_cvt_pk_bf16_f32 v69, v26, v27
mav 4,20,36
v_cvt_pk_bf16_f32 v70, v28, v29
v_cvt_pk_bf16_f32 v71, v30, v31
mav 8,24,36
mav 12,28,36
s_waitcnt lgkmcnt(1)
buffer_load_dwordx4 v[148:151], v88, s[36:39], s49 offen sc1
mav 0,32,40
R 52,5120
mav 4,36,40
mav 8,40,40
mav 12,44,40
buffer_load_dwordx4 v[152:155], v88, s[36:39], s49 offen offset:1024 sc1
mav 0,48,44
R 56,6144
mav 4,52,44
mav 8,56,44
mav 12,60,44
s_waitcnt lgkmcnt(1)
buffer_load_dwordx4 v[156:159], v89, s[36:39], s49 offen sc1
mav 0,64,48
R 60,7168
mav 4,68,48
mav 8,72,48
mav 12,76,48
buffer_load_dwordx4 v[160:163], v89, s[36:39], s49 offen offset:1024 sc1
mav 0,80,52
R 32,8192
mav 4,84,52
mav 8,88,52
s_add_u32 m0, s50, 0
mav 12,92,52
s_waitcnt lgkmcnt(1)
mav 0,96,56
R 36,9216
mav 4,100,56
mav 8,104,56
mav 12,108,56
D 83,0
mav 0,112,60
R 40,10240
mav 4,116,60
mav 8,120,60
mav 12,124,60
s_waitcnt lgkmcnt(1)
D 83,1024
mav 0,128,32
R 44,11264
mav 4,132,32
ds_write_b128 v94, v[64:67] offset:0
mav 8,136,32
ds_write_b128 v95, v[68:71] offset:0
mav 12,140,32
ds_read_b128 v[64:67], v96 offset:0
mav 0,144,36
R 48,12288
mav 4,148,36
ds_read_b128 v[68:71], v96 offset:1024
mav 8,152,36
mav 12,156,36
s_waitcnt lgkmcnt(4)
mav 0,160,40
R 52,13312
mav 4,164,40
mav 8,168,40
mav 12,172,40
mav 0,176,44
R 56,14336
mav 4,180,44
mav 8,184,44
mav 12,188,44
s_waitcnt lgkmcnt(1)
D 83,2048
mav 0,192,48
R 60,15360
mav 4,196,48
mav 8,200,48
mav 12,204,48
D 83,3072
mav 0,208,52
mav 4,212,52
mav 8,216,52
mav 12,220,52
buffer_store_dwordx4 v[64:67], v91, s[44:47], s54 offen
s_waitcnt lgkmcnt(0)
mav 0,224,56
mav 4,228,56
mav 8,232,56
mav 12,236,56
buffer_store_dwordx4 v[68:71], v92, s[44:47], s54 offen
mav 0,240,60
mav 4,244,60
mav 8,248,60
mav 12,252,60
s_waitcnt vmcnt(15)
buffer_load_dwordx4 v[164:167], v90, s[36:39], s49 offen sc1
mvv 0,116,124
mvv 4,120,124
s_waitcnt vmcnt(15)
mvv 8,116,128
s_sub_u32 s54, s54, 32768
mvv 12,120,128
s_waitcnt vmcnt(14)
buffer_load_dwordx4 v[168:171], v90, s[36:39], s49 offen offset:1024 sc1
mvv 0,116,132
mvv 4,120,132
s_waitcnt vmcnt(14)
mvv 8,116,136
s_waitcnt vmcnt(13)
s_add_u32 s49, s49, 313344
mvv 12,120,136
s_barrier
mvv 0,116,140
R 32,32768
mvv 4,120,140
s_waitcnt vmcnt(12)
mvv 8,116,144
mvv 12,120,144
s_waitcnt vmcnt(11)
buffer_load_dwordx4 v[172:175], v86, s[36:39], s49 offen sc1
mvv 0,116,148
R 36,33792
mvv 4,120,148
s_waitcnt vmcnt(11)
mvv 8,116,152
mvv 12,120,152
s_waitcnt vmcnt(10)
buffer_load_dwordx4 v[176:179], v86, s[36:39], s49 offen offset:1024 sc1
mvv 0,116,156
R 40,34816
mvv 4,120,156
s_waitcnt vmcnt(10)
mvv 8,116,160
mvv 12,120,160
s_waitcnt lgkmcnt(1)
buffer_load_dwordx4 v[180:183], v87, s[36:39], s49 offen sc1
v_mfma_f32_16x16x32_bf16 v[16:19], a[0:3], v[32:35], v[100:103]
R 44,35840
v_cvt_pk_bf16_f32 v64, v0, v1
v_mfma_f32_16x16x32_bf16 v[20:23], a[4:7], v[32:35], v[104:107]
v_cvt_pk_bf16_f32 v65, v2, v3
v_cvt_pk_bf16_f32 v66, v4, v5
v_mfma_f32_16x16x32_bf16 v[24:27], a[8:11], v[32:35], v[108:111]
v_cvt_pk_bf16_f32 v67, v6, v7
v_cvt_pk_bf16_f32 v68, v8, v9
v_mfma_f32_16x16x32_bf16 v[28:31], a[12:15], v[32:35], v[112:115]
buffer_load_dwordx4 v[184:187], v87, s[36:39], s49 offen offset:1024 sc1
mav 16,16,36
R 48,36864
v_cvt_pk_bf16_f32 v69, v10, v11
mav 20,20,36
v_cvt_pk_bf16_f32 v70, v12, v13
v_cvt_pk_bf16_f32 v71, v14, v15
mav 24,24,36
mav 28,28,36
s_waitcnt lgkmcnt(1)
buffer_load_dwordx4 v[188:191], v88, s[36:39], s49 offen sc1
mav 16,32,40
R 52,37888
mav 20,36,40
mav 24,40,40
mav 28,44,40
buffer_load_dwordx4 v[192:195], v88, s[36:39], s49 offen offset:1024 sc1
mav 16,48,44
R 56,38912
mav 20,52,44
mav 24,56,44
mav 28,60,44
s_waitcnt lgkmcnt(1)
buffer_load_dwordx4 v[196:199], v89, s[36:39], s49 offen sc1
mav 16,64,48
R 60,39936
mav 20,68,48
mav 24,72,48
mav 28,76,48
buffer_load_dwordx4 v[200:203], v89, s[36:39], s49 offen offset:1024 sc1
mav 16,80,52
R 32,40960
mav 20,84,52
mav 24,88,52
s_add_u32 m0, s50, 32768
mav 28,92,52
s_waitcnt lgkmcnt(1)
mav 16,96,56
R 36,41984
mav 20,100,56
mav 24,104,56
mav 28,108,56
D 83,0
mav 16,112,60
R 40,43008
mav 20,116,60
mav 24,120,60
mav 28,124,60
s_waitcnt lgkmcnt(1)
D 83,1024
mav 16,128,32
R 44,44032
mav 20,132,32
ds_write_b128 v94, v[64:67] offset:0
mav 24,136,32
ds_write_b128 v95, v[68:71] offset:0
mav 28,140,32
ds_read_b128 v[64:67], v96 offset:0
mav 16,144,36
R 48,45056
mav 20,148,36
ds_read_b128 v[68:71], v96 offset:1024
mav 24,152,36
mav 28,156,36
s_waitcnt lgkmcnt(4)
s_add_u32 s60, s60, 1
mav 16,160,40
s_cbranch_scc0 LR3_2
mav 20,164,40
R 52,46080
mav 24,168,40
mav 28,172,40
mav 16,176,44
R 56,47104
mav 20,180,44
mav 24,184,44
mav 28,188,44
s_waitcnt lgkmcnt(1)
D 83,2048
mav 16,192,48
R 60,48128
mav 20,196,48
mav 24,200,48
mav 28,204,48
D 83,3072
mav 16,208,52
mav 20,212,52
mav 24,216,52
mav 28,220,52
buffer_store_dwordx4 v[64:67], v91, s[44:47], s54 offen
s_waitcnt lgkmcnt(0)
mav 16,224,56
mav 20,228,56
mav 24,232,56
mav 28,236,56
buffer_store_dwordx4 v[68:71], v92, s[44:47], s54 offen
mav 16,240,60
mav 20,244,60
mav 24,248,60
mav 28,252,60
s_waitcnt vmcnt(15)
buffer_load_dwordx4 v[124:127], v90, s[36:39], s49 offen sc1
mvv 16,116,164
mvv 20,120,164
s_waitcnt vmcnt(15)
mvv 24,116,168
s_sub_u32 s54, s54, 32768
mvv 28,120,168
s_waitcnt vmcnt(14)
buffer_load_dwordx4 v[128:131], v90, s[36:39], s49 offen offset:1024 sc1
mvv 16,116,172
mvv 20,120,172
s_waitcnt vmcnt(14)
v_xor_b32 v80, 0x10000, v80
mvv 24,116,176
s_waitcnt vmcnt(13)
s_add_u32 s49, s49, 313344
mvv 28,120,176
s_barrier
mvv 16,116,180
R 32,0
mvv 20,120,180
s_waitcnt vmcnt(12)
mvv 24,116,184
mvv 28,120,184
s_waitcnt vmcnt(11)
buffer_load_dwordx4 v[132:135], v86, s[36:39], s49 offen sc1
mvv 16,116,188
R 36,1024
mvv 20,120,188
s_waitcnt vmcnt(11)
mvv 24,116,192
mvv 28,120,192
s_waitcnt vmcnt(10)
buffer_load_dwordx4 v[136:139], v86, s[36:39], s49 offen offset:1024 sc1
mvv 16,116,196
R 40,2048
mvv 20,120,196
s_waitcnt vmcnt(10)
mvv 24,116,200
mvv 28,120,200
s_waitcnt lgkmcnt(1)
buffer_load_dwordx4 v[140:143], v87, s[36:39], s49 offen sc1
v_mfma_f32_16x16x32_bf16 v[0:3], a[0:3], v[32:35], v[100:103]
R 44,3072
v_cvt_pk_bf16_f32 v64, v16, v17
v_mfma_f32_16x16x32_bf16 v[4:7], a[4:7], v[32:35], v[104:107]
v_cvt_pk_bf16_f32 v65, v18, v19
v_cvt_pk_bf16_f32 v66, v20, v21
v_mfma_f32_16x16x32_bf16 v[8:11], a[8:11], v[32:35], v[108:111]
v_cvt_pk_bf16_f32 v67, v22, v23
v_cvt_pk_bf16_f32 v68, v24, v25
v_mfma_f32_16x16x32_bf16 v[12:15], a[12:15], v[32:35], v[112:115]
buffer_load_dwordx4 v[144:147], v87, s[36:39], s49 offen offset:1024 sc1
mav 0,16,36
R 48,4096
v_cvt_pk_bf16_f32 v69, v26, v27
mav 4,20,36
v_cvt_pk_bf16_f32 v70, v28, v29
v_cvt_pk_bf16_f32 v71, v30, v31
mav 8,24,36
mav 12,28,36
s_waitcnt lgkmcnt(1)
buffer_load_dwordx4 v[148:151], v88, s[36:39], s49 offen sc1
mav 0,32,40
R 52,5120
mav 4,36,40
mav 8,40,40
mav 12,44,40
buffer_load_dwordx4 v[152:155], v88, s[36:39], s49 offen offset:1024 sc1
mav 0,48,44
R 56,6144
mav 4,52,44
mav 8,56,44
mav 12,60,44
s_waitcnt lgkmcnt(1)
buffer_load_dwordx4 v[156:159], v89, s[36:39], s49 offen sc1
mav 0,64,48
R 60,7168
mav 4,68,48
mav 8,72,48
mav 12,76,48
buffer_load_dwordx4 v[160:163], v89, s[36:39], s49 offen offset:1024 sc1
mav 0,80,52
R 32,8192
mav 4,84,52
mav 8,88,52
mav 12,92,52
s_waitcnt lgkmcnt(1)
mav 0,96,56
R 36,9216
mav 4,100,56
mav 8,104,56
mav 12,108,56
mav 0,112,60
R 40,10240
mav 4,116,60
mav 8,120,60
mav 12,124,60
s_waitcnt lgkmcnt(1)
ds_write_b128 v94, v[64:67] offset:0
mav 0,128,32
R 44,11264
mav 4,132,32
ds_write_b128 v95, v[68:71] offset:0
mav 8,136,32
ds_read_b128 v[64:67], v96 offset:0
mav 12,140,32
ds_read_b128 v[68:71], v96 offset:1024
mav 0,144,36
R 48,12288
mav 4,148,36
mav 8,152,36
mav 12,156,36
s_waitcnt lgkmcnt(3)
mav 0,160,40
R 52,13312
mav 4,164,40
mav 8,168,40
mav 12,172,40
mav 0,176,44
R 56,14336
mav 4,180,44
mav 8,184,44
mav 12,188,44
s_waitcnt lgkmcnt(1)
mav 0,192,48
R 60,15360
mav 4,196,48
mav 8,200,48
mav 12,204,48
mav 0,208,52
mav 4,212,52
mav 8,216,52
mav 12,220,52
buffer_store_dwordx4 v[64:67], v91, s[44:47], s54 offen
s_waitcnt lgkmcnt(0)
mav 0,224,56
mav 4,228,56
mav 8,232,56
mav 12,236,56
buffer_store_dwordx4 v[68:71], v92, s[44:47], s54 offen
mav 0,240,60
mav 4,244,60
mav 8,248,60
mav 12,252,60
s_waitcnt vmcnt(11)
buffer_load_dwordx4 v[164:167], v90, s[36:39], s49 offen sc1
mvv 0,116,124
mvv 4,120,124
s_waitcnt vmcnt(11)
mvv 8,116,128
s_sub_u32 s54, s54, 32768
mvv 12,120,128
s_waitcnt vmcnt(10)
buffer_load_dwordx4 v[168:171], v90, s[36:39], s49 offen offset:1024 sc1
mvv 0,116,132
mvv 4,120,132
s_waitcnt vmcnt(10)
mvv 8,116,136
s_waitcnt vmcnt(9)
s_add_u32 s49, s49, 313344
mvv 12,120,136
s_barrier
mvv 0,116,140
R 32,32768
mvv 4,120,140
s_waitcnt vmcnt(8)
mvv 8,116,144
mvv 12,120,144
s_waitcnt vmcnt(7)
buffer_load_dwordx4 v[172:175], v86, s[36:39], s49 offen sc1
mvv 0,116,148
R 36,33792
mvv 4,120,148
s_waitcnt vmcnt(7)
mvv 8,116,152
mvv 12,120,152
s_waitcnt vmcnt(6)
buffer_load_dwordx4 v[176:179], v86, s[36:39], s49 offen offset:1024 sc1
mvv 0,116,156
R 40,34816
mvv 4,120,156
s_waitcnt vmcnt(6)
mvv 8,116,160
mvv 12,120,160
s_waitcnt lgkmcnt(1)
buffer_load_dwordx4 v[180:183], v87, s[36:39], s49 offen sc1
v_mfma_f32_16x16x32_bf16 v[16:19], a[0:3], v[32:35], v[100:103]
R 44,35840
v_cvt_pk_bf16_f32 v64, v0, v1
v_mfma_f32_16x16x32_bf16 v[20:23], a[4:7], v[32:35], v[104:107]
v_cvt_pk_bf16_f32 v65, v2, v3
v_cvt_pk_bf16_f32 v66, v4, v5
v_mfma_f32_16x16x32_bf16 v[24:27], a[8:11], v[32:35], v[108:111]
v_cvt_pk_bf16_f32 v67, v6, v7
v_cvt_pk_bf16_f32 v68, v8, v9
v_mfma_f32_16x16x32_bf16 v[28:31], a[12:15], v[32:35], v[112:115]
buffer_load_dwordx4 v[184:187], v87, s[36:39], s49 offen offset:1024 sc1
mav 16,16,36
R 48,36864
v_cvt_pk_bf16_f32 v69, v10, v11
mav 20,20,36
ds_write_b128 v94, v[64:67] offset:0
mav 24,24,36
LsR21:
s_cbranch_execz LwR
v_cvt_pk_bf16_f32 v70, v12, v13
v_cvt_pk_bf16_f32 v71, v14, v15
mav 28,28,36
s_waitcnt lgkmcnt(2)
buffer_load_dwordx4 v[188:191], v88, s[36:39], s49 offen sc1
mav 16,32,40
R 52,37888
mav 20,36,40
ds_write_b128 v95, v[68:71] offset:0
mav 24,40,40
ds_read_b128 v[64:67], v96 offset:0
mav 28,44,40
buffer_load_dwordx4 v[192:195], v88, s[36:39], s49 offen offset:1024 sc1
mav 16,48,44
R 56,38912
mav 20,52,44
ds_read_b128 v[68:71], v96 offset:1024
mav 24,56,44
mav 28,60,44
s_waitcnt lgkmcnt(3)
buffer_load_dwordx4 v[196:199], v89, s[36:39], s49 offen sc1
mav 16,64,48
R 60,39936
mav 20,68,48
s_waitcnt lgkmcnt(3)
mav 24,72,48
mav 28,76,48
buffer_store_dwordx4 v[64:67], v91, s[44:47], s54 offen
buffer_load_dwordx4 v[200:203], v89, s[36:39], s49 offen offset:1024 sc1
mav 16,80,52
s_waitcnt lgkmcnt(1)
R 32,40960
mav 20,84,52
mav 24,88,52
mav 28,92,52
buffer_store_dwordx4 v[68:71], v92, s[44:47], s54 offen
mav 16,96,56
R 36,41984
mav 20,100,56
mav 24,104,56
mav 28,108,56
s_waitcnt lgkmcnt(1)
mav 16,112,60
R 40,43008
mav 20,116,60
mav 24,120,60
mav 28,124,60
mav 16,128,32
R 44,44032
mav 20,132,32
mav 24,136,32
mav 28,140,32
s_waitcnt lgkmcnt(1)
mav 16,144,36
R 48,45056
mav 20,148,36
mav 24,152,36
mav 28,156,36
mav 16,160,40
mav 20,164,40
R 52,46080
mav 24,168,40
mav 28,172,40
s_waitcnt lgkmcnt(1)
mav 16,176,44
R 56,47104
mav 20,180,44
mav 24,184,44
mav 28,188,44
mav 16,192,48
R 60,48128
mav 20,196,48
mav 24,200,48
mav 28,204,48
s_waitcnt lgkmcnt(1)
mav 16,208,52
mav 20,212,52
mav 24,216,52
mav 28,220,52
mav 16,224,56
mav 20,228,56
mav 24,232,56
mav 28,236,56
s_waitcnt lgkmcnt(0)
mav 16,240,60
mav 20,244,60
mav 24,248,60
mav 28,252,60
s_waitcnt vmcnt(11)
mvv 16,116,164
mvv 20,120,164
s_waitcnt vmcnt(10)
mvv 24,116,168
s_sub_u32 s54, s54, 32768
mvv 28,120,168
s_waitcnt vmcnt(9)
mvv 16,116,172
mvv 20,120,172
s_waitcnt vmcnt(8)
mvv 24,116,176
mvv 28,120,176
s_waitcnt vmcnt(7)
mvv 16,116,180
mvv 20,120,180
s_waitcnt vmcnt(6)
mvv 24,116,184
mvv 28,120,184
s_waitcnt vmcnt(5)
mvv 16,116,188
mvv 20,120,188
s_waitcnt vmcnt(4)
mvv 24,116,192
mvv 28,120,192
s_waitcnt vmcnt(3)
mvv 16,116,196
mvv 20,120,196
s_waitcnt vmcnt(1)
mvv 24,116,200
mvv 28,120,200
s_nop 3
v_cvt_pk_bf16_f32 v64, v16, v17
v_cvt_pk_bf16_f32 v65, v18, v19
v_cvt_pk_bf16_f32 v66, v20, v21
v_cvt_pk_bf16_f32 v67, v22, v23
ds_write_b128 v94, v[64:67] offset:0
v_cvt_pk_bf16_f32 v68, v24, v25
v_cvt_pk_bf16_f32 v69, v26, v27
v_cvt_pk_bf16_f32 v70, v28, v29
v_cvt_pk_bf16_f32 v71, v30, v31
ds_write_b128 v95, v[68:71] offset:0
ds_read_b128 v[64:67], v96 offset:0
ds_read_b128 v[68:71], v96 offset:1024
s_waitcnt lgkmcnt(0)
buffer_store_dwordx4 v[64:67], v91, s[44:47], s54 offen
buffer_store_dwordx4 v[68:71], v92, s[44:47], s54 offen
s_endpgm
Ct:
v_lshrrev_b32 v156, 5, v80
v_mul_u32_u24 v156, 96, v156
v_add_u32 v156, v80, v156
Dt:
s_mov_b32 s60, 4096
buffer_load_dwordx4 v[124:127], v156, s[36:39], s60 offen sc1
s_add_u32 s60, s60, 4096
buffer_load_dwordx4 v[128:131], v156, s[36:39], s60 offen sc1
s_add_u32 s60, s60, 4096
buffer_load_dwordx4 v[132:135], v156, s[36:39], s60 offen sc1
s_add_u32 s60, s60, 4096
buffer_load_dwordx4 v[136:139], v156, s[36:39], s60 offen sc1
s_add_u32 s60, s60, 4096
buffer_load_dwordx4 v[140:143], v156, s[36:39], s60 offen sc1
s_add_u32 s60, s60, 4096
buffer_load_dwordx4 v[144:147], v156, s[36:39], s60 offen sc1
s_add_u32 s60, s60, 4096
buffer_load_dwordx4 v[148:151], v156, s[36:39], s60 offen sc1
s_add_u32 s60, s60, 4096
buffer_load_dwordx4 v[152:155], v156, s[36:39], s60 offen sc1
s_add_u32 s60, s60, 4096
s_mov_b64 s[66:67], 0
s_waitcnt vmcnt(0)
v_sub_u32 v124, v124, v72
v_xor_b32 v125, v125, v73
v_cmp_lt_u64 vcc, s[62:63], v[124:125]
s_or_b64 s[66:67], s[66:67], vcc
v_sub_u32 v126, v126, v72
v_xor_b32 v127, v127, v73
v_cmp_lt_u64 vcc, s[62:63], v[126:127]
s_or_b64 s[66:67], s[66:67], vcc
v_sub_u32 v128, v128, v72
v_xor_b32 v129, v129, v73
v_cmp_lt_u64 vcc, s[62:63], v[128:129]
s_or_b64 s[66:67], s[66:67], vcc
v_sub_u32 v130, v130, v72
v_xor_b32 v131, v131, v73
v_cmp_lt_u64 vcc, s[62:63], v[130:131]
s_or_b64 s[66:67], s[66:67], vcc
v_sub_u32 v132, v132, v72
v_xor_b32 v133, v133, v73
v_cmp_lt_u64 vcc, s[62:63], v[132:133]
s_or_b64 s[66:67], s[66:67], vcc
v_sub_u32 v134, v134, v72
v_xor_b32 v135, v135, v73
v_cmp_lt_u64 vcc, s[62:63], v[134:135]
s_or_b64 s[66:67], s[66:67], vcc
v_sub_u32 v136, v136, v72
v_xor_b32 v137, v137, v73
v_cmp_lt_u64 vcc, s[62:63], v[136:137]
s_or_b64 s[66:67], s[66:67], vcc
v_sub_u32 v138, v138, v72
v_xor_b32 v139, v139, v73
v_cmp_lt_u64 vcc, s[62:63], v[138:139]
s_or_b64 s[66:67], s[66:67], vcc
v_sub_u32 v140, v140, v72
v_xor_b32 v141, v141, v73
v_cmp_lt_u64 vcc, s[62:63], v[140:141]
s_or_b64 s[66:67], s[66:67], vcc
v_sub_u32 v142, v142, v72
v_xor_b32 v143, v143, v73
v_cmp_lt_u64 vcc, s[62:63], v[142:143]
s_or_b64 s[66:67], s[66:67], vcc
v_sub_u32 v144, v144, v72
v_xor_b32 v145, v145, v73
v_cmp_lt_u64 vcc, s[62:63], v[144:145]
s_or_b64 s[66:67], s[66:67], vcc
v_sub_u32 v146, v146, v72
v_xor_b32 v147, v147, v73
v_cmp_lt_u64 vcc, s[62:63], v[146:147]
s_or_b64 s[66:67], s[66:67], vcc
v_sub_u32 v148, v148, v72
v_xor_b32 v149, v149, v73
v_cmp_lt_u64 vcc, s[62:63], v[148:149]
s_or_b64 s[66:67], s[66:67], vcc
v_sub_u32 v150, v150, v72
v_xor_b32 v151, v151, v73
v_cmp_lt_u64 vcc, s[62:63], v[150:151]
s_or_b64 s[66:67], s[66:67], vcc
v_sub_u32 v152, v152, v72
v_xor_b32 v153, v153, v73
v_cmp_lt_u64 vcc, s[62:63], v[152:153]
s_or_b64 s[66:67], s[66:67], vcc
v_sub_u32 v154, v154, v72
v_xor_b32 v155, v155, v73
v_cmp_lt_u64 vcc, s[62:63], v[154:155]
s_or_b64 s[66:67], s[66:67], vcc
s_cbranch_scc0 Et
s_cmp_eq_u32 s61, 0
s_cbranch_scc1 Et
s_sub_u32 s61, s61, 1
s_sleep 64
s_branch Dt
Et:
buffer_store_dword v73, v81, s[36:39], 0 offen sc1
s_branch Bt
Lfin:
.purgem mav
.purgem Ta
.purgem mva
.purgem mvv
.purgem Tv
.purgem R
.purgem D
.purgem D2
.purgem E
)"
    : "+{v[0:7]}"(v0), "+{v[80:95]}"(c0), "+{v[96:111]}"(c1), "+{v[112:127]}"(c2), "+{s[36:51]}"(s0), "+{s[52:59]}"(s1)
    :
    : "v8", "v9", "v10", "v11", "v12", "v13", "v14", "v15", "v16", "v17", "v18", "v19", "v20", "v21", "v22", "v23", "v24", "v25", "v26", "v27", "v28", "v29", "v30", "v31", "v32", "v33", "v34", "v35", "v36", "v37", "v38", "v39", "v40", "v41", "v42", "v43", "v44", "v45", "v46", "v47", "v48", "v49", "v50", "v51", "v52", "v53", "v54", "v55", "v56", "v57", "v58", "v59", "v60", "v61", "v62", "v63", "v64", "v65", "v66", "v67", "v68", "v69", "v70", "v71", "v72", "v73", "v74", "v75", "v76", "v77", "v78", "v79", "v128", "v129", "v130", "v131", "v132", "v133", "v134", "v135", "v136", "v137", "v138", "v139", "v140", "v141", "v142", "v143", "v144", "v145", "v146", "v147", "v148", "v149", "v150", "v151", "v152", "v153", "v154", "v155", "v156", "v157", "v158", "v159", "v160", "v161", "v162", "v163", "v164", "v165", "v166", "v167", "v168", "v169", "v170", "v171", "v172", "v173", "v174", "v175", "v176", "v177", "v178", "v179", "v180", "v181", "v182", "v183", "v184", "v185", "v186", "v187", "v188", "v189", "v190", "v191", "v192", "v193", "v194", "v195", "v196", "v197", "v198", "v199", "v200", "v201", "v202", "v203", "v204", "v205", "v206", "v207", "v208", "v209", "v210", "v211", "v212", "v213", "v214", "v215", "v216", "v217", "v218", "v219", "v220", "v221", "v222", "v223", "v224", "v225", "v226", "v227", "v228", "v229", "v230", "v231", "v232", "v233", "v234", "v235", "v236", "v237", "v238", "v239", "v240", "v241", "v242", "v243", "v244", "v245", "v246", "v247", "v248", "v249", "v250", "v251", "v252", "v253", "v254", "v255", "a0", "a1", "a2", "a3", "a4", "a5", "a6", "a7", "a8", "a9", "a10", "a11", "a12", "a13", "a14", "a15", "a16", "a17", "a18", "a19", "a20", "a21", "a22", "a23", "a24", "a25", "a26", "a27", "a28", "a29", "a30", "a31", "a32", "a33", "a34", "a35", "a36", "a37", "a38", "a39", "a40", "a41", "a42", "a43", "a44", "a45", "a46", "a47", "a48", "a49", "a50", "a51", "a52", "a53", "a54", "a55", "a56", "a57", "a58", "a59", "a60", "a61", "a62", "a63", "a64", "a65", "a66", "a67", "a68", "a69", "a70", "a71", "a72", "a73", "a74", "a75", "a76", "a77", "a78", "a79", "a80", "a81", "a82", "a83", "a84", "a85", "a86", "a87", "a88", "a89", "a90", "a91", "a92", "a93", "a94", "a95", "a96", "a97", "a98", "a99", "a100", "a101", "a102", "a103", "a104", "a105", "a106", "a107", "a108", "a109", "a110", "a111", "a112", "a113", "a114", "a115", "a116", "a117", "a118", "a119", "a120", "a121", "a122", "a123", "a124", "a125", "a126", "a127", "a128", "a129", "a130", "a131", "a132", "a133", "a134", "a135", "a136", "a137", "a138", "a139", "a140", "a141", "a142", "a143", "a144", "a145", "a146", "a147", "a148", "a149", "a150", "a151", "a152", "a153", "a154", "a155", "a156", "a157", "a158", "a159", "a160", "a161", "a162", "a163", "a164", "a165", "a166", "a167", "a168", "a169", "a170", "a171", "a172", "a173", "a174", "a175", "a176", "a177", "a178", "a179", "a180", "a181", "a182", "a183", "a184", "a185", "a186", "a187", "a188", "a189", "a190", "a191", "a192", "a193", "a194", "a195", "a196", "a197", "a198", "a199", "a200", "a201", "a202", "a203", "a204", "a205", "a206", "a207", "a208", "a209", "a210", "a211", "a212", "a213", "a214", "a215", "a216", "a217", "a218", "a219", "a220", "a221", "a222", "a223", "a224", "a225", "a226", "a227", "a228", "a229", "a230", "a231", "a232", "a233", "a234", "a235", "a236", "a237", "a238", "a239", "a240", "a241", "a242", "a243", "a244", "a245", "a246", "a247", "a248", "a249", "a250", "a251", "a252", "a253", "a254", "a255", "s60", "s61", "s62", "s63", "s64", "s65", "s66", "s67", "s68", "s69", "s70", "s71", "s72", "s73", "s74", "s75", "s76", "s77", "s78", "s79", "s80", "s81", "s82", "s83", "s84", "s85", "s86", "s87", "s88", "s89", "s90", "s91", "s92", "s93", "s94", "s95", "s96", "s97", "s98", "s99", "vcc", "scc", "memory");
}

extern "C" void kernel_launch(void* const* d_in, const int* in_sizes, int n_in, void* d_out, int out_size,
                              void* d_ws, size_t ws_size, hipStream_t stream) {
  Args m{};
  m.x = (const u16*)d_in[0]; m.wqkv = (const u16*)d_in[1]; m.wo = (const u16*)d_in[2]; m.w1 = (const u16*)d_in[4];
  m.b1 = (const u16*)d_in[5]; m.w2 = (const u16*)d_in[6]; m.b2 = (const u16*)d_in[7];
  m.out = (u16*)d_out; m.ws = (char*)d_ws;
  const unsigned tab[64] = {0x22022000u, 0x3814b010u, 0x26062404u, 0x781cf018u, 0x2a0a2808u, 0xb8943090u, 0x2e0e2c0cu, 0xf89c7098u, 0x62426040u, 0x3954b150u, 0x66466444u, 0x795cf158u, 0x6a4a6848u, 0xb9d431d0u, 0x6e4e6c4cu, 0xf9dc71d8u, 0xa282a080u, 0x3a15b211u, 0xa686a484u, 0x7a1df219u, 0xaa8aa888u, 0xba953291u, 0xae8eac8cu, 0xfa9d7299u, 0xe2c2e0c0u, 0x3b55b351u, 0xe6c6e4c4u, 0x7b5df359u, 0xeacae8c8u, 0xbbd533d1u, 0xeeceecccu, 0xfbdd73d9u, 0x23032101u, 0x7c16b412u, 0x27072505u, 0x3c1ef41au, 0x2b0b2909u, 0xfc963492u, 0x2f0f2d0du, 0xbc9e749au, 0x63436141u, 0x7d56b552u, 0x67476545u, 0x3d5ef55au, 0x6b4b6949u, 0xfdd635d2u, 0x6f4f6d4du, 0xbdde75dau, 0xa383a181u, 0x7e17b613u, 0xa787a585u, 0x3e1ff61bu, 0xab8ba989u, 0xfe973693u, 0xaf8fad8du, 0xbe9f769bu, 0xe3c3e1c1u, 0x7f57b753u, 0xe7c7e5c5u, 0x3f5ff75bu, 0xebcbe9c9u, 0xffd737d3u, 0xefcfedcdu, 0xbfdf77dbu};
  for (int i = 0; i < 64; ++i) m.tab[i] = tab[i];
  (void)hipFuncSetAttribute((const void*)df_kernel, hipFuncAttributeMaxDynamicSharedMemorySize, LDS_BYTES);
  hipLaunchKernelGGL(df_kernel, dim3(256), dim3(256), LDS_BYTES, stream, m);
}
```

```cpp
#include <hip/hip_runtime.h>
#include <stdint.h>

typedef __attribute__((ext_vector_type(16))) unsigned u32x16;
typedef __attribute__((ext_vector_type(8))) unsigned u32x8;
typedef __attribute__((ext_vector_type(4))) unsigned u32x4;
typedef __attribute__((ext_vector_type(2))) unsigned u32x2;
typedef __attribute__((ext_vector_type(2))) __bf16 bf2_t;
typedef unsigned short u16;
typedef __amdgpu_buffer_rsrc_t rsrc_t;
typedef __attribute__((address_space(3))) void lds_t;

constexpr unsigned FLAGS = 4096, RB = 8388608, REC = 313344, SX = 311296, WS_END = 88604672, DBG = 1048576;
constexpr int LDS_BYTES = 163840;
#define NEGB 0xff61b1e6u
#define FW(p, s, w) (FLAGS + (unsigned)(((p) * 64 + (s)) * 128 + (w) * 8))
#define blk0(s) (RB + (unsigned)(p * 64 + (s) - 2) * REC)
#define TA(seg, hf, sub) (unsigned)((seg) * 4096 + (8 * lg + (li >> 2)) * 128 + ((((hf) * 4 + (li & 3)) ^ ((li >> 1) & 4)) << 4) + (sub))
#define TO(seg, hf, half) (unsigned)((seg) * 4096 + (8 * lg + (li >> 2)) * 128 + ((((hf) * 4 + (half) * 2 + ((li & 3) >> 1)) ^ ((li >> 1) & 4)) << 4) + (li & 1) * 8)
#define BIAS(ptr, n, i) ((unsigned)__builtin_amdgcn_raw_buffer_load_b16(__builtin_amdgcn_make_buffer_rsrc((void*)(ptr), 0, (n) * 2, 0x00020000), (i) * 2, 0, 0) << 16)
#define XRD (unsigned)((li & 7) * 4128 + (li >> 3) * 2048 + lg * 16)

struct Args {
  const u16 *x, *wqkv, *wo, *w1, *w2;
  char* ws;
  u16* out;
  const u16 *b1, *b2;
  unsigned tab[64];
};

__device__ __forceinline__ float rows_sum(float v) {
  const auto a = __builtin_amdgcn_permlane16_swap(__float_as_uint(v), __float_as_uint(v), false, false);
  const float m = __uint_as_float(a[0]) + __uint_as_float(a[1]);
  const auto b = __builtin_amdgcn_permlane32_swap(__float_as_uint(m), __float_as_uint(m), false, false);
  return __uint_as_float(b[0]) + __uint_as_float(b[1]);
}

__device__ __forceinline__ const u16* arg_b2() {
  auto ka = (const __attribute__((address_space(4))) Args*)__builtin_amdgcn_kernarg_segment_ptr();
  asm volatile("" : "+s"(ka));
  return ka->b2;
}

__global__ __launch_bounds__(256, 1) void df_kernel(Args m) {
  const int w = __builtin_amdgcn_readfirstlane(threadIdx.x >> 6);
  const int bid = blockIdx.x, xc = bid & 7, j_ = bid >> 3;
  const int te_ = ((const __attribute__((address_space(4))) Args*)__builtin_amdgcn_kernarg_segment_ptr())->tab[(j_ & 28) * 2 + xc] >> ((j_ & 3) * 8), slot = te_ & 63, p = (te_ >> 6) & 3;
  {
    const unsigned tid = threadIdx.x;
    lds_t* const sink = (lds_t*)(size_t)(65536 + w * 1024);
    if (w & 1) {
      unsigned long long pc; unsigned o = tid < 112 ? (tid & 63) * 128 : (tid < 128 ? tid - 48 : tid & 63) * 4096 + j_ * 128;
      asm volatile("s_mov_b32 m0, %2\n\ts_getpc_b64 %0\nLbeg:\n\tv_min_u32 %1, Lfin - Lbeg + 60, %1\n\tglobal_load_lds_dword %1, %0" : "=&s"(pc), "+v"(o) : "s"(65536 + w * 1024) : "memory");
    }
    {
      const u32x4 rs = {(unsigned)(size_t)m.ws, (unsigned)((size_t)m.ws >> 32) & 0xffffu, WS_END, 0x00020000};
      asm volatile("s_nop 4\n\tbuffer_load_dword a255, off, %0, 0 sc1" :: "s"(rs) : "a255", "memory");
    }
    const int ts = slot, tp = p;
    if (ts >> 4 == 2) {
      const rsrc_t rT = __builtin_amdgcn_make_buffer_rsrc((void*)m.wqkv, 0, 1024 * 3072 * 2, 0x00020000);
      const rsrc_t rX = __builtin_amdgcn_make_buffer_rsrc((void*)m.x, 0, 2048 * 1024 * 2, 0x00020000);
      const unsigned t = ((slot & 3) * 4 + p) * 256 + tid;
#pragma unroll
      for (int i = 0; i < 3; ++i) {
        __builtin_amdgcn_raw_ptr_buffer_load_lds(rT, sink, 4, (t >> 2) * 2048 + (t & ((1 << 2) - 1)) * 128, (ts & 12) * 128 + i * (2048 * 3072 / 3), 0, 0);
        if (i < 1) __builtin_amdgcn_raw_ptr_buffer_load_lds(rX, sink, 4, (t >> 10) * 1048576 + (t & 1023) * 128, 28 * 32768, 0, 0);
      }
    }
    if (0 && ts >= 56) {
      const rsrc_t rU = __builtin_amdgcn_make_buffer_rsrc((void*)m.wo, 0, 1024 * 1024 * 2, 0x00020000);
#pragma unroll
      for (int i = 0; i < 4; ++i) __builtin_amdgcn_raw_ptr_buffer_load_lds(rU, sink, 4, ((ts - 56) * 256 + tid) * 128, ((tp * 1 * 2 + 4 + i) & 7) * 262144, 0, 0);
    }
    if (0 && (j_ & 3) == 3) {
      const rsrc_t rS = __builtin_amdgcn_make_buffer_rsrc((void*)m.ws, 0, WS_END, 0x00020000);
      const rsrc_t rO = __builtin_amdgcn_make_buffer_rsrc((void*)m.out, 0, 4 << 20, 0x00020000);
      const unsigned t = (j_ >> 2) * 256 + tid;
      if (0 & 1) __builtin_amdgcn_raw_ptr_buffer_load_lds(rS, sink, 4, t < 64 ? t << 21 : 0x80000000u, 0, 0, 16);
      if (0 & 2) __builtin_amdgcn_raw_ptr_buffer_load_lds(rS, sink, 4, t << 16, 0, 0, 16);
      if (0 & 4) __builtin_amdgcn_raw_ptr_buffer_load_lds(rS, sink, 4, (t >> 9) * (64 * REC) + (t & 511) * 4096, RB, 0, 16);
      if (0 & 3) __builtin_amdgcn_raw_ptr_buffer_load_lds(rO, sink, 4, t << (0 & 2 ? 16 : 21) & 0x7fffffffu, 0, 0, 16);
    }
    if (slot < 16) {
      const rsrc_t rW = __builtin_amdgcn_make_buffer_rsrc((void*)m.wqkv, 0, 1024 * 3072 * 2, 0x00020000);
      const rsrc_t rX = __builtin_amdgcn_make_buffer_rsrc((void*)m.x, 0, 2048 * 1024 * 2, 0x00020000);
      const unsigned ln = tid & 63, vw = (ln >> 3) * 6144 + (((ln & 7) ^ ((ln >> 2) & 4)) << 4), vx = (ln >> 4) * 2048 + (((ln & 15) ^ ((ln >> 4) * 4)) << 4);
#pragma unroll
      for (int g = 0; g < 1; ++g) {
#pragma unroll
        for (int i = 0; i < 6; ++i) {
          __builtin_amdgcn_raw_ptr_buffer_load_lds(rW, (lds_t*)(size_t)(36864 + (3 - g) * 24576 + i * 4096 + w * 1024), 16, vw, w * 8 * 6144 + slot * 128 + (2 * g + i / 3) * 32 * 6144 + i % 3 * 2048, 0, 0);
          if (!(g & 1) && (i == 1 || i == 3))
            __builtin_amdgcn_raw_ptr_buffer_load_lds(rX, (lds_t*)(size_t)(w * 8208 + 2048 + (i >> 1) * 4096 + g * 512), 16, vx, (p * 32 + 31 - (i >> 1)) * 32768 + w * 8192 + g * 128, 0, 0);
        }
      }
    }
  }
  const int si = slot >> 4 == 1 ? slot & 15 : -1;
  if (si >= 0) {
    int lane;
    asm volatile("v_mbcnt_lo_u32_b32 %0, -1, 0\n\tv_mbcnt_hi_u32_b32 %0, -1, %0" : "=v"(lane));
    const int lg = lane >> 4, li = lane & 15;
    const rsrc_t rS = __builtin_amdgcn_make_buffer_rsrc((void*)m.ws, 0, WS_END, 0x00020000);
    const rsrc_t rX = __builtin_amdgcn_make_buffer_rsrc((void*)m.x, 0, 2048 * 1024 * 2, 0x00020000);
    const bf2_t one2 = {(__bf16)1.0f, (__bf16)1.0f};
    const unsigned go = lg * 2048 + w * 512 + ((li ^ (lg * 4)) << 4), lb = 73728 + w * 16512, lo = lb + (li >> 2) * 2064 + (li & 3) * 256 + lg * 16;
#pragma unroll
    for (int i = 0; i < 16; ++i)
      __builtin_amdgcn_raw_ptr_buffer_load_lds(rX, (lds_t*)(size_t)(lb + (i >> 1) * 2064 + (i & 1) * 1024), 16, go + ((i >> 1) * 8192 + (i & 1) * 256), (p * 16 + 15 - si) * 65536, 0, 0);
    asm volatile("s_waitcnt vmcnt(0)" ::: "memory");
#pragma unroll
    for (int b = 0; b < 2; ++b) {
      float s = 0.f, q = 0.f;
#pragma unroll
      for (int k = 0; k < 8; ++k) {
        const u32x4 v = *(const __attribute__((address_space(3))) u32x4*)(size_t)(lo + b * 8256 + (k >> 2) * 1024 + (((k ^ li) & 3) << 6));
#pragma unroll
        for (int e = 0; e < 4; ++e) {
          const unsigned xe = v[e];
          const bf2_t x = __builtin_bit_cast(bf2_t, xe);
          s = __builtin_amdgcn_fdot2_f32_bf16(x, one2, s, false);
          q = __builtin_amdgcn_fdot2_f32_bf16(x, x, q, false);
        }
      }
      s = rows_sum(s); q = rows_sum(q);
      __builtin_amdgcn_raw_buffer_store_b64((u32x2){__float_as_uint(s), __float_as_uint(q)}, rS, lg ? 0x80000000u : w * 128 + li * 8 + SX, RB + (unsigned)(p * 64 + si * 2 + 1 - b) * REC, 16);
      if (0 && si == 1) {
        unsigned tg;
        asm volatile("v_accvgpr_read_b32 %0, a255" : "=v"(tg));
        tg += 0x9e3779b9u;
        __builtin_amdgcn_raw_buffer_store_b128((u32x4){__float_as_uint(s), tg, __float_as_uint(q), tg}, rS, lg ? 0x80000000u : w * 256 + li * 16, 196608 + p * 4096 + (1 - b) * 2048, 16);
      }
    }
  }
  const int lane = threadIdx.x & 63, lg = lane >> 4, li = lane & 15;
  const unsigned l16 = lane * 16;
  u32x16 c0 = 0, c1 = 0, c2 = 0, s0 = 0;
  u32x8 v0 = 0, s1 = 0;
  const u16 *wp, *op = m.out; unsigned wn, on = 4 << 20;
  const u16 *rQp = m.wqkv, *rOp = m.wo, *r1p = m.w1, *r2p = m.w2;
  constexpr unsigned rQn = 1024 * 3072 * 2, rOn = 1024 * 1024 * 2, r1n = 1024 * 3584 * 2, r2n = r1n;
  const int rot = p * 1, wr = (w + rot) & 3;
  if (slot < 16 && !(w & 1)) {
    s0[12] = 0 + (w >> 1); wp = rQp; wn = rQn;
    op = m.x - 512 * 1024; on = 5 << 20;
    v0[3] = (p * 32 + 62) * 32768 + (w * 4 + lg) * 2048 + ((li ^ (lg * 4)) << 4); v0[7] = 2048 + (li >> 2) * 8208 + (li & 3) * 256 + lg * 16;
    s0[13] = blk0(0) + 0u + (slot * 2 + (w >> 1)) * 1024 - (133120u + (w >> 1) * 2048);
    s0[15] = w * 8 * 6144 + slot * 128 + 393216;
    s1[2] = 262144u + (p * 32 + slot * 2 + (w >> 1)) * 4096 - (133120u + (w >> 1) * 2048);
    s1[3] = p ? 262144u + ((p - 1) * 32 + slot * 2 + (w >> 1)) * 4096 - (133120u + (w >> 1) * 2048) : 0x80000000u;
    s1[4] = p ? 0u : NEGB;
    v0[0] = (lane >> 3) * 6144 + (((lane & 7) ^ ((lane >> 2) & 4)) << 4) + 0u;
    v0[1] = (lane >> 3) * 6144 + (((lane & 7) ^ ((lane >> 2) & 4)) << 4) + 2048u;
    v0[2] = (lane >> 3) * 6144 + (((lane & 7) ^ ((lane >> 2) & 4)) << 4) + 4096u;
    v0[4] = TA(0, w >> 1, 0);
    v0[5] = TA(0, w >> 1, 8);
    v0[6] = TA(2, w >> 1, 0);
    c0[0] = 66048u + XRD;
    c0[1] = (66048u + XRD) ^ XRD;
    c0[2] = 133120u + (w >> 1) * 2048 + l16;
    c0[3] = 141312u + li * 8;
    c0[4] = 141312u + lg * 32;
    c0[6] = (((4 * lg + 0) ^ li) & 1) == 0 && (4 * lg + 0 <= li) ? 0u : NEGB;
    c0[7] = (((4 * lg + 1) ^ li) & 1) == 0 && (4 * lg + 1 <= li) ? 0u : NEGB;
    c0[8] = (((4 * lg + 2) ^ li) & 1) == 0 && (4 * lg + 2 <= li) ? 0u : NEGB;
    c0[9] = (((4 * lg + 3) ^ li) & 1) == 0 && (4 * lg + 3 <= li) ? 0u : NEGB;
    c0[10] = (((4 * lg + 0) ^ li) & 1) == 0 && (4 * lg + 0 > li) ? 0u : NEGB;
    c0[11] = (((4 * lg + 1) ^ li) & 1) == 0 && (4 * lg + 1 > li) ? 0u : NEGB;
    c0[12] = (((4 * lg + 2) ^ li) & 1) == 0 && (4 * lg + 2 > li) ? 0u : NEGB;
    c0[13] = (((4 * lg + 3) ^ li) & 1) == 0 && (4 * lg + 3 > li) ? 0u : NEGB;
    c0[14] = (4 * lg + 0 + (li & 1) <= li) ? ((li & 1) ? 0xffff0000u : 0xffffu) : 0u;
    c0[15] = (4 * lg + 2 + (li & 1) <= li) ? ((li & 1) ? 0xffff0000u : 0xffffu) : 0u;
    c1[0] = (4 * lg + 0 + (li & 1) > li) ? ((li & 1) ? 0xffff0000u : 0xffffu) : 0u;
    c1[1] = (4 * lg + 2 + (li & 1) > li) ? ((li & 1) ? 0xffff0000u : 0xffffu) : 0u;
    c1[2] = 133120u + (w >> 1) * 2048 + lane * 8;
  } else if (slot < 16) {
    s0[12] = 4 + (w >> 1); wp = rQp; wn = rQn;
    op = m.x - 512 * 1024; on = 5 << 20;
    v0[3] = (p * 32 + 62) * 32768 + (w * 4 + lg) * 2048 + ((li ^ (lg * 4)) << 4); v0[7] = 2048 + (li >> 2) * 8208 + (li & 3) * 256 + lg * 16;
    s0[13] = blk0(0);
    s0[14] = 66048 + (w >> 1) * 4128;
    s0[15] = w * 8 * 6144 + slot * 128 + 393216;
    s1[2] = (p * 32 + 61) * 32768;
    s1[3] = (p * 32 + 61) * 32768 + 14336;
    s1[4] = (p * 32 + 32) * 32768;
    s1[5] = (66048 + (w >> 1) * 4128) ^ ((w >> 1) * 4128);
    v0[0] = (lane >> 3) * 6144 + (((lane & 7) ^ ((lane >> 2) & 4)) << 4) + 0u;
    v0[1] = (lane >> 3) * 6144 + (((lane & 7) ^ ((lane >> 2) & 4)) << 4) + 2048u;
    v0[2] = (lane >> 3) * 6144 + (((lane & 7) ^ ((lane >> 2) & 4)) << 4) + 4096u;
    v0[4] = TA(1, w >> 1, 0);
    v0[5] = TA(1, w >> 1, 8);
    v0[6] = TA(2, w >> 1, 8);
    c0[0] = 66048u + XRD;
    c0[1] = (66048u + XRD) ^ XRD;
    c0[2] = lane ? 0x80000000u : 0u;
    c0[3] = 144384u + lane * 4;
    c0[4] = 133120u + (w >> 1) * 2048 + l16;
    c0[5] = 141312u + li * 8;
    c0[6] = 141312u + lg * 32;
    c0[7] = (0 + (w >> 1)) * 2048 + l16;
    c0[8] = (2 + (w >> 1)) * 2048 + l16;
    c0[9] = (4 + (w >> 1)) * 2048 + l16;
    c0[10] = (6 + (w >> 1)) * 2048 + l16;
    c0[11] = (lane >> 1) == 24 && p ? FW(p - 1, 48 + (slot & 7), 2) : FW(p, 60 + ((lane & 15) >> 2), lane & 3);
    c0[12] = lg == 1 ? FW(p, 16 + li, 0) : (lane >> 1) == 24 && p ? FW(p - 1, 48 + (slot & 7), 2) : FW(p, 60 + ((lane & 15) >> 2), lane & 3);
    c0[13] = (unsigned)-26;
    c0[14] = 937984u + lane * 8;
  } else if (slot >> 3 == 6) {
    s0[12] = 8 + (w); wp = rOp; wn = rOn;
    op = m.x - 512 * 1024; on = 5 << 20;
    s0[13] = blk0(-2);
    s0[14] = w * 8192;
    s0[15] = w * 8 * 2048 + (slot - 48) * 256 + rot * 524288;
    s1[2] = (p * 32 + 63) * 32768;
    s1[4] = (p * 32 + 32) * 32768;
    s1[5] = w * 8 * 2048 + (slot - 48) * 256 + 2097152u;
    v0[0] = (lane >> 3) * 2048 + (((lane & 7) ^ ((lane >> 2) & 4)) << 4) + 0u;
    v0[1] = (lane >> 3) * 2048 + (((lane & 7) ^ ((lane >> 2) & 4)) << 4) + 128u;
    v0[4] = TA(w >> 1, w & 1, 0);
    v0[5] = TA(w >> 1, w & 1, 8);
    c0[0] = l16;
    c0[1] = lane ? 0x80000000u : 0u;
    c0[2] = 144384u + lane * 4;
    c0[3] = 659456u + ((slot - 48) * 4 + w) * 1024 + l16;
    c0[4] = 940032u + wr * 8192 + l16;
    c0[5] = 944128u + wr * 8192 + l16;
    c0[6] = FW(p, (lane & 31) >> 1, (lane & 1) * 2);
    c0[7] = 1u;
    c0[8] = li * 2048 + ((slot - 48) * 4 + w) * 64 + lg * 16;
    c0[9] = 142336u + ((li * 2 + (lg >> 1)) * 8 + w * 2 + (lg & 1)) * 4;
    c0[10] = 142336u + l16;
    c0[11] = (lane & 1) ? 0x80000000u : 625664u + (slot - 48) * 128 + (lane >> 1) * 4;
  } else if (slot < 32) {
    s0[12] = 12 + (w); wp = r1p; wn = r1n;
    s0[13] = blk0(-2);
    s0[14] = w * 8192;
    s0[15] = w * 8 * 7168 + (slot - 16) * 384 + rot * 1835008;
    s1[5] = w * 8 * 7168 + (slot - 16) * 384 + 7340032u;
    v0[0] = (lane >> 3) * 7168 + (((lane & 7) ^ ((lane >> 2) & 4)) << 4) + 0u;
    v0[1] = (lane >> 3) * 7168 + (((lane & 7) ^ ((lane >> 2) & 4)) << 4) + 128u;
    v0[2] = (lane >> 3) * 7168 + (((lane & 7) ^ ((lane >> 2) & 4)) << 4) + 256u;
    v0[4] = TA(((w >> 1) * 3 + (w & 1)) >> 1, ((w >> 1) * 3 + (w & 1)) & 1, 0);
    v0[5] = TA(((w >> 1) * 3 + (w & 1)) >> 1, ((w >> 1) * 3 + (w & 1)) & 1, 8);
    v0[6] = TO(((w >> 1) * 3 + 2) >> 1, ((w >> 1) * 3 + 2) & 1, w & 1);
    c0[0] = l16;
    c0[1] = lane ? 0x80000000u : 0u;
    c0[2] = 144384u + lane * 4;
    c0[3] = 378880u + ((slot - 16) * 6 + (w >> 1) * 3 + (w & 1)) * 1024 + l16;
    c0[4] = !(lg & 1) ? 378880u + ((slot - 16) * 6 + (w >> 1) * 3 + 2) * 1024 + ((w & 1) * 2 + (lg >> 1)) * 256 + li * 16 : 0x80000000u;
    c0[5] = 972800u + wr * 8192 + l16;
    c0[6] = 976896u + wr * 8192 + l16;
    c0[7] = FW(p, 48 + ((lane & 31) >> 2), lane & 3);
    c0[8] = 1u;
    c0[9] = 141312u + li * 8;
    c0[10] = 939008u + lane * 8;
    c0[12] = BIAS(m.b1, 3584, ((slot - 16) * 6 + (w >> 1) * 3 + (w & 1)) * 32 + lg * 8 + 0);
    c0[13] = BIAS(m.b1, 3584, ((slot - 16) * 6 + (w >> 1) * 3 + (w & 1)) * 32 + lg * 8 + 1);
    c0[14] = BIAS(m.b1, 3584, ((slot - 16) * 6 + (w >> 1) * 3 + (w & 1)) * 32 + lg * 8 + 2);
    c0[15] = BIAS(m.b1, 3584, ((slot - 16) * 6 + (w >> 1) * 3 + (w & 1)) * 32 + lg * 8 + 3);
    c1[0] = BIAS(m.b1, 3584, ((slot - 16) * 6 + (w >> 1) * 3 + (w & 1)) * 32 + lg * 8 + 4);
    c1[1] = BIAS(m.b1, 3584, ((slot - 16) * 6 + (w >> 1) * 3 + (w & 1)) * 32 + lg * 8 + 5);
    c1[2] = BIAS(m.b1, 3584, ((slot - 16) * 6 + (w >> 1) * 3 + (w & 1)) * 32 + lg * 8 + 6);
    c1[3] = BIAS(m.b1, 3584, ((slot - 16) * 6 + (w >> 1) * 3 + (w & 1)) * 32 + lg * 8 + 7);
    c1[4] = BIAS(m.b1, 3584, ((slot - 16) * 6 + (w >> 1) * 3 + 2) * 32 + (w & 1) * 16 + lg * 4 + 0);
    c1[5] = BIAS(m.b1, 3584, ((slot - 16) * 6 + (w >> 1) * 3 + 2) * 32 + (w & 1) * 16 + lg * 4 + 1);
    c1[6] = BIAS(m.b1, 3584, ((slot - 16) * 6 + (w >> 1) * 3 + 2) * 32 + (w & 1) * 16 + lg * 4 + 2);
    c1[7] = BIAS(m.b1, 3584, ((slot - 16) * 6 + (w >> 1) * 3 + 2) * 32 + (w & 1) * 16 + lg * 4 + 3);
  } else if (slot >> 2 == 14) {
    s0[12] = 16 + (w); wp = r1p; wn = r1n;
    s0[13] = blk0(-2);
    s0[14] = w * 8192;
    s0[15] = w * 8 * 7168 + (slot - 32) * 256 + rot * 1835008;
    s1[5] = w * 8 * 7168 + (slot - 32) * 256 + 7340032u;
    v0[0] = (lane >> 3) * 7168 + (((lane & 7) ^ ((lane >> 2) & 4)) << 4) + 0u;
    v0[1] = (lane >> 3) * 7168 + (((lane & 7) ^ ((lane >> 2) & 4)) << 4) + 128u;
    v0[4] = TA(w >> 1, w & 1, 0);
    v0[5] = TA(w >> 1, w & 1, 8);
    c0[0] = l16;
    c0[1] = lane ? 0x80000000u : 0u;
    c0[2] = 144384u + lane * 4;
    c0[3] = 378880u + ((slot - 32) * 4 + w) * 1024 + l16;
    c0[4] = 972800u + wr * 8192 + l16;
    c0[5] = 976896u + wr * 8192 + l16;
    c0[6] = FW(p, 48 + ((lane & 31) >> 2), lane & 3);
    c0[7] = 1u;
    c0[8] = 141312u + li * 8;
    c0[9] = 939008u + lane * 8;
    c0[10] = BIAS(m.b1, 3584, ((slot - 32) * 4 + w) * 32 + lg * 8 + 0);
    c0[11] = BIAS(m.b1, 3584, ((slot - 32) * 4 + w) * 32 + lg * 8 + 1);
    c0[12] = BIAS(m.b1, 3584, ((slot - 32) * 4 + w) * 32 + lg * 8 + 2);
    c0[13] = BIAS(m.b1, 3584, ((slot - 32) * 4 + w) * 32 + lg * 8 + 3);
    c0[14] = BIAS(m.b1, 3584, ((slot - 32) * 4 + w) * 32 + lg * 8 + 4);
    c0[15] = BIAS(m.b1, 3584, ((slot - 32) * 4 + w) * 32 + lg * 8 + 5);
    c1[0] = BIAS(m.b1, 3584, ((slot - 32) * 4 + w) * 32 + lg * 8 + 6);
    c1[1] = BIAS(m.b1, 3584, ((slot - 32) * 4 + w) * 32 + lg * 8 + 7);
  } else if (slot < 48) {
    s0[12] = 20 + (w); wp = r2p; wn = r2n;
    s0[13] = blk0(-2);
    s0[14] = w * 6144;
    s0[15] = (((slot - 32) >> 2) * 768 + w * 8) * 2048 + (slot & 3) * 512 + rot * 393216;
    s1[5] = (((slot - 32) >> 2) * 768 + w * 8) * 2048 + (slot & 3) * 512 + 1572864u;
    v0[0] = (lane >> 3) * 2048 + (((lane & 7) ^ ((lane >> 2) & 4)) << 4) + 0u;
    v0[1] = (lane >> 3) * 2048 + (((lane & 7) ^ ((lane >> 2) & 4)) << 4) + 128u;
    v0[2] = (lane >> 3) * 2048 + (((lane & 7) ^ ((lane >> 2) & 4)) << 4) + 256u;
    v0[3] = (lane >> 3) * 2048 + (((lane & 7) ^ ((lane >> 2) & 4)) << 4) + 384u;
    v0[4] = TA(w, 0, 0);
    v0[5] = TA(w, 0, 8);
    v0[6] = TA(w, 1, 0);
    v0[7] = TA(w, 1, 8);
    c0[0] = l16;
    c0[1] = lane ? 0x80000000u : 0u;
    c0[2] = 144384u + lane * 4;
    c0[3] = 1005568u + ((slot - 32) >> 2) * 24576 + wr * 6144 + l16;
    c0[4] = 1009664u + ((slot - 32) >> 2) * 24576 + wr * 6144 + l16;
    c0[5] = FW(p, (lane >= 48 && slot >= 44 ? 56 : (slot - 16) & ~3) + (li >> 2), lane & 3);
    c0[6] = 1u;
    c0[7] = 493568u + (slot - 32) * 8192 + w * 2048 + l16;
  } else if (true) {
    s0[12] = 24 + (w); wp = r2p; wn = r2n;
    const u16* const b2p = arg_b2();
    s0[13] = blk0(-3);
    s0[14] = w * 4096;
    s0[15] = (3072 + w * 8) * 2048 + (slot - 60) * 512 + rot * 262144;
    s1[2] = (p * 32 + 31) * 32768 + 4194304;
    s1[5] = (3072 + w * 8) * 2048 + (slot - 60) * 512 + 1048576u;
    v0[0] = (lane >> 3) * 2048 + (((lane & 7) ^ ((lane >> 2) & 4)) << 4) + 0u;
    v0[1] = (lane >> 3) * 2048 + (((lane & 7) ^ ((lane >> 2) & 4)) << 4) + 128u;
    v0[2] = (lane >> 3) * 2048 + (((lane & 7) ^ ((lane >> 2) & 4)) << 4) + 256u;
    v0[3] = (lane >> 3) * 2048 + (((lane & 7) ^ ((lane >> 2) & 4)) << 4) + 384u;
    v0[4] = TA(w, 0, 0);
    v0[5] = TA(w, 0, 8);
    v0[6] = TA(w, 1, 0);
    v0[7] = TA(w, 1, 8);
    c0[0] = l16;
    c0[1] = lane ? 0x80000000u : 0u;
    c0[2] = 144384u + lane * 4;
    c0[3] = 1417216u + wr * 4096 + l16;
    c0[4] = FW(p, 32 + (lane & 12) + slot - 60, lane & 3);
    c0[5] = 1u;
    c0[6] = 806912u + (slot - 60) * 8192 + w * 2048 + l16;
    c0[7] = 839680u + (slot - 60) * 8192 + w * 2048 + l16;
    c0[8] = 872448u + (slot - 60) * 8192 + w * 2048 + l16;
    c0[9] = 905216u + (slot - 60) * 8192 + w * 2048 + l16;
    c0[10] = 972800u + (slot - 60) * 8192 + w * 2048 + l16;
    c0[11] = (lane >> 3) * 2048 + (lane & 7) * 16 + (slot - 60) * 512 + w * 128;
    c0[12] = ((lane >> 3) + 8) * 2048 + (lane & 7) * 16 + (slot - 60) * 512 + w * 128;
    c0[14] = 133120u + w * 2048 + li * 128 + (((0 + lg) ^ (li & 7)) << 4);
    c0[15] = 133120u + w * 2048 + li * 128 + (((4 + lg) ^ (li & 7)) << 4);
    c1[0] = 133120u + w * 2048 + (lane >> 3) * 128 + (((lane ^ (lane >> 3)) & 7) << 4);
    c1[1] = 142336u + ((li * 2 + (lg >> 1)) * 8 + w * 2 + (lg & 1)) * 4;
    c1[2] = 142336u + l16;
    c1[3] = (lane & 1) ? 0x80000000u : 10024960u + (slot - 60) * 128 + (lane >> 1) * 4;
    c1[4] = BIAS(b2p, 1024, (slot - 60) * 256 + w * 64 + 0 + lg * 8);
    c1[5] = BIAS(b2p, 1024, (slot - 60) * 256 + w * 64 + 1 + lg * 8);
    c1[6] = BIAS(b2p, 1024, (slot - 60) * 256 + w * 64 + 2 + lg * 8);
    c1[7] = BIAS(b2p, 1024, (slot - 60) * 256 + w * 64 + 3 + lg * 8);
    c1[8] = BIAS(b2p, 1024, (slot - 60) * 256 + w * 64 + 4 + lg * 8);
    c1[9] = BIAS(b2p, 1024, (slot - 60) * 256 + w * 64 + 5 + lg * 8);
    c1[10] = BIAS(b2p, 1024, (slot - 60) * 256 + w * 64 + 6 + lg * 8);
    c1[11] = BIAS(b2p, 1024, (slot - 60) * 256 + w * 64 + 7 + lg * 8);
    c1[12] = BIAS(b2p, 1024, (slot - 60) * 256 + w * 64 + 32 + lg * 8);
    c1[13] = BIAS(b2p, 1024, (slot - 60) * 256 + w * 64 + 33 + lg * 8);
    c1[14] = BIAS(b2p, 1024, (slot - 60) * 256 + w * 64 + 34 + lg * 8);
    c1[15] = BIAS(b2p, 1024, (slot - 60) * 256 + w * 64 + 35 + lg * 8);
    c2[0] = BIAS(b2p, 1024, (slot - 60) * 256 + w * 64 + 36 + lg * 8);
    c2[1] = BIAS(b2p, 1024, (slot - 60) * 256 + w * 64 + 37 + lg * 8);
    c2[2] = BIAS(b2p, 1024, (slot - 60) * 256 + w * 64 + 38 + lg * 8);
    c2[3] = BIAS(b2p, 1024, (slot - 60) * 256 + w * 64 + 39 + lg * 8);
    c2[4] = (lg == (li >> 2) && ((li & 3) >> 1) + 0 == 0) ? ((li & 1) ? 0x3f800000u : 0x3f80u) : 0u;
    c2[5] = (lg == (li >> 2) && ((li & 3) >> 1) + 0 == 1) ? ((li & 1) ? 0x3f800000u : 0x3f80u) : 0u;
    c2[6] = (lg == (li >> 2) && ((li & 3) >> 1) + 0 == 2) ? ((li & 1) ? 0x3f800000u : 0x3f80u) : 0u;
    c2[7] = (lg == (li >> 2) && ((li & 3) >> 1) + 0 == 3) ? ((li & 1) ? 0x3f800000u : 0x3f80u) : 0u;
    c2[8] = (lg == (li >> 2) && ((li & 3) >> 1) + 2 == 0) ? ((li & 1) ? 0x3f800000u : 0x3f80u) : 0u;
    c2[9] = (lg == (li >> 2) && ((li & 3) >> 1) + 2 == 1) ? ((li & 1) ? 0x3f800000u : 0x3f80u) : 0u;
    c2[10] = (lg == (li >> 2) && ((li & 3) >> 1) + 2 == 2) ? ((li & 1) ? 0x3f800000u : 0x3f80u) : 0u;
    c2[11] = (lg == (li >> 2) && ((li & 3) >> 1) + 2 == 3) ? ((li & 1) ? 0x3f800000u : 0x3f80u) : 0u;
  }
  s0[0] = (unsigned)(size_t)m.ws; s0[1] = (unsigned)((size_t)m.ws >> 32) & 0xffffu; s0[2] = WS_END; s0[3] = 0x00020000;
  s0[4] = (unsigned)(size_t)wp; s0[5] = (unsigned)((size_t)wp >> 32) & 0xffffu; s0[6] = wn; s0[7] = 0x00020000;
  s0[8] = (unsigned)(size_t)op; s0[9] = (unsigned)((size_t)op >> 32) & 0xffffu; s0[10] = on; s0[11] = 0x00020000;
  s1[0] = FW(p, slot, w); s1[1] = w * 1024; s1[7] = DBG + ((p * 64 + slot) * 4 + w) * 1024;
#pragma unroll
  for (int i = 0; i < 16; ++i) s0[i] = __builtin_amdgcn_readfirstlane(s0[i]);
#pragma unroll
  for (int i = 0; i < 8; ++i) s1[i] = __builtin_amdgcn_readfirstlane(s1[i]);
  asm volatile(
R"(
.macro mav d,a,b
v_mfma_f32_16x16x32_bf16 v[\d:\d+3], a[\a:\a+3], v[\b:\b+3], v[\d:\d+3]
.endm
.macro Ta d,a,o
ds_read_b64_tr_b16 a[\d:\d+1], v\a offset:\o
ds_read_b64_tr_b16 a[\d+2:\d+3], v\a offset:\o+512
.endm
.macro mva d,a,b
v_mfma_f32_16x16x32_bf16 v[\d:\d+3], v[\a:\a+3], a[\b:\b+3], v[\d:\d+3]
.endm
.macro mvv d,a,b
v_mfma_f32_16x16x32_bf16 v[\d:\d+3], v[\a:\a+3], v[\b:\b+3], v[\d:\d+3]
.endm
.macro Tv d,a,o
ds_read_b64_tr_b16 v[\d:\d+1], v\a offset:\o
ds_read_b64_tr_b16 v[\d+2:\d+3], v\a offset:\o+512
.endm
.macro R d,o
ds_read_b128 v[\d:\d+3], v80 offset:\o
.endm
.macro D v,o
buffer_load_dwordx4 v\v, s[36:39], s49 offen offset:\o sc1 lds
.endm
.macro D2 v,s,o
buffer_load_dwordx4 v\v, s[44:47], s\s offen offset:\o sc1 lds
.endm
.macro E v,o
s_add_u32 m0, s53, \o
s_nop 0
buffer_load_dwordx4 v\v, s[40:43], s51 offen lds
.endm
s_branch La
.p2align 12
La:
s_nop 4
s_mov_b32 s61, 200000
s_mov_b64 s[62:63], 255
s_mov_b32 s64, 0x3f803f80
s_mov_b32 s65, 0x3e8293ee
s_lshr_b32 s60, s48, 2
Lr0:
s_cmp_eq_u32 s60, 0
s_cbranch_scc0 Lr1
s_lshr_b32 s66, s53, 10
s_mul_i32 s66, s66, 8208
s_mov_b32 s67, 0x8000
s_mov_b64 exec, 0
s_cmp_eq_u32 s53, 0
s_cbranch_scc1 LsQ0
s_branch LsQ2
LwQA:
s_mov_b64 exec, -1
E 0,86016
E 1,90112
E 2,94208
s_add_u32 s51, s51, 196608
E 0,98304
E 1,102400
E 2,106496
s_add_u32 s51, s51, 196608
E 0,61440
E 1,65536
s_add_u32 m0, s66, 2816
s_nop 0
buffer_load_dwordx4 v3, s[44:47], s67 offen offset:256 lds
E 2,69632
s_add_u32 s51, s51, 196608
E 0,73728
s_add_u32 m0, s66, 6912
s_nop 0
buffer_load_dwordx4 v3, s[44:47], 0 offen offset:256 lds
E 1,77824
E 2,81920
s_add_u32 s51, s51, 196608
v_lshrrev_b32 v12, 2, v7
v_and_b32 v12, 0xc0, v12
v_xor_b32 v13, 0, v12
v_add_u32 v8, v7, v13
v_xor_b32 v13, 64, v12
v_add_u32 v9, v7, v13
v_xor_b32 v13, 128, v12
v_add_u32 v10, v7, v13
v_xor_b32 v13, 192, v12
v_add_u32 v11, v7, v13
v_mov_b32 v114, s64
v_mov_b32 v115, s64
v_mov_b32 v116, s64
v_mov_b32 v117, s64
s_mov_b32 s92, 0xaaaaaaaa
s_mov_b32 s93, s92
s_mov_b32 s94, 0xcccccccc
s_mov_b32 s95, s94
s_mov_b32 s96, 0xf0000f
s_mov_b32 s97, 0xf0000f00
s_waitcnt vmcnt(14)
s_barrier
v_add_u32 v4, 69632, v4
v_add_u32 v5, 69632, v5
v_add_u32 v6, 69632, v6
Ta 0,4,40960
Ta 4,5,40960
Ta 8,6,40960
Ta 12,4,53248
Ta 16,5,53248
Ta 20,6,53248
ds_read_b128 v[38:41], v8 offset:0
ds_read_b128 v[42:45], v9 offset:0
ds_read_b128 v[46:49], v8 offset:4096
ds_read_b128 v[50:53], v9 offset:4096
v_accvgpr_read_b32 v79, a255
v_mov_b32 v78, 0x100
v_add_u32 v79, 0x9e3779b9, v79
buffer_store_dwordx2 v[78:79], off, s[36:39], s52 sc1
E 0,36864
E 1,40960
E 2,45056
s_add_u32 s51, s51, 196608
E 0,49152
E 1,53248
E 2,57344
s_add_u32 s51, s51, 196608
s_waitcnt lgkmcnt(0)
s_waitcnt vmcnt(15)
s_barrier
E 0,110592
Ta 24,4,16384
v_mfma_f32_16x16x32_bf16 v[74:77], v[114:117], v[38:41], 0
v_mfma_f32_16x16x32_bf16 v[118:121], v[38:41], v[38:41], 0
Ta 28,5,16384
mvv 74,114,42
Ta 32,6,16384
E 1,114688
mvv 118,42,42
Ta 36,4,28672
v_mfma_f32_16x16x32_bf16 v[62:65], a[0:3], v[114:117], 0
v_mfma_f32_16x16x32_bf16 v[66:69], a[4:7], v[114:117], 0
Ta 40,5,28672
s_add_u32 m0, s66, 3584
s_nop 0
buffer_load_dwordx4 v3, s[44:47], s67 offen offset:512 lds
v_mfma_f32_16x16x32_bf16 v[70:73], v[114:117], a[8:11], 0
Ta 44,6,28672
v_mfma_f32_16x16x32_bf16 v[14:17], a[0:3], v[38:41], 0
ds_read_b128 v[54:57], v10 offset:0
v_mfma_f32_16x16x32_bf16 v[18:21], a[4:7], v[38:41], 0
E 2,118784
s_add_u32 s51, s51, 196608
v_mfma_f32_16x16x32_bf16 v[22:25], v[38:41], a[8:11], 0
ds_read_b128 v[58:61], v11 offset:0
v_mfma_f32_16x16x32_bf16 v[26:29], a[0:3], v[46:49], 0
ds_read_b128 v[100:103], v10 offset:4096
E 0,122880
v_mfma_f32_16x16x32_bf16 v[30:33], a[4:7], v[46:49], 0
ds_read_b128 v[104:107], v11 offset:4096
v_mfma_f32_16x16x32_bf16 v[34:37], v[46:49], a[8:11], 0
mav 62,12,114
s_add_u32 m0, s66, 7680
s_nop 0
buffer_load_dwordx4 v3, s[44:47], 0 offen offset:512 lds
mav 66,16,114
mva 70,114,20
mav 14,12,42
E 1,126976
mav 18,16,42
mva 22,42,20
mav 26,12,50
E 2,131072
s_add_u32 s51, s51, 196608
mav 30,16,50
mva 34,50,20
s_waitcnt lgkmcnt(0)
s_waitcnt vmcnt(15)
s_barrier
v_add_u32 v4, -49152, v4
v_add_u32 v5, -49152, v5
v_add_u32 v6, -49152, v6
E 0,86016
Ta 48,4,40960
mvv 74,114,54
mvv 118,54,54
Ta 52,5,40960
mvv 74,114,58
Ta 56,6,40960
mvv 118,58,58
E 1,90112
Ta 60,4,53248
mav 62,24,114
mav 66,28,114
Ta 64,5,53248
mva 70,114,32
Ta 68,6,53248
mav 14,24,54
E 2,94208
s_add_u32 s51, s51, 196608
ds_read_b128 v[38:41], v8 offset:1024
mav 18,28,54
mva 22,54,32
ds_read_b128 v[42:45], v9 offset:1024
mav 26,24,100
ds_read_b128 v[46:49], v8 offset:5120
E 0,98304
mav 30,28,100
ds_read_b128 v[50:53], v9 offset:5120
mva 34,100,32
mav 62,36,114
mav 66,40,114
E 1,102400
mva 70,114,44
mav 14,36,58
mav 18,40,58
mva 22,58,44
E 2,106496
s_add_u32 s51, s51, 196608
mav 26,36,104
mav 30,40,104
mva 34,104,44
s_waitcnt lgkmcnt(0)
s_waitcnt vmcnt(14)
s_barrier
E 0,61440
Ta 72,4,16384
mvv 74,114,38
mvv 118,38,38
Ta 76,5,16384
mvv 74,114,42
Ta 80,6,16384
E 1,65536
mvv 118,42,42
Ta 84,4,28672
mav 62,48,114
mav 66,52,114
Ta 88,5,28672
s_add_u32 m0, s66, 4352
s_nop 0
buffer_load_dwordx4 v3, s[44:47], s67 offen offset:768 lds
mva 70,114,56
Ta 92,6,28672
mav 14,48,38
ds_read_b128 v[54:57], v10 offset:1024
mav 18,52,38
E 2,69632
s_add_u32 s51, s51, 196608
mva 22,38,56
ds_read_b128 v[58:61], v11 offset:1024
mav 26,48,46
ds_read_b128 v[100:103], v10 offset:5120
E 0,73728
mav 30,52,46
ds_read_b128 v[104:107], v11 offset:5120
mva 34,46,56
mav 62,60,114
s_add_u32 m0, s66, 8448
s_nop 0
buffer_load_dwordx4 v3, s[44:47], 0 offen offset:768 lds
mav 66,64,114
mva 70,114,68
mav 14,60,42
E 1,77824
mav 18,64,42
mva 22,42,68
mav 26,60,50
E 2,81920
s_add_u32 s51, s51, 196608
mav 30,64,50
mva 34,50,68
s_waitcnt lgkmcnt(0)
s_waitcnt vmcnt(14)
s_barrier
v_add_u32 v4, 49152, v4
v_add_u32 v5, 49152, v5
v_add_u32 v6, 49152, v6
E 0,36864
Ta 96,4,40960
mvv 74,114,54
mvv 118,54,54
Ta 100,5,40960
mvv 74,114,58
Ta 104,6,40960
mvv 118,58,58
E 1,40960
Ta 108,4,53248
mav 62,72,114
mav 66,76,114
Ta 112,5,53248
mva 70,114,80
Ta 116,6,53248
mav 14,72,54
E 2,45056
s_add_u32 s51, s51, 196608
ds_read_b128 v[38:41], v8 offset:2048
mav 18,76,54
mva 22,54,80
ds_read_b128 v[42:45], v9 offset:2048
mav 26,72,100
ds_read_b128 v[46:49], v8 offset:6144
E 0,49152
mav 30,76,100
ds_read_b128 v[50:53], v9 offset:6144
mva 34,100,80
mav 62,84,114
mav 66,88,114
E 1,53248
mva 70,114,92
mav 14,84,58
mav 18,88,58
mva 22,58,92
E 2,57344
s_add_u32 s51, s51, 196608
mav 26,84,104
mav 30,88,104
mva 34,104,92
s_waitcnt lgkmcnt(0)
s_waitcnt vmcnt(14)
s_barrier
E 0,110592
Ta 120,4,16384
mvv 74,114,38
mvv 118,38,38
Ta 124,5,16384
mvv 74,114,42
Ta 128,6,16384
E 1,114688
mvv 118,42,42
Ta 132,4,28672
mav 62,96,114
mav 66,100,114
Ta 136,5,28672
s_add_u32 m0, s66, 1024
s_nop 0
buffer_load_dwordx4 v3, s[44:47], s67 offen offset:1024 lds
mva 70,114,104
Ta 140,6,28672
mav 14,96,38
ds_read_b128 v[54:57], v10 offset:2048
mav 18,100,38
E 2,118784
s_add_u32 s51, s51, 196608
mva 22,38,104
ds_read_b128 v[58:61], v11 offset:2048
mav 26,96,46
ds_read_b128 v[100:103], v10 offset:6144
E 0,122880
mav 30,100,46
ds_read_b128 v[104:107], v11 offset:6144
mva 34,46,104
mav 62,108,114
s_add_u32 m0, s66, 5120
s_nop 0
buffer_load_dwordx4 v3, s[44:47], 0 offen offset:1024 lds
mav 66,112,114
mva 70,114,116
mav 14,108,42
E 1,126976
mav 18,112,42
mva 22,42,116
mav 26,108,50
E 2,131072
s_add_u32 s51, s51, 196608
mav 30,112,50
mva 34,50,116
s_waitcnt lgkmcnt(0)
s_waitcnt vmcnt(14)
s_barrier
v_add_u32 v4, -49152, v4
v_add_u32 v5, -49152, v5
v_add_u32 v6, -49152, v6
E 0,86016
Ta 144,4,40960
mvv 74,114,54
mvv 118,54,54
Ta 148,5,40960
mvv 74,114,58
Ta 152,6,40960
mvv 118,58,58
E 1,90112
Ta 156,4,53248
mav 62,120,114
mav 66,124,114
Ta 160,5,53248
mva 70,114,128
Ta 164,6,53248
mav 14,120,54
E 2,94208
s_add_u32 s51, s51, 196608
ds_read_b128 v[38:41], v8 offset:3072
mav 18,124,54
mva 22,54,128
ds_read_b128 v[42:45], v9 offset:3072
mav 26,120,100
ds_read_b128 v[46:49], v8 offset:7168
E 0,98304
mav 30,124,100
ds_read_b128 v[50:53], v9 offset:7168
mva 34,100,128
mav 62,132,114
mav 66,136,114
E 1,102400
mva 70,114,140
mav 14,132,58
mav 18,136,58
mva 22,58,140
E 2,106496
s_add_u32 s51, s51, 196608
mav 26,132,104
mav 30,136,104
mva 34,104,140
s_waitcnt lgkmcnt(0)
s_waitcnt vmcnt(14)
s_barrier
E 0,61440
Ta 168,4,16384
mvv 74,114,38
mvv 118,38,38
Ta 172,5,16384
mvv 74,114,42
Ta 176,6,16384
E 1,65536
mvv 118,42,42
Ta 180,4,28672
mav 62,144,114
mav 66,148,114
Ta 184,5,28672
s_add_u32 m0, s66, 1792
s_nop 0
buffer_load_dwordx4 v3, s[44:47], s67 offen offset:1280 lds
mva 70,114,152
Ta 188,6,28672
mav 14,144,38
ds_read_b128 v[54:57], v10 offset:3072
mav 18,148,38
E 2,69632
s_add_u32 s51, s51, 196608
mva 22,38,152
ds_read_b128 v[58:61], v11 offset:3072
mav 26,144,46
ds_read_b128 v[100:103], v10 offset:7168
E 0,73728
mav 30,148,46
ds_read_b128 v[104:107], v11 offset:7168
mva 34,46,152
mav 62,156,114
LsQ0:
s_cbranch_execz LsQ4
s_add_u32 m0, s66, 5888
s_nop 0
buffer_load_dwordx4 v3, s[44:47], 0 offen offset:1280 lds
mav 66,160,114
mva 70,114,164
mav 14,156,42
E 1,77824
mav 18,160,42
mva 22,42,164
mav 26,156,50
E 2,81920
s_add_u32 s51, s51, 196608
mav 30,160,50
mva 34,50,164
s_waitcnt lgkmcnt(0)
s_waitcnt vmcnt(14)
s_barrier
v_add_u32 v4, 49152, v4
v_add_u32 v5, 49152, v5
v_add_u32 v6, 49152, v6
E 0,36864
Ta 192,4,40960
mvv 74,114,54
mvv 118,54,54
Ta 196,5,40960
mvv 74,114,58
Ta 200,6,40960
mvv 118,58,58
E 1,40960
Ta 204,4,53248
mav 62,168,114
mav 66,172,114
Ta 208,5,53248
mva 70,114,176
Ta 212,6,53248
mav 14,168,54
E 2,45056
s_add_u32 s51, s51, 196608
ds_read_b128 v[38:41], v8 offset:0
mav 18,172,54
mva 22,54,176
ds_read_b128 v[42:45], v9 offset:0
mav 26,168,100
ds_read_b128 v[46:49], v8 offset:4096
E 0,49152
mav 30,172,100
ds_read_b128 v[50:53], v9 offset:4096
mva 34,100,176
mav 62,180,114
mav 66,184,114
E 1,53248
mva 70,114,188
mav 14,180,58
mav 18,184,58
mva 22,58,188
E 2,57344
s_add_u32 s51, s51, 196608
mav 26,180,104
mav 30,184,104
mva 34,104,188
s_waitcnt lgkmcnt(0)
s_waitcnt vmcnt(14)
s_barrier
E 0,110592
Ta 216,4,16384
mvv 74,114,38
mvv 118,38,38
Ta 220,5,16384
mvv 74,114,42
Ta 224,6,16384
E 1,114688
mvv 118,42,42
Ta 228,4,28672
mav 62,192,114
mav 66,196,114
Ta 232,5,28672
s_add_u32 m0, s66, 2560
s_nop 0
buffer_load_dwordx4 v3, s[44:47], s67 offen offset:1536 lds
mva 70,114,200
Ta 236,6,28672
mav 14,192,38
ds_read_b128 v[54:57], v10 offset:0
mav 18,196,38
E 2,118784
s_add_u32 s51, s51, 196608
mva 22,38,200
ds_read_b128 v[58:61], v11 offset:0
mav 26,192,46
ds_read_b128 v[100:103], v10 offset:4096
E 0,122880
mav 30,196,46
ds_read_b128 v[104:107], v11 offset:4096
mva 34,46,200
mav 62,204,114
s_add_u32 m0, s66, 6656
s_nop 0
buffer_load_dwordx4 v3, s[44:47], 0 offen offset:1536 lds
mav 66,208,114
mva 70,114,212
mav 14,204,42
E 1,126976
mav 18,208,42
mva 22,42,212
mav 26,204,50
E 2,131072
s_add_u32 s51, s51, 196608
mav 30,208,50
mva 34,50,212
s_waitcnt lgkmcnt(0)
s_waitcnt vmcnt(14)
s_barrier
v_add_u32 v4, -49152, v4
v_add_u32 v5, -49152, v5
v_add_u32 v6, -49152, v6
E 0,86016
Ta 240,4,40960
mvv 74,114,54
mvv 118,54,54
Ta 244,5,40960
mvv 74,114,58
Ta 248,6,40960
mvv 118,58,58
E 1,90112
Ta 252,4,53248
mav 62,216,114
mav 66,220,114
Tv 128,5,53248
mva 70,114,224
Tv 132,6,53248
mav 14,216,54
E 2,94208
s_add_u32 s51, s51, 196608
ds_read_b128 v[38:41], v8 offset:1024
mav 18,220,54
mva 22,54,224
ds_read_b128 v[42:45], v9 offset:1024
mav 26,216,100
ds_read_b128 v[46:49], v8 offset:5120
E 0,98304
mav 30,220,100
ds_read_b128 v[50:53], v9 offset:5120
mva 34,100,224
mav 62,228,114
mav 66,232,114
E 1,102400
mva 70,114,236
mav 14,228,58
mav 18,232,58
mva 22,58,236
E 2,106496
s_add_u32 s51, s51, 196608
mav 26,228,104
mav 30,232,104
mva 34,104,236
s_waitcnt lgkmcnt(0)
s_waitcnt vmcnt(14)
s_barrier
E 0,61440
Tv 136,4,16384
mvv 74,114,38
mvv 118,38,38
Tv 140,5,16384
mvv 74,114,42
Tv 144,6,16384
E 1,65536
mvv 118,42,42
Tv 148,4,28672
mav 62,240,114
mav 66,244,114
Tv 152,5,28672
s_add_u32 m0, s66, 3328
s_nop 0
buffer_load_dwordx4 v3, s[44:47], s67 offen offset:1792 lds
mva 70,114,248
Tv 156,6,28672
mav 14,240,38
ds_read_b128 v[54:57], v10 offset:1024
mav 18,244,38
E 2,69632
s_add_u32 s51, s51, 196608
mva 22,38,248
ds_read_b128 v[58:61], v11 offset:1024
mav 26,240,46
ds_read_b128 v[100:103], v10 offset:5120
E 0,73728
mav 30,244,46
ds_read_b128 v[104:107], v11 offset:5120
mva 34,46,248
mav 62,252,114
s_add_u32 m0, s66, 7424
s_nop 0
buffer_load_dwordx4 v3, s[44:47], 0 offen offset:1792 lds
mvv 66,128,114
mvv 70,114,132
mav 14,252,42
E 1,77824
mvv 18,128,42
mvv 22,42,132
mav 26,252,50
E 2,81920
s_add_u32 s51, s51, 196608
mvv 30,128,50
mvv 34,50,132
s_waitcnt lgkmcnt(0)
s_waitcnt vmcnt(14)
s_barrier
v_add_u32 v4, 49152, v4
v_add_u32 v5, 49152, v5
v_add_u32 v6, 49152, v6
E 0,36864
Tv 160,4,40960
mvv 74,114,54
mvv 118,54,54
Tv 164,5,40960
mvv 74,114,58
Tv 168,6,40960
mvv 118,58,58
E 1,40960
Tv 172,4,53248
mvv 62,136,114
mvv 66,140,114
Tv 176,5,53248
mvv 70,114,144
Tv 180,6,53248
mvv 14,136,54
E 2,45056
s_add_u32 s51, s51, 196608
ds_read_b128 v[38:41], v8 offset:2048
mvv 18,140,54
mvv 22,54,144
ds_read_b128 v[42:45], v9 offset:2048
mvv 26,136,100
ds_read_b128 v[46:49], v8 offset:6144
E 0,49152
mvv 30,140,100
ds_read_b128 v[50:53], v9 offset:6144
mvv 34,100,144
mvv 62,148,114
mvv 66,152,114
E 1,53248
mvv 70,114,156
mvv 14,148,58
mvv 18,152,58
mvv 22,58,156
E 2,57344
s_add_u32 s51, s51, 196608
mvv 26,148,104
mvv 30,152,104
mvv 34,104,156
s_waitcnt lgkmcnt(0)
s_waitcnt vmcnt(14)
s_barrier
Tv 184,4,16384
mvv 74,114,38
mvv 118,38,38
Tv 188,5,16384
mvv 74,114,42
Tv 192,6,16384
mvv 118,42,42
Tv 196,4,28672
mvv 62,160,114
mvv 66,164,114
Tv 200,5,28672
mvv 70,114,168
Tv 204,6,28672
mvv 14,160,38
ds_read_b128 v[54:57], v10 offset:2048
mvv 18,164,38
mvv 22,38,168
ds_read_b128 v[58:61], v11 offset:2048
mvv 26,160,46
ds_read_b128 v[100:103], v10 offset:6144
mvv 30,164,46
ds_read_b128 v[104:107], v11 offset:6144
mvv 34,46,168
mvv 62,172,114
mvv 66,176,114
mvv 70,114,180
mvv 14,172,42
mvv 18,176,42
mvv 22,42,180
mvv 26,172,50
mvv 30,176,50
mvv 34,50,180
s_waitcnt lgkmcnt(0)
s_waitcnt vmcnt(6)
s_barrier
v_add_u32 v4, -49152, v4
v_add_u32 v5, -49152, v5
v_add_u32 v6, -49152, v6
Tv 208,4,40960
mvv 74,114,54
mvv 118,54,54
Tv 212,5,40960
mvv 74,114,58
Tv 216,6,40960
mvv 118,58,58
Tv 220,4,53248
mvv 62,184,114
mvv 66,188,114
Tv 224,5,53248
mvv 70,114,192
Tv 228,6,53248
mvv 14,184,54
ds_read_b128 v[38:41], v8 offset:3072
mvv 18,188,54
mvv 22,54,192
ds_read_b128 v[42:45], v9 offset:3072
mvv 26,184,100
ds_read_b128 v[46:49], v8 offset:7168
mvv 30,188,100
ds_read_b128 v[50:53], v9 offset:7168
mvv 34,100,192
mvv 62,196,114
mvv 66,200,114
mvv 70,114,204
mvv 14,196,58
mvv 18,200,58
mvv 22,58,204
mvv 26,196,104
mvv 30,200,104
mvv 34,104,204
s_waitcnt lgkmcnt(0)
s_waitcnt vmcnt(0)
s_barrier
Tv 232,4,16384
mvv 74,114,38
mvv 118,38,38
Tv 236,5,16384
mvv 74,114,42
Tv 240,6,16384
mvv 118,42,42
Tv 244,4,28672
mvv 62,208,114
mvv 66,212,114
Tv 248,5,28672
mvv 70,114,216
Tv 252,6,28672
mvv 14,208,38
ds_read_b128 v[54:57], v10 offset:3072
mvv 18,212,38
mvv 22,38,216
ds_read_b128 v[58:61], v11 offset:3072
mvv 26,208,46
ds_read_b128 v[100:103], v10 offset:7168
mvv 30,212,46
ds_read_b128 v[104:107], v11 offset:7168
mvv 34,46,216
mvv 62,220,114
mvv 66,224,114
mvv 70,114,228
mvv 14,220,42
mvv 18,224,42
mvv 22,42,228
mvv 26,220,50
mvv 30,224,50
mvv 34,50,228
s_waitcnt lgkmcnt(0)
mvv 74,114,54
mvv 118,54,54
mvv 74,114,58
mvv 118,58,58
mvv 62,232,114
mvv 66,236,114
mvv 70,114,240
mvv 14,232,54
mvv 18,236,54
mvv 22,54,240
mvv 26,232,100
mvv 30,236,100
mvv 34,100,240
v_cndmask_b32 v118, v118, v119, s[92:93]
mvv 62,244,114
v_cndmask_b32 v120, v120, v121, s[92:93]
mvv 66,248,114
v_mul_f32 v74, 0x3a800000, v74
v_cndmask_b32 v118, v118, v120, s[94:95]
mvv 70,114,252
v_mul_f32 v118, 0x3a800000, v118
mvv 14,244,58
v_fma_f32 v118, -v74, v74, v118
mvv 18,248,58
v_max_f32 v118, 0, v118
mvv 22,58,252
v_add_f32 v118, 0x3727c5ac, v118
mvv 26,244,104
v_rsq_f32 v118, v118
mvv 30,248,104
s_nop 0
v_mul_f32_e64 v119, -v74, v118
mvv 34,104,252
s_mov_b64 exec, s[96:97]
ds_write_b64 v83, v[118:119] offset:0
s_mov_b64 exec, -1
s_waitcnt lgkmcnt(0)
s_barrier
s_nop 7
v_mov_b32 v99, v70
s_and_b32 s60, s48, 3
s_cmp_eq_u32 s60, 1
s_cbranch_scc1 LvQA1
LvQA0:
LsQ1:
s_cbranch_execz LsQ5
ds_read_b64 v[122:123], v83 offset:0
ds_read_b128 v[70:73], v84 offset:0
ds_read_b128 v[74:77], v84 offset:16
s_add_u32 s49, s49, 313344
s_waitcnt lgkmcnt(0)
v_mul_f32 v71, v71, v99
v_fma_f32 v22, v22, v70, v71
v_mul_f32 v73, v73, v99
v_fma_f32 v23, v23, v72, v73
v_mul_f32 v75, v75, v99
v_fma_f32 v24, v24, v74, v75
v_mul_f32 v77, v77, v99
v_fma_f32 v25, v25, v76, v77
v_cvt_pk_bf16_f32 v8, v22, v23
v_cvt_pk_bf16_f32 v9, v24, v25
v_mul_f32 v124, v123, v62
v_fma_f32 v14, v14, v122, v124
v_mul_f32 v125, v123, v63
v_fma_f32 v15, v15, v122, v125
v_mul_f32 v124, v123, v64
v_fma_f32 v16, v16, v122, v124
v_mul_f32 v125, v123, v65
v_fma_f32 v17, v17, v122, v125
v_mul_f32 v124, v123, v66
v_fma_f32 v18, v18, v122, v124
v_mul_f32 v125, v123, v67
v_fma_f32 v19, v19, v122, v125
v_mul_f32 v124, v123, v68
v_fma_f32 v20, v20, v122, v124
v_mul_f32 v125, v123, v69
v_fma_f32 v21, v21, v122, v125
v_cvt_pk_bf16_f32 v0, v14, v15
v_cvt_pk_bf16_f32 v1, v16, v17
v_cvt_pk_bf16_f32 v2, v18, v19
v_cvt_pk_bf16_f32 v3, v20, v21
s_add_u32 s49, s49, 313344
s_barrier
ds_read_b128 v[100:103], v82 offset:0
ds_read_b64 v[108:109], v82 offset:1024
R 38,33024
R 42,33088
LsQ2:
s_cbranch_execz LsQ6
R 46,33152
R 50,33216
ds_read_b64 v[122:123], v83 offset:128
ds_read_b128 v[70:73], v84 offset:128
ds_read_b128 v[74:77], v84 offset:144
s_waitcnt lgkmcnt(0)
v_mfma_f32_16x16x32_bf16 v[14:17], a[0:3], v[38:41], 0
R 54,33280
v_mul_f32 v71, v71, v99
v_mfma_f32_16x16x32_bf16 v[18:21], a[4:7], v[38:41], 0
v_fma_f32 v34, v34, v70, v71
v_mul_f32 v73, v73, v99
v_mfma_f32_16x16x32_bf16 v[22:25], v[38:41], a[8:11], 0
v_fma_f32 v35, v35, v72, v73
v_mul_f32 v75, v75, v99
mav 14,12,42
R 58,33344
v_fma_f32 v36, v36, v74, v75
mav 18,16,42
v_mul_f32 v77, v77, v99
v_fma_f32 v37, v37, v76, v77
mva 22,42,20
v_cvt_pk_bf16_f32 v10, v34, v35
v_cvt_pk_bf16_f32 v11, v36, v37
mav 14,24,46
R 38,33408
v_mul_f32 v124, v123, v62
v_mfma_f32_16x16x32_bf16 v[70:73], v[100:103], v[0:3], v[86:89]
v_fma_f32 v26, v26, v122, v124
v_mul_f32 v125, v123, v63
mav 18,28,46
v_fma_f32 v27, v27, v122, v125
v_mul_f32 v124, v123, v64
mva 22,46,32
v_fma_f32 v28, v28, v122, v124
v_mul_f32 v125, v123, v65
mav 14,36,50
R 42,33472
v_fma_f32 v29, v29, v122, v125
mav 18,40,50
v_mul_f32 v124, v123, v66
v_fma_f32 v30, v30, v122, v124
mva 22,50,44
s_waitcnt lgkmcnt(2)
v_mul_f32 v125, v123, v67
v_fma_f32 v31, v31, v122, v125
mav 14,48,54
R 46,33536
v_mul_f32 v124, v123, v68
mav 18,52,54
v_fma_f32 v32, v32, v122, v124
v_mul_f32 v125, v123, v69
mva 22,54,56
v_fma_f32 v33, v33, v122, v125
mav 14,60,58
R 50,33600
mav 18,64,58
mva 22,58,68
s_waitcnt lgkmcnt(2)
mav 14,72,38
R 54,33664
mav 18,76,38
mva 22,38,80
buffer_store_dwordx4 v[100:103], v82, s[36:39], s54 offen sc1
mav 14,84,42
R 58,33728
mav 18,88,42
buffer_store_dwordx2 v[8:9], v98, s[36:39], s54 offen offset:1024 sc1
mva 22,42,92
s_waitcnt lgkmcnt(2)
mav 14,96,46
buffer_store_dwordx2 v[108:109], v98, s[36:39], s54 offen offset:1536 sc1
R 38,33792
mav 18,100,46
s_barrier
mva 22,46,104
ds_read_b128 v[104:107], v82 offset:4096
mav 14,108,50
ds_read_b64 v[110:111], v82 offset:5120
mav 18,112,50
R 42,33856
mva 22,50,116
s_waitcnt lgkmcnt(4)
mav 14,120,54
R 46,33920
mav 18,124,54
mva 22,54,128
mav 14,132,58
s_waitcnt lgkmcnt(2)
R 50,33984
v_mfma_f32_16x16x32_bf16 v[74:77], v[104:107], v[0:3], v[90:93]
v_cvt_pk_bf16_f32 v0, v26, v27
v_cvt_pk_bf16_f32 v1, v28, v29
mav 18,136,58
v_cvt_pk_bf16_f32 v2, v30, v31
v_cvt_pk_bf16_f32 v3, v32, v33
mva 22,58,140
mav 14,144,38
R 54,34048
v_max3_f32 v118, v70, v71, v74
mav 18,148,38
v_max3_f32 v119, v72, v73, v76
v_max_f32 v118, v118, v75
mva 22,38,152
s_waitcnt lgkmcnt(2)
v_max_f32 v119, v119, v77
v_max_f32 v120, v118, v119
mav 14,156,42
R 38,34112
v_max_f32 v121, v118, v119
mav 18,160,42
mva 22,42,164
v_permlane32_swap_b32 v120, v121
mav 14,168,46
R 42,34176
v_max_f32 v114, v120, v121
mav 18,172,46
v_max_f32 v115, v120, v121
mva 22,46,176
s_waitcnt lgkmcnt(2)
v_permlane16_swap_b32 v114, v115
mav 14,180,50
R 46,34240
v_max_f32 v120, v114, v115
mav 18,184,50
v_mul_f32 v120, s65, v120
v_fma_f32 v118, v118, s65, -v120
mva 22,50,188
v_fma_f32 v119, v119, s65, -v120
mav 14,192,54
R 50,34304
mav 18,196,54
v_exp_f32 v118, v118
mva 22,54,200
s_waitcnt lgkmcnt(2)
v_exp_f32 v119, v119
mav 14,204,38
R 54,34368
v_add_f32 v120, v118, v119
mav 18,208,38
v_add_f32 v121, v118, v119
mva 22,38,212
mav 14,216,42
R 38,34432
mav 18,220,42
v_permlane32_swap_b32 v120, v121
mva 22,42,224
s_waitcnt lgkmcnt(2)
v_add_f32 v114, v120, v121
v_add_f32 v115, v120, v121
mav 14,228,46
R 42,34496
mav 18,232,46
v_permlane16_swap_b32 v114, v115
mva 22,46,236
v_add_f32 v120, v114, v115
mav 14,240,50
R 46,34560
mav 18,244,50
v_rcp_f32 v120, v120
mva 22,50,248
s_waitcnt lgkmcnt(2)
v_mul_f32 v118, v118, v120
v_mul_f32 v119, v119, v120
mav 14,252,54
R 50,34624
v_cvt_pk_bf16_f32 v118, v118, v118
mvv 18,128,54
v_cvt_pk_bf16_f32 v119, v119, v119
v_and_b32 v114, v118, v94
mvv 22,54,132
v_and_b32 v115, v119, v95
v_and_b32 v116, v118, v96
mvv 14,136,38
R 54,34688
v_and_b32 v117, v119, v97
mvv 18,140,38
mvv 22,38,144
s_waitcnt lgkmcnt(2)
v_mfma_f32_16x16x32_bf16 v[70:73], v[8:11], v[114:117], 0
v_mfma_f32_16x16x32_bf16 v[74:77], v[108:111], v[114:117], 0
mvv 14,148,42
R 38,34752
mvv 18,152,42
mvv 22,42,156
mvv 14,160,46
R 42,34816
mvv 18,164,46
v_cvt_pk_bf16_f32 v114, v70, v71
v_cvt_pk_bf16_f32 v115, v72, v73
mvv 22,46,168
s_waitcnt lgkmcnt(2)
v_cvt_pk_bf16_f32 v116, v74, v75
v_cvt_pk_bf16_f32 v117, v76, v77
mvv 14,172,50
R 46,34880
mvv 18,176,50
buffer_store_dwordx4 v[114:117], v82, s[36:39], s49 offen sc1
s_add_u32 s49, s49, 313344
mvv 22,50,180
mvv 14,184,54
R 50,34944
mvv 18,188,54
mvv 22,54,192
s_waitcnt lgkmcnt(2)
mvv 14,196,38
R 54,35008
mvv 18,200,38
mvv 22,38,204
mvv 14,208,42
R 38,0
mvv 18,212,42
ds_read_b64 v[122:123], v83 offset:0
mvv 22,42,216
s_waitcnt lgkmcnt(3)
ds_read_b128 v[70:73], v84 offset:0
mvv 14,220,46
R 42,64
mvv 18,224,46
ds_read_b128 v[74:77], v84 offset:16
mvv 22,46,228
mvv 14,232,50
R 46,128
mvv 18,236,50
mvv 22,50,240
s_waitcnt lgkmcnt(3)
mvv 14,244,54
R 50,192
mvv 18,248,54
mvv 22,54,252
v_mfma_f32_16x16x32_bf16 v[26:29], a[0:3], v[38:41], 0
s_waitcnt lgkmcnt(1)
R 54,256
v_mul_f32 v71, v71, v99
v_mfma_f32_16x16x32_bf16 v[30:33], a[4:7], v[38:41], 0
v_mul_f32 v73, v73, v99
v_mul_f32 v75, v75, v99
v_mfma_f32_16x16x32_bf16 v[34:37], v[38:41], a[8:11], 0
v_fma_f32 v22, v22, v70, v71
v_fma_f32 v23, v23, v72, v73
mav 26,12,42
R 58,320
v_fma_f32 v24, v24, v74, v75
mav 30,16,42
v_mul_f32 v77, v77, v99
v_fma_f32 v25, v25, v76, v77
mva 34,42,20
v_cvt_pk_bf16_f32 v8, v22, v23
v_cvt_pk_bf16_f32 v9, v24, v25
mav 26,24,46
R 38,384
v_mul_f32 v124, v123, v62
v_mfma_f32_16x16x32_bf16 v[70:73], v[104:107], v[0:3], v[86:89]
v_fma_f32 v14, v14, v122, v124
v_mul_f32 v125, v123, v63
mav 30,28,46
v_fma_f32 v15, v15, v122, v125
v_mul_f32 v124, v123, v64
mva 34,46,32
s_waitcnt lgkmcnt(2)
v_fma_f32 v16, v16, v122, v124
v_mul_f32 v125, v123, v65
mav 26,36,50
R 42,448
v_fma_f32 v17, v17, v122, v125
mav 30,40,50
v_mul_f32 v124, v123, v66
v_fma_f32 v18, v18, v122, v124
mva 34,50,44
v_mul_f32 v125, v123, v67
v_fma_f32 v19, v19, v122, v125
mav 26,48,54
R 46,512
v_mul_f32 v124, v123, v68
mav 30,52,54
v_fma_f32 v20, v20, v122, v124
v_mul_f32 v125, v123, v69
mva 34,54,56
s_waitcnt lgkmcnt(2)
v_fma_f32 v21, v21, v122, v125
v_add_u32 v78, 1, v78
mav 26,60,58
R 50,576
mav 30,64,58
mva 34,58,68
mav 26,72,38
R 54,640
mav 30,76,38
mva 34,38,80
s_waitcnt lgkmcnt(2)
mav 26,84,42
R 58,704
mav 30,88,42
mva 34,42,92
mav 26,96,46
R 38,768
mav 30,100,46
s_barrier
mva 34,46,104
s_waitcnt lgkmcnt(2)
ds_read_b128 v[100:103], v82 offset:0
mav 26,108,50
ds_read_b64 v[108:109], v82 offset:1024
mav 30,112,50
R 42,832
mva 34,50,116
mav 26,120,54
s_waitcnt vmcnt(0)
buffer_store_dword v78, off, s[36:39], s52 sc1
mav 30,124,54
R 46,896
mva 34,54,128
s_waitcnt lgkmcnt(3)
v_mfma_f32_16x16x32_bf16 v[74:77], v[100:103], v[0:3], v[90:93]
v_cvt_pk_bf16_f32 v0, v14, v15
v_cvt_pk_bf16_f32 v1, v16, v17
mav 26,132,58
R 50,960
v_cvt_pk_bf16_f32 v2, v18, v19
mav 30,136,58
v_cvt_pk_bf16_f32 v3, v20, v21
mva 34,58,140
v_max3_f32 v118, v70, v71, v74
v_max3_f32 v119, v72, v73, v76
mav 26,144,38
R 54,1024
v_max_f32 v118, v118, v75
mav 30,148,38
v_max_f32 v119, v119, v77
v_max_f32 v120, v118, v119
mva 34,38,152
s_waitcnt lgkmcnt(2)
v_max_f32 v121, v118, v119
mav 26,156,42
R 38,1088
mav 30,160,42
v_permlane32_swap_b32 v120, v121
mva 34,42,164
v_max_f32 v114, v120, v121
v_max_f32 v115, v120, v121
mav 26,168,46
R 42,1152
mav 30,172,46
v_permlane16_swap_b32 v114, v115
mva 34,46,176
s_waitcnt lgkmcnt(2)
v_max_f32 v120, v114, v115
v_mul_f32 v120, s65, v120
mav 26,180,50
R 46,1216
v_fma_f32 v118, v118, s65, -v120
mav 30,184,50
v_fma_f32 v119, v119, s65, -v120
mva 34,50,188
v_exp_f32 v118, v118
mav 26,192,54
R 50,1280
mav 30,196,54
v_exp_f32 v119, v119
mva 34,54,200
s_waitcnt lgkmcnt(2)
v_add_f32 v120, v118, v119
v_add_f32 v121, v118, v119
mav 26,204,38
R 54,1344
mav 30,208,38
v_permlane32_swap_b32 v120, v121
mva 34,38,212
v_add_f32 v114, v120, v121
v_add_f32 v115, v120, v121
mav 26,216,42
R 38,1408
mav 30,220,42
v_permlane16_swap_b32 v114, v115
mva 34,42,224
s_waitcnt lgkmcnt(2)
v_add_f32 v120, v114, v115
mav 26,228,46
R 42,1472
mav 30,232,46
v_rcp_f32 v120, v120
mva 34,46,236
v_mul_f32 v118, v118, v120
v_mul_f32 v119, v119, v120
mav 26,240,50
R 46,1536
v_cvt_pk_bf16_f32 v118, v118, v118
mav 30,244,50
v_cvt_pk_bf16_f32 v119, v119, v119
v_and_b32 v114, v118, v96
mva 34,50,248
s_waitcnt lgkmcnt(2)
v_and_b32 v115, v119, v97
v_and_b32 v116, v118, v94
mav 26,252,54
R 50,1600
v_and_b32 v117, v119, v95
mvv 30,128,54
mvv 34,54,132
v_mfma_f32_16x16x32_bf16 v[70:73], v[8:11], v[114:117], 0
v_mfma_f32_16x16x32_bf16 v[74:77], v[108:111], v[114:117], 0
mvv 26,136,38
R 54,1664
mvv 30,140,38
mvv 34,38,144
s_waitcnt lgkmcnt(2)
mvv 26,148,42
R 38,1728
v_cvt_pk_bf16_f32 v114, v70, v71
mvv 30,152,42
v_cvt_pk_bf16_f32 v115, v72, v73
v_cvt_pk_bf16_f32 v116, v74, v75
mvv 34,42,156
v_cvt_pk_bf16_f32 v117, v76, v77
mvv 26,160,46
buffer_store_dwordx4 v[114:117], v82, s[36:39], s49 offen sc1
R 42,1792
s_add_u32 s49, s49, 313344
mvv 30,164,46
mvv 34,46,168
s_waitcnt lgkmcnt(2)
mvv 26,172,50
R 46,1856
mvv 30,176,50
mvv 34,50,180
mvv 26,184,54
R 50,1920
mvv 30,188,54
s_waitcnt lgkmcnt(2)
mvv 34,54,192
s_mov_b32 s60, -13
.p2align 6
LQA0_1:
mvv 26,196,38
R 54,1984
v_xor_b32 v80, v81, v80
mvv 30,200,38
mvv 34,38,204
mvv 26,208,42
R 38,33024
mvv 30,212,42
ds_read_b64 v[122:123], v83 offset:128
mvv 34,42,216
s_waitcnt lgkmcnt(3)
ds_read_b128 v[70:73], v84 offset:128
mvv 26,220,46
R 42,33088
mvv 30,224,46
ds_read_b128 v[74:77], v84 offset:144
mvv 34,46,228
mvv 26,232,50
R 46,33152
mvv 30,236,50
mvv 34,50,240
s_waitcnt lgkmcnt(3)
mvv 26,244,54
R 50,33216
mvv 30,248,54
mvv 34,54,252
v_mfma_f32_16x16x32_bf16 v[14:17], a[0:3], v[38:41], 0
s_waitcnt lgkmcnt(1)
R 54,33280
v_mul_f32 v71, v71, v99
v_mfma_f32_16x16x32_bf16 v[18:21], a[4:7], v[38:41], 0
v_mul_f32 v73, v73, v99
v_mul_f32 v75, v75, v99
v_mfma_f32_16x16x32_bf16 v[22:25], v[38:41], a[8:11], 0
v_fma_f32 v34, v34, v70, v71
v_fma_f32 v35, v35, v72, v73
mav 14,12,42
R 58,33344
v_fma_f32 v36, v36, v74, v75
mav 18,16,42
v_mul_f32 v77, v77, v99
v_fma_f32 v37, v37, v76, v77
mva 22,42,20
v_cvt_pk_bf16_f32 v10, v34, v35
v_cvt_pk_bf16_f32 v11, v36, v37
mav 14,24,46
R 38,33408
v_mul_f32 v124, v123, v62
v_mfma_f32_16x16x32_bf16 v[70:73], v[100:103], v[0:3], v[86:89]
v_fma_f32 v26, v26, v122, v124
v_mul_f32 v125, v123, v63
mav 18,28,46
v_fma_f32 v27, v27, v122, v125
v_mul_f32 v124, v123, v64
mva 22,46,32
s_waitcnt lgkmcnt(2)
v_fma_f32 v28, v28, v122, v124
v_mul_f32 v125, v123, v65
mav 14,36,50
R 42,33472
v_fma_f32 v29, v29, v122, v125
mav 18,40,50
v_mul_f32 v124, v123, v66
v_fma_f32 v30, v30, v122, v124
mva 22,50,44
v_mul_f32 v125, v123, v67
v_fma_f32 v31, v31, v122, v125
mav 14,48,54
R 46,33536
v_mul_f32 v124, v123, v68
mav 18,52,54
v_fma_f32 v32, v32, v122, v124
v_mul_f32 v125, v123, v69
mva 22,54,56
s_waitcnt lgkmcnt(2)
v_fma_f32 v33, v33, v122, v125
v_add_u32 v78, 1, v78
mav 14,60,58
R 50,33600
mav 18,64,58
mva 22,58,68
mav 14,72,38
R 54,33664
mav 18,76,38
mva 22,38,80
s_waitcnt lgkmcnt(2)
mav 14,84,42
R 58,33728
mav 18,88,42
mva 22,42,92
mav 14,96,46
R 38,33792
mav 18,100,46
s_barrier
mva 22,46,104
s_waitcnt lgkmcnt(2)
ds_read_b128 v[104:107], v82 offset:4096
mav 14,108,50
ds_read_b64 v[110:111], v82 offset:5120
mav 18,112,50
R 42,33856
mva 22,50,116
mav 14,120,54
s_waitcnt vmcnt(0)
buffer_store_dword v78, off, s[36:39], s52 sc1
mav 18,124,54
R 46,33920
mva 22,54,128
s_waitcnt lgkmcnt(3)
v_mfma_f32_16x16x32_bf16 v[74:77], v[104:107], v[0:3], v[90:93]
v_cvt_pk_bf16_f32 v0, v26, v27
v_cvt_pk_bf16_f32 v1, v28, v29
mav 14,132,58
R 50,33984
v_cvt_pk_bf16_f32 v2, v30, v31
mav 18,136,58
v_cvt_pk_bf16_f32 v3, v32, v33
mva 22,58,140
v_max3_f32 v118, v70, v71, v74
v_max3_f32 v119, v72, v73, v76
mav 14,144,38
R 54,34048
v_max_f32 v118, v118, v75
mav 18,148,38
v_max_f32 v119, v119, v77
v_max_f32 v120, v118, v119
mva 22,38,152
s_waitcnt lgkmcnt(2)
v_max_f32 v121, v118, v119
mav 14,156,42
R 38,34112
mav 18,160,42
v_permlane32_swap_b32 v120, v121
mva 22,42,164
v_max_f32 v114, v120, v121
v_max_f32 v115, v120, v121
mav 14,168,46
R 42,34176
mav 18,172,46
v_permlane16_swap_b32 v114, v115
mva 22,46,176
s_waitcnt lgkmcnt(2)
v_max_f32 v120, v114, v115
v_mul_f32 v120, s65, v120
mav 14,180,50
R 46,34240
LsQ3:
s_cbranch_execz LsQ7
v_fma_f32 v118, v118, s65, -v120
mav 18,184,50
v_fma_f32 v119, v119, s65, -v120
mva 22,50,188
v_exp_f32 v118, v118
mav 14,192,54
R 50,34304
mav 18,196,54
v_exp_f32 v119, v119
mva 22,54,200
s_waitcnt lgkmcnt(2)
v_add_f32 v120, v118, v119
v_add_f32 v121, v118, v119
mav 14,204,38
R 54,34368
mav 18,208,38
v_permlane32_swap_b32 v120, v121
mva 22,38,212
v_add_f32 v114, v120, v121
v_add_f32 v115, v120, v121
mav 14,216,42
R 38,34432
mav 18,220,42
v_permlane16_swap_b32 v114, v115
mva 22,42,224
s_waitcnt lgkmcnt(2)
v_add_f32 v120, v114, v115
mav 14,228,46
R 42,34496
mav 18,232,46
v_rcp_f32 v120, v120
mva 22,46,236
v_mul_f32 v118, v118, v120
v_mul_f32 v119, v119, v120
mav 14,240,50
R 46,34560
v_cvt_pk_bf16_f32 v118, v118, v118
mav 18,244,50
v_cvt_pk_bf16_f32 v119, v119, v119
v_and_b32 v114, v118, v94
mva 22,50,248
s_waitcnt lgkmcnt(2)
v_and_b32 v115, v119, v95
v_and_b32 v116, v118, v96
mav 14,252,54
R 50,34624
v_and_b32 v117, v119, v97
mvv 18,128,54
mvv 22,54,132
v_mfma_f32_16x16x32_bf16 v[70:73], v[8:11], v[114:117], 0
v_mfma_f32_16x16x32_bf16 v[74:77], v[108:111], v[114:117], 0
mvv 14,136,38
R 54,34688
mvv 18,140,38
mvv 22,38,144
s_waitcnt lgkmcnt(2)
mvv 14,148,42
R 38,34752
v_cvt_pk_bf16_f32 v114, v70, v71
mvv 18,152,42
v_cvt_pk_bf16_f32 v115, v72, v73
v_cvt_pk_bf16_f32 v116, v74, v75
mvv 22,42,156
v_cvt_pk_bf16_f32 v117, v76, v77
mvv 14,160,46
buffer_store_dwordx4 v[114:117], v82, s[36:39], s49 offen sc1
R 42,34816
s_add_u32 s49, s49, 313344
mvv 18,164,46
mvv 22,46,168
s_waitcnt lgkmcnt(2)
mvv 14,172,50
R 46,34880
mvv 18,176,50
mvv 22,50,180
mvv 14,184,54
R 50,34944
mvv 18,188,54
mvv 22,54,192
s_waitcnt lgkmcnt(2)
mvv 14,196,38
R 54,35008
mvv 18,200,38
mvv 22,38,204
mvv 14,208,42
R 38,0
mvv 18,212,42
ds_read_b64 v[122:123], v83 offset:0
mvv 22,42,216
s_waitcnt lgkmcnt(3)
ds_read_b128 v[70:73], v84 offset:0
mvv 14,220,46
R 42,64
mvv 18,224,46
ds_read_b128 v[74:77], v84 offset:16
mvv 22,46,228
mvv 14,232,50
R 46,128
mvv 18,236,50
mvv 22,50,240
s_waitcnt lgkmcnt(3)
mvv 14,244,54
R 50,192
mvv 18,248,54
mvv 22,54,252
v_mfma_f32_16x16x32_bf16 v[26:29], a[0:3], v[38:41], 0
s_waitcnt lgkmcnt(1)
R 54,256
v_mul_f32 v71, v71, v99
v_mfma_f32_16x16x32_bf16 v[30:33], a[4:7], v[38:41], 0
v_mul_f32 v73, v73, v99
v_mul_f32 v75, v75, v99
v_mfma_f32_16x16x32_bf16 v[34:37], v[38:41], a[8:11], 0
v_fma_f32 v22, v22, v70, v71
v_fma_f32 v23, v23, v72, v73
mav 26,12,42
R 58,320
v_fma_f32 v24, v24, v74, v75
mav 30,16,42
v_mul_f32 v77, v77, v99
v_fma_f32 v25, v25, v76, v77
mva 34,42,20
v_cvt_pk_bf16_f32 v8, v22, v23
v_cvt_pk_bf16_f32 v9, v24, v25
mav 26,24,46
R 38,384
v_mul_f32 v124, v123, v62
v_mfma_f32_16x16x32_bf16 v[70:73], v[104:107], v[0:3], v[86:89]
v_fma_f32 v14, v14, v122, v124
v_mul_f32 v125, v123, v63
mav 30,28,46
v_fma_f32 v15, v15, v122, v125
v_mul_f32 v124, v123, v64
mva 34,46,32
s_waitcnt lgkmcnt(2)
v_fma_f32 v16, v16, v122, v124
v_mul_f32 v125, v123, v65
mav 26,36,50
R 42,448
v_fma_f32 v17, v17, v122, v125
mav 30,40,50
v_mul_f32 v124, v123, v66
v_fma_f32 v18, v18, v122, v124
mva 34,50,44
v_mul_f32 v125, v123, v67
v_fma_f32 v19, v19, v122, v125
mav 26,48,54
R 46,512
v_mul_f32 v124, v123, v68
mav 30,52,54
v_fma_f32 v20, v20, v122, v124
v_mul_f32 v125, v123, v69
mva 34,54,56
s_waitcnt lgkmcnt(2)
v_fma_f32 v21, v21, v122, v125
v_add_u32 v78, 1, v78
mav 26,60,58
R 50,576
mav 30,64,58
mva 34,58,68
mav 26,72,38
R 54,640
mav 30,76,38
mva 34,38,80
s_waitcnt lgkmcnt(2)
mav 26,84,42
R 58,704
mav 30,88,42
mva 34,42,92
mav 26,96,46
R 38,768
mav 30,100,46
s_barrier
mva 34,46,104
s_waitcnt lgkmcnt(2)
ds_read_b128 v[100:103], v82 offset:0
mav 26,108,50
ds_read_b64 v[108:109], v82 offset:1024
mav 30,112,50
R 42,832
mva 34,50,116
mav 26,120,54
s_waitcnt vmcnt(0)
buffer_store_dword v78, off, s[36:39], s52 sc1
mav 30,124,54
R 46,896
mva 34,54,128
s_waitcnt lgkmcnt(3)
v_mfma_f32_16x16x32_bf16 v[74:77], v[100:103], v[0:3], v[90:93]
v_cvt_pk_bf16_f32 v0, v14, v15
v_cvt_pk_bf16_f32 v1, v16, v17
mav 26,132,58
R 50,960
v_cvt_pk_bf16_f32 v2, v18, v19
mav 30,136,58
v_cvt_pk_bf16_f32 v3, v20, v21
mva 34,58,140
v_max3_f32 v118, v70, v71, v74
v_max3_f32 v119, v72, v73, v76
mav 26,144,38
R 54,1024
v_max_f32 v118, v118, v75
mav 30,148,38
v_max_f32 v119, v119, v77
v_max_f32 v120, v118, v119
mva 34,38,152
s_waitcnt lgkmcnt(2)
v_max_f32 v121, v118, v119
mav 26,156,42
R 38,1088
mav 30,160,42
v_permlane32_swap_b32 v120, v121
mva 34,42,164
v_max_f32 v114, v120, v121
v_max_f32 v115, v120, v121
mav 26,168,46
R 42,1152
mav 30,172,46
v_permlane16_swap_b32 v114, v115
mva 34,46,176
s_waitcnt lgkmcnt(2)
v_max_f32 v120, v114, v115
v_mul_f32 v120, s65, v120
mav 26,180,50
R 46,1216
v_fma_f32 v118, v118, s65, -v120
mav 30,184,50
v_fma_f32 v119, v119, s65, -v120
mva 34,50,188
v_exp_f32 v118, v118
mav 26,192,54
R 50,1280
mav 30,196,54
v_exp_f32 v119, v119
mva 34,54,200
s_waitcnt lgkmcnt(2)
v_add_f32 v120, v118, v119
v_add_f32 v121, v118, v119
mav 26,204,38
R 54,1344
mav 30,208,38
v_permlane32_swap_b32 v120, v121
mva 34,38,212
v_add_f32 v114, v120, v121
v_add_f32 v115, v120, v121
mav 26,216,42
R 38,1408
mav 30,220,42
v_permlane16_swap_b32 v114, v115
mva 34,42,224
s_waitcnt lgkmcnt(2)
v_add_f32 v120, v114, v115
mav 26,228,46
R 42,1472
mav 30,232,46
v_rcp_f32 v120, v120
mva 34,46,236
v_mul_f32 v118, v118, v120
v_mul_f32 v119, v119, v120
mav 26,240,50
R 46,1536
v_cvt_pk_bf16_f32 v118, v118, v118
mav 30,244,50
v_cvt_pk_bf16_f32 v119, v119, v119
v_and_b32 v114, v118, v96
mva 34,50,248
s_waitcnt lgkmcnt(2)
v_and_b32 v115, v119, v97
v_and_b32 v116, v118, v94
mav 26,252,54
R 50,1600
v_and_b32 v117, v119, v95
mvv 30,128,54
mvv 34,54,132
v_mfma_f32_16x16x32_bf16 v[70:73], v[8:11], v[114:117], 0
v_mfma_f32_16x16x32_bf16 v[74:77], v[108:111], v[114:117], 0
mvv 26,136,38
R 54,1664
mvv 30,140,38
mvv 34,38,144
s_waitcnt lgkmcnt(2)
mvv 26,148,42
R 38,1728
v_cvt_pk_bf16_f32 v114, v70, v71
mvv 30,152,42
v_cvt_pk_bf16_f32 v115, v72, v73
v_cvt_pk_bf16_f32 v116, v74, v75
mvv 34,42,156
v_cvt_pk_bf16_f32 v117, v76, v77
mvv 26,160,46
buffer_store_dwordx4 v[114:117], v82, s[36:39], s49 offen sc1
R 42,1792
s_add_u32 s49, s49, 313344
mvv 30,164,46
mvv 34,46,168
s_waitcnt lgkmcnt(2)
mvv 26,172,50
R 46,1856
mvv 30,176,50
mvv 34,50,180
mvv 26,184,54
R 50,1920
mvv 30,188,54
s_waitcnt lgkmcnt(2)
s_add_u32 s60, s60, 1
mvv 34,54,192
s_cbranch_scc0 LQA0_1
mvv 26,196,38
R 54,1984
v_xor_b32 v80, v81, v80
mvv 30,200,38
mvv 34,38,204
mvv 26,208,42
R 38,33024
mvv 30,212,42
ds_read_b64 v[122:123], v83 offset:128
mvv 34,42,216
s_waitcnt lgkmcnt(3)
ds_read_b128 v[70:73], v84 offset:128
mvv 26,220,46
R 42,33088
mvv 30,224,46
ds_read_b128 v[74:77], v84 offset:144
mvv 34,46,228
mvv 26,232,50
R 46,33152
mvv 30,236,50
mvv 34,50,240
s_waitcnt lgkmcnt(3)
mvv 26,244,54
R 50,33216
mvv 30,248,54
mvv 34,54,252
v_mfma_f32_16x16x32_bf16 v[14:17], a[0:3], v[38:41], 0
s_waitcnt lgkmcnt(1)
R 54,33280
v_mul_f32 v71, v71, v99
v_mfma_f32_16x16x32_bf16 v[18:21], a[4:7], v[38:41], 0
v_mul_f32 v73, v73, v99
v_mul_f32 v75, v75, v99
v_mfma_f32_16x16x32_bf16 v[22:25], v[38:41], a[8:11], 0
v_fma_f32 v34, v34, v70, v71
v_fma_f32 v35, v35, v72, v73
mav 14,12,42
R 58,33344
v_fma_f32 v36, v36, v74, v75
mav 18,16,42
v_mul_f32 v77, v77, v99
v_fma_f32 v37, v37, v76, v77
mva 22,42,20
v_cvt_pk_bf16_f32 v10, v34, v35
v_cvt_pk_bf16_f32 v11, v36, v37
mav 14,24,46
R 38,33408
v_mul_f32 v124, v123, v62
v_mfma_f32_16x16x32_bf16 v[70:73], v[100:103], v[0:3], v[86:89]
v_fma_f32 v26, v26, v122, v124
v_mul_f32 v125, v123, v63
mav 18,28,46
v_fma_f32 v27, v27, v122, v125
v_mul_f32 v124, v123, v64
mva 22,46,32
s_waitcnt lgkmcnt(2)
v_fma_f32 v28, v28, v122, v124
v_mul_f32 v125, v123, v65
mav 14,36,50
R 42,33472
v_fma_f32 v29, v29, v122, v125
mav 18,40,50
v_mul_f32 v124, v123, v66
v_fma_f32 v30, v30, v122, v124
mva 22,50,44
v_mul_f32 v125, v123, v67
v_fma_f32 v31, v31, v122, v125
mav 14,48,54
R 46,33536
v_mul_f32 v124, v123, v68
mav 18,52,54
v_fma_f32 v32, v32, v122, v124
v_mul_f32 v125, v123, v69
mva 22,54,56
s_waitcnt lgkmcnt(2)
v_fma_f32 v33, v33, v122, v125
v_add_u32 v78, 1, v78
mav 14,60,58
R 50,33600
mav 18,64,58
mva 22,58,68
mav 14,72,38
R 54,33664
mav 18,76,38
mva 22,38,80
s_waitcnt lgkmcnt(2)
mav 14,84,42
R 58,33728
mav 18,88,42
mva 22,42,92
mav 14,96,46
R 38,33792
mav 18,100,46
s_barrier
mva 22,46,104
s_waitcnt lgkmcnt(2)
ds_read_b128 v[104:107], v82 offset:4096
mav 14,108,50
ds_read_b64 v[110:111], v82 offset:5120
mav 18,112,50
R 42,33856
mva 22,50,116
mav 14,120,54
s_waitcnt vmcnt(0)
buffer_store_dword v78, off, s[36:39], s52 sc1
mav 18,124,54
R 46,33920
mva 22,54,128
s_waitcnt lgkmcnt(3)
v_mfma_f32_16x16x32_bf16 v[74:77], v[104:107], v[0:3], v[90:93]
v_cvt_pk_bf16_f32 v0, v26, v27
v_cvt_pk_bf16_f32 v1, v28, v29
mav 14,132,58
R 50,33984
v_cvt_pk_bf16_f32 v2, v30, v31
mav 18,136,58
v_cvt_pk_bf16_f32 v3, v32, v33
mva 22,58,140
v_max3_f32 v118, v70, v71, v74
v_max3_f32 v119, v72, v73, v76
mav 14,144,38
R 54,34048
v_max_f32 v118, v118, v75
mav 18,148,38
v_max_f32 v119, v119, v77
v_max_f32 v120, v118, v119
mva 22,38,152
s_waitcnt lgkmcnt(2)
v_max_f32 v121, v118, v119
mav 14,156,42
R 38,34112
mav 18,160,42
v_permlane32_swap_b32 v120, v121
mva 22,42,164
v_max_f32 v114, v120, v121
v_max_f32 v115, v120, v121
mav 14,168,46
R 42,34176
mav 18,172,46
v_permlane16_swap_b32 v114, v115
mva 22,46,176
s_waitcnt lgkmcnt(2)
v_max_f32 v120, v114, v115
v_mul_f32 v120, s65, v120
mav 14,180,50
R 46,34240
v_fma_f32 v118, v118, s65, -v120
mav 18,184,50
v_fma_f32 v119, v119, s65, -v120
mva 22,50,188
v_exp_f32 v118, v118
mav 14,192,54
R 50,34304
mav 18,196,54
v_exp_f32 v119, v119
mva 22,54,200
s_waitcnt lgkmcnt(2)
v_add_f32 v120, v118, v119
v_add_f32 v121, v118, v119
mav 14,204,38
R 54,34368
mav 18,208,38
v_permlane32_swap_b32 v120, v121
mva 22,38,212
v_add_f32 v114, v120, v121
v_add_f32 v115, v120, v121
mav 14,216,42
R 38,34432
mav 18,220,42
v_permlane16_swap_b32 v114, v115
mva 22,42,224
s_waitcnt lgkmcnt(2)
v_add_f32 v120, v114, v115
mav 14,228,46
R 42,34496
mav 18,232,46
v_rcp_f32 v120, v120
mva 22,46,236
v_mul_f32 v118, v118, v120
v_mul_f32 v119, v119, v120
mav 14,240,50
R 46,34560
v_cvt_pk_bf16_f32 v118, v118, v118
mav 18,244,50
v_cvt_pk_bf16_f32 v119, v119, v119
v_and_b32 v114, v118, v94
mva 22,50,248
s_waitcnt lgkmcnt(2)
v_and_b32 v115, v119, v95
v_and_b32 v116, v118, v96
mav 14,252,54
R 50,34624
v_and_b32 v117, v119, v97
mvv 18,128,54
mvv 22,54,132
v_mfma_f32_16x16x32_bf16 v[70:73], v[8:11], v[114:117], 0
v_mfma_f32_16x16x32_bf16 v[74:77], v[108:111], v[114:117], 0
mvv 14,136,38
R 54,34688
mvv 18,140,38
mvv 22,38,144
s_waitcnt lgkmcnt(2)
mvv 14,148,42
R 38,34752
v_cvt_pk_bf16_f32 v114, v70, v71
mvv 18,152,42
v_cvt_pk_bf16_f32 v115, v72, v73
v_cvt_pk_bf16_f32 v116, v74, v75
mvv 22,42,156
v_cvt_pk_bf16_f32 v117, v76, v77
mvv 14,160,46
buffer_store_dwordx4 v[114:117], v82, s[36:39], s49 offen sc1
R 42,34816
s_add_u32 s49, s49, 313344
mvv 18,164,46
mvv 22,46,168
s_waitcnt lgkmcnt(2)
mvv 14,172,50
R 46,34880
mvv 18,176,50
mvv 22,50,180
)"
R"(
mvv 14,184,54
R 50,34944
mvv 18,188,54
mvv 22,54,192
s_waitcnt lgkmcnt(2)
mvv 14,196,38
R 54,35008
mvv 18,200,38
mvv 22,38,204
mvv 14,208,42
R 38,0
mvv 18,212,42
ds_read_b64 v[122:123], v83 offset:0
mvv 22,42,216
s_waitcnt lgkmcnt(3)
ds_read_b128 v[70:73], v84 offset:0
mvv 14,220,46
R 42,64
mvv 18,224,46
ds_read_b128 v[74:77], v84 offset:16
mvv 22,46,228
mvv 14,232,50
R 46,128
mvv 18,236,50
mvv 22,50,240
s_waitcnt lgkmcnt(3)
mvv 14,244,54
R 50,192
mvv 18,248,54
mvv 22,54,252
v_mfma_f32_16x16x32_bf16 v[26:29], a[0:3], v[38:41], 0
s_waitcnt lgkmcnt(1)
R 54,256
v_mul_f32 v71, v71, v99
v_mfma_f32_16x16x32_bf16 v[30:33], a[4:7], v[38:41], 0
v_mul_f32 v73, v73, v99
v_mul_f32 v75, v75, v99
v_mfma_f32_16x16x32_bf16 v[34:37], v[38:41], a[8:11], 0
v_fma_f32 v22, v22, v70, v71
v_fma_f32 v23, v23, v72, v73
mav 26,12,42
R 58,320
v_fma_f32 v24, v24, v74, v75
mav 30,16,42
v_mul_f32 v77, v77, v99
v_fma_f32 v25, v25, v76, v77
mva 34,42,20
v_cvt_pk_bf16_f32 v8, v22, v23
v_cvt_pk_bf16_f32 v9, v24, v25
mav 26,24,46
R 38,384
v_mul_f32 v124, v123, v62
v_mfma_f32_16x16x32_bf16 v[70:73], v[104:107], v[0:3], v[86:89]
v_fma_f32 v14, v14, v122, v124
v_mul_f32 v125, v123, v63
mav 30,28,46
v_fma_f32 v15, v15, v122, v125
v_mul_f32 v124, v123, v64
LsQ4:
s_cbranch_execz LsQ8
mva 34,46,32
s_waitcnt lgkmcnt(2)
v_fma_f32 v16, v16, v122, v124
v_mul_f32 v125, v123, v65
mav 26,36,50
R 42,448
v_fma_f32 v17, v17, v122, v125
mav 30,40,50
buffer_load_dwordx4 v[4:7], v82, s[36:39], s55 offen sc1
mva 34,50,44
v_mul_f32 v124, v123, v66
v_fma_f32 v18, v18, v122, v124
mav 26,48,54
buffer_load_dwordx2 v[12:13], v98, s[36:39], s55 offen offset:1024 sc1
mav 30,52,54
R 46,512
v_mul_f32 v125, v123, v67
mva 34,54,56
s_waitcnt lgkmcnt(2)
buffer_load_dwordx2 v[112:113], v98, s[36:39], s55 offen offset:1536 sc1
mav 26,60,58
R 50,576
v_fma_f32 v19, v19, v122, v125
mav 30,64,58
v_mul_f32 v124, v123, v68
v_fma_f32 v20, v20, v122, v124
mva 34,58,68
v_mul_f32 v125, v123, v69
v_fma_f32 v21, v21, v122, v125
mav 26,72,38
R 54,640
v_add_u32 v78, 1, v78
mav 30,76,38
mva 34,38,80
s_waitcnt lgkmcnt(2)
mav 26,84,42
R 58,704
mav 30,88,42
mva 34,42,92
mav 26,96,46
R 38,768
mav 30,100,46
s_barrier
mva 34,46,104
s_waitcnt lgkmcnt(2)
ds_read_b128 v[100:103], v82 offset:0
mav 26,108,50
ds_read_b64 v[108:109], v82 offset:1024
mav 30,112,50
R 42,832
mva 34,50,116
mav 26,120,54
s_waitcnt vmcnt(3)
buffer_store_dword v78, off, s[36:39], s52 sc1
mav 30,124,54
R 46,896
mva 34,54,128
s_waitcnt lgkmcnt(3)
v_mfma_f32_16x16x32_bf16 v[74:77], v[100:103], v[0:3], v[90:93]
v_cvt_pk_bf16_f32 v0, v14, v15
v_cvt_pk_bf16_f32 v1, v16, v17
mav 26,132,58
R 50,960
v_cvt_pk_bf16_f32 v2, v18, v19
mav 30,136,58
v_cvt_pk_bf16_f32 v3, v20, v21
mva 34,58,140
v_max3_f32 v118, v70, v71, v74
v_max3_f32 v119, v72, v73, v76
mav 26,144,38
R 54,1024
v_max_f32 v118, v118, v75
mav 30,148,38
v_max_f32 v119, v119, v77
v_max_f32 v120, v118, v119
mva 34,38,152
s_waitcnt lgkmcnt(2)
v_max_f32 v121, v118, v119
mav 26,156,42
R 38,1088
mav 30,160,42
v_permlane32_swap_b32 v120, v121
mva 34,42,164
v_max_f32 v114, v120, v121
v_max_f32 v115, v120, v121
mav 26,168,46
R 42,1152
mav 30,172,46
v_permlane16_swap_b32 v114, v115
mva 34,46,176
s_waitcnt lgkmcnt(2)
v_max_f32 v120, v114, v115
v_mul_f32 v120, s65, v120
mav 26,180,50
R 46,1216
v_fma_f32 v118, v118, s65, -v120
mav 30,184,50
v_fma_f32 v119, v119, s65, -v120
mva 34,50,188
v_exp_f32 v118, v118
mav 26,192,54
R 50,1280
mav 30,196,54
v_exp_f32 v119, v119
mva 34,54,200
s_waitcnt lgkmcnt(2)
v_add_f32 v120, v118, v119
v_add_f32 v121, v118, v119
mav 26,204,38
R 54,1344
mav 30,208,38
v_permlane32_swap_b32 v120, v121
mva 34,38,212
v_add_f32 v114, v120, v121
v_add_f32 v115, v120, v121
mav 26,216,42
R 38,1408
mav 30,220,42
v_permlane16_swap_b32 v114, v115
mva 34,42,224
s_waitcnt lgkmcnt(2)
v_add_f32 v120, v114, v115
mav 26,228,46
R 42,1472
mav 30,232,46
v_rcp_f32 v120, v120
mva 34,46,236
v_mul_f32 v118, v118, v120
v_mul_f32 v119, v119, v120
mav 26,240,50
R 46,1536
v_cvt_pk_bf16_f32 v118, v118, v118
mav 30,244,50
v_cvt_pk_bf16_f32 v119, v119, v119
v_and_b32 v114, v118, v96
mva 34,50,248
s_waitcnt lgkmcnt(2)
v_and_b32 v115, v119, v97
v_and_b32 v116, v118, v94
mav 26,252,54
R 50,1600
v_and_b32 v117, v119, v95
mvv 30,128,54
mvv 34,54,132
v_mfma_f32_16x16x32_bf16 v[70:73], v[8:11], v[114:117], 0
v_mfma_f32_16x16x32_bf16 v[74:77], v[108:111], v[114:117], 0
mvv 26,136,38
R 54,1664
mvv 30,140,38
mvv 34,38,144
s_waitcnt lgkmcnt(2)
mvv 26,148,42
R 38,1728
v_cvt_pk_bf16_f32 v114, v70, v71
mvv 30,152,42
v_cvt_pk_bf16_f32 v115, v72, v73
v_cvt_pk_bf16_f32 v116, v74, v75
mvv 34,42,156
v_cvt_pk_bf16_f32 v117, v76, v77
mvv 26,160,46
buffer_store_dwordx4 v[114:117], v82, s[36:39], s49 offen sc1
R 42,1792
s_add_u32 s49, s49, 313344
mvv 30,164,46
mvv 34,46,168
s_waitcnt lgkmcnt(2)
mvv 26,172,50
R 46,1856
mvv 30,176,50
mvv 34,50,180
mvv 26,184,54
R 50,1920
mvv 30,188,54
s_waitcnt lgkmcnt(2)
mvv 34,54,192
mvv 26,196,38
R 54,1984
v_xor_b32 v80, v81, v80
mvv 30,200,38
mvv 34,38,204
mvv 26,208,42
R 38,33024
mvv 30,212,42
ds_read_b64 v[122:123], v83 offset:128
mvv 34,42,216
s_waitcnt lgkmcnt(3)
ds_read_b128 v[70:73], v84 offset:128
mvv 26,220,46
R 42,33088
mvv 30,224,46
ds_read_b128 v[74:77], v84 offset:144
mvv 34,46,228
mvv 26,232,50
R 46,33152
mvv 30,236,50
mvv 34,50,240
s_waitcnt lgkmcnt(3)
mvv 26,244,54
R 50,33216
mvv 30,248,54
mvv 34,54,252
v_mfma_f32_16x16x32_bf16 v[14:17], a[0:3], v[38:41], 0
s_waitcnt lgkmcnt(1)
R 54,33280
v_mul_f32 v71, v71, v99
v_mfma_f32_16x16x32_bf16 v[18:21], a[4:7], v[38:41], 0
v_mul_f32 v73, v73, v99
v_mul_f32 v75, v75, v99
v_mfma_f32_16x16x32_bf16 v[22:25], v[38:41], a[8:11], 0
v_fma_f32 v34, v34, v70, v71
v_fma_f32 v35, v35, v72, v73
mav 14,12,42
R 58,33344
v_fma_f32 v36, v36, v74, v75
mav 18,16,42
v_mul_f32 v77, v77, v99
v_fma_f32 v37, v37, v76, v77
mva 22,42,20
v_cvt_pk_bf16_f32 v10, v34, v35
v_cvt_pk_bf16_f32 v11, v36, v37
mav 14,24,46
R 38,33408
v_mul_f32 v124, v123, v62
v_mfma_f32_16x16x32_bf16 v[70:73], v[100:103], v[0:3], v[86:89]
v_fma_f32 v26, v26, v122, v124
v_mul_f32 v125, v123, v63
mav 18,28,46
v_fma_f32 v27, v27, v122, v125
v_mul_f32 v124, v123, v64
mva 22,46,32
s_waitcnt lgkmcnt(2)
v_fma_f32 v28, v28, v122, v124
v_mul_f32 v125, v123, v65
mav 14,36,50
R 42,33472
v_fma_f32 v29, v29, v122, v125
mav 18,40,50
v_mul_f32 v124, v123, v66
v_fma_f32 v30, v30, v122, v124
mva 22,50,44
v_mul_f32 v125, v123, v67
v_fma_f32 v31, v31, v122, v125
mav 14,48,54
R 46,33536
v_mul_f32 v124, v123, v68
mav 18,52,54
v_fma_f32 v32, v32, v122, v124
v_mul_f32 v125, v123, v69
mva 22,54,56
s_waitcnt lgkmcnt(2)
v_fma_f32 v33, v33, v122, v125
v_add_u32 v78, 1, v78
mav 14,60,58
R 50,33600
mav 18,64,58
mva 22,58,68
mav 14,72,38
R 54,33664
mav 18,76,38
mva 22,38,80
s_waitcnt lgkmcnt(2)
mav 14,84,42
R 58,33728
mav 18,88,42
mva 22,42,92
mav 14,96,46
R 38,33792
mav 18,100,46
s_barrier
mva 22,46,104
s_waitcnt lgkmcnt(2)
ds_read_b128 v[104:107], v82 offset:4096
mav 14,108,50
ds_read_b64 v[110:111], v82 offset:5120
mav 18,112,50
R 42,33856
mva 22,50,116
mav 14,120,54
s_waitcnt vmcnt(0)
buffer_store_dword v78, off, s[36:39], s52 sc1
mav 18,124,54
R 46,33920
mva 22,54,128
s_waitcnt lgkmcnt(3)
v_mfma_f32_16x16x32_bf16 v[74:77], v[104:107], v[0:3], v[90:93]
v_cvt_pk_bf16_f32 v0, v26, v27
v_cvt_pk_bf16_f32 v1, v28, v29
mav 14,132,58
R 50,33984
v_cvt_pk_bf16_f32 v2, v30, v31
mav 18,136,58
v_cvt_pk_bf16_f32 v3, v32, v33
mva 22,58,140
v_max3_f32 v118, v70, v71, v74
v_max3_f32 v119, v72, v73, v76
mav 14,144,38
R 54,34048
v_max_f32 v118, v118, v75
mav 18,148,38
v_max_f32 v119, v119, v77
v_max_f32 v120, v118, v119
mva 22,38,152
s_waitcnt lgkmcnt(2)
v_max_f32 v121, v118, v119
mav 14,156,42
R 38,34112
mav 18,160,42
v_permlane32_swap_b32 v120, v121
mva 22,42,164
v_max_f32 v114, v120, v121
v_max_f32 v115, v120, v121
mav 14,168,46
R 42,34176
mav 18,172,46
v_permlane16_swap_b32 v114, v115
mva 22,46,176
s_waitcnt lgkmcnt(2)
v_max_f32 v120, v114, v115
v_mul_f32 v120, s65, v120
mav 14,180,50
R 46,34240
v_fma_f32 v118, v118, s65, -v120
mav 18,184,50
v_fma_f32 v119, v119, s65, -v120
mva 22,50,188
v_exp_f32 v118, v118
mav 14,192,54
R 50,34304
mav 18,196,54
v_exp_f32 v119, v119
mva 22,54,200
s_waitcnt lgkmcnt(2)
v_add_f32 v120, v118, v119
v_add_f32 v121, v118, v119
mav 14,204,38
R 54,34368
mav 18,208,38
v_permlane32_swap_b32 v120, v121
mva 22,38,212
v_add_f32 v114, v120, v121
v_add_f32 v115, v120, v121
mav 14,216,42
R 38,34432
mav 18,220,42
v_permlane16_swap_b32 v114, v115
mva 22,42,224
s_waitcnt lgkmcnt(2)
v_add_f32 v120, v114, v115
mav 14,228,46
R 42,34496
mav 18,232,46
v_rcp_f32 v120, v120
mva 22,46,236
v_mul_f32 v118, v118, v120
v_mul_f32 v119, v119, v120
mav 14,240,50
R 46,34560
v_cvt_pk_bf16_f32 v118, v118, v118
mav 18,244,50
v_cvt_pk_bf16_f32 v119, v119, v119
v_and_b32 v114, v118, v94
mva 22,50,248
s_waitcnt lgkmcnt(2)
v_and_b32 v115, v119, v95
v_and_b32 v116, v118, v96
mav 14,252,54
R 50,34624
v_and_b32 v117, v119, v97
mvv 18,128,54
mvv 22,54,132
v_mfma_f32_16x16x32_bf16 v[70:73], v[8:11], v[114:117], 0
v_mfma_f32_16x16x32_bf16 v[74:77], v[108:111], v[114:117], 0
mvv 14,136,38
R 54,34688
mvv 18,140,38
mvv 22,38,144
s_waitcnt lgkmcnt(2)
mvv 14,148,42
R 38,34752
v_cvt_pk_bf16_f32 v114, v70, v71
mvv 18,152,42
v_cvt_pk_bf16_f32 v115, v72, v73
v_cvt_pk_bf16_f32 v116, v74, v75
mvv 22,42,156
v_cvt_pk_bf16_f32 v117, v76, v77
mvv 14,160,46
buffer_store_dwordx4 v[114:117], v82, s[36:39], s49 offen sc1
R 42,34816
s_add_u32 s49, s49, 313344
mvv 18,164,46
mvv 22,46,168
s_waitcnt lgkmcnt(2)
mvv 14,172,50
R 46,34880
mvv 18,176,50
mvv 22,50,180
mvv 14,184,54
R 50,34944
mvv 18,188,54
mvv 22,54,192
s_waitcnt lgkmcnt(2)
mvv 14,196,38
R 54,35008
mvv 18,200,38
mvv 22,38,204
mvv 14,208,42
R 38,0
mvv 18,212,42
ds_read_b64 v[122:123], v83 offset:0
mvv 22,42,216
s_waitcnt lgkmcnt(3)
ds_read_b128 v[70:73], v84 offset:0
mvv 14,220,46
R 42,64
mvv 18,224,46
ds_read_b128 v[74:77], v84 offset:16
mvv 22,46,228
mvv 14,232,50
R 46,128
mvv 18,236,50
mvv 22,50,240
s_waitcnt lgkmcnt(3)
mvv 14,244,54
R 50,192
mvv 18,248,54
mvv 22,54,252
v_mfma_f32_16x16x32_bf16 v[26:29], a[0:3], v[38:41], 0
s_waitcnt lgkmcnt(1)
R 54,256
v_mul_f32 v71, v71, v99
v_mfma_f32_16x16x32_bf16 v[30:33], a[4:7], v[38:41], 0
v_mul_f32 v73, v73, v99
v_mul_f32 v75, v75, v99
v_mfma_f32_16x16x32_bf16 v[34:37], v[38:41], a[8:11], 0
v_fma_f32 v22, v22, v70, v71
v_fma_f32 v23, v23, v72, v73
mav 26,12,42
R 58,320
v_fma_f32 v24, v24, v74, v75
mav 30,16,42
v_mul_f32 v77, v77, v99
v_fma_f32 v25, v25, v76, v77
mva 34,42,20
v_cvt_pk_bf16_f32 v8, v22, v23
v_cvt_pk_bf16_f32 v9, v24, v25
mav 26,24,46
R 38,384
v_min_f32 v74, s56, v90
v_mfma_f32_16x16x32_bf16 v[70:73], v[104:107], v[0:3], v[86:89]
v_min_f32 v75, s56, v91
v_min_f32 v76, s56, v92
mav 30,28,46
v_min_f32 v77, s56, v93
v_mul_f32 v124, v123, v62
mva 34,46,32
s_waitcnt lgkmcnt(2)
v_fma_f32 v14, v14, v122, v124
v_mul_f32 v125, v123, v63
mvv 74,4,0
v_fma_f32 v15, v15, v122, v125
v_mul_f32 v124, v123, v64
mav 26,36,50
R 42,448
v_fma_f32 v16, v16, v122, v124
mav 30,40,50
v_mul_f32 v125, v123, v65
v_fma_f32 v17, v17, v122, v125
mva 34,50,44
v_max3_f32 v118, v70, v71, v74
v_max3_f32 v119, v72, v73, v76
mav 26,48,54
R 46,512
v_max_f32 v118, v118, v75
mav 30,52,54
v_max_f32 v119, v119, v77
v_max_f32 v120, v118, v119
mva 34,54,56
s_waitcnt lgkmcnt(2)
v_max_f32 v121, v118, v119
v_mul_f32 v124, v123, v66
mav 26,60,58
R 50,576
v_fma_f32 v18, v18, v122, v124
mav 30,64,58
v_permlane32_swap_b32 v120, v121
mva 34,58,68
v_max_f32 v114, v120, v121
v_max_f32 v115, v120, v121
mav 26,72,38
R 54,640
v_mul_f32 v125, v123, v67
mav 30,76,38
v_permlane16_swap_b32 v114, v115
mva 34,38,80
s_waitcnt lgkmcnt(2)
v_max_f32 v120, v114, v115
v_mul_f32 v120, s65, v120
mav 26,84,42
R 58,704
v_fma_f32 v118, v118, s65, -v120
mav 30,88,42
v_fma_f32 v119, v119, s65, -v120
v_fma_f32 v19, v19, v122, v125
mva 34,42,92
v_exp_f32 v118, v118
mav 26,96,46
R 38,768
v_mul_f32 v124, v123, v68
mav 30,100,46
s_barrier
mva 34,46,104
s_waitcnt lgkmcnt(2)
ds_read_b128 v[100:103], v82 offset:0
v_fma_f32 v20, v20, v122, v124
mav 26,108,50
ds_read_b64 v[108:109], v82 offset:1024
v_mul_f32 v125, v123, v69
mav 30,112,50
R 42,832
v_fma_f32 v21, v21, v122, v125
mva 34,50,116
v_exp_f32 v119, v119
mav 26,120,54
R 46,896
v_add_f32 v120, v118, v119
mav 30,124,54
v_add_f32 v121, v118, v119
v_cvt_pk_bf16_f32 v0, v14, v15
mva 34,54,128
s_waitcnt lgkmcnt(3)
v_permlane32_swap_b32 v120, v121
mav 26,132,58
R 50,960
v_add_f32 v114, v120, v121
mav 30,136,58
v_add_f32 v115, v120, v121
v_cvt_pk_bf16_f32 v1, v16, v17
mva 34,58,140
v_permlane16_swap_b32 v114, v115
mav 26,144,38
R 54,1024
v_add_f32 v120, v114, v115
mav 30,148,38
v_rcp_f32 v120, v120
mva 34,38,152
s_waitcnt lgkmcnt(2)
v_mul_f32 v118, v118, v120
v_mul_f32 v119, v119, v120
mav 26,156,42
R 38,1088
v_cvt_pk_bf16_f32 v118, v118, v118
mav 30,160,42
buffer_store_dwordx4 v[100:103], v82, s[36:39], s54 offen offset:2048 sc1
v_cvt_pk_bf16_f32 v119, v119, v119
v_and_b32 v114, v118, v94
v_and_b32 v115, v119, v95
mva 34,42,164
v_and_b32 v116, v118, v96
v_and_b32 v117, v119, v97
mav 26,168,46
buffer_store_dwordx2 v[8:9], v98, s[36:39], s54 offen offset:3072 sc1
R 42,1152
v_mfma_f32_16x16x32_bf16 v[70:73], v[10:13], v[114:117], 0
v_cvt_pk_bf16_f32 v2, v18, v19
v_cvt_pk_bf16_f32 v3, v20, v21
v_mfma_f32_16x16x32_bf16 v[74:77], v[110:113], v[114:117], 0
v_add_u32 v78, 1, v78
mav 30,172,46
s_waitcnt vmcnt(2)
mva 34,46,176
s_waitcnt lgkmcnt(2)
buffer_store_dword v78, off, s[36:39], s52 sc1
mav 26,180,50
R 46,1216
v_cvt_pk_bf16_f32 v114, v70, v71
mav 30,184,50
buffer_store_dwordx2 v[108:109], v98, s[36:39], s54 offen offset:3584 sc1
v_cvt_pk_bf16_f32 v115, v72, v73
mva 34,50,188
v_cvt_pk_bf16_f32 v116, v74, v75
v_cvt_pk_bf16_f32 v117, v76, v77
mav 26,192,54
buffer_store_dwordx4 v[114:117], v82, s[36:39], s49 offen sc1
R 50,1280
s_add_u32 s49, s49, 313344
mav 30,196,54
mva 34,54,200
s_waitcnt lgkmcnt(2)
mav 26,204,38
R 54,1344
mav 30,208,38
mva 34,38,212
LsQ5:
s_cbranch_execz LsQ9
mav 26,216,42
R 38,1408
mav 30,220,42
mva 34,42,224
s_waitcnt lgkmcnt(2)
mav 26,228,46
R 42,1472
mav 30,232,46
mva 34,46,236
mav 26,240,50
R 46,1536
mav 30,244,50
mva 34,50,248
s_waitcnt lgkmcnt(2)
mav 26,252,54
R 50,1600
mvv 30,128,54
mvv 34,54,132
mvv 26,136,38
R 54,1664
mvv 30,140,38
mvv 34,38,144
s_waitcnt lgkmcnt(2)
mvv 26,148,42
R 38,1728
mvv 30,152,42
mvv 34,42,156
mvv 26,160,46
R 42,1792
mvv 30,164,46
mvv 34,46,168
s_waitcnt lgkmcnt(2)
mvv 26,172,50
R 46,1856
mvv 30,176,50
mvv 34,50,180
mvv 26,184,54
R 50,1920
mvv 30,188,54
s_waitcnt lgkmcnt(2)
mvv 34,54,192
s_mov_b32 s60, -14
.p2align 6
LQA0_2:
mvv 26,196,38
R 54,1984
v_xor_b32 v80, v81, v80
mvv 30,200,38
mvv 34,38,204
mvv 26,208,42
R 38,33024
mvv 30,212,42
ds_read_b64 v[122:123], v83 offset:128
mvv 34,42,216
s_waitcnt lgkmcnt(3)
ds_read_b128 v[70:73], v84 offset:128
mvv 26,220,46
R 42,33088
mvv 30,224,46
ds_read_b128 v[74:77], v84 offset:144
mvv 34,46,228
mvv 26,232,50
R 46,33152
mvv 30,236,50
mvv 34,50,240
s_waitcnt lgkmcnt(3)
mvv 26,244,54
R 50,33216
mvv 30,248,54
mvv 34,54,252
v_mfma_f32_16x16x32_bf16 v[14:17], a[0:3], v[38:41], 0
s_waitcnt lgkmcnt(1)
R 54,33280
v_mul_f32 v71, v71, v99
v_mfma_f32_16x16x32_bf16 v[18:21], a[4:7], v[38:41], 0
v_mul_f32 v73, v73, v99
v_mul_f32 v75, v75, v99
v_mfma_f32_16x16x32_bf16 v[22:25], v[38:41], a[8:11], 0
v_fma_f32 v34, v34, v70, v71
v_fma_f32 v35, v35, v72, v73
mav 14,12,42
R 58,33344
v_fma_f32 v36, v36, v74, v75
mav 18,16,42
v_mul_f32 v77, v77, v99
v_fma_f32 v37, v37, v76, v77
mva 22,42,20
v_cvt_pk_bf16_f32 v10, v34, v35
v_cvt_pk_bf16_f32 v11, v36, v37
mav 14,24,46
R 38,33408
v_mul_f32 v124, v123, v62
v_mfma_f32_16x16x32_bf16 v[70:73], v[100:103], v[0:3], v[86:89]
v_fma_f32 v26, v26, v122, v124
v_mul_f32 v125, v123, v63
mav 18,28,46
v_fma_f32 v27, v27, v122, v125
v_mul_f32 v124, v123, v64
mva 22,46,32
s_waitcnt lgkmcnt(2)
v_fma_f32 v28, v28, v122, v124
v_mul_f32 v125, v123, v65
mav 14,36,50
R 42,33472
v_fma_f32 v29, v29, v122, v125
mav 18,40,50
v_mul_f32 v124, v123, v66
v_fma_f32 v30, v30, v122, v124
mva 22,50,44
v_mul_f32 v125, v123, v67
v_fma_f32 v31, v31, v122, v125
mav 14,48,54
R 46,33536
v_mul_f32 v124, v123, v68
mav 18,52,54
v_fma_f32 v32, v32, v122, v124
v_mul_f32 v125, v123, v69
mva 22,54,56
s_waitcnt lgkmcnt(2)
v_fma_f32 v33, v33, v122, v125
v_add_u32 v78, 1, v78
mav 14,60,58
R 50,33600
mav 18,64,58
mva 22,58,68
mav 14,72,38
R 54,33664
mav 18,76,38
mva 22,38,80
s_waitcnt lgkmcnt(2)
mav 14,84,42
R 58,33728
mav 18,88,42
mva 22,42,92
mav 14,96,46
R 38,33792
mav 18,100,46
s_barrier
mva 22,46,104
s_waitcnt lgkmcnt(2)
ds_read_b128 v[104:107], v82 offset:4096
mav 14,108,50
ds_read_b64 v[110:111], v82 offset:5120
mav 18,112,50
R 42,33856
mva 22,50,116
mav 14,120,54
s_waitcnt vmcnt(0)
buffer_store_dword v78, off, s[36:39], s52 sc1
mav 18,124,54
R 46,33920
mva 22,54,128
s_waitcnt lgkmcnt(3)
v_mfma_f32_16x16x32_bf16 v[74:77], v[104:107], v[0:3], v[90:93]
v_cvt_pk_bf16_f32 v0, v26, v27
v_cvt_pk_bf16_f32 v1, v28, v29
mav 14,132,58
R 50,33984
v_cvt_pk_bf16_f32 v2, v30, v31
mav 18,136,58
v_cvt_pk_bf16_f32 v3, v32, v33
mva 22,58,140
v_max3_f32 v118, v70, v71, v74
v_max3_f32 v119, v72, v73, v76
mav 14,144,38
R 54,34048
v_max_f32 v118, v118, v75
mav 18,148,38
v_max_f32 v119, v119, v77
v_max_f32 v120, v118, v119
mva 22,38,152
s_waitcnt lgkmcnt(2)
v_max_f32 v121, v118, v119
mav 14,156,42
R 38,34112
mav 18,160,42
v_permlane32_swap_b32 v120, v121
mva 22,42,164
v_max_f32 v114, v120, v121
v_max_f32 v115, v120, v121
mav 14,168,46
R 42,34176
mav 18,172,46
v_permlane16_swap_b32 v114, v115
mva 22,46,176
s_waitcnt lgkmcnt(2)
v_max_f32 v120, v114, v115
v_mul_f32 v120, s65, v120
mav 14,180,50
R 46,34240
v_fma_f32 v118, v118, s65, -v120
mav 18,184,50
v_fma_f32 v119, v119, s65, -v120
mva 22,50,188
v_exp_f32 v118, v118
mav 14,192,54
R 50,34304
mav 18,196,54
v_exp_f32 v119, v119
mva 22,54,200
s_waitcnt lgkmcnt(2)
v_add_f32 v120, v118, v119
v_add_f32 v121, v118, v119
mav 14,204,38
R 54,34368
mav 18,208,38
v_permlane32_swap_b32 v120, v121
mva 22,38,212
v_add_f32 v114, v120, v121
v_add_f32 v115, v120, v121
mav 14,216,42
R 38,34432
mav 18,220,42
v_permlane16_swap_b32 v114, v115
mva 22,42,224
s_waitcnt lgkmcnt(2)
v_add_f32 v120, v114, v115
mav 14,228,46
R 42,34496
mav 18,232,46
v_rcp_f32 v120, v120
mva 22,46,236
v_mul_f32 v118, v118, v120
v_mul_f32 v119, v119, v120
mav 14,240,50
R 46,34560
v_cvt_pk_bf16_f32 v118, v118, v118
mav 18,244,50
v_cvt_pk_bf16_f32 v119, v119, v119
v_and_b32 v114, v118, v94
mva 22,50,248
s_waitcnt lgkmcnt(2)
v_and_b32 v115, v119, v95
v_and_b32 v116, v118, v96
mav 14,252,54
R 50,34624
v_and_b32 v117, v119, v97
mvv 18,128,54
mvv 22,54,132
v_mfma_f32_16x16x32_bf16 v[70:73], v[8:11], v[114:117], 0
v_mfma_f32_16x16x32_bf16 v[74:77], v[108:111], v[114:117], 0
mvv 14,136,38
R 54,34688
mvv 18,140,38
mvv 22,38,144
s_waitcnt lgkmcnt(2)
mvv 14,148,42
R 38,34752
v_cvt_pk_bf16_f32 v114, v70, v71
mvv 18,152,42
v_cvt_pk_bf16_f32 v115, v72, v73
v_cvt_pk_bf16_f32 v116, v74, v75
mvv 22,42,156
v_cvt_pk_bf16_f32 v117, v76, v77
mvv 14,160,46
buffer_store_dwordx4 v[114:117], v82, s[36:39], s49 offen sc1
R 42,34816
s_add_u32 s49, s49, 313344
mvv 18,164,46
mvv 22,46,168
s_waitcnt lgkmcnt(2)
mvv 14,172,50
R 46,34880
mvv 18,176,50
mvv 22,50,180
mvv 14,184,54
R 50,34944
mvv 18,188,54
mvv 22,54,192
s_waitcnt lgkmcnt(2)
mvv 14,196,38
R 54,35008
mvv 18,200,38
mvv 22,38,204
mvv 14,208,42
R 38,0
mvv 18,212,42
ds_read_b64 v[122:123], v83 offset:0
mvv 22,42,216
s_waitcnt lgkmcnt(3)
ds_read_b128 v[70:73], v84 offset:0
mvv 14,220,46
R 42,64
mvv 18,224,46
ds_read_b128 v[74:77], v84 offset:16
mvv 22,46,228
mvv 14,232,50
R 46,128
mvv 18,236,50
mvv 22,50,240
s_waitcnt lgkmcnt(3)
mvv 14,244,54
R 50,192
mvv 18,248,54
mvv 22,54,252
v_mfma_f32_16x16x32_bf16 v[26:29], a[0:3], v[38:41], 0
s_waitcnt lgkmcnt(1)
R 54,256
v_mul_f32 v71, v71, v99
v_mfma_f32_16x16x32_bf16 v[30:33], a[4:7], v[38:41], 0
v_mul_f32 v73, v73, v99
v_mul_f32 v75, v75, v99
v_mfma_f32_16x16x32_bf16 v[34:37], v[38:41], a[8:11], 0
v_fma_f32 v22, v22, v70, v71
v_fma_f32 v23, v23, v72, v73
mav 26,12,42
R 58,320
v_fma_f32 v24, v24, v74, v75
mav 30,16,42
v_mul_f32 v77, v77, v99
v_fma_f32 v25, v25, v76, v77
mva 34,42,20
v_cvt_pk_bf16_f32 v8, v22, v23
v_cvt_pk_bf16_f32 v9, v24, v25
mav 26,24,46
R 38,384
v_mul_f32 v124, v123, v62
v_mfma_f32_16x16x32_bf16 v[70:73], v[104:107], v[0:3], v[86:89]
v_fma_f32 v14, v14, v122, v124
v_mul_f32 v125, v123, v63
mav 30,28,46
v_fma_f32 v15, v15, v122, v125
v_mul_f32 v124, v123, v64
mva 34,46,32
s_waitcnt lgkmcnt(2)
v_fma_f32 v16, v16, v122, v124
v_mul_f32 v125, v123, v65
mav 26,36,50
R 42,448
v_fma_f32 v17, v17, v122, v125
mav 30,40,50
v_mul_f32 v124, v123, v66
v_fma_f32 v18, v18, v122, v124
mva 34,50,44
v_mul_f32 v125, v123, v67
v_fma_f32 v19, v19, v122, v125
mav 26,48,54
R 46,512
v_mul_f32 v124, v123, v68
mav 30,52,54
v_fma_f32 v20, v20, v122, v124
v_mul_f32 v125, v123, v69
mva 34,54,56
s_waitcnt lgkmcnt(2)
v_fma_f32 v21, v21, v122, v125
v_add_u32 v78, 1, v78
mav 26,60,58
R 50,576
mav 30,64,58
mva 34,58,68
mav 26,72,38
R 54,640
mav 30,76,38
mva 34,38,80
s_waitcnt lgkmcnt(2)
mav 26,84,42
R 58,704
mav 30,88,42
mva 34,42,92
mav 26,96,46
R 38,768
mav 30,100,46
s_barrier
mva 34,46,104
s_waitcnt lgkmcnt(2)
ds_read_b128 v[100:103], v82 offset:0
mav 26,108,50
ds_read_b64 v[108:109], v82 offset:1024
mav 30,112,50
R 42,832
mva 34,50,116
mav 26,120,54
s_waitcnt vmcnt(0)
buffer_store_dword v78, off, s[36:39], s52 sc1
mav 30,124,54
R 46,896
mva 34,54,128
s_waitcnt lgkmcnt(3)
v_mfma_f32_16x16x32_bf16 v[74:77], v[100:103], v[0:3], v[90:93]
v_cvt_pk_bf16_f32 v0, v14, v15
v_cvt_pk_bf16_f32 v1, v16, v17
mav 26,132,58
R 50,960
v_cvt_pk_bf16_f32 v2, v18, v19
mav 30,136,58
v_cvt_pk_bf16_f32 v3, v20, v21
mva 34,58,140
v_max3_f32 v118, v70, v71, v74
v_max3_f32 v119, v72, v73, v76
mav 26,144,38
R 54,1024
v_max_f32 v118, v118, v75
mav 30,148,38
v_max_f32 v119, v119, v77
v_max_f32 v120, v118, v119
mva 34,38,152
s_waitcnt lgkmcnt(2)
v_max_f32 v121, v118, v119
mav 26,156,42
R 38,1088
mav 30,160,42
v_permlane32_swap_b32 v120, v121
mva 34,42,164
v_max_f32 v114, v120, v121
v_max_f32 v115, v120, v121
mav 26,168,46
R 42,1152
mav 30,172,46
v_permlane16_swap_b32 v114, v115
mva 34,46,176
s_waitcnt lgkmcnt(2)
v_max_f32 v120, v114, v115
v_mul_f32 v120, s65, v120
mav 26,180,50
R 46,1216
v_fma_f32 v118, v118, s65, -v120
mav 30,184,50
v_fma_f32 v119, v119, s65, -v120
mva 34,50,188
v_exp_f32 v118, v118
mav 26,192,54
R 50,1280
mav 30,196,54
v_exp_f32 v119, v119
mva 34,54,200
s_waitcnt lgkmcnt(2)
v_add_f32 v120, v118, v119
v_add_f32 v121, v118, v119
mav 26,204,38
R 54,1344
mav 30,208,38
v_permlane32_swap_b32 v120, v121
mva 34,38,212
v_add_f32 v114, v120, v121
v_add_f32 v115, v120, v121
mav 26,216,42
R 38,1408
mav 30,220,42
v_permlane16_swap_b32 v114, v115
mva 34,42,224
s_waitcnt lgkmcnt(2)
v_add_f32 v120, v114, v115
mav 26,228,46
R 42,1472
mav 30,232,46
v_rcp_f32 v120, v120
mva 34,46,236
v_mul_f32 v118, v118, v120
v_mul_f32 v119, v119, v120
mav 26,240,50
R 46,1536
v_cvt_pk_bf16_f32 v118, v118, v118
mav 30,244,50
v_cvt_pk_bf16_f32 v119, v119, v119
v_and_b32 v114, v118, v96
mva 34,50,248
s_waitcnt lgkmcnt(2)
v_and_b32 v115, v119, v97
v_and_b32 v116, v118, v94
mav 26,252,54
R 50,1600
v_and_b32 v117, v119, v95
mvv 30,128,54
mvv 34,54,132
v_mfma_f32_16x16x32_bf16 v[70:73], v[8:11], v[114:117], 0
v_mfma_f32_16x16x32_bf16 v[74:77], v[108:111], v[114:117], 0
mvv 26,136,38
R 54,1664
mvv 30,140,38
mvv 34,38,144
s_waitcnt lgkmcnt(2)
mvv 26,148,42
R 38,1728
v_cvt_pk_bf16_f32 v114, v70, v71
mvv 30,152,42
v_cvt_pk_bf16_f32 v115, v72, v73
v_cvt_pk_bf16_f32 v116, v74, v75
mvv 34,42,156
v_cvt_pk_bf16_f32 v117, v76, v77
mvv 26,160,46
buffer_store_dwordx4 v[114:117], v82, s[36:39], s49 offen sc1
R 42,1792
s_add_u32 s49, s49, 313344
mvv 30,164,46
mvv 34,46,168
s_waitcnt lgkmcnt(2)
mvv 26,172,50
R 46,1856
mvv 30,176,50
mvv 34,50,180
mvv 26,184,54
R 50,1920
mvv 30,188,54
s_waitcnt lgkmcnt(2)
s_add_u32 s60, s60, 1
mvv 34,54,192
s_cbranch_scc0 LQA0_2
mvv 26,196,38
R 54,1984
v_xor_b32 v80, v81, v80
mvv 30,200,38
mvv 34,38,204
mvv 26,208,42
R 38,33024
mvv 30,212,42
ds_read_b64 v[122:123], v83 offset:128
mvv 34,42,216
s_waitcnt lgkmcnt(3)
ds_read_b128 v[70:73], v84 offset:128
mvv 26,220,46
R 42,33088
mvv 30,224,46
ds_read_b128 v[74:77], v84 offset:144
mvv 34,46,228
mvv 26,232,50
R 46,33152
mvv 30,236,50
mvv 34,50,240
s_waitcnt lgkmcnt(3)
mvv 26,244,54
R 50,33216
mvv 30,248,54
mvv 34,54,252
v_mfma_f32_16x16x32_bf16 v[14:17], a[0:3], v[38:41], 0
s_waitcnt lgkmcnt(1)
R 54,33280
v_mul_f32 v71, v71, v99
v_mfma_f32_16x16x32_bf16 v[18:21], a[4:7], v[38:41], 0
v_mul_f32 v73, v73, v99
v_mul_f32 v75, v75, v99
v_mfma_f32_16x16x32_bf16 v[22:25], v[38:41], a[8:11], 0
v_fma_f32 v34, v34, v70, v71
v_fma_f32 v35, v35, v72, v73
mav 14,12,42
R 58,33344
v_fma_f32 v36, v36, v74, v75
mav 18,16,42
v_mul_f32 v77, v77, v99
v_fma_f32 v37, v37, v76, v77
mva 22,42,20
v_cvt_pk_bf16_f32 v10, v34, v35
v_cvt_pk_bf16_f32 v11, v36, v37
mav 14,24,46
R 38,33408
v_mul_f32 v124, v123, v62
v_mfma_f32_16x16x32_bf16 v[70:73], v[100:103], v[0:3], v[86:89]
v_fma_f32 v26, v26, v122, v124
v_mul_f32 v125, v123, v63
mav 18,28,46
v_fma_f32 v27, v27, v122, v125
v_mul_f32 v124, v123, v64
mva 22,46,32
s_waitcnt lgkmcnt(2)
v_fma_f32 v28, v28, v122, v124
v_mul_f32 v125, v123, v65
mav 14,36,50
R 42,33472
v_fma_f32 v29, v29, v122, v125
mav 18,40,50
v_mul_f32 v124, v123, v66
v_fma_f32 v30, v30, v122, v124
mva 22,50,44
v_mul_f32 v125, v123, v67
v_fma_f32 v31, v31, v122, v125
mav 14,48,54
R 46,33536
v_mul_f32 v124, v123, v68
mav 18,52,54
v_fma_f32 v32, v32, v122, v124
v_mul_f32 v125, v123, v69
mva 22,54,56
s_waitcnt lgkmcnt(2)
v_fma_f32 v33, v33, v122, v125
v_add_u32 v78, 1, v78
mav 14,60,58
R 50,33600
mav 18,64,58
mva 22,58,68
mav 14,72,38
R 54,33664
mav 18,76,38
mva 22,38,80
s_waitcnt lgkmcnt(2)
mav 14,84,42
R 58,33728
LsQ6:
s_cbranch_execz LsQ10
mav 18,88,42
mva 22,42,92
mav 14,96,46
R 38,33792
mav 18,100,46
s_barrier
mva 22,46,104
s_waitcnt lgkmcnt(2)
ds_read_b128 v[104:107], v82 offset:4096
mav 14,108,50
ds_read_b64 v[110:111], v82 offset:5120
mav 18,112,50
R 42,33856
mva 22,50,116
mav 14,120,54
s_waitcnt vmcnt(0)
buffer_store_dword v78, off, s[36:39], s52 sc1
mav 18,124,54
R 46,33920
mva 22,54,128
s_waitcnt lgkmcnt(3)
v_mfma_f32_16x16x32_bf16 v[74:77], v[104:107], v[0:3], v[90:93]
v_cvt_pk_bf16_f32 v0, v26, v27
v_cvt_pk_bf16_f32 v1, v28, v29
mav 14,132,58
R 50,33984
v_cvt_pk_bf16_f32 v2, v30, v31
mav 18,136,58
v_cvt_pk_bf16_f32 v3, v32, v33
mva 22,58,140
v_max3_f32 v118, v70, v71, v74
v_max3_f32 v119, v72, v73, v76
mav 14,144,38
R 54,34048
v_max_f32 v118, v118, v75
mav 18,148,38
v_max_f32 v119, v119, v77
v_max_f32 v120, v118, v119
mva 22,38,152
s_waitcnt lgkmcnt(2)
v_max_f32 v121, v118, v119
mav 14,156,42
R 38,34112
mav 18,160,42
v_permlane32_swap_b32 v120, v121
mva 22,42,164
v_max_f32 v114, v120, v121
v_max_f32 v115, v120, v121
mav 14,168,46
R 42,34176
mav 18,172,46
v_permlane16_swap_b32 v114, v115
mva 22,46,176
s_waitcnt lgkmcnt(2)
v_max_f32 v120, v114, v115
v_mul_f32 v120, s65, v120
mav 14,180,50
R 46,34240
v_fma_f32 v118, v118, s65, -v120
mav 18,184,50
v_fma_f32 v119, v119, s65, -v120
mva 22,50,188
v_exp_f32 v118, v118
mav 14,192,54
R 50,34304
mav 18,196,54
v_exp_f32 v119, v119
mva 22,54,200
s_waitcnt lgkmcnt(2)
v_add_f32 v120, v118, v119
v_add_f32 v121, v118, v119
mav 14,204,38
R 54,34368
mav 18,208,38
v_permlane32_swap_b32 v120, v121
mva 22,38,212
v_add_f32 v114, v120, v121
v_add_f32 v115, v120, v121
mav 14,216,42
R 38,34432
mav 18,220,42
v_permlane16_swap_b32 v114, v115
mva 22,42,224
s_waitcnt lgkmcnt(2)
v_add_f32 v120, v114, v115
mav 14,228,46
R 42,34496
mav 18,232,46
v_rcp_f32 v120, v120
mva 22,46,236
v_mul_f32 v118, v118, v120
v_mul_f32 v119, v119, v120
mav 14,240,50
R 46,34560
v_cvt_pk_bf16_f32 v118, v118, v118
mav 18,244,50
v_cvt_pk_bf16_f32 v119, v119, v119
v_and_b32 v114, v118, v94
mva 22,50,248
s_waitcnt lgkmcnt(2)
v_and_b32 v115, v119, v95
v_and_b32 v116, v118, v96
mav 14,252,54
R 50,34624
v_and_b32 v117, v119, v97
mvv 18,128,54
mvv 22,54,132
v_mfma_f32_16x16x32_bf16 v[70:73], v[8:11], v[114:117], 0
v_mfma_f32_16x16x32_bf16 v[74:77], v[108:111], v[114:117], 0
mvv 14,136,38
R 54,34688
mvv 18,140,38
mvv 22,38,144
s_waitcnt lgkmcnt(2)
mvv 14,148,42
R 38,34752
v_cvt_pk_bf16_f32 v114, v70, v71
mvv 18,152,42
v_cvt_pk_bf16_f32 v115, v72, v73
v_cvt_pk_bf16_f32 v116, v74, v75
mvv 22,42,156
v_cvt_pk_bf16_f32 v117, v76, v77
mvv 14,160,46
buffer_store_dwordx4 v[114:117], v82, s[36:39], s49 offen sc1
R 42,34816
s_add_u32 s49, s49, 313344
mvv 18,164,46
mvv 22,46,168
s_waitcnt lgkmcnt(2)
mvv 14,172,50
R 46,34880
mvv 18,176,50
mvv 22,50,180
mvv 14,184,54
R 50,34944
mvv 18,188,54
mvv 22,54,192
s_waitcnt lgkmcnt(2)
mvv 14,196,38
R 54,35008
mvv 18,200,38
mvv 22,38,204
mvv 14,208,42
R 38,0
mvv 18,212,42
ds_read_b64 v[122:123], v83 offset:0
mvv 22,42,216
s_waitcnt lgkmcnt(3)
ds_read_b128 v[70:73], v84 offset:0
mvv 14,220,46
R 42,64
mvv 18,224,46
ds_read_b128 v[74:77], v84 offset:16
mvv 22,46,228
mvv 14,232,50
R 46,128
mvv 18,236,50
mvv 22,50,240
s_waitcnt lgkmcnt(3)
mvv 14,244,54
R 50,192
mvv 18,248,54
mvv 22,54,252
v_mfma_f32_16x16x32_bf16 v[26:29], a[0:3], v[38:41], 0
s_waitcnt lgkmcnt(1)
R 54,256
v_mul_f32 v71, v71, v99
v_mfma_f32_16x16x32_bf16 v[30:33], a[4:7], v[38:41], 0
v_mul_f32 v73, v73, v99
v_mul_f32 v75, v75, v99
v_mfma_f32_16x16x32_bf16 v[34:37], v[38:41], a[8:11], 0
v_fma_f32 v22, v22, v70, v71
v_fma_f32 v23, v23, v72, v73
mav 26,12,42
R 58,320
v_fma_f32 v24, v24, v74, v75
mav 30,16,42
v_mul_f32 v77, v77, v99
v_fma_f32 v25, v25, v76, v77
mva 34,42,20
v_cvt_pk_bf16_f32 v8, v22, v23
v_cvt_pk_bf16_f32 v9, v24, v25
mav 26,24,46
R 38,384
v_mul_f32 v124, v123, v62
v_mfma_f32_16x16x32_bf16 v[70:73], v[104:107], v[0:3], v[86:89]
v_fma_f32 v14, v14, v122, v124
v_mul_f32 v125, v123, v63
mav 30,28,46
v_fma_f32 v15, v15, v122, v125
v_mul_f32 v124, v123, v64
mva 34,46,32
s_waitcnt lgkmcnt(2)
v_fma_f32 v16, v16, v122, v124
v_mul_f32 v125, v123, v65
mav 26,36,50
R 42,448
v_fma_f32 v17, v17, v122, v125
mav 30,40,50
buffer_load_dwordx4 v[4:7], v82, s[36:39], s55 offen offset:2048 sc1
mva 34,50,44
v_mul_f32 v124, v123, v66
v_fma_f32 v18, v18, v122, v124
mav 26,48,54
buffer_load_dwordx2 v[12:13], v98, s[36:39], s55 offen offset:3072 sc1
mav 30,52,54
R 46,512
v_mul_f32 v125, v123, v67
mva 34,54,56
s_waitcnt lgkmcnt(2)
buffer_load_dwordx2 v[112:113], v98, s[36:39], s55 offen offset:3584 sc1
mav 26,60,58
R 50,576
v_fma_f32 v19, v19, v122, v125
mav 30,64,58
v_mul_f32 v124, v123, v68
v_fma_f32 v20, v20, v122, v124
mva 34,58,68
v_mul_f32 v125, v123, v69
v_fma_f32 v21, v21, v122, v125
mav 26,72,38
R 54,640
v_add_u32 v78, 1, v78
mav 30,76,38
mva 34,38,80
s_waitcnt lgkmcnt(2)
mav 26,84,42
R 58,704
mav 30,88,42
mva 34,42,92
mav 26,96,46
R 38,768
mav 30,100,46
s_barrier
mva 34,46,104
s_waitcnt lgkmcnt(2)
ds_read_b128 v[100:103], v82 offset:0
mav 26,108,50
ds_read_b64 v[108:109], v82 offset:1024
mav 30,112,50
R 42,832
mva 34,50,116
mav 26,120,54
s_waitcnt vmcnt(3)
buffer_store_dword v78, off, s[36:39], s52 sc1
mav 30,124,54
R 46,896
mva 34,54,128
s_waitcnt lgkmcnt(3)
v_mfma_f32_16x16x32_bf16 v[74:77], v[100:103], v[0:3], v[90:93]
v_cvt_pk_bf16_f32 v0, v14, v15
v_cvt_pk_bf16_f32 v1, v16, v17
mav 26,132,58
R 50,960
v_cvt_pk_bf16_f32 v2, v18, v19
mav 30,136,58
v_cvt_pk_bf16_f32 v3, v20, v21
mva 34,58,140
v_max3_f32 v118, v70, v71, v74
v_max3_f32 v119, v72, v73, v76
mav 26,144,38
R 54,1024
v_max_f32 v118, v118, v75
mav 30,148,38
v_max_f32 v119, v119, v77
v_max_f32 v120, v118, v119
mva 34,38,152
s_waitcnt lgkmcnt(2)
v_max_f32 v121, v118, v119
mav 26,156,42
R 38,1088
mav 30,160,42
v_permlane32_swap_b32 v120, v121
mva 34,42,164
v_max_f32 v114, v120, v121
v_max_f32 v115, v120, v121
mav 26,168,46
R 42,1152
mav 30,172,46
v_permlane16_swap_b32 v114, v115
mva 34,46,176
s_waitcnt lgkmcnt(2)
v_max_f32 v120, v114, v115
v_mul_f32 v120, s65, v120
mav 26,180,50
R 46,1216
v_fma_f32 v118, v118, s65, -v120
mav 30,184,50
v_fma_f32 v119, v119, s65, -v120
mva 34,50,188
v_exp_f32 v118, v118
mav 26,192,54
R 50,1280
mav 30,196,54
v_exp_f32 v119, v119
mva 34,54,200
s_waitcnt lgkmcnt(2)
v_add_f32 v120, v118, v119
v_add_f32 v121, v118, v119
mav 26,204,38
R 54,1344
mav 30,208,38
v_permlane32_swap_b32 v120, v121
mva 34,38,212
v_add_f32 v114, v120, v121
v_add_f32 v115, v120, v121
mav 26,216,42
R 38,1408
mav 30,220,42
v_permlane16_swap_b32 v114, v115
mva 34,42,224
s_waitcnt lgkmcnt(2)
v_add_f32 v120, v114, v115
mav 26,228,46
R 42,1472
mav 30,232,46
v_rcp_f32 v120, v120
mva 34,46,236
v_mul_f32 v118, v118, v120
v_mul_f32 v119, v119, v120
mav 26,240,50
R 46,1536
v_cvt_pk_bf16_f32 v118, v118, v118
mav 30,244,50
v_cvt_pk_bf16_f32 v119, v119, v119
v_and_b32 v114, v118, v96
mva 34,50,248
s_waitcnt lgkmcnt(2)
v_and_b32 v115, v119, v97
v_and_b32 v116, v118, v94
mav 26,252,54
R 50,1600
v_and_b32 v117, v119, v95
mvv 30,128,54
mvv 34,54,132
v_mfma_f32_16x16x32_bf16 v[70:73], v[8:11], v[114:117], 0
v_mfma_f32_16x16x32_bf16 v[74:77], v[108:111], v[114:117], 0
mvv 26,136,38
R 54,1664
mvv 30,140,38
mvv 34,38,144
s_waitcnt lgkmcnt(2)
mvv 26,148,42
R 38,1728
v_cvt_pk_bf16_f32 v114, v70, v71
mvv 30,152,42
v_cvt_pk_bf16_f32 v115, v72, v73
v_cvt_pk_bf16_f32 v116, v74, v75
mvv 34,42,156
v_cvt_pk_bf16_f32 v117, v76, v77
mvv 26,160,46
buffer_store_dwordx4 v[114:117], v82, s[36:39], s49 offen sc1
R 42,1792
s_add_u32 s49, s49, 313344
mvv 30,164,46
mvv 34,46,168
s_waitcnt lgkmcnt(2)
mvv 26,172,50
R 46,1856
mvv 30,176,50
mvv 34,50,180
mvv 26,184,54
R 50,1920
mvv 30,188,54
s_waitcnt lgkmcnt(2)
mvv 34,54,192
mvv 26,196,38
R 54,1984
mvv 30,200,38
mvv 34,38,204
mvv 26,208,42
mvv 30,212,42
ds_read_b64 v[122:123], v83 offset:128
mvv 34,42,216
s_waitcnt lgkmcnt(2)
ds_read_b128 v[70:73], v84 offset:128
mvv 26,220,46
ds_read_b128 v[74:77], v84 offset:144
mvv 30,224,46
mvv 34,46,228
mvv 26,232,50
mvv 30,236,50
mvv 34,50,240
s_waitcnt lgkmcnt(1)
mvv 26,244,54
mvv 30,248,54
mvv 34,54,252
v_mul_f32 v71, v71, v99
s_nop 6
v_fma_f32 v34, v34, v70, v71
v_mul_f32 v73, v73, v99
v_fma_f32 v35, v35, v72, v73
s_waitcnt lgkmcnt(0)
v_mul_f32 v75, v75, v99
v_fma_f32 v36, v36, v74, v75
v_mul_f32 v77, v77, v99
v_fma_f32 v37, v37, v76, v77
v_cvt_pk_bf16_f32 v10, v34, v35
v_cvt_pk_bf16_f32 v11, v36, v37
s_nop 1
v_mfma_f32_16x16x32_bf16 v[70:73], v[100:103], v[0:3], v[86:89]
v_mul_f32 v124, v123, v62
v_fma_f32 v26, v26, v122, v124
v_mul_f32 v125, v123, v63
v_fma_f32 v27, v27, v122, v125
v_mul_f32 v124, v123, v64
v_fma_f32 v28, v28, v122, v124
v_mul_f32 v125, v123, v65
v_fma_f32 v29, v29, v122, v125
v_mul_f32 v124, v123, v66
v_fma_f32 v30, v30, v122, v124
v_mul_f32 v125, v123, v67
v_fma_f32 v31, v31, v122, v125
v_mul_f32 v124, v123, v68
v_fma_f32 v32, v32, v122, v124
v_mul_f32 v125, v123, v69
v_fma_f32 v33, v33, v122, v125
s_barrier
ds_read_b128 v[104:107], v82 offset:4096
ds_read_b64 v[110:111], v82 offset:5120
s_waitcnt lgkmcnt(0)
v_mfma_f32_16x16x32_bf16 v[74:77], v[104:107], v[0:3], v[90:93]
s_nop 7
v_max3_f32 v118, v70, v71, v74
v_max3_f32 v119, v72, v73, v76
v_max_f32 v118, v118, v75
v_max_f32 v119, v119, v77
v_max_f32 v120, v118, v119
v_max_f32 v121, v118, v119
s_nop 1
v_permlane32_swap_b32 v120, v121
v_max_f32 v114, v120, v121
v_max_f32 v115, v120, v121
s_nop 1
v_permlane16_swap_b32 v114, v115
v_max_f32 v120, v114, v115
v_mul_f32 v120, s65, v120
v_fma_f32 v118, v118, s65, -v120
v_fma_f32 v119, v119, s65, -v120
v_exp_f32 v118, v118
v_exp_f32 v119, v119
s_nop 0
v_add_f32 v120, v118, v119
v_add_f32 v121, v118, v119
s_nop 1
v_permlane32_swap_b32 v120, v121
v_add_f32 v114, v120, v121
v_add_f32 v115, v120, v121
s_nop 1
v_permlane16_swap_b32 v114, v115
v_add_f32 v120, v114, v115
v_rcp_f32 v120, v120
s_nop 0
v_mul_f32 v118, v118, v120
v_mul_f32 v119, v119, v120
v_cvt_pk_bf16_f32 v118, v118, v118
v_cvt_pk_bf16_f32 v119, v119, v119
v_and_b32 v114, v118, v94
v_and_b32 v115, v119, v95
v_and_b32 v116, v118, v96
v_and_b32 v117, v119, v97
s_nop 1
v_mfma_f32_16x16x32_bf16 v[70:73], v[8:11], v[114:117], 0
v_mfma_f32_16x16x32_bf16 v[74:77], v[108:111], v[114:117], 0
s_nop 6
v_cvt_pk_bf16_f32 v114, v70, v71
v_cvt_pk_bf16_f32 v115, v72, v73
v_cvt_pk_bf16_f32 v116, v74, v75
v_cvt_pk_bf16_f32 v117, v76, v77
buffer_store_dwordx4 v[114:117], v82, s[36:39], s49 offen sc1
s_add_u32 s49, s49, 313344
v_cvt_pk_bf16_f32 v0, v26, v27
v_cvt_pk_bf16_f32 v1, v28, v29
v_cvt_pk_bf16_f32 v2, v30, v31
v_cvt_pk_bf16_f32 v3, v32, v33
v_add_u32 v78, 1, v78
s_waitcnt vmcnt(1)
buffer_store_dword v78, off, s[36:39], s52 sc1
v_mfma_f32_16x16x32_bf16 v[70:73], v[104:107], v[0:3], v[86:89]
v_min_f32 v74, s56, v90
v_min_f32 v75, s56, v91
v_min_f32 v76, s56, v92
v_min_f32 v77, s56, v93
s_nop 1
mvv 74,4,0
s_nop 7
v_max3_f32 v118, v70, v71, v74
v_max3_f32 v119, v72, v73, v76
v_max_f32 v118, v118, v75
v_max_f32 v119, v119, v77
v_max_f32 v120, v118, v119
v_max_f32 v121, v118, v119
s_nop 1
v_permlane32_swap_b32 v120, v121
v_max_f32 v114, v120, v121
v_max_f32 v115, v120, v121
s_nop 1
v_permlane16_swap_b32 v114, v115
v_max_f32 v120, v114, v115
v_mul_f32 v120, s65, v120
v_fma_f32 v118, v118, s65, -v120
v_fma_f32 v119, v119, s65, -v120
v_exp_f32 v118, v118
v_exp_f32 v119, v119
s_nop 0
v_add_f32 v120, v118, v119
v_add_f32 v121, v118, v119
s_nop 1
v_permlane32_swap_b32 v120, v121
v_add_f32 v114, v120, v121
v_add_f32 v115, v120, v121
s_nop 1
v_permlane16_swap_b32 v114, v115
v_add_f32 v120, v114, v115
v_rcp_f32 v120, v120
s_nop 0
v_mul_f32 v118, v118, v120
v_mul_f32 v119, v119, v120
v_cvt_pk_bf16_f32 v118, v118, v118
v_cvt_pk_bf16_f32 v119, v119, v119
v_and_b32 v114, v118, v94
v_and_b32 v115, v119, v95
v_and_b32 v116, v118, v96
v_and_b32 v117, v119, v97
s_nop 1
v_mfma_f32_16x16x32_bf16 v[70:73], v[10:13], v[114:117], 0
v_mfma_f32_16x16x32_bf16 v[74:77], v[110:113], v[114:117], 0
s_nop 6
v_cvt_pk_bf16_f32 v114, v70, v71
v_cvt_pk_bf16_f32 v115, v72, v73
v_cvt_pk_bf16_f32 v116, v74, v75
v_cvt_pk_bf16_f32 v117, v76, v77
buffer_store_dwordx4 v[114:117], v82, s[36:39], s49 offen sc1
v_add_u32 v78, 1, v78
s_waitcnt vmcnt(2)
buffer_store_dword v78, off, s[36:39], s52 sc1
v_add_u32 v78, 37, v78
s_waitcnt vmcnt(1)
buffer_store_dword v78, off, s[36:39], s52 sc1
s_endpgm
LvQA1:
LsQ7:
s_cbranch_execz LsQ11
ds_read_b64 v[122:123], v83 offset:0
ds_read_b128 v[70:73], v84 offset:0
ds_read_b128 v[74:77], v84 offset:16
s_add_u32 s49, s49, 313344
s_waitcnt lgkmcnt(0)
v_mul_f32 v71, v71, v99
v_fma_f32 v22, v22, v70, v71
v_mul_f32 v73, v73, v99
v_fma_f32 v23, v23, v72, v73
v_mul_f32 v75, v75, v99
v_fma_f32 v24, v24, v74, v75
v_mul_f32 v77, v77, v99
v_fma_f32 v25, v25, v76, v77
v_cvt_pk_bf16_f32 v8, v22, v23
v_cvt_pk_bf16_f32 v9, v24, v25
v_mul_f32 v124, v123, v62
v_fma_f32 v14, v14, v122, v124
v_mul_f32 v125, v123, v63
v_fma_f32 v15, v15, v122, v125
v_mul_f32 v124, v123, v64
v_fma_f32 v16, v16, v122, v124
v_mul_f32 v125, v123, v65
v_fma_f32 v17, v17, v122, v125
v_mul_f32 v124, v123, v66
v_fma_f32 v18, v18, v122, v124
v_mul_f32 v125, v123, v67
v_fma_f32 v19, v19, v122, v125
v_mul_f32 v124, v123, v68
v_fma_f32 v20, v20, v122, v124
v_mul_f32 v125, v123, v69
v_fma_f32 v21, v21, v122, v125
v_cvt_pk_bf16_f32 v0, v14, v15
v_cvt_pk_bf16_f32 v1, v16, v17
v_cvt_pk_bf16_f32 v2, v18, v19
v_cvt_pk_bf16_f32 v3, v20, v21
s_add_u32 s49, s49, 313344
s_barrier
ds_read_b128 v[100:103], v82 offset:0
ds_read_b64 v[108:109], v82 offset:1024
R 38,33024
R 42,33088
R 46,33152
R 50,33216
ds_read_b64 v[122:123], v83 offset:128
ds_read_b128 v[70:73], v84 offset:128
ds_read_b128 v[74:77], v84 offset:144
s_waitcnt lgkmcnt(0)
v_mfma_f32_16x16x32_bf16 v[14:17], a[0:3], v[38:41], 0
R 54,33280
v_mul_f32 v71, v71, v99
v_mfma_f32_16x16x32_bf16 v[18:21], a[4:7], v[38:41], 0
v_fma_f32 v34, v34, v70, v71
v_mul_f32 v73, v73, v99
v_mfma_f32_16x16x32_bf16 v[22:25], v[38:41], a[8:11], 0
v_fma_f32 v35, v35, v72, v73
v_mul_f32 v75, v75, v99
mav 14,12,42
R 58,33344
v_fma_f32 v36, v36, v74, v75
mav 18,16,42
v_mul_f32 v77, v77, v99
v_fma_f32 v37, v37, v76, v77
mva 22,42,20
v_cvt_pk_bf16_f32 v10, v34, v35
v_cvt_pk_bf16_f32 v11, v36, v37
mav 14,24,46
R 38,33408
v_mul_f32 v124, v123, v62
v_mfma_f32_16x16x32_bf16 v[70:73], v[100:103], v[0:3], v[86:89]
v_fma_f32 v26, v26, v122, v124
v_mul_f32 v125, v123, v63
mav 18,28,46
v_fma_f32 v27, v27, v122, v125
v_mul_f32 v124, v123, v64
mva 22,46,32
v_fma_f32 v28, v28, v122, v124
v_mul_f32 v125, v123, v65
mav 14,36,50
R 42,33472
v_fma_f32 v29, v29, v122, v125
LsQ8:
s_cbranch_execz LsQ12
mav 18,40,50
v_mul_f32 v124, v123, v66
v_fma_f32 v30, v30, v122, v124
mva 22,50,44
s_waitcnt lgkmcnt(2)
v_mul_f32 v125, v123, v67
v_fma_f32 v31, v31, v122, v125
mav 14,48,54
R 46,33536
v_mul_f32 v124, v123, v68
mav 18,52,54
v_fma_f32 v32, v32, v122, v124
v_mul_f32 v125, v123, v69
mva 22,54,56
v_fma_f32 v33, v33, v122, v125
mav 14,60,58
R 50,33600
mav 18,64,58
mva 22,58,68
s_waitcnt lgkmcnt(2)
mav 14,72,38
R 54,33664
mav 18,76,38
buffer_store_dwordx4 v[100:103], v82, s[36:39], s54 offen sc1
mva 22,38,80
mav 14,84,42
buffer_store_dwordx2 v[8:9], v98, s[36:39], s54 offen offset:1024 sc1
R 58,33728
mav 18,88,42
mva 22,42,92
s_waitcnt lgkmcnt(2)
buffer_store_dwordx2 v[108:109], v98, s[36:39], s54 offen offset:1536 sc1
mav 14,96,46
R 38,33792
mav 18,100,46
s_barrier
mva 22,46,104
ds_read_b128 v[104:107], v82 offset:4096
mav 14,108,50
ds_read_b64 v[110:111], v82 offset:5120
mav 18,112,50
R 42,33856
mva 22,50,116
s_waitcnt lgkmcnt(4)
mav 14,120,54
R 46,33920
mav 18,124,54
mva 22,54,128
mav 14,132,58
s_waitcnt lgkmcnt(2)
R 50,33984
v_mfma_f32_16x16x32_bf16 v[74:77], v[104:107], v[0:3], v[90:93]
v_cvt_pk_bf16_f32 v0, v26, v27
v_cvt_pk_bf16_f32 v1, v28, v29
mav 18,136,58
v_cvt_pk_bf16_f32 v2, v30, v31
v_cvt_pk_bf16_f32 v3, v32, v33
mva 22,58,140
mav 14,144,38
R 54,34048
v_max3_f32 v118, v70, v71, v74
mav 18,148,38
v_max3_f32 v119, v72, v73, v76
v_max_f32 v118, v118, v75
mva 22,38,152
s_waitcnt lgkmcnt(2)
v_max_f32 v119, v119, v77
v_max_f32 v120, v118, v119
mav 14,156,42
R 38,34112
v_max_f32 v121, v118, v119
mav 18,160,42
mva 22,42,164
v_permlane32_swap_b32 v120, v121
mav 14,168,46
R 42,34176
v_max_f32 v114, v120, v121
mav 18,172,46
v_max_f32 v115, v120, v121
mva 22,46,176
s_waitcnt lgkmcnt(2)
v_permlane16_swap_b32 v114, v115
mav 14,180,50
R 46,34240
v_max_f32 v120, v114, v115
mav 18,184,50
v_mul_f32 v120, s65, v120
v_fma_f32 v118, v118, s65, -v120
mva 22,50,188
v_fma_f32 v119, v119, s65, -v120
mav 14,192,54
R 50,34304
mav 18,196,54
v_exp_f32 v118, v118
mva 22,54,200
s_waitcnt lgkmcnt(2)
v_exp_f32 v119, v119
mav 14,204,38
R 54,34368
v_add_f32 v120, v118, v119
mav 18,208,38
v_add_f32 v121, v118, v119
mva 22,38,212
mav 14,216,42
R 38,34432
mav 18,220,42
v_permlane32_swap_b32 v120, v121
mva 22,42,224
s_waitcnt lgkmcnt(2)
v_add_f32 v114, v120, v121
v_add_f32 v115, v120, v121
mav 14,228,46
R 42,34496
mav 18,232,46
v_permlane16_swap_b32 v114, v115
mva 22,46,236
v_add_f32 v120, v114, v115
mav 14,240,50
R 46,34560
mav 18,244,50
v_rcp_f32 v120, v120
mva 22,50,248
s_waitcnt lgkmcnt(2)
v_mul_f32 v118, v118, v120
v_mul_f32 v119, v119, v120
mav 14,252,54
R 50,34624
v_cvt_pk_bf16_f32 v118, v118, v118
mvv 18,128,54
v_cvt_pk_bf16_f32 v119, v119, v119
v_and_b32 v114, v118, v94
mvv 22,54,132
v_and_b32 v115, v119, v95
v_and_b32 v116, v118, v96
)"
R"(
mvv 14,136,38
R 54,34688
v_and_b32 v117, v119, v97
mvv 18,140,38
mvv 22,38,144
s_waitcnt lgkmcnt(2)
v_mfma_f32_16x16x32_bf16 v[70:73], v[8:11], v[114:117], 0
v_mfma_f32_16x16x32_bf16 v[74:77], v[108:111], v[114:117], 0
mvv 14,148,42
R 38,34752
mvv 18,152,42
mvv 22,42,156
mvv 14,160,46
R 42,34816
mvv 18,164,46
v_cvt_pk_bf16_f32 v114, v70, v71
v_cvt_pk_bf16_f32 v115, v72, v73
mvv 22,46,168
s_waitcnt lgkmcnt(2)
v_cvt_pk_bf16_f32 v116, v74, v75
v_cvt_pk_bf16_f32 v117, v76, v77
mvv 14,172,50
buffer_store_dwordx4 v[114:117], v82, s[36:39], s49 offen sc1
R 46,34880
s_add_u32 s49, s49, 313344
mvv 18,176,50
mvv 22,50,180
mvv 14,184,54
R 50,34944
mvv 18,188,54
mvv 22,54,192
s_waitcnt lgkmcnt(2)
mvv 14,196,38
R 54,35008
mvv 18,200,38
mvv 22,38,204
mvv 14,208,42
R 38,0
mvv 18,212,42
ds_read_b64 v[122:123], v83 offset:0
mvv 22,42,216
s_waitcnt lgkmcnt(3)
ds_read_b128 v[70:73], v84 offset:0
mvv 14,220,46
R 42,64
mvv 18,224,46
ds_read_b128 v[74:77], v84 offset:16
mvv 22,46,228
mvv 14,232,50
R 46,128
mvv 18,236,50
mvv 22,50,240
s_waitcnt lgkmcnt(3)
mvv 14,244,54
R 50,192
mvv 18,248,54
mvv 22,54,252
v_mfma_f32_16x16x32_bf16 v[26:29], a[0:3], v[38:41], 0
s_waitcnt lgkmcnt(1)
R 54,256
v_mul_f32 v71, v71, v99
v_mfma_f32_16x16x32_bf16 v[30:33], a[4:7], v[38:41], 0
v_mul_f32 v73, v73, v99
v_mul_f32 v75, v75, v99
v_mfma_f32_16x16x32_bf16 v[34:37], v[38:41], a[8:11], 0
v_fma_f32 v22, v22, v70, v71
v_fma_f32 v23, v23, v72, v73
mav 26,12,42
R 58,320
v_fma_f32 v24, v24, v74, v75
mav 30,16,42
v_mul_f32 v77, v77, v99
v_fma_f32 v25, v25, v76, v77
mva 34,42,20
v_cvt_pk_bf16_f32 v8, v22, v23
v_cvt_pk_bf16_f32 v9, v24, v25
mav 26,24,46
R 38,384
v_mul_f32 v124, v123, v62
v_mfma_f32_16x16x32_bf16 v[70:73], v[104:107], v[0:3], v[86:89]
v_fma_f32 v14, v14, v122, v124
v_mul_f32 v125, v123, v63
mav 30,28,46
v_fma_f32 v15, v15, v122, v125
v_mul_f32 v124, v123, v64
mva 34,46,32
s_waitcnt lgkmcnt(2)
v_fma_f32 v16, v16, v122, v124
v_mul_f32 v125, v123, v65
mav 26,36,50
R 42,448
v_fma_f32 v17, v17, v122, v125
mav 30,40,50
v_mul_f32 v124, v123, v66
v_fma_f32 v18, v18, v122, v124
mva 34,50,44
v_mul_f32 v125, v123, v67
v_fma_f32 v19, v19, v122, v125
mav 26,48,54
R 46,512
v_mul_f32 v124, v123, v68
mav 30,52,54
v_fma_f32 v20, v20, v122, v124
v_mul_f32 v125, v123, v69
mva 34,54,56
s_waitcnt lgkmcnt(2)
v_fma_f32 v21, v21, v122, v125
v_add_u32 v78, 1, v78
mav 26,60,58
R 50,576
mav 30,64,58
mva 34,58,68
mav 26,72,38
R 54,640
mav 30,76,38
mva 34,38,80
s_waitcnt lgkmcnt(2)
mav 26,84,42
R 58,704
mav 30,88,42
mva 34,42,92
mav 26,96,46
R 38,768
mav 30,100,46
s_barrier
mva 34,46,104
s_waitcnt lgkmcnt(2)
ds_read_b128 v[100:103], v82 offset:0
mav 26,108,50
ds_read_b64 v[108:109], v82 offset:1024
mav 30,112,50
R 42,832
mva 34,50,116
mav 26,120,54
s_waitcnt vmcnt(0)
R 46,896
mav 30,124,54
buffer_store_dword v78, off, s[36:39], s52 sc1
mva 34,54,128
s_waitcnt lgkmcnt(3)
v_mfma_f32_16x16x32_bf16 v[74:77], v[100:103], v[0:3], v[90:93]
v_cvt_pk_bf16_f32 v0, v14, v15
v_cvt_pk_bf16_f32 v1, v16, v17
mav 26,132,58
R 50,960
v_cvt_pk_bf16_f32 v2, v18, v19
mav 30,136,58
v_cvt_pk_bf16_f32 v3, v20, v21
mva 34,58,140
v_max3_f32 v118, v70, v71, v74
v_max3_f32 v119, v72, v73, v76
mav 26,144,38
R 54,1024
v_max_f32 v118, v118, v75
mav 30,148,38
v_max_f32 v119, v119, v77
v_max_f32 v120, v118, v119
mva 34,38,152
s_waitcnt lgkmcnt(2)
v_max_f32 v121, v118, v119
mav 26,156,42
R 38,1088
mav 30,160,42
v_permlane32_swap_b32 v120, v121
mva 34,42,164
v_max_f32 v114, v120, v121
v_max_f32 v115, v120, v121
mav 26,168,46
R 42,1152
mav 30,172,46
v_permlane16_swap_b32 v114, v115
mva 34,46,176
s_waitcnt lgkmcnt(2)
v_max_f32 v120, v114, v115
v_mul_f32 v120, s65, v120
mav 26,180,50
R 46,1216
v_fma_f32 v118, v118, s65, -v120
mav 30,184,50
v_fma_f32 v119, v119, s65, -v120
mva 34,50,188
v_exp_f32 v118, v118
mav 26,192,54
R 50,1280
mav 30,196,54
v_exp_f32 v119, v119
mva 34,54,200
s_waitcnt lgkmcnt(2)
v_add_f32 v120, v118, v119
v_add_f32 v121, v118, v119
mav 26,204,38
R 54,1344
mav 30,208,38
v_permlane32_swap_b32 v120, v121
mva 34,38,212
v_add_f32 v114, v120, v121
v_add_f32 v115, v120, v121
mav 26,216,42
R 38,1408
mav 30,220,42
v_permlane16_swap_b32 v114, v115
mva 34,42,224
s_waitcnt lgkmcnt(2)
v_add_f32 v120, v114, v115
mav 26,228,46
R 42,1472
mav 30,232,46
v_rcp_f32 v120, v120
mva 34,46,236
v_mul_f32 v118, v118, v120
v_mul_f32 v119, v119, v120
mav 26,240,50
R 46,1536
v_cvt_pk_bf16_f32 v118, v118, v118
mav 30,244,50
v_cvt_pk_bf16_f32 v119, v119, v119
v_and_b32 v114, v118, v96
mva 34,50,248
s_waitcnt lgkmcnt(2)
v_and_b32 v115, v119, v97
v_and_b32 v116, v118, v94
mav 26,252,54
R 50,1600
v_and_b32 v117, v119, v95
mvv 30,128,54
mvv 34,54,132
v_mfma_f32_16x16x32_bf16 v[70:73], v[8:11], v[114:117], 0
v_mfma_f32_16x16x32_bf16 v[74:77], v[108:111], v[114:117], 0
mvv 26,136,38
R 54,1664
mvv 30,140,38
mvv 34,38,144
s_waitcnt lgkmcnt(2)
mvv 26,148,42
R 38,1728
v_cvt_pk_bf16_f32 v114, v70, v71
mvv 30,152,42
v_cvt_pk_bf16_f32 v115, v72, v73
v_cvt_pk_bf16_f32 v116, v74, v75
mvv 34,42,156
v_cvt_pk_bf16_f32 v117, v76, v77
mvv 26,160,46
R 42,1792
mvv 30,164,46
buffer_store_dwordx4 v[114:117], v82, s[36:39], s49 offen sc1
s_add_u32 s49, s49, 313344
mvv 34,46,168
s_waitcnt lgkmcnt(2)
mvv 26,172,50
R 46,1856
mvv 30,176,50
mvv 34,50,180
mvv 26,184,54
R 50,1920
mvv 30,188,54
s_waitcnt lgkmcnt(2)
mvv 34,54,192
s_mov_b32 s60, -13
.p2align 6
LQA1_1:
mvv 26,196,38
R 54,1984
v_xor_b32 v80, v81, v80
mvv 30,200,38
mvv 34,38,204
mvv 26,208,42
R 38,33024
mvv 30,212,42
ds_read_b64 v[122:123], v83 offset:128
mvv 34,42,216
s_waitcnt lgkmcnt(3)
ds_read_b128 v[70:73], v84 offset:128
mvv 26,220,46
R 42,33088
mvv 30,224,46
ds_read_b128 v[74:77], v84 offset:144
mvv 34,46,228
mvv 26,232,50
R 46,33152
mvv 30,236,50
mvv 34,50,240
s_waitcnt lgkmcnt(3)
mvv 26,244,54
R 50,33216
mvv 30,248,54
mvv 34,54,252
v_mfma_f32_16x16x32_bf16 v[14:17], a[0:3], v[38:41], 0
s_waitcnt lgkmcnt(1)
R 54,33280
v_mul_f32 v71, v71, v99
v_mfma_f32_16x16x32_bf16 v[18:21], a[4:7], v[38:41], 0
v_mul_f32 v73, v73, v99
v_mul_f32 v75, v75, v99
v_mfma_f32_16x16x32_bf16 v[22:25], v[38:41], a[8:11], 0
v_fma_f32 v34, v34, v70, v71
v_fma_f32 v35, v35, v72, v73
mav 14,12,42
R 58,33344
v_fma_f32 v36, v36, v74, v75
mav 18,16,42
v_mul_f32 v77, v77, v99
v_fma_f32 v37, v37, v76, v77
mva 22,42,20
v_cvt_pk_bf16_f32 v10, v34, v35
v_cvt_pk_bf16_f32 v11, v36, v37
mav 14,24,46
R 38,33408
v_mul_f32 v124, v123, v62
v_mfma_f32_16x16x32_bf16 v[70:73], v[100:103], v[0:3], v[86:89]
v_fma_f32 v26, v26, v122, v124
v_mul_f32 v125, v123, v63
mav 18,28,46
v_fma_f32 v27, v27, v122, v125
v_mul_f32 v124, v123, v64
mva 22,46,32
s_waitcnt lgkmcnt(2)
v_fma_f32 v28, v28, v122, v124
v_mul_f32 v125, v123, v65
mav 14,36,50
R 42,33472
v_fma_f32 v29, v29, v122, v125
mav 18,40,50
v_mul_f32 v124, v123, v66
v_fma_f32 v30, v30, v122, v124
mva 22,50,44
v_mul_f32 v125, v123, v67
v_fma_f32 v31, v31, v122, v125
mav 14,48,54
R 46,33536
v_mul_f32 v124, v123, v68
mav 18,52,54
v_fma_f32 v32, v32, v122, v124
v_mul_f32 v125, v123, v69
mva 22,54,56
s_waitcnt lgkmcnt(2)
v_fma_f32 v33, v33, v122, v125
v_add_u32 v78, 1, v78
mav 14,60,58
R 50,33600
mav 18,64,58
mva 22,58,68
mav 14,72,38
R 54,33664
mav 18,76,38
mva 22,38,80
s_waitcnt lgkmcnt(2)
mav 14,84,42
R 58,33728
mav 18,88,42
mva 22,42,92
mav 14,96,46
R 38,33792
mav 18,100,46
s_barrier
mva 22,46,104
s_waitcnt lgkmcnt(2)
ds_read_b128 v[104:107], v82 offset:4096
mav 14,108,50
ds_read_b64 v[110:111], v82 offset:5120
mav 18,112,50
R 42,33856
mva 22,50,116
mav 14,120,54
s_waitcnt vmcnt(0)
R 46,33920
mav 18,124,54
buffer_store_dword v78, off, s[36:39], s52 sc1
mva 22,54,128
s_waitcnt lgkmcnt(3)
v_mfma_f32_16x16x32_bf16 v[74:77], v[104:107], v[0:3], v[90:93]
v_cvt_pk_bf16_f32 v0, v26, v27
v_cvt_pk_bf16_f32 v1, v28, v29
mav 14,132,58
R 50,33984
v_cvt_pk_bf16_f32 v2, v30, v31
mav 18,136,58
v_cvt_pk_bf16_f32 v3, v32, v33
mva 22,58,140
v_max3_f32 v118, v70, v71, v74
v_max3_f32 v119, v72, v73, v76
mav 14,144,38
R 54,34048
v_max_f32 v118, v118, v75
mav 18,148,38
v_max_f32 v119, v119, v77
v_max_f32 v120, v118, v119
mva 22,38,152
s_waitcnt lgkmcnt(2)
v_max_f32 v121, v118, v119
mav 14,156,42
R 38,34112
mav 18,160,42
v_permlane32_swap_b32 v120, v121
mva 22,42,164
v_max_f32 v114, v120, v121
v_max_f32 v115, v120, v121
mav 14,168,46
R 42,34176
mav 18,172,46
v_permlane16_swap_b32 v114, v115
mva 22,46,176
s_waitcnt lgkmcnt(2)
v_max_f32 v120, v114, v115
v_mul_f32 v120, s65, v120
mav 14,180,50
R 46,34240
v_fma_f32 v118, v118, s65, -v120
mav 18,184,50
v_fma_f32 v119, v119, s65, -v120
mva 22,50,188
v_exp_f32 v118, v118
mav 14,192,54
R 50,34304
mav 18,196,54
v_exp_f32 v119, v119
mva 22,54,200
s_waitcnt lgkmcnt(2)
v_add_f32 v120, v118, v119
v_add_f32 v121, v118, v119
mav 14,204,38
R 54,34368
mav 18,208,38
v_permlane32_swap_b32 v120, v121
mva 22,38,212
v_add_f32 v114, v120, v121
v_add_f32 v115, v120, v121
mav 14,216,42
R 38,34432
mav 18,220,42
v_permlane16_swap_b32 v114, v115
mva 22,42,224
s_waitcnt lgkmcnt(2)
v_add_f32 v120, v114, v115
mav 14,228,46
R 42,34496
mav 18,232,46
v_rcp_f32 v120, v120
mva 22,46,236
v_mul_f32 v118, v118, v120
v_mul_f32 v119, v119, v120
mav 14,240,50
R 46,34560
v_cvt_pk_bf16_f32 v118, v118, v118
mav 18,244,50
v_cvt_pk_bf16_f32 v119, v119, v119
v_and_b32 v114, v118, v94
mva 22,50,248
s_waitcnt lgkmcnt(2)
v_and_b32 v115, v119, v95
v_and_b32 v116, v118, v96
mav 14,252,54
R 50,34624
v_and_b32 v117, v119, v97
mvv 18,128,54
mvv 22,54,132
v_mfma_f32_16x16x32_bf16 v[70:73], v[8:11], v[114:117], 0
LsQ9:
s_cbranch_execz LsQ13
v_mfma_f32_16x16x32_bf16 v[74:77], v[108:111], v[114:117], 0
mvv 14,136,38
R 54,34688
mvv 18,140,38
mvv 22,38,144
s_waitcnt lgkmcnt(2)
mvv 14,148,42
R 38,34752
v_cvt_pk_bf16_f32 v114, v70, v71
mvv 18,152,42
v_cvt_pk_bf16_f32 v115, v72, v73
v_cvt_pk_bf16_f32 v116, v74, v75
mvv 22,42,156
v_cvt_pk_bf16_f32 v117, v76, v77
mvv 14,160,46
R 42,34816
mvv 18,164,46
buffer_store_dwordx4 v[114:117], v82, s[36:39], s49 offen sc1
s_add_u32 s49, s49, 313344
mvv 22,46,168
s_waitcnt lgkmcnt(2)
mvv 14,172,50
R 46,34880
mvv 18,176,50
mvv 22,50,180
mvv 14,184,54
R 50,34944
mvv 18,188,54
mvv 22,54,192
s_waitcnt lgkmcnt(2)
mvv 14,196,38
R 54,35008
mvv 18,200,38
mvv 22,38,204
mvv 14,208,42
R 38,0
mvv 18,212,42
ds_read_b64 v[122:123], v83 offset:0
mvv 22,42,216
s_waitcnt lgkmcnt(3)
ds_read_b128 v[70:73], v84 offset:0
mvv 14,220,46
R 42,64
mvv 18,224,46
ds_read_b128 v[74:77], v84 offset:16
mvv 22,46,228
mvv 14,232,50
R 46,128
mvv 18,236,50
mvv 22,50,240
s_waitcnt lgkmcnt(3)
mvv 14,244,54
R 50,192
mvv 18,248,54
mvv 22,54,252
v_mfma_f32_16x16x32_bf16 v[26:29], a[0:3], v[38:41], 0
s_waitcnt lgkmcnt(1)
R 54,256
v_mul_f32 v71, v71, v99
v_mfma_f32_16x16x32_bf16 v[30:33], a[4:7], v[38:41], 0
v_mul_f32 v73, v73, v99
v_mul_f32 v75, v75, v99
v_mfma_f32_16x16x32_bf16 v[34:37], v[38:41], a[8:11], 0
v_fma_f32 v22, v22, v70, v71
v_fma_f32 v23, v23, v72, v73
mav 26,12,42
R 58,320
v_fma_f32 v24, v24, v74, v75
mav 30,16,42
v_mul_f32 v77, v77, v99
v_fma_f32 v25, v25, v76, v77
mva 34,42,20
v_cvt_pk_bf16_f32 v8, v22, v23
v_cvt_pk_bf16_f32 v9, v24, v25
mav 26,24,46
R 38,384
v_mul_f32 v124, v123, v62
v_mfma_f32_16x16x32_bf16 v[70:73], v[104:107], v[0:3], v[86:89]
v_fma_f32 v14, v14, v122, v124
v_mul_f32 v125, v123, v63
mav 30,28,46
v_fma_f32 v15, v15, v122, v125
v_mul_f32 v124, v123, v64
mva 34,46,32
s_waitcnt lgkmcnt(2)
v_fma_f32 v16, v16, v122, v124
v_mul_f32 v125, v123, v65
mav 26,36,50
R 42,448
v_fma_f32 v17, v17, v122, v125
mav 30,40,50
v_mul_f32 v124, v123, v66
v_fma_f32 v18, v18, v122, v124
mva 34,50,44
v_mul_f32 v125, v123, v67
v_fma_f32 v19, v19, v122, v125
mav 26,48,54
R 46,512
v_mul_f32 v124, v123, v68
mav 30,52,54
v_fma_f32 v20, v20, v122, v124
v_mul_f32 v125, v123, v69
mva 34,54,56
s_waitcnt lgkmcnt(2)
v_fma_f32 v21, v21, v122, v125
v_add_u32 v78, 1, v78
mav 26,60,58
R 50,576
mav 30,64,58
mva 34,58,68
mav 26,72,38
R 54,640
mav 30,76,38
mva 34,38,80
s_waitcnt lgkmcnt(2)
mav 26,84,42
R 58,704
mav 30,88,42
mva 34,42,92
mav 26,96,46
R 38,768
mav 30,100,46
s_barrier
mva 34,46,104
s_waitcnt lgkmcnt(2)
ds_read_b128 v[100:103], v82 offset:0
mav 26,108,50
ds_read_b64 v[108:109], v82 offset:1024
mav 30,112,50
R 42,832
mva 34,50,116
mav 26,120,54
s_waitcnt vmcnt(0)
R 46,896
mav 30,124,54
buffer_store_dword v78, off, s[36:39], s52 sc1
mva 34,54,128
s_waitcnt lgkmcnt(3)
v_mfma_f32_16x16x32_bf16 v[74:77], v[100:103], v[0:3], v[90:93]
v_cvt_pk_bf16_f32 v0, v14, v15
v_cvt_pk_bf16_f32 v1, v16, v17
mav 26,132,58
R 50,960
v_cvt_pk_bf16_f32 v2, v18, v19
mav 30,136,58
v_cvt_pk_bf16_f32 v3, v20, v21
mva 34,58,140
v_max3_f32 v118, v70, v71, v74
v_max3_f32 v119, v72, v73, v76
mav 26,144,38
R 54,1024
v_max_f32 v118, v118, v75
mav 30,148,38
v_max_f32 v119, v119, v77
v_max_f32 v120, v118, v119
mva 34,38,152
s_waitcnt lgkmcnt(2)
v_max_f32 v121, v118, v119
mav 26,156,42
R 38,1088
mav 30,160,42
v_permlane32_swap_b32 v120, v121
mva 34,42,164
v_max_f32 v114, v120, v121
v_max_f32 v115, v120, v121
mav 26,168,46
R 42,1152
mav 30,172,46
v_permlane16_swap_b32 v114, v115
mva 34,46,176
s_waitcnt lgkmcnt(2)
v_max_f32 v120, v114, v115
v_mul_f32 v120, s65, v120
mav 26,180,50
R 46,1216
v_fma_f32 v118, v118, s65, -v120
mav 30,184,50
v_fma_f32 v119, v119, s65, -v120
mva 34,50,188
v_exp_f32 v118, v118
mav 26,192,54
R 50,1280
mav 30,196,54
v_exp_f32 v119, v119
mva 34,54,200
s_waitcnt lgkmcnt(2)
v_add_f32 v120, v118, v119
v_add_f32 v121, v118, v119
mav 26,204,38
R 54,1344
mav 30,208,38
v_permlane32_swap_b32 v120, v121
mva 34,38,212
v_add_f32 v114, v120, v121
v_add_f32 v115, v120, v121
mav 26,216,42
R 38,1408
mav 30,220,42
v_permlane16_swap_b32 v114, v115
mva 34,42,224
s_waitcnt lgkmcnt(2)
v_add_f32 v120, v114, v115
mav 26,228,46
R 42,1472
mav 30,232,46
v_rcp_f32 v120, v120
mva 34,46,236
v_mul_f32 v118, v118, v120
v_mul_f32 v119, v119, v120
mav 26,240,50
R 46,1536
v_cvt_pk_bf16_f32 v118, v118, v118
mav 30,244,50
v_cvt_pk_bf16_f32 v119, v119, v119
v_and_b32 v114, v118, v96
mva 34,50,248
s_waitcnt lgkmcnt(2)
v_and_b32 v115, v119, v97
v_and_b32 v116, v118, v94
mav 26,252,54
R 50,1600
v_and_b32 v117, v119, v95
mvv 30,128,54
mvv 34,54,132
v_mfma_f32_16x16x32_bf16 v[70:73], v[8:11], v[114:117], 0
v_mfma_f32_16x16x32_bf16 v[74:77], v[108:111], v[114:117], 0
mvv 26,136,38
R 54,1664
mvv 30,140,38
mvv 34,38,144
s_waitcnt lgkmcnt(2)
mvv 26,148,42
R 38,1728
v_cvt_pk_bf16_f32 v114, v70, v71
mvv 30,152,42
v_cvt_pk_bf16_f32 v115, v72, v73
v_cvt_pk_bf16_f32 v116, v74, v75
mvv 34,42,156
v_cvt_pk_bf16_f32 v117, v76, v77
mvv 26,160,46
R 42,1792
mvv 30,164,46
buffer_store_dwordx4 v[114:117], v82, s[36:39], s49 offen sc1
s_add_u32 s49, s49, 313344
mvv 34,46,168
s_waitcnt lgkmcnt(2)
mvv 26,172,50
R 46,1856
mvv 30,176,50
mvv 34,50,180
mvv 26,184,54
R 50,1920
mvv 30,188,54
s_waitcnt lgkmcnt(2)
s_add_u32 s60, s60, 1
mvv 34,54,192
s_cbranch_scc0 LQA1_1
mvv 26,196,38
R 54,1984
v_xor_b32 v80, v81, v80
mvv 30,200,38
mvv 34,38,204
mvv 26,208,42
R 38,33024
mvv 30,212,42
ds_read_b64 v[122:123], v83 offset:128
mvv 34,42,216
s_waitcnt lgkmcnt(3)
ds_read_b128 v[70:73], v84 offset:128
mvv 26,220,46
R 42,33088
mvv 30,224,46
ds_read_b128 v[74:77], v84 offset:144
mvv 34,46,228
mvv 26,232,50
R 46,33152
mvv 30,236,50
mvv 34,50,240
s_waitcnt lgkmcnt(3)
mvv 26,244,54
R 50,33216
mvv 30,248,54
mvv 34,54,252
v_mfma_f32_16x16x32_bf16 v[14:17], a[0:3], v[38:41], 0
s_waitcnt lgkmcnt(1)
R 54,33280
v_mul_f32 v71, v71, v99
v_mfma_f32_16x16x32_bf16 v[18:21], a[4:7], v[38:41], 0
v_mul_f32 v73, v73, v99
v_mul_f32 v75, v75, v99
v_mfma_f32_16x16x32_bf16 v[22:25], v[38:41], a[8:11], 0
v_fma_f32 v34, v34, v70, v71
v_fma_f32 v35, v35, v72, v73
mav 14,12,42
R 58,33344
v_fma_f32 v36, v36, v74, v75
mav 18,16,42
v_mul_f32 v77, v77, v99
v_fma_f32 v37, v37, v76, v77
mva 22,42,20
v_cvt_pk_bf16_f32 v10, v34, v35
v_cvt_pk_bf16_f32 v11, v36, v37
mav 14,24,46
R 38,33408
v_mul_f32 v124, v123, v62
v_mfma_f32_16x16x32_bf16 v[70:73], v[100:103], v[0:3], v[86:89]
v_fma_f32 v26, v26, v122, v124
v_mul_f32 v125, v123, v63
mav 18,28,46
v_fma_f32 v27, v27, v122, v125
v_mul_f32 v124, v123, v64
mva 22,46,32
s_waitcnt lgkmcnt(2)
v_fma_f32 v28, v28, v122, v124
v_mul_f32 v125, v123, v65
mav 14,36,50
R 42,33472
v_fma_f32 v29, v29, v122, v125
mav 18,40,50
v_mul_f32 v124, v123, v66
v_fma_f32 v30, v30, v122, v124
mva 22,50,44
v_mul_f32 v125, v123, v67
v_fma_f32 v31, v31, v122, v125
mav 14,48,54
R 46,33536
v_mul_f32 v124, v123, v68
mav 18,52,54
v_fma_f32 v32, v32, v122, v124
v_mul_f32 v125, v123, v69
mva 22,54,56
s_waitcnt lgkmcnt(2)
v_fma_f32 v33, v33, v122, v125
v_add_u32 v78, 1, v78
mav 14,60,58
R 50,33600
mav 18,64,58
mva 22,58,68
mav 14,72,38
R 54,33664
mav 18,76,38
mva 22,38,80
s_waitcnt lgkmcnt(2)
mav 14,84,42
R 58,33728
mav 18,88,42
mva 22,42,92
mav 14,96,46
R 38,33792
mav 18,100,46
s_barrier
mva 22,46,104
s_waitcnt lgkmcnt(2)
ds_read_b128 v[104:107], v82 offset:4096
mav 14,108,50
ds_read_b64 v[110:111], v82 offset:5120
mav 18,112,50
R 42,33856
mva 22,50,116
mav 14,120,54
s_waitcnt vmcnt(0)
R 46,33920
mav 18,124,54
buffer_store_dword v78, off, s[36:39], s52 sc1
mva 22,54,128
s_waitcnt lgkmcnt(3)
v_mfma_f32_16x16x32_bf16 v[74:77], v[104:107], v[0:3], v[90:93]
v_cvt_pk_bf16_f32 v0, v26, v27
v_cvt_pk_bf16_f32 v1, v28, v29
mav 14,132,58
R 50,33984
v_cvt_pk_bf16_f32 v2, v30, v31
mav 18,136,58
v_cvt_pk_bf16_f32 v3, v32, v33
mva 22,58,140
v_max3_f32 v118, v70, v71, v74
v_max3_f32 v119, v72, v73, v76
mav 14,144,38
R 54,34048
v_max_f32 v118, v118, v75
mav 18,148,38
v_max_f32 v119, v119, v77
v_max_f32 v120, v118, v119
mva 22,38,152
s_waitcnt lgkmcnt(2)
v_max_f32 v121, v118, v119
mav 14,156,42
R 38,34112
mav 18,160,42
v_permlane32_swap_b32 v120, v121
mva 22,42,164
v_max_f32 v114, v120, v121
v_max_f32 v115, v120, v121
mav 14,168,46
R 42,34176
mav 18,172,46
v_permlane16_swap_b32 v114, v115
mva 22,46,176
s_waitcnt lgkmcnt(2)
v_max_f32 v120, v114, v115
v_mul_f32 v120, s65, v120
mav 14,180,50
R 46,34240
v_fma_f32 v118, v118, s65, -v120
mav 18,184,50
v_fma_f32 v119, v119, s65, -v120
mva 22,50,188
v_exp_f32 v118, v118
mav 14,192,54
R 50,34304
mav 18,196,54
v_exp_f32 v119, v119
mva 22,54,200
s_waitcnt lgkmcnt(2)
v_add_f32 v120, v118, v119
v_add_f32 v121, v118, v119
mav 14,204,38
R 54,34368
mav 18,208,38
v_permlane32_swap_b32 v120, v121
mva 22,38,212
v_add_f32 v114, v120, v121
v_add_f32 v115, v120, v121
mav 14,216,42
R 38,34432
mav 18,220,42
v_permlane16_swap_b32 v114, v115
mva 22,42,224
s_waitcnt lgkmcnt(2)
v_add_f32 v120, v114, v115
mav 14,228,46
R 42,34496
mav 18,232,46
v_rcp_f32 v120, v120
mva 22,46,236
v_mul_f32 v118, v118, v120
v_mul_f32 v119, v119, v120
mav 14,240,50
R 46,34560
v_cvt_pk_bf16_f32 v118, v118, v118
mav 18,244,50
v_cvt_pk_bf16_f32 v119, v119, v119
v_and_b32 v114, v118, v94
mva 22,50,248
s_waitcnt lgkmcnt(2)
v_and_b32 v115, v119, v95
v_and_b32 v116, v118, v96
mav 14,252,54
R 50,34624
v_and_b32 v117, v119, v97
mvv 18,128,54
mvv 22,54,132
v_mfma_f32_16x16x32_bf16 v[70:73], v[8:11], v[114:117], 0
v_mfma_f32_16x16x32_bf16 v[74:77], v[108:111], v[114:117], 0
mvv 14,136,38
R 54,34688
mvv 18,140,38
mvv 22,38,144
s_waitcnt lgkmcnt(2)
mvv 14,148,42
R 38,34752
v_cvt_pk_bf16_f32 v114, v70, v71
mvv 18,152,42
v_cvt_pk_bf16_f32 v115, v72, v73
v_cvt_pk_bf16_f32 v116, v74, v75
mvv 22,42,156
v_cvt_pk_bf16_f32 v117, v76, v77
mvv 14,160,46
R 42,34816
mvv 18,164,46
buffer_store_dwordx4 v[114:117], v82, s[36:39], s49 offen sc1
s_add_u32 s49, s49, 313344
mvv 22,46,168
s_waitcnt lgkmcnt(2)
mvv 14,172,50
R 46,34880
mvv 18,176,50
mvv 22,50,180
mvv 14,184,54
R 50,34944
mvv 18,188,54
mvv 22,54,192
s_waitcnt lgkmcnt(2)
mvv 14,196,38
R 54,35008
mvv 18,200,38
mvv 22,38,204
mvv 14,208,42
R 38,0
mvv 18,212,42
ds_read_b64 v[122:123], v83 offset:0
mvv 22,42,216
s_waitcnt lgkmcnt(3)
ds_read_b128 v[70:73], v84 offset:0
mvv 14,220,46
R 42,64
mvv 18,224,46
ds_read_b128 v[74:77], v84 offset:16
mvv 22,46,228
mvv 14,232,50
R 46,128
mvv 18,236,50
mvv 22,50,240
s_waitcnt lgkmcnt(3)
mvv 14,244,54
R 50,192
mvv 18,248,54
mvv 22,54,252
v_mfma_f32_16x16x32_bf16 v[26:29], a[0:3], v[38:41], 0
s_waitcnt lgkmcnt(1)
R 54,256
v_mul_f32 v71, v71, v99
v_mfma_f32_16x16x32_bf16 v[30:33], a[4:7], v[38:41], 0
v_mul_f32 v73, v73, v99
v_mul_f32 v75, v75, v99
v_mfma_f32_16x16x32_bf16 v[34:37], v[38:41], a[8:11], 0
v_fma_f32 v22, v22, v70, v71
v_fma_f32 v23, v23, v72, v73
mav 26,12,42
R 58,320
v_fma_f32 v24, v24, v74, v75
mav 30,16,42
v_mul_f32 v77, v77, v99
v_fma_f32 v25, v25, v76, v77
mva 34,42,20
v_cvt_pk_bf16_f32 v8, v22, v23
v_cvt_pk_bf16_f32 v9, v24, v25
mav 26,24,46
R 38,384
v_mul_f32 v124, v123, v62
v_mfma_f32_16x16x32_bf16 v[70:73], v[104:107], v[0:3], v[86:89]
v_fma_f32 v14, v14, v122, v124
v_mul_f32 v125, v123, v63
mav 30,28,46
v_fma_f32 v15, v15, v122, v125
v_mul_f32 v124, v123, v64
mva 34,46,32
s_waitcnt lgkmcnt(2)
v_fma_f32 v16, v16, v122, v124
v_mul_f32 v125, v123, v65
mav 26,36,50
R 42,448
v_fma_f32 v17, v17, v122, v125
mav 30,40,50
v_mul_f32 v124, v123, v66
v_fma_f32 v18, v18, v122, v124
mva 34,50,44
buffer_load_dwordx4 v[4:7], v82, s[36:39], s55 offen sc1
mav 26,48,54
R 46,512
v_mul_f32 v125, v123, v67
mav 30,52,54
buffer_load_dwordx2 v[12:13], v98, s[36:39], s55 offen offset:1024 sc1
mva 34,54,56
s_waitcnt lgkmcnt(2)
v_fma_f32 v19, v19, v122, v125
v_mul_f32 v124, v123, v68
mav 26,60,58
buffer_load_dwordx2 v[112:113], v98, s[36:39], s55 offen offset:1536 sc1
mav 30,64,58
R 50,576
v_fma_f32 v20, v20, v122, v124
mva 34,58,68
v_mul_f32 v125, v123, v69
v_fma_f32 v21, v21, v122, v125
mav 26,72,38
R 54,640
v_add_u32 v78, 1, v78
mav 30,76,38
mva 34,38,80
s_waitcnt lgkmcnt(2)
mav 26,84,42
R 58,704
mav 30,88,42
mva 34,42,92
mav 26,96,46
R 38,768
mav 30,100,46
s_barrier
mva 34,46,104
s_waitcnt lgkmcnt(2)
ds_read_b128 v[100:103], v82 offset:0
LsQ10:
s_cbranch_execz LsQ14
mav 26,108,50
ds_read_b64 v[108:109], v82 offset:1024
mav 30,112,50
R 42,832
mva 34,50,116
mav 26,120,54
s_waitcnt vmcnt(3)
R 46,896
mav 30,124,54
buffer_store_dword v78, off, s[36:39], s52 sc1
mva 34,54,128
s_waitcnt lgkmcnt(3)
v_mfma_f32_16x16x32_bf16 v[74:77], v[100:103], v[0:3], v[90:93]
v_cvt_pk_bf16_f32 v0, v14, v15
v_cvt_pk_bf16_f32 v1, v16, v17
mav 26,132,58
R 50,960
v_cvt_pk_bf16_f32 v2, v18, v19
mav 30,136,58
v_cvt_pk_bf16_f32 v3, v20, v21
mva 34,58,140
v_max3_f32 v118, v70, v71, v74
v_max3_f32 v119, v72, v73, v76
mav 26,144,38
R 54,1024
v_max_f32 v118, v118, v75
mav 30,148,38
v_max_f32 v119, v119, v77
v_max_f32 v120, v118, v119
mva 34,38,152
s_waitcnt lgkmcnt(2)
v_max_f32 v121, v118, v119
mav 26,156,42
R 38,1088
mav 30,160,42
v_permlane32_swap_b32 v120, v121
mva 34,42,164
v_max_f32 v114, v120, v121
v_max_f32 v115, v120, v121
mav 26,168,46
R 42,1152
mav 30,172,46
v_permlane16_swap_b32 v114, v115
mva 34,46,176
s_waitcnt lgkmcnt(2)
v_max_f32 v120, v114, v115
v_mul_f32 v120, s65, v120
mav 26,180,50
R 46,1216
v_fma_f32 v118, v118, s65, -v120
mav 30,184,50
v_fma_f32 v119, v119, s65, -v120
mva 34,50,188
v_exp_f32 v118, v118
mav 26,192,54
R 50,1280
mav 30,196,54
v_exp_f32 v119, v119
mva 34,54,200
s_waitcnt lgkmcnt(2)
v_add_f32 v120, v118, v119
v_add_f32 v121, v118, v119
mav 26,204,38
R 54,1344
mav 30,208,38
v_permlane32_swap_b32 v120, v121
mva 34,38,212
v_add_f32 v114, v120, v121
v_add_f32 v115, v120, v121
mav 26,216,42
R 38,1408
mav 30,220,42
v_permlane16_swap_b32 v114, v115
mva 34,42,224
s_waitcnt lgkmcnt(2)
v_add_f32 v120, v114, v115
mav 26,228,46
R 42,1472
mav 30,232,46
v_rcp_f32 v120, v120
mva 34,46,236
v_mul_f32 v118, v118, v120
v_mul_f32 v119, v119, v120
mav 26,240,50
R 46,1536
v_cvt_pk_bf16_f32 v118, v118, v118
mav 30,244,50
v_cvt_pk_bf16_f32 v119, v119, v119
v_and_b32 v114, v118, v96
mva 34,50,248
s_waitcnt lgkmcnt(2)
v_and_b32 v115, v119, v97
v_and_b32 v116, v118, v94
mav 26,252,54
R 50,1600
v_and_b32 v117, v119, v95
mvv 30,128,54
mvv 34,54,132
v_mfma_f32_16x16x32_bf16 v[70:73], v[8:11], v[114:117], 0
v_mfma_f32_16x16x32_bf16 v[74:77], v[108:111], v[114:117], 0
mvv 26,136,38
R 54,1664
mvv 30,140,38
mvv 34,38,144
s_waitcnt lgkmcnt(2)
mvv 26,148,42
R 38,1728
v_cvt_pk_bf16_f32 v114, v70, v71
mvv 30,152,42
v_cvt_pk_bf16_f32 v115, v72, v73
v_cvt_pk_bf16_f32 v116, v74, v75
mvv 34,42,156
v_cvt_pk_bf16_f32 v117, v76, v77
mvv 26,160,46
R 42,1792
mvv 30,164,46
buffer_store_dwordx4 v[114:117], v82, s[36:39], s49 offen sc1
s_add_u32 s49, s49, 313344
mvv 34,46,168
s_waitcnt lgkmcnt(2)
mvv 26,172,50
R 46,1856
mvv 30,176,50
mvv 34,50,180
mvv 26,184,54
R 50,1920
mvv 30,188,54
s_waitcnt lgkmcnt(2)
mvv 34,54,192
mvv 26,196,38
R 54,1984
v_xor_b32 v80, v81, v80
mvv 30,200,38
mvv 34,38,204
mvv 26,208,42
R 38,33024
mvv 30,212,42
ds_read_b64 v[122:123], v83 offset:128
mvv 34,42,216
s_waitcnt lgkmcnt(3)
ds_read_b128 v[70:73], v84 offset:128
mvv 26,220,46
R 42,33088
mvv 30,224,46
ds_read_b128 v[74:77], v84 offset:144
mvv 34,46,228
mvv 26,232,50
R 46,33152
mvv 30,236,50
mvv 34,50,240
s_waitcnt lgkmcnt(3)
mvv 26,244,54
R 50,33216
mvv 30,248,54
mvv 34,54,252
v_mfma_f32_16x16x32_bf16 v[14:17], a[0:3], v[38:41], 0
s_waitcnt lgkmcnt(1)
R 54,33280
v_mul_f32 v71, v71, v99
v_mfma_f32_16x16x32_bf16 v[18:21], a[4:7], v[38:41], 0
v_mul_f32 v73, v73, v99
v_mul_f32 v75, v75, v99
v_mfma_f32_16x16x32_bf16 v[22:25], v[38:41], a[8:11], 0
v_fma_f32 v34, v34, v70, v71
v_fma_f32 v35, v35, v72, v73
mav 14,12,42
R 58,33344
v_fma_f32 v36, v36, v74, v75
mav 18,16,42
v_mul_f32 v77, v77, v99
v_fma_f32 v37, v37, v76, v77
mva 22,42,20
v_cvt_pk_bf16_f32 v10, v34, v35
v_cvt_pk_bf16_f32 v11, v36, v37
mav 14,24,46
R 38,33408
v_mul_f32 v124, v123, v62
v_mfma_f32_16x16x32_bf16 v[70:73], v[100:103], v[0:3], v[86:89]
v_fma_f32 v26, v26, v122, v124
v_mul_f32 v125, v123, v63
mav 18,28,46
v_fma_f32 v27, v27, v122, v125
v_mul_f32 v124, v123, v64
mva 22,46,32
s_waitcnt lgkmcnt(2)
v_fma_f32 v28, v28, v122, v124
v_mul_f32 v125, v123, v65
mav 14,36,50
R 42,33472
v_fma_f32 v29, v29, v122, v125
mav 18,40,50
v_mul_f32 v124, v123, v66
v_fma_f32 v30, v30, v122, v124
mva 22,50,44
v_mul_f32 v125, v123, v67
v_fma_f32 v31, v31, v122, v125
mav 14,48,54
R 46,33536
v_mul_f32 v124, v123, v68
mav 18,52,54
v_fma_f32 v32, v32, v122, v124
v_mul_f32 v125, v123, v69
mva 22,54,56
s_waitcnt lgkmcnt(2)
v_fma_f32 v33, v33, v122, v125
v_add_u32 v78, 1, v78
mav 14,60,58
R 50,33600
mav 18,64,58
mva 22,58,68
mav 14,72,38
R 54,33664
mav 18,76,38
mva 22,38,80
s_waitcnt lgkmcnt(2)
mav 14,84,42
R 58,33728
mav 18,88,42
mva 22,42,92
mav 14,96,46
R 38,33792
mav 18,100,46
s_barrier
mva 22,46,104
s_waitcnt lgkmcnt(2)
ds_read_b128 v[104:107], v82 offset:4096
mav 14,108,50
ds_read_b64 v[110:111], v82 offset:5120
mav 18,112,50
R 42,33856
mva 22,50,116
mav 14,120,54
s_waitcnt vmcnt(0)
R 46,33920
mav 18,124,54
buffer_store_dword v78, off, s[36:39], s52 sc1
mva 22,54,128
s_waitcnt lgkmcnt(3)
v_mfma_f32_16x16x32_bf16 v[74:77], v[104:107], v[0:3], v[90:93]
v_cvt_pk_bf16_f32 v0, v26, v27
v_cvt_pk_bf16_f32 v1, v28, v29
mav 14,132,58
R 50,33984
v_cvt_pk_bf16_f32 v2, v30, v31
mav 18,136,58
v_cvt_pk_bf16_f32 v3, v32, v33
mva 22,58,140
v_max3_f32 v118, v70, v71, v74
v_max3_f32 v119, v72, v73, v76
mav 14,144,38
R 54,34048
v_max_f32 v118, v118, v75
mav 18,148,38
v_max_f32 v119, v119, v77
v_max_f32 v120, v118, v119
mva 22,38,152
s_waitcnt lgkmcnt(2)
v_max_f32 v121, v118, v119
mav 14,156,42
R 38,34112
mav 18,160,42
v_permlane32_swap_b32 v120, v121
mva 22,42,164
v_max_f32 v114, v120, v121
v_max_f32 v115, v120, v121
mav 14,168,46
R 42,34176
mav 18,172,46
v_permlane16_swap_b32 v114, v115
mva 22,46,176
s_waitcnt lgkmcnt(2)
v_max_f32 v120, v114, v115
v_mul_f32 v120, s65, v120
mav 14,180,50
R 46,34240
v_fma_f32 v118, v118, s65, -v120
mav 18,184,50
v_fma_f32 v119, v119, s65, -v120
mva 22,50,188
v_exp_f32 v118, v118
mav 14,192,54
R 50,34304
mav 18,196,54
v_exp_f32 v119, v119
mva 22,54,200
s_waitcnt lgkmcnt(2)
v_add_f32 v120, v118, v119
v_add_f32 v121, v118, v119
mav 14,204,38
R 54,34368
mav 18,208,38
v_permlane32_swap_b32 v120, v121
mva 22,38,212
v_add_f32 v114, v120, v121
v_add_f32 v115, v120, v121
mav 14,216,42
R 38,34432
mav 18,220,42
v_permlane16_swap_b32 v114, v115
mva 22,42,224
s_waitcnt lgkmcnt(2)
v_add_f32 v120, v114, v115
mav 14,228,46
R 42,34496
mav 18,232,46
v_rcp_f32 v120, v120
mva 22,46,236
v_mul_f32 v118, v118, v120
v_mul_f32 v119, v119, v120
mav 14,240,50
R 46,34560
v_cvt_pk_bf16_f32 v118, v118, v118
mav 18,244,50
v_cvt_pk_bf16_f32 v119, v119, v119
v_and_b32 v114, v118, v94
mva 22,50,248
s_waitcnt lgkmcnt(2)
v_and_b32 v115, v119, v95
v_and_b32 v116, v118, v96
mav 14,252,54
R 50,34624
v_and_b32 v117, v119, v97
mvv 18,128,54
mvv 22,54,132
v_mfma_f32_16x16x32_bf16 v[70:73], v[8:11], v[114:117], 0
v_mfma_f32_16x16x32_bf16 v[74:77], v[108:111], v[114:117], 0
mvv 14,136,38
R 54,34688
mvv 18,140,38
mvv 22,38,144
s_waitcnt lgkmcnt(2)
mvv 14,148,42
R 38,34752
v_cvt_pk_bf16_f32 v114, v70, v71
mvv 18,152,42
v_cvt_pk_bf16_f32 v115, v72, v73
v_cvt_pk_bf16_f32 v116, v74, v75
mvv 22,42,156
v_cvt_pk_bf16_f32 v117, v76, v77
mvv 14,160,46
R 42,34816
mvv 18,164,46
buffer_store_dwordx4 v[114:117], v82, s[36:39], s49 offen sc1
s_add_u32 s49, s49, 313344
mvv 22,46,168
s_waitcnt lgkmcnt(2)
mvv 14,172,50
R 46,34880
mvv 18,176,50
mvv 22,50,180
mvv 14,184,54
R 50,34944
mvv 18,188,54
mvv 22,54,192
s_waitcnt lgkmcnt(2)
mvv 14,196,38
R 54,35008
mvv 18,200,38
mvv 22,38,204
mvv 14,208,42
R 38,0
mvv 18,212,42
ds_read_b64 v[122:123], v83 offset:0
mvv 22,42,216
s_waitcnt lgkmcnt(3)
ds_read_b128 v[70:73], v84 offset:0
mvv 14,220,46
R 42,64
mvv 18,224,46
ds_read_b128 v[74:77], v84 offset:16
mvv 22,46,228
mvv 14,232,50
R 46,128
mvv 18,236,50
mvv 22,50,240
s_waitcnt lgkmcnt(3)
mvv 14,244,54
R 50,192
mvv 18,248,54
mvv 22,54,252
v_mfma_f32_16x16x32_bf16 v[26:29], a[0:3], v[38:41], 0
s_waitcnt lgkmcnt(1)
R 54,256
v_mul_f32 v71, v71, v99
v_mfma_f32_16x16x32_bf16 v[30:33], a[4:7], v[38:41], 0
v_mul_f32 v73, v73, v99
v_mul_f32 v75, v75, v99
v_mfma_f32_16x16x32_bf16 v[34:37], v[38:41], a[8:11], 0
v_fma_f32 v22, v22, v70, v71
v_fma_f32 v23, v23, v72, v73
mav 26,12,42
R 58,320
v_fma_f32 v24, v24, v74, v75
mav 30,16,42
v_mul_f32 v77, v77, v99
v_fma_f32 v25, v25, v76, v77
mva 34,42,20
v_cvt_pk_bf16_f32 v8, v22, v23
v_cvt_pk_bf16_f32 v9, v24, v25
mav 26,24,46
R 38,384
v_min_f32 v74, s56, v90
v_mfma_f32_16x16x32_bf16 v[70:73], v[104:107], v[0:3], v[86:89]
v_min_f32 v75, s56, v91
v_min_f32 v76, s56, v92
mav 30,28,46
v_min_f32 v77, s56, v93
v_mul_f32 v124, v123, v62
mva 34,46,32
s_waitcnt lgkmcnt(2)
v_fma_f32 v14, v14, v122, v124
v_mul_f32 v125, v123, v63
mvv 74,4,0
v_fma_f32 v15, v15, v122, v125
v_mul_f32 v124, v123, v64
mav 26,36,50
R 42,448
v_fma_f32 v16, v16, v122, v124
mav 30,40,50
v_mul_f32 v125, v123, v65
v_fma_f32 v17, v17, v122, v125
mva 34,50,44
v_max3_f32 v118, v70, v71, v74
v_max3_f32 v119, v72, v73, v76
mav 26,48,54
R 46,512
v_max_f32 v118, v118, v75
mav 30,52,54
v_max_f32 v119, v119, v77
v_max_f32 v120, v118, v119
mva 34,54,56
s_waitcnt lgkmcnt(2)
v_max_f32 v121, v118, v119
v_mul_f32 v124, v123, v66
mav 26,60,58
R 50,576
v_fma_f32 v18, v18, v122, v124
mav 30,64,58
v_permlane32_swap_b32 v120, v121
mva 34,58,68
v_max_f32 v114, v120, v121
v_max_f32 v115, v120, v121
mav 26,72,38
R 54,640
v_mul_f32 v125, v123, v67
mav 30,76,38
v_permlane16_swap_b32 v114, v115
mva 34,38,80
s_waitcnt lgkmcnt(2)
v_max_f32 v120, v114, v115
v_mul_f32 v120, s65, v120
mav 26,84,42
R 58,704
v_fma_f32 v118, v118, s65, -v120
mav 30,88,42
v_fma_f32 v119, v119, s65, -v120
v_fma_f32 v19, v19, v122, v125
mva 34,42,92
v_exp_f32 v118, v118
mav 26,96,46
R 38,768
v_mul_f32 v124, v123, v68
mav 30,100,46
s_barrier
mva 34,46,104
s_waitcnt lgkmcnt(2)
ds_read_b128 v[100:103], v82 offset:0
v_fma_f32 v20, v20, v122, v124
mav 26,108,50
ds_read_b64 v[108:109], v82 offset:1024
v_mul_f32 v125, v123, v69
mav 30,112,50
R 42,832
v_fma_f32 v21, v21, v122, v125
mva 34,50,116
v_exp_f32 v119, v119
mav 26,120,54
R 46,896
v_add_f32 v120, v118, v119
mav 30,124,54
v_add_f32 v121, v118, v119
v_cvt_pk_bf16_f32 v0, v14, v15
mva 34,54,128
s_waitcnt lgkmcnt(3)
v_permlane32_swap_b32 v120, v121
mav 26,132,58
R 50,960
v_add_f32 v114, v120, v121
mav 30,136,58
v_add_f32 v115, v120, v121
v_cvt_pk_bf16_f32 v1, v16, v17
mva 34,58,140
v_permlane16_swap_b32 v114, v115
mav 26,144,38
R 54,1024
v_add_f32 v120, v114, v115
mav 30,148,38
v_rcp_f32 v120, v120
mva 34,38,152
s_waitcnt lgkmcnt(2)
v_mul_f32 v118, v118, v120
v_mul_f32 v119, v119, v120
mav 26,156,42
R 38,1088
v_cvt_pk_bf16_f32 v118, v118, v118
mav 30,160,42
v_cvt_pk_bf16_f32 v119, v119, v119
v_and_b32 v114, v118, v94
mva 34,42,164
buffer_store_dwordx4 v[100:103], v82, s[36:39], s54 offen offset:2048 sc1
v_and_b32 v115, v119, v95
v_and_b32 v116, v118, v96
v_and_b32 v117, v119, v97
mav 26,168,46
R 42,1152
v_cvt_pk_bf16_f32 v2, v18, v19
v_mfma_f32_16x16x32_bf16 v[70:73], v[10:13], v[114:117], 0
v_cvt_pk_bf16_f32 v3, v20, v21
v_add_u32 v78, 1, v78
v_mfma_f32_16x16x32_bf16 v[74:77], v[110:113], v[114:117], 0
s_waitcnt vmcnt(1)
mav 30,172,46
buffer_store_dword v78, off, s[36:39], s52 sc1
mva 34,46,176
s_waitcnt lgkmcnt(2)
v_cvt_pk_bf16_f32 v114, v70, v71
v_cvt_pk_bf16_f32 v115, v72, v73
mav 26,180,50
buffer_store_dwordx2 v[8:9], v98, s[36:39], s54 offen offset:3072 sc1
R 46,1216
mav 30,184,50
v_cvt_pk_bf16_f32 v116, v74, v75
v_cvt_pk_bf16_f32 v117, v76, v77
mva 34,50,188
buffer_store_dwordx4 v[114:117], v82, s[36:39], s49 offen sc1
s_add_u32 s49, s49, 313344
mav 26,192,54
R 50,1280
mav 30,196,54
buffer_store_dwordx2 v[108:109], v98, s[36:39], s54 offen offset:3584 sc1
mva 34,54,200
s_waitcnt lgkmcnt(2)
mav 26,204,38
R 54,1344
mav 30,208,38
mva 34,38,212
mav 26,216,42
R 38,1408
mav 30,220,42
mva 34,42,224
s_waitcnt lgkmcnt(2)
mav 26,228,46
R 42,1472
mav 30,232,46
mva 34,46,236
mav 26,240,50
R 46,1536
mav 30,244,50
mva 34,50,248
s_waitcnt lgkmcnt(2)
mav 26,252,54
R 50,1600
mvv 30,128,54
mvv 34,54,132
mvv 26,136,38
R 54,1664
mvv 30,140,38
mvv 34,38,144
s_waitcnt lgkmcnt(2)
mvv 26,148,42
R 38,1728
mvv 30,152,42
mvv 34,42,156
mvv 26,160,46
R 42,1792
mvv 30,164,46
mvv 34,46,168
s_waitcnt lgkmcnt(2)
mvv 26,172,50
R 46,1856
mvv 30,176,50
mvv 34,50,180
mvv 26,184,54
R 50,1920
mvv 30,188,54
s_waitcnt lgkmcnt(2)
mvv 34,54,192
mvv 26,196,38
R 54,1984
LsQ11:
s_cbranch_execz LsQ15
v_xor_b32 v80, v81, v80
mvv 30,200,38
mvv 34,38,204
mvv 26,208,42
R 38,33024
mvv 30,212,42
ds_read_b64 v[122:123], v83 offset:128
mvv 34,42,216
s_waitcnt lgkmcnt(3)
ds_read_b128 v[70:73], v84 offset:128
mvv 26,220,46
R 42,33088
mvv 30,224,46
ds_read_b128 v[74:77], v84 offset:144
mvv 34,46,228
mvv 26,232,50
R 46,33152
mvv 30,236,50
mvv 34,50,240
s_waitcnt lgkmcnt(3)
mvv 26,244,54
R 50,33216
mvv 30,248,54
mvv 34,54,252
v_mfma_f32_16x16x32_bf16 v[14:17], a[0:3], v[38:41], 0
s_waitcnt lgkmcnt(1)
R 54,33280
v_mul_f32 v71, v71, v99
v_mfma_f32_16x16x32_bf16 v[18:21], a[4:7], v[38:41], 0
v_mul_f32 v73, v73, v99
v_mul_f32 v75, v75, v99
v_mfma_f32_16x16x32_bf16 v[22:25], v[38:41], a[8:11], 0
v_fma_f32 v34, v34, v70, v71
v_fma_f32 v35, v35, v72, v73
mav 14,12,42
R 58,33344
v_fma_f32 v36, v36, v74, v75
mav 18,16,42
v_mul_f32 v77, v77, v99
v_fma_f32 v37, v37, v76, v77
mva 22,42,20
v_cvt_pk_bf16_f32 v10, v34, v35
v_cvt_pk_bf16_f32 v11, v36, v37
mav 14,24,46
R 38,33408
v_mul_f32 v124, v123, v62
v_mfma_f32_16x16x32_bf16 v[70:73], v[100:103], v[0:3], v[86:89]
v_fma_f32 v26, v26, v122, v124
v_mul_f32 v125, v123, v63
mav 18,28,46
v_fma_f32 v27, v27, v122, v125
v_mul_f32 v124, v123, v64
mva 22,46,32
s_waitcnt lgkmcnt(2)
v_fma_f32 v28, v28, v122, v124
v_mul_f32 v125, v123, v65
mav 14,36,50
R 42,33472
v_fma_f32 v29, v29, v122, v125
mav 18,40,50
v_mul_f32 v124, v123, v66
v_fma_f32 v30, v30, v122, v124
mva 22,50,44
v_mul_f32 v125, v123, v67
v_fma_f32 v31, v31, v122, v125
mav 14,48,54
R 46,33536
v_mul_f32 v124, v123, v68
mav 18,52,54
v_fma_f32 v32, v32, v122, v124
v_mul_f32 v125, v123, v69
mva 22,54,56
s_waitcnt lgkmcnt(2)
v_fma_f32 v33, v33, v122, v125
v_add_u32 v78, 1, v78
mav 14,60,58
R 50,33600
mav 18,64,58
mva 22,58,68
mav 14,72,38
R 54,33664
mav 18,76,38
mva 22,38,80
s_waitcnt lgkmcnt(2)
mav 14,84,42
R 58,33728
mav 18,88,42
mva 22,42,92
mav 14,96,46
R 38,33792
mav 18,100,46
s_barrier
mva 22,46,104
s_waitcnt lgkmcnt(2)
ds_read_b128 v[104:107], v82 offset:4096
mav 14,108,50
ds_read_b64 v[110:111], v82 offset:5120
mav 18,112,50
R 42,33856
mva 22,50,116
mav 14,120,54
s_waitcnt vmcnt(1)
R 46,33920
mav 18,124,54
buffer_store_dword v78, off, s[36:39], s52 sc1
mva 22,54,128
s_waitcnt lgkmcnt(3)
v_mfma_f32_16x16x32_bf16 v[74:77], v[104:107], v[0:3], v[90:93]
v_cvt_pk_bf16_f32 v0, v26, v27
v_cvt_pk_bf16_f32 v1, v28, v29
mav 14,132,58
R 50,33984
v_cvt_pk_bf16_f32 v2, v30, v31
mav 18,136,58
v_cvt_pk_bf16_f32 v3, v32, v33
mva 22,58,140
v_max3_f32 v118, v70, v71, v74
v_max3_f32 v119, v72, v73, v76
mav 14,144,38
R 54,34048
v_max_f32 v118, v118, v75
mav 18,148,38
v_max_f32 v119, v119, v77
v_max_f32 v120, v118, v119
mva 22,38,152
s_waitcnt lgkmcnt(2)
v_max_f32 v121, v118, v119
mav 14,156,42
R 38,34112
mav 18,160,42
v_permlane32_swap_b32 v120, v121
mva 22,42,164
v_max_f32 v114, v120, v121
v_max_f32 v115, v120, v121
mav 14,168,46
R 42,34176
mav 18,172,46
v_permlane16_swap_b32 v114, v115
mva 22,46,176
s_waitcnt lgkmcnt(2)
v_max_f32 v120, v114, v115
v_mul_f32 v120, s65, v120
mav 14,180,50
R 46,34240
v_fma_f32 v118, v118, s65, -v120
mav 18,184,50
v_fma_f32 v119, v119, s65, -v120
mva 22,50,188
v_exp_f32 v118, v118
mav 14,192,54
R 50,34304
mav 18,196,54
v_exp_f32 v119, v119
mva 22,54,200
s_waitcnt lgkmcnt(2)
v_add_f32 v120, v118, v119
v_add_f32 v121, v118, v119
mav 14,204,38
R 54,34368
mav 18,208,38
v_permlane32_swap_b32 v120, v121
mva 22,38,212
v_add_f32 v114, v120, v121
v_add_f32 v115, v120, v121
mav 14,216,42
R 38,34432
mav 18,220,42
v_permlane16_swap_b32 v114, v115
mva 22,42,224
s_waitcnt lgkmcnt(2)
v_add_f32 v120, v114, v115
mav 14,228,46
R 42,34496
mav 18,232,46
v_rcp_f32 v120, v120
mva 22,46,236
v_mul_f32 v118, v118, v120
v_mul_f32 v119, v119, v120
mav 14,240,50
R 46,34560
v_cvt_pk_bf16_f32 v118, v118, v118
mav 18,244,50
v_cvt_pk_bf16_f32 v119, v119, v119
v_and_b32 v114, v118, v94
mva 22,50,248
s_waitcnt lgkmcnt(2)
v_and_b32 v115, v119, v95
v_and_b32 v116, v118, v96
mav 14,252,54
R 50,34624
v_and_b32 v117, v119, v97
mvv 18,128,54
mvv 22,54,132
v_mfma_f32_16x16x32_bf16 v[70:73], v[8:11], v[114:117], 0
v_mfma_f32_16x16x32_bf16 v[74:77], v[108:111], v[114:117], 0
mvv 14,136,38
R 54,34688
mvv 18,140,38
mvv 22,38,144
s_waitcnt lgkmcnt(2)
mvv 14,148,42
R 38,34752
v_cvt_pk_bf16_f32 v114, v70, v71
mvv 18,152,42
v_cvt_pk_bf16_f32 v115, v72, v73
v_cvt_pk_bf16_f32 v116, v74, v75
mvv 22,42,156
v_cvt_pk_bf16_f32 v117, v76, v77
mvv 14,160,46
R 42,34816
mvv 18,164,46
buffer_store_dwordx4 v[114:117], v82, s[36:39], s49 offen sc1
s_add_u32 s49, s49, 313344
mvv 22,46,168
s_waitcnt lgkmcnt(2)
mvv 14,172,50
R 46,34880
mvv 18,176,50
mvv 22,50,180
mvv 14,184,54
R 50,34944
mvv 18,188,54
mvv 22,54,192
s_waitcnt lgkmcnt(2)
mvv 14,196,38
R 54,35008
mvv 18,200,38
mvv 22,38,204
mvv 14,208,42
R 38,0
mvv 18,212,42
ds_read_b64 v[122:123], v83 offset:0
mvv 22,42,216
s_waitcnt lgkmcnt(3)
ds_read_b128 v[70:73], v84 offset:0
mvv 14,220,46
R 42,64
mvv 18,224,46
ds_read_b128 v[74:77], v84 offset:16
mvv 22,46,228
mvv 14,232,50
R 46,128
mvv 18,236,50
mvv 22,50,240
s_waitcnt lgkmcnt(3)
mvv 14,244,54
R 50,192
mvv 18,248,54
mvv 22,54,252
v_mfma_f32_16x16x32_bf16 v[26:29], a[0:3], v[38:41], 0
s_waitcnt lgkmcnt(1)
R 54,256
v_mul_f32 v71, v71, v99
v_mfma_f32_16x16x32_bf16 v[30:33], a[4:7], v[38:41], 0
v_mul_f32 v73, v73, v99
v_mul_f32 v75, v75, v99
v_mfma_f32_16x16x32_bf16 v[34:37], v[38:41], a[8:11], 0
v_fma_f32 v22, v22, v70, v71
v_fma_f32 v23, v23, v72, v73
mav 26,12,42
R 58,320
v_fma_f32 v24, v24, v74, v75
mav 30,16,42
v_mul_f32 v77, v77, v99
v_fma_f32 v25, v25, v76, v77
mva 34,42,20
v_cvt_pk_bf16_f32 v8, v22, v23
v_cvt_pk_bf16_f32 v9, v24, v25
mav 26,24,46
R 38,384
v_mul_f32 v124, v123, v62
v_mfma_f32_16x16x32_bf16 v[70:73], v[104:107], v[0:3], v[86:89]
v_fma_f32 v14, v14, v122, v124
v_mul_f32 v125, v123, v63
mav 30,28,46
v_fma_f32 v15, v15, v122, v125
v_mul_f32 v124, v123, v64
mva 34,46,32
s_waitcnt lgkmcnt(2)
v_fma_f32 v16, v16, v122, v124
v_mul_f32 v125, v123, v65
mav 26,36,50
R 42,448
v_fma_f32 v17, v17, v122, v125
mav 30,40,50
v_mul_f32 v124, v123, v66
v_fma_f32 v18, v18, v122, v124
mva 34,50,44
v_mul_f32 v125, v123, v67
v_fma_f32 v19, v19, v122, v125
mav 26,48,54
R 46,512
v_mul_f32 v124, v123, v68
mav 30,52,54
v_fma_f32 v20, v20, v122, v124
v_mul_f32 v125, v123, v69
mva 34,54,56
s_waitcnt lgkmcnt(2)
v_fma_f32 v21, v21, v122, v125
v_add_u32 v78, 1, v78
mav 26,60,58
R 50,576
mav 30,64,58
mva 34,58,68
mav 26,72,38
R 54,640
mav 30,76,38
mva 34,38,80
s_waitcnt lgkmcnt(2)
mav 26,84,42
R 58,704
mav 30,88,42
mva 34,42,92
mav 26,96,46
R 38,768
mav 30,100,46
s_barrier
mva 34,46,104
s_waitcnt lgkmcnt(2)
ds_read_b128 v[100:103], v82 offset:0
mav 26,108,50
ds_read_b64 v[108:109], v82 offset:1024
mav 30,112,50
R 42,832
mva 34,50,116
mav 26,120,54
s_waitcnt vmcnt(0)
R 46,896
mav 30,124,54
buffer_store_dword v78, off, s[36:39], s52 sc1
mva 34,54,128
s_waitcnt lgkmcnt(3)
v_mfma_f32_16x16x32_bf16 v[74:77], v[100:103], v[0:3], v[90:93]
v_cvt_pk_bf16_f32 v0, v14, v15
v_cvt_pk_bf16_f32 v1, v16, v17
mav 26,132,58
R 50,960
v_cvt_pk_bf16_f32 v2, v18, v19
mav 30,136,58
v_cvt_pk_bf16_f32 v3, v20, v21
mva 34,58,140
v_max3_f32 v118, v70, v71, v74
v_max3_f32 v119, v72, v73, v76
mav 26,144,38
R 54,1024
v_max_f32 v118, v118, v75
mav 30,148,38
v_max_f32 v119, v119, v77
v_max_f32 v120, v118, v119
mva 34,38,152
s_waitcnt lgkmcnt(2)
v_max_f32 v121, v118, v119
mav 26,156,42
R 38,1088
mav 30,160,42
v_permlane32_swap_b32 v120, v121
mva 34,42,164
v_max_f32 v114, v120, v121
v_max_f32 v115, v120, v121
mav 26,168,46
R 42,1152
mav 30,172,46
v_permlane16_swap_b32 v114, v115
mva 34,46,176
s_waitcnt lgkmcnt(2)
v_max_f32 v120, v114, v115
v_mul_f32 v120, s65, v120
)"
R"(
mav 26,180,50
R 46,1216
v_fma_f32 v118, v118, s65, -v120
mav 30,184,50
v_fma_f32 v119, v119, s65, -v120
mva 34,50,188
v_exp_f32 v118, v118
mav 26,192,54
R 50,1280
mav 30,196,54
v_exp_f32 v119, v119
mva 34,54,200
s_waitcnt lgkmcnt(2)
v_add_f32 v120, v118, v119
v_add_f32 v121, v118, v119
mav 26,204,38
R 54,1344
mav 30,208,38
v_permlane32_swap_b32 v120, v121
mva 34,38,212
v_add_f32 v114, v120, v121
v_add_f32 v115, v120, v121
mav 26,216,42
R 38,1408
mav 30,220,42
v_permlane16_swap_b32 v114, v115
mva 34,42,224
s_waitcnt lgkmcnt(2)
v_add_f32 v120, v114, v115
mav 26,228,46
R 42,1472
mav 30,232,46
v_rcp_f32 v120, v120
mva 34,46,236
v_mul_f32 v118, v118, v120
v_mul_f32 v119, v119, v120
mav 26,240,50
R 46,1536
v_cvt_pk_bf16_f32 v118, v118, v118
mav 30,244,50
v_cvt_pk_bf16_f32 v119, v119, v119
v_and_b32 v114, v118, v96
mva 34,50,248
s_waitcnt lgkmcnt(2)
v_and_b32 v115, v119, v97
v_and_b32 v116, v118, v94
mav 26,252,54
R 50,1600
v_and_b32 v117, v119, v95
mvv 30,128,54
mvv 34,54,132
v_mfma_f32_16x16x32_bf16 v[70:73], v[8:11], v[114:117], 0
v_mfma_f32_16x16x32_bf16 v[74:77], v[108:111], v[114:117], 0
mvv 26,136,38
R 54,1664
mvv 30,140,38
mvv 34,38,144
s_waitcnt lgkmcnt(2)
mvv 26,148,42
R 38,1728
v_cvt_pk_bf16_f32 v114, v70, v71
mvv 30,152,42
v_cvt_pk_bf16_f32 v115, v72, v73
v_cvt_pk_bf16_f32 v116, v74, v75
mvv 34,42,156
v_cvt_pk_bf16_f32 v117, v76, v77
mvv 26,160,46
R 42,1792
mvv 30,164,46
buffer_store_dwordx4 v[114:117], v82, s[36:39], s49 offen sc1
s_add_u32 s49, s49, 313344
mvv 34,46,168
s_waitcnt lgkmcnt(2)
mvv 26,172,50
R 46,1856
mvv 30,176,50
mvv 34,50,180
mvv 26,184,54
R 50,1920
mvv 30,188,54
s_waitcnt lgkmcnt(2)
mvv 34,54,192
s_mov_b32 s60, -13
.p2align 6
LQA1_2:
mvv 26,196,38
R 54,1984
v_xor_b32 v80, v81, v80
mvv 30,200,38
mvv 34,38,204
mvv 26,208,42
R 38,33024
mvv 30,212,42
ds_read_b64 v[122:123], v83 offset:128
mvv 34,42,216
s_waitcnt lgkmcnt(3)
ds_read_b128 v[70:73], v84 offset:128
mvv 26,220,46
R 42,33088
mvv 30,224,46
ds_read_b128 v[74:77], v84 offset:144
mvv 34,46,228
mvv 26,232,50
R 46,33152
mvv 30,236,50
mvv 34,50,240
s_waitcnt lgkmcnt(3)
mvv 26,244,54
R 50,33216
mvv 30,248,54
mvv 34,54,252
v_mfma_f32_16x16x32_bf16 v[14:17], a[0:3], v[38:41], 0
s_waitcnt lgkmcnt(1)
R 54,33280
v_mul_f32 v71, v71, v99
v_mfma_f32_16x16x32_bf16 v[18:21], a[4:7], v[38:41], 0
v_mul_f32 v73, v73, v99
v_mul_f32 v75, v75, v99
v_mfma_f32_16x16x32_bf16 v[22:25], v[38:41], a[8:11], 0
v_fma_f32 v34, v34, v70, v71
v_fma_f32 v35, v35, v72, v73
mav 14,12,42
R 58,33344
v_fma_f32 v36, v36, v74, v75
mav 18,16,42
v_mul_f32 v77, v77, v99
v_fma_f32 v37, v37, v76, v77
mva 22,42,20
v_cvt_pk_bf16_f32 v10, v34, v35
v_cvt_pk_bf16_f32 v11, v36, v37
mav 14,24,46
R 38,33408
v_mul_f32 v124, v123, v62
v_mfma_f32_16x16x32_bf16 v[70:73], v[100:103], v[0:3], v[86:89]
v_fma_f32 v26, v26, v122, v124
v_mul_f32 v125, v123, v63
mav 18,28,46
v_fma_f32 v27, v27, v122, v125
v_mul_f32 v124, v123, v64
mva 22,46,32
s_waitcnt lgkmcnt(2)
v_fma_f32 v28, v28, v122, v124
v_mul_f32 v125, v123, v65
mav 14,36,50
R 42,33472
v_fma_f32 v29, v29, v122, v125
mav 18,40,50
v_mul_f32 v124, v123, v66
v_fma_f32 v30, v30, v122, v124
mva 22,50,44
v_mul_f32 v125, v123, v67
v_fma_f32 v31, v31, v122, v125
mav 14,48,54
R 46,33536
v_mul_f32 v124, v123, v68
mav 18,52,54
v_fma_f32 v32, v32, v122, v124
v_mul_f32 v125, v123, v69
mva 22,54,56
s_waitcnt lgkmcnt(2)
v_fma_f32 v33, v33, v122, v125
v_add_u32 v78, 1, v78
mav 14,60,58
R 50,33600
mav 18,64,58
mva 22,58,68
mav 14,72,38
R 54,33664
mav 18,76,38
mva 22,38,80
s_waitcnt lgkmcnt(2)
mav 14,84,42
R 58,33728
mav 18,88,42
mva 22,42,92
mav 14,96,46
R 38,33792
mav 18,100,46
s_barrier
mva 22,46,104
s_waitcnt lgkmcnt(2)
ds_read_b128 v[104:107], v82 offset:4096
mav 14,108,50
ds_read_b64 v[110:111], v82 offset:5120
mav 18,112,50
R 42,33856
mva 22,50,116
mav 14,120,54
s_waitcnt vmcnt(0)
R 46,33920
mav 18,124,54
buffer_store_dword v78, off, s[36:39], s52 sc1
mva 22,54,128
s_waitcnt lgkmcnt(3)
v_mfma_f32_16x16x32_bf16 v[74:77], v[104:107], v[0:3], v[90:93]
v_cvt_pk_bf16_f32 v0, v26, v27
v_cvt_pk_bf16_f32 v1, v28, v29
mav 14,132,58
R 50,33984
v_cvt_pk_bf16_f32 v2, v30, v31
mav 18,136,58
v_cvt_pk_bf16_f32 v3, v32, v33
mva 22,58,140
v_max3_f32 v118, v70, v71, v74
v_max3_f32 v119, v72, v73, v76
mav 14,144,38
R 54,34048
v_max_f32 v118, v118, v75
mav 18,148,38
v_max_f32 v119, v119, v77
LsQ12:
s_cbranch_execz LsQ16
v_max_f32 v120, v118, v119
mva 22,38,152
s_waitcnt lgkmcnt(2)
v_max_f32 v121, v118, v119
mav 14,156,42
R 38,34112
mav 18,160,42
v_permlane32_swap_b32 v120, v121
mva 22,42,164
v_max_f32 v114, v120, v121
v_max_f32 v115, v120, v121
mav 14,168,46
R 42,34176
mav 18,172,46
v_permlane16_swap_b32 v114, v115
mva 22,46,176
s_waitcnt lgkmcnt(2)
v_max_f32 v120, v114, v115
v_mul_f32 v120, s65, v120
mav 14,180,50
R 46,34240
v_fma_f32 v118, v118, s65, -v120
mav 18,184,50
v_fma_f32 v119, v119, s65, -v120
mva 22,50,188
v_exp_f32 v118, v118
mav 14,192,54
R 50,34304
mav 18,196,54
v_exp_f32 v119, v119
mva 22,54,200
s_waitcnt lgkmcnt(2)
v_add_f32 v120, v118, v119
v_add_f32 v121, v118, v119
mav 14,204,38
R 54,34368
mav 18,208,38
v_permlane32_swap_b32 v120, v121
mva 22,38,212
v_add_f32 v114, v120, v121
v_add_f32 v115, v120, v121
mav 14,216,42
R 38,34432
mav 18,220,42
v_permlane16_swap_b32 v114, v115
mva 22,42,224
s_waitcnt lgkmcnt(2)
v_add_f32 v120, v114, v115
mav 14,228,46
R 42,34496
mav 18,232,46
v_rcp_f32 v120, v120
mva 22,46,236
v_mul_f32 v118, v118, v120
v_mul_f32 v119, v119, v120
mav 14,240,50
R 46,34560
v_cvt_pk_bf16_f32 v118, v118, v118
mav 18,244,50
v_cvt_pk_bf16_f32 v119, v119, v119
v_and_b32 v114, v118, v94
mva 22,50,248
s_waitcnt lgkmcnt(2)
v_and_b32 v115, v119, v95
v_and_b32 v116, v118, v96
mav 14,252,54
R 50,34624
v_and_b32 v117, v119, v97
mvv 18,128,54
mvv 22,54,132
v_mfma_f32_16x16x32_bf16 v[70:73], v[8:11], v[114:117], 0
v_mfma_f32_16x16x32_bf16 v[74:77], v[108:111], v[114:117], 0
mvv 14,136,38
R 54,34688
mvv 18,140,38
mvv 22,38,144
s_waitcnt lgkmcnt(2)
mvv 14,148,42
R 38,34752
v_cvt_pk_bf16_f32 v114, v70, v71
mvv 18,152,42
v_cvt_pk_bf16_f32 v115, v72, v73
v_cvt_pk_bf16_f32 v116, v74, v75
mvv 22,42,156
v_cvt_pk_bf16_f32 v117, v76, v77
mvv 14,160,46
R 42,34816
mvv 18,164,46
buffer_store_dwordx4 v[114:117], v82, s[36:39], s49 offen sc1
s_add_u32 s49, s49, 313344
mvv 22,46,168
s_waitcnt lgkmcnt(2)
mvv 14,172,50
R 46,34880
mvv 18,176,50
mvv 22,50,180
mvv 14,184,54
R 50,34944
mvv 18,188,54
mvv 22,54,192
s_waitcnt lgkmcnt(2)
mvv 14,196,38
R 54,35008
mvv 18,200,38
mvv 22,38,204
mvv 14,208,42
R 38,0
mvv 18,212,42
ds_read_b64 v[122:123], v83 offset:0
mvv 22,42,216
s_waitcnt lgkmcnt(3)
ds_read_b128 v[70:73], v84 offset:0
mvv 14,220,46
R 42,64
mvv 18,224,46
ds_read_b128 v[74:77], v84 offset:16
mvv 22,46,228
mvv 14,232,50
R 46,128
mvv 18,236,50
mvv 22,50,240
s_waitcnt lgkmcnt(3)
mvv 14,244,54
R 50,192
mvv 18,248,54
mvv 22,54,252
v_mfma_f32_16x16x32_bf16 v[26:29], a[0:3], v[38:41], 0
s_waitcnt lgkmcnt(1)
R 54,256
v_mul_f32 v71, v71, v99
v_mfma_f32_16x16x32_bf16 v[30:33], a[4:7], v[38:41], 0
v_mul_f32 v73, v73, v99
v_mul_f32 v75, v75, v99
v_mfma_f32_16x16x32_bf16 v[34:37], v[38:41], a[8:11], 0
v_fma_f32 v22, v22, v70, v71
v_fma_f32 v23, v23, v72, v73
mav 26,12,42
R 58,320
v_fma_f32 v24, v24, v74, v75
mav 30,16,42
v_mul_f32 v77, v77, v99
v_fma_f32 v25, v25, v76, v77
mva 34,42,20
v_cvt_pk_bf16_f32 v8, v22, v23
v_cvt_pk_bf16_f32 v9, v24, v25
mav 26,24,46
R 38,384
v_mul_f32 v124, v123, v62
v_mfma_f32_16x16x32_bf16 v[70:73], v[104:107], v[0:3], v[86:89]
v_fma_f32 v14, v14, v122, v124
v_mul_f32 v125, v123, v63
mav 30,28,46
v_fma_f32 v15, v15, v122, v125
v_mul_f32 v124, v123, v64
mva 34,46,32
s_waitcnt lgkmcnt(2)
v_fma_f32 v16, v16, v122, v124
v_mul_f32 v125, v123, v65
mav 26,36,50
R 42,448
v_fma_f32 v17, v17, v122, v125
mav 30,40,50
v_mul_f32 v124, v123, v66
v_fma_f32 v18, v18, v122, v124
mva 34,50,44
v_mul_f32 v125, v123, v67
v_fma_f32 v19, v19, v122, v125
mav 26,48,54
R 46,512
v_mul_f32 v124, v123, v68
mav 30,52,54
v_fma_f32 v20, v20, v122, v124
v_mul_f32 v125, v123, v69
mva 34,54,56
s_waitcnt lgkmcnt(2)
v_fma_f32 v21, v21, v122, v125
v_add_u32 v78, 1, v78
mav 26,60,58
R 50,576
mav 30,64,58
mva 34,58,68
mav 26,72,38
R 54,640
mav 30,76,38
mva 34,38,80
s_waitcnt lgkmcnt(2)
mav 26,84,42
R 58,704
mav 30,88,42
mva 34,42,92
mav 26,96,46
R 38,768
mav 30,100,46
s_barrier
mva 34,46,104
s_waitcnt lgkmcnt(2)
ds_read_b128 v[100:103], v82 offset:0
mav 26,108,50
ds_read_b64 v[108:109], v82 offset:1024
mav 30,112,50
R 42,832
mva 34,50,116
mav 26,120,54
s_waitcnt vmcnt(0)
R 46,896
mav 30,124,54
buffer_store_dword v78, off, s[36:39], s52 sc1
mva 34,54,128
s_waitcnt lgkmcnt(3)
v_mfma_f32_16x16x32_bf16 v[74:77], v[100:103], v[0:3], v[90:93]
v_cvt_pk_bf16_f32 v0, v14, v15
v_cvt_pk_bf16_f32 v1, v16, v17
mav 26,132,58
R 50,960
v_cvt_pk_bf16_f32 v2, v18, v19
mav 30,136,58
v_cvt_pk_bf16_f32 v3, v20, v21
mva 34,58,140
v_max3_f32 v118, v70, v71, v74
v_max3_f32 v119, v72, v73, v76
mav 26,144,38
R 54,1024
v_max_f32 v118, v118, v75
mav 30,148,38
v_max_f32 v119, v119, v77
v_max_f32 v120, v118, v119
mva 34,38,152
s_waitcnt lgkmcnt(2)
v_max_f32 v121, v118, v119
mav 26,156,42
R 38,1088
mav 30,160,42
v_permlane32_swap_b32 v120, v121
mva 34,42,164
v_max_f32 v114, v120, v121
v_max_f32 v115, v120, v121
mav 26,168,46
R 42,1152
mav 30,172,46
v_permlane16_swap_b32 v114, v115
mva 34,46,176
s_waitcnt lgkmcnt(2)
v_max_f32 v120, v114, v115
v_mul_f32 v120, s65, v120
mav 26,180,50
R 46,1216
v_fma_f32 v118, v118, s65, -v120
mav 30,184,50
v_fma_f32 v119, v119, s65, -v120
mva 34,50,188
v_exp_f32 v118, v118
mav 26,192,54
R 50,1280
mav 30,196,54
v_exp_f32 v119, v119
mva 34,54,200
s_waitcnt lgkmcnt(2)
v_add_f32 v120, v118, v119
v_add_f32 v121, v118, v119
mav 26,204,38
R 54,1344
mav 30,208,38
v_permlane32_swap_b32 v120, v121
mva 34,38,212
v_add_f32 v114, v120, v121
v_add_f32 v115, v120, v121
mav 26,216,42
R 38,1408
mav 30,220,42
v_permlane16_swap_b32 v114, v115
mva 34,42,224
s_waitcnt lgkmcnt(2)
v_add_f32 v120, v114, v115
mav 26,228,46
R 42,1472
mav 30,232,46
v_rcp_f32 v120, v120
mva 34,46,236
v_mul_f32 v118, v118, v120
v_mul_f32 v119, v119, v120
mav 26,240,50
R 46,1536
v_cvt_pk_bf16_f32 v118, v118, v118
mav 30,244,50
v_cvt_pk_bf16_f32 v119, v119, v119
v_and_b32 v114, v118, v96
mva 34,50,248
s_waitcnt lgkmcnt(2)
v_and_b32 v115, v119, v97
v_and_b32 v116, v118, v94
mav 26,252,54
R 50,1600
v_and_b32 v117, v119, v95
mvv 30,128,54
mvv 34,54,132
v_mfma_f32_16x16x32_bf16 v[70:73], v[8:11], v[114:117], 0
v_mfma_f32_16x16x32_bf16 v[74:77], v[108:111], v[114:117], 0
mvv 26,136,38
R 54,1664
mvv 30,140,38
mvv 34,38,144
s_waitcnt lgkmcnt(2)
mvv 26,148,42
R 38,1728
v_cvt_pk_bf16_f32 v114, v70, v71
mvv 30,152,42
v_cvt_pk_bf16_f32 v115, v72, v73
v_cvt_pk_bf16_f32 v116, v74, v75
mvv 34,42,156
v_cvt_pk_bf16_f32 v117, v76, v77
mvv 26,160,46
R 42,1792
mvv 30,164,46
buffer_store_dwordx4 v[114:117], v82, s[36:39], s49 offen sc1
s_add_u32 s49, s49, 313344
mvv 34,46,168
s_waitcnt lgkmcnt(2)
mvv 26,172,50
R 46,1856
mvv 30,176,50
mvv 34,50,180
mvv 26,184,54
R 50,1920
mvv 30,188,54
s_waitcnt lgkmcnt(2)
s_add_u32 s60, s60, 1
mvv 34,54,192
s_cbranch_scc0 LQA1_2
mvv 26,196,38
R 54,1984
v_xor_b32 v80, v81, v80
mvv 30,200,38
mvv 34,38,204
mvv 26,208,42
R 38,33024
mvv 30,212,42
ds_read_b64 v[122:123], v83 offset:128
mvv 34,42,216
s_waitcnt lgkmcnt(3)
ds_read_b128 v[70:73], v84 offset:128
mvv 26,220,46
R 42,33088
mvv 30,224,46
ds_read_b128 v[74:77], v84 offset:144
mvv 34,46,228
mvv 26,232,50
R 46,33152
mvv 30,236,50
mvv 34,50,240
s_waitcnt lgkmcnt(3)
mvv 26,244,54
R 50,33216
mvv 30,248,54
mvv 34,54,252
v_mfma_f32_16x16x32_bf16 v[14:17], a[0:3], v[38:41], 0
s_waitcnt lgkmcnt(1)
R 54,33280
v_mul_f32 v71, v71, v99
v_mfma_f32_16x16x32_bf16 v[18:21], a[4:7], v[38:41], 0
v_mul_f32 v73, v73, v99
v_mul_f32 v75, v75, v99
v_mfma_f32_16x16x32_bf16 v[22:25], v[38:41], a[8:11], 0
v_fma_f32 v34, v34, v70, v71
v_fma_f32 v35, v35, v72, v73
mav 14,12,42
R 58,33344
v_fma_f32 v36, v36, v74, v75
mav 18,16,42
v_mul_f32 v77, v77, v99
v_fma_f32 v37, v37, v76, v77
mva 22,42,20
v_cvt_pk_bf16_f32 v10, v34, v35
v_cvt_pk_bf16_f32 v11, v36, v37
mav 14,24,46
R 38,33408
v_mul_f32 v124, v123, v62
v_mfma_f32_16x16x32_bf16 v[70:73], v[100:103], v[0:3], v[86:89]
v_fma_f32 v26, v26, v122, v124
v_mul_f32 v125, v123, v63
mav 18,28,46
v_fma_f32 v27, v27, v122, v125
v_mul_f32 v124, v123, v64
mva 22,46,32
s_waitcnt lgkmcnt(2)
v_fma_f32 v28, v28, v122, v124
v_mul_f32 v125, v123, v65
mav 14,36,50
R 42,33472
v_fma_f32 v29, v29, v122, v125
mav 18,40,50
v_mul_f32 v124, v123, v66
v_fma_f32 v30, v30, v122, v124
mva 22,50,44
v_mul_f32 v125, v123, v67
v_fma_f32 v31, v31, v122, v125
mav 14,48,54
R 46,33536
v_mul_f32 v124, v123, v68
mav 18,52,54
v_fma_f32 v32, v32, v122, v124
v_mul_f32 v125, v123, v69
mva 22,54,56
s_waitcnt lgkmcnt(2)
v_fma_f32 v33, v33, v122, v125
v_add_u32 v78, 1, v78
mav 14,60,58
R 50,33600
mav 18,64,58
mva 22,58,68
mav 14,72,38
R 54,33664
mav 18,76,38
mva 22,38,80
s_waitcnt lgkmcnt(2)
mav 14,84,42
R 58,33728
mav 18,88,42
mva 22,42,92
mav 14,96,46
R 38,33792
mav 18,100,46
s_barrier
mva 22,46,104
s_waitcnt lgkmcnt(2)
ds_read_b128 v[104:107], v82 offset:4096
mav 14,108,50
ds_read_b64 v[110:111], v82 offset:5120
mav 18,112,50
R 42,33856
mva 22,50,116
mav 14,120,54
s_waitcnt vmcnt(0)
R 46,33920
mav 18,124,54
buffer_store_dword v78, off, s[36:39], s52 sc1
mva 22,54,128
s_waitcnt lgkmcnt(3)
v_mfma_f32_16x16x32_bf16 v[74:77], v[104:107], v[0:3], v[90:93]
v_cvt_pk_bf16_f32 v0, v26, v27
v_cvt_pk_bf16_f32 v1, v28, v29
mav 14,132,58
R 50,33984
v_cvt_pk_bf16_f32 v2, v30, v31
mav 18,136,58
v_cvt_pk_bf16_f32 v3, v32, v33
mva 22,58,140
v_max3_f32 v118, v70, v71, v74
v_max3_f32 v119, v72, v73, v76
mav 14,144,38
R 54,34048
v_max_f32 v118, v118, v75
mav 18,148,38
v_max_f32 v119, v119, v77
v_max_f32 v120, v118, v119
mva 22,38,152
s_waitcnt lgkmcnt(2)
v_max_f32 v121, v118, v119
mav 14,156,42
R 38,34112
mav 18,160,42
v_permlane32_swap_b32 v120, v121
mva 22,42,164
v_max_f32 v114, v120, v121
v_max_f32 v115, v120, v121
mav 14,168,46
R 42,34176
mav 18,172,46
v_permlane16_swap_b32 v114, v115
mva 22,46,176
s_waitcnt lgkmcnt(2)
v_max_f32 v120, v114, v115
v_mul_f32 v120, s65, v120
mav 14,180,50
R 46,34240
v_fma_f32 v118, v118, s65, -v120
mav 18,184,50
v_fma_f32 v119, v119, s65, -v120
mva 22,50,188
v_exp_f32 v118, v118
mav 14,192,54
R 50,34304
mav 18,196,54
v_exp_f32 v119, v119
mva 22,54,200
s_waitcnt lgkmcnt(2)
v_add_f32 v120, v118, v119
v_add_f32 v121, v118, v119
mav 14,204,38
R 54,34368
mav 18,208,38
v_permlane32_swap_b32 v120, v121
mva 22,38,212
v_add_f32 v114, v120, v121
v_add_f32 v115, v120, v121
mav 14,216,42
R 38,34432
mav 18,220,42
v_permlane16_swap_b32 v114, v115
mva 22,42,224
s_waitcnt lgkmcnt(2)
v_add_f32 v120, v114, v115
mav 14,228,46
R 42,34496
mav 18,232,46
v_rcp_f32 v120, v120
mva 22,46,236
v_mul_f32 v118, v118, v120
v_mul_f32 v119, v119, v120
mav 14,240,50
R 46,34560
v_cvt_pk_bf16_f32 v118, v118, v118
mav 18,244,50
v_cvt_pk_bf16_f32 v119, v119, v119
v_and_b32 v114, v118, v94
mva 22,50,248
s_waitcnt lgkmcnt(2)
v_and_b32 v115, v119, v95
v_and_b32 v116, v118, v96
mav 14,252,54
R 50,34624
v_and_b32 v117, v119, v97
mvv 18,128,54
mvv 22,54,132
v_mfma_f32_16x16x32_bf16 v[70:73], v[8:11], v[114:117], 0
v_mfma_f32_16x16x32_bf16 v[74:77], v[108:111], v[114:117], 0
mvv 14,136,38
R 54,34688
mvv 18,140,38
mvv 22,38,144
s_waitcnt lgkmcnt(2)
mvv 14,148,42
R 38,34752
v_cvt_pk_bf16_f32 v114, v70, v71
mvv 18,152,42
v_cvt_pk_bf16_f32 v115, v72, v73
v_cvt_pk_bf16_f32 v116, v74, v75
mvv 22,42,156
v_cvt_pk_bf16_f32 v117, v76, v77
mvv 14,160,46
R 42,34816
mvv 18,164,46
buffer_store_dwordx4 v[114:117], v82, s[36:39], s49 offen sc1
s_add_u32 s49, s49, 313344
mvv 22,46,168
s_waitcnt lgkmcnt(2)
mvv 14,172,50
R 46,34880
mvv 18,176,50
mvv 22,50,180
mvv 14,184,54
R 50,34944
mvv 18,188,54
mvv 22,54,192
s_waitcnt lgkmcnt(2)
mvv 14,196,38
R 54,35008
mvv 18,200,38
mvv 22,38,204
mvv 14,208,42
R 38,0
mvv 18,212,42
ds_read_b64 v[122:123], v83 offset:0
mvv 22,42,216
s_waitcnt lgkmcnt(3)
ds_read_b128 v[70:73], v84 offset:0
mvv 14,220,46
R 42,64
mvv 18,224,46
ds_read_b128 v[74:77], v84 offset:16
mvv 22,46,228
mvv 14,232,50
R 46,128
mvv 18,236,50
mvv 22,50,240
s_waitcnt lgkmcnt(3)
mvv 14,244,54
R 50,192
mvv 18,248,54
mvv 22,54,252
v_mfma_f32_16x16x32_bf16 v[26:29], a[0:3], v[38:41], 0
s_waitcnt lgkmcnt(1)
R 54,256
v_mul_f32 v71, v71, v99
v_mfma_f32_16x16x32_bf16 v[30:33], a[4:7], v[38:41], 0
v_mul_f32 v73, v73, v99
v_mul_f32 v75, v75, v99
v_mfma_f32_16x16x32_bf16 v[34:37], v[38:41], a[8:11], 0
v_fma_f32 v22, v22, v70, v71
v_fma_f32 v23, v23, v72, v73
LsQ13:
s_cbranch_execz LsQ17
mav 26,12,42
R 58,320
v_fma_f32 v24, v24, v74, v75
mav 30,16,42
v_mul_f32 v77, v77, v99
v_fma_f32 v25, v25, v76, v77
mva 34,42,20
v_cvt_pk_bf16_f32 v8, v22, v23
v_cvt_pk_bf16_f32 v9, v24, v25
mav 26,24,46
R 38,384
v_mul_f32 v124, v123, v62
v_mfma_f32_16x16x32_bf16 v[70:73], v[104:107], v[0:3], v[86:89]
v_fma_f32 v14, v14, v122, v124
v_mul_f32 v125, v123, v63
mav 30,28,46
v_fma_f32 v15, v15, v122, v125
v_mul_f32 v124, v123, v64
mva 34,46,32
s_waitcnt lgkmcnt(2)
v_fma_f32 v16, v16, v122, v124
v_mul_f32 v125, v123, v65
mav 26,36,50
R 42,448
v_fma_f32 v17, v17, v122, v125
mav 30,40,50
v_mul_f32 v124, v123, v66
v_fma_f32 v18, v18, v122, v124
mva 34,50,44
buffer_load_dwordx4 v[4:7], v82, s[36:39], s55 offen offset:2048 sc1
mav 26,48,54
R 46,512
v_mul_f32 v125, v123, v67
mav 30,52,54
buffer_load_dwordx2 v[12:13], v98, s[36:39], s55 offen offset:3072 sc1
mva 34,54,56
s_waitcnt lgkmcnt(2)
v_fma_f32 v19, v19, v122, v125
v_mul_f32 v124, v123, v68
mav 26,60,58
buffer_load_dwordx2 v[112:113], v98, s[36:39], s55 offen offset:3584 sc1
mav 30,64,58
R 50,576
v_fma_f32 v20, v20, v122, v124
mva 34,58,68
v_mul_f32 v125, v123, v69
v_fma_f32 v21, v21, v122, v125
mav 26,72,38
R 54,640
v_add_u32 v78, 1, v78
mav 30,76,38
mva 34,38,80
s_waitcnt lgkmcnt(2)
mav 26,84,42
R 58,704
mav 30,88,42
mva 34,42,92
mav 26,96,46
R 38,768
mav 30,100,46
s_barrier
mva 34,46,104
s_waitcnt lgkmcnt(2)
ds_read_b128 v[100:103], v82 offset:0
mav 26,108,50
ds_read_b64 v[108:109], v82 offset:1024
mav 30,112,50
R 42,832
mva 34,50,116
mav 26,120,54
s_waitcnt vmcnt(3)
R 46,896
mav 30,124,54
buffer_store_dword v78, off, s[36:39], s52 sc1
mva 34,54,128
s_waitcnt lgkmcnt(3)
v_mfma_f32_16x16x32_bf16 v[74:77], v[100:103], v[0:3], v[90:93]
v_cvt_pk_bf16_f32 v0, v14, v15
v_cvt_pk_bf16_f32 v1, v16, v17
mav 26,132,58
R 50,960
v_cvt_pk_bf16_f32 v2, v18, v19
mav 30,136,58
v_cvt_pk_bf16_f32 v3, v20, v21
mva 34,58,140
v_max3_f32 v118, v70, v71, v74
v_max3_f32 v119, v72, v73, v76
mav 26,144,38
R 54,1024
v_max_f32 v118, v118, v75
mav 30,148,38
v_max_f32 v119, v119, v77
v_max_f32 v120, v118, v119
mva 34,38,152
s_waitcnt lgkmcnt(2)
v_max_f32 v121, v118, v119
mav 26,156,42
R 38,1088
mav 30,160,42
v_permlane32_swap_b32 v120, v121
mva 34,42,164
v_max_f32 v114, v120, v121
v_max_f32 v115, v120, v121
mav 26,168,46
R 42,1152
mav 30,172,46
v_permlane16_swap_b32 v114, v115
mva 34,46,176
s_waitcnt lgkmcnt(2)
v_max_f32 v120, v114, v115
v_mul_f32 v120, s65, v120
mav 26,180,50
R 46,1216
v_fma_f32 v118, v118, s65, -v120
mav 30,184,50
v_fma_f32 v119, v119, s65, -v120
mva 34,50,188
v_exp_f32 v118, v118
mav 26,192,54
R 50,1280
mav 30,196,54
v_exp_f32 v119, v119
mva 34,54,200
s_waitcnt lgkmcnt(2)
v_add_f32 v120, v118, v119
v_add_f32 v121, v118, v119
mav 26,204,38
R 54,1344
mav 30,208,38
v_permlane32_swap_b32 v120, v121
mva 34,38,212
v_add_f32 v114, v120, v121
v_add_f32 v115, v120, v121
mav 26,216,42
R 38,1408
mav 30,220,42
v_permlane16_swap_b32 v114, v115
mva 34,42,224
s_waitcnt lgkmcnt(2)
v_add_f32 v120, v114, v115
mav 26,228,46
R 42,1472
mav 30,232,46
v_rcp_f32 v120, v120
mva 34,46,236
v_mul_f32 v118, v118, v120
v_mul_f32 v119, v119, v120
mav 26,240,50
R 46,1536
v_cvt_pk_bf16_f32 v118, v118, v118
mav 30,244,50
v_cvt_pk_bf16_f32 v119, v119, v119
v_and_b32 v114, v118, v96
mva 34,50,248
s_waitcnt lgkmcnt(2)
v_and_b32 v115, v119, v97
v_and_b32 v116, v118, v94
mav 26,252,54
R 50,1600
v_and_b32 v117, v119, v95
mvv 30,128,54
mvv 34,54,132
v_mfma_f32_16x16x32_bf16 v[70:73], v[8:11], v[114:117], 0
v_mfma_f32_16x16x32_bf16 v[74:77], v[108:111], v[114:117], 0
mvv 26,136,38
R 54,1664
mvv 30,140,38
mvv 34,38,144
s_waitcnt lgkmcnt(2)
mvv 26,148,42
R 38,1728
v_cvt_pk_bf16_f32 v114, v70, v71
mvv 30,152,42
v_cvt_pk_bf16_f32 v115, v72, v73
v_cvt_pk_bf16_f32 v116, v74, v75
mvv 34,42,156
v_cvt_pk_bf16_f32 v117, v76, v77
mvv 26,160,46
R 42,1792
mvv 30,164,46
buffer_store_dwordx4 v[114:117], v82, s[36:39], s49 offen sc1
s_add_u32 s49, s49, 313344
mvv 34,46,168
s_waitcnt lgkmcnt(2)
mvv 26,172,50
R 46,1856
mvv 30,176,50
mvv 34,50,180
mvv 26,184,54
R 50,1920
mvv 30,188,54
s_waitcnt lgkmcnt(2)
mvv 34,54,192
mvv 26,196,38
R 54,1984
mvv 30,200,38
mvv 34,38,204
mvv 26,208,42
mvv 30,212,42
ds_read_b64 v[122:123], v83 offset:128
mvv 34,42,216
s_waitcnt lgkmcnt(2)
ds_read_b128 v[70:73], v84 offset:128
mvv 26,220,46
ds_read_b128 v[74:77], v84 offset:144
mvv 30,224,46
mvv 34,46,228
mvv 26,232,50
mvv 30,236,50
mvv 34,50,240
s_waitcnt lgkmcnt(1)
mvv 26,244,54
mvv 30,248,54
mvv 34,54,252
v_mul_f32 v71, v71, v99
s_nop 6
v_fma_f32 v34, v34, v70, v71
v_mul_f32 v73, v73, v99
v_fma_f32 v35, v35, v72, v73
s_waitcnt lgkmcnt(0)
v_mul_f32 v75, v75, v99
v_fma_f32 v36, v36, v74, v75
v_mul_f32 v77, v77, v99
v_fma_f32 v37, v37, v76, v77
v_cvt_pk_bf16_f32 v10, v34, v35
v_cvt_pk_bf16_f32 v11, v36, v37
s_nop 1
v_mfma_f32_16x16x32_bf16 v[70:73], v[100:103], v[0:3], v[86:89]
v_mul_f32 v124, v123, v62
v_fma_f32 v26, v26, v122, v124
v_mul_f32 v125, v123, v63
v_fma_f32 v27, v27, v122, v125
v_mul_f32 v124, v123, v64
v_fma_f32 v28, v28, v122, v124
v_mul_f32 v125, v123, v65
v_fma_f32 v29, v29, v122, v125
v_mul_f32 v124, v123, v66
v_fma_f32 v30, v30, v122, v124
v_mul_f32 v125, v123, v67
v_fma_f32 v31, v31, v122, v125
v_mul_f32 v124, v123, v68
v_fma_f32 v32, v32, v122, v124
v_mul_f32 v125, v123, v69
v_fma_f32 v33, v33, v122, v125
s_barrier
ds_read_b128 v[104:107], v82 offset:4096
ds_read_b64 v[110:111], v82 offset:5120
s_waitcnt lgkmcnt(0)
v_mfma_f32_16x16x32_bf16 v[74:77], v[104:107], v[0:3], v[90:93]
s_nop 7
v_max3_f32 v118, v70, v71, v74
v_max3_f32 v119, v72, v73, v76
v_max_f32 v118, v118, v75
v_max_f32 v119, v119, v77
v_max_f32 v120, v118, v119
v_max_f32 v121, v118, v119
s_nop 1
v_permlane32_swap_b32 v120, v121
v_max_f32 v114, v120, v121
v_max_f32 v115, v120, v121
s_nop 1
v_permlane16_swap_b32 v114, v115
v_max_f32 v120, v114, v115
v_mul_f32 v120, s65, v120
v_fma_f32 v118, v118, s65, -v120
v_fma_f32 v119, v119, s65, -v120
v_exp_f32 v118, v118
v_exp_f32 v119, v119
s_nop 0
v_add_f32 v120, v118, v119
v_add_f32 v121, v118, v119
s_nop 1
v_permlane32_swap_b32 v120, v121
v_add_f32 v114, v120, v121
v_add_f32 v115, v120, v121
s_nop 1
v_permlane16_swap_b32 v114, v115
v_add_f32 v120, v114, v115
v_rcp_f32 v120, v120
s_nop 0
v_mul_f32 v118, v118, v120
v_mul_f32 v119, v119, v120
v_cvt_pk_bf16_f32 v118, v118, v118
v_cvt_pk_bf16_f32 v119, v119, v119
v_and_b32 v114, v118, v94
v_and_b32 v115, v119, v95
v_and_b32 v116, v118, v96
v_and_b32 v117, v119, v97
s_nop 1
v_mfma_f32_16x16x32_bf16 v[70:73], v[8:11], v[114:117], 0
v_mfma_f32_16x16x32_bf16 v[74:77], v[108:111], v[114:117], 0
s_nop 6
v_cvt_pk_bf16_f32 v114, v70, v71
v_cvt_pk_bf16_f32 v115, v72, v73
v_cvt_pk_bf16_f32 v116, v74, v75
v_cvt_pk_bf16_f32 v117, v76, v77
buffer_store_dwordx4 v[114:117], v82, s[36:39], s49 offen sc1
s_add_u32 s49, s49, 313344
v_cvt_pk_bf16_f32 v0, v26, v27
v_cvt_pk_bf16_f32 v1, v28, v29
v_cvt_pk_bf16_f32 v2, v30, v31
v_cvt_pk_bf16_f32 v3, v32, v33
v_add_u32 v78, 1, v78
s_waitcnt vmcnt(1)
buffer_store_dword v78, off, s[36:39], s52 sc1
v_mfma_f32_16x16x32_bf16 v[70:73], v[104:107], v[0:3], v[86:89]
v_min_f32 v74, s56, v90
v_min_f32 v75, s56, v91
v_min_f32 v76, s56, v92
v_min_f32 v77, s56, v93
s_nop 1
mvv 74,4,0
s_nop 7
v_max3_f32 v118, v70, v71, v74
v_max3_f32 v119, v72, v73, v76
v_max_f32 v118, v118, v75
v_max_f32 v119, v119, v77
v_max_f32 v120, v118, v119
v_max_f32 v121, v118, v119
s_nop 1
v_permlane32_swap_b32 v120, v121
v_max_f32 v114, v120, v121
v_max_f32 v115, v120, v121
s_nop 1
v_permlane16_swap_b32 v114, v115
v_max_f32 v120, v114, v115
v_mul_f32 v120, s65, v120
v_fma_f32 v118, v118, s65, -v120
v_fma_f32 v119, v119, s65, -v120
v_exp_f32 v118, v118
v_exp_f32 v119, v119
s_nop 0
v_add_f32 v120, v118, v119
v_add_f32 v121, v118, v119
s_nop 1
v_permlane32_swap_b32 v120, v121
v_add_f32 v114, v120, v121
v_add_f32 v115, v120, v121
s_nop 1
v_permlane16_swap_b32 v114, v115
v_add_f32 v120, v114, v115
v_rcp_f32 v120, v120
s_nop 0
v_mul_f32 v118, v118, v120
v_mul_f32 v119, v119, v120
v_cvt_pk_bf16_f32 v118, v118, v118
v_cvt_pk_bf16_f32 v119, v119, v119
v_and_b32 v114, v118, v94
v_and_b32 v115, v119, v95
v_and_b32 v116, v118, v96
v_and_b32 v117, v119, v97
s_nop 1
v_mfma_f32_16x16x32_bf16 v[70:73], v[10:13], v[114:117], 0
v_mfma_f32_16x16x32_bf16 v[74:77], v[110:113], v[114:117], 0
s_nop 6
v_cvt_pk_bf16_f32 v114, v70, v71
v_cvt_pk_bf16_f32 v115, v72, v73
v_cvt_pk_bf16_f32 v116, v74, v75
v_cvt_pk_bf16_f32 v117, v76, v77
buffer_store_dwordx4 v[114:117], v82, s[36:39], s49 offen sc1
v_add_u32 v78, 1, v78
s_waitcnt vmcnt(2)
buffer_store_dword v78, off, s[36:39], s52 sc1
v_add_u32 v78, 37, v78
s_waitcnt vmcnt(1)
buffer_store_dword v78, off, s[36:39], s52 sc1
s_endpgm
Lr1:
s_cmp_eq_u32 s60, 1
s_cbranch_scc0 Lr2
s_lshr_b32 s66, s53, 10
s_mul_i32 s66, s66, 8208
s_mov_b32 s67, 0x8000
s_mov_b64 exec, 0
s_cmp_eq_u32 s53, 1024
s_cbranch_scc1 LsQ1
s_branch LsQ3
LwQB:
s_mov_b64 exec, -1
E 0,86016
E 1,90112
E 2,94208
s_add_u32 s51, s51, 196608
E 0,98304
E 1,102400
E 2,106496
s_add_u32 s51, s51, 196608
E 0,61440
E 1,65536
s_add_u32 m0, s66, 2816
s_nop 0
buffer_load_dwordx4 v3, s[44:47], s67 offen offset:256 lds
E 2,69632
s_add_u32 s51, s51, 196608
E 0,73728
s_add_u32 m0, s66, 6912
s_nop 0
buffer_load_dwordx4 v3, s[44:47], 0 offen offset:256 lds
E 1,77824
E 2,81920
s_add_u32 s51, s51, 196608
v_lshrrev_b32 v12, 2, v7
v_and_b32 v12, 0xc0, v12
v_xor_b32 v13, 0, v12
v_add_u32 v8, v7, v13
v_xor_b32 v13, 64, v12
v_add_u32 v9, v7, v13
v_xor_b32 v13, 128, v12
v_add_u32 v10, v7, v13
v_xor_b32 v13, 192, v12
v_add_u32 v11, v7, v13
v_mov_b32 v112, s64
v_mov_b32 v113, s64
v_mov_b32 v114, s64
v_mov_b32 v115, s64
s_mov_b32 s92, 0xaaaaaaaa
s_mov_b32 s93, s92
s_mov_b32 s94, 0xcccccccc
s_mov_b32 s95, s94
s_mov_b32 s96, 0xf0000f
s_mov_b32 s97, 0xf0000f00
s_waitcnt vmcnt(14)
s_barrier
v_add_u32 v4, 69632, v4
v_add_u32 v5, 69632, v5
v_add_u32 v6, 69632, v6
Ta 0,4,40960
Ta 4,5,40960
Ta 8,6,40960
Ta 12,4,53248
Ta 16,5,53248
Ta 20,6,53248
ds_read_b128 v[46:49], v8 offset:0
ds_read_b128 v[50:53], v9 offset:0
ds_read_b128 v[54:57], v8 offset:4096
ds_read_b128 v[58:61], v9 offset:4096
v_accvgpr_read_b32 v117, a255
v_mov_b32 v116, 0x100
v_add_u32 v117, 0x9e3779b9, v117
buffer_store_dwordx2 v[116:117], v82, s[36:39], s52 offen sc1
v_add_u32 v93, v93, v116
E 0,36864
E 1,40960
E 2,45056
s_add_u32 s51, s51, 196608
E 0,49152
E 1,53248
E 2,57344
s_add_u32 s51, s51, 196608
s_waitcnt lgkmcnt(0)
s_waitcnt vmcnt(15)
s_barrier
E 0,110592
Ta 24,4,16384
v_mfma_f32_16x16x32_bf16 v[108:111], v[112:115], v[54:57], 0
v_mfma_f32_16x16x32_bf16 v[18:21], v[54:57], v[54:57], 0
Ta 28,5,16384
mvv 108,112,58
Ta 32,6,16384
E 1,114688
mvv 18,58,58
Ta 36,4,28672
v_mfma_f32_16x16x32_bf16 v[96:99], a[0:3], v[112:115], 0
v_mfma_f32_16x16x32_bf16 v[100:103], a[4:7], v[112:115], 0
Ta 40,5,28672
s_add_u32 m0, s66, 3584
s_nop 0
buffer_load_dwordx4 v3, s[44:47], s67 offen offset:512 lds
v_mfma_f32_16x16x32_bf16 v[104:107], v[112:115], a[8:11], 0
Ta 44,6,28672
v_mfma_f32_16x16x32_bf16 v[22:25], a[0:3], v[46:49], 0
ds_read_b128 v[62:65], v10 offset:0
v_mfma_f32_16x16x32_bf16 v[26:29], a[4:7], v[46:49], 0
E 2,118784
s_add_u32 s51, s51, 196608
v_mfma_f32_16x16x32_bf16 v[30:33], v[46:49], a[8:11], 0
ds_read_b128 v[66:69], v11 offset:0
v_mfma_f32_16x16x32_bf16 v[34:37], a[0:3], v[54:57], 0
ds_read_b128 v[70:73], v10 offset:4096
E 0,122880
v_mfma_f32_16x16x32_bf16 v[38:41], a[4:7], v[54:57], 0
ds_read_b128 v[74:77], v11 offset:4096
v_mfma_f32_16x16x32_bf16 v[42:45], v[54:57], a[8:11], 0
mav 96,12,112
s_add_u32 m0, s66, 7680
s_nop 0
buffer_load_dwordx4 v3, s[44:47], 0 offen offset:512 lds
mav 100,16,112
mva 104,112,20
mav 22,12,50
E 1,126976
mav 26,16,50
mva 30,50,20
mav 34,12,58
E 2,131072
s_add_u32 s51, s51, 196608
mav 38,16,58
mva 42,58,20
s_waitcnt lgkmcnt(0)
s_waitcnt vmcnt(15)
s_barrier
v_add_u32 v4, -49152, v4
v_add_u32 v5, -49152, v5
v_add_u32 v6, -49152, v6
E 0,86016
Ta 48,4,40960
mvv 108,112,70
mvv 18,70,70
Ta 52,5,40960
mvv 108,112,74
Ta 56,6,40960
mvv 18,74,74
E 1,90112
Ta 60,4,53248
mav 96,24,112
mav 100,28,112
Ta 64,5,53248
mva 104,112,32
Ta 68,6,53248
mav 22,24,62
E 2,94208
s_add_u32 s51, s51, 196608
ds_read_b128 v[46:49], v8 offset:1024
mav 26,28,62
mva 30,62,32
ds_read_b128 v[50:53], v9 offset:1024
mav 34,24,70
ds_read_b128 v[54:57], v8 offset:5120
E 0,98304
mav 38,28,70
ds_read_b128 v[58:61], v9 offset:5120
mva 42,70,32
mav 96,36,112
mav 100,40,112
E 1,102400
mva 104,112,44
mav 22,36,66
mav 26,40,66
mva 30,66,44
E 2,106496
s_add_u32 s51, s51, 196608
mav 34,36,74
mav 38,40,74
LsQ14:
s_cbranch_execz LsQ18
mva 42,74,44
s_waitcnt lgkmcnt(0)
s_waitcnt vmcnt(14)
s_barrier
E 0,61440
Ta 72,4,16384
mvv 108,112,54
mvv 18,54,54
Ta 76,5,16384
mvv 108,112,58
Ta 80,6,16384
E 1,65536
mvv 18,58,58
Ta 84,4,28672
mav 96,48,112
mav 100,52,112
Ta 88,5,28672
s_add_u32 m0, s66, 4352
s_nop 0
buffer_load_dwordx4 v3, s[44:47], s67 offen offset:768 lds
mva 104,112,56
Ta 92,6,28672
mav 22,48,46
ds_read_b128 v[62:65], v10 offset:1024
mav 26,52,46
E 2,69632
s_add_u32 s51, s51, 196608
mva 30,46,56
ds_read_b128 v[66:69], v11 offset:1024
mav 34,48,54
ds_read_b128 v[70:73], v10 offset:5120
E 0,73728
mav 38,52,54
ds_read_b128 v[74:77], v11 offset:5120
mva 42,54,56
mav 96,60,112
s_add_u32 m0, s66, 8448
s_nop 0
buffer_load_dwordx4 v3, s[44:47], 0 offen offset:768 lds
mav 100,64,112
mva 104,112,68
mav 22,60,50
E 1,77824
mav 26,64,50
mva 30,50,68
mav 34,60,58
E 2,81920
s_add_u32 s51, s51, 196608
mav 38,64,58
mva 42,58,68
s_waitcnt lgkmcnt(0)
s_waitcnt vmcnt(14)
s_barrier
v_add_u32 v4, 49152, v4
v_add_u32 v5, 49152, v5
v_add_u32 v6, 49152, v6
E 0,36864
Ta 96,4,40960
mvv 108,112,70
mvv 18,70,70
Ta 100,5,40960
mvv 108,112,74
Ta 104,6,40960
mvv 18,74,74
E 1,40960
Ta 108,4,53248
mav 96,72,112
mav 100,76,112
Ta 112,5,53248
mva 104,112,80
Ta 116,6,53248
mav 22,72,62
E 2,45056
s_add_u32 s51, s51, 196608
ds_read_b128 v[46:49], v8 offset:2048
mav 26,76,62
mva 30,62,80
ds_read_b128 v[50:53], v9 offset:2048
mav 34,72,70
ds_read_b128 v[54:57], v8 offset:6144
E 0,49152
mav 38,76,70
ds_read_b128 v[58:61], v9 offset:6144
mva 42,70,80
mav 96,84,112
mav 100,88,112
E 1,53248
mva 104,112,92
mav 22,84,66
mav 26,88,66
mva 30,66,92
E 2,57344
s_add_u32 s51, s51, 196608
mav 34,84,74
mav 38,88,74
mva 42,74,92
s_waitcnt lgkmcnt(0)
s_waitcnt vmcnt(14)
s_barrier
E 0,110592
Ta 120,4,16384
mvv 108,112,54
mvv 18,54,54
Ta 124,5,16384
mvv 108,112,58
Ta 128,6,16384
E 1,114688
mvv 18,58,58
Ta 132,4,28672
mav 96,96,112
mav 100,100,112
Ta 136,5,28672
s_add_u32 m0, s66, 1024
s_nop 0
buffer_load_dwordx4 v3, s[44:47], s67 offen offset:1024 lds
mva 104,112,104
Ta 140,6,28672
mav 22,96,46
ds_read_b128 v[62:65], v10 offset:2048
mav 26,100,46
E 2,118784
s_add_u32 s51, s51, 196608
mva 30,46,104
ds_read_b128 v[66:69], v11 offset:2048
mav 34,96,54
ds_read_b128 v[70:73], v10 offset:6144
E 0,122880
mav 38,100,54
ds_read_b128 v[74:77], v11 offset:6144
mva 42,54,104
mav 96,108,112
s_add_u32 m0, s66, 5120
s_nop 0
buffer_load_dwordx4 v3, s[44:47], 0 offen offset:1024 lds
mav 100,112,112
mva 104,112,116
mav 22,108,50
E 1,126976
mav 26,112,50
mva 30,50,116
mav 34,108,58
E 2,131072
s_add_u32 s51, s51, 196608
mav 38,112,58
mva 42,58,116
s_waitcnt lgkmcnt(0)
s_waitcnt vmcnt(14)
s_barrier
v_add_u32 v4, -49152, v4
v_add_u32 v5, -49152, v5
v_add_u32 v6, -49152, v6
E 0,86016
Ta 144,4,40960
mvv 108,112,70
mvv 18,70,70
Ta 148,5,40960
mvv 108,112,74
Ta 152,6,40960
mvv 18,74,74
E 1,90112
Ta 156,4,53248
mav 96,120,112
mav 100,124,112
Ta 160,5,53248
mva 104,112,128
Ta 164,6,53248
mav 22,120,62
E 2,94208
s_add_u32 s51, s51, 196608
ds_read_b128 v[46:49], v8 offset:3072
mav 26,124,62
mva 30,62,128
ds_read_b128 v[50:53], v9 offset:3072
mav 34,120,70
ds_read_b128 v[54:57], v8 offset:7168
E 0,98304
mav 38,124,70
ds_read_b128 v[58:61], v9 offset:7168
mva 42,70,128
mav 96,132,112
mav 100,136,112
E 1,102400
mva 104,112,140
mav 22,132,66
mav 26,136,66
mva 30,66,140
E 2,106496
s_add_u32 s51, s51, 196608
mav 34,132,74
mav 38,136,74
mva 42,74,140
s_waitcnt lgkmcnt(0)
s_waitcnt vmcnt(14)
s_barrier
E 0,61440
Ta 168,4,16384
mvv 108,112,54
mvv 18,54,54
Ta 172,5,16384
mvv 108,112,58
Ta 176,6,16384
E 1,65536
mvv 18,58,58
Ta 180,4,28672
mav 96,144,112
mav 100,148,112
Ta 184,5,28672
s_add_u32 m0, s66, 1792
s_nop 0
buffer_load_dwordx4 v3, s[44:47], s67 offen offset:1280 lds
mva 104,112,152
Ta 188,6,28672
mav 22,144,46
ds_read_b128 v[62:65], v10 offset:3072
mav 26,148,46
E 2,69632
s_add_u32 s51, s51, 196608
mva 30,46,152
ds_read_b128 v[66:69], v11 offset:3072
mav 34,144,54
ds_read_b128 v[70:73], v10 offset:7168
E 0,73728
mav 38,148,54
ds_read_b128 v[74:77], v11 offset:7168
mva 42,54,152
mav 96,156,112
s_add_u32 m0, s66, 5888
s_nop 0
buffer_load_dwordx4 v3, s[44:47], 0 offen offset:1280 lds
mav 100,160,112
mva 104,112,164
mav 22,156,50
E 1,77824
mav 26,160,50
mva 30,50,164
mav 34,156,58
E 2,81920
s_add_u32 s51, s51, 196608
mav 38,160,58
mva 42,58,164
s_waitcnt lgkmcnt(0)
s_waitcnt vmcnt(14)
s_barrier
v_add_u32 v4, 49152, v4
v_add_u32 v5, 49152, v5
v_add_u32 v6, 49152, v6
E 0,36864
Ta 192,4,40960
mvv 108,112,70
mvv 18,70,70
Ta 196,5,40960
mvv 108,112,74
Ta 200,6,40960
mvv 18,74,74
E 1,40960
Ta 204,4,53248
mav 96,168,112
mav 100,172,112
Ta 208,5,53248
mva 104,112,176
Ta 212,6,53248
mav 22,168,62
E 2,45056
s_add_u32 s51, s51, 196608
ds_read_b128 v[46:49], v8 offset:0
mav 26,172,62
mva 30,62,176
ds_read_b128 v[50:53], v9 offset:0
mav 34,168,70
ds_read_b128 v[54:57], v8 offset:4096
E 0,49152
mav 38,172,70
ds_read_b128 v[58:61], v9 offset:4096
mva 42,70,176
mav 96,180,112
mav 100,184,112
E 1,53248
mva 104,112,188
mav 22,180,66
mav 26,184,66
mva 30,66,188
E 2,57344
s_add_u32 s51, s51, 196608
mav 34,180,74
mav 38,184,74
mva 42,74,188
s_waitcnt lgkmcnt(0)
s_waitcnt vmcnt(14)
s_barrier
E 0,110592
Ta 216,4,16384
mvv 108,112,54
mvv 18,54,54
Ta 220,5,16384
mvv 108,112,58
Ta 224,6,16384
E 1,114688
mvv 18,58,58
Ta 228,4,28672
mav 96,192,112
mav 100,196,112
Ta 232,5,28672
s_add_u32 m0, s66, 2560
s_nop 0
buffer_load_dwordx4 v3, s[44:47], s67 offen offset:1536 lds
mva 104,112,200
Ta 236,6,28672
mav 22,192,46
ds_read_b128 v[62:65], v10 offset:0
mav 26,196,46
E 2,118784
s_add_u32 s51, s51, 196608
mva 30,46,200
ds_read_b128 v[66:69], v11 offset:0
mav 34,192,54
ds_read_b128 v[70:73], v10 offset:4096
E 0,122880
mav 38,196,54
ds_read_b128 v[74:77], v11 offset:4096
mva 42,54,200
mav 96,204,112
s_add_u32 m0, s66, 6656
s_nop 0
buffer_load_dwordx4 v3, s[44:47], 0 offen offset:1536 lds
mav 100,208,112
mva 104,112,212
mav 22,204,50
E 1,126976
mav 26,208,50
mva 30,50,212
mav 34,204,58
E 2,131072
s_add_u32 s51, s51, 196608
mav 38,208,58
mva 42,58,212
s_waitcnt lgkmcnt(0)
s_waitcnt vmcnt(14)
s_barrier
v_add_u32 v4, -49152, v4
v_add_u32 v5, -49152, v5
v_add_u32 v6, -49152, v6
E 0,86016
Ta 240,4,40960
mvv 108,112,70
mvv 18,70,70
Ta 244,5,40960
mvv 108,112,74
Ta 248,6,40960
mvv 18,74,74
E 1,90112
Ta 252,4,53248
mav 96,216,112
mav 100,220,112
Tv 128,5,53248
mva 104,112,224
Tv 132,6,53248
mav 22,216,62
E 2,94208
s_add_u32 s51, s51, 196608
ds_read_b128 v[46:49], v8 offset:1024
mav 26,220,62
mva 30,62,224
ds_read_b128 v[50:53], v9 offset:1024
mav 34,216,70
ds_read_b128 v[54:57], v8 offset:5120
E 0,98304
mav 38,220,70
ds_read_b128 v[58:61], v9 offset:5120
mva 42,70,224
mav 96,228,112
mav 100,232,112
E 1,102400
mva 104,112,236
mav 22,228,66
mav 26,232,66
mva 30,66,236
E 2,106496
s_add_u32 s51, s51, 196608
mav 34,228,74
mav 38,232,74
mva 42,74,236
s_waitcnt lgkmcnt(0)
s_waitcnt vmcnt(14)
s_barrier
E 0,61440
Tv 136,4,16384
mvv 108,112,54
mvv 18,54,54
Tv 140,5,16384
mvv 108,112,58
Tv 144,6,16384
E 1,65536
mvv 18,58,58
Tv 148,4,28672
mav 96,240,112
mav 100,244,112
Tv 152,5,28672
s_add_u32 m0, s66, 3328
s_nop 0
buffer_load_dwordx4 v3, s[44:47], s67 offen offset:1792 lds
mva 104,112,248
Tv 156,6,28672
LsQ15:
s_cbranch_execz LsQ19
mav 22,240,46
ds_read_b128 v[62:65], v10 offset:1024
mav 26,244,46
E 2,69632
s_add_u32 s51, s51, 196608
mva 30,46,248
ds_read_b128 v[66:69], v11 offset:1024
mav 34,240,54
ds_read_b128 v[70:73], v10 offset:5120
E 0,73728
mav 38,244,54
ds_read_b128 v[74:77], v11 offset:5120
mva 42,54,248
mav 96,252,112
s_add_u32 m0, s66, 7424
s_nop 0
buffer_load_dwordx4 v3, s[44:47], 0 offen offset:1792 lds
mvv 100,128,112
mvv 104,112,132
mav 22,252,50
E 1,77824
mvv 26,128,50
mvv 30,50,132
mav 34,252,58
E 2,81920
s_add_u32 s51, s51, 196608
mvv 38,128,58
mvv 42,58,132
s_waitcnt lgkmcnt(0)
s_waitcnt vmcnt(14)
s_barrier
v_add_u32 v4, 49152, v4
v_add_u32 v5, 49152, v5
v_add_u32 v6, 49152, v6
E 0,36864
Tv 160,4,40960
mvv 108,112,70
mvv 18,70,70
Tv 164,5,40960
mvv 108,112,74
Tv 168,6,40960
mvv 18,74,74
E 1,40960
Tv 172,4,53248
mvv 96,136,112
mvv 100,140,112
Tv 176,5,53248
mvv 104,112,144
E 2,45056
s_add_u32 s51, s51, 196608
Tv 180,6,53248
mvv 22,136,62
ds_read_b128 v[46:49], v8 offset:2048
mvv 26,140,62
mvv 30,62,144
ds_read_b128 v[50:53], v9 offset:2048
E 0,49152
mvv 34,136,70
ds_read_b128 v[54:57], v8 offset:6144
mvv 38,140,70
ds_read_b128 v[58:61], v9 offset:6144
mvv 42,70,144
E 1,53248
mvv 96,148,112
mvv 100,152,112
mvv 104,112,156
E 2,57344
s_add_u32 s51, s51, 196608
mvv 22,148,66
mvv 26,152,66
mvv 30,66,156
buffer_load_dwordx2 v[122:123], v92, s[36:39], 0 offen sc1
mvv 34,148,74
mvv 38,152,74
mvv 42,74,156
s_waitcnt lgkmcnt(0)
s_waitcnt vmcnt(15)
s_barrier
s_add_u32 m0, s50, 49536
s_nop 0
D2 89,54,0
Tv 184,4,16384
mvv 108,112,54
mvv 18,54,54
Tv 188,5,16384
mvv 108,112,58
Tv 192,6,16384
D2 89,54,1024
mvv 18,58,58
Tv 196,4,28672
mvv 96,160,112
mvv 100,164,112
Tv 200,5,28672
D2 89,55,2048
mvv 104,112,168
Tv 204,6,28672
mvv 22,160,46
ds_read_b128 v[62:65], v10 offset:2048
mvv 26,164,46
D2 89,55,3072
mvv 30,46,168
ds_read_b128 v[66:69], v11 offset:2048
mvv 34,160,54
ds_read_b128 v[70:73], v10 offset:6144
s_add_u32 m0, s50, 57792
s_nop 0
D2 90,54,0
mvv 38,164,54
ds_read_b128 v[74:77], v11 offset:6144
mvv 42,54,168
mvv 96,172,112
D2 90,54,1024
mvv 100,176,112
mvv 104,112,180
mvv 22,172,50
D2 90,55,2048
mvv 26,176,50
mvv 30,50,180
mvv 34,172,58
D2 90,55,3072
mvv 38,176,58
mvv 42,58,180
s_waitcnt lgkmcnt(0)
s_waitcnt vmcnt(15)
s_barrier
v_add_u32 v4, -49152, v4
v_add_u32 v5, -49152, v5
v_add_u32 v6, -49152, v6
s_add_u32 m0, s50, 33024
s_nop 0
D2 87,54,0
Tv 208,4,40960
mvv 108,112,70
mvv 18,70,70
Tv 212,5,40960
mvv 108,112,74
Tv 216,6,40960
D2 87,54,1024
mvv 18,74,74
Tv 220,4,53248
mvv 96,184,112
mvv 100,188,112
Tv 224,5,53248
D2 87,55,2048
mvv 104,112,192
Tv 228,6,53248
mvv 22,184,62
ds_read_b128 v[46:49], v8 offset:3072
mvv 26,188,62
D2 87,55,3072
mvv 30,62,192
ds_read_b128 v[50:53], v9 offset:3072
mvv 34,184,70
ds_read_b128 v[54:57], v8 offset:7168
s_add_u32 m0, s50, 41280
s_nop 0
D2 88,54,0
mvv 38,188,70
ds_read_b128 v[58:61], v9 offset:7168
mvv 42,70,192
mvv 96,196,112
D2 88,54,1024
mvv 100,200,112
mvv 104,112,204
mvv 22,196,66
D2 88,55,2048
mvv 26,200,66
mvv 30,66,204
mvv 34,196,74
D2 88,55,3072
mvv 38,200,74
mvv 42,74,204
s_waitcnt lgkmcnt(0)
s_waitcnt vmcnt(17)
s_barrier
Tv 232,4,16384
Tv 236,5,16384
Tv 240,6,16384
Tv 244,4,28672
Tv 248,5,28672
Tv 252,6,28672
ds_read_b128 v[62:65], v10 offset:3072
ds_read_b128 v[66:69], v11 offset:3072
ds_read_b128 v[70:73], v10 offset:7168
ds_read_b128 v[74:77], v11 offset:7168
s_lshr_b32 s60, s53, 11
s_mul_i32 s60, s60, 313344
s_add_u32 s60, s60, 626688
s_add_u32 s60, s60, s49
s_sub_u32 s54, s54, 0x8000
s_sub_u32 s55, s55, 0x8000
s_waitcnt vmcnt(16)
ds_write_b32 v83, v122
v_sub_u32 v122, v122, v93
v_xor_b32 v123, v123, v117
v_cmp_lt_u64 vcc, s[62:63], v[122:123]
s_cbranch_vccnz Cq
Bq:
v_add_u32 v93, 1, v93
buffer_load_dwordx2 v[14:15], v94, s[36:39], s60 offen sc1
mvv 108,112,54
mvv 18,54,54
mvv 108,112,58
s_add_u32 m0, s50, 0
s_nop 0
D2 87,54,0
mvv 18,58,58
mvv 96,208,112
D2 87,54,1024
mvv 100,212,112
mvv 104,112,216
mvv 22,208,46
D2 87,55,2048
mvv 26,212,46
mvv 30,46,216
D2 87,55,3072
mvv 34,208,54
mvv 38,212,54
mvv 42,54,216
s_add_u32 m0, s50, 8256
s_nop 0
D2 88,54,0
mvv 96,220,112
mvv 100,224,112
D2 88,54,1024
mvv 104,112,228
mvv 22,220,50
mvv 26,224,50
D2 88,55,2048
mvv 30,50,228
mvv 34,220,58
D2 88,55,3072
mvv 38,224,58
mvv 42,58,228
s_waitcnt lgkmcnt(0)
s_add_u32 m0, s50, 16512
s_nop 0
D2 89,54,0
mvv 108,112,70
mvv 18,70,70
mvv 108,112,74
D2 89,54,1024
mvv 18,74,74
mvv 96,232,112
mvv 100,236,112
D2 89,55,2048
mvv 104,112,240
mvv 22,232,62
)"
R"(
mvv 26,236,62
D2 89,55,3072
mvv 30,62,240
mvv 34,232,70
s_add_u32 m0, s50, 24768
s_nop 0
D2 90,54,0
mvv 38,236,70
mvv 42,70,240
v_cndmask_b32 v18, v18, v19, s[92:93]
mvv 96,244,112
v_cndmask_b32 v20, v20, v21, s[92:93]
D2 90,54,1024
mvv 100,248,112
v_mul_f32 v108, 0x3a800000, v108
v_cndmask_b32 v18, v18, v20, s[94:95]
mvv 104,112,252
v_mul_f32 v18, 0x3a800000, v18
mvv 22,244,66
D2 90,55,2048
v_fma_f32 v18, -v108, v108, v18
mvv 26,248,66
v_max_f32 v18, 0, v18
mvv 30,66,252
v_add_f32 v18, 0x3727c5ac, v18
mvv 34,244,74
D2 90,55,3072
v_rsq_f32 v18, v18
mvv 38,248,74
s_nop 0
v_mul_f32_e64 v19, -v108, v18
mvv 42,74,252
s_mov_b64 exec, s[96:97]
ds_write_b64 v85, v[18:19] offset:128
s_mov_b64 exec, -1
s_waitcnt lgkmcnt(0)
s_barrier
s_nop 7
v_mov_b32 v95, v104
s_add_u32 s40, s36, 4194304
s_addc_u32 s41, s37, 0
s_and_b32 s60, s48, 3
s_cmp_eq_u32 s60, 1
s_cbranch_scc1 LvQB1
LvQB0:
LsQ16:
s_cbranch_execz LsQ20
ds_read_b64 v[118:119], v85 offset:0
ds_read_b128 v[104:107], v86 offset:0
ds_read_b128 v[108:111], v86 offset:16
s_waitcnt lgkmcnt(0)
v_mul_f32 v120, v119, v96
v_fma_f32 v22, v22, v118, v120
v_mul_f32 v121, v119, v97
v_fma_f32 v23, v23, v118, v121
v_mul_f32 v120, v119, v98
v_fma_f32 v24, v24, v118, v120
v_mul_f32 v121, v119, v99
v_fma_f32 v25, v25, v118, v121
v_mul_f32 v120, v119, v100
v_fma_f32 v26, v26, v118, v120
v_mul_f32 v121, v119, v101
v_fma_f32 v27, v27, v118, v121
v_mul_f32 v120, v119, v102
v_fma_f32 v28, v28, v118, v120
v_mul_f32 v121, v119, v103
v_fma_f32 v29, v29, v118, v121
v_cvt_pk_bf16_f32 v112, v22, v23
v_cvt_pk_bf16_f32 v113, v24, v25
v_cvt_pk_bf16_f32 v114, v26, v27
v_cvt_pk_bf16_f32 v115, v28, v29
ds_write_b128 v84, v[112:115] offset:0
v_mul_f32 v105, v105, v95
v_fma_f32 v30, v30, v104, v105
v_mul_f32 v107, v107, v95
v_fma_f32 v31, v31, v106, v107
v_mul_f32 v109, v109, v95
v_fma_f32 v32, v32, v108, v109
v_mul_f32 v111, v111, v95
v_fma_f32 v33, v33, v110, v111
v_cvt_pk_bf16_f32 v78, v30, v31
v_cvt_pk_bf16_f32 v79, v32, v33
ds_write_b64 v84, v[78:79] offset:1024
s_xor_b32 s50, s50, s57
s_waitcnt vmcnt(17) lgkmcnt(0)
s_barrier
R 46,33024
R 50,33088
R 54,33152
R 58,33216
s_waitcnt vmcnt(16)
v_permlane32_swap_b32 v14, v15
v_add_f32 v16, v14, v15
v_add_f32 v17, v14, v15
s_nop 1
v_permlane16_swap_b32 v16, v17
v_add_f32 v14, v16, v17
v_add_f32 v15, v16, v17
s_nop 1
v_permlane32_swap_b32 v14, v15
v_mul_f32 v16, 0x3a800000, v14
v_mul_f32 v17, 0x3a800000, v15
v_fma_f32 v17, -v16, v16, v17
v_max_f32 v17, 0, v17
v_add_f32 v17, 0x3727c5ac, v17
v_rsq_f32 v14, v17
s_nop 0
v_mul_f32_e64 v15, -v16, v14
ds_write_b64 v85, v[14:15] offset:0
ds_read_b64 v[118:119], v85 offset:128
ds_read_b128 v[104:107], v86 offset:128
ds_read_b128 v[108:111], v86 offset:144
s_waitcnt lgkmcnt(0)
v_mfma_f32_16x16x32_bf16 v[22:25], a[0:3], v[46:49], 0
R 62,33280
v_mul_f32 v120, v119, v96
v_mfma_f32_16x16x32_bf16 v[26:29], a[4:7], v[46:49], 0
v_fma_f32 v34, v34, v118, v120
v_mul_f32 v121, v119, v97
v_mfma_f32_16x16x32_bf16 v[30:33], v[46:49], a[8:11], 0
v_fma_f32 v35, v35, v118, v121
v_mul_f32 v120, v119, v98
mav 22,12,50
R 66,33344
v_fma_f32 v36, v36, v118, v120
mav 26,16,50
v_mul_f32 v121, v119, v99
v_fma_f32 v37, v37, v118, v121
mva 30,50,20
v_mul_f32 v120, v119, v100
v_fma_f32 v38, v38, v118, v120
mav 22,24,54
R 70,33408
v_mul_f32 v121, v119, v101
mav 26,28,54
v_fma_f32 v39, v39, v118, v121
v_mul_f32 v120, v119, v102
mva 30,54,32
v_fma_f32 v40, v40, v118, v120
v_mul_f32 v121, v119, v103
mav 22,36,58
R 74,33472
v_fma_f32 v41, v41, v118, v121
mav 26,40,58
v_cvt_pk_bf16_f32 v112, v34, v35
v_cvt_pk_bf16_f32 v113, v36, v37
mva 30,58,44
s_waitcnt lgkmcnt(2)
v_cvt_pk_bf16_f32 v114, v38, v39
v_cvt_pk_bf16_f32 v115, v40, v41
mav 22,48,62
R 46,33536
v_mul_f32 v105, v105, v95
mav 26,52,62
ds_write_b128 v84, v[112:115] offset:4096
mva 30,62,56
v_fma_f32 v42, v42, v104, v105
v_mul_f32 v107, v107, v95
mav 22,60,66
R 50,33600
v_fma_f32 v43, v43, v106, v107
mav 26,64,66
v_mul_f32 v109, v109, v95
v_fma_f32 v44, v44, v108, v109
mva 30,66,68
s_waitcnt lgkmcnt(3)
v_mul_f32 v111, v111, v95
v_fma_f32 v45, v45, v110, v111
mav 22,72,70
R 54,33664
v_cvt_pk_bf16_f32 v78, v42, v43
mav 26,76,70
v_cvt_pk_bf16_f32 v79, v44, v45
mva 30,70,80
ds_write_b64 v84, v[78:79] offset:5120
mav 22,84,74
R 58,33728
mav 26,88,74
mva 30,74,92
s_waitcnt lgkmcnt(3)
mav 22,96,46
s_waitcnt vmcnt(0) lgkmcnt(1)
R 62,33792
s_mov_b32 m0, 144384
mav 26,100,46
s_barrier
mva 30,46,104
buffer_load_dword v91, s[36:39], 0 offen sc1 lds
mav 22,108,50
s_sub_u32 s54, s54, 0x8000
R 66,33856
s_add_u32 m0, s50, 33024
mav 26,112,50
s_cmp_lt_u32 s54, s56
s_cselect_b64 s[44:45], s[40:41], s[44:45]
mva 30,50,116
mav 22,120,54
D2 87,54,0
mav 26,124,54
s_add_u32 s55, s54, 14336
R 70,33920
mva 30,54,128
s_waitcnt lgkmcnt(2)
D2 87,54,1024
mav 22,132,58
R 74,33984
mav 26,136,58
D2 87,55,2048
mva 30,58,140
mav 22,144,62
D2 87,55,3072
mav 26,148,62
s_add_u32 m0, s50, 41280
R 46,34048
mva 30,62,152
s_waitcnt lgkmcnt(2)
D2 88,54,0
mav 22,156,66
R 50,34112
mav 26,160,66
D2 88,54,1024
mva 30,66,164
mav 22,168,70
D2 88,55,2048
mav 26,172,70
R 54,34176
mva 30,70,176
s_waitcnt lgkmcnt(2)
D2 88,55,3072
mav 22,180,74
s_add_u32 m0, s50, 49536
R 58,34240
mav 26,184,74
D2 89,54,0
mva 30,74,188
mav 22,192,46
D2 89,54,1024
mav 26,196,46
R 62,34304
mva 30,46,200
s_waitcnt lgkmcnt(2)
D2 89,55,2048
mav 22,204,50
R 66,34368
mav 26,208,50
D2 89,55,3072
mva 30,50,212
s_add_u32 m0, s50, 57792
mav 22,216,54
D2 90,54,0
mav 26,220,54
R 70,34432
mva 30,54,224
s_waitcnt lgkmcnt(2)
D2 90,54,1024
mav 22,228,58
R 74,34496
mav 26,232,58
D2 90,55,2048
mva 30,58,236
s_sub_u32 s54, s54, 0x8000
mav 22,240,62
D2 90,55,3072
mav 26,244,62
s_add_u32 m0, s50, 0
R 46,34560
mva 30,62,248
s_waitcnt lgkmcnt(2)
D2 87,54,0
mav 22,252,66
R 50,34624
mvv 26,128,66
D2 87,54,1024
mvv 30,66,132
s_add_u32 s55, s54, 14336
mvv 22,136,70
D2 87,55,2048
LsQ17:
s_cbranch_execz LsQ21
mvv 26,140,70
R 54,34688
mvv 30,70,144
s_waitcnt lgkmcnt(2)
D2 87,55,3072
mvv 22,148,74
s_add_u32 m0, s50, 8256
R 58,34752
mvv 26,152,74
D2 88,54,0
mvv 30,74,156
mvv 22,160,46
D2 88,54,1024
mvv 26,164,46
R 62,34816
mvv 30,46,168
s_waitcnt lgkmcnt(2)
D2 88,55,2048
mvv 22,172,50
R 66,34880
mvv 26,176,50
D2 88,55,3072
mvv 30,50,180
s_add_u32 m0, s50, 16512
mvv 22,184,54
D2 89,54,0
mvv 26,188,54
R 70,34944
mvv 30,54,192
s_waitcnt lgkmcnt(2)
D2 89,54,1024
mvv 22,196,58
R 74,35008
mvv 26,200,58
D2 89,55,2048
mvv 30,58,204
mvv 22,208,62
D2 89,55,3072
mvv 26,212,62
s_add_u32 m0, s50, 24768
R 46,0
mvv 30,62,216
s_waitcnt lgkmcnt(2)
D2 90,54,0
mvv 22,220,66
ds_read_b32 v122, v83 offset:0
mvv 26,224,66
D2 90,54,1024
mvv 30,66,228
R 50,64
mvv 22,232,70
D2 90,55,2048
mvv 26,236,70
R 54,128
mvv 30,70,240
s_waitcnt lgkmcnt(3)
D2 90,55,3072
mvv 22,244,74
R 58,192
mvv 26,248,74
s_waitcnt lgkmcnt(3)
v_cmp_gt_i32 vcc, v93, v122
mvv 30,74,252
ds_read_b64 v[118:119], v85 offset:0
v_mfma_f32_16x16x32_bf16 v[34:37], a[0:3], v[46:49], 0
s_cbranch_vccnz CQB0_1
BQB0_1:
v_mfma_f32_16x16x32_bf16 v[38:41], a[4:7], v[46:49], 0
R 62,256
v_mfma_f32_16x16x32_bf16 v[42:45], v[46:49], a[8:11], 0
s_waitcnt lgkmcnt(3)
ds_read_b128 v[104:107], v86 offset:0
mav 34,12,50
R 66,320
mav 38,16,50
ds_read_b128 v[108:111], v86 offset:16
mva 42,50,20
mav 34,24,54
s_waitcnt lgkmcnt(4)
R 70,384
v_mul_f32 v120, v119, v96
mav 38,28,54
v_fma_f32 v22, v22, v118, v120
v_mul_f32 v121, v119, v97
mva 42,54,32
v_fma_f32 v23, v23, v118, v121
v_mul_f32 v120, v119, v98
mav 34,36,58
s_waitcnt lgkmcnt(2)
R 74,448
v_fma_f32 v24, v24, v118, v120
mav 38,40,58
v_mul_f32 v121, v119, v99
v_fma_f32 v25, v25, v118, v121
mva 42,58,44
s_waitcnt lgkmcnt(2)
v_mul_f32 v120, v119, v100
v_fma_f32 v26, v26, v118, v120
mav 34,48,62
R 46,512
v_mul_f32 v121, v119, v101
mav 38,52,62
v_fma_f32 v27, v27, v118, v121
v_mul_f32 v120, v119, v102
mva 42,62,56
v_fma_f32 v28, v28, v118, v120
v_mul_f32 v121, v119, v103
mav 34,60,66
R 50,576
v_fma_f32 v29, v29, v118, v121
mav 38,64,66
v_cvt_pk_bf16_f32 v112, v22, v23
v_cvt_pk_bf16_f32 v113, v24, v25
mva 42,66,68
s_waitcnt lgkmcnt(2)
v_cvt_pk_bf16_f32 v114, v26, v27
v_cvt_pk_bf16_f32 v115, v28, v29
mav 34,72,70
R 54,640
v_mul_f32 v105, v105, v95
mav 38,76,70
ds_write_b128 v84, v[112:115] offset:0
mva 42,70,80
s_xor_b32 s50, s50, s57
v_fma_f32 v30, v30, v104, v105
v_mul_f32 v107, v107, v95
mav 34,84,74
R 58,704
v_fma_f32 v31, v31, v106, v107
mav 38,88,74
v_mul_f32 v109, v109, v95
v_fma_f32 v32, v32, v108, v109
mva 42,74,92
s_waitcnt lgkmcnt(3)
v_mul_f32 v111, v111, v95
v_fma_f32 v33, v33, v110, v111
mav 34,96,46
s_mov_b32 m0, 144384
R 62,768
v_cvt_pk_bf16_f32 v78, v30, v31
mav 38,100,46
s_sub_u32 s54, s54, 0x8000
v_cvt_pk_bf16_f32 v79, v32, v33
mva 42,46,104
buffer_load_dword v91, s[36:39], 0 offen sc1 lds
mav 34,108,50
s_add_u32 m0, s50, 33024
R 66,832
mav 38,112,50
ds_write_b64 v84, v[78:79] offset:1024
mva 42,50,116
s_waitcnt lgkmcnt(3)
s_cmp_lt_u32 s54, s56
s_cselect_b64 s[44:45], s[40:41], s[44:45]
mav 34,120,54
R 70,896
mav 38,124,54
s_waitcnt vmcnt(17) lgkmcnt(1)
s_add_u32 s55, s54, 14336
mva 42,54,128
s_barrier
mav 34,132,58
R 74,960
mav 38,136,58
D2 87,54,0
mva 42,58,140
mav 34,144,62
D2 87,54,1024
mav 38,148,62
R 46,1024
mva 42,62,152
D2 87,55,2048
mav 34,156,66
R 50,1088
mav 38,160,66
D2 87,55,3072
mva 42,66,164
s_waitcnt lgkmcnt(2)
s_add_u32 m0, s50, 41280
v_add_u32 v93, 1, v93
mav 34,168,70
D2 88,54,0
mav 38,172,70
R 54,1152
mva 42,70,176
D2 88,54,1024
mav 34,180,74
R 58,1216
mav 38,184,74
D2 88,55,2048
mva 42,74,188
s_waitcnt lgkmcnt(2)
mav 34,192,46
D2 88,55,3072
mav 38,196,46
s_add_u32 m0, s50, 49536
R 62,1280
mva 42,46,200
D2 89,54,0
mav 34,204,50
R 66,1344
mav 38,208,50
D2 89,54,1024
mva 42,50,212
s_waitcnt lgkmcnt(2)
mav 34,216,54
D2 89,55,2048
mav 38,220,54
R 70,1408
mva 42,54,224
D2 89,55,3072
mav 34,228,58
s_add_u32 m0, s50, 57792
R 74,1472
mav 38,232,58
D2 90,54,0
mva 42,58,236
s_waitcnt lgkmcnt(2)
mav 34,240,62
D2 90,54,1024
mav 38,244,62
R 46,1536
mva 42,62,248
D2 90,55,2048
mav 34,252,66
R 50,1600
mvv 38,128,66
D2 90,55,3072
mvv 42,66,132
s_waitcnt lgkmcnt(2)
mvv 34,136,70
R 54,1664
mvv 38,140,70
mvv 42,70,144
mvv 34,148,74
R 58,1728
mvv 38,152,74
mvv 42,74,156
s_waitcnt lgkmcnt(2)
mvv 34,160,46
R 62,1792
mvv 38,164,46
mvv 42,46,168
mvv 34,172,50
R 66,1856
mvv 38,176,50
mvv 42,50,180
s_waitcnt lgkmcnt(2)
mvv 34,184,54
R 70,1920
mvv 38,188,54
mvv 42,54,192
mvv 34,196,58
R 74,1984
v_xor_b32 v80, v81, v80
mvv 38,200,58
mvv 42,58,204
s_waitcnt lgkmcnt(2)
mvv 34,208,62
R 46,33024
mvv 38,212,62
ds_read_b32 v122, v83 offset:0
mvv 42,62,216
ds_read_b64 v[118:119], v85 offset:128
mvv 34,220,66
R 50,33088
mvv 38,224,66
ds_read_b128 v[104:107], v86 offset:128
mvv 42,66,228
s_waitcnt lgkmcnt(5)
ds_read_b128 v[108:111], v86 offset:144
mvv 34,232,70
R 54,33152
mvv 38,236,70
mvv 42,70,240
s_waitcnt lgkmcnt(4)
v_cmp_gt_i32 vcc, v93, v122
mvv 34,244,74
R 58,33216
mvv 38,248,74
mvv 42,74,252
s_cbranch_vccnz CQB0_2
BQB0_2:
v_mfma_f32_16x16x32_bf16 v[22:25], a[0:3], v[46:49], 0
R 62,33280
v_mul_f32 v120, v119, v96
v_mfma_f32_16x16x32_bf16 v[26:29], a[4:7], v[46:49], 0
s_waitcnt lgkmcnt(2)
v_fma_f32 v34, v34, v118, v120
v_mul_f32 v121, v119, v97
v_mfma_f32_16x16x32_bf16 v[30:33], v[46:49], a[8:11], 0
v_fma_f32 v35, v35, v118, v121
v_mul_f32 v120, v119, v98
mav 22,12,50
R 66,33344
v_fma_f32 v36, v36, v118, v120
mav 26,16,50
v_mul_f32 v121, v119, v99
v_fma_f32 v37, v37, v118, v121
mva 30,50,20
v_mul_f32 v120, v119, v100
v_fma_f32 v38, v38, v118, v120
mav 22,24,54
R 70,33408
v_mul_f32 v121, v119, v101
mav 26,28,54
v_fma_f32 v39, v39, v118, v121
v_mul_f32 v120, v119, v102
mva 30,54,32
s_waitcnt lgkmcnt(2)
v_fma_f32 v40, v40, v118, v120
v_mul_f32 v121, v119, v103
mav 22,36,58
R 74,33472
v_fma_f32 v41, v41, v118, v121
mav 26,40,58
v_cvt_pk_bf16_f32 v112, v34, v35
v_cvt_pk_bf16_f32 v113, v36, v37
mva 30,58,44
v_cvt_pk_bf16_f32 v114, v38, v39
v_cvt_pk_bf16_f32 v115, v40, v41
mav 22,48,62
R 46,33536
v_mul_f32 v105, v105, v95
mav 26,52,62
ds_write_b128 v84, v[112:115] offset:4096
mva 30,62,56
s_waitcnt lgkmcnt(3)
v_fma_f32 v42, v42, v104, v105
v_mul_f32 v107, v107, v95
mav 22,60,66
R 50,33600
v_fma_f32 v43, v43, v106, v107
mav 26,64,66
v_mul_f32 v109, v109, v95
v_fma_f32 v44, v44, v108, v109
mva 30,66,68
v_mul_f32 v111, v111, v95
v_fma_f32 v45, v45, v110, v111
mav 22,72,70
R 54,33664
v_cvt_pk_bf16_f32 v78, v42, v43
mav 26,76,70
v_cvt_pk_bf16_f32 v79, v44, v45
mva 30,70,80
s_waitcnt lgkmcnt(2)
ds_write_b64 v84, v[78:79] offset:5120
mav 22,84,74
R 58,33728
mav 26,88,74
mva 30,74,92
mav 22,96,46
s_waitcnt vmcnt(17) lgkmcnt(1)
R 62,33792
s_mov_b32 m0, 144384
mav 26,100,46
s_barrier
mva 30,46,104
buffer_load_dword v91, s[36:39], 0 offen sc1 lds
mav 22,108,50
s_sub_u32 s54, s54, 0x8000
R 66,33856
s_add_u32 m0, s50, 0
mav 26,112,50
mva 30,50,116
mav 22,120,54
D2 87,54,0
mav 26,124,54
s_add_u32 s55, s54, 14336
R 70,33920
mva 30,54,128
s_waitcnt lgkmcnt(2)
D2 87,54,1024
mav 22,132,58
R 74,33984
mav 26,136,58
D2 87,55,2048
mva 30,58,140
mav 22,144,62
D2 87,55,3072
mav 26,148,62
s_add_u32 m0, s50, 8256
R 46,34048
mva 30,62,152
s_waitcnt lgkmcnt(2)
D2 88,54,0
mav 22,156,66
R 50,34112
mav 26,160,66
D2 88,54,1024
mva 30,66,164
v_add_u32 v93, 1, v93
mav 22,168,70
D2 88,55,2048
mav 26,172,70
R 54,34176
mva 30,70,176
s_waitcnt lgkmcnt(2)
D2 88,55,3072
mav 22,180,74
s_add_u32 m0, s50, 16512
R 58,34240
mav 26,184,74
D2 89,54,0
mva 30,74,188
mav 22,192,46
D2 89,54,1024
mav 26,196,46
R 62,34304
mva 30,46,200
s_waitcnt lgkmcnt(2)
D2 89,55,2048
mav 22,204,50
R 66,34368
mav 26,208,50
D2 89,55,3072
mva 30,50,212
s_add_u32 m0, s50, 24768
mav 22,216,54
D2 90,54,0
mav 26,220,54
R 70,34432
mva 30,54,224
s_waitcnt lgkmcnt(2)
D2 90,54,1024
mav 22,228,58
R 74,34496
mav 26,232,58
D2 90,55,2048
mva 30,58,236
mav 22,240,62
D2 90,55,3072
mav 26,244,62
R 46,34560
mva 30,62,248
s_waitcnt lgkmcnt(2)
mav 22,252,66
R 50,34624
mvv 26,128,66
mvv 30,66,132
mvv 22,136,70
R 54,34688
mvv 26,140,70
mvv 30,70,144
s_waitcnt lgkmcnt(2)
mvv 22,148,74
R 58,34752
mvv 26,152,74
mvv 30,74,156
mvv 22,160,46
R 62,34816
mvv 26,164,46
mvv 30,46,168
s_waitcnt lgkmcnt(2)
mvv 22,172,50
R 66,34880
mvv 26,176,50
mvv 30,50,180
mvv 22,184,54
R 70,34944
mvv 26,188,54
mvv 30,54,192
s_waitcnt lgkmcnt(2)
mvv 22,196,58
R 74,35008
mvv 26,200,58
mvv 30,58,204
mvv 22,208,62
R 46,0
mvv 26,212,62
ds_read_b32 v122, v83 offset:0
mvv 30,62,216
s_waitcnt lgkmcnt(3)
ds_read_b64 v[118:119], v85 offset:0
mvv 22,220,66
R 50,64
mvv 26,224,66
ds_read_b128 v[104:107], v86 offset:0
mvv 30,66,228
ds_read_b128 v[108:111], v86 offset:16
mvv 22,232,70
R 54,128
mvv 26,236,70
mvv 30,70,240
s_waitcnt lgkmcnt(4)
v_cmp_gt_i32 vcc, v93, v122
mvv 22,244,74
R 58,192
mvv 26,248,74
mvv 30,74,252
s_cbranch_vccnz CQB0_3
BQB0_3:
v_mfma_f32_16x16x32_bf16 v[34:37], a[0:3], v[46:49], 0
R 62,256
v_mul_f32 v120, v119, v96
v_mfma_f32_16x16x32_bf16 v[38:41], a[4:7], v[46:49], 0
s_waitcnt lgkmcnt(2)
v_fma_f32 v22, v22, v118, v120
v_mul_f32 v121, v119, v97
v_mfma_f32_16x16x32_bf16 v[42:45], v[46:49], a[8:11], 0
v_fma_f32 v23, v23, v118, v121
v_mul_f32 v120, v119, v98
mav 34,12,50
R 66,320
v_fma_f32 v24, v24, v118, v120
mav 38,16,50
v_mul_f32 v121, v119, v99
v_fma_f32 v25, v25, v118, v121
mva 42,50,20
v_mul_f32 v120, v119, v100
v_fma_f32 v26, v26, v118, v120
mav 34,24,54
R 70,384
v_mul_f32 v121, v119, v101
mav 38,28,54
v_fma_f32 v27, v27, v118, v121
v_mul_f32 v120, v119, v102
mva 42,54,32
s_waitcnt lgkmcnt(2)
v_fma_f32 v28, v28, v118, v120
v_mul_f32 v121, v119, v103
mav 34,36,58
R 74,448
v_fma_f32 v29, v29, v118, v121
mav 38,40,58
v_cvt_pk_bf16_f32 v112, v22, v23
v_cvt_pk_bf16_f32 v113, v24, v25
mva 42,58,44
v_cvt_pk_bf16_f32 v114, v26, v27
v_cvt_pk_bf16_f32 v115, v28, v29
mav 34,48,62
R 46,512
v_mul_f32 v105, v105, v95
mav 38,52,62
ds_write_b128 v84, v[112:115] offset:0
mva 42,62,56
s_waitcnt lgkmcnt(3)
v_fma_f32 v30, v30, v104, v105
v_mul_f32 v107, v107, v95
mav 34,60,66
R 50,576
v_fma_f32 v31, v31, v106, v107
mav 38,64,66
v_mul_f32 v109, v109, v95
v_fma_f32 v32, v32, v108, v109
mva 42,66,68
v_mul_f32 v111, v111, v95
v_fma_f32 v33, v33, v110, v111
mav 34,72,70
R 54,640
v_cvt_pk_bf16_f32 v78, v30, v31
mav 38,76,70
v_cvt_pk_bf16_f32 v79, v32, v33
mva 42,70,80
s_waitcnt lgkmcnt(2)
ds_write_b64 v84, v[78:79] offset:1024
mav 34,84,74
s_xor_b32 s50, s50, s57
R 58,704
mav 38,88,74
mva 42,74,92
mav 34,96,46
LsQ18:
s_cbranch_execz LsQ22
s_waitcnt vmcnt(17) lgkmcnt(1)
R 62,768
s_mov_b32 m0, 144384
mav 38,100,46
s_barrier
mva 42,46,104
buffer_load_dword v91, s[36:39], 0 offen sc1 lds
mav 34,108,50
s_sub_u32 s54, s54, 0x8000
R 66,832
s_add_u32 m0, s50, 33024
mav 38,112,50
s_cmp_lt_u32 s54, s56
s_cselect_b64 s[44:45], s[40:41], s[44:45]
mva 42,50,116
mav 34,120,54
D2 87,54,0
mav 38,124,54
s_add_u32 s55, s54, 14336
R 70,896
mva 42,54,128
s_waitcnt lgkmcnt(2)
D2 87,54,1024
mav 34,132,58
R 74,960
mav 38,136,58
D2 87,55,2048
mva 42,58,140
mav 34,144,62
D2 87,55,3072
mav 38,148,62
s_add_u32 m0, s50, 41280
R 46,1024
mva 42,62,152
s_waitcnt lgkmcnt(2)
D2 88,54,0
mav 34,156,66
R 50,1088
mav 38,160,66
D2 88,54,1024
mva 42,66,164
v_add_u32 v93, 1, v93
mav 34,168,70
D2 88,55,2048
mav 38,172,70
R 54,1152
mva 42,70,176
s_waitcnt lgkmcnt(2)
D2 88,55,3072
mav 34,180,74
s_add_u32 m0, s50, 49536
R 58,1216
mav 38,184,74
D2 89,54,0
mva 42,74,188
mav 34,192,46
D2 89,54,1024
mav 38,196,46
R 62,1280
mva 42,46,200
s_waitcnt lgkmcnt(2)
D2 89,55,2048
mav 34,204,50
R 66,1344
mav 38,208,50
D2 89,55,3072
mva 42,50,212
s_add_u32 m0, s50, 57792
mav 34,216,54
D2 90,54,0
mav 38,220,54
R 70,1408
mva 42,54,224
s_waitcnt lgkmcnt(2)
D2 90,54,1024
mav 34,228,58
R 74,1472
mav 38,232,58
D2 90,55,2048
mva 42,58,236
mav 34,240,62
D2 90,55,3072
mav 38,244,62
R 46,1536
mva 42,62,248
s_waitcnt lgkmcnt(2)
mav 34,252,66
R 50,1600
mvv 38,128,66
mvv 42,66,132
mvv 34,136,70
R 54,1664
mvv 38,140,70
mvv 42,70,144
s_waitcnt lgkmcnt(2)
mvv 34,148,74
R 58,1728
mvv 38,152,74
mvv 42,74,156
mvv 34,160,46
R 62,1792
mvv 38,164,46
mvv 42,46,168
s_waitcnt lgkmcnt(2)
mvv 34,172,50
R 66,1856
mvv 38,176,50
s_mov_b32 s60, -27
.p2align 6
LQB0_4:
mvv 42,50,180
mvv 34,184,54
R 70,1920
mvv 38,188,54
mvv 42,54,192
s_waitcnt lgkmcnt(2)
mvv 34,196,58
R 74,1984
v_xor_b32 v80, v81, v80
mvv 38,200,58
mvv 42,58,204
mvv 34,208,62
R 46,33024
mvv 38,212,62
ds_read_b32 v122, v83 offset:0
mvv 42,62,216
s_waitcnt lgkmcnt(3)
ds_read_b64 v[118:119], v85 offset:128
mvv 34,220,66
R 50,33088
mvv 38,224,66
ds_read_b128 v[104:107], v86 offset:128
mvv 42,66,228
ds_read_b128 v[108:111], v86 offset:144
mvv 34,232,70
R 54,33152
mvv 38,236,70
mvv 42,70,240
s_waitcnt lgkmcnt(4)
v_cmp_gt_i32 vcc, v93, v122
mvv 34,244,74
R 58,33216
mvv 38,248,74
mvv 42,74,252
s_cbranch_vccnz CQB0_5
BQB0_5:
v_mfma_f32_16x16x32_bf16 v[22:25], a[0:3], v[46:49], 0
R 62,33280
v_mul_f32 v120, v119, v96
v_mfma_f32_16x16x32_bf16 v[26:29], a[4:7], v[46:49], 0
s_waitcnt lgkmcnt(2)
v_fma_f32 v34, v34, v118, v120
v_mul_f32 v121, v119, v97
v_mfma_f32_16x16x32_bf16 v[30:33], v[46:49], a[8:11], 0
v_fma_f32 v35, v35, v118, v121
v_mul_f32 v120, v119, v98
mav 22,12,50
R 66,33344
v_fma_f32 v36, v36, v118, v120
mav 26,16,50
v_mul_f32 v121, v119, v99
v_fma_f32 v37, v37, v118, v121
mva 30,50,20
v_mul_f32 v120, v119, v100
v_fma_f32 v38, v38, v118, v120
mav 22,24,54
R 70,33408
v_mul_f32 v121, v119, v101
mav 26,28,54
v_fma_f32 v39, v39, v118, v121
v_mul_f32 v120, v119, v102
mva 30,54,32
s_waitcnt lgkmcnt(2)
v_fma_f32 v40, v40, v118, v120
v_mul_f32 v121, v119, v103
mav 22,36,58
R 74,33472
v_fma_f32 v41, v41, v118, v121
mav 26,40,58
v_cvt_pk_bf16_f32 v112, v34, v35
v_cvt_pk_bf16_f32 v113, v36, v37
mva 30,58,44
v_cvt_pk_bf16_f32 v114, v38, v39
v_cvt_pk_bf16_f32 v115, v40, v41
mav 22,48,62
R 46,33536
v_mul_f32 v105, v105, v95
mav 26,52,62
ds_write_b128 v84, v[112:115] offset:4096
mva 30,62,56
s_waitcnt lgkmcnt(3)
v_fma_f32 v42, v42, v104, v105
v_mul_f32 v107, v107, v95
mav 22,60,66
R 50,33600
v_fma_f32 v43, v43, v106, v107
mav 26,64,66
v_mul_f32 v109, v109, v95
v_fma_f32 v44, v44, v108, v109
mva 30,66,68
v_mul_f32 v111, v111, v95
v_fma_f32 v45, v45, v110, v111
mav 22,72,70
R 54,33664
v_cvt_pk_bf16_f32 v78, v42, v43
mav 26,76,70
v_cvt_pk_bf16_f32 v79, v44, v45
mva 30,70,80
s_waitcnt lgkmcnt(2)
ds_write_b64 v84, v[78:79] offset:5120
mav 22,84,74
R 58,33728
mav 26,88,74
mva 30,74,92
mav 22,96,46
s_waitcnt vmcnt(17) lgkmcnt(1)
R 62,33792
s_mov_b32 m0, 144384
mav 26,100,46
s_barrier
mva 30,46,104
buffer_load_dword v91, s[36:39], 0 offen sc1 lds
mav 22,108,50
s_sub_u32 s54, s54, 0x8000
R 66,33856
s_add_u32 m0, s50, 0
mav 26,112,50
mva 30,50,116
mav 22,120,54
D2 87,54,0
mav 26,124,54
s_add_u32 s55, s54, 14336
R 70,33920
mva 30,54,128
s_waitcnt lgkmcnt(2)
D2 87,54,1024
mav 22,132,58
R 74,33984
mav 26,136,58
D2 87,55,2048
mva 30,58,140
mav 22,144,62
D2 87,55,3072
mav 26,148,62
s_add_u32 m0, s50, 8256
R 46,34048
mva 30,62,152
s_waitcnt lgkmcnt(2)
D2 88,54,0
mav 22,156,66
R 50,34112
mav 26,160,66
D2 88,54,1024
mva 30,66,164
v_add_u32 v93, 1, v93
mav 22,168,70
D2 88,55,2048
mav 26,172,70
R 54,34176
mva 30,70,176
s_waitcnt lgkmcnt(2)
D2 88,55,3072
mav 22,180,74
s_add_u32 m0, s50, 16512
R 58,34240
mav 26,184,74
D2 89,54,0
mva 30,74,188
mav 22,192,46
D2 89,54,1024
mav 26,196,46
R 62,34304
mva 30,46,200
s_waitcnt lgkmcnt(2)
D2 89,55,2048
mav 22,204,50
R 66,34368
mav 26,208,50
D2 89,55,3072
mva 30,50,212
s_add_u32 m0, s50, 24768
mav 22,216,54
D2 90,54,0
mav 26,220,54
R 70,34432
mva 30,54,224
s_waitcnt lgkmcnt(2)
D2 90,54,1024
mav 22,228,58
R 74,34496
mav 26,232,58
D2 90,55,2048
mva 30,58,236
mav 22,240,62
D2 90,55,3072
mav 26,244,62
R 46,34560
mva 30,62,248
s_waitcnt lgkmcnt(2)
mav 22,252,66
R 50,34624
mvv 26,128,66
mvv 30,66,132
mvv 22,136,70
R 54,34688
mvv 26,140,70
mvv 30,70,144
s_waitcnt lgkmcnt(2)
mvv 22,148,74
R 58,34752
mvv 26,152,74
mvv 30,74,156
mvv 22,160,46
R 62,34816
mvv 26,164,46
mvv 30,46,168
s_waitcnt lgkmcnt(2)
mvv 22,172,50
R 66,34880
mvv 26,176,50
mvv 30,50,180
mvv 22,184,54
R 70,34944
mvv 26,188,54
mvv 30,54,192
s_waitcnt lgkmcnt(2)
mvv 22,196,58
R 74,35008
mvv 26,200,58
mvv 30,58,204
mvv 22,208,62
R 46,0
mvv 26,212,62
ds_read_b32 v122, v83 offset:0
mvv 30,62,216
s_waitcnt lgkmcnt(3)
ds_read_b64 v[118:119], v85 offset:0
mvv 22,220,66
R 50,64
mvv 26,224,66
ds_read_b128 v[104:107], v86 offset:0
mvv 30,66,228
ds_read_b128 v[108:111], v86 offset:16
mvv 22,232,70
R 54,128
mvv 26,236,70
mvv 30,70,240
s_waitcnt lgkmcnt(4)
v_cmp_gt_i32 vcc, v93, v122
mvv 22,244,74
R 58,192
mvv 26,248,74
mvv 30,74,252
s_cbranch_vccnz CQB0_6
BQB0_6:
v_mfma_f32_16x16x32_bf16 v[34:37], a[0:3], v[46:49], 0
R 62,256
v_mul_f32 v120, v119, v96
v_mfma_f32_16x16x32_bf16 v[38:41], a[4:7], v[46:49], 0
s_waitcnt lgkmcnt(2)
v_fma_f32 v22, v22, v118, v120
v_mul_f32 v121, v119, v97
v_mfma_f32_16x16x32_bf16 v[42:45], v[46:49], a[8:11], 0
v_fma_f32 v23, v23, v118, v121
v_mul_f32 v120, v119, v98
mav 34,12,50
R 66,320
v_fma_f32 v24, v24, v118, v120
mav 38,16,50
v_mul_f32 v121, v119, v99
v_fma_f32 v25, v25, v118, v121
mva 42,50,20
v_mul_f32 v120, v119, v100
v_fma_f32 v26, v26, v118, v120
mav 34,24,54
R 70,384
v_mul_f32 v121, v119, v101
mav 38,28,54
v_fma_f32 v27, v27, v118, v121
v_mul_f32 v120, v119, v102
mva 42,54,32
s_waitcnt lgkmcnt(2)
v_fma_f32 v28, v28, v118, v120
v_mul_f32 v121, v119, v103
mav 34,36,58
R 74,448
v_fma_f32 v29, v29, v118, v121
mav 38,40,58
v_cvt_pk_bf16_f32 v112, v22, v23
v_cvt_pk_bf16_f32 v113, v24, v25
mva 42,58,44
v_cvt_pk_bf16_f32 v114, v26, v27
v_cvt_pk_bf16_f32 v115, v28, v29
mav 34,48,62
R 46,512
v_mul_f32 v105, v105, v95
mav 38,52,62
ds_write_b128 v84, v[112:115] offset:0
mva 42,62,56
s_waitcnt lgkmcnt(3)
v_fma_f32 v30, v30, v104, v105
v_mul_f32 v107, v107, v95
mav 34,60,66
R 50,576
v_fma_f32 v31, v31, v106, v107
mav 38,64,66
v_mul_f32 v109, v109, v95
v_fma_f32 v32, v32, v108, v109
mva 42,66,68
v_mul_f32 v111, v111, v95
v_fma_f32 v33, v33, v110, v111
mav 34,72,70
R 54,640
v_cvt_pk_bf16_f32 v78, v30, v31
mav 38,76,70
v_cvt_pk_bf16_f32 v79, v32, v33
mva 42,70,80
s_waitcnt lgkmcnt(2)
ds_write_b64 v84, v[78:79] offset:1024
mav 34,84,74
s_xor_b32 s50, s50, s57
R 58,704
mav 38,88,74
mva 42,74,92
mav 34,96,46
s_waitcnt vmcnt(17) lgkmcnt(1)
R 62,768
s_mov_b32 m0, 144384
mav 38,100,46
s_barrier
mva 42,46,104
buffer_load_dword v91, s[36:39], 0 offen sc1 lds
mav 34,108,50
s_sub_u32 s54, s54, 0x8000
R 66,832
s_add_u32 m0, s50, 33024
mav 38,112,50
s_cmp_lt_u32 s54, s56
s_cselect_b64 s[44:45], s[40:41], s[44:45]
mva 42,50,116
mav 34,120,54
D2 87,54,0
mav 38,124,54
s_add_u32 s55, s54, 14336
R 70,896
mva 42,54,128
s_waitcnt lgkmcnt(2)
D2 87,54,1024
mav 34,132,58
R 74,960
mav 38,136,58
D2 87,55,2048
mva 42,58,140
mav 34,144,62
D2 87,55,3072
mav 38,148,62
s_add_u32 m0, s50, 41280
R 46,1024
mva 42,62,152
s_waitcnt lgkmcnt(2)
D2 88,54,0
mav 34,156,66
R 50,1088
mav 38,160,66
D2 88,54,1024
mva 42,66,164
v_add_u32 v93, 1, v93
mav 34,168,70
D2 88,55,2048
mav 38,172,70
R 54,1152
mva 42,70,176
s_waitcnt lgkmcnt(2)
D2 88,55,3072
mav 34,180,74
s_add_u32 m0, s50, 49536
R 58,1216
mav 38,184,74
D2 89,54,0
mva 42,74,188
mav 34,192,46
D2 89,54,1024
mav 38,196,46
R 62,1280
mva 42,46,200
s_waitcnt lgkmcnt(2)
D2 89,55,2048
mav 34,204,50
R 66,1344
mav 38,208,50
D2 89,55,3072
mva 42,50,212
s_add_u32 m0, s50, 57792
mav 34,216,54
D2 90,54,0
mav 38,220,54
R 70,1408
mva 42,54,224
s_waitcnt lgkmcnt(2)
D2 90,54,1024
mav 34,228,58
R 74,1472
mav 38,232,58
D2 90,55,2048
mva 42,58,236
mav 34,240,62
D2 90,55,3072
mav 38,244,62
R 46,1536
mva 42,62,248
s_waitcnt lgkmcnt(2)
mav 34,252,66
R 50,1600
mvv 38,128,66
mvv 42,66,132
mvv 34,136,70
R 54,1664
mvv 38,140,70
mvv 42,70,144
s_waitcnt lgkmcnt(2)
mvv 34,148,74
R 58,1728
mvv 38,152,74
mvv 42,74,156
mvv 34,160,46
R 62,1792
mvv 38,164,46
mvv 42,46,168
s_waitcnt lgkmcnt(2)
mvv 34,172,50
s_add_u32 s60, s60, 1
R 66,1856
mvv 38,176,50
s_cbranch_scc0 LQB0_4
mvv 42,50,180
mvv 34,184,54
R 70,1920
mvv 38,188,54
mvv 42,54,192
s_waitcnt lgkmcnt(2)
mvv 34,196,58
R 74,1984
v_xor_b32 v80, v81, v80
mvv 38,200,58
mvv 42,58,204
mvv 34,208,62
R 46,33024
mvv 38,212,62
ds_read_b32 v122, v83 offset:0
mvv 42,62,216
s_waitcnt lgkmcnt(3)
ds_read_b64 v[118:119], v85 offset:128
mvv 34,220,66
R 50,33088
mvv 38,224,66
ds_read_b128 v[104:107], v86 offset:128
mvv 42,66,228
LsQ19:
s_cbranch_execz LsQ23
ds_read_b128 v[108:111], v86 offset:144
mvv 34,232,70
R 54,33152
mvv 38,236,70
mvv 42,70,240
s_waitcnt lgkmcnt(4)
v_cmp_gt_i32 vcc, v93, v122
mvv 34,244,74
R 58,33216
mvv 38,248,74
mvv 42,74,252
s_cbranch_vccnz CQB0_7
BQB0_7:
v_mfma_f32_16x16x32_bf16 v[22:25], a[0:3], v[46:49], 0
R 62,33280
v_mul_f32 v120, v119, v96
v_mfma_f32_16x16x32_bf16 v[26:29], a[4:7], v[46:49], 0
s_waitcnt lgkmcnt(2)
v_fma_f32 v34, v34, v118, v120
v_mul_f32 v121, v119, v97
v_mfma_f32_16x16x32_bf16 v[30:33], v[46:49], a[8:11], 0
v_fma_f32 v35, v35, v118, v121
v_mul_f32 v120, v119, v98
mav 22,12,50
R 66,33344
v_fma_f32 v36, v36, v118, v120
mav 26,16,50
v_mul_f32 v121, v119, v99
v_fma_f32 v37, v37, v118, v121
mva 30,50,20
v_mul_f32 v120, v119, v100
v_fma_f32 v38, v38, v118, v120
mav 22,24,54
R 70,33408
v_mul_f32 v121, v119, v101
mav 26,28,54
v_fma_f32 v39, v39, v118, v121
v_mul_f32 v120, v119, v102
mva 30,54,32
s_waitcnt lgkmcnt(2)
v_fma_f32 v40, v40, v118, v120
v_mul_f32 v121, v119, v103
mav 22,36,58
R 74,33472
v_fma_f32 v41, v41, v118, v121
mav 26,40,58
v_cvt_pk_bf16_f32 v112, v34, v35
v_cvt_pk_bf16_f32 v113, v36, v37
mva 30,58,44
v_cvt_pk_bf16_f32 v114, v38, v39
v_cvt_pk_bf16_f32 v115, v40, v41
mav 22,48,62
R 46,33536
v_mul_f32 v105, v105, v95
mav 26,52,62
ds_write_b128 v84, v[112:115] offset:4096
mva 30,62,56
s_waitcnt lgkmcnt(3)
v_fma_f32 v42, v42, v104, v105
v_mul_f32 v107, v107, v95
mav 22,60,66
R 50,33600
v_fma_f32 v43, v43, v106, v107
mav 26,64,66
v_mul_f32 v109, v109, v95
v_fma_f32 v44, v44, v108, v109
mva 30,66,68
v_mul_f32 v111, v111, v95
v_fma_f32 v45, v45, v110, v111
mav 22,72,70
R 54,33664
v_cvt_pk_bf16_f32 v78, v42, v43
mav 26,76,70
v_cvt_pk_bf16_f32 v79, v44, v45
mva 30,70,80
s_waitcnt lgkmcnt(2)
ds_write_b64 v84, v[78:79] offset:5120
mav 22,84,74
R 58,33728
mav 26,88,74
mva 30,74,92
mav 22,96,46
s_waitcnt vmcnt(17) lgkmcnt(1)
R 62,33792
s_mov_b32 m0, 144384
mav 26,100,46
s_barrier
mva 30,46,104
buffer_load_dword v91, s[36:39], 0 offen sc1 lds
mav 22,108,50
s_sub_u32 s54, s54, 0x8000
R 66,33856
s_add_u32 m0, s50, 0
mav 26,112,50
mva 30,50,116
mav 22,120,54
D2 87,54,0
mav 26,124,54
s_add_u32 s55, s54, 14336
R 70,33920
mva 30,54,128
s_waitcnt lgkmcnt(2)
D2 87,54,1024
mav 22,132,58
R 74,33984
mav 26,136,58
D2 87,55,2048
mva 30,58,140
mav 22,144,62
D2 87,55,3072
mav 26,148,62
s_add_u32 m0, s50, 8256
R 46,34048
mva 30,62,152
s_waitcnt lgkmcnt(2)
D2 88,54,0
mav 22,156,66
R 50,34112
mav 26,160,66
D2 88,54,1024
mva 30,66,164
v_add_u32 v93, 1, v93
mav 22,168,70
D2 88,55,2048
mav 26,172,70
R 54,34176
mva 30,70,176
s_waitcnt lgkmcnt(2)
D2 88,55,3072
mav 22,180,74
s_add_u32 m0, s50, 16512
R 58,34240
mav 26,184,74
D2 89,54,0
mva 30,74,188
mav 22,192,46
D2 89,54,1024
mav 26,196,46
R 62,34304
mva 30,46,200
s_waitcnt lgkmcnt(2)
D2 89,55,2048
mav 22,204,50
R 66,34368
mav 26,208,50
D2 89,55,3072
mva 30,50,212
s_add_u32 m0, s50, 24768
mav 22,216,54
D2 90,54,0
mav 26,220,54
R 70,34432
mva 30,54,224
s_waitcnt lgkmcnt(2)
D2 90,54,1024
mav 22,228,58
R 74,34496
mav 26,232,58
D2 90,55,2048
mva 30,58,236
mav 22,240,62
D2 90,55,3072
mav 26,244,62
R 46,34560
mva 30,62,248
s_waitcnt lgkmcnt(2)
mav 22,252,66
R 50,34624
mvv 26,128,66
mvv 30,66,132
mvv 22,136,70
R 54,34688
mvv 26,140,70
mvv 30,70,144
s_waitcnt lgkmcnt(2)
mvv 22,148,74
R 58,34752
mvv 26,152,74
mvv 30,74,156
mvv 22,160,46
R 62,34816
mvv 26,164,46
mvv 30,46,168
s_waitcnt lgkmcnt(2)
mvv 22,172,50
R 66,34880
mvv 26,176,50
mvv 30,50,180
mvv 22,184,54
R 70,34944
mvv 26,188,54
mvv 30,54,192
s_waitcnt lgkmcnt(2)
mvv 22,196,58
R 74,35008
mvv 26,200,58
mvv 30,58,204
mvv 22,208,62
R 46,0
mvv 26,212,62
ds_read_b32 v122, v83 offset:0
mvv 30,62,216
s_waitcnt lgkmcnt(3)
ds_read_b64 v[118:119], v85 offset:0
mvv 22,220,66
R 50,64
mvv 26,224,66
ds_read_b128 v[104:107], v86 offset:0
mvv 30,66,228
ds_read_b128 v[108:111], v86 offset:16
mvv 22,232,70
R 54,128
mvv 26,236,70
mvv 30,70,240
s_waitcnt lgkmcnt(4)
v_cmp_gt_i32 vcc, v93, v122
mvv 22,244,74
R 58,192
mvv 26,248,74
mvv 30,74,252
s_cbranch_vccnz CQB0_8
BQB0_8:
v_mfma_f32_16x16x32_bf16 v[34:37], a[0:3], v[46:49], 0
R 62,256
v_mul_f32 v120, v119, v96
v_mfma_f32_16x16x32_bf16 v[38:41], a[4:7], v[46:49], 0
s_waitcnt lgkmcnt(2)
v_fma_f32 v22, v22, v118, v120
v_mul_f32 v121, v119, v97
v_mfma_f32_16x16x32_bf16 v[42:45], v[46:49], a[8:11], 0
v_fma_f32 v23, v23, v118, v121
v_mul_f32 v120, v119, v98
mav 34,12,50
R 66,320
v_fma_f32 v24, v24, v118, v120
mav 38,16,50
v_mul_f32 v121, v119, v99
v_fma_f32 v25, v25, v118, v121
mva 42,50,20
v_mul_f32 v120, v119, v100
v_fma_f32 v26, v26, v118, v120
mav 34,24,54
R 70,384
v_mul_f32 v121, v119, v101
mav 38,28,54
v_fma_f32 v27, v27, v118, v121
v_mul_f32 v120, v119, v102
mva 42,54,32
s_waitcnt lgkmcnt(2)
v_fma_f32 v28, v28, v118, v120
v_mul_f32 v121, v119, v103
mav 34,36,58
R 74,448
v_fma_f32 v29, v29, v118, v121
mav 38,40,58
v_cvt_pk_bf16_f32 v112, v22, v23
v_cvt_pk_bf16_f32 v113, v24, v25
mva 42,58,44
v_cvt_pk_bf16_f32 v114, v26, v27
v_cvt_pk_bf16_f32 v115, v28, v29
mav 34,48,62
R 46,512
v_mul_f32 v105, v105, v95
mav 38,52,62
ds_write_b128 v84, v[112:115] offset:0
mva 42,62,56
s_waitcnt lgkmcnt(3)
v_fma_f32 v30, v30, v104, v105
v_mul_f32 v107, v107, v95
mav 34,60,66
R 50,576
v_fma_f32 v31, v31, v106, v107
mav 38,64,66
v_mul_f32 v109, v109, v95
v_fma_f32 v32, v32, v108, v109
mva 42,66,68
v_mul_f32 v111, v111, v95
v_fma_f32 v33, v33, v110, v111
mav 34,72,70
R 54,640
v_cvt_pk_bf16_f32 v78, v30, v31
mav 38,76,70
v_cvt_pk_bf16_f32 v79, v32, v33
mva 42,70,80
s_waitcnt lgkmcnt(2)
ds_write_b64 v84, v[78:79] offset:1024
mav 34,84,74
R 58,704
mav 38,88,74
mva 42,74,92
mav 34,96,46
s_waitcnt vmcnt(17) lgkmcnt(1)
R 62,768
mav 38,100,46
s_barrier
mva 42,46,104
mav 34,108,50
R 66,832
mav 38,112,50
mva 42,50,116
mav 34,120,54
R 70,896
mav 38,124,54
mva 42,54,128
s_waitcnt lgkmcnt(2)
mav 34,132,58
R 74,960
mav 38,136,58
mva 42,58,140
mav 34,144,62
R 46,1024
mav 38,148,62
mva 42,62,152
s_waitcnt lgkmcnt(2)
mav 34,156,66
R 50,1088
mav 38,160,66
mva 42,66,164
mav 34,168,70
R 54,1152
mav 38,172,70
mva 42,70,176
s_waitcnt lgkmcnt(2)
mav 34,180,74
R 58,1216
mav 38,184,74
mva 42,74,188
mav 34,192,46
R 62,1280
mav 38,196,46
mva 42,46,200
s_waitcnt lgkmcnt(2)
mav 34,204,50
R 66,1344
mav 38,208,50
mva 42,50,212
mav 34,216,54
R 70,1408
mav 38,220,54
mva 42,54,224
s_waitcnt lgkmcnt(2)
mav 34,228,58
R 74,1472
mav 38,232,58
mva 42,58,236
mav 34,240,62
R 46,1536
mav 38,244,62
mva 42,62,248
s_waitcnt lgkmcnt(2)
mav 34,252,66
R 50,1600
mvv 38,128,66
mvv 42,66,132
mvv 34,136,70
R 54,1664
mvv 38,140,70
mvv 42,70,144
s_waitcnt lgkmcnt(2)
mvv 34,148,74
R 58,1728
mvv 38,152,74
mvv 42,74,156
mvv 34,160,46
R 62,1792
mvv 38,164,46
mvv 42,46,168
s_waitcnt lgkmcnt(2)
mvv 34,172,50
R 66,1856
mvv 38,176,50
mvv 42,50,180
mvv 34,184,54
R 70,1920
mvv 38,188,54
mvv 42,54,192
s_waitcnt lgkmcnt(2)
mvv 34,196,58
R 74,1984
v_xor_b32 v80, v81, v80
mvv 38,200,58
mvv 42,58,204
mvv 34,208,62
R 46,33024
mvv 38,212,62
ds_read_b64 v[118:119], v85 offset:128
mvv 42,62,216
s_waitcnt lgkmcnt(3)
ds_read_b128 v[104:107], v86 offset:128
mvv 34,220,66
R 50,33088
mvv 38,224,66
ds_read_b128 v[108:111], v86 offset:144
mvv 42,66,228
mvv 34,232,70
R 54,33152
mvv 38,236,70
mvv 42,70,240
s_waitcnt lgkmcnt(3)
mvv 34,244,74
R 58,33216
mvv 38,248,74
mvv 42,74,252
v_mfma_f32_16x16x32_bf16 v[22:25], a[0:3], v[46:49], 0
R 62,33280
v_mul_f32 v120, v119, v96
v_mfma_f32_16x16x32_bf16 v[26:29], a[4:7], v[46:49], 0
s_waitcnt lgkmcnt(2)
v_fma_f32 v34, v34, v118, v120
v_mul_f32 v121, v119, v97
v_mfma_f32_16x16x32_bf16 v[30:33], v[46:49], a[8:11], 0
v_fma_f32 v35, v35, v118, v121
v_mul_f32 v120, v119, v98
mav 22,12,50
R 66,33344
v_fma_f32 v36, v36, v118, v120
mav 26,16,50
v_mul_f32 v121, v119, v99
v_fma_f32 v37, v37, v118, v121
mva 30,50,20
v_mul_f32 v120, v119, v100
v_fma_f32 v38, v38, v118, v120
mav 22,24,54
R 70,33408
v_mul_f32 v121, v119, v101
mav 26,28,54
v_fma_f32 v39, v39, v118, v121
v_mul_f32 v120, v119, v102
mva 30,54,32
s_waitcnt lgkmcnt(2)
v_fma_f32 v40, v40, v118, v120
v_mul_f32 v121, v119, v103
mav 22,36,58
R 74,33472
v_fma_f32 v41, v41, v118, v121
mav 26,40,58
v_cvt_pk_bf16_f32 v112, v34, v35
v_cvt_pk_bf16_f32 v113, v36, v37
mva 30,58,44
v_cvt_pk_bf16_f32 v114, v38, v39
v_cvt_pk_bf16_f32 v115, v40, v41
mav 22,48,62
R 46,33536
v_mul_f32 v105, v105, v95
mav 26,52,62
ds_write_b128 v84, v[112:115] offset:4096
mva 30,62,56
s_waitcnt lgkmcnt(3)
v_fma_f32 v42, v42, v104, v105
v_mul_f32 v107, v107, v95
mav 22,60,66
R 50,33600
v_fma_f32 v43, v43, v106, v107
mav 26,64,66
v_mul_f32 v109, v109, v95
v_fma_f32 v44, v44, v108, v109
mva 30,66,68
v_mul_f32 v111, v111, v95
v_fma_f32 v45, v45, v110, v111
mav 22,72,70
R 54,33664
v_cvt_pk_bf16_f32 v78, v42, v43
mav 26,76,70
v_cvt_pk_bf16_f32 v79, v44, v45
mva 30,70,80
s_waitcnt lgkmcnt(2)
ds_write_b64 v84, v[78:79] offset:5120
mav 22,84,74
R 58,33728
mav 26,88,74
mva 30,74,92
mav 22,96,46
s_waitcnt vmcnt(0) lgkmcnt(1)
R 62,33792
mav 26,100,46
s_barrier
mva 30,46,104
mav 22,108,50
R 66,33856
mav 26,112,50
mva 30,50,116
mav 22,120,54
R 70,33920
mav 26,124,54
mva 30,54,128
s_waitcnt lgkmcnt(2)
mav 22,132,58
R 74,33984
mav 26,136,58
mva 30,58,140
mav 22,144,62
R 46,34048
mav 26,148,62
mva 30,62,152
s_waitcnt lgkmcnt(2)
mav 22,156,66
R 50,34112
mav 26,160,66
mva 30,66,164
mav 22,168,70
R 54,34176
mav 26,172,70
mva 30,70,176
s_waitcnt lgkmcnt(2)
mav 22,180,74
R 58,34240
mav 26,184,74
mva 30,74,188
mav 22,192,46
R 62,34304
mav 26,196,46
mva 30,46,200
s_waitcnt lgkmcnt(2)
mav 22,204,50
R 66,34368
mav 26,208,50
mva 30,50,212
mav 22,216,54
R 70,34432
mav 26,220,54
mva 30,54,224
s_waitcnt lgkmcnt(2)
mav 22,228,58
R 74,34496
mav 26,232,58
mva 30,58,236
mav 22,240,62
R 46,34560
mav 26,244,62
mva 30,62,248
s_waitcnt lgkmcnt(2)
mav 22,252,66
R 50,34624
mvv 26,128,66
mvv 30,66,132
mvv 22,136,70
R 54,34688
mvv 26,140,70
mvv 30,70,144
s_waitcnt lgkmcnt(2)
mvv 22,148,74
R 58,34752
mvv 26,152,74
mvv 30,74,156
mvv 22,160,46
R 62,34816
mvv 26,164,46
mvv 30,46,168
s_waitcnt lgkmcnt(2)
mvv 22,172,50
R 66,34880
mvv 26,176,50
mvv 30,50,180
mvv 22,184,54
R 70,34944
mvv 26,188,54
mvv 30,54,192
s_waitcnt lgkmcnt(2)
mvv 22,196,58
R 74,35008
LsQ20:
s_cbranch_execz LsQ24
mvv 26,200,58
mvv 30,58,204
mvv 22,208,62
R 46,0
mvv 26,212,62
ds_read_b64 v[118:119], v85 offset:0
mvv 30,62,216
s_waitcnt lgkmcnt(3)
ds_read_b128 v[104:107], v86 offset:0
mvv 22,220,66
R 50,64
mvv 26,224,66
ds_read_b128 v[108:111], v86 offset:16
mvv 30,66,228
mvv 22,232,70
R 54,128
mvv 26,236,70
mvv 30,70,240
s_waitcnt lgkmcnt(3)
mvv 22,244,74
R 58,192
mvv 26,248,74
mvv 30,74,252
v_mfma_f32_16x16x32_bf16 v[34:37], a[0:3], v[46:49], 0
R 62,256
v_mul_f32 v120, v119, v96
v_mfma_f32_16x16x32_bf16 v[38:41], a[4:7], v[46:49], 0
s_waitcnt lgkmcnt(2)
v_fma_f32 v22, v22, v118, v120
v_mul_f32 v121, v119, v97
v_mfma_f32_16x16x32_bf16 v[42:45], v[46:49], a[8:11], 0
v_fma_f32 v23, v23, v118, v121
v_mul_f32 v120, v119, v98
mav 34,12,50
R 66,320
v_fma_f32 v24, v24, v118, v120
)"
R"(
mav 38,16,50
v_mul_f32 v121, v119, v99
v_fma_f32 v25, v25, v118, v121
mva 42,50,20
v_mul_f32 v120, v119, v100
v_fma_f32 v26, v26, v118, v120
mav 34,24,54
R 70,384
v_mul_f32 v121, v119, v101
mav 38,28,54
v_fma_f32 v27, v27, v118, v121
v_mul_f32 v120, v119, v102
mva 42,54,32
s_waitcnt lgkmcnt(2)
v_fma_f32 v28, v28, v118, v120
v_mul_f32 v121, v119, v103
mav 34,36,58
R 74,448
v_fma_f32 v29, v29, v118, v121
mav 38,40,58
v_cvt_pk_bf16_f32 v112, v22, v23
v_cvt_pk_bf16_f32 v113, v24, v25
mva 42,58,44
v_cvt_pk_bf16_f32 v114, v26, v27
v_cvt_pk_bf16_f32 v115, v28, v29
mav 34,48,62
R 46,512
v_mul_f32 v105, v105, v95
mav 38,52,62
ds_write_b128 v84, v[112:115] offset:0
mva 42,62,56
s_waitcnt lgkmcnt(3)
v_fma_f32 v30, v30, v104, v105
v_mul_f32 v107, v107, v95
mav 34,60,66
R 50,576
v_fma_f32 v31, v31, v106, v107
mav 38,64,66
v_mul_f32 v109, v109, v95
v_fma_f32 v32, v32, v108, v109
mva 42,66,68
v_mul_f32 v111, v111, v95
v_fma_f32 v33, v33, v110, v111
mav 34,72,70
R 54,640
v_cvt_pk_bf16_f32 v78, v30, v31
mav 38,76,70
v_cvt_pk_bf16_f32 v79, v32, v33
mva 42,70,80
s_waitcnt lgkmcnt(2)
ds_write_b64 v84, v[78:79] offset:1024
mav 34,84,74
R 58,704
mav 38,88,74
mva 42,74,92
mav 34,96,46
s_waitcnt lgkmcnt(1)
R 62,768
mav 38,100,46
s_barrier
mva 42,46,104
mav 34,108,50
R 66,832
mav 38,112,50
mva 42,50,116
mav 34,120,54
R 70,896
mav 38,124,54
mva 42,54,128
s_waitcnt lgkmcnt(2)
mav 34,132,58
R 74,960
mav 38,136,58
mva 42,58,140
mav 34,144,62
R 46,1024
mav 38,148,62
mva 42,62,152
s_waitcnt lgkmcnt(2)
mav 34,156,66
R 50,1088
mav 38,160,66
mva 42,66,164
mav 34,168,70
R 54,1152
mav 38,172,70
mva 42,70,176
s_waitcnt lgkmcnt(2)
mav 34,180,74
R 58,1216
mav 38,184,74
mva 42,74,188
mav 34,192,46
R 62,1280
mav 38,196,46
mva 42,46,200
s_waitcnt lgkmcnt(2)
mav 34,204,50
R 66,1344
mav 38,208,50
mva 42,50,212
mav 34,216,54
R 70,1408
mav 38,220,54
mva 42,54,224
s_waitcnt lgkmcnt(2)
mav 34,228,58
R 74,1472
mav 38,232,58
mva 42,58,236
mav 34,240,62
R 46,1536
mav 38,244,62
mva 42,62,248
s_waitcnt lgkmcnt(2)
mav 34,252,66
R 50,1600
mvv 38,128,66
mvv 42,66,132
mvv 34,136,70
R 54,1664
mvv 38,140,70
mvv 42,70,144
s_waitcnt lgkmcnt(2)
mvv 34,148,74
R 58,1728
mvv 38,152,74
mvv 42,74,156
mvv 34,160,46
R 62,1792
mvv 38,164,46
mvv 42,46,168
s_waitcnt lgkmcnt(2)
mvv 34,172,50
R 66,1856
mvv 38,176,50
mvv 42,50,180
mvv 34,184,54
R 70,1920
mvv 38,188,54
mvv 42,54,192
s_waitcnt lgkmcnt(2)
mvv 34,196,58
R 74,1984
mvv 38,200,58
mvv 42,58,204
mvv 34,208,62
mvv 38,212,62
ds_read_b64 v[118:119], v85 offset:128
mvv 42,62,216
s_waitcnt lgkmcnt(2)
ds_read_b128 v[104:107], v86 offset:128
mvv 34,220,66
ds_read_b128 v[108:111], v86 offset:144
mvv 38,224,66
mvv 42,66,228
mvv 34,232,70
mvv 38,236,70
mvv 42,70,240
s_waitcnt lgkmcnt(1)
mvv 34,244,74
mvv 38,248,74
mvv 42,74,252
v_mul_f32 v120, v119, v96
s_nop 4
v_fma_f32 v34, v34, v118, v120
v_mul_f32 v121, v119, v97
v_fma_f32 v35, v35, v118, v121
v_mul_f32 v120, v119, v98
v_fma_f32 v36, v36, v118, v120
v_mul_f32 v121, v119, v99
v_fma_f32 v37, v37, v118, v121
v_mul_f32 v120, v119, v100
v_fma_f32 v38, v38, v118, v120
v_mul_f32 v121, v119, v101
v_fma_f32 v39, v39, v118, v121
v_mul_f32 v120, v119, v102
v_fma_f32 v40, v40, v118, v120
v_mul_f32 v121, v119, v103
v_fma_f32 v41, v41, v118, v121
v_cvt_pk_bf16_f32 v112, v34, v35
v_cvt_pk_bf16_f32 v113, v36, v37
v_cvt_pk_bf16_f32 v114, v38, v39
v_cvt_pk_bf16_f32 v115, v40, v41
ds_write_b128 v84, v[112:115] offset:4096
v_mul_f32 v105, v105, v95
v_fma_f32 v42, v42, v104, v105
v_mul_f32 v107, v107, v95
v_fma_f32 v43, v43, v106, v107
s_waitcnt lgkmcnt(1)
v_mul_f32 v109, v109, v95
v_fma_f32 v44, v44, v108, v109
v_mul_f32 v111, v111, v95
v_fma_f32 v45, v45, v110, v111
v_cvt_pk_bf16_f32 v78, v42, v43
v_cvt_pk_bf16_f32 v79, v44, v45
ds_write_b64 v84, v[78:79] offset:5120
s_waitcnt lgkmcnt(0)
s_barrier
s_endpgm
CQB0_1:
s_mov_b32 s58, m0
s_mov_b32 m0, 144384
DQB0_1:
s_cmp_eq_u32 s61, 0
s_cbranch_scc1 EQB0_1
s_sub_u32 s61, s61, 1
buffer_load_dword v91, s[36:39], 0 offen sc1 lds
s_sleep 1
ds_read_b32 v122, v83
s_waitcnt lgkmcnt(0)
v_cmp_gt_i32 vcc, v93, v122
s_cbranch_vccnz DQB0_1
EQB0_1:
s_mov_b32 m0, s58
s_branch BQB0_1
CQB0_2:
s_mov_b32 s58, m0
s_mov_b32 m0, 144384
DQB0_2:
s_cmp_eq_u32 s61, 0
s_cbranch_scc1 EQB0_2
s_sub_u32 s61, s61, 1
buffer_load_dword v91, s[36:39], 0 offen sc1 lds
s_sleep 1
ds_read_b32 v122, v83
s_waitcnt lgkmcnt(0)
v_cmp_gt_i32 vcc, v93, v122
s_cbranch_vccnz DQB0_2
EQB0_2:
s_mov_b32 m0, s58
s_branch BQB0_2
CQB0_3:
s_mov_b32 s58, m0
s_mov_b32 m0, 144384
DQB0_3:
s_cmp_eq_u32 s61, 0
s_cbranch_scc1 EQB0_3
s_sub_u32 s61, s61, 1
buffer_load_dword v91, s[36:39], 0 offen sc1 lds
s_sleep 1
ds_read_b32 v122, v83
s_waitcnt lgkmcnt(0)
v_cmp_gt_i32 vcc, v93, v122
s_cbranch_vccnz DQB0_3
EQB0_3:
s_mov_b32 m0, s58
s_branch BQB0_3
CQB0_5:
s_mov_b32 s58, m0
s_mov_b32 m0, 144384
DQB0_5:
s_cmp_eq_u32 s61, 0
s_cbranch_scc1 EQB0_5
s_sub_u32 s61, s61, 1
buffer_load_dword v91, s[36:39], 0 offen sc1 lds
s_sleep 1
ds_read_b32 v122, v83
s_waitcnt lgkmcnt(0)
v_cmp_gt_i32 vcc, v93, v122
s_cbranch_vccnz DQB0_5
EQB0_5:
s_mov_b32 m0, s58
s_branch BQB0_5
CQB0_6:
s_mov_b32 s58, m0
s_mov_b32 m0, 144384
DQB0_6:
s_cmp_eq_u32 s61, 0
s_cbranch_scc1 EQB0_6
s_sub_u32 s61, s61, 1
buffer_load_dword v91, s[36:39], 0 offen sc1 lds
s_sleep 1
ds_read_b32 v122, v83
s_waitcnt lgkmcnt(0)
v_cmp_gt_i32 vcc, v93, v122
s_cbranch_vccnz DQB0_6
EQB0_6:
s_mov_b32 m0, s58
s_branch BQB0_6
CQB0_7:
s_mov_b32 s58, m0
s_mov_b32 m0, 144384
DQB0_7:
s_cmp_eq_u32 s61, 0
s_cbranch_scc1 EQB0_7
s_sub_u32 s61, s61, 1
buffer_load_dword v91, s[36:39], 0 offen sc1 lds
s_sleep 1
ds_read_b32 v122, v83
s_waitcnt lgkmcnt(0)
v_cmp_gt_i32 vcc, v93, v122
s_cbranch_vccnz DQB0_7
EQB0_7:
s_mov_b32 m0, s58
s_branch BQB0_7
CQB0_8:
s_mov_b32 s58, m0
s_mov_b32 m0, 144384
DQB0_8:
s_cmp_eq_u32 s61, 0
s_cbranch_scc1 EQB0_8
s_sub_u32 s61, s61, 1
buffer_load_dword v91, s[36:39], 0 offen sc1 lds
s_sleep 1
ds_read_b32 v122, v83
s_waitcnt lgkmcnt(0)
v_cmp_gt_i32 vcc, v93, v122
s_cbranch_vccnz DQB0_8
EQB0_8:
s_mov_b32 m0, s58
s_branch BQB0_8
LvQB1:
LsQ21:
s_cbranch_execz LsQ25
ds_read_b64 v[118:119], v85 offset:0
ds_read_b128 v[104:107], v86 offset:0
ds_read_b128 v[108:111], v86 offset:16
s_add_u32 s49, s49, 313344
s_waitcnt lgkmcnt(0)
v_mul_f32 v120, v119, v96
v_fma_f32 v22, v22, v118, v120
v_mul_f32 v121, v119, v97
v_fma_f32 v23, v23, v118, v121
v_mul_f32 v120, v119, v98
v_fma_f32 v24, v24, v118, v120
v_mul_f32 v121, v119, v99
v_fma_f32 v25, v25, v118, v121
v_mul_f32 v120, v119, v100
v_fma_f32 v26, v26, v118, v120
v_mul_f32 v121, v119, v101
v_fma_f32 v27, v27, v118, v121
v_mul_f32 v120, v119, v102
v_fma_f32 v28, v28, v118, v120
v_mul_f32 v121, v119, v103
v_fma_f32 v29, v29, v118, v121
v_cvt_pk_bf16_f32 v112, v22, v23
v_cvt_pk_bf16_f32 v113, v24, v25
v_cvt_pk_bf16_f32 v114, v26, v27
v_cvt_pk_bf16_f32 v115, v28, v29
ds_write_b128 v84, v[112:115] offset:0
v_mul_f32 v105, v105, v95
v_fma_f32 v30, v30, v104, v105
v_mul_f32 v107, v107, v95
v_fma_f32 v31, v31, v106, v107
v_mul_f32 v109, v109, v95
v_fma_f32 v32, v32, v108, v109
v_mul_f32 v111, v111, v95
v_fma_f32 v33, v33, v110, v111
v_cvt_pk_bf16_f32 v78, v30, v31
v_cvt_pk_bf16_f32 v79, v32, v33
ds_write_b64 v84, v[78:79] offset:1024
s_xor_b32 s50, s50, s57
s_waitcnt vmcnt(17) lgkmcnt(0)
s_add_u32 s49, s49, 313344
s_barrier
R 46,33024
R 50,33088
R 54,33152
R 58,33216
s_waitcnt vmcnt(16)
v_permlane32_swap_b32 v14, v15
v_add_f32 v16, v14, v15
v_add_f32 v17, v14, v15
s_nop 1
v_permlane16_swap_b32 v16, v17
v_add_f32 v14, v16, v17
v_add_f32 v15, v16, v17
s_nop 1
v_permlane32_swap_b32 v14, v15
v_mul_f32 v16, 0x3a800000, v14
v_mul_f32 v17, 0x3a800000, v15
v_fma_f32 v17, -v16, v16, v17
v_max_f32 v17, 0, v17
v_add_f32 v17, 0x3727c5ac, v17
v_rsq_f32 v14, v17
s_nop 0
v_mul_f32_e64 v15, -v16, v14
ds_read_b64 v[118:119], v85 offset:128
ds_read_b128 v[104:107], v86 offset:128
ds_read_b128 v[108:111], v86 offset:144
s_waitcnt lgkmcnt(0)
v_mfma_f32_16x16x32_bf16 v[22:25], a[0:3], v[46:49], 0
R 62,33280
v_mul_f32 v120, v119, v96
v_mfma_f32_16x16x32_bf16 v[26:29], a[4:7], v[46:49], 0
v_fma_f32 v34, v34, v118, v120
v_mul_f32 v121, v119, v97
v_mfma_f32_16x16x32_bf16 v[30:33], v[46:49], a[8:11], 0
v_fma_f32 v35, v35, v118, v121
v_mul_f32 v120, v119, v98
mav 22,12,50
R 66,33344
v_fma_f32 v36, v36, v118, v120
mav 26,16,50
v_mul_f32 v121, v119, v99
v_fma_f32 v37, v37, v118, v121
mva 30,50,20
v_mul_f32 v120, v119, v100
v_fma_f32 v38, v38, v118, v120
mav 22,24,54
R 70,33408
v_mul_f32 v121, v119, v101
mav 26,28,54
v_fma_f32 v39, v39, v118, v121
v_mul_f32 v120, v119, v102
mva 30,54,32
v_fma_f32 v40, v40, v118, v120
v_mul_f32 v121, v119, v103
mav 22,36,58
R 74,33472
v_fma_f32 v41, v41, v118, v121
mav 26,40,58
v_cvt_pk_bf16_f32 v112, v34, v35
v_cvt_pk_bf16_f32 v113, v36, v37
mva 30,58,44
s_waitcnt lgkmcnt(2)
v_cvt_pk_bf16_f32 v114, v38, v39
v_cvt_pk_bf16_f32 v115, v40, v41
mav 22,48,62
R 46,33536
v_mul_f32 v105, v105, v95
mav 26,52,62
ds_write_b128 v84, v[112:115] offset:4096
mva 30,62,56
v_fma_f32 v42, v42, v104, v105
v_mul_f32 v107, v107, v95
mav 22,60,66
R 50,33600
v_fma_f32 v43, v43, v106, v107
mav 26,64,66
v_mul_f32 v109, v109, v95
v_fma_f32 v44, v44, v108, v109
mva 30,66,68
s_waitcnt lgkmcnt(3)
v_mul_f32 v111, v111, v95
v_fma_f32 v45, v45, v110, v111
mav 22,72,70
R 54,33664
v_cvt_pk_bf16_f32 v78, v42, v43
mav 26,76,70
v_cvt_pk_bf16_f32 v79, v44, v45
mva 30,70,80
ds_write_b64 v84, v[78:79] offset:5120
mav 22,84,74
R 58,33728
mav 26,88,74
mva 30,74,92
s_waitcnt lgkmcnt(3)
mav 22,96,46
s_waitcnt vmcnt(0) lgkmcnt(1)
R 62,33792
s_add_u32 s49, s49, 313344
mav 26,100,46
s_barrier
mva 30,46,104
ds_write_b64 v85, v[14:15] offset:128
mav 22,108,50
s_sub_u32 s54, s54, 0x8000
R 66,33856
s_add_u32 m0, s50, 33024
mav 26,112,50
s_cmp_lt_u32 s54, s56
s_cselect_b64 s[44:45], s[40:41], s[44:45]
mva 30,50,116
D2 87,54,0
mav 22,120,54
R 70,33920
mav 26,124,54
D2 87,54,1024
mva 30,54,128
s_waitcnt lgkmcnt(2)
s_add_u32 s55, s54, 14336
mav 22,132,58
D2 87,55,2048
mav 26,136,58
R 74,33984
mva 30,58,140
D2 87,55,3072
mav 22,144,62
s_add_u32 m0, s50, 41280
R 46,34048
mav 26,148,62
D2 88,54,0
mva 30,62,152
s_waitcnt lgkmcnt(2)
mav 22,156,66
D2 88,54,1024
mav 26,160,66
R 50,34112
mva 30,66,164
D2 88,55,2048
mav 22,168,70
R 54,34176
mav 26,172,70
D2 88,55,3072
mva 30,70,176
s_waitcnt lgkmcnt(2)
s_add_u32 m0, s50, 49536
mav 22,180,74
D2 89,54,0
mav 26,184,74
R 58,34240
mva 30,74,188
D2 89,54,1024
mav 22,192,46
R 62,34304
mav 26,196,46
D2 89,55,2048
mva 30,46,200
s_waitcnt lgkmcnt(2)
mav 22,204,50
D2 89,55,3072
mav 26,208,50
s_add_u32 m0, s50, 57792
R 66,34368
mva 30,50,212
D2 90,54,0
mav 22,216,54
R 70,34432
mav 26,220,54
D2 90,54,1024
mva 30,54,224
s_waitcnt lgkmcnt(2)
mav 22,228,58
D2 90,55,2048
mav 26,232,58
R 74,34496
mva 30,58,236
D2 90,55,3072
mav 22,240,62
s_sub_u32 s54, s54, 0x8000
R 46,34560
s_add_u32 m0, s50, 0
mav 26,244,62
mva 30,62,248
s_waitcnt lgkmcnt(2)
mav 22,252,66
D2 87,54,0
mvv 26,128,66
s_add_u32 s55, s54, 14336
R 50,34624
mvv 30,66,132
D2 87,54,1024
mvv 22,136,70
R 54,34688
mvv 26,140,70
D2 87,55,2048
mvv 30,70,144
s_waitcnt lgkmcnt(2)
mvv 22,148,74
D2 87,55,3072
mvv 26,152,74
s_add_u32 m0, s50, 8256
R 58,34752
mvv 30,74,156
D2 88,54,0
mvv 22,160,46
R 62,34816
mvv 26,164,46
D2 88,54,1024
mvv 30,46,168
s_waitcnt lgkmcnt(2)
mvv 22,172,50
D2 88,55,2048
mvv 26,176,50
R 66,34880
mvv 30,50,180
D2 88,55,3072
mvv 22,184,54
s_add_u32 m0, s50, 16512
R 70,34944
mvv 26,188,54
D2 89,54,0
mvv 30,54,192
s_waitcnt lgkmcnt(2)
mvv 22,196,58
D2 89,54,1024
mvv 26,200,58
R 74,35008
mvv 30,58,204
D2 89,55,2048
mvv 22,208,62
R 46,0
mvv 26,212,62
D2 89,55,3072
mvv 30,62,216
s_waitcnt lgkmcnt(2)
LsQ22:
s_cbranch_execz LwQA
ds_read_b64 v[118:119], v85 offset:0
s_add_u32 m0, s50, 24768
mvv 22,220,66
D2 90,54,0
mvv 26,224,66
R 50,64
mvv 30,66,228
D2 90,54,1024
mvv 22,232,70
R 54,128
mvv 26,236,70
D2 90,55,2048
mvv 30,70,240
s_waitcnt lgkmcnt(2)
ds_read_b128 v[104:107], v86 offset:0
mvv 22,244,74
D2 90,55,3072
mvv 26,248,74
R 58,192
mvv 30,74,252
ds_read_b128 v[108:111], v86 offset:16
v_mfma_f32_16x16x32_bf16 v[34:37], a[0:3], v[46:49], 0
R 62,256
v_mul_f32 v120, v119, v96
v_mfma_f32_16x16x32_bf16 v[38:41], a[4:7], v[46:49], 0
v_fma_f32 v22, v22, v118, v120
v_mul_f32 v121, v119, v97
v_mfma_f32_16x16x32_bf16 v[42:45], v[46:49], a[8:11], 0
s_waitcnt lgkmcnt(3)
v_fma_f32 v23, v23, v118, v121
v_mul_f32 v120, v119, v98
mav 34,12,50
R 66,320
v_fma_f32 v24, v24, v118, v120
mav 38,16,50
v_mul_f32 v121, v119, v99
v_fma_f32 v25, v25, v118, v121
mva 42,50,20
v_mul_f32 v120, v119, v100
v_fma_f32 v26, v26, v118, v120
mav 34,24,54
s_waitcnt lgkmcnt(1)
R 70,384
v_mul_f32 v121, v119, v101
mav 38,28,54
v_fma_f32 v27, v27, v118, v121
v_mul_f32 v120, v119, v102
mva 42,54,32
v_fma_f32 v28, v28, v118, v120
v_mul_f32 v121, v119, v103
mav 34,36,58
R 74,448
v_fma_f32 v29, v29, v118, v121
mav 38,40,58
v_cvt_pk_bf16_f32 v112, v22, v23
v_cvt_pk_bf16_f32 v113, v24, v25
mva 42,58,44
v_cvt_pk_bf16_f32 v114, v26, v27
v_cvt_pk_bf16_f32 v115, v28, v29
mav 34,48,62
R 46,512
v_mul_f32 v105, v105, v95
mav 38,52,62
ds_write_b128 v84, v[112:115] offset:0
mva 42,62,56
s_waitcnt lgkmcnt(3)
v_fma_f32 v30, v30, v104, v105
v_mul_f32 v107, v107, v95
mav 34,60,66
R 50,576
v_fma_f32 v31, v31, v106, v107
mav 38,64,66
v_mul_f32 v109, v109, v95
v_fma_f32 v32, v32, v108, v109
mva 42,66,68
v_mul_f32 v111, v111, v95
v_fma_f32 v33, v33, v110, v111
mav 34,72,70
R 54,640
v_cvt_pk_bf16_f32 v78, v30, v31
mav 38,76,70
v_cvt_pk_bf16_f32 v79, v32, v33
mva 42,70,80
s_waitcnt lgkmcnt(2)
ds_write_b64 v84, v[78:79] offset:1024
mav 34,84,74
s_xor_b32 s50, s50, s57
R 58,704
mav 38,88,74
mva 42,74,92
mav 34,96,46
s_waitcnt vmcnt(16) lgkmcnt(1)
R 62,768
s_add_u32 s49, s49, 313344
mav 38,100,46
s_barrier
mva 42,46,104
s_sub_u32 s54, s54, 0x8000
s_cmp_lt_u32 s54, s56
s_cselect_b64 s[44:45], s[40:41], s[44:45]
mav 34,108,50
buffer_load_dwordx2 v[14:15], v94, s[36:39], s49 offen sc1
mav 38,112,50
s_add_u32 m0, s50, 33024
R 66,832
mva 42,50,116
D2 87,54,0
mav 34,120,54
R 70,896
mav 38,124,54
D2 87,54,1024
mva 42,54,128
s_waitcnt lgkmcnt(2)
s_add_u32 s55, s54, 14336
mav 34,132,58
D2 87,55,2048
mav 38,136,58
R 74,960
mva 42,58,140
D2 87,55,3072
mav 34,144,62
s_add_u32 m0, s50, 41280
R 46,1024
mav 38,148,62
D2 88,54,0
mva 42,62,152
s_waitcnt lgkmcnt(2)
mav 34,156,66
D2 88,54,1024
mav 38,160,66
R 50,1088
mva 42,66,164
D2 88,55,2048
mav 34,168,70
R 54,1152
mav 38,172,70
D2 88,55,3072
mva 42,70,176
s_waitcnt lgkmcnt(2)
s_add_u32 m0, s50, 49536
mav 34,180,74
D2 89,54,0
mav 38,184,74
R 58,1216
mva 42,74,188
D2 89,54,1024
mav 34,192,46
R 62,1280
mav 38,196,46
D2 89,55,2048
mva 42,46,200
s_waitcnt lgkmcnt(2)
mav 34,204,50
D2 89,55,3072
mav 38,208,50
s_add_u32 m0, s50, 57792
R 66,1344
mva 42,50,212
D2 90,54,0
mav 34,216,54
R 70,1408
mav 38,220,54
D2 90,54,1024
mva 42,54,224
s_waitcnt lgkmcnt(2)
mav 34,228,58
D2 90,55,2048
mav 38,232,58
R 74,1472
mva 42,58,236
D2 90,55,3072
mav 34,240,62
R 46,1536
mav 38,244,62
mva 42,62,248
s_waitcnt lgkmcnt(2)
mav 34,252,66
R 50,1600
mvv 38,128,66
mvv 42,66,132
mvv 34,136,70
R 54,1664
mvv 38,140,70
mvv 42,70,144
s_waitcnt lgkmcnt(2)
mvv 34,148,74
R 58,1728
mvv 38,152,74
mvv 42,74,156
mvv 34,160,46
R 62,1792
mvv 38,164,46
mvv 42,46,168
s_waitcnt vmcnt(16) lgkmcnt(2)
v_permlane32_swap_b32 v14, v15
mvv 34,172,50
R 66,1856
v_add_f32 v16, v14, v15
mvv 38,176,50
mvv 42,50,180
v_add_f32 v17, v14, v15
mvv 34,184,54
R 70,1920
mvv 38,188,54
v_permlane16_swap_b32 v16, v17
mvv 42,54,192
s_waitcnt lgkmcnt(2)
v_add_f32 v14, v16, v17
v_add_f32 v15, v16, v17
mvv 34,196,58
R 74,1984
v_xor_b32 v80, v81, v80
mvv 38,200,58
v_permlane32_swap_b32 v14, v15
mvv 42,58,204
v_mul_f32 v16, 0x3a800000, v14
v_mul_f32 v17, 0x3a800000, v15
mvv 34,208,62
R 46,33024
v_fma_f32 v17, -v16, v16, v17
mvv 38,212,62
ds_read_b64 v[118:119], v85 offset:128
v_max_f32 v17, 0, v17
mvv 42,62,216
s_waitcnt lgkmcnt(3)
ds_read_b128 v[104:107], v86 offset:128
v_add_f32 v17, 0x3727c5ac, v17
mvv 34,220,66
R 50,33088
mvv 38,224,66
ds_read_b128 v[108:111], v86 offset:144
mvv 42,66,228
v_rsq_f32 v14, v17
mvv 34,232,70
R 54,33152
v_mul_f32_e64 v15, -v16, v14
mvv 38,236,70
mvv 42,70,240
s_waitcnt lgkmcnt(3)
mvv 34,244,74
R 58,33216
mvv 38,248,74
mvv 42,74,252
v_mfma_f32_16x16x32_bf16 v[22:25], a[0:3], v[46:49], 0
R 62,33280
v_mul_f32 v120, v119, v96
v_mfma_f32_16x16x32_bf16 v[26:29], a[4:7], v[46:49], 0
s_waitcnt lgkmcnt(2)
v_fma_f32 v34, v34, v118, v120
v_mul_f32 v121, v119, v97
v_mfma_f32_16x16x32_bf16 v[30:33], v[46:49], a[8:11], 0
v_fma_f32 v35, v35, v118, v121
v_mul_f32 v120, v119, v98
mav 22,12,50
R 66,33344
v_fma_f32 v36, v36, v118, v120
mav 26,16,50
v_mul_f32 v121, v119, v99
v_fma_f32 v37, v37, v118, v121
mva 30,50,20
v_mul_f32 v120, v119, v100
v_fma_f32 v38, v38, v118, v120
mav 22,24,54
R 70,33408
v_mul_f32 v121, v119, v101
mav 26,28,54
v_fma_f32 v39, v39, v118, v121
v_mul_f32 v120, v119, v102
mva 30,54,32
s_waitcnt lgkmcnt(2)
v_fma_f32 v40, v40, v118, v120
v_mul_f32 v121, v119, v103
mav 22,36,58
R 74,33472
v_fma_f32 v41, v41, v118, v121
mav 26,40,58
v_cvt_pk_bf16_f32 v112, v34, v35
v_cvt_pk_bf16_f32 v113, v36, v37
mva 30,58,44
v_cvt_pk_bf16_f32 v114, v38, v39
v_cvt_pk_bf16_f32 v115, v40, v41
mav 22,48,62
R 46,33536
v_mul_f32 v105, v105, v95
mav 26,52,62
ds_write_b128 v84, v[112:115] offset:4096
mva 30,62,56
s_waitcnt lgkmcnt(3)
v_fma_f32 v42, v42, v104, v105
v_mul_f32 v107, v107, v95
mav 22,60,66
R 50,33600
v_fma_f32 v43, v43, v106, v107
mav 26,64,66
v_mul_f32 v109, v109, v95
v_fma_f32 v44, v44, v108, v109
mva 30,66,68
v_mul_f32 v111, v111, v95
v_fma_f32 v45, v45, v110, v111
mav 22,72,70
R 54,33664
v_cvt_pk_bf16_f32 v78, v42, v43
mav 26,76,70
v_cvt_pk_bf16_f32 v79, v44, v45
mva 30,70,80
s_waitcnt lgkmcnt(2)
ds_write_b64 v84, v[78:79] offset:5120
mav 22,84,74
R 58,33728
mav 26,88,74
ds_write_b64 v85, v[14:15] offset:0
mva 30,74,92
mav 22,96,46
s_add_u32 s49, s49, 313344
R 62,33792
mav 26,100,46
s_waitcnt lgkmcnt(1)
mva 30,46,104
s_barrier
mav 22,108,50
buffer_load_dwordx2 v[14:15], v94, s[36:39], s49 offen sc1
mav 26,112,50
s_sub_u32 s54, s54, 0x8000
R 66,33856
s_add_u32 m0, s50, 0
mva 30,50,116
D2 87,54,0
mav 22,120,54
R 70,33920
mav 26,124,54
D2 87,54,1024
mva 30,54,128
s_add_u32 s55, s54, 14336
mav 22,132,58
D2 87,55,2048
mav 26,136,58
R 74,33984
mva 30,58,140
s_waitcnt lgkmcnt(2)
D2 87,55,3072
mav 22,144,62
s_add_u32 m0, s50, 8256
R 46,34048
mav 26,148,62
D2 88,54,0
mva 30,62,152
mav 22,156,66
D2 88,54,1024
mav 26,160,66
R 50,34112
mva 30,66,164
s_waitcnt lgkmcnt(2)
D2 88,55,2048
mav 22,168,70
R 54,34176
mav 26,172,70
D2 88,55,3072
mva 30,70,176
s_add_u32 m0, s50, 16512
mav 22,180,74
D2 89,54,0
mav 26,184,74
R 58,34240
mva 30,74,188
s_waitcnt lgkmcnt(2)
D2 89,54,1024
mav 22,192,46
R 62,34304
mav 26,196,46
D2 89,55,2048
mva 30,46,200
mav 22,204,50
D2 89,55,3072
mav 26,208,50
s_add_u32 m0, s50, 24768
R 66,34368
mva 30,50,212
s_waitcnt lgkmcnt(2)
D2 90,54,0
mav 22,216,54
R 70,34432
mav 26,220,54
D2 90,54,1024
mva 30,54,224
mav 22,228,58
D2 90,55,2048
mav 26,232,58
R 74,34496
mva 30,58,236
s_waitcnt lgkmcnt(2)
D2 90,55,3072
mav 22,240,62
R 46,34560
mav 26,244,62
mva 30,62,248
mav 22,252,66
R 50,34624
mvv 26,128,66
mvv 30,66,132
s_waitcnt lgkmcnt(2)
mvv 22,136,70
R 54,34688
mvv 26,140,70
mvv 30,70,144
mvv 22,148,74
R 58,34752
mvv 26,152,74
mvv 30,74,156
s_waitcnt lgkmcnt(2)
mvv 22,160,46
R 62,34816
mvv 26,164,46
mvv 30,46,168
s_waitcnt vmcnt(16)
v_permlane32_swap_b32 v14, v15
mvv 22,172,50
R 66,34880
v_add_f32 v16, v14, v15
mvv 26,176,50
v_add_f32 v17, v14, v15
mvv 30,50,180
s_waitcnt lgkmcnt(2)
v_permlane16_swap_b32 v16, v17
mvv 22,184,54
R 70,34944
v_add_f32 v14, v16, v17
mvv 26,188,54
v_add_f32 v15, v16, v17
mvv 30,54,192
mvv 22,196,58
R 74,35008
mvv 26,200,58
v_permlane32_swap_b32 v14, v15
mvv 30,58,204
s_waitcnt lgkmcnt(2)
v_mul_f32 v16, 0x3a800000, v14
v_mul_f32 v17, 0x3a800000, v15
mvv 22,208,62
R 46,0
v_fma_f32 v17, -v16, v16, v17
mvv 26,212,62
ds_read_b64 v[118:119], v85 offset:0
v_max_f32 v17, 0, v17
mvv 30,62,216
ds_read_b128 v[104:107], v86 offset:0
v_add_f32 v17, 0x3727c5ac, v17
mvv 22,220,66
R 50,64
mvv 26,224,66
ds_read_b128 v[108:111], v86 offset:16
mvv 30,66,228
s_waitcnt lgkmcnt(5)
v_rsq_f32 v14, v17
mvv 22,232,70
R 54,128
v_mul_f32_e64 v15, -v16, v14
mvv 26,236,70
mvv 30,70,240
mvv 22,244,74
R 58,192
mvv 26,248,74
mvv 30,74,252
s_waitcnt lgkmcnt(2)
v_mfma_f32_16x16x32_bf16 v[34:37], a[0:3], v[46:49], 0
R 62,256
v_mul_f32 v120, v119, v96
v_mfma_f32_16x16x32_bf16 v[38:41], a[4:7], v[46:49], 0
v_fma_f32 v22, v22, v118, v120
v_mul_f32 v121, v119, v97
v_mfma_f32_16x16x32_bf16 v[42:45], v[46:49], a[8:11], 0
v_fma_f32 v23, v23, v118, v121
v_mul_f32 v120, v119, v98
mav 34,12,50
R 66,320
v_fma_f32 v24, v24, v118, v120
mav 38,16,50
v_mul_f32 v121, v119, v99
v_fma_f32 v25, v25, v118, v121
mva 42,50,20
s_waitcnt lgkmcnt(2)
v_mul_f32 v120, v119, v100
v_fma_f32 v26, v26, v118, v120
mav 34,24,54
R 70,384
v_mul_f32 v121, v119, v101
mav 38,28,54
v_fma_f32 v27, v27, v118, v121
v_mul_f32 v120, v119, v102
mva 42,54,32
v_fma_f32 v28, v28, v118, v120
v_mul_f32 v121, v119, v103
mav 34,36,58
R 74,448
v_fma_f32 v29, v29, v118, v121
mav 38,40,58
v_cvt_pk_bf16_f32 v112, v22, v23
v_cvt_pk_bf16_f32 v113, v24, v25
mva 42,58,44
s_waitcnt lgkmcnt(2)
v_cvt_pk_bf16_f32 v114, v26, v27
v_cvt_pk_bf16_f32 v115, v28, v29
mav 34,48,62
R 46,512
v_mul_f32 v105, v105, v95
mav 38,52,62
ds_write_b128 v84, v[112:115] offset:0
mva 42,62,56
v_fma_f32 v30, v30, v104, v105
v_mul_f32 v107, v107, v95
mav 34,60,66
R 50,576
v_fma_f32 v31, v31, v106, v107
mav 38,64,66
v_mul_f32 v109, v109, v95
v_fma_f32 v32, v32, v108, v109
mva 42,66,68
s_waitcnt lgkmcnt(3)
v_mul_f32 v111, v111, v95
v_fma_f32 v33, v33, v110, v111
mav 34,72,70
R 54,640
v_cvt_pk_bf16_f32 v78, v30, v31
mav 38,76,70
v_cvt_pk_bf16_f32 v79, v32, v33
mva 42,70,80
ds_write_b64 v84, v[78:79] offset:1024
mav 34,84,74
s_xor_b32 s50, s50, s57
R 58,704
mav 38,88,74
ds_write_b64 v85, v[14:15] offset:128
mva 42,74,92
s_waitcnt lgkmcnt(4)
mav 34,96,46
s_add_u32 s49, s49, 313344
R 62,768
mav 38,100,46
s_waitcnt lgkmcnt(1)
s_sub_u32 s54, s54, 0x8000
mva 42,46,104
s_barrier
mav 34,108,50
buffer_load_dwordx2 v[14:15], v94, s[36:39], s49 offen sc1
mav 38,112,50
s_add_u32 m0, s50, 33024
R 66,832
mva 42,50,116
s_cmp_lt_u32 s54, s56
s_cselect_b64 s[44:45], s[40:41], s[44:45]
mav 34,120,54
R 70,896
mav 38,124,54
D2 87,54,0
mva 42,54,128
s_add_u32 s55, s54, 14336
mav 34,132,58
D2 87,54,1024
mav 38,136,58
R 74,960
mva 42,58,140
s_waitcnt lgkmcnt(2)
D2 87,55,2048
mav 34,144,62
R 46,1024
LsQ23:
s_cbranch_execz LwQB
mav 38,148,62
D2 87,55,3072
mva 42,62,152
s_add_u32 m0, s50, 41280
mav 34,156,66
D2 88,54,0
mav 38,160,66
R 50,1088
mva 42,66,164
s_waitcnt lgkmcnt(2)
D2 88,54,1024
mav 34,168,70
R 54,1152
mav 38,172,70
D2 88,55,2048
mva 42,70,176
mav 34,180,74
D2 88,55,3072
mav 38,184,74
s_add_u32 m0, s50, 49536
R 58,1216
mva 42,74,188
s_waitcnt lgkmcnt(2)
D2 89,54,0
mav 34,192,46
R 62,1280
mav 38,196,46
D2 89,54,1024
mva 42,46,200
mav 34,204,50
D2 89,55,2048
mav 38,208,50
R 66,1344
mva 42,50,212
s_waitcnt lgkmcnt(2)
D2 89,55,3072
mav 34,216,54
s_add_u32 m0, s50, 57792
R 70,1408
mav 38,220,54
D2 90,54,0
mva 42,54,224
mav 34,228,58
D2 90,54,1024
mav 38,232,58
R 74,1472
mva 42,58,236
s_waitcnt lgkmcnt(2)
D2 90,55,2048
mav 34,240,62
R 46,1536
mav 38,244,62
D2 90,55,3072
mva 42,62,248
mav 34,252,66
R 50,1600
mvv 38,128,66
mvv 42,66,132
s_waitcnt lgkmcnt(2)
mvv 34,136,70
R 54,1664
mvv 38,140,70
mvv 42,70,144
mvv 34,148,74
R 58,1728
mvv 38,152,74
mvv 42,74,156
s_waitcnt lgkmcnt(2)
mvv 34,160,46
R 62,1792
mvv 38,164,46
mvv 42,46,168
s_waitcnt vmcnt(16)
v_permlane32_swap_b32 v14, v15
mvv 34,172,50
R 66,1856
v_add_f32 v16, v14, v15
mvv 38,176,50
s_mov_b32 s60, -27
.p2align 6
LQB1_1:
mvv 42,50,180
s_waitcnt lgkmcnt(2)
v_add_f32 v17, v14, v15
mvv 34,184,54
R 70,1920
mvv 38,188,54
v_permlane16_swap_b32 v16, v17
mvv 42,54,192
v_add_f32 v14, v16, v17
v_add_f32 v15, v16, v17
mvv 34,196,58
R 74,1984
v_xor_b32 v80, v81, v80
mvv 38,200,58
v_permlane32_swap_b32 v14, v15
mvv 42,58,204
s_waitcnt lgkmcnt(2)
v_mul_f32 v16, 0x3a800000, v14
v_mul_f32 v17, 0x3a800000, v15
mvv 34,208,62
R 46,33024
v_fma_f32 v17, -v16, v16, v17
mvv 38,212,62
ds_read_b64 v[118:119], v85 offset:128
v_max_f32 v17, 0, v17
mvv 42,62,216
ds_read_b128 v[104:107], v86 offset:128
v_add_f32 v17, 0x3727c5ac, v17
mvv 34,220,66
R 50,33088
mvv 38,224,66
ds_read_b128 v[108:111], v86 offset:144
mvv 42,66,228
s_waitcnt lgkmcnt(5)
v_rsq_f32 v14, v17
mvv 34,232,70
R 54,33152
v_mul_f32_e64 v15, -v16, v14
mvv 38,236,70
mvv 42,70,240
mvv 34,244,74
R 58,33216
mvv 38,248,74
mvv 42,74,252
s_waitcnt lgkmcnt(2)
v_mfma_f32_16x16x32_bf16 v[22:25], a[0:3], v[46:49], 0
R 62,33280
v_mul_f32 v120, v119, v96
v_mfma_f32_16x16x32_bf16 v[26:29], a[4:7], v[46:49], 0
v_fma_f32 v34, v34, v118, v120
v_mul_f32 v121, v119, v97
v_mfma_f32_16x16x32_bf16 v[30:33], v[46:49], a[8:11], 0
v_fma_f32 v35, v35, v118, v121
v_mul_f32 v120, v119, v98
mav 22,12,50
R 66,33344
v_fma_f32 v36, v36, v118, v120
mav 26,16,50
v_mul_f32 v121, v119, v99
v_fma_f32 v37, v37, v118, v121
mva 30,50,20
s_waitcnt lgkmcnt(2)
v_mul_f32 v120, v119, v100
v_fma_f32 v38, v38, v118, v120
mav 22,24,54
R 70,33408
v_mul_f32 v121, v119, v101
mav 26,28,54
v_fma_f32 v39, v39, v118, v121
v_mul_f32 v120, v119, v102
mva 30,54,32
v_fma_f32 v40, v40, v118, v120
v_mul_f32 v121, v119, v103
mav 22,36,58
R 74,33472
v_fma_f32 v41, v41, v118, v121
mav 26,40,58
v_cvt_pk_bf16_f32 v112, v34, v35
v_cvt_pk_bf16_f32 v113, v36, v37
mva 30,58,44
s_waitcnt lgkmcnt(2)
v_cvt_pk_bf16_f32 v114, v38, v39
v_cvt_pk_bf16_f32 v115, v40, v41
mav 22,48,62
R 46,33536
v_mul_f32 v105, v105, v95
mav 26,52,62
ds_write_b128 v84, v[112:115] offset:4096
mva 30,62,56
v_fma_f32 v42, v42, v104, v105
v_mul_f32 v107, v107, v95
mav 22,60,66
R 50,33600
v_fma_f32 v43, v43, v106, v107
mav 26,64,66
v_mul_f32 v109, v109, v95
v_fma_f32 v44, v44, v108, v109
mva 30,66,68
s_waitcnt lgkmcnt(3)
v_mul_f32 v111, v111, v95
v_fma_f32 v45, v45, v110, v111
mav 22,72,70
R 54,33664
v_cvt_pk_bf16_f32 v78, v42, v43
mav 26,76,70
v_cvt_pk_bf16_f32 v79, v44, v45
mva 30,70,80
ds_write_b64 v84, v[78:79] offset:5120
mav 22,84,74
R 58,33728
mav 26,88,74
ds_write_b64 v85, v[14:15] offset:0
mva 30,74,92
s_waitcnt lgkmcnt(4)
mav 22,96,46
s_add_u32 s49, s49, 313344
R 62,33792
mav 26,100,46
s_waitcnt lgkmcnt(1)
mva 30,46,104
s_barrier
mav 22,108,50
buffer_load_dwordx2 v[14:15], v94, s[36:39], s49 offen sc1
mav 26,112,50
s_sub_u32 s54, s54, 0x8000
R 66,33856
s_add_u32 m0, s50, 0
mva 30,50,116
D2 87,54,0
mav 22,120,54
R 70,33920
mav 26,124,54
D2 87,54,1024
mva 30,54,128
s_add_u32 s55, s54, 14336
mav 22,132,58
D2 87,55,2048
mav 26,136,58
R 74,33984
mva 30,58,140
s_waitcnt lgkmcnt(2)
D2 87,55,3072
mav 22,144,62
s_add_u32 m0, s50, 8256
R 46,34048
mav 26,148,62
D2 88,54,0
mva 30,62,152
mav 22,156,66
D2 88,54,1024
mav 26,160,66
R 50,34112
mva 30,66,164
s_waitcnt lgkmcnt(2)
D2 88,55,2048
mav 22,168,70
R 54,34176
mav 26,172,70
D2 88,55,3072
mva 30,70,176
s_add_u32 m0, s50, 16512
mav 22,180,74
D2 89,54,0
mav 26,184,74
R 58,34240
mva 30,74,188
s_waitcnt lgkmcnt(2)
D2 89,54,1024
mav 22,192,46
R 62,34304
mav 26,196,46
D2 89,55,2048
mva 30,46,200
mav 22,204,50
D2 89,55,3072
mav 26,208,50
s_add_u32 m0, s50, 24768
R 66,34368
mva 30,50,212
s_waitcnt lgkmcnt(2)
D2 90,54,0
mav 22,216,54
R 70,34432
mav 26,220,54
D2 90,54,1024
mva 30,54,224
mav 22,228,58
D2 90,55,2048
mav 26,232,58
R 74,34496
mva 30,58,236
s_waitcnt lgkmcnt(2)
D2 90,55,3072
mav 22,240,62
R 46,34560
mav 26,244,62
mva 30,62,248
mav 22,252,66
R 50,34624
mvv 26,128,66
mvv 30,66,132
s_waitcnt lgkmcnt(2)
mvv 22,136,70
R 54,34688
mvv 26,140,70
mvv 30,70,144
mvv 22,148,74
R 58,34752
mvv 26,152,74
mvv 30,74,156
s_waitcnt lgkmcnt(2)
mvv 22,160,46
R 62,34816
mvv 26,164,46
mvv 30,46,168
s_waitcnt vmcnt(16)
v_permlane32_swap_b32 v14, v15
mvv 22,172,50
R 66,34880
v_add_f32 v16, v14, v15
mvv 26,176,50
v_add_f32 v17, v14, v15
mvv 30,50,180
s_waitcnt lgkmcnt(2)
v_permlane16_swap_b32 v16, v17
mvv 22,184,54
R 70,34944
v_add_f32 v14, v16, v17
mvv 26,188,54
v_add_f32 v15, v16, v17
mvv 30,54,192
mvv 22,196,58
R 74,35008
mvv 26,200,58
v_permlane32_swap_b32 v14, v15
mvv 30,58,204
s_waitcnt lgkmcnt(2)
v_mul_f32 v16, 0x3a800000, v14
v_mul_f32 v17, 0x3a800000, v15
mvv 22,208,62
R 46,0
v_fma_f32 v17, -v16, v16, v17
mvv 26,212,62
ds_read_b64 v[118:119], v85 offset:0
v_max_f32 v17, 0, v17
mvv 30,62,216
ds_read_b128 v[104:107], v86 offset:0
v_add_f32 v17, 0x3727c5ac, v17
mvv 22,220,66
R 50,64
mvv 26,224,66
ds_read_b128 v[108:111], v86 offset:16
mvv 30,66,228
s_waitcnt lgkmcnt(5)
v_rsq_f32 v14, v17
mvv 22,232,70
R 54,128
v_mul_f32_e64 v15, -v16, v14
mvv 26,236,70
mvv 30,70,240
mvv 22,244,74
R 58,192
mvv 26,248,74
mvv 30,74,252
s_waitcnt lgkmcnt(2)
v_mfma_f32_16x16x32_bf16 v[34:37], a[0:3], v[46:49], 0
R 62,256
v_mul_f32 v120, v119, v96
v_mfma_f32_16x16x32_bf16 v[38:41], a[4:7], v[46:49], 0
v_fma_f32 v22, v22, v118, v120
v_mul_f32 v121, v119, v97
v_mfma_f32_16x16x32_bf16 v[42:45], v[46:49], a[8:11], 0
v_fma_f32 v23, v23, v118, v121
v_mul_f32 v120, v119, v98
mav 34,12,50
R 66,320
v_fma_f32 v24, v24, v118, v120
mav 38,16,50
v_mul_f32 v121, v119, v99
v_fma_f32 v25, v25, v118, v121
mva 42,50,20
s_waitcnt lgkmcnt(2)
v_mul_f32 v120, v119, v100
v_fma_f32 v26, v26, v118, v120
mav 34,24,54
R 70,384
v_mul_f32 v121, v119, v101
mav 38,28,54
v_fma_f32 v27, v27, v118, v121
v_mul_f32 v120, v119, v102
mva 42,54,32
v_fma_f32 v28, v28, v118, v120
v_mul_f32 v121, v119, v103
mav 34,36,58
R 74,448
v_fma_f32 v29, v29, v118, v121
mav 38,40,58
v_cvt_pk_bf16_f32 v112, v22, v23
v_cvt_pk_bf16_f32 v113, v24, v25
mva 42,58,44
s_waitcnt lgkmcnt(2)
v_cvt_pk_bf16_f32 v114, v26, v27
v_cvt_pk_bf16_f32 v115, v28, v29
mav 34,48,62
R 46,512
v_mul_f32 v105, v105, v95
mav 38,52,62
ds_write_b128 v84, v[112:115] offset:0
mva 42,62,56
v_fma_f32 v30, v30, v104, v105
v_mul_f32 v107, v107, v95
mav 34,60,66
R 50,576
v_fma_f32 v31, v31, v106, v107
mav 38,64,66
v_mul_f32 v109, v109, v95
v_fma_f32 v32, v32, v108, v109
mva 42,66,68
s_waitcnt lgkmcnt(3)
v_mul_f32 v111, v111, v95
v_fma_f32 v33, v33, v110, v111
mav 34,72,70
R 54,640
v_cvt_pk_bf16_f32 v78, v30, v31
mav 38,76,70
v_cvt_pk_bf16_f32 v79, v32, v33
mva 42,70,80
ds_write_b64 v84, v[78:79] offset:1024
mav 34,84,74
s_xor_b32 s50, s50, s57
R 58,704
mav 38,88,74
ds_write_b64 v85, v[14:15] offset:128
mva 42,74,92
s_waitcnt lgkmcnt(4)
mav 34,96,46
s_add_u32 s49, s49, 313344
R 62,768
mav 38,100,46
s_waitcnt lgkmcnt(1)
s_sub_u32 s54, s54, 0x8000
mva 42,46,104
s_barrier
mav 34,108,50
buffer_load_dwordx2 v[14:15], v94, s[36:39], s49 offen sc1
mav 38,112,50
s_add_u32 m0, s50, 33024
R 66,832
mva 42,50,116
s_cmp_lt_u32 s54, s56
s_cselect_b64 s[44:45], s[40:41], s[44:45]
mav 34,120,54
R 70,896
mav 38,124,54
D2 87,54,0
mva 42,54,128
s_add_u32 s55, s54, 14336
mav 34,132,58
D2 87,54,1024
mav 38,136,58
R 74,960
mva 42,58,140
s_waitcnt lgkmcnt(2)
D2 87,55,2048
mav 34,144,62
R 46,1024
mav 38,148,62
D2 87,55,3072
mva 42,62,152
s_add_u32 m0, s50, 41280
mav 34,156,66
D2 88,54,0
mav 38,160,66
R 50,1088
mva 42,66,164
s_waitcnt lgkmcnt(2)
D2 88,54,1024
mav 34,168,70
R 54,1152
mav 38,172,70
D2 88,55,2048
mva 42,70,176
mav 34,180,74
D2 88,55,3072
mav 38,184,74
s_add_u32 m0, s50, 49536
R 58,1216
mva 42,74,188
s_waitcnt lgkmcnt(2)
D2 89,54,0
mav 34,192,46
R 62,1280
mav 38,196,46
D2 89,54,1024
mva 42,46,200
mav 34,204,50
D2 89,55,2048
mav 38,208,50
R 66,1344
mva 42,50,212
s_waitcnt lgkmcnt(2)
D2 89,55,3072
mav 34,216,54
s_add_u32 m0, s50, 57792
R 70,1408
mav 38,220,54
D2 90,54,0
mva 42,54,224
mav 34,228,58
D2 90,54,1024
mav 38,232,58
R 74,1472
mva 42,58,236
s_waitcnt lgkmcnt(2)
D2 90,55,2048
mav 34,240,62
R 46,1536
mav 38,244,62
D2 90,55,3072
mva 42,62,248
mav 34,252,66
R 50,1600
mvv 38,128,66
mvv 42,66,132
s_waitcnt lgkmcnt(2)
mvv 34,136,70
R 54,1664
mvv 38,140,70
mvv 42,70,144
mvv 34,148,74
R 58,1728
mvv 38,152,74
mvv 42,74,156
s_waitcnt lgkmcnt(2)
mvv 34,160,46
R 62,1792
mvv 38,164,46
mvv 42,46,168
s_waitcnt vmcnt(16)
v_permlane32_swap_b32 v14, v15
mvv 34,172,50
s_add_u32 s60, s60, 1
R 66,1856
v_add_f32 v16, v14, v15
mvv 38,176,50
s_cbranch_scc0 LQB1_1
mvv 42,50,180
s_waitcnt lgkmcnt(2)
v_add_f32 v17, v14, v15
mvv 34,184,54
R 70,1920
mvv 38,188,54
v_permlane16_swap_b32 v16, v17
mvv 42,54,192
v_add_f32 v14, v16, v17
v_add_f32 v15, v16, v17
mvv 34,196,58
R 74,1984
v_xor_b32 v80, v81, v80
mvv 38,200,58
v_permlane32_swap_b32 v14, v15
mvv 42,58,204
s_waitcnt lgkmcnt(2)
v_mul_f32 v16, 0x3a800000, v14
v_mul_f32 v17, 0x3a800000, v15
mvv 34,208,62
R 46,33024
v_fma_f32 v17, -v16, v16, v17
mvv 38,212,62
ds_read_b64 v[118:119], v85 offset:128
v_max_f32 v17, 0, v17
mvv 42,62,216
ds_read_b128 v[104:107], v86 offset:128
v_add_f32 v17, 0x3727c5ac, v17
mvv 34,220,66
R 50,33088
mvv 38,224,66
ds_read_b128 v[108:111], v86 offset:144
mvv 42,66,228
LsQ24:
s_cbranch_execz LwQA
s_waitcnt lgkmcnt(5)
v_rsq_f32 v14, v17
mvv 34,232,70
R 54,33152
v_mul_f32_e64 v15, -v16, v14
mvv 38,236,70
mvv 42,70,240
mvv 34,244,74
R 58,33216
mvv 38,248,74
mvv 42,74,252
s_waitcnt lgkmcnt(2)
v_mfma_f32_16x16x32_bf16 v[22:25], a[0:3], v[46:49], 0
R 62,33280
v_mul_f32 v120, v119, v96
v_mfma_f32_16x16x32_bf16 v[26:29], a[4:7], v[46:49], 0
v_fma_f32 v34, v34, v118, v120
v_mul_f32 v121, v119, v97
v_mfma_f32_16x16x32_bf16 v[30:33], v[46:49], a[8:11], 0
v_fma_f32 v35, v35, v118, v121
v_mul_f32 v120, v119, v98
mav 22,12,50
R 66,33344
v_fma_f32 v36, v36, v118, v120
mav 26,16,50
v_mul_f32 v121, v119, v99
v_fma_f32 v37, v37, v118, v121
mva 30,50,20
s_waitcnt lgkmcnt(2)
v_mul_f32 v120, v119, v100
v_fma_f32 v38, v38, v118, v120
mav 22,24,54
R 70,33408
v_mul_f32 v121, v119, v101
mav 26,28,54
v_fma_f32 v39, v39, v118, v121
v_mul_f32 v120, v119, v102
mva 30,54,32
v_fma_f32 v40, v40, v118, v120
v_mul_f32 v121, v119, v103
mav 22,36,58
R 74,33472
v_fma_f32 v41, v41, v118, v121
mav 26,40,58
v_cvt_pk_bf16_f32 v112, v34, v35
v_cvt_pk_bf16_f32 v113, v36, v37
mva 30,58,44
s_waitcnt lgkmcnt(2)
v_cvt_pk_bf16_f32 v114, v38, v39
v_cvt_pk_bf16_f32 v115, v40, v41
mav 22,48,62
R 46,33536
v_mul_f32 v105, v105, v95
mav 26,52,62
ds_write_b128 v84, v[112:115] offset:4096
mva 30,62,56
v_fma_f32 v42, v42, v104, v105
v_mul_f32 v107, v107, v95
mav 22,60,66
R 50,33600
v_fma_f32 v43, v43, v106, v107
mav 26,64,66
v_mul_f32 v109, v109, v95
v_fma_f32 v44, v44, v108, v109
mva 30,66,68
s_waitcnt lgkmcnt(3)
v_mul_f32 v111, v111, v95
v_fma_f32 v45, v45, v110, v111
mav 22,72,70
R 54,33664
v_cvt_pk_bf16_f32 v78, v42, v43
mav 26,76,70
v_cvt_pk_bf16_f32 v79, v44, v45
mva 30,70,80
ds_write_b64 v84, v[78:79] offset:5120
mav 22,84,74
R 58,33728
mav 26,88,74
ds_write_b64 v85, v[14:15] offset:0
mva 30,74,92
s_waitcnt lgkmcnt(4)
mav 22,96,46
s_add_u32 s49, s49, 313344
R 62,33792
mav 26,100,46
s_waitcnt lgkmcnt(1)
mva 30,46,104
s_barrier
mav 22,108,50
buffer_load_dwordx2 v[14:15], v94, s[36:39], s49 offen sc1
mav 26,112,50
s_sub_u32 s54, s54, 0x8000
R 66,33856
s_add_u32 m0, s50, 0
mva 30,50,116
D2 87,54,0
mav 22,120,54
R 70,33920
mav 26,124,54
D2 87,54,1024
mva 30,54,128
s_add_u32 s55, s54, 14336
mav 22,132,58
D2 87,55,2048
mav 26,136,58
R 74,33984
mva 30,58,140
s_waitcnt lgkmcnt(2)
D2 87,55,3072
mav 22,144,62
s_add_u32 m0, s50, 8256
R 46,34048
mav 26,148,62
D2 88,54,0
mva 30,62,152
mav 22,156,66
D2 88,54,1024
mav 26,160,66
R 50,34112
mva 30,66,164
s_waitcnt lgkmcnt(2)
D2 88,55,2048
mav 22,168,70
R 54,34176
mav 26,172,70
D2 88,55,3072
mva 30,70,176
s_add_u32 m0, s50, 16512
mav 22,180,74
D2 89,54,0
mav 26,184,74
R 58,34240
mva 30,74,188
s_waitcnt lgkmcnt(2)
D2 89,54,1024
mav 22,192,46
R 62,34304
mav 26,196,46
D2 89,55,2048
mva 30,46,200
mav 22,204,50
D2 89,55,3072
mav 26,208,50
s_add_u32 m0, s50, 24768
R 66,34368
mva 30,50,212
s_waitcnt lgkmcnt(2)
D2 90,54,0
mav 22,216,54
R 70,34432
mav 26,220,54
D2 90,54,1024
mva 30,54,224
mav 22,228,58
D2 90,55,2048
mav 26,232,58
R 74,34496
mva 30,58,236
s_waitcnt lgkmcnt(2)
D2 90,55,3072
mav 22,240,62
R 46,34560
mav 26,244,62
mva 30,62,248
mav 22,252,66
R 50,34624
mvv 26,128,66
mvv 30,66,132
s_waitcnt lgkmcnt(2)
mvv 22,136,70
R 54,34688
mvv 26,140,70
mvv 30,70,144
mvv 22,148,74
R 58,34752
mvv 26,152,74
mvv 30,74,156
s_waitcnt lgkmcnt(2)
mvv 22,160,46
R 62,34816
mvv 26,164,46
mvv 30,46,168
s_waitcnt vmcnt(16)
v_permlane32_swap_b32 v14, v15
mvv 22,172,50
R 66,34880
v_add_f32 v16, v14, v15
mvv 26,176,50
v_add_f32 v17, v14, v15
mvv 30,50,180
s_waitcnt lgkmcnt(2)
v_permlane16_swap_b32 v16, v17
mvv 22,184,54
R 70,34944
v_add_f32 v14, v16, v17
mvv 26,188,54
v_add_f32 v15, v16, v17
mvv 30,54,192
mvv 22,196,58
R 74,35008
mvv 26,200,58
v_permlane32_swap_b32 v14, v15
mvv 30,58,204
s_waitcnt lgkmcnt(2)
v_mul_f32 v16, 0x3a800000, v14
v_mul_f32 v17, 0x3a800000, v15
mvv 22,208,62
R 46,0
v_fma_f32 v17, -v16, v16, v17
mvv 26,212,62
ds_read_b64 v[118:119], v85 offset:0
v_max_f32 v17, 0, v17
mvv 30,62,216
ds_read_b128 v[104:107], v86 offset:0
v_add_f32 v17, 0x3727c5ac, v17
mvv 22,220,66
R 50,64
mvv 26,224,66
ds_read_b128 v[108:111], v86 offset:16
mvv 30,66,228
s_waitcnt lgkmcnt(5)
v_rsq_f32 v14, v17
mvv 22,232,70
R 54,128
v_mul_f32_e64 v15, -v16, v14
mvv 26,236,70
mvv 30,70,240
mvv 22,244,74
R 58,192
mvv 26,248,74
mvv 30,74,252
s_waitcnt lgkmcnt(2)
v_mfma_f32_16x16x32_bf16 v[34:37], a[0:3], v[46:49], 0
R 62,256
v_mul_f32 v120, v119, v96
v_mfma_f32_16x16x32_bf16 v[38:41], a[4:7], v[46:49], 0
v_fma_f32 v22, v22, v118, v120
v_mul_f32 v121, v119, v97
v_mfma_f32_16x16x32_bf16 v[42:45], v[46:49], a[8:11], 0
v_fma_f32 v23, v23, v118, v121
v_mul_f32 v120, v119, v98
mav 34,12,50
R 66,320
v_fma_f32 v24, v24, v118, v120
mav 38,16,50
v_mul_f32 v121, v119, v99
v_fma_f32 v25, v25, v118, v121
mva 42,50,20
s_waitcnt lgkmcnt(2)
v_mul_f32 v120, v119, v100
v_fma_f32 v26, v26, v118, v120
mav 34,24,54
R 70,384
v_mul_f32 v121, v119, v101
mav 38,28,54
v_fma_f32 v27, v27, v118, v121
v_mul_f32 v120, v119, v102
mva 42,54,32
v_fma_f32 v28, v28, v118, v120
v_mul_f32 v121, v119, v103
mav 34,36,58
R 74,448
v_fma_f32 v29, v29, v118, v121
mav 38,40,58
v_cvt_pk_bf16_f32 v112, v22, v23
v_cvt_pk_bf16_f32 v113, v24, v25
mva 42,58,44
s_waitcnt lgkmcnt(2)
v_cvt_pk_bf16_f32 v114, v26, v27
v_cvt_pk_bf16_f32 v115, v28, v29
mav 34,48,62
R 46,512
v_mul_f32 v105, v105, v95
mav 38,52,62
ds_write_b128 v84, v[112:115] offset:0
mva 42,62,56
v_fma_f32 v30, v30, v104, v105
v_mul_f32 v107, v107, v95
mav 34,60,66
R 50,576
v_fma_f32 v31, v31, v106, v107
mav 38,64,66
)"
R"(
v_mul_f32 v109, v109, v95
v_fma_f32 v32, v32, v108, v109
mva 42,66,68
s_waitcnt lgkmcnt(3)
v_mul_f32 v111, v111, v95
v_fma_f32 v33, v33, v110, v111
mav 34,72,70
R 54,640
v_cvt_pk_bf16_f32 v78, v30, v31
mav 38,76,70
v_cvt_pk_bf16_f32 v79, v32, v33
mva 42,70,80
ds_write_b64 v84, v[78:79] offset:1024
mav 34,84,74
R 58,704
mav 38,88,74
ds_write_b64 v85, v[14:15] offset:128
mva 42,74,92
s_waitcnt lgkmcnt(4)
mav 34,96,46
s_add_u32 s49, s49, 313344
R 62,768
mav 38,100,46
s_waitcnt lgkmcnt(1)
mva 42,46,104
s_barrier
mav 34,108,50
buffer_load_dwordx2 v[14:15], v94, s[36:39], s49 offen sc1
mav 38,112,50
R 66,832
mva 42,50,116
mav 34,120,54
R 70,896
mav 38,124,54
mva 42,54,128
mav 34,132,58
R 74,960
mav 38,136,58
mva 42,58,140
s_waitcnt lgkmcnt(2)
mav 34,144,62
R 46,1024
mav 38,148,62
mva 42,62,152
mav 34,156,66
R 50,1088
mav 38,160,66
mva 42,66,164
s_waitcnt lgkmcnt(2)
mav 34,168,70
R 54,1152
mav 38,172,70
mva 42,70,176
mav 34,180,74
R 58,1216
mav 38,184,74
mva 42,74,188
s_waitcnt lgkmcnt(2)
mav 34,192,46
R 62,1280
mav 38,196,46
mva 42,46,200
mav 34,204,50
R 66,1344
mav 38,208,50
mva 42,50,212
s_waitcnt lgkmcnt(2)
mav 34,216,54
R 70,1408
mav 38,220,54
mva 42,54,224
mav 34,228,58
R 74,1472
mav 38,232,58
mva 42,58,236
s_waitcnt lgkmcnt(2)
mav 34,240,62
R 46,1536
mav 38,244,62
mva 42,62,248
mav 34,252,66
R 50,1600
mvv 38,128,66
mvv 42,66,132
s_waitcnt lgkmcnt(2)
mvv 34,136,70
R 54,1664
mvv 38,140,70
mvv 42,70,144
mvv 34,148,74
R 58,1728
mvv 38,152,74
mvv 42,74,156
s_waitcnt lgkmcnt(2)
mvv 34,160,46
R 62,1792
mvv 38,164,46
mvv 42,46,168
s_waitcnt vmcnt(0)
v_permlane32_swap_b32 v14, v15
mvv 34,172,50
R 66,1856
v_add_f32 v16, v14, v15
mvv 38,176,50
mvv 42,50,180
s_waitcnt lgkmcnt(2)
v_add_f32 v17, v14, v15
mvv 34,184,54
R 70,1920
mvv 38,188,54
v_permlane16_swap_b32 v16, v17
mvv 42,54,192
v_add_f32 v14, v16, v17
v_add_f32 v15, v16, v17
mvv 34,196,58
R 74,1984
v_xor_b32 v80, v81, v80
mvv 38,200,58
v_permlane32_swap_b32 v14, v15
mvv 42,58,204
s_waitcnt lgkmcnt(2)
v_mul_f32 v16, 0x3a800000, v14
v_mul_f32 v17, 0x3a800000, v15
mvv 34,208,62
R 46,33024
v_fma_f32 v17, -v16, v16, v17
mvv 38,212,62
ds_read_b64 v[118:119], v85 offset:128
v_max_f32 v17, 0, v17
mvv 42,62,216
ds_read_b128 v[104:107], v86 offset:128
v_add_f32 v17, 0x3727c5ac, v17
mvv 34,220,66
R 50,33088
mvv 38,224,66
ds_read_b128 v[108:111], v86 offset:144
mvv 42,66,228
s_waitcnt lgkmcnt(5)
v_rsq_f32 v14, v17
mvv 34,232,70
R 54,33152
v_mul_f32_e64 v15, -v16, v14
mvv 38,236,70
mvv 42,70,240
mvv 34,244,74
R 58,33216
mvv 38,248,74
mvv 42,74,252
s_waitcnt lgkmcnt(2)
v_mfma_f32_16x16x32_bf16 v[22:25], a[0:3], v[46:49], 0
R 62,33280
v_mul_f32 v120, v119, v96
v_mfma_f32_16x16x32_bf16 v[26:29], a[4:7], v[46:49], 0
v_fma_f32 v34, v34, v118, v120
v_mul_f32 v121, v119, v97
v_mfma_f32_16x16x32_bf16 v[30:33], v[46:49], a[8:11], 0
v_fma_f32 v35, v35, v118, v121
v_mul_f32 v120, v119, v98
mav 22,12,50
R 66,33344
v_fma_f32 v36, v36, v118, v120
mav 26,16,50
v_mul_f32 v121, v119, v99
v_fma_f32 v37, v37, v118, v121
mva 30,50,20
s_waitcnt lgkmcnt(2)
v_mul_f32 v120, v119, v100
v_fma_f32 v38, v38, v118, v120
mav 22,24,54
R 70,33408
v_mul_f32 v121, v119, v101
mav 26,28,54
v_fma_f32 v39, v39, v118, v121
v_mul_f32 v120, v119, v102
mva 30,54,32
v_fma_f32 v40, v40, v118, v120
v_mul_f32 v121, v119, v103
mav 22,36,58
R 74,33472
v_fma_f32 v41, v41, v118, v121
mav 26,40,58
v_cvt_pk_bf16_f32 v112, v34, v35
v_cvt_pk_bf16_f32 v113, v36, v37
mva 30,58,44
s_waitcnt lgkmcnt(2)
v_cvt_pk_bf16_f32 v114, v38, v39
v_cvt_pk_bf16_f32 v115, v40, v41
mav 22,48,62
R 46,33536
v_mul_f32 v105, v105, v95
mav 26,52,62
ds_write_b128 v84, v[112:115] offset:4096
mva 30,62,56
v_fma_f32 v42, v42, v104, v105
v_mul_f32 v107, v107, v95
mav 22,60,66
R 50,33600
v_fma_f32 v43, v43, v106, v107
mav 26,64,66
v_mul_f32 v109, v109, v95
v_fma_f32 v44, v44, v108, v109
mva 30,66,68
s_waitcnt lgkmcnt(3)
v_mul_f32 v111, v111, v95
v_fma_f32 v45, v45, v110, v111
mav 22,72,70
R 54,33664
v_cvt_pk_bf16_f32 v78, v42, v43
mav 26,76,70
v_cvt_pk_bf16_f32 v79, v44, v45
mva 30,70,80
ds_write_b64 v84, v[78:79] offset:5120
mav 22,84,74
R 58,33728
mav 26,88,74
ds_write_b64 v85, v[14:15] offset:0
mva 30,74,92
s_waitcnt lgkmcnt(4)
mav 22,96,46
s_add_u32 s49, s49, 313344
R 62,33792
mav 26,100,46
s_waitcnt lgkmcnt(1)
mva 30,46,104
s_barrier
mav 22,108,50
buffer_load_dwordx2 v[14:15], v94, s[36:39], s49 offen sc1
mav 26,112,50
R 66,33856
mva 30,50,116
mav 22,120,54
R 70,33920
mav 26,124,54
mva 30,54,128
mav 22,132,58
R 74,33984
mav 26,136,58
mva 30,58,140
s_waitcnt lgkmcnt(2)
mav 22,144,62
R 46,34048
mav 26,148,62
mva 30,62,152
mav 22,156,66
R 50,34112
mav 26,160,66
mva 30,66,164
s_waitcnt lgkmcnt(2)
mav 22,168,70
R 54,34176
mav 26,172,70
mva 30,70,176
mav 22,180,74
R 58,34240
mav 26,184,74
mva 30,74,188
s_waitcnt lgkmcnt(2)
mav 22,192,46
R 62,34304
mav 26,196,46
mva 30,46,200
mav 22,204,50
R 66,34368
mav 26,208,50
mva 30,50,212
s_waitcnt lgkmcnt(2)
mav 22,216,54
R 70,34432
mav 26,220,54
mva 30,54,224
mav 22,228,58
R 74,34496
mav 26,232,58
mva 30,58,236
s_waitcnt lgkmcnt(2)
mav 22,240,62
R 46,34560
mav 26,244,62
mva 30,62,248
mav 22,252,66
R 50,34624
mvv 26,128,66
mvv 30,66,132
LsQ25:
s_cbranch_execz LwQB
s_waitcnt lgkmcnt(2)
mvv 22,136,70
R 54,34688
mvv 26,140,70
mvv 30,70,144
mvv 22,148,74
R 58,34752
mvv 26,152,74
mvv 30,74,156
s_waitcnt lgkmcnt(2)
mvv 22,160,46
R 62,34816
mvv 26,164,46
mvv 30,46,168
s_waitcnt vmcnt(0)
v_permlane32_swap_b32 v14, v15
mvv 22,172,50
R 66,34880
v_add_f32 v16, v14, v15
mvv 26,176,50
v_add_f32 v17, v14, v15
mvv 30,50,180
s_waitcnt lgkmcnt(2)
v_permlane16_swap_b32 v16, v17
mvv 22,184,54
R 70,34944
v_add_f32 v14, v16, v17
mvv 26,188,54
v_add_f32 v15, v16, v17
mvv 30,54,192
mvv 22,196,58
R 74,35008
mvv 26,200,58
v_permlane32_swap_b32 v14, v15
mvv 30,58,204
s_waitcnt lgkmcnt(2)
v_mul_f32 v16, 0x3a800000, v14
v_mul_f32 v17, 0x3a800000, v15
mvv 22,208,62
R 46,0
v_fma_f32 v17, -v16, v16, v17
mvv 26,212,62
ds_read_b64 v[118:119], v85 offset:0
v_max_f32 v17, 0, v17
mvv 30,62,216
ds_read_b128 v[104:107], v86 offset:0
v_add_f32 v17, 0x3727c5ac, v17
mvv 22,220,66
R 50,64
mvv 26,224,66
ds_read_b128 v[108:111], v86 offset:16
mvv 30,66,228
s_waitcnt lgkmcnt(5)
v_rsq_f32 v14, v17
mvv 22,232,70
R 54,128
v_mul_f32_e64 v15, -v16, v14
mvv 26,236,70
mvv 30,70,240
mvv 22,244,74
R 58,192
mvv 26,248,74
mvv 30,74,252
s_waitcnt lgkmcnt(2)
v_mfma_f32_16x16x32_bf16 v[34:37], a[0:3], v[46:49], 0
R 62,256
v_mul_f32 v120, v119, v96
v_mfma_f32_16x16x32_bf16 v[38:41], a[4:7], v[46:49], 0
v_fma_f32 v22, v22, v118, v120
v_mul_f32 v121, v119, v97
v_mfma_f32_16x16x32_bf16 v[42:45], v[46:49], a[8:11], 0
v_fma_f32 v23, v23, v118, v121
v_mul_f32 v120, v119, v98
mav 34,12,50
R 66,320
v_fma_f32 v24, v24, v118, v120
mav 38,16,50
v_mul_f32 v121, v119, v99
v_fma_f32 v25, v25, v118, v121
mva 42,50,20
s_waitcnt lgkmcnt(2)
v_mul_f32 v120, v119, v100
v_fma_f32 v26, v26, v118, v120
mav 34,24,54
R 70,384
v_mul_f32 v121, v119, v101
mav 38,28,54
v_fma_f32 v27, v27, v118, v121
v_mul_f32 v120, v119, v102
mva 42,54,32
v_fma_f32 v28, v28, v118, v120
v_mul_f32 v121, v119, v103
mav 34,36,58
R 74,448
v_fma_f32 v29, v29, v118, v121
mav 38,40,58
v_cvt_pk_bf16_f32 v112, v22, v23
v_cvt_pk_bf16_f32 v113, v24, v25
mva 42,58,44
s_waitcnt lgkmcnt(2)
v_cvt_pk_bf16_f32 v114, v26, v27
v_cvt_pk_bf16_f32 v115, v28, v29
mav 34,48,62
R 46,512
v_mul_f32 v105, v105, v95
mav 38,52,62
ds_write_b128 v84, v[112:115] offset:0
mva 42,62,56
v_fma_f32 v30, v30, v104, v105
v_mul_f32 v107, v107, v95
mav 34,60,66
R 50,576
v_fma_f32 v31, v31, v106, v107
mav 38,64,66
v_mul_f32 v109, v109, v95
v_fma_f32 v32, v32, v108, v109
mva 42,66,68
s_waitcnt lgkmcnt(3)
v_mul_f32 v111, v111, v95
v_fma_f32 v33, v33, v110, v111
mav 34,72,70
R 54,640
v_cvt_pk_bf16_f32 v78, v30, v31
mav 38,76,70
v_cvt_pk_bf16_f32 v79, v32, v33
mva 42,70,80
ds_write_b64 v84, v[78:79] offset:1024
mav 34,84,74
R 58,704
mav 38,88,74
ds_write_b64 v85, v[14:15] offset:128
mva 42,74,92
s_waitcnt lgkmcnt(4)
mav 34,96,46
R 62,768
mav 38,100,46
s_waitcnt lgkmcnt(1)
mva 42,46,104
s_barrier
mav 34,108,50
R 66,832
mav 38,112,50
mva 42,50,116
mav 34,120,54
R 70,896
mav 38,124,54
mva 42,54,128
mav 34,132,58
R 74,960
mav 38,136,58
mva 42,58,140
s_waitcnt lgkmcnt(2)
mav 34,144,62
R 46,1024
mav 38,148,62
mva 42,62,152
mav 34,156,66
R 50,1088
mav 38,160,66
mva 42,66,164
s_waitcnt lgkmcnt(2)
mav 34,168,70
R 54,1152
mav 38,172,70
mva 42,70,176
mav 34,180,74
R 58,1216
mav 38,184,74
mva 42,74,188
s_waitcnt lgkmcnt(2)
mav 34,192,46
R 62,1280
mav 38,196,46
mva 42,46,200
mav 34,204,50
R 66,1344
mav 38,208,50
mva 42,50,212
s_waitcnt lgkmcnt(2)
mav 34,216,54
R 70,1408
mav 38,220,54
mva 42,54,224
mav 34,228,58
R 74,1472
mav 38,232,58
mva 42,58,236
s_waitcnt lgkmcnt(2)
mav 34,240,62
R 46,1536
mav 38,244,62
mva 42,62,248
mav 34,252,66
R 50,1600
mvv 38,128,66
mvv 42,66,132
s_waitcnt lgkmcnt(2)
mvv 34,136,70
R 54,1664
mvv 38,140,70
mvv 42,70,144
mvv 34,148,74
R 58,1728
mvv 38,152,74
mvv 42,74,156
s_waitcnt lgkmcnt(2)
mvv 34,160,46
R 62,1792
mvv 38,164,46
mvv 42,46,168
mvv 34,172,50
R 66,1856
mvv 38,176,50
mvv 42,50,180
s_waitcnt lgkmcnt(2)
mvv 34,184,54
R 70,1920
mvv 38,188,54
mvv 42,54,192
mvv 34,196,58
R 74,1984
mvv 38,200,58
mvv 42,58,204
s_waitcnt lgkmcnt(2)
mvv 34,208,62
mvv 38,212,62
ds_read_b64 v[118:119], v85 offset:128
mvv 42,62,216
ds_read_b128 v[104:107], v86 offset:128
mvv 34,220,66
ds_read_b128 v[108:111], v86 offset:144
mvv 38,224,66
mvv 42,66,228
s_waitcnt lgkmcnt(3)
mvv 34,232,70
mvv 38,236,70
mvv 42,70,240
mvv 34,244,74
mvv 38,248,74
mvv 42,74,252
s_waitcnt lgkmcnt(0)
v_mul_f32 v120, v119, v96
s_nop 3
v_fma_f32 v34, v34, v118, v120
v_mul_f32 v121, v119, v97
v_fma_f32 v35, v35, v118, v121
v_mul_f32 v120, v119, v98
v_fma_f32 v36, v36, v118, v120
v_mul_f32 v121, v119, v99
v_fma_f32 v37, v37, v118, v121
v_mul_f32 v120, v119, v100
v_fma_f32 v38, v38, v118, v120
v_mul_f32 v121, v119, v101
v_fma_f32 v39, v39, v118, v121
v_mul_f32 v120, v119, v102
v_fma_f32 v40, v40, v118, v120
v_mul_f32 v121, v119, v103
v_fma_f32 v41, v41, v118, v121
v_cvt_pk_bf16_f32 v112, v34, v35
v_cvt_pk_bf16_f32 v113, v36, v37
v_cvt_pk_bf16_f32 v114, v38, v39
v_cvt_pk_bf16_f32 v115, v40, v41
ds_write_b128 v84, v[112:115] offset:4096
v_mul_f32 v105, v105, v95
v_fma_f32 v42, v42, v104, v105
v_mul_f32 v107, v107, v95
v_fma_f32 v43, v43, v106, v107
v_mul_f32 v109, v109, v95
v_fma_f32 v44, v44, v108, v109
v_mul_f32 v111, v111, v95
v_fma_f32 v45, v45, v110, v111
v_cvt_pk_bf16_f32 v78, v42, v43
v_cvt_pk_bf16_f32 v79, v44, v45
ds_write_b64 v84, v[78:79] offset:5120
s_waitcnt lgkmcnt(0)
s_barrier
s_endpgm
Cq:
s_cmp_eq_u32 s61, 0
s_cbranch_scc1 Bq
s_sub_u32 s61, s61, 1
s_sleep 2
buffer_load_dwordx2 v[122:123], v92, s[36:39], 0 offen sc1
s_waitcnt vmcnt(0)
ds_write_b32 v83, v122
v_sub_u32 v122, v122, v93
v_xor_b32 v123, v123, v117
v_cmp_lt_u64 vcc, s[62:63], v[122:123]
s_cbranch_vccnz Cq
s_branch Bq
Lr2:
s_cmp_eq_u32 s60, 2
s_cbranch_scc0 Lr3
s_waitcnt vmcnt(0)
v_accvgpr_read_b32 v57, a255
v_mov_b32 v56, 0x100
v_add_u32 v57, 0x9e3779b9, v57
buffer_store_dwordx2 v[56:57], v81, s[36:39], s52 offen sc1
v_add_u32 v87, v87, v56
s_sleep 32
s_sleep 32
E 0,65536
E 1,69632
s_add_u32 s51, s51, 65536
E 0,73728
E 1,77824
s_add_u32 s51, s51, 65536
E 0,81920
E 1,86016
s_add_u32 s51, s51, 65536
E 0,90112
E 1,94208
s_add_u32 s51, s51, 65536
s_mov_b64 exec, 0
s_cmp_eq_u32 s53, 0
s_cbranch_scc1 LsO0
s_cmp_eq_u32 s53, 1024
s_cbranch_scc1 LsO1
s_cmp_eq_u32 s53, 2048
s_cbranch_scc1 LsO2
s_branch LsO3
LwO:
s_mov_b64 exec, -1
s_waitcnt vmcnt(4)
s_barrier
v_add_u32 v4, 65536, v4
v_add_u32 v5, 65536, v5
Ta 0,4,0
Ta 4,5,0
Ta 8,4,8192
Ta 12,5,8192
E 0,98304
E 1,102400
s_add_u32 s51, s51, 65536
E 0,106496
E 1,110592
s_add_u32 s51, s51, 65536
E 0,114688
E 1,118784
s_add_u32 s51, s51, 65536
E 0,122880
E 1,126976
s_add_u32 s51, s51, 65536
s_cmp_ge_u32 s51, s57
s_cselect_b32 s60, 2097152, 0
s_sub_u32 s51, s51, s60
E 0,131072
E 1,135168
s_add_u32 s51, s51, 65536
E 0,139264
E 1,143360
s_add_u32 s51, s51, 65536
E 0,147456
E 1,151552
s_add_u32 s51, s51, 65536
E 0,155648
E 1,159744
s_add_u32 s51, s51, 65536
s_waitcnt lgkmcnt(0)
s_waitcnt vmcnt(16)
s_barrier
E 0,65536
Ta 16,4,16384
Ta 20,5,16384
E 1,69632
s_add_u32 s51, s51, 65536
Ta 24,4,24576
Ta 28,5,24576
E 0,73728
E 1,77824
s_add_u32 s51, s51, 65536
s_waitcnt lgkmcnt(0)
s_waitcnt vmcnt(16)
s_barrier
E 0,81920
Ta 32,4,32768
Ta 36,5,32768
E 1,86016
s_add_u32 s51, s51, 65536
Ta 40,4,40960
Ta 44,5,40960
E 0,90112
E 1,94208
s_add_u32 s51, s51, 65536
s_cmp_ge_u32 s51, s57
s_cselect_b32 s60, 2097152, 0
s_sub_u32 s51, s51, s60
s_waitcnt lgkmcnt(0)
s_waitcnt vmcnt(16)
s_barrier
E 0,98304
Ta 48,4,49152
Ta 52,5,49152
E 1,102400
s_add_u32 s51, s51, 65536
Ta 56,4,57344
Ta 60,5,57344
E 0,106496
E 1,110592
s_add_u32 s51, s51, 65536
s_waitcnt lgkmcnt(0)
s_waitcnt vmcnt(16)
s_barrier
v_add_u32 v4, 65536, v4
v_add_u32 v5, 65536, v5
E 0,114688
Ta 64,4,0
Ta 68,5,0
E 1,118784
s_add_u32 s51, s51, 65536
Ta 72,4,8192
Ta 76,5,8192
E 0,122880
E 1,126976
s_add_u32 s51, s51, 65536
s_waitcnt lgkmcnt(0)
s_waitcnt vmcnt(16)
s_barrier
E 0,131072
Ta 80,4,16384
Ta 84,5,16384
E 1,135168
s_add_u32 s51, s51, 65536
Ta 88,4,24576
Ta 92,5,24576
E 0,139264
E 1,143360
s_add_u32 s51, s51, 65536
s_waitcnt lgkmcnt(0)
s_waitcnt vmcnt(16)
s_barrier
v_add_u32 v4, -65536, v4
v_add_u32 v5, -65536, v5
E 0,147456
Ta 96,4,0
Ta 100,5,0
E 1,151552
s_add_u32 s51, s51, 65536
Ta 104,4,8192
Ta 108,5,8192
E 0,155648
E 1,159744
s_add_u32 s51, s51, 65536
s_cmp_ge_u32 s51, s57
s_cselect_b32 s60, 2097152, 0
s_sub_u32 s51, s51, s60
s_waitcnt lgkmcnt(0)
s_waitcnt vmcnt(16)
s_barrier
E 0,65536
Ta 112,4,16384
Ta 116,5,16384
E 1,69632
s_add_u32 s51, s51, 65536
Ta 120,4,24576
Ta 124,5,24576
E 0,73728
E 1,77824
s_add_u32 s51, s51, 65536
s_waitcnt lgkmcnt(0)
s_waitcnt vmcnt(16)
s_barrier
LsO0:
s_cbranch_execz LsO4
E 0,81920
Ta 128,4,32768
Ta 132,5,32768
E 1,86016
s_add_u32 s51, s51, 65536
Ta 136,4,40960
Ta 140,5,40960
E 0,90112
E 1,94208
s_add_u32 s51, s51, 65536
s_waitcnt lgkmcnt(0)
s_waitcnt vmcnt(16)
s_barrier
E 0,98304
Ta 144,4,49152
Ta 148,5,49152
E 1,102400
s_add_u32 s51, s51, 65536
Ta 152,4,57344
Ta 156,5,57344
E 0,106496
E 1,110592
s_add_u32 s51, s51, 65536
s_waitcnt lgkmcnt(0)
s_waitcnt vmcnt(16)
s_barrier
v_add_u32 v4, 65536, v4
v_add_u32 v5, 65536, v5
E 0,114688
Ta 160,4,0
Ta 164,5,0
E 1,118784
s_add_u32 s51, s51, 65536
Ta 168,4,8192
Ta 172,5,8192
E 0,122880
E 1,126976
s_add_u32 s51, s51, 65536
s_cmp_ge_u32 s51, s57
s_cselect_b32 s60, 2097152, 0
s_sub_u32 s51, s51, s60
s_waitcnt lgkmcnt(0)
s_waitcnt vmcnt(16)
s_barrier
Ta 176,4,16384
Ta 180,5,16384
Ta 184,4,24576
Ta 188,5,24576
s_waitcnt lgkmcnt(0)
s_waitcnt vmcnt(12)
s_barrier
v_add_u32 v4, -65536, v4
v_add_u32 v5, -65536, v5
Ta 192,4,0
Ta 196,5,0
Ta 200,4,8192
Ta 204,5,8192
s_waitcnt lgkmcnt(0)
s_waitcnt vmcnt(8)
s_barrier
Ta 208,4,16384
Ta 212,5,16384
Ta 216,4,24576
Ta 220,5,24576
s_waitcnt lgkmcnt(0)
s_waitcnt vmcnt(4)
s_barrier
Ta 224,4,32768
Ta 228,5,32768
Ta 232,4,40960
Ta 236,5,40960
s_waitcnt lgkmcnt(0)
s_waitcnt vmcnt(0)
s_barrier
Ta 240,4,49152
Ta 244,5,49152
Ta 248,4,57344
Ta 252,5,57344
s_waitcnt lgkmcnt(0)
s_waitcnt vmcnt(0)
s_barrier
s_nop 7
s_add_u32 s40, s36, 4194304
s_addc_u32 s41, s37, 0
s_and_b32 s60, s48, 3
s_cmp_eq_u32 s60, 1
s_cbranch_scc1 LvO1
s_cmp_eq_u32 s60, 2
s_cbranch_scc1 LvO2
s_cmp_eq_u32 s60, 3
s_cbranch_scc1 LvO3
LvO0:
LsO1:
s_cbranch_execz LsO5
buffer_load_dwordx2 v[60:61], v86, s[36:39], 0 offen sc1
s_waitcnt vmcnt(0)
ds_write_b32 v82, v60
v_sub_u32 v60, v60, v87
v_xor_b32 v61, v61, v57
v_cmp_lt_u64 vcc, s[62:63], v[60:61]
s_waitcnt lgkmcnt(0)
s_cbranch_vccnz CO0_1
BO0_1:
s_add_u32 s49, s49, 313344
s_barrier
buffer_load_dwordx4 v[48:51], v88, s[44:47], s54 offen sc1
s_add_u32 m0, s50, 0
s_nop 0
D 84,0
D 84,1024
D 84,2048
D 84,3072
s_add_u32 m0, s50, 4096
s_nop 0
D 85,0
D 85,1024
D 85,2048
D 85,3072
s_mov_b32 m0, 144384
s_nop 0
buffer_load_dword v86, s[36:39], 0 offen sc1 lds
s_waitcnt vmcnt(9)
v_lshlrev_b32 v0, 16, v48
v_and_b32 v1, 0xffff0000, v48
v_lshlrev_b32 v2, 16, v49
v_and_b32 v3, 0xffff0000, v49
v_lshlrev_b32 v4, 16, v50
v_and_b32 v5, 0xffff0000, v50
v_lshlrev_b32 v6, 16, v51
v_and_b32 v7, 0xffff0000, v51
s_sub_u32 s54, s54, 0x8000
buffer_load_dwordx4 v[48:51], v88, s[44:47], s54 offen sc1
buffer_load_dword v86, s[36:39], 0 offen sc1 lds
v_add_u32 v87, 1, v87
ds_read_b32 v60, v82 offset:0
s_waitcnt lgkmcnt(0)
v_cmp_gt_i32 vcc, v87, v60
s_waitcnt vmcnt(3)
s_add_u32 s49, s49, 313344
s_cbranch_vccnz CO0_2
BO0_2:
s_barrier
s_add_u32 m0, s50, 32768
R 16,0
D 84,0
R 20,1024
R 24,2048
D 84,1024
R 28,3072
R 32,4096
D 84,2048
s_waitcnt lgkmcnt(3)
mav 0,0,16
R 36,5120
mav 4,4,16
mav 0,8,20
R 40,6144
mav 4,12,20
s_waitcnt lgkmcnt(3)
D 84,3072
mav 0,16,24
R 44,7168
mav 4,20,24
s_add_u32 m0, s50, 36864
mav 0,24,28
R 16,8192
mav 4,28,28
s_waitcnt lgkmcnt(3)
D 85,0
mav 0,32,32
R 20,9216
mav 4,36,32
mav 0,40,36
R 24,10240
mav 4,44,36
s_waitcnt lgkmcnt(3)
D 85,1024
mav 0,48,40
R 28,11264
mav 4,52,40
mav 0,56,44
R 32,12288
mav 4,60,44
s_waitcnt lgkmcnt(3)
D 85,2048
mav 0,64,16
R 36,13312
mav 4,68,16
mav 0,72,20
R 40,14336
mav 4,76,20
s_waitcnt lgkmcnt(3)
D 85,3072
mav 0,80,24
R 44,15360
mav 4,84,24
mav 0,88,28
R 16,16384
mav 4,92,28
s_waitcnt lgkmcnt(3)
mav 0,96,32
R 20,17408
mav 4,100,32
mav 0,104,36
s_mov_b32 m0, 144384
R 24,18432
mav 4,108,36
s_waitcnt lgkmcnt(3)
mav 0,112,40
R 28,19456
mav 4,116,40
mav 0,120,44
s_waitcnt vmcnt(9)
R 32,20480
v_lshlrev_b32 v8, 16, v48
mav 4,124,44
s_waitcnt lgkmcnt(3)
buffer_load_dword v86, s[36:39], 0 offen sc1 lds
mav 0,128,16
R 36,21504
v_and_b32 v9, 0xffff0000, v48
mav 4,132,16
v_lshlrev_b32 v10, 16, v49
v_and_b32 v11, 0xffff0000, v49
mav 0,136,20
R 40,22528
v_lshlrev_b32 v12, 16, v50
mav 4,140,20
s_waitcnt lgkmcnt(3)
v_and_b32 v13, 0xffff0000, v50
v_lshlrev_b32 v14, 16, v51
mav 0,144,24
R 44,23552
v_and_b32 v15, 0xffff0000, v51
mav 4,148,24
s_sub_u32 s54, s54, 0x8000
mav 0,152,28
R 16,24576
mav 4,156,28
s_waitcnt lgkmcnt(3)
s_cmp_lt_u32 s54, s56
s_cselect_b64 s[44:45], s[40:41], s[44:45]
mav 0,160,32
R 20,25600
v_add_u32 v87, 1, v87
mav 4,164,32
mav 0,168,36
R 24,26624
mav 4,172,36
s_waitcnt lgkmcnt(3)
buffer_load_dwordx4 v[48:51], v88, s[44:47], s54 offen sc1
mav 0,176,40
R 28,27648
mav 4,180,40
mav 0,184,44
R 32,28672
mav 4,188,44
s_waitcnt lgkmcnt(3)
buffer_load_dword v86, s[36:39], 0 offen sc1 lds
mav 0,192,16
R 36,29696
mav 4,196,16
mav 0,200,20
R 40,30720
mav 4,204,20
s_waitcnt lgkmcnt(3)
mav 0,208,24
R 44,31744
mav 4,212,24
mav 0,216,28
mav 4,220,28
s_waitcnt lgkmcnt(2)
mav 0,224,32
mav 4,228,32
mav 0,232,36
mav 4,236,36
s_waitcnt lgkmcnt(0)
mav 0,240,40
mav 4,244,40
mav 0,248,44
mav 4,252,44
ds_read_b32 v60, v82 offset:0
s_nop 5
v_cvt_pk_bf16_f32 v52, v0, v1
v_cvt_pk_bf16_f32 v53, v2, v3
v_cvt_pk_bf16_f32 v54, v4, v5
v_cvt_pk_bf16_f32 v55, v6, v7
buffer_store_dwordx4 v[52:55], v83, s[36:39], s49 offen sc1
v_mul_f32 v59, v0, v0
v_add_f32 v58, v1, v0
v_fmac_f32 v59, v1, v1
v_add_f32 v58, v2, v58
v_fmac_f32 v59, v2, v2
v_add_f32 v58, v3, v58
v_fmac_f32 v59, v3, v3
v_add_f32 v58, v4, v58
v_fmac_f32 v59, v4, v4
v_add_f32 v58, v5, v58
v_fmac_f32 v59, v5, v5
v_add_f32 v58, v6, v58
v_fmac_f32 v59, v6, v6
v_add_f32 v58, v7, v58
v_fmac_f32 v59, v7, v7
s_nop 1
v_permlane32_swap_b32 v58, v59
v_add_f32 v58, v58, v59
ds_write_b32 v89, v58 offset:0
s_xor_b32 s50, s50, 0x10000
s_waitcnt lgkmcnt(1)
v_cmp_gt_i32 vcc, v87, v60
s_waitcnt vmcnt(4) lgkmcnt(0)
s_add_u32 s49, s49, 313344
s_cbranch_vccnz CO0_3
BO0_3:
s_barrier
s_add_u32 m0, s50, 0
R 16,32768
D 84,0
R 20,33792
R 24,34816
D 84,1024
R 28,35840
R 32,36864
D 84,2048
s_waitcnt lgkmcnt(3)
mav 8,0,16
R 36,37888
mav 12,4,16
mav 8,8,20
R 40,38912
mav 12,12,20
s_waitcnt lgkmcnt(3)
D 84,3072
mav 8,16,24
R 44,39936
mav 12,20,24
s_add_u32 m0, s50, 4096
mav 8,24,28
R 16,40960
mav 12,28,28
s_waitcnt lgkmcnt(3)
D 85,0
mav 8,32,32
R 20,41984
mav 12,36,32
mav 8,40,36
R 24,43008
mav 12,44,36
s_waitcnt lgkmcnt(3)
D 85,1024
mav 8,48,40
R 28,44032
v_add_u32 v56, 1, v56
mav 12,52,40
mav 8,56,44
R 32,45056
mav 12,60,44
s_waitcnt lgkmcnt(3)
D 85,2048
mav 8,64,16
R 36,46080
mav 12,68,16
mav 8,72,20
R 40,47104
mav 12,76,20
s_waitcnt lgkmcnt(3)
D 85,3072
mav 8,80,24
R 44,48128
mav 12,84,24
mav 8,88,28
R 16,49152
mav 12,92,28
s_waitcnt lgkmcnt(3)
mav 8,96,32
R 20,50176
mav 12,100,32
mav 8,104,36
s_mov_b32 m0, 144384
R 24,51200
mav 12,108,36
s_waitcnt lgkmcnt(3)
mav 8,112,40
R 28,52224
mav 12,116,40
mav 8,120,44
s_waitcnt vmcnt(10)
R 32,53248
v_lshlrev_b32 v0, 16, v48
mav 12,124,44
s_waitcnt lgkmcnt(3)
buffer_load_dword v86, s[36:39], 0 offen sc1 lds
mav 8,128,16
R 36,54272
v_and_b32 v1, 0xffff0000, v48
mav 12,132,16
v_lshlrev_b32 v2, 16, v49
v_and_b32 v3, 0xffff0000, v49
mav 8,136,20
R 40,55296
v_lshlrev_b32 v4, 16, v50
mav 12,140,20
s_waitcnt lgkmcnt(3)
v_and_b32 v5, 0xffff0000, v50
v_lshlrev_b32 v6, 16, v51
mav 8,144,24
s_waitcnt vmcnt(9)
R 44,56320
v_and_b32 v7, 0xffff0000, v51
mav 12,148,24
mav 8,152,28
s_sub_u32 s54, s54, 0x8000
R 16,57344
mav 12,156,28
s_waitcnt lgkmcnt(3)
buffer_store_dword v56, v81, s[36:39], s52 offen sc1
mav 8,160,32
R 20,58368
v_add_u32 v87, 1, v87
mav 12,164,32
mav 8,168,36
R 24,59392
mav 12,172,36
s_waitcnt lgkmcnt(3)
buffer_load_dwordx4 v[48:51], v88, s[44:47], s54 offen sc1
mav 8,176,40
R 28,60416
mav 12,180,40
mav 8,184,44
R 32,61440
mav 12,188,44
s_waitcnt lgkmcnt(3)
buffer_load_dword v86, s[36:39], 0 offen sc1 lds
mav 8,192,16
R 36,62464
mav 12,196,16
mav 8,200,20
R 40,63488
mav 12,204,20
s_waitcnt lgkmcnt(3)
mav 8,208,24
R 44,64512
mav 12,212,24
mav 8,216,28
mav 12,220,28
s_waitcnt lgkmcnt(2)
mav 8,224,32
mav 12,228,32
mav 8,232,36
s_mov_b32 s60, -30
.p2align 6
LO0_4:
mav 12,236,36
s_waitcnt lgkmcnt(0)
mav 8,240,40
mav 12,244,40
mav 8,248,44
mav 12,252,44
ds_read_b32 v60, v82 offset:0
s_nop 5
v_cvt_pk_bf16_f32 v52, v8, v9
v_cvt_pk_bf16_f32 v53, v10, v11
v_cvt_pk_bf16_f32 v54, v12, v13
v_cvt_pk_bf16_f32 v55, v14, v15
buffer_store_dwordx4 v[52:55], v83, s[36:39], s49 offen sc1
v_mul_f32 v59, v8, v8
v_add_f32 v58, v9, v8
v_fmac_f32 v59, v9, v9
v_add_f32 v58, v10, v58
v_fmac_f32 v59, v10, v10
v_add_f32 v58, v11, v58
v_fmac_f32 v59, v11, v11
v_add_f32 v58, v12, v58
v_fmac_f32 v59, v12, v12
v_add_f32 v58, v13, v58
v_fmac_f32 v59, v13, v13
v_add_f32 v58, v14, v58
v_fmac_f32 v59, v14, v14
v_add_f32 v58, v15, v58
v_fmac_f32 v59, v15, v15
s_nop 1
v_permlane32_swap_b32 v58, v59
v_add_f32 v58, v58, v59
ds_write_b32 v89, v58 offset:1024
s_waitcnt lgkmcnt(1)
v_cmp_gt_i32 vcc, v87, v60
v_xor_b32 v80, 0x10000, v80
s_waitcnt vmcnt(5) lgkmcnt(0)
s_add_u32 s49, s49, 313344
s_cbranch_vccnz CO0_5
BO0_5:
s_barrier
s_add_u32 m0, s50, 32768
R 16,0
D 84,0
R 20,1024
R 24,2048
D 84,1024
R 28,3072
R 32,4096
D 84,2048
s_waitcnt lgkmcnt(3)
mav 0,0,16
R 36,5120
mav 4,4,16
mav 0,8,20
R 40,6144
mav 4,12,20
s_waitcnt lgkmcnt(3)
D 84,3072
mav 0,16,24
R 44,7168
mav 4,20,24
s_add_u32 m0, s50, 36864
mav 0,24,28
R 16,8192
mav 4,28,28
s_waitcnt lgkmcnt(3)
D 85,0
mav 0,32,32
R 20,9216
mav 4,36,32
mav 0,40,36
R 24,10240
mav 4,44,36
s_waitcnt lgkmcnt(3)
D 85,1024
mav 0,48,40
R 28,11264
v_add_u32 v56, 1, v56
mav 4,52,40
mav 0,56,44
R 32,12288
mav 4,60,44
s_waitcnt lgkmcnt(3)
D 85,2048
mav 0,64,16
R 36,13312
mav 4,68,16
mav 0,72,20
R 40,14336
mav 4,76,20
s_waitcnt lgkmcnt(3)
D 85,3072
mav 0,80,24
R 44,15360
mav 4,84,24
mav 0,88,28
R 16,16384
LsO2:
s_cbranch_execz LsO6
mav 4,92,28
s_waitcnt lgkmcnt(3)
mav 0,96,32
R 20,17408
mav 4,100,32
mav 0,104,36
s_mov_b32 m0, 144384
R 24,18432
mav 4,108,36
s_waitcnt lgkmcnt(3)
mav 0,112,40
R 28,19456
mav 4,116,40
mav 0,120,44
s_waitcnt vmcnt(10)
R 32,20480
v_lshlrev_b32 v8, 16, v48
mav 4,124,44
s_waitcnt lgkmcnt(3)
buffer_load_dword v86, s[36:39], 0 offen sc1 lds
mav 0,128,16
R 36,21504
v_and_b32 v9, 0xffff0000, v48
mav 4,132,16
v_lshlrev_b32 v10, 16, v49
v_and_b32 v11, 0xffff0000, v49
mav 0,136,20
R 40,22528
v_lshlrev_b32 v12, 16, v50
mav 4,140,20
s_waitcnt lgkmcnt(3)
v_and_b32 v13, 0xffff0000, v50
v_lshlrev_b32 v14, 16, v51
mav 0,144,24
s_waitcnt vmcnt(9)
R 44,23552
v_and_b32 v15, 0xffff0000, v51
mav 4,148,24
s_sub_u32 s54, s54, 0x8000
mav 0,152,28
R 16,24576
mav 4,156,28
s_waitcnt lgkmcnt(3)
buffer_store_dword v56, v81, s[36:39], s52 offen sc1
mav 0,160,32
R 20,25600
v_add_u32 v87, 1, v87
mav 4,164,32
s_cmp_lt_u32 s54, s56
s_cselect_b64 s[44:45], s[40:41], s[44:45]
mav 0,168,36
R 24,26624
mav 4,172,36
s_waitcnt lgkmcnt(3)
buffer_load_dwordx4 v[48:51], v88, s[44:47], s54 offen sc1
mav 0,176,40
R 28,27648
mav 4,180,40
mav 0,184,44
R 32,28672
mav 4,188,44
s_waitcnt lgkmcnt(3)
buffer_load_dword v86, s[36:39], 0 offen sc1 lds
mav 0,192,16
R 36,29696
mav 4,196,16
mav 0,200,20
R 40,30720
mav 4,204,20
s_waitcnt lgkmcnt(3)
mav 0,208,24
R 44,31744
mav 4,212,24
mav 0,216,28
mav 4,220,28
s_waitcnt lgkmcnt(2)
mav 0,224,32
mav 4,228,32
mav 0,232,36
mav 4,236,36
s_waitcnt lgkmcnt(0)
mav 0,240,40
mav 4,244,40
mav 0,248,44
mav 4,252,44
ds_read_b32 v60, v82 offset:0
s_nop 5
v_cvt_pk_bf16_f32 v52, v0, v1
v_cvt_pk_bf16_f32 v53, v2, v3
v_cvt_pk_bf16_f32 v54, v4, v5
v_cvt_pk_bf16_f32 v55, v6, v7
buffer_store_dwordx4 v[52:55], v83, s[36:39], s49 offen sc1
v_mul_f32 v59, v0, v0
v_add_f32 v58, v1, v0
v_fmac_f32 v59, v1, v1
v_add_f32 v58, v2, v58
v_fmac_f32 v59, v2, v2
v_add_f32 v58, v3, v58
v_fmac_f32 v59, v3, v3
v_add_f32 v58, v4, v58
v_fmac_f32 v59, v4, v4
v_add_f32 v58, v5, v58
v_fmac_f32 v59, v5, v5
v_add_f32 v58, v6, v58
v_fmac_f32 v59, v6, v6
v_add_f32 v58, v7, v58
v_fmac_f32 v59, v7, v7
s_nop 1
v_permlane32_swap_b32 v58, v59
v_add_f32 v58, v58, v59
ds_write_b32 v89, v58 offset:0
s_xor_b32 s50, s50, 0x10000
s_waitcnt lgkmcnt(1)
v_cmp_gt_i32 vcc, v87, v60
s_waitcnt vmcnt(5) lgkmcnt(0)
s_add_u32 s49, s49, 313344
s_cbranch_vccnz CO0_6
BO0_6:
s_barrier
s_add_u32 m0, s50, 0
R 16,32768
D 84,0
R 20,33792
R 24,34816
D 84,1024
R 28,35840
R 32,36864
D 84,2048
s_waitcnt lgkmcnt(3)
mav 8,0,16
R 36,37888
mav 12,4,16
mav 8,8,20
R 40,38912
mav 12,12,20
s_waitcnt lgkmcnt(3)
D 84,3072
mav 8,16,24
R 44,39936
mav 12,20,24
s_add_u32 m0, s50, 4096
mav 8,24,28
R 16,40960
mav 12,28,28
s_waitcnt lgkmcnt(3)
D 85,0
mav 8,32,32
R 20,41984
mav 12,36,32
mav 8,40,36
R 24,43008
mav 12,44,36
s_waitcnt lgkmcnt(3)
D 85,1024
mav 8,48,40
R 28,44032
v_add_u32 v56, 1, v56
mav 12,52,40
mav 8,56,44
R 32,45056
mav 12,60,44
s_waitcnt lgkmcnt(3)
D 85,2048
mav 8,64,16
R 36,46080
mav 12,68,16
mav 8,72,20
R 40,47104
mav 12,76,20
s_waitcnt lgkmcnt(3)
D 85,3072
mav 8,80,24
R 44,48128
mav 12,84,24
mav 8,88,28
R 16,49152
mav 12,92,28
s_waitcnt lgkmcnt(3)
mav 8,96,32
R 20,50176
mav 12,100,32
mav 8,104,36
s_mov_b32 m0, 144384
R 24,51200
mav 12,108,36
s_waitcnt lgkmcnt(3)
mav 8,112,40
R 28,52224
mav 12,116,40
mav 8,120,44
s_waitcnt vmcnt(10)
R 32,53248
v_lshlrev_b32 v0, 16, v48
mav 12,124,44
s_waitcnt lgkmcnt(3)
buffer_load_dword v86, s[36:39], 0 offen sc1 lds
mav 8,128,16
R 36,54272
v_and_b32 v1, 0xffff0000, v48
mav 12,132,16
v_lshlrev_b32 v2, 16, v49
v_and_b32 v3, 0xffff0000, v49
mav 8,136,20
R 40,55296
v_lshlrev_b32 v4, 16, v50
mav 12,140,20
s_waitcnt lgkmcnt(3)
v_and_b32 v5, 0xffff0000, v50
v_lshlrev_b32 v6, 16, v51
mav 8,144,24
s_waitcnt vmcnt(9)
R 44,56320
v_and_b32 v7, 0xffff0000, v51
mav 12,148,24
mav 8,152,28
s_sub_u32 s54, s54, 0x8000
R 16,57344
mav 12,156,28
s_waitcnt lgkmcnt(3)
buffer_store_dword v56, v81, s[36:39], s52 offen sc1
mav 8,160,32
R 20,58368
v_add_u32 v87, 1, v87
mav 12,164,32
mav 8,168,36
R 24,59392
mav 12,172,36
s_waitcnt lgkmcnt(3)
buffer_load_dwordx4 v[48:51], v88, s[44:47], s54 offen sc1
mav 8,176,40
R 28,60416
mav 12,180,40
mav 8,184,44
R 32,61440
mav 12,188,44
s_waitcnt lgkmcnt(3)
buffer_load_dword v86, s[36:39], 0 offen sc1 lds
mav 8,192,16
R 36,62464
mav 12,196,16
mav 8,200,20
R 40,63488
mav 12,204,20
s_waitcnt lgkmcnt(3)
mav 8,208,24
R 44,64512
mav 12,212,24
mav 8,216,28
mav 12,220,28
s_waitcnt lgkmcnt(2)
mav 8,224,32
mav 12,228,32
s_add_u32 s60, s60, 1
mav 8,232,36
s_cbranch_scc0 LO0_4
mav 12,236,36
s_waitcnt lgkmcnt(0)
mav 8,240,40
mav 12,244,40
mav 8,248,44
mav 12,252,44
ds_read_b32 v60, v82 offset:0
s_nop 5
v_cvt_pk_bf16_f32 v52, v8, v9
v_cvt_pk_bf16_f32 v53, v10, v11
v_cvt_pk_bf16_f32 v54, v12, v13
v_cvt_pk_bf16_f32 v55, v14, v15
buffer_store_dwordx4 v[52:55], v83, s[36:39], s49 offen sc1
v_mul_f32 v59, v8, v8
v_add_f32 v58, v9, v8
v_fmac_f32 v59, v9, v9
v_add_f32 v58, v10, v58
v_fmac_f32 v59, v10, v10
v_add_f32 v58, v11, v58
v_fmac_f32 v59, v11, v11
v_add_f32 v58, v12, v58
v_fmac_f32 v59, v12, v12
v_add_f32 v58, v13, v58
v_fmac_f32 v59, v13, v13
v_add_f32 v58, v14, v58
v_fmac_f32 v59, v14, v14
v_add_f32 v58, v15, v58
v_fmac_f32 v59, v15, v15
s_nop 1
v_permlane32_swap_b32 v58, v59
v_add_f32 v58, v58, v59
ds_write_b32 v89, v58 offset:1024
s_waitcnt lgkmcnt(1)
v_cmp_gt_i32 vcc, v87, v60
v_xor_b32 v80, 0x10000, v80
s_waitcnt vmcnt(5) lgkmcnt(0)
s_add_u32 s49, s49, 313344
s_cbranch_vccnz CO0_7
BO0_7:
s_barrier
s_add_u32 m0, s50, 32768
R 16,0
D 84,0
R 20,1024
R 24,2048
D 84,1024
R 28,3072
R 32,4096
D 84,2048
s_waitcnt lgkmcnt(3)
mav 0,0,16
R 36,5120
mav 4,4,16
mav 0,8,20
R 40,6144
mav 4,12,20
s_waitcnt lgkmcnt(3)
D 84,3072
mav 0,16,24
R 44,7168
mav 4,20,24
s_add_u32 m0, s50, 36864
mav 0,24,28
R 16,8192
mav 4,28,28
s_waitcnt lgkmcnt(3)
D 85,0
mav 0,32,32
R 20,9216
mav 4,36,32
mav 0,40,36
R 24,10240
mav 4,44,36
s_waitcnt lgkmcnt(3)
D 85,1024
mav 0,48,40
R 28,11264
v_add_u32 v56, 1, v56
mav 4,52,40
mav 0,56,44
R 32,12288
mav 4,60,44
s_waitcnt lgkmcnt(3)
D 85,2048
mav 0,64,16
R 36,13312
mav 4,68,16
mav 0,72,20
R 40,14336
mav 4,76,20
s_waitcnt lgkmcnt(3)
D 85,3072
mav 0,80,24
R 44,15360
mav 4,84,24
mav 0,88,28
R 16,16384
mav 4,92,28
s_waitcnt lgkmcnt(3)
mav 0,96,32
R 20,17408
mav 4,100,32
mav 0,104,36
R 24,18432
mav 4,108,36
s_waitcnt lgkmcnt(3)
mav 0,112,40
R 28,19456
mav 4,116,40
mav 0,120,44
s_waitcnt vmcnt(10)
R 32,20480
v_lshlrev_b32 v8, 16, v48
mav 4,124,44
s_waitcnt lgkmcnt(3)
v_and_b32 v9, 0xffff0000, v48
v_lshlrev_b32 v10, 16, v49
mav 0,128,16
R 36,21504
v_and_b32 v11, 0xffff0000, v49
mav 4,132,16
v_lshlrev_b32 v12, 16, v50
v_and_b32 v13, 0xffff0000, v50
mav 0,136,20
R 40,22528
v_lshlrev_b32 v14, 16, v51
mav 4,140,20
s_waitcnt lgkmcnt(3)
v_and_b32 v15, 0xffff0000, v51
mav 0,144,24
s_waitcnt vmcnt(8)
R 44,23552
mav 4,148,24
mav 0,152,28
R 16,24576
mav 4,156,28
s_waitcnt lgkmcnt(3)
buffer_store_dword v56, v81, s[36:39], s52 offen sc1
mav 0,160,32
R 20,25600
mav 4,164,32
mav 0,168,36
R 24,26624
mav 4,172,36
s_waitcnt lgkmcnt(3)
mav 0,176,40
R 28,27648
mav 4,180,40
mav 0,184,44
R 32,28672
mav 4,188,44
s_waitcnt lgkmcnt(3)
mav 0,192,16
R 36,29696
mav 4,196,16
mav 0,200,20
R 40,30720
mav 4,204,20
s_waitcnt lgkmcnt(3)
mav 0,208,24
R 44,31744
mav 4,212,24
mav 0,216,28
mav 4,220,28
s_waitcnt lgkmcnt(2)
mav 0,224,32
mav 4,228,32
mav 0,232,36
mav 4,236,36
s_waitcnt lgkmcnt(0)
mav 0,240,40
mav 4,244,40
mav 0,248,44
mav 4,252,44
s_nop 6
v_cvt_pk_bf16_f32 v52, v0, v1
v_cvt_pk_bf16_f32 v53, v2, v3
v_cvt_pk_bf16_f32 v54, v4, v5
v_cvt_pk_bf16_f32 v55, v6, v7
buffer_store_dwordx4 v[52:55], v83, s[36:39], s49 offen sc1
v_mul_f32 v59, v0, v0
v_add_f32 v58, v1, v0
v_fmac_f32 v59, v1, v1
v_add_f32 v58, v2, v58
v_fmac_f32 v59, v2, v2
v_add_f32 v58, v3, v58
v_fmac_f32 v59, v3, v3
v_add_f32 v58, v4, v58
v_fmac_f32 v59, v4, v4
v_add_f32 v58, v5, v58
v_fmac_f32 v59, v5, v5
v_add_f32 v58, v6, v58
v_fmac_f32 v59, v6, v6
v_add_f32 v58, v7, v58
v_fmac_f32 v59, v7, v7
s_nop 1
v_permlane32_swap_b32 v58, v59
v_add_f32 v58, v58, v59
ds_write_b32 v89, v58 offset:0
s_waitcnt vmcnt(2) lgkmcnt(0)
s_add_u32 s49, s49, 313344
s_barrier
R 16,32768
R 20,33792
R 24,34816
R 28,35840
R 32,36864
s_waitcnt lgkmcnt(3)
mav 8,0,16
R 36,37888
mav 12,4,16
mav 8,8,20
R 40,38912
mav 12,12,20
s_waitcnt lgkmcnt(3)
mav 8,16,24
R 44,39936
mav 12,20,24
mav 8,24,28
R 16,40960
mav 12,28,28
s_waitcnt lgkmcnt(3)
mav 8,32,32
R 20,41984
mav 12,36,32
mav 8,40,36
R 24,43008
mav 12,44,36
s_waitcnt lgkmcnt(3)
mav 8,48,40
R 28,44032
v_add_u32 v56, 1, v56
mav 12,52,40
mav 8,56,44
R 32,45056
mav 12,60,44
s_waitcnt lgkmcnt(3)
mav 8,64,16
R 36,46080
mav 12,68,16
mav 8,72,20
R 40,47104
mav 12,76,20
s_waitcnt lgkmcnt(3)
mav 8,80,24
R 44,48128
mav 12,84,24
mav 8,88,28
R 16,49152
mav 12,92,28
s_waitcnt lgkmcnt(3)
mav 8,96,32
R 20,50176
mav 12,100,32
mav 8,104,36
R 24,51200
mav 12,108,36
s_waitcnt lgkmcnt(3)
mav 8,112,40
R 28,52224
mav 12,116,40
mav 8,120,44
R 32,53248
mav 12,124,44
s_waitcnt lgkmcnt(3)
mav 8,128,16
R 36,54272
mav 12,132,16
mav 8,136,20
R 40,55296
mav 12,140,20
s_waitcnt lgkmcnt(3)
mav 8,144,24
s_waitcnt vmcnt(0)
R 44,56320
mav 12,148,24
mav 8,152,28
R 16,57344
mav 12,156,28
s_waitcnt lgkmcnt(3)
buffer_store_dword v56, v81, s[36:39], s52 offen sc1
mav 8,160,32
R 20,58368
mav 12,164,32
mav 8,168,36
R 24,59392
mav 12,172,36
s_waitcnt lgkmcnt(3)
mav 8,176,40
R 28,60416
mav 12,180,40
mav 8,184,44
R 32,61440
mav 12,188,44
s_waitcnt lgkmcnt(3)
mav 8,192,16
R 36,62464
mav 12,196,16
mav 8,200,20
R 40,63488
mav 12,204,20
s_waitcnt lgkmcnt(3)
mav 8,208,24
R 44,64512
mav 12,212,24
mav 8,216,28
mav 12,220,28
s_waitcnt lgkmcnt(2)
mav 8,224,32
mav 12,228,32
mav 8,232,36
mav 12,236,36
s_waitcnt lgkmcnt(0)
mav 8,240,40
mav 12,244,40
mav 8,248,44
mav 12,252,44
s_nop 6
v_cvt_pk_bf16_f32 v52, v8, v9
v_cvt_pk_bf16_f32 v53, v10, v11
v_cvt_pk_bf16_f32 v54, v12, v13
v_cvt_pk_bf16_f32 v55, v14, v15
buffer_store_dwordx4 v[52:55], v83, s[36:39], s49 offen sc1
v_mul_f32 v59, v8, v8
v_add_f32 v58, v9, v8
v_fmac_f32 v59, v9, v9
v_add_f32 v58, v10, v58
v_fmac_f32 v59, v10, v10
v_add_f32 v58, v11, v58
v_fmac_f32 v59, v11, v11
v_add_f32 v58, v12, v58
v_fmac_f32 v59, v12, v12
v_add_f32 v58, v13, v58
v_fmac_f32 v59, v13, v13
v_add_f32 v58, v14, v58
v_fmac_f32 v59, v14, v14
v_add_f32 v58, v15, v58
v_fmac_f32 v59, v15, v15
s_nop 1
v_permlane32_swap_b32 v58, v59
v_add_f32 v58, v58, v59
ds_write_b32 v89, v58 offset:1024
s_waitcnt lgkmcnt(0)
s_add_u32 s49, s49, 313344
s_barrier
v_add_u32 v56, 37, v56
s_waitcnt vmcnt(0)
buffer_store_dword v56, v81, s[36:39], s52 offen sc1
LsO3:
s_cbranch_execz LsO7
s_endpgm
CO0_1:
s_cmp_eq_u32 s61, 0
s_cbranch_scc1 BO0_1
s_sub_u32 s61, s61, 1
s_sleep 8
buffer_load_dwordx2 v[60:61], v86, s[36:39], 0 offen sc1
s_waitcnt vmcnt(0)
ds_write_b32 v82, v60
v_sub_u32 v60, v60, v87
v_xor_b32 v61, v61, v57
v_cmp_lt_u64 vcc, s[62:63], v[60:61]
s_waitcnt lgkmcnt(0)
s_cbranch_vccnz CO0_1
s_branch BO0_1
CO0_2:
s_mov_b32 s58, m0
s_mov_b32 m0, 144384
DO0_2:
s_cmp_eq_u32 s61, 0
s_cbranch_scc1 EO0_2
s_sub_u32 s61, s61, 1
buffer_load_dword v86, s[36:39], 0 offen sc1 lds
s_sleep 1
ds_read_b32 v60, v82
s_waitcnt lgkmcnt(0)
v_cmp_gt_i32 vcc, v87, v60
s_cbranch_vccnz DO0_2
EO0_2:
s_mov_b32 m0, s58
s_branch BO0_2
CO0_3:
s_mov_b32 s58, m0
s_mov_b32 m0, 144384
DO0_3:
s_cmp_eq_u32 s61, 0
s_cbranch_scc1 EO0_3
s_sub_u32 s61, s61, 1
buffer_load_dword v86, s[36:39], 0 offen sc1 lds
s_sleep 1
ds_read_b32 v60, v82
s_waitcnt lgkmcnt(0)
v_cmp_gt_i32 vcc, v87, v60
s_cbranch_vccnz DO0_3
EO0_3:
s_mov_b32 m0, s58
s_branch BO0_3
CO0_5:
s_mov_b32 s58, m0
s_mov_b32 m0, 144384
DO0_5:
s_cmp_eq_u32 s61, 0
s_cbranch_scc1 EO0_5
s_sub_u32 s61, s61, 1
buffer_load_dword v86, s[36:39], 0 offen sc1 lds
s_sleep 1
ds_read_b32 v60, v82
s_waitcnt lgkmcnt(0)
v_cmp_gt_i32 vcc, v87, v60
s_cbranch_vccnz DO0_5
EO0_5:
s_mov_b32 m0, s58
s_branch BO0_5
CO0_6:
s_mov_b32 s58, m0
s_mov_b32 m0, 144384
DO0_6:
s_cmp_eq_u32 s61, 0
s_cbranch_scc1 EO0_6
s_sub_u32 s61, s61, 1
buffer_load_dword v86, s[36:39], 0 offen sc1 lds
s_sleep 1
ds_read_b32 v60, v82
s_waitcnt lgkmcnt(0)
v_cmp_gt_i32 vcc, v87, v60
s_cbranch_vccnz DO0_6
EO0_6:
s_mov_b32 m0, s58
s_branch BO0_6
CO0_7:
s_mov_b32 s58, m0
s_mov_b32 m0, 144384
DO0_7:
s_cmp_eq_u32 s61, 0
s_cbranch_scc1 EO0_7
s_sub_u32 s61, s61, 1
buffer_load_dword v86, s[36:39], 0 offen sc1 lds
s_sleep 1
ds_read_b32 v60, v82
s_waitcnt lgkmcnt(0)
v_cmp_gt_i32 vcc, v87, v60
s_cbranch_vccnz DO0_7
EO0_7:
s_mov_b32 m0, s58
s_branch BO0_7
)"
R"(
LvO1:
LsO4:
s_cbranch_execz LsO8
s_add_u32 s49, s49, 313344
s_barrier
buffer_load_dwordx4 v[48:51], v88, s[44:47], s54 offen sc1
s_add_u32 m0, s50, 0
s_nop 0
D 84,0
D 84,1024
D 84,2048
D 84,3072
s_add_u32 m0, s50, 4096
s_nop 0
D 85,0
D 85,1024
D 85,2048
D 85,3072
s_waitcnt vmcnt(8)
v_lshlrev_b32 v0, 16, v48
v_and_b32 v1, 0xffff0000, v48
v_lshlrev_b32 v2, 16, v49
v_and_b32 v3, 0xffff0000, v49
v_lshlrev_b32 v4, 16, v50
v_and_b32 v5, 0xffff0000, v50
v_lshlrev_b32 v6, 16, v51
v_and_b32 v7, 0xffff0000, v51
s_sub_u32 s54, s54, 0x8000
buffer_load_dwordx4 v[48:51], v88, s[44:47], s54 offen sc1
s_waitcnt vmcnt(1)
s_add_u32 s49, s49, 313344
s_barrier
s_add_u32 m0, s50, 32768
R 16,0
D 84,0
R 20,1024
R 24,2048
D 84,1024
R 28,3072
R 32,4096
D 84,2048
s_waitcnt lgkmcnt(3)
mav 0,0,16
R 36,5120
mav 4,4,16
mav 0,8,20
R 40,6144
mav 4,12,20
s_waitcnt lgkmcnt(3)
mav 0,16,24
R 44,7168
mav 4,20,24
mav 0,24,28
D 84,3072
mav 4,28,28
s_waitcnt lgkmcnt(2)
R 16,8192
s_add_u32 m0, s50, 36864
mav 0,32,32
R 20,9216
mav 4,36,32
mav 0,40,36
D 85,0
mav 4,44,36
s_waitcnt lgkmcnt(2)
R 24,10240
mav 0,48,40
R 28,11264
mav 4,52,40
mav 0,56,44
D 85,1024
mav 4,60,44
s_waitcnt lgkmcnt(2)
R 32,12288
mav 0,64,16
R 36,13312
mav 4,68,16
mav 0,72,20
D 85,2048
mav 4,76,20
s_waitcnt lgkmcnt(2)
R 40,14336
mav 0,80,24
R 44,15360
mav 4,84,24
mav 0,88,28
D 85,3072
mav 4,92,28
s_waitcnt lgkmcnt(2)
R 16,16384
mav 0,96,32
R 20,17408
mav 4,100,32
mav 0,104,36
R 24,18432
mav 4,108,36
s_waitcnt lgkmcnt(3)
mav 0,112,40
R 28,19456
mav 4,116,40
mav 0,120,44
s_waitcnt vmcnt(8)
R 32,20480
v_lshlrev_b32 v8, 16, v48
mav 4,124,44
s_waitcnt lgkmcnt(3)
v_and_b32 v9, 0xffff0000, v48
v_lshlrev_b32 v10, 16, v49
mav 0,128,16
R 36,21504
v_and_b32 v11, 0xffff0000, v49
mav 4,132,16
v_lshlrev_b32 v12, 16, v50
v_and_b32 v13, 0xffff0000, v50
mav 0,136,20
R 40,22528
v_lshlrev_b32 v14, 16, v51
mav 4,140,20
s_waitcnt lgkmcnt(3)
v_and_b32 v15, 0xffff0000, v51
mav 0,144,24
R 44,23552
mav 4,148,24
s_sub_u32 s54, s54, 0x8000
mav 0,152,28
R 16,24576
mav 4,156,28
s_waitcnt lgkmcnt(3)
s_cmp_lt_u32 s54, s56
s_cselect_b64 s[44:45], s[40:41], s[44:45]
mav 0,160,32
R 20,25600
mav 4,164,32
mav 0,168,36
buffer_load_dwordx4 v[48:51], v88, s[44:47], s54 offen sc1
mav 4,172,36
s_waitcnt lgkmcnt(2)
R 24,26624
mav 0,176,40
R 28,27648
mav 4,180,40
mav 0,184,44
R 32,28672
mav 4,188,44
s_waitcnt lgkmcnt(3)
mav 0,192,16
R 36,29696
mav 4,196,16
mav 0,200,20
R 40,30720
mav 4,204,20
s_waitcnt lgkmcnt(3)
mav 0,208,24
R 44,31744
mav 4,212,24
mav 0,216,28
mav 4,220,28
s_waitcnt lgkmcnt(2)
mav 0,224,32
mav 4,228,32
mav 0,232,36
mav 4,236,36
s_waitcnt lgkmcnt(0)
mav 0,240,40
mav 4,244,40
mav 0,248,44
mav 4,252,44
s_nop 6
v_cvt_pk_bf16_f32 v52, v0, v1
v_cvt_pk_bf16_f32 v53, v2, v3
v_cvt_pk_bf16_f32 v54, v4, v5
v_cvt_pk_bf16_f32 v55, v6, v7
buffer_store_dwordx4 v[52:55], v83, s[36:39], s49 offen sc1
v_mul_f32 v59, v0, v0
v_add_f32 v58, v1, v0
v_fmac_f32 v59, v1, v1
v_add_f32 v58, v2, v58
v_fmac_f32 v59, v2, v2
v_add_f32 v58, v3, v58
v_fmac_f32 v59, v3, v3
v_add_f32 v58, v4, v58
v_fmac_f32 v59, v4, v4
v_add_f32 v58, v5, v58
v_fmac_f32 v59, v5, v5
v_add_f32 v58, v6, v58
v_fmac_f32 v59, v6, v6
v_add_f32 v58, v7, v58
v_fmac_f32 v59, v7, v7
s_nop 1
v_permlane32_swap_b32 v58, v59
v_add_f32 v58, v58, v59
ds_write_b32 v89, v58 offset:0
s_xor_b32 s50, s50, 0x10000
s_waitcnt vmcnt(2) lgkmcnt(0)
s_add_u32 s49, s49, 313344
s_barrier
s_add_u32 m0, s50, 0
R 16,32768
ds_read_b128 v[62:65], v90 offset:0
D 84,0
R 20,33792
R 24,34816
D 84,1024
R 28,35840
s_waitcnt lgkmcnt(3)
v_add_f32 v62, v62, v63
v_add_f32 v64, v64, v65
v_add_f32 v62, v62, v64
s_nop 1
v_add_f32_dpp v63, v62, v62 quad_perm:[1,0,3,2] row_mask:0xf bank_mask:0xf
buffer_store_dword v63, v91, s[36:39], s49 offen sc1
R 32,36864
D 84,2048
mav 8,0,16
R 36,37888
mav 12,4,16
s_waitcnt lgkmcnt(3)
mav 8,8,20
R 40,38912
mav 12,12,20
mav 8,16,24
R 44,39936
mav 12,20,24
s_waitcnt lgkmcnt(3)
mav 8,24,28
D 84,3072
mav 12,28,28
s_add_u32 m0, s50, 4096
R 16,40960
mav 8,32,32
R 20,41984
mav 12,36,32
s_waitcnt lgkmcnt(3)
mav 8,40,36
D 85,0
mav 12,44,36
R 24,43008
mav 8,48,40
R 28,44032
v_add_u32 v56, 1, v56
mav 12,52,40
s_waitcnt lgkmcnt(3)
mav 8,56,44
D 85,1024
mav 12,60,44
R 32,45056
mav 8,64,16
R 36,46080
mav 12,68,16
s_waitcnt lgkmcnt(3)
mav 8,72,20
D 85,2048
mav 12,76,20
R 40,47104
mav 8,80,24
R 44,48128
mav 12,84,24
s_waitcnt lgkmcnt(3)
mav 8,88,28
D 85,3072
mav 12,92,28
R 16,49152
mav 8,96,32
R 20,50176
mav 12,100,32
s_waitcnt lgkmcnt(3)
mav 8,104,36
R 24,51200
mav 12,108,36
mav 8,112,40
R 28,52224
mav 12,116,40
s_waitcnt lgkmcnt(3)
mav 8,120,44
s_waitcnt vmcnt(10)
R 32,53248
v_lshlrev_b32 v0, 16, v48
mav 12,124,44
v_and_b32 v1, 0xffff0000, v48
v_lshlrev_b32 v2, 16, v49
mav 8,128,16
R 36,54272
v_and_b32 v3, 0xffff0000, v49
mav 12,132,16
s_waitcnt lgkmcnt(3)
v_lshlrev_b32 v4, 16, v50
v_and_b32 v5, 0xffff0000, v50
mav 8,136,20
R 40,55296
v_lshlrev_b32 v6, 16, v51
mav 12,140,20
v_and_b32 v7, 0xffff0000, v51
mav 8,144,24
s_waitcnt vmcnt(9)
R 44,56320
mav 12,148,24
s_waitcnt lgkmcnt(3)
mav 8,152,28
buffer_store_dword v56, v81, s[36:39], s52 offen sc1
mav 12,156,28
s_sub_u32 s54, s54, 0x8000
R 16,57344
mav 8,160,32
R 20,58368
mav 12,164,32
s_waitcnt lgkmcnt(3)
mav 8,168,36
buffer_load_dwordx4 v[48:51], v88, s[44:47], s54 offen sc1
mav 12,172,36
R 24,59392
mav 8,176,40
R 28,60416
mav 12,180,40
s_waitcnt lgkmcnt(3)
mav 8,184,44
R 32,61440
mav 12,188,44
mav 8,192,16
R 36,62464
mav 12,196,16
s_waitcnt lgkmcnt(3)
mav 8,200,20
R 40,63488
mav 12,204,20
mav 8,208,24
R 44,64512
mav 12,212,24
s_waitcnt lgkmcnt(3)
mav 8,216,28
mav 12,220,28
mav 8,224,32
mav 12,228,32
s_waitcnt lgkmcnt(1)
mav 8,232,36
s_mov_b32 s60, -30
.p2align 6
LO1_1:
mav 12,236,36
mav 8,240,40
mav 12,244,40
s_waitcnt lgkmcnt(0)
mav 8,248,44
mav 12,252,44
s_nop 6
v_cvt_pk_bf16_f32 v52, v8, v9
v_cvt_pk_bf16_f32 v53, v10, v11
v_cvt_pk_bf16_f32 v54, v12, v13
v_cvt_pk_bf16_f32 v55, v14, v15
buffer_store_dwordx4 v[52:55], v83, s[36:39], s49 offen sc1
v_mul_f32 v59, v8, v8
v_add_f32 v58, v9, v8
v_fmac_f32 v59, v9, v9
v_add_f32 v58, v10, v58
v_fmac_f32 v59, v10, v10
v_add_f32 v58, v11, v58
v_fmac_f32 v59, v11, v11
v_add_f32 v58, v12, v58
v_fmac_f32 v59, v12, v12
v_add_f32 v58, v13, v58
v_fmac_f32 v59, v13, v13
v_add_f32 v58, v14, v58
v_fmac_f32 v59, v14, v14
v_add_f32 v58, v15, v58
v_fmac_f32 v59, v15, v15
s_nop 1
v_permlane32_swap_b32 v58, v59
v_add_f32 v58, v58, v59
ds_write_b32 v89, v58 offset:1024
v_xor_b32 v80, 0x10000, v80
s_waitcnt vmcnt(3) lgkmcnt(0)
s_add_u32 s49, s49, 313344
s_barrier
s_add_u32 m0, s50, 32768
R 16,0
ds_read_b128 v[62:65], v90 offset:1024
D 84,0
R 20,1024
R 24,2048
D 84,1024
R 28,3072
s_waitcnt lgkmcnt(3)
v_add_f32 v62, v62, v63
v_add_f32 v64, v64, v65
v_add_f32 v62, v62, v64
s_nop 1
v_add_f32_dpp v63, v62, v62 quad_perm:[1,0,3,2] row_mask:0xf bank_mask:0xf
buffer_store_dword v63, v91, s[36:39], s49 offen sc1
R 32,4096
D 84,2048
mav 0,0,16
R 36,5120
mav 4,4,16
s_waitcnt lgkmcnt(3)
mav 0,8,20
R 40,6144
mav 4,12,20
mav 0,16,24
R 44,7168
mav 4,20,24
s_waitcnt lgkmcnt(3)
mav 0,24,28
D 84,3072
mav 4,28,28
s_add_u32 m0, s50, 36864
R 16,8192
mav 0,32,32
R 20,9216
mav 4,36,32
s_waitcnt lgkmcnt(3)
mav 0,40,36
D 85,0
mav 4,44,36
R 24,10240
mav 0,48,40
R 28,11264
v_add_u32 v56, 1, v56
mav 4,52,40
s_waitcnt lgkmcnt(3)
mav 0,56,44
D 85,1024
mav 4,60,44
R 32,12288
mav 0,64,16
R 36,13312
mav 4,68,16
s_waitcnt lgkmcnt(3)
mav 0,72,20
D 85,2048
mav 4,76,20
R 40,14336
mav 0,80,24
R 44,15360
mav 4,84,24
s_waitcnt lgkmcnt(3)
mav 0,88,28
D 85,3072
mav 4,92,28
R 16,16384
mav 0,96,32
R 20,17408
mav 4,100,32
s_waitcnt lgkmcnt(3)
mav 0,104,36
R 24,18432
mav 4,108,36
mav 0,112,40
R 28,19456
mav 4,116,40
s_waitcnt lgkmcnt(3)
mav 0,120,44
s_waitcnt vmcnt(10)
R 32,20480
v_lshlrev_b32 v8, 16, v48
mav 4,124,44
v_and_b32 v9, 0xffff0000, v48
v_lshlrev_b32 v10, 16, v49
mav 0,128,16
R 36,21504
v_and_b32 v11, 0xffff0000, v49
mav 4,132,16
s_waitcnt lgkmcnt(3)
v_lshlrev_b32 v12, 16, v50
v_and_b32 v13, 0xffff0000, v50
mav 0,136,20
R 40,22528
v_lshlrev_b32 v14, 16, v51
mav 4,140,20
v_and_b32 v15, 0xffff0000, v51
mav 0,144,24
s_waitcnt vmcnt(9)
R 44,23552
mav 4,148,24
s_waitcnt lgkmcnt(3)
s_sub_u32 s54, s54, 0x8000
mav 0,152,28
buffer_store_dword v56, v81, s[36:39], s52 offen sc1
mav 4,156,28
R 16,24576
mav 0,160,32
R 20,25600
mav 4,164,32
s_waitcnt lgkmcnt(3)
s_cmp_lt_u32 s54, s56
s_cselect_b64 s[44:45], s[40:41], s[44:45]
mav 0,168,36
buffer_load_dwordx4 v[48:51], v88, s[44:47], s54 offen sc1
mav 4,172,36
R 24,26624
mav 0,176,40
R 28,27648
mav 4,180,40
s_waitcnt lgkmcnt(3)
mav 0,184,44
R 32,28672
mav 4,188,44
mav 0,192,16
R 36,29696
mav 4,196,16
s_waitcnt lgkmcnt(3)
mav 0,200,20
R 40,30720
mav 4,204,20
mav 0,208,24
R 44,31744
mav 4,212,24
s_waitcnt lgkmcnt(3)
mav 0,216,28
mav 4,220,28
mav 0,224,32
mav 4,228,32
s_waitcnt lgkmcnt(1)
mav 0,232,36
mav 4,236,36
mav 0,240,40
mav 4,244,40
s_waitcnt lgkmcnt(0)
mav 0,248,44
mav 4,252,44
s_nop 6
v_cvt_pk_bf16_f32 v52, v0, v1
v_cvt_pk_bf16_f32 v53, v2, v3
v_cvt_pk_bf16_f32 v54, v4, v5
v_cvt_pk_bf16_f32 v55, v6, v7
buffer_store_dwordx4 v[52:55], v83, s[36:39], s49 offen sc1
v_mul_f32 v59, v0, v0
v_add_f32 v58, v1, v0
v_fmac_f32 v59, v1, v1
v_add_f32 v58, v2, v58
v_fmac_f32 v59, v2, v2
v_add_f32 v58, v3, v58
v_fmac_f32 v59, v3, v3
v_add_f32 v58, v4, v58
v_fmac_f32 v59, v4, v4
v_add_f32 v58, v5, v58
v_fmac_f32 v59, v5, v5
v_add_f32 v58, v6, v58
v_fmac_f32 v59, v6, v6
v_add_f32 v58, v7, v58
v_fmac_f32 v59, v7, v7
LsO5:
s_cbranch_execz LsO9
s_nop 1
v_permlane32_swap_b32 v58, v59
v_add_f32 v58, v58, v59
ds_write_b32 v89, v58 offset:0
s_xor_b32 s50, s50, 0x10000
s_waitcnt vmcnt(3) lgkmcnt(0)
s_add_u32 s49, s49, 313344
s_barrier
s_add_u32 m0, s50, 0
R 16,32768
ds_read_b128 v[62:65], v90 offset:0
D 84,0
R 20,33792
R 24,34816
D 84,1024
R 28,35840
s_waitcnt lgkmcnt(3)
v_add_f32 v62, v62, v63
v_add_f32 v64, v64, v65
v_add_f32 v62, v62, v64
s_nop 1
v_add_f32_dpp v63, v62, v62 quad_perm:[1,0,3,2] row_mask:0xf bank_mask:0xf
buffer_store_dword v63, v91, s[36:39], s49 offen sc1
R 32,36864
D 84,2048
mav 8,0,16
R 36,37888
mav 12,4,16
s_waitcnt lgkmcnt(3)
mav 8,8,20
R 40,38912
mav 12,12,20
mav 8,16,24
R 44,39936
mav 12,20,24
s_waitcnt lgkmcnt(3)
mav 8,24,28
D 84,3072
mav 12,28,28
s_add_u32 m0, s50, 4096
R 16,40960
mav 8,32,32
R 20,41984
mav 12,36,32
s_waitcnt lgkmcnt(3)
mav 8,40,36
D 85,0
mav 12,44,36
R 24,43008
mav 8,48,40
R 28,44032
v_add_u32 v56, 1, v56
mav 12,52,40
s_waitcnt lgkmcnt(3)
mav 8,56,44
D 85,1024
mav 12,60,44
R 32,45056
mav 8,64,16
R 36,46080
mav 12,68,16
s_waitcnt lgkmcnt(3)
mav 8,72,20
D 85,2048
mav 12,76,20
R 40,47104
mav 8,80,24
R 44,48128
mav 12,84,24
s_waitcnt lgkmcnt(3)
mav 8,88,28
D 85,3072
mav 12,92,28
R 16,49152
mav 8,96,32
R 20,50176
mav 12,100,32
s_waitcnt lgkmcnt(3)
mav 8,104,36
R 24,51200
mav 12,108,36
mav 8,112,40
R 28,52224
mav 12,116,40
s_waitcnt lgkmcnt(3)
mav 8,120,44
s_waitcnt vmcnt(10)
R 32,53248
v_lshlrev_b32 v0, 16, v48
mav 12,124,44
v_and_b32 v1, 0xffff0000, v48
v_lshlrev_b32 v2, 16, v49
mav 8,128,16
R 36,54272
v_and_b32 v3, 0xffff0000, v49
mav 12,132,16
s_waitcnt lgkmcnt(3)
v_lshlrev_b32 v4, 16, v50
v_and_b32 v5, 0xffff0000, v50
mav 8,136,20
R 40,55296
v_lshlrev_b32 v6, 16, v51
mav 12,140,20
v_and_b32 v7, 0xffff0000, v51
mav 8,144,24
s_waitcnt vmcnt(9)
R 44,56320
mav 12,148,24
s_waitcnt lgkmcnt(3)
mav 8,152,28
buffer_store_dword v56, v81, s[36:39], s52 offen sc1
mav 12,156,28
s_sub_u32 s54, s54, 0x8000
R 16,57344
mav 8,160,32
R 20,58368
mav 12,164,32
s_waitcnt lgkmcnt(3)
mav 8,168,36
buffer_load_dwordx4 v[48:51], v88, s[44:47], s54 offen sc1
mav 12,172,36
R 24,59392
mav 8,176,40
R 28,60416
mav 12,180,40
s_waitcnt lgkmcnt(3)
mav 8,184,44
R 32,61440
mav 12,188,44
mav 8,192,16
R 36,62464
mav 12,196,16
s_waitcnt lgkmcnt(3)
mav 8,200,20
R 40,63488
mav 12,204,20
mav 8,208,24
R 44,64512
mav 12,212,24
s_waitcnt lgkmcnt(3)
mav 8,216,28
mav 12,220,28
mav 8,224,32
mav 12,228,32
s_waitcnt lgkmcnt(1)
s_add_u32 s60, s60, 1
mav 8,232,36
s_cbranch_scc0 LO1_1
mav 12,236,36
mav 8,240,40
mav 12,244,40
s_waitcnt lgkmcnt(0)
mav 8,248,44
mav 12,252,44
s_nop 6
v_cvt_pk_bf16_f32 v52, v8, v9
v_cvt_pk_bf16_f32 v53, v10, v11
v_cvt_pk_bf16_f32 v54, v12, v13
v_cvt_pk_bf16_f32 v55, v14, v15
buffer_store_dwordx4 v[52:55], v83, s[36:39], s49 offen sc1
v_mul_f32 v59, v8, v8
v_add_f32 v58, v9, v8
v_fmac_f32 v59, v9, v9
v_add_f32 v58, v10, v58
v_fmac_f32 v59, v10, v10
v_add_f32 v58, v11, v58
v_fmac_f32 v59, v11, v11
v_add_f32 v58, v12, v58
v_fmac_f32 v59, v12, v12
v_add_f32 v58, v13, v58
v_fmac_f32 v59, v13, v13
v_add_f32 v58, v14, v58
v_fmac_f32 v59, v14, v14
v_add_f32 v58, v15, v58
v_fmac_f32 v59, v15, v15
s_nop 1
v_permlane32_swap_b32 v58, v59
v_add_f32 v58, v58, v59
ds_write_b32 v89, v58 offset:1024
v_xor_b32 v80, 0x10000, v80
s_waitcnt vmcnt(3) lgkmcnt(0)
s_add_u32 s49, s49, 313344
s_barrier
s_add_u32 m0, s50, 32768
R 16,0
ds_read_b128 v[62:65], v90 offset:1024
D 84,0
R 20,1024
R 24,2048
D 84,1024
R 28,3072
s_waitcnt lgkmcnt(3)
v_add_f32 v62, v62, v63
v_add_f32 v64, v64, v65
v_add_f32 v62, v62, v64
s_nop 1
v_add_f32_dpp v63, v62, v62 quad_perm:[1,0,3,2] row_mask:0xf bank_mask:0xf
buffer_store_dword v63, v91, s[36:39], s49 offen sc1
R 32,4096
D 84,2048
mav 0,0,16
R 36,5120
mav 4,4,16
s_waitcnt lgkmcnt(3)
mav 0,8,20
R 40,6144
mav 4,12,20
mav 0,16,24
R 44,7168
mav 4,20,24
s_waitcnt lgkmcnt(3)
mav 0,24,28
D 84,3072
mav 4,28,28
s_add_u32 m0, s50, 36864
R 16,8192
mav 0,32,32
R 20,9216
mav 4,36,32
s_waitcnt lgkmcnt(3)
mav 0,40,36
D 85,0
mav 4,44,36
R 24,10240
mav 0,48,40
R 28,11264
v_add_u32 v56, 1, v56
mav 4,52,40
s_waitcnt lgkmcnt(3)
mav 0,56,44
D 85,1024
mav 4,60,44
R 32,12288
mav 0,64,16
R 36,13312
mav 4,68,16
s_waitcnt lgkmcnt(3)
mav 0,72,20
D 85,2048
mav 4,76,20
R 40,14336
mav 0,80,24
R 44,15360
mav 4,84,24
s_waitcnt lgkmcnt(3)
mav 0,88,28
D 85,3072
mav 4,92,28
R 16,16384
mav 0,96,32
R 20,17408
mav 4,100,32
s_waitcnt lgkmcnt(3)
mav 0,104,36
R 24,18432
mav 4,108,36
mav 0,112,40
R 28,19456
mav 4,116,40
s_waitcnt lgkmcnt(3)
mav 0,120,44
s_waitcnt vmcnt(10)
R 32,20480
v_lshlrev_b32 v8, 16, v48
mav 4,124,44
v_and_b32 v9, 0xffff0000, v48
v_lshlrev_b32 v10, 16, v49
mav 0,128,16
R 36,21504
v_and_b32 v11, 0xffff0000, v49
mav 4,132,16
s_waitcnt lgkmcnt(3)
v_lshlrev_b32 v12, 16, v50
v_and_b32 v13, 0xffff0000, v50
mav 0,136,20
R 40,22528
v_lshlrev_b32 v14, 16, v51
mav 4,140,20
v_and_b32 v15, 0xffff0000, v51
mav 0,144,24
s_waitcnt vmcnt(9)
R 44,23552
mav 4,148,24
s_waitcnt lgkmcnt(3)
mav 0,152,28
buffer_store_dword v56, v81, s[36:39], s52 offen sc1
mav 4,156,28
R 16,24576
mav 0,160,32
R 20,25600
mav 4,164,32
s_waitcnt lgkmcnt(3)
mav 0,168,36
R 24,26624
mav 4,172,36
mav 0,176,40
R 28,27648
mav 4,180,40
s_waitcnt lgkmcnt(3)
mav 0,184,44
R 32,28672
mav 4,188,44
mav 0,192,16
R 36,29696
mav 4,196,16
s_waitcnt lgkmcnt(3)
mav 0,200,20
R 40,30720
mav 4,204,20
mav 0,208,24
R 44,31744
mav 4,212,24
s_waitcnt lgkmcnt(3)
mav 0,216,28
mav 4,220,28
mav 0,224,32
mav 4,228,32
s_waitcnt lgkmcnt(1)
mav 0,232,36
mav 4,236,36
mav 0,240,40
mav 4,244,40
s_waitcnt lgkmcnt(0)
mav 0,248,44
mav 4,252,44
s_nop 6
v_cvt_pk_bf16_f32 v52, v0, v1
v_cvt_pk_bf16_f32 v53, v2, v3
v_cvt_pk_bf16_f32 v54, v4, v5
v_cvt_pk_bf16_f32 v55, v6, v7
buffer_store_dwordx4 v[52:55], v83, s[36:39], s49 offen sc1
v_mul_f32 v59, v0, v0
v_add_f32 v58, v1, v0
v_fmac_f32 v59, v1, v1
v_add_f32 v58, v2, v58
v_fmac_f32 v59, v2, v2
v_add_f32 v58, v3, v58
v_fmac_f32 v59, v3, v3
v_add_f32 v58, v4, v58
v_fmac_f32 v59, v4, v4
v_add_f32 v58, v5, v58
v_fmac_f32 v59, v5, v5
v_add_f32 v58, v6, v58
v_fmac_f32 v59, v6, v6
v_add_f32 v58, v7, v58
v_fmac_f32 v59, v7, v7
s_nop 1
v_permlane32_swap_b32 v58, v59
v_add_f32 v58, v58, v59
ds_write_b32 v89, v58 offset:0
s_waitcnt vmcnt(2) lgkmcnt(0)
s_add_u32 s49, s49, 313344
s_barrier
R 16,32768
ds_read_b128 v[62:65], v90 offset:0
R 20,33792
R 24,34816
R 28,35840
s_waitcnt lgkmcnt(3)
v_add_f32 v62, v62, v63
v_add_f32 v64, v64, v65
v_add_f32 v62, v62, v64
s_nop 1
v_add_f32_dpp v63, v62, v62 quad_perm:[1,0,3,2] row_mask:0xf bank_mask:0xf
buffer_store_dword v63, v91, s[36:39], s49 offen sc1
R 32,36864
mav 8,0,16
R 36,37888
mav 12,4,16
s_waitcnt lgkmcnt(3)
mav 8,8,20
R 40,38912
mav 12,12,20
mav 8,16,24
R 44,39936
mav 12,20,24
s_waitcnt lgkmcnt(3)
mav 8,24,28
R 16,40960
mav 12,28,28
mav 8,32,32
R 20,41984
mav 12,36,32
s_waitcnt lgkmcnt(3)
mav 8,40,36
R 24,43008
mav 12,44,36
mav 8,48,40
R 28,44032
v_add_u32 v56, 1, v56
mav 12,52,40
s_waitcnt lgkmcnt(3)
mav 8,56,44
R 32,45056
mav 12,60,44
mav 8,64,16
R 36,46080
mav 12,68,16
s_waitcnt lgkmcnt(3)
mav 8,72,20
R 40,47104
mav 12,76,20
mav 8,80,24
R 44,48128
mav 12,84,24
s_waitcnt lgkmcnt(3)
mav 8,88,28
R 16,49152
mav 12,92,28
mav 8,96,32
R 20,50176
mav 12,100,32
s_waitcnt lgkmcnt(3)
mav 8,104,36
R 24,51200
mav 12,108,36
mav 8,112,40
R 28,52224
mav 12,116,40
s_waitcnt lgkmcnt(3)
mav 8,120,44
R 32,53248
mav 12,124,44
mav 8,128,16
R 36,54272
mav 12,132,16
s_waitcnt lgkmcnt(3)
mav 8,136,20
R 40,55296
mav 12,140,20
mav 8,144,24
s_waitcnt vmcnt(1)
R 44,56320
mav 12,148,24
s_waitcnt lgkmcnt(3)
mav 8,152,28
buffer_store_dword v56, v81, s[36:39], s52 offen sc1
mav 12,156,28
R 16,57344
mav 8,160,32
R 20,58368
mav 12,164,32
s_waitcnt lgkmcnt(3)
mav 8,168,36
R 24,59392
mav 12,172,36
mav 8,176,40
R 28,60416
mav 12,180,40
s_waitcnt lgkmcnt(3)
mav 8,184,44
R 32,61440
mav 12,188,44
mav 8,192,16
R 36,62464
mav 12,196,16
s_waitcnt lgkmcnt(3)
mav 8,200,20
R 40,63488
mav 12,204,20
mav 8,208,24
R 44,64512
mav 12,212,24
s_waitcnt lgkmcnt(3)
mav 8,216,28
mav 12,220,28
mav 8,224,32
mav 12,228,32
s_waitcnt lgkmcnt(1)
mav 8,232,36
mav 12,236,36
mav 8,240,40
mav 12,244,40
s_waitcnt lgkmcnt(0)
mav 8,248,44
mav 12,252,44
s_nop 6
v_cvt_pk_bf16_f32 v52, v8, v9
v_cvt_pk_bf16_f32 v53, v10, v11
v_cvt_pk_bf16_f32 v54, v12, v13
v_cvt_pk_bf16_f32 v55, v14, v15
buffer_store_dwordx4 v[52:55], v83, s[36:39], s49 offen sc1
v_mul_f32 v59, v8, v8
v_add_f32 v58, v9, v8
v_fmac_f32 v59, v9, v9
v_add_f32 v58, v10, v58
v_fmac_f32 v59, v10, v10
v_add_f32 v58, v11, v58
v_fmac_f32 v59, v11, v11
v_add_f32 v58, v12, v58
v_fmac_f32 v59, v12, v12
v_add_f32 v58, v13, v58
v_fmac_f32 v59, v13, v13
v_add_f32 v58, v14, v58
v_fmac_f32 v59, v14, v14
v_add_f32 v58, v15, v58
v_fmac_f32 v59, v15, v15
s_nop 1
v_permlane32_swap_b32 v58, v59
v_add_f32 v58, v58, v59
ds_write_b32 v89, v58 offset:1024
s_waitcnt lgkmcnt(0)
s_add_u32 s49, s49, 313344
s_barrier
ds_read_b128 v[62:65], v90 offset:1024
s_waitcnt lgkmcnt(0)
v_add_f32 v62, v62, v63
v_add_f32 v64, v64, v65
v_add_f32 v62, v62, v64
s_nop 1
v_add_f32_dpp v63, v62, v62 quad_perm:[1,0,3,2] row_mask:0xf bank_mask:0xf
buffer_store_dword v63, v91, s[36:39], s49 offen sc1
v_add_u32 v56, 37, v56
s_waitcnt vmcnt(0)
buffer_store_dword v56, v81, s[36:39], s52 offen sc1
s_endpgm
LvO2:
LsO6:
s_cbranch_execz LsO10
s_add_u32 s49, s49, 313344
s_barrier
buffer_load_dwordx4 v[48:51], v88, s[44:47], s54 offen sc1
s_add_u32 m0, s50, 0
s_nop 0
D 84,0
D 84,1024
D 84,2048
D 84,3072
s_add_u32 m0, s50, 4096
s_nop 0
D 85,0
D 85,1024
D 85,2048
D 85,3072
s_waitcnt vmcnt(8)
v_lshlrev_b32 v0, 16, v48
v_and_b32 v1, 0xffff0000, v48
v_lshlrev_b32 v2, 16, v49
v_and_b32 v3, 0xffff0000, v49
v_lshlrev_b32 v4, 16, v50
v_and_b32 v5, 0xffff0000, v50
v_lshlrev_b32 v6, 16, v51
v_and_b32 v7, 0xffff0000, v51
s_sub_u32 s54, s54, 0x8000
buffer_load_dwordx4 v[48:51], v88, s[44:47], s54 offen sc1
s_waitcnt vmcnt(1)
s_add_u32 s49, s49, 313344
s_barrier
s_add_u32 m0, s50, 32768
R 16,0
D 84,0
R 20,1024
R 24,2048
D 84,1024
R 28,3072
R 32,4096
D 84,2048
s_waitcnt lgkmcnt(3)
mav 0,0,16
R 36,5120
mav 4,4,16
mav 0,8,20
R 40,6144
mav 4,12,20
s_waitcnt lgkmcnt(3)
mav 0,16,24
D 84,3072
mav 4,20,24
s_add_u32 m0, s50, 36864
R 44,7168
mav 0,24,28
R 16,8192
mav 4,28,28
s_waitcnt lgkmcnt(3)
mav 0,32,32
D 85,0
mav 4,36,32
R 20,9216
mav 0,40,36
R 24,10240
mav 4,44,36
s_waitcnt lgkmcnt(3)
mav 0,48,40
D 85,1024
mav 4,52,40
R 28,11264
mav 0,56,44
R 32,12288
mav 4,60,44
s_waitcnt lgkmcnt(3)
mav 0,64,16
D 85,2048
mav 4,68,16
R 36,13312
mav 0,72,20
R 40,14336
mav 4,76,20
s_waitcnt lgkmcnt(3)
mav 0,80,24
D 85,3072
mav 4,84,24
R 44,15360
LsO7:
s_cbranch_execz LsO11
mav 0,88,28
R 16,16384
mav 4,92,28
s_waitcnt lgkmcnt(3)
mav 0,96,32
R 20,17408
mav 4,100,32
mav 0,104,36
R 24,18432
mav 4,108,36
s_waitcnt lgkmcnt(3)
mav 0,112,40
R 28,19456
mav 4,116,40
mav 0,120,44
s_waitcnt vmcnt(8)
R 32,20480
v_lshlrev_b32 v8, 16, v48
mav 4,124,44
s_waitcnt lgkmcnt(3)
v_and_b32 v9, 0xffff0000, v48
v_lshlrev_b32 v10, 16, v49
mav 0,128,16
R 36,21504
v_and_b32 v11, 0xffff0000, v49
mav 4,132,16
v_lshlrev_b32 v12, 16, v50
v_and_b32 v13, 0xffff0000, v50
mav 0,136,20
R 40,22528
v_lshlrev_b32 v14, 16, v51
mav 4,140,20
s_waitcnt lgkmcnt(3)
v_and_b32 v15, 0xffff0000, v51
mav 0,144,24
R 44,23552
mav 4,148,24
s_sub_u32 s54, s54, 0x8000
mav 0,152,28
R 16,24576
mav 4,156,28
s_waitcnt lgkmcnt(3)
s_cmp_lt_u32 s54, s56
s_cselect_b64 s[44:45], s[40:41], s[44:45]
mav 0,160,32
buffer_load_dwordx4 v[48:51], v88, s[44:47], s54 offen sc1
mav 4,164,32
R 20,25600
mav 0,168,36
R 24,26624
mav 4,172,36
s_waitcnt lgkmcnt(3)
mav 0,176,40
R 28,27648
mav 4,180,40
mav 0,184,44
R 32,28672
mav 4,188,44
s_waitcnt lgkmcnt(3)
mav 0,192,16
R 36,29696
mav 4,196,16
mav 0,200,20
R 40,30720
mav 4,204,20
s_waitcnt lgkmcnt(3)
mav 0,208,24
R 44,31744
mav 4,212,24
mav 0,216,28
mav 4,220,28
s_waitcnt lgkmcnt(2)
mav 0,224,32
mav 4,228,32
mav 0,232,36
mav 4,236,36
s_waitcnt lgkmcnt(0)
mav 0,240,40
mav 4,244,40
mav 0,248,44
mav 4,252,44
s_nop 6
v_cvt_pk_bf16_f32 v52, v0, v1
v_cvt_pk_bf16_f32 v53, v2, v3
v_cvt_pk_bf16_f32 v54, v4, v5
v_cvt_pk_bf16_f32 v55, v6, v7
buffer_store_dwordx4 v[52:55], v83, s[36:39], s49 offen sc1
v_mul_f32 v59, v0, v0
v_add_f32 v58, v1, v0
v_fmac_f32 v59, v1, v1
v_add_f32 v58, v2, v58
v_fmac_f32 v59, v2, v2
v_add_f32 v58, v3, v58
v_fmac_f32 v59, v3, v3
v_add_f32 v58, v4, v58
v_fmac_f32 v59, v4, v4
v_add_f32 v58, v5, v58
v_fmac_f32 v59, v5, v5
v_add_f32 v58, v6, v58
v_fmac_f32 v59, v6, v6
v_add_f32 v58, v7, v58
v_fmac_f32 v59, v7, v7
s_nop 1
v_permlane32_swap_b32 v58, v59
v_add_f32 v58, v58, v59
ds_write_b32 v89, v58 offset:0
s_xor_b32 s50, s50, 0x10000
s_waitcnt vmcnt(2) lgkmcnt(0)
s_add_u32 s49, s49, 313344
s_barrier
s_add_u32 m0, s50, 0
R 16,32768
D 84,0
R 20,33792
R 24,34816
D 84,1024
R 28,35840
R 32,36864
D 84,2048
s_waitcnt lgkmcnt(3)
mav 8,0,16
R 36,37888
mav 12,4,16
mav 8,8,20
R 40,38912
mav 12,12,20
s_waitcnt lgkmcnt(3)
mav 8,16,24
D 84,3072
mav 12,20,24
s_add_u32 m0, s50, 4096
R 44,39936
mav 8,24,28
R 16,40960
mav 12,28,28
s_waitcnt lgkmcnt(3)
mav 8,32,32
D 85,0
mav 12,36,32
R 20,41984
mav 8,40,36
R 24,43008
mav 12,44,36
s_waitcnt lgkmcnt(3)
mav 8,48,40
D 85,1024
mav 12,52,40
R 28,44032
v_add_u32 v56, 1, v56
mav 8,56,44
R 32,45056
mav 12,60,44
s_waitcnt lgkmcnt(3)
mav 8,64,16
D 85,2048
mav 12,68,16
R 36,46080
mav 8,72,20
R 40,47104
mav 12,76,20
s_waitcnt lgkmcnt(3)
mav 8,80,24
D 85,3072
mav 12,84,24
R 44,48128
mav 8,88,28
R 16,49152
mav 12,92,28
s_waitcnt lgkmcnt(3)
mav 8,96,32
R 20,50176
mav 12,100,32
mav 8,104,36
R 24,51200
mav 12,108,36
s_waitcnt lgkmcnt(3)
mav 8,112,40
R 28,52224
mav 12,116,40
mav 8,120,44
s_waitcnt vmcnt(9)
R 32,53248
v_lshlrev_b32 v0, 16, v48
mav 12,124,44
s_waitcnt lgkmcnt(3)
v_and_b32 v1, 0xffff0000, v48
v_lshlrev_b32 v2, 16, v49
mav 8,128,16
R 36,54272
v_and_b32 v3, 0xffff0000, v49
mav 12,132,16
v_lshlrev_b32 v4, 16, v50
v_and_b32 v5, 0xffff0000, v50
mav 8,136,20
R 40,55296
v_lshlrev_b32 v6, 16, v51
mav 12,140,20
s_waitcnt lgkmcnt(3)
v_and_b32 v7, 0xffff0000, v51
mav 8,144,24
s_waitcnt vmcnt(8)
buffer_store_dword v56, v81, s[36:39], s52 offen sc1
mav 12,148,24
R 44,56320
mav 8,152,28
s_sub_u32 s54, s54, 0x8000
R 16,57344
mav 12,156,28
s_waitcnt lgkmcnt(3)
mav 8,160,32
buffer_load_dwordx4 v[48:51], v88, s[44:47], s54 offen sc1
mav 12,164,32
R 20,58368
mav 8,168,36
R 24,59392
mav 12,172,36
s_waitcnt lgkmcnt(3)
mav 8,176,40
R 28,60416
mav 12,180,40
mav 8,184,44
R 32,61440
mav 12,188,44
s_waitcnt lgkmcnt(3)
mav 8,192,16
R 36,62464
mav 12,196,16
mav 8,200,20
R 40,63488
mav 12,204,20
s_waitcnt lgkmcnt(3)
mav 8,208,24
R 44,64512
mav 12,212,24
mav 8,216,28
mav 12,220,28
s_waitcnt lgkmcnt(2)
mav 8,224,32
mav 12,228,32
mav 8,232,36
s_mov_b32 s60, -30
.p2align 6
LO2_1:
mav 12,236,36
s_waitcnt lgkmcnt(0)
mav 8,240,40
mav 12,244,40
mav 8,248,44
mav 12,252,44
s_nop 6
v_cvt_pk_bf16_f32 v52, v8, v9
v_cvt_pk_bf16_f32 v53, v10, v11
v_cvt_pk_bf16_f32 v54, v12, v13
v_cvt_pk_bf16_f32 v55, v14, v15
buffer_store_dwordx4 v[52:55], v83, s[36:39], s49 offen sc1
v_mul_f32 v59, v8, v8
v_add_f32 v58, v9, v8
v_fmac_f32 v59, v9, v9
v_add_f32 v58, v10, v58
v_fmac_f32 v59, v10, v10
v_add_f32 v58, v11, v58
v_fmac_f32 v59, v11, v11
v_add_f32 v58, v12, v58
v_fmac_f32 v59, v12, v12
v_add_f32 v58, v13, v58
v_fmac_f32 v59, v13, v13
v_add_f32 v58, v14, v58
v_fmac_f32 v59, v14, v14
v_add_f32 v58, v15, v58
v_fmac_f32 v59, v15, v15
s_nop 1
v_permlane32_swap_b32 v58, v59
v_add_f32 v58, v58, v59
ds_write_b32 v89, v58 offset:1024
v_xor_b32 v80, 0x10000, v80
s_waitcnt vmcnt(3) lgkmcnt(0)
s_add_u32 s49, s49, 313344
s_barrier
s_add_u32 m0, s50, 32768
R 16,0
D 84,0
R 20,1024
R 24,2048
D 84,1024
R 28,3072
R 32,4096
D 84,2048
s_waitcnt lgkmcnt(3)
mav 0,0,16
R 36,5120
mav 4,4,16
mav 0,8,20
R 40,6144
mav 4,12,20
s_waitcnt lgkmcnt(3)
mav 0,16,24
D 84,3072
mav 4,20,24
s_add_u32 m0, s50, 36864
R 44,7168
mav 0,24,28
R 16,8192
mav 4,28,28
s_waitcnt lgkmcnt(3)
mav 0,32,32
D 85,0
mav 4,36,32
R 20,9216
mav 0,40,36
R 24,10240
mav 4,44,36
s_waitcnt lgkmcnt(3)
mav 0,48,40
D 85,1024
mav 4,52,40
R 28,11264
v_add_u32 v56, 1, v56
mav 0,56,44
R 32,12288
mav 4,60,44
s_waitcnt lgkmcnt(3)
mav 0,64,16
D 85,2048
mav 4,68,16
R 36,13312
mav 0,72,20
R 40,14336
mav 4,76,20
s_waitcnt lgkmcnt(3)
mav 0,80,24
D 85,3072
mav 4,84,24
R 44,15360
mav 0,88,28
R 16,16384
mav 4,92,28
s_waitcnt lgkmcnt(3)
mav 0,96,32
R 20,17408
mav 4,100,32
mav 0,104,36
R 24,18432
mav 4,108,36
s_waitcnt lgkmcnt(3)
mav 0,112,40
R 28,19456
mav 4,116,40
mav 0,120,44
s_waitcnt vmcnt(9)
R 32,20480
v_lshlrev_b32 v8, 16, v48
mav 4,124,44
s_waitcnt lgkmcnt(3)
v_and_b32 v9, 0xffff0000, v48
v_lshlrev_b32 v10, 16, v49
mav 0,128,16
R 36,21504
v_and_b32 v11, 0xffff0000, v49
mav 4,132,16
v_lshlrev_b32 v12, 16, v50
v_and_b32 v13, 0xffff0000, v50
mav 0,136,20
R 40,22528
v_lshlrev_b32 v14, 16, v51
mav 4,140,20
s_waitcnt lgkmcnt(3)
v_and_b32 v15, 0xffff0000, v51
mav 0,144,24
s_waitcnt vmcnt(8)
buffer_store_dword v56, v81, s[36:39], s52 offen sc1
mav 4,148,24
s_sub_u32 s54, s54, 0x8000
R 44,23552
mav 0,152,28
R 16,24576
mav 4,156,28
s_waitcnt lgkmcnt(3)
s_cmp_lt_u32 s54, s56
s_cselect_b64 s[44:45], s[40:41], s[44:45]
mav 0,160,32
buffer_load_dwordx4 v[48:51], v88, s[44:47], s54 offen sc1
mav 4,164,32
R 20,25600
mav 0,168,36
R 24,26624
mav 4,172,36
s_waitcnt lgkmcnt(3)
mav 0,176,40
R 28,27648
mav 4,180,40
mav 0,184,44
R 32,28672
mav 4,188,44
s_waitcnt lgkmcnt(3)
mav 0,192,16
R 36,29696
mav 4,196,16
mav 0,200,20
R 40,30720
mav 4,204,20
s_waitcnt lgkmcnt(3)
mav 0,208,24
R 44,31744
mav 4,212,24
mav 0,216,28
mav 4,220,28
s_waitcnt lgkmcnt(2)
mav 0,224,32
mav 4,228,32
mav 0,232,36
mav 4,236,36
s_waitcnt lgkmcnt(0)
mav 0,240,40
mav 4,244,40
mav 0,248,44
mav 4,252,44
s_nop 6
v_cvt_pk_bf16_f32 v52, v0, v1
v_cvt_pk_bf16_f32 v53, v2, v3
v_cvt_pk_bf16_f32 v54, v4, v5
v_cvt_pk_bf16_f32 v55, v6, v7
buffer_store_dwordx4 v[52:55], v83, s[36:39], s49 offen sc1
v_mul_f32 v59, v0, v0
v_add_f32 v58, v1, v0
v_fmac_f32 v59, v1, v1
v_add_f32 v58, v2, v58
v_fmac_f32 v59, v2, v2
v_add_f32 v58, v3, v58
v_fmac_f32 v59, v3, v3
v_add_f32 v58, v4, v58
v_fmac_f32 v59, v4, v4
v_add_f32 v58, v5, v58
v_fmac_f32 v59, v5, v5
v_add_f32 v58, v6, v58
v_fmac_f32 v59, v6, v6
v_add_f32 v58, v7, v58
v_fmac_f32 v59, v7, v7
s_nop 1
v_permlane32_swap_b32 v58, v59
v_add_f32 v58, v58, v59
ds_write_b32 v89, v58 offset:0
s_xor_b32 s50, s50, 0x10000
s_waitcnt vmcnt(3) lgkmcnt(0)
s_add_u32 s49, s49, 313344
s_barrier
s_add_u32 m0, s50, 0
R 16,32768
D 84,0
R 20,33792
R 24,34816
D 84,1024
R 28,35840
R 32,36864
D 84,2048
s_waitcnt lgkmcnt(3)
mav 8,0,16
R 36,37888
mav 12,4,16
mav 8,8,20
R 40,38912
mav 12,12,20
s_waitcnt lgkmcnt(3)
mav 8,16,24
D 84,3072
mav 12,20,24
s_add_u32 m0, s50, 4096
R 44,39936
mav 8,24,28
R 16,40960
mav 12,28,28
s_waitcnt lgkmcnt(3)
mav 8,32,32
D 85,0
mav 12,36,32
R 20,41984
mav 8,40,36
R 24,43008
mav 12,44,36
s_waitcnt lgkmcnt(3)
mav 8,48,40
D 85,1024
mav 12,52,40
R 28,44032
v_add_u32 v56, 1, v56
mav 8,56,44
R 32,45056
mav 12,60,44
s_waitcnt lgkmcnt(3)
mav 8,64,16
D 85,2048
mav 12,68,16
R 36,46080
mav 8,72,20
R 40,47104
mav 12,76,20
s_waitcnt lgkmcnt(3)
mav 8,80,24
D 85,3072
mav 12,84,24
R 44,48128
mav 8,88,28
R 16,49152
mav 12,92,28
s_waitcnt lgkmcnt(3)
mav 8,96,32
R 20,50176
mav 12,100,32
mav 8,104,36
R 24,51200
mav 12,108,36
s_waitcnt lgkmcnt(3)
mav 8,112,40
R 28,52224
mav 12,116,40
mav 8,120,44
s_waitcnt vmcnt(9)
R 32,53248
v_lshlrev_b32 v0, 16, v48
mav 12,124,44
s_waitcnt lgkmcnt(3)
v_and_b32 v1, 0xffff0000, v48
v_lshlrev_b32 v2, 16, v49
mav 8,128,16
R 36,54272
v_and_b32 v3, 0xffff0000, v49
mav 12,132,16
v_lshlrev_b32 v4, 16, v50
v_and_b32 v5, 0xffff0000, v50
mav 8,136,20
R 40,55296
v_lshlrev_b32 v6, 16, v51
mav 12,140,20
s_waitcnt lgkmcnt(3)
v_and_b32 v7, 0xffff0000, v51
mav 8,144,24
s_waitcnt vmcnt(8)
buffer_store_dword v56, v81, s[36:39], s52 offen sc1
mav 12,148,24
R 44,56320
mav 8,152,28
s_sub_u32 s54, s54, 0x8000
R 16,57344
mav 12,156,28
s_waitcnt lgkmcnt(3)
mav 8,160,32
buffer_load_dwordx4 v[48:51], v88, s[44:47], s54 offen sc1
mav 12,164,32
R 20,58368
mav 8,168,36
R 24,59392
mav 12,172,36
s_waitcnt lgkmcnt(3)
mav 8,176,40
R 28,60416
mav 12,180,40
mav 8,184,44
R 32,61440
mav 12,188,44
s_waitcnt lgkmcnt(3)
mav 8,192,16
R 36,62464
mav 12,196,16
mav 8,200,20
R 40,63488
mav 12,204,20
s_waitcnt lgkmcnt(3)
mav 8,208,24
R 44,64512
mav 12,212,24
mav 8,216,28
mav 12,220,28
s_waitcnt lgkmcnt(2)
mav 8,224,32
mav 12,228,32
s_add_u32 s60, s60, 1
mav 8,232,36
s_cbranch_scc0 LO2_1
mav 12,236,36
s_waitcnt lgkmcnt(0)
mav 8,240,40
mav 12,244,40
mav 8,248,44
mav 12,252,44
s_nop 6
v_cvt_pk_bf16_f32 v52, v8, v9
v_cvt_pk_bf16_f32 v53, v10, v11
v_cvt_pk_bf16_f32 v54, v12, v13
LsO8:
s_cbranch_execz LwO
v_cvt_pk_bf16_f32 v55, v14, v15
buffer_store_dwordx4 v[52:55], v83, s[36:39], s49 offen sc1
v_mul_f32 v59, v8, v8
v_add_f32 v58, v9, v8
v_fmac_f32 v59, v9, v9
v_add_f32 v58, v10, v58
v_fmac_f32 v59, v10, v10
v_add_f32 v58, v11, v58
v_fmac_f32 v59, v11, v11
v_add_f32 v58, v12, v58
v_fmac_f32 v59, v12, v12
v_add_f32 v58, v13, v58
v_fmac_f32 v59, v13, v13
v_add_f32 v58, v14, v58
v_fmac_f32 v59, v14, v14
v_add_f32 v58, v15, v58
v_fmac_f32 v59, v15, v15
s_nop 1
v_permlane32_swap_b32 v58, v59
v_add_f32 v58, v58, v59
ds_write_b32 v89, v58 offset:1024
v_xor_b32 v80, 0x10000, v80
s_waitcnt vmcnt(3) lgkmcnt(0)
s_add_u32 s49, s49, 313344
s_barrier
s_add_u32 m0, s50, 32768
R 16,0
D 84,0
R 20,1024
R 24,2048
D 84,1024
R 28,3072
R 32,4096
D 84,2048
s_waitcnt lgkmcnt(3)
mav 0,0,16
R 36,5120
mav 4,4,16
mav 0,8,20
R 40,6144
mav 4,12,20
s_waitcnt lgkmcnt(3)
mav 0,16,24
D 84,3072
mav 4,20,24
s_add_u32 m0, s50, 36864
R 44,7168
mav 0,24,28
R 16,8192
mav 4,28,28
s_waitcnt lgkmcnt(3)
mav 0,32,32
D 85,0
mav 4,36,32
R 20,9216
mav 0,40,36
R 24,10240
mav 4,44,36
s_waitcnt lgkmcnt(3)
mav 0,48,40
D 85,1024
mav 4,52,40
R 28,11264
v_add_u32 v56, 1, v56
mav 0,56,44
R 32,12288
mav 4,60,44
s_waitcnt lgkmcnt(3)
mav 0,64,16
D 85,2048
mav 4,68,16
R 36,13312
mav 0,72,20
R 40,14336
mav 4,76,20
s_waitcnt lgkmcnt(3)
mav 0,80,24
D 85,3072
mav 4,84,24
R 44,15360
mav 0,88,28
R 16,16384
mav 4,92,28
s_waitcnt lgkmcnt(3)
mav 0,96,32
R 20,17408
mav 4,100,32
mav 0,104,36
R 24,18432
mav 4,108,36
s_waitcnt lgkmcnt(3)
mav 0,112,40
R 28,19456
mav 4,116,40
mav 0,120,44
s_waitcnt vmcnt(9)
R 32,20480
v_lshlrev_b32 v8, 16, v48
mav 4,124,44
s_waitcnt lgkmcnt(3)
v_and_b32 v9, 0xffff0000, v48
v_lshlrev_b32 v10, 16, v49
mav 0,128,16
R 36,21504
v_and_b32 v11, 0xffff0000, v49
mav 4,132,16
v_lshlrev_b32 v12, 16, v50
v_and_b32 v13, 0xffff0000, v50
mav 0,136,20
R 40,22528
v_lshlrev_b32 v14, 16, v51
mav 4,140,20
s_waitcnt lgkmcnt(3)
v_and_b32 v15, 0xffff0000, v51
mav 0,144,24
s_waitcnt vmcnt(8)
buffer_store_dword v56, v81, s[36:39], s52 offen sc1
mav 4,148,24
R 44,23552
mav 0,152,28
R 16,24576
mav 4,156,28
s_waitcnt lgkmcnt(3)
mav 0,160,32
R 20,25600
mav 4,164,32
mav 0,168,36
R 24,26624
mav 4,172,36
s_waitcnt lgkmcnt(3)
mav 0,176,40
R 28,27648
mav 4,180,40
mav 0,184,44
R 32,28672
mav 4,188,44
s_waitcnt lgkmcnt(3)
mav 0,192,16
R 36,29696
mav 4,196,16
mav 0,200,20
R 40,30720
mav 4,204,20
s_waitcnt lgkmcnt(3)
mav 0,208,24
R 44,31744
mav 4,212,24
mav 0,216,28
mav 4,220,28
s_waitcnt lgkmcnt(2)
mav 0,224,32
mav 4,228,32
mav 0,232,36
mav 4,236,36
s_waitcnt lgkmcnt(0)
mav 0,240,40
mav 4,244,40
mav 0,248,44
mav 4,252,44
s_nop 6
v_cvt_pk_bf16_f32 v52, v0, v1
v_cvt_pk_bf16_f32 v53, v2, v3
v_cvt_pk_bf16_f32 v54, v4, v5
v_cvt_pk_bf16_f32 v55, v6, v7
buffer_store_dwordx4 v[52:55], v83, s[36:39], s49 offen sc1
v_mul_f32 v59, v0, v0
v_add_f32 v58, v1, v0
v_fmac_f32 v59, v1, v1
v_add_f32 v58, v2, v58
v_fmac_f32 v59, v2, v2
v_add_f32 v58, v3, v58
v_fmac_f32 v59, v3, v3
v_add_f32 v58, v4, v58
v_fmac_f32 v59, v4, v4
v_add_f32 v58, v5, v58
v_fmac_f32 v59, v5, v5
v_add_f32 v58, v6, v58
v_fmac_f32 v59, v6, v6
v_add_f32 v58, v7, v58
v_fmac_f32 v59, v7, v7
s_nop 1
v_permlane32_swap_b32 v58, v59
v_add_f32 v58, v58, v59
ds_write_b32 v89, v58 offset:0
s_waitcnt vmcnt(2) lgkmcnt(0)
s_add_u32 s49, s49, 313344
s_barrier
R 16,32768
R 20,33792
R 24,34816
R 28,35840
R 32,36864
s_waitcnt lgkmcnt(3)
mav 8,0,16
R 36,37888
mav 12,4,16
mav 8,8,20
R 40,38912
mav 12,12,20
s_waitcnt lgkmcnt(3)
mav 8,16,24
R 44,39936
mav 12,20,24
mav 8,24,28
R 16,40960
mav 12,28,28
s_waitcnt lgkmcnt(3)
mav 8,32,32
R 20,41984
mav 12,36,32
mav 8,40,36
R 24,43008
mav 12,44,36
s_waitcnt lgkmcnt(3)
mav 8,48,40
R 28,44032
v_add_u32 v56, 1, v56
mav 12,52,40
mav 8,56,44
R 32,45056
mav 12,60,44
s_waitcnt lgkmcnt(3)
mav 8,64,16
R 36,46080
mav 12,68,16
mav 8,72,20
R 40,47104
mav 12,76,20
s_waitcnt lgkmcnt(3)
mav 8,80,24
R 44,48128
mav 12,84,24
mav 8,88,28
R 16,49152
mav 12,92,28
s_waitcnt lgkmcnt(3)
mav 8,96,32
R 20,50176
mav 12,100,32
mav 8,104,36
R 24,51200
mav 12,108,36
s_waitcnt lgkmcnt(3)
mav 8,112,40
R 28,52224
mav 12,116,40
mav 8,120,44
R 32,53248
mav 12,124,44
s_waitcnt lgkmcnt(3)
mav 8,128,16
R 36,54272
mav 12,132,16
mav 8,136,20
R 40,55296
mav 12,140,20
s_waitcnt lgkmcnt(3)
mav 8,144,24
s_waitcnt vmcnt(0)
buffer_store_dword v56, v81, s[36:39], s52 offen sc1
mav 12,148,24
R 44,56320
mav 8,152,28
R 16,57344
mav 12,156,28
s_waitcnt lgkmcnt(3)
)"
R"(
mav 8,160,32
R 20,58368
mav 12,164,32
mav 8,168,36
R 24,59392
mav 12,172,36
s_waitcnt lgkmcnt(3)
mav 8,176,40
R 28,60416
mav 12,180,40
mav 8,184,44
R 32,61440
mav 12,188,44
s_waitcnt lgkmcnt(3)
mav 8,192,16
R 36,62464
mav 12,196,16
mav 8,200,20
R 40,63488
mav 12,204,20
s_waitcnt lgkmcnt(3)
mav 8,208,24
R 44,64512
mav 12,212,24
mav 8,216,28
mav 12,220,28
s_waitcnt lgkmcnt(2)
mav 8,224,32
mav 12,228,32
mav 8,232,36
mav 12,236,36
s_waitcnt lgkmcnt(0)
mav 8,240,40
mav 12,244,40
mav 8,248,44
mav 12,252,44
s_nop 6
v_cvt_pk_bf16_f32 v52, v8, v9
v_cvt_pk_bf16_f32 v53, v10, v11
v_cvt_pk_bf16_f32 v54, v12, v13
v_cvt_pk_bf16_f32 v55, v14, v15
buffer_store_dwordx4 v[52:55], v83, s[36:39], s49 offen sc1
v_mul_f32 v59, v8, v8
v_add_f32 v58, v9, v8
v_fmac_f32 v59, v9, v9
v_add_f32 v58, v10, v58
v_fmac_f32 v59, v10, v10
v_add_f32 v58, v11, v58
v_fmac_f32 v59, v11, v11
v_add_f32 v58, v12, v58
v_fmac_f32 v59, v12, v12
v_add_f32 v58, v13, v58
v_fmac_f32 v59, v13, v13
v_add_f32 v58, v14, v58
v_fmac_f32 v59, v14, v14
v_add_f32 v58, v15, v58
v_fmac_f32 v59, v15, v15
s_nop 1
v_permlane32_swap_b32 v58, v59
v_add_f32 v58, v58, v59
ds_write_b32 v89, v58 offset:1024
s_waitcnt lgkmcnt(0)
s_add_u32 s49, s49, 313344
s_barrier
v_add_u32 v56, 37, v56
s_waitcnt vmcnt(0)
buffer_store_dword v56, v81, s[36:39], s52 offen sc1
s_endpgm
LvO3:
LsO9:
s_cbranch_execz LwO
s_add_u32 s49, s49, 313344
s_barrier
buffer_load_dwordx4 v[48:51], v88, s[44:47], s54 offen sc1
s_add_u32 m0, s50, 0
s_nop 0
D 84,0
D 84,1024
D 84,2048
D 84,3072
s_add_u32 m0, s50, 4096
s_nop 0
D 85,0
D 85,1024
D 85,2048
D 85,3072
s_waitcnt vmcnt(8)
v_lshlrev_b32 v0, 16, v48
v_and_b32 v1, 0xffff0000, v48
v_lshlrev_b32 v2, 16, v49
v_and_b32 v3, 0xffff0000, v49
v_lshlrev_b32 v4, 16, v50
v_and_b32 v5, 0xffff0000, v50
v_lshlrev_b32 v6, 16, v51
v_and_b32 v7, 0xffff0000, v51
s_sub_u32 s54, s54, 0x8000
buffer_load_dwordx4 v[48:51], v88, s[44:47], s54 offen sc1
s_waitcnt vmcnt(1)
s_add_u32 s49, s49, 313344
s_barrier
s_add_u32 m0, s50, 32768
R 16,0
D 84,0
R 20,1024
R 24,2048
D 84,1024
R 28,3072
R 32,4096
D 84,2048
s_waitcnt lgkmcnt(3)
mav 0,0,16
R 36,5120
mav 4,4,16
mav 0,8,20
R 40,6144
mav 4,12,20
s_waitcnt lgkmcnt(3)
mav 0,16,24
R 44,7168
mav 4,20,24
D 84,3072
mav 0,24,28
s_add_u32 m0, s50, 36864
R 16,8192
mav 4,28,28
s_waitcnt lgkmcnt(3)
mav 0,32,32
R 20,9216
mav 4,36,32
D 85,0
mav 0,40,36
R 24,10240
mav 4,44,36
s_waitcnt lgkmcnt(3)
mav 0,48,40
R 28,11264
mav 4,52,40
D 85,1024
mav 0,56,44
R 32,12288
mav 4,60,44
s_waitcnt lgkmcnt(3)
mav 0,64,16
R 36,13312
mav 4,68,16
D 85,2048
mav 0,72,20
R 40,14336
mav 4,76,20
s_waitcnt lgkmcnt(3)
mav 0,80,24
R 44,15360
mav 4,84,24
D 85,3072
mav 0,88,28
R 16,16384
mav 4,92,28
s_waitcnt lgkmcnt(3)
mav 0,96,32
R 20,17408
mav 4,100,32
mav 0,104,36
R 24,18432
mav 4,108,36
s_waitcnt lgkmcnt(3)
mav 0,112,40
R 28,19456
mav 4,116,40
mav 0,120,44
s_waitcnt vmcnt(8)
R 32,20480
v_lshlrev_b32 v8, 16, v48
mav 4,124,44
s_waitcnt lgkmcnt(3)
v_and_b32 v9, 0xffff0000, v48
v_lshlrev_b32 v10, 16, v49
mav 0,128,16
R 36,21504
v_and_b32 v11, 0xffff0000, v49
mav 4,132,16
v_lshlrev_b32 v12, 16, v50
v_and_b32 v13, 0xffff0000, v50
mav 0,136,20
R 40,22528
v_lshlrev_b32 v14, 16, v51
mav 4,140,20
s_waitcnt lgkmcnt(3)
v_and_b32 v15, 0xffff0000, v51
mav 0,144,24
R 44,23552
mav 4,148,24
s_sub_u32 s54, s54, 0x8000
mav 0,152,28
R 16,24576
mav 4,156,28
s_waitcnt lgkmcnt(3)
s_cmp_lt_u32 s54, s56
s_cselect_b64 s[44:45], s[40:41], s[44:45]
mav 0,160,32
R 20,25600
mav 4,164,32
buffer_load_dwordx4 v[48:51], v88, s[44:47], s54 offen sc1
mav 0,168,36
R 24,26624
mav 4,172,36
s_waitcnt lgkmcnt(3)
mav 0,176,40
R 28,27648
mav 4,180,40
mav 0,184,44
R 32,28672
mav 4,188,44
s_waitcnt lgkmcnt(3)
mav 0,192,16
R 36,29696
mav 4,196,16
mav 0,200,20
R 40,30720
mav 4,204,20
s_waitcnt lgkmcnt(3)
mav 0,208,24
R 44,31744
mav 4,212,24
mav 0,216,28
mav 4,220,28
s_waitcnt lgkmcnt(2)
mav 0,224,32
mav 4,228,32
mav 0,232,36
mav 4,236,36
s_waitcnt lgkmcnt(0)
mav 0,240,40
mav 4,244,40
mav 0,248,44
mav 4,252,44
s_nop 6
v_cvt_pk_bf16_f32 v52, v0, v1
v_cvt_pk_bf16_f32 v53, v2, v3
v_cvt_pk_bf16_f32 v54, v4, v5
v_cvt_pk_bf16_f32 v55, v6, v7
buffer_store_dwordx4 v[52:55], v83, s[36:39], s49 offen sc1
v_mul_f32 v59, v0, v0
v_add_f32 v58, v1, v0
v_fmac_f32 v59, v1, v1
v_add_f32 v58, v2, v58
v_fmac_f32 v59, v2, v2
v_add_f32 v58, v3, v58
v_fmac_f32 v59, v3, v3
v_add_f32 v58, v4, v58
v_fmac_f32 v59, v4, v4
v_add_f32 v58, v5, v58
v_fmac_f32 v59, v5, v5
v_add_f32 v58, v6, v58
v_fmac_f32 v59, v6, v6
v_add_f32 v58, v7, v58
v_fmac_f32 v59, v7, v7
s_nop 1
v_permlane32_swap_b32 v58, v59
v_add_f32 v58, v58, v59
ds_write_b32 v89, v58 offset:0
s_xor_b32 s50, s50, 0x10000
s_waitcnt vmcnt(2) lgkmcnt(0)
s_add_u32 s49, s49, 313344
s_barrier
s_add_u32 m0, s50, 0
R 16,32768
D 84,0
R 20,33792
R 24,34816
D 84,1024
R 28,35840
R 32,36864
D 84,2048
s_waitcnt lgkmcnt(3)
mav 8,0,16
R 36,37888
mav 12,4,16
mav 8,8,20
R 40,38912
mav 12,12,20
s_waitcnt lgkmcnt(3)
mav 8,16,24
R 44,39936
mav 12,20,24
D 84,3072
mav 8,24,28
s_add_u32 m0, s50, 4096
R 16,40960
mav 12,28,28
s_waitcnt lgkmcnt(3)
mav 8,32,32
R 20,41984
mav 12,36,32
D 85,0
mav 8,40,36
R 24,43008
mav 12,44,36
s_waitcnt lgkmcnt(3)
mav 8,48,40
R 28,44032
v_add_u32 v56, 1, v56
mav 12,52,40
D 85,1024
mav 8,56,44
R 32,45056
mav 12,60,44
s_waitcnt lgkmcnt(3)
mav 8,64,16
R 36,46080
mav 12,68,16
D 85,2048
mav 8,72,20
R 40,47104
mav 12,76,20
s_waitcnt lgkmcnt(3)
mav 8,80,24
R 44,48128
mav 12,84,24
D 85,3072
mav 8,88,28
R 16,49152
mav 12,92,28
s_waitcnt lgkmcnt(3)
mav 8,96,32
R 20,50176
mav 12,100,32
mav 8,104,36
R 24,51200
mav 12,108,36
s_waitcnt lgkmcnt(3)
mav 8,112,40
R 28,52224
mav 12,116,40
LsO10:
s_cbranch_execz LwO
mav 8,120,44
s_waitcnt vmcnt(9)
R 32,53248
v_lshlrev_b32 v0, 16, v48
mav 12,124,44
s_waitcnt lgkmcnt(3)
v_and_b32 v1, 0xffff0000, v48
v_lshlrev_b32 v2, 16, v49
mav 8,128,16
R 36,54272
v_and_b32 v3, 0xffff0000, v49
mav 12,132,16
v_lshlrev_b32 v4, 16, v50
v_and_b32 v5, 0xffff0000, v50
mav 8,136,20
R 40,55296
v_lshlrev_b32 v6, 16, v51
mav 12,140,20
s_waitcnt lgkmcnt(3)
v_and_b32 v7, 0xffff0000, v51
mav 8,144,24
s_waitcnt vmcnt(8)
R 44,56320
mav 12,148,24
buffer_store_dword v56, v81, s[36:39], s52 offen sc1
mav 8,152,28
s_sub_u32 s54, s54, 0x8000
R 16,57344
mav 12,156,28
s_waitcnt lgkmcnt(3)
mav 8,160,32
R 20,58368
mav 12,164,32
buffer_load_dwordx4 v[48:51], v88, s[44:47], s54 offen sc1
mav 8,168,36
R 24,59392
mav 12,172,36
s_waitcnt lgkmcnt(3)
mav 8,176,40
R 28,60416
mav 12,180,40
mav 8,184,44
R 32,61440
mav 12,188,44
s_waitcnt lgkmcnt(3)
mav 8,192,16
R 36,62464
mav 12,196,16
mav 8,200,20
R 40,63488
mav 12,204,20
s_waitcnt lgkmcnt(3)
mav 8,208,24
R 44,64512
mav 12,212,24
mav 8,216,28
mav 12,220,28
s_waitcnt lgkmcnt(2)
mav 8,224,32
mav 12,228,32
mav 8,232,36
s_mov_b32 s60, -30
.p2align 6
LO3_1:
mav 12,236,36
s_waitcnt lgkmcnt(0)
mav 8,240,40
mav 12,244,40
mav 8,248,44
mav 12,252,44
s_nop 6
v_cvt_pk_bf16_f32 v52, v8, v9
v_cvt_pk_bf16_f32 v53, v10, v11
v_cvt_pk_bf16_f32 v54, v12, v13
v_cvt_pk_bf16_f32 v55, v14, v15
buffer_store_dwordx4 v[52:55], v83, s[36:39], s49 offen sc1
v_mul_f32 v59, v8, v8
v_add_f32 v58, v9, v8
v_fmac_f32 v59, v9, v9
v_add_f32 v58, v10, v58
v_fmac_f32 v59, v10, v10
v_add_f32 v58, v11, v58
v_fmac_f32 v59, v11, v11
v_add_f32 v58, v12, v58
v_fmac_f32 v59, v12, v12
v_add_f32 v58, v13, v58
v_fmac_f32 v59, v13, v13
v_add_f32 v58, v14, v58
v_fmac_f32 v59, v14, v14
v_add_f32 v58, v15, v58
v_fmac_f32 v59, v15, v15
s_nop 1
v_permlane32_swap_b32 v58, v59
v_add_f32 v58, v58, v59
ds_write_b32 v89, v58 offset:1024
v_xor_b32 v80, 0x10000, v80
s_waitcnt vmcnt(3) lgkmcnt(0)
s_add_u32 s49, s49, 313344
s_barrier
s_add_u32 m0, s50, 32768
R 16,0
D 84,0
R 20,1024
R 24,2048
D 84,1024
R 28,3072
R 32,4096
D 84,2048
s_waitcnt lgkmcnt(3)
mav 0,0,16
R 36,5120
mav 4,4,16
mav 0,8,20
R 40,6144
mav 4,12,20
s_waitcnt lgkmcnt(3)
mav 0,16,24
R 44,7168
mav 4,20,24
D 84,3072
mav 0,24,28
s_add_u32 m0, s50, 36864
R 16,8192
mav 4,28,28
s_waitcnt lgkmcnt(3)
mav 0,32,32
R 20,9216
mav 4,36,32
D 85,0
mav 0,40,36
R 24,10240
mav 4,44,36
s_waitcnt lgkmcnt(3)
mav 0,48,40
R 28,11264
v_add_u32 v56, 1, v56
mav 4,52,40
D 85,1024
mav 0,56,44
R 32,12288
mav 4,60,44
s_waitcnt lgkmcnt(3)
mav 0,64,16
R 36,13312
mav 4,68,16
D 85,2048
mav 0,72,20
R 40,14336
mav 4,76,20
s_waitcnt lgkmcnt(3)
mav 0,80,24
R 44,15360
mav 4,84,24
D 85,3072
mav 0,88,28
R 16,16384
mav 4,92,28
s_waitcnt lgkmcnt(3)
mav 0,96,32
R 20,17408
mav 4,100,32
mav 0,104,36
R 24,18432
mav 4,108,36
s_waitcnt lgkmcnt(3)
mav 0,112,40
R 28,19456
mav 4,116,40
mav 0,120,44
s_waitcnt vmcnt(9)
R 32,20480
v_lshlrev_b32 v8, 16, v48
mav 4,124,44
s_waitcnt lgkmcnt(3)
v_and_b32 v9, 0xffff0000, v48
v_lshlrev_b32 v10, 16, v49
mav 0,128,16
R 36,21504
v_and_b32 v11, 0xffff0000, v49
mav 4,132,16
v_lshlrev_b32 v12, 16, v50
v_and_b32 v13, 0xffff0000, v50
mav 0,136,20
R 40,22528
v_lshlrev_b32 v14, 16, v51
mav 4,140,20
s_waitcnt lgkmcnt(3)
v_and_b32 v15, 0xffff0000, v51
mav 0,144,24
s_waitcnt vmcnt(8)
R 44,23552
mav 4,148,24
buffer_store_dword v56, v81, s[36:39], s52 offen sc1
mav 0,152,28
s_sub_u32 s54, s54, 0x8000
R 16,24576
mav 4,156,28
s_waitcnt lgkmcnt(3)
s_cmp_lt_u32 s54, s56
s_cselect_b64 s[44:45], s[40:41], s[44:45]
mav 0,160,32
R 20,25600
mav 4,164,32
buffer_load_dwordx4 v[48:51], v88, s[44:47], s54 offen sc1
mav 0,168,36
R 24,26624
mav 4,172,36
s_waitcnt lgkmcnt(3)
mav 0,176,40
R 28,27648
mav 4,180,40
mav 0,184,44
R 32,28672
mav 4,188,44
s_waitcnt lgkmcnt(3)
mav 0,192,16
R 36,29696
mav 4,196,16
mav 0,200,20
R 40,30720
mav 4,204,20
s_waitcnt lgkmcnt(3)
mav 0,208,24
R 44,31744
mav 4,212,24
mav 0,216,28
mav 4,220,28
s_waitcnt lgkmcnt(2)
mav 0,224,32
mav 4,228,32
mav 0,232,36
mav 4,236,36
s_waitcnt lgkmcnt(0)
mav 0,240,40
mav 4,244,40
mav 0,248,44
mav 4,252,44
s_nop 6
v_cvt_pk_bf16_f32 v52, v0, v1
v_cvt_pk_bf16_f32 v53, v2, v3
v_cvt_pk_bf16_f32 v54, v4, v5
v_cvt_pk_bf16_f32 v55, v6, v7
buffer_store_dwordx4 v[52:55], v83, s[36:39], s49 offen sc1
v_mul_f32 v59, v0, v0
v_add_f32 v58, v1, v0
v_fmac_f32 v59, v1, v1
v_add_f32 v58, v2, v58
v_fmac_f32 v59, v2, v2
v_add_f32 v58, v3, v58
v_fmac_f32 v59, v3, v3
v_add_f32 v58, v4, v58
v_fmac_f32 v59, v4, v4
v_add_f32 v58, v5, v58
v_fmac_f32 v59, v5, v5
v_add_f32 v58, v6, v58
v_fmac_f32 v59, v6, v6
v_add_f32 v58, v7, v58
v_fmac_f32 v59, v7, v7
s_nop 1
v_permlane32_swap_b32 v58, v59
v_add_f32 v58, v58, v59
ds_write_b32 v89, v58 offset:0
s_xor_b32 s50, s50, 0x10000
s_waitcnt vmcnt(3) lgkmcnt(0)
s_add_u32 s49, s49, 313344
s_barrier
s_add_u32 m0, s50, 0
R 16,32768
D 84,0
R 20,33792
R 24,34816
D 84,1024
R 28,35840
R 32,36864
D 84,2048
s_waitcnt lgkmcnt(3)
mav 8,0,16
R 36,37888
mav 12,4,16
mav 8,8,20
R 40,38912
mav 12,12,20
s_waitcnt lgkmcnt(3)
mav 8,16,24
R 44,39936
mav 12,20,24
D 84,3072
mav 8,24,28
s_add_u32 m0, s50, 4096
R 16,40960
mav 12,28,28
s_waitcnt lgkmcnt(3)
mav 8,32,32
R 20,41984
mav 12,36,32
D 85,0
mav 8,40,36
R 24,43008
mav 12,44,36
s_waitcnt lgkmcnt(3)
mav 8,48,40
R 28,44032
v_add_u32 v56, 1, v56
mav 12,52,40
D 85,1024
mav 8,56,44
R 32,45056
mav 12,60,44
s_waitcnt lgkmcnt(3)
mav 8,64,16
R 36,46080
mav 12,68,16
D 85,2048
mav 8,72,20
R 40,47104
mav 12,76,20
s_waitcnt lgkmcnt(3)
mav 8,80,24
R 44,48128
mav 12,84,24
D 85,3072
mav 8,88,28
R 16,49152
mav 12,92,28
s_waitcnt lgkmcnt(3)
mav 8,96,32
R 20,50176
mav 12,100,32
mav 8,104,36
R 24,51200
mav 12,108,36
s_waitcnt lgkmcnt(3)
mav 8,112,40
R 28,52224
mav 12,116,40
mav 8,120,44
s_waitcnt vmcnt(9)
R 32,53248
v_lshlrev_b32 v0, 16, v48
mav 12,124,44
s_waitcnt lgkmcnt(3)
v_and_b32 v1, 0xffff0000, v48
v_lshlrev_b32 v2, 16, v49
mav 8,128,16
R 36,54272
v_and_b32 v3, 0xffff0000, v49
mav 12,132,16
v_lshlrev_b32 v4, 16, v50
v_and_b32 v5, 0xffff0000, v50
mav 8,136,20
R 40,55296
v_lshlrev_b32 v6, 16, v51
mav 12,140,20
s_waitcnt lgkmcnt(3)
v_and_b32 v7, 0xffff0000, v51
mav 8,144,24
s_waitcnt vmcnt(8)
R 44,56320
mav 12,148,24
buffer_store_dword v56, v81, s[36:39], s52 offen sc1
mav 8,152,28
s_sub_u32 s54, s54, 0x8000
R 16,57344
mav 12,156,28
s_waitcnt lgkmcnt(3)
mav 8,160,32
R 20,58368
mav 12,164,32
buffer_load_dwordx4 v[48:51], v88, s[44:47], s54 offen sc1
mav 8,168,36
R 24,59392
mav 12,172,36
s_waitcnt lgkmcnt(3)
mav 8,176,40
R 28,60416
mav 12,180,40
mav 8,184,44
R 32,61440
mav 12,188,44
s_waitcnt lgkmcnt(3)
mav 8,192,16
R 36,62464
mav 12,196,16
mav 8,200,20
R 40,63488
mav 12,204,20
s_waitcnt lgkmcnt(3)
mav 8,208,24
R 44,64512
mav 12,212,24
mav 8,216,28
mav 12,220,28
s_waitcnt lgkmcnt(2)
mav 8,224,32
mav 12,228,32
s_add_u32 s60, s60, 1
mav 8,232,36
s_cbranch_scc0 LO3_1
mav 12,236,36
s_waitcnt lgkmcnt(0)
mav 8,240,40
mav 12,244,40
mav 8,248,44
mav 12,252,44
s_nop 6
v_cvt_pk_bf16_f32 v52, v8, v9
v_cvt_pk_bf16_f32 v53, v10, v11
v_cvt_pk_bf16_f32 v54, v12, v13
v_cvt_pk_bf16_f32 v55, v14, v15
buffer_store_dwordx4 v[52:55], v83, s[36:39], s49 offen sc1
v_mul_f32 v59, v8, v8
v_add_f32 v58, v9, v8
v_fmac_f32 v59, v9, v9
v_add_f32 v58, v10, v58
v_fmac_f32 v59, v10, v10
v_add_f32 v58, v11, v58
v_fmac_f32 v59, v11, v11
v_add_f32 v58, v12, v58
v_fmac_f32 v59, v12, v12
v_add_f32 v58, v13, v58
v_fmac_f32 v59, v13, v13
v_add_f32 v58, v14, v58
v_fmac_f32 v59, v14, v14
v_add_f32 v58, v15, v58
v_fmac_f32 v59, v15, v15
s_nop 1
v_permlane32_swap_b32 v58, v59
v_add_f32 v58, v58, v59
ds_write_b32 v89, v58 offset:1024
v_xor_b32 v80, 0x10000, v80
s_waitcnt vmcnt(3) lgkmcnt(0)
s_add_u32 s49, s49, 313344
s_barrier
s_add_u32 m0, s50, 32768
R 16,0
D 84,0
R 20,1024
R 24,2048
D 84,1024
R 28,3072
R 32,4096
D 84,2048
s_waitcnt lgkmcnt(3)
mav 0,0,16
R 36,5120
mav 4,4,16
mav 0,8,20
R 40,6144
mav 4,12,20
s_waitcnt lgkmcnt(3)
mav 0,16,24
R 44,7168
mav 4,20,24
D 84,3072
mav 0,24,28
s_add_u32 m0, s50, 36864
R 16,8192
mav 4,28,28
s_waitcnt lgkmcnt(3)
mav 0,32,32
R 20,9216
mav 4,36,32
D 85,0
mav 0,40,36
R 24,10240
mav 4,44,36
s_waitcnt lgkmcnt(3)
mav 0,48,40
R 28,11264
v_add_u32 v56, 1, v56
mav 4,52,40
D 85,1024
mav 0,56,44
R 32,12288
mav 4,60,44
s_waitcnt lgkmcnt(3)
mav 0,64,16
R 36,13312
mav 4,68,16
D 85,2048
mav 0,72,20
R 40,14336
mav 4,76,20
s_waitcnt lgkmcnt(3)
mav 0,80,24
R 44,15360
mav 4,84,24
D 85,3072
mav 0,88,28
R 16,16384
mav 4,92,28
s_waitcnt lgkmcnt(3)
mav 0,96,32
R 20,17408
mav 4,100,32
mav 0,104,36
R 24,18432
mav 4,108,36
s_waitcnt lgkmcnt(3)
mav 0,112,40
R 28,19456
mav 4,116,40
mav 0,120,44
s_waitcnt vmcnt(9)
R 32,20480
v_lshlrev_b32 v8, 16, v48
mav 4,124,44
s_waitcnt lgkmcnt(3)
v_and_b32 v9, 0xffff0000, v48
v_lshlrev_b32 v10, 16, v49
mav 0,128,16
R 36,21504
v_and_b32 v11, 0xffff0000, v49
mav 4,132,16
v_lshlrev_b32 v12, 16, v50
v_and_b32 v13, 0xffff0000, v50
mav 0,136,20
R 40,22528
v_lshlrev_b32 v14, 16, v51
mav 4,140,20
s_waitcnt lgkmcnt(3)
v_and_b32 v15, 0xffff0000, v51
mav 0,144,24
s_waitcnt vmcnt(8)
R 44,23552
mav 4,148,24
buffer_store_dword v56, v81, s[36:39], s52 offen sc1
mav 0,152,28
R 16,24576
mav 4,156,28
s_waitcnt lgkmcnt(3)
mav 0,160,32
R 20,25600
mav 4,164,32
mav 0,168,36
R 24,26624
mav 4,172,36
s_waitcnt lgkmcnt(3)
mav 0,176,40
R 28,27648
mav 4,180,40
mav 0,184,44
R 32,28672
mav 4,188,44
s_waitcnt lgkmcnt(3)
mav 0,192,16
R 36,29696
mav 4,196,16
mav 0,200,20
R 40,30720
mav 4,204,20
s_waitcnt lgkmcnt(3)
mav 0,208,24
R 44,31744
mav 4,212,24
mav 0,216,28
mav 4,220,28
s_waitcnt lgkmcnt(2)
mav 0,224,32
mav 4,228,32
mav 0,232,36
mav 4,236,36
s_waitcnt lgkmcnt(0)
mav 0,240,40
mav 4,244,40
mav 0,248,44
mav 4,252,44
s_nop 6
v_cvt_pk_bf16_f32 v52, v0, v1
v_cvt_pk_bf16_f32 v53, v2, v3
v_cvt_pk_bf16_f32 v54, v4, v5
v_cvt_pk_bf16_f32 v55, v6, v7
buffer_store_dwordx4 v[52:55], v83, s[36:39], s49 offen sc1
v_mul_f32 v59, v0, v0
v_add_f32 v58, v1, v0
v_fmac_f32 v59, v1, v1
v_add_f32 v58, v2, v58
v_fmac_f32 v59, v2, v2
v_add_f32 v58, v3, v58
v_fmac_f32 v59, v3, v3
v_add_f32 v58, v4, v58
v_fmac_f32 v59, v4, v4
v_add_f32 v58, v5, v58
v_fmac_f32 v59, v5, v5
v_add_f32 v58, v6, v58
v_fmac_f32 v59, v6, v6
v_add_f32 v58, v7, v58
v_fmac_f32 v59, v7, v7
s_nop 1
v_permlane32_swap_b32 v58, v59
v_add_f32 v58, v58, v59
ds_write_b32 v89, v58 offset:0
s_waitcnt vmcnt(2) lgkmcnt(0)
s_add_u32 s49, s49, 313344
s_barrier
R 16,32768
R 20,33792
R 24,34816
R 28,35840
R 32,36864
LsO11:
s_cbranch_execz LwO
s_waitcnt lgkmcnt(3)
mav 8,0,16
R 36,37888
mav 12,4,16
mav 8,8,20
R 40,38912
mav 12,12,20
s_waitcnt lgkmcnt(3)
mav 8,16,24
R 44,39936
mav 12,20,24
mav 8,24,28
R 16,40960
mav 12,28,28
s_waitcnt lgkmcnt(3)
mav 8,32,32
R 20,41984
mav 12,36,32
mav 8,40,36
R 24,43008
mav 12,44,36
s_waitcnt lgkmcnt(3)
mav 8,48,40
R 28,44032
v_add_u32 v56, 1, v56
mav 12,52,40
mav 8,56,44
R 32,45056
mav 12,60,44
s_waitcnt lgkmcnt(3)
mav 8,64,16
R 36,46080
mav 12,68,16
mav 8,72,20
R 40,47104
mav 12,76,20
s_waitcnt lgkmcnt(3)
mav 8,80,24
R 44,48128
mav 12,84,24
mav 8,88,28
R 16,49152
mav 12,92,28
s_waitcnt lgkmcnt(3)
mav 8,96,32
R 20,50176
mav 12,100,32
mav 8,104,36
R 24,51200
mav 12,108,36
s_waitcnt lgkmcnt(3)
mav 8,112,40
R 28,52224
mav 12,116,40
mav 8,120,44
R 32,53248
mav 12,124,44
s_waitcnt lgkmcnt(3)
mav 8,128,16
R 36,54272
mav 12,132,16
mav 8,136,20
R 40,55296
mav 12,140,20
s_waitcnt lgkmcnt(3)
mav 8,144,24
s_waitcnt vmcnt(0)
R 44,56320
mav 12,148,24
buffer_store_dword v56, v81, s[36:39], s52 offen sc1
mav 8,152,28
R 16,57344
mav 12,156,28
s_waitcnt lgkmcnt(3)
mav 8,160,32
R 20,58368
mav 12,164,32
mav 8,168,36
R 24,59392
mav 12,172,36
s_waitcnt lgkmcnt(3)
mav 8,176,40
R 28,60416
mav 12,180,40
mav 8,184,44
R 32,61440
mav 12,188,44
s_waitcnt lgkmcnt(3)
mav 8,192,16
R 36,62464
mav 12,196,16
mav 8,200,20
R 40,63488
mav 12,204,20
s_waitcnt lgkmcnt(3)
mav 8,208,24
R 44,64512
mav 12,212,24
mav 8,216,28
mav 12,220,28
s_waitcnt lgkmcnt(2)
mav 8,224,32
mav 12,228,32
mav 8,232,36
mav 12,236,36
s_waitcnt lgkmcnt(0)
mav 8,240,40
mav 12,244,40
mav 8,248,44
mav 12,252,44
s_nop 6
v_cvt_pk_bf16_f32 v52, v8, v9
v_cvt_pk_bf16_f32 v53, v10, v11
v_cvt_pk_bf16_f32 v54, v12, v13
v_cvt_pk_bf16_f32 v55, v14, v15
buffer_store_dwordx4 v[52:55], v83, s[36:39], s49 offen sc1
v_mul_f32 v59, v8, v8
v_add_f32 v58, v9, v8
v_fmac_f32 v59, v9, v9
v_add_f32 v58, v10, v58
v_fmac_f32 v59, v10, v10
v_add_f32 v58, v11, v58
v_fmac_f32 v59, v11, v11
v_add_f32 v58, v12, v58
v_fmac_f32 v59, v12, v12
v_add_f32 v58, v13, v58
v_fmac_f32 v59, v13, v13
v_add_f32 v58, v14, v58
v_fmac_f32 v59, v14, v14
v_add_f32 v58, v15, v58
v_fmac_f32 v59, v15, v15
s_nop 1
v_permlane32_swap_b32 v58, v59
v_add_f32 v58, v58, v59
ds_write_b32 v89, v58 offset:1024
s_waitcnt lgkmcnt(0)
s_add_u32 s49, s49, 313344
s_barrier
v_add_u32 v56, 37, v56
s_waitcnt vmcnt(0)
buffer_store_dword v56, v81, s[36:39], s52 offen sc1
s_endpgm
Lr3:
s_cmp_eq_u32 s60, 3
s_cbranch_scc0 Lr4
s_waitcnt vmcnt(0)
s_barrier
v_accvgpr_read_b32 v77, a255
v_mov_b32 v76, 0x100
v_add_u32 v77, 0x9e3779b9, v77
buffer_store_dwordx2 v[76:77], v81, s[36:39], s52 offen sc1
v_add_u32 v88, v88, v76
E 0,65536
E 1,69632
E 2,73728
s_add_u32 s51, s51, 229376
E 0,77824
E 1,81920
E 2,86016
s_add_u32 s51, s51, 229376
E 0,90112
E 1,94208
E 2,98304
s_add_u32 s51, s51, 229376
E 0,102400
E 1,106496
E 2,110592
s_add_u32 s51, s51, 229376
s_mov_b64 exec, 0
s_cmp_eq_u32 s53, 0
s_cbranch_scc1 LsF0
s_cmp_eq_u32 s53, 1024
s_cbranch_scc1 LsF1
s_cmp_eq_u32 s53, 2048
s_cbranch_scc1 LsF2
s_branch LsF3
LwF:
s_mov_b64 exec, -1
v_mov_b32 v24, s64
v_mov_b32 v25, s64
v_mov_b32 v26, s64
v_mov_b32 v27, s64
s_waitcnt vmcnt(6)
s_barrier
v_add_u32 v4, 65536, v4
v_add_u32 v5, 65536, v5
v_add_u32 v6, 65536, v6
Ta 0,4,0
Ta 4,5,0
Ta 8,6,0
Ta 12,4,12288
Ta 16,5,12288
Ta 20,6,12288
E 0,114688
E 1,118784
E 2,122880
s_add_u32 s51, s51, 229376
E 0,126976
E 1,131072
E 2,135168
s_add_u32 s51, s51, 229376
E 0,139264
E 1,143360
E 2,147456
s_add_u32 s51, s51, 229376
E 0,151552
E 1,155648
E 2,159744
s_add_u32 s51, s51, 229376
s_cmp_ge_u32 s51, s57
s_cselect_b32 s60, 7340032, 0
s_sub_u32 s51, s51, s60
s_waitcnt lgkmcnt(0)
s_waitcnt vmcnt(12)
s_barrier
E 0,65536
Ta 24,4,24576
v_mfma_f32_16x16x32_bf16 v[56:59], a[0:3], v[24:27], 0
Ta 28,5,24576
E 1,69632
v_mfma_f32_16x16x32_bf16 v[60:63], a[4:7], v[24:27], 0
Ta 32,6,24576
Ta 36,4,36864
E 2,73728
s_add_u32 s51, s51, 229376
v_mfma_f32_16x16x32_bf16 v[64:67], a[8:11], v[24:27], 0
Ta 40,5,36864
E 0,77824
Ta 44,6,36864
mav 56,12,24
E 1,81920
mav 60,16,24
E 2,86016
s_add_u32 s51, s51, 229376
mav 64,20,24
s_waitcnt lgkmcnt(0)
s_waitcnt vmcnt(12)
s_barrier
v_add_u32 v4, 49152, v4
v_add_u32 v5, 49152, v5
v_add_u32 v6, 49152, v6
E 0,90112
Ta 48,4,0
mav 56,24,24
Ta 52,5,0
E 1,94208
mav 60,28,24
Ta 56,6,0
Ta 60,4,12288
E 2,98304
s_add_u32 s51, s51, 229376
mav 64,32,24
Ta 64,5,12288
E 0,102400
Ta 68,6,12288
mav 56,36,24
E 1,106496
mav 60,40,24
E 2,110592
s_add_u32 s51, s51, 229376
mav 64,44,24
s_waitcnt lgkmcnt(0)
s_waitcnt vmcnt(12)
s_barrier
E 0,114688
Ta 72,4,24576
mav 56,48,24
Ta 76,5,24576
E 1,118784
mav 60,52,24
Ta 80,6,24576
Ta 84,4,36864
E 2,122880
s_add_u32 s51, s51, 229376
mav 64,56,24
Ta 88,5,36864
E 0,126976
Ta 92,6,36864
mav 56,60,24
E 1,131072
mav 60,64,24
E 2,135168
s_add_u32 s51, s51, 229376
mav 64,68,24
s_waitcnt lgkmcnt(0)
s_waitcnt vmcnt(12)
s_barrier
v_add_u32 v4, -49152, v4
v_add_u32 v5, -49152, v5
v_add_u32 v6, -49152, v6
E 0,139264
Ta 96,4,0
mav 56,72,24
Ta 100,5,0
E 1,143360
mav 60,76,24
Ta 104,6,0
Ta 108,4,12288
E 2,147456
s_add_u32 s51, s51, 229376
mav 64,80,24
Ta 112,5,12288
E 0,151552
Ta 116,6,12288
mav 56,84,24
E 1,155648
mav 60,88,24
E 2,159744
s_add_u32 s51, s51, 229376
s_cmp_ge_u32 s51, s57
s_cselect_b32 s60, 7340032, 0
s_sub_u32 s51, s51, s60
mav 64,92,24
s_waitcnt lgkmcnt(0)
s_waitcnt vmcnt(12)
s_barrier
E 0,65536
Ta 120,4,24576
mav 56,96,24
Ta 124,5,24576
E 1,69632
mav 60,100,24
Ta 128,6,24576
Ta 132,4,36864
E 2,73728
s_add_u32 s51, s51, 229376
mav 64,104,24
Ta 136,5,36864
E 0,77824
Ta 140,6,36864
mav 56,108,24
E 1,81920
mav 60,112,24
E 2,86016
s_add_u32 s51, s51, 229376
mav 64,116,24
s_waitcnt lgkmcnt(0)
s_waitcnt vmcnt(12)
s_barrier
v_add_u32 v4, 49152, v4
v_add_u32 v5, 49152, v5
v_add_u32 v6, 49152, v6
E 0,90112
Ta 144,4,0
mav 56,120,24
Ta 148,5,0
E 1,94208
mav 60,124,24
Ta 152,6,0
Ta 156,4,12288
E 2,98304
s_add_u32 s51, s51, 229376
mav 64,128,24
Ta 160,5,12288
E 0,102400
Ta 164,6,12288
mav 56,132,24
E 1,106496
mav 60,136,24
E 2,110592
s_add_u32 s51, s51, 229376
mav 64,140,24
s_waitcnt lgkmcnt(0)
s_waitcnt vmcnt(12)
s_barrier
E 0,114688
Ta 168,4,24576
mav 56,144,24
Ta 172,5,24576
E 1,118784
mav 60,148,24
Ta 176,6,24576
Ta 180,4,36864
E 2,122880
s_add_u32 s51, s51, 229376
mav 64,152,24
Ta 184,5,36864
E 0,126976
Ta 188,6,36864
mav 56,156,24
E 1,131072
mav 60,160,24
E 2,135168
s_add_u32 s51, s51, 229376
mav 64,164,24
s_waitcnt lgkmcnt(0)
s_waitcnt vmcnt(12)
s_barrier
v_add_u32 v4, -49152, v4
v_add_u32 v5, -49152, v5
v_add_u32 v6, -49152, v6
E 0,139264
Ta 192,4,0
mav 56,168,24
Ta 196,5,0
E 1,143360
mav 60,172,24
Ta 200,6,0
Ta 204,4,12288
E 2,147456
s_add_u32 s51, s51, 229376
mav 64,176,24
LsF0:
s_cbranch_execz LsF4
Ta 208,5,12288
E 0,151552
Ta 212,6,12288
mav 56,180,24
E 1,155648
mav 60,184,24
E 2,159744
s_add_u32 s51, s51, 229376
s_cmp_ge_u32 s51, s57
s_cselect_b32 s60, 7340032, 0
s_sub_u32 s51, s51, s60
mav 64,188,24
s_waitcnt lgkmcnt(0)
s_waitcnt vmcnt(12)
s_barrier
E 0,65536
Ta 216,4,24576
mav 56,192,24
Ta 220,5,24576
E 1,69632
mav 60,196,24
Ta 224,6,24576
Ta 228,4,36864
E 2,73728
s_add_u32 s51, s51, 229376
mav 64,200,24
Ta 232,5,36864
E 0,77824
Ta 236,6,36864
mav 56,204,24
E 1,81920
mav 60,208,24
E 2,86016
s_add_u32 s51, s51, 229376
mav 64,212,24
s_waitcnt lgkmcnt(0)
s_waitcnt vmcnt(12)
s_barrier
v_add_u32 v4, 49152, v4
v_add_u32 v5, 49152, v5
v_add_u32 v6, 49152, v6
E 0,90112
Ta 240,4,0
mav 56,216,24
Ta 244,5,0
E 1,94208
mav 60,220,24
Ta 248,6,0
Ta 252,4,12288
E 2,98304
s_add_u32 s51, s51, 229376
mav 64,224,24
Tv 128,5,12288
E 0,102400
Tv 132,6,12288
mav 56,228,24
E 1,106496
mav 60,232,24
E 2,110592
s_add_u32 s51, s51, 229376
mav 64,236,24
s_waitcnt lgkmcnt(0)
s_waitcnt vmcnt(12)
s_barrier
E 0,114688
Tv 136,4,24576
mav 56,240,24
Tv 140,5,24576
E 1,118784
mav 60,244,24
Tv 144,6,24576
Tv 148,4,36864
E 2,122880
s_add_u32 s51, s51, 229376
mav 64,248,24
Tv 152,5,36864
E 0,126976
Tv 156,6,36864
mav 56,252,24
E 1,131072
mvv 60,128,24
E 2,135168
s_add_u32 s51, s51, 229376
mvv 64,132,24
s_waitcnt lgkmcnt(0)
s_waitcnt vmcnt(12)
s_barrier
v_add_u32 v4, -49152, v4
v_add_u32 v5, -49152, v5
v_add_u32 v6, -49152, v6
E 0,139264
Tv 160,4,0
mvv 56,136,24
Tv 164,5,0
E 1,143360
mvv 60,140,24
Tv 168,6,0
Tv 172,4,12288
E 2,147456
s_add_u32 s51, s51, 229376
mvv 64,144,24
Tv 176,5,12288
E 0,151552
Tv 180,6,12288
mvv 56,148,24
E 1,155648
mvv 60,152,24
E 2,159744
s_add_u32 s51, s51, 229376
s_cmp_ge_u32 s51, s57
s_cselect_b32 s60, 7340032, 0
s_sub_u32 s51, s51, s60
mvv 64,156,24
s_waitcnt lgkmcnt(0)
s_waitcnt vmcnt(12)
s_barrier
Tv 184,4,24576
mvv 56,160,24
Tv 188,5,24576
mvv 60,164,24
Tv 192,6,24576
Tv 196,4,36864
mvv 64,168,24
Tv 200,5,36864
Tv 204,6,36864
mvv 56,172,24
mvv 60,176,24
mvv 64,180,24
s_waitcnt lgkmcnt(0)
s_waitcnt vmcnt(6)
s_barrier
v_add_u32 v4, 49152, v4
v_add_u32 v5, 49152, v5
v_add_u32 v6, 49152, v6
Tv 208,4,0
mvv 56,184,24
Tv 212,5,0
mvv 60,188,24
Tv 216,6,0
Tv 220,4,12288
mvv 64,192,24
Tv 224,5,12288
Tv 228,6,12288
mvv 56,196,24
mvv 60,200,24
mvv 64,204,24
s_waitcnt lgkmcnt(0)
s_waitcnt vmcnt(0)
s_barrier
Tv 232,4,24576
mvv 56,208,24
Tv 236,5,24576
mvv 60,212,24
Tv 240,6,24576
Tv 244,4,36864
mvv 64,216,24
Tv 248,5,36864
Tv 252,6,36864
mvv 56,220,24
mvv 60,224,24
mvv 64,228,24
s_waitcnt lgkmcnt(0)
mvv 56,232,24
mvv 60,236,24
mvv 64,240,24
mvv 56,244,24
mvv 60,248,24
mvv 64,252,24
s_waitcnt vmcnt(0)
s_barrier
s_nop 7
s_and_b32 s60, s48, 3
s_cmp_eq_u32 s60, 1
s_cbranch_scc1 LvF1
s_cmp_eq_u32 s60, 2
s_cbranch_scc1 LvF2
s_cmp_eq_u32 s60, 3
s_cbranch_scc1 LvF3
LvF0:
LsF1:
s_cbranch_execz LsF5
buffer_load_dwordx2 v[106:107], v87, s[36:39], 0 offen sc1
s_waitcnt vmcnt(0)
ds_write_b32 v82, v106
v_sub_u32 v106, v106, v88
v_xor_b32 v107, v107, v77
v_cmp_lt_u64 vcc, s[62:63], v[106:107]
s_waitcnt lgkmcnt(0)
s_cbranch_vccnz CF0_1
BF0_1:
s_add_u32 s49, s49, 313344
s_barrier
s_add_u32 m0, s50, 0
s_nop 0
D 85,0
D 85,1024
D 85,2048
D 85,3072
s_add_u32 m0, s50, 4096
s_nop 0
D 86,0
D 86,1024
D 86,2048
D 86,3072
s_mov_b32 m0, 144384
s_nop 0
buffer_load_dword v87, s[36:39], 0 offen sc1 lds
v_add_u32 v88, 1, v88
ds_read_b32 v106, v82 offset:0
s_waitcnt lgkmcnt(0)
v_cmp_gt_i32 vcc, v88, v106
s_waitcnt vmcnt(1)
s_add_u32 s49, s49, 313344
s_cbranch_vccnz CF0_2
BF0_2:
s_barrier
s_add_u32 m0, s50, 32768
R 24,0
D 85,0
R 28,1024
D 85,1024
R 32,2048
R 36,3072
D 85,2048
s_waitcnt lgkmcnt(2)
v_mfma_f32_16x16x32_bf16 v[0:3], a[0:3], v[24:27], 0
R 40,4096
v_mfma_f32_16x16x32_bf16 v[4:7], a[4:7], v[24:27], 0
D 85,3072
v_mfma_f32_16x16x32_bf16 v[8:11], a[8:11], v[24:27], 0
mav 0,12,28
s_add_u32 m0, s50, 36864
R 44,5120
mav 4,16,28
mav 8,20,28
s_waitcnt lgkmcnt(2)
D 86,0
mav 0,24,32
R 48,6144
mav 4,28,32
mav 8,32,32
mav 0,36,36
D 86,1024
mav 4,40,36
R 52,7168
mav 8,44,36
s_waitcnt lgkmcnt(2)
mav 0,48,40
R 24,8192
mav 4,52,40
D 86,2048
mav 8,56,40
mav 0,60,44
R 28,9216
mav 4,64,44
mav 8,68,44
s_waitcnt lgkmcnt(2)
D 86,3072
mav 0,72,48
R 32,10240
mav 4,76,48
mav 8,80,48
mav 0,84,52
R 36,11264
mav 4,88,52
mav 8,92,52
s_waitcnt lgkmcnt(2)
mav 0,96,24
R 40,12288
mav 4,100,24
mav 8,104,24
mav 0,108,28
R 44,13312
mav 4,112,28
mav 8,116,28
s_waitcnt lgkmcnt(2)
mav 0,120,32
R 48,14336
mav 4,124,32
mav 8,128,32
mav 0,132,36
R 52,15360
mav 4,136,36
s_mov_b32 m0, 144384
mav 8,140,36
s_waitcnt lgkmcnt(2)
mav 0,144,40
R 24,16384
mav 4,148,40
buffer_load_dword v87, s[36:39], 0 offen sc1 lds
mav 8,152,40
mav 0,156,44
R 28,17408
mav 4,160,44
v_add_u32 v88, 1, v88
mav 8,164,44
s_waitcnt lgkmcnt(2)
mav 0,168,48
R 32,18432
mav 4,172,48
mav 8,176,48
mav 0,180,52
R 36,19456
mav 4,184,52
mav 8,188,52
s_waitcnt lgkmcnt(2)
mav 0,192,24
R 40,20480
mav 4,196,24
mav 8,200,24
mav 0,204,28
R 44,21504
mav 4,208,28
mav 8,212,28
s_waitcnt lgkmcnt(2)
mav 0,216,32
R 48,22528
mav 4,220,32
mav 8,224,32
mav 0,228,36
R 52,23552
mav 4,232,36
mav 8,236,36
s_waitcnt lgkmcnt(2)
mav 0,240,40
R 24,24576
mav 4,244,40
mav 8,248,40
mav 0,252,44
R 28,25600
mvv 4,128,44
mvv 8,132,44
s_waitcnt lgkmcnt(2)
mvv 0,136,48
R 32,26624
mvv 4,140,48
mvv 8,144,48
mvv 0,148,52
R 36,27648
mvv 4,152,52
mvv 8,156,52
s_waitcnt lgkmcnt(2)
mvv 0,160,24
R 40,28672
mvv 4,164,24
ds_read_b32 v106, v82 offset:0
mvv 8,168,24
mvv 0,172,28
R 44,29696
mvv 4,176,28
mvv 8,180,28
s_waitcnt lgkmcnt(3)
mvv 0,184,32
s_xor_b32 s50, s50, 0x10000
R 48,30720
mvv 4,188,32
mvv 8,192,32
s_waitcnt lgkmcnt(2)
v_cmp_gt_i32 vcc, v88, v106
mvv 0,196,36
R 52,31744
mvv 4,200,36
s_waitcnt vmcnt(1)
s_add_u32 s49, s49, 313344
mvv 8,204,36
s_cbranch_vccnz CF0_3
BF0_3:
s_barrier
mvv 0,208,40
s_add_u32 m0, s50, 0
R 24,32768
mvv 4,212,40
D 85,0
mvv 8,216,40
s_waitcnt lgkmcnt(2)
ds_read_b64 v[78:79], v89 offset:0
mvv 0,220,44
R 28,33792
mvv 4,224,44
mvv 8,228,44
D 85,1024
mvv 0,232,48
R 32,34816
mvv 4,236,48
mvv 8,240,48
s_waitcnt lgkmcnt(2)
mvv 0,244,52
D 85,2048
mvv 4,248,52
R 36,35840
mvv 8,252,52
v_mfma_f32_16x16x32_bf16 v[12:15], a[0:3], v[24:27], 0
R 40,36864
v_fma_f32 v104, v79, v56, v92
v_mfma_f32_16x16x32_bf16 v[16:19], a[4:7], v[24:27], 0
D 85,3072
v_mfma_f32_16x16x32_bf16 v[20:23], a[8:11], v[24:27], 0
s_waitcnt lgkmcnt(2)
v_fma_f32 v0, v0, v78, v104
v_fma_f32 v105, v79, v57, v93
mav 12,12,28
s_add_u32 m0, s50, 4096
R 44,37888
v_fma_f32 v1, v1, v78, v105
mav 16,16,28
v_mul_f32_e64 v0, v0, |v0|
v_mul_f32_e64 v1, v1, |v1|
mav 20,20,28
D 86,0
mav 12,24,32
R 48,38912
v_cvt_pk_bf16_f32 v0, v0, v1
mav 16,28,32
v_pk_max_i16 v68, v0, 0
v_fma_f32 v104, v79, v58, v94
mav 20,32,32
s_waitcnt lgkmcnt(2)
v_fma_f32 v2, v2, v78, v104
v_fma_f32 v105, v79, v59, v95
mav 12,36,36
D 86,1024
mav 16,40,36
R 52,39936
v_fma_f32 v3, v3, v78, v105
mav 20,44,36
v_mul_f32_e64 v2, v2, |v2|
v_mul_f32_e64 v3, v3, |v3|
mav 12,48,40
R 24,40960
v_cvt_pk_bf16_f32 v2, v2, v3
mav 16,52,40
D 86,2048
mav 20,56,40
s_waitcnt lgkmcnt(2)
v_pk_max_i16 v69, v2, 0
v_fma_f32 v104, v79, v60, v96
mav 12,60,44
R 28,41984
v_fma_f32 v4, v4, v78, v104
mav 16,64,44
v_fma_f32 v105, v79, v61, v97
v_fma_f32 v5, v5, v78, v105
mav 20,68,44
D 86,3072
mav 12,72,48
R 32,43008
v_mul_f32_e64 v4, v4, |v4|
mav 16,76,48
v_mul_f32_e64 v5, v5, |v5|
v_cvt_pk_bf16_f32 v4, v4, v5
mav 20,80,48
s_waitcnt lgkmcnt(2)
v_pk_max_i16 v70, v4, 0
v_fma_f32 v104, v79, v62, v98
mav 12,84,52
R 36,44032
v_fma_f32 v6, v6, v78, v104
mav 16,88,52
v_fma_f32 v105, v79, v63, v99
v_fma_f32 v7, v7, v78, v105
mav 20,92,52
v_mul_f32_e64 v6, v6, |v6|
v_mul_f32_e64 v7, v7, |v7|
mav 12,96,24
R 40,45056
v_cvt_pk_bf16_f32 v6, v6, v7
mav 16,100,24
v_pk_max_i16 v71, v6, 0
v_fma_f32 v104, v79, v64, v100
mav 20,104,24
s_waitcnt lgkmcnt(2)
v_fma_f32 v8, v8, v78, v104
v_fma_f32 v105, v79, v65, v101
LsF2:
s_cbranch_execz LsF6
mav 12,108,28
R 44,46080
v_fma_f32 v9, v9, v78, v105
mav 16,112,28
v_mul_f32_e64 v8, v8, |v8|
v_mul_f32_e64 v9, v9, |v9|
mav 20,116,28
buffer_store_dwordx4 v[68:71], v83, s[36:39], s49 offen sc1
v_cvt_pk_bf16_f32 v8, v8, v9
v_pk_max_i16 v72, v8, 0
v_pk_max_i16 v74, v8, 0
mav 12,120,32
R 48,47104
v_fma_f32 v104, v79, v66, v102
mav 16,124,32
v_fma_f32 v10, v10, v78, v104
v_fma_f32 v105, v79, v67, v103
mav 20,128,32
s_waitcnt lgkmcnt(2)
v_fma_f32 v11, v11, v78, v105
v_mul_f32_e64 v10, v10, |v10|
mav 12,132,36
R 52,48128
v_mul_f32_e64 v11, v11, |v11|
mav 16,136,36
s_mov_b32 m0, 144384
v_cvt_pk_bf16_f32 v10, v10, v11
v_pk_max_i16 v73, v10, 0
mav 20,140,36
v_pk_max_i16 v75, v10, 0
mav 12,144,40
R 24,49152
mav 16,148,40
buffer_load_dword v87, s[36:39], 0 offen sc1 lds
mav 20,152,40
s_waitcnt lgkmcnt(2)
v_permlane16_swap_b32 v72, v74
mav 12,156,44
R 28,50176
mav 16,160,44
v_permlane16_swap_b32 v73, v75
mav 20,164,44
buffer_store_dwordx4 v[72:75], v84, s[36:39], s49 offen sc1
v_add_u32 v88, 1, v88
mav 12,168,48
R 32,51200
mav 16,172,48
mav 20,176,48
s_waitcnt lgkmcnt(2)
mav 12,180,52
R 36,52224
mav 16,184,52
mav 20,188,52
mav 12,192,24
R 40,53248
mav 16,196,24
mav 20,200,24
s_waitcnt lgkmcnt(2)
mav 12,204,28
R 44,54272
mav 16,208,28
mav 20,212,28
mav 12,216,32
R 48,55296
mav 16,220,32
mav 20,224,32
s_waitcnt lgkmcnt(2)
mav 12,228,36
R 52,56320
mav 16,232,36
mav 20,236,36
mav 12,240,40
R 24,57344
mav 16,244,40
mav 20,248,40
s_waitcnt lgkmcnt(2)
mav 12,252,44
R 28,58368
mvv 16,128,44
mvv 20,132,44
mvv 12,136,48
R 32,59392
mvv 16,140,48
mvv 20,144,48
s_waitcnt lgkmcnt(2)
mvv 12,148,52
R 36,60416
mvv 16,152,52
mvv 20,156,52
mvv 12,160,24
s_mov_b32 s60, -30
.p2align 6
LF0_4:
mvv 16,164,24
R 40,61440
mvv 20,168,24
s_waitcnt lgkmcnt(2)
ds_read_b32 v106, v82 offset:0
mvv 12,172,28
R 44,62464
mvv 16,176,28
v_add_u32 v76, 1, v76
mvv 20,180,28
mvv 12,184,32
R 48,63488
mvv 16,188,32
mvv 20,192,32
s_waitcnt lgkmcnt(2)
v_cmp_gt_i32 vcc, v88, v106
mvv 12,196,36
R 52,64512
v_xor_b32 v80, 0x10000, v80
mvv 16,200,36
s_waitcnt vmcnt(3)
s_add_u32 s49, s49, 313344
mvv 20,204,36
s_cbranch_vccnz CF0_5
BF0_5:
s_barrier
mvv 12,208,40
s_add_u32 m0, s50, 32768
R 24,0
mvv 16,212,40
D 85,0
mvv 20,216,40
s_waitcnt lgkmcnt(2)
ds_read_b64 v[78:79], v89 offset:128
mvv 12,220,44
R 28,1024
mvv 16,224,44
mvv 20,228,44
D 85,1024
mvv 12,232,48
R 32,2048
mvv 16,236,48
mvv 20,240,48
s_waitcnt lgkmcnt(2)
mvv 12,244,52
D 85,2048
mvv 16,248,52
R 36,3072
mvv 20,252,52
v_mfma_f32_16x16x32_bf16 v[0:3], a[0:3], v[24:27], 0
R 40,4096
v_fma_f32 v104, v79, v56, v92
v_mfma_f32_16x16x32_bf16 v[4:7], a[4:7], v[24:27], 0
D 85,3072
v_mfma_f32_16x16x32_bf16 v[8:11], a[8:11], v[24:27], 0
s_waitcnt lgkmcnt(2)
v_fma_f32 v12, v12, v78, v104
v_fma_f32 v105, v79, v57, v93
mav 0,12,28
s_add_u32 m0, s50, 36864
R 44,5120
v_fma_f32 v13, v13, v78, v105
mav 4,16,28
s_waitcnt vmcnt(4)
v_mul_f32_e64 v12, v12, |v12|
v_mul_f32_e64 v13, v13, |v13|
mav 8,20,28
buffer_store_dword v76, v81, s[36:39], s52 offen sc1
mav 0,24,32
R 48,6144
v_cvt_pk_bf16_f32 v12, v12, v13
mav 4,28,32
v_pk_max_i16 v68, v12, 0
v_fma_f32 v104, v79, v58, v94
mav 8,32,32
s_waitcnt lgkmcnt(2)
v_fma_f32 v14, v14, v78, v104
v_fma_f32 v105, v79, v59, v95
mav 0,36,36
D 86,0
mav 4,40,36
R 52,7168
v_fma_f32 v15, v15, v78, v105
mav 8,44,36
v_mul_f32_e64 v14, v14, |v14|
v_mul_f32_e64 v15, v15, |v15|
mav 0,48,40
R 24,8192
v_cvt_pk_bf16_f32 v14, v14, v15
mav 4,52,40
D 86,1024
)"
R"(
mav 8,56,40
s_waitcnt lgkmcnt(2)
v_pk_max_i16 v69, v14, 0
v_fma_f32 v104, v79, v60, v96
mav 0,60,44
R 28,9216
v_fma_f32 v16, v16, v78, v104
mav 4,64,44
v_fma_f32 v105, v79, v61, v97
v_fma_f32 v17, v17, v78, v105
mav 8,68,44
D 86,2048
mav 0,72,48
R 32,10240
v_mul_f32_e64 v16, v16, |v16|
mav 4,76,48
v_mul_f32_e64 v17, v17, |v17|
v_cvt_pk_bf16_f32 v16, v16, v17
mav 8,80,48
s_waitcnt lgkmcnt(2)
v_pk_max_i16 v70, v16, 0
v_fma_f32 v104, v79, v62, v98
mav 0,84,52
D 86,3072
mav 4,88,52
R 36,11264
v_fma_f32 v18, v18, v78, v104
mav 8,92,52
v_fma_f32 v105, v79, v63, v99
v_fma_f32 v19, v19, v78, v105
mav 0,96,24
R 40,12288
v_mul_f32_e64 v18, v18, |v18|
mav 4,100,24
v_mul_f32_e64 v19, v19, |v19|
v_cvt_pk_bf16_f32 v18, v18, v19
mav 8,104,24
s_waitcnt lgkmcnt(2)
v_pk_max_i16 v71, v18, 0
v_fma_f32 v104, v79, v64, v100
mav 0,108,28
R 44,13312
v_fma_f32 v20, v20, v78, v104
mav 4,112,28
v_fma_f32 v105, v79, v65, v101
v_fma_f32 v21, v21, v78, v105
mav 8,116,28
buffer_store_dwordx4 v[68:71], v83, s[36:39], s49 offen sc1
v_mul_f32_e64 v20, v20, |v20|
v_mul_f32_e64 v21, v21, |v21|
v_cvt_pk_bf16_f32 v20, v20, v21
mav 0,120,32
R 48,14336
v_pk_max_i16 v72, v20, 0
mav 4,124,32
v_pk_max_i16 v74, v20, 0
v_fma_f32 v104, v79, v66, v102
mav 8,128,32
s_waitcnt lgkmcnt(2)
v_fma_f32 v22, v22, v78, v104
v_fma_f32 v105, v79, v67, v103
mav 0,132,36
R 52,15360
v_fma_f32 v23, v23, v78, v105
mav 4,136,36
s_mov_b32 m0, 144384
v_mul_f32_e64 v22, v22, |v22|
v_mul_f32_e64 v23, v23, |v23|
mav 8,140,36
v_cvt_pk_bf16_f32 v22, v22, v23
v_pk_max_i16 v73, v22, 0
mav 0,144,40
R 24,16384
v_pk_max_i16 v75, v22, 0
mav 4,148,40
buffer_load_dword v87, s[36:39], 0 offen sc1 lds
mav 8,152,40
s_waitcnt lgkmcnt(2)
v_permlane16_swap_b32 v72, v74
mav 0,156,44
R 28,17408
mav 4,160,44
v_permlane16_swap_b32 v73, v75
mav 8,164,44
buffer_store_dwordx4 v[72:75], v84, s[36:39], s49 offen sc1
v_add_u32 v88, 1, v88
mav 0,168,48
R 32,18432
mav 4,172,48
mav 8,176,48
s_waitcnt lgkmcnt(2)
mav 0,180,52
R 36,19456
mav 4,184,52
mav 8,188,52
mav 0,192,24
R 40,20480
mav 4,196,24
mav 8,200,24
s_waitcnt lgkmcnt(2)
mav 0,204,28
R 44,21504
mav 4,208,28
mav 8,212,28
mav 0,216,32
R 48,22528
mav 4,220,32
mav 8,224,32
s_waitcnt lgkmcnt(2)
mav 0,228,36
R 52,23552
mav 4,232,36
mav 8,236,36
mav 0,240,40
R 24,24576
mav 4,244,40
mav 8,248,40
s_waitcnt lgkmcnt(2)
mav 0,252,44
R 28,25600
mvv 4,128,44
mvv 8,132,44
mvv 0,136,48
R 32,26624
mvv 4,140,48
mvv 8,144,48
s_waitcnt lgkmcnt(2)
mvv 0,148,52
R 36,27648
mvv 4,152,52
mvv 8,156,52
mvv 0,160,24
R 40,28672
mvv 4,164,24
ds_read_b32 v106, v82 offset:0
mvv 8,168,24
s_waitcnt lgkmcnt(3)
mvv 0,172,28
R 44,29696
mvv 4,176,28
v_add_u32 v76, 1, v76
mvv 8,180,28
mvv 0,184,32
s_xor_b32 s50, s50, 0x10000
R 48,30720
mvv 4,188,32
mvv 8,192,32
s_waitcnt lgkmcnt(2)
v_cmp_gt_i32 vcc, v88, v106
mvv 0,196,36
R 52,31744
mvv 4,200,36
s_waitcnt vmcnt(3)
s_add_u32 s49, s49, 313344
mvv 8,204,36
s_cbranch_vccnz CF0_6
BF0_6:
s_barrier
mvv 0,208,40
s_add_u32 m0, s50, 0
R 24,32768
mvv 4,212,40
D 85,0
mvv 8,216,40
s_waitcnt lgkmcnt(2)
ds_read_b64 v[78:79], v89 offset:0
mvv 0,220,44
R 28,33792
mvv 4,224,44
mvv 8,228,44
D 85,1024
mvv 0,232,48
R 32,34816
mvv 4,236,48
mvv 8,240,48
s_waitcnt lgkmcnt(2)
mvv 0,244,52
D 85,2048
mvv 4,248,52
R 36,35840
mvv 8,252,52
v_mfma_f32_16x16x32_bf16 v[12:15], a[0:3], v[24:27], 0
R 40,36864
v_fma_f32 v104, v79, v56, v92
v_mfma_f32_16x16x32_bf16 v[16:19], a[4:7], v[24:27], 0
D 85,3072
v_mfma_f32_16x16x32_bf16 v[20:23], a[8:11], v[24:27], 0
s_waitcnt lgkmcnt(2)
v_fma_f32 v0, v0, v78, v104
v_fma_f32 v105, v79, v57, v93
mav 12,12,28
s_add_u32 m0, s50, 4096
R 44,37888
v_fma_f32 v1, v1, v78, v105
mav 16,16,28
s_waitcnt vmcnt(4)
v_mul_f32_e64 v0, v0, |v0|
v_mul_f32_e64 v1, v1, |v1|
mav 20,20,28
buffer_store_dword v76, v81, s[36:39], s52 offen sc1
mav 12,24,32
R 48,38912
v_cvt_pk_bf16_f32 v0, v0, v1
mav 16,28,32
v_pk_max_i16 v68, v0, 0
v_fma_f32 v104, v79, v58, v94
mav 20,32,32
s_waitcnt lgkmcnt(2)
v_fma_f32 v2, v2, v78, v104
v_fma_f32 v105, v79, v59, v95
mav 12,36,36
D 86,0
mav 16,40,36
R 52,39936
v_fma_f32 v3, v3, v78, v105
mav 20,44,36
v_mul_f32_e64 v2, v2, |v2|
v_mul_f32_e64 v3, v3, |v3|
mav 12,48,40
R 24,40960
v_cvt_pk_bf16_f32 v2, v2, v3
mav 16,52,40
D 86,1024
mav 20,56,40
s_waitcnt lgkmcnt(2)
v_pk_max_i16 v69, v2, 0
v_fma_f32 v104, v79, v60, v96
mav 12,60,44
R 28,41984
v_fma_f32 v4, v4, v78, v104
mav 16,64,44
v_fma_f32 v105, v79, v61, v97
v_fma_f32 v5, v5, v78, v105
mav 20,68,44
D 86,2048
mav 12,72,48
R 32,43008
v_mul_f32_e64 v4, v4, |v4|
mav 16,76,48
v_mul_f32_e64 v5, v5, |v5|
v_cvt_pk_bf16_f32 v4, v4, v5
mav 20,80,48
s_waitcnt lgkmcnt(2)
v_pk_max_i16 v70, v4, 0
v_fma_f32 v104, v79, v62, v98
mav 12,84,52
D 86,3072
mav 16,88,52
R 36,44032
v_fma_f32 v6, v6, v78, v104
mav 20,92,52
v_fma_f32 v105, v79, v63, v99
v_fma_f32 v7, v7, v78, v105
mav 12,96,24
R 40,45056
v_mul_f32_e64 v6, v6, |v6|
mav 16,100,24
v_mul_f32_e64 v7, v7, |v7|
v_cvt_pk_bf16_f32 v6, v6, v7
mav 20,104,24
s_waitcnt lgkmcnt(2)
v_pk_max_i16 v71, v6, 0
v_fma_f32 v104, v79, v64, v100
mav 12,108,28
R 44,46080
v_fma_f32 v8, v8, v78, v104
mav 16,112,28
v_fma_f32 v105, v79, v65, v101
v_fma_f32 v9, v9, v78, v105
mav 20,116,28
buffer_store_dwordx4 v[68:71], v83, s[36:39], s49 offen sc1
v_mul_f32_e64 v8, v8, |v8|
v_mul_f32_e64 v9, v9, |v9|
v_cvt_pk_bf16_f32 v8, v8, v9
mav 12,120,32
R 48,47104
v_pk_max_i16 v72, v8, 0
mav 16,124,32
v_pk_max_i16 v74, v8, 0
v_fma_f32 v104, v79, v66, v102
mav 20,128,32
s_waitcnt lgkmcnt(2)
v_fma_f32 v10, v10, v78, v104
v_fma_f32 v105, v79, v67, v103
mav 12,132,36
R 52,48128
v_fma_f32 v11, v11, v78, v105
mav 16,136,36
s_mov_b32 m0, 144384
v_mul_f32_e64 v10, v10, |v10|
v_mul_f32_e64 v11, v11, |v11|
mav 20,140,36
v_cvt_pk_bf16_f32 v10, v10, v11
v_pk_max_i16 v73, v10, 0
mav 12,144,40
R 24,49152
v_pk_max_i16 v75, v10, 0
mav 16,148,40
buffer_load_dword v87, s[36:39], 0 offen sc1 lds
mav 20,152,40
s_waitcnt lgkmcnt(2)
v_permlane16_swap_b32 v72, v74
mav 12,156,44
R 28,50176
mav 16,160,44
v_permlane16_swap_b32 v73, v75
mav 20,164,44
buffer_store_dwordx4 v[72:75], v84, s[36:39], s49 offen sc1
v_add_u32 v88, 1, v88
mav 12,168,48
R 32,51200
mav 16,172,48
mav 20,176,48
s_waitcnt lgkmcnt(2)
mav 12,180,52
R 36,52224
mav 16,184,52
mav 20,188,52
mav 12,192,24
R 40,53248
mav 16,196,24
mav 20,200,24
s_waitcnt lgkmcnt(2)
mav 12,204,28
R 44,54272
mav 16,208,28
mav 20,212,28
mav 12,216,32
R 48,55296
mav 16,220,32
mav 20,224,32
s_waitcnt lgkmcnt(2)
mav 12,228,36
R 52,56320
mav 16,232,36
mav 20,236,36
mav 12,240,40
R 24,57344
mav 16,244,40
mav 20,248,40
s_waitcnt lgkmcnt(2)
mav 12,252,44
R 28,58368
mvv 16,128,44
mvv 20,132,44
mvv 12,136,48
R 32,59392
mvv 16,140,48
mvv 20,144,48
s_waitcnt lgkmcnt(2)
mvv 12,148,52
R 36,60416
mvv 16,152,52
mvv 20,156,52
s_add_u32 s60, s60, 1
mvv 12,160,24
s_cbranch_scc0 LF0_4
mvv 16,164,24
R 40,61440
mvv 20,168,24
s_waitcnt lgkmcnt(2)
ds_read_b32 v106, v82 offset:0
LsF3:
s_cbranch_execz LsF7
mvv 12,172,28
R 44,62464
mvv 16,176,28
v_add_u32 v76, 1, v76
mvv 20,180,28
mvv 12,184,32
R 48,63488
mvv 16,188,32
mvv 20,192,32
s_waitcnt lgkmcnt(2)
v_cmp_gt_i32 vcc, v88, v106
mvv 12,196,36
R 52,64512
v_xor_b32 v80, 0x10000, v80
mvv 16,200,36
s_waitcnt vmcnt(3)
s_add_u32 s49, s49, 313344
mvv 20,204,36
s_cbranch_vccnz CF0_7
BF0_7:
s_barrier
mvv 12,208,40
s_add_u32 m0, s50, 32768
R 24,0
mvv 16,212,40
D 85,0
mvv 20,216,40
s_waitcnt lgkmcnt(2)
ds_read_b64 v[78:79], v89 offset:128
mvv 12,220,44
R 28,1024
mvv 16,224,44
mvv 20,228,44
D 85,1024
mvv 12,232,48
R 32,2048
mvv 16,236,48
mvv 20,240,48
s_waitcnt lgkmcnt(2)
mvv 12,244,52
D 85,2048
mvv 16,248,52
R 36,3072
mvv 20,252,52
v_mfma_f32_16x16x32_bf16 v[0:3], a[0:3], v[24:27], 0
R 40,4096
v_fma_f32 v104, v79, v56, v92
v_mfma_f32_16x16x32_bf16 v[4:7], a[4:7], v[24:27], 0
D 85,3072
v_mfma_f32_16x16x32_bf16 v[8:11], a[8:11], v[24:27], 0
s_waitcnt lgkmcnt(2)
v_fma_f32 v12, v12, v78, v104
v_fma_f32 v105, v79, v57, v93
mav 0,12,28
s_add_u32 m0, s50, 36864
R 44,5120
v_fma_f32 v13, v13, v78, v105
mav 4,16,28
s_waitcnt vmcnt(4)
v_mul_f32_e64 v12, v12, |v12|
v_mul_f32_e64 v13, v13, |v13|
mav 8,20,28
buffer_store_dword v76, v81, s[36:39], s52 offen sc1
mav 0,24,32
R 48,6144
v_cvt_pk_bf16_f32 v12, v12, v13
mav 4,28,32
v_pk_max_i16 v68, v12, 0
v_fma_f32 v104, v79, v58, v94
mav 8,32,32
s_waitcnt lgkmcnt(2)
v_fma_f32 v14, v14, v78, v104
v_fma_f32 v105, v79, v59, v95
mav 0,36,36
D 86,0
mav 4,40,36
R 52,7168
v_fma_f32 v15, v15, v78, v105
mav 8,44,36
v_mul_f32_e64 v14, v14, |v14|
v_mul_f32_e64 v15, v15, |v15|
mav 0,48,40
R 24,8192
v_cvt_pk_bf16_f32 v14, v14, v15
mav 4,52,40
D 86,1024
mav 8,56,40
s_waitcnt lgkmcnt(2)
v_pk_max_i16 v69, v14, 0
v_fma_f32 v104, v79, v60, v96
mav 0,60,44
R 28,9216
v_fma_f32 v16, v16, v78, v104
mav 4,64,44
v_fma_f32 v105, v79, v61, v97
v_fma_f32 v17, v17, v78, v105
mav 8,68,44
D 86,2048
mav 0,72,48
R 32,10240
v_mul_f32_e64 v16, v16, |v16|
mav 4,76,48
v_mul_f32_e64 v17, v17, |v17|
v_cvt_pk_bf16_f32 v16, v16, v17
mav 8,80,48
s_waitcnt lgkmcnt(2)
v_pk_max_i16 v70, v16, 0
v_fma_f32 v104, v79, v62, v98
mav 0,84,52
D 86,3072
mav 4,88,52
R 36,11264
v_fma_f32 v18, v18, v78, v104
mav 8,92,52
v_fma_f32 v105, v79, v63, v99
v_fma_f32 v19, v19, v78, v105
mav 0,96,24
R 40,12288
v_mul_f32_e64 v18, v18, |v18|
mav 4,100,24
v_mul_f32_e64 v19, v19, |v19|
v_cvt_pk_bf16_f32 v18, v18, v19
mav 8,104,24
s_waitcnt lgkmcnt(2)
v_pk_max_i16 v71, v18, 0
v_fma_f32 v104, v79, v64, v100
mav 0,108,28
R 44,13312
v_fma_f32 v20, v20, v78, v104
mav 4,112,28
v_fma_f32 v105, v79, v65, v101
v_fma_f32 v21, v21, v78, v105
mav 8,116,28
buffer_store_dwordx4 v[68:71], v83, s[36:39], s49 offen sc1
v_mul_f32_e64 v20, v20, |v20|
v_mul_f32_e64 v21, v21, |v21|
v_cvt_pk_bf16_f32 v20, v20, v21
mav 0,120,32
R 48,14336
v_pk_max_i16 v72, v20, 0
mav 4,124,32
v_pk_max_i16 v74, v20, 0
v_fma_f32 v104, v79, v66, v102
mav 8,128,32
s_waitcnt lgkmcnt(2)
v_fma_f32 v22, v22, v78, v104
v_fma_f32 v105, v79, v67, v103
mav 0,132,36
R 52,15360
v_fma_f32 v23, v23, v78, v105
mav 4,136,36
s_mov_b32 m0, 144384
v_mul_f32_e64 v22, v22, |v22|
v_mul_f32_e64 v23, v23, |v23|
mav 8,140,36
v_cvt_pk_bf16_f32 v22, v22, v23
v_pk_max_i16 v73, v22, 0
mav 0,144,40
R 24,16384
v_pk_max_i16 v75, v22, 0
mav 4,148,40
buffer_load_dword v87, s[36:39], 0 offen sc1 lds
mav 8,152,40
s_waitcnt lgkmcnt(2)
v_permlane16_swap_b32 v72, v74
mav 0,156,44
R 28,17408
mav 4,160,44
v_permlane16_swap_b32 v73, v75
mav 8,164,44
buffer_store_dwordx4 v[72:75], v84, s[36:39], s49 offen sc1
v_add_u32 v88, 1, v88
mav 0,168,48
R 32,18432
mav 4,172,48
mav 8,176,48
s_waitcnt lgkmcnt(2)
mav 0,180,52
R 36,19456
mav 4,184,52
mav 8,188,52
mav 0,192,24
R 40,20480
mav 4,196,24
mav 8,200,24
s_waitcnt lgkmcnt(2)
mav 0,204,28
R 44,21504
mav 4,208,28
mav 8,212,28
mav 0,216,32
R 48,22528
mav 4,220,32
mav 8,224,32
s_waitcnt lgkmcnt(2)
mav 0,228,36
R 52,23552
mav 4,232,36
mav 8,236,36
mav 0,240,40
R 24,24576
mav 4,244,40
mav 8,248,40
s_waitcnt lgkmcnt(2)
mav 0,252,44
R 28,25600
mvv 4,128,44
mvv 8,132,44
mvv 0,136,48
R 32,26624
mvv 4,140,48
mvv 8,144,48
s_waitcnt lgkmcnt(2)
mvv 0,148,52
R 36,27648
mvv 4,152,52
mvv 8,156,52
mvv 0,160,24
R 40,28672
mvv 4,164,24
ds_read_b32 v106, v82 offset:0
mvv 8,168,24
s_waitcnt lgkmcnt(3)
mvv 0,172,28
R 44,29696
mvv 4,176,28
v_add_u32 v76, 1, v76
mvv 8,180,28
mvv 0,184,32
R 48,30720
mvv 4,188,32
mvv 8,192,32
s_waitcnt lgkmcnt(2)
v_cmp_gt_i32 vcc, v88, v106
mvv 0,196,36
R 52,31744
mvv 4,200,36
s_waitcnt vmcnt(3)
s_add_u32 s49, s49, 313344
mvv 8,204,36
s_cbranch_vccnz CF0_8
BF0_8:
s_barrier
mvv 0,208,40
R 24,32768
mvv 4,212,40
ds_read_b64 v[78:79], v89 offset:0
mvv 8,216,40
s_waitcnt lgkmcnt(3)
mvv 0,220,44
R 28,33792
mvv 4,224,44
mvv 8,228,44
mvv 0,232,48
R 32,34816
mvv 4,236,48
mvv 8,240,48
s_waitcnt lgkmcnt(2)
mvv 0,244,52
R 36,35840
mvv 4,248,52
mvv 8,252,52
v_mfma_f32_16x16x32_bf16 v[12:15], a[0:3], v[24:27], 0
R 40,36864
v_fma_f32 v104, v79, v56, v92
v_mfma_f32_16x16x32_bf16 v[16:19], a[4:7], v[24:27], 0
v_fma_f32 v105, v79, v57, v93
v_fma_f32 v0, v0, v78, v104
v_mfma_f32_16x16x32_bf16 v[20:23], a[8:11], v[24:27], 0
s_waitcnt lgkmcnt(2)
v_fma_f32 v1, v1, v78, v105
v_mul_f32_e64 v0, v0, |v0|
mav 12,12,28
R 44,37888
v_mul_f32_e64 v1, v1, |v1|
mav 16,16,28
s_waitcnt vmcnt(0)
v_cvt_pk_bf16_f32 v0, v0, v1
v_pk_max_i16 v68, v0, 0
mav 20,20,28
buffer_store_dword v76, v81, s[36:39], s52 offen sc1
mav 12,24,32
R 48,38912
v_fma_f32 v104, v79, v58, v94
mav 16,28,32
v_fma_f32 v2, v2, v78, v104
v_fma_f32 v105, v79, v59, v95
mav 20,32,32
s_waitcnt lgkmcnt(2)
v_fma_f32 v3, v3, v78, v105
v_mul_f32_e64 v2, v2, |v2|
mav 12,36,36
R 52,39936
v_mul_f32_e64 v3, v3, |v3|
mav 16,40,36
v_cvt_pk_bf16_f32 v2, v2, v3
v_pk_max_i16 v69, v2, 0
mav 20,44,36
v_fma_f32 v104, v79, v60, v96
v_fma_f32 v4, v4, v78, v104
mav 12,48,40
R 24,40960
v_fma_f32 v105, v79, v61, v97
mav 16,52,40
v_fma_f32 v5, v5, v78, v105
v_mul_f32_e64 v4, v4, |v4|
mav 20,56,40
s_waitcnt lgkmcnt(2)
v_mul_f32_e64 v5, v5, |v5|
v_cvt_pk_bf16_f32 v4, v4, v5
mav 12,60,44
R 28,41984
v_pk_max_i16 v70, v4, 0
mav 16,64,44
v_fma_f32 v104, v79, v62, v98
v_fma_f32 v6, v6, v78, v104
mav 20,68,44
v_fma_f32 v105, v79, v63, v99
v_fma_f32 v7, v7, v78, v105
mav 12,72,48
R 32,43008
v_mul_f32_e64 v6, v6, |v6|
mav 16,76,48
v_mul_f32_e64 v7, v7, |v7|
v_cvt_pk_bf16_f32 v6, v6, v7
mav 20,80,48
s_waitcnt lgkmcnt(2)
v_pk_max_i16 v71, v6, 0
v_fma_f32 v104, v79, v64, v100
mav 12,84,52
buffer_store_dwordx4 v[68:71], v83, s[36:39], s49 offen sc1
R 36,44032
v_fma_f32 v8, v8, v78, v104
mav 16,88,52
v_fma_f32 v105, v79, v65, v101
v_fma_f32 v9, v9, v78, v105
mav 20,92,52
v_mul_f32_e64 v8, v8, |v8|
v_mul_f32_e64 v9, v9, |v9|
mav 12,96,24
R 40,45056
v_cvt_pk_bf16_f32 v8, v8, v9
mav 16,100,24
v_pk_max_i16 v72, v8, 0
v_pk_max_i16 v74, v8, 0
mav 20,104,24
s_waitcnt lgkmcnt(2)
v_fma_f32 v104, v79, v66, v102
v_fma_f32 v10, v10, v78, v104
mav 12,108,28
R 44,46080
v_fma_f32 v105, v79, v67, v103
mav 16,112,28
v_fma_f32 v11, v11, v78, v105
v_mul_f32_e64 v10, v10, |v10|
mav 20,116,28
v_mul_f32_e64 v11, v11, |v11|
v_cvt_pk_bf16_f32 v10, v10, v11
mav 12,120,32
R 48,47104
v_pk_max_i16 v73, v10, 0
mav 16,124,32
v_pk_max_i16 v75, v10, 0
mav 20,128,32
s_waitcnt lgkmcnt(2)
v_permlane16_swap_b32 v72, v74
mav 12,132,36
R 52,48128
mav 16,136,36
v_permlane16_swap_b32 v73, v75
mav 20,140,36
mav 12,144,40
R 24,49152
mav 16,148,40
buffer_store_dwordx4 v[72:75], v84, s[36:39], s49 offen sc1
mav 20,152,40
s_waitcnt lgkmcnt(2)
mav 12,156,44
R 28,50176
mav 16,160,44
mav 20,164,44
mav 12,168,48
R 32,51200
mav 16,172,48
mav 20,176,48
s_waitcnt lgkmcnt(2)
mav 12,180,52
R 36,52224
mav 16,184,52
mav 20,188,52
mav 12,192,24
R 40,53248
mav 16,196,24
mav 20,200,24
s_waitcnt lgkmcnt(2)
mav 12,204,28
R 44,54272
mav 16,208,28
mav 20,212,28
mav 12,216,32
R 48,55296
mav 16,220,32
mav 20,224,32
s_waitcnt lgkmcnt(2)
mav 12,228,36
R 52,56320
mav 16,232,36
mav 20,236,36
mav 12,240,40
R 24,57344
mav 16,244,40
mav 20,248,40
s_waitcnt lgkmcnt(2)
mav 12,252,44
R 28,58368
mvv 16,128,44
mvv 20,132,44
mvv 12,136,48
R 32,59392
mvv 16,140,48
mvv 20,144,48
s_waitcnt lgkmcnt(2)
mvv 12,148,52
R 36,60416
mvv 16,152,52
mvv 20,156,52
mvv 12,160,24
mvv 16,164,24
R 40,61440
mvv 20,168,24
s_waitcnt lgkmcnt(2)
mvv 12,172,28
R 44,62464
mvv 16,176,28
v_add_u32 v76, 1, v76
mvv 20,180,28
mvv 12,184,32
R 48,63488
mvv 16,188,32
mvv 20,192,32
s_waitcnt lgkmcnt(2)
mvv 12,196,36
R 52,64512
mvv 16,200,36
s_add_u32 s49, s49, 313344
mvv 20,204,36
s_barrier
mvv 12,208,40
mvv 16,212,40
ds_read_b64 v[78:79], v89 offset:128
mvv 20,216,40
s_waitcnt lgkmcnt(2)
mvv 12,220,44
mvv 16,224,44
mvv 20,228,44
mvv 12,232,48
mvv 16,236,48
mvv 20,240,48
s_waitcnt lgkmcnt(0)
mvv 12,244,52
mvv 16,248,52
mvv 20,252,52
v_fma_f32 v104, v79, v56, v92
s_nop 4
v_fma_f32 v12, v12, v78, v104
v_fma_f32 v105, v79, v57, v93
v_fma_f32 v13, v13, v78, v105
v_mul_f32_e64 v12, v12, |v12|
v_mul_f32_e64 v13, v13, |v13|
v_cvt_pk_bf16_f32 v12, v12, v13
v_pk_max_i16 v68, v12, 0
v_fma_f32 v104, v79, v58, v94
v_fma_f32 v14, v14, v78, v104
v_fma_f32 v105, v79, v59, v95
v_fma_f32 v15, v15, v78, v105
v_mul_f32_e64 v14, v14, |v14|
v_mul_f32_e64 v15, v15, |v15|
v_cvt_pk_bf16_f32 v14, v14, v15
v_pk_max_i16 v69, v14, 0
v_fma_f32 v104, v79, v60, v96
v_fma_f32 v16, v16, v78, v104
v_fma_f32 v105, v79, v61, v97
v_fma_f32 v17, v17, v78, v105
v_mul_f32_e64 v16, v16, |v16|
v_mul_f32_e64 v17, v17, |v17|
v_cvt_pk_bf16_f32 v16, v16, v17
v_pk_max_i16 v70, v16, 0
v_fma_f32 v104, v79, v62, v98
v_fma_f32 v18, v18, v78, v104
v_fma_f32 v105, v79, v63, v99
v_fma_f32 v19, v19, v78, v105
v_mul_f32_e64 v18, v18, |v18|
v_mul_f32_e64 v19, v19, |v19|
v_cvt_pk_bf16_f32 v18, v18, v19
v_pk_max_i16 v71, v18, 0
buffer_store_dwordx4 v[68:71], v83, s[36:39], s49 offen sc1
v_fma_f32 v104, v79, v64, v100
v_fma_f32 v20, v20, v78, v104
v_fma_f32 v105, v79, v65, v101
v_fma_f32 v21, v21, v78, v105
v_mul_f32_e64 v20, v20, |v20|
v_mul_f32_e64 v21, v21, |v21|
v_cvt_pk_bf16_f32 v20, v20, v21
v_pk_max_i16 v72, v20, 0
v_pk_max_i16 v74, v20, 0
v_fma_f32 v104, v79, v66, v102
v_fma_f32 v22, v22, v78, v104
v_fma_f32 v105, v79, v67, v103
v_fma_f32 v23, v23, v78, v105
v_mul_f32_e64 v22, v22, |v22|
v_mul_f32_e64 v23, v23, |v23|
v_cvt_pk_bf16_f32 v22, v22, v23
v_pk_max_i16 v73, v22, 0
v_pk_max_i16 v75, v22, 0
v_permlane16_swap_b32 v72, v74
s_nop 0
v_permlane16_swap_b32 v73, v75
buffer_store_dwordx4 v[72:75], v84, s[36:39], s49 offen sc1
s_waitcnt vmcnt(2)
buffer_store_dword v76, v81, s[36:39], s52 offen sc1
v_add_u32 v76, 37, v76
s_waitcnt vmcnt(1)
buffer_store_dword v76, v81, s[36:39], s52 offen sc1
s_endpgm
CF0_1:
s_cmp_eq_u32 s61, 0
s_cbranch_scc1 BF0_1
s_sub_u32 s61, s61, 1
s_sleep 8
buffer_load_dwordx2 v[106:107], v87, s[36:39], 0 offen sc1
s_waitcnt vmcnt(0)
ds_write_b32 v82, v106
v_sub_u32 v106, v106, v88
v_xor_b32 v107, v107, v77
v_cmp_lt_u64 vcc, s[62:63], v[106:107]
s_waitcnt lgkmcnt(0)
s_cbranch_vccnz CF0_1
s_branch BF0_1
CF0_2:
s_mov_b32 s58, m0
s_mov_b32 m0, 144384
DF0_2:
s_cmp_eq_u32 s61, 0
s_cbranch_scc1 EF0_2
s_sub_u32 s61, s61, 1
buffer_load_dword v87, s[36:39], 0 offen sc1 lds
s_sleep 1
ds_read_b32 v106, v82
s_waitcnt lgkmcnt(0)
v_cmp_gt_i32 vcc, v88, v106
s_cbranch_vccnz DF0_2
EF0_2:
s_mov_b32 m0, s58
s_branch BF0_2
CF0_3:
s_mov_b32 s58, m0
s_mov_b32 m0, 144384
DF0_3:
s_cmp_eq_u32 s61, 0
s_cbranch_scc1 EF0_3
s_sub_u32 s61, s61, 1
buffer_load_dword v87, s[36:39], 0 offen sc1 lds
s_sleep 1
ds_read_b32 v106, v82
LsF4:
s_cbranch_execz LsF8
s_waitcnt lgkmcnt(0)
v_cmp_gt_i32 vcc, v88, v106
s_cbranch_vccnz DF0_3
EF0_3:
s_mov_b32 m0, s58
s_branch BF0_3
CF0_5:
s_mov_b32 s58, m0
s_mov_b32 m0, 144384
DF0_5:
s_cmp_eq_u32 s61, 0
s_cbranch_scc1 EF0_5
s_sub_u32 s61, s61, 1
buffer_load_dword v87, s[36:39], 0 offen sc1 lds
s_sleep 1
ds_read_b32 v106, v82
s_waitcnt lgkmcnt(0)
v_cmp_gt_i32 vcc, v88, v106
s_cbranch_vccnz DF0_5
EF0_5:
s_mov_b32 m0, s58
s_branch BF0_5
CF0_6:
s_mov_b32 s58, m0
s_mov_b32 m0, 144384
DF0_6:
s_cmp_eq_u32 s61, 0
s_cbranch_scc1 EF0_6
s_sub_u32 s61, s61, 1
buffer_load_dword v87, s[36:39], 0 offen sc1 lds
s_sleep 1
ds_read_b32 v106, v82
s_waitcnt lgkmcnt(0)
v_cmp_gt_i32 vcc, v88, v106
s_cbranch_vccnz DF0_6
EF0_6:
s_mov_b32 m0, s58
s_branch BF0_6
CF0_7:
s_mov_b32 s58, m0
s_mov_b32 m0, 144384
DF0_7:
s_cmp_eq_u32 s61, 0
s_cbranch_scc1 EF0_7
s_sub_u32 s61, s61, 1
buffer_load_dword v87, s[36:39], 0 offen sc1 lds
s_sleep 1
ds_read_b32 v106, v82
s_waitcnt lgkmcnt(0)
v_cmp_gt_i32 vcc, v88, v106
s_cbranch_vccnz DF0_7
EF0_7:
s_mov_b32 m0, s58
s_branch BF0_7
CF0_8:
s_mov_b32 s58, m0
s_mov_b32 m0, 144384
DF0_8:
s_cmp_eq_u32 s61, 0
s_cbranch_scc1 EF0_8
s_sub_u32 s61, s61, 1
buffer_load_dword v87, s[36:39], 0 offen sc1 lds
s_sleep 1
ds_read_b32 v106, v82
s_waitcnt lgkmcnt(0)
v_cmp_gt_i32 vcc, v88, v106
s_cbranch_vccnz DF0_8
EF0_8:
s_mov_b32 m0, s58
s_branch BF0_8
LvF1:
LsF5:
s_cbranch_execz LsF9
s_add_u32 s49, s49, 313344
s_barrier
s_add_u32 m0, s50, 0
s_nop 0
D 85,0
D 85,1024
D 85,2048
D 85,3072
s_add_u32 m0, s50, 4096
s_nop 0
D 86,0
D 86,1024
D 86,2048
D 86,3072
s_waitcnt vmcnt(0)
s_add_u32 s49, s49, 313344
s_barrier
s_add_u32 m0, s50, 32768
R 24,0
buffer_load_dwordx2 v[108:109], v90, s[36:39], s49 offen sc1
buffer_load_dwordx2 v[110:111], v90, s[36:39], s49 offen offset:512 sc1
D 85,0
R 28,1024
D 85,1024
R 32,2048
R 36,3072
D 85,2048
s_waitcnt lgkmcnt(2)
v_mfma_f32_16x16x32_bf16 v[0:3], a[0:3], v[24:27], 0
R 40,4096
v_mfma_f32_16x16x32_bf16 v[4:7], a[4:7], v[24:27], 0
v_mfma_f32_16x16x32_bf16 v[8:11], a[8:11], v[24:27], 0
mav 0,12,28
R 44,5120
mav 4,16,28
D 85,3072
mav 8,20,28
s_waitcnt lgkmcnt(2)
s_add_u32 m0, s50, 36864
mav 0,24,32
R 48,6144
mav 4,28,32
mav 8,32,32
D 86,0
mav 0,36,36
R 52,7168
mav 4,40,36
mav 8,44,36
s_waitcnt lgkmcnt(2)
mav 0,48,40
D 86,1024
mav 4,52,40
R 24,8192
mav 8,56,40
mav 0,60,44
R 28,9216
mav 4,64,44
D 86,2048
mav 8,68,44
s_waitcnt lgkmcnt(2)
mav 0,72,48
R 32,10240
mav 4,76,48
mav 8,80,48
D 86,3072
mav 0,84,52
R 36,11264
mav 4,88,52
mav 8,92,52
s_waitcnt lgkmcnt(2)
mav 0,96,24
R 40,12288
mav 4,100,24
mav 8,104,24
mav 0,108,28
R 44,13312
mav 4,112,28
mav 8,116,28
s_waitcnt lgkmcnt(2)
mav 0,120,32
R 48,14336
mav 4,124,32
mav 8,128,32
mav 0,132,36
R 52,15360
mav 4,136,36
mav 8,140,36
s_waitcnt lgkmcnt(2)
mav 0,144,40
R 24,16384
mav 4,148,40
mav 8,152,40
mav 0,156,44
R 28,17408
mav 4,160,44
mav 8,164,44
s_waitcnt lgkmcnt(2)
mav 0,168,48
R 32,18432
mav 4,172,48
mav 8,176,48
mav 0,180,52
R 36,19456
mav 4,184,52
mav 8,188,52
s_waitcnt lgkmcnt(2)
mav 0,192,24
R 40,20480
mav 4,196,24
mav 8,200,24
s_waitcnt vmcnt(8)
v_add_f32 v108, v108, v110
v_add_f32 v109, v109, v111
mav 0,204,28
R 44,21504
mav 4,208,28
v_permlane32_swap_b32 v108, v109
mav 8,212,28
s_waitcnt lgkmcnt(2)
v_add_f32 v110, v108, v109
v_add_f32 v111, v108, v109
mav 0,216,32
R 48,22528
mav 4,220,32
v_permlane16_swap_b32 v110, v111
mav 8,224,32
v_add_f32 v108, v110, v111
v_add_f32 v109, v110, v111
mav 0,228,36
R 52,23552
mav 4,232,36
v_permlane32_swap_b32 v108, v109
mav 8,236,36
s_waitcnt lgkmcnt(2)
v_mul_f32 v110, 0x3a800000, v108
v_mul_f32 v111, 0x3a800000, v109
mav 0,240,40
R 24,24576
v_fma_f32 v111, -v110, v110, v111
mav 4,244,40
v_max_f32 v111, 0, v111
v_add_f32 v111, 0x3727c5ac, v111
mav 8,248,40
v_rsq_f32 v108, v111
mav 0,252,44
R 28,25600
v_mul_f32_e64 v109, -v110, v108
mvv 4,128,44
mvv 8,132,44
s_waitcnt lgkmcnt(2)
mvv 0,136,48
R 32,26624
mvv 4,140,48
mvv 8,144,48
mvv 0,148,52
R 36,27648
mvv 4,152,52
mvv 8,156,52
s_waitcnt lgkmcnt(2)
mvv 0,160,24
R 40,28672
mvv 4,164,24
mvv 8,168,24
mvv 0,172,28
R 44,29696
mvv 4,176,28
mvv 8,180,28
s_waitcnt lgkmcnt(2)
ds_write_b64 v89, v[108:109] offset:0
mvv 0,184,32
s_xor_b32 s50, s50, 0x10000
R 48,30720
mvv 4,188,32
mvv 8,192,32
mvv 0,196,36
R 52,31744
mvv 4,200,36
s_waitcnt vmcnt(0) lgkmcnt(2)
s_add_u32 s49, s49, 313344
mvv 8,204,36
s_barrier
mvv 0,208,40
buffer_load_dwordx2 v[108:109], v90, s[36:39], s49 offen sc1
mvv 4,212,40
s_add_u32 m0, s50, 0
R 24,32768
mvv 8,216,40
ds_read_b64 v[78:79], v89 offset:0
mvv 0,220,44
R 28,33792
mvv 4,224,44
buffer_load_dwordx2 v[110:111], v90, s[36:39], s49 offen offset:512 sc1
mvv 8,228,44
s_waitcnt lgkmcnt(3)
mvv 0,232,48
R 32,34816
mvv 4,236,48
mvv 8,240,48
D 85,0
mvv 0,244,52
R 36,35840
mvv 4,248,52
mvv 8,252,52
s_waitcnt lgkmcnt(2)
v_mfma_f32_16x16x32_bf16 v[12:15], a[0:3], v[24:27], 0
D 85,1024
v_mfma_f32_16x16x32_bf16 v[16:19], a[4:7], v[24:27], 0
R 40,36864
v_fma_f32 v104, v79, v56, v92
v_mfma_f32_16x16x32_bf16 v[20:23], a[8:11], v[24:27], 0
v_fma_f32 v0, v0, v78, v104
v_fma_f32 v105, v79, v57, v93
mav 12,12,28
R 44,37888
v_fma_f32 v1, v1, v78, v105
mav 16,16,28
D 85,2048
mav 20,20,28
s_waitcnt lgkmcnt(2)
v_mul_f32_e64 v0, v0, |v0|
v_mul_f32_e64 v1, v1, |v1|
mav 12,24,32
R 48,38912
v_cvt_pk_bf16_f32 v0, v0, v1
mav 16,28,32
v_pk_max_i16 v68, v0, 0
v_fma_f32 v104, v79, v58, v94
mav 20,32,32
D 85,3072
mav 12,36,36
s_add_u32 m0, s50, 4096
R 52,39936
v_fma_f32 v2, v2, v78, v104
mav 16,40,36
v_fma_f32 v105, v79, v59, v95
v_fma_f32 v3, v3, v78, v105
mav 20,44,36
s_waitcnt lgkmcnt(2)
v_mul_f32_e64 v2, v2, |v2|
v_mul_f32_e64 v3, v3, |v3|
mav 12,48,40
D 86,0
mav 16,52,40
R 24,40960
v_cvt_pk_bf16_f32 v2, v2, v3
mav 20,56,40
v_pk_max_i16 v69, v2, 0
v_fma_f32 v104, v79, v60, v96
mav 12,60,44
R 28,41984
v_fma_f32 v4, v4, v78, v104
mav 16,64,44
D 86,1024
mav 20,68,44
s_waitcnt lgkmcnt(2)
v_fma_f32 v105, v79, v61, v97
v_fma_f32 v5, v5, v78, v105
mav 12,72,48
R 32,43008
v_mul_f32_e64 v4, v4, |v4|
mav 16,76,48
v_mul_f32_e64 v5, v5, |v5|
v_cvt_pk_bf16_f32 v4, v4, v5
mav 20,80,48
D 86,2048
mav 12,84,52
R 36,44032
v_pk_max_i16 v70, v4, 0
mav 16,88,52
v_fma_f32 v104, v79, v62, v98
v_fma_f32 v6, v6, v78, v104
mav 20,92,52
s_waitcnt lgkmcnt(2)
v_fma_f32 v105, v79, v63, v99
v_fma_f32 v7, v7, v78, v105
mav 12,96,24
D 86,3072
mav 16,100,24
R 40,45056
v_mul_f32_e64 v6, v6, |v6|
mav 20,104,24
v_mul_f32_e64 v7, v7, |v7|
v_cvt_pk_bf16_f32 v6, v6, v7
mav 12,108,28
R 44,46080
v_pk_max_i16 v71, v6, 0
mav 16,112,28
buffer_store_dwordx4 v[68:71], v83, s[36:39], s49 offen sc1
v_fma_f32 v104, v79, v64, v100
v_fma_f32 v8, v8, v78, v104
v_fma_f32 v105, v79, v65, v101
mav 20,116,28
s_waitcnt lgkmcnt(2)
v_fma_f32 v9, v9, v78, v105
v_mul_f32_e64 v8, v8, |v8|
mav 12,120,32
R 48,47104
v_mul_f32_e64 v9, v9, |v9|
mav 16,124,32
v_cvt_pk_bf16_f32 v8, v8, v9
v_pk_max_i16 v72, v8, 0
mav 20,128,32
v_pk_max_i16 v74, v8, 0
v_fma_f32 v104, v79, v66, v102
mav 12,132,36
R 52,48128
v_fma_f32 v10, v10, v78, v104
mav 16,136,36
v_fma_f32 v105, v79, v67, v103
v_fma_f32 v11, v11, v78, v105
mav 20,140,36
s_waitcnt lgkmcnt(2)
v_mul_f32_e64 v10, v10, |v10|
v_mul_f32_e64 v11, v11, |v11|
mav 12,144,40
R 24,49152
v_cvt_pk_bf16_f32 v10, v10, v11
mav 16,148,40
v_pk_max_i16 v73, v10, 0
v_pk_max_i16 v75, v10, 0
mav 20,152,40
v_permlane16_swap_b32 v72, v74
mav 12,156,44
R 28,50176
mav 16,160,44
v_permlane16_swap_b32 v73, v75
mav 20,164,44
s_waitcnt lgkmcnt(2)
mav 12,168,48
R 32,51200
mav 16,172,48
mav 20,176,48
buffer_store_dwordx4 v[72:75], v84, s[36:39], s49 offen sc1
mav 12,180,52
R 36,52224
mav 16,184,52
mav 20,188,52
s_waitcnt lgkmcnt(2)
mav 12,192,24
R 40,53248
mav 16,196,24
mav 20,200,24
s_waitcnt vmcnt(10)
v_add_f32 v108, v108, v110
v_add_f32 v109, v109, v111
mav 12,204,28
R 44,54272
mav 16,208,28
v_permlane32_swap_b32 v108, v109
mav 20,212,28
s_waitcnt lgkmcnt(2)
v_add_f32 v110, v108, v109
v_add_f32 v111, v108, v109
mav 12,216,32
R 48,55296
mav 16,220,32
v_permlane16_swap_b32 v110, v111
mav 20,224,32
v_add_f32 v108, v110, v111
v_add_f32 v109, v110, v111
mav 12,228,36
R 52,56320
mav 16,232,36
v_permlane32_swap_b32 v108, v109
mav 20,236,36
s_waitcnt lgkmcnt(2)
v_mul_f32 v110, 0x3a800000, v108
v_mul_f32 v111, 0x3a800000, v109
mav 12,240,40
R 24,57344
v_fma_f32 v111, -v110, v110, v111
mav 16,244,40
v_max_f32 v111, 0, v111
v_add_f32 v111, 0x3727c5ac, v111
mav 20,248,40
v_rsq_f32 v108, v111
mav 12,252,44
R 28,58368
v_mul_f32_e64 v109, -v110, v108
mvv 16,128,44
mvv 20,132,44
s_waitcnt lgkmcnt(2)
mvv 12,136,48
R 32,59392
mvv 16,140,48
mvv 20,144,48
mvv 12,148,52
R 36,60416
mvv 16,152,52
mvv 20,156,52
s_waitcnt lgkmcnt(2)
mvv 12,160,24
mvv 16,164,24
R 40,61440
mvv 20,168,24
mvv 12,172,28
R 44,62464
mvv 16,176,28
v_add_u32 v76, 1, v76
mvv 20,180,28
s_waitcnt lgkmcnt(2)
ds_write_b64 v89, v[108:109] offset:128
mvv 12,184,32
R 48,63488
mvv 16,188,32
mvv 20,192,32
mvv 12,196,36
R 52,64512
v_xor_b32 v80, 0x10000, v80
mvv 16,200,36
s_waitcnt vmcnt(2) lgkmcnt(2)
s_add_u32 s49, s49, 313344
mvv 20,204,36
s_barrier
mvv 12,208,40
buffer_load_dwordx2 v[108:109], v90, s[36:39], s49 offen sc1
mvv 16,212,40
s_add_u32 m0, s50, 32768
R 24,0
mvv 20,216,40
ds_read_b64 v[78:79], v89 offset:128
mvv 12,220,44
R 28,1024
mvv 16,224,44
buffer_load_dwordx2 v[110:111], v90, s[36:39], s49 offen offset:512 sc1
mvv 20,228,44
s_waitcnt lgkmcnt(3)
mvv 12,232,48
R 32,2048
mvv 16,236,48
mvv 20,240,48
D 85,0
mvv 12,244,52
R 36,3072
mvv 16,248,52
mvv 20,252,52
s_waitcnt lgkmcnt(2)
v_mfma_f32_16x16x32_bf16 v[0:3], a[0:3], v[24:27], 0
D 85,1024
v_mfma_f32_16x16x32_bf16 v[4:7], a[4:7], v[24:27], 0
R 40,4096
v_fma_f32 v104, v79, v56, v92
v_mfma_f32_16x16x32_bf16 v[8:11], a[8:11], v[24:27], 0
v_fma_f32 v12, v12, v78, v104
v_fma_f32 v105, v79, v57, v93
mav 0,12,28
R 44,5120
v_fma_f32 v13, v13, v78, v105
mav 4,16,28
s_waitcnt vmcnt(4)
buffer_store_dword v76, v81, s[36:39], s52 offen sc1
mav 8,20,28
s_waitcnt lgkmcnt(2)
v_mul_f32_e64 v12, v12, |v12|
v_mul_f32_e64 v13, v13, |v13|
mav 0,24,32
R 48,6144
v_cvt_pk_bf16_f32 v12, v12, v13
mav 4,28,32
v_pk_max_i16 v68, v12, 0
v_fma_f32 v104, v79, v58, v94
mav 8,32,32
D 85,2048
mav 0,36,36
R 52,7168
v_fma_f32 v14, v14, v78, v104
mav 4,40,36
v_fma_f32 v105, v79, v59, v95
v_fma_f32 v15, v15, v78, v105
mav 8,44,36
s_waitcnt lgkmcnt(2)
v_mul_f32_e64 v14, v14, |v14|
v_mul_f32_e64 v15, v15, |v15|
mav 0,48,40
D 85,3072
mav 4,52,40
s_add_u32 m0, s50, 36864
R 24,8192
v_cvt_pk_bf16_f32 v14, v14, v15
mav 8,56,40
v_pk_max_i16 v69, v14, 0
v_fma_f32 v104, v79, v60, v96
mav 0,60,44
R 28,9216
v_fma_f32 v16, v16, v78, v104
mav 4,64,44
D 86,0
mav 8,68,44
s_waitcnt lgkmcnt(2)
v_fma_f32 v105, v79, v61, v97
v_fma_f32 v17, v17, v78, v105
mav 0,72,48
R 32,10240
v_mul_f32_e64 v16, v16, |v16|
mav 4,76,48
v_mul_f32_e64 v17, v17, |v17|
v_cvt_pk_bf16_f32 v16, v16, v17
mav 8,80,48
D 86,1024
mav 0,84,52
R 36,11264
v_pk_max_i16 v70, v16, 0
mav 4,88,52
v_fma_f32 v104, v79, v62, v98
v_fma_f32 v18, v18, v78, v104
mav 8,92,52
s_waitcnt lgkmcnt(2)
v_fma_f32 v105, v79, v63, v99
v_fma_f32 v19, v19, v78, v105
mav 0,96,24
D 86,2048
mav 4,100,24
R 40,12288
LsF6:
s_cbranch_execz LsF10
v_mul_f32_e64 v18, v18, |v18|
mav 8,104,24
v_mul_f32_e64 v19, v19, |v19|
v_cvt_pk_bf16_f32 v18, v18, v19
mav 0,108,28
R 44,13312
v_pk_max_i16 v71, v18, 0
mav 4,112,28
buffer_store_dwordx4 v[68:71], v83, s[36:39], s49 offen sc1
D 86,3072
mav 8,116,28
s_waitcnt lgkmcnt(2)
v_fma_f32 v104, v79, v64, v100
v_fma_f32 v20, v20, v78, v104
mav 0,120,32
R 48,14336
v_fma_f32 v105, v79, v65, v101
mav 4,124,32
v_fma_f32 v21, v21, v78, v105
v_mul_f32_e64 v20, v20, |v20|
mav 8,128,32
v_mul_f32_e64 v21, v21, |v21|
v_cvt_pk_bf16_f32 v20, v20, v21
mav 0,132,36
R 52,15360
v_pk_max_i16 v72, v20, 0
mav 4,136,36
v_pk_max_i16 v74, v20, 0
v_fma_f32 v104, v79, v66, v102
mav 8,140,36
s_waitcnt lgkmcnt(2)
v_fma_f32 v22, v22, v78, v104
v_fma_f32 v105, v79, v67, v103
mav 0,144,40
R 24,16384
v_fma_f32 v23, v23, v78, v105
mav 4,148,40
v_mul_f32_e64 v22, v22, |v22|
v_mul_f32_e64 v23, v23, |v23|
mav 8,152,40
v_cvt_pk_bf16_f32 v22, v22, v23
v_pk_max_i16 v73, v22, 0
mav 0,156,44
R 28,17408
v_pk_max_i16 v75, v22, 0
mav 4,160,44
v_permlane16_swap_b32 v72, v74
mav 8,164,44
s_waitcnt lgkmcnt(2)
v_permlane16_swap_b32 v73, v75
mav 0,168,48
R 32,18432
mav 4,172,48
mav 8,176,48
buffer_store_dwordx4 v[72:75], v84, s[36:39], s49 offen sc1
mav 0,180,52
R 36,19456
mav 4,184,52
mav 8,188,52
s_waitcnt lgkmcnt(2)
mav 0,192,24
R 40,20480
mav 4,196,24
mav 8,200,24
s_waitcnt vmcnt(11)
v_add_f32 v108, v108, v110
v_add_f32 v109, v109, v111
mav 0,204,28
R 44,21504
mav 4,208,28
v_permlane32_swap_b32 v108, v109
mav 8,212,28
s_waitcnt lgkmcnt(2)
v_add_f32 v110, v108, v109
v_add_f32 v111, v108, v109
mav 0,216,32
R 48,22528
mav 4,220,32
v_permlane16_swap_b32 v110, v111
mav 8,224,32
v_add_f32 v108, v110, v111
v_add_f32 v109, v110, v111
mav 0,228,36
R 52,23552
mav 4,232,36
v_permlane32_swap_b32 v108, v109
mav 8,236,36
s_waitcnt lgkmcnt(2)
v_mul_f32 v110, 0x3a800000, v108
v_mul_f32 v111, 0x3a800000, v109
mav 0,240,40
R 24,24576
v_fma_f32 v111, -v110, v110, v111
mav 4,244,40
v_max_f32 v111, 0, v111
v_add_f32 v111, 0x3727c5ac, v111
mav 8,248,40
v_rsq_f32 v108, v111
mav 0,252,44
R 28,25600
v_mul_f32_e64 v109, -v110, v108
mvv 4,128,44
mvv 8,132,44
s_waitcnt lgkmcnt(2)
mvv 0,136,48
R 32,26624
mvv 4,140,48
mvv 8,144,48
mvv 0,148,52
R 36,27648
mvv 4,152,52
mvv 8,156,52
s_waitcnt lgkmcnt(2)
mvv 0,160,24
R 40,28672
mvv 4,164,24
mvv 8,168,24
mvv 0,172,28
R 44,29696
mvv 4,176,28
v_add_u32 v76, 1, v76
mvv 8,180,28
s_waitcnt lgkmcnt(2)
ds_write_b64 v89, v[108:109] offset:0
mvv 0,184,32
s_xor_b32 s50, s50, 0x10000
R 48,30720
mvv 4,188,32
mvv 8,192,32
mvv 0,196,36
R 52,31744
mvv 4,200,36
s_waitcnt vmcnt(1) lgkmcnt(2)
s_add_u32 s49, s49, 313344
mvv 8,204,36
s_barrier
mvv 0,208,40
buffer_load_dwordx2 v[108:109], v90, s[36:39], s49 offen sc1
mvv 4,212,40
s_add_u32 m0, s50, 0
R 24,32768
mvv 8,216,40
ds_read_b64 v[78:79], v89 offset:0
mvv 0,220,44
R 28,33792
mvv 4,224,44
buffer_load_dwordx2 v[110:111], v90, s[36:39], s49 offen offset:512 sc1
mvv 8,228,44
s_waitcnt lgkmcnt(3)
mvv 0,232,48
R 32,34816
mvv 4,236,48
mvv 8,240,48
D 85,0
mvv 0,244,52
R 36,35840
mvv 4,248,52
mvv 8,252,52
s_waitcnt lgkmcnt(2)
v_mfma_f32_16x16x32_bf16 v[12:15], a[0:3], v[24:27], 0
D 85,1024
v_mfma_f32_16x16x32_bf16 v[16:19], a[4:7], v[24:27], 0
R 40,36864
v_fma_f32 v104, v79, v56, v92
v_mfma_f32_16x16x32_bf16 v[20:23], a[8:11], v[24:27], 0
v_fma_f32 v0, v0, v78, v104
v_fma_f32 v105, v79, v57, v93
mav 12,12,28
R 44,37888
v_fma_f32 v1, v1, v78, v105
mav 16,16,28
s_waitcnt vmcnt(4)
buffer_store_dword v76, v81, s[36:39], s52 offen sc1
mav 20,20,28
s_waitcnt lgkmcnt(2)
v_mul_f32_e64 v0, v0, |v0|
v_mul_f32_e64 v1, v1, |v1|
mav 12,24,32
R 48,38912
v_cvt_pk_bf16_f32 v0, v0, v1
mav 16,28,32
v_pk_max_i16 v68, v0, 0
v_fma_f32 v104, v79, v58, v94
mav 20,32,32
D 85,2048
mav 12,36,36
R 52,39936
v_fma_f32 v2, v2, v78, v104
mav 16,40,36
v_fma_f32 v105, v79, v59, v95
v_fma_f32 v3, v3, v78, v105
mav 20,44,36
s_waitcnt lgkmcnt(2)
v_mul_f32_e64 v2, v2, |v2|
v_mul_f32_e64 v3, v3, |v3|
mav 12,48,40
D 85,3072
mav 16,52,40
s_add_u32 m0, s50, 4096
R 24,40960
v_cvt_pk_bf16_f32 v2, v2, v3
mav 20,56,40
v_pk_max_i16 v69, v2, 0
v_fma_f32 v104, v79, v60, v96
mav 12,60,44
R 28,41984
v_fma_f32 v4, v4, v78, v104
mav 16,64,44
D 86,0
mav 20,68,44
s_waitcnt lgkmcnt(2)
v_fma_f32 v105, v79, v61, v97
v_fma_f32 v5, v5, v78, v105
mav 12,72,48
R 32,43008
v_mul_f32_e64 v4, v4, |v4|
mav 16,76,48
v_mul_f32_e64 v5, v5, |v5|
v_cvt_pk_bf16_f32 v4, v4, v5
mav 20,80,48
D 86,1024
mav 12,84,52
R 36,44032
v_pk_max_i16 v70, v4, 0
mav 16,88,52
v_fma_f32 v104, v79, v62, v98
v_fma_f32 v6, v6, v78, v104
mav 20,92,52
s_waitcnt lgkmcnt(2)
v_fma_f32 v105, v79, v63, v99
v_fma_f32 v7, v7, v78, v105
mav 12,96,24
D 86,2048
mav 16,100,24
R 40,45056
v_mul_f32_e64 v6, v6, |v6|
mav 20,104,24
v_mul_f32_e64 v7, v7, |v7|
v_cvt_pk_bf16_f32 v6, v6, v7
mav 12,108,28
R 44,46080
v_pk_max_i16 v71, v6, 0
mav 16,112,28
buffer_store_dwordx4 v[68:71], v83, s[36:39], s49 offen sc1
D 86,3072
mav 20,116,28
s_waitcnt lgkmcnt(2)
v_fma_f32 v104, v79, v64, v100
v_fma_f32 v8, v8, v78, v104
mav 12,120,32
R 48,47104
v_fma_f32 v105, v79, v65, v101
mav 16,124,32
v_fma_f32 v9, v9, v78, v105
v_mul_f32_e64 v8, v8, |v8|
mav 20,128,32
v_mul_f32_e64 v9, v9, |v9|
v_cvt_pk_bf16_f32 v8, v8, v9
mav 12,132,36
R 52,48128
v_pk_max_i16 v72, v8, 0
mav 16,136,36
v_pk_max_i16 v74, v8, 0
v_fma_f32 v104, v79, v66, v102
mav 20,140,36
s_waitcnt lgkmcnt(2)
v_fma_f32 v10, v10, v78, v104
v_fma_f32 v105, v79, v67, v103
mav 12,144,40
R 24,49152
v_fma_f32 v11, v11, v78, v105
mav 16,148,40
v_mul_f32_e64 v10, v10, |v10|
v_mul_f32_e64 v11, v11, |v11|
mav 20,152,40
v_cvt_pk_bf16_f32 v10, v10, v11
v_pk_max_i16 v73, v10, 0
mav 12,156,44
R 28,50176
v_pk_max_i16 v75, v10, 0
mav 16,160,44
v_permlane16_swap_b32 v72, v74
mav 20,164,44
s_waitcnt lgkmcnt(2)
v_permlane16_swap_b32 v73, v75
mav 12,168,48
R 32,51200
mav 16,172,48
mav 20,176,48
buffer_store_dwordx4 v[72:75], v84, s[36:39], s49 offen sc1
mav 12,180,52
R 36,52224
mav 16,184,52
mav 20,188,52
s_waitcnt lgkmcnt(2)
mav 12,192,24
R 40,53248
mav 16,196,24
mav 20,200,24
s_waitcnt vmcnt(11)
v_add_f32 v108, v108, v110
v_add_f32 v109, v109, v111
mav 12,204,28
R 44,54272
mav 16,208,28
v_permlane32_swap_b32 v108, v109
mav 20,212,28
s_waitcnt lgkmcnt(2)
v_add_f32 v110, v108, v109
v_add_f32 v111, v108, v109
mav 12,216,32
R 48,55296
mav 16,220,32
v_permlane16_swap_b32 v110, v111
mav 20,224,32
v_add_f32 v108, v110, v111
v_add_f32 v109, v110, v111
mav 12,228,36
R 52,56320
mav 16,232,36
v_permlane32_swap_b32 v108, v109
mav 20,236,36
s_waitcnt lgkmcnt(2)
v_mul_f32 v110, 0x3a800000, v108
v_mul_f32 v111, 0x3a800000, v109
mav 12,240,40
R 24,57344
v_fma_f32 v111, -v110, v110, v111
mav 16,244,40
v_max_f32 v111, 0, v111
v_add_f32 v111, 0x3727c5ac, v111
mav 20,248,40
v_rsq_f32 v108, v111
mav 12,252,44
R 28,58368
v_mul_f32_e64 v109, -v110, v108
mvv 16,128,44
mvv 20,132,44
s_waitcnt lgkmcnt(2)
mvv 12,136,48
R 32,59392
mvv 16,140,48
mvv 20,144,48
mvv 12,148,52
R 36,60416
mvv 16,152,52
mvv 20,156,52
s_waitcnt lgkmcnt(2)
mvv 12,160,24
s_mov_b32 s60, -29
.p2align 6
LF1_1:
mvv 16,164,24
R 40,61440
mvv 20,168,24
mvv 12,172,28
R 44,62464
mvv 16,176,28
v_add_u32 v76, 1, v76
mvv 20,180,28
s_waitcnt lgkmcnt(2)
ds_write_b64 v89, v[108:109] offset:128
mvv 12,184,32
R 48,63488
mvv 16,188,32
mvv 20,192,32
mvv 12,196,36
R 52,64512
v_xor_b32 v80, 0x10000, v80
mvv 16,200,36
s_waitcnt vmcnt(1) lgkmcnt(2)
s_add_u32 s49, s49, 313344
mvv 20,204,36
s_barrier
mvv 12,208,40
buffer_load_dwordx2 v[108:109], v90, s[36:39], s49 offen sc1
mvv 16,212,40
s_add_u32 m0, s50, 32768
R 24,0
mvv 20,216,40
ds_read_b64 v[78:79], v89 offset:128
mvv 12,220,44
R 28,1024
mvv 16,224,44
buffer_load_dwordx2 v[110:111], v90, s[36:39], s49 offen offset:512 sc1
mvv 20,228,44
s_waitcnt lgkmcnt(3)
mvv 12,232,48
R 32,2048
mvv 16,236,48
mvv 20,240,48
D 85,0
mvv 12,244,52
R 36,3072
mvv 16,248,52
mvv 20,252,52
s_waitcnt lgkmcnt(2)
v_mfma_f32_16x16x32_bf16 v[0:3], a[0:3], v[24:27], 0
D 85,1024
v_mfma_f32_16x16x32_bf16 v[4:7], a[4:7], v[24:27], 0
R 40,4096
v_fma_f32 v104, v79, v56, v92
v_mfma_f32_16x16x32_bf16 v[8:11], a[8:11], v[24:27], 0
v_fma_f32 v12, v12, v78, v104
v_fma_f32 v105, v79, v57, v93
mav 0,12,28
R 44,5120
v_fma_f32 v13, v13, v78, v105
mav 4,16,28
s_waitcnt vmcnt(4)
buffer_store_dword v76, v81, s[36:39], s52 offen sc1
mav 8,20,28
s_waitcnt lgkmcnt(2)
v_mul_f32_e64 v12, v12, |v12|
v_mul_f32_e64 v13, v13, |v13|
mav 0,24,32
R 48,6144
v_cvt_pk_bf16_f32 v12, v12, v13
mav 4,28,32
v_pk_max_i16 v68, v12, 0
v_fma_f32 v104, v79, v58, v94
mav 8,32,32
D 85,2048
mav 0,36,36
R 52,7168
v_fma_f32 v14, v14, v78, v104
mav 4,40,36
v_fma_f32 v105, v79, v59, v95
v_fma_f32 v15, v15, v78, v105
mav 8,44,36
s_waitcnt lgkmcnt(2)
v_mul_f32_e64 v14, v14, |v14|
v_mul_f32_e64 v15, v15, |v15|
mav 0,48,40
D 85,3072
mav 4,52,40
s_add_u32 m0, s50, 36864
R 24,8192
v_cvt_pk_bf16_f32 v14, v14, v15
mav 8,56,40
v_pk_max_i16 v69, v14, 0
v_fma_f32 v104, v79, v60, v96
mav 0,60,44
R 28,9216
v_fma_f32 v16, v16, v78, v104
mav 4,64,44
D 86,0
mav 8,68,44
s_waitcnt lgkmcnt(2)
v_fma_f32 v105, v79, v61, v97
v_fma_f32 v17, v17, v78, v105
mav 0,72,48
R 32,10240
v_mul_f32_e64 v16, v16, |v16|
mav 4,76,48
v_mul_f32_e64 v17, v17, |v17|
v_cvt_pk_bf16_f32 v16, v16, v17
mav 8,80,48
D 86,1024
mav 0,84,52
R 36,11264
v_pk_max_i16 v70, v16, 0
mav 4,88,52
v_fma_f32 v104, v79, v62, v98
v_fma_f32 v18, v18, v78, v104
mav 8,92,52
s_waitcnt lgkmcnt(2)
v_fma_f32 v105, v79, v63, v99
v_fma_f32 v19, v19, v78, v105
mav 0,96,24
D 86,2048
mav 4,100,24
R 40,12288
v_mul_f32_e64 v18, v18, |v18|
mav 8,104,24
v_mul_f32_e64 v19, v19, |v19|
v_cvt_pk_bf16_f32 v18, v18, v19
mav 0,108,28
R 44,13312
v_pk_max_i16 v71, v18, 0
mav 4,112,28
buffer_store_dwordx4 v[68:71], v83, s[36:39], s49 offen sc1
)"
R"(
D 86,3072
mav 8,116,28
s_waitcnt lgkmcnt(2)
v_fma_f32 v104, v79, v64, v100
v_fma_f32 v20, v20, v78, v104
mav 0,120,32
R 48,14336
v_fma_f32 v105, v79, v65, v101
mav 4,124,32
v_fma_f32 v21, v21, v78, v105
v_mul_f32_e64 v20, v20, |v20|
mav 8,128,32
v_mul_f32_e64 v21, v21, |v21|
v_cvt_pk_bf16_f32 v20, v20, v21
mav 0,132,36
R 52,15360
v_pk_max_i16 v72, v20, 0
mav 4,136,36
v_pk_max_i16 v74, v20, 0
v_fma_f32 v104, v79, v66, v102
mav 8,140,36
s_waitcnt lgkmcnt(2)
v_fma_f32 v22, v22, v78, v104
v_fma_f32 v105, v79, v67, v103
mav 0,144,40
R 24,16384
v_fma_f32 v23, v23, v78, v105
mav 4,148,40
v_mul_f32_e64 v22, v22, |v22|
v_mul_f32_e64 v23, v23, |v23|
mav 8,152,40
v_cvt_pk_bf16_f32 v22, v22, v23
v_pk_max_i16 v73, v22, 0
mav 0,156,44
R 28,17408
v_pk_max_i16 v75, v22, 0
mav 4,160,44
v_permlane16_swap_b32 v72, v74
mav 8,164,44
s_waitcnt lgkmcnt(2)
v_permlane16_swap_b32 v73, v75
mav 0,168,48
R 32,18432
mav 4,172,48
mav 8,176,48
buffer_store_dwordx4 v[72:75], v84, s[36:39], s49 offen sc1
mav 0,180,52
R 36,19456
mav 4,184,52
mav 8,188,52
s_waitcnt lgkmcnt(2)
mav 0,192,24
R 40,20480
mav 4,196,24
mav 8,200,24
s_waitcnt vmcnt(11)
v_add_f32 v108, v108, v110
v_add_f32 v109, v109, v111
mav 0,204,28
R 44,21504
mav 4,208,28
v_permlane32_swap_b32 v108, v109
mav 8,212,28
s_waitcnt lgkmcnt(2)
v_add_f32 v110, v108, v109
v_add_f32 v111, v108, v109
LsF7:
s_cbranch_execz LsF11
mav 0,216,32
R 48,22528
mav 4,220,32
v_permlane16_swap_b32 v110, v111
mav 8,224,32
v_add_f32 v108, v110, v111
v_add_f32 v109, v110, v111
mav 0,228,36
R 52,23552
mav 4,232,36
v_permlane32_swap_b32 v108, v109
mav 8,236,36
s_waitcnt lgkmcnt(2)
v_mul_f32 v110, 0x3a800000, v108
v_mul_f32 v111, 0x3a800000, v109
mav 0,240,40
R 24,24576
v_fma_f32 v111, -v110, v110, v111
mav 4,244,40
v_max_f32 v111, 0, v111
v_add_f32 v111, 0x3727c5ac, v111
mav 8,248,40
v_rsq_f32 v108, v111
mav 0,252,44
R 28,25600
v_mul_f32_e64 v109, -v110, v108
mvv 4,128,44
mvv 8,132,44
s_waitcnt lgkmcnt(2)
mvv 0,136,48
R 32,26624
mvv 4,140,48
mvv 8,144,48
mvv 0,148,52
R 36,27648
mvv 4,152,52
mvv 8,156,52
s_waitcnt lgkmcnt(2)
mvv 0,160,24
R 40,28672
mvv 4,164,24
mvv 8,168,24
mvv 0,172,28
R 44,29696
mvv 4,176,28
v_add_u32 v76, 1, v76
mvv 8,180,28
s_waitcnt lgkmcnt(2)
ds_write_b64 v89, v[108:109] offset:0
mvv 0,184,32
s_xor_b32 s50, s50, 0x10000
R 48,30720
mvv 4,188,32
mvv 8,192,32
mvv 0,196,36
R 52,31744
mvv 4,200,36
s_waitcnt vmcnt(1) lgkmcnt(2)
s_add_u32 s49, s49, 313344
mvv 8,204,36
s_barrier
mvv 0,208,40
buffer_load_dwordx2 v[108:109], v90, s[36:39], s49 offen sc1
mvv 4,212,40
s_add_u32 m0, s50, 0
R 24,32768
mvv 8,216,40
ds_read_b64 v[78:79], v89 offset:0
mvv 0,220,44
R 28,33792
mvv 4,224,44
buffer_load_dwordx2 v[110:111], v90, s[36:39], s49 offen offset:512 sc1
mvv 8,228,44
s_waitcnt lgkmcnt(3)
mvv 0,232,48
R 32,34816
mvv 4,236,48
mvv 8,240,48
D 85,0
mvv 0,244,52
R 36,35840
mvv 4,248,52
mvv 8,252,52
s_waitcnt lgkmcnt(2)
v_mfma_f32_16x16x32_bf16 v[12:15], a[0:3], v[24:27], 0
D 85,1024
v_mfma_f32_16x16x32_bf16 v[16:19], a[4:7], v[24:27], 0
R 40,36864
v_fma_f32 v104, v79, v56, v92
v_mfma_f32_16x16x32_bf16 v[20:23], a[8:11], v[24:27], 0
v_fma_f32 v0, v0, v78, v104
v_fma_f32 v105, v79, v57, v93
mav 12,12,28
R 44,37888
v_fma_f32 v1, v1, v78, v105
mav 16,16,28
s_waitcnt vmcnt(4)
buffer_store_dword v76, v81, s[36:39], s52 offen sc1
mav 20,20,28
s_waitcnt lgkmcnt(2)
v_mul_f32_e64 v0, v0, |v0|
v_mul_f32_e64 v1, v1, |v1|
mav 12,24,32
R 48,38912
v_cvt_pk_bf16_f32 v0, v0, v1
mav 16,28,32
v_pk_max_i16 v68, v0, 0
v_fma_f32 v104, v79, v58, v94
mav 20,32,32
D 85,2048
mav 12,36,36
R 52,39936
v_fma_f32 v2, v2, v78, v104
mav 16,40,36
v_fma_f32 v105, v79, v59, v95
v_fma_f32 v3, v3, v78, v105
mav 20,44,36
s_waitcnt lgkmcnt(2)
v_mul_f32_e64 v2, v2, |v2|
v_mul_f32_e64 v3, v3, |v3|
mav 12,48,40
D 85,3072
mav 16,52,40
s_add_u32 m0, s50, 4096
R 24,40960
v_cvt_pk_bf16_f32 v2, v2, v3
mav 20,56,40
v_pk_max_i16 v69, v2, 0
v_fma_f32 v104, v79, v60, v96
mav 12,60,44
R 28,41984
v_fma_f32 v4, v4, v78, v104
mav 16,64,44
D 86,0
mav 20,68,44
s_waitcnt lgkmcnt(2)
v_fma_f32 v105, v79, v61, v97
v_fma_f32 v5, v5, v78, v105
mav 12,72,48
R 32,43008
v_mul_f32_e64 v4, v4, |v4|
mav 16,76,48
v_mul_f32_e64 v5, v5, |v5|
v_cvt_pk_bf16_f32 v4, v4, v5
mav 20,80,48
D 86,1024
mav 12,84,52
R 36,44032
v_pk_max_i16 v70, v4, 0
mav 16,88,52
v_fma_f32 v104, v79, v62, v98
v_fma_f32 v6, v6, v78, v104
mav 20,92,52
s_waitcnt lgkmcnt(2)
v_fma_f32 v105, v79, v63, v99
v_fma_f32 v7, v7, v78, v105
mav 12,96,24
D 86,2048
mav 16,100,24
R 40,45056
v_mul_f32_e64 v6, v6, |v6|
mav 20,104,24
v_mul_f32_e64 v7, v7, |v7|
v_cvt_pk_bf16_f32 v6, v6, v7
mav 12,108,28
R 44,46080
v_pk_max_i16 v71, v6, 0
mav 16,112,28
buffer_store_dwordx4 v[68:71], v83, s[36:39], s49 offen sc1
D 86,3072
mav 20,116,28
s_waitcnt lgkmcnt(2)
v_fma_f32 v104, v79, v64, v100
v_fma_f32 v8, v8, v78, v104
mav 12,120,32
R 48,47104
v_fma_f32 v105, v79, v65, v101
mav 16,124,32
v_fma_f32 v9, v9, v78, v105
v_mul_f32_e64 v8, v8, |v8|
mav 20,128,32
v_mul_f32_e64 v9, v9, |v9|
v_cvt_pk_bf16_f32 v8, v8, v9
mav 12,132,36
R 52,48128
v_pk_max_i16 v72, v8, 0
mav 16,136,36
v_pk_max_i16 v74, v8, 0
v_fma_f32 v104, v79, v66, v102
mav 20,140,36
s_waitcnt lgkmcnt(2)
v_fma_f32 v10, v10, v78, v104
v_fma_f32 v105, v79, v67, v103
mav 12,144,40
R 24,49152
v_fma_f32 v11, v11, v78, v105
mav 16,148,40
v_mul_f32_e64 v10, v10, |v10|
v_mul_f32_e64 v11, v11, |v11|
mav 20,152,40
v_cvt_pk_bf16_f32 v10, v10, v11
v_pk_max_i16 v73, v10, 0
mav 12,156,44
R 28,50176
v_pk_max_i16 v75, v10, 0
mav 16,160,44
v_permlane16_swap_b32 v72, v74
mav 20,164,44
s_waitcnt lgkmcnt(2)
v_permlane16_swap_b32 v73, v75
mav 12,168,48
R 32,51200
mav 16,172,48
mav 20,176,48
buffer_store_dwordx4 v[72:75], v84, s[36:39], s49 offen sc1
mav 12,180,52
R 36,52224
mav 16,184,52
mav 20,188,52
s_waitcnt lgkmcnt(2)
mav 12,192,24
R 40,53248
mav 16,196,24
mav 20,200,24
s_waitcnt vmcnt(11)
v_add_f32 v108, v108, v110
v_add_f32 v109, v109, v111
mav 12,204,28
R 44,54272
mav 16,208,28
v_permlane32_swap_b32 v108, v109
mav 20,212,28
s_waitcnt lgkmcnt(2)
v_add_f32 v110, v108, v109
v_add_f32 v111, v108, v109
mav 12,216,32
R 48,55296
mav 16,220,32
v_permlane16_swap_b32 v110, v111
mav 20,224,32
v_add_f32 v108, v110, v111
v_add_f32 v109, v110, v111
mav 12,228,36
R 52,56320
mav 16,232,36
v_permlane32_swap_b32 v108, v109
mav 20,236,36
s_waitcnt lgkmcnt(2)
v_mul_f32 v110, 0x3a800000, v108
v_mul_f32 v111, 0x3a800000, v109
mav 12,240,40
R 24,57344
v_fma_f32 v111, -v110, v110, v111
mav 16,244,40
v_max_f32 v111, 0, v111
v_add_f32 v111, 0x3727c5ac, v111
mav 20,248,40
v_rsq_f32 v108, v111
mav 12,252,44
R 28,58368
v_mul_f32_e64 v109, -v110, v108
mvv 16,128,44
mvv 20,132,44
s_waitcnt lgkmcnt(2)
mvv 12,136,48
R 32,59392
mvv 16,140,48
mvv 20,144,48
mvv 12,148,52
R 36,60416
mvv 16,152,52
mvv 20,156,52
s_waitcnt lgkmcnt(2)
s_add_u32 s60, s60, 1
mvv 12,160,24
s_cbranch_scc0 LF1_1
mvv 16,164,24
R 40,61440
mvv 20,168,24
mvv 12,172,28
R 44,62464
mvv 16,176,28
v_add_u32 v76, 1, v76
mvv 20,180,28
s_waitcnt lgkmcnt(2)
ds_write_b64 v89, v[108:109] offset:128
mvv 12,184,32
R 48,63488
mvv 16,188,32
mvv 20,192,32
mvv 12,196,36
R 52,64512
v_xor_b32 v80, 0x10000, v80
mvv 16,200,36
s_waitcnt vmcnt(1) lgkmcnt(2)
s_add_u32 s49, s49, 313344
mvv 20,204,36
s_barrier
mvv 12,208,40
buffer_load_dwordx2 v[108:109], v90, s[36:39], s49 offen sc1
mvv 16,212,40
s_add_u32 m0, s50, 32768
R 24,0
mvv 20,216,40
ds_read_b64 v[78:79], v89 offset:128
mvv 12,220,44
R 28,1024
mvv 16,224,44
buffer_load_dwordx2 v[110:111], v90, s[36:39], s49 offen offset:512 sc1
mvv 20,228,44
s_waitcnt lgkmcnt(3)
mvv 12,232,48
R 32,2048
mvv 16,236,48
mvv 20,240,48
D 85,0
mvv 12,244,52
R 36,3072
mvv 16,248,52
mvv 20,252,52
s_waitcnt lgkmcnt(2)
v_mfma_f32_16x16x32_bf16 v[0:3], a[0:3], v[24:27], 0
D 85,1024
v_mfma_f32_16x16x32_bf16 v[4:7], a[4:7], v[24:27], 0
R 40,4096
v_fma_f32 v104, v79, v56, v92
v_mfma_f32_16x16x32_bf16 v[8:11], a[8:11], v[24:27], 0
v_fma_f32 v12, v12, v78, v104
v_fma_f32 v105, v79, v57, v93
mav 0,12,28
R 44,5120
v_fma_f32 v13, v13, v78, v105
mav 4,16,28
s_waitcnt vmcnt(4)
buffer_store_dword v76, v81, s[36:39], s52 offen sc1
mav 8,20,28
s_waitcnt lgkmcnt(2)
v_mul_f32_e64 v12, v12, |v12|
v_mul_f32_e64 v13, v13, |v13|
mav 0,24,32
R 48,6144
v_cvt_pk_bf16_f32 v12, v12, v13
mav 4,28,32
v_pk_max_i16 v68, v12, 0
v_fma_f32 v104, v79, v58, v94
mav 8,32,32
D 85,2048
mav 0,36,36
R 52,7168
v_fma_f32 v14, v14, v78, v104
mav 4,40,36
v_fma_f32 v105, v79, v59, v95
v_fma_f32 v15, v15, v78, v105
mav 8,44,36
s_waitcnt lgkmcnt(2)
v_mul_f32_e64 v14, v14, |v14|
v_mul_f32_e64 v15, v15, |v15|
mav 0,48,40
D 85,3072
mav 4,52,40
s_add_u32 m0, s50, 36864
R 24,8192
v_cvt_pk_bf16_f32 v14, v14, v15
mav 8,56,40
v_pk_max_i16 v69, v14, 0
v_fma_f32 v104, v79, v60, v96
mav 0,60,44
R 28,9216
v_fma_f32 v16, v16, v78, v104
mav 4,64,44
D 86,0
mav 8,68,44
s_waitcnt lgkmcnt(2)
v_fma_f32 v105, v79, v61, v97
v_fma_f32 v17, v17, v78, v105
mav 0,72,48
R 32,10240
v_mul_f32_e64 v16, v16, |v16|
mav 4,76,48
v_mul_f32_e64 v17, v17, |v17|
v_cvt_pk_bf16_f32 v16, v16, v17
mav 8,80,48
D 86,1024
mav 0,84,52
R 36,11264
v_pk_max_i16 v70, v16, 0
mav 4,88,52
v_fma_f32 v104, v79, v62, v98
v_fma_f32 v18, v18, v78, v104
mav 8,92,52
s_waitcnt lgkmcnt(2)
v_fma_f32 v105, v79, v63, v99
v_fma_f32 v19, v19, v78, v105
mav 0,96,24
D 86,2048
mav 4,100,24
R 40,12288
v_mul_f32_e64 v18, v18, |v18|
mav 8,104,24
v_mul_f32_e64 v19, v19, |v19|
v_cvt_pk_bf16_f32 v18, v18, v19
mav 0,108,28
R 44,13312
v_pk_max_i16 v71, v18, 0
mav 4,112,28
buffer_store_dwordx4 v[68:71], v83, s[36:39], s49 offen sc1
D 86,3072
mav 8,116,28
s_waitcnt lgkmcnt(2)
v_fma_f32 v104, v79, v64, v100
v_fma_f32 v20, v20, v78, v104
mav 0,120,32
R 48,14336
v_fma_f32 v105, v79, v65, v101
mav 4,124,32
v_fma_f32 v21, v21, v78, v105
v_mul_f32_e64 v20, v20, |v20|
mav 8,128,32
v_mul_f32_e64 v21, v21, |v21|
v_cvt_pk_bf16_f32 v20, v20, v21
mav 0,132,36
R 52,15360
v_pk_max_i16 v72, v20, 0
mav 4,136,36
v_pk_max_i16 v74, v20, 0
v_fma_f32 v104, v79, v66, v102
mav 8,140,36
s_waitcnt lgkmcnt(2)
v_fma_f32 v22, v22, v78, v104
v_fma_f32 v105, v79, v67, v103
mav 0,144,40
R 24,16384
v_fma_f32 v23, v23, v78, v105
mav 4,148,40
v_mul_f32_e64 v22, v22, |v22|
v_mul_f32_e64 v23, v23, |v23|
mav 8,152,40
v_cvt_pk_bf16_f32 v22, v22, v23
v_pk_max_i16 v73, v22, 0
mav 0,156,44
R 28,17408
v_pk_max_i16 v75, v22, 0
mav 4,160,44
v_permlane16_swap_b32 v72, v74
mav 8,164,44
s_waitcnt lgkmcnt(2)
v_permlane16_swap_b32 v73, v75
mav 0,168,48
R 32,18432
mav 4,172,48
mav 8,176,48
buffer_store_dwordx4 v[72:75], v84, s[36:39], s49 offen sc1
mav 0,180,52
R 36,19456
mav 4,184,52
mav 8,188,52
s_waitcnt lgkmcnt(2)
mav 0,192,24
R 40,20480
mav 4,196,24
mav 8,200,24
s_waitcnt vmcnt(11)
v_add_f32 v108, v108, v110
v_add_f32 v109, v109, v111
mav 0,204,28
R 44,21504
mav 4,208,28
v_permlane32_swap_b32 v108, v109
mav 8,212,28
s_waitcnt lgkmcnt(2)
v_add_f32 v110, v108, v109
v_add_f32 v111, v108, v109
mav 0,216,32
R 48,22528
mav 4,220,32
v_permlane16_swap_b32 v110, v111
mav 8,224,32
v_add_f32 v108, v110, v111
v_add_f32 v109, v110, v111
mav 0,228,36
R 52,23552
mav 4,232,36
v_permlane32_swap_b32 v108, v109
mav 8,236,36
s_waitcnt lgkmcnt(2)
v_mul_f32 v110, 0x3a800000, v108
v_mul_f32 v111, 0x3a800000, v109
mav 0,240,40
R 24,24576
v_fma_f32 v111, -v110, v110, v111
mav 4,244,40
v_max_f32 v111, 0, v111
v_add_f32 v111, 0x3727c5ac, v111
mav 8,248,40
v_rsq_f32 v108, v111
mav 0,252,44
R 28,25600
v_mul_f32_e64 v109, -v110, v108
mvv 4,128,44
mvv 8,132,44
s_waitcnt lgkmcnt(2)
mvv 0,136,48
R 32,26624
mvv 4,140,48
mvv 8,144,48
mvv 0,148,52
R 36,27648
mvv 4,152,52
mvv 8,156,52
s_waitcnt lgkmcnt(2)
mvv 0,160,24
R 40,28672
mvv 4,164,24
mvv 8,168,24
mvv 0,172,28
R 44,29696
mvv 4,176,28
v_add_u32 v76, 1, v76
mvv 8,180,28
s_waitcnt lgkmcnt(2)
ds_write_b64 v89, v[108:109] offset:0
mvv 0,184,32
R 48,30720
mvv 4,188,32
mvv 8,192,32
mvv 0,196,36
R 52,31744
mvv 4,200,36
s_waitcnt vmcnt(1) lgkmcnt(2)
s_add_u32 s49, s49, 313344
mvv 8,204,36
s_barrier
mvv 0,208,40
buffer_load_dwordx2 v[108:109], v90, s[36:39], s49 offen sc1
mvv 4,212,40
R 24,32768
mvv 8,216,40
ds_read_b64 v[78:79], v89 offset:0
mvv 0,220,44
R 28,33792
mvv 4,224,44
buffer_load_dwordx2 v[110:111], v90, s[36:39], s49 offen offset:512 sc1
mvv 8,228,44
s_waitcnt lgkmcnt(3)
mvv 0,232,48
R 32,34816
mvv 4,236,48
mvv 8,240,48
mvv 0,244,52
R 36,35840
mvv 4,248,52
mvv 8,252,52
s_waitcnt lgkmcnt(2)
v_mfma_f32_16x16x32_bf16 v[12:15], a[0:3], v[24:27], 0
LsF8:
s_cbranch_execz LsF12
R 40,36864
v_fma_f32 v104, v79, v56, v92
v_mfma_f32_16x16x32_bf16 v[16:19], a[4:7], v[24:27], 0
v_fma_f32 v0, v0, v78, v104
v_fma_f32 v105, v79, v57, v93
v_mfma_f32_16x16x32_bf16 v[20:23], a[8:11], v[24:27], 0
v_fma_f32 v1, v1, v78, v105
v_mul_f32_e64 v0, v0, |v0|
mav 12,12,28
R 44,37888
v_mul_f32_e64 v1, v1, |v1|
mav 16,16,28
s_waitcnt vmcnt(2)
buffer_store_dword v76, v81, s[36:39], s52 offen sc1
mav 20,20,28
s_waitcnt lgkmcnt(2)
v_cvt_pk_bf16_f32 v0, v0, v1
v_pk_max_i16 v68, v0, 0
mav 12,24,32
R 48,38912
v_fma_f32 v104, v79, v58, v94
mav 16,28,32
v_fma_f32 v2, v2, v78, v104
v_fma_f32 v105, v79, v59, v95
mav 20,32,32
v_fma_f32 v3, v3, v78, v105
v_mul_f32_e64 v2, v2, |v2|
mav 12,36,36
R 52,39936
v_mul_f32_e64 v3, v3, |v3|
mav 16,40,36
v_cvt_pk_bf16_f32 v2, v2, v3
v_pk_max_i16 v69, v2, 0
mav 20,44,36
s_waitcnt lgkmcnt(2)
v_fma_f32 v104, v79, v60, v96
v_fma_f32 v4, v4, v78, v104
mav 12,48,40
R 24,40960
v_fma_f32 v105, v79, v61, v97
mav 16,52,40
v_fma_f32 v5, v5, v78, v105
v_mul_f32_e64 v4, v4, |v4|
mav 20,56,40
v_mul_f32_e64 v5, v5, |v5|
v_cvt_pk_bf16_f32 v4, v4, v5
mav 12,60,44
R 28,41984
v_pk_max_i16 v70, v4, 0
mav 16,64,44
v_fma_f32 v104, v79, v62, v98
v_fma_f32 v6, v6, v78, v104
mav 20,68,44
s_waitcnt lgkmcnt(2)
v_fma_f32 v105, v79, v63, v99
v_fma_f32 v7, v7, v78, v105
mav 12,72,48
R 32,43008
v_mul_f32_e64 v6, v6, |v6|
mav 16,76,48
v_mul_f32_e64 v7, v7, |v7|
v_cvt_pk_bf16_f32 v6, v6, v7
mav 20,80,48
v_pk_max_i16 v71, v6, 0
v_fma_f32 v104, v79, v64, v100
mav 12,84,52
R 36,44032
v_fma_f32 v8, v8, v78, v104
mav 16,88,52
v_fma_f32 v105, v79, v65, v101
v_fma_f32 v9, v9, v78, v105
mav 20,92,52
s_waitcnt lgkmcnt(2)
v_mul_f32_e64 v8, v8, |v8|
v_mul_f32_e64 v9, v9, |v9|
mav 12,96,24
buffer_store_dwordx4 v[68:71], v83, s[36:39], s49 offen sc1
R 40,45056
v_cvt_pk_bf16_f32 v8, v8, v9
mav 16,100,24
v_pk_max_i16 v72, v8, 0
v_pk_max_i16 v74, v8, 0
mav 20,104,24
v_fma_f32 v104, v79, v66, v102
v_fma_f32 v10, v10, v78, v104
mav 12,108,28
R 44,46080
v_fma_f32 v105, v79, v67, v103
mav 16,112,28
v_fma_f32 v11, v11, v78, v105
v_mul_f32_e64 v10, v10, |v10|
mav 20,116,28
s_waitcnt lgkmcnt(2)
v_mul_f32_e64 v11, v11, |v11|
v_cvt_pk_bf16_f32 v10, v10, v11
mav 12,120,32
R 48,47104
v_pk_max_i16 v73, v10, 0
mav 16,124,32
v_pk_max_i16 v75, v10, 0
mav 20,128,32
v_permlane16_swap_b32 v72, v74
mav 12,132,36
R 52,48128
mav 16,136,36
v_permlane16_swap_b32 v73, v75
mav 20,140,36
s_waitcnt lgkmcnt(2)
mav 12,144,40
buffer_store_dwordx4 v[72:75], v84, s[36:39], s49 offen sc1
R 24,49152
mav 16,148,40
mav 20,152,40
mav 12,156,44
R 28,50176
mav 16,160,44
mav 20,164,44
s_waitcnt lgkmcnt(2)
mav 12,168,48
R 32,51200
mav 16,172,48
mav 20,176,48
mav 12,180,52
R 36,52224
mav 16,184,52
mav 20,188,52
s_waitcnt lgkmcnt(2)
mav 12,192,24
R 40,53248
mav 16,196,24
mav 20,200,24
s_waitcnt vmcnt(3)
v_add_f32 v108, v108, v110
v_add_f32 v109, v109, v111
mav 12,204,28
R 44,54272
mav 16,208,28
v_permlane32_swap_b32 v108, v109
mav 20,212,28
s_waitcnt lgkmcnt(2)
v_add_f32 v110, v108, v109
v_add_f32 v111, v108, v109
mav 12,216,32
R 48,55296
mav 16,220,32
v_permlane16_swap_b32 v110, v111
mav 20,224,32
v_add_f32 v108, v110, v111
v_add_f32 v109, v110, v111
mav 12,228,36
R 52,56320
mav 16,232,36
v_permlane32_swap_b32 v108, v109
mav 20,236,36
s_waitcnt lgkmcnt(2)
v_mul_f32 v110, 0x3a800000, v108
v_mul_f32 v111, 0x3a800000, v109
mav 12,240,40
R 24,57344
v_fma_f32 v111, -v110, v110, v111
mav 16,244,40
v_max_f32 v111, 0, v111
v_add_f32 v111, 0x3727c5ac, v111
mav 20,248,40
v_rsq_f32 v108, v111
mav 12,252,44
R 28,58368
v_mul_f32_e64 v109, -v110, v108
mvv 16,128,44
mvv 20,132,44
s_waitcnt lgkmcnt(2)
mvv 12,136,48
R 32,59392
mvv 16,140,48
mvv 20,144,48
mvv 12,148,52
R 36,60416
mvv 16,152,52
mvv 20,156,52
s_waitcnt lgkmcnt(2)
mvv 12,160,24
mvv 16,164,24
R 40,61440
mvv 20,168,24
mvv 12,172,28
R 44,62464
mvv 16,176,28
v_add_u32 v76, 1, v76
mvv 20,180,28
s_waitcnt lgkmcnt(2)
ds_write_b64 v89, v[108:109] offset:128
mvv 12,184,32
R 48,63488
mvv 16,188,32
mvv 20,192,32
mvv 12,196,36
R 52,64512
mvv 16,200,36
s_waitcnt lgkmcnt(2)
s_add_u32 s49, s49, 313344
mvv 20,204,36
s_barrier
mvv 12,208,40
mvv 16,212,40
ds_read_b64 v[78:79], v89 offset:128
mvv 20,216,40
mvv 12,220,44
mvv 16,224,44
mvv 20,228,44
s_waitcnt lgkmcnt(1)
mvv 12,232,48
mvv 16,236,48
mvv 20,240,48
mvv 12,244,52
mvv 16,248,52
mvv 20,252,52
s_waitcnt lgkmcnt(0)
v_fma_f32 v104, v79, v56, v92
s_nop 3
v_fma_f32 v12, v12, v78, v104
v_fma_f32 v105, v79, v57, v93
v_fma_f32 v13, v13, v78, v105
v_mul_f32_e64 v12, v12, |v12|
v_mul_f32_e64 v13, v13, |v13|
v_cvt_pk_bf16_f32 v12, v12, v13
v_pk_max_i16 v68, v12, 0
v_fma_f32 v104, v79, v58, v94
v_fma_f32 v14, v14, v78, v104
v_fma_f32 v105, v79, v59, v95
v_fma_f32 v15, v15, v78, v105
v_mul_f32_e64 v14, v14, |v14|
v_mul_f32_e64 v15, v15, |v15|
v_cvt_pk_bf16_f32 v14, v14, v15
v_pk_max_i16 v69, v14, 0
v_fma_f32 v104, v79, v60, v96
v_fma_f32 v16, v16, v78, v104
v_fma_f32 v105, v79, v61, v97
v_fma_f32 v17, v17, v78, v105
v_mul_f32_e64 v16, v16, |v16|
v_mul_f32_e64 v17, v17, |v17|
v_cvt_pk_bf16_f32 v16, v16, v17
v_pk_max_i16 v70, v16, 0
v_fma_f32 v104, v79, v62, v98
v_fma_f32 v18, v18, v78, v104
v_fma_f32 v105, v79, v63, v99
v_fma_f32 v19, v19, v78, v105
v_mul_f32_e64 v18, v18, |v18|
v_mul_f32_e64 v19, v19, |v19|
v_cvt_pk_bf16_f32 v18, v18, v19
v_pk_max_i16 v71, v18, 0
buffer_store_dwordx4 v[68:71], v83, s[36:39], s49 offen sc1
v_fma_f32 v104, v79, v64, v100
v_fma_f32 v20, v20, v78, v104
v_fma_f32 v105, v79, v65, v101
v_fma_f32 v21, v21, v78, v105
v_mul_f32_e64 v20, v20, |v20|
v_mul_f32_e64 v21, v21, |v21|
v_cvt_pk_bf16_f32 v20, v20, v21
v_pk_max_i16 v72, v20, 0
v_pk_max_i16 v74, v20, 0
v_fma_f32 v104, v79, v66, v102
v_fma_f32 v22, v22, v78, v104
v_fma_f32 v105, v79, v67, v103
v_fma_f32 v23, v23, v78, v105
v_mul_f32_e64 v22, v22, |v22|
v_mul_f32_e64 v23, v23, |v23|
v_cvt_pk_bf16_f32 v22, v22, v23
v_pk_max_i16 v73, v22, 0
v_pk_max_i16 v75, v22, 0
v_permlane16_swap_b32 v72, v74
s_nop 0
v_permlane16_swap_b32 v73, v75
buffer_store_dwordx4 v[72:75], v84, s[36:39], s49 offen sc1
s_waitcnt vmcnt(2)
buffer_store_dword v76, v81, s[36:39], s52 offen sc1
v_add_u32 v76, 37, v76
s_waitcnt vmcnt(1)
buffer_store_dword v76, v81, s[36:39], s52 offen sc1
s_endpgm
LvF2:
LsF9:
s_cbranch_execz LsF13
s_add_u32 s49, s49, 313344
s_barrier
s_add_u32 m0, s50, 0
s_nop 0
D 85,0
D 85,1024
D 85,2048
D 85,3072
s_add_u32 m0, s50, 4096
s_nop 0
D 86,0
D 86,1024
D 86,2048
D 86,3072
s_waitcnt vmcnt(0)
s_add_u32 s49, s49, 313344
s_barrier
s_add_u32 m0, s50, 32768
R 24,0
D 85,0
R 28,1024
D 85,1024
R 32,2048
R 36,3072
D 85,2048
s_waitcnt lgkmcnt(2)
v_mfma_f32_16x16x32_bf16 v[0:3], a[0:3], v[24:27], 0
R 40,4096
v_mfma_f32_16x16x32_bf16 v[4:7], a[4:7], v[24:27], 0
v_mfma_f32_16x16x32_bf16 v[8:11], a[8:11], v[24:27], 0
D 85,3072
mav 0,12,28
s_add_u32 m0, s50, 36864
R 44,5120
mav 4,16,28
mav 8,20,28
s_waitcnt lgkmcnt(2)
mav 0,24,32
D 86,0
mav 4,28,32
R 48,6144
mav 8,32,32
mav 0,36,36
R 52,7168
mav 4,40,36
D 86,1024
mav 8,44,36
s_waitcnt lgkmcnt(2)
mav 0,48,40
R 24,8192
mav 4,52,40
mav 8,56,40
D 86,2048
mav 0,60,44
R 28,9216
mav 4,64,44
mav 8,68,44
s_waitcnt lgkmcnt(2)
mav 0,72,48
D 86,3072
mav 4,76,48
R 32,10240
mav 8,80,48
mav 0,84,52
R 36,11264
mav 4,88,52
mav 8,92,52
s_waitcnt lgkmcnt(2)
mav 0,96,24
R 40,12288
mav 4,100,24
mav 8,104,24
mav 0,108,28
R 44,13312
mav 4,112,28
mav 8,116,28
s_waitcnt lgkmcnt(2)
mav 0,120,32
R 48,14336
mav 4,124,32
mav 8,128,32
mav 0,132,36
R 52,15360
mav 4,136,36
mav 8,140,36
s_waitcnt lgkmcnt(2)
mav 0,144,40
R 24,16384
mav 4,148,40
mav 8,152,40
mav 0,156,44
R 28,17408
mav 4,160,44
mav 8,164,44
s_waitcnt lgkmcnt(2)
mav 0,168,48
R 32,18432
mav 4,172,48
mav 8,176,48
mav 0,180,52
R 36,19456
mav 4,184,52
mav 8,188,52
s_waitcnt lgkmcnt(2)
mav 0,192,24
R 40,20480
mav 4,196,24
mav 8,200,24
mav 0,204,28
R 44,21504
mav 4,208,28
mav 8,212,28
s_waitcnt lgkmcnt(2)
mav 0,216,32
R 48,22528
mav 4,220,32
mav 8,224,32
mav 0,228,36
R 52,23552
mav 4,232,36
mav 8,236,36
s_waitcnt lgkmcnt(2)
mav 0,240,40
R 24,24576
mav 4,244,40
mav 8,248,40
mav 0,252,44
R 28,25600
mvv 4,128,44
mvv 8,132,44
s_waitcnt lgkmcnt(2)
mvv 0,136,48
R 32,26624
mvv 4,140,48
mvv 8,144,48
mvv 0,148,52
R 36,27648
mvv 4,152,52
mvv 8,156,52
s_waitcnt lgkmcnt(2)
mvv 0,160,24
R 40,28672
mvv 4,164,24
mvv 8,168,24
mvv 0,172,28
R 44,29696
mvv 4,176,28
mvv 8,180,28
s_waitcnt lgkmcnt(2)
mvv 0,184,32
s_xor_b32 s50, s50, 0x10000
R 48,30720
mvv 4,188,32
mvv 8,192,32
mvv 0,196,36
R 52,31744
mvv 4,200,36
s_waitcnt vmcnt(0) lgkmcnt(2)
s_add_u32 s49, s49, 313344
mvv 8,204,36
s_barrier
mvv 0,208,40
s_add_u32 m0, s50, 0
R 24,32768
mvv 4,212,40
ds_read_b64 v[78:79], v89 offset:0
mvv 8,216,40
D 85,0
mvv 0,220,44
R 28,33792
mvv 4,224,44
mvv 8,228,44
s_waitcnt lgkmcnt(3)
mvv 0,232,48
D 85,1024
mvv 4,236,48
R 32,34816
mvv 8,240,48
mvv 0,244,52
R 36,35840
mvv 4,248,52
D 85,2048
mvv 8,252,52
s_waitcnt lgkmcnt(2)
v_mfma_f32_16x16x32_bf16 v[12:15], a[0:3], v[24:27], 0
R 40,36864
v_fma_f32 v104, v79, v56, v92
v_mfma_f32_16x16x32_bf16 v[16:19], a[4:7], v[24:27], 0
v_fma_f32 v0, v0, v78, v104
v_fma_f32 v105, v79, v57, v93
v_mfma_f32_16x16x32_bf16 v[20:23], a[8:11], v[24:27], 0
D 85,3072
mav 12,12,28
s_add_u32 m0, s50, 4096
R 44,37888
v_fma_f32 v1, v1, v78, v105
mav 16,16,28
v_mul_f32_e64 v0, v0, |v0|
v_mul_f32_e64 v1, v1, |v1|
mav 20,20,28
s_waitcnt lgkmcnt(2)
v_cvt_pk_bf16_f32 v0, v0, v1
v_pk_max_i16 v68, v0, 0
mav 12,24,32
D 86,0
mav 16,28,32
R 48,38912
v_fma_f32 v104, v79, v58, v94
mav 20,32,32
v_fma_f32 v2, v2, v78, v104
v_fma_f32 v105, v79, v59, v95
mav 12,36,36
R 52,39936
v_fma_f32 v3, v3, v78, v105
mav 16,40,36
D 86,1024
mav 20,44,36
s_waitcnt lgkmcnt(2)
v_mul_f32_e64 v2, v2, |v2|
v_mul_f32_e64 v3, v3, |v3|
mav 12,48,40
R 24,40960
v_cvt_pk_bf16_f32 v2, v2, v3
mav 16,52,40
v_pk_max_i16 v69, v2, 0
v_fma_f32 v104, v79, v60, v96
mav 20,56,40
D 86,2048
mav 12,60,44
R 28,41984
v_fma_f32 v4, v4, v78, v104
mav 16,64,44
v_fma_f32 v105, v79, v61, v97
v_fma_f32 v5, v5, v78, v105
mav 20,68,44
s_waitcnt lgkmcnt(2)
v_mul_f32_e64 v4, v4, |v4|
v_mul_f32_e64 v5, v5, |v5|
mav 12,72,48
D 86,3072
mav 16,76,48
R 32,43008
v_cvt_pk_bf16_f32 v4, v4, v5
mav 20,80,48
v_pk_max_i16 v70, v4, 0
v_fma_f32 v104, v79, v62, v98
mav 12,84,52
R 36,44032
v_fma_f32 v6, v6, v78, v104
mav 16,88,52
v_fma_f32 v105, v79, v63, v99
v_fma_f32 v7, v7, v78, v105
mav 20,92,52
s_waitcnt lgkmcnt(2)
v_mul_f32_e64 v6, v6, |v6|
v_mul_f32_e64 v7, v7, |v7|
mav 12,96,24
R 40,45056
v_cvt_pk_bf16_f32 v6, v6, v7
mav 16,100,24
v_pk_max_i16 v71, v6, 0
v_fma_f32 v104, v79, v64, v100
mav 20,104,24
buffer_store_dwordx4 v[68:71], v83, s[36:39], s49 offen sc1
v_fma_f32 v8, v8, v78, v104
v_fma_f32 v105, v79, v65, v101
v_fma_f32 v9, v9, v78, v105
LsF10:
s_cbranch_execz LsF14
mav 12,108,28
R 44,46080
v_mul_f32_e64 v8, v8, |v8|
mav 16,112,28
v_mul_f32_e64 v9, v9, |v9|
v_cvt_pk_bf16_f32 v8, v8, v9
mav 20,116,28
s_waitcnt lgkmcnt(2)
v_pk_max_i16 v72, v8, 0
v_pk_max_i16 v74, v8, 0
mav 12,120,32
R 48,47104
v_fma_f32 v104, v79, v66, v102
mav 16,124,32
v_fma_f32 v10, v10, v78, v104
v_fma_f32 v105, v79, v67, v103
mav 20,128,32
v_fma_f32 v11, v11, v78, v105
v_mul_f32_e64 v10, v10, |v10|
mav 12,132,36
R 52,48128
v_mul_f32_e64 v11, v11, |v11|
mav 16,136,36
v_cvt_pk_bf16_f32 v10, v10, v11
v_pk_max_i16 v73, v10, 0
mav 20,140,36
s_waitcnt lgkmcnt(2)
v_pk_max_i16 v75, v10, 0
mav 12,144,40
R 24,49152
mav 16,148,40
v_permlane16_swap_b32 v72, v74
mav 20,152,40
v_permlane16_swap_b32 v73, v75
mav 12,156,44
R 28,50176
mav 16,160,44
mav 20,164,44
s_waitcnt lgkmcnt(2)
mav 12,168,48
buffer_store_dwordx4 v[72:75], v84, s[36:39], s49 offen sc1
R 32,51200
mav 16,172,48
mav 20,176,48
mav 12,180,52
R 36,52224
mav 16,184,52
mav 20,188,52
s_waitcnt lgkmcnt(2)
mav 12,192,24
R 40,53248
mav 16,196,24
mav 20,200,24
mav 12,204,28
R 44,54272
mav 16,208,28
mav 20,212,28
s_waitcnt lgkmcnt(2)
mav 12,216,32
R 48,55296
mav 16,220,32
mav 20,224,32
mav 12,228,36
R 52,56320
mav 16,232,36
mav 20,236,36
s_waitcnt lgkmcnt(2)
mav 12,240,40
R 24,57344
mav 16,244,40
mav 20,248,40
mav 12,252,44
R 28,58368
mvv 16,128,44
mvv 20,132,44
s_waitcnt lgkmcnt(2)
mvv 12,136,48
R 32,59392
mvv 16,140,48
mvv 20,144,48
mvv 12,148,52
R 36,60416
mvv 16,152,52
mvv 20,156,52
s_waitcnt lgkmcnt(2)
mvv 12,160,24
s_mov_b32 s60, -30
.p2align 6
LF2_1:
mvv 16,164,24
R 40,61440
mvv 20,168,24
mvv 12,172,28
R 44,62464
mvv 16,176,28
v_add_u32 v76, 1, v76
mvv 20,180,28
s_waitcnt lgkmcnt(2)
mvv 12,184,32
R 48,63488
mvv 16,188,32
mvv 20,192,32
mvv 12,196,36
R 52,64512
v_xor_b32 v80, 0x10000, v80
mvv 16,200,36
s_waitcnt vmcnt(2) lgkmcnt(2)
s_add_u32 s49, s49, 313344
mvv 20,204,36
s_barrier
mvv 12,208,40
s_add_u32 m0, s50, 32768
R 24,0
mvv 16,212,40
ds_read_b64 v[78:79], v89 offset:128
mvv 20,216,40
D 85,0
mvv 12,220,44
R 28,1024
mvv 16,224,44
mvv 20,228,44
s_waitcnt lgkmcnt(3)
mvv 12,232,48
D 85,1024
mvv 16,236,48
R 32,2048
mvv 20,240,48
mvv 12,244,52
R 36,3072
mvv 16,248,52
D 85,2048
mvv 20,252,52
s_waitcnt lgkmcnt(2)
v_mfma_f32_16x16x32_bf16 v[0:3], a[0:3], v[24:27], 0
R 40,4096
v_fma_f32 v104, v79, v56, v92
v_mfma_f32_16x16x32_bf16 v[4:7], a[4:7], v[24:27], 0
v_fma_f32 v12, v12, v78, v104
v_fma_f32 v105, v79, v57, v93
v_mfma_f32_16x16x32_bf16 v[8:11], a[8:11], v[24:27], 0
D 85,3072
mav 0,12,28
s_add_u32 m0, s50, 36864
R 44,5120
v_fma_f32 v13, v13, v78, v105
mav 4,16,28
s_waitcnt vmcnt(4)
v_mul_f32_e64 v12, v12, |v12|
v_mul_f32_e64 v13, v13, |v13|
mav 8,20,28
s_waitcnt lgkmcnt(2)
v_cvt_pk_bf16_f32 v12, v12, v13
v_pk_max_i16 v68, v12, 0
mav 0,24,32
buffer_store_dword v76, v81, s[36:39], s52 offen sc1
mav 4,28,32
R 48,6144
v_fma_f32 v104, v79, v58, v94
mav 8,32,32
v_fma_f32 v14, v14, v78, v104
v_fma_f32 v105, v79, v59, v95
mav 0,36,36
R 52,7168
v_fma_f32 v15, v15, v78, v105
mav 4,40,36
D 86,0
mav 8,44,36
s_waitcnt lgkmcnt(2)
v_mul_f32_e64 v14, v14, |v14|
v_mul_f32_e64 v15, v15, |v15|
mav 0,48,40
R 24,8192
v_cvt_pk_bf16_f32 v14, v14, v15
mav 4,52,40
v_pk_max_i16 v69, v14, 0
v_fma_f32 v104, v79, v60, v96
mav 8,56,40
D 86,1024
mav 0,60,44
R 28,9216
v_fma_f32 v16, v16, v78, v104
mav 4,64,44
v_fma_f32 v105, v79, v61, v97
v_fma_f32 v17, v17, v78, v105
mav 8,68,44
s_waitcnt lgkmcnt(2)
v_mul_f32_e64 v16, v16, |v16|
v_mul_f32_e64 v17, v17, |v17|
mav 0,72,48
D 86,2048
mav 4,76,48
R 32,10240
v_cvt_pk_bf16_f32 v16, v16, v17
mav 8,80,48
v_pk_max_i16 v70, v16, 0
v_fma_f32 v104, v79, v62, v98
mav 0,84,52
R 36,11264
v_fma_f32 v18, v18, v78, v104
mav 4,88,52
D 86,3072
mav 8,92,52
s_waitcnt lgkmcnt(2)
v_fma_f32 v105, v79, v63, v99
v_fma_f32 v19, v19, v78, v105
mav 0,96,24
R 40,12288
v_mul_f32_e64 v18, v18, |v18|
mav 4,100,24
v_mul_f32_e64 v19, v19, |v19|
v_cvt_pk_bf16_f32 v18, v18, v19
mav 8,104,24
v_pk_max_i16 v71, v18, 0
v_fma_f32 v104, v79, v64, v100
mav 0,108,28
R 44,13312
v_fma_f32 v20, v20, v78, v104
mav 4,112,28
v_fma_f32 v105, v79, v65, v101
v_fma_f32 v21, v21, v78, v105
mav 8,116,28
s_waitcnt lgkmcnt(2)
v_mul_f32_e64 v20, v20, |v20|
v_mul_f32_e64 v21, v21, |v21|
mav 0,120,32
buffer_store_dwordx4 v[68:71], v83, s[36:39], s49 offen sc1
R 48,14336
v_cvt_pk_bf16_f32 v20, v20, v21
mav 4,124,32
v_pk_max_i16 v72, v20, 0
v_pk_max_i16 v74, v20, 0
mav 8,128,32
v_fma_f32 v104, v79, v66, v102
v_fma_f32 v22, v22, v78, v104
mav 0,132,36
R 52,15360
v_fma_f32 v105, v79, v67, v103
mav 4,136,36
v_fma_f32 v23, v23, v78, v105
v_mul_f32_e64 v22, v22, |v22|
mav 8,140,36
s_waitcnt lgkmcnt(2)
v_mul_f32_e64 v23, v23, |v23|
v_cvt_pk_bf16_f32 v22, v22, v23
mav 0,144,40
R 24,16384
v_pk_max_i16 v73, v22, 0
mav 4,148,40
v_pk_max_i16 v75, v22, 0
mav 8,152,40
v_permlane16_swap_b32 v72, v74
mav 0,156,44
R 28,17408
mav 4,160,44
v_permlane16_swap_b32 v73, v75
mav 8,164,44
s_waitcnt lgkmcnt(2)
mav 0,168,48
buffer_store_dwordx4 v[72:75], v84, s[36:39], s49 offen sc1
R 32,18432
mav 4,172,48
mav 8,176,48
mav 0,180,52
R 36,19456
mav 4,184,52
mav 8,188,52
s_waitcnt lgkmcnt(2)
mav 0,192,24
R 40,20480
mav 4,196,24
mav 8,200,24
mav 0,204,28
R 44,21504
mav 4,208,28
mav 8,212,28
s_waitcnt lgkmcnt(2)
mav 0,216,32
R 48,22528
mav 4,220,32
mav 8,224,32
mav 0,228,36
R 52,23552
mav 4,232,36
mav 8,236,36
s_waitcnt lgkmcnt(2)
mav 0,240,40
R 24,24576
mav 4,244,40
mav 8,248,40
mav 0,252,44
R 28,25600
mvv 4,128,44
mvv 8,132,44
s_waitcnt lgkmcnt(2)
mvv 0,136,48
R 32,26624
mvv 4,140,48
mvv 8,144,48
mvv 0,148,52
R 36,27648
mvv 4,152,52
mvv 8,156,52
s_waitcnt lgkmcnt(2)
mvv 0,160,24
R 40,28672
mvv 4,164,24
mvv 8,168,24
mvv 0,172,28
R 44,29696
mvv 4,176,28
v_add_u32 v76, 1, v76
mvv 8,180,28
s_waitcnt lgkmcnt(2)
mvv 0,184,32
s_xor_b32 s50, s50, 0x10000
R 48,30720
mvv 4,188,32
mvv 8,192,32
mvv 0,196,36
R 52,31744
mvv 4,200,36
s_waitcnt vmcnt(2) lgkmcnt(2)
s_add_u32 s49, s49, 313344
mvv 8,204,36
s_barrier
mvv 0,208,40
s_add_u32 m0, s50, 0
R 24,32768
mvv 4,212,40
ds_read_b64 v[78:79], v89 offset:0
mvv 8,216,40
D 85,0
mvv 0,220,44
R 28,33792
mvv 4,224,44
mvv 8,228,44
s_waitcnt lgkmcnt(3)
mvv 0,232,48
D 85,1024
mvv 4,236,48
R 32,34816
mvv 8,240,48
mvv 0,244,52
R 36,35840
mvv 4,248,52
D 85,2048
mvv 8,252,52
s_waitcnt lgkmcnt(2)
v_mfma_f32_16x16x32_bf16 v[12:15], a[0:3], v[24:27], 0
R 40,36864
v_fma_f32 v104, v79, v56, v92
v_mfma_f32_16x16x32_bf16 v[16:19], a[4:7], v[24:27], 0
v_fma_f32 v0, v0, v78, v104
v_fma_f32 v105, v79, v57, v93
v_mfma_f32_16x16x32_bf16 v[20:23], a[8:11], v[24:27], 0
D 85,3072
mav 12,12,28
s_add_u32 m0, s50, 4096
R 44,37888
v_fma_f32 v1, v1, v78, v105
mav 16,16,28
s_waitcnt vmcnt(4)
v_mul_f32_e64 v0, v0, |v0|
v_mul_f32_e64 v1, v1, |v1|
mav 20,20,28
s_waitcnt lgkmcnt(2)
v_cvt_pk_bf16_f32 v0, v0, v1
v_pk_max_i16 v68, v0, 0
mav 12,24,32
buffer_store_dword v76, v81, s[36:39], s52 offen sc1
mav 16,28,32
R 48,38912
v_fma_f32 v104, v79, v58, v94
mav 20,32,32
v_fma_f32 v2, v2, v78, v104
v_fma_f32 v105, v79, v59, v95
mav 12,36,36
R 52,39936
v_fma_f32 v3, v3, v78, v105
mav 16,40,36
D 86,0
mav 20,44,36
s_waitcnt lgkmcnt(2)
v_mul_f32_e64 v2, v2, |v2|
v_mul_f32_e64 v3, v3, |v3|
mav 12,48,40
R 24,40960
v_cvt_pk_bf16_f32 v2, v2, v3
mav 16,52,40
v_pk_max_i16 v69, v2, 0
v_fma_f32 v104, v79, v60, v96
mav 20,56,40
D 86,1024
mav 12,60,44
R 28,41984
v_fma_f32 v4, v4, v78, v104
mav 16,64,44
v_fma_f32 v105, v79, v61, v97
v_fma_f32 v5, v5, v78, v105
mav 20,68,44
s_waitcnt lgkmcnt(2)
v_mul_f32_e64 v4, v4, |v4|
v_mul_f32_e64 v5, v5, |v5|
mav 12,72,48
D 86,2048
mav 16,76,48
R 32,43008
v_cvt_pk_bf16_f32 v4, v4, v5
mav 20,80,48
v_pk_max_i16 v70, v4, 0
v_fma_f32 v104, v79, v62, v98
mav 12,84,52
R 36,44032
v_fma_f32 v6, v6, v78, v104
mav 16,88,52
D 86,3072
mav 20,92,52
s_waitcnt lgkmcnt(2)
v_fma_f32 v105, v79, v63, v99
v_fma_f32 v7, v7, v78, v105
mav 12,96,24
R 40,45056
v_mul_f32_e64 v6, v6, |v6|
mav 16,100,24
v_mul_f32_e64 v7, v7, |v7|
v_cvt_pk_bf16_f32 v6, v6, v7
mav 20,104,24
v_pk_max_i16 v71, v6, 0
v_fma_f32 v104, v79, v64, v100
mav 12,108,28
R 44,46080
v_fma_f32 v8, v8, v78, v104
mav 16,112,28
v_fma_f32 v105, v79, v65, v101
v_fma_f32 v9, v9, v78, v105
mav 20,116,28
s_waitcnt lgkmcnt(2)
v_mul_f32_e64 v8, v8, |v8|
v_mul_f32_e64 v9, v9, |v9|
mav 12,120,32
buffer_store_dwordx4 v[68:71], v83, s[36:39], s49 offen sc1
R 48,47104
v_cvt_pk_bf16_f32 v8, v8, v9
mav 16,124,32
v_pk_max_i16 v72, v8, 0
v_pk_max_i16 v74, v8, 0
mav 20,128,32
v_fma_f32 v104, v79, v66, v102
v_fma_f32 v10, v10, v78, v104
mav 12,132,36
R 52,48128
v_fma_f32 v105, v79, v67, v103
mav 16,136,36
v_fma_f32 v11, v11, v78, v105
v_mul_f32_e64 v10, v10, |v10|
mav 20,140,36
s_waitcnt lgkmcnt(2)
v_mul_f32_e64 v11, v11, |v11|
v_cvt_pk_bf16_f32 v10, v10, v11
mav 12,144,40
R 24,49152
v_pk_max_i16 v73, v10, 0
mav 16,148,40
v_pk_max_i16 v75, v10, 0
mav 20,152,40
v_permlane16_swap_b32 v72, v74
mav 12,156,44
R 28,50176
mav 16,160,44
v_permlane16_swap_b32 v73, v75
mav 20,164,44
s_waitcnt lgkmcnt(2)
mav 12,168,48
buffer_store_dwordx4 v[72:75], v84, s[36:39], s49 offen sc1
R 32,51200
mav 16,172,48
mav 20,176,48
mav 12,180,52
R 36,52224
mav 16,184,52
mav 20,188,52
s_waitcnt lgkmcnt(2)
mav 12,192,24
R 40,53248
mav 16,196,24
mav 20,200,24
mav 12,204,28
R 44,54272
mav 16,208,28
mav 20,212,28
s_waitcnt lgkmcnt(2)
mav 12,216,32
R 48,55296
mav 16,220,32
mav 20,224,32
mav 12,228,36
R 52,56320
mav 16,232,36
mav 20,236,36
s_waitcnt lgkmcnt(2)
mav 12,240,40
R 24,57344
mav 16,244,40
mav 20,248,40
mav 12,252,44
R 28,58368
mvv 16,128,44
mvv 20,132,44
s_waitcnt lgkmcnt(2)
mvv 12,136,48
R 32,59392
mvv 16,140,48
mvv 20,144,48
mvv 12,148,52
R 36,60416
mvv 16,152,52
mvv 20,156,52
s_waitcnt lgkmcnt(2)
s_add_u32 s60, s60, 1
mvv 12,160,24
s_cbranch_scc0 LF2_1
mvv 16,164,24
R 40,61440
mvv 20,168,24
mvv 12,172,28
R 44,62464
mvv 16,176,28
v_add_u32 v76, 1, v76
mvv 20,180,28
s_waitcnt lgkmcnt(2)
mvv 12,184,32
R 48,63488
mvv 16,188,32
mvv 20,192,32
mvv 12,196,36
R 52,64512
v_xor_b32 v80, 0x10000, v80
LsF11:
s_cbranch_execz LsF15
mvv 16,200,36
s_waitcnt vmcnt(2) lgkmcnt(2)
s_add_u32 s49, s49, 313344
mvv 20,204,36
s_barrier
mvv 12,208,40
s_add_u32 m0, s50, 32768
R 24,0
mvv 16,212,40
ds_read_b64 v[78:79], v89 offset:128
mvv 20,216,40
D 85,0
mvv 12,220,44
R 28,1024
mvv 16,224,44
mvv 20,228,44
s_waitcnt lgkmcnt(3)
mvv 12,232,48
D 85,1024
mvv 16,236,48
R 32,2048
mvv 20,240,48
mvv 12,244,52
R 36,3072
mvv 16,248,52
D 85,2048
mvv 20,252,52
s_waitcnt lgkmcnt(2)
v_mfma_f32_16x16x32_bf16 v[0:3], a[0:3], v[24:27], 0
R 40,4096
v_fma_f32 v104, v79, v56, v92
v_mfma_f32_16x16x32_bf16 v[4:7], a[4:7], v[24:27], 0
v_fma_f32 v12, v12, v78, v104
v_fma_f32 v105, v79, v57, v93
v_mfma_f32_16x16x32_bf16 v[8:11], a[8:11], v[24:27], 0
D 85,3072
mav 0,12,28
s_add_u32 m0, s50, 36864
R 44,5120
v_fma_f32 v13, v13, v78, v105
mav 4,16,28
s_waitcnt vmcnt(4)
v_mul_f32_e64 v12, v12, |v12|
v_mul_f32_e64 v13, v13, |v13|
mav 8,20,28
s_waitcnt lgkmcnt(2)
v_cvt_pk_bf16_f32 v12, v12, v13
v_pk_max_i16 v68, v12, 0
mav 0,24,32
buffer_store_dword v76, v81, s[36:39], s52 offen sc1
mav 4,28,32
R 48,6144
v_fma_f32 v104, v79, v58, v94
mav 8,32,32
v_fma_f32 v14, v14, v78, v104
v_fma_f32 v105, v79, v59, v95
mav 0,36,36
R 52,7168
v_fma_f32 v15, v15, v78, v105
mav 4,40,36
D 86,0
mav 8,44,36
s_waitcnt lgkmcnt(2)
v_mul_f32_e64 v14, v14, |v14|
v_mul_f32_e64 v15, v15, |v15|
mav 0,48,40
R 24,8192
v_cvt_pk_bf16_f32 v14, v14, v15
mav 4,52,40
v_pk_max_i16 v69, v14, 0
v_fma_f32 v104, v79, v60, v96
mav 8,56,40
D 86,1024
mav 0,60,44
R 28,9216
v_fma_f32 v16, v16, v78, v104
mav 4,64,44
v_fma_f32 v105, v79, v61, v97
v_fma_f32 v17, v17, v78, v105
mav 8,68,44
s_waitcnt lgkmcnt(2)
v_mul_f32_e64 v16, v16, |v16|
v_mul_f32_e64 v17, v17, |v17|
mav 0,72,48
D 86,2048
mav 4,76,48
R 32,10240
v_cvt_pk_bf16_f32 v16, v16, v17
mav 8,80,48
v_pk_max_i16 v70, v16, 0
v_fma_f32 v104, v79, v62, v98
mav 0,84,52
R 36,11264
v_fma_f32 v18, v18, v78, v104
mav 4,88,52
D 86,3072
mav 8,92,52
s_waitcnt lgkmcnt(2)
v_fma_f32 v105, v79, v63, v99
v_fma_f32 v19, v19, v78, v105
mav 0,96,24
R 40,12288
v_mul_f32_e64 v18, v18, |v18|
mav 4,100,24
v_mul_f32_e64 v19, v19, |v19|
v_cvt_pk_bf16_f32 v18, v18, v19
mav 8,104,24
v_pk_max_i16 v71, v18, 0
v_fma_f32 v104, v79, v64, v100
mav 0,108,28
R 44,13312
v_fma_f32 v20, v20, v78, v104
mav 4,112,28
v_fma_f32 v105, v79, v65, v101
v_fma_f32 v21, v21, v78, v105
mav 8,116,28
s_waitcnt lgkmcnt(2)
v_mul_f32_e64 v20, v20, |v20|
v_mul_f32_e64 v21, v21, |v21|
mav 0,120,32
buffer_store_dwordx4 v[68:71], v83, s[36:39], s49 offen sc1
R 48,14336
v_cvt_pk_bf16_f32 v20, v20, v21
mav 4,124,32
v_pk_max_i16 v72, v20, 0
v_pk_max_i16 v74, v20, 0
mav 8,128,32
v_fma_f32 v104, v79, v66, v102
v_fma_f32 v22, v22, v78, v104
mav 0,132,36
R 52,15360
v_fma_f32 v105, v79, v67, v103
mav 4,136,36
v_fma_f32 v23, v23, v78, v105
v_mul_f32_e64 v22, v22, |v22|
mav 8,140,36
s_waitcnt lgkmcnt(2)
v_mul_f32_e64 v23, v23, |v23|
v_cvt_pk_bf16_f32 v22, v22, v23
mav 0,144,40
R 24,16384
v_pk_max_i16 v73, v22, 0
mav 4,148,40
v_pk_max_i16 v75, v22, 0
mav 8,152,40
v_permlane16_swap_b32 v72, v74
mav 0,156,44
R 28,17408
mav 4,160,44
v_permlane16_swap_b32 v73, v75
mav 8,164,44
s_waitcnt lgkmcnt(2)
mav 0,168,48
buffer_store_dwordx4 v[72:75], v84, s[36:39], s49 offen sc1
R 32,18432
mav 4,172,48
mav 8,176,48
mav 0,180,52
R 36,19456
mav 4,184,52
mav 8,188,52
s_waitcnt lgkmcnt(2)
mav 0,192,24
R 40,20480
mav 4,196,24
mav 8,200,24
mav 0,204,28
R 44,21504
mav 4,208,28
mav 8,212,28
s_waitcnt lgkmcnt(2)
mav 0,216,32
R 48,22528
mav 4,220,32
mav 8,224,32
mav 0,228,36
R 52,23552
mav 4,232,36
mav 8,236,36
s_waitcnt lgkmcnt(2)
mav 0,240,40
R 24,24576
mav 4,244,40
mav 8,248,40
mav 0,252,44
R 28,25600
mvv 4,128,44
mvv 8,132,44
s_waitcnt lgkmcnt(2)
mvv 0,136,48
R 32,26624
mvv 4,140,48
mvv 8,144,48
mvv 0,148,52
R 36,27648
mvv 4,152,52
mvv 8,156,52
s_waitcnt lgkmcnt(2)
mvv 0,160,24
R 40,28672
mvv 4,164,24
mvv 8,168,24
mvv 0,172,28
R 44,29696
mvv 4,176,28
v_add_u32 v76, 1, v76
mvv 8,180,28
s_waitcnt lgkmcnt(2)
mvv 0,184,32
R 48,30720
mvv 4,188,32
mvv 8,192,32
mvv 0,196,36
R 52,31744
mvv 4,200,36
s_waitcnt vmcnt(2) lgkmcnt(2)
s_add_u32 s49, s49, 313344
mvv 8,204,36
s_barrier
mvv 0,208,40
R 24,32768
mvv 4,212,40
ds_read_b64 v[78:79], v89 offset:0
mvv 8,216,40
mvv 0,220,44
R 28,33792
mvv 4,224,44
mvv 8,228,44
s_waitcnt lgkmcnt(3)
mvv 0,232,48
R 32,34816
mvv 4,236,48
mvv 8,240,48
mvv 0,244,52
R 36,35840
mvv 4,248,52
mvv 8,252,52
s_waitcnt lgkmcnt(2)
v_mfma_f32_16x16x32_bf16 v[12:15], a[0:3], v[24:27], 0
R 40,36864
v_fma_f32 v104, v79, v56, v92
v_mfma_f32_16x16x32_bf16 v[16:19], a[4:7], v[24:27], 0
v_fma_f32 v0, v0, v78, v104
v_fma_f32 v105, v79, v57, v93
v_mfma_f32_16x16x32_bf16 v[20:23], a[8:11], v[24:27], 0
v_fma_f32 v1, v1, v78, v105
v_mul_f32_e64 v0, v0, |v0|
mav 12,12,28
R 44,37888
v_mul_f32_e64 v1, v1, |v1|
mav 16,16,28
s_waitcnt vmcnt(0)
v_cvt_pk_bf16_f32 v0, v0, v1
v_pk_max_i16 v68, v0, 0
mav 20,20,28
s_waitcnt lgkmcnt(2)
v_fma_f32 v104, v79, v58, v94
v_fma_f32 v2, v2, v78, v104
mav 12,24,32
buffer_store_dword v76, v81, s[36:39], s52 offen sc1
mav 16,28,32
R 48,38912
v_fma_f32 v105, v79, v59, v95
mav 20,32,32
v_fma_f32 v3, v3, v78, v105
v_mul_f32_e64 v2, v2, |v2|
mav 12,36,36
R 52,39936
v_mul_f32_e64 v3, v3, |v3|
mav 16,40,36
v_cvt_pk_bf16_f32 v2, v2, v3
v_pk_max_i16 v69, v2, 0
mav 20,44,36
s_waitcnt lgkmcnt(2)
v_fma_f32 v104, v79, v60, v96
v_fma_f32 v4, v4, v78, v104
mav 12,48,40
R 24,40960
v_fma_f32 v105, v79, v61, v97
mav 16,52,40
v_fma_f32 v5, v5, v78, v105
v_mul_f32_e64 v4, v4, |v4|
mav 20,56,40
v_mul_f32_e64 v5, v5, |v5|
v_cvt_pk_bf16_f32 v4, v4, v5
mav 12,60,44
)"
R"(
R 28,41984
v_pk_max_i16 v70, v4, 0
mav 16,64,44
v_fma_f32 v104, v79, v62, v98
v_fma_f32 v6, v6, v78, v104
mav 20,68,44
s_waitcnt lgkmcnt(2)
v_fma_f32 v105, v79, v63, v99
v_fma_f32 v7, v7, v78, v105
mav 12,72,48
R 32,43008
v_mul_f32_e64 v6, v6, |v6|
mav 16,76,48
v_mul_f32_e64 v7, v7, |v7|
v_cvt_pk_bf16_f32 v6, v6, v7
mav 20,80,48
v_pk_max_i16 v71, v6, 0
v_fma_f32 v104, v79, v64, v100
mav 12,84,52
R 36,44032
v_fma_f32 v8, v8, v78, v104
mav 16,88,52
buffer_store_dwordx4 v[68:71], v83, s[36:39], s49 offen sc1
v_fma_f32 v105, v79, v65, v101
v_fma_f32 v9, v9, v78, v105
v_mul_f32_e64 v8, v8, |v8|
mav 20,92,52
s_waitcnt lgkmcnt(2)
v_mul_f32_e64 v9, v9, |v9|
v_cvt_pk_bf16_f32 v8, v8, v9
mav 12,96,24
R 40,45056
v_pk_max_i16 v72, v8, 0
mav 16,100,24
v_pk_max_i16 v74, v8, 0
v_fma_f32 v104, v79, v66, v102
mav 20,104,24
v_fma_f32 v10, v10, v78, v104
v_fma_f32 v105, v79, v67, v103
mav 12,108,28
R 44,46080
v_fma_f32 v11, v11, v78, v105
mav 16,112,28
v_mul_f32_e64 v10, v10, |v10|
v_mul_f32_e64 v11, v11, |v11|
mav 20,116,28
s_waitcnt lgkmcnt(2)
v_cvt_pk_bf16_f32 v10, v10, v11
v_pk_max_i16 v73, v10, 0
mav 12,120,32
R 48,47104
v_pk_max_i16 v75, v10, 0
mav 16,124,32
v_permlane16_swap_b32 v72, v74
mav 20,128,32
v_permlane16_swap_b32 v73, v75
mav 12,132,36
R 52,48128
mav 16,136,36
buffer_store_dwordx4 v[72:75], v84, s[36:39], s49 offen sc1
mav 20,140,36
s_waitcnt lgkmcnt(2)
mav 12,144,40
R 24,49152
mav 16,148,40
mav 20,152,40
mav 12,156,44
R 28,50176
mav 16,160,44
mav 20,164,44
s_waitcnt lgkmcnt(2)
mav 12,168,48
R 32,51200
mav 16,172,48
mav 20,176,48
mav 12,180,52
R 36,52224
mav 16,184,52
mav 20,188,52
s_waitcnt lgkmcnt(2)
mav 12,192,24
R 40,53248
mav 16,196,24
mav 20,200,24
mav 12,204,28
R 44,54272
mav 16,208,28
mav 20,212,28
s_waitcnt lgkmcnt(2)
mav 12,216,32
R 48,55296
mav 16,220,32
mav 20,224,32
mav 12,228,36
R 52,56320
mav 16,232,36
mav 20,236,36
s_waitcnt lgkmcnt(2)
mav 12,240,40
R 24,57344
mav 16,244,40
mav 20,248,40
mav 12,252,44
R 28,58368
mvv 16,128,44
mvv 20,132,44
s_waitcnt lgkmcnt(2)
mvv 12,136,48
R 32,59392
mvv 16,140,48
mvv 20,144,48
mvv 12,148,52
R 36,60416
mvv 16,152,52
mvv 20,156,52
s_waitcnt lgkmcnt(2)
mvv 12,160,24
mvv 16,164,24
R 40,61440
mvv 20,168,24
mvv 12,172,28
R 44,62464
mvv 16,176,28
v_add_u32 v76, 1, v76
mvv 20,180,28
s_waitcnt lgkmcnt(2)
mvv 12,184,32
R 48,63488
mvv 16,188,32
mvv 20,192,32
mvv 12,196,36
R 52,64512
mvv 16,200,36
s_waitcnt lgkmcnt(2)
s_add_u32 s49, s49, 313344
mvv 20,204,36
s_barrier
mvv 12,208,40
mvv 16,212,40
ds_read_b64 v[78:79], v89 offset:128
mvv 20,216,40
mvv 12,220,44
mvv 16,224,44
mvv 20,228,44
s_waitcnt lgkmcnt(1)
mvv 12,232,48
mvv 16,236,48
mvv 20,240,48
mvv 12,244,52
mvv 16,248,52
mvv 20,252,52
s_waitcnt lgkmcnt(0)
v_fma_f32 v104, v79, v56, v92
s_nop 3
v_fma_f32 v12, v12, v78, v104
v_fma_f32 v105, v79, v57, v93
v_fma_f32 v13, v13, v78, v105
v_mul_f32_e64 v12, v12, |v12|
v_mul_f32_e64 v13, v13, |v13|
v_cvt_pk_bf16_f32 v12, v12, v13
v_pk_max_i16 v68, v12, 0
v_fma_f32 v104, v79, v58, v94
v_fma_f32 v14, v14, v78, v104
v_fma_f32 v105, v79, v59, v95
v_fma_f32 v15, v15, v78, v105
v_mul_f32_e64 v14, v14, |v14|
v_mul_f32_e64 v15, v15, |v15|
v_cvt_pk_bf16_f32 v14, v14, v15
v_pk_max_i16 v69, v14, 0
v_fma_f32 v104, v79, v60, v96
v_fma_f32 v16, v16, v78, v104
v_fma_f32 v105, v79, v61, v97
v_fma_f32 v17, v17, v78, v105
v_mul_f32_e64 v16, v16, |v16|
v_mul_f32_e64 v17, v17, |v17|
v_cvt_pk_bf16_f32 v16, v16, v17
v_pk_max_i16 v70, v16, 0
v_fma_f32 v104, v79, v62, v98
v_fma_f32 v18, v18, v78, v104
v_fma_f32 v105, v79, v63, v99
v_fma_f32 v19, v19, v78, v105
v_mul_f32_e64 v18, v18, |v18|
v_mul_f32_e64 v19, v19, |v19|
v_cvt_pk_bf16_f32 v18, v18, v19
v_pk_max_i16 v71, v18, 0
buffer_store_dwordx4 v[68:71], v83, s[36:39], s49 offen sc1
v_fma_f32 v104, v79, v64, v100
v_fma_f32 v20, v20, v78, v104
v_fma_f32 v105, v79, v65, v101
v_fma_f32 v21, v21, v78, v105
v_mul_f32_e64 v20, v20, |v20|
v_mul_f32_e64 v21, v21, |v21|
v_cvt_pk_bf16_f32 v20, v20, v21
v_pk_max_i16 v72, v20, 0
v_pk_max_i16 v74, v20, 0
v_fma_f32 v104, v79, v66, v102
v_fma_f32 v22, v22, v78, v104
v_fma_f32 v105, v79, v67, v103
v_fma_f32 v23, v23, v78, v105
v_mul_f32_e64 v22, v22, |v22|
v_mul_f32_e64 v23, v23, |v23|
v_cvt_pk_bf16_f32 v22, v22, v23
v_pk_max_i16 v73, v22, 0
v_pk_max_i16 v75, v22, 0
v_permlane16_swap_b32 v72, v74
s_nop 0
v_permlane16_swap_b32 v73, v75
buffer_store_dwordx4 v[72:75], v84, s[36:39], s49 offen sc1
s_waitcnt vmcnt(2)
buffer_store_dword v76, v81, s[36:39], s52 offen sc1
v_add_u32 v76, 37, v76
s_waitcnt vmcnt(1)
buffer_store_dword v76, v81, s[36:39], s52 offen sc1
s_endpgm
LvF3:
LsF12:
s_cbranch_execz LwF
s_add_u32 s49, s49, 313344
s_barrier
s_add_u32 m0, s50, 0
s_nop 0
D 85,0
D 85,1024
D 85,2048
D 85,3072
s_add_u32 m0, s50, 4096
s_nop 0
D 86,0
D 86,1024
D 86,2048
D 86,3072
s_waitcnt vmcnt(0)
s_add_u32 s49, s49, 313344
s_barrier
s_add_u32 m0, s50, 32768
R 24,0
D 85,0
R 28,1024
D 85,1024
R 32,2048
R 36,3072
D 85,2048
s_waitcnt lgkmcnt(2)
v_mfma_f32_16x16x32_bf16 v[0:3], a[0:3], v[24:27], 0
R 40,4096
v_mfma_f32_16x16x32_bf16 v[4:7], a[4:7], v[24:27], 0
v_mfma_f32_16x16x32_bf16 v[8:11], a[8:11], v[24:27], 0
mav 0,12,28
D 85,3072
mav 4,16,28
s_add_u32 m0, s50, 36864
R 44,5120
mav 8,20,28
s_waitcnt lgkmcnt(2)
mav 0,24,32
R 48,6144
mav 4,28,32
D 86,0
mav 8,32,32
LsF13:
s_cbranch_execz LwF
mav 0,36,36
R 52,7168
mav 4,40,36
mav 8,44,36
s_waitcnt lgkmcnt(2)
D 86,1024
mav 0,48,40
R 24,8192
mav 4,52,40
mav 8,56,40
mav 0,60,44
D 86,2048
mav 4,64,44
R 28,9216
mav 8,68,44
s_waitcnt lgkmcnt(2)
mav 0,72,48
R 32,10240
mav 4,76,48
D 86,3072
mav 8,80,48
mav 0,84,52
R 36,11264
mav 4,88,52
mav 8,92,52
s_waitcnt lgkmcnt(2)
mav 0,96,24
R 40,12288
mav 4,100,24
mav 8,104,24
mav 0,108,28
R 44,13312
mav 4,112,28
mav 8,116,28
s_waitcnt lgkmcnt(2)
mav 0,120,32
R 48,14336
mav 4,124,32
mav 8,128,32
mav 0,132,36
R 52,15360
mav 4,136,36
mav 8,140,36
s_waitcnt lgkmcnt(2)
mav 0,144,40
R 24,16384
mav 4,148,40
mav 8,152,40
mav 0,156,44
R 28,17408
mav 4,160,44
mav 8,164,44
s_waitcnt lgkmcnt(2)
mav 0,168,48
R 32,18432
mav 4,172,48
mav 8,176,48
mav 0,180,52
R 36,19456
mav 4,184,52
mav 8,188,52
s_waitcnt lgkmcnt(2)
mav 0,192,24
R 40,20480
mav 4,196,24
mav 8,200,24
mav 0,204,28
R 44,21504
mav 4,208,28
mav 8,212,28
s_waitcnt lgkmcnt(2)
mav 0,216,32
R 48,22528
mav 4,220,32
mav 8,224,32
mav 0,228,36
R 52,23552
mav 4,232,36
mav 8,236,36
s_waitcnt lgkmcnt(2)
mav 0,240,40
R 24,24576
mav 4,244,40
mav 8,248,40
mav 0,252,44
R 28,25600
mvv 4,128,44
mvv 8,132,44
s_waitcnt lgkmcnt(2)
mvv 0,136,48
R 32,26624
mvv 4,140,48
mvv 8,144,48
mvv 0,148,52
R 36,27648
mvv 4,152,52
mvv 8,156,52
s_waitcnt lgkmcnt(2)
mvv 0,160,24
R 40,28672
mvv 4,164,24
mvv 8,168,24
mvv 0,172,28
R 44,29696
mvv 4,176,28
mvv 8,180,28
s_waitcnt lgkmcnt(2)
mvv 0,184,32
s_xor_b32 s50, s50, 0x10000
R 48,30720
mvv 4,188,32
mvv 8,192,32
mvv 0,196,36
R 52,31744
mvv 4,200,36
s_waitcnt vmcnt(0) lgkmcnt(2)
s_add_u32 s49, s49, 313344
mvv 8,204,36
s_barrier
mvv 0,208,40
s_add_u32 m0, s50, 0
R 24,32768
mvv 4,212,40
ds_read_b64 v[78:79], v89 offset:0
mvv 8,216,40
mvv 0,220,44
D 85,0
mvv 4,224,44
R 28,33792
mvv 8,228,44
s_waitcnt lgkmcnt(3)
mvv 0,232,48
R 32,34816
mvv 4,236,48
D 85,1024
mvv 8,240,48
mvv 0,244,52
R 36,35840
mvv 4,248,52
mvv 8,252,52
s_waitcnt lgkmcnt(2)
D 85,2048
v_mfma_f32_16x16x32_bf16 v[12:15], a[0:3], v[24:27], 0
R 40,36864
v_fma_f32 v104, v79, v56, v92
v_mfma_f32_16x16x32_bf16 v[16:19], a[4:7], v[24:27], 0
v_fma_f32 v0, v0, v78, v104
v_fma_f32 v105, v79, v57, v93
v_mfma_f32_16x16x32_bf16 v[20:23], a[8:11], v[24:27], 0
v_fma_f32 v1, v1, v78, v105
v_mul_f32_e64 v0, v0, |v0|
mav 12,12,28
D 85,3072
mav 16,16,28
s_add_u32 m0, s50, 4096
R 44,37888
v_mul_f32_e64 v1, v1, |v1|
mav 20,20,28
s_waitcnt lgkmcnt(2)
v_cvt_pk_bf16_f32 v0, v0, v1
v_pk_max_i16 v68, v0, 0
mav 12,24,32
R 48,38912
v_fma_f32 v104, v79, v58, v94
mav 16,28,32
D 86,0
mav 20,32,32
v_fma_f32 v2, v2, v78, v104
v_fma_f32 v105, v79, v59, v95
mav 12,36,36
R 52,39936
v_fma_f32 v3, v3, v78, v105
mav 16,40,36
v_mul_f32_e64 v2, v2, |v2|
v_mul_f32_e64 v3, v3, |v3|
mav 20,44,36
s_waitcnt lgkmcnt(2)
D 86,1024
mav 12,48,40
R 24,40960
v_cvt_pk_bf16_f32 v2, v2, v3
mav 16,52,40
v_pk_max_i16 v69, v2, 0
v_fma_f32 v104, v79, v60, v96
mav 20,56,40
v_fma_f32 v4, v4, v78, v104
v_fma_f32 v105, v79, v61, v97
mav 12,60,44
D 86,2048
mav 16,64,44
R 28,41984
v_fma_f32 v5, v5, v78, v105
mav 20,68,44
s_waitcnt lgkmcnt(2)
v_mul_f32_e64 v4, v4, |v4|
v_mul_f32_e64 v5, v5, |v5|
mav 12,72,48
R 32,43008
v_cvt_pk_bf16_f32 v4, v4, v5
mav 16,76,48
D 86,3072
mav 20,80,48
v_pk_max_i16 v70, v4, 0
v_fma_f32 v104, v79, v62, v98
mav 12,84,52
R 36,44032
v_fma_f32 v6, v6, v78, v104
mav 16,88,52
v_fma_f32 v105, v79, v63, v99
v_fma_f32 v7, v7, v78, v105
mav 20,92,52
s_waitcnt lgkmcnt(2)
v_mul_f32_e64 v6, v6, |v6|
v_mul_f32_e64 v7, v7, |v7|
mav 12,96,24
R 40,45056
v_cvt_pk_bf16_f32 v6, v6, v7
mav 16,100,24
v_pk_max_i16 v71, v6, 0
v_fma_f32 v104, v79, v64, v100
mav 20,104,24
v_fma_f32 v8, v8, v78, v104
v_fma_f32 v105, v79, v65, v101
mav 12,108,28
buffer_store_dwordx4 v[68:71], v83, s[36:39], s49 offen sc1
R 44,46080
v_fma_f32 v9, v9, v78, v105
mav 16,112,28
v_mul_f32_e64 v8, v8, |v8|
v_mul_f32_e64 v9, v9, |v9|
mav 20,116,28
s_waitcnt lgkmcnt(2)
v_cvt_pk_bf16_f32 v8, v8, v9
v_pk_max_i16 v72, v8, 0
mav 12,120,32
R 48,47104
v_pk_max_i16 v74, v8, 0
mav 16,124,32
v_fma_f32 v104, v79, v66, v102
v_fma_f32 v10, v10, v78, v104
mav 20,128,32
v_fma_f32 v105, v79, v67, v103
v_fma_f32 v11, v11, v78, v105
mav 12,132,36
R 52,48128
v_mul_f32_e64 v10, v10, |v10|
mav 16,136,36
v_mul_f32_e64 v11, v11, |v11|
v_cvt_pk_bf16_f32 v10, v10, v11
mav 20,140,36
s_waitcnt lgkmcnt(2)
v_pk_max_i16 v73, v10, 0
v_pk_max_i16 v75, v10, 0
mav 12,144,40
R 24,49152
mav 16,148,40
v_permlane16_swap_b32 v72, v74
mav 20,152,40
v_permlane16_swap_b32 v73, v75
mav 12,156,44
buffer_store_dwordx4 v[72:75], v84, s[36:39], s49 offen sc1
R 28,50176
mav 16,160,44
mav 20,164,44
s_waitcnt lgkmcnt(2)
mav 12,168,48
R 32,51200
mav 16,172,48
mav 20,176,48
mav 12,180,52
R 36,52224
mav 16,184,52
mav 20,188,52
s_waitcnt lgkmcnt(2)
mav 12,192,24
R 40,53248
mav 16,196,24
mav 20,200,24
mav 12,204,28
R 44,54272
mav 16,208,28
mav 20,212,28
s_waitcnt lgkmcnt(2)
mav 12,216,32
R 48,55296
mav 16,220,32
mav 20,224,32
mav 12,228,36
R 52,56320
mav 16,232,36
mav 20,236,36
s_waitcnt lgkmcnt(2)
mav 12,240,40
R 24,57344
mav 16,244,40
mav 20,248,40
mav 12,252,44
R 28,58368
mvv 16,128,44
mvv 20,132,44
s_waitcnt lgkmcnt(2)
mvv 12,136,48
R 32,59392
mvv 16,140,48
mvv 20,144,48
mvv 12,148,52
R 36,60416
mvv 16,152,52
mvv 20,156,52
s_waitcnt lgkmcnt(2)
mvv 12,160,24
s_mov_b32 s60, -30
.p2align 6
LF3_1:
mvv 16,164,24
R 40,61440
mvv 20,168,24
mvv 12,172,28
R 44,62464
mvv 16,176,28
v_add_u32 v76, 1, v76
mvv 20,180,28
s_waitcnt lgkmcnt(2)
mvv 12,184,32
R 48,63488
mvv 16,188,32
mvv 20,192,32
mvv 12,196,36
R 52,64512
v_xor_b32 v80, 0x10000, v80
mvv 16,200,36
s_waitcnt vmcnt(2) lgkmcnt(2)
s_add_u32 s49, s49, 313344
mvv 20,204,36
s_barrier
mvv 12,208,40
s_add_u32 m0, s50, 32768
R 24,0
mvv 16,212,40
ds_read_b64 v[78:79], v89 offset:128
mvv 20,216,40
mvv 12,220,44
D 85,0
mvv 16,224,44
R 28,1024
mvv 20,228,44
s_waitcnt lgkmcnt(3)
mvv 12,232,48
R 32,2048
mvv 16,236,48
D 85,1024
mvv 20,240,48
mvv 12,244,52
R 36,3072
mvv 16,248,52
mvv 20,252,52
s_waitcnt lgkmcnt(2)
D 85,2048
v_mfma_f32_16x16x32_bf16 v[0:3], a[0:3], v[24:27], 0
R 40,4096
v_fma_f32 v104, v79, v56, v92
v_mfma_f32_16x16x32_bf16 v[4:7], a[4:7], v[24:27], 0
v_fma_f32 v12, v12, v78, v104
v_fma_f32 v105, v79, v57, v93
v_mfma_f32_16x16x32_bf16 v[8:11], a[8:11], v[24:27], 0
v_fma_f32 v13, v13, v78, v105
v_mul_f32_e64 v12, v12, |v12|
mav 0,12,28
D 85,3072
mav 4,16,28
s_waitcnt vmcnt(4)
R 44,5120
s_add_u32 m0, s50, 36864
mav 8,20,28
s_waitcnt lgkmcnt(2)
v_mul_f32_e64 v13, v13, |v13|
v_cvt_pk_bf16_f32 v12, v12, v13
mav 0,24,32
R 48,6144
v_pk_max_i16 v68, v12, 0
mav 4,28,32
buffer_store_dword v76, v81, s[36:39], s52 offen sc1
mav 8,32,32
v_fma_f32 v104, v79, v58, v94
v_fma_f32 v14, v14, v78, v104
mav 0,36,36
R 52,7168
v_fma_f32 v105, v79, v59, v95
mav 4,40,36
v_fma_f32 v15, v15, v78, v105
v_mul_f32_e64 v14, v14, |v14|
mav 8,44,36
s_waitcnt lgkmcnt(2)
D 86,0
mav 0,48,40
R 24,8192
v_mul_f32_e64 v15, v15, |v15|
mav 4,52,40
v_cvt_pk_bf16_f32 v14, v14, v15
v_pk_max_i16 v69, v14, 0
mav 8,56,40
v_fma_f32 v104, v79, v60, v96
v_fma_f32 v16, v16, v78, v104
mav 0,60,44
D 86,1024
mav 4,64,44
R 28,9216
v_fma_f32 v105, v79, v61, v97
mav 8,68,44
s_waitcnt lgkmcnt(2)
v_fma_f32 v17, v17, v78, v105
v_mul_f32_e64 v16, v16, |v16|
mav 0,72,48
R 32,10240
v_mul_f32_e64 v17, v17, |v17|
mav 4,76,48
D 86,2048
mav 8,80,48
v_cvt_pk_bf16_f32 v16, v16, v17
v_pk_max_i16 v70, v16, 0
mav 0,84,52
R 36,11264
v_fma_f32 v104, v79, v62, v98
mav 4,88,52
v_fma_f32 v18, v18, v78, v104
v_fma_f32 v105, v79, v63, v99
mav 8,92,52
s_waitcnt lgkmcnt(2)
D 86,3072
mav 0,96,24
R 40,12288
v_fma_f32 v19, v19, v78, v105
mav 4,100,24
v_mul_f32_e64 v18, v18, |v18|
v_mul_f32_e64 v19, v19, |v19|
mav 8,104,24
v_cvt_pk_bf16_f32 v18, v18, v19
v_pk_max_i16 v71, v18, 0
mav 0,108,28
buffer_store_dwordx4 v[68:71], v83, s[36:39], s49 offen sc1
R 44,13312
v_fma_f32 v104, v79, v64, v100
mav 4,112,28
v_fma_f32 v20, v20, v78, v104
v_fma_f32 v105, v79, v65, v101
mav 8,116,28
s_waitcnt lgkmcnt(2)
v_fma_f32 v21, v21, v78, v105
v_mul_f32_e64 v20, v20, |v20|
mav 0,120,32
R 48,14336
v_mul_f32_e64 v21, v21, |v21|
mav 4,124,32
v_cvt_pk_bf16_f32 v20, v20, v21
v_pk_max_i16 v72, v20, 0
mav 8,128,32
v_pk_max_i16 v74, v20, 0
v_fma_f32 v104, v79, v66, v102
mav 0,132,36
R 52,15360
v_fma_f32 v22, v22, v78, v104
mav 4,136,36
v_fma_f32 v105, v79, v67, v103
v_fma_f32 v23, v23, v78, v105
mav 8,140,36
s_waitcnt lgkmcnt(2)
v_mul_f32_e64 v22, v22, |v22|
v_mul_f32_e64 v23, v23, |v23|
mav 0,144,40
R 24,16384
v_cvt_pk_bf16_f32 v22, v22, v23
mav 4,148,40
v_pk_max_i16 v73, v22, 0
v_pk_max_i16 v75, v22, 0
mav 8,152,40
v_permlane16_swap_b32 v72, v74
mav 0,156,44
R 28,17408
mav 4,160,44
v_permlane16_swap_b32 v73, v75
mav 8,164,44
s_waitcnt lgkmcnt(2)
mav 0,168,48
R 32,18432
mav 4,172,48
buffer_store_dwordx4 v[72:75], v84, s[36:39], s49 offen sc1
mav 8,176,48
mav 0,180,52
R 36,19456
mav 4,184,52
mav 8,188,52
s_waitcnt lgkmcnt(2)
mav 0,192,24
R 40,20480
mav 4,196,24
mav 8,200,24
mav 0,204,28
R 44,21504
mav 4,208,28
mav 8,212,28
s_waitcnt lgkmcnt(2)
mav 0,216,32
R 48,22528
mav 4,220,32
mav 8,224,32
mav 0,228,36
R 52,23552
mav 4,232,36
mav 8,236,36
s_waitcnt lgkmcnt(2)
mav 0,240,40
R 24,24576
mav 4,244,40
mav 8,248,40
mav 0,252,44
R 28,25600
mvv 4,128,44
mvv 8,132,44
s_waitcnt lgkmcnt(2)
mvv 0,136,48
R 32,26624
mvv 4,140,48
mvv 8,144,48
mvv 0,148,52
R 36,27648
mvv 4,152,52
mvv 8,156,52
s_waitcnt lgkmcnt(2)
mvv 0,160,24
R 40,28672
mvv 4,164,24
mvv 8,168,24
mvv 0,172,28
LsF14:
s_cbranch_execz LwF
R 44,29696
mvv 4,176,28
v_add_u32 v76, 1, v76
mvv 8,180,28
s_waitcnt lgkmcnt(2)
mvv 0,184,32
s_xor_b32 s50, s50, 0x10000
R 48,30720
mvv 4,188,32
mvv 8,192,32
mvv 0,196,36
R 52,31744
mvv 4,200,36
s_waitcnt vmcnt(2) lgkmcnt(2)
s_add_u32 s49, s49, 313344
mvv 8,204,36
s_barrier
mvv 0,208,40
s_add_u32 m0, s50, 0
R 24,32768
mvv 4,212,40
ds_read_b64 v[78:79], v89 offset:0
mvv 8,216,40
mvv 0,220,44
D 85,0
mvv 4,224,44
R 28,33792
mvv 8,228,44
s_waitcnt lgkmcnt(3)
mvv 0,232,48
R 32,34816
mvv 4,236,48
D 85,1024
mvv 8,240,48
mvv 0,244,52
R 36,35840
mvv 4,248,52
mvv 8,252,52
s_waitcnt lgkmcnt(2)
D 85,2048
v_mfma_f32_16x16x32_bf16 v[12:15], a[0:3], v[24:27], 0
R 40,36864
v_fma_f32 v104, v79, v56, v92
v_mfma_f32_16x16x32_bf16 v[16:19], a[4:7], v[24:27], 0
v_fma_f32 v0, v0, v78, v104
v_fma_f32 v105, v79, v57, v93
v_mfma_f32_16x16x32_bf16 v[20:23], a[8:11], v[24:27], 0
v_fma_f32 v1, v1, v78, v105
v_mul_f32_e64 v0, v0, |v0|
mav 12,12,28
D 85,3072
mav 16,16,28
s_waitcnt vmcnt(4)
R 44,37888
s_add_u32 m0, s50, 4096
mav 20,20,28
s_waitcnt lgkmcnt(2)
v_mul_f32_e64 v1, v1, |v1|
v_cvt_pk_bf16_f32 v0, v0, v1
mav 12,24,32
R 48,38912
v_pk_max_i16 v68, v0, 0
mav 16,28,32
buffer_store_dword v76, v81, s[36:39], s52 offen sc1
mav 20,32,32
v_fma_f32 v104, v79, v58, v94
v_fma_f32 v2, v2, v78, v104
mav 12,36,36
R 52,39936
v_fma_f32 v105, v79, v59, v95
mav 16,40,36
v_fma_f32 v3, v3, v78, v105
v_mul_f32_e64 v2, v2, |v2|
mav 20,44,36
s_waitcnt lgkmcnt(2)
D 86,0
mav 12,48,40
R 24,40960
v_mul_f32_e64 v3, v3, |v3|
mav 16,52,40
v_cvt_pk_bf16_f32 v2, v2, v3
v_pk_max_i16 v69, v2, 0
mav 20,56,40
v_fma_f32 v104, v79, v60, v96
v_fma_f32 v4, v4, v78, v104
mav 12,60,44
D 86,1024
mav 16,64,44
R 28,41984
v_fma_f32 v105, v79, v61, v97
mav 20,68,44
s_waitcnt lgkmcnt(2)
v_fma_f32 v5, v5, v78, v105
v_mul_f32_e64 v4, v4, |v4|
mav 12,72,48
R 32,43008
v_mul_f32_e64 v5, v5, |v5|
mav 16,76,48
D 86,2048
mav 20,80,48
v_cvt_pk_bf16_f32 v4, v4, v5
v_pk_max_i16 v70, v4, 0
mav 12,84,52
R 36,44032
v_fma_f32 v104, v79, v62, v98
mav 16,88,52
v_fma_f32 v6, v6, v78, v104
v_fma_f32 v105, v79, v63, v99
mav 20,92,52
s_waitcnt lgkmcnt(2)
D 86,3072
mav 12,96,24
R 40,45056
v_fma_f32 v7, v7, v78, v105
mav 16,100,24
v_mul_f32_e64 v6, v6, |v6|
v_mul_f32_e64 v7, v7, |v7|
mav 20,104,24
v_cvt_pk_bf16_f32 v6, v6, v7
v_pk_max_i16 v71, v6, 0
mav 12,108,28
buffer_store_dwordx4 v[68:71], v83, s[36:39], s49 offen sc1
R 44,46080
v_fma_f32 v104, v79, v64, v100
mav 16,112,28
v_fma_f32 v8, v8, v78, v104
v_fma_f32 v105, v79, v65, v101
mav 20,116,28
s_waitcnt lgkmcnt(2)
v_fma_f32 v9, v9, v78, v105
v_mul_f32_e64 v8, v8, |v8|
mav 12,120,32
R 48,47104
v_mul_f32_e64 v9, v9, |v9|
mav 16,124,32
v_cvt_pk_bf16_f32 v8, v8, v9
v_pk_max_i16 v72, v8, 0
mav 20,128,32
v_pk_max_i16 v74, v8, 0
v_fma_f32 v104, v79, v66, v102
mav 12,132,36
R 52,48128
v_fma_f32 v10, v10, v78, v104
mav 16,136,36
v_fma_f32 v105, v79, v67, v103
v_fma_f32 v11, v11, v78, v105
mav 20,140,36
s_waitcnt lgkmcnt(2)
v_mul_f32_e64 v10, v10, |v10|
v_mul_f32_e64 v11, v11, |v11|
mav 12,144,40
R 24,49152
v_cvt_pk_bf16_f32 v10, v10, v11
mav 16,148,40
v_pk_max_i16 v73, v10, 0
v_pk_max_i16 v75, v10, 0
mav 20,152,40
v_permlane16_swap_b32 v72, v74
mav 12,156,44
R 28,50176
mav 16,160,44
v_permlane16_swap_b32 v73, v75
mav 20,164,44
s_waitcnt lgkmcnt(2)
mav 12,168,48
R 32,51200
mav 16,172,48
buffer_store_dwordx4 v[72:75], v84, s[36:39], s49 offen sc1
mav 20,176,48
mav 12,180,52
R 36,52224
mav 16,184,52
mav 20,188,52
s_waitcnt lgkmcnt(2)
mav 12,192,24
R 40,53248
mav 16,196,24
mav 20,200,24
mav 12,204,28
R 44,54272
mav 16,208,28
mav 20,212,28
s_waitcnt lgkmcnt(2)
mav 12,216,32
R 48,55296
mav 16,220,32
mav 20,224,32
mav 12,228,36
R 52,56320
mav 16,232,36
mav 20,236,36
s_waitcnt lgkmcnt(2)
mav 12,240,40
R 24,57344
mav 16,244,40
mav 20,248,40
mav 12,252,44
R 28,58368
mvv 16,128,44
mvv 20,132,44
s_waitcnt lgkmcnt(2)
mvv 12,136,48
R 32,59392
mvv 16,140,48
mvv 20,144,48
mvv 12,148,52
R 36,60416
mvv 16,152,52
mvv 20,156,52
s_waitcnt lgkmcnt(2)
s_add_u32 s60, s60, 1
mvv 12,160,24
s_cbranch_scc0 LF3_1
mvv 16,164,24
R 40,61440
mvv 20,168,24
mvv 12,172,28
R 44,62464
mvv 16,176,28
v_add_u32 v76, 1, v76
mvv 20,180,28
s_waitcnt lgkmcnt(2)
mvv 12,184,32
R 48,63488
mvv 16,188,32
mvv 20,192,32
mvv 12,196,36
R 52,64512
v_xor_b32 v80, 0x10000, v80
mvv 16,200,36
s_waitcnt vmcnt(2) lgkmcnt(2)
s_add_u32 s49, s49, 313344
mvv 20,204,36
s_barrier
mvv 12,208,40
s_add_u32 m0, s50, 32768
R 24,0
mvv 16,212,40
ds_read_b64 v[78:79], v89 offset:128
mvv 20,216,40
mvv 12,220,44
D 85,0
mvv 16,224,44
R 28,1024
mvv 20,228,44
s_waitcnt lgkmcnt(3)
mvv 12,232,48
R 32,2048
mvv 16,236,48
D 85,1024
mvv 20,240,48
mvv 12,244,52
R 36,3072
mvv 16,248,52
mvv 20,252,52
s_waitcnt lgkmcnt(2)
D 85,2048
v_mfma_f32_16x16x32_bf16 v[0:3], a[0:3], v[24:27], 0
R 40,4096
v_fma_f32 v104, v79, v56, v92
v_mfma_f32_16x16x32_bf16 v[4:7], a[4:7], v[24:27], 0
v_fma_f32 v12, v12, v78, v104
v_fma_f32 v105, v79, v57, v93
v_mfma_f32_16x16x32_bf16 v[8:11], a[8:11], v[24:27], 0
v_fma_f32 v13, v13, v78, v105
v_mul_f32_e64 v12, v12, |v12|
mav 0,12,28
D 85,3072
mav 4,16,28
s_waitcnt vmcnt(4)
R 44,5120
s_add_u32 m0, s50, 36864
mav 8,20,28
s_waitcnt lgkmcnt(2)
v_mul_f32_e64 v13, v13, |v13|
v_cvt_pk_bf16_f32 v12, v12, v13
mav 0,24,32
R 48,6144
v_pk_max_i16 v68, v12, 0
mav 4,28,32
buffer_store_dword v76, v81, s[36:39], s52 offen sc1
mav 8,32,32
v_fma_f32 v104, v79, v58, v94
v_fma_f32 v14, v14, v78, v104
mav 0,36,36
R 52,7168
v_fma_f32 v105, v79, v59, v95
mav 4,40,36
v_fma_f32 v15, v15, v78, v105
v_mul_f32_e64 v14, v14, |v14|
mav 8,44,36
s_waitcnt lgkmcnt(2)
D 86,0
mav 0,48,40
R 24,8192
v_mul_f32_e64 v15, v15, |v15|
mav 4,52,40
v_cvt_pk_bf16_f32 v14, v14, v15
v_pk_max_i16 v69, v14, 0
mav 8,56,40
v_fma_f32 v104, v79, v60, v96
v_fma_f32 v16, v16, v78, v104
mav 0,60,44
D 86,1024
mav 4,64,44
R 28,9216
v_fma_f32 v105, v79, v61, v97
mav 8,68,44
s_waitcnt lgkmcnt(2)
v_fma_f32 v17, v17, v78, v105
v_mul_f32_e64 v16, v16, |v16|
mav 0,72,48
R 32,10240
v_mul_f32_e64 v17, v17, |v17|
mav 4,76,48
D 86,2048
mav 8,80,48
v_cvt_pk_bf16_f32 v16, v16, v17
v_pk_max_i16 v70, v16, 0
mav 0,84,52
R 36,11264
v_fma_f32 v104, v79, v62, v98
mav 4,88,52
v_fma_f32 v18, v18, v78, v104
v_fma_f32 v105, v79, v63, v99
mav 8,92,52
s_waitcnt lgkmcnt(2)
D 86,3072
mav 0,96,24
R 40,12288
v_fma_f32 v19, v19, v78, v105
mav 4,100,24
v_mul_f32_e64 v18, v18, |v18|
v_mul_f32_e64 v19, v19, |v19|
mav 8,104,24
v_cvt_pk_bf16_f32 v18, v18, v19
v_pk_max_i16 v71, v18, 0
mav 0,108,28
buffer_store_dwordx4 v[68:71], v83, s[36:39], s49 offen sc1
R 44,13312
v_fma_f32 v104, v79, v64, v100
mav 4,112,28
v_fma_f32 v20, v20, v78, v104
v_fma_f32 v105, v79, v65, v101
mav 8,116,28
s_waitcnt lgkmcnt(2)
v_fma_f32 v21, v21, v78, v105
v_mul_f32_e64 v20, v20, |v20|
mav 0,120,32
R 48,14336
v_mul_f32_e64 v21, v21, |v21|
mav 4,124,32
v_cvt_pk_bf16_f32 v20, v20, v21
v_pk_max_i16 v72, v20, 0
mav 8,128,32
v_pk_max_i16 v74, v20, 0
v_fma_f32 v104, v79, v66, v102
mav 0,132,36
R 52,15360
v_fma_f32 v22, v22, v78, v104
mav 4,136,36
v_fma_f32 v105, v79, v67, v103
v_fma_f32 v23, v23, v78, v105
mav 8,140,36
s_waitcnt lgkmcnt(2)
v_mul_f32_e64 v22, v22, |v22|
v_mul_f32_e64 v23, v23, |v23|
mav 0,144,40
R 24,16384
v_cvt_pk_bf16_f32 v22, v22, v23
mav 4,148,40
v_pk_max_i16 v73, v22, 0
v_pk_max_i16 v75, v22, 0
mav 8,152,40
v_permlane16_swap_b32 v72, v74
mav 0,156,44
R 28,17408
mav 4,160,44
v_permlane16_swap_b32 v73, v75
mav 8,164,44
s_waitcnt lgkmcnt(2)
mav 0,168,48
R 32,18432
mav 4,172,48
buffer_store_dwordx4 v[72:75], v84, s[36:39], s49 offen sc1
mav 8,176,48
mav 0,180,52
R 36,19456
mav 4,184,52
mav 8,188,52
s_waitcnt lgkmcnt(2)
mav 0,192,24
R 40,20480
mav 4,196,24
mav 8,200,24
mav 0,204,28
R 44,21504
mav 4,208,28
mav 8,212,28
s_waitcnt lgkmcnt(2)
mav 0,216,32
R 48,22528
mav 4,220,32
mav 8,224,32
mav 0,228,36
R 52,23552
mav 4,232,36
mav 8,236,36
s_waitcnt lgkmcnt(2)
mav 0,240,40
R 24,24576
mav 4,244,40
mav 8,248,40
mav 0,252,44
R 28,25600
mvv 4,128,44
mvv 8,132,44
s_waitcnt lgkmcnt(2)
mvv 0,136,48
R 32,26624
mvv 4,140,48
mvv 8,144,48
mvv 0,148,52
R 36,27648
mvv 4,152,52
mvv 8,156,52
s_waitcnt lgkmcnt(2)
mvv 0,160,24
R 40,28672
mvv 4,164,24
mvv 8,168,24
mvv 0,172,28
R 44,29696
mvv 4,176,28
v_add_u32 v76, 1, v76
mvv 8,180,28
s_waitcnt lgkmcnt(2)
mvv 0,184,32
R 48,30720
mvv 4,188,32
mvv 8,192,32
mvv 0,196,36
R 52,31744
mvv 4,200,36
s_waitcnt vmcnt(2) lgkmcnt(2)
s_add_u32 s49, s49, 313344
mvv 8,204,36
s_barrier
mvv 0,208,40
R 24,32768
mvv 4,212,40
ds_read_b64 v[78:79], v89 offset:0
mvv 8,216,40
mvv 0,220,44
R 28,33792
mvv 4,224,44
mvv 8,228,44
s_waitcnt lgkmcnt(3)
mvv 0,232,48
R 32,34816
mvv 4,236,48
mvv 8,240,48
mvv 0,244,52
R 36,35840
mvv 4,248,52
mvv 8,252,52
s_waitcnt lgkmcnt(2)
v_mfma_f32_16x16x32_bf16 v[12:15], a[0:3], v[24:27], 0
R 40,36864
v_fma_f32 v104, v79, v56, v92
v_mfma_f32_16x16x32_bf16 v[16:19], a[4:7], v[24:27], 0
v_fma_f32 v0, v0, v78, v104
v_fma_f32 v105, v79, v57, v93
v_mfma_f32_16x16x32_bf16 v[20:23], a[8:11], v[24:27], 0
v_fma_f32 v1, v1, v78, v105
v_mul_f32_e64 v0, v0, |v0|
mav 12,12,28
R 44,37888
v_mul_f32_e64 v1, v1, |v1|
mav 16,16,28
s_waitcnt vmcnt(0)
v_cvt_pk_bf16_f32 v0, v0, v1
v_pk_max_i16 v68, v0, 0
mav 20,20,28
s_waitcnt lgkmcnt(2)
v_fma_f32 v104, v79, v58, v94
v_fma_f32 v2, v2, v78, v104
mav 12,24,32
R 48,38912
v_fma_f32 v105, v79, v59, v95
mav 16,28,32
buffer_store_dword v76, v81, s[36:39], s52 offen sc1
mav 20,32,32
v_fma_f32 v3, v3, v78, v105
v_mul_f32_e64 v2, v2, |v2|
mav 12,36,36
R 52,39936
v_mul_f32_e64 v3, v3, |v3|
mav 16,40,36
v_cvt_pk_bf16_f32 v2, v2, v3
v_pk_max_i16 v69, v2, 0
mav 20,44,36
s_waitcnt lgkmcnt(2)
v_fma_f32 v104, v79, v60, v96
v_fma_f32 v4, v4, v78, v104
mav 12,48,40
R 24,40960
v_fma_f32 v105, v79, v61, v97
mav 16,52,40
v_fma_f32 v5, v5, v78, v105
v_mul_f32_e64 v4, v4, |v4|
mav 20,56,40
v_mul_f32_e64 v5, v5, |v5|
v_cvt_pk_bf16_f32 v4, v4, v5
mav 12,60,44
R 28,41984
v_pk_max_i16 v70, v4, 0
mav 16,64,44
v_fma_f32 v104, v79, v62, v98
v_fma_f32 v6, v6, v78, v104
mav 20,68,44
s_waitcnt lgkmcnt(2)
v_fma_f32 v105, v79, v63, v99
v_fma_f32 v7, v7, v78, v105
mav 12,72,48
R 32,43008
v_mul_f32_e64 v6, v6, |v6|
mav 16,76,48
v_mul_f32_e64 v7, v7, |v7|
v_cvt_pk_bf16_f32 v6, v6, v7
mav 20,80,48
v_pk_max_i16 v71, v6, 0
v_fma_f32 v104, v79, v64, v100
mav 12,84,52
R 36,44032
v_fma_f32 v8, v8, v78, v104
mav 16,88,52
v_fma_f32 v105, v79, v65, v101
v_fma_f32 v9, v9, v78, v105
LsF15:
s_cbranch_execz LwF
mav 20,92,52
buffer_store_dwordx4 v[68:71], v83, s[36:39], s49 offen sc1
s_waitcnt lgkmcnt(2)
v_mul_f32_e64 v8, v8, |v8|
v_mul_f32_e64 v9, v9, |v9|
mav 12,96,24
R 40,45056
v_cvt_pk_bf16_f32 v8, v8, v9
mav 16,100,24
v_pk_max_i16 v72, v8, 0
v_pk_max_i16 v74, v8, 0
mav 20,104,24
v_fma_f32 v104, v79, v66, v102
v_fma_f32 v10, v10, v78, v104
mav 12,108,28
R 44,46080
v_fma_f32 v105, v79, v67, v103
mav 16,112,28
v_fma_f32 v11, v11, v78, v105
v_mul_f32_e64 v10, v10, |v10|
mav 20,116,28
s_waitcnt lgkmcnt(2)
v_mul_f32_e64 v11, v11, |v11|
v_cvt_pk_bf16_f32 v10, v10, v11
mav 12,120,32
R 48,47104
v_pk_max_i16 v73, v10, 0
mav 16,124,32
v_pk_max_i16 v75, v10, 0
mav 20,128,32
v_permlane16_swap_b32 v72, v74
mav 12,132,36
R 52,48128
mav 16,136,36
v_permlane16_swap_b32 v73, v75
mav 20,140,36
buffer_store_dwordx4 v[72:75], v84, s[36:39], s49 offen sc1
s_waitcnt lgkmcnt(2)
mav 12,144,40
R 24,49152
mav 16,148,40
mav 20,152,40
mav 12,156,44
R 28,50176
mav 16,160,44
mav 20,164,44
s_waitcnt lgkmcnt(2)
mav 12,168,48
R 32,51200
mav 16,172,48
mav 20,176,48
mav 12,180,52
R 36,52224
mav 16,184,52
mav 20,188,52
s_waitcnt lgkmcnt(2)
mav 12,192,24
R 40,53248
mav 16,196,24
mav 20,200,24
mav 12,204,28
R 44,54272
mav 16,208,28
mav 20,212,28
s_waitcnt lgkmcnt(2)
mav 12,216,32
R 48,55296
mav 16,220,32
mav 20,224,32
mav 12,228,36
R 52,56320
mav 16,232,36
mav 20,236,36
s_waitcnt lgkmcnt(2)
mav 12,240,40
R 24,57344
mav 16,244,40
mav 20,248,40
mav 12,252,44
R 28,58368
mvv 16,128,44
mvv 20,132,44
s_waitcnt lgkmcnt(2)
mvv 12,136,48
R 32,59392
mvv 16,140,48
mvv 20,144,48
mvv 12,148,52
R 36,60416
mvv 16,152,52
mvv 20,156,52
s_waitcnt lgkmcnt(2)
mvv 12,160,24
mvv 16,164,24
R 40,61440
mvv 20,168,24
mvv 12,172,28
R 44,62464
mvv 16,176,28
v_add_u32 v76, 1, v76
mvv 20,180,28
s_waitcnt lgkmcnt(2)
mvv 12,184,32
R 48,63488
mvv 16,188,32
mvv 20,192,32
mvv 12,196,36
R 52,64512
mvv 16,200,36
s_waitcnt lgkmcnt(2)
s_add_u32 s49, s49, 313344
mvv 20,204,36
s_barrier
mvv 12,208,40
mvv 16,212,40
ds_read_b64 v[78:79], v89 offset:128
mvv 20,216,40
mvv 12,220,44
mvv 16,224,44
mvv 20,228,44
s_waitcnt lgkmcnt(1)
mvv 12,232,48
mvv 16,236,48
mvv 20,240,48
mvv 12,244,52
mvv 16,248,52
mvv 20,252,52
s_waitcnt lgkmcnt(0)
v_fma_f32 v104, v79, v56, v92
s_nop 3
v_fma_f32 v12, v12, v78, v104
v_fma_f32 v105, v79, v57, v93
v_fma_f32 v13, v13, v78, v105
v_mul_f32_e64 v12, v12, |v12|
v_mul_f32_e64 v13, v13, |v13|
v_cvt_pk_bf16_f32 v12, v12, v13
v_pk_max_i16 v68, v12, 0
v_fma_f32 v104, v79, v58, v94
v_fma_f32 v14, v14, v78, v104
v_fma_f32 v105, v79, v59, v95
v_fma_f32 v15, v15, v78, v105
v_mul_f32_e64 v14, v14, |v14|
v_mul_f32_e64 v15, v15, |v15|
v_cvt_pk_bf16_f32 v14, v14, v15
v_pk_max_i16 v69, v14, 0
v_fma_f32 v104, v79, v60, v96
v_fma_f32 v16, v16, v78, v104
v_fma_f32 v105, v79, v61, v97
v_fma_f32 v17, v17, v78, v105
v_mul_f32_e64 v16, v16, |v16|
v_mul_f32_e64 v17, v17, |v17|
v_cvt_pk_bf16_f32 v16, v16, v17
v_pk_max_i16 v70, v16, 0
v_fma_f32 v104, v79, v62, v98
v_fma_f32 v18, v18, v78, v104
v_fma_f32 v105, v79, v63, v99
v_fma_f32 v19, v19, v78, v105
v_mul_f32_e64 v18, v18, |v18|
v_mul_f32_e64 v19, v19, |v19|
v_cvt_pk_bf16_f32 v18, v18, v19
v_pk_max_i16 v71, v18, 0
buffer_store_dwordx4 v[68:71], v83, s[36:39], s49 offen sc1
v_fma_f32 v104, v79, v64, v100
v_fma_f32 v20, v20, v78, v104
v_fma_f32 v105, v79, v65, v101
v_fma_f32 v21, v21, v78, v105
v_mul_f32_e64 v20, v20, |v20|
v_mul_f32_e64 v21, v21, |v21|
v_cvt_pk_bf16_f32 v20, v20, v21
v_pk_max_i16 v72, v20, 0
v_pk_max_i16 v74, v20, 0
v_fma_f32 v104, v79, v66, v102
v_fma_f32 v22, v22, v78, v104
v_fma_f32 v105, v79, v67, v103
v_fma_f32 v23, v23, v78, v105
v_mul_f32_e64 v22, v22, |v22|
v_mul_f32_e64 v23, v23, |v23|
v_cvt_pk_bf16_f32 v22, v22, v23
v_pk_max_i16 v73, v22, 0
v_pk_max_i16 v75, v22, 0
v_permlane16_swap_b32 v72, v74
s_nop 0
v_permlane16_swap_b32 v73, v75
buffer_store_dwordx4 v[72:75], v84, s[36:39], s49 offen sc1
s_waitcnt vmcnt(2)
buffer_store_dword v76, v81, s[36:39], s52 offen sc1
v_add_u32 v76, 37, v76
s_waitcnt vmcnt(1)
buffer_store_dword v76, v81, s[36:39], s52 offen sc1
s_endpgm
Lr4:
s_cmp_eq_u32 s60, 4
s_cbranch_scc0 Lr5
s_waitcnt vmcnt(0)
v_accvgpr_read_b32 v65, a255
v_mov_b32 v64, 0x100
v_add_u32 v65, 0x9e3779b9, v65
buffer_store_dwordx2 v[64:65], v81, s[36:39], s52 offen sc1
v_add_u32 v87, v87, v64
s_sleep 32
s_sleep 32
s_sleep 32
E 0,65536
E 1,69632
s_add_u32 s51, s51, 229376
E 0,73728
E 1,77824
s_add_u32 s51, s51, 229376
E 0,81920
E 1,86016
s_add_u32 s51, s51, 229376
E 0,90112
E 1,94208
s_add_u32 s51, s51, 229376
s_mov_b64 exec, 0
s_cmp_eq_u32 s53, 0
s_cbranch_scc1 LsE0
s_cmp_eq_u32 s53, 1024
s_cbranch_scc1 LsE1
s_cmp_eq_u32 s53, 2048
s_cbranch_scc1 LsE2
s_branch LsE3
LwE:
s_mov_b64 exec, -1
v_mov_b32 v16, s64
v_mov_b32 v17, s64
v_mov_b32 v18, s64
v_mov_b32 v19, s64
s_waitcnt vmcnt(4)
s_barrier
v_add_u32 v4, 65536, v4
v_add_u32 v5, 65536, v5
Ta 0,4,0
Ta 4,5,0
Ta 8,4,8192
Ta 12,5,8192
E 0,98304
E 1,102400
s_add_u32 s51, s51, 229376
E 0,106496
E 1,110592
s_add_u32 s51, s51, 229376
E 0,114688
E 1,118784
s_add_u32 s51, s51, 229376
E 0,122880
E 1,126976
s_add_u32 s51, s51, 229376
s_cmp_ge_u32 s51, s57
s_cselect_b32 s60, 7340032, 0
s_sub_u32 s51, s51, s60
E 0,131072
E 1,135168
s_add_u32 s51, s51, 229376
E 0,139264
E 1,143360
s_add_u32 s51, s51, 229376
E 0,147456
E 1,151552
s_add_u32 s51, s51, 229376
E 0,155648
E 1,159744
s_add_u32 s51, s51, 229376
s_waitcnt lgkmcnt(0)
s_waitcnt vmcnt(16)
s_barrier
E 0,65536
Ta 16,4,16384
v_mfma_f32_16x16x32_bf16 v[48:51], a[0:3], v[16:19], 0
Ta 20,5,16384
E 1,69632
s_add_u32 s51, s51, 229376
v_mfma_f32_16x16x32_bf16 v[52:55], a[4:7], v[16:19], 0
Ta 24,4,24576
Ta 28,5,24576
E 0,73728
mav 48,8,16
E 1,77824
s_add_u32 s51, s51, 229376
mav 52,12,16
s_waitcnt lgkmcnt(0)
s_waitcnt vmcnt(16)
s_barrier
E 0,81920
Ta 32,4,32768
mav 48,16,16
Ta 36,5,32768
E 1,86016
s_add_u32 s51, s51, 229376
mav 52,20,16
Ta 40,4,40960
Ta 44,5,40960
E 0,90112
mav 48,24,16
E 1,94208
s_add_u32 s51, s51, 229376
s_cmp_ge_u32 s51, s57
s_cselect_b32 s60, 7340032, 0
s_sub_u32 s51, s51, s60
mav 52,28,16
s_waitcnt lgkmcnt(0)
s_waitcnt vmcnt(16)
s_barrier
E 0,98304
Ta 48,4,49152
mav 48,32,16
Ta 52,5,49152
E 1,102400
s_add_u32 s51, s51, 229376
mav 52,36,16
Ta 56,4,57344
Ta 60,5,57344
E 0,106496
mav 48,40,16
E 1,110592
s_add_u32 s51, s51, 229376
mav 52,44,16
s_waitcnt lgkmcnt(0)
s_waitcnt vmcnt(16)
s_barrier
v_add_u32 v4, 65536, v4
v_add_u32 v5, 65536, v5
E 0,114688
Ta 64,4,0
mav 48,48,16
Ta 68,5,0
E 1,118784
s_add_u32 s51, s51, 229376
mav 52,52,16
Ta 72,4,8192
Ta 76,5,8192
E 0,122880
mav 48,56,16
E 1,126976
s_add_u32 s51, s51, 229376
mav 52,60,16
s_waitcnt lgkmcnt(0)
s_waitcnt vmcnt(16)
s_barrier
E 0,131072
Ta 80,4,16384
mav 48,64,16
Ta 84,5,16384
E 1,135168
s_add_u32 s51, s51, 229376
mav 52,68,16
Ta 88,4,24576
Ta 92,5,24576
E 0,139264
mav 48,72,16
E 1,143360
s_add_u32 s51, s51, 229376
mav 52,76,16
s_waitcnt lgkmcnt(0)
s_waitcnt vmcnt(16)
s_barrier
v_add_u32 v4, -65536, v4
v_add_u32 v5, -65536, v5
E 0,147456
Ta 96,4,0
mav 48,80,16
Ta 100,5,0
E 1,151552
s_add_u32 s51, s51, 229376
mav 52,84,16
Ta 104,4,8192
Ta 108,5,8192
E 0,155648
mav 48,88,16
E 1,159744
s_add_u32 s51, s51, 229376
s_cmp_ge_u32 s51, s57
s_cselect_b32 s60, 7340032, 0
s_sub_u32 s51, s51, s60
mav 52,92,16
s_waitcnt lgkmcnt(0)
s_waitcnt vmcnt(16)
s_barrier
E 0,65536
Ta 112,4,16384
mav 48,96,16
Ta 116,5,16384
E 1,69632
s_add_u32 s51, s51, 229376
mav 52,100,16
Ta 120,4,24576
Ta 124,5,24576
E 0,73728
mav 48,104,16
E 1,77824
s_add_u32 s51, s51, 229376
mav 52,108,16
s_waitcnt lgkmcnt(0)
s_waitcnt vmcnt(16)
s_barrier
E 0,81920
Ta 128,4,32768
mav 48,112,16
Ta 132,5,32768
E 1,86016
s_add_u32 s51, s51, 229376
mav 52,116,16
Ta 136,4,40960
Ta 140,5,40960
E 0,90112
mav 48,120,16
E 1,94208
s_add_u32 s51, s51, 229376
mav 52,124,16
s_waitcnt lgkmcnt(0)
s_waitcnt vmcnt(16)
s_barrier
E 0,98304
Ta 144,4,49152
mav 48,128,16
Ta 148,5,49152
E 1,102400
s_add_u32 s51, s51, 229376
mav 52,132,16
Ta 152,4,57344
Ta 156,5,57344
E 0,106496
mav 48,136,16
E 1,110592
LsE0:
s_cbranch_execz LsE4
s_add_u32 s51, s51, 229376
mav 52,140,16
s_waitcnt lgkmcnt(0)
s_waitcnt vmcnt(16)
s_barrier
v_add_u32 v4, 65536, v4
v_add_u32 v5, 65536, v5
E 0,114688
Ta 160,4,0
mav 48,144,16
Ta 164,5,0
E 1,118784
s_add_u32 s51, s51, 229376
mav 52,148,16
Ta 168,4,8192
Ta 172,5,8192
E 0,122880
mav 48,152,16
E 1,126976
s_add_u32 s51, s51, 229376
s_cmp_ge_u32 s51, s57
s_cselect_b32 s60, 7340032, 0
s_sub_u32 s51, s51, s60
mav 52,156,16
s_waitcnt lgkmcnt(0)
s_waitcnt vmcnt(16)
s_barrier
Ta 176,4,16384
mav 48,160,16
Ta 180,5,16384
mav 52,164,16
Ta 184,4,24576
Ta 188,5,24576
mav 48,168,16
mav 52,172,16
s_waitcnt lgkmcnt(0)
s_waitcnt vmcnt(12)
s_barrier
v_add_u32 v4, -65536, v4
v_add_u32 v5, -65536, v5
Ta 192,4,0
mav 48,176,16
Ta 196,5,0
mav 52,180,16
Ta 200,4,8192
Ta 204,5,8192
mav 48,184,16
mav 52,188,16
s_waitcnt lgkmcnt(0)
s_waitcnt vmcnt(8)
s_barrier
Ta 208,4,16384
mav 48,192,16
Ta 212,5,16384
mav 52,196,16
Ta 216,4,24576
Ta 220,5,24576
mav 48,200,16
mav 52,204,16
s_waitcnt lgkmcnt(0)
s_waitcnt vmcnt(4)
s_barrier
Ta 224,4,32768
mav 48,208,16
Ta 228,5,32768
mav 52,212,16
Ta 232,4,40960
Ta 236,5,40960
mav 48,216,16
mav 52,220,16
s_waitcnt lgkmcnt(0)
s_waitcnt vmcnt(0)
s_barrier
Ta 240,4,49152
mav 48,224,16
Ta 244,5,49152
mav 52,228,16
Ta 248,4,57344
Ta 252,5,57344
mav 48,232,16
mav 52,236,16
s_waitcnt lgkmcnt(0)
mav 48,240,16
mav 52,244,16
mav 48,248,16
mav 52,252,16
s_waitcnt vmcnt(0)
s_barrier
s_nop 7
s_and_b32 s60, s48, 3
s_cmp_eq_u32 s60, 1
s_cbranch_scc1 LvE1
s_cmp_eq_u32 s60, 2
s_cbranch_scc1 LvE2
s_cmp_eq_u32 s60, 3
s_cbranch_scc1 LvE3
LvE0:
LsE1:
s_cbranch_execz LsE5
buffer_load_dwordx2 v[70:71], v86, s[36:39], 0 offen sc1
s_waitcnt vmcnt(0)
ds_write_b32 v82, v70
v_sub_u32 v70, v70, v87
v_xor_b32 v71, v71, v65
v_cmp_lt_u64 vcc, s[62:63], v[70:71]
s_waitcnt lgkmcnt(0)
s_cbranch_vccnz CE0_1
BE0_1:
s_add_u32 s49, s49, 313344
s_barrier
s_add_u32 m0, s50, 0
s_nop 0
D 84,0
D 84,1024
D 84,2048
D 84,3072
s_add_u32 m0, s50, 4096
s_nop 0
D 85,0
D 85,1024
D 85,2048
D 85,3072
s_mov_b32 m0, 144384
s_nop 0
buffer_load_dword v86, s[36:39], 0 offen sc1 lds
v_add_u32 v87, 1, v87
ds_read_b32 v70, v82 offset:0
s_waitcnt lgkmcnt(0)
v_cmp_gt_i32 vcc, v87, v70
s_waitcnt vmcnt(1)
s_add_u32 s49, s49, 313344
s_cbranch_vccnz CE0_2
BE0_2:
s_barrier
s_add_u32 m0, s50, 32768
R 16,0
D 84,0
R 20,1024
R 24,2048
D 84,1024
R 28,3072
R 32,4096
D 84,2048
s_waitcnt lgkmcnt(3)
v_mfma_f32_16x16x32_bf16 v[0:3], a[0:3], v[16:19], 0
R 36,5120
v_mfma_f32_16x16x32_bf16 v[4:7], a[4:7], v[16:19], 0
mav 0,8,20
R 40,6144
mav 4,12,20
s_waitcnt lgkmcnt(3)
D 84,3072
mav 0,16,24
R 44,7168
mav 4,20,24
s_add_u32 m0, s50, 36864
mav 0,24,28
R 16,8192
mav 4,28,28
s_waitcnt lgkmcnt(3)
D 85,0
mav 0,32,32
R 20,9216
mav 4,36,32
mav 0,40,36
R 24,10240
mav 4,44,36
s_waitcnt lgkmcnt(3)
D 85,1024
mav 0,48,40
R 28,11264
mav 4,52,40
mav 0,56,44
R 32,12288
mav 4,60,44
s_waitcnt lgkmcnt(3)
D 85,2048
mav 0,64,16
R 36,13312
mav 4,68,16
mav 0,72,20
R 40,14336
mav 4,76,20
s_waitcnt lgkmcnt(3)
D 85,3072
mav 0,80,24
R 44,15360
mav 4,84,24
mav 0,88,28
R 16,16384
mav 4,92,28
s_waitcnt lgkmcnt(3)
mav 0,96,32
R 20,17408
mav 4,100,32
mav 0,104,36
R 24,18432
mav 4,108,36
s_waitcnt lgkmcnt(3)
mav 0,112,40
s_mov_b32 m0, 144384
R 28,19456
mav 4,116,40
mav 0,120,44
R 32,20480
mav 4,124,44
s_waitcnt lgkmcnt(3)
buffer_load_dword v86, s[36:39], 0 offen sc1 lds
mav 0,128,16
R 36,21504
mav 4,132,16
mav 0,136,20
R 40,22528
mav 4,140,20
s_waitcnt lgkmcnt(3)
mav 0,144,24
R 44,23552
mav 4,148,24
mav 0,152,28
R 16,24576
mav 4,156,28
s_waitcnt lgkmcnt(3)
mav 0,160,32
R 20,25600
v_add_u32 v87, 1, v87
mav 4,164,32
mav 0,168,36
R 24,26624
mav 4,172,36
s_waitcnt lgkmcnt(3)
mav 0,176,40
R 28,27648
mav 4,180,40
mav 0,184,44
R 32,28672
mav 4,188,44
s_waitcnt lgkmcnt(3)
)"
R"(
mav 0,192,16
R 36,29696
mav 4,196,16
mav 0,200,20
R 40,30720
mav 4,204,20
s_waitcnt lgkmcnt(3)
mav 0,208,24
R 44,31744
mav 4,212,24
mav 0,216,28
mav 4,220,28
s_waitcnt lgkmcnt(2)
mav 0,224,32
mav 4,228,32
mav 0,232,36
mav 4,236,36
s_waitcnt lgkmcnt(0)
mav 0,240,40
mav 4,244,40
mav 0,248,44
mav 4,252,44
ds_read_b32 v70, v82 offset:0
s_xor_b32 s50, s50, 0x10000
s_waitcnt lgkmcnt(0)
v_cmp_gt_i32 vcc, v87, v70
s_waitcnt vmcnt(1)
s_add_u32 s49, s49, 313344
s_cbranch_vccnz CE0_3
BE0_3:
s_barrier
s_add_u32 m0, s50, 0
R 16,32768
D 84,0
ds_read_b64 v[66:67], v88 offset:0
R 20,33792
R 24,34816
D 84,1024
R 28,35840
R 32,36864
D 84,2048
s_waitcnt lgkmcnt(3)
v_mfma_f32_16x16x32_bf16 v[8:11], a[0:3], v[16:19], 0
R 36,37888
v_fma_f32 v68, v67, v48, v90
v_mfma_f32_16x16x32_bf16 v[12:15], a[4:7], v[16:19], 0
v_fma_f32 v0, v0, v66, v68
v_fma_f32 v69, v67, v49, v91
mav 8,8,20
R 40,38912
v_fma_f32 v1, v1, v66, v69
mav 12,12,20
s_waitcnt lgkmcnt(3)
D 84,3072
mav 8,16,24
R 44,39936
v_mul_f32_e64 v0, v0, |v0|
mav 12,20,24
s_add_u32 m0, s50, 4096
v_mul_f32_e64 v1, v1, |v1|
v_cvt_pk_bf16_f32 v0, v0, v1
mav 8,24,28
R 16,40960
v_pk_max_i16 v56, v0, 0
mav 12,28,28
s_waitcnt lgkmcnt(3)
D 85,0
mav 8,32,32
R 20,41984
v_fma_f32 v68, v67, v50, v92
mav 12,36,32
v_fma_f32 v2, v2, v66, v68
v_fma_f32 v69, v67, v51, v93
mav 8,40,36
R 24,43008
v_fma_f32 v3, v3, v66, v69
mav 12,44,36
s_waitcnt lgkmcnt(3)
D 85,1024
mav 8,48,40
R 28,44032
v_mul_f32_e64 v2, v2, |v2|
mav 12,52,40
v_mul_f32_e64 v3, v3, |v3|
v_cvt_pk_bf16_f32 v2, v2, v3
mav 8,56,44
R 32,45056
v_pk_max_i16 v57, v2, 0
mav 12,60,44
s_waitcnt lgkmcnt(3)
D 85,2048
mav 8,64,16
R 36,46080
v_fma_f32 v68, v67, v52, v94
mav 12,68,16
v_fma_f32 v4, v4, v66, v68
v_fma_f32 v69, v67, v53, v95
mav 8,72,20
R 40,47104
v_fma_f32 v5, v5, v66, v69
mav 12,76,20
s_waitcnt lgkmcnt(3)
D 85,3072
mav 8,80,24
R 44,48128
v_mul_f32_e64 v4, v4, |v4|
mav 12,84,24
v_mul_f32_e64 v5, v5, |v5|
v_cvt_pk_bf16_f32 v4, v4, v5
mav 8,88,28
R 16,49152
v_pk_max_i16 v58, v4, 0
mav 12,92,28
s_waitcnt lgkmcnt(3)
v_fma_f32 v68, v67, v54, v96
v_fma_f32 v6, v6, v66, v68
mav 8,96,32
R 20,50176
v_fma_f32 v69, v67, v55, v97
mav 12,100,32
v_fma_f32 v7, v7, v66, v69
v_mul_f32_e64 v6, v6, |v6|
mav 8,104,36
R 24,51200
v_mul_f32_e64 v7, v7, |v7|
mav 12,108,36
s_waitcnt lgkmcnt(3)
v_cvt_pk_bf16_f32 v6, v6, v7
v_pk_max_i16 v59, v6, 0
mav 8,112,40
s_mov_b32 m0, 144384
R 28,52224
mav 12,116,40
mav 8,120,44
R 32,53248
mav 12,124,44
buffer_store_dwordx4 v[56:59], v83, s[36:39], s49 offen sc1
s_waitcnt lgkmcnt(3)
mav 8,128,16
R 36,54272
mav 12,132,16
mav 8,136,20
R 40,55296
mav 12,140,20
s_waitcnt lgkmcnt(3)
buffer_load_dword v86, s[36:39], 0 offen sc1 lds
mav 8,144,24
R 44,56320
mav 12,148,24
mav 8,152,28
R 16,57344
mav 12,156,28
s_waitcnt lgkmcnt(3)
mav 8,160,32
R 20,58368
v_add_u32 v87, 1, v87
mav 12,164,32
mav 8,168,36
R 24,59392
mav 12,172,36
s_waitcnt lgkmcnt(3)
mav 8,176,40
R 28,60416
mav 12,180,40
mav 8,184,44
R 32,61440
mav 12,188,44
s_waitcnt lgkmcnt(3)
mav 8,192,16
R 36,62464
mav 12,196,16
mav 8,200,20
R 40,63488
mav 12,204,20
s_waitcnt lgkmcnt(3)
mav 8,208,24
R 44,64512
mav 12,212,24
mav 8,216,28
mav 12,220,28
s_waitcnt lgkmcnt(2)
mav 8,224,32
v_add_u32 v64, 1, v64
mav 12,228,32
mav 8,232,36
s_mov_b32 s60, -30
.p2align 6
LE0_4:
mav 12,236,36
s_waitcnt lgkmcnt(0)
mav 8,240,40
mav 12,244,40
mav 8,248,44
mav 12,252,44
ds_read_b32 v70, v82 offset:0
s_waitcnt lgkmcnt(0)
v_cmp_gt_i32 vcc, v87, v70
v_xor_b32 v80, 0x10000, v80
s_waitcnt vmcnt(2)
s_add_u32 s49, s49, 313344
s_cbranch_vccnz CE0_5
BE0_5:
s_barrier
s_add_u32 m0, s50, 32768
R 16,0
D 84,0
ds_read_b64 v[66:67], v88 offset:128
R 20,1024
R 24,2048
D 84,1024
R 28,3072
s_waitcnt vmcnt(3)
buffer_store_dword v64, v81, s[36:39], s52 offen sc1
R 32,4096
D 84,2048
s_waitcnt lgkmcnt(3)
v_mfma_f32_16x16x32_bf16 v[0:3], a[0:3], v[16:19], 0
R 36,5120
v_fma_f32 v68, v67, v48, v90
v_mfma_f32_16x16x32_bf16 v[4:7], a[4:7], v[16:19], 0
v_fma_f32 v8, v8, v66, v68
v_fma_f32 v69, v67, v49, v91
mav 0,8,20
R 40,6144
v_fma_f32 v9, v9, v66, v69
mav 4,12,20
s_waitcnt lgkmcnt(3)
D 84,3072
mav 0,16,24
R 44,7168
v_mul_f32_e64 v8, v8, |v8|
mav 4,20,24
s_add_u32 m0, s50, 36864
v_mul_f32_e64 v9, v9, |v9|
v_cvt_pk_bf16_f32 v8, v8, v9
mav 0,24,28
R 16,8192
v_pk_max_i16 v56, v8, 0
mav 4,28,28
s_waitcnt lgkmcnt(3)
D 85,0
mav 0,32,32
R 20,9216
v_fma_f32 v68, v67, v50, v92
mav 4,36,32
v_fma_f32 v10, v10, v66, v68
v_fma_f32 v69, v67, v51, v93
mav 0,40,36
R 24,10240
v_fma_f32 v11, v11, v66, v69
mav 4,44,36
s_waitcnt lgkmcnt(3)
D 85,1024
mav 0,48,40
R 28,11264
v_mul_f32_e64 v10, v10, |v10|
mav 4,52,40
v_mul_f32_e64 v11, v11, |v11|
v_cvt_pk_bf16_f32 v10, v10, v11
mav 0,56,44
R 32,12288
v_pk_max_i16 v57, v10, 0
mav 4,60,44
s_waitcnt lgkmcnt(3)
D 85,2048
mav 0,64,16
R 36,13312
v_fma_f32 v68, v67, v52, v94
mav 4,68,16
v_fma_f32 v12, v12, v66, v68
v_fma_f32 v69, v67, v53, v95
mav 0,72,20
R 40,14336
v_fma_f32 v13, v13, v66, v69
mav 4,76,20
s_waitcnt lgkmcnt(3)
D 85,3072
mav 0,80,24
R 44,15360
v_mul_f32_e64 v12, v12, |v12|
mav 4,84,24
v_mul_f32_e64 v13, v13, |v13|
v_cvt_pk_bf16_f32 v12, v12, v13
mav 0,88,28
R 16,16384
v_pk_max_i16 v58, v12, 0
mav 4,92,28
s_waitcnt lgkmcnt(3)
v_fma_f32 v68, v67, v54, v96
v_fma_f32 v14, v14, v66, v68
mav 0,96,32
R 20,17408
v_fma_f32 v69, v67, v55, v97
mav 4,100,32
v_fma_f32 v15, v15, v66, v69
v_mul_f32_e64 v14, v14, |v14|
mav 0,104,36
R 24,18432
v_mul_f32_e64 v15, v15, |v15|
mav 4,108,36
LsE2:
s_cbranch_execz LsE6
s_waitcnt lgkmcnt(3)
v_cvt_pk_bf16_f32 v14, v14, v15
v_pk_max_i16 v59, v14, 0
mav 0,112,40
s_mov_b32 m0, 144384
R 28,19456
mav 4,116,40
mav 0,120,44
R 32,20480
mav 4,124,44
buffer_store_dwordx4 v[56:59], v83, s[36:39], s49 offen sc1
s_waitcnt lgkmcnt(3)
mav 0,128,16
R 36,21504
mav 4,132,16
mav 0,136,20
R 40,22528
mav 4,140,20
s_waitcnt lgkmcnt(3)
buffer_load_dword v86, s[36:39], 0 offen sc1 lds
mav 0,144,24
R 44,23552
mav 4,148,24
mav 0,152,28
R 16,24576
mav 4,156,28
s_waitcnt lgkmcnt(3)
mav 0,160,32
R 20,25600
v_add_u32 v87, 1, v87
mav 4,164,32
mav 0,168,36
R 24,26624
mav 4,172,36
s_waitcnt lgkmcnt(3)
mav 0,176,40
R 28,27648
mav 4,180,40
mav 0,184,44
R 32,28672
mav 4,188,44
s_waitcnt lgkmcnt(3)
mav 0,192,16
R 36,29696
mav 4,196,16
mav 0,200,20
R 40,30720
mav 4,204,20
s_waitcnt lgkmcnt(3)
mav 0,208,24
R 44,31744
mav 4,212,24
mav 0,216,28
mav 4,220,28
s_waitcnt lgkmcnt(2)
mav 0,224,32
v_add_u32 v64, 1, v64
mav 4,228,32
mav 0,232,36
mav 4,236,36
s_waitcnt lgkmcnt(0)
mav 0,240,40
mav 4,244,40
mav 0,248,44
mav 4,252,44
ds_read_b32 v70, v82 offset:0
s_xor_b32 s50, s50, 0x10000
s_waitcnt lgkmcnt(0)
v_cmp_gt_i32 vcc, v87, v70
s_waitcnt vmcnt(2)
s_add_u32 s49, s49, 313344
s_cbranch_vccnz CE0_6
BE0_6:
s_barrier
s_add_u32 m0, s50, 0
R 16,32768
D 84,0
ds_read_b64 v[66:67], v88 offset:0
R 20,33792
R 24,34816
D 84,1024
R 28,35840
s_waitcnt vmcnt(3)
buffer_store_dword v64, v81, s[36:39], s52 offen sc1
R 32,36864
D 84,2048
s_waitcnt lgkmcnt(3)
v_mfma_f32_16x16x32_bf16 v[8:11], a[0:3], v[16:19], 0
R 36,37888
v_fma_f32 v68, v67, v48, v90
v_mfma_f32_16x16x32_bf16 v[12:15], a[4:7], v[16:19], 0
v_fma_f32 v0, v0, v66, v68
v_fma_f32 v69, v67, v49, v91
mav 8,8,20
R 40,38912
v_fma_f32 v1, v1, v66, v69
mav 12,12,20
s_waitcnt lgkmcnt(3)
D 84,3072
mav 8,16,24
R 44,39936
v_mul_f32_e64 v0, v0, |v0|
mav 12,20,24
s_add_u32 m0, s50, 4096
v_mul_f32_e64 v1, v1, |v1|
v_cvt_pk_bf16_f32 v0, v0, v1
mav 8,24,28
R 16,40960
v_pk_max_i16 v56, v0, 0
mav 12,28,28
s_waitcnt lgkmcnt(3)
D 85,0
mav 8,32,32
R 20,41984
v_fma_f32 v68, v67, v50, v92
mav 12,36,32
v_fma_f32 v2, v2, v66, v68
v_fma_f32 v69, v67, v51, v93
mav 8,40,36
R 24,43008
v_fma_f32 v3, v3, v66, v69
mav 12,44,36
s_waitcnt lgkmcnt(3)
D 85,1024
mav 8,48,40
R 28,44032
v_mul_f32_e64 v2, v2, |v2|
mav 12,52,40
v_mul_f32_e64 v3, v3, |v3|
v_cvt_pk_bf16_f32 v2, v2, v3
mav 8,56,44
R 32,45056
v_pk_max_i16 v57, v2, 0
mav 12,60,44
s_waitcnt lgkmcnt(3)
D 85,2048
mav 8,64,16
R 36,46080
v_fma_f32 v68, v67, v52, v94
mav 12,68,16
v_fma_f32 v4, v4, v66, v68
v_fma_f32 v69, v67, v53, v95
mav 8,72,20
R 40,47104
v_fma_f32 v5, v5, v66, v69
mav 12,76,20
s_waitcnt lgkmcnt(3)
D 85,3072
mav 8,80,24
R 44,48128
v_mul_f32_e64 v4, v4, |v4|
mav 12,84,24
v_mul_f32_e64 v5, v5, |v5|
v_cvt_pk_bf16_f32 v4, v4, v5
mav 8,88,28
R 16,49152
v_pk_max_i16 v58, v4, 0
mav 12,92,28
s_waitcnt lgkmcnt(3)
v_fma_f32 v68, v67, v54, v96
v_fma_f32 v6, v6, v66, v68
mav 8,96,32
R 20,50176
v_fma_f32 v69, v67, v55, v97
mav 12,100,32
v_fma_f32 v7, v7, v66, v69
v_mul_f32_e64 v6, v6, |v6|
mav 8,104,36
R 24,51200
v_mul_f32_e64 v7, v7, |v7|
mav 12,108,36
s_waitcnt lgkmcnt(3)
v_cvt_pk_bf16_f32 v6, v6, v7
v_pk_max_i16 v59, v6, 0
mav 8,112,40
s_mov_b32 m0, 144384
R 28,52224
mav 12,116,40
mav 8,120,44
R 32,53248
mav 12,124,44
buffer_store_dwordx4 v[56:59], v83, s[36:39], s49 offen sc1
s_waitcnt lgkmcnt(3)
mav 8,128,16
R 36,54272
mav 12,132,16
mav 8,136,20
R 40,55296
mav 12,140,20
s_waitcnt lgkmcnt(3)
buffer_load_dword v86, s[36:39], 0 offen sc1 lds
mav 8,144,24
R 44,56320
mav 12,148,24
mav 8,152,28
R 16,57344
mav 12,156,28
s_waitcnt lgkmcnt(3)
mav 8,160,32
R 20,58368
v_add_u32 v87, 1, v87
mav 12,164,32
mav 8,168,36
R 24,59392
mav 12,172,36
s_waitcnt lgkmcnt(3)
mav 8,176,40
R 28,60416
mav 12,180,40
mav 8,184,44
R 32,61440
mav 12,188,44
s_waitcnt lgkmcnt(3)
mav 8,192,16
R 36,62464
mav 12,196,16
mav 8,200,20
R 40,63488
mav 12,204,20
s_waitcnt lgkmcnt(3)
mav 8,208,24
R 44,64512
mav 12,212,24
mav 8,216,28
mav 12,220,28
s_waitcnt lgkmcnt(2)
mav 8,224,32
v_add_u32 v64, 1, v64
mav 12,228,32
s_add_u32 s60, s60, 1
mav 8,232,36
s_cbranch_scc0 LE0_4
mav 12,236,36
s_waitcnt lgkmcnt(0)
mav 8,240,40
mav 12,244,40
mav 8,248,44
mav 12,252,44
ds_read_b32 v70, v82 offset:0
s_waitcnt lgkmcnt(0)
v_cmp_gt_i32 vcc, v87, v70
v_xor_b32 v80, 0x10000, v80
s_waitcnt vmcnt(2)
s_add_u32 s49, s49, 313344
s_cbranch_vccnz CE0_7
BE0_7:
s_barrier
s_add_u32 m0, s50, 32768
R 16,0
D 84,0
ds_read_b64 v[66:67], v88 offset:128
R 20,1024
R 24,2048
D 84,1024
R 28,3072
s_waitcnt vmcnt(3)
buffer_store_dword v64, v81, s[36:39], s52 offen sc1
R 32,4096
D 84,2048
s_waitcnt lgkmcnt(3)
v_mfma_f32_16x16x32_bf16 v[0:3], a[0:3], v[16:19], 0
R 36,5120
v_fma_f32 v68, v67, v48, v90
v_mfma_f32_16x16x32_bf16 v[4:7], a[4:7], v[16:19], 0
v_fma_f32 v8, v8, v66, v68
v_fma_f32 v69, v67, v49, v91
mav 0,8,20
R 40,6144
v_fma_f32 v9, v9, v66, v69
mav 4,12,20
s_waitcnt lgkmcnt(3)
D 84,3072
mav 0,16,24
R 44,7168
v_mul_f32_e64 v8, v8, |v8|
mav 4,20,24
s_add_u32 m0, s50, 36864
v_mul_f32_e64 v9, v9, |v9|
v_cvt_pk_bf16_f32 v8, v8, v9
mav 0,24,28
R 16,8192
v_pk_max_i16 v56, v8, 0
mav 4,28,28
s_waitcnt lgkmcnt(3)
D 85,0
mav 0,32,32
R 20,9216
v_fma_f32 v68, v67, v50, v92
mav 4,36,32
v_fma_f32 v10, v10, v66, v68
v_fma_f32 v69, v67, v51, v93
mav 0,40,36
R 24,10240
v_fma_f32 v11, v11, v66, v69
mav 4,44,36
s_waitcnt lgkmcnt(3)
D 85,1024
mav 0,48,40
R 28,11264
v_mul_f32_e64 v10, v10, |v10|
mav 4,52,40
v_mul_f32_e64 v11, v11, |v11|
v_cvt_pk_bf16_f32 v10, v10, v11
mav 0,56,44
R 32,12288
v_pk_max_i16 v57, v10, 0
mav 4,60,44
s_waitcnt lgkmcnt(3)
D 85,2048
mav 0,64,16
R 36,13312
v_fma_f32 v68, v67, v52, v94
mav 4,68,16
v_fma_f32 v12, v12, v66, v68
v_fma_f32 v69, v67, v53, v95
mav 0,72,20
R 40,14336
v_fma_f32 v13, v13, v66, v69
mav 4,76,20
s_waitcnt lgkmcnt(3)
D 85,3072
mav 0,80,24
R 44,15360
v_mul_f32_e64 v12, v12, |v12|
mav 4,84,24
v_mul_f32_e64 v13, v13, |v13|
v_cvt_pk_bf16_f32 v12, v12, v13
mav 0,88,28
R 16,16384
v_pk_max_i16 v58, v12, 0
mav 4,92,28
s_waitcnt lgkmcnt(3)
v_fma_f32 v68, v67, v54, v96
v_fma_f32 v14, v14, v66, v68
mav 0,96,32
R 20,17408
v_fma_f32 v69, v67, v55, v97
mav 4,100,32
v_fma_f32 v15, v15, v66, v69
v_mul_f32_e64 v14, v14, |v14|
mav 0,104,36
R 24,18432
v_mul_f32_e64 v15, v15, |v15|
mav 4,108,36
s_waitcnt lgkmcnt(3)
v_cvt_pk_bf16_f32 v14, v14, v15
v_pk_max_i16 v59, v14, 0
mav 0,112,40
s_mov_b32 m0, 144384
R 28,19456
mav 4,116,40
mav 0,120,44
R 32,20480
mav 4,124,44
buffer_store_dwordx4 v[56:59], v83, s[36:39], s49 offen sc1
s_waitcnt lgkmcnt(3)
mav 0,128,16
R 36,21504
mav 4,132,16
mav 0,136,20
R 40,22528
mav 4,140,20
s_waitcnt lgkmcnt(3)
buffer_load_dword v86, s[36:39], 0 offen sc1 lds
mav 0,144,24
R 44,23552
mav 4,148,24
mav 0,152,28
R 16,24576
mav 4,156,28
s_waitcnt lgkmcnt(3)
mav 0,160,32
R 20,25600
v_add_u32 v87, 1, v87
mav 4,164,32
mav 0,168,36
R 24,26624
mav 4,172,36
s_waitcnt lgkmcnt(3)
mav 0,176,40
R 28,27648
mav 4,180,40
mav 0,184,44
R 32,28672
mav 4,188,44
s_waitcnt lgkmcnt(3)
mav 0,192,16
R 36,29696
mav 4,196,16
mav 0,200,20
R 40,30720
mav 4,204,20
s_waitcnt lgkmcnt(3)
mav 0,208,24
R 44,31744
mav 4,212,24
mav 0,216,28
mav 4,220,28
s_waitcnt lgkmcnt(2)
mav 0,224,32
v_add_u32 v64, 1, v64
mav 4,228,32
mav 0,232,36
mav 4,236,36
s_waitcnt lgkmcnt(0)
mav 0,240,40
mav 4,244,40
mav 0,248,44
mav 4,252,44
ds_read_b32 v70, v82 offset:0
s_waitcnt lgkmcnt(0)
v_cmp_gt_i32 vcc, v87, v70
s_waitcnt vmcnt(2)
s_add_u32 s49, s49, 313344
s_cbranch_vccnz CE0_8
BE0_8:
s_barrier
R 16,32768
ds_read_b64 v[66:67], v88 offset:0
R 20,33792
R 24,34816
R 28,35840
s_waitcnt vmcnt(1)
buffer_store_dword v64, v81, s[36:39], s52 offen sc1
R 32,36864
s_waitcnt lgkmcnt(3)
v_mfma_f32_16x16x32_bf16 v[8:11], a[0:3], v[16:19], 0
R 36,37888
v_fma_f32 v68, v67, v48, v90
v_mfma_f32_16x16x32_bf16 v[12:15], a[4:7], v[16:19], 0
v_fma_f32 v0, v0, v66, v68
v_fma_f32 v69, v67, v49, v91
mav 8,8,20
R 40,38912
v_fma_f32 v1, v1, v66, v69
mav 12,12,20
s_waitcnt lgkmcnt(3)
v_mul_f32_e64 v0, v0, |v0|
v_mul_f32_e64 v1, v1, |v1|
mav 8,16,24
R 44,39936
v_cvt_pk_bf16_f32 v0, v0, v1
mav 12,20,24
v_pk_max_i16 v56, v0, 0
v_fma_f32 v68, v67, v50, v92
mav 8,24,28
R 16,40960
v_fma_f32 v2, v2, v66, v68
mav 12,28,28
s_waitcnt lgkmcnt(3)
v_fma_f32 v69, v67, v51, v93
v_fma_f32 v3, v3, v66, v69
mav 8,32,32
R 20,41984
v_mul_f32_e64 v2, v2, |v2|
mav 12,36,32
v_mul_f32_e64 v3, v3, |v3|
v_cvt_pk_bf16_f32 v2, v2, v3
mav 8,40,36
R 24,43008
v_pk_max_i16 v57, v2, 0
mav 12,44,36
s_waitcnt lgkmcnt(3)
v_fma_f32 v68, v67, v52, v94
v_fma_f32 v4, v4, v66, v68
mav 8,48,40
R 28,44032
v_fma_f32 v69, v67, v53, v95
mav 12,52,40
v_fma_f32 v5, v5, v66, v69
v_mul_f32_e64 v4, v4, |v4|
mav 8,56,44
R 32,45056
v_mul_f32_e64 v5, v5, |v5|
mav 12,60,44
s_waitcnt lgkmcnt(3)
v_cvt_pk_bf16_f32 v4, v4, v5
v_pk_max_i16 v58, v4, 0
mav 8,64,16
R 36,46080
v_fma_f32 v68, v67, v54, v96
mav 12,68,16
v_fma_f32 v6, v6, v66, v68
v_fma_f32 v69, v67, v55, v97
mav 8,72,20
R 40,47104
v_fma_f32 v7, v7, v66, v69
mav 12,76,20
s_waitcnt lgkmcnt(3)
v_mul_f32_e64 v6, v6, |v6|
v_mul_f32_e64 v7, v7, |v7|
mav 8,80,24
R 44,48128
v_cvt_pk_bf16_f32 v6, v6, v7
mav 12,84,24
v_pk_max_i16 v59, v6, 0
mav 8,88,28
R 16,49152
mav 12,92,28
buffer_store_dwordx4 v[56:59], v83, s[36:39], s49 offen sc1
s_waitcnt lgkmcnt(3)
mav 8,96,32
R 20,50176
mav 12,100,32
mav 8,104,36
R 24,51200
mav 12,108,36
s_waitcnt lgkmcnt(3)
mav 8,112,40
R 28,52224
mav 12,116,40
mav 8,120,44
R 32,53248
mav 12,124,44
s_waitcnt lgkmcnt(3)
mav 8,128,16
R 36,54272
mav 12,132,16
mav 8,136,20
R 40,55296
mav 12,140,20
s_waitcnt lgkmcnt(3)
mav 8,144,24
R 44,56320
mav 12,148,24
mav 8,152,28
R 16,57344
mav 12,156,28
s_waitcnt lgkmcnt(3)
mav 8,160,32
R 20,58368
mav 12,164,32
mav 8,168,36
R 24,59392
mav 12,172,36
s_waitcnt lgkmcnt(3)
mav 8,176,40
R 28,60416
mav 12,180,40
mav 8,184,44
R 32,61440
mav 12,188,44
s_waitcnt lgkmcnt(3)
mav 8,192,16
R 36,62464
mav 12,196,16
mav 8,200,20
R 40,63488
mav 12,204,20
s_waitcnt lgkmcnt(3)
mav 8,208,24
R 44,64512
mav 12,212,24
mav 8,216,28
mav 12,220,28
s_waitcnt lgkmcnt(2)
LsE3:
s_cbranch_execz LsE7
mav 8,224,32
v_add_u32 v64, 1, v64
mav 12,228,32
mav 8,232,36
mav 12,236,36
s_waitcnt lgkmcnt(0)
mav 8,240,40
mav 12,244,40
mav 8,248,44
mav 12,252,44
s_add_u32 s49, s49, 313344
s_barrier
ds_read_b64 v[66:67], v88 offset:128
s_waitcnt vmcnt(0)
buffer_store_dword v64, v81, s[36:39], s52 offen sc1
s_waitcnt lgkmcnt(0)
v_fma_f32 v68, v67, v48, v90
v_fma_f32 v8, v8, v66, v68
v_fma_f32 v69, v67, v49, v91
v_fma_f32 v9, v9, v66, v69
v_mul_f32_e64 v8, v8, |v8|
v_mul_f32_e64 v9, v9, |v9|
v_cvt_pk_bf16_f32 v8, v8, v9
v_pk_max_i16 v56, v8, 0
v_fma_f32 v68, v67, v50, v92
v_fma_f32 v10, v10, v66, v68
v_fma_f32 v69, v67, v51, v93
v_fma_f32 v11, v11, v66, v69
v_mul_f32_e64 v10, v10, |v10|
v_mul_f32_e64 v11, v11, |v11|
v_cvt_pk_bf16_f32 v10, v10, v11
v_pk_max_i16 v57, v10, 0
v_fma_f32 v68, v67, v52, v94
v_fma_f32 v12, v12, v66, v68
v_fma_f32 v69, v67, v53, v95
v_fma_f32 v13, v13, v66, v69
v_mul_f32_e64 v12, v12, |v12|
v_mul_f32_e64 v13, v13, |v13|
v_cvt_pk_bf16_f32 v12, v12, v13
v_pk_max_i16 v58, v12, 0
v_fma_f32 v68, v67, v54, v96
v_fma_f32 v14, v14, v66, v68
v_fma_f32 v69, v67, v55, v97
v_fma_f32 v15, v15, v66, v69
v_mul_f32_e64 v14, v14, |v14|
v_mul_f32_e64 v15, v15, |v15|
v_cvt_pk_bf16_f32 v14, v14, v15
v_pk_max_i16 v59, v14, 0
buffer_store_dwordx4 v[56:59], v83, s[36:39], s49 offen sc1
v_add_u32 v64, 37, v64
s_waitcnt vmcnt(0)
buffer_store_dword v64, v81, s[36:39], s52 offen sc1
s_endpgm
CE0_1:
s_cmp_eq_u32 s61, 0
s_cbranch_scc1 BE0_1
s_sub_u32 s61, s61, 1
s_sleep 8
buffer_load_dwordx2 v[70:71], v86, s[36:39], 0 offen sc1
s_waitcnt vmcnt(0)
ds_write_b32 v82, v70
v_sub_u32 v70, v70, v87
v_xor_b32 v71, v71, v65
v_cmp_lt_u64 vcc, s[62:63], v[70:71]
s_waitcnt lgkmcnt(0)
s_cbranch_vccnz CE0_1
s_branch BE0_1
CE0_2:
s_mov_b32 s58, m0
s_mov_b32 m0, 144384
DE0_2:
s_cmp_eq_u32 s61, 0
s_cbranch_scc1 EE0_2
s_sub_u32 s61, s61, 1
buffer_load_dword v86, s[36:39], 0 offen sc1 lds
s_sleep 1
ds_read_b32 v70, v82
s_waitcnt lgkmcnt(0)
v_cmp_gt_i32 vcc, v87, v70
s_cbranch_vccnz DE0_2
EE0_2:
s_mov_b32 m0, s58
s_branch BE0_2
CE0_3:
s_mov_b32 s58, m0
s_mov_b32 m0, 144384
DE0_3:
s_cmp_eq_u32 s61, 0
s_cbranch_scc1 EE0_3
s_sub_u32 s61, s61, 1
buffer_load_dword v86, s[36:39], 0 offen sc1 lds
s_sleep 1
ds_read_b32 v70, v82
s_waitcnt lgkmcnt(0)
v_cmp_gt_i32 vcc, v87, v70
s_cbranch_vccnz DE0_3
EE0_3:
s_mov_b32 m0, s58
s_branch BE0_3
CE0_5:
s_mov_b32 s58, m0
s_mov_b32 m0, 144384
DE0_5:
s_cmp_eq_u32 s61, 0
s_cbranch_scc1 EE0_5
s_sub_u32 s61, s61, 1
buffer_load_dword v86, s[36:39], 0 offen sc1 lds
s_sleep 1
ds_read_b32 v70, v82
s_waitcnt lgkmcnt(0)
v_cmp_gt_i32 vcc, v87, v70
s_cbranch_vccnz DE0_5
EE0_5:
s_mov_b32 m0, s58
s_branch BE0_5
CE0_6:
s_mov_b32 s58, m0
s_mov_b32 m0, 144384
DE0_6:
s_cmp_eq_u32 s61, 0
s_cbranch_scc1 EE0_6
s_sub_u32 s61, s61, 1
buffer_load_dword v86, s[36:39], 0 offen sc1 lds
s_sleep 1
ds_read_b32 v70, v82
s_waitcnt lgkmcnt(0)
v_cmp_gt_i32 vcc, v87, v70
s_cbranch_vccnz DE0_6
EE0_6:
s_mov_b32 m0, s58
s_branch BE0_6
CE0_7:
s_mov_b32 s58, m0
s_mov_b32 m0, 144384
DE0_7:
s_cmp_eq_u32 s61, 0
s_cbranch_scc1 EE0_7
s_sub_u32 s61, s61, 1
buffer_load_dword v86, s[36:39], 0 offen sc1 lds
s_sleep 1
ds_read_b32 v70, v82
s_waitcnt lgkmcnt(0)
v_cmp_gt_i32 vcc, v87, v70
s_cbranch_vccnz DE0_7
EE0_7:
s_mov_b32 m0, s58
s_branch BE0_7
CE0_8:
s_mov_b32 s58, m0
s_mov_b32 m0, 144384
DE0_8:
s_cmp_eq_u32 s61, 0
s_cbranch_scc1 EE0_8
s_sub_u32 s61, s61, 1
buffer_load_dword v86, s[36:39], 0 offen sc1 lds
s_sleep 1
ds_read_b32 v70, v82
s_waitcnt lgkmcnt(0)
v_cmp_gt_i32 vcc, v87, v70
s_cbranch_vccnz DE0_8
EE0_8:
s_mov_b32 m0, s58
s_branch BE0_8
LvE1:
LsE4:
s_cbranch_execz LsE8
s_add_u32 s49, s49, 313344
s_barrier
s_add_u32 m0, s50, 0
s_nop 0
D 84,0
D 84,1024
D 84,2048
D 84,3072
s_add_u32 m0, s50, 4096
s_nop 0
D 85,0
D 85,1024
D 85,2048
D 85,3072
s_waitcnt vmcnt(0)
s_add_u32 s49, s49, 313344
s_barrier
s_add_u32 m0, s50, 32768
R 16,0
buffer_load_dwordx2 v[72:73], v89, s[36:39], s49 offen sc1
buffer_load_dwordx2 v[74:75], v89, s[36:39], s49 offen offset:512 sc1
D 84,0
R 20,1024
R 24,2048
D 84,1024
R 28,3072
R 32,4096
D 84,2048
s_waitcnt lgkmcnt(3)
v_mfma_f32_16x16x32_bf16 v[0:3], a[0:3], v[16:19], 0
R 36,5120
v_mfma_f32_16x16x32_bf16 v[4:7], a[4:7], v[16:19], 0
mav 0,8,20
R 40,6144
mav 4,12,20
s_waitcnt lgkmcnt(3)
mav 0,16,24
R 44,7168
mav 4,20,24
mav 0,24,28
D 84,3072
mav 4,28,28
s_waitcnt lgkmcnt(2)
R 16,8192
s_add_u32 m0, s50, 36864
mav 0,32,32
R 20,9216
mav 4,36,32
mav 0,40,36
D 85,0
mav 4,44,36
s_waitcnt lgkmcnt(2)
R 24,10240
mav 0,48,40
R 28,11264
mav 4,52,40
mav 0,56,44
D 85,1024
mav 4,60,44
s_waitcnt lgkmcnt(2)
R 32,12288
mav 0,64,16
R 36,13312
mav 4,68,16
mav 0,72,20
D 85,2048
mav 4,76,20
s_waitcnt lgkmcnt(2)
R 40,14336
mav 0,80,24
R 44,15360
mav 4,84,24
mav 0,88,28
D 85,3072
mav 4,92,28
s_waitcnt lgkmcnt(2)
R 16,16384
mav 0,96,32
R 20,17408
mav 4,100,32
mav 0,104,36
R 24,18432
mav 4,108,36
s_waitcnt lgkmcnt(3)
mav 0,112,40
R 28,19456
mav 4,116,40
mav 0,120,44
R 32,20480
mav 4,124,44
s_waitcnt lgkmcnt(3)
mav 0,128,16
R 36,21504
mav 4,132,16
mav 0,136,20
R 40,22528
mav 4,140,20
s_waitcnt vmcnt(8) lgkmcnt(3)
v_add_f32 v72, v72, v74
v_add_f32 v73, v73, v75
mav 0,144,24
R 44,23552
mav 4,148,24
v_permlane32_swap_b32 v72, v73
mav 0,152,28
R 16,24576
v_add_f32 v74, v72, v73
mav 4,156,28
s_waitcnt lgkmcnt(3)
v_add_f32 v75, v72, v73
mav 0,160,32
R 20,25600
mav 4,164,32
v_permlane16_swap_b32 v74, v75
mav 0,168,36
R 24,26624
v_add_f32 v72, v74, v75
mav 4,172,36
s_waitcnt lgkmcnt(3)
v_add_f32 v73, v74, v75
mav 0,176,40
R 28,27648
mav 4,180,40
v_permlane32_swap_b32 v72, v73
mav 0,184,44
R 32,28672
v_mul_f32 v74, 0x3a800000, v72
mav 4,188,44
s_waitcnt lgkmcnt(3)
v_mul_f32 v75, 0x3a800000, v73
v_fma_f32 v75, -v74, v74, v75
mav 0,192,16
R 36,29696
v_max_f32 v75, 0, v75
mav 4,196,16
v_add_f32 v75, 0x3727c5ac, v75
mav 0,200,20
R 40,30720
mav 4,204,20
s_waitcnt lgkmcnt(3)
v_rsq_f32 v72, v75
mav 0,208,24
R 44,31744
v_mul_f32_e64 v73, -v74, v72
mav 4,212,24
mav 0,216,28
mav 4,220,28
s_waitcnt lgkmcnt(2)
mav 0,224,32
mav 4,228,32
mav 0,232,36
mav 4,236,36
s_waitcnt lgkmcnt(0)
mav 0,240,40
mav 4,244,40
mav 0,248,44
mav 4,252,44
ds_write_b64 v88, v[72:73] offset:0
s_xor_b32 s50, s50, 0x10000
s_waitcnt vmcnt(0) lgkmcnt(0)
s_add_u32 s49, s49, 313344
s_barrier
s_add_u32 m0, s50, 0
R 16,32768
buffer_load_dwordx2 v[72:73], v89, s[36:39], s49 offen sc1
buffer_load_dwordx2 v[74:75], v89, s[36:39], s49 offen offset:512 sc1
D 84,0
ds_read_b64 v[66:67], v88 offset:0
R 20,33792
R 24,34816
D 84,1024
R 28,35840
R 32,36864
D 84,2048
s_waitcnt lgkmcnt(3)
v_mfma_f32_16x16x32_bf16 v[8:11], a[0:3], v[16:19], 0
R 36,37888
v_fma_f32 v68, v67, v48, v90
v_mfma_f32_16x16x32_bf16 v[12:15], a[4:7], v[16:19], 0
v_fma_f32 v0, v0, v66, v68
v_fma_f32 v69, v67, v49, v91
mav 8,8,20
R 40,38912
v_fma_f32 v1, v1, v66, v69
mav 12,12,20
s_waitcnt lgkmcnt(3)
v_mul_f32_e64 v0, v0, |v0|
v_mul_f32_e64 v1, v1, |v1|
mav 8,16,24
R 44,39936
v_cvt_pk_bf16_f32 v0, v0, v1
mav 12,20,24
v_pk_max_i16 v56, v0, 0
v_fma_f32 v68, v67, v50, v92
mav 8,24,28
D 84,3072
mav 12,28,28
s_waitcnt lgkmcnt(2)
R 16,40960
s_add_u32 m0, s50, 4096
mav 8,32,32
R 20,41984
v_fma_f32 v2, v2, v66, v68
mav 12,36,32
v_fma_f32 v69, v67, v51, v93
v_fma_f32 v3, v3, v66, v69
mav 8,40,36
D 85,0
mav 12,44,36
s_waitcnt lgkmcnt(2)
R 24,43008
v_mul_f32_e64 v2, v2, |v2|
mav 8,48,40
R 28,44032
v_mul_f32_e64 v3, v3, |v3|
mav 12,52,40
v_cvt_pk_bf16_f32 v2, v2, v3
v_pk_max_i16 v57, v2, 0
mav 8,56,44
D 85,1024
mav 12,60,44
s_waitcnt lgkmcnt(2)
R 32,45056
v_fma_f32 v68, v67, v52, v94
mav 8,64,16
R 36,46080
v_fma_f32 v4, v4, v66, v68
mav 12,68,16
v_fma_f32 v69, v67, v53, v95
v_fma_f32 v5, v5, v66, v69
mav 8,72,20
D 85,2048
mav 12,76,20
s_waitcnt lgkmcnt(2)
R 40,47104
v_mul_f32_e64 v4, v4, |v4|
mav 8,80,24
R 44,48128
v_mul_f32_e64 v5, v5, |v5|
mav 12,84,24
v_cvt_pk_bf16_f32 v4, v4, v5
v_pk_max_i16 v58, v4, 0
mav 8,88,28
D 85,3072
mav 12,92,28
s_waitcnt lgkmcnt(2)
R 16,49152
v_fma_f32 v68, v67, v54, v96
mav 8,96,32
R 20,50176
v_fma_f32 v6, v6, v66, v68
mav 12,100,32
v_fma_f32 v69, v67, v55, v97
v_fma_f32 v7, v7, v66, v69
mav 8,104,36
R 24,51200
v_mul_f32_e64 v6, v6, |v6|
mav 12,108,36
s_waitcnt lgkmcnt(3)
v_mul_f32_e64 v7, v7, |v7|
v_cvt_pk_bf16_f32 v6, v6, v7
mav 8,112,40
R 28,52224
v_pk_max_i16 v59, v6, 0
mav 12,116,40
mav 8,120,44
buffer_store_dwordx4 v[56:59], v83, s[36:39], s49 offen sc1
R 32,53248
mav 12,124,44
s_waitcnt lgkmcnt(3)
mav 8,128,16
R 36,54272
mav 12,132,16
mav 8,136,20
R 40,55296
mav 12,140,20
s_waitcnt vmcnt(9) lgkmcnt(3)
v_add_f32 v72, v72, v74
v_add_f32 v73, v73, v75
mav 8,144,24
R 44,56320
mav 12,148,24
v_permlane32_swap_b32 v72, v73
mav 8,152,28
R 16,57344
v_add_f32 v74, v72, v73
mav 12,156,28
s_waitcnt lgkmcnt(3)
v_add_f32 v75, v72, v73
mav 8,160,32
R 20,58368
mav 12,164,32
v_permlane16_swap_b32 v74, v75
mav 8,168,36
R 24,59392
v_add_f32 v72, v74, v75
mav 12,172,36
s_waitcnt lgkmcnt(3)
v_add_f32 v73, v74, v75
mav 8,176,40
R 28,60416
mav 12,180,40
v_permlane32_swap_b32 v72, v73
mav 8,184,44
R 32,61440
v_mul_f32 v74, 0x3a800000, v72
mav 12,188,44
s_waitcnt lgkmcnt(3)
v_mul_f32 v75, 0x3a800000, v73
v_fma_f32 v75, -v74, v74, v75
mav 8,192,16
R 36,62464
v_max_f32 v75, 0, v75
mav 12,196,16
v_add_f32 v75, 0x3727c5ac, v75
mav 8,200,20
R 40,63488
mav 12,204,20
s_waitcnt lgkmcnt(3)
v_rsq_f32 v72, v75
mav 8,208,24
R 44,64512
v_mul_f32_e64 v73, -v74, v72
mav 12,212,24
mav 8,216,28
mav 12,220,28
s_waitcnt lgkmcnt(2)
mav 8,224,32
v_add_u32 v64, 1, v64
mav 12,228,32
mav 8,232,36
s_mov_b32 s60, -30
.p2align 6
LE1_1:
mav 12,236,36
s_waitcnt lgkmcnt(0)
mav 8,240,40
mav 12,244,40
mav 8,248,44
mav 12,252,44
ds_write_b64 v88, v[72:73] offset:128
v_xor_b32 v80, 0x10000, v80
s_waitcnt vmcnt(1) lgkmcnt(0)
s_add_u32 s49, s49, 313344
s_barrier
s_add_u32 m0, s50, 32768
R 16,0
buffer_load_dwordx2 v[72:73], v89, s[36:39], s49 offen sc1
buffer_load_dwordx2 v[74:75], v89, s[36:39], s49 offen offset:512 sc1
D 84,0
ds_read_b64 v[66:67], v88 offset:128
R 20,1024
R 24,2048
D 84,1024
R 28,3072
s_waitcnt vmcnt(4)
buffer_store_dword v64, v81, s[36:39], s52 offen sc1
R 32,4096
D 84,2048
s_waitcnt lgkmcnt(3)
v_mfma_f32_16x16x32_bf16 v[0:3], a[0:3], v[16:19], 0
R 36,5120
v_fma_f32 v68, v67, v48, v90
v_mfma_f32_16x16x32_bf16 v[4:7], a[4:7], v[16:19], 0
v_fma_f32 v8, v8, v66, v68
v_fma_f32 v69, v67, v49, v91
mav 0,8,20
R 40,6144
v_fma_f32 v9, v9, v66, v69
mav 4,12,20
s_waitcnt lgkmcnt(3)
v_mul_f32_e64 v8, v8, |v8|
v_mul_f32_e64 v9, v9, |v9|
mav 0,16,24
R 44,7168
v_cvt_pk_bf16_f32 v8, v8, v9
mav 4,20,24
v_pk_max_i16 v56, v8, 0
v_fma_f32 v68, v67, v50, v92
mav 0,24,28
D 84,3072
mav 4,28,28
s_waitcnt lgkmcnt(2)
R 16,8192
s_add_u32 m0, s50, 36864
mav 0,32,32
R 20,9216
v_fma_f32 v10, v10, v66, v68
mav 4,36,32
v_fma_f32 v69, v67, v51, v93
v_fma_f32 v11, v11, v66, v69
mav 0,40,36
D 85,0
mav 4,44,36
s_waitcnt lgkmcnt(2)
R 24,10240
v_mul_f32_e64 v10, v10, |v10|
mav 0,48,40
R 28,11264
v_mul_f32_e64 v11, v11, |v11|
mav 4,52,40
v_cvt_pk_bf16_f32 v10, v10, v11
v_pk_max_i16 v57, v10, 0
mav 0,56,44
D 85,1024
mav 4,60,44
s_waitcnt lgkmcnt(2)
R 32,12288
v_fma_f32 v68, v67, v52, v94
mav 0,64,16
R 36,13312
v_fma_f32 v12, v12, v66, v68
mav 4,68,16
v_fma_f32 v69, v67, v53, v95
v_fma_f32 v13, v13, v66, v69
mav 0,72,20
D 85,2048
mav 4,76,20
s_waitcnt lgkmcnt(2)
R 40,14336
v_mul_f32_e64 v12, v12, |v12|
mav 0,80,24
R 44,15360
v_mul_f32_e64 v13, v13, |v13|
mav 4,84,24
v_cvt_pk_bf16_f32 v12, v12, v13
v_pk_max_i16 v58, v12, 0
mav 0,88,28
D 85,3072
mav 4,92,28
s_waitcnt lgkmcnt(2)
R 16,16384
v_fma_f32 v68, v67, v54, v96
mav 0,96,32
R 20,17408
v_fma_f32 v14, v14, v66, v68
mav 4,100,32
v_fma_f32 v69, v67, v55, v97
v_fma_f32 v15, v15, v66, v69
mav 0,104,36
R 24,18432
v_mul_f32_e64 v14, v14, |v14|
mav 4,108,36
s_waitcnt lgkmcnt(3)
LsE5:
s_cbranch_execz LsE9
v_mul_f32_e64 v15, v15, |v15|
v_cvt_pk_bf16_f32 v14, v14, v15
mav 0,112,40
R 28,19456
v_pk_max_i16 v59, v14, 0
mav 4,116,40
mav 0,120,44
buffer_store_dwordx4 v[56:59], v83, s[36:39], s49 offen sc1
R 32,20480
mav 4,124,44
s_waitcnt lgkmcnt(3)
mav 0,128,16
R 36,21504
mav 4,132,16
mav 0,136,20
R 40,22528
mav 4,140,20
s_waitcnt vmcnt(10) lgkmcnt(3)
v_add_f32 v72, v72, v74
v_add_f32 v73, v73, v75
mav 0,144,24
R 44,23552
mav 4,148,24
v_permlane32_swap_b32 v72, v73
mav 0,152,28
R 16,24576
v_add_f32 v74, v72, v73
mav 4,156,28
s_waitcnt lgkmcnt(3)
v_add_f32 v75, v72, v73
mav 0,160,32
R 20,25600
mav 4,164,32
v_permlane16_swap_b32 v74, v75
mav 0,168,36
R 24,26624
v_add_f32 v72, v74, v75
mav 4,172,36
s_waitcnt lgkmcnt(3)
v_add_f32 v73, v74, v75
mav 0,176,40
R 28,27648
mav 4,180,40
v_permlane32_swap_b32 v72, v73
mav 0,184,44
R 32,28672
v_mul_f32 v74, 0x3a800000, v72
mav 4,188,44
s_waitcnt lgkmcnt(3)
v_mul_f32 v75, 0x3a800000, v73
v_fma_f32 v75, -v74, v74, v75
mav 0,192,16
R 36,29696
v_max_f32 v75, 0, v75
mav 4,196,16
v_add_f32 v75, 0x3727c5ac, v75
mav 0,200,20
R 40,30720
mav 4,204,20
s_waitcnt lgkmcnt(3)
v_rsq_f32 v72, v75
mav 0,208,24
R 44,31744
v_mul_f32_e64 v73, -v74, v72
mav 4,212,24
mav 0,216,28
mav 4,220,28
s_waitcnt lgkmcnt(2)
mav 0,224,32
v_add_u32 v64, 1, v64
mav 4,228,32
mav 0,232,36
mav 4,236,36
s_waitcnt lgkmcnt(0)
mav 0,240,40
mav 4,244,40
mav 0,248,44
mav 4,252,44
ds_write_b64 v88, v[72:73] offset:0
s_xor_b32 s50, s50, 0x10000
s_waitcnt vmcnt(1) lgkmcnt(0)
s_add_u32 s49, s49, 313344
s_barrier
s_add_u32 m0, s50, 0
R 16,32768
buffer_load_dwordx2 v[72:73], v89, s[36:39], s49 offen sc1
buffer_load_dwordx2 v[74:75], v89, s[36:39], s49 offen offset:512 sc1
D 84,0
ds_read_b64 v[66:67], v88 offset:0
R 20,33792
R 24,34816
D 84,1024
R 28,35840
s_waitcnt vmcnt(4)
buffer_store_dword v64, v81, s[36:39], s52 offen sc1
R 32,36864
D 84,2048
s_waitcnt lgkmcnt(3)
v_mfma_f32_16x16x32_bf16 v[8:11], a[0:3], v[16:19], 0
R 36,37888
v_fma_f32 v68, v67, v48, v90
v_mfma_f32_16x16x32_bf16 v[12:15], a[4:7], v[16:19], 0
v_fma_f32 v0, v0, v66, v68
v_fma_f32 v69, v67, v49, v91
mav 8,8,20
R 40,38912
v_fma_f32 v1, v1, v66, v69
mav 12,12,20
s_waitcnt lgkmcnt(3)
v_mul_f32_e64 v0, v0, |v0|
v_mul_f32_e64 v1, v1, |v1|
mav 8,16,24
R 44,39936
v_cvt_pk_bf16_f32 v0, v0, v1
mav 12,20,24
v_pk_max_i16 v56, v0, 0
v_fma_f32 v68, v67, v50, v92
mav 8,24,28
D 84,3072
mav 12,28,28
s_waitcnt lgkmcnt(2)
R 16,40960
s_add_u32 m0, s50, 4096
mav 8,32,32
R 20,41984
v_fma_f32 v2, v2, v66, v68
mav 12,36,32
v_fma_f32 v69, v67, v51, v93
v_fma_f32 v3, v3, v66, v69
mav 8,40,36
D 85,0
mav 12,44,36
s_waitcnt lgkmcnt(2)
R 24,43008
v_mul_f32_e64 v2, v2, |v2|
mav 8,48,40
R 28,44032
v_mul_f32_e64 v3, v3, |v3|
mav 12,52,40
v_cvt_pk_bf16_f32 v2, v2, v3
v_pk_max_i16 v57, v2, 0
mav 8,56,44
D 85,1024
mav 12,60,44
s_waitcnt lgkmcnt(2)
R 32,45056
v_fma_f32 v68, v67, v52, v94
mav 8,64,16
R 36,46080
v_fma_f32 v4, v4, v66, v68
mav 12,68,16
v_fma_f32 v69, v67, v53, v95
v_fma_f32 v5, v5, v66, v69
mav 8,72,20
D 85,2048
mav 12,76,20
s_waitcnt lgkmcnt(2)
R 40,47104
v_mul_f32_e64 v4, v4, |v4|
mav 8,80,24
R 44,48128
v_mul_f32_e64 v5, v5, |v5|
mav 12,84,24
v_cvt_pk_bf16_f32 v4, v4, v5
v_pk_max_i16 v58, v4, 0
mav 8,88,28
D 85,3072
mav 12,92,28
s_waitcnt lgkmcnt(2)
R 16,49152
v_fma_f32 v68, v67, v54, v96
mav 8,96,32
R 20,50176
v_fma_f32 v6, v6, v66, v68
mav 12,100,32
v_fma_f32 v69, v67, v55, v97
v_fma_f32 v7, v7, v66, v69
mav 8,104,36
R 24,51200
v_mul_f32_e64 v6, v6, |v6|
mav 12,108,36
s_waitcnt lgkmcnt(3)
v_mul_f32_e64 v7, v7, |v7|
v_cvt_pk_bf16_f32 v6, v6, v7
mav 8,112,40
R 28,52224
v_pk_max_i16 v59, v6, 0
mav 12,116,40
mav 8,120,44
buffer_store_dwordx4 v[56:59], v83, s[36:39], s49 offen sc1
R 32,53248
mav 12,124,44
s_waitcnt lgkmcnt(3)
mav 8,128,16
R 36,54272
mav 12,132,16
mav 8,136,20
R 40,55296
mav 12,140,20
s_waitcnt vmcnt(10) lgkmcnt(3)
v_add_f32 v72, v72, v74
v_add_f32 v73, v73, v75
mav 8,144,24
R 44,56320
mav 12,148,24
v_permlane32_swap_b32 v72, v73
mav 8,152,28
R 16,57344
v_add_f32 v74, v72, v73
mav 12,156,28
s_waitcnt lgkmcnt(3)
v_add_f32 v75, v72, v73
mav 8,160,32
R 20,58368
mav 12,164,32
v_permlane16_swap_b32 v74, v75
mav 8,168,36
R 24,59392
v_add_f32 v72, v74, v75
mav 12,172,36
s_waitcnt lgkmcnt(3)
v_add_f32 v73, v74, v75
mav 8,176,40
R 28,60416
mav 12,180,40
v_permlane32_swap_b32 v72, v73
mav 8,184,44
R 32,61440
v_mul_f32 v74, 0x3a800000, v72
mav 12,188,44
s_waitcnt lgkmcnt(3)
v_mul_f32 v75, 0x3a800000, v73
v_fma_f32 v75, -v74, v74, v75
mav 8,192,16
R 36,62464
v_max_f32 v75, 0, v75
mav 12,196,16
v_add_f32 v75, 0x3727c5ac, v75
mav 8,200,20
R 40,63488
mav 12,204,20
s_waitcnt lgkmcnt(3)
v_rsq_f32 v72, v75
mav 8,208,24
R 44,64512
v_mul_f32_e64 v73, -v74, v72
mav 12,212,24
mav 8,216,28
mav 12,220,28
s_waitcnt lgkmcnt(2)
mav 8,224,32
v_add_u32 v64, 1, v64
mav 12,228,32
s_add_u32 s60, s60, 1
mav 8,232,36
s_cbranch_scc0 LE1_1
mav 12,236,36
s_waitcnt lgkmcnt(0)
mav 8,240,40
mav 12,244,40
mav 8,248,44
mav 12,252,44
ds_write_b64 v88, v[72:73] offset:128
v_xor_b32 v80, 0x10000, v80
s_waitcnt vmcnt(1) lgkmcnt(0)
s_add_u32 s49, s49, 313344
s_barrier
s_add_u32 m0, s50, 32768
R 16,0
buffer_load_dwordx2 v[72:73], v89, s[36:39], s49 offen sc1
buffer_load_dwordx2 v[74:75], v89, s[36:39], s49 offen offset:512 sc1
D 84,0
ds_read_b64 v[66:67], v88 offset:128
R 20,1024
R 24,2048
D 84,1024
R 28,3072
s_waitcnt vmcnt(4)
buffer_store_dword v64, v81, s[36:39], s52 offen sc1
R 32,4096
D 84,2048
s_waitcnt lgkmcnt(3)
v_mfma_f32_16x16x32_bf16 v[0:3], a[0:3], v[16:19], 0
R 36,5120
v_fma_f32 v68, v67, v48, v90
v_mfma_f32_16x16x32_bf16 v[4:7], a[4:7], v[16:19], 0
v_fma_f32 v8, v8, v66, v68
v_fma_f32 v69, v67, v49, v91
mav 0,8,20
R 40,6144
v_fma_f32 v9, v9, v66, v69
mav 4,12,20
s_waitcnt lgkmcnt(3)
v_mul_f32_e64 v8, v8, |v8|
v_mul_f32_e64 v9, v9, |v9|
mav 0,16,24
R 44,7168
v_cvt_pk_bf16_f32 v8, v8, v9
mav 4,20,24
v_pk_max_i16 v56, v8, 0
v_fma_f32 v68, v67, v50, v92
mav 0,24,28
D 84,3072
mav 4,28,28
s_waitcnt lgkmcnt(2)
R 16,8192
s_add_u32 m0, s50, 36864
mav 0,32,32
R 20,9216
v_fma_f32 v10, v10, v66, v68
mav 4,36,32
v_fma_f32 v69, v67, v51, v93
v_fma_f32 v11, v11, v66, v69
mav 0,40,36
D 85,0
mav 4,44,36
s_waitcnt lgkmcnt(2)
R 24,10240
v_mul_f32_e64 v10, v10, |v10|
mav 0,48,40
R 28,11264
v_mul_f32_e64 v11, v11, |v11|
mav 4,52,40
v_cvt_pk_bf16_f32 v10, v10, v11
v_pk_max_i16 v57, v10, 0
mav 0,56,44
D 85,1024
mav 4,60,44
s_waitcnt lgkmcnt(2)
R 32,12288
v_fma_f32 v68, v67, v52, v94
mav 0,64,16
R 36,13312
v_fma_f32 v12, v12, v66, v68
mav 4,68,16
v_fma_f32 v69, v67, v53, v95
v_fma_f32 v13, v13, v66, v69
mav 0,72,20
D 85,2048
mav 4,76,20
s_waitcnt lgkmcnt(2)
R 40,14336
v_mul_f32_e64 v12, v12, |v12|
mav 0,80,24
R 44,15360
v_mul_f32_e64 v13, v13, |v13|
mav 4,84,24
v_cvt_pk_bf16_f32 v12, v12, v13
v_pk_max_i16 v58, v12, 0
mav 0,88,28
D 85,3072
mav 4,92,28
s_waitcnt lgkmcnt(2)
R 16,16384
v_fma_f32 v68, v67, v54, v96
mav 0,96,32
R 20,17408
v_fma_f32 v14, v14, v66, v68
mav 4,100,32
v_fma_f32 v69, v67, v55, v97
v_fma_f32 v15, v15, v66, v69
mav 0,104,36
R 24,18432
v_mul_f32_e64 v14, v14, |v14|
mav 4,108,36
s_waitcnt lgkmcnt(3)
v_mul_f32_e64 v15, v15, |v15|
v_cvt_pk_bf16_f32 v14, v14, v15
mav 0,112,40
R 28,19456
v_pk_max_i16 v59, v14, 0
mav 4,116,40
mav 0,120,44
buffer_store_dwordx4 v[56:59], v83, s[36:39], s49 offen sc1
R 32,20480
mav 4,124,44
s_waitcnt lgkmcnt(3)
mav 0,128,16
R 36,21504
mav 4,132,16
mav 0,136,20
R 40,22528
mav 4,140,20
s_waitcnt vmcnt(10) lgkmcnt(3)
v_add_f32 v72, v72, v74
v_add_f32 v73, v73, v75
mav 0,144,24
R 44,23552
mav 4,148,24
v_permlane32_swap_b32 v72, v73
mav 0,152,28
R 16,24576
v_add_f32 v74, v72, v73
mav 4,156,28
s_waitcnt lgkmcnt(3)
v_add_f32 v75, v72, v73
mav 0,160,32
R 20,25600
mav 4,164,32
v_permlane16_swap_b32 v74, v75
mav 0,168,36
R 24,26624
v_add_f32 v72, v74, v75
mav 4,172,36
s_waitcnt lgkmcnt(3)
v_add_f32 v73, v74, v75
mav 0,176,40
R 28,27648
mav 4,180,40
v_permlane32_swap_b32 v72, v73
mav 0,184,44
R 32,28672
v_mul_f32 v74, 0x3a800000, v72
mav 4,188,44
s_waitcnt lgkmcnt(3)
v_mul_f32 v75, 0x3a800000, v73
v_fma_f32 v75, -v74, v74, v75
mav 0,192,16
R 36,29696
v_max_f32 v75, 0, v75
mav 4,196,16
v_add_f32 v75, 0x3727c5ac, v75
mav 0,200,20
R 40,30720
mav 4,204,20
s_waitcnt lgkmcnt(3)
v_rsq_f32 v72, v75
mav 0,208,24
R 44,31744
v_mul_f32_e64 v73, -v74, v72
mav 4,212,24
mav 0,216,28
mav 4,220,28
s_waitcnt lgkmcnt(2)
mav 0,224,32
v_add_u32 v64, 1, v64
mav 4,228,32
mav 0,232,36
mav 4,236,36
s_waitcnt lgkmcnt(0)
mav 0,240,40
mav 4,244,40
mav 0,248,44
mav 4,252,44
ds_write_b64 v88, v[72:73] offset:0
s_waitcnt vmcnt(1) lgkmcnt(0)
s_add_u32 s49, s49, 313344
s_barrier
R 16,32768
buffer_load_dwordx2 v[72:73], v89, s[36:39], s49 offen sc1
buffer_load_dwordx2 v[74:75], v89, s[36:39], s49 offen offset:512 sc1
ds_read_b64 v[66:67], v88 offset:0
R 20,33792
R 24,34816
R 28,35840
s_waitcnt vmcnt(2)
buffer_store_dword v64, v81, s[36:39], s52 offen sc1
R 32,36864
s_waitcnt lgkmcnt(3)
v_mfma_f32_16x16x32_bf16 v[8:11], a[0:3], v[16:19], 0
R 36,37888
v_fma_f32 v68, v67, v48, v90
v_mfma_f32_16x16x32_bf16 v[12:15], a[4:7], v[16:19], 0
v_fma_f32 v0, v0, v66, v68
v_fma_f32 v69, v67, v49, v91
mav 8,8,20
R 40,38912
v_fma_f32 v1, v1, v66, v69
mav 12,12,20
s_waitcnt lgkmcnt(3)
v_mul_f32_e64 v0, v0, |v0|
v_mul_f32_e64 v1, v1, |v1|
mav 8,16,24
R 44,39936
v_cvt_pk_bf16_f32 v0, v0, v1
mav 12,20,24
v_pk_max_i16 v56, v0, 0
v_fma_f32 v68, v67, v50, v92
mav 8,24,28
R 16,40960
v_fma_f32 v2, v2, v66, v68
mav 12,28,28
s_waitcnt lgkmcnt(3)
v_fma_f32 v69, v67, v51, v93
v_fma_f32 v3, v3, v66, v69
mav 8,32,32
R 20,41984
v_mul_f32_e64 v2, v2, |v2|
mav 12,36,32
v_mul_f32_e64 v3, v3, |v3|
v_cvt_pk_bf16_f32 v2, v2, v3
mav 8,40,36
R 24,43008
v_pk_max_i16 v57, v2, 0
mav 12,44,36
s_waitcnt lgkmcnt(3)
v_fma_f32 v68, v67, v52, v94
v_fma_f32 v4, v4, v66, v68
mav 8,48,40
R 28,44032
v_fma_f32 v69, v67, v53, v95
mav 12,52,40
v_fma_f32 v5, v5, v66, v69
v_mul_f32_e64 v4, v4, |v4|
mav 8,56,44
R 32,45056
v_mul_f32_e64 v5, v5, |v5|
mav 12,60,44
s_waitcnt lgkmcnt(3)
v_cvt_pk_bf16_f32 v4, v4, v5
v_pk_max_i16 v58, v4, 0
mav 8,64,16
R 36,46080
v_fma_f32 v68, v67, v54, v96
mav 12,68,16
v_fma_f32 v6, v6, v66, v68
v_fma_f32 v69, v67, v55, v97
mav 8,72,20
R 40,47104
v_fma_f32 v7, v7, v66, v69
mav 12,76,20
s_waitcnt lgkmcnt(3)
v_mul_f32_e64 v6, v6, |v6|
v_mul_f32_e64 v7, v7, |v7|
mav 8,80,24
R 44,48128
v_cvt_pk_bf16_f32 v6, v6, v7
mav 12,84,24
)"
R"(
v_pk_max_i16 v59, v6, 0
mav 8,88,28
buffer_store_dwordx4 v[56:59], v83, s[36:39], s49 offen sc1
R 16,49152
mav 12,92,28
s_waitcnt lgkmcnt(3)
mav 8,96,32
R 20,50176
mav 12,100,32
mav 8,104,36
R 24,51200
mav 12,108,36
s_waitcnt lgkmcnt(3)
mav 8,112,40
R 28,52224
mav 12,116,40
mav 8,120,44
R 32,53248
mav 12,124,44
s_waitcnt lgkmcnt(3)
mav 8,128,16
R 36,54272
mav 12,132,16
mav 8,136,20
R 40,55296
mav 12,140,20
s_waitcnt vmcnt(2) lgkmcnt(3)
v_add_f32 v72, v72, v74
v_add_f32 v73, v73, v75
mav 8,144,24
R 44,56320
mav 12,148,24
v_permlane32_swap_b32 v72, v73
mav 8,152,28
LsE6:
s_cbranch_execz LsE10
R 16,57344
v_add_f32 v74, v72, v73
mav 12,156,28
s_waitcnt lgkmcnt(3)
v_add_f32 v75, v72, v73
mav 8,160,32
R 20,58368
mav 12,164,32
v_permlane16_swap_b32 v74, v75
mav 8,168,36
R 24,59392
v_add_f32 v72, v74, v75
mav 12,172,36
s_waitcnt lgkmcnt(3)
v_add_f32 v73, v74, v75
mav 8,176,40
R 28,60416
mav 12,180,40
v_permlane32_swap_b32 v72, v73
mav 8,184,44
R 32,61440
v_mul_f32 v74, 0x3a800000, v72
mav 12,188,44
s_waitcnt lgkmcnt(3)
v_mul_f32 v75, 0x3a800000, v73
v_fma_f32 v75, -v74, v74, v75
mav 8,192,16
R 36,62464
v_max_f32 v75, 0, v75
mav 12,196,16
v_add_f32 v75, 0x3727c5ac, v75
mav 8,200,20
R 40,63488
mav 12,204,20
s_waitcnt lgkmcnt(3)
v_rsq_f32 v72, v75
mav 8,208,24
R 44,64512
v_mul_f32_e64 v73, -v74, v72
mav 12,212,24
mav 8,216,28
mav 12,220,28
s_waitcnt lgkmcnt(2)
mav 8,224,32
v_add_u32 v64, 1, v64
mav 12,228,32
mav 8,232,36
mav 12,236,36
s_waitcnt lgkmcnt(0)
mav 8,240,40
mav 12,244,40
mav 8,248,44
mav 12,252,44
ds_write_b64 v88, v[72:73] offset:128
s_waitcnt lgkmcnt(0)
s_add_u32 s49, s49, 313344
s_barrier
ds_read_b64 v[66:67], v88 offset:128
s_waitcnt vmcnt(0)
buffer_store_dword v64, v81, s[36:39], s52 offen sc1
s_waitcnt lgkmcnt(0)
v_fma_f32 v68, v67, v48, v90
v_fma_f32 v8, v8, v66, v68
v_fma_f32 v69, v67, v49, v91
v_fma_f32 v9, v9, v66, v69
v_mul_f32_e64 v8, v8, |v8|
v_mul_f32_e64 v9, v9, |v9|
v_cvt_pk_bf16_f32 v8, v8, v9
v_pk_max_i16 v56, v8, 0
v_fma_f32 v68, v67, v50, v92
v_fma_f32 v10, v10, v66, v68
v_fma_f32 v69, v67, v51, v93
v_fma_f32 v11, v11, v66, v69
v_mul_f32_e64 v10, v10, |v10|
v_mul_f32_e64 v11, v11, |v11|
v_cvt_pk_bf16_f32 v10, v10, v11
v_pk_max_i16 v57, v10, 0
v_fma_f32 v68, v67, v52, v94
v_fma_f32 v12, v12, v66, v68
v_fma_f32 v69, v67, v53, v95
v_fma_f32 v13, v13, v66, v69
v_mul_f32_e64 v12, v12, |v12|
v_mul_f32_e64 v13, v13, |v13|
v_cvt_pk_bf16_f32 v12, v12, v13
v_pk_max_i16 v58, v12, 0
v_fma_f32 v68, v67, v54, v96
v_fma_f32 v14, v14, v66, v68
v_fma_f32 v69, v67, v55, v97
v_fma_f32 v15, v15, v66, v69
v_mul_f32_e64 v14, v14, |v14|
v_mul_f32_e64 v15, v15, |v15|
v_cvt_pk_bf16_f32 v14, v14, v15
v_pk_max_i16 v59, v14, 0
buffer_store_dwordx4 v[56:59], v83, s[36:39], s49 offen sc1
v_add_u32 v64, 37, v64
s_waitcnt vmcnt(0)
buffer_store_dword v64, v81, s[36:39], s52 offen sc1
s_endpgm
LvE2:
LsE7:
s_cbranch_execz LsE11
s_add_u32 s49, s49, 313344
s_barrier
s_add_u32 m0, s50, 0
s_nop 0
D 84,0
D 84,1024
D 84,2048
D 84,3072
s_add_u32 m0, s50, 4096
s_nop 0
D 85,0
D 85,1024
D 85,2048
D 85,3072
s_waitcnt vmcnt(0)
s_add_u32 s49, s49, 313344
s_barrier
s_add_u32 m0, s50, 32768
R 16,0
D 84,0
R 20,1024
R 24,2048
D 84,1024
R 28,3072
R 32,4096
D 84,2048
s_waitcnt lgkmcnt(3)
v_mfma_f32_16x16x32_bf16 v[0:3], a[0:3], v[16:19], 0
R 36,5120
v_mfma_f32_16x16x32_bf16 v[4:7], a[4:7], v[16:19], 0
mav 0,8,20
R 40,6144
mav 4,12,20
s_waitcnt lgkmcnt(3)
mav 0,16,24
D 84,3072
mav 4,20,24
s_add_u32 m0, s50, 36864
R 44,7168
mav 0,24,28
R 16,8192
mav 4,28,28
s_waitcnt lgkmcnt(3)
mav 0,32,32
D 85,0
mav 4,36,32
R 20,9216
mav 0,40,36
R 24,10240
mav 4,44,36
s_waitcnt lgkmcnt(3)
mav 0,48,40
D 85,1024
mav 4,52,40
R 28,11264
mav 0,56,44
R 32,12288
mav 4,60,44
s_waitcnt lgkmcnt(3)
mav 0,64,16
D 85,2048
mav 4,68,16
R 36,13312
mav 0,72,20
R 40,14336
mav 4,76,20
s_waitcnt lgkmcnt(3)
mav 0,80,24
D 85,3072
mav 4,84,24
R 44,15360
mav 0,88,28
R 16,16384
mav 4,92,28
s_waitcnt lgkmcnt(3)
mav 0,96,32
R 20,17408
mav 4,100,32
mav 0,104,36
R 24,18432
mav 4,108,36
s_waitcnt lgkmcnt(3)
mav 0,112,40
R 28,19456
mav 4,116,40
mav 0,120,44
R 32,20480
mav 4,124,44
s_waitcnt lgkmcnt(3)
mav 0,128,16
R 36,21504
mav 4,132,16
mav 0,136,20
R 40,22528
mav 4,140,20
s_waitcnt lgkmcnt(3)
mav 0,144,24
R 44,23552
mav 4,148,24
mav 0,152,28
R 16,24576
mav 4,156,28
s_waitcnt lgkmcnt(3)
mav 0,160,32
R 20,25600
mav 4,164,32
mav 0,168,36
R 24,26624
mav 4,172,36
s_waitcnt lgkmcnt(3)
mav 0,176,40
R 28,27648
mav 4,180,40
mav 0,184,44
R 32,28672
mav 4,188,44
s_waitcnt lgkmcnt(3)
mav 0,192,16
R 36,29696
mav 4,196,16
mav 0,200,20
R 40,30720
mav 4,204,20
s_waitcnt lgkmcnt(3)
mav 0,208,24
R 44,31744
mav 4,212,24
mav 0,216,28
mav 4,220,28
s_waitcnt lgkmcnt(2)
mav 0,224,32
mav 4,228,32
mav 0,232,36
mav 4,236,36
s_waitcnt lgkmcnt(0)
mav 0,240,40
mav 4,244,40
mav 0,248,44
mav 4,252,44
s_xor_b32 s50, s50, 0x10000
s_waitcnt vmcnt(0)
s_add_u32 s49, s49, 313344
s_barrier
s_add_u32 m0, s50, 0
R 16,32768
D 84,0
ds_read_b64 v[66:67], v88 offset:0
R 20,33792
R 24,34816
D 84,1024
R 28,35840
R 32,36864
D 84,2048
s_waitcnt lgkmcnt(3)
v_mfma_f32_16x16x32_bf16 v[8:11], a[0:3], v[16:19], 0
R 36,37888
v_fma_f32 v68, v67, v48, v90
v_mfma_f32_16x16x32_bf16 v[12:15], a[4:7], v[16:19], 0
v_fma_f32 v0, v0, v66, v68
v_fma_f32 v69, v67, v49, v91
mav 8,8,20
R 40,38912
v_fma_f32 v1, v1, v66, v69
mav 12,12,20
s_waitcnt lgkmcnt(3)
v_mul_f32_e64 v0, v0, |v0|
v_mul_f32_e64 v1, v1, |v1|
mav 8,16,24
D 84,3072
mav 12,20,24
s_add_u32 m0, s50, 4096
R 44,39936
v_cvt_pk_bf16_f32 v0, v0, v1
mav 8,24,28
R 16,40960
v_pk_max_i16 v56, v0, 0
mav 12,28,28
s_waitcnt lgkmcnt(3)
v_fma_f32 v68, v67, v50, v92
v_fma_f32 v2, v2, v66, v68
mav 8,32,32
D 85,0
mav 12,36,32
R 20,41984
v_fma_f32 v69, v67, v51, v93
mav 8,40,36
R 24,43008
v_fma_f32 v3, v3, v66, v69
mav 12,44,36
s_waitcnt lgkmcnt(3)
v_mul_f32_e64 v2, v2, |v2|
v_mul_f32_e64 v3, v3, |v3|
mav 8,48,40
D 85,1024
mav 12,52,40
R 28,44032
v_cvt_pk_bf16_f32 v2, v2, v3
mav 8,56,44
R 32,45056
v_pk_max_i16 v57, v2, 0
mav 12,60,44
s_waitcnt lgkmcnt(3)
v_fma_f32 v68, v67, v52, v94
v_fma_f32 v4, v4, v66, v68
mav 8,64,16
D 85,2048
mav 12,68,16
R 36,46080
v_fma_f32 v69, v67, v53, v95
mav 8,72,20
R 40,47104
v_fma_f32 v5, v5, v66, v69
mav 12,76,20
s_waitcnt lgkmcnt(3)
v_mul_f32_e64 v4, v4, |v4|
v_mul_f32_e64 v5, v5, |v5|
mav 8,80,24
D 85,3072
mav 12,84,24
R 44,48128
v_cvt_pk_bf16_f32 v4, v4, v5
mav 8,88,28
R 16,49152
v_pk_max_i16 v58, v4, 0
mav 12,92,28
s_waitcnt lgkmcnt(3)
v_fma_f32 v68, v67, v54, v96
v_fma_f32 v6, v6, v66, v68
mav 8,96,32
R 20,50176
v_fma_f32 v69, v67, v55, v97
mav 12,100,32
v_fma_f32 v7, v7, v66, v69
v_mul_f32_e64 v6, v6, |v6|
mav 8,104,36
R 24,51200
v_mul_f32_e64 v7, v7, |v7|
mav 12,108,36
s_waitcnt lgkmcnt(3)
v_cvt_pk_bf16_f32 v6, v6, v7
v_pk_max_i16 v59, v6, 0
mav 8,112,40
buffer_store_dwordx4 v[56:59], v83, s[36:39], s49 offen sc1
R 28,52224
mav 12,116,40
mav 8,120,44
R 32,53248
mav 12,124,44
s_waitcnt lgkmcnt(3)
mav 8,128,16
R 36,54272
mav 12,132,16
mav 8,136,20
R 40,55296
mav 12,140,20
s_waitcnt lgkmcnt(3)
mav 8,144,24
R 44,56320
mav 12,148,24
mav 8,152,28
R 16,57344
mav 12,156,28
s_waitcnt lgkmcnt(3)
mav 8,160,32
R 20,58368
mav 12,164,32
mav 8,168,36
R 24,59392
mav 12,172,36
s_waitcnt lgkmcnt(3)
mav 8,176,40
R 28,60416
mav 12,180,40
mav 8,184,44
R 32,61440
mav 12,188,44
s_waitcnt lgkmcnt(3)
mav 8,192,16
R 36,62464
mav 12,196,16
mav 8,200,20
R 40,63488
mav 12,204,20
s_waitcnt lgkmcnt(3)
mav 8,208,24
R 44,64512
mav 12,212,24
mav 8,216,28
mav 12,220,28
s_waitcnt lgkmcnt(2)
mav 8,224,32
v_add_u32 v64, 1, v64
mav 12,228,32
mav 8,232,36
s_mov_b32 s60, -30
.p2align 6
LE2_1:
mav 12,236,36
s_waitcnt lgkmcnt(0)
mav 8,240,40
mav 12,244,40
mav 8,248,44
mav 12,252,44
v_xor_b32 v80, 0x10000, v80
s_waitcnt vmcnt(1)
s_add_u32 s49, s49, 313344
s_barrier
s_add_u32 m0, s50, 32768
R 16,0
D 84,0
ds_read_b64 v[66:67], v88 offset:128
R 20,1024
R 24,2048
D 84,1024
R 28,3072
s_waitcnt vmcnt(2)
buffer_store_dword v64, v81, s[36:39], s52 offen sc1
R 32,4096
D 84,2048
s_waitcnt lgkmcnt(3)
v_mfma_f32_16x16x32_bf16 v[0:3], a[0:3], v[16:19], 0
R 36,5120
v_fma_f32 v68, v67, v48, v90
v_mfma_f32_16x16x32_bf16 v[4:7], a[4:7], v[16:19], 0
v_fma_f32 v8, v8, v66, v68
v_fma_f32 v69, v67, v49, v91
mav 0,8,20
R 40,6144
v_fma_f32 v9, v9, v66, v69
mav 4,12,20
s_waitcnt lgkmcnt(3)
v_mul_f32_e64 v8, v8, |v8|
v_mul_f32_e64 v9, v9, |v9|
mav 0,16,24
D 84,3072
mav 4,20,24
s_add_u32 m0, s50, 36864
R 44,7168
v_cvt_pk_bf16_f32 v8, v8, v9
mav 0,24,28
R 16,8192
v_pk_max_i16 v56, v8, 0
mav 4,28,28
s_waitcnt lgkmcnt(3)
v_fma_f32 v68, v67, v50, v92
v_fma_f32 v10, v10, v66, v68
mav 0,32,32
D 85,0
mav 4,36,32
R 20,9216
v_fma_f32 v69, v67, v51, v93
mav 0,40,36
R 24,10240
v_fma_f32 v11, v11, v66, v69
mav 4,44,36
s_waitcnt lgkmcnt(3)
v_mul_f32_e64 v10, v10, |v10|
v_mul_f32_e64 v11, v11, |v11|
mav 0,48,40
D 85,1024
mav 4,52,40
R 28,11264
v_cvt_pk_bf16_f32 v10, v10, v11
mav 0,56,44
R 32,12288
v_pk_max_i16 v57, v10, 0
mav 4,60,44
s_waitcnt lgkmcnt(3)
v_fma_f32 v68, v67, v52, v94
v_fma_f32 v12, v12, v66, v68
mav 0,64,16
D 85,2048
mav 4,68,16
R 36,13312
v_fma_f32 v69, v67, v53, v95
mav 0,72,20
R 40,14336
v_fma_f32 v13, v13, v66, v69
mav 4,76,20
s_waitcnt lgkmcnt(3)
v_mul_f32_e64 v12, v12, |v12|
v_mul_f32_e64 v13, v13, |v13|
mav 0,80,24
D 85,3072
mav 4,84,24
R 44,15360
v_cvt_pk_bf16_f32 v12, v12, v13
mav 0,88,28
R 16,16384
v_pk_max_i16 v58, v12, 0
mav 4,92,28
s_waitcnt lgkmcnt(3)
v_fma_f32 v68, v67, v54, v96
v_fma_f32 v14, v14, v66, v68
mav 0,96,32
R 20,17408
v_fma_f32 v69, v67, v55, v97
mav 4,100,32
v_fma_f32 v15, v15, v66, v69
v_mul_f32_e64 v14, v14, |v14|
mav 0,104,36
R 24,18432
v_mul_f32_e64 v15, v15, |v15|
mav 4,108,36
s_waitcnt lgkmcnt(3)
v_cvt_pk_bf16_f32 v14, v14, v15
v_pk_max_i16 v59, v14, 0
mav 0,112,40
buffer_store_dwordx4 v[56:59], v83, s[36:39], s49 offen sc1
R 28,19456
mav 4,116,40
mav 0,120,44
R 32,20480
mav 4,124,44
s_waitcnt lgkmcnt(3)
mav 0,128,16
R 36,21504
mav 4,132,16
mav 0,136,20
R 40,22528
mav 4,140,20
s_waitcnt lgkmcnt(3)
mav 0,144,24
R 44,23552
mav 4,148,24
mav 0,152,28
R 16,24576
mav 4,156,28
s_waitcnt lgkmcnt(3)
mav 0,160,32
R 20,25600
mav 4,164,32
mav 0,168,36
R 24,26624
mav 4,172,36
s_waitcnt lgkmcnt(3)
mav 0,176,40
R 28,27648
mav 4,180,40
mav 0,184,44
R 32,28672
mav 4,188,44
s_waitcnt lgkmcnt(3)
mav 0,192,16
R 36,29696
mav 4,196,16
mav 0,200,20
R 40,30720
mav 4,204,20
s_waitcnt lgkmcnt(3)
LsE8:
s_cbranch_execz LwE
mav 0,208,24
R 44,31744
mav 4,212,24
mav 0,216,28
mav 4,220,28
s_waitcnt lgkmcnt(2)
mav 0,224,32
v_add_u32 v64, 1, v64
mav 4,228,32
mav 0,232,36
mav 4,236,36
s_waitcnt lgkmcnt(0)
mav 0,240,40
mav 4,244,40
mav 0,248,44
mav 4,252,44
s_xor_b32 s50, s50, 0x10000
s_waitcnt vmcnt(1)
s_add_u32 s49, s49, 313344
s_barrier
s_add_u32 m0, s50, 0
R 16,32768
D 84,0
ds_read_b64 v[66:67], v88 offset:0
R 20,33792
R 24,34816
D 84,1024
R 28,35840
s_waitcnt vmcnt(2)
buffer_store_dword v64, v81, s[36:39], s52 offen sc1
R 32,36864
D 84,2048
s_waitcnt lgkmcnt(3)
v_mfma_f32_16x16x32_bf16 v[8:11], a[0:3], v[16:19], 0
R 36,37888
v_fma_f32 v68, v67, v48, v90
v_mfma_f32_16x16x32_bf16 v[12:15], a[4:7], v[16:19], 0
v_fma_f32 v0, v0, v66, v68
v_fma_f32 v69, v67, v49, v91
mav 8,8,20
R 40,38912
v_fma_f32 v1, v1, v66, v69
mav 12,12,20
s_waitcnt lgkmcnt(3)
v_mul_f32_e64 v0, v0, |v0|
v_mul_f32_e64 v1, v1, |v1|
mav 8,16,24
D 84,3072
mav 12,20,24
s_add_u32 m0, s50, 4096
R 44,39936
v_cvt_pk_bf16_f32 v0, v0, v1
mav 8,24,28
R 16,40960
v_pk_max_i16 v56, v0, 0
mav 12,28,28
s_waitcnt lgkmcnt(3)
v_fma_f32 v68, v67, v50, v92
v_fma_f32 v2, v2, v66, v68
mav 8,32,32
D 85,0
mav 12,36,32
R 20,41984
v_fma_f32 v69, v67, v51, v93
mav 8,40,36
R 24,43008
v_fma_f32 v3, v3, v66, v69
mav 12,44,36
s_waitcnt lgkmcnt(3)
v_mul_f32_e64 v2, v2, |v2|
v_mul_f32_e64 v3, v3, |v3|
mav 8,48,40
D 85,1024
mav 12,52,40
R 28,44032
v_cvt_pk_bf16_f32 v2, v2, v3
mav 8,56,44
R 32,45056
v_pk_max_i16 v57, v2, 0
mav 12,60,44
s_waitcnt lgkmcnt(3)
v_fma_f32 v68, v67, v52, v94
v_fma_f32 v4, v4, v66, v68
mav 8,64,16
D 85,2048
mav 12,68,16
R 36,46080
v_fma_f32 v69, v67, v53, v95
mav 8,72,20
R 40,47104
v_fma_f32 v5, v5, v66, v69
mav 12,76,20
s_waitcnt lgkmcnt(3)
v_mul_f32_e64 v4, v4, |v4|
v_mul_f32_e64 v5, v5, |v5|
mav 8,80,24
D 85,3072
mav 12,84,24
R 44,48128
v_cvt_pk_bf16_f32 v4, v4, v5
mav 8,88,28
R 16,49152
v_pk_max_i16 v58, v4, 0
mav 12,92,28
s_waitcnt lgkmcnt(3)
v_fma_f32 v68, v67, v54, v96
v_fma_f32 v6, v6, v66, v68
mav 8,96,32
R 20,50176
v_fma_f32 v69, v67, v55, v97
mav 12,100,32
v_fma_f32 v7, v7, v66, v69
v_mul_f32_e64 v6, v6, |v6|
mav 8,104,36
R 24,51200
v_mul_f32_e64 v7, v7, |v7|
mav 12,108,36
s_waitcnt lgkmcnt(3)
v_cvt_pk_bf16_f32 v6, v6, v7
v_pk_max_i16 v59, v6, 0
mav 8,112,40
buffer_store_dwordx4 v[56:59], v83, s[36:39], s49 offen sc1
R 28,52224
mav 12,116,40
mav 8,120,44
R 32,53248
mav 12,124,44
s_waitcnt lgkmcnt(3)
mav 8,128,16
R 36,54272
mav 12,132,16
mav 8,136,20
R 40,55296
mav 12,140,20
s_waitcnt lgkmcnt(3)
mav 8,144,24
R 44,56320
mav 12,148,24
mav 8,152,28
R 16,57344
mav 12,156,28
s_waitcnt lgkmcnt(3)
mav 8,160,32
R 20,58368
mav 12,164,32
mav 8,168,36
R 24,59392
mav 12,172,36
s_waitcnt lgkmcnt(3)
mav 8,176,40
R 28,60416
mav 12,180,40
mav 8,184,44
R 32,61440
mav 12,188,44
s_waitcnt lgkmcnt(3)
mav 8,192,16
R 36,62464
mav 12,196,16
mav 8,200,20
R 40,63488
mav 12,204,20
s_waitcnt lgkmcnt(3)
mav 8,208,24
R 44,64512
mav 12,212,24
mav 8,216,28
mav 12,220,28
s_waitcnt lgkmcnt(2)
mav 8,224,32
v_add_u32 v64, 1, v64
mav 12,228,32
s_add_u32 s60, s60, 1
mav 8,232,36
s_cbranch_scc0 LE2_1
mav 12,236,36
s_waitcnt lgkmcnt(0)
mav 8,240,40
mav 12,244,40
mav 8,248,44
mav 12,252,44
v_xor_b32 v80, 0x10000, v80
s_waitcnt vmcnt(1)
s_add_u32 s49, s49, 313344
s_barrier
s_add_u32 m0, s50, 32768
R 16,0
D 84,0
ds_read_b64 v[66:67], v88 offset:128
R 20,1024
R 24,2048
D 84,1024
R 28,3072
s_waitcnt vmcnt(2)
buffer_store_dword v64, v81, s[36:39], s52 offen sc1
R 32,4096
D 84,2048
s_waitcnt lgkmcnt(3)
v_mfma_f32_16x16x32_bf16 v[0:3], a[0:3], v[16:19], 0
R 36,5120
v_fma_f32 v68, v67, v48, v90
v_mfma_f32_16x16x32_bf16 v[4:7], a[4:7], v[16:19], 0
v_fma_f32 v8, v8, v66, v68
v_fma_f32 v69, v67, v49, v91
mav 0,8,20
R 40,6144
v_fma_f32 v9, v9, v66, v69
mav 4,12,20
s_waitcnt lgkmcnt(3)
v_mul_f32_e64 v8, v8, |v8|
v_mul_f32_e64 v9, v9, |v9|
mav 0,16,24
D 84,3072
mav 4,20,24
s_add_u32 m0, s50, 36864
R 44,7168
v_cvt_pk_bf16_f32 v8, v8, v9
mav 0,24,28
R 16,8192
v_pk_max_i16 v56, v8, 0
mav 4,28,28
s_waitcnt lgkmcnt(3)
v_fma_f32 v68, v67, v50, v92
v_fma_f32 v10, v10, v66, v68
mav 0,32,32
D 85,0
mav 4,36,32
R 20,9216
v_fma_f32 v69, v67, v51, v93
mav 0,40,36
R 24,10240
v_fma_f32 v11, v11, v66, v69
mav 4,44,36
s_waitcnt lgkmcnt(3)
v_mul_f32_e64 v10, v10, |v10|
v_mul_f32_e64 v11, v11, |v11|
mav 0,48,40
D 85,1024
mav 4,52,40
R 28,11264
v_cvt_pk_bf16_f32 v10, v10, v11
mav 0,56,44
R 32,12288
v_pk_max_i16 v57, v10, 0
mav 4,60,44
s_waitcnt lgkmcnt(3)
v_fma_f32 v68, v67, v52, v94
v_fma_f32 v12, v12, v66, v68
mav 0,64,16
D 85,2048
mav 4,68,16
R 36,13312
v_fma_f32 v69, v67, v53, v95
mav 0,72,20
R 40,14336
v_fma_f32 v13, v13, v66, v69
mav 4,76,20
s_waitcnt lgkmcnt(3)
v_mul_f32_e64 v12, v12, |v12|
v_mul_f32_e64 v13, v13, |v13|
mav 0,80,24
D 85,3072
mav 4,84,24
R 44,15360
v_cvt_pk_bf16_f32 v12, v12, v13
mav 0,88,28
R 16,16384
v_pk_max_i16 v58, v12, 0
mav 4,92,28
s_waitcnt lgkmcnt(3)
v_fma_f32 v68, v67, v54, v96
v_fma_f32 v14, v14, v66, v68
mav 0,96,32
R 20,17408
v_fma_f32 v69, v67, v55, v97
mav 4,100,32
v_fma_f32 v15, v15, v66, v69
v_mul_f32_e64 v14, v14, |v14|
mav 0,104,36
R 24,18432
v_mul_f32_e64 v15, v15, |v15|
mav 4,108,36
s_waitcnt lgkmcnt(3)
v_cvt_pk_bf16_f32 v14, v14, v15
v_pk_max_i16 v59, v14, 0
mav 0,112,40
buffer_store_dwordx4 v[56:59], v83, s[36:39], s49 offen sc1
R 28,19456
mav 4,116,40
mav 0,120,44
R 32,20480
mav 4,124,44
s_waitcnt lgkmcnt(3)
mav 0,128,16
R 36,21504
mav 4,132,16
mav 0,136,20
R 40,22528
mav 4,140,20
s_waitcnt lgkmcnt(3)
mav 0,144,24
R 44,23552
mav 4,148,24
mav 0,152,28
R 16,24576
mav 4,156,28
s_waitcnt lgkmcnt(3)
mav 0,160,32
R 20,25600
mav 4,164,32
mav 0,168,36
R 24,26624
mav 4,172,36
s_waitcnt lgkmcnt(3)
mav 0,176,40
R 28,27648
mav 4,180,40
mav 0,184,44
R 32,28672
mav 4,188,44
s_waitcnt lgkmcnt(3)
mav 0,192,16
R 36,29696
mav 4,196,16
mav 0,200,20
R 40,30720
mav 4,204,20
s_waitcnt lgkmcnt(3)
mav 0,208,24
R 44,31744
mav 4,212,24
mav 0,216,28
mav 4,220,28
s_waitcnt lgkmcnt(2)
mav 0,224,32
v_add_u32 v64, 1, v64
mav 4,228,32
mav 0,232,36
mav 4,236,36
s_waitcnt lgkmcnt(0)
mav 0,240,40
mav 4,244,40
mav 0,248,44
mav 4,252,44
s_waitcnt vmcnt(1)
s_add_u32 s49, s49, 313344
s_barrier
R 16,32768
ds_read_b64 v[66:67], v88 offset:0
R 20,33792
R 24,34816
R 28,35840
s_waitcnt vmcnt(0)
buffer_store_dword v64, v81, s[36:39], s52 offen sc1
R 32,36864
s_waitcnt lgkmcnt(3)
v_mfma_f32_16x16x32_bf16 v[8:11], a[0:3], v[16:19], 0
R 36,37888
v_fma_f32 v68, v67, v48, v90
v_mfma_f32_16x16x32_bf16 v[12:15], a[4:7], v[16:19], 0
v_fma_f32 v0, v0, v66, v68
v_fma_f32 v69, v67, v49, v91
mav 8,8,20
R 40,38912
v_fma_f32 v1, v1, v66, v69
mav 12,12,20
s_waitcnt lgkmcnt(3)
v_mul_f32_e64 v0, v0, |v0|
v_mul_f32_e64 v1, v1, |v1|
mav 8,16,24
R 44,39936
v_cvt_pk_bf16_f32 v0, v0, v1
mav 12,20,24
v_pk_max_i16 v56, v0, 0
v_fma_f32 v68, v67, v50, v92
mav 8,24,28
R 16,40960
v_fma_f32 v2, v2, v66, v68
mav 12,28,28
s_waitcnt lgkmcnt(3)
v_fma_f32 v69, v67, v51, v93
v_fma_f32 v3, v3, v66, v69
mav 8,32,32
R 20,41984
v_mul_f32_e64 v2, v2, |v2|
mav 12,36,32
v_mul_f32_e64 v3, v3, |v3|
v_cvt_pk_bf16_f32 v2, v2, v3
mav 8,40,36
R 24,43008
v_pk_max_i16 v57, v2, 0
mav 12,44,36
s_waitcnt lgkmcnt(3)
v_fma_f32 v68, v67, v52, v94
v_fma_f32 v4, v4, v66, v68
mav 8,48,40
R 28,44032
v_fma_f32 v69, v67, v53, v95
mav 12,52,40
v_fma_f32 v5, v5, v66, v69
v_mul_f32_e64 v4, v4, |v4|
mav 8,56,44
R 32,45056
v_mul_f32_e64 v5, v5, |v5|
mav 12,60,44
s_waitcnt lgkmcnt(3)
v_cvt_pk_bf16_f32 v4, v4, v5
v_pk_max_i16 v58, v4, 0
mav 8,64,16
R 36,46080
v_fma_f32 v68, v67, v54, v96
mav 12,68,16
v_fma_f32 v6, v6, v66, v68
v_fma_f32 v69, v67, v55, v97
mav 8,72,20
R 40,47104
v_fma_f32 v7, v7, v66, v69
mav 12,76,20
s_waitcnt lgkmcnt(3)
v_mul_f32_e64 v6, v6, |v6|
v_mul_f32_e64 v7, v7, |v7|
mav 8,80,24
R 44,48128
v_cvt_pk_bf16_f32 v6, v6, v7
mav 12,84,24
v_pk_max_i16 v59, v6, 0
mav 8,88,28
R 16,49152
mav 12,92,28
s_waitcnt lgkmcnt(3)
mav 8,96,32
buffer_store_dwordx4 v[56:59], v83, s[36:39], s49 offen sc1
R 20,50176
mav 12,100,32
mav 8,104,36
R 24,51200
mav 12,108,36
s_waitcnt lgkmcnt(3)
mav 8,112,40
R 28,52224
mav 12,116,40
mav 8,120,44
R 32,53248
mav 12,124,44
s_waitcnt lgkmcnt(3)
mav 8,128,16
R 36,54272
mav 12,132,16
mav 8,136,20
R 40,55296
mav 12,140,20
s_waitcnt lgkmcnt(3)
mav 8,144,24
R 44,56320
mav 12,148,24
mav 8,152,28
R 16,57344
mav 12,156,28
s_waitcnt lgkmcnt(3)
mav 8,160,32
R 20,58368
mav 12,164,32
mav 8,168,36
R 24,59392
mav 12,172,36
s_waitcnt lgkmcnt(3)
mav 8,176,40
R 28,60416
mav 12,180,40
mav 8,184,44
R 32,61440
mav 12,188,44
s_waitcnt lgkmcnt(3)
mav 8,192,16
R 36,62464
mav 12,196,16
mav 8,200,20
R 40,63488
mav 12,204,20
s_waitcnt lgkmcnt(3)
mav 8,208,24
R 44,64512
mav 12,212,24
mav 8,216,28
mav 12,220,28
s_waitcnt lgkmcnt(2)
mav 8,224,32
v_add_u32 v64, 1, v64
mav 12,228,32
mav 8,232,36
mav 12,236,36
s_waitcnt lgkmcnt(0)
mav 8,240,40
mav 12,244,40
mav 8,248,44
mav 12,252,44
s_add_u32 s49, s49, 313344
s_barrier
ds_read_b64 v[66:67], v88 offset:128
s_waitcnt vmcnt(0)
buffer_store_dword v64, v81, s[36:39], s52 offen sc1
s_waitcnt lgkmcnt(0)
v_fma_f32 v68, v67, v48, v90
v_fma_f32 v8, v8, v66, v68
v_fma_f32 v69, v67, v49, v91
v_fma_f32 v9, v9, v66, v69
v_mul_f32_e64 v8, v8, |v8|
v_mul_f32_e64 v9, v9, |v9|
v_cvt_pk_bf16_f32 v8, v8, v9
v_pk_max_i16 v56, v8, 0
v_fma_f32 v68, v67, v50, v92
v_fma_f32 v10, v10, v66, v68
v_fma_f32 v69, v67, v51, v93
v_fma_f32 v11, v11, v66, v69
v_mul_f32_e64 v10, v10, |v10|
v_mul_f32_e64 v11, v11, |v11|
v_cvt_pk_bf16_f32 v10, v10, v11
v_pk_max_i16 v57, v10, 0
v_fma_f32 v68, v67, v52, v94
v_fma_f32 v12, v12, v66, v68
v_fma_f32 v69, v67, v53, v95
v_fma_f32 v13, v13, v66, v69
v_mul_f32_e64 v12, v12, |v12|
v_mul_f32_e64 v13, v13, |v13|
v_cvt_pk_bf16_f32 v12, v12, v13
v_pk_max_i16 v58, v12, 0
v_fma_f32 v68, v67, v54, v96
v_fma_f32 v14, v14, v66, v68
v_fma_f32 v69, v67, v55, v97
v_fma_f32 v15, v15, v66, v69
v_mul_f32_e64 v14, v14, |v14|
v_mul_f32_e64 v15, v15, |v15|
v_cvt_pk_bf16_f32 v14, v14, v15
v_pk_max_i16 v59, v14, 0
buffer_store_dwordx4 v[56:59], v83, s[36:39], s49 offen sc1
v_add_u32 v64, 37, v64
s_waitcnt vmcnt(0)
buffer_store_dword v64, v81, s[36:39], s52 offen sc1
s_endpgm
LvE3:
LsE9:
s_cbranch_execz LwE
s_add_u32 s49, s49, 313344
s_barrier
s_add_u32 m0, s50, 0
s_nop 0
D 84,0
D 84,1024
D 84,2048
D 84,3072
s_add_u32 m0, s50, 4096
s_nop 0
LsE10:
s_cbranch_execz LwE
D 85,0
D 85,1024
D 85,2048
D 85,3072
s_waitcnt vmcnt(0)
s_add_u32 s49, s49, 313344
s_barrier
s_add_u32 m0, s50, 32768
R 16,0
D 84,0
R 20,1024
R 24,2048
D 84,1024
R 28,3072
R 32,4096
D 84,2048
s_waitcnt lgkmcnt(3)
v_mfma_f32_16x16x32_bf16 v[0:3], a[0:3], v[16:19], 0
R 36,5120
v_mfma_f32_16x16x32_bf16 v[4:7], a[4:7], v[16:19], 0
mav 0,8,20
R 40,6144
mav 4,12,20
s_waitcnt lgkmcnt(3)
mav 0,16,24
R 44,7168
mav 4,20,24
D 84,3072
mav 0,24,28
s_add_u32 m0, s50, 36864
R 16,8192
mav 4,28,28
s_waitcnt lgkmcnt(3)
mav 0,32,32
R 20,9216
mav 4,36,32
D 85,0
mav 0,40,36
R 24,10240
mav 4,44,36
s_waitcnt lgkmcnt(3)
mav 0,48,40
R 28,11264
mav 4,52,40
D 85,1024
mav 0,56,44
R 32,12288
mav 4,60,44
s_waitcnt lgkmcnt(3)
mav 0,64,16
R 36,13312
mav 4,68,16
D 85,2048
mav 0,72,20
R 40,14336
mav 4,76,20
s_waitcnt lgkmcnt(3)
mav 0,80,24
R 44,15360
mav 4,84,24
D 85,3072
mav 0,88,28
R 16,16384
mav 4,92,28
s_waitcnt lgkmcnt(3)
mav 0,96,32
R 20,17408
mav 4,100,32
mav 0,104,36
R 24,18432
mav 4,108,36
s_waitcnt lgkmcnt(3)
mav 0,112,40
R 28,19456
mav 4,116,40
mav 0,120,44
R 32,20480
mav 4,124,44
s_waitcnt lgkmcnt(3)
mav 0,128,16
R 36,21504
mav 4,132,16
mav 0,136,20
R 40,22528
mav 4,140,20
s_waitcnt lgkmcnt(3)
mav 0,144,24
R 44,23552
mav 4,148,24
mav 0,152,28
R 16,24576
mav 4,156,28
s_waitcnt lgkmcnt(3)
mav 0,160,32
R 20,25600
mav 4,164,32
mav 0,168,36
R 24,26624
mav 4,172,36
s_waitcnt lgkmcnt(3)
mav 0,176,40
R 28,27648
mav 4,180,40
mav 0,184,44
R 32,28672
mav 4,188,44
s_waitcnt lgkmcnt(3)
mav 0,192,16
R 36,29696
mav 4,196,16
mav 0,200,20
R 40,30720
mav 4,204,20
s_waitcnt lgkmcnt(3)
mav 0,208,24
R 44,31744
mav 4,212,24
mav 0,216,28
mav 4,220,28
s_waitcnt lgkmcnt(2)
mav 0,224,32
mav 4,228,32
mav 0,232,36
mav 4,236,36
s_waitcnt lgkmcnt(0)
mav 0,240,40
mav 4,244,40
mav 0,248,44
mav 4,252,44
s_xor_b32 s50, s50, 0x10000
s_waitcnt vmcnt(0)
s_add_u32 s49, s49, 313344
s_barrier
s_add_u32 m0, s50, 0
R 16,32768
D 84,0
ds_read_b64 v[66:67], v88 offset:0
R 20,33792
R 24,34816
D 84,1024
R 28,35840
R 32,36864
D 84,2048
s_waitcnt lgkmcnt(3)
v_mfma_f32_16x16x32_bf16 v[8:11], a[0:3], v[16:19], 0
R 36,37888
v_fma_f32 v68, v67, v48, v90
v_mfma_f32_16x16x32_bf16 v[12:15], a[4:7], v[16:19], 0
v_fma_f32 v0, v0, v66, v68
v_fma_f32 v69, v67, v49, v91
mav 8,8,20
R 40,38912
v_fma_f32 v1, v1, v66, v69
mav 12,12,20
s_waitcnt lgkmcnt(3)
v_mul_f32_e64 v0, v0, |v0|
v_mul_f32_e64 v1, v1, |v1|
mav 8,16,24
R 44,39936
v_cvt_pk_bf16_f32 v0, v0, v1
mav 12,20,24
D 84,3072
mav 8,24,28
s_add_u32 m0, s50, 4096
R 16,40960
v_pk_max_i16 v56, v0, 0
mav 12,28,28
s_waitcnt lgkmcnt(3)
v_fma_f32 v68, v67, v50, v92
v_fma_f32 v2, v2, v66, v68
mav 8,32,32
R 20,41984
v_fma_f32 v69, v67, v51, v93
mav 12,36,32
D 85,0
mav 8,40,36
R 24,43008
v_fma_f32 v3, v3, v66, v69
mav 12,44,36
s_waitcnt lgkmcnt(3)
v_mul_f32_e64 v2, v2, |v2|
v_mul_f32_e64 v3, v3, |v3|
mav 8,48,40
R 28,44032
v_cvt_pk_bf16_f32 v2, v2, v3
mav 12,52,40
D 85,1024
mav 8,56,44
R 32,45056
v_pk_max_i16 v57, v2, 0
mav 12,60,44
s_waitcnt lgkmcnt(3)
v_fma_f32 v68, v67, v52, v94
v_fma_f32 v4, v4, v66, v68
mav 8,64,16
R 36,46080
v_fma_f32 v69, v67, v53, v95
mav 12,68,16
D 85,2048
mav 8,72,20
R 40,47104
v_fma_f32 v5, v5, v66, v69
mav 12,76,20
s_waitcnt lgkmcnt(3)
v_mul_f32_e64 v4, v4, |v4|
v_mul_f32_e64 v5, v5, |v5|
mav 8,80,24
R 44,48128
v_cvt_pk_bf16_f32 v4, v4, v5
mav 12,84,24
D 85,3072
mav 8,88,28
R 16,49152
v_pk_max_i16 v58, v4, 0
mav 12,92,28
s_waitcnt lgkmcnt(3)
v_fma_f32 v68, v67, v54, v96
v_fma_f32 v6, v6, v66, v68
mav 8,96,32
R 20,50176
v_fma_f32 v69, v67, v55, v97
mav 12,100,32
v_fma_f32 v7, v7, v66, v69
v_mul_f32_e64 v6, v6, |v6|
mav 8,104,36
R 24,51200
v_mul_f32_e64 v7, v7, |v7|
mav 12,108,36
s_waitcnt lgkmcnt(3)
v_cvt_pk_bf16_f32 v6, v6, v7
v_pk_max_i16 v59, v6, 0
mav 8,112,40
R 28,52224
mav 12,116,40
buffer_store_dwordx4 v[56:59], v83, s[36:39], s49 offen sc1
mav 8,120,44
R 32,53248
mav 12,124,44
s_waitcnt lgkmcnt(3)
mav 8,128,16
R 36,54272
mav 12,132,16
mav 8,136,20
R 40,55296
mav 12,140,20
s_waitcnt lgkmcnt(3)
mav 8,144,24
R 44,56320
mav 12,148,24
mav 8,152,28
R 16,57344
mav 12,156,28
s_waitcnt lgkmcnt(3)
mav 8,160,32
R 20,58368
mav 12,164,32
mav 8,168,36
R 24,59392
mav 12,172,36
s_waitcnt lgkmcnt(3)
mav 8,176,40
R 28,60416
mav 12,180,40
mav 8,184,44
R 32,61440
mav 12,188,44
s_waitcnt lgkmcnt(3)
mav 8,192,16
R 36,62464
mav 12,196,16
mav 8,200,20
R 40,63488
mav 12,204,20
s_waitcnt lgkmcnt(3)
mav 8,208,24
R 44,64512
mav 12,212,24
mav 8,216,28
mav 12,220,28
s_waitcnt lgkmcnt(2)
mav 8,224,32
v_add_u32 v64, 1, v64
mav 12,228,32
mav 8,232,36
s_mov_b32 s60, -30
.p2align 6
LE3_1:
mav 12,236,36
s_waitcnt lgkmcnt(0)
mav 8,240,40
mav 12,244,40
mav 8,248,44
mav 12,252,44
v_xor_b32 v80, 0x10000, v80
s_waitcnt vmcnt(1)
s_add_u32 s49, s49, 313344
s_barrier
s_add_u32 m0, s50, 32768
R 16,0
D 84,0
ds_read_b64 v[66:67], v88 offset:128
R 20,1024
R 24,2048
D 84,1024
R 28,3072
s_waitcnt vmcnt(2)
buffer_store_dword v64, v81, s[36:39], s52 offen sc1
R 32,4096
D 84,2048
s_waitcnt lgkmcnt(3)
v_mfma_f32_16x16x32_bf16 v[0:3], a[0:3], v[16:19], 0
R 36,5120
v_fma_f32 v68, v67, v48, v90
v_mfma_f32_16x16x32_bf16 v[4:7], a[4:7], v[16:19], 0
v_fma_f32 v8, v8, v66, v68
v_fma_f32 v69, v67, v49, v91
mav 0,8,20
R 40,6144
v_fma_f32 v9, v9, v66, v69
mav 4,12,20
s_waitcnt lgkmcnt(3)
v_mul_f32_e64 v8, v8, |v8|
v_mul_f32_e64 v9, v9, |v9|
mav 0,16,24
R 44,7168
v_cvt_pk_bf16_f32 v8, v8, v9
mav 4,20,24
D 84,3072
mav 0,24,28
s_add_u32 m0, s50, 36864
R 16,8192
v_pk_max_i16 v56, v8, 0
mav 4,28,28
s_waitcnt lgkmcnt(3)
v_fma_f32 v68, v67, v50, v92
v_fma_f32 v10, v10, v66, v68
mav 0,32,32
R 20,9216
v_fma_f32 v69, v67, v51, v93
mav 4,36,32
D 85,0
mav 0,40,36
R 24,10240
v_fma_f32 v11, v11, v66, v69
mav 4,44,36
s_waitcnt lgkmcnt(3)
v_mul_f32_e64 v10, v10, |v10|
v_mul_f32_e64 v11, v11, |v11|
mav 0,48,40
R 28,11264
v_cvt_pk_bf16_f32 v10, v10, v11
mav 4,52,40
D 85,1024
mav 0,56,44
R 32,12288
v_pk_max_i16 v57, v10, 0
mav 4,60,44
s_waitcnt lgkmcnt(3)
v_fma_f32 v68, v67, v52, v94
v_fma_f32 v12, v12, v66, v68
mav 0,64,16
R 36,13312
v_fma_f32 v69, v67, v53, v95
mav 4,68,16
D 85,2048
mav 0,72,20
R 40,14336
v_fma_f32 v13, v13, v66, v69
mav 4,76,20
s_waitcnt lgkmcnt(3)
v_mul_f32_e64 v12, v12, |v12|
v_mul_f32_e64 v13, v13, |v13|
mav 0,80,24
R 44,15360
v_cvt_pk_bf16_f32 v12, v12, v13
mav 4,84,24
D 85,3072
mav 0,88,28
R 16,16384
v_pk_max_i16 v58, v12, 0
mav 4,92,28
s_waitcnt lgkmcnt(3)
v_fma_f32 v68, v67, v54, v96
v_fma_f32 v14, v14, v66, v68
mav 0,96,32
R 20,17408
v_fma_f32 v69, v67, v55, v97
mav 4,100,32
v_fma_f32 v15, v15, v66, v69
v_mul_f32_e64 v14, v14, |v14|
mav 0,104,36
R 24,18432
v_mul_f32_e64 v15, v15, |v15|
mav 4,108,36
s_waitcnt lgkmcnt(3)
v_cvt_pk_bf16_f32 v14, v14, v15
v_pk_max_i16 v59, v14, 0
mav 0,112,40
R 28,19456
mav 4,116,40
buffer_store_dwordx4 v[56:59], v83, s[36:39], s49 offen sc1
mav 0,120,44
R 32,20480
mav 4,124,44
s_waitcnt lgkmcnt(3)
mav 0,128,16
R 36,21504
mav 4,132,16
mav 0,136,20
R 40,22528
mav 4,140,20
s_waitcnt lgkmcnt(3)
mav 0,144,24
R 44,23552
mav 4,148,24
mav 0,152,28
R 16,24576
mav 4,156,28
s_waitcnt lgkmcnt(3)
mav 0,160,32
R 20,25600
mav 4,164,32
mav 0,168,36
R 24,26624
mav 4,172,36
s_waitcnt lgkmcnt(3)
mav 0,176,40
R 28,27648
mav 4,180,40
mav 0,184,44
R 32,28672
mav 4,188,44
s_waitcnt lgkmcnt(3)
mav 0,192,16
R 36,29696
mav 4,196,16
mav 0,200,20
R 40,30720
mav 4,204,20
s_waitcnt lgkmcnt(3)
mav 0,208,24
R 44,31744
mav 4,212,24
mav 0,216,28
mav 4,220,28
s_waitcnt lgkmcnt(2)
mav 0,224,32
v_add_u32 v64, 1, v64
mav 4,228,32
mav 0,232,36
mav 4,236,36
s_waitcnt lgkmcnt(0)
mav 0,240,40
mav 4,244,40
mav 0,248,44
mav 4,252,44
s_xor_b32 s50, s50, 0x10000
s_waitcnt vmcnt(1)
s_add_u32 s49, s49, 313344
s_barrier
s_add_u32 m0, s50, 0
R 16,32768
D 84,0
ds_read_b64 v[66:67], v88 offset:0
R 20,33792
R 24,34816
D 84,1024
R 28,35840
s_waitcnt vmcnt(2)
buffer_store_dword v64, v81, s[36:39], s52 offen sc1
R 32,36864
D 84,2048
s_waitcnt lgkmcnt(3)
v_mfma_f32_16x16x32_bf16 v[8:11], a[0:3], v[16:19], 0
R 36,37888
v_fma_f32 v68, v67, v48, v90
v_mfma_f32_16x16x32_bf16 v[12:15], a[4:7], v[16:19], 0
v_fma_f32 v0, v0, v66, v68
v_fma_f32 v69, v67, v49, v91
mav 8,8,20
R 40,38912
v_fma_f32 v1, v1, v66, v69
mav 12,12,20
s_waitcnt lgkmcnt(3)
v_mul_f32_e64 v0, v0, |v0|
v_mul_f32_e64 v1, v1, |v1|
mav 8,16,24
R 44,39936
v_cvt_pk_bf16_f32 v0, v0, v1
mav 12,20,24
D 84,3072
mav 8,24,28
s_add_u32 m0, s50, 4096
R 16,40960
v_pk_max_i16 v56, v0, 0
mav 12,28,28
s_waitcnt lgkmcnt(3)
v_fma_f32 v68, v67, v50, v92
v_fma_f32 v2, v2, v66, v68
mav 8,32,32
R 20,41984
v_fma_f32 v69, v67, v51, v93
mav 12,36,32
D 85,0
mav 8,40,36
R 24,43008
v_fma_f32 v3, v3, v66, v69
mav 12,44,36
s_waitcnt lgkmcnt(3)
v_mul_f32_e64 v2, v2, |v2|
v_mul_f32_e64 v3, v3, |v3|
mav 8,48,40
R 28,44032
v_cvt_pk_bf16_f32 v2, v2, v3
mav 12,52,40
D 85,1024
mav 8,56,44
R 32,45056
v_pk_max_i16 v57, v2, 0
mav 12,60,44
s_waitcnt lgkmcnt(3)
v_fma_f32 v68, v67, v52, v94
v_fma_f32 v4, v4, v66, v68
mav 8,64,16
R 36,46080
v_fma_f32 v69, v67, v53, v95
mav 12,68,16
D 85,2048
mav 8,72,20
R 40,47104
v_fma_f32 v5, v5, v66, v69
mav 12,76,20
s_waitcnt lgkmcnt(3)
v_mul_f32_e64 v4, v4, |v4|
v_mul_f32_e64 v5, v5, |v5|
mav 8,80,24
R 44,48128
v_cvt_pk_bf16_f32 v4, v4, v5
mav 12,84,24
D 85,3072
mav 8,88,28
R 16,49152
v_pk_max_i16 v58, v4, 0
LsE11:
s_cbranch_execz LwE
mav 12,92,28
s_waitcnt lgkmcnt(3)
v_fma_f32 v68, v67, v54, v96
v_fma_f32 v6, v6, v66, v68
mav 8,96,32
R 20,50176
v_fma_f32 v69, v67, v55, v97
mav 12,100,32
v_fma_f32 v7, v7, v66, v69
v_mul_f32_e64 v6, v6, |v6|
mav 8,104,36
R 24,51200
v_mul_f32_e64 v7, v7, |v7|
mav 12,108,36
s_waitcnt lgkmcnt(3)
v_cvt_pk_bf16_f32 v6, v6, v7
v_pk_max_i16 v59, v6, 0
mav 8,112,40
R 28,52224
mav 12,116,40
buffer_store_dwordx4 v[56:59], v83, s[36:39], s49 offen sc1
mav 8,120,44
R 32,53248
mav 12,124,44
s_waitcnt lgkmcnt(3)
mav 8,128,16
R 36,54272
mav 12,132,16
mav 8,136,20
R 40,55296
mav 12,140,20
s_waitcnt lgkmcnt(3)
mav 8,144,24
R 44,56320
mav 12,148,24
mav 8,152,28
R 16,57344
mav 12,156,28
s_waitcnt lgkmcnt(3)
mav 8,160,32
R 20,58368
mav 12,164,32
mav 8,168,36
R 24,59392
mav 12,172,36
s_waitcnt lgkmcnt(3)
mav 8,176,40
R 28,60416
mav 12,180,40
mav 8,184,44
R 32,61440
mav 12,188,44
s_waitcnt lgkmcnt(3)
mav 8,192,16
R 36,62464
mav 12,196,16
mav 8,200,20
R 40,63488
mav 12,204,20
s_waitcnt lgkmcnt(3)
mav 8,208,24
R 44,64512
mav 12,212,24
mav 8,216,28
mav 12,220,28
s_waitcnt lgkmcnt(2)
mav 8,224,32
v_add_u32 v64, 1, v64
mav 12,228,32
s_add_u32 s60, s60, 1
mav 8,232,36
s_cbranch_scc0 LE3_1
mav 12,236,36
s_waitcnt lgkmcnt(0)
mav 8,240,40
mav 12,244,40
mav 8,248,44
mav 12,252,44
v_xor_b32 v80, 0x10000, v80
s_waitcnt vmcnt(1)
s_add_u32 s49, s49, 313344
s_barrier
s_add_u32 m0, s50, 32768
R 16,0
D 84,0
ds_read_b64 v[66:67], v88 offset:128
R 20,1024
R 24,2048
D 84,1024
R 28,3072
s_waitcnt vmcnt(2)
buffer_store_dword v64, v81, s[36:39], s52 offen sc1
R 32,4096
D 84,2048
s_waitcnt lgkmcnt(3)
v_mfma_f32_16x16x32_bf16 v[0:3], a[0:3], v[16:19], 0
R 36,5120
v_fma_f32 v68, v67, v48, v90
v_mfma_f32_16x16x32_bf16 v[4:7], a[4:7], v[16:19], 0
v_fma_f32 v8, v8, v66, v68
v_fma_f32 v69, v67, v49, v91
mav 0,8,20
R 40,6144
v_fma_f32 v9, v9, v66, v69
mav 4,12,20
s_waitcnt lgkmcnt(3)
v_mul_f32_e64 v8, v8, |v8|
v_mul_f32_e64 v9, v9, |v9|
mav 0,16,24
R 44,7168
v_cvt_pk_bf16_f32 v8, v8, v9
mav 4,20,24
D 84,3072
mav 0,24,28
s_add_u32 m0, s50, 36864
R 16,8192
v_pk_max_i16 v56, v8, 0
mav 4,28,28
s_waitcnt lgkmcnt(3)
v_fma_f32 v68, v67, v50, v92
v_fma_f32 v10, v10, v66, v68
mav 0,32,32
R 20,9216
v_fma_f32 v69, v67, v51, v93
mav 4,36,32
D 85,0
mav 0,40,36
R 24,10240
v_fma_f32 v11, v11, v66, v69
mav 4,44,36
s_waitcnt lgkmcnt(3)
v_mul_f32_e64 v10, v10, |v10|
v_mul_f32_e64 v11, v11, |v11|
mav 0,48,40
R 28,11264
v_cvt_pk_bf16_f32 v10, v10, v11
mav 4,52,40
D 85,1024
mav 0,56,44
R 32,12288
v_pk_max_i16 v57, v10, 0
mav 4,60,44
s_waitcnt lgkmcnt(3)
v_fma_f32 v68, v67, v52, v94
v_fma_f32 v12, v12, v66, v68
mav 0,64,16
R 36,13312
v_fma_f32 v69, v67, v53, v95
mav 4,68,16
D 85,2048
mav 0,72,20
R 40,14336
v_fma_f32 v13, v13, v66, v69
mav 4,76,20
s_waitcnt lgkmcnt(3)
v_mul_f32_e64 v12, v12, |v12|
v_mul_f32_e64 v13, v13, |v13|
mav 0,80,24
R 44,15360
v_cvt_pk_bf16_f32 v12, v12, v13
mav 4,84,24
D 85,3072
mav 0,88,28
R 16,16384
v_pk_max_i16 v58, v12, 0
mav 4,92,28
s_waitcnt lgkmcnt(3)
v_fma_f32 v68, v67, v54, v96
v_fma_f32 v14, v14, v66, v68
mav 0,96,32
R 20,17408
v_fma_f32 v69, v67, v55, v97
mav 4,100,32
v_fma_f32 v15, v15, v66, v69
v_mul_f32_e64 v14, v14, |v14|
mav 0,104,36
R 24,18432
v_mul_f32_e64 v15, v15, |v15|
mav 4,108,36
s_waitcnt lgkmcnt(3)
v_cvt_pk_bf16_f32 v14, v14, v15
v_pk_max_i16 v59, v14, 0
mav 0,112,40
R 28,19456
mav 4,116,40
buffer_store_dwordx4 v[56:59], v83, s[36:39], s49 offen sc1
mav 0,120,44
R 32,20480
mav 4,124,44
s_waitcnt lgkmcnt(3)
mav 0,128,16
R 36,21504
mav 4,132,16
mav 0,136,20
R 40,22528
mav 4,140,20
s_waitcnt lgkmcnt(3)
mav 0,144,24
R 44,23552
mav 4,148,24
mav 0,152,28
R 16,24576
mav 4,156,28
s_waitcnt lgkmcnt(3)
mav 0,160,32
R 20,25600
mav 4,164,32
mav 0,168,36
R 24,26624
mav 4,172,36
s_waitcnt lgkmcnt(3)
mav 0,176,40
R 28,27648
mav 4,180,40
mav 0,184,44
R 32,28672
mav 4,188,44
s_waitcnt lgkmcnt(3)
mav 0,192,16
R 36,29696
mav 4,196,16
mav 0,200,20
R 40,30720
mav 4,204,20
s_waitcnt lgkmcnt(3)
mav 0,208,24
R 44,31744
mav 4,212,24
mav 0,216,28
mav 4,220,28
s_waitcnt lgkmcnt(2)
mav 0,224,32
v_add_u32 v64, 1, v64
mav 4,228,32
mav 0,232,36
mav 4,236,36
s_waitcnt lgkmcnt(0)
mav 0,240,40
mav 4,244,40
mav 0,248,44
mav 4,252,44
s_waitcnt vmcnt(1)
s_add_u32 s49, s49, 313344
s_barrier
R 16,32768
ds_read_b64 v[66:67], v88 offset:0
R 20,33792
R 24,34816
R 28,35840
s_waitcnt vmcnt(0)
buffer_store_dword v64, v81, s[36:39], s52 offen sc1
R 32,36864
s_waitcnt lgkmcnt(3)
v_mfma_f32_16x16x32_bf16 v[8:11], a[0:3], v[16:19], 0
R 36,37888
v_fma_f32 v68, v67, v48, v90
v_mfma_f32_16x16x32_bf16 v[12:15], a[4:7], v[16:19], 0
v_fma_f32 v0, v0, v66, v68
v_fma_f32 v69, v67, v49, v91
mav 8,8,20
R 40,38912
v_fma_f32 v1, v1, v66, v69
mav 12,12,20
s_waitcnt lgkmcnt(3)
v_mul_f32_e64 v0, v0, |v0|
v_mul_f32_e64 v1, v1, |v1|
mav 8,16,24
R 44,39936
v_cvt_pk_bf16_f32 v0, v0, v1
mav 12,20,24
v_pk_max_i16 v56, v0, 0
v_fma_f32 v68, v67, v50, v92
mav 8,24,28
R 16,40960
v_fma_f32 v2, v2, v66, v68
mav 12,28,28
s_waitcnt lgkmcnt(3)
v_fma_f32 v69, v67, v51, v93
v_fma_f32 v3, v3, v66, v69
mav 8,32,32
R 20,41984
v_mul_f32_e64 v2, v2, |v2|
mav 12,36,32
v_mul_f32_e64 v3, v3, |v3|
v_cvt_pk_bf16_f32 v2, v2, v3
mav 8,40,36
R 24,43008
v_pk_max_i16 v57, v2, 0
mav 12,44,36
s_waitcnt lgkmcnt(3)
v_fma_f32 v68, v67, v52, v94
v_fma_f32 v4, v4, v66, v68
mav 8,48,40
R 28,44032
v_fma_f32 v69, v67, v53, v95
mav 12,52,40
v_fma_f32 v5, v5, v66, v69
v_mul_f32_e64 v4, v4, |v4|
mav 8,56,44
R 32,45056
v_mul_f32_e64 v5, v5, |v5|
mav 12,60,44
s_waitcnt lgkmcnt(3)
v_cvt_pk_bf16_f32 v4, v4, v5
v_pk_max_i16 v58, v4, 0
mav 8,64,16
R 36,46080
v_fma_f32 v68, v67, v54, v96
mav 12,68,16
v_fma_f32 v6, v6, v66, v68
v_fma_f32 v69, v67, v55, v97
mav 8,72,20
)"
R"(
R 40,47104
v_fma_f32 v7, v7, v66, v69
mav 12,76,20
s_waitcnt lgkmcnt(3)
v_mul_f32_e64 v6, v6, |v6|
v_mul_f32_e64 v7, v7, |v7|
mav 8,80,24
R 44,48128
v_cvt_pk_bf16_f32 v6, v6, v7
mav 12,84,24
v_pk_max_i16 v59, v6, 0
mav 8,88,28
R 16,49152
mav 12,92,28
s_waitcnt lgkmcnt(3)
mav 8,96,32
R 20,50176
mav 12,100,32
buffer_store_dwordx4 v[56:59], v83, s[36:39], s49 offen sc1
mav 8,104,36
R 24,51200
mav 12,108,36
s_waitcnt lgkmcnt(3)
mav 8,112,40
R 28,52224
mav 12,116,40
mav 8,120,44
R 32,53248
mav 12,124,44
s_waitcnt lgkmcnt(3)
mav 8,128,16
R 36,54272
mav 12,132,16
mav 8,136,20
R 40,55296
mav 12,140,20
s_waitcnt lgkmcnt(3)
mav 8,144,24
R 44,56320
mav 12,148,24
mav 8,152,28
R 16,57344
mav 12,156,28
s_waitcnt lgkmcnt(3)
mav 8,160,32
R 20,58368
mav 12,164,32
mav 8,168,36
R 24,59392
mav 12,172,36
s_waitcnt lgkmcnt(3)
mav 8,176,40
R 28,60416
mav 12,180,40
mav 8,184,44
R 32,61440
mav 12,188,44
s_waitcnt lgkmcnt(3)
mav 8,192,16
R 36,62464
mav 12,196,16
mav 8,200,20
R 40,63488
mav 12,204,20
s_waitcnt lgkmcnt(3)
mav 8,208,24
R 44,64512
mav 12,212,24
mav 8,216,28
mav 12,220,28
s_waitcnt lgkmcnt(2)
mav 8,224,32
v_add_u32 v64, 1, v64
mav 12,228,32
mav 8,232,36
mav 12,236,36
s_waitcnt lgkmcnt(0)
mav 8,240,40
mav 12,244,40
mav 8,248,44
mav 12,252,44
s_add_u32 s49, s49, 313344
s_barrier
ds_read_b64 v[66:67], v88 offset:128
s_waitcnt vmcnt(0)
buffer_store_dword v64, v81, s[36:39], s52 offen sc1
s_waitcnt lgkmcnt(0)
v_fma_f32 v68, v67, v48, v90
v_fma_f32 v8, v8, v66, v68
v_fma_f32 v69, v67, v49, v91
v_fma_f32 v9, v9, v66, v69
v_mul_f32_e64 v8, v8, |v8|
v_mul_f32_e64 v9, v9, |v9|
v_cvt_pk_bf16_f32 v8, v8, v9
v_pk_max_i16 v56, v8, 0
v_fma_f32 v68, v67, v50, v92
v_fma_f32 v10, v10, v66, v68
v_fma_f32 v69, v67, v51, v93
v_fma_f32 v11, v11, v66, v69
v_mul_f32_e64 v10, v10, |v10|
v_mul_f32_e64 v11, v11, |v11|
v_cvt_pk_bf16_f32 v10, v10, v11
v_pk_max_i16 v57, v10, 0
v_fma_f32 v68, v67, v52, v94
v_fma_f32 v12, v12, v66, v68
v_fma_f32 v69, v67, v53, v95
v_fma_f32 v13, v13, v66, v69
v_mul_f32_e64 v12, v12, |v12|
v_mul_f32_e64 v13, v13, |v13|
v_cvt_pk_bf16_f32 v12, v12, v13
v_pk_max_i16 v58, v12, 0
v_fma_f32 v68, v67, v54, v96
v_fma_f32 v14, v14, v66, v68
v_fma_f32 v69, v67, v55, v97
v_fma_f32 v15, v15, v66, v69
v_mul_f32_e64 v14, v14, |v14|
v_mul_f32_e64 v15, v15, |v15|
v_cvt_pk_bf16_f32 v14, v14, v15
v_pk_max_i16 v59, v14, 0
buffer_store_dwordx4 v[56:59], v83, s[36:39], s49 offen sc1
v_add_u32 v64, 37, v64
s_waitcnt vmcnt(0)
buffer_store_dword v64, v81, s[36:39], s52 offen sc1
s_endpgm
Lr5:
s_cmp_eq_u32 s60, 5
s_cbranch_scc0 Lr6
s_waitcnt vmcnt(0)
v_accvgpr_read_b32 v73, a255
v_mov_b32 v72, 0x100
v_add_u32 v73, 0x9e3779b9, v73
buffer_store_dwordx2 v[72:73], v81, s[36:39], s52 offen sc1
v_add_u32 v86, v86, v72
s_sleep 32
s_sleep 32
s_sleep 32
s_sleep 32
s_sleep 32
s_sleep 32
E 0,65536
E 1,69632
E 2,73728
E 3,77824
s_add_u32 s51, s51, 65536
E 0,81920
E 1,86016
E 2,90112
E 3,94208
s_add_u32 s51, s51, 65536
E 0,98304
E 1,102400
E 2,106496
E 3,110592
s_add_u32 s51, s51, 65536
E 0,114688
E 1,118784
E 2,122880
E 3,126976
s_add_u32 s51, s51, 65536
s_mov_b64 exec, 0
s_cmp_eq_u32 s53, 0
s_cbranch_scc1 LsG0
s_cmp_eq_u32 s53, 1024
s_cbranch_scc1 LsG1
s_cmp_eq_u32 s53, 2048
s_cbranch_scc1 LsG2
s_branch LsG3
LwG:
s_mov_b64 exec, -1
s_waitcnt vmcnt(8)
s_barrier
v_add_u32 v4, 65536, v4
v_add_u32 v5, 65536, v5
v_add_u32 v6, 65536, v6
v_add_u32 v7, 65536, v7
Ta 0,4,0
Ta 4,5,0
Ta 8,6,0
Ta 12,7,0
Ta 16,4,16384
Ta 20,5,16384
Ta 24,6,16384
Ta 28,7,16384
E 0,131072
E 1,135168
E 2,139264
E 3,143360
s_add_u32 s51, s51, 65536
E 0,147456
E 1,151552
E 2,155648
E 3,159744
s_add_u32 s51, s51, 65536
s_cmp_ge_u32 s51, s57
s_cselect_b32 s60, 1572864, 0
s_sub_u32 s51, s51, s60
s_waitcnt lgkmcnt(0)
s_waitcnt vmcnt(8)
LsG0:
s_cbranch_execz LsG4
s_barrier
E 0,65536
Ta 32,4,32768
Ta 36,5,32768
E 1,69632
Ta 40,6,32768
Ta 44,7,32768
E 2,73728
Ta 48,4,49152
E 3,77824
s_add_u32 s51, s51, 65536
Ta 52,5,49152
Ta 56,6,49152
E 0,81920
Ta 60,7,49152
E 1,86016
E 2,90112
E 3,94208
s_add_u32 s51, s51, 65536
s_waitcnt lgkmcnt(0)
s_waitcnt vmcnt(8)
s_barrier
v_add_u32 v4, 65536, v4
v_add_u32 v5, 65536, v5
v_add_u32 v6, 65536, v6
v_add_u32 v7, 65536, v7
E 0,98304
Ta 64,4,0
Ta 68,5,0
E 1,102400
Ta 72,6,0
Ta 76,7,0
E 2,106496
Ta 80,4,16384
E 3,110592
s_add_u32 s51, s51, 65536
Ta 84,5,16384
Ta 88,6,16384
E 0,114688
Ta 92,7,16384
E 1,118784
E 2,122880
E 3,126976
s_add_u32 s51, s51, 65536
s_waitcnt lgkmcnt(0)
s_waitcnt vmcnt(8)
s_barrier
v_add_u32 v4, -65536, v4
v_add_u32 v5, -65536, v5
v_add_u32 v6, -65536, v6
v_add_u32 v7, -65536, v7
E 0,131072
Ta 96,4,0
Ta 100,5,0
E 1,135168
Ta 104,6,0
Ta 108,7,0
E 2,139264
Ta 112,4,16384
E 3,143360
s_add_u32 s51, s51, 65536
Ta 116,5,16384
Ta 120,6,16384
E 0,147456
Ta 124,7,16384
E 1,151552
E 2,155648
E 3,159744
s_add_u32 s51, s51, 65536
s_cmp_ge_u32 s51, s57
s_cselect_b32 s60, 1572864, 0
s_sub_u32 s51, s51, s60
s_waitcnt lgkmcnt(0)
s_waitcnt vmcnt(8)
s_barrier
E 0,65536
Ta 128,4,32768
Ta 132,5,32768
E 1,69632
Ta 136,6,32768
Ta 140,7,32768
E 2,73728
Ta 144,4,49152
E 3,77824
s_add_u32 s51, s51, 65536
Ta 148,5,49152
Ta 152,6,49152
E 0,81920
Ta 156,7,49152
E 1,86016
E 2,90112
E 3,94208
s_add_u32 s51, s51, 65536
s_waitcnt lgkmcnt(0)
s_waitcnt vmcnt(8)
s_barrier
v_add_u32 v4, 65536, v4
v_add_u32 v5, 65536, v5
v_add_u32 v6, 65536, v6
v_add_u32 v7, 65536, v7
E 0,98304
Ta 160,4,0
Ta 164,5,0
E 1,102400
Ta 168,6,0
Ta 172,7,0
E 2,106496
Ta 176,4,16384
E 3,110592
s_add_u32 s51, s51, 65536
Ta 180,5,16384
Ta 184,6,16384
E 0,114688
Ta 188,7,16384
E 1,118784
E 2,122880
E 3,126976
s_add_u32 s51, s51, 65536
s_waitcnt lgkmcnt(0)
s_waitcnt vmcnt(8)
s_barrier
v_add_u32 v4, -65536, v4
v_add_u32 v5, -65536, v5
v_add_u32 v6, -65536, v6
v_add_u32 v7, -65536, v7
E 0,131072
Ta 192,4,0
Ta 196,5,0
E 1,135168
Ta 200,6,0
Ta 204,7,0
E 2,139264
Ta 208,4,16384
E 3,143360
s_add_u32 s51, s51, 65536
Ta 212,5,16384
Ta 216,6,16384
E 0,147456
Ta 220,7,16384
E 1,151552
E 2,155648
E 3,159744
s_add_u32 s51, s51, 65536
s_cmp_ge_u32 s51, s57
s_cselect_b32 s60, 1572864, 0
s_sub_u32 s51, s51, s60
s_waitcnt lgkmcnt(0)
s_waitcnt vmcnt(8)
s_barrier
E 0,65536
Ta 224,4,32768
Ta 228,5,32768
E 1,69632
Ta 232,6,32768
Ta 236,7,32768
E 2,73728
Ta 240,4,49152
E 3,77824
s_add_u32 s51, s51, 65536
Ta 244,5,49152
Ta 248,6,49152
E 0,81920
Ta 252,7,49152
E 1,86016
E 2,90112
E 3,94208
s_add_u32 s51, s51, 65536
s_waitcnt lgkmcnt(0)
s_waitcnt vmcnt(8)
s_barrier
v_add_u32 v4, 65536, v4
v_add_u32 v5, 65536, v5
v_add_u32 v6, 65536, v6
v_add_u32 v7, 65536, v7
E 0,98304
Tv 128,4,0
Tv 132,5,0
E 1,102400
Tv 136,6,0
Tv 140,7,0
E 2,106496
Tv 144,4,16384
E 3,110592
s_add_u32 s51, s51, 65536
Tv 148,5,16384
Tv 152,6,16384
E 0,114688
Tv 156,7,16384
E 1,118784
E 2,122880
E 3,126976
s_add_u32 s51, s51, 65536
s_waitcnt lgkmcnt(0)
s_waitcnt vmcnt(8)
s_barrier
v_add_u32 v4, -65536, v4
v_add_u32 v5, -65536, v5
v_add_u32 v6, -65536, v6
v_add_u32 v7, -65536, v7
E 0,131072
Tv 160,4,0
Tv 164,5,0
E 1,135168
Tv 168,6,0
Tv 172,7,0
E 2,139264
Tv 176,4,16384
E 3,143360
s_add_u32 s51, s51, 65536
Tv 180,5,16384
Tv 184,6,16384
E 0,147456
Tv 188,7,16384
E 1,151552
E 2,155648
E 3,159744
s_add_u32 s51, s51, 65536
s_cmp_ge_u32 s51, s57
s_cselect_b32 s60, 1572864, 0
s_sub_u32 s51, s51, s60
s_waitcnt lgkmcnt(0)
s_waitcnt vmcnt(8)
s_barrier
Tv 192,4,32768
Tv 196,5,32768
Tv 200,6,32768
Tv 204,7,32768
Tv 208,4,49152
Tv 212,5,49152
Tv 216,6,49152
Tv 220,7,49152
s_waitcnt lgkmcnt(0)
s_waitcnt vmcnt(0)
s_barrier
v_add_u32 v4, 65536, v4
v_add_u32 v5, 65536, v5
v_add_u32 v6, 65536, v6
v_add_u32 v7, 65536, v7
Tv 224,4,0
Tv 228,5,0
Tv 232,6,0
Tv 236,7,0
Tv 240,4,16384
Tv 244,5,16384
Tv 248,6,16384
Tv 252,7,16384
s_waitcnt lgkmcnt(0)
s_waitcnt vmcnt(0)
s_barrier
s_nop 7
s_and_b32 s60, s48, 3
s_cmp_eq_u32 s60, 1
s_cbranch_scc1 LvG1
s_cmp_eq_u32 s60, 2
s_cbranch_scc1 LvG2
s_cmp_eq_u32 s60, 3
s_cbranch_scc1 LvG3
LvG0:
LsG1:
s_cbranch_execz LsG5
v_add_u32 v72, 1, v72
buffer_load_dwordx2 v[74:75], v85, s[36:39], 0 offen sc1
s_waitcnt vmcnt(0)
ds_write_b32 v82, v74
v_sub_u32 v74, v74, v86
v_xor_b32 v75, v75, v73
v_cmp_lt_u64 vcc, s[62:63], v[74:75]
s_waitcnt lgkmcnt(0)
s_cbranch_vccnz CG0_1
BG0_1:
s_add_u32 s49, s49, 313344
s_barrier
s_add_u32 m0, s50, 0
buffer_store_dword v72, v81, s[36:39], s52 offen sc1
D 83,0
D 83,1024
D 83,2048
D 83,3072
s_add_u32 m0, s50, 4096
s_nop 0
D 84,0
D 84,1024
s_mov_b32 m0, 144384
s_nop 0
buffer_load_dword v85, s[36:39], 0 offen sc1 lds
v_add_u32 v86, 1, v86
v_add_u32 v72, 1, v72
ds_read_b32 v74, v82 offset:0
s_waitcnt lgkmcnt(0)
v_cmp_gt_i32 vcc, v86, v74
s_waitcnt vmcnt(1)
s_add_u32 s49, s49, 313344
s_cbranch_vccnz CG0_2
BG0_2:
s_barrier
s_add_u32 m0, s50, 32768
R 32,0
buffer_store_dword v72, v81, s[36:39], s52 offen sc1
D 83,0
R 36,1024
D 83,1024
R 40,2048
D 83,2048
s_waitcnt lgkmcnt(1)
v_mfma_f32_16x16x32_bf16 v[0:3], a[0:3], v[32:35], 0
R 44,3072
v_mfma_f32_16x16x32_bf16 v[4:7], a[4:7], v[32:35], 0
D 83,3072
v_mfma_f32_16x16x32_bf16 v[8:11], a[8:11], v[32:35], 0
v_mfma_f32_16x16x32_bf16 v[12:15], a[12:15], v[32:35], 0
s_add_u32 m0, s50, 36864
mav 0,16,36
R 48,4096
mav 4,20,36
D 84,0
mav 8,24,36
mav 12,28,36
s_waitcnt lgkmcnt(1)
mav 0,32,40
R 52,5120
mav 4,36,40
D 84,1024
mav 8,40,40
mav 12,44,40
mav 0,48,44
R 56,6144
mav 4,52,44
mav 8,56,44
mav 12,60,44
s_waitcnt lgkmcnt(1)
mav 0,64,48
R 60,7168
mav 4,68,48
mav 8,72,48
mav 12,76,48
mav 0,80,52
R 32,8192
mav 4,84,52
mav 8,88,52
mav 12,92,52
s_waitcnt lgkmcnt(1)
mav 0,96,56
R 36,9216
mav 4,100,56
mav 8,104,56
mav 12,108,56
mav 0,112,60
R 40,10240
mav 4,116,60
mav 8,120,60
mav 12,124,60
s_waitcnt lgkmcnt(1)
mav 0,128,32
R 44,11264
mav 4,132,32
mav 8,136,32
LsG2:
s_cbranch_execz LsG6
s_mov_b32 m0, 144384
mav 12,140,32
mav 0,144,36
R 48,12288
mav 4,148,36
buffer_load_dword v85, s[36:39], 0 offen sc1 lds
mav 8,152,36
mav 12,156,36
s_waitcnt lgkmcnt(1)
mav 0,160,40
R 52,13312
v_add_u32 v86, 1, v86
mav 4,164,40
mav 8,168,40
mav 12,172,40
mav 0,176,44
R 56,14336
mav 4,180,44
mav 8,184,44
mav 12,188,44
s_waitcnt lgkmcnt(1)
mav 0,192,48
R 60,15360
mav 4,196,48
mav 8,200,48
mav 12,204,48
mav 0,208,52
R 32,16384
mav 4,212,52
mav 8,216,52
mav 12,220,52
s_waitcnt lgkmcnt(1)
mav 0,224,56
R 36,17408
mav 4,228,56
mav 8,232,56
mav 12,236,56
mav 0,240,60
R 40,18432
mav 4,244,60
mav 8,248,60
mav 12,252,60
s_waitcnt lgkmcnt(1)
mvv 0,128,32
R 44,19456
mvv 4,132,32
mvv 8,136,32
mvv 12,140,32
mvv 0,144,36
R 48,20480
mvv 4,148,36
mvv 8,152,36
mvv 12,156,36
s_waitcnt lgkmcnt(1)
mvv 0,160,40
R 52,21504
mvv 4,164,40
ds_read_b32 v74, v82 offset:0
mvv 8,168,40
mvv 12,172,40
mvv 0,176,44
R 56,22528
mvv 4,180,44
mvv 8,184,44
s_xor_b32 s50, s50, 0x10000
mvv 12,188,44
s_waitcnt lgkmcnt(1)
mvv 0,192,48
R 60,23552
v_cmp_gt_i32 vcc, v86, v74
mvv 4,196,48
mvv 8,200,48
s_waitcnt vmcnt(1)
s_add_u32 s49, s49, 313344
mvv 12,204,48
s_cbranch_vccnz CG0_3
BG0_3:
s_barrier
mvv 0,208,52
s_add_u32 m0, s50, 0
R 32,32768
mvv 4,212,52
D 83,0
mvv 8,216,52
mvv 12,220,52
s_waitcnt lgkmcnt(1)
mvv 0,224,56
R 36,33792
mvv 4,228,56
D 83,1024
mvv 8,232,56
mvv 12,236,56
mvv 0,240,60
R 40,34816
mvv 4,244,60
D 83,2048
mvv 8,248,60
mvv 12,252,60
s_waitcnt lgkmcnt(1)
v_mfma_f32_16x16x32_bf16 v[16:19], a[0:3], v[32:35], 0
R 44,35840
v_cvt_pk_bf16_f32 v64, v0, v1
v_mfma_f32_16x16x32_bf16 v[20:23], a[4:7], v[32:35], 0
D 83,3072
v_mfma_f32_16x16x32_bf16 v[24:27], a[8:11], v[32:35], 0
v_cvt_pk_bf16_f32 v65, v2, v3
v_cvt_pk_bf16_f32 v66, v4, v5
v_mfma_f32_16x16x32_bf16 v[28:31], a[12:15], v[32:35], 0
s_add_u32 m0, s50, 4096
v_cvt_pk_bf16_f32 v67, v6, v7
v_cvt_pk_bf16_f32 v68, v8, v9
mav 16,16,36
R 48,36864
v_cvt_pk_bf16_f32 v69, v10, v11
mav 20,20,36
buffer_store_dwordx4 v[64:67], v87, s[36:39], s49 offen sc1
D 84,0
mav 24,24,36
v_cvt_pk_bf16_f32 v70, v12, v13
v_cvt_pk_bf16_f32 v71, v14, v15
mav 28,28,36
s_waitcnt lgkmcnt(1)
mav 16,32,40
R 52,37888
mav 20,36,40
buffer_store_dwordx4 v[68:71], v87, s[36:39], s49 offen offset:1024 sc1
D 84,1024
mav 24,40,40
mav 28,44,40
mav 16,48,44
R 56,38912
mav 20,52,44
mav 24,56,44
mav 28,60,44
s_waitcnt lgkmcnt(1)
mav 16,64,48
R 60,39936
mav 20,68,48
mav 24,72,48
mav 28,76,48
mav 16,80,52
R 32,40960
mav 20,84,52
mav 24,88,52
mav 28,92,52
s_waitcnt lgkmcnt(1)
mav 16,96,56
R 36,41984
mav 20,100,56
mav 24,104,56
mav 28,108,56
mav 16,112,60
R 40,43008
mav 20,116,60
mav 24,120,60
mav 28,124,60
s_waitcnt lgkmcnt(1)
mav 16,128,32
R 44,44032
mav 20,132,32
mav 24,136,32
s_mov_b32 m0, 144384
mav 28,140,32
mav 16,144,36
R 48,45056
v_add_u32 v72, 1, v72
mav 20,148,36
buffer_load_dword v85, s[36:39], 0 offen sc1 lds
mav 24,152,36
mav 28,156,36
s_waitcnt lgkmcnt(1)
mav 16,160,40
R 52,46080
v_add_u32 v86, 1, v86
mav 20,164,40
mav 24,168,40
mav 28,172,40
mav 16,176,44
R 56,47104
mav 20,180,44
mav 24,184,44
mav 28,188,44
s_waitcnt lgkmcnt(1)
mav 16,192,48
R 60,48128
mav 20,196,48
mav 24,200,48
s_mov_b32 s60, -30
.p2align 6
LG0_4:
mav 28,204,48
mav 16,208,52
R 32,49152
mav 20,212,52
mav 24,216,52
mav 28,220,52
s_waitcnt lgkmcnt(1)
mav 16,224,56
R 36,50176
mav 20,228,56
mav 24,232,56
mav 28,236,56
mav 16,240,60
s_waitcnt vmcnt(2)
R 40,51200
mav 20,244,60
buffer_store_dword v72, v81, s[36:39], s52 offen sc1
mav 24,248,60
mav 28,252,60
s_waitcnt lgkmcnt(1)
mvv 16,128,32
R 44,52224
mvv 20,132,32
mvv 24,136,32
mvv 28,140,32
mvv 16,144,36
R 48,53248
mvv 20,148,36
mvv 24,152,36
mvv 28,156,36
s_waitcnt lgkmcnt(1)
mvv 16,160,40
R 52,54272
mvv 20,164,40
ds_read_b32 v74, v82 offset:0
mvv 24,168,40
mvv 28,172,40
mvv 16,176,44
R 56,55296
mvv 20,180,44
mvv 24,184,44
mvv 28,188,44
s_waitcnt lgkmcnt(1)
mvv 16,192,48
R 60,56320
v_cmp_gt_i32 vcc, v86, v74
mvv 20,196,48
v_xor_b32 v80, 0x10000, v80
mvv 24,200,48
s_waitcnt vmcnt(2)
s_add_u32 s49, s49, 313344
mvv 28,204,48
s_cbranch_vccnz CG0_5
BG0_5:
s_barrier
mvv 16,208,52
s_add_u32 m0, s50, 32768
R 32,0
mvv 20,212,52
D 83,0
mvv 24,216,52
mvv 28,220,52
s_waitcnt lgkmcnt(1)
mvv 16,224,56
R 36,1024
mvv 20,228,56
D 83,1024
mvv 24,232,56
mvv 28,236,56
mvv 16,240,60
R 40,2048
mvv 20,244,60
D 83,2048
mvv 24,248,60
mvv 28,252,60
s_waitcnt lgkmcnt(1)
v_mfma_f32_16x16x32_bf16 v[0:3], a[0:3], v[32:35], 0
R 44,3072
v_cvt_pk_bf16_f32 v64, v16, v17
v_mfma_f32_16x16x32_bf16 v[4:7], a[4:7], v[32:35], 0
D 83,3072
v_mfma_f32_16x16x32_bf16 v[8:11], a[8:11], v[32:35], 0
v_cvt_pk_bf16_f32 v65, v18, v19
v_cvt_pk_bf16_f32 v66, v20, v21
v_mfma_f32_16x16x32_bf16 v[12:15], a[12:15], v[32:35], 0
s_add_u32 m0, s50, 36864
v_cvt_pk_bf16_f32 v67, v22, v23
v_cvt_pk_bf16_f32 v68, v24, v25
mav 0,16,36
R 48,4096
v_cvt_pk_bf16_f32 v69, v26, v27
mav 4,20,36
buffer_store_dwordx4 v[64:67], v87, s[36:39], s49 offen sc1
D 84,0
mav 8,24,36
v_cvt_pk_bf16_f32 v70, v28, v29
v_cvt_pk_bf16_f32 v71, v30, v31
mav 12,28,36
s_waitcnt lgkmcnt(1)
mav 0,32,40
R 52,5120
mav 4,36,40
buffer_store_dwordx4 v[68:71], v87, s[36:39], s49 offen offset:1024 sc1
D 84,1024
mav 8,40,40
mav 12,44,40
mav 0,48,44
R 56,6144
mav 4,52,44
mav 8,56,44
mav 12,60,44
s_waitcnt lgkmcnt(1)
mav 0,64,48
R 60,7168
mav 4,68,48
mav 8,72,48
mav 12,76,48
mav 0,80,52
R 32,8192
mav 4,84,52
mav 8,88,52
mav 12,92,52
s_waitcnt lgkmcnt(1)
mav 0,96,56
R 36,9216
mav 4,100,56
mav 8,104,56
mav 12,108,56
mav 0,112,60
R 40,10240
mav 4,116,60
mav 8,120,60
mav 12,124,60
s_waitcnt lgkmcnt(1)
mav 0,128,32
R 44,11264
mav 4,132,32
mav 8,136,32
s_mov_b32 m0, 144384
mav 12,140,32
mav 0,144,36
R 48,12288
v_add_u32 v72, 1, v72
mav 4,148,36
buffer_load_dword v85, s[36:39], 0 offen sc1 lds
mav 8,152,36
mav 12,156,36
s_waitcnt lgkmcnt(1)
mav 0,160,40
R 52,13312
v_add_u32 v86, 1, v86
mav 4,164,40
mav 8,168,40
mav 12,172,40
mav 0,176,44
R 56,14336
mav 4,180,44
mav 8,184,44
mav 12,188,44
s_waitcnt lgkmcnt(1)
mav 0,192,48
R 60,15360
mav 4,196,48
mav 8,200,48
mav 12,204,48
mav 0,208,52
R 32,16384
mav 4,212,52
mav 8,216,52
mav 12,220,52
s_waitcnt lgkmcnt(1)
mav 0,224,56
R 36,17408
mav 4,228,56
mav 8,232,56
mav 12,236,56
mav 0,240,60
s_waitcnt vmcnt(2)
R 40,18432
mav 4,244,60
buffer_store_dword v72, v81, s[36:39], s52 offen sc1
mav 8,248,60
mav 12,252,60
s_waitcnt lgkmcnt(1)
mvv 0,128,32
R 44,19456
mvv 4,132,32
mvv 8,136,32
mvv 12,140,32
mvv 0,144,36
R 48,20480
mvv 4,148,36
mvv 8,152,36
mvv 12,156,36
s_waitcnt lgkmcnt(1)
mvv 0,160,40
R 52,21504
mvv 4,164,40
ds_read_b32 v74, v82 offset:0
mvv 8,168,40
mvv 12,172,40
mvv 0,176,44
R 56,22528
mvv 4,180,44
mvv 8,184,44
s_xor_b32 s50, s50, 0x10000
mvv 12,188,44
s_waitcnt lgkmcnt(1)
mvv 0,192,48
R 60,23552
v_cmp_gt_i32 vcc, v86, v74
mvv 4,196,48
mvv 8,200,48
s_waitcnt vmcnt(2)
s_add_u32 s49, s49, 313344
mvv 12,204,48
s_cbranch_vccnz CG0_6
BG0_6:
s_barrier
mvv 0,208,52
s_add_u32 m0, s50, 0
R 32,32768
mvv 4,212,52
D 83,0
mvv 8,216,52
mvv 12,220,52
s_waitcnt lgkmcnt(1)
mvv 0,224,56
R 36,33792
mvv 4,228,56
D 83,1024
mvv 8,232,56
mvv 12,236,56
mvv 0,240,60
R 40,34816
mvv 4,244,60
D 83,2048
mvv 8,248,60
mvv 12,252,60
s_waitcnt lgkmcnt(1)
v_mfma_f32_16x16x32_bf16 v[16:19], a[0:3], v[32:35], 0
R 44,35840
v_cvt_pk_bf16_f32 v64, v0, v1
v_mfma_f32_16x16x32_bf16 v[20:23], a[4:7], v[32:35], 0
D 83,3072
v_mfma_f32_16x16x32_bf16 v[24:27], a[8:11], v[32:35], 0
v_cvt_pk_bf16_f32 v65, v2, v3
v_cvt_pk_bf16_f32 v66, v4, v5
v_mfma_f32_16x16x32_bf16 v[28:31], a[12:15], v[32:35], 0
s_add_u32 m0, s50, 4096
v_cvt_pk_bf16_f32 v67, v6, v7
v_cvt_pk_bf16_f32 v68, v8, v9
mav 16,16,36
R 48,36864
v_cvt_pk_bf16_f32 v69, v10, v11
mav 20,20,36
buffer_store_dwordx4 v[64:67], v87, s[36:39], s49 offen sc1
D 84,0
mav 24,24,36
v_cvt_pk_bf16_f32 v70, v12, v13
v_cvt_pk_bf16_f32 v71, v14, v15
mav 28,28,36
s_waitcnt lgkmcnt(1)
mav 16,32,40
R 52,37888
mav 20,36,40
buffer_store_dwordx4 v[68:71], v87, s[36:39], s49 offen offset:1024 sc1
D 84,1024
mav 24,40,40
mav 28,44,40
mav 16,48,44
R 56,38912
mav 20,52,44
mav 24,56,44
mav 28,60,44
s_waitcnt lgkmcnt(1)
mav 16,64,48
R 60,39936
mav 20,68,48
mav 24,72,48
mav 28,76,48
mav 16,80,52
R 32,40960
mav 20,84,52
mav 24,88,52
mav 28,92,52
s_waitcnt lgkmcnt(1)
mav 16,96,56
R 36,41984
mav 20,100,56
mav 24,104,56
mav 28,108,56
mav 16,112,60
R 40,43008
mav 20,116,60
mav 24,120,60
mav 28,124,60
s_waitcnt lgkmcnt(1)
mav 16,128,32
R 44,44032
mav 20,132,32
mav 24,136,32
s_mov_b32 m0, 144384
mav 28,140,32
mav 16,144,36
R 48,45056
v_add_u32 v72, 1, v72
mav 20,148,36
buffer_load_dword v85, s[36:39], 0 offen sc1 lds
mav 24,152,36
mav 28,156,36
s_waitcnt lgkmcnt(1)
mav 16,160,40
R 52,46080
v_add_u32 v86, 1, v86
mav 20,164,40
mav 24,168,40
mav 28,172,40
mav 16,176,44
R 56,47104
mav 20,180,44
mav 24,184,44
mav 28,188,44
s_waitcnt lgkmcnt(1)
mav 16,192,48
R 60,48128
mav 20,196,48
s_add_u32 s60, s60, 1
mav 24,200,48
s_cbranch_scc0 LG0_4
mav 28,204,48
mav 16,208,52
R 32,49152
mav 20,212,52
mav 24,216,52
mav 28,220,52
s_waitcnt lgkmcnt(1)
mav 16,224,56
R 36,50176
mav 20,228,56
mav 24,232,56
mav 28,236,56
mav 16,240,60
s_waitcnt vmcnt(2)
R 40,51200
mav 20,244,60
buffer_store_dword v72, v81, s[36:39], s52 offen sc1
mav 24,248,60
mav 28,252,60
s_waitcnt lgkmcnt(1)
mvv 16,128,32
R 44,52224
LsG3:
s_cbranch_execz LsG7
mvv 20,132,32
mvv 24,136,32
mvv 28,140,32
mvv 16,144,36
R 48,53248
mvv 20,148,36
mvv 24,152,36
mvv 28,156,36
s_waitcnt lgkmcnt(1)
mvv 16,160,40
R 52,54272
mvv 20,164,40
ds_read_b32 v74, v82 offset:0
mvv 24,168,40
mvv 28,172,40
mvv 16,176,44
R 56,55296
mvv 20,180,44
mvv 24,184,44
mvv 28,188,44
s_waitcnt lgkmcnt(1)
mvv 16,192,48
R 60,56320
v_cmp_gt_i32 vcc, v86, v74
mvv 20,196,48
v_xor_b32 v80, 0x10000, v80
mvv 24,200,48
s_waitcnt vmcnt(2)
s_add_u32 s49, s49, 313344
mvv 28,204,48
s_cbranch_vccnz CG0_7
BG0_7:
s_barrier
mvv 16,208,52
s_add_u32 m0, s50, 32768
R 32,0
mvv 20,212,52
D 83,0
mvv 24,216,52
mvv 28,220,52
s_waitcnt lgkmcnt(1)
mvv 16,224,56
R 36,1024
mvv 20,228,56
D 83,1024
mvv 24,232,56
mvv 28,236,56
mvv 16,240,60
R 40,2048
mvv 20,244,60
D 83,2048
mvv 24,248,60
mvv 28,252,60
s_waitcnt lgkmcnt(1)
v_mfma_f32_16x16x32_bf16 v[0:3], a[0:3], v[32:35], 0
R 44,3072
v_cvt_pk_bf16_f32 v64, v16, v17
v_mfma_f32_16x16x32_bf16 v[4:7], a[4:7], v[32:35], 0
D 83,3072
v_mfma_f32_16x16x32_bf16 v[8:11], a[8:11], v[32:35], 0
v_cvt_pk_bf16_f32 v65, v18, v19
v_cvt_pk_bf16_f32 v66, v20, v21
v_mfma_f32_16x16x32_bf16 v[12:15], a[12:15], v[32:35], 0
s_add_u32 m0, s50, 36864
v_cvt_pk_bf16_f32 v67, v22, v23
v_cvt_pk_bf16_f32 v68, v24, v25
mav 0,16,36
R 48,4096
v_cvt_pk_bf16_f32 v69, v26, v27
mav 4,20,36
buffer_store_dwordx4 v[64:67], v87, s[36:39], s49 offen sc1
D 84,0
mav 8,24,36
v_cvt_pk_bf16_f32 v70, v28, v29
v_cvt_pk_bf16_f32 v71, v30, v31
mav 12,28,36
s_waitcnt lgkmcnt(1)
mav 0,32,40
R 52,5120
mav 4,36,40
buffer_store_dwordx4 v[68:71], v87, s[36:39], s49 offen offset:1024 sc1
D 84,1024
mav 8,40,40
mav 12,44,40
mav 0,48,44
R 56,6144
mav 4,52,44
mav 8,56,44
mav 12,60,44
s_waitcnt lgkmcnt(1)
mav 0,64,48
R 60,7168
mav 4,68,48
mav 8,72,48
mav 12,76,48
mav 0,80,52
R 32,8192
mav 4,84,52
mav 8,88,52
mav 12,92,52
s_waitcnt lgkmcnt(1)
mav 0,96,56
R 36,9216
mav 4,100,56
mav 8,104,56
mav 12,108,56
mav 0,112,60
R 40,10240
mav 4,116,60
mav 8,120,60
mav 12,124,60
s_waitcnt lgkmcnt(1)
mav 0,128,32
R 44,11264
mav 4,132,32
mav 8,136,32
mav 12,140,32
mav 0,144,36
R 48,12288
v_add_u32 v72, 1, v72
mav 4,148,36
mav 8,152,36
mav 12,156,36
s_waitcnt lgkmcnt(1)
mav 0,160,40
R 52,13312
mav 4,164,40
mav 8,168,40
mav 12,172,40
mav 0,176,44
R 56,14336
mav 4,180,44
mav 8,184,44
mav 12,188,44
s_waitcnt lgkmcnt(1)
mav 0,192,48
R 60,15360
mav 4,196,48
mav 8,200,48
mav 12,204,48
mav 0,208,52
R 32,16384
mav 4,212,52
mav 8,216,52
mav 12,220,52
s_waitcnt lgkmcnt(1)
mav 0,224,56
R 36,17408
mav 4,228,56
mav 8,232,56
mav 12,236,56
mav 0,240,60
s_waitcnt vmcnt(1)
R 40,18432
mav 4,244,60
buffer_store_dword v72, v81, s[36:39], s52 offen sc1
mav 8,248,60
mav 12,252,60
s_waitcnt lgkmcnt(1)
mvv 0,128,32
R 44,19456
mvv 4,132,32
mvv 8,136,32
mvv 12,140,32
mvv 0,144,36
R 48,20480
mvv 4,148,36
mvv 8,152,36
mvv 12,156,36
s_waitcnt lgkmcnt(1)
mvv 0,160,40
R 52,21504
mvv 4,164,40
mvv 8,168,40
mvv 12,172,40
mvv 0,176,44
R 56,22528
mvv 4,180,44
mvv 8,184,44
mvv 12,188,44
s_waitcnt lgkmcnt(1)
mvv 0,192,48
R 60,23552
mvv 4,196,48
mvv 8,200,48
s_waitcnt vmcnt(1)
s_add_u32 s49, s49, 313344
mvv 12,204,48
s_barrier
mvv 0,208,52
R 32,32768
mvv 4,212,52
mvv 8,216,52
mvv 12,220,52
s_waitcnt lgkmcnt(1)
mvv 0,224,56
R 36,33792
mvv 4,228,56
mvv 8,232,56
mvv 12,236,56
mvv 0,240,60
R 40,34816
mvv 4,244,60
mvv 8,248,60
mvv 12,252,60
s_waitcnt lgkmcnt(1)
v_mfma_f32_16x16x32_bf16 v[16:19], a[0:3], v[32:35], 0
R 44,35840
v_mfma_f32_16x16x32_bf16 v[20:23], a[4:7], v[32:35], 0
v_cvt_pk_bf16_f32 v64, v0, v1
v_cvt_pk_bf16_f32 v65, v2, v3
v_mfma_f32_16x16x32_bf16 v[24:27], a[8:11], v[32:35], 0
v_cvt_pk_bf16_f32 v66, v4, v5
v_cvt_pk_bf16_f32 v67, v6, v7
v_mfma_f32_16x16x32_bf16 v[28:31], a[12:15], v[32:35], 0
v_cvt_pk_bf16_f32 v68, v8, v9
v_cvt_pk_bf16_f32 v69, v10, v11
mav 16,16,36
R 48,36864
v_cvt_pk_bf16_f32 v70, v12, v13
mav 20,20,36
buffer_store_dwordx4 v[64:67], v87, s[36:39], s49 offen sc1
v_cvt_pk_bf16_f32 v71, v14, v15
mav 24,24,36
mav 28,28,36
s_waitcnt lgkmcnt(1)
mav 16,32,40
R 52,37888
mav 20,36,40
buffer_store_dwordx4 v[68:71], v87, s[36:39], s49 offen offset:1024 sc1
mav 24,40,40
mav 28,44,40
mav 16,48,44
R 56,38912
mav 20,52,44
mav 24,56,44
mav 28,60,44
s_waitcnt lgkmcnt(1)
mav 16,64,48
R 60,39936
mav 20,68,48
mav 24,72,48
mav 28,76,48
mav 16,80,52
R 32,40960
mav 20,84,52
mav 24,88,52
mav 28,92,52
s_waitcnt lgkmcnt(1)
mav 16,96,56
R 36,41984
mav 20,100,56
mav 24,104,56
mav 28,108,56
mav 16,112,60
R 40,43008
mav 20,116,60
mav 24,120,60
mav 28,124,60
s_waitcnt lgkmcnt(1)
mav 16,128,32
R 44,44032
mav 20,132,32
mav 24,136,32
mav 28,140,32
mav 16,144,36
R 48,45056
v_add_u32 v72, 1, v72
mav 20,148,36
mav 24,152,36
mav 28,156,36
s_waitcnt lgkmcnt(1)
mav 16,160,40
R 52,46080
mav 20,164,40
mav 24,168,40
mav 28,172,40
mav 16,176,44
R 56,47104
mav 20,180,44
mav 24,184,44
mav 28,188,44
s_waitcnt lgkmcnt(1)
mav 16,192,48
R 60,48128
mav 20,196,48
mav 24,200,48
mav 28,204,48
mav 16,208,52
R 32,49152
mav 20,212,52
mav 24,216,52
mav 28,220,52
s_waitcnt lgkmcnt(1)
mav 16,224,56
R 36,50176
mav 20,228,56
mav 24,232,56
mav 28,236,56
mav 16,240,60
s_waitcnt vmcnt(0)
R 40,51200
mav 20,244,60
buffer_store_dword v72, v81, s[36:39], s52 offen sc1
mav 24,248,60
mav 28,252,60
s_waitcnt lgkmcnt(1)
mvv 16,128,32
R 44,52224
mvv 20,132,32
mvv 24,136,32
mvv 28,140,32
mvv 16,144,36
R 48,53248
mvv 20,148,36
mvv 24,152,36
mvv 28,156,36
s_waitcnt lgkmcnt(1)
mvv 16,160,40
R 52,54272
mvv 20,164,40
mvv 24,168,40
mvv 28,172,40
mvv 16,176,44
R 56,55296
mvv 20,180,44
mvv 24,184,44
mvv 28,188,44
s_waitcnt lgkmcnt(1)
mvv 16,192,48
R 60,56320
mvv 20,196,48
mvv 24,200,48
s_add_u32 s49, s49, 313344
mvv 28,204,48
mvv 16,208,52
mvv 20,212,52
mvv 24,216,52
mvv 28,220,52
s_waitcnt lgkmcnt(0)
mvv 16,224,56
mvv 20,228,56
mvv 24,232,56
mvv 28,236,56
mvv 16,240,60
mvv 20,244,60
mvv 24,248,60
mvv 28,252,60
s_nop 4
v_cvt_pk_bf16_f32 v64, v16, v17
v_cvt_pk_bf16_f32 v65, v18, v19
v_cvt_pk_bf16_f32 v66, v20, v21
v_cvt_pk_bf16_f32 v67, v22, v23
buffer_store_dwordx4 v[64:67], v87, s[36:39], s49 offen sc1
v_cvt_pk_bf16_f32 v68, v24, v25
v_cvt_pk_bf16_f32 v69, v26, v27
v_cvt_pk_bf16_f32 v70, v28, v29
v_cvt_pk_bf16_f32 v71, v30, v31
buffer_store_dwordx4 v[68:71], v87, s[36:39], s49 offen offset:1024 sc1
v_add_u32 v72, 35, v72
s_waitcnt vmcnt(0)
buffer_store_dword v72, v81, s[36:39], s52 offen sc1
s_endpgm
CG0_1:
s_cmp_eq_u32 s61, 0
s_cbranch_scc1 BG0_1
s_sub_u32 s61, s61, 1
s_sleep 8
buffer_load_dwordx2 v[74:75], v85, s[36:39], 0 offen sc1
s_waitcnt vmcnt(0)
ds_write_b32 v82, v74
v_sub_u32 v74, v74, v86
v_xor_b32 v75, v75, v73
v_cmp_lt_u64 vcc, s[62:63], v[74:75]
s_waitcnt lgkmcnt(0)
s_cbranch_vccnz CG0_1
s_branch BG0_1
CG0_2:
s_mov_b32 s58, m0
s_mov_b32 m0, 144384
DG0_2:
s_cmp_eq_u32 s61, 0
s_cbranch_scc1 EG0_2
s_sub_u32 s61, s61, 1
buffer_load_dword v85, s[36:39], 0 offen sc1 lds
s_sleep 1
ds_read_b32 v74, v82
s_waitcnt lgkmcnt(0)
v_cmp_gt_i32 vcc, v86, v74
s_cbranch_vccnz DG0_2
EG0_2:
s_mov_b32 m0, s58
s_branch BG0_2
CG0_3:
s_mov_b32 s58, m0
s_mov_b32 m0, 144384
DG0_3:
s_cmp_eq_u32 s61, 0
s_cbranch_scc1 EG0_3
s_sub_u32 s61, s61, 1
buffer_load_dword v85, s[36:39], 0 offen sc1 lds
s_sleep 1
ds_read_b32 v74, v82
s_waitcnt lgkmcnt(0)
v_cmp_gt_i32 vcc, v86, v74
s_cbranch_vccnz DG0_3
EG0_3:
s_mov_b32 m0, s58
s_branch BG0_3
CG0_5:
s_mov_b32 s58, m0
s_mov_b32 m0, 144384
DG0_5:
s_cmp_eq_u32 s61, 0
s_cbranch_scc1 EG0_5
s_sub_u32 s61, s61, 1
buffer_load_dword v85, s[36:39], 0 offen sc1 lds
s_sleep 1
ds_read_b32 v74, v82
s_waitcnt lgkmcnt(0)
v_cmp_gt_i32 vcc, v86, v74
s_cbranch_vccnz DG0_5
EG0_5:
s_mov_b32 m0, s58
s_branch BG0_5
CG0_6:
s_mov_b32 s58, m0
s_mov_b32 m0, 144384
DG0_6:
s_cmp_eq_u32 s61, 0
s_cbranch_scc1 EG0_6
s_sub_u32 s61, s61, 1
buffer_load_dword v85, s[36:39], 0 offen sc1 lds
s_sleep 1
ds_read_b32 v74, v82
s_waitcnt lgkmcnt(0)
v_cmp_gt_i32 vcc, v86, v74
s_cbranch_vccnz DG0_6
EG0_6:
s_mov_b32 m0, s58
s_branch BG0_6
CG0_7:
s_mov_b32 s58, m0
s_mov_b32 m0, 144384
DG0_7:
s_cmp_eq_u32 s61, 0
s_cbranch_scc1 EG0_7
s_sub_u32 s61, s61, 1
buffer_load_dword v85, s[36:39], 0 offen sc1 lds
s_sleep 1
ds_read_b32 v74, v82
s_waitcnt lgkmcnt(0)
v_cmp_gt_i32 vcc, v86, v74
s_cbranch_vccnz DG0_7
EG0_7:
s_mov_b32 m0, s58
s_branch BG0_7
LvG1:
LsG4:
s_cbranch_execz LsG8
v_add_u32 v72, 1, v72
s_add_u32 s49, s49, 313344
s_barrier
s_add_u32 m0, s50, 0
buffer_store_dword v72, v81, s[36:39], s52 offen sc1
D 83,0
D 83,1024
D 83,2048
D 83,3072
s_add_u32 m0, s50, 4096
s_nop 0
D 84,0
D 84,1024
v_add_u32 v72, 1, v72
s_waitcnt vmcnt(0)
s_add_u32 s49, s49, 313344
s_barrier
s_add_u32 m0, s50, 32768
R 32,0
buffer_store_dword v72, v81, s[36:39], s52 offen sc1
D 83,0
R 36,1024
D 83,1024
R 40,2048
D 83,2048
s_waitcnt lgkmcnt(1)
v_mfma_f32_16x16x32_bf16 v[0:3], a[0:3], v[32:35], 0
R 44,3072
v_mfma_f32_16x16x32_bf16 v[4:7], a[4:7], v[32:35], 0
v_mfma_f32_16x16x32_bf16 v[8:11], a[8:11], v[32:35], 0
v_mfma_f32_16x16x32_bf16 v[12:15], a[12:15], v[32:35], 0
mav 0,16,36
D 83,3072
mav 4,20,36
s_add_u32 m0, s50, 36864
R 48,4096
mav 8,24,36
mav 12,28,36
s_waitcnt lgkmcnt(1)
mav 0,32,40
D 84,0
mav 4,36,40
R 52,5120
mav 8,40,40
mav 12,44,40
mav 0,48,44
D 84,1024
mav 4,52,44
R 56,6144
mav 8,56,44
mav 12,60,44
s_waitcnt lgkmcnt(1)
mav 0,64,48
R 60,7168
mav 4,68,48
mav 8,72,48
mav 12,76,48
mav 0,80,52
R 32,8192
mav 4,84,52
mav 8,88,52
mav 12,92,52
s_waitcnt lgkmcnt(1)
mav 0,96,56
R 36,9216
mav 4,100,56
mav 8,104,56
mav 12,108,56
mav 0,112,60
R 40,10240
mav 4,116,60
mav 8,120,60
mav 12,124,60
s_waitcnt lgkmcnt(1)
mav 0,128,32
R 44,11264
mav 4,132,32
mav 8,136,32
mav 12,140,32
mav 0,144,36
R 48,12288
mav 4,148,36
mav 8,152,36
mav 12,156,36
s_waitcnt lgkmcnt(1)
mav 0,160,40
R 52,13312
mav 4,164,40
mav 8,168,40
mav 12,172,40
mav 0,176,44
R 56,14336
mav 4,180,44
mav 8,184,44
mav 12,188,44
s_waitcnt lgkmcnt(1)
mav 0,192,48
R 60,15360
mav 4,196,48
mav 8,200,48
mav 12,204,48
mav 0,208,52
R 32,16384
mav 4,212,52
mav 8,216,52
mav 12,220,52
s_waitcnt lgkmcnt(1)
mav 0,224,56
R 36,17408
mav 4,228,56
mav 8,232,56
mav 12,236,56
mav 0,240,60
R 40,18432
mav 4,244,60
mav 8,248,60
mav 12,252,60
s_waitcnt lgkmcnt(1)
mvv 0,128,32
R 44,19456
mvv 4,132,32
mvv 8,136,32
mvv 12,140,32
mvv 0,144,36
R 48,20480
mvv 4,148,36
mvv 8,152,36
mvv 12,156,36
s_waitcnt lgkmcnt(1)
mvv 0,160,40
R 52,21504
LsG5:
s_cbranch_execz LsG9
mvv 4,164,40
mvv 8,168,40
mvv 12,172,40
mvv 0,176,44
R 56,22528
mvv 4,180,44
mvv 8,184,44
s_xor_b32 s50, s50, 0x10000
mvv 12,188,44
s_waitcnt lgkmcnt(1)
mvv 0,192,48
R 60,23552
mvv 4,196,48
mvv 8,200,48
s_waitcnt vmcnt(0)
s_add_u32 s49, s49, 313344
mvv 12,204,48
s_barrier
mvv 0,208,52
s_add_u32 m0, s50, 0
R 32,32768
mvv 4,212,52
mvv 8,216,52
mvv 12,220,52
s_waitcnt lgkmcnt(1)
mvv 0,224,56
D 83,0
mvv 4,228,56
R 36,33792
mvv 8,232,56
mvv 12,236,56
mvv 0,240,60
D 83,1024
mvv 4,244,60
R 40,34816
mvv 8,248,60
mvv 12,252,60
s_waitcnt lgkmcnt(1)
v_mfma_f32_16x16x32_bf16 v[16:19], a[0:3], v[32:35], 0
D 83,2048
v_mfma_f32_16x16x32_bf16 v[20:23], a[4:7], v[32:35], 0
R 44,35840
v_cvt_pk_bf16_f32 v64, v0, v1
v_mfma_f32_16x16x32_bf16 v[24:27], a[8:11], v[32:35], 0
v_cvt_pk_bf16_f32 v65, v2, v3
v_cvt_pk_bf16_f32 v66, v4, v5
v_mfma_f32_16x16x32_bf16 v[28:31], a[12:15], v[32:35], 0
v_cvt_pk_bf16_f32 v67, v6, v7
v_cvt_pk_bf16_f32 v68, v8, v9
mav 16,16,36
buffer_store_dwordx4 v[64:67], v87, s[36:39], s49 offen sc1
D 83,3072
mav 20,20,36
s_add_u32 m0, s50, 4096
R 48,36864
v_cvt_pk_bf16_f32 v69, v10, v11
mav 24,24,36
v_cvt_pk_bf16_f32 v70, v12, v13
v_cvt_pk_bf16_f32 v71, v14, v15
mav 28,28,36
s_waitcnt lgkmcnt(1)
mav 16,32,40
buffer_store_dwordx4 v[68:71], v87, s[36:39], s49 offen offset:1024 sc1
D 84,0
mav 20,36,40
R 52,37888
mav 24,40,40
mav 28,44,40
mav 16,48,44
D 84,1024
mav 20,52,44
R 56,38912
mav 24,56,44
mav 28,60,44
s_waitcnt lgkmcnt(1)
mav 16,64,48
R 60,39936
mav 20,68,48
mav 24,72,48
mav 28,76,48
mav 16,80,52
R 32,40960
mav 20,84,52
mav 24,88,52
mav 28,92,52
s_waitcnt lgkmcnt(1)
mav 16,96,56
R 36,41984
mav 20,100,56
mav 24,104,56
mav 28,108,56
mav 16,112,60
R 40,43008
mav 20,116,60
mav 24,120,60
mav 28,124,60
s_waitcnt lgkmcnt(1)
mav 16,128,32
R 44,44032
mav 20,132,32
mav 24,136,32
mav 28,140,32
mav 16,144,36
R 48,45056
v_add_u32 v72, 1, v72
mav 20,148,36
mav 24,152,36
mav 28,156,36
s_waitcnt lgkmcnt(1)
mav 16,160,40
R 52,46080
mav 20,164,40
mav 24,168,40
mav 28,172,40
mav 16,176,44
R 56,47104
mav 20,180,44
mav 24,184,44
mav 28,188,44
s_waitcnt lgkmcnt(1)
mav 16,192,48
R 60,48128
mav 20,196,48
mav 24,200,48
s_mov_b32 s60, -30
.p2align 6
LG1_1:
mav 28,204,48
mav 16,208,52
R 32,49152
mav 20,212,52
mav 24,216,52
mav 28,220,52
s_waitcnt lgkmcnt(1)
mav 16,224,56
R 36,50176
mav 20,228,56
mav 24,232,56
mav 28,236,56
mav 16,240,60
s_waitcnt vmcnt(2)
buffer_store_dword v72, v81, s[36:39], s52 offen sc1
mav 20,244,60
R 40,51200
mav 24,248,60
mav 28,252,60
s_waitcnt lgkmcnt(1)
mvv 16,128,32
R 44,52224
mvv 20,132,32
mvv 24,136,32
mvv 28,140,32
mvv 16,144,36
R 48,53248
mvv 20,148,36
mvv 24,152,36
mvv 28,156,36
s_waitcnt lgkmcnt(1)
mvv 16,160,40
R 52,54272
mvv 20,164,40
mvv 24,168,40
mvv 28,172,40
mvv 16,176,44
R 56,55296
mvv 20,180,44
mvv 24,184,44
mvv 28,188,44
s_waitcnt lgkmcnt(1)
mvv 16,192,48
R 60,56320
mvv 20,196,48
v_xor_b32 v80, 0x10000, v80
mvv 24,200,48
s_waitcnt vmcnt(1)
s_add_u32 s49, s49, 313344
mvv 28,204,48
s_barrier
mvv 16,208,52
s_add_u32 m0, s50, 32768
R 32,0
mvv 20,212,52
mvv 24,216,52
mvv 28,220,52
s_waitcnt lgkmcnt(1)
mvv 16,224,56
D 83,0
mvv 20,228,56
R 36,1024
mvv 24,232,56
mvv 28,236,56
mvv 16,240,60
D 83,1024
mvv 20,244,60
R 40,2048
mvv 24,248,60
mvv 28,252,60
s_waitcnt lgkmcnt(1)
v_mfma_f32_16x16x32_bf16 v[0:3], a[0:3], v[32:35], 0
D 83,2048
v_mfma_f32_16x16x32_bf16 v[4:7], a[4:7], v[32:35], 0
R 44,3072
v_cvt_pk_bf16_f32 v64, v16, v17
v_mfma_f32_16x16x32_bf16 v[8:11], a[8:11], v[32:35], 0
v_cvt_pk_bf16_f32 v65, v18, v19
v_cvt_pk_bf16_f32 v66, v20, v21
v_mfma_f32_16x16x32_bf16 v[12:15], a[12:15], v[32:35], 0
v_cvt_pk_bf16_f32 v67, v22, v23
v_cvt_pk_bf16_f32 v68, v24, v25
mav 0,16,36
buffer_store_dwordx4 v[64:67], v87, s[36:39], s49 offen sc1
D 83,3072
mav 4,20,36
s_add_u32 m0, s50, 36864
R 48,4096
v_cvt_pk_bf16_f32 v69, v26, v27
mav 8,24,36
v_cvt_pk_bf16_f32 v70, v28, v29
v_cvt_pk_bf16_f32 v71, v30, v31
mav 12,28,36
s_waitcnt lgkmcnt(1)
mav 0,32,40
buffer_store_dwordx4 v[68:71], v87, s[36:39], s49 offen offset:1024 sc1
D 84,0
mav 4,36,40
R 52,5120
mav 8,40,40
mav 12,44,40
mav 0,48,44
D 84,1024
mav 4,52,44
R 56,6144
mav 8,56,44
mav 12,60,44
s_waitcnt lgkmcnt(1)
mav 0,64,48
R 60,7168
mav 4,68,48
mav 8,72,48
mav 12,76,48
mav 0,80,52
R 32,8192
mav 4,84,52
mav 8,88,52
mav 12,92,52
s_waitcnt lgkmcnt(1)
mav 0,96,56
R 36,9216
mav 4,100,56
mav 8,104,56
mav 12,108,56
mav 0,112,60
R 40,10240
mav 4,116,60
mav 8,120,60
mav 12,124,60
s_waitcnt lgkmcnt(1)
mav 0,128,32
R 44,11264
mav 4,132,32
mav 8,136,32
mav 12,140,32
mav 0,144,36
R 48,12288
v_add_u32 v72, 1, v72
mav 4,148,36
mav 8,152,36
mav 12,156,36
s_waitcnt lgkmcnt(1)
mav 0,160,40
R 52,13312
mav 4,164,40
mav 8,168,40
mav 12,172,40
mav 0,176,44
R 56,14336
mav 4,180,44
mav 8,184,44
mav 12,188,44
s_waitcnt lgkmcnt(1)
mav 0,192,48
R 60,15360
mav 4,196,48
mav 8,200,48
mav 12,204,48
mav 0,208,52
R 32,16384
mav 4,212,52
mav 8,216,52
mav 12,220,52
s_waitcnt lgkmcnt(1)
mav 0,224,56
R 36,17408
mav 4,228,56
mav 8,232,56
mav 12,236,56
mav 0,240,60
s_waitcnt vmcnt(2)
buffer_store_dword v72, v81, s[36:39], s52 offen sc1
mav 4,244,60
R 40,18432
mav 8,248,60
mav 12,252,60
s_waitcnt lgkmcnt(1)
mvv 0,128,32
)"
R"(
R 44,19456
mvv 4,132,32
mvv 8,136,32
mvv 12,140,32
mvv 0,144,36
R 48,20480
mvv 4,148,36
mvv 8,152,36
mvv 12,156,36
s_waitcnt lgkmcnt(1)
mvv 0,160,40
R 52,21504
mvv 4,164,40
mvv 8,168,40
mvv 12,172,40
mvv 0,176,44
R 56,22528
mvv 4,180,44
mvv 8,184,44
s_xor_b32 s50, s50, 0x10000
mvv 12,188,44
s_waitcnt lgkmcnt(1)
mvv 0,192,48
R 60,23552
mvv 4,196,48
mvv 8,200,48
s_waitcnt vmcnt(1)
s_add_u32 s49, s49, 313344
mvv 12,204,48
s_barrier
mvv 0,208,52
s_add_u32 m0, s50, 0
R 32,32768
mvv 4,212,52
mvv 8,216,52
mvv 12,220,52
s_waitcnt lgkmcnt(1)
mvv 0,224,56
D 83,0
mvv 4,228,56
R 36,33792
mvv 8,232,56
mvv 12,236,56
mvv 0,240,60
D 83,1024
mvv 4,244,60
R 40,34816
mvv 8,248,60
mvv 12,252,60
s_waitcnt lgkmcnt(1)
v_mfma_f32_16x16x32_bf16 v[16:19], a[0:3], v[32:35], 0
D 83,2048
v_mfma_f32_16x16x32_bf16 v[20:23], a[4:7], v[32:35], 0
R 44,35840
v_cvt_pk_bf16_f32 v64, v0, v1
v_mfma_f32_16x16x32_bf16 v[24:27], a[8:11], v[32:35], 0
v_cvt_pk_bf16_f32 v65, v2, v3
v_cvt_pk_bf16_f32 v66, v4, v5
v_mfma_f32_16x16x32_bf16 v[28:31], a[12:15], v[32:35], 0
v_cvt_pk_bf16_f32 v67, v6, v7
v_cvt_pk_bf16_f32 v68, v8, v9
mav 16,16,36
buffer_store_dwordx4 v[64:67], v87, s[36:39], s49 offen sc1
D 83,3072
mav 20,20,36
s_add_u32 m0, s50, 4096
R 48,36864
v_cvt_pk_bf16_f32 v69, v10, v11
mav 24,24,36
v_cvt_pk_bf16_f32 v70, v12, v13
v_cvt_pk_bf16_f32 v71, v14, v15
mav 28,28,36
s_waitcnt lgkmcnt(1)
mav 16,32,40
buffer_store_dwordx4 v[68:71], v87, s[36:39], s49 offen offset:1024 sc1
D 84,0
mav 20,36,40
R 52,37888
mav 24,40,40
mav 28,44,40
mav 16,48,44
D 84,1024
mav 20,52,44
R 56,38912
mav 24,56,44
mav 28,60,44
s_waitcnt lgkmcnt(1)
mav 16,64,48
R 60,39936
mav 20,68,48
mav 24,72,48
mav 28,76,48
mav 16,80,52
R 32,40960
mav 20,84,52
mav 24,88,52
mav 28,92,52
s_waitcnt lgkmcnt(1)
mav 16,96,56
R 36,41984
mav 20,100,56
mav 24,104,56
mav 28,108,56
mav 16,112,60
R 40,43008
mav 20,116,60
mav 24,120,60
mav 28,124,60
s_waitcnt lgkmcnt(1)
mav 16,128,32
R 44,44032
mav 20,132,32
mav 24,136,32
mav 28,140,32
mav 16,144,36
R 48,45056
v_add_u32 v72, 1, v72
mav 20,148,36
mav 24,152,36
mav 28,156,36
s_waitcnt lgkmcnt(1)
mav 16,160,40
R 52,46080
mav 20,164,40
mav 24,168,40
mav 28,172,40
mav 16,176,44
R 56,47104
mav 20,180,44
mav 24,184,44
mav 28,188,44
s_waitcnt lgkmcnt(1)
mav 16,192,48
R 60,48128
mav 20,196,48
s_add_u32 s60, s60, 1
mav 24,200,48
s_cbranch_scc0 LG1_1
mav 28,204,48
mav 16,208,52
R 32,49152
mav 20,212,52
mav 24,216,52
mav 28,220,52
s_waitcnt lgkmcnt(1)
mav 16,224,56
R 36,50176
mav 20,228,56
mav 24,232,56
mav 28,236,56
mav 16,240,60
s_waitcnt vmcnt(2)
buffer_store_dword v72, v81, s[36:39], s52 offen sc1
mav 20,244,60
R 40,51200
mav 24,248,60
mav 28,252,60
s_waitcnt lgkmcnt(1)
mvv 16,128,32
R 44,52224
mvv 20,132,32
mvv 24,136,32
mvv 28,140,32
mvv 16,144,36
R 48,53248
mvv 20,148,36
mvv 24,152,36
mvv 28,156,36
s_waitcnt lgkmcnt(1)
mvv 16,160,40
R 52,54272
mvv 20,164,40
mvv 24,168,40
mvv 28,172,40
mvv 16,176,44
R 56,55296
mvv 20,180,44
mvv 24,184,44
mvv 28,188,44
s_waitcnt lgkmcnt(1)
mvv 16,192,48
R 60,56320
mvv 20,196,48
v_xor_b32 v80, 0x10000, v80
mvv 24,200,48
s_waitcnt vmcnt(1)
s_add_u32 s49, s49, 313344
mvv 28,204,48
s_barrier
mvv 16,208,52
s_add_u32 m0, s50, 32768
R 32,0
mvv 20,212,52
mvv 24,216,52
mvv 28,220,52
s_waitcnt lgkmcnt(1)
mvv 16,224,56
D 83,0
mvv 20,228,56
R 36,1024
mvv 24,232,56
mvv 28,236,56
mvv 16,240,60
D 83,1024
mvv 20,244,60
R 40,2048
mvv 24,248,60
mvv 28,252,60
s_waitcnt lgkmcnt(1)
v_mfma_f32_16x16x32_bf16 v[0:3], a[0:3], v[32:35], 0
D 83,2048
v_mfma_f32_16x16x32_bf16 v[4:7], a[4:7], v[32:35], 0
R 44,3072
v_cvt_pk_bf16_f32 v64, v16, v17
v_mfma_f32_16x16x32_bf16 v[8:11], a[8:11], v[32:35], 0
v_cvt_pk_bf16_f32 v65, v18, v19
v_cvt_pk_bf16_f32 v66, v20, v21
v_mfma_f32_16x16x32_bf16 v[12:15], a[12:15], v[32:35], 0
v_cvt_pk_bf16_f32 v67, v22, v23
v_cvt_pk_bf16_f32 v68, v24, v25
mav 0,16,36
buffer_store_dwordx4 v[64:67], v87, s[36:39], s49 offen sc1
D 83,3072
mav 4,20,36
s_add_u32 m0, s50, 36864
R 48,4096
v_cvt_pk_bf16_f32 v69, v26, v27
mav 8,24,36
v_cvt_pk_bf16_f32 v70, v28, v29
v_cvt_pk_bf16_f32 v71, v30, v31
mav 12,28,36
s_waitcnt lgkmcnt(1)
mav 0,32,40
buffer_store_dwordx4 v[68:71], v87, s[36:39], s49 offen offset:1024 sc1
D 84,0
mav 4,36,40
R 52,5120
LsG6:
s_cbranch_execz LsG10
mav 8,40,40
mav 12,44,40
mav 0,48,44
D 84,1024
mav 4,52,44
R 56,6144
mav 8,56,44
mav 12,60,44
s_waitcnt lgkmcnt(1)
mav 0,64,48
R 60,7168
mav 4,68,48
mav 8,72,48
mav 12,76,48
mav 0,80,52
R 32,8192
mav 4,84,52
mav 8,88,52
mav 12,92,52
s_waitcnt lgkmcnt(1)
mav 0,96,56
R 36,9216
mav 4,100,56
mav 8,104,56
mav 12,108,56
mav 0,112,60
R 40,10240
mav 4,116,60
mav 8,120,60
mav 12,124,60
s_waitcnt lgkmcnt(1)
mav 0,128,32
R 44,11264
mav 4,132,32
mav 8,136,32
mav 12,140,32
mav 0,144,36
R 48,12288
v_add_u32 v72, 1, v72
mav 4,148,36
mav 8,152,36
mav 12,156,36
s_waitcnt lgkmcnt(1)
mav 0,160,40
R 52,13312
mav 4,164,40
mav 8,168,40
mav 12,172,40
mav 0,176,44
R 56,14336
mav 4,180,44
mav 8,184,44
mav 12,188,44
s_waitcnt lgkmcnt(1)
mav 0,192,48
R 60,15360
mav 4,196,48
mav 8,200,48
mav 12,204,48
mav 0,208,52
R 32,16384
mav 4,212,52
mav 8,216,52
mav 12,220,52
s_waitcnt lgkmcnt(1)
mav 0,224,56
R 36,17408
mav 4,228,56
mav 8,232,56
mav 12,236,56
mav 0,240,60
s_waitcnt vmcnt(2)
buffer_store_dword v72, v81, s[36:39], s52 offen sc1
mav 4,244,60
R 40,18432
mav 8,248,60
mav 12,252,60
s_waitcnt lgkmcnt(1)
mvv 0,128,32
R 44,19456
mvv 4,132,32
mvv 8,136,32
mvv 12,140,32
mvv 0,144,36
R 48,20480
mvv 4,148,36
mvv 8,152,36
mvv 12,156,36
s_waitcnt lgkmcnt(1)
mvv 0,160,40
R 52,21504
mvv 4,164,40
mvv 8,168,40
mvv 12,172,40
mvv 0,176,44
R 56,22528
mvv 4,180,44
mvv 8,184,44
mvv 12,188,44
s_waitcnt lgkmcnt(1)
mvv 0,192,48
R 60,23552
mvv 4,196,48
mvv 8,200,48
s_waitcnt vmcnt(1)
s_add_u32 s49, s49, 313344
mvv 12,204,48
s_barrier
mvv 0,208,52
R 32,32768
mvv 4,212,52
mvv 8,216,52
mvv 12,220,52
s_waitcnt lgkmcnt(1)
mvv 0,224,56
R 36,33792
mvv 4,228,56
mvv 8,232,56
mvv 12,236,56
mvv 0,240,60
R 40,34816
mvv 4,244,60
mvv 8,248,60
mvv 12,252,60
s_waitcnt lgkmcnt(1)
v_mfma_f32_16x16x32_bf16 v[16:19], a[0:3], v[32:35], 0
R 44,35840
v_mfma_f32_16x16x32_bf16 v[20:23], a[4:7], v[32:35], 0
v_cvt_pk_bf16_f32 v64, v0, v1
v_cvt_pk_bf16_f32 v65, v2, v3
v_mfma_f32_16x16x32_bf16 v[24:27], a[8:11], v[32:35], 0
v_cvt_pk_bf16_f32 v66, v4, v5
v_cvt_pk_bf16_f32 v67, v6, v7
v_mfma_f32_16x16x32_bf16 v[28:31], a[12:15], v[32:35], 0
v_cvt_pk_bf16_f32 v68, v8, v9
v_cvt_pk_bf16_f32 v69, v10, v11
mav 16,16,36
buffer_store_dwordx4 v[64:67], v87, s[36:39], s49 offen sc1
R 48,36864
v_cvt_pk_bf16_f32 v70, v12, v13
mav 20,20,36
v_cvt_pk_bf16_f32 v71, v14, v15
mav 24,24,36
mav 28,28,36
s_waitcnt lgkmcnt(1)
mav 16,32,40
buffer_store_dwordx4 v[68:71], v87, s[36:39], s49 offen offset:1024 sc1
R 52,37888
mav 20,36,40
mav 24,40,40
mav 28,44,40
mav 16,48,44
R 56,38912
mav 20,52,44
mav 24,56,44
mav 28,60,44
s_waitcnt lgkmcnt(1)
mav 16,64,48
R 60,39936
mav 20,68,48
mav 24,72,48
mav 28,76,48
mav 16,80,52
R 32,40960
mav 20,84,52
mav 24,88,52
mav 28,92,52
s_waitcnt lgkmcnt(1)
mav 16,96,56
R 36,41984
mav 20,100,56
mav 24,104,56
mav 28,108,56
mav 16,112,60
R 40,43008
mav 20,116,60
mav 24,120,60
mav 28,124,60
s_waitcnt lgkmcnt(1)
mav 16,128,32
R 44,44032
mav 20,132,32
mav 24,136,32
mav 28,140,32
mav 16,144,36
R 48,45056
v_add_u32 v72, 1, v72
mav 20,148,36
mav 24,152,36
mav 28,156,36
s_waitcnt lgkmcnt(1)
mav 16,160,40
R 52,46080
mav 20,164,40
mav 24,168,40
mav 28,172,40
mav 16,176,44
R 56,47104
mav 20,180,44
mav 24,184,44
mav 28,188,44
s_waitcnt lgkmcnt(1)
mav 16,192,48
R 60,48128
mav 20,196,48
mav 24,200,48
mav 28,204,48
mav 16,208,52
R 32,49152
mav 20,212,52
mav 24,216,52
mav 28,220,52
s_waitcnt lgkmcnt(1)
mav 16,224,56
R 36,50176
mav 20,228,56
mav 24,232,56
mav 28,236,56
mav 16,240,60
s_waitcnt vmcnt(0)
buffer_store_dword v72, v81, s[36:39], s52 offen sc1
mav 20,244,60
R 40,51200
mav 24,248,60
mav 28,252,60
s_waitcnt lgkmcnt(1)
mvv 16,128,32
R 44,52224
mvv 20,132,32
mvv 24,136,32
mvv 28,140,32
mvv 16,144,36
R 48,53248
mvv 20,148,36
mvv 24,152,36
mvv 28,156,36
s_waitcnt lgkmcnt(1)
mvv 16,160,40
R 52,54272
mvv 20,164,40
mvv 24,168,40
mvv 28,172,40
mvv 16,176,44
R 56,55296
mvv 20,180,44
mvv 24,184,44
mvv 28,188,44
s_waitcnt lgkmcnt(1)
mvv 16,192,48
R 60,56320
mvv 20,196,48
mvv 24,200,48
s_add_u32 s49, s49, 313344
mvv 28,204,48
mvv 16,208,52
mvv 20,212,52
mvv 24,216,52
mvv 28,220,52
s_waitcnt lgkmcnt(0)
mvv 16,224,56
mvv 20,228,56
mvv 24,232,56
mvv 28,236,56
mvv 16,240,60
mvv 20,244,60
mvv 24,248,60
mvv 28,252,60
s_nop 4
v_cvt_pk_bf16_f32 v64, v16, v17
v_cvt_pk_bf16_f32 v65, v18, v19
v_cvt_pk_bf16_f32 v66, v20, v21
v_cvt_pk_bf16_f32 v67, v22, v23
buffer_store_dwordx4 v[64:67], v87, s[36:39], s49 offen sc1
v_cvt_pk_bf16_f32 v68, v24, v25
v_cvt_pk_bf16_f32 v69, v26, v27
v_cvt_pk_bf16_f32 v70, v28, v29
v_cvt_pk_bf16_f32 v71, v30, v31
buffer_store_dwordx4 v[68:71], v87, s[36:39], s49 offen offset:1024 sc1
v_add_u32 v72, 35, v72
s_waitcnt vmcnt(0)
buffer_store_dword v72, v81, s[36:39], s52 offen sc1
s_endpgm
LvG2:
LsG7:
s_cbranch_execz LsG11
v_add_u32 v72, 1, v72
s_add_u32 s49, s49, 313344
s_barrier
s_add_u32 m0, s50, 0
buffer_store_dword v72, v81, s[36:39], s52 offen sc1
D 83,0
D 83,1024
D 83,2048
D 83,3072
s_add_u32 m0, s50, 4096
s_nop 0
D 84,0
D 84,1024
v_add_u32 v72, 1, v72
s_waitcnt vmcnt(0)
s_add_u32 s49, s49, 313344
s_barrier
s_add_u32 m0, s50, 32768
R 32,0
buffer_store_dword v72, v81, s[36:39], s52 offen sc1
D 83,0
R 36,1024
D 83,1024
R 40,2048
D 83,2048
s_waitcnt lgkmcnt(1)
v_mfma_f32_16x16x32_bf16 v[0:3], a[0:3], v[32:35], 0
R 44,3072
v_mfma_f32_16x16x32_bf16 v[4:7], a[4:7], v[32:35], 0
v_mfma_f32_16x16x32_bf16 v[8:11], a[8:11], v[32:35], 0
D 83,3072
v_mfma_f32_16x16x32_bf16 v[12:15], a[12:15], v[32:35], 0
s_add_u32 m0, s50, 36864
mav 0,16,36
R 48,4096
mav 4,20,36
mav 8,24,36
D 84,0
mav 12,28,36
s_waitcnt lgkmcnt(1)
mav 0,32,40
R 52,5120
mav 4,36,40
mav 8,40,40
D 84,1024
mav 12,44,40
mav 0,48,44
R 56,6144
mav 4,52,44
mav 8,56,44
mav 12,60,44
s_waitcnt lgkmcnt(1)
mav 0,64,48
R 60,7168
mav 4,68,48
mav 8,72,48
mav 12,76,48
mav 0,80,52
R 32,8192
mav 4,84,52
mav 8,88,52
mav 12,92,52
s_waitcnt lgkmcnt(1)
mav 0,96,56
R 36,9216
mav 4,100,56
mav 8,104,56
mav 12,108,56
mav 0,112,60
R 40,10240
mav 4,116,60
mav 8,120,60
mav 12,124,60
s_waitcnt lgkmcnt(1)
mav 0,128,32
R 44,11264
mav 4,132,32
mav 8,136,32
mav 12,140,32
mav 0,144,36
R 48,12288
mav 4,148,36
mav 8,152,36
mav 12,156,36
s_waitcnt lgkmcnt(1)
mav 0,160,40
R 52,13312
mav 4,164,40
mav 8,168,40
mav 12,172,40
mav 0,176,44
R 56,14336
mav 4,180,44
mav 8,184,44
mav 12,188,44
s_waitcnt lgkmcnt(1)
mav 0,192,48
R 60,15360
mav 4,196,48
mav 8,200,48
mav 12,204,48
mav 0,208,52
R 32,16384
mav 4,212,52
mav 8,216,52
mav 12,220,52
s_waitcnt lgkmcnt(1)
mav 0,224,56
R 36,17408
mav 4,228,56
mav 8,232,56
mav 12,236,56
mav 0,240,60
R 40,18432
mav 4,244,60
mav 8,248,60
mav 12,252,60
s_waitcnt lgkmcnt(1)
mvv 0,128,32
R 44,19456
mvv 4,132,32
mvv 8,136,32
mvv 12,140,32
mvv 0,144,36
R 48,20480
mvv 4,148,36
mvv 8,152,36
mvv 12,156,36
s_waitcnt lgkmcnt(1)
mvv 0,160,40
R 52,21504
mvv 4,164,40
mvv 8,168,40
mvv 12,172,40
mvv 0,176,44
R 56,22528
mvv 4,180,44
mvv 8,184,44
s_xor_b32 s50, s50, 0x10000
mvv 12,188,44
s_waitcnt lgkmcnt(1)
mvv 0,192,48
R 60,23552
mvv 4,196,48
mvv 8,200,48
s_waitcnt vmcnt(0)
s_add_u32 s49, s49, 313344
mvv 12,204,48
s_barrier
mvv 0,208,52
s_add_u32 m0, s50, 0
R 32,32768
mvv 4,212,52
mvv 8,216,52
D 83,0
mvv 12,220,52
s_waitcnt lgkmcnt(1)
mvv 0,224,56
R 36,33792
mvv 4,228,56
mvv 8,232,56
D 83,1024
mvv 12,236,56
mvv 0,240,60
R 40,34816
mvv 4,244,60
mvv 8,248,60
D 83,2048
mvv 12,252,60
s_waitcnt lgkmcnt(1)
v_mfma_f32_16x16x32_bf16 v[16:19], a[0:3], v[32:35], 0
R 44,35840
v_cvt_pk_bf16_f32 v64, v0, v1
v_mfma_f32_16x16x32_bf16 v[20:23], a[4:7], v[32:35], 0
v_cvt_pk_bf16_f32 v65, v2, v3
v_cvt_pk_bf16_f32 v66, v4, v5
v_mfma_f32_16x16x32_bf16 v[24:27], a[8:11], v[32:35], 0
D 83,3072
v_mfma_f32_16x16x32_bf16 v[28:31], a[12:15], v[32:35], 0
s_add_u32 m0, s50, 4096
v_cvt_pk_bf16_f32 v67, v6, v7
v_cvt_pk_bf16_f32 v68, v8, v9
mav 16,16,36
R 48,36864
v_cvt_pk_bf16_f32 v69, v10, v11
mav 20,20,36
v_cvt_pk_bf16_f32 v70, v12, v13
v_cvt_pk_bf16_f32 v71, v14, v15
mav 24,24,36
buffer_store_dwordx4 v[64:67], v87, s[36:39], s49 offen sc1
D 84,0
mav 28,28,36
s_waitcnt lgkmcnt(1)
mav 16,32,40
R 52,37888
mav 20,36,40
mav 24,40,40
buffer_store_dwordx4 v[68:71], v87, s[36:39], s49 offen offset:1024 sc1
D 84,1024
mav 28,44,40
mav 16,48,44
R 56,38912
mav 20,52,44
mav 24,56,44
mav 28,60,44
s_waitcnt lgkmcnt(1)
mav 16,64,48
R 60,39936
mav 20,68,48
mav 24,72,48
mav 28,76,48
mav 16,80,52
R 32,40960
mav 20,84,52
mav 24,88,52
mav 28,92,52
s_waitcnt lgkmcnt(1)
mav 16,96,56
R 36,41984
mav 20,100,56
mav 24,104,56
mav 28,108,56
mav 16,112,60
R 40,43008
mav 20,116,60
mav 24,120,60
mav 28,124,60
s_waitcnt lgkmcnt(1)
mav 16,128,32
R 44,44032
mav 20,132,32
mav 24,136,32
mav 28,140,32
mav 16,144,36
R 48,45056
v_add_u32 v72, 1, v72
mav 20,148,36
mav 24,152,36
mav 28,156,36
s_waitcnt lgkmcnt(1)
mav 16,160,40
R 52,46080
mav 20,164,40
mav 24,168,40
mav 28,172,40
mav 16,176,44
R 56,47104
mav 20,180,44
mav 24,184,44
mav 28,188,44
s_waitcnt lgkmcnt(1)
mav 16,192,48
R 60,48128
mav 20,196,48
mav 24,200,48
s_mov_b32 s60, -30
.p2align 6
LG2_1:
LsG8:
s_cbranch_execz LsG12
mav 28,204,48
mav 16,208,52
R 32,49152
mav 20,212,52
mav 24,216,52
mav 28,220,52
s_waitcnt lgkmcnt(1)
mav 16,224,56
R 36,50176
mav 20,228,56
mav 24,232,56
mav 28,236,56
mav 16,240,60
s_waitcnt vmcnt(1)
R 40,51200
mav 20,244,60
mav 24,248,60
buffer_store_dword v72, v81, s[36:39], s52 offen sc1
mav 28,252,60
s_waitcnt lgkmcnt(1)
mvv 16,128,32
R 44,52224
mvv 20,132,32
mvv 24,136,32
mvv 28,140,32
mvv 16,144,36
R 48,53248
mvv 20,148,36
mvv 24,152,36
mvv 28,156,36
s_waitcnt lgkmcnt(1)
mvv 16,160,40
R 52,54272
mvv 20,164,40
mvv 24,168,40
mvv 28,172,40
mvv 16,176,44
R 56,55296
mvv 20,180,44
mvv 24,184,44
mvv 28,188,44
s_waitcnt lgkmcnt(1)
mvv 16,192,48
R 60,56320
mvv 20,196,48
v_xor_b32 v80, 0x10000, v80
mvv 24,200,48
s_waitcnt vmcnt(1)
s_add_u32 s49, s49, 313344
mvv 28,204,48
s_barrier
mvv 16,208,52
s_add_u32 m0, s50, 32768
R 32,0
mvv 20,212,52
mvv 24,216,52
D 83,0
mvv 28,220,52
s_waitcnt lgkmcnt(1)
mvv 16,224,56
R 36,1024
mvv 20,228,56
mvv 24,232,56
D 83,1024
mvv 28,236,56
mvv 16,240,60
R 40,2048
mvv 20,244,60
mvv 24,248,60
D 83,2048
mvv 28,252,60
s_waitcnt lgkmcnt(1)
v_mfma_f32_16x16x32_bf16 v[0:3], a[0:3], v[32:35], 0
R 44,3072
v_cvt_pk_bf16_f32 v64, v16, v17
v_mfma_f32_16x16x32_bf16 v[4:7], a[4:7], v[32:35], 0
v_cvt_pk_bf16_f32 v65, v18, v19
v_cvt_pk_bf16_f32 v66, v20, v21
v_mfma_f32_16x16x32_bf16 v[8:11], a[8:11], v[32:35], 0
D 83,3072
v_mfma_f32_16x16x32_bf16 v[12:15], a[12:15], v[32:35], 0
s_add_u32 m0, s50, 36864
v_cvt_pk_bf16_f32 v67, v22, v23
v_cvt_pk_bf16_f32 v68, v24, v25
mav 0,16,36
R 48,4096
v_cvt_pk_bf16_f32 v69, v26, v27
mav 4,20,36
v_cvt_pk_bf16_f32 v70, v28, v29
v_cvt_pk_bf16_f32 v71, v30, v31
mav 8,24,36
buffer_store_dwordx4 v[64:67], v87, s[36:39], s49 offen sc1
D 84,0
mav 12,28,36
s_waitcnt lgkmcnt(1)
mav 0,32,40
R 52,5120
mav 4,36,40
mav 8,40,40
buffer_store_dwordx4 v[68:71], v87, s[36:39], s49 offen offset:1024 sc1
D 84,1024
mav 12,44,40
mav 0,48,44
R 56,6144
mav 4,52,44
mav 8,56,44
mav 12,60,44
s_waitcnt lgkmcnt(1)
mav 0,64,48
R 60,7168
mav 4,68,48
mav 8,72,48
mav 12,76,48
mav 0,80,52
R 32,8192
mav 4,84,52
mav 8,88,52
mav 12,92,52
s_waitcnt lgkmcnt(1)
mav 0,96,56
R 36,9216
mav 4,100,56
mav 8,104,56
mav 12,108,56
mav 0,112,60
R 40,10240
mav 4,116,60
mav 8,120,60
mav 12,124,60
s_waitcnt lgkmcnt(1)
mav 0,128,32
R 44,11264
mav 4,132,32
mav 8,136,32
mav 12,140,32
mav 0,144,36
R 48,12288
v_add_u32 v72, 1, v72
mav 4,148,36
mav 8,152,36
mav 12,156,36
s_waitcnt lgkmcnt(1)
mav 0,160,40
R 52,13312
mav 4,164,40
mav 8,168,40
mav 12,172,40
mav 0,176,44
R 56,14336
mav 4,180,44
mav 8,184,44
mav 12,188,44
s_waitcnt lgkmcnt(1)
mav 0,192,48
R 60,15360
mav 4,196,48
mav 8,200,48
mav 12,204,48
mav 0,208,52
R 32,16384
mav 4,212,52
mav 8,216,52
mav 12,220,52
s_waitcnt lgkmcnt(1)
mav 0,224,56
R 36,17408
mav 4,228,56
mav 8,232,56
mav 12,236,56
mav 0,240,60
s_waitcnt vmcnt(1)
R 40,18432
mav 4,244,60
mav 8,248,60
buffer_store_dword v72, v81, s[36:39], s52 offen sc1
mav 12,252,60
s_waitcnt lgkmcnt(1)
mvv 0,128,32
R 44,19456
mvv 4,132,32
mvv 8,136,32
mvv 12,140,32
mvv 0,144,36
R 48,20480
mvv 4,148,36
mvv 8,152,36
mvv 12,156,36
s_waitcnt lgkmcnt(1)
mvv 0,160,40
R 52,21504
mvv 4,164,40
mvv 8,168,40
mvv 12,172,40
mvv 0,176,44
R 56,22528
mvv 4,180,44
mvv 8,184,44
s_xor_b32 s50, s50, 0x10000
mvv 12,188,44
s_waitcnt lgkmcnt(1)
mvv 0,192,48
R 60,23552
mvv 4,196,48
mvv 8,200,48
s_waitcnt vmcnt(1)
s_add_u32 s49, s49, 313344
mvv 12,204,48
s_barrier
mvv 0,208,52
s_add_u32 m0, s50, 0
R 32,32768
mvv 4,212,52
mvv 8,216,52
D 83,0
mvv 12,220,52
s_waitcnt lgkmcnt(1)
mvv 0,224,56
R 36,33792
mvv 4,228,56
mvv 8,232,56
D 83,1024
mvv 12,236,56
mvv 0,240,60
R 40,34816
mvv 4,244,60
mvv 8,248,60
D 83,2048
mvv 12,252,60
s_waitcnt lgkmcnt(1)
v_mfma_f32_16x16x32_bf16 v[16:19], a[0:3], v[32:35], 0
R 44,35840
v_cvt_pk_bf16_f32 v64, v0, v1
v_mfma_f32_16x16x32_bf16 v[20:23], a[4:7], v[32:35], 0
v_cvt_pk_bf16_f32 v65, v2, v3
v_cvt_pk_bf16_f32 v66, v4, v5
v_mfma_f32_16x16x32_bf16 v[24:27], a[8:11], v[32:35], 0
D 83,3072
v_mfma_f32_16x16x32_bf16 v[28:31], a[12:15], v[32:35], 0
s_add_u32 m0, s50, 4096
v_cvt_pk_bf16_f32 v67, v6, v7
v_cvt_pk_bf16_f32 v68, v8, v9
mav 16,16,36
R 48,36864
v_cvt_pk_bf16_f32 v69, v10, v11
mav 20,20,36
v_cvt_pk_bf16_f32 v70, v12, v13
v_cvt_pk_bf16_f32 v71, v14, v15
mav 24,24,36
buffer_store_dwordx4 v[64:67], v87, s[36:39], s49 offen sc1
D 84,0
mav 28,28,36
s_waitcnt lgkmcnt(1)
mav 16,32,40
R 52,37888
mav 20,36,40
mav 24,40,40
buffer_store_dwordx4 v[68:71], v87, s[36:39], s49 offen offset:1024 sc1
D 84,1024
mav 28,44,40
mav 16,48,44
R 56,38912
mav 20,52,44
mav 24,56,44
mav 28,60,44
s_waitcnt lgkmcnt(1)
mav 16,64,48
R 60,39936
mav 20,68,48
mav 24,72,48
mav 28,76,48
mav 16,80,52
R 32,40960
mav 20,84,52
mav 24,88,52
mav 28,92,52
s_waitcnt lgkmcnt(1)
mav 16,96,56
R 36,41984
mav 20,100,56
mav 24,104,56
mav 28,108,56
mav 16,112,60
R 40,43008
mav 20,116,60
mav 24,120,60
mav 28,124,60
s_waitcnt lgkmcnt(1)
mav 16,128,32
R 44,44032
mav 20,132,32
mav 24,136,32
mav 28,140,32
mav 16,144,36
R 48,45056
v_add_u32 v72, 1, v72
mav 20,148,36
mav 24,152,36
mav 28,156,36
s_waitcnt lgkmcnt(1)
mav 16,160,40
R 52,46080
mav 20,164,40
mav 24,168,40
mav 28,172,40
mav 16,176,44
R 56,47104
mav 20,180,44
mav 24,184,44
mav 28,188,44
s_waitcnt lgkmcnt(1)
mav 16,192,48
R 60,48128
mav 20,196,48
s_add_u32 s60, s60, 1
mav 24,200,48
s_cbranch_scc0 LG2_1
mav 28,204,48
mav 16,208,52
R 32,49152
mav 20,212,52
mav 24,216,52
mav 28,220,52
s_waitcnt lgkmcnt(1)
mav 16,224,56
R 36,50176
mav 20,228,56
mav 24,232,56
mav 28,236,56
mav 16,240,60
s_waitcnt vmcnt(1)
R 40,51200
mav 20,244,60
mav 24,248,60
buffer_store_dword v72, v81, s[36:39], s52 offen sc1
mav 28,252,60
s_waitcnt lgkmcnt(1)
mvv 16,128,32
R 44,52224
mvv 20,132,32
mvv 24,136,32
mvv 28,140,32
mvv 16,144,36
R 48,53248
mvv 20,148,36
mvv 24,152,36
mvv 28,156,36
s_waitcnt lgkmcnt(1)
mvv 16,160,40
R 52,54272
mvv 20,164,40
mvv 24,168,40
mvv 28,172,40
mvv 16,176,44
R 56,55296
mvv 20,180,44
mvv 24,184,44
mvv 28,188,44
s_waitcnt lgkmcnt(1)
mvv 16,192,48
R 60,56320
mvv 20,196,48
v_xor_b32 v80, 0x10000, v80
mvv 24,200,48
s_waitcnt vmcnt(1)
s_add_u32 s49, s49, 313344
mvv 28,204,48
s_barrier
mvv 16,208,52
s_add_u32 m0, s50, 32768
R 32,0
mvv 20,212,52
mvv 24,216,52
D 83,0
mvv 28,220,52
s_waitcnt lgkmcnt(1)
mvv 16,224,56
R 36,1024
mvv 20,228,56
mvv 24,232,56
D 83,1024
mvv 28,236,56
mvv 16,240,60
R 40,2048
mvv 20,244,60
mvv 24,248,60
D 83,2048
mvv 28,252,60
s_waitcnt lgkmcnt(1)
v_mfma_f32_16x16x32_bf16 v[0:3], a[0:3], v[32:35], 0
R 44,3072
v_cvt_pk_bf16_f32 v64, v16, v17
v_mfma_f32_16x16x32_bf16 v[4:7], a[4:7], v[32:35], 0
v_cvt_pk_bf16_f32 v65, v18, v19
v_cvt_pk_bf16_f32 v66, v20, v21
v_mfma_f32_16x16x32_bf16 v[8:11], a[8:11], v[32:35], 0
D 83,3072
v_mfma_f32_16x16x32_bf16 v[12:15], a[12:15], v[32:35], 0
s_add_u32 m0, s50, 36864
v_cvt_pk_bf16_f32 v67, v22, v23
v_cvt_pk_bf16_f32 v68, v24, v25
mav 0,16,36
R 48,4096
v_cvt_pk_bf16_f32 v69, v26, v27
mav 4,20,36
v_cvt_pk_bf16_f32 v70, v28, v29
v_cvt_pk_bf16_f32 v71, v30, v31
mav 8,24,36
buffer_store_dwordx4 v[64:67], v87, s[36:39], s49 offen sc1
D 84,0
mav 12,28,36
s_waitcnt lgkmcnt(1)
mav 0,32,40
R 52,5120
mav 4,36,40
mav 8,40,40
buffer_store_dwordx4 v[68:71], v87, s[36:39], s49 offen offset:1024 sc1
D 84,1024
mav 12,44,40
mav 0,48,44
R 56,6144
mav 4,52,44
mav 8,56,44
mav 12,60,44
s_waitcnt lgkmcnt(1)
mav 0,64,48
R 60,7168
mav 4,68,48
mav 8,72,48
mav 12,76,48
mav 0,80,52
R 32,8192
mav 4,84,52
mav 8,88,52
mav 12,92,52
s_waitcnt lgkmcnt(1)
mav 0,96,56
R 36,9216
mav 4,100,56
mav 8,104,56
mav 12,108,56
mav 0,112,60
R 40,10240
mav 4,116,60
mav 8,120,60
mav 12,124,60
s_waitcnt lgkmcnt(1)
mav 0,128,32
R 44,11264
mav 4,132,32
mav 8,136,32
mav 12,140,32
mav 0,144,36
R 48,12288
v_add_u32 v72, 1, v72
mav 4,148,36
mav 8,152,36
mav 12,156,36
s_waitcnt lgkmcnt(1)
mav 0,160,40
R 52,13312
mav 4,164,40
mav 8,168,40
mav 12,172,40
mav 0,176,44
R 56,14336
mav 4,180,44
mav 8,184,44
mav 12,188,44
s_waitcnt lgkmcnt(1)
mav 0,192,48
R 60,15360
mav 4,196,48
mav 8,200,48
mav 12,204,48
mav 0,208,52
R 32,16384
mav 4,212,52
mav 8,216,52
mav 12,220,52
s_waitcnt lgkmcnt(1)
mav 0,224,56
R 36,17408
mav 4,228,56
mav 8,232,56
mav 12,236,56
mav 0,240,60
s_waitcnt vmcnt(1)
R 40,18432
mav 4,244,60
mav 8,248,60
buffer_store_dword v72, v81, s[36:39], s52 offen sc1
mav 12,252,60
s_waitcnt lgkmcnt(1)
mvv 0,128,32
R 44,19456
mvv 4,132,32
mvv 8,136,32
mvv 12,140,32
mvv 0,144,36
R 48,20480
mvv 4,148,36
mvv 8,152,36
mvv 12,156,36
s_waitcnt lgkmcnt(1)
mvv 0,160,40
R 52,21504
mvv 4,164,40
mvv 8,168,40
mvv 12,172,40
mvv 0,176,44
R 56,22528
mvv 4,180,44
mvv 8,184,44
mvv 12,188,44
s_waitcnt lgkmcnt(1)
mvv 0,192,48
R 60,23552
mvv 4,196,48
mvv 8,200,48
s_waitcnt vmcnt(1)
s_add_u32 s49, s49, 313344
mvv 12,204,48
s_barrier
mvv 0,208,52
R 32,32768
mvv 4,212,52
mvv 8,216,52
mvv 12,220,52
s_waitcnt lgkmcnt(1)
mvv 0,224,56
R 36,33792
mvv 4,228,56
mvv 8,232,56
mvv 12,236,56
mvv 0,240,60
R 40,34816
mvv 4,244,60
mvv 8,248,60
mvv 12,252,60
s_waitcnt lgkmcnt(1)
v_mfma_f32_16x16x32_bf16 v[16:19], a[0:3], v[32:35], 0
R 44,35840
LsG9:
s_cbranch_execz LwG
v_mfma_f32_16x16x32_bf16 v[20:23], a[4:7], v[32:35], 0
v_cvt_pk_bf16_f32 v64, v0, v1
v_cvt_pk_bf16_f32 v65, v2, v3
v_mfma_f32_16x16x32_bf16 v[24:27], a[8:11], v[32:35], 0
v_cvt_pk_bf16_f32 v66, v4, v5
v_cvt_pk_bf16_f32 v67, v6, v7
v_mfma_f32_16x16x32_bf16 v[28:31], a[12:15], v[32:35], 0
v_cvt_pk_bf16_f32 v68, v8, v9
v_cvt_pk_bf16_f32 v69, v10, v11
mav 16,16,36
R 48,36864
v_cvt_pk_bf16_f32 v70, v12, v13
mav 20,20,36
v_cvt_pk_bf16_f32 v71, v14, v15
mav 24,24,36
buffer_store_dwordx4 v[64:67], v87, s[36:39], s49 offen sc1
mav 28,28,36
s_waitcnt lgkmcnt(1)
mav 16,32,40
R 52,37888
mav 20,36,40
mav 24,40,40
buffer_store_dwordx4 v[68:71], v87, s[36:39], s49 offen offset:1024 sc1
mav 28,44,40
mav 16,48,44
R 56,38912
mav 20,52,44
mav 24,56,44
mav 28,60,44
s_waitcnt lgkmcnt(1)
mav 16,64,48
R 60,39936
mav 20,68,48
mav 24,72,48
mav 28,76,48
mav 16,80,52
R 32,40960
mav 20,84,52
mav 24,88,52
mav 28,92,52
s_waitcnt lgkmcnt(1)
mav 16,96,56
R 36,41984
mav 20,100,56
mav 24,104,56
mav 28,108,56
mav 16,112,60
R 40,43008
mav 20,116,60
mav 24,120,60
mav 28,124,60
s_waitcnt lgkmcnt(1)
mav 16,128,32
R 44,44032
mav 20,132,32
mav 24,136,32
mav 28,140,32
mav 16,144,36
R 48,45056
v_add_u32 v72, 1, v72
mav 20,148,36
mav 24,152,36
mav 28,156,36
s_waitcnt lgkmcnt(1)
mav 16,160,40
R 52,46080
mav 20,164,40
mav 24,168,40
mav 28,172,40
mav 16,176,44
R 56,47104
mav 20,180,44
mav 24,184,44
mav 28,188,44
s_waitcnt lgkmcnt(1)
mav 16,192,48
R 60,48128
mav 20,196,48
mav 24,200,48
mav 28,204,48
mav 16,208,52
R 32,49152
mav 20,212,52
mav 24,216,52
mav 28,220,52
s_waitcnt lgkmcnt(1)
mav 16,224,56
R 36,50176
mav 20,228,56
mav 24,232,56
mav 28,236,56
mav 16,240,60
s_waitcnt vmcnt(0)
R 40,51200
mav 20,244,60
mav 24,248,60
buffer_store_dword v72, v81, s[36:39], s52 offen sc1
mav 28,252,60
s_waitcnt lgkmcnt(1)
mvv 16,128,32
R 44,52224
mvv 20,132,32
mvv 24,136,32
mvv 28,140,32
mvv 16,144,36
R 48,53248
mvv 20,148,36
mvv 24,152,36
mvv 28,156,36
s_waitcnt lgkmcnt(1)
mvv 16,160,40
R 52,54272
mvv 20,164,40
mvv 24,168,40
mvv 28,172,40
mvv 16,176,44
R 56,55296
mvv 20,180,44
mvv 24,184,44
mvv 28,188,44
s_waitcnt lgkmcnt(1)
mvv 16,192,48
R 60,56320
mvv 20,196,48
mvv 24,200,48
s_add_u32 s49, s49, 313344
mvv 28,204,48
mvv 16,208,52
mvv 20,212,52
mvv 24,216,52
mvv 28,220,52
s_waitcnt lgkmcnt(0)
mvv 16,224,56
mvv 20,228,56
mvv 24,232,56
mvv 28,236,56
mvv 16,240,60
mvv 20,244,60
mvv 24,248,60
mvv 28,252,60
s_nop 4
v_cvt_pk_bf16_f32 v64, v16, v17
v_cvt_pk_bf16_f32 v65, v18, v19
v_cvt_pk_bf16_f32 v66, v20, v21
v_cvt_pk_bf16_f32 v67, v22, v23
buffer_store_dwordx4 v[64:67], v87, s[36:39], s49 offen sc1
v_cvt_pk_bf16_f32 v68, v24, v25
v_cvt_pk_bf16_f32 v69, v26, v27
v_cvt_pk_bf16_f32 v70, v28, v29
v_cvt_pk_bf16_f32 v71, v30, v31
buffer_store_dwordx4 v[68:71], v87, s[36:39], s49 offen offset:1024 sc1
v_add_u32 v72, 35, v72
s_waitcnt vmcnt(0)
buffer_store_dword v72, v81, s[36:39], s52 offen sc1
s_endpgm
LvG3:
LsG10:
s_cbranch_execz LwG
v_add_u32 v72, 1, v72
s_add_u32 s49, s49, 313344
s_barrier
s_add_u32 m0, s50, 0
buffer_store_dword v72, v81, s[36:39], s52 offen sc1
D 83,0
D 83,1024
D 83,2048
D 83,3072
s_add_u32 m0, s50, 4096
s_nop 0
D 84,0
D 84,1024
v_add_u32 v72, 1, v72
s_waitcnt vmcnt(0)
s_add_u32 s49, s49, 313344
s_barrier
s_add_u32 m0, s50, 32768
R 32,0
buffer_store_dword v72, v81, s[36:39], s52 offen sc1
D 83,0
R 36,1024
D 83,1024
R 40,2048
D 83,2048
s_waitcnt lgkmcnt(1)
v_mfma_f32_16x16x32_bf16 v[0:3], a[0:3], v[32:35], 0
R 44,3072
v_mfma_f32_16x16x32_bf16 v[4:7], a[4:7], v[32:35], 0
v_mfma_f32_16x16x32_bf16 v[8:11], a[8:11], v[32:35], 0
v_mfma_f32_16x16x32_bf16 v[12:15], a[12:15], v[32:35], 0
D 83,3072
mav 0,16,36
s_add_u32 m0, s50, 36864
R 48,4096
mav 4,20,36
mav 8,24,36
mav 12,28,36
s_waitcnt lgkmcnt(1)
D 84,0
mav 0,32,40
R 52,5120
mav 4,36,40
mav 8,40,40
mav 12,44,40
D 84,1024
mav 0,48,44
R 56,6144
mav 4,52,44
mav 8,56,44
mav 12,60,44
s_waitcnt lgkmcnt(1)
mav 0,64,48
R 60,7168
mav 4,68,48
mav 8,72,48
mav 12,76,48
mav 0,80,52
R 32,8192
mav 4,84,52
mav 8,88,52
mav 12,92,52
s_waitcnt lgkmcnt(1)
mav 0,96,56
R 36,9216
mav 4,100,56
mav 8,104,56
mav 12,108,56
mav 0,112,60
R 40,10240
mav 4,116,60
mav 8,120,60
mav 12,124,60
s_waitcnt lgkmcnt(1)
mav 0,128,32
R 44,11264
mav 4,132,32
mav 8,136,32
mav 12,140,32
mav 0,144,36
R 48,12288
mav 4,148,36
mav 8,152,36
mav 12,156,36
s_waitcnt lgkmcnt(1)
mav 0,160,40
R 52,13312
mav 4,164,40
mav 8,168,40
mav 12,172,40
mav 0,176,44
R 56,14336
mav 4,180,44
mav 8,184,44
mav 12,188,44
s_waitcnt lgkmcnt(1)
mav 0,192,48
R 60,15360
mav 4,196,48
mav 8,200,48
mav 12,204,48
mav 0,208,52
R 32,16384
mav 4,212,52
mav 8,216,52
mav 12,220,52
s_waitcnt lgkmcnt(1)
mav 0,224,56
R 36,17408
mav 4,228,56
mav 8,232,56
mav 12,236,56
mav 0,240,60
R 40,18432
mav 4,244,60
mav 8,248,60
mav 12,252,60
s_waitcnt lgkmcnt(1)
mvv 0,128,32
R 44,19456
mvv 4,132,32
mvv 8,136,32
mvv 12,140,32
mvv 0,144,36
R 48,20480
mvv 4,148,36
mvv 8,152,36
mvv 12,156,36
s_waitcnt lgkmcnt(1)
mvv 0,160,40
R 52,21504
mvv 4,164,40
mvv 8,168,40
mvv 12,172,40
mvv 0,176,44
R 56,22528
mvv 4,180,44
mvv 8,184,44
s_xor_b32 s50, s50, 0x10000
mvv 12,188,44
s_waitcnt lgkmcnt(1)
mvv 0,192,48
R 60,23552
mvv 4,196,48
mvv 8,200,48
s_waitcnt vmcnt(0)
s_add_u32 s49, s49, 313344
mvv 12,204,48
s_barrier
mvv 0,208,52
s_add_u32 m0, s50, 0
R 32,32768
mvv 4,212,52
mvv 8,216,52
mvv 12,220,52
s_waitcnt lgkmcnt(1)
D 83,0
mvv 0,224,56
R 36,33792
mvv 4,228,56
mvv 8,232,56
mvv 12,236,56
D 83,1024
mvv 0,240,60
R 40,34816
mvv 4,244,60
mvv 8,248,60
mvv 12,252,60
s_waitcnt lgkmcnt(1)
D 83,2048
v_mfma_f32_16x16x32_bf16 v[16:19], a[0:3], v[32:35], 0
R 44,35840
v_cvt_pk_bf16_f32 v64, v0, v1
v_mfma_f32_16x16x32_bf16 v[20:23], a[4:7], v[32:35], 0
v_cvt_pk_bf16_f32 v65, v2, v3
v_cvt_pk_bf16_f32 v66, v4, v5
v_mfma_f32_16x16x32_bf16 v[24:27], a[8:11], v[32:35], 0
v_cvt_pk_bf16_f32 v67, v6, v7
v_cvt_pk_bf16_f32 v68, v8, v9
v_mfma_f32_16x16x32_bf16 v[28:31], a[12:15], v[32:35], 0
buffer_store_dwordx4 v[64:67], v87, s[36:39], s49 offen sc1
D 83,3072
mav 16,16,36
s_add_u32 m0, s50, 4096
R 48,36864
v_cvt_pk_bf16_f32 v69, v10, v11
mav 20,20,36
v_cvt_pk_bf16_f32 v70, v12, v13
v_cvt_pk_bf16_f32 v71, v14, v15
mav 24,24,36
mav 28,28,36
buffer_store_dwordx4 v[68:71], v87, s[36:39], s49 offen offset:1024 sc1
s_waitcnt lgkmcnt(1)
mav 16,32,40
R 52,37888
mav 20,36,40
mav 24,40,40
mav 28,44,40
D 84,0
mav 16,48,44
R 56,38912
mav 20,52,44
mav 24,56,44
mav 28,60,44
s_waitcnt lgkmcnt(1)
D 84,1024
mav 16,64,48
R 60,39936
mav 20,68,48
mav 24,72,48
mav 28,76,48
mav 16,80,52
R 32,40960
mav 20,84,52
mav 24,88,52
mav 28,92,52
s_waitcnt lgkmcnt(1)
mav 16,96,56
R 36,41984
mav 20,100,56
mav 24,104,56
mav 28,108,56
mav 16,112,60
R 40,43008
mav 20,116,60
mav 24,120,60
mav 28,124,60
s_waitcnt lgkmcnt(1)
mav 16,128,32
R 44,44032
mav 20,132,32
mav 24,136,32
mav 28,140,32
mav 16,144,36
R 48,45056
v_add_u32 v72, 1, v72
mav 20,148,36
mav 24,152,36
mav 28,156,36
s_waitcnt lgkmcnt(1)
mav 16,160,40
R 52,46080
mav 20,164,40
mav 24,168,40
mav 28,172,40
mav 16,176,44
R 56,47104
mav 20,180,44
mav 24,184,44
mav 28,188,44
s_waitcnt lgkmcnt(1)
mav 16,192,48
R 60,48128
mav 20,196,48
mav 24,200,48
s_mov_b32 s60, -30
.p2align 6
LG3_1:
mav 28,204,48
mav 16,208,52
R 32,49152
mav 20,212,52
mav 24,216,52
mav 28,220,52
s_waitcnt lgkmcnt(1)
mav 16,224,56
R 36,50176
mav 20,228,56
mav 24,232,56
mav 28,236,56
mav 16,240,60
s_waitcnt vmcnt(2)
R 40,51200
mav 20,244,60
mav 24,248,60
mav 28,252,60
s_waitcnt lgkmcnt(1)
buffer_store_dword v72, v81, s[36:39], s52 offen sc1
mvv 16,128,32
R 44,52224
mvv 20,132,32
mvv 24,136,32
mvv 28,140,32
mvv 16,144,36
R 48,53248
mvv 20,148,36
mvv 24,152,36
mvv 28,156,36
s_waitcnt lgkmcnt(1)
mvv 16,160,40
R 52,54272
mvv 20,164,40
mvv 24,168,40
mvv 28,172,40
mvv 16,176,44
R 56,55296
mvv 20,180,44
mvv 24,184,44
mvv 28,188,44
s_waitcnt lgkmcnt(1)
mvv 16,192,48
R 60,56320
mvv 20,196,48
v_xor_b32 v80, 0x10000, v80
mvv 24,200,48
s_waitcnt vmcnt(1)
s_add_u32 s49, s49, 313344
mvv 28,204,48
s_barrier
mvv 16,208,52
s_add_u32 m0, s50, 32768
R 32,0
mvv 20,212,52
mvv 24,216,52
mvv 28,220,52
s_waitcnt lgkmcnt(1)
D 83,0
mvv 16,224,56
R 36,1024
mvv 20,228,56
mvv 24,232,56
mvv 28,236,56
D 83,1024
mvv 16,240,60
R 40,2048
mvv 20,244,60
mvv 24,248,60
mvv 28,252,60
s_waitcnt lgkmcnt(1)
D 83,2048
v_mfma_f32_16x16x32_bf16 v[0:3], a[0:3], v[32:35], 0
R 44,3072
v_cvt_pk_bf16_f32 v64, v16, v17
v_mfma_f32_16x16x32_bf16 v[4:7], a[4:7], v[32:35], 0
v_cvt_pk_bf16_f32 v65, v18, v19
v_cvt_pk_bf16_f32 v66, v20, v21
v_mfma_f32_16x16x32_bf16 v[8:11], a[8:11], v[32:35], 0
v_cvt_pk_bf16_f32 v67, v22, v23
v_cvt_pk_bf16_f32 v68, v24, v25
v_mfma_f32_16x16x32_bf16 v[12:15], a[12:15], v[32:35], 0
buffer_store_dwordx4 v[64:67], v87, s[36:39], s49 offen sc1
D 83,3072
mav 0,16,36
s_add_u32 m0, s50, 36864
R 48,4096
v_cvt_pk_bf16_f32 v69, v26, v27
mav 4,20,36
v_cvt_pk_bf16_f32 v70, v28, v29
v_cvt_pk_bf16_f32 v71, v30, v31
mav 8,24,36
mav 12,28,36
buffer_store_dwordx4 v[68:71], v87, s[36:39], s49 offen offset:1024 sc1
s_waitcnt lgkmcnt(1)
mav 0,32,40
R 52,5120
mav 4,36,40
mav 8,40,40
mav 12,44,40
D 84,0
mav 0,48,44
R 56,6144
mav 4,52,44
mav 8,56,44
mav 12,60,44
s_waitcnt lgkmcnt(1)
D 84,1024
mav 0,64,48
R 60,7168
mav 4,68,48
mav 8,72,48
mav 12,76,48
mav 0,80,52
R 32,8192
mav 4,84,52
mav 8,88,52
mav 12,92,52
s_waitcnt lgkmcnt(1)
mav 0,96,56
R 36,9216
mav 4,100,56
mav 8,104,56
mav 12,108,56
mav 0,112,60
R 40,10240
LsG11:
s_cbranch_execz LwG
mav 4,116,60
mav 8,120,60
mav 12,124,60
s_waitcnt lgkmcnt(1)
mav 0,128,32
R 44,11264
mav 4,132,32
mav 8,136,32
mav 12,140,32
mav 0,144,36
R 48,12288
v_add_u32 v72, 1, v72
mav 4,148,36
mav 8,152,36
mav 12,156,36
s_waitcnt lgkmcnt(1)
mav 0,160,40
R 52,13312
mav 4,164,40
mav 8,168,40
mav 12,172,40
mav 0,176,44
R 56,14336
mav 4,180,44
mav 8,184,44
mav 12,188,44
s_waitcnt lgkmcnt(1)
mav 0,192,48
R 60,15360
mav 4,196,48
mav 8,200,48
mav 12,204,48
mav 0,208,52
R 32,16384
mav 4,212,52
mav 8,216,52
mav 12,220,52
s_waitcnt lgkmcnt(1)
mav 0,224,56
R 36,17408
mav 4,228,56
mav 8,232,56
mav 12,236,56
mav 0,240,60
s_waitcnt vmcnt(2)
R 40,18432
mav 4,244,60
mav 8,248,60
mav 12,252,60
s_waitcnt lgkmcnt(1)
buffer_store_dword v72, v81, s[36:39], s52 offen sc1
mvv 0,128,32
R 44,19456
mvv 4,132,32
mvv 8,136,32
mvv 12,140,32
mvv 0,144,36
R 48,20480
mvv 4,148,36
mvv 8,152,36
mvv 12,156,36
s_waitcnt lgkmcnt(1)
mvv 0,160,40
R 52,21504
mvv 4,164,40
mvv 8,168,40
mvv 12,172,40
mvv 0,176,44
R 56,22528
mvv 4,180,44
mvv 8,184,44
s_xor_b32 s50, s50, 0x10000
mvv 12,188,44
s_waitcnt lgkmcnt(1)
mvv 0,192,48
R 60,23552
mvv 4,196,48
mvv 8,200,48
s_waitcnt vmcnt(1)
s_add_u32 s49, s49, 313344
mvv 12,204,48
s_barrier
mvv 0,208,52
s_add_u32 m0, s50, 0
R 32,32768
mvv 4,212,52
mvv 8,216,52
mvv 12,220,52
s_waitcnt lgkmcnt(1)
D 83,0
mvv 0,224,56
R 36,33792
mvv 4,228,56
mvv 8,232,56
mvv 12,236,56
D 83,1024
mvv 0,240,60
R 40,34816
mvv 4,244,60
mvv 8,248,60
mvv 12,252,60
s_waitcnt lgkmcnt(1)
D 83,2048
v_mfma_f32_16x16x32_bf16 v[16:19], a[0:3], v[32:35], 0
R 44,35840
v_cvt_pk_bf16_f32 v64, v0, v1
v_mfma_f32_16x16x32_bf16 v[20:23], a[4:7], v[32:35], 0
v_cvt_pk_bf16_f32 v65, v2, v3
v_cvt_pk_bf16_f32 v66, v4, v5
v_mfma_f32_16x16x32_bf16 v[24:27], a[8:11], v[32:35], 0
v_cvt_pk_bf16_f32 v67, v6, v7
v_cvt_pk_bf16_f32 v68, v8, v9
v_mfma_f32_16x16x32_bf16 v[28:31], a[12:15], v[32:35], 0
buffer_store_dwordx4 v[64:67], v87, s[36:39], s49 offen sc1
D 83,3072
mav 16,16,36
s_add_u32 m0, s50, 4096
R 48,36864
v_cvt_pk_bf16_f32 v69, v10, v11
mav 20,20,36
v_cvt_pk_bf16_f32 v70, v12, v13
v_cvt_pk_bf16_f32 v71, v14, v15
mav 24,24,36
mav 28,28,36
buffer_store_dwordx4 v[68:71], v87, s[36:39], s49 offen offset:1024 sc1
s_waitcnt lgkmcnt(1)
mav 16,32,40
R 52,37888
)"
R"(
mav 20,36,40
mav 24,40,40
mav 28,44,40
D 84,0
mav 16,48,44
R 56,38912
mav 20,52,44
mav 24,56,44
mav 28,60,44
s_waitcnt lgkmcnt(1)
D 84,1024
mav 16,64,48
R 60,39936
mav 20,68,48
mav 24,72,48
mav 28,76,48
mav 16,80,52
R 32,40960
mav 20,84,52
mav 24,88,52
mav 28,92,52
s_waitcnt lgkmcnt(1)
mav 16,96,56
R 36,41984
mav 20,100,56
mav 24,104,56
mav 28,108,56
mav 16,112,60
R 40,43008
mav 20,116,60
mav 24,120,60
mav 28,124,60
s_waitcnt lgkmcnt(1)
mav 16,128,32
R 44,44032
mav 20,132,32
mav 24,136,32
mav 28,140,32
mav 16,144,36
R 48,45056
v_add_u32 v72, 1, v72
mav 20,148,36
mav 24,152,36
mav 28,156,36
s_waitcnt lgkmcnt(1)
mav 16,160,40
R 52,46080
mav 20,164,40
mav 24,168,40
mav 28,172,40
mav 16,176,44
R 56,47104
mav 20,180,44
mav 24,184,44
mav 28,188,44
s_waitcnt lgkmcnt(1)
mav 16,192,48
R 60,48128
mav 20,196,48
s_add_u32 s60, s60, 1
mav 24,200,48
s_cbranch_scc0 LG3_1
mav 28,204,48
mav 16,208,52
R 32,49152
mav 20,212,52
mav 24,216,52
mav 28,220,52
s_waitcnt lgkmcnt(1)
mav 16,224,56
R 36,50176
mav 20,228,56
mav 24,232,56
mav 28,236,56
mav 16,240,60
s_waitcnt vmcnt(2)
R 40,51200
mav 20,244,60
mav 24,248,60
mav 28,252,60
s_waitcnt lgkmcnt(1)
buffer_store_dword v72, v81, s[36:39], s52 offen sc1
mvv 16,128,32
R 44,52224
mvv 20,132,32
mvv 24,136,32
mvv 28,140,32
mvv 16,144,36
R 48,53248
mvv 20,148,36
mvv 24,152,36
mvv 28,156,36
s_waitcnt lgkmcnt(1)
mvv 16,160,40
R 52,54272
mvv 20,164,40
mvv 24,168,40
mvv 28,172,40
mvv 16,176,44
R 56,55296
mvv 20,180,44
mvv 24,184,44
mvv 28,188,44
s_waitcnt lgkmcnt(1)
mvv 16,192,48
R 60,56320
mvv 20,196,48
v_xor_b32 v80, 0x10000, v80
mvv 24,200,48
s_waitcnt vmcnt(1)
s_add_u32 s49, s49, 313344
mvv 28,204,48
s_barrier
mvv 16,208,52
s_add_u32 m0, s50, 32768
R 32,0
mvv 20,212,52
mvv 24,216,52
mvv 28,220,52
s_waitcnt lgkmcnt(1)
D 83,0
mvv 16,224,56
R 36,1024
mvv 20,228,56
mvv 24,232,56
mvv 28,236,56
D 83,1024
mvv 16,240,60
R 40,2048
mvv 20,244,60
mvv 24,248,60
mvv 28,252,60
s_waitcnt lgkmcnt(1)
D 83,2048
v_mfma_f32_16x16x32_bf16 v[0:3], a[0:3], v[32:35], 0
R 44,3072
v_cvt_pk_bf16_f32 v64, v16, v17
v_mfma_f32_16x16x32_bf16 v[4:7], a[4:7], v[32:35], 0
v_cvt_pk_bf16_f32 v65, v18, v19
v_cvt_pk_bf16_f32 v66, v20, v21
v_mfma_f32_16x16x32_bf16 v[8:11], a[8:11], v[32:35], 0
v_cvt_pk_bf16_f32 v67, v22, v23
v_cvt_pk_bf16_f32 v68, v24, v25
v_mfma_f32_16x16x32_bf16 v[12:15], a[12:15], v[32:35], 0
buffer_store_dwordx4 v[64:67], v87, s[36:39], s49 offen sc1
D 83,3072
mav 0,16,36
s_add_u32 m0, s50, 36864
R 48,4096
v_cvt_pk_bf16_f32 v69, v26, v27
mav 4,20,36
v_cvt_pk_bf16_f32 v70, v28, v29
v_cvt_pk_bf16_f32 v71, v30, v31
mav 8,24,36
mav 12,28,36
buffer_store_dwordx4 v[68:71], v87, s[36:39], s49 offen offset:1024 sc1
s_waitcnt lgkmcnt(1)
mav 0,32,40
R 52,5120
mav 4,36,40
mav 8,40,40
mav 12,44,40
D 84,0
mav 0,48,44
R 56,6144
mav 4,52,44
mav 8,56,44
mav 12,60,44
s_waitcnt lgkmcnt(1)
D 84,1024
mav 0,64,48
R 60,7168
mav 4,68,48
mav 8,72,48
mav 12,76,48
mav 0,80,52
R 32,8192
mav 4,84,52
mav 8,88,52
mav 12,92,52
s_waitcnt lgkmcnt(1)
mav 0,96,56
R 36,9216
mav 4,100,56
mav 8,104,56
mav 12,108,56
mav 0,112,60
R 40,10240
mav 4,116,60
mav 8,120,60
mav 12,124,60
s_waitcnt lgkmcnt(1)
mav 0,128,32
R 44,11264
mav 4,132,32
mav 8,136,32
mav 12,140,32
mav 0,144,36
R 48,12288
v_add_u32 v72, 1, v72
mav 4,148,36
mav 8,152,36
mav 12,156,36
s_waitcnt lgkmcnt(1)
mav 0,160,40
R 52,13312
mav 4,164,40
mav 8,168,40
mav 12,172,40
mav 0,176,44
R 56,14336
mav 4,180,44
mav 8,184,44
mav 12,188,44
s_waitcnt lgkmcnt(1)
mav 0,192,48
R 60,15360
mav 4,196,48
mav 8,200,48
mav 12,204,48
mav 0,208,52
R 32,16384
mav 4,212,52
mav 8,216,52
mav 12,220,52
s_waitcnt lgkmcnt(1)
mav 0,224,56
R 36,17408
mav 4,228,56
mav 8,232,56
mav 12,236,56
mav 0,240,60
s_waitcnt vmcnt(2)
R 40,18432
mav 4,244,60
mav 8,248,60
mav 12,252,60
s_waitcnt lgkmcnt(1)
buffer_store_dword v72, v81, s[36:39], s52 offen sc1
mvv 0,128,32
R 44,19456
mvv 4,132,32
mvv 8,136,32
mvv 12,140,32
mvv 0,144,36
R 48,20480
mvv 4,148,36
mvv 8,152,36
mvv 12,156,36
s_waitcnt lgkmcnt(1)
mvv 0,160,40
R 52,21504
mvv 4,164,40
mvv 8,168,40
mvv 12,172,40
mvv 0,176,44
R 56,22528
mvv 4,180,44
mvv 8,184,44
mvv 12,188,44
s_waitcnt lgkmcnt(1)
mvv 0,192,48
R 60,23552
mvv 4,196,48
mvv 8,200,48
s_waitcnt vmcnt(1)
s_add_u32 s49, s49, 313344
mvv 12,204,48
s_barrier
mvv 0,208,52
R 32,32768
mvv 4,212,52
mvv 8,216,52
mvv 12,220,52
s_waitcnt lgkmcnt(1)
mvv 0,224,56
R 36,33792
mvv 4,228,56
mvv 8,232,56
mvv 12,236,56
mvv 0,240,60
R 40,34816
mvv 4,244,60
mvv 8,248,60
mvv 12,252,60
s_waitcnt lgkmcnt(1)
v_mfma_f32_16x16x32_bf16 v[16:19], a[0:3], v[32:35], 0
R 44,35840
v_mfma_f32_16x16x32_bf16 v[20:23], a[4:7], v[32:35], 0
v_cvt_pk_bf16_f32 v64, v0, v1
v_cvt_pk_bf16_f32 v65, v2, v3
v_mfma_f32_16x16x32_bf16 v[24:27], a[8:11], v[32:35], 0
v_cvt_pk_bf16_f32 v66, v4, v5
v_cvt_pk_bf16_f32 v67, v6, v7
v_mfma_f32_16x16x32_bf16 v[28:31], a[12:15], v[32:35], 0
buffer_store_dwordx4 v[64:67], v87, s[36:39], s49 offen sc1
v_cvt_pk_bf16_f32 v68, v8, v9
v_cvt_pk_bf16_f32 v69, v10, v11
v_cvt_pk_bf16_f32 v70, v12, v13
mav 16,16,36
R 48,36864
v_cvt_pk_bf16_f32 v71, v14, v15
mav 20,20,36
mav 24,24,36
mav 28,28,36
buffer_store_dwordx4 v[68:71], v87, s[36:39], s49 offen offset:1024 sc1
s_waitcnt lgkmcnt(1)
mav 16,32,40
R 52,37888
mav 20,36,40
mav 24,40,40
mav 28,44,40
mav 16,48,44
R 56,38912
mav 20,52,44
mav 24,56,44
mav 28,60,44
s_waitcnt lgkmcnt(1)
mav 16,64,48
R 60,39936
mav 20,68,48
mav 24,72,48
mav 28,76,48
mav 16,80,52
R 32,40960
mav 20,84,52
mav 24,88,52
mav 28,92,52
s_waitcnt lgkmcnt(1)
mav 16,96,56
R 36,41984
mav 20,100,56
mav 24,104,56
mav 28,108,56
mav 16,112,60
R 40,43008
mav 20,116,60
mav 24,120,60
mav 28,124,60
s_waitcnt lgkmcnt(1)
mav 16,128,32
R 44,44032
mav 20,132,32
mav 24,136,32
mav 28,140,32
mav 16,144,36
R 48,45056
v_add_u32 v72, 1, v72
mav 20,148,36
mav 24,152,36
mav 28,156,36
s_waitcnt lgkmcnt(1)
mav 16,160,40
R 52,46080
mav 20,164,40
mav 24,168,40
mav 28,172,40
mav 16,176,44
R 56,47104
mav 20,180,44
mav 24,184,44
mav 28,188,44
s_waitcnt lgkmcnt(1)
mav 16,192,48
R 60,48128
mav 20,196,48
mav 24,200,48
mav 28,204,48
mav 16,208,52
R 32,49152
mav 20,212,52
mav 24,216,52
mav 28,220,52
s_waitcnt lgkmcnt(1)
mav 16,224,56
R 36,50176
mav 20,228,56
mav 24,232,56
mav 28,236,56
mav 16,240,60
s_waitcnt vmcnt(0)
R 40,51200
mav 20,244,60
mav 24,248,60
mav 28,252,60
s_waitcnt lgkmcnt(1)
buffer_store_dword v72, v81, s[36:39], s52 offen sc1
mvv 16,128,32
R 44,52224
mvv 20,132,32
mvv 24,136,32
mvv 28,140,32
mvv 16,144,36
R 48,53248
mvv 20,148,36
mvv 24,152,36
mvv 28,156,36
s_waitcnt lgkmcnt(1)
mvv 16,160,40
R 52,54272
mvv 20,164,40
mvv 24,168,40
mvv 28,172,40
mvv 16,176,44
R 56,55296
mvv 20,180,44
mvv 24,184,44
mvv 28,188,44
s_waitcnt lgkmcnt(1)
mvv 16,192,48
R 60,56320
mvv 20,196,48
mvv 24,200,48
s_add_u32 s49, s49, 313344
LsG12:
s_cbranch_execz LwG
mvv 28,204,48
mvv 16,208,52
mvv 20,212,52
mvv 24,216,52
mvv 28,220,52
s_waitcnt lgkmcnt(0)
mvv 16,224,56
mvv 20,228,56
mvv 24,232,56
mvv 28,236,56
mvv 16,240,60
mvv 20,244,60
mvv 24,248,60
mvv 28,252,60
s_nop 4
v_cvt_pk_bf16_f32 v64, v16, v17
v_cvt_pk_bf16_f32 v65, v18, v19
v_cvt_pk_bf16_f32 v66, v20, v21
v_cvt_pk_bf16_f32 v67, v22, v23
buffer_store_dwordx4 v[64:67], v87, s[36:39], s49 offen sc1
v_cvt_pk_bf16_f32 v68, v24, v25
v_cvt_pk_bf16_f32 v69, v26, v27
v_cvt_pk_bf16_f32 v70, v28, v29
v_cvt_pk_bf16_f32 v71, v30, v31
buffer_store_dwordx4 v[68:71], v87, s[36:39], s49 offen offset:1024 sc1
v_add_u32 v72, 35, v72
s_waitcnt vmcnt(0)
buffer_store_dword v72, v81, s[36:39], s52 offen sc1
s_endpgm
Lr6:
s_waitcnt vmcnt(0)
v_accvgpr_read_b32 v73, a255
v_mov_b32 v72, 0x100
v_add_u32 v73, 0x9e3779b9, v73
buffer_store_dwordx2 v[72:73], v81, s[36:39], s52 offen sc1
v_add_u32 v85, v85, v72
s_sleep 32
s_sleep 32
s_sleep 32
s_sleep 32
E 0,65536
E 1,69632
E 2,73728
E 3,77824
s_add_u32 s51, s51, 65536
E 0,81920
E 1,86016
E 2,90112
E 3,94208
s_add_u32 s51, s51, 65536
E 0,98304
E 1,102400
E 2,106496
E 3,110592
s_add_u32 s51, s51, 65536
E 0,114688
E 1,118784
E 2,122880
E 3,126976
s_add_u32 s51, s51, 65536
s_cmp_ge_u32 s51, s57
s_cselect_b32 s60, 1048576, 0
s_sub_u32 s51, s51, s60
s_mov_b64 exec, 0
s_cmp_eq_u32 s53, 0
s_cbranch_scc1 LsR0
s_cmp_eq_u32 s53, 1024
s_cbranch_scc1 LsR1
s_cmp_eq_u32 s53, 2048
s_cbranch_scc1 LsR2
s_branch LsR3
LwR:
s_mov_b64 exec, -1
s_waitcnt vmcnt(8)
s_barrier
v_add_u32 v4, 65536, v4
v_add_u32 v5, 65536, v5
v_add_u32 v6, 65536, v6
v_add_u32 v7, 65536, v7
Ta 0,4,0
Ta 4,5,0
Ta 8,6,0
Ta 12,7,0
Ta 16,4,16384
Ta 20,5,16384
Ta 24,6,16384
Ta 28,7,16384
E 0,131072
E 1,135168
E 2,139264
E 3,143360
s_add_u32 s51, s51, 65536
E 0,147456
E 1,151552
E 2,155648
E 3,159744
s_add_u32 s51, s51, 65536
s_waitcnt lgkmcnt(0)
s_waitcnt vmcnt(8)
s_barrier
E 0,65536
Ta 32,4,32768
Ta 36,5,32768
E 1,69632
Ta 40,6,32768
Ta 44,7,32768
E 2,73728
Ta 48,4,49152
E 3,77824
s_add_u32 s51, s51, 65536
Ta 52,5,49152
Ta 56,6,49152
E 0,81920
Ta 60,7,49152
E 1,86016
E 2,90112
E 3,94208
s_add_u32 s51, s51, 65536
s_cmp_ge_u32 s51, s57
s_cselect_b32 s60, 1048576, 0
s_sub_u32 s51, s51, s60
s_waitcnt lgkmcnt(0)
s_waitcnt vmcnt(8)
s_barrier
v_add_u32 v4, 65536, v4
v_add_u32 v5, 65536, v5
v_add_u32 v6, 65536, v6
v_add_u32 v7, 65536, v7
E 0,98304
Ta 64,4,0
Ta 68,5,0
E 1,102400
Ta 72,6,0
Ta 76,7,0
E 2,106496
Ta 80,4,16384
E 3,110592
s_add_u32 s51, s51, 65536
Ta 84,5,16384
Ta 88,6,16384
E 0,114688
Ta 92,7,16384
E 1,118784
E 2,122880
E 3,126976
s_add_u32 s51, s51, 65536
s_waitcnt lgkmcnt(0)
s_waitcnt vmcnt(8)
s_barrier
v_add_u32 v4, -65536, v4
v_add_u32 v5, -65536, v5
v_add_u32 v6, -65536, v6
v_add_u32 v7, -65536, v7
E 0,131072
Ta 96,4,0
Ta 100,5,0
E 1,135168
Ta 104,6,0
Ta 108,7,0
E 2,139264
Ta 112,4,16384
E 3,143360
s_add_u32 s51, s51, 65536
Ta 116,5,16384
Ta 120,6,16384
E 0,147456
Ta 124,7,16384
E 1,151552
E 2,155648
E 3,159744
s_add_u32 s51, s51, 65536
s_cmp_ge_u32 s51, s57
s_cselect_b32 s60, 1048576, 0
s_sub_u32 s51, s51, s60
s_waitcnt lgkmcnt(0)
s_waitcnt vmcnt(8)
s_barrier
E 0,65536
Ta 128,4,32768
Ta 132,5,32768
E 1,69632
Ta 136,6,32768
Ta 140,7,32768
E 2,73728
Ta 144,4,49152
E 3,77824
s_add_u32 s51, s51, 65536
Ta 148,5,49152
Ta 152,6,49152
E 0,81920
Ta 156,7,49152
E 1,86016
E 2,90112
E 3,94208
s_add_u32 s51, s51, 65536
s_waitcnt lgkmcnt(0)
s_waitcnt vmcnt(8)
s_barrier
v_add_u32 v4, 65536, v4
v_add_u32 v5, 65536, v5
v_add_u32 v6, 65536, v6
v_add_u32 v7, 65536, v7
E 0,98304
Ta 160,4,0
Ta 164,5,0
E 1,102400
Ta 168,6,0
Ta 172,7,0
E 2,106496
Ta 176,4,16384
E 3,110592
s_add_u32 s51, s51, 65536
Ta 180,5,16384
Ta 184,6,16384
E 0,114688
Ta 188,7,16384
E 1,118784
E 2,122880
E 3,126976
s_add_u32 s51, s51, 65536
s_cmp_ge_u32 s51, s57
s_cselect_b32 s60, 1048576, 0
s_sub_u32 s51, s51, s60
s_waitcnt lgkmcnt(0)
s_waitcnt vmcnt(8)
s_barrier
v_add_u32 v4, -65536, v4
v_add_u32 v5, -65536, v5
v_add_u32 v6, -65536, v6
v_add_u32 v7, -65536, v7
Ta 192,4,0
Ta 196,5,0
Ta 200,6,0
Ta 204,7,0
Ta 208,4,16384
Ta 212,5,16384
Ta 216,6,16384
Ta 220,7,16384
s_waitcnt lgkmcnt(0)
s_waitcnt vmcnt(0)
s_barrier
Ta 224,4,32768
Ta 228,5,32768
Ta 232,6,32768
Ta 236,7,32768
Ta 240,4,49152
Ta 244,5,49152
Ta 248,6,49152
Ta 252,7,49152
s_waitcnt lgkmcnt(0)
s_waitcnt vmcnt(0)
s_barrier
s_nop 7
s_cmp_eq_u32 s52, 36760
s_cbranch_scc1 Ct
Bt:
s_and_b32 s60, s48, 3
s_cmp_eq_u32 s60, 1
s_cbranch_scc1 LvR1
s_cmp_eq_u32 s60, 2
s_cbranch_scc1 LvR2
s_cmp_eq_u32 s60, 3
s_cbranch_scc1 LvR3
LvR0:
LsR0:
s_cbranch_execz LsR4
buffer_load_dwordx2 v[76:77], v84, s[36:39], 0 offen sc1
s_waitcnt vmcnt(0)
ds_write_b32 v82, v76
v_sub_u32 v76, v76, v85
v_xor_b32 v77, v77, v73
v_cmp_lt_u64 vcc, s[62:63], v[76:77]
s_waitcnt lgkmcnt(0)
s_cbranch_vccnz CR0_1
BR0_1:
s_add_u32 s49, s49, 313344
s_barrier
s_mov_b32 m0, 144384
s_nop 0
buffer_load_dword v84, s[36:39], 0 offen sc1 lds
s_add_u32 m0, s50, 0
s_nop 0
D 83,0
D 83,1024
v_add_u32 v85, 1, v85
D 83,2048
D 83,3072
ds_read_b32 v76, v82 offset:0
s_waitcnt lgkmcnt(0)
v_cmp_gt_i32 vcc, v85, v76
s_add_u32 s49, s49, 313344
s_cbranch_vccnz CR0_2
BR0_2:
s_barrier
s_mov_b32 m0, 144384
s_nop 0
buffer_load_dword v84, s[36:39], 0 offen sc1 lds
s_add_u32 m0, s50, 32768
s_nop 0
D 83,0
D 83,1024
v_add_u32 v85, 1, v85
D 83,2048
D 83,3072
buffer_load_dwordx4 v[124:127], v90, s[36:39], s49 offen sc1
ds_read_b32 v76, v82 offset:0
buffer_load_dwordx4 v[128:131], v90, s[36:39], s49 offen offset:1024 sc1
s_xor_b32 s50, s50, 0x10000
s_waitcnt lgkmcnt(0)
v_cmp_gt_i32 vcc, v85, v76
s_waitcnt vmcnt(7)
s_add_u32 s49, s49, 313344
s_cbranch_vccnz CR0_3
BR0_3:
s_barrier
R 32,0
buffer_load_dwordx4 v[132:135], v86, s[36:39], s49 offen sc1
R 36,1024
buffer_load_dwordx4 v[136:139], v86, s[36:39], s49 offen offset:1024 sc1
R 40,2048
buffer_load_dwordx4 v[140:143], v87, s[36:39], s49 offen sc1
s_waitcnt lgkmcnt(1)
v_mfma_f32_16x16x32_bf16 v[0:3], a[0:3], v[32:35], v[100:103]
R 44,3072
v_mfma_f32_16x16x32_bf16 v[4:7], a[4:7], v[32:35], v[104:107]
buffer_load_dwordx4 v[144:147], v87, s[36:39], s49 offen offset:1024 sc1
v_mfma_f32_16x16x32_bf16 v[8:11], a[8:11], v[32:35], v[108:111]
v_mfma_f32_16x16x32_bf16 v[12:15], a[12:15], v[32:35], v[112:115]
mav 0,16,36
R 48,4096
mav 4,20,36
buffer_load_dwordx4 v[148:151], v88, s[36:39], s49 offen sc1
mav 8,24,36
mav 12,28,36
s_waitcnt lgkmcnt(1)
mav 0,32,40
R 52,5120
mav 4,36,40
buffer_load_dwordx4 v[152:155], v88, s[36:39], s49 offen offset:1024 sc1
mav 8,40,40
mav 12,44,40
mav 0,48,44
R 56,6144
mav 4,52,44
buffer_load_dwordx4 v[156:159], v89, s[36:39], s49 offen sc1
mav 8,56,44
mav 12,60,44
s_waitcnt lgkmcnt(1)
mav 0,64,48
R 60,7168
mav 4,68,48
buffer_load_dwordx4 v[160:163], v89, s[36:39], s49 offen offset:1024 sc1
mav 8,72,48
mav 12,76,48
s_mov_b32 m0, 144384
mav 0,80,52
R 32,8192
mav 4,84,52
mav 8,88,52
buffer_load_dword v84, s[36:39], 0 offen sc1 lds
mav 12,92,52
s_waitcnt lgkmcnt(1)
s_add_u32 m0, s50, 0
mav 0,96,56
R 36,9216
mav 4,100,56
D 83,0
mav 8,104,56
mav 12,108,56
mav 0,112,60
R 40,10240
mav 4,116,60
D 83,1024
mav 8,120,60
mav 12,124,60
s_waitcnt lgkmcnt(1)
mav 0,128,32
R 44,11264
mav 4,132,32
mav 8,136,32
mav 12,140,32
mav 0,144,36
R 48,12288
mav 4,148,36
mav 8,152,36
mav 12,156,36
s_waitcnt lgkmcnt(1)
mav 0,160,40
R 52,13312
v_add_u32 v85, 1, v85
mav 4,164,40
mav 8,168,40
mav 12,172,40
mav 0,176,44
R 56,14336
mav 4,180,44
D 83,2048
mav 8,184,44
mav 12,188,44
s_waitcnt lgkmcnt(1)
mav 0,192,48
R 60,15360
mav 4,196,48
D 83,3072
mav 8,200,48
mav 12,204,48
LsR1:
s_cbranch_execz LsR5
mav 0,208,52
mav 4,212,52
mav 8,216,52
mav 12,220,52
s_waitcnt lgkmcnt(0)
mav 0,224,56
mav 4,228,56
mav 8,232,56
mav 12,236,56
mav 0,240,60
mav 4,244,60
buffer_load_dwordx4 v[164:167], v90, s[36:39], s49 offen sc1
mav 8,248,60
ds_read_b32 v76, v82 offset:0
mav 12,252,60
s_waitcnt vmcnt(15)
mvv 0,116,124
mvv 4,120,124
s_waitcnt vmcnt(14)
buffer_load_dwordx4 v[168:171], v90, s[36:39], s49 offen offset:1024 sc1
mvv 8,116,128
mvv 12,120,128
s_waitcnt vmcnt(14)
mvv 0,116,132
mvv 4,120,132
s_waitcnt vmcnt(13) lgkmcnt(0)
v_cmp_gt_i32 vcc, v85, v76
mvv 8,116,136
s_waitcnt vmcnt(12)
s_add_u32 s49, s49, 313344
mvv 12,120,136
s_cbranch_vccnz CR0_4
BR0_4:
s_barrier
mvv 0,116,140
R 32,32768
mvv 4,120,140
s_waitcnt vmcnt(11)
buffer_load_dwordx4 v[172:175], v86, s[36:39], s49 offen sc1
mvv 8,116,144
mvv 12,120,144
s_waitcnt vmcnt(11)
mvv 0,116,148
R 36,33792
mvv 4,120,148
s_waitcnt vmcnt(10)
buffer_load_dwordx4 v[176:179], v86, s[36:39], s49 offen offset:1024 sc1
mvv 8,116,152
mvv 12,120,152
s_waitcnt vmcnt(10)
mvv 0,116,156
R 40,34816
mvv 4,120,156
s_waitcnt vmcnt(9)
buffer_load_dwordx4 v[180:183], v87, s[36:39], s49 offen sc1
mvv 8,116,160
mvv 12,120,160
s_waitcnt lgkmcnt(1)
v_mfma_f32_16x16x32_bf16 v[16:19], a[0:3], v[32:35], v[100:103]
R 44,35840
v_cvt_pk_bf16_f32 v64, v0, v1
v_mfma_f32_16x16x32_bf16 v[20:23], a[4:7], v[32:35], v[104:107]
buffer_load_dwordx4 v[184:187], v87, s[36:39], s49 offen offset:1024 sc1
v_mfma_f32_16x16x32_bf16 v[24:27], a[8:11], v[32:35], v[108:111]
v_cvt_pk_bf16_f32 v65, v2, v3
v_cvt_pk_bf16_f32 v66, v4, v5
v_mfma_f32_16x16x32_bf16 v[28:31], a[12:15], v[32:35], v[112:115]
v_cvt_pk_bf16_f32 v67, v6, v7
v_cvt_pk_bf16_f32 v68, v8, v9
mav 16,16,36
R 48,36864
v_cvt_pk_bf16_f32 v69, v10, v11
mav 20,20,36
buffer_load_dwordx4 v[188:191], v88, s[36:39], s49 offen sc1
mav 24,24,36
v_cvt_pk_bf16_f32 v70, v12, v13
v_cvt_pk_bf16_f32 v71, v14, v15
mav 28,28,36
s_waitcnt lgkmcnt(1)
v_mul_f32 v75, v0, v0
v_add_f32 v74, v1, v0
mav 16,32,40
R 52,37888
v_fmac_f32 v75, v1, v1
mav 20,36,40
buffer_load_dwordx4 v[192:195], v88, s[36:39], s49 offen offset:1024 sc1
mav 24,40,40
v_add_f32 v74, v2, v74
v_fmac_f32 v75, v2, v2
mav 28,44,40
v_add_f32 v74, v3, v74
v_fmac_f32 v75, v3, v3
mav 16,48,44
R 56,38912
v_add_f32 v74, v4, v74
mav 20,52,44
buffer_load_dwordx4 v[196:199], v89, s[36:39], s49 offen sc1
mav 24,56,44
v_fmac_f32 v75, v4, v4
v_add_f32 v74, v5, v74
mav 28,60,44
s_waitcnt lgkmcnt(1)
v_fmac_f32 v75, v5, v5
v_add_f32 v74, v6, v74
mav 16,64,48
R 60,39936
v_fmac_f32 v75, v6, v6
mav 20,68,48
buffer_load_dwordx4 v[200:203], v89, s[36:39], s49 offen offset:1024 sc1
mav 24,72,48
v_add_f32 v74, v7, v74
v_fmac_f32 v75, v7, v7
mav 28,76,48
ds_write_b128 v94, v[64:67] offset:0
mav 16,80,52
s_mov_b32 m0, 144384
R 32,40960
v_add_f32 v74, v8, v74
mav 20,84,52
ds_write_b128 v95, v[68:71] offset:0
mav 24,88,52
buffer_load_dword v84, s[36:39], 0 offen sc1 lds
mav 28,92,52
s_waitcnt lgkmcnt(3)
ds_read_b128 v[64:67], v96 offset:0
s_add_u32 m0, s50, 32768
mav 16,96,56
R 36,41984
v_fmac_f32 v75, v8, v8
mav 20,100,56
D 83,0
mav 24,104,56
ds_read_b128 v[68:71], v96 offset:1024
v_add_f32 v74, v9, v74
mav 28,108,56
v_fmac_f32 v75, v9, v9
v_add_f32 v74, v10, v74
mav 16,112,60
R 40,43008
v_fmac_f32 v75, v10, v10
mav 20,116,60
D 83,1024
mav 24,120,60
v_add_f32 v74, v11, v74
v_fmac_f32 v75, v11, v11
mav 28,124,60
s_waitcnt lgkmcnt(2)
v_add_f32 v74, v12, v74
v_fmac_f32 v75, v12, v12
mav 16,128,32
R 44,44032
v_add_f32 v74, v13, v74
mav 20,132,32
v_fmac_f32 v75, v13, v13
v_add_f32 v74, v14, v74
mav 24,136,32
v_fmac_f32 v75, v14, v14
v_add_f32 v74, v15, v74
mav 28,140,32
v_fmac_f32 v75, v15, v15
mav 16,144,36
R 48,45056
mav 20,148,36
v_permlane32_swap_b32 v74, v75
mav 24,152,36
v_add_f32 v74, v74, v75
mav 28,156,36
s_waitcnt lgkmcnt(1)
mav 16,160,40
mav 20,164,40
R 52,46080
v_add_u32 v85, 1, v85
mav 24,168,40
mav 28,172,40
mav 16,176,44
R 56,47104
mav 20,180,44
D 83,2048
mav 24,184,44
mav 28,188,44
s_waitcnt lgkmcnt(1)
mav 16,192,48
R 60,48128
mav 20,196,48
D 83,3072
mav 24,200,48
mav 28,204,48
mav 16,208,52
mav 20,212,52
buffer_store_dwordx4 v[64:67], v91, s[36:39], s54 offen sc1
mav 24,216,52
mav 28,220,52
s_waitcnt lgkmcnt(0)
mav 16,224,56
mav 20,228,56
buffer_store_dwordx4 v[68:71], v92, s[36:39], s54 offen sc1
mav 24,232,56
mav 28,236,56
mav 16,240,60
mav 20,244,60
buffer_load_dwordx4 v[124:127], v90, s[36:39], s49 offen sc1
mav 24,248,60
ds_read_b32 v76, v82 offset:0
mav 28,252,60
s_waitcnt vmcnt(17)
mvv 16,116,164
mvv 20,120,164
s_waitcnt vmcnt(16)
buffer_load_dwordx4 v[128:131], v90, s[36:39], s49 offen offset:1024 sc1
mvv 24,116,168
s_xor_b32 s50, s50, 0x10000
s_sub_u32 s54, s54, 32768
mvv 28,120,168
s_waitcnt vmcnt(16)
mvv 16,116,172
mvv 20,120,172
s_waitcnt vmcnt(15) lgkmcnt(0)
v_cmp_gt_i32 vcc, v85, v76
v_xor_b32 v80, 0x10000, v80
mvv 24,116,176
s_waitcnt vmcnt(14)
s_add_u32 s49, s49, 313344
mvv 28,120,176
s_cbranch_vccnz CR0_5
BR0_5:
s_barrier
mvv 16,116,180
R 32,0
mvv 20,120,180
s_waitcnt vmcnt(13)
buffer_load_dwordx4 v[132:135], v86, s[36:39], s49 offen sc1
mvv 24,116,184
mvv 28,120,184
s_waitcnt vmcnt(13)
mvv 16,116,188
R 36,1024
mvv 20,120,188
s_waitcnt vmcnt(12)
buffer_load_dwordx4 v[136:139], v86, s[36:39], s49 offen offset:1024 sc1
mvv 24,116,192
mvv 28,120,192
s_waitcnt vmcnt(12)
mvv 16,116,196
R 40,2048
mvv 20,120,196
s_waitcnt vmcnt(11)
buffer_load_dwordx4 v[140:143], v87, s[36:39], s49 offen sc1
mvv 24,116,200
mvv 28,120,200
s_waitcnt lgkmcnt(1)
v_mfma_f32_16x16x32_bf16 v[0:3], a[0:3], v[32:35], v[100:103]
R 44,3072
v_cvt_pk_bf16_f32 v64, v16, v17
v_mfma_f32_16x16x32_bf16 v[4:7], a[4:7], v[32:35], v[104:107]
buffer_load_dwordx4 v[144:147], v87, s[36:39], s49 offen offset:1024 sc1
v_mfma_f32_16x16x32_bf16 v[8:11], a[8:11], v[32:35], v[108:111]
v_cvt_pk_bf16_f32 v65, v18, v19
v_cvt_pk_bf16_f32 v66, v20, v21
v_mfma_f32_16x16x32_bf16 v[12:15], a[12:15], v[32:35], v[112:115]
v_cvt_pk_bf16_f32 v67, v22, v23
v_cvt_pk_bf16_f32 v68, v24, v25
mav 0,16,36
R 48,4096
v_cvt_pk_bf16_f32 v69, v26, v27
mav 4,20,36
buffer_load_dwordx4 v[148:151], v88, s[36:39], s49 offen sc1
mav 8,24,36
v_cvt_pk_bf16_f32 v70, v28, v29
v_cvt_pk_bf16_f32 v71, v30, v31
mav 12,28,36
s_waitcnt lgkmcnt(1)
v_mul_f32 v75, v16, v16
v_fmac_f32 v75, v17, v17
mav 0,32,40
R 52,5120
v_fmac_f32 v75, v18, v18
mav 4,36,40
buffer_load_dwordx4 v[152:155], v88, s[36:39], s49 offen offset:1024 sc1
mav 8,40,40
v_fmac_f32 v75, v19, v19
v_fmac_f32 v75, v20, v20
mav 12,44,40
v_fmac_f32 v75, v21, v21
v_fmac_f32 v75, v22, v22
mav 0,48,44
R 56,6144
v_fmac_f32 v75, v23, v23
mav 4,52,44
buffer_load_dwordx4 v[156:159], v89, s[36:39], s49 offen sc1
mav 8,56,44
v_fmac_f32 v75, v24, v24
v_fmac_f32 v75, v25, v25
mav 12,60,44
s_waitcnt lgkmcnt(1)
v_fmac_f32 v75, v26, v26
v_fmac_f32 v75, v27, v27
mav 0,64,48
R 60,7168
v_fmac_f32 v75, v28, v28
mav 4,68,48
buffer_load_dwordx4 v[160:163], v89, s[36:39], s49 offen offset:1024 sc1
mav 8,72,48
ds_write_b32 v97, v74 offset:0
mav 12,76,48
ds_write_b128 v94, v[64:67] offset:0
mav 0,80,52
s_mov_b32 m0, 144384
R 32,8192
v_add_f32 v74, v17, v16
mav 4,84,52
ds_write_b128 v95, v[68:71] offset:0
mav 8,88,52
buffer_load_dword v84, s[36:39], 0 offen sc1 lds
mav 12,92,52
s_waitcnt lgkmcnt(4)
ds_read_b128 v[64:67], v96 offset:0
s_add_u32 m0, s50, 0
mav 0,96,56
R 36,9216
v_add_f32 v74, v18, v74
mav 4,100,56
D 83,0
mav 8,104,56
ds_read_b128 v[68:71], v96 offset:1024
v_add_f32 v74, v19, v74
mav 12,108,56
v_add_f32 v74, v20, v74
v_add_f32 v74, v21, v74
mav 0,112,60
R 40,10240
v_add_f32 v74, v22, v74
mav 4,116,60
D 83,1024
mav 8,120,60
v_add_f32 v74, v23, v74
v_add_f32 v74, v24, v74
mav 12,124,60
s_waitcnt lgkmcnt(2)
v_add_f32 v74, v25, v74
v_add_f32 v74, v26, v74
mav 0,128,32
R 44,11264
v_add_f32 v74, v27, v74
mav 4,132,32
v_add_f32 v74, v28, v74
v_add_f32 v74, v29, v74
mav 8,136,32
v_fmac_f32 v75, v29, v29
v_add_f32 v74, v30, v74
mav 12,140,32
v_fmac_f32 v75, v30, v30
v_add_f32 v74, v31, v74
mav 0,144,36
R 48,12288
v_fmac_f32 v75, v31, v31
mav 4,148,36
mav 8,152,36
v_permlane32_swap_b32 v74, v75
mav 12,156,36
s_waitcnt lgkmcnt(1)
v_add_f32 v74, v74, v75
mav 0,160,40
R 52,13312
v_add_u32 v85, 1, v85
mav 4,164,40
v_add_u32 v72, 1, v72
mav 8,168,40
mav 12,172,40
mav 0,176,44
R 56,14336
mav 4,180,44
D 83,2048
mav 8,184,44
mav 12,188,44
s_waitcnt lgkmcnt(1)
mav 0,192,48
R 60,15360
mav 4,196,48
D 83,3072
mav 8,200,48
mav 12,204,48
mav 0,208,52
mav 4,212,52
buffer_store_dwordx4 v[64:67], v91, s[36:39], s54 offen sc1
mav 8,216,52
mav 12,220,52
s_waitcnt lgkmcnt(0)
mav 0,224,56
mav 4,228,56
buffer_store_dwordx4 v[68:71], v92, s[36:39], s54 offen sc1
mav 8,232,56
mav 12,236,56
mav 0,240,60
mav 4,244,60
buffer_load_dwordx4 v[164:167], v90, s[36:39], s49 offen sc1
mav 8,248,60
ds_read_b32 v76, v82 offset:0
mav 12,252,60
s_waitcnt vmcnt(17)
mvv 0,116,124
buffer_store_dword v72, v81, s[36:39], s52 offen sc1
mvv 4,120,124
s_waitcnt vmcnt(17)
buffer_load_dwordx4 v[168:171], v90, s[36:39], s49 offen offset:1024 sc1
mvv 8,116,128
s_sub_u32 s54, s54, 32768
mvv 12,120,128
s_waitcnt vmcnt(17)
mvv 0,116,132
mvv 4,120,132
s_waitcnt vmcnt(16) lgkmcnt(0)
v_cmp_gt_i32 vcc, v85, v76
mvv 8,116,136
s_waitcnt vmcnt(15)
s_add_u32 s49, s49, 313344
mvv 12,120,136
s_cbranch_vccnz CR0_6
BR0_6:
s_barrier
mvv 0,116,140
R 32,32768
mvv 4,120,140
s_waitcnt vmcnt(14)
buffer_load_dwordx4 v[172:175], v86, s[36:39], s49 offen sc1
mvv 8,116,144
mvv 12,120,144
s_waitcnt vmcnt(14)
mvv 0,116,148
R 36,33792
mvv 4,120,148
s_waitcnt vmcnt(13)
buffer_load_dwordx4 v[176:179], v86, s[36:39], s49 offen offset:1024 sc1
mvv 8,116,152
mvv 12,120,152
s_waitcnt vmcnt(13)
mvv 0,116,156
R 40,34816
mvv 4,120,156
s_waitcnt vmcnt(12)
buffer_load_dwordx4 v[180:183], v87, s[36:39], s49 offen sc1
mvv 8,116,160
mvv 12,120,160
s_waitcnt lgkmcnt(1)
v_mfma_f32_16x16x32_bf16 v[16:19], a[0:3], v[32:35], v[100:103]
R 44,35840
v_cvt_pk_bf16_f32 v64, v0, v1
v_mfma_f32_16x16x32_bf16 v[20:23], a[4:7], v[32:35], v[104:107]
buffer_load_dwordx4 v[184:187], v87, s[36:39], s49 offen offset:1024 sc1
v_mfma_f32_16x16x32_bf16 v[24:27], a[8:11], v[32:35], v[108:111]
v_cvt_pk_bf16_f32 v65, v2, v3
v_cvt_pk_bf16_f32 v66, v4, v5
v_mfma_f32_16x16x32_bf16 v[28:31], a[12:15], v[32:35], v[112:115]
v_cvt_pk_bf16_f32 v67, v6, v7
v_cvt_pk_bf16_f32 v68, v8, v9
mav 16,16,36
R 48,36864
v_cvt_pk_bf16_f32 v69, v10, v11
mav 20,20,36
buffer_load_dwordx4 v[188:191], v88, s[36:39], s49 offen sc1
mav 24,24,36
v_cvt_pk_bf16_f32 v70, v12, v13
v_cvt_pk_bf16_f32 v71, v14, v15
mav 28,28,36
s_waitcnt lgkmcnt(1)
v_mul_f32 v75, v0, v0
v_fmac_f32 v75, v1, v1
mav 16,32,40
R 52,37888
v_fmac_f32 v75, v2, v2
mav 20,36,40
buffer_load_dwordx4 v[192:195], v88, s[36:39], s49 offen offset:1024 sc1
mav 24,40,40
v_fmac_f32 v75, v3, v3
v_fmac_f32 v75, v4, v4
mav 28,44,40
v_fmac_f32 v75, v5, v5
v_fmac_f32 v75, v6, v6
mav 16,48,44
R 56,38912
v_fmac_f32 v75, v7, v7
mav 20,52,44
buffer_load_dwordx4 v[196:199], v89, s[36:39], s49 offen sc1
mav 24,56,44
v_fmac_f32 v75, v8, v8
v_fmac_f32 v75, v9, v9
mav 28,60,44
s_waitcnt lgkmcnt(1)
v_fmac_f32 v75, v10, v10
v_fmac_f32 v75, v11, v11
mav 16,64,48
R 60,39936
v_fmac_f32 v75, v12, v12
mav 20,68,48
buffer_load_dwordx4 v[200:203], v89, s[36:39], s49 offen offset:1024 sc1
mav 24,72,48
ds_write_b32 v97, v74 offset:1024
mav 28,76,48
ds_write_b128 v94, v[64:67] offset:0
mav 16,80,52
s_mov_b32 m0, 144384
R 32,40960
v_add_f32 v74, v1, v0
mav 20,84,52
ds_write_b128 v95, v[68:71] offset:0
mav 24,88,52
buffer_load_dword v84, s[36:39], 0 offen sc1 lds
mav 28,92,52
s_waitcnt lgkmcnt(4)
ds_read_b128 v[64:67], v96 offset:0
s_add_u32 m0, s50, 32768
mav 16,96,56
R 36,41984
v_add_f32 v74, v2, v74
mav 20,100,56
D 83,0
mav 24,104,56
ds_read_b128 v[68:71], v96 offset:1024
v_add_f32 v74, v3, v74
mav 28,108,56
v_add_f32 v74, v4, v74
v_add_f32 v74, v5, v74
mav 16,112,60
R 40,43008
v_add_f32 v74, v6, v74
mav 20,116,60
D 83,1024
mav 24,120,60
v_add_f32 v74, v7, v74
v_add_f32 v74, v8, v74
mav 28,124,60
s_waitcnt lgkmcnt(2)
v_add_f32 v74, v9, v74
v_add_f32 v74, v10, v74
mav 16,128,32
R 44,44032
v_add_f32 v74, v11, v74
mav 20,132,32
v_add_f32 v74, v12, v74
v_add_f32 v74, v13, v74
mav 24,136,32
v_fmac_f32 v75, v13, v13
v_add_f32 v74, v14, v74
mav 28,140,32
v_fmac_f32 v75, v14, v14
v_add_f32 v74, v15, v74
mav 16,144,36
R 48,45056
v_fmac_f32 v75, v15, v15
mav 20,148,36
mav 24,152,36
v_permlane32_swap_b32 v74, v75
mav 28,156,36
s_waitcnt lgkmcnt(1)
v_add_f32 v74, v74, v75
mav 16,160,40
s_mov_b32 s60, -14
.p2align 6
LR0_7:
mav 20,164,40
R 52,46080
v_add_u32 v85, 1, v85
mav 24,168,40
v_add_u32 v72, 1, v72
mav 28,172,40
mav 16,176,44
R 56,47104
mav 20,180,44
D 83,2048
mav 24,184,44
mav 28,188,44
s_waitcnt lgkmcnt(1)
mav 16,192,48
R 60,48128
mav 20,196,48
D 83,3072
mav 24,200,48
mav 28,204,48
mav 16,208,52
mav 20,212,52
buffer_store_dwordx4 v[64:67], v91, s[36:39], s54 offen sc1
mav 24,216,52
mav 28,220,52
s_waitcnt lgkmcnt(0)
mav 16,224,56
mav 20,228,56
buffer_store_dwordx4 v[68:71], v92, s[36:39], s54 offen sc1
mav 24,232,56
mav 28,236,56
mav 16,240,60
mav 20,244,60
buffer_load_dwordx4 v[124:127], v90, s[36:39], s49 offen sc1
mav 24,248,60
ds_read_b32 v76, v82 offset:0
mav 28,252,60
s_waitcnt vmcnt(18)
mvv 16,116,164
buffer_store_dword v72, v81, s[36:39], s52 offen sc1
mvv 20,120,164
s_waitcnt vmcnt(17)
buffer_load_dwordx4 v[128:131], v90, s[36:39], s49 offen offset:1024 sc1
mvv 24,116,168
LsR2:
s_cbranch_execz LsR6
s_xor_b32 s50, s50, 0x10000
s_sub_u32 s54, s54, 32768
mvv 28,120,168
s_waitcnt vmcnt(17)
mvv 16,116,172
mvv 20,120,172
s_waitcnt vmcnt(16) lgkmcnt(0)
v_cmp_gt_i32 vcc, v85, v76
v_xor_b32 v80, 0x10000, v80
mvv 24,116,176
s_waitcnt vmcnt(15)
s_add_u32 s49, s49, 313344
mvv 28,120,176
s_cbranch_vccnz CR0_8
BR0_8:
s_barrier
mvv 16,116,180
R 32,0
mvv 20,120,180
s_waitcnt vmcnt(14)
buffer_load_dwordx4 v[132:135], v86, s[36:39], s49 offen sc1
mvv 24,116,184
mvv 28,120,184
s_waitcnt vmcnt(14)
mvv 16,116,188
R 36,1024
mvv 20,120,188
s_waitcnt vmcnt(13)
buffer_load_dwordx4 v[136:139], v86, s[36:39], s49 offen offset:1024 sc1
mvv 24,116,192
mvv 28,120,192
s_waitcnt vmcnt(13)
mvv 16,116,196
R 40,2048
mvv 20,120,196
s_waitcnt vmcnt(12)
buffer_load_dwordx4 v[140:143], v87, s[36:39], s49 offen sc1
mvv 24,116,200
mvv 28,120,200
s_waitcnt lgkmcnt(1)
v_mfma_f32_16x16x32_bf16 v[0:3], a[0:3], v[32:35], v[100:103]
R 44,3072
v_cvt_pk_bf16_f32 v64, v16, v17
v_mfma_f32_16x16x32_bf16 v[4:7], a[4:7], v[32:35], v[104:107]
buffer_load_dwordx4 v[144:147], v87, s[36:39], s49 offen offset:1024 sc1
v_mfma_f32_16x16x32_bf16 v[8:11], a[8:11], v[32:35], v[108:111]
v_cvt_pk_bf16_f32 v65, v18, v19
v_cvt_pk_bf16_f32 v66, v20, v21
v_mfma_f32_16x16x32_bf16 v[12:15], a[12:15], v[32:35], v[112:115]
v_cvt_pk_bf16_f32 v67, v22, v23
v_cvt_pk_bf16_f32 v68, v24, v25
mav 0,16,36
R 48,4096
v_cvt_pk_bf16_f32 v69, v26, v27
mav 4,20,36
buffer_load_dwordx4 v[148:151], v88, s[36:39], s49 offen sc1
mav 8,24,36
v_cvt_pk_bf16_f32 v70, v28, v29
v_cvt_pk_bf16_f32 v71, v30, v31
mav 12,28,36
s_waitcnt lgkmcnt(1)
v_mul_f32 v75, v16, v16
v_fmac_f32 v75, v17, v17
mav 0,32,40
R 52,5120
v_fmac_f32 v75, v18, v18
mav 4,36,40
buffer_load_dwordx4 v[152:155], v88, s[36:39], s49 offen offset:1024 sc1
mav 8,40,40
v_fmac_f32 v75, v19, v19
v_fmac_f32 v75, v20, v20
mav 12,44,40
v_fmac_f32 v75, v21, v21
v_fmac_f32 v75, v22, v22
mav 0,48,44
R 56,6144
v_fmac_f32 v75, v23, v23
mav 4,52,44
buffer_load_dwordx4 v[156:159], v89, s[36:39], s49 offen sc1
mav 8,56,44
v_fmac_f32 v75, v24, v24
v_fmac_f32 v75, v25, v25
mav 12,60,44
s_waitcnt lgkmcnt(1)
v_fmac_f32 v75, v26, v26
v_fmac_f32 v75, v27, v27
mav 0,64,48
R 60,7168
v_fmac_f32 v75, v28, v28
mav 4,68,48
buffer_load_dwordx4 v[160:163], v89, s[36:39], s49 offen offset:1024 sc1
mav 8,72,48
ds_write_b32 v97, v74 offset:0
mav 12,76,48
ds_write_b128 v94, v[64:67] offset:0
mav 0,80,52
s_mov_b32 m0, 144384
R 32,8192
v_add_f32 v74, v17, v16
mav 4,84,52
ds_write_b128 v95, v[68:71] offset:0
mav 8,88,52
buffer_load_dword v84, s[36:39], 0 offen sc1 lds
mav 12,92,52
s_waitcnt lgkmcnt(4)
ds_read_b128 v[64:67], v96 offset:0
s_add_u32 m0, s50, 0
mav 0,96,56
R 36,9216
v_add_f32 v74, v18, v74
mav 4,100,56
D 83,0
mav 8,104,56
ds_read_b128 v[68:71], v96 offset:1024
v_add_f32 v74, v19, v74
mav 12,108,56
v_add_f32 v74, v20, v74
v_add_f32 v74, v21, v74
mav 0,112,60
R 40,10240
v_add_f32 v74, v22, v74
mav 4,116,60
D 83,1024
mav 8,120,60
v_add_f32 v74, v23, v74
v_add_f32 v74, v24, v74
mav 12,124,60
s_waitcnt lgkmcnt(2)
v_add_f32 v74, v25, v74
v_add_f32 v74, v26, v74
mav 0,128,32
R 44,11264
v_add_f32 v74, v27, v74
mav 4,132,32
v_add_f32 v74, v28, v74
v_add_f32 v74, v29, v74
mav 8,136,32
v_fmac_f32 v75, v29, v29
v_add_f32 v74, v30, v74
mav 12,140,32
v_fmac_f32 v75, v30, v30
v_add_f32 v74, v31, v74
mav 0,144,36
R 48,12288
v_fmac_f32 v75, v31, v31
mav 4,148,36
mav 8,152,36
v_permlane32_swap_b32 v74, v75
mav 12,156,36
s_waitcnt lgkmcnt(1)
v_add_f32 v74, v74, v75
mav 0,160,40
R 52,13312
v_add_u32 v85, 1, v85
mav 4,164,40
v_add_u32 v72, 1, v72
mav 8,168,40
mav 12,172,40
mav 0,176,44
R 56,14336
mav 4,180,44
D 83,2048
mav 8,184,44
mav 12,188,44
s_waitcnt lgkmcnt(1)
mav 0,192,48
R 60,15360
mav 4,196,48
D 83,3072
mav 8,200,48
mav 12,204,48
mav 0,208,52
mav 4,212,52
buffer_store_dwordx4 v[64:67], v91, s[36:39], s54 offen sc1
mav 8,216,52
mav 12,220,52
s_waitcnt lgkmcnt(0)
mav 0,224,56
mav 4,228,56
buffer_store_dwordx4 v[68:71], v92, s[36:39], s54 offen sc1
mav 8,232,56
mav 12,236,56
mav 0,240,60
mav 4,244,60
buffer_load_dwordx4 v[164:167], v90, s[36:39], s49 offen sc1
mav 8,248,60
ds_read_b32 v76, v82 offset:0
mav 12,252,60
s_waitcnt vmcnt(18)
mvv 0,116,124
buffer_store_dword v72, v81, s[36:39], s52 offen sc1
mvv 4,120,124
s_waitcnt vmcnt(17)
buffer_load_dwordx4 v[168:171], v90, s[36:39], s49 offen offset:1024 sc1
mvv 8,116,128
s_sub_u32 s54, s54, 32768
mvv 12,120,128
s_waitcnt vmcnt(17)
mvv 0,116,132
mvv 4,120,132
s_waitcnt vmcnt(16) lgkmcnt(0)
v_cmp_gt_i32 vcc, v85, v76
mvv 8,116,136
s_waitcnt vmcnt(15)
s_add_u32 s49, s49, 313344
mvv 12,120,136
s_cbranch_vccnz CR0_9
BR0_9:
s_barrier
mvv 0,116,140
R 32,32768
mvv 4,120,140
s_waitcnt vmcnt(14)
buffer_load_dwordx4 v[172:175], v86, s[36:39], s49 offen sc1
mvv 8,116,144
mvv 12,120,144
s_waitcnt vmcnt(14)
mvv 0,116,148
R 36,33792
mvv 4,120,148
s_waitcnt vmcnt(13)
buffer_load_dwordx4 v[176:179], v86, s[36:39], s49 offen offset:1024 sc1
mvv 8,116,152
mvv 12,120,152
s_waitcnt vmcnt(13)
mvv 0,116,156
R 40,34816
mvv 4,120,156
s_waitcnt vmcnt(12)
buffer_load_dwordx4 v[180:183], v87, s[36:39], s49 offen sc1
mvv 8,116,160
mvv 12,120,160
s_waitcnt lgkmcnt(1)
v_mfma_f32_16x16x32_bf16 v[16:19], a[0:3], v[32:35], v[100:103]
R 44,35840
v_cvt_pk_bf16_f32 v64, v0, v1
v_mfma_f32_16x16x32_bf16 v[20:23], a[4:7], v[32:35], v[104:107]
buffer_load_dwordx4 v[184:187], v87, s[36:39], s49 offen offset:1024 sc1
v_mfma_f32_16x16x32_bf16 v[24:27], a[8:11], v[32:35], v[108:111]
v_cvt_pk_bf16_f32 v65, v2, v3
v_cvt_pk_bf16_f32 v66, v4, v5
v_mfma_f32_16x16x32_bf16 v[28:31], a[12:15], v[32:35], v[112:115]
v_cvt_pk_bf16_f32 v67, v6, v7
v_cvt_pk_bf16_f32 v68, v8, v9
mav 16,16,36
R 48,36864
v_cvt_pk_bf16_f32 v69, v10, v11
mav 20,20,36
buffer_load_dwordx4 v[188:191], v88, s[36:39], s49 offen sc1
mav 24,24,36
v_cvt_pk_bf16_f32 v70, v12, v13
v_cvt_pk_bf16_f32 v71, v14, v15
mav 28,28,36
s_waitcnt lgkmcnt(1)
v_mul_f32 v75, v0, v0
v_fmac_f32 v75, v1, v1
mav 16,32,40
R 52,37888
v_fmac_f32 v75, v2, v2
mav 20,36,40
buffer_load_dwordx4 v[192:195], v88, s[36:39], s49 offen offset:1024 sc1
mav 24,40,40
v_fmac_f32 v75, v3, v3
v_fmac_f32 v75, v4, v4
mav 28,44,40
v_fmac_f32 v75, v5, v5
v_fmac_f32 v75, v6, v6
mav 16,48,44
R 56,38912
v_fmac_f32 v75, v7, v7
mav 20,52,44
buffer_load_dwordx4 v[196:199], v89, s[36:39], s49 offen sc1
mav 24,56,44
v_fmac_f32 v75, v8, v8
v_fmac_f32 v75, v9, v9
mav 28,60,44
s_waitcnt lgkmcnt(1)
v_fmac_f32 v75, v10, v10
v_fmac_f32 v75, v11, v11
mav 16,64,48
R 60,39936
v_fmac_f32 v75, v12, v12
mav 20,68,48
buffer_load_dwordx4 v[200:203], v89, s[36:39], s49 offen offset:1024 sc1
mav 24,72,48
ds_write_b32 v97, v74 offset:1024
mav 28,76,48
ds_write_b128 v94, v[64:67] offset:0
mav 16,80,52
s_mov_b32 m0, 144384
R 32,40960
v_add_f32 v74, v1, v0
mav 20,84,52
ds_write_b128 v95, v[68:71] offset:0
mav 24,88,52
buffer_load_dword v84, s[36:39], 0 offen sc1 lds
mav 28,92,52
s_waitcnt lgkmcnt(4)
ds_read_b128 v[64:67], v96 offset:0
s_add_u32 m0, s50, 32768
mav 16,96,56
R 36,41984
v_add_f32 v74, v2, v74
mav 20,100,56
D 83,0
mav 24,104,56
ds_read_b128 v[68:71], v96 offset:1024
v_add_f32 v74, v3, v74
mav 28,108,56
v_add_f32 v74, v4, v74
v_add_f32 v74, v5, v74
mav 16,112,60
R 40,43008
v_add_f32 v74, v6, v74
mav 20,116,60
D 83,1024
mav 24,120,60
v_add_f32 v74, v7, v74
v_add_f32 v74, v8, v74
mav 28,124,60
s_waitcnt lgkmcnt(2)
v_add_f32 v74, v9, v74
v_add_f32 v74, v10, v74
mav 16,128,32
R 44,44032
v_add_f32 v74, v11, v74
mav 20,132,32
v_add_f32 v74, v12, v74
v_add_f32 v74, v13, v74
mav 24,136,32
v_fmac_f32 v75, v13, v13
v_add_f32 v74, v14, v74
mav 28,140,32
v_fmac_f32 v75, v14, v14
v_add_f32 v74, v15, v74
mav 16,144,36
R 48,45056
v_fmac_f32 v75, v15, v15
mav 20,148,36
mav 24,152,36
v_permlane32_swap_b32 v74, v75
mav 28,156,36
s_waitcnt lgkmcnt(1)
s_add_u32 s60, s60, 1
v_add_f32 v74, v74, v75
mav 16,160,40
s_cbranch_scc0 LR0_7
mav 20,164,40
R 52,46080
v_add_u32 v85, 1, v85
mav 24,168,40
v_add_u32 v72, 1, v72
mav 28,172,40
mav 16,176,44
R 56,47104
mav 20,180,44
D 83,2048
mav 24,184,44
mav 28,188,44
s_waitcnt lgkmcnt(1)
mav 16,192,48
R 60,48128
mav 20,196,48
D 83,3072
mav 24,200,48
mav 28,204,48
mav 16,208,52
mav 20,212,52
buffer_store_dwordx4 v[64:67], v91, s[36:39], s54 offen sc1
mav 24,216,52
mav 28,220,52
s_waitcnt lgkmcnt(0)
mav 16,224,56
mav 20,228,56
buffer_store_dwordx4 v[68:71], v92, s[36:39], s54 offen sc1
mav 24,232,56
mav 28,236,56
mav 16,240,60
mav 20,244,60
buffer_load_dwordx4 v[124:127], v90, s[36:39], s49 offen sc1
mav 24,248,60
ds_read_b32 v76, v82 offset:0
mav 28,252,60
s_waitcnt vmcnt(18)
mvv 16,116,164
buffer_store_dword v72, v81, s[36:39], s52 offen sc1
mvv 20,120,164
s_waitcnt vmcnt(17)
buffer_load_dwordx4 v[128:131], v90, s[36:39], s49 offen offset:1024 sc1
mvv 24,116,168
s_xor_b32 s50, s50, 0x10000
s_sub_u32 s54, s54, 32768
mvv 28,120,168
s_waitcnt vmcnt(17)
mvv 16,116,172
mvv 20,120,172
s_waitcnt vmcnt(16) lgkmcnt(0)
v_cmp_gt_i32 vcc, v85, v76
v_xor_b32 v80, 0x10000, v80
mvv 24,116,176
s_waitcnt vmcnt(15)
s_add_u32 s49, s49, 313344
mvv 28,120,176
s_cbranch_vccnz CR0_10
BR0_10:
s_barrier
mvv 16,116,180
R 32,0
mvv 20,120,180
s_waitcnt vmcnt(14)
buffer_load_dwordx4 v[132:135], v86, s[36:39], s49 offen sc1
mvv 24,116,184
mvv 28,120,184
s_waitcnt vmcnt(14)
mvv 16,116,188
R 36,1024
mvv 20,120,188
s_waitcnt vmcnt(13)
buffer_load_dwordx4 v[136:139], v86, s[36:39], s49 offen offset:1024 sc1
mvv 24,116,192
mvv 28,120,192
s_waitcnt vmcnt(13)
mvv 16,116,196
R 40,2048
mvv 20,120,196
s_waitcnt vmcnt(12)
buffer_load_dwordx4 v[140:143], v87, s[36:39], s49 offen sc1
mvv 24,116,200
mvv 28,120,200
s_waitcnt lgkmcnt(1)
v_mfma_f32_16x16x32_bf16 v[0:3], a[0:3], v[32:35], v[100:103]
R 44,3072
v_cvt_pk_bf16_f32 v64, v16, v17
v_mfma_f32_16x16x32_bf16 v[4:7], a[4:7], v[32:35], v[104:107]
buffer_load_dwordx4 v[144:147], v87, s[36:39], s49 offen offset:1024 sc1
v_mfma_f32_16x16x32_bf16 v[8:11], a[8:11], v[32:35], v[108:111]
v_cvt_pk_bf16_f32 v65, v18, v19
v_cvt_pk_bf16_f32 v66, v20, v21
v_mfma_f32_16x16x32_bf16 v[12:15], a[12:15], v[32:35], v[112:115]
v_cvt_pk_bf16_f32 v67, v22, v23
v_cvt_pk_bf16_f32 v68, v24, v25
mav 0,16,36
R 48,4096
v_cvt_pk_bf16_f32 v69, v26, v27
mav 4,20,36
buffer_load_dwordx4 v[148:151], v88, s[36:39], s49 offen sc1
mav 8,24,36
v_cvt_pk_bf16_f32 v70, v28, v29
v_cvt_pk_bf16_f32 v71, v30, v31
mav 12,28,36
s_waitcnt lgkmcnt(1)
v_mul_f32 v75, v16, v16
v_fmac_f32 v75, v17, v17
mav 0,32,40
R 52,5120
v_fmac_f32 v75, v18, v18
mav 4,36,40
buffer_load_dwordx4 v[152:155], v88, s[36:39], s49 offen offset:1024 sc1
mav 8,40,40
v_fmac_f32 v75, v19, v19
v_fmac_f32 v75, v20, v20
mav 12,44,40
v_fmac_f32 v75, v21, v21
v_fmac_f32 v75, v22, v22
mav 0,48,44
R 56,6144
v_fmac_f32 v75, v23, v23
mav 4,52,44
buffer_load_dwordx4 v[156:159], v89, s[36:39], s49 offen sc1
mav 8,56,44
v_fmac_f32 v75, v24, v24
v_fmac_f32 v75, v25, v25
mav 12,60,44
s_waitcnt lgkmcnt(1)
v_fmac_f32 v75, v26, v26
v_fmac_f32 v75, v27, v27
mav 0,64,48
R 60,7168
v_fmac_f32 v75, v28, v28
mav 4,68,48
buffer_load_dwordx4 v[160:163], v89, s[36:39], s49 offen offset:1024 sc1
mav 8,72,48
ds_write_b32 v97, v74 offset:0
mav 12,76,48
ds_write_b128 v94, v[64:67] offset:0
mav 0,80,52
s_mov_b32 m0, 144384
R 32,8192
v_add_f32 v74, v17, v16
mav 4,84,52
ds_write_b128 v95, v[68:71] offset:0
mav 8,88,52
buffer_load_dword v84, s[36:39], 0 offen sc1 lds
mav 12,92,52
s_waitcnt lgkmcnt(4)
ds_read_b128 v[64:67], v96 offset:0
s_add_u32 m0, s50, 0
mav 0,96,56
R 36,9216
v_add_f32 v74, v18, v74
mav 4,100,56
D 83,0
mav 8,104,56
ds_read_b128 v[68:71], v96 offset:1024
v_add_f32 v74, v19, v74
mav 12,108,56
v_add_f32 v74, v20, v74
v_add_f32 v74, v21, v74
mav 0,112,60
R 40,10240
v_add_f32 v74, v22, v74
mav 4,116,60
D 83,1024
mav 8,120,60
v_add_f32 v74, v23, v74
v_add_f32 v74, v24, v74
mav 12,124,60
s_waitcnt lgkmcnt(2)
v_add_f32 v74, v25, v74
v_add_f32 v74, v26, v74
mav 0,128,32
R 44,11264
v_add_f32 v74, v27, v74
mav 4,132,32
v_add_f32 v74, v28, v74
v_add_f32 v74, v29, v74
mav 8,136,32
v_fmac_f32 v75, v29, v29
v_add_f32 v74, v30, v74
mav 12,140,32
v_fmac_f32 v75, v30, v30
v_add_f32 v74, v31, v74
mav 0,144,36
R 48,12288
v_fmac_f32 v75, v31, v31
mav 4,148,36
mav 8,152,36
v_permlane32_swap_b32 v74, v75
mav 12,156,36
s_waitcnt lgkmcnt(1)
v_add_f32 v74, v74, v75
mav 0,160,40
R 52,13312
v_add_u32 v85, 1, v85
mav 4,164,40
v_add_u32 v72, 1, v72
mav 8,168,40
mav 12,172,40
mav 0,176,44
R 56,14336
mav 4,180,44
D 83,2048
mav 8,184,44
mav 12,188,44
s_waitcnt lgkmcnt(1)
mav 0,192,48
R 60,15360
mav 4,196,48
D 83,3072
mav 8,200,48
mav 12,204,48
mav 0,208,52
mav 4,212,52
buffer_store_dwordx4 v[64:67], v91, s[36:39], s54 offen sc1
mav 8,216,52
mav 12,220,52
s_waitcnt lgkmcnt(0)
mav 0,224,56
mav 4,228,56
buffer_store_dwordx4 v[68:71], v92, s[36:39], s54 offen sc1
mav 8,232,56
mav 12,236,56
mav 0,240,60
)"
R"(
mav 4,244,60
buffer_load_dwordx4 v[164:167], v90, s[36:39], s49 offen sc1
mav 8,248,60
ds_read_b32 v76, v82 offset:0
mav 12,252,60
s_waitcnt vmcnt(18)
mvv 0,116,124
buffer_store_dword v72, v81, s[36:39], s52 offen sc1
mvv 4,120,124
s_waitcnt vmcnt(17)
buffer_load_dwordx4 v[168:171], v90, s[36:39], s49 offen offset:1024 sc1
mvv 8,116,128
s_sub_u32 s54, s54, 3178496
mvv 12,120,128
s_waitcnt vmcnt(17)
mvv 0,116,132
mvv 4,120,132
s_waitcnt vmcnt(16) lgkmcnt(0)
v_cmp_gt_i32 vcc, v85, v76
mvv 8,116,136
s_waitcnt vmcnt(15)
s_add_u32 s49, s49, 313344
mvv 12,120,136
s_cbranch_vccnz CR0_11
BR0_11:
s_barrier
mvv 0,116,140
R 32,32768
mvv 4,120,140
s_waitcnt vmcnt(14)
buffer_load_dwordx4 v[172:175], v86, s[36:39], s49 offen sc1
mvv 8,116,144
LsR3:
s_cbranch_execz LsR7
mvv 12,120,144
s_waitcnt vmcnt(14)
mvv 0,116,148
R 36,33792
mvv 4,120,148
s_waitcnt vmcnt(13)
buffer_load_dwordx4 v[176:179], v86, s[36:39], s49 offen offset:1024 sc1
mvv 8,116,152
mvv 12,120,152
s_waitcnt vmcnt(13)
mvv 0,116,156
R 40,34816
mvv 4,120,156
s_waitcnt vmcnt(12)
buffer_load_dwordx4 v[180:183], v87, s[36:39], s49 offen sc1
mvv 8,116,160
mvv 12,120,160
s_waitcnt lgkmcnt(1)
v_mfma_f32_16x16x32_bf16 v[16:19], a[0:3], v[32:35], v[100:103]
R 44,35840
v_cvt_pk_bf16_f32 v64, v0, v1
v_mfma_f32_16x16x32_bf16 v[20:23], a[4:7], v[32:35], v[104:107]
buffer_load_dwordx4 v[184:187], v87, s[36:39], s49 offen offset:1024 sc1
v_mfma_f32_16x16x32_bf16 v[24:27], a[8:11], v[32:35], v[108:111]
v_cvt_pk_bf16_f32 v65, v2, v3
v_cvt_pk_bf16_f32 v66, v4, v5
v_mfma_f32_16x16x32_bf16 v[28:31], a[12:15], v[32:35], v[112:115]
v_cvt_pk_bf16_f32 v67, v6, v7
v_cvt_pk_bf16_f32 v68, v8, v9
mav 16,16,36
R 48,36864
v_cvt_pk_bf16_f32 v69, v10, v11
mav 20,20,36
buffer_load_dwordx4 v[188:191], v88, s[36:39], s49 offen sc1
mav 24,24,36
v_cvt_pk_bf16_f32 v70, v12, v13
v_cvt_pk_bf16_f32 v71, v14, v15
mav 28,28,36
s_waitcnt lgkmcnt(1)
mav 16,32,40
R 52,37888
mav 20,36,40
buffer_load_dwordx4 v[192:195], v88, s[36:39], s49 offen offset:1024 sc1
mav 24,40,40
mav 28,44,40
mav 16,48,44
R 56,38912
mav 20,52,44
buffer_load_dwordx4 v[196:199], v89, s[36:39], s49 offen sc1
mav 24,56,44
mav 28,60,44
s_waitcnt lgkmcnt(1)
mav 16,64,48
R 60,39936
mav 20,68,48
buffer_load_dwordx4 v[200:203], v89, s[36:39], s49 offen offset:1024 sc1
mav 24,72,48
ds_write_b32 v97, v74 offset:1024
mav 28,76,48
ds_write_b128 v94, v[64:67] offset:0
mav 16,80,52
s_mov_b32 m0, 144384
R 32,40960
mav 20,84,52
ds_write_b128 v95, v[68:71] offset:0
mav 24,88,52
buffer_load_dword v84, s[36:39], 0 offen sc1 lds
mav 28,92,52
s_waitcnt lgkmcnt(4)
ds_read_b128 v[64:67], v96 offset:0
s_add_u32 m0, s50, 32768
mav 16,96,56
R 36,41984
mav 20,100,56
D 83,0
mav 24,104,56
ds_read_b128 v[68:71], v96 offset:1024
mav 28,108,56
mav 16,112,60
R 40,43008
mav 20,116,60
D 83,1024
mav 24,120,60
mav 28,124,60
s_waitcnt lgkmcnt(2)
mav 16,128,32
R 44,44032
mav 20,132,32
mav 24,136,32
mav 28,140,32
mav 16,144,36
R 48,45056
mav 20,148,36
mav 24,152,36
mav 28,156,36
s_waitcnt lgkmcnt(1)
mav 16,160,40
mav 20,164,40
R 52,46080
v_add_u32 v85, 1, v85
mav 24,168,40
v_add_u32 v72, 69, v72
mav 28,172,40
mav 16,176,44
R 56,47104
mav 20,180,44
D 83,2048
mav 24,184,44
mav 28,188,44
s_waitcnt lgkmcnt(1)
mav 16,192,48
R 60,48128
mav 20,196,48
D 83,3072
mav 24,200,48
mav 28,204,48
mav 16,208,52
mav 20,212,52
buffer_store_dwordx4 v[64:67], v91, s[44:47], s54 offen sc1
mav 24,216,52
mav 28,220,52
s_waitcnt lgkmcnt(0)
mav 16,224,56
mav 20,228,56
buffer_store_dwordx4 v[68:71], v92, s[44:47], s54 offen sc1
mav 24,232,56
mav 28,236,56
mav 16,240,60
mav 20,244,60
buffer_load_dwordx4 v[124:127], v90, s[36:39], s49 offen sc1
mav 24,248,60
ds_read_b32 v76, v82 offset:0
mav 28,252,60
s_waitcnt vmcnt(18)
mvv 16,116,164
buffer_store_dword v72, v81, s[36:39], s52 offen sc1
mvv 20,120,164
s_waitcnt vmcnt(17)
buffer_load_dwordx4 v[128:131], v90, s[36:39], s49 offen offset:1024 sc1
mvv 24,116,168
s_xor_b32 s50, s50, 0x10000
s_sub_u32 s54, s54, 32768
mvv 28,120,168
s_waitcnt vmcnt(17)
mvv 16,116,172
mvv 20,120,172
s_waitcnt vmcnt(16) lgkmcnt(0)
v_cmp_gt_i32 vcc, v85, v76
v_xor_b32 v80, 0x10000, v80
mvv 24,116,176
s_waitcnt vmcnt(15)
s_add_u32 s49, s49, 313344
mvv 28,120,176
s_cbranch_vccnz CR0_12
BR0_12:
s_barrier
mvv 16,116,180
R 32,0
mvv 20,120,180
s_waitcnt vmcnt(14)
buffer_load_dwordx4 v[132:135], v86, s[36:39], s49 offen sc1
mvv 24,116,184
mvv 28,120,184
s_waitcnt vmcnt(14)
mvv 16,116,188
R 36,1024
mvv 20,120,188
s_waitcnt vmcnt(13)
buffer_load_dwordx4 v[136:139], v86, s[36:39], s49 offen offset:1024 sc1
mvv 24,116,192
mvv 28,120,192
s_waitcnt vmcnt(13)
mvv 16,116,196
R 40,2048
mvv 20,120,196
s_waitcnt vmcnt(12)
buffer_load_dwordx4 v[140:143], v87, s[36:39], s49 offen sc1
mvv 24,116,200
mvv 28,120,200
s_waitcnt lgkmcnt(1)
v_mfma_f32_16x16x32_bf16 v[0:3], a[0:3], v[32:35], v[100:103]
R 44,3072
v_cvt_pk_bf16_f32 v64, v16, v17
v_mfma_f32_16x16x32_bf16 v[4:7], a[4:7], v[32:35], v[104:107]
buffer_load_dwordx4 v[144:147], v87, s[36:39], s49 offen offset:1024 sc1
v_mfma_f32_16x16x32_bf16 v[8:11], a[8:11], v[32:35], v[108:111]
v_cvt_pk_bf16_f32 v65, v18, v19
v_cvt_pk_bf16_f32 v66, v20, v21
v_mfma_f32_16x16x32_bf16 v[12:15], a[12:15], v[32:35], v[112:115]
v_cvt_pk_bf16_f32 v67, v22, v23
v_cvt_pk_bf16_f32 v68, v24, v25
mav 0,16,36
R 48,4096
v_cvt_pk_bf16_f32 v69, v26, v27
mav 4,20,36
buffer_load_dwordx4 v[148:151], v88, s[36:39], s49 offen sc1
mav 8,24,36
v_cvt_pk_bf16_f32 v70, v28, v29
v_cvt_pk_bf16_f32 v71, v30, v31
mav 12,28,36
s_waitcnt lgkmcnt(1)
mav 0,32,40
R 52,5120
mav 4,36,40
buffer_load_dwordx4 v[152:155], v88, s[36:39], s49 offen offset:1024 sc1
mav 8,40,40
mav 12,44,40
mav 0,48,44
R 56,6144
mav 4,52,44
buffer_load_dwordx4 v[156:159], v89, s[36:39], s49 offen sc1
mav 8,56,44
mav 12,60,44
s_waitcnt lgkmcnt(1)
mav 0,64,48
R 60,7168
mav 4,68,48
buffer_load_dwordx4 v[160:163], v89, s[36:39], s49 offen offset:1024 sc1
mav 8,72,48
mav 12,76,48
ds_write_b128 v94, v[64:67] offset:0
mav 0,80,52
s_mov_b32 m0, 144384
R 32,8192
mav 4,84,52
ds_write_b128 v95, v[68:71] offset:0
mav 8,88,52
buffer_load_dword v84, s[36:39], 0 offen sc1 lds
mav 12,92,52
s_waitcnt lgkmcnt(3)
ds_read_b128 v[64:67], v96 offset:0
s_add_u32 m0, s50, 0
mav 0,96,56
R 36,9216
mav 4,100,56
D 83,0
mav 8,104,56
ds_read_b128 v[68:71], v96 offset:1024
mav 12,108,56
mav 0,112,60
R 40,10240
mav 4,116,60
D 83,1024
mav 8,120,60
mav 12,124,60
s_waitcnt lgkmcnt(2)
mav 0,128,32
R 44,11264
mav 4,132,32
mav 8,136,32
mav 12,140,32
mav 0,144,36
R 48,12288
mav 4,148,36
mav 8,152,36
mav 12,156,36
s_waitcnt lgkmcnt(1)
mav 0,160,40
R 52,13312
v_add_u32 v85, 1, v85
mav 4,164,40
mav 8,168,40
mav 12,172,40
mav 0,176,44
R 56,14336
mav 4,180,44
D 83,2048
mav 8,184,44
mav 12,188,44
s_waitcnt lgkmcnt(1)
mav 0,192,48
R 60,15360
mav 4,196,48
D 83,3072
mav 8,200,48
mav 12,204,48
mav 0,208,52
mav 4,212,52
buffer_store_dwordx4 v[64:67], v91, s[44:47], s54 offen sc1
mav 8,216,52
mav 12,220,52
s_waitcnt lgkmcnt(0)
mav 0,224,56
mav 4,228,56
buffer_store_dwordx4 v[68:71], v92, s[44:47], s54 offen sc1
mav 8,232,56
mav 12,236,56
mav 0,240,60
mav 4,244,60
buffer_load_dwordx4 v[164:167], v90, s[36:39], s49 offen sc1
mav 8,248,60
ds_read_b32 v76, v82 offset:0
mav 12,252,60
s_waitcnt vmcnt(18)
mvv 0,116,124
mvv 4,120,124
s_waitcnt vmcnt(16)
buffer_load_dwordx4 v[168:171], v90, s[36:39], s49 offen offset:1024 sc1
mvv 8,116,128
s_sub_u32 s54, s54, 32768
mvv 12,120,128
s_waitcnt vmcnt(16)
mvv 0,116,132
mvv 4,120,132
s_waitcnt vmcnt(15) lgkmcnt(0)
v_cmp_gt_i32 vcc, v85, v76
mvv 8,116,136
s_waitcnt vmcnt(14)
s_add_u32 s49, s49, 313344
mvv 12,120,136
s_cbranch_vccnz CR0_13
BR0_13:
s_barrier
mvv 0,116,140
R 32,32768
mvv 4,120,140
s_waitcnt vmcnt(13)
buffer_load_dwordx4 v[172:175], v86, s[36:39], s49 offen sc1
mvv 8,116,144
mvv 12,120,144
s_waitcnt vmcnt(13)
mvv 0,116,148
R 36,33792
mvv 4,120,148
s_waitcnt vmcnt(12)
buffer_load_dwordx4 v[176:179], v86, s[36:39], s49 offen offset:1024 sc1
mvv 8,116,152
mvv 12,120,152
s_waitcnt vmcnt(12)
mvv 0,116,156
R 40,34816
mvv 4,120,156
s_waitcnt vmcnt(11)
buffer_load_dwordx4 v[180:183], v87, s[36:39], s49 offen sc1
mvv 8,116,160
mvv 12,120,160
s_waitcnt lgkmcnt(1)
v_mfma_f32_16x16x32_bf16 v[16:19], a[0:3], v[32:35], v[100:103]
R 44,35840
v_cvt_pk_bf16_f32 v64, v0, v1
v_mfma_f32_16x16x32_bf16 v[20:23], a[4:7], v[32:35], v[104:107]
buffer_load_dwordx4 v[184:187], v87, s[36:39], s49 offen offset:1024 sc1
v_mfma_f32_16x16x32_bf16 v[24:27], a[8:11], v[32:35], v[108:111]
v_cvt_pk_bf16_f32 v65, v2, v3
v_cvt_pk_bf16_f32 v66, v4, v5
v_mfma_f32_16x16x32_bf16 v[28:31], a[12:15], v[32:35], v[112:115]
v_cvt_pk_bf16_f32 v67, v6, v7
v_cvt_pk_bf16_f32 v68, v8, v9
mav 16,16,36
R 48,36864
v_cvt_pk_bf16_f32 v69, v10, v11
mav 20,20,36
buffer_load_dwordx4 v[188:191], v88, s[36:39], s49 offen sc1
mav 24,24,36
v_cvt_pk_bf16_f32 v70, v12, v13
v_cvt_pk_bf16_f32 v71, v14, v15
mav 28,28,36
s_waitcnt lgkmcnt(1)
mav 16,32,40
R 52,37888
mav 20,36,40
buffer_load_dwordx4 v[192:195], v88, s[36:39], s49 offen offset:1024 sc1
mav 24,40,40
mav 28,44,40
mav 16,48,44
R 56,38912
mav 20,52,44
buffer_load_dwordx4 v[196:199], v89, s[36:39], s49 offen sc1
mav 24,56,44
mav 28,60,44
s_waitcnt lgkmcnt(1)
mav 16,64,48
R 60,39936
mav 20,68,48
buffer_load_dwordx4 v[200:203], v89, s[36:39], s49 offen offset:1024 sc1
mav 24,72,48
mav 28,76,48
ds_write_b128 v94, v[64:67] offset:0
mav 16,80,52
s_mov_b32 m0, 144384
R 32,40960
mav 20,84,52
ds_write_b128 v95, v[68:71] offset:0
mav 24,88,52
buffer_load_dword v84, s[36:39], 0 offen sc1 lds
mav 28,92,52
s_waitcnt lgkmcnt(3)
ds_read_b128 v[64:67], v96 offset:0
s_add_u32 m0, s50, 32768
mav 16,96,56
R 36,41984
mav 20,100,56
D 83,0
mav 24,104,56
ds_read_b128 v[68:71], v96 offset:1024
mav 28,108,56
mav 16,112,60
R 40,43008
mav 20,116,60
D 83,1024
mav 24,120,60
mav 28,124,60
s_waitcnt lgkmcnt(2)
mav 16,128,32
R 44,44032
mav 20,132,32
mav 24,136,32
mav 28,140,32
mav 16,144,36
R 48,45056
mav 20,148,36
mav 24,152,36
mav 28,156,36
s_waitcnt lgkmcnt(1)
mav 16,160,40
s_mov_b32 s60, -13
.p2align 6
LR0_14:
mav 20,164,40
R 52,46080
v_add_u32 v85, 1, v85
mav 24,168,40
mav 28,172,40
mav 16,176,44
R 56,47104
mav 20,180,44
D 83,2048
mav 24,184,44
mav 28,188,44
s_waitcnt lgkmcnt(1)
mav 16,192,48
R 60,48128
mav 20,196,48
D 83,3072
mav 24,200,48
mav 28,204,48
mav 16,208,52
mav 20,212,52
buffer_store_dwordx4 v[64:67], v91, s[44:47], s54 offen sc1
mav 24,216,52
mav 28,220,52
s_waitcnt lgkmcnt(0)
mav 16,224,56
mav 20,228,56
buffer_store_dwordx4 v[68:71], v92, s[44:47], s54 offen sc1
mav 24,232,56
mav 28,236,56
mav 16,240,60
mav 20,244,60
buffer_load_dwordx4 v[124:127], v90, s[36:39], s49 offen sc1
mav 24,248,60
ds_read_b32 v76, v82 offset:0
mav 28,252,60
s_waitcnt vmcnt(17)
mvv 16,116,164
mvv 20,120,164
s_waitcnt vmcnt(16)
buffer_load_dwordx4 v[128:131], v90, s[36:39], s49 offen offset:1024 sc1
mvv 24,116,168
s_xor_b32 s50, s50, 0x10000
s_sub_u32 s54, s54, 32768
mvv 28,120,168
s_waitcnt vmcnt(16)
mvv 16,116,172
mvv 20,120,172
s_waitcnt vmcnt(15) lgkmcnt(0)
v_cmp_gt_i32 vcc, v85, v76
v_xor_b32 v80, 0x10000, v80
mvv 24,116,176
s_waitcnt vmcnt(14)
s_add_u32 s49, s49, 313344
mvv 28,120,176
s_cbranch_vccnz CR0_15
BR0_15:
s_barrier
mvv 16,116,180
R 32,0
mvv 20,120,180
s_waitcnt vmcnt(13)
buffer_load_dwordx4 v[132:135], v86, s[36:39], s49 offen sc1
mvv 24,116,184
mvv 28,120,184
s_waitcnt vmcnt(13)
mvv 16,116,188
R 36,1024
mvv 20,120,188
s_waitcnt vmcnt(12)
buffer_load_dwordx4 v[136:139], v86, s[36:39], s49 offen offset:1024 sc1
mvv 24,116,192
mvv 28,120,192
s_waitcnt vmcnt(12)
mvv 16,116,196
R 40,2048
mvv 20,120,196
s_waitcnt vmcnt(11)
buffer_load_dwordx4 v[140:143], v87, s[36:39], s49 offen sc1
mvv 24,116,200
mvv 28,120,200
s_waitcnt lgkmcnt(1)
v_mfma_f32_16x16x32_bf16 v[0:3], a[0:3], v[32:35], v[100:103]
R 44,3072
v_cvt_pk_bf16_f32 v64, v16, v17
v_mfma_f32_16x16x32_bf16 v[4:7], a[4:7], v[32:35], v[104:107]
buffer_load_dwordx4 v[144:147], v87, s[36:39], s49 offen offset:1024 sc1
v_mfma_f32_16x16x32_bf16 v[8:11], a[8:11], v[32:35], v[108:111]
v_cvt_pk_bf16_f32 v65, v18, v19
v_cvt_pk_bf16_f32 v66, v20, v21
v_mfma_f32_16x16x32_bf16 v[12:15], a[12:15], v[32:35], v[112:115]
v_cvt_pk_bf16_f32 v67, v22, v23
v_cvt_pk_bf16_f32 v68, v24, v25
mav 0,16,36
R 48,4096
v_cvt_pk_bf16_f32 v69, v26, v27
mav 4,20,36
buffer_load_dwordx4 v[148:151], v88, s[36:39], s49 offen sc1
mav 8,24,36
v_cvt_pk_bf16_f32 v70, v28, v29
v_cvt_pk_bf16_f32 v71, v30, v31
mav 12,28,36
s_waitcnt lgkmcnt(1)
mav 0,32,40
R 52,5120
mav 4,36,40
buffer_load_dwordx4 v[152:155], v88, s[36:39], s49 offen offset:1024 sc1
mav 8,40,40
mav 12,44,40
mav 0,48,44
R 56,6144
mav 4,52,44
buffer_load_dwordx4 v[156:159], v89, s[36:39], s49 offen sc1
mav 8,56,44
mav 12,60,44
s_waitcnt lgkmcnt(1)
mav 0,64,48
R 60,7168
mav 4,68,48
buffer_load_dwordx4 v[160:163], v89, s[36:39], s49 offen offset:1024 sc1
mav 8,72,48
mav 12,76,48
ds_write_b128 v94, v[64:67] offset:0
mav 0,80,52
s_mov_b32 m0, 144384
R 32,8192
mav 4,84,52
ds_write_b128 v95, v[68:71] offset:0
mav 8,88,52
buffer_load_dword v84, s[36:39], 0 offen sc1 lds
mav 12,92,52
s_waitcnt lgkmcnt(3)
LsR4:
s_cbranch_execz LsR8
ds_read_b128 v[64:67], v96 offset:0
s_add_u32 m0, s50, 0
mav 0,96,56
R 36,9216
mav 4,100,56
D 83,0
mav 8,104,56
ds_read_b128 v[68:71], v96 offset:1024
mav 12,108,56
mav 0,112,60
R 40,10240
mav 4,116,60
D 83,1024
mav 8,120,60
mav 12,124,60
s_waitcnt lgkmcnt(2)
mav 0,128,32
R 44,11264
mav 4,132,32
mav 8,136,32
mav 12,140,32
mav 0,144,36
R 48,12288
mav 4,148,36
mav 8,152,36
mav 12,156,36
s_waitcnt lgkmcnt(1)
mav 0,160,40
R 52,13312
v_add_u32 v85, 1, v85
mav 4,164,40
mav 8,168,40
mav 12,172,40
mav 0,176,44
R 56,14336
mav 4,180,44
D 83,2048
mav 8,184,44
mav 12,188,44
s_waitcnt lgkmcnt(1)
mav 0,192,48
R 60,15360
mav 4,196,48
D 83,3072
mav 8,200,48
mav 12,204,48
mav 0,208,52
mav 4,212,52
buffer_store_dwordx4 v[64:67], v91, s[44:47], s54 offen sc1
mav 8,216,52
mav 12,220,52
s_waitcnt lgkmcnt(0)
mav 0,224,56
mav 4,228,56
buffer_store_dwordx4 v[68:71], v92, s[44:47], s54 offen sc1
mav 8,232,56
mav 12,236,56
mav 0,240,60
mav 4,244,60
buffer_load_dwordx4 v[164:167], v90, s[36:39], s49 offen sc1
mav 8,248,60
ds_read_b32 v76, v82 offset:0
mav 12,252,60
s_waitcnt vmcnt(17)
mvv 0,116,124
mvv 4,120,124
s_waitcnt vmcnt(16)
buffer_load_dwordx4 v[168:171], v90, s[36:39], s49 offen offset:1024 sc1
mvv 8,116,128
s_sub_u32 s54, s54, 32768
mvv 12,120,128
s_waitcnt vmcnt(16)
mvv 0,116,132
mvv 4,120,132
s_waitcnt vmcnt(15) lgkmcnt(0)
v_cmp_gt_i32 vcc, v85, v76
mvv 8,116,136
s_waitcnt vmcnt(14)
s_add_u32 s49, s49, 313344
mvv 12,120,136
s_cbranch_vccnz CR0_16
BR0_16:
s_barrier
mvv 0,116,140
R 32,32768
mvv 4,120,140
s_waitcnt vmcnt(13)
buffer_load_dwordx4 v[172:175], v86, s[36:39], s49 offen sc1
mvv 8,116,144
mvv 12,120,144
s_waitcnt vmcnt(13)
mvv 0,116,148
R 36,33792
mvv 4,120,148
s_waitcnt vmcnt(12)
buffer_load_dwordx4 v[176:179], v86, s[36:39], s49 offen offset:1024 sc1
mvv 8,116,152
mvv 12,120,152
s_waitcnt vmcnt(12)
mvv 0,116,156
R 40,34816
mvv 4,120,156
s_waitcnt vmcnt(11)
buffer_load_dwordx4 v[180:183], v87, s[36:39], s49 offen sc1
mvv 8,116,160
mvv 12,120,160
s_waitcnt lgkmcnt(1)
v_mfma_f32_16x16x32_bf16 v[16:19], a[0:3], v[32:35], v[100:103]
R 44,35840
v_cvt_pk_bf16_f32 v64, v0, v1
v_mfma_f32_16x16x32_bf16 v[20:23], a[4:7], v[32:35], v[104:107]
buffer_load_dwordx4 v[184:187], v87, s[36:39], s49 offen offset:1024 sc1
v_mfma_f32_16x16x32_bf16 v[24:27], a[8:11], v[32:35], v[108:111]
v_cvt_pk_bf16_f32 v65, v2, v3
v_cvt_pk_bf16_f32 v66, v4, v5
v_mfma_f32_16x16x32_bf16 v[28:31], a[12:15], v[32:35], v[112:115]
v_cvt_pk_bf16_f32 v67, v6, v7
v_cvt_pk_bf16_f32 v68, v8, v9
mav 16,16,36
R 48,36864
v_cvt_pk_bf16_f32 v69, v10, v11
mav 20,20,36
buffer_load_dwordx4 v[188:191], v88, s[36:39], s49 offen sc1
mav 24,24,36
v_cvt_pk_bf16_f32 v70, v12, v13
v_cvt_pk_bf16_f32 v71, v14, v15
mav 28,28,36
s_waitcnt lgkmcnt(1)
mav 16,32,40
R 52,37888
mav 20,36,40
buffer_load_dwordx4 v[192:195], v88, s[36:39], s49 offen offset:1024 sc1
mav 24,40,40
mav 28,44,40
mav 16,48,44
R 56,38912
mav 20,52,44
buffer_load_dwordx4 v[196:199], v89, s[36:39], s49 offen sc1
mav 24,56,44
mav 28,60,44
s_waitcnt lgkmcnt(1)
mav 16,64,48
R 60,39936
mav 20,68,48
buffer_load_dwordx4 v[200:203], v89, s[36:39], s49 offen offset:1024 sc1
mav 24,72,48
mav 28,76,48
ds_write_b128 v94, v[64:67] offset:0
mav 16,80,52
s_mov_b32 m0, 144384
R 32,40960
mav 20,84,52
ds_write_b128 v95, v[68:71] offset:0
mav 24,88,52
buffer_load_dword v84, s[36:39], 0 offen sc1 lds
mav 28,92,52
s_waitcnt lgkmcnt(3)
ds_read_b128 v[64:67], v96 offset:0
s_add_u32 m0, s50, 32768
mav 16,96,56
R 36,41984
mav 20,100,56
D 83,0
mav 24,104,56
ds_read_b128 v[68:71], v96 offset:1024
mav 28,108,56
mav 16,112,60
R 40,43008
mav 20,116,60
D 83,1024
mav 24,120,60
mav 28,124,60
s_waitcnt lgkmcnt(2)
mav 16,128,32
R 44,44032
mav 20,132,32
mav 24,136,32
mav 28,140,32
mav 16,144,36
R 48,45056
mav 20,148,36
mav 24,152,36
mav 28,156,36
s_waitcnt lgkmcnt(1)
s_add_u32 s60, s60, 1
mav 16,160,40
s_cbranch_scc0 LR0_14
mav 20,164,40
R 52,46080
v_add_u32 v85, 1, v85
mav 24,168,40
mav 28,172,40
mav 16,176,44
R 56,47104
mav 20,180,44
D 83,2048
mav 24,184,44
mav 28,188,44
s_waitcnt lgkmcnt(1)
mav 16,192,48
R 60,48128
mav 20,196,48
D 83,3072
mav 24,200,48
mav 28,204,48
mav 16,208,52
mav 20,212,52
buffer_store_dwordx4 v[64:67], v91, s[44:47], s54 offen sc1
mav 24,216,52
mav 28,220,52
s_waitcnt lgkmcnt(0)
mav 16,224,56
mav 20,228,56
buffer_store_dwordx4 v[68:71], v92, s[44:47], s54 offen sc1
mav 24,232,56
mav 28,236,56
mav 16,240,60
mav 20,244,60
buffer_load_dwordx4 v[124:127], v90, s[36:39], s49 offen sc1
mav 24,248,60
ds_read_b32 v76, v82 offset:0
mav 28,252,60
s_waitcnt vmcnt(17)
mvv 16,116,164
mvv 20,120,164
s_waitcnt vmcnt(16)
buffer_load_dwordx4 v[128:131], v90, s[36:39], s49 offen offset:1024 sc1
mvv 24,116,168
s_sub_u32 s54, s54, 32768
mvv 28,120,168
s_waitcnt vmcnt(16)
mvv 16,116,172
mvv 20,120,172
s_waitcnt vmcnt(15) lgkmcnt(0)
v_cmp_gt_i32 vcc, v85, v76
v_xor_b32 v80, 0x10000, v80
mvv 24,116,176
s_waitcnt vmcnt(14)
s_add_u32 s49, s49, 313344
mvv 28,120,176
s_cbranch_vccnz CR0_17
BR0_17:
s_barrier
mvv 16,116,180
R 32,0
mvv 20,120,180
s_waitcnt vmcnt(13)
buffer_load_dwordx4 v[132:135], v86, s[36:39], s49 offen sc1
mvv 24,116,184
mvv 28,120,184
s_waitcnt vmcnt(13)
mvv 16,116,188
R 36,1024
mvv 20,120,188
s_waitcnt vmcnt(12)
buffer_load_dwordx4 v[136:139], v86, s[36:39], s49 offen offset:1024 sc1
mvv 24,116,192
mvv 28,120,192
s_waitcnt vmcnt(12)
mvv 16,116,196
R 40,2048
mvv 20,120,196
s_waitcnt vmcnt(11)
buffer_load_dwordx4 v[140:143], v87, s[36:39], s49 offen sc1
mvv 24,116,200
mvv 28,120,200
s_waitcnt lgkmcnt(1)
v_mfma_f32_16x16x32_bf16 v[0:3], a[0:3], v[32:35], v[100:103]
R 44,3072
v_cvt_pk_bf16_f32 v64, v16, v17
v_mfma_f32_16x16x32_bf16 v[4:7], a[4:7], v[32:35], v[104:107]
buffer_load_dwordx4 v[144:147], v87, s[36:39], s49 offen offset:1024 sc1
v_mfma_f32_16x16x32_bf16 v[8:11], a[8:11], v[32:35], v[108:111]
v_cvt_pk_bf16_f32 v65, v18, v19
v_cvt_pk_bf16_f32 v66, v20, v21
v_mfma_f32_16x16x32_bf16 v[12:15], a[12:15], v[32:35], v[112:115]
v_cvt_pk_bf16_f32 v67, v22, v23
v_cvt_pk_bf16_f32 v68, v24, v25
mav 0,16,36
R 48,4096
v_cvt_pk_bf16_f32 v69, v26, v27
mav 4,20,36
buffer_load_dwordx4 v[148:151], v88, s[36:39], s49 offen sc1
mav 8,24,36
v_cvt_pk_bf16_f32 v70, v28, v29
v_cvt_pk_bf16_f32 v71, v30, v31
mav 12,28,36
s_waitcnt lgkmcnt(1)
mav 0,32,40
R 52,5120
mav 4,36,40
buffer_load_dwordx4 v[152:155], v88, s[36:39], s49 offen offset:1024 sc1
mav 8,40,40
mav 12,44,40
mav 0,48,44
R 56,6144
mav 4,52,44
buffer_load_dwordx4 v[156:159], v89, s[36:39], s49 offen sc1
mav 8,56,44
mav 12,60,44
s_waitcnt lgkmcnt(1)
mav 0,64,48
R 60,7168
mav 4,68,48
buffer_load_dwordx4 v[160:163], v89, s[36:39], s49 offen offset:1024 sc1
mav 8,72,48
mav 12,76,48
ds_write_b128 v94, v[64:67] offset:0
mav 0,80,52
s_mov_b32 m0, 144384
R 32,8192
mav 4,84,52
ds_write_b128 v95, v[68:71] offset:0
mav 8,88,52
buffer_load_dword v84, s[36:39], 0 offen sc1 lds
mav 12,92,52
s_waitcnt lgkmcnt(3)
ds_read_b128 v[64:67], v96 offset:0
mav 0,96,56
R 36,9216
mav 4,100,56
ds_read_b128 v[68:71], v96 offset:1024
mav 8,104,56
mav 12,108,56
mav 0,112,60
R 40,10240
mav 4,116,60
mav 8,120,60
mav 12,124,60
s_waitcnt lgkmcnt(1)
mav 0,128,32
R 44,11264
mav 4,132,32
mav 8,136,32
mav 12,140,32
mav 0,144,36
R 48,12288
mav 4,148,36
mav 8,152,36
mav 12,156,36
s_waitcnt lgkmcnt(1)
mav 0,160,40
R 52,13312
v_add_u32 v85, 1, v85
mav 4,164,40
mav 8,168,40
mav 12,172,40
mav 0,176,44
R 56,14336
mav 4,180,44
mav 8,184,44
mav 12,188,44
s_waitcnt lgkmcnt(1)
mav 0,192,48
R 60,15360
mav 4,196,48
mav 8,200,48
mav 12,204,48
mav 0,208,52
mav 4,212,52
buffer_store_dwordx4 v[64:67], v91, s[44:47], s54 offen sc1
mav 8,216,52
mav 12,220,52
s_waitcnt lgkmcnt(0)
mav 0,224,56
mav 4,228,56
buffer_store_dwordx4 v[68:71], v92, s[44:47], s54 offen sc1
mav 8,232,56
mav 12,236,56
mav 0,240,60
mav 4,244,60
buffer_load_dwordx4 v[164:167], v90, s[36:39], s49 offen sc1
mav 8,248,60
ds_read_b32 v76, v82 offset:0
mav 12,252,60
s_waitcnt vmcnt(13)
mvv 0,116,124
mvv 4,120,124
s_waitcnt vmcnt(12)
buffer_load_dwordx4 v[168:171], v90, s[36:39], s49 offen offset:1024 sc1
mvv 8,116,128
s_sub_u32 s54, s54, 32768
mvv 12,120,128
s_waitcnt vmcnt(12)
mvv 0,116,132
mvv 4,120,132
s_waitcnt vmcnt(11) lgkmcnt(0)
v_cmp_gt_i32 vcc, v85, v76
mvv 8,116,136
s_waitcnt vmcnt(10)
s_add_u32 s49, s49, 313344
mvv 12,120,136
s_cbranch_vccnz CR0_18
BR0_18:
s_barrier
mvv 0,116,140
R 32,32768
mvv 4,120,140
s_waitcnt vmcnt(9)
buffer_load_dwordx4 v[172:175], v86, s[36:39], s49 offen sc1
mvv 8,116,144
mvv 12,120,144
s_waitcnt vmcnt(9)
mvv 0,116,148
R 36,33792
mvv 4,120,148
s_waitcnt vmcnt(8)
buffer_load_dwordx4 v[176:179], v86, s[36:39], s49 offen offset:1024 sc1
mvv 8,116,152
mvv 12,120,152
s_waitcnt vmcnt(8)
mvv 0,116,156
R 40,34816
mvv 4,120,156
s_waitcnt vmcnt(7)
buffer_load_dwordx4 v[180:183], v87, s[36:39], s49 offen sc1
mvv 8,116,160
mvv 12,120,160
s_waitcnt lgkmcnt(1)
v_mfma_f32_16x16x32_bf16 v[16:19], a[0:3], v[32:35], v[100:103]
R 44,35840
v_cvt_pk_bf16_f32 v64, v0, v1
v_mfma_f32_16x16x32_bf16 v[20:23], a[4:7], v[32:35], v[104:107]
buffer_load_dwordx4 v[184:187], v87, s[36:39], s49 offen offset:1024 sc1
v_mfma_f32_16x16x32_bf16 v[24:27], a[8:11], v[32:35], v[108:111]
v_cvt_pk_bf16_f32 v65, v2, v3
v_cvt_pk_bf16_f32 v66, v4, v5
v_mfma_f32_16x16x32_bf16 v[28:31], a[12:15], v[32:35], v[112:115]
v_cvt_pk_bf16_f32 v67, v6, v7
v_cvt_pk_bf16_f32 v68, v8, v9
mav 16,16,36
R 48,36864
v_cvt_pk_bf16_f32 v69, v10, v11
mav 20,20,36
buffer_load_dwordx4 v[188:191], v88, s[36:39], s49 offen sc1
mav 24,24,36
ds_write_b128 v94, v[64:67] offset:0
mav 28,28,36
s_waitcnt lgkmcnt(2)
v_cvt_pk_bf16_f32 v70, v12, v13
v_cvt_pk_bf16_f32 v71, v14, v15
mav 16,32,40
R 52,37888
mav 20,36,40
buffer_load_dwordx4 v[192:195], v88, s[36:39], s49 offen offset:1024 sc1
mav 24,40,40
ds_write_b128 v95, v[68:71] offset:0
mav 28,44,40
ds_read_b128 v[64:67], v96 offset:0
mav 16,48,44
R 56,38912
mav 20,52,44
buffer_load_dwordx4 v[196:199], v89, s[36:39], s49 offen sc1
mav 24,56,44
ds_read_b128 v[68:71], v96 offset:1024
mav 28,60,44
s_waitcnt lgkmcnt(4)
mav 16,64,48
R 60,39936
mav 20,68,48
buffer_load_dwordx4 v[200:203], v89, s[36:39], s49 offen offset:1024 sc1
mav 24,72,48
s_waitcnt lgkmcnt(2)
mav 28,76,48
mav 16,80,52
R 32,40960
mav 20,84,52
buffer_store_dwordx4 v[64:67], v91, s[44:47], s54 offen sc1
s_waitcnt lgkmcnt(2)
mav 24,88,52
mav 28,92,52
mav 16,96,56
R 36,41984
mav 20,100,56
buffer_store_dwordx4 v[68:71], v92, s[44:47], s54 offen sc1
mav 24,104,56
mav 28,108,56
s_waitcnt lgkmcnt(1)
mav 16,112,60
R 40,43008
mav 20,116,60
mav 24,120,60
mav 28,124,60
mav 16,128,32
R 44,44032
mav 20,132,32
mav 24,136,32
mav 28,140,32
s_waitcnt lgkmcnt(1)
mav 16,144,36
R 48,45056
mav 20,148,36
mav 24,152,36
mav 28,156,36
mav 16,160,40
mav 20,164,40
R 52,46080
mav 24,168,40
mav 28,172,40
s_waitcnt lgkmcnt(1)
mav 16,176,44
R 56,47104
mav 20,180,44
mav 24,184,44
mav 28,188,44
mav 16,192,48
R 60,48128
mav 20,196,48
mav 24,200,48
mav 28,204,48
s_waitcnt lgkmcnt(1)
mav 16,208,52
mav 20,212,52
mav 24,216,52
mav 28,220,52
mav 16,224,56
mav 20,228,56
mav 24,232,56
mav 28,236,56
s_waitcnt lgkmcnt(0)
mav 16,240,60
mav 20,244,60
mav 24,248,60
mav 28,252,60
s_waitcnt vmcnt(11)
mvv 16,116,164
mvv 20,120,164
s_waitcnt vmcnt(10)
mvv 24,116,168
s_sub_u32 s54, s54, 32768
mvv 28,120,168
s_waitcnt vmcnt(9)
mvv 16,116,172
mvv 20,120,172
s_waitcnt vmcnt(8)
mvv 24,116,176
mvv 28,120,176
s_waitcnt vmcnt(7)
mvv 16,116,180
mvv 20,120,180
s_waitcnt vmcnt(6)
mvv 24,116,184
mvv 28,120,184
s_waitcnt vmcnt(5)
mvv 16,116,188
mvv 20,120,188
s_waitcnt vmcnt(4)
mvv 24,116,192
mvv 28,120,192
s_waitcnt vmcnt(3)
mvv 16,116,196
mvv 20,120,196
s_waitcnt vmcnt(2)
mvv 24,116,200
mvv 28,120,200
s_nop 3
v_cvt_pk_bf16_f32 v64, v16, v17
v_cvt_pk_bf16_f32 v65, v18, v19
LsR5:
s_cbranch_execz LsR9
v_cvt_pk_bf16_f32 v66, v20, v21
v_cvt_pk_bf16_f32 v67, v22, v23
ds_write_b128 v94, v[64:67] offset:0
v_cvt_pk_bf16_f32 v68, v24, v25
v_cvt_pk_bf16_f32 v69, v26, v27
v_cvt_pk_bf16_f32 v70, v28, v29
v_cvt_pk_bf16_f32 v71, v30, v31
ds_write_b128 v95, v[68:71] offset:0
ds_read_b128 v[64:67], v96 offset:0
ds_read_b128 v[68:71], v96 offset:1024
s_waitcnt lgkmcnt(0)
buffer_store_dwordx4 v[64:67], v91, s[44:47], s54 offen sc1
buffer_store_dwordx4 v[68:71], v92, s[44:47], s54 offen sc1
s_endpgm
CR0_1:
s_cmp_eq_u32 s61, 0
s_cbranch_scc1 BR0_1
s_sub_u32 s61, s61, 1
s_sleep 8
buffer_load_dwordx2 v[76:77], v84, s[36:39], 0 offen sc1
s_waitcnt vmcnt(0)
ds_write_b32 v82, v76
v_sub_u32 v76, v76, v85
v_xor_b32 v77, v77, v73
v_cmp_lt_u64 vcc, s[62:63], v[76:77]
s_waitcnt lgkmcnt(0)
s_cbranch_vccnz CR0_1
s_branch BR0_1
CR0_2:
s_mov_b32 s58, m0
s_mov_b32 m0, 144384
DR0_2:
s_cmp_eq_u32 s61, 0
s_cbranch_scc1 ER0_2
s_sub_u32 s61, s61, 1
buffer_load_dword v84, s[36:39], 0 offen sc1 lds
s_sleep 1
ds_read_b32 v76, v82
s_waitcnt lgkmcnt(0)
v_cmp_gt_i32 vcc, v85, v76
s_cbranch_vccnz DR0_2
ER0_2:
s_mov_b32 m0, s58
s_branch BR0_2
CR0_3:
s_mov_b32 s58, m0
s_mov_b32 m0, 144384
DR0_3:
s_cmp_eq_u32 s61, 0
s_cbranch_scc1 ER0_3
s_sub_u32 s61, s61, 1
buffer_load_dword v84, s[36:39], 0 offen sc1 lds
s_sleep 1
ds_read_b32 v76, v82
s_waitcnt lgkmcnt(0)
v_cmp_gt_i32 vcc, v85, v76
s_cbranch_vccnz DR0_3
ER0_3:
s_mov_b32 m0, s58
s_branch BR0_3
CR0_4:
s_mov_b32 s58, m0
s_mov_b32 m0, 144384
DR0_4:
s_cmp_eq_u32 s61, 0
s_cbranch_scc1 ER0_4
s_sub_u32 s61, s61, 1
buffer_load_dword v84, s[36:39], 0 offen sc1 lds
s_sleep 1
ds_read_b32 v76, v82
s_waitcnt lgkmcnt(0)
v_cmp_gt_i32 vcc, v85, v76
s_cbranch_vccnz DR0_4
ER0_4:
s_mov_b32 m0, s58
s_branch BR0_4
CR0_5:
s_mov_b32 s58, m0
s_mov_b32 m0, 144384
DR0_5:
s_cmp_eq_u32 s61, 0
s_cbranch_scc1 ER0_5
s_sub_u32 s61, s61, 1
buffer_load_dword v84, s[36:39], 0 offen sc1 lds
s_sleep 1
ds_read_b32 v76, v82
s_waitcnt lgkmcnt(0)
v_cmp_gt_i32 vcc, v85, v76
s_cbranch_vccnz DR0_5
ER0_5:
s_mov_b32 m0, s58
s_branch BR0_5
CR0_6:
s_mov_b32 s58, m0
s_mov_b32 m0, 144384
DR0_6:
s_cmp_eq_u32 s61, 0
s_cbranch_scc1 ER0_6
s_sub_u32 s61, s61, 1
buffer_load_dword v84, s[36:39], 0 offen sc1 lds
s_sleep 1
ds_read_b32 v76, v82
s_waitcnt lgkmcnt(0)
v_cmp_gt_i32 vcc, v85, v76
s_cbranch_vccnz DR0_6
ER0_6:
s_mov_b32 m0, s58
s_branch BR0_6
CR0_8:
s_mov_b32 s58, m0
s_mov_b32 m0, 144384
DR0_8:
s_cmp_eq_u32 s61, 0
s_cbranch_scc1 ER0_8
s_sub_u32 s61, s61, 1
buffer_load_dword v84, s[36:39], 0 offen sc1 lds
s_sleep 1
ds_read_b32 v76, v82
s_waitcnt lgkmcnt(0)
v_cmp_gt_i32 vcc, v85, v76
s_cbranch_vccnz DR0_8
ER0_8:
s_mov_b32 m0, s58
s_branch BR0_8
CR0_9:
s_mov_b32 s58, m0
s_mov_b32 m0, 144384
DR0_9:
s_cmp_eq_u32 s61, 0
s_cbranch_scc1 ER0_9
s_sub_u32 s61, s61, 1
buffer_load_dword v84, s[36:39], 0 offen sc1 lds
s_sleep 1
ds_read_b32 v76, v82
s_waitcnt lgkmcnt(0)
v_cmp_gt_i32 vcc, v85, v76
s_cbranch_vccnz DR0_9
ER0_9:
s_mov_b32 m0, s58
s_branch BR0_9
CR0_10:
s_mov_b32 s58, m0
s_mov_b32 m0, 144384
DR0_10:
s_cmp_eq_u32 s61, 0
s_cbranch_scc1 ER0_10
s_sub_u32 s61, s61, 1
buffer_load_dword v84, s[36:39], 0 offen sc1 lds
s_sleep 1
ds_read_b32 v76, v82
s_waitcnt lgkmcnt(0)
v_cmp_gt_i32 vcc, v85, v76
s_cbranch_vccnz DR0_10
ER0_10:
s_mov_b32 m0, s58
s_branch BR0_10
CR0_11:
s_mov_b32 s58, m0
s_mov_b32 m0, 144384
DR0_11:
s_cmp_eq_u32 s61, 0
s_cbranch_scc1 ER0_11
s_sub_u32 s61, s61, 1
buffer_load_dword v84, s[36:39], 0 offen sc1 lds
s_sleep 1
ds_read_b32 v76, v82
s_waitcnt lgkmcnt(0)
v_cmp_gt_i32 vcc, v85, v76
s_cbranch_vccnz DR0_11
ER0_11:
s_mov_b32 m0, s58
s_branch BR0_11
CR0_12:
s_mov_b32 s58, m0
s_mov_b32 m0, 144384
DR0_12:
s_cmp_eq_u32 s61, 0
s_cbranch_scc1 ER0_12
s_sub_u32 s61, s61, 1
buffer_load_dword v84, s[36:39], 0 offen sc1 lds
s_sleep 1
ds_read_b32 v76, v82
s_waitcnt lgkmcnt(0)
v_cmp_gt_i32 vcc, v85, v76
s_cbranch_vccnz DR0_12
ER0_12:
s_mov_b32 m0, s58
s_branch BR0_12
CR0_13:
s_mov_b32 s58, m0
s_mov_b32 m0, 144384
DR0_13:
s_cmp_eq_u32 s61, 0
s_cbranch_scc1 ER0_13
s_sub_u32 s61, s61, 1
buffer_load_dword v84, s[36:39], 0 offen sc1 lds
s_sleep 1
ds_read_b32 v76, v82
s_waitcnt lgkmcnt(0)
v_cmp_gt_i32 vcc, v85, v76
s_cbranch_vccnz DR0_13
ER0_13:
s_mov_b32 m0, s58
s_branch BR0_13
CR0_15:
s_mov_b32 s58, m0
s_mov_b32 m0, 144384
DR0_15:
s_cmp_eq_u32 s61, 0
s_cbranch_scc1 ER0_15
s_sub_u32 s61, s61, 1
buffer_load_dword v84, s[36:39], 0 offen sc1 lds
s_sleep 1
ds_read_b32 v76, v82
s_waitcnt lgkmcnt(0)
v_cmp_gt_i32 vcc, v85, v76
s_cbranch_vccnz DR0_15
ER0_15:
s_mov_b32 m0, s58
s_branch BR0_15
CR0_16:
s_mov_b32 s58, m0
s_mov_b32 m0, 144384
DR0_16:
s_cmp_eq_u32 s61, 0
s_cbranch_scc1 ER0_16
s_sub_u32 s61, s61, 1
buffer_load_dword v84, s[36:39], 0 offen sc1 lds
s_sleep 1
ds_read_b32 v76, v82
s_waitcnt lgkmcnt(0)
v_cmp_gt_i32 vcc, v85, v76
s_cbranch_vccnz DR0_16
ER0_16:
s_mov_b32 m0, s58
s_branch BR0_16
CR0_17:
s_mov_b32 s58, m0
s_mov_b32 m0, 144384
DR0_17:
s_cmp_eq_u32 s61, 0
s_cbranch_scc1 ER0_17
s_sub_u32 s61, s61, 1
buffer_load_dword v84, s[36:39], 0 offen sc1 lds
s_sleep 1
ds_read_b32 v76, v82
s_waitcnt lgkmcnt(0)
v_cmp_gt_i32 vcc, v85, v76
s_cbranch_vccnz DR0_17
ER0_17:
s_mov_b32 m0, s58
s_branch BR0_17
CR0_18:
s_mov_b32 s58, m0
s_mov_b32 m0, 144384
DR0_18:
s_cmp_eq_u32 s61, 0
s_cbranch_scc1 ER0_18
s_sub_u32 s61, s61, 1
buffer_load_dword v84, s[36:39], 0 offen sc1 lds
s_sleep 1
ds_read_b32 v76, v82
s_waitcnt lgkmcnt(0)
v_cmp_gt_i32 vcc, v85, v76
s_cbranch_vccnz DR0_18
ER0_18:
s_mov_b32 m0, s58
s_branch BR0_18
LvR1:
LsR6:
s_cbranch_execz LsR10
s_add_u32 s49, s49, 313344
s_barrier
s_add_u32 m0, s50, 0
s_nop 0
D 83,0
D 83,1024
D 83,2048
D 83,3072
s_add_u32 s49, s49, 313344
s_barrier
s_add_u32 m0, s50, 32768
s_nop 0
D 83,0
D 83,1024
D 83,2048
D 83,3072
buffer_load_dwordx4 v[124:127], v90, s[36:39], s49 offen sc1
buffer_load_dwordx4 v[128:131], v90, s[36:39], s49 offen offset:1024 sc1
s_xor_b32 s50, s50, 0x10000
s_waitcnt vmcnt(6)
s_add_u32 s49, s49, 313344
s_barrier
R 32,0
buffer_load_dwordx4 v[132:135], v86, s[36:39], s49 offen sc1
R 36,1024
buffer_load_dwordx4 v[136:139], v86, s[36:39], s49 offen offset:1024 sc1
R 40,2048
buffer_load_dwordx4 v[140:143], v87, s[36:39], s49 offen sc1
s_waitcnt lgkmcnt(1)
v_mfma_f32_16x16x32_bf16 v[0:3], a[0:3], v[32:35], v[100:103]
R 44,3072
v_mfma_f32_16x16x32_bf16 v[4:7], a[4:7], v[32:35], v[104:107]
v_mfma_f32_16x16x32_bf16 v[8:11], a[8:11], v[32:35], v[108:111]
v_mfma_f32_16x16x32_bf16 v[12:15], a[12:15], v[32:35], v[112:115]
mav 0,16,36
buffer_load_dwordx4 v[144:147], v87, s[36:39], s49 offen offset:1024 sc1
mav 4,20,36
R 48,4096
mav 8,24,36
mav 12,28,36
s_waitcnt lgkmcnt(1)
mav 0,32,40
buffer_load_dwordx4 v[148:151], v88, s[36:39], s49 offen sc1
mav 4,36,40
R 52,5120
mav 8,40,40
mav 12,44,40
mav 0,48,44
buffer_load_dwordx4 v[152:155], v88, s[36:39], s49 offen offset:1024 sc1
mav 4,52,44
R 56,6144
mav 8,56,44
mav 12,60,44
s_waitcnt lgkmcnt(1)
mav 0,64,48
buffer_load_dwordx4 v[156:159], v89, s[36:39], s49 offen sc1
mav 4,68,48
R 60,7168
mav 8,72,48
mav 12,76,48
mav 0,80,52
buffer_load_dwordx4 v[160:163], v89, s[36:39], s49 offen offset:1024 sc1
mav 4,84,52
R 32,8192
mav 8,88,52
s_add_u32 m0, s50, 0
mav 12,92,52
s_waitcnt lgkmcnt(1)
mav 0,96,56
D 83,0
mav 4,100,56
R 36,9216
mav 8,104,56
mav 12,108,56
mav 0,112,60
D 83,1024
mav 4,116,60
R 40,10240
mav 8,120,60
mav 12,124,60
s_waitcnt lgkmcnt(1)
mav 0,128,32
R 44,11264
mav 4,132,32
mav 8,136,32
mav 12,140,32
mav 0,144,36
R 48,12288
mav 4,148,36
mav 8,152,36
mav 12,156,36
s_waitcnt lgkmcnt(1)
mav 0,160,40
R 52,13312
mav 4,164,40
mav 8,168,40
mav 12,172,40
mav 0,176,44
D 83,2048
mav 4,180,44
R 56,14336
mav 8,184,44
mav 12,188,44
s_waitcnt lgkmcnt(1)
mav 0,192,48
D 83,3072
mav 4,196,48
R 60,15360
mav 8,200,48
mav 12,204,48
mav 0,208,52
mav 4,212,52
mav 8,216,52
mav 12,220,52
s_waitcnt lgkmcnt(0)
mav 0,224,56
mav 4,228,56
mav 8,232,56
mav 12,236,56
mav 0,240,60
buffer_load_dwordx4 v[164:167], v90, s[36:39], s49 offen sc1
mav 4,244,60
mav 8,248,60
mav 12,252,60
s_waitcnt vmcnt(14)
mvv 0,116,124
buffer_load_dwordx4 v[168:171], v90, s[36:39], s49 offen offset:1024 sc1
mvv 4,120,124
s_waitcnt vmcnt(14)
mvv 8,116,128
mvv 12,120,128
s_waitcnt vmcnt(13)
mvv 0,116,132
mvv 4,120,132
s_waitcnt vmcnt(12)
mvv 8,116,136
s_waitcnt vmcnt(11)
s_add_u32 s49, s49, 313344
mvv 12,120,136
s_barrier
mvv 0,116,140
R 32,32768
mvv 4,120,140
s_waitcnt vmcnt(10)
mvv 8,116,144
mvv 12,120,144
s_waitcnt vmcnt(9)
mvv 0,116,148
buffer_load_dwordx4 v[172:175], v86, s[36:39], s49 offen sc1
mvv 4,120,148
s_waitcnt vmcnt(9)
R 36,33792
mvv 8,116,152
mvv 12,120,152
s_waitcnt vmcnt(8)
mvv 0,116,156
buffer_load_dwordx4 v[176:179], v86, s[36:39], s49 offen offset:1024 sc1
mvv 4,120,156
s_waitcnt vmcnt(8)
R 40,34816
mvv 8,116,160
mvv 12,120,160
s_waitcnt lgkmcnt(1)
v_mfma_f32_16x16x32_bf16 v[16:19], a[0:3], v[32:35], v[100:103]
buffer_load_dwordx4 v[180:183], v87, s[36:39], s49 offen sc1
v_mfma_f32_16x16x32_bf16 v[20:23], a[4:7], v[32:35], v[104:107]
R 44,35840
v_cvt_pk_bf16_f32 v64, v0, v1
v_mfma_f32_16x16x32_bf16 v[24:27], a[8:11], v[32:35], v[108:111]
v_cvt_pk_bf16_f32 v65, v2, v3
v_cvt_pk_bf16_f32 v66, v4, v5
v_mfma_f32_16x16x32_bf16 v[28:31], a[12:15], v[32:35], v[112:115]
v_cvt_pk_bf16_f32 v67, v6, v7
v_cvt_pk_bf16_f32 v68, v8, v9
mav 16,16,36
buffer_load_dwordx4 v[184:187], v87, s[36:39], s49 offen offset:1024 sc1
mav 20,20,36
R 48,36864
v_cvt_pk_bf16_f32 v69, v10, v11
mav 24,24,36
v_cvt_pk_bf16_f32 v70, v12, v13
v_cvt_pk_bf16_f32 v71, v14, v15
mav 28,28,36
s_waitcnt lgkmcnt(1)
v_mul_f32 v75, v0, v0
v_add_f32 v74, v1, v0
mav 16,32,40
buffer_load_dwordx4 v[188:191], v88, s[36:39], s49 offen sc1
mav 20,36,40
R 52,37888
v_fmac_f32 v75, v1, v1
mav 24,40,40
v_add_f32 v74, v2, v74
v_fmac_f32 v75, v2, v2
mav 28,44,40
v_add_f32 v74, v3, v74
v_fmac_f32 v75, v3, v3
mav 16,48,44
buffer_load_dwordx4 v[192:195], v88, s[36:39], s49 offen offset:1024 sc1
mav 20,52,44
R 56,38912
v_add_f32 v74, v4, v74
mav 24,56,44
v_fmac_f32 v75, v4, v4
v_add_f32 v74, v5, v74
mav 28,60,44
s_waitcnt lgkmcnt(1)
v_fmac_f32 v75, v5, v5
v_add_f32 v74, v6, v74
mav 16,64,48
buffer_load_dwordx4 v[196:199], v89, s[36:39], s49 offen sc1
mav 20,68,48
R 60,39936
v_fmac_f32 v75, v6, v6
mav 24,72,48
v_add_f32 v74, v7, v74
v_fmac_f32 v75, v7, v7
mav 28,76,48
v_add_f32 v74, v8, v74
v_fmac_f32 v75, v8, v8
mav 16,80,52
buffer_load_dwordx4 v[200:203], v89, s[36:39], s49 offen offset:1024 sc1
mav 20,84,52
R 32,40960
v_add_f32 v74, v9, v74
mav 24,88,52
s_add_u32 m0, s50, 32768
v_fmac_f32 v75, v9, v9
v_add_f32 v74, v10, v74
mav 28,92,52
s_waitcnt lgkmcnt(1)
ds_write_b128 v94, v[64:67] offset:0
mav 16,96,56
D 83,0
mav 20,100,56
R 36,41984
v_fmac_f32 v75, v10, v10
mav 24,104,56
ds_write_b128 v95, v[68:71] offset:0
mav 28,108,56
ds_read_b128 v[64:67], v96 offset:0
v_add_f32 v74, v11, v74
mav 16,112,60
D 83,1024
mav 20,116,60
R 40,43008
v_fmac_f32 v75, v11, v11
mav 24,120,60
ds_read_b128 v[68:71], v96 offset:1024
v_add_f32 v74, v12, v74
mav 28,124,60
s_waitcnt lgkmcnt(4)
v_fmac_f32 v75, v12, v12
v_add_f32 v74, v13, v74
mav 16,128,32
R 44,44032
v_fmac_f32 v75, v13, v13
mav 20,132,32
v_add_f32 v74, v14, v74
v_fmac_f32 v75, v14, v14
mav 24,136,32
v_add_f32 v74, v15, v74
v_fmac_f32 v75, v15, v15
mav 28,140,32
mav 16,144,36
R 48,45056
mav 20,148,36
v_permlane32_swap_b32 v74, v75
mav 24,152,36
v_add_f32 v74, v74, v75
mav 28,156,36
s_waitcnt lgkmcnt(1)
mav 16,160,40
mav 20,164,40
R 52,46080
mav 24,168,40
mav 28,172,40
mav 16,176,44
D 83,2048
mav 20,180,44
R 56,47104
mav 24,184,44
mav 28,188,44
s_waitcnt lgkmcnt(1)
mav 16,192,48
D 83,3072
mav 20,196,48
R 60,48128
mav 24,200,48
mav 28,204,48
mav 16,208,52
buffer_store_dwordx4 v[64:67], v91, s[36:39], s54 offen sc1
mav 20,212,52
mav 24,216,52
mav 28,220,52
s_waitcnt lgkmcnt(0)
mav 16,224,56
buffer_store_dwordx4 v[68:71], v92, s[36:39], s54 offen sc1
mav 20,228,56
mav 24,232,56
mav 28,236,56
mav 16,240,60
buffer_load_dwordx4 v[124:127], v90, s[36:39], s49 offen sc1
mav 20,244,60
mav 24,248,60
mav 28,252,60
s_waitcnt vmcnt(16)
mvv 16,116,164
buffer_load_dwordx4 v[128:131], v90, s[36:39], s49 offen offset:1024 sc1
mvv 20,120,164
s_waitcnt vmcnt(16)
mvv 24,116,168
s_xor_b32 s50, s50, 0x10000
s_sub_u32 s54, s54, 32768
mvv 28,120,168
s_waitcnt vmcnt(15)
mvv 16,116,172
mvv 20,120,172
s_waitcnt vmcnt(14)
v_xor_b32 v80, 0x10000, v80
mvv 24,116,176
s_waitcnt vmcnt(13)
s_add_u32 s49, s49, 313344
mvv 28,120,176
s_barrier
mvv 16,116,180
R 32,0
mvv 20,120,180
s_waitcnt vmcnt(12)
mvv 24,116,184
mvv 28,120,184
s_waitcnt vmcnt(11)
mvv 16,116,188
buffer_load_dwordx4 v[132:135], v86, s[36:39], s49 offen sc1
mvv 20,120,188
s_waitcnt vmcnt(11)
R 36,1024
mvv 24,116,192
mvv 28,120,192
s_waitcnt vmcnt(10)
mvv 16,116,196
buffer_load_dwordx4 v[136:139], v86, s[36:39], s49 offen offset:1024 sc1
mvv 20,120,196
s_waitcnt vmcnt(10)
R 40,2048
mvv 24,116,200
mvv 28,120,200
s_waitcnt lgkmcnt(1)
v_mfma_f32_16x16x32_bf16 v[0:3], a[0:3], v[32:35], v[100:103]
buffer_load_dwordx4 v[140:143], v87, s[36:39], s49 offen sc1
v_mfma_f32_16x16x32_bf16 v[4:7], a[4:7], v[32:35], v[104:107]
R 44,3072
v_cvt_pk_bf16_f32 v64, v16, v17
v_mfma_f32_16x16x32_bf16 v[8:11], a[8:11], v[32:35], v[108:111]
v_cvt_pk_bf16_f32 v65, v18, v19
v_cvt_pk_bf16_f32 v66, v20, v21
v_mfma_f32_16x16x32_bf16 v[12:15], a[12:15], v[32:35], v[112:115]
v_cvt_pk_bf16_f32 v67, v22, v23
v_cvt_pk_bf16_f32 v68, v24, v25
mav 0,16,36
buffer_load_dwordx4 v[144:147], v87, s[36:39], s49 offen offset:1024 sc1
mav 4,20,36
R 48,4096
v_cvt_pk_bf16_f32 v69, v26, v27
mav 8,24,36
v_cvt_pk_bf16_f32 v70, v28, v29
v_cvt_pk_bf16_f32 v71, v30, v31
mav 12,28,36
s_waitcnt lgkmcnt(1)
v_mul_f32 v75, v16, v16
v_fmac_f32 v75, v17, v17
mav 0,32,40
buffer_load_dwordx4 v[148:151], v88, s[36:39], s49 offen sc1
mav 4,36,40
R 52,5120
v_fmac_f32 v75, v18, v18
mav 8,40,40
v_fmac_f32 v75, v19, v19
v_fmac_f32 v75, v20, v20
mav 12,44,40
v_fmac_f32 v75, v21, v21
v_fmac_f32 v75, v22, v22
mav 0,48,44
buffer_load_dwordx4 v[152:155], v88, s[36:39], s49 offen offset:1024 sc1
mav 4,52,44
R 56,6144
v_fmac_f32 v75, v23, v23
mav 8,56,44
v_fmac_f32 v75, v24, v24
v_fmac_f32 v75, v25, v25
mav 12,60,44
s_waitcnt lgkmcnt(1)
v_fmac_f32 v75, v26, v26
v_fmac_f32 v75, v27, v27
mav 0,64,48
buffer_load_dwordx4 v[156:159], v89, s[36:39], s49 offen sc1
mav 4,68,48
R 60,7168
v_fmac_f32 v75, v28, v28
mav 8,72,48
v_fmac_f32 v75, v29, v29
v_fmac_f32 v75, v30, v30
mav 12,76,48
v_fmac_f32 v75, v31, v31
mav 0,80,52
buffer_load_dwordx4 v[160:163], v89, s[36:39], s49 offen offset:1024 sc1
mav 4,84,52
R 32,8192
mav 8,88,52
ds_write_b32 v97, v74 offset:0
mav 12,92,52
s_waitcnt lgkmcnt(2)
ds_write_b128 v94, v[64:67] offset:0
mav 0,96,56
s_add_u32 m0, s50, 0
R 36,9216
v_add_f32 v74, v17, v16
mav 4,100,56
ds_write_b128 v95, v[68:71] offset:0
mav 8,104,56
ds_read_b128 v[64:67], v96 offset:0
LsR7:
s_cbranch_execz LsR11
v_add_f32 v74, v18, v74
mav 12,108,56
ds_read_b128 v[68:71], v96 offset:1024
v_add_f32 v74, v19, v74
mav 0,112,60
D 83,0
mav 4,116,60
R 40,10240
v_add_f32 v74, v20, v74
mav 8,120,60
v_add_f32 v74, v21, v74
v_add_f32 v74, v22, v74
mav 12,124,60
s_waitcnt lgkmcnt(3)
v_add_f32 v74, v23, v74
v_add_f32 v74, v24, v74
mav 0,128,32
D 83,1024
mav 4,132,32
R 44,11264
v_add_f32 v74, v25, v74
mav 8,136,32
v_add_f32 v74, v26, v74
v_add_f32 v74, v27, v74
mav 12,140,32
v_add_f32 v74, v28, v74
v_add_f32 v74, v29, v74
mav 0,144,36
R 48,12288
v_add_f32 v74, v30, v74
mav 4,148,36
v_add_f32 v74, v31, v74
mav 8,152,36
mav 12,156,36
s_waitcnt lgkmcnt(1)
v_permlane32_swap_b32 v74, v75
mav 0,160,40
R 52,13312
v_add_f32 v74, v74, v75
mav 4,164,40
v_add_u32 v72, 1, v72
mav 8,168,40
mav 12,172,40
mav 0,176,44
D 83,2048
mav 4,180,44
R 56,14336
mav 8,184,44
mav 12,188,44
s_waitcnt lgkmcnt(1)
mav 0,192,48
D 83,3072
mav 4,196,48
R 60,15360
mav 8,200,48
mav 12,204,48
mav 0,208,52
buffer_store_dwordx4 v[64:67], v91, s[36:39], s54 offen sc1
mav 4,212,52
mav 8,216,52
mav 12,220,52
s_waitcnt lgkmcnt(0)
mav 0,224,56
buffer_store_dwordx4 v[68:71], v92, s[36:39], s54 offen sc1
mav 4,228,56
mav 8,232,56
mav 12,236,56
mav 0,240,60
buffer_load_dwordx4 v[164:167], v90, s[36:39], s49 offen sc1
mav 4,244,60
mav 8,248,60
mav 12,252,60
s_waitcnt vmcnt(16)
mvv 0,116,124
buffer_load_dwordx4 v[168:171], v90, s[36:39], s49 offen offset:1024 sc1
mvv 4,120,124
s_waitcnt vmcnt(16)
buffer_store_dword v72, v81, s[36:39], s52 offen sc1
mvv 8,116,128
s_sub_u32 s54, s54, 32768
mvv 12,120,128
s_waitcnt vmcnt(16)
mvv 0,116,132
mvv 4,120,132
s_waitcnt vmcnt(15)
mvv 8,116,136
s_waitcnt vmcnt(14)
s_add_u32 s49, s49, 313344
mvv 12,120,136
s_barrier
mvv 0,116,140
R 32,32768
mvv 4,120,140
s_waitcnt vmcnt(13)
ds_read_b128 v[204:207], v98 offset:0
mvv 8,116,144
mvv 12,120,144
s_waitcnt vmcnt(12)
mvv 0,116,148
buffer_load_dwordx4 v[172:175], v86, s[36:39], s49 offen sc1
mvv 4,120,148
s_waitcnt vmcnt(12)
R 36,33792
mvv 8,116,152
mvv 12,120,152
s_waitcnt vmcnt(11)
mvv 0,116,156
s_waitcnt lgkmcnt(1)
buffer_load_dwordx4 v[176:179], v86, s[36:39], s49 offen offset:1024 sc1
mvv 4,120,156
s_waitcnt vmcnt(11)
R 40,34816
v_add_f32 v204, v204, v205
mvv 8,116,160
v_add_f32 v206, v206, v207
v_add_f32 v204, v204, v206
mvv 12,120,160
v_mfma_f32_16x16x32_bf16 v[16:19], a[0:3], v[32:35], v[100:103]
buffer_load_dwordx4 v[180:183], v87, s[36:39], s49 offen sc1
v_mfma_f32_16x16x32_bf16 v[20:23], a[4:7], v[32:35], v[104:107]
R 44,35840
v_add_f32_dpp v205, v204, v204 quad_perm:[1,0,3,2] row_mask:0xf bank_mask:0xf
v_mfma_f32_16x16x32_bf16 v[24:27], a[8:11], v[32:35], v[108:111]
buffer_store_dword v205, v99, s[36:39], s49 offen sc1
v_mfma_f32_16x16x32_bf16 v[28:31], a[12:15], v[32:35], v[112:115]
s_waitcnt lgkmcnt(1)
v_cvt_pk_bf16_f32 v64, v0, v1
v_cvt_pk_bf16_f32 v65, v2, v3
mav 16,16,36
buffer_load_dwordx4 v[184:187], v87, s[36:39], s49 offen offset:1024 sc1
mav 20,20,36
R 48,36864
v_cvt_pk_bf16_f32 v66, v4, v5
mav 24,24,36
v_cvt_pk_bf16_f32 v67, v6, v7
v_cvt_pk_bf16_f32 v68, v8, v9
mav 28,28,36
v_cvt_pk_bf16_f32 v69, v10, v11
v_cvt_pk_bf16_f32 v70, v12, v13
mav 16,32,40
buffer_load_dwordx4 v[188:191], v88, s[36:39], s49 offen sc1
mav 20,36,40
R 52,37888
v_cvt_pk_bf16_f32 v71, v14, v15
mav 24,40,40
v_mul_f32 v75, v0, v0
v_fmac_f32 v75, v1, v1
mav 28,44,40
s_waitcnt lgkmcnt(1)
v_fmac_f32 v75, v2, v2
v_fmac_f32 v75, v3, v3
mav 16,48,44
buffer_load_dwordx4 v[192:195], v88, s[36:39], s49 offen offset:1024 sc1
mav 20,52,44
R 56,38912
v_fmac_f32 v75, v4, v4
mav 24,56,44
v_fmac_f32 v75, v5, v5
v_fmac_f32 v75, v6, v6
mav 28,60,44
v_fmac_f32 v75, v7, v7
)"
R"(
v_fmac_f32 v75, v8, v8
mav 16,64,48
buffer_load_dwordx4 v[196:199], v89, s[36:39], s49 offen sc1
mav 20,68,48
R 60,39936
v_fmac_f32 v75, v9, v9
mav 24,72,48
v_fmac_f32 v75, v10, v10
v_fmac_f32 v75, v11, v11
mav 28,76,48
s_waitcnt lgkmcnt(1)
v_fmac_f32 v75, v12, v12
v_fmac_f32 v75, v13, v13
mav 16,80,52
buffer_load_dwordx4 v[200:203], v89, s[36:39], s49 offen offset:1024 sc1
mav 20,84,52
R 32,40960
v_fmac_f32 v75, v14, v14
mav 24,88,52
ds_write_b32 v97, v74 offset:1024
mav 28,92,52
ds_write_b128 v94, v[64:67] offset:0
mav 16,96,56
s_add_u32 m0, s50, 32768
R 36,41984
v_add_f32 v74, v1, v0
mav 20,100,56
ds_write_b128 v95, v[68:71] offset:0
mav 24,104,56
ds_read_b128 v[64:67], v96 offset:0
v_add_f32 v74, v2, v74
mav 28,108,56
s_waitcnt lgkmcnt(5)
ds_read_b128 v[68:71], v96 offset:1024
v_add_f32 v74, v3, v74
mav 16,112,60
D 83,0
mav 20,116,60
R 40,43008
v_add_f32 v74, v4, v74
mav 24,120,60
v_add_f32 v74, v5, v74
v_add_f32 v74, v6, v74
mav 28,124,60
v_add_f32 v74, v7, v74
v_add_f32 v74, v8, v74
mav 16,128,32
D 83,1024
mav 20,132,32
R 44,44032
v_add_f32 v74, v9, v74
mav 24,136,32
v_add_f32 v74, v10, v74
v_add_f32 v74, v11, v74
mav 28,140,32
s_waitcnt lgkmcnt(1)
v_add_f32 v74, v12, v74
v_add_f32 v74, v13, v74
mav 16,144,36
R 48,45056
v_add_f32 v74, v14, v74
mav 20,148,36
v_add_f32 v74, v15, v74
v_fmac_f32 v75, v15, v15
mav 24,152,36
mav 28,156,36
v_permlane32_swap_b32 v74, v75
mav 16,160,40
s_mov_b32 s60, -14
.p2align 6
LR1_1:
mav 20,164,40
R 52,46080
v_add_f32 v74, v74, v75
mav 24,168,40
v_add_u32 v72, 1, v72
mav 28,172,40
s_waitcnt lgkmcnt(1)
mav 16,176,44
D 83,2048
mav 20,180,44
R 56,47104
mav 24,184,44
mav 28,188,44
mav 16,192,48
D 83,3072
mav 20,196,48
R 60,48128
mav 24,200,48
mav 28,204,48
s_waitcnt lgkmcnt(1)
mav 16,208,52
buffer_store_dwordx4 v[64:67], v91, s[36:39], s54 offen sc1
mav 20,212,52
mav 24,216,52
mav 28,220,52
mav 16,224,56
buffer_store_dwordx4 v[68:71], v92, s[36:39], s54 offen sc1
mav 20,228,56
mav 24,232,56
mav 28,236,56
s_waitcnt lgkmcnt(0)
mav 16,240,60
buffer_load_dwordx4 v[124:127], v90, s[36:39], s49 offen sc1
mav 20,244,60
mav 24,248,60
mav 28,252,60
s_waitcnt vmcnt(18)
mvv 16,116,164
s_waitcnt vmcnt(12)
buffer_load_dwordx4 v[128:131], v90, s[36:39], s49 offen offset:1024 sc1
mvv 20,120,164
buffer_store_dword v72, v81, s[36:39], s52 offen sc1
mvv 24,116,168
s_xor_b32 s50, s50, 0x10000
s_sub_u32 s54, s54, 32768
mvv 28,120,168
mvv 16,116,172
mvv 20,120,172
v_xor_b32 v80, 0x10000, v80
mvv 24,116,176
s_add_u32 s49, s49, 313344
mvv 28,120,176
s_barrier
mvv 16,116,180
R 32,0
mvv 20,120,180
s_waitcnt vmcnt(13)
ds_read_b128 v[204:207], v98 offset:1024
mvv 24,116,184
mvv 28,120,184
s_waitcnt vmcnt(12)
mvv 16,116,188
buffer_load_dwordx4 v[132:135], v86, s[36:39], s49 offen sc1
mvv 20,120,188
s_waitcnt vmcnt(12)
R 36,1024
mvv 24,116,192
mvv 28,120,192
s_waitcnt vmcnt(11)
mvv 16,116,196
s_waitcnt lgkmcnt(1)
buffer_load_dwordx4 v[136:139], v86, s[36:39], s49 offen offset:1024 sc1
mvv 20,120,196
s_waitcnt vmcnt(11)
R 40,2048
v_add_f32 v204, v204, v205
mvv 24,116,200
v_add_f32 v206, v206, v207
v_add_f32 v204, v204, v206
mvv 28,120,200
v_mfma_f32_16x16x32_bf16 v[0:3], a[0:3], v[32:35], v[100:103]
buffer_load_dwordx4 v[140:143], v87, s[36:39], s49 offen sc1
v_mfma_f32_16x16x32_bf16 v[4:7], a[4:7], v[32:35], v[104:107]
R 44,3072
v_add_f32_dpp v205, v204, v204 quad_perm:[1,0,3,2] row_mask:0xf bank_mask:0xf
v_mfma_f32_16x16x32_bf16 v[8:11], a[8:11], v[32:35], v[108:111]
buffer_store_dword v205, v99, s[36:39], s49 offen sc1
v_mfma_f32_16x16x32_bf16 v[12:15], a[12:15], v[32:35], v[112:115]
s_waitcnt lgkmcnt(1)
v_cvt_pk_bf16_f32 v64, v16, v17
v_cvt_pk_bf16_f32 v65, v18, v19
mav 0,16,36
buffer_load_dwordx4 v[144:147], v87, s[36:39], s49 offen offset:1024 sc1
mav 4,20,36
R 48,4096
v_cvt_pk_bf16_f32 v66, v20, v21
mav 8,24,36
v_cvt_pk_bf16_f32 v67, v22, v23
v_cvt_pk_bf16_f32 v68, v24, v25
mav 12,28,36
v_cvt_pk_bf16_f32 v69, v26, v27
v_cvt_pk_bf16_f32 v70, v28, v29
mav 0,32,40
buffer_load_dwordx4 v[148:151], v88, s[36:39], s49 offen sc1
mav 4,36,40
R 52,5120
v_cvt_pk_bf16_f32 v71, v30, v31
mav 8,40,40
v_mul_f32 v75, v16, v16
v_fmac_f32 v75, v17, v17
mav 12,44,40
s_waitcnt lgkmcnt(1)
v_fmac_f32 v75, v18, v18
v_fmac_f32 v75, v19, v19
mav 0,48,44
buffer_load_dwordx4 v[152:155], v88, s[36:39], s49 offen offset:1024 sc1
mav 4,52,44
R 56,6144
v_fmac_f32 v75, v20, v20
mav 8,56,44
v_fmac_f32 v75, v21, v21
v_fmac_f32 v75, v22, v22
mav 12,60,44
v_fmac_f32 v75, v23, v23
v_fmac_f32 v75, v24, v24
mav 0,64,48
buffer_load_dwordx4 v[156:159], v89, s[36:39], s49 offen sc1
mav 4,68,48
R 60,7168
v_fmac_f32 v75, v25, v25
mav 8,72,48
v_fmac_f32 v75, v26, v26
v_fmac_f32 v75, v27, v27
mav 12,76,48
s_waitcnt lgkmcnt(1)
v_fmac_f32 v75, v28, v28
v_fmac_f32 v75, v29, v29
mav 0,80,52
buffer_load_dwordx4 v[160:163], v89, s[36:39], s49 offen offset:1024 sc1
mav 4,84,52
R 32,8192
v_fmac_f32 v75, v30, v30
mav 8,88,52
ds_write_b32 v97, v74 offset:0
mav 12,92,52
ds_write_b128 v94, v[64:67] offset:0
mav 0,96,56
s_add_u32 m0, s50, 0
R 36,9216
v_add_f32 v74, v17, v16
mav 4,100,56
ds_write_b128 v95, v[68:71] offset:0
mav 8,104,56
ds_read_b128 v[64:67], v96 offset:0
v_add_f32 v74, v18, v74
mav 12,108,56
s_waitcnt lgkmcnt(5)
ds_read_b128 v[68:71], v96 offset:1024
v_add_f32 v74, v19, v74
mav 0,112,60
D 83,0
mav 4,116,60
R 40,10240
v_add_f32 v74, v20, v74
mav 8,120,60
v_add_f32 v74, v21, v74
v_add_f32 v74, v22, v74
mav 12,124,60
v_add_f32 v74, v23, v74
v_add_f32 v74, v24, v74
mav 0,128,32
D 83,1024
mav 4,132,32
R 44,11264
v_add_f32 v74, v25, v74
mav 8,136,32
v_add_f32 v74, v26, v74
v_add_f32 v74, v27, v74
mav 12,140,32
s_waitcnt lgkmcnt(1)
v_add_f32 v74, v28, v74
v_add_f32 v74, v29, v74
mav 0,144,36
R 48,12288
v_add_f32 v74, v30, v74
mav 4,148,36
v_add_f32 v74, v31, v74
v_fmac_f32 v75, v31, v31
mav 8,152,36
mav 12,156,36
v_permlane32_swap_b32 v74, v75
mav 0,160,40
R 52,13312
v_add_f32 v74, v74, v75
mav 4,164,40
v_add_u32 v72, 1, v72
mav 8,168,40
mav 12,172,40
s_waitcnt lgkmcnt(1)
mav 0,176,44
D 83,2048
mav 4,180,44
R 56,14336
mav 8,184,44
mav 12,188,44
mav 0,192,48
D 83,3072
mav 4,196,48
R 60,15360
mav 8,200,48
mav 12,204,48
s_waitcnt lgkmcnt(1)
mav 0,208,52
buffer_store_dwordx4 v[64:67], v91, s[36:39], s54 offen sc1
mav 4,212,52
mav 8,216,52
mav 12,220,52
mav 0,224,56
buffer_store_dwordx4 v[68:71], v92, s[36:39], s54 offen sc1
mav 4,228,56
mav 8,232,56
mav 12,236,56
s_waitcnt lgkmcnt(0)
mav 0,240,60
buffer_load_dwordx4 v[164:167], v90, s[36:39], s49 offen sc1
mav 4,244,60
mav 8,248,60
mav 12,252,60
s_waitcnt vmcnt(18)
mvv 0,116,124
s_waitcnt vmcnt(12)
buffer_load_dwordx4 v[168:171], v90, s[36:39], s49 offen offset:1024 sc1
mvv 4,120,124
buffer_store_dword v72, v81, s[36:39], s52 offen sc1
mvv 8,116,128
s_sub_u32 s54, s54, 32768
mvv 12,120,128
mvv 0,116,132
mvv 4,120,132
mvv 8,116,136
s_add_u32 s49, s49, 313344
mvv 12,120,136
s_barrier
mvv 0,116,140
R 32,32768
mvv 4,120,140
s_waitcnt vmcnt(13)
ds_read_b128 v[204:207], v98 offset:0
mvv 8,116,144
mvv 12,120,144
s_waitcnt vmcnt(12)
mvv 0,116,148
buffer_load_dwordx4 v[172:175], v86, s[36:39], s49 offen sc1
mvv 4,120,148
s_waitcnt vmcnt(12)
R 36,33792
mvv 8,116,152
mvv 12,120,152
s_waitcnt vmcnt(11)
mvv 0,116,156
s_waitcnt lgkmcnt(1)
buffer_load_dwordx4 v[176:179], v86, s[36:39], s49 offen offset:1024 sc1
mvv 4,120,156
s_waitcnt vmcnt(11)
R 40,34816
v_add_f32 v204, v204, v205
mvv 8,116,160
v_add_f32 v206, v206, v207
v_add_f32 v204, v204, v206
mvv 12,120,160
v_mfma_f32_16x16x32_bf16 v[16:19], a[0:3], v[32:35], v[100:103]
buffer_load_dwordx4 v[180:183], v87, s[36:39], s49 offen sc1
v_mfma_f32_16x16x32_bf16 v[20:23], a[4:7], v[32:35], v[104:107]
R 44,35840
v_add_f32_dpp v205, v204, v204 quad_perm:[1,0,3,2] row_mask:0xf bank_mask:0xf
v_mfma_f32_16x16x32_bf16 v[24:27], a[8:11], v[32:35], v[108:111]
buffer_store_dword v205, v99, s[36:39], s49 offen sc1
v_mfma_f32_16x16x32_bf16 v[28:31], a[12:15], v[32:35], v[112:115]
s_waitcnt lgkmcnt(1)
v_cvt_pk_bf16_f32 v64, v0, v1
v_cvt_pk_bf16_f32 v65, v2, v3
mav 16,16,36
buffer_load_dwordx4 v[184:187], v87, s[36:39], s49 offen offset:1024 sc1
mav 20,20,36
R 48,36864
v_cvt_pk_bf16_f32 v66, v4, v5
mav 24,24,36
v_cvt_pk_bf16_f32 v67, v6, v7
v_cvt_pk_bf16_f32 v68, v8, v9
mav 28,28,36
v_cvt_pk_bf16_f32 v69, v10, v11
v_cvt_pk_bf16_f32 v70, v12, v13
mav 16,32,40
buffer_load_dwordx4 v[188:191], v88, s[36:39], s49 offen sc1
mav 20,36,40
R 52,37888
v_cvt_pk_bf16_f32 v71, v14, v15
mav 24,40,40
v_mul_f32 v75, v0, v0
v_fmac_f32 v75, v1, v1
mav 28,44,40
s_waitcnt lgkmcnt(1)
v_fmac_f32 v75, v2, v2
v_fmac_f32 v75, v3, v3
mav 16,48,44
buffer_load_dwordx4 v[192:195], v88, s[36:39], s49 offen offset:1024 sc1
mav 20,52,44
R 56,38912
v_fmac_f32 v75, v4, v4
mav 24,56,44
v_fmac_f32 v75, v5, v5
v_fmac_f32 v75, v6, v6
mav 28,60,44
v_fmac_f32 v75, v7, v7
v_fmac_f32 v75, v8, v8
mav 16,64,48
buffer_load_dwordx4 v[196:199], v89, s[36:39], s49 offen sc1
mav 20,68,48
R 60,39936
v_fmac_f32 v75, v9, v9
mav 24,72,48
v_fmac_f32 v75, v10, v10
v_fmac_f32 v75, v11, v11
mav 28,76,48
s_waitcnt lgkmcnt(1)
v_fmac_f32 v75, v12, v12
v_fmac_f32 v75, v13, v13
mav 16,80,52
buffer_load_dwordx4 v[200:203], v89, s[36:39], s49 offen offset:1024 sc1
mav 20,84,52
R 32,40960
v_fmac_f32 v75, v14, v14
mav 24,88,52
ds_write_b32 v97, v74 offset:1024
mav 28,92,52
ds_write_b128 v94, v[64:67] offset:0
mav 16,96,56
s_add_u32 m0, s50, 32768
R 36,41984
v_add_f32 v74, v1, v0
mav 20,100,56
ds_write_b128 v95, v[68:71] offset:0
mav 24,104,56
ds_read_b128 v[64:67], v96 offset:0
v_add_f32 v74, v2, v74
mav 28,108,56
s_waitcnt lgkmcnt(5)
ds_read_b128 v[68:71], v96 offset:1024
v_add_f32 v74, v3, v74
mav 16,112,60
D 83,0
mav 20,116,60
R 40,43008
v_add_f32 v74, v4, v74
mav 24,120,60
v_add_f32 v74, v5, v74
v_add_f32 v74, v6, v74
mav 28,124,60
v_add_f32 v74, v7, v74
v_add_f32 v74, v8, v74
mav 16,128,32
D 83,1024
LsR8:
s_cbranch_execz LsR12
mav 20,132,32
R 44,44032
v_add_f32 v74, v9, v74
mav 24,136,32
v_add_f32 v74, v10, v74
v_add_f32 v74, v11, v74
mav 28,140,32
s_waitcnt lgkmcnt(1)
v_add_f32 v74, v12, v74
v_add_f32 v74, v13, v74
mav 16,144,36
R 48,45056
v_add_f32 v74, v14, v74
mav 20,148,36
v_add_f32 v74, v15, v74
v_fmac_f32 v75, v15, v15
mav 24,152,36
mav 28,156,36
s_add_u32 s60, s60, 1
v_permlane32_swap_b32 v74, v75
mav 16,160,40
s_cbranch_scc0 LR1_1
mav 20,164,40
R 52,46080
v_add_f32 v74, v74, v75
mav 24,168,40
v_add_u32 v72, 1, v72
mav 28,172,40
s_waitcnt lgkmcnt(1)
mav 16,176,44
D 83,2048
mav 20,180,44
R 56,47104
mav 24,184,44
mav 28,188,44
mav 16,192,48
D 83,3072
mav 20,196,48
R 60,48128
mav 24,200,48
mav 28,204,48
s_waitcnt lgkmcnt(1)
mav 16,208,52
buffer_store_dwordx4 v[64:67], v91, s[36:39], s54 offen sc1
mav 20,212,52
mav 24,216,52
mav 28,220,52
mav 16,224,56
buffer_store_dwordx4 v[68:71], v92, s[36:39], s54 offen sc1
mav 20,228,56
mav 24,232,56
mav 28,236,56
s_waitcnt lgkmcnt(0)
mav 16,240,60
buffer_load_dwordx4 v[124:127], v90, s[36:39], s49 offen sc1
mav 20,244,60
mav 24,248,60
mav 28,252,60
s_waitcnt vmcnt(18)
mvv 16,116,164
s_waitcnt vmcnt(12)
buffer_load_dwordx4 v[128:131], v90, s[36:39], s49 offen offset:1024 sc1
mvv 20,120,164
buffer_store_dword v72, v81, s[36:39], s52 offen sc1
mvv 24,116,168
s_xor_b32 s50, s50, 0x10000
s_sub_u32 s54, s54, 32768
mvv 28,120,168
mvv 16,116,172
mvv 20,120,172
v_xor_b32 v80, 0x10000, v80
mvv 24,116,176
s_add_u32 s49, s49, 313344
mvv 28,120,176
s_barrier
mvv 16,116,180
R 32,0
mvv 20,120,180
s_waitcnt vmcnt(13)
ds_read_b128 v[204:207], v98 offset:1024
mvv 24,116,184
mvv 28,120,184
s_waitcnt vmcnt(12)
mvv 16,116,188
buffer_load_dwordx4 v[132:135], v86, s[36:39], s49 offen sc1
mvv 20,120,188
s_waitcnt vmcnt(12)
R 36,1024
mvv 24,116,192
mvv 28,120,192
s_waitcnt vmcnt(11)
mvv 16,116,196
s_waitcnt lgkmcnt(1)
buffer_load_dwordx4 v[136:139], v86, s[36:39], s49 offen offset:1024 sc1
mvv 20,120,196
s_waitcnt vmcnt(11)
R 40,2048
v_add_f32 v204, v204, v205
mvv 24,116,200
v_add_f32 v206, v206, v207
v_add_f32 v204, v204, v206
mvv 28,120,200
v_mfma_f32_16x16x32_bf16 v[0:3], a[0:3], v[32:35], v[100:103]
buffer_load_dwordx4 v[140:143], v87, s[36:39], s49 offen sc1
v_mfma_f32_16x16x32_bf16 v[4:7], a[4:7], v[32:35], v[104:107]
R 44,3072
v_add_f32_dpp v205, v204, v204 quad_perm:[1,0,3,2] row_mask:0xf bank_mask:0xf
v_mfma_f32_16x16x32_bf16 v[8:11], a[8:11], v[32:35], v[108:111]
buffer_store_dword v205, v99, s[36:39], s49 offen sc1
v_mfma_f32_16x16x32_bf16 v[12:15], a[12:15], v[32:35], v[112:115]
s_waitcnt lgkmcnt(1)
v_cvt_pk_bf16_f32 v64, v16, v17
v_cvt_pk_bf16_f32 v65, v18, v19
mav 0,16,36
buffer_load_dwordx4 v[144:147], v87, s[36:39], s49 offen offset:1024 sc1
mav 4,20,36
R 48,4096
v_cvt_pk_bf16_f32 v66, v20, v21
mav 8,24,36
v_cvt_pk_bf16_f32 v67, v22, v23
v_cvt_pk_bf16_f32 v68, v24, v25
mav 12,28,36
v_cvt_pk_bf16_f32 v69, v26, v27
v_cvt_pk_bf16_f32 v70, v28, v29
mav 0,32,40
buffer_load_dwordx4 v[148:151], v88, s[36:39], s49 offen sc1
mav 4,36,40
R 52,5120
v_cvt_pk_bf16_f32 v71, v30, v31
mav 8,40,40
v_mul_f32 v75, v16, v16
v_fmac_f32 v75, v17, v17
mav 12,44,40
s_waitcnt lgkmcnt(1)
v_fmac_f32 v75, v18, v18
v_fmac_f32 v75, v19, v19
mav 0,48,44
buffer_load_dwordx4 v[152:155], v88, s[36:39], s49 offen offset:1024 sc1
mav 4,52,44
R 56,6144
v_fmac_f32 v75, v20, v20
mav 8,56,44
v_fmac_f32 v75, v21, v21
v_fmac_f32 v75, v22, v22
mav 12,60,44
v_fmac_f32 v75, v23, v23
v_fmac_f32 v75, v24, v24
mav 0,64,48
buffer_load_dwordx4 v[156:159], v89, s[36:39], s49 offen sc1
mav 4,68,48
R 60,7168
v_fmac_f32 v75, v25, v25
mav 8,72,48
v_fmac_f32 v75, v26, v26
v_fmac_f32 v75, v27, v27
mav 12,76,48
s_waitcnt lgkmcnt(1)
v_fmac_f32 v75, v28, v28
v_fmac_f32 v75, v29, v29
mav 0,80,52
buffer_load_dwordx4 v[160:163], v89, s[36:39], s49 offen offset:1024 sc1
mav 4,84,52
R 32,8192
v_fmac_f32 v75, v30, v30
mav 8,88,52
ds_write_b32 v97, v74 offset:0
mav 12,92,52
ds_write_b128 v94, v[64:67] offset:0
mav 0,96,56
s_add_u32 m0, s50, 0
R 36,9216
v_add_f32 v74, v17, v16
mav 4,100,56
ds_write_b128 v95, v[68:71] offset:0
mav 8,104,56
ds_read_b128 v[64:67], v96 offset:0
v_add_f32 v74, v18, v74
mav 12,108,56
s_waitcnt lgkmcnt(5)
ds_read_b128 v[68:71], v96 offset:1024
v_add_f32 v74, v19, v74
mav 0,112,60
D 83,0
mav 4,116,60
R 40,10240
v_add_f32 v74, v20, v74
mav 8,120,60
v_add_f32 v74, v21, v74
v_add_f32 v74, v22, v74
mav 12,124,60
v_add_f32 v74, v23, v74
v_add_f32 v74, v24, v74
mav 0,128,32
D 83,1024
mav 4,132,32
R 44,11264
v_add_f32 v74, v25, v74
mav 8,136,32
v_add_f32 v74, v26, v74
v_add_f32 v74, v27, v74
mav 12,140,32
s_waitcnt lgkmcnt(1)
v_add_f32 v74, v28, v74
v_add_f32 v74, v29, v74
mav 0,144,36
R 48,12288
v_add_f32 v74, v30, v74
mav 4,148,36
v_add_f32 v74, v31, v74
v_fmac_f32 v75, v31, v31
mav 8,152,36
mav 12,156,36
v_permlane32_swap_b32 v74, v75
mav 0,160,40
R 52,13312
v_add_f32 v74, v74, v75
mav 4,164,40
v_add_u32 v72, 1, v72
mav 8,168,40
mav 12,172,40
s_waitcnt lgkmcnt(1)
mav 0,176,44
D 83,2048
mav 4,180,44
R 56,14336
mav 8,184,44
mav 12,188,44
mav 0,192,48
D 83,3072
mav 4,196,48
R 60,15360
mav 8,200,48
mav 12,204,48
s_waitcnt lgkmcnt(1)
mav 0,208,52
buffer_store_dwordx4 v[64:67], v91, s[36:39], s54 offen sc1
mav 4,212,52
mav 8,216,52
mav 12,220,52
mav 0,224,56
buffer_store_dwordx4 v[68:71], v92, s[36:39], s54 offen sc1
mav 4,228,56
mav 8,232,56
mav 12,236,56
s_waitcnt lgkmcnt(0)
mav 0,240,60
buffer_load_dwordx4 v[164:167], v90, s[36:39], s49 offen sc1
mav 4,244,60
mav 8,248,60
mav 12,252,60
s_waitcnt vmcnt(18)
mvv 0,116,124
s_waitcnt vmcnt(12)
buffer_load_dwordx4 v[168:171], v90, s[36:39], s49 offen offset:1024 sc1
mvv 4,120,124
buffer_store_dword v72, v81, s[36:39], s52 offen sc1
mvv 8,116,128
s_sub_u32 s54, s54, 3178496
mvv 12,120,128
mvv 0,116,132
mvv 4,120,132
mvv 8,116,136
s_add_u32 s49, s49, 313344
mvv 12,120,136
s_barrier
mvv 0,116,140
R 32,32768
mvv 4,120,140
s_waitcnt vmcnt(13)
ds_read_b128 v[204:207], v98 offset:0
mvv 8,116,144
mvv 12,120,144
s_waitcnt vmcnt(12)
mvv 0,116,148
buffer_load_dwordx4 v[172:175], v86, s[36:39], s49 offen sc1
mvv 4,120,148
s_waitcnt vmcnt(12)
R 36,33792
mvv 8,116,152
mvv 12,120,152
s_waitcnt vmcnt(11)
mvv 0,116,156
s_waitcnt lgkmcnt(1)
buffer_load_dwordx4 v[176:179], v86, s[36:39], s49 offen offset:1024 sc1
mvv 4,120,156
s_waitcnt vmcnt(11)
R 40,34816
v_add_f32 v204, v204, v205
mvv 8,116,160
v_add_f32 v206, v206, v207
v_add_f32 v204, v204, v206
mvv 12,120,160
v_mfma_f32_16x16x32_bf16 v[16:19], a[0:3], v[32:35], v[100:103]
buffer_load_dwordx4 v[180:183], v87, s[36:39], s49 offen sc1
v_mfma_f32_16x16x32_bf16 v[20:23], a[4:7], v[32:35], v[104:107]
R 44,35840
v_add_f32_dpp v205, v204, v204 quad_perm:[1,0,3,2] row_mask:0xf bank_mask:0xf
v_mfma_f32_16x16x32_bf16 v[24:27], a[8:11], v[32:35], v[108:111]
buffer_store_dword v205, v99, s[36:39], s49 offen sc1
v_mfma_f32_16x16x32_bf16 v[28:31], a[12:15], v[32:35], v[112:115]
s_waitcnt lgkmcnt(1)
v_cvt_pk_bf16_f32 v64, v0, v1
v_cvt_pk_bf16_f32 v65, v2, v3
mav 16,16,36
buffer_load_dwordx4 v[184:187], v87, s[36:39], s49 offen offset:1024 sc1
mav 20,20,36
R 48,36864
v_cvt_pk_bf16_f32 v66, v4, v5
mav 24,24,36
v_cvt_pk_bf16_f32 v67, v6, v7
v_cvt_pk_bf16_f32 v68, v8, v9
mav 28,28,36
v_cvt_pk_bf16_f32 v69, v10, v11
v_cvt_pk_bf16_f32 v70, v12, v13
mav 16,32,40
buffer_load_dwordx4 v[188:191], v88, s[36:39], s49 offen sc1
mav 20,36,40
R 52,37888
v_cvt_pk_bf16_f32 v71, v14, v15
mav 24,40,40
mav 28,44,40
s_waitcnt lgkmcnt(1)
mav 16,48,44
buffer_load_dwordx4 v[192:195], v88, s[36:39], s49 offen offset:1024 sc1
mav 20,52,44
R 56,38912
mav 24,56,44
mav 28,60,44
mav 16,64,48
buffer_load_dwordx4 v[196:199], v89, s[36:39], s49 offen sc1
mav 20,68,48
R 60,39936
mav 24,72,48
mav 28,76,48
s_waitcnt lgkmcnt(1)
mav 16,80,52
buffer_load_dwordx4 v[200:203], v89, s[36:39], s49 offen offset:1024 sc1
mav 20,84,52
R 32,40960
mav 24,88,52
ds_write_b32 v97, v74 offset:1024
mav 28,92,52
ds_write_b128 v94, v[64:67] offset:0
mav 16,96,56
s_add_u32 m0, s50, 32768
R 36,41984
mav 20,100,56
ds_write_b128 v95, v[68:71] offset:0
mav 24,104,56
ds_read_b128 v[64:67], v96 offset:0
mav 28,108,56
s_waitcnt lgkmcnt(5)
ds_read_b128 v[68:71], v96 offset:1024
mav 16,112,60
D 83,0
mav 20,116,60
R 40,43008
mav 24,120,60
mav 28,124,60
mav 16,128,32
D 83,1024
mav 20,132,32
R 44,44032
mav 24,136,32
mav 28,140,32
s_waitcnt lgkmcnt(1)
mav 16,144,36
R 48,45056
mav 20,148,36
mav 24,152,36
mav 28,156,36
mav 16,160,40
mav 20,164,40
R 52,46080
v_add_u32 v72, 1, v72
mav 24,168,40
mav 28,172,40
s_waitcnt lgkmcnt(1)
mav 16,176,44
D 83,2048
mav 20,180,44
R 56,47104
mav 24,184,44
mav 28,188,44
mav 16,192,48
D 83,3072
mav 20,196,48
R 60,48128
mav 24,200,48
mav 28,204,48
s_waitcnt lgkmcnt(1)
mav 16,208,52
buffer_store_dwordx4 v[64:67], v91, s[44:47], s54 offen sc1
mav 20,212,52
mav 24,216,52
mav 28,220,52
mav 16,224,56
buffer_store_dwordx4 v[68:71], v92, s[44:47], s54 offen sc1
mav 20,228,56
mav 24,232,56
mav 28,236,56
s_waitcnt lgkmcnt(0)
mav 16,240,60
buffer_load_dwordx4 v[124:127], v90, s[36:39], s49 offen sc1
mav 20,244,60
mav 24,248,60
mav 28,252,60
s_waitcnt vmcnt(18)
mvv 16,116,164
s_waitcnt vmcnt(12)
buffer_load_dwordx4 v[128:131], v90, s[36:39], s49 offen offset:1024 sc1
mvv 20,120,164
buffer_store_dword v72, v81, s[36:39], s52 offen sc1
mvv 24,116,168
s_xor_b32 s50, s50, 0x10000
s_sub_u32 s54, s54, 32768
mvv 28,120,168
mvv 16,116,172
mvv 20,120,172
v_xor_b32 v80, 0x10000, v80
mvv 24,116,176
s_add_u32 s49, s49, 313344
mvv 28,120,176
s_barrier
mvv 16,116,180
R 32,0
mvv 20,120,180
s_waitcnt vmcnt(13)
ds_read_b128 v[204:207], v98 offset:1024
mvv 24,116,184
mvv 28,120,184
s_waitcnt vmcnt(12)
mvv 16,116,188
buffer_load_dwordx4 v[132:135], v86, s[36:39], s49 offen sc1
mvv 20,120,188
s_waitcnt vmcnt(12)
R 36,1024
mvv 24,116,192
mvv 28,120,192
s_waitcnt vmcnt(11)
mvv 16,116,196
s_waitcnt lgkmcnt(1)
buffer_load_dwordx4 v[136:139], v86, s[36:39], s49 offen offset:1024 sc1
mvv 20,120,196
s_waitcnt vmcnt(11)
R 40,2048
v_add_f32 v204, v204, v205
mvv 24,116,200
v_add_f32 v206, v206, v207
v_add_f32 v204, v204, v206
mvv 28,120,200
v_mfma_f32_16x16x32_bf16 v[0:3], a[0:3], v[32:35], v[100:103]
buffer_load_dwordx4 v[140:143], v87, s[36:39], s49 offen sc1
v_mfma_f32_16x16x32_bf16 v[4:7], a[4:7], v[32:35], v[104:107]
R 44,3072
v_add_f32_dpp v205, v204, v204 quad_perm:[1,0,3,2] row_mask:0xf bank_mask:0xf
v_mfma_f32_16x16x32_bf16 v[8:11], a[8:11], v[32:35], v[108:111]
buffer_store_dword v205, v99, s[36:39], s49 offen sc1
v_mfma_f32_16x16x32_bf16 v[12:15], a[12:15], v[32:35], v[112:115]
s_waitcnt lgkmcnt(1)
v_cvt_pk_bf16_f32 v64, v16, v17
v_cvt_pk_bf16_f32 v65, v18, v19
mav 0,16,36
buffer_load_dwordx4 v[144:147], v87, s[36:39], s49 offen offset:1024 sc1
mav 4,20,36
R 48,4096
v_cvt_pk_bf16_f32 v66, v20, v21
mav 8,24,36
v_cvt_pk_bf16_f32 v67, v22, v23
v_cvt_pk_bf16_f32 v68, v24, v25
mav 12,28,36
v_cvt_pk_bf16_f32 v69, v26, v27
v_cvt_pk_bf16_f32 v70, v28, v29
mav 0,32,40
buffer_load_dwordx4 v[148:151], v88, s[36:39], s49 offen sc1
mav 4,36,40
R 52,5120
v_cvt_pk_bf16_f32 v71, v30, v31
mav 8,40,40
mav 12,44,40
s_waitcnt lgkmcnt(1)
mav 0,48,44
buffer_load_dwordx4 v[152:155], v88, s[36:39], s49 offen offset:1024 sc1
mav 4,52,44
R 56,6144
mav 8,56,44
mav 12,60,44
mav 0,64,48
buffer_load_dwordx4 v[156:159], v89, s[36:39], s49 offen sc1
mav 4,68,48
R 60,7168
mav 8,72,48
mav 12,76,48
s_waitcnt lgkmcnt(1)
mav 0,80,52
buffer_load_dwordx4 v[160:163], v89, s[36:39], s49 offen offset:1024 sc1
mav 4,84,52
R 32,8192
mav 8,88,52
s_add_u32 m0, s50, 0
mav 12,92,52
ds_write_b128 v94, v[64:67] offset:0
mav 0,96,56
D 83,0
mav 4,100,56
R 36,9216
mav 8,104,56
ds_write_b128 v95, v[68:71] offset:0
mav 12,108,56
s_waitcnt lgkmcnt(3)
ds_read_b128 v[64:67], v96 offset:0
mav 0,112,60
D 83,1024
mav 4,116,60
R 40,10240
mav 8,120,60
ds_read_b128 v[68:71], v96 offset:1024
mav 12,124,60
mav 0,128,32
R 44,11264
mav 4,132,32
mav 8,136,32
mav 12,140,32
s_waitcnt lgkmcnt(2)
mav 0,144,36
R 48,12288
mav 4,148,36
mav 8,152,36
mav 12,156,36
mav 0,160,40
R 52,13312
v_add_u32 v72, 68, v72
mav 4,164,40
mav 8,168,40
mav 12,172,40
s_waitcnt lgkmcnt(1)
mav 0,176,44
D 83,2048
mav 4,180,44
R 56,14336
mav 8,184,44
mav 12,188,44
mav 0,192,48
D 83,3072
mav 4,196,48
R 60,15360
mav 8,200,48
mav 12,204,48
s_waitcnt lgkmcnt(1)
mav 0,208,52
buffer_store_dwordx4 v[64:67], v91, s[44:47], s54 offen sc1
mav 4,212,52
mav 8,216,52
mav 12,220,52
mav 0,224,56
buffer_store_dwordx4 v[68:71], v92, s[44:47], s54 offen sc1
mav 4,228,56
mav 8,232,56
mav 12,236,56
s_waitcnt lgkmcnt(0)
mav 0,240,60
buffer_load_dwordx4 v[164:167], v90, s[36:39], s49 offen sc1
mav 4,244,60
mav 8,248,60
mav 12,252,60
s_waitcnt vmcnt(18)
mvv 0,116,124
s_waitcnt vmcnt(12)
buffer_load_dwordx4 v[168:171], v90, s[36:39], s49 offen offset:1024 sc1
LsR9:
s_cbranch_execz LsR13
mvv 4,120,124
buffer_store_dword v72, v81, s[36:39], s52 offen sc1
mvv 8,116,128
s_sub_u32 s54, s54, 32768
mvv 12,120,128
mvv 0,116,132
mvv 4,120,132
mvv 8,116,136
s_add_u32 s49, s49, 313344
mvv 12,120,136
s_barrier
mvv 0,116,140
R 32,32768
mvv 4,120,140
s_waitcnt vmcnt(13)
mvv 8,116,144
mvv 12,120,144
s_waitcnt vmcnt(12)
mvv 0,116,148
buffer_load_dwordx4 v[172:175], v86, s[36:39], s49 offen sc1
mvv 4,120,148
s_waitcnt vmcnt(12)
R 36,33792
mvv 8,116,152
mvv 12,120,152
s_waitcnt vmcnt(11)
mvv 0,116,156
buffer_load_dwordx4 v[176:179], v86, s[36:39], s49 offen offset:1024 sc1
mvv 4,120,156
s_waitcnt vmcnt(11)
R 40,34816
mvv 8,116,160
mvv 12,120,160
s_waitcnt lgkmcnt(1)
v_mfma_f32_16x16x32_bf16 v[16:19], a[0:3], v[32:35], v[100:103]
buffer_load_dwordx4 v[180:183], v87, s[36:39], s49 offen sc1
v_mfma_f32_16x16x32_bf16 v[20:23], a[4:7], v[32:35], v[104:107]
R 44,35840
v_cvt_pk_bf16_f32 v64, v0, v1
v_mfma_f32_16x16x32_bf16 v[24:27], a[8:11], v[32:35], v[108:111]
v_cvt_pk_bf16_f32 v65, v2, v3
v_cvt_pk_bf16_f32 v66, v4, v5
v_mfma_f32_16x16x32_bf16 v[28:31], a[12:15], v[32:35], v[112:115]
v_cvt_pk_bf16_f32 v67, v6, v7
v_cvt_pk_bf16_f32 v68, v8, v9
mav 16,16,36
buffer_load_dwordx4 v[184:187], v87, s[36:39], s49 offen offset:1024 sc1
mav 20,20,36
R 48,36864
v_cvt_pk_bf16_f32 v69, v10, v11
mav 24,24,36
v_cvt_pk_bf16_f32 v70, v12, v13
v_cvt_pk_bf16_f32 v71, v14, v15
mav 28,28,36
s_waitcnt lgkmcnt(1)
mav 16,32,40
buffer_load_dwordx4 v[188:191], v88, s[36:39], s49 offen sc1
mav 20,36,40
R 52,37888
mav 24,40,40
mav 28,44,40
mav 16,48,44
buffer_load_dwordx4 v[192:195], v88, s[36:39], s49 offen offset:1024 sc1
mav 20,52,44
R 56,38912
mav 24,56,44
mav 28,60,44
s_waitcnt lgkmcnt(1)
mav 16,64,48
buffer_load_dwordx4 v[196:199], v89, s[36:39], s49 offen sc1
mav 20,68,48
R 60,39936
mav 24,72,48
mav 28,76,48
mav 16,80,52
buffer_load_dwordx4 v[200:203], v89, s[36:39], s49 offen offset:1024 sc1
mav 20,84,52
R 32,40960
mav 24,88,52
s_add_u32 m0, s50, 32768
mav 28,92,52
s_waitcnt lgkmcnt(1)
ds_write_b128 v94, v[64:67] offset:0
mav 16,96,56
D 83,0
mav 20,100,56
R 36,41984
mav 24,104,56
ds_write_b128 v95, v[68:71] offset:0
mav 28,108,56
ds_read_b128 v[64:67], v96 offset:0
mav 16,112,60
D 83,1024
mav 20,116,60
R 40,43008
mav 24,120,60
ds_read_b128 v[68:71], v96 offset:1024
mav 28,124,60
s_waitcnt lgkmcnt(4)
mav 16,128,32
R 44,44032
mav 20,132,32
mav 24,136,32
mav 28,140,32
mav 16,144,36
R 48,45056
mav 20,148,36
mav 24,152,36
mav 28,156,36
s_waitcnt lgkmcnt(1)
mav 16,160,40
mav 20,164,40
R 52,46080
mav 24,168,40
mav 28,172,40
mav 16,176,44
D 83,2048
mav 20,180,44
R 56,47104
mav 24,184,44
mav 28,188,44
s_waitcnt lgkmcnt(1)
mav 16,192,48
D 83,3072
mav 20,196,48
R 60,48128
mav 24,200,48
mav 28,204,48
mav 16,208,52
buffer_store_dwordx4 v[64:67], v91, s[44:47], s54 offen sc1
mav 20,212,52
mav 24,216,52
mav 28,220,52
s_waitcnt lgkmcnt(0)
mav 16,224,56
buffer_store_dwordx4 v[68:71], v92, s[44:47], s54 offen sc1
mav 20,228,56
mav 24,232,56
mav 28,236,56
mav 16,240,60
buffer_load_dwordx4 v[124:127], v90, s[36:39], s49 offen sc1
mav 20,244,60
mav 24,248,60
mav 28,252,60
s_waitcnt vmcnt(17)
mvv 16,116,164
buffer_load_dwordx4 v[128:131], v90, s[36:39], s49 offen offset:1024 sc1
mvv 20,120,164
s_waitcnt vmcnt(17)
mvv 24,116,168
s_xor_b32 s50, s50, 0x10000
s_sub_u32 s54, s54, 32768
mvv 28,120,168
s_waitcnt vmcnt(15)
mvv 16,116,172
mvv 20,120,172
s_waitcnt vmcnt(14)
v_xor_b32 v80, 0x10000, v80
mvv 24,116,176
s_waitcnt vmcnt(13)
s_add_u32 s49, s49, 313344
mvv 28,120,176
s_barrier
mvv 16,116,180
R 32,0
mvv 20,120,180
s_waitcnt vmcnt(12)
mvv 24,116,184
mvv 28,120,184
s_waitcnt vmcnt(11)
mvv 16,116,188
buffer_load_dwordx4 v[132:135], v86, s[36:39], s49 offen sc1
mvv 20,120,188
s_waitcnt vmcnt(11)
R 36,1024
mvv 24,116,192
mvv 28,120,192
s_waitcnt vmcnt(10)
mvv 16,116,196
buffer_load_dwordx4 v[136:139], v86, s[36:39], s49 offen offset:1024 sc1
mvv 20,120,196
s_waitcnt vmcnt(10)
R 40,2048
mvv 24,116,200
mvv 28,120,200
s_waitcnt lgkmcnt(1)
v_mfma_f32_16x16x32_bf16 v[0:3], a[0:3], v[32:35], v[100:103]
buffer_load_dwordx4 v[140:143], v87, s[36:39], s49 offen sc1
v_mfma_f32_16x16x32_bf16 v[4:7], a[4:7], v[32:35], v[104:107]
R 44,3072
v_cvt_pk_bf16_f32 v64, v16, v17
v_mfma_f32_16x16x32_bf16 v[8:11], a[8:11], v[32:35], v[108:111]
v_cvt_pk_bf16_f32 v65, v18, v19
v_cvt_pk_bf16_f32 v66, v20, v21
v_mfma_f32_16x16x32_bf16 v[12:15], a[12:15], v[32:35], v[112:115]
v_cvt_pk_bf16_f32 v67, v22, v23
v_cvt_pk_bf16_f32 v68, v24, v25
mav 0,16,36
buffer_load_dwordx4 v[144:147], v87, s[36:39], s49 offen offset:1024 sc1
mav 4,20,36
R 48,4096
v_cvt_pk_bf16_f32 v69, v26, v27
mav 8,24,36
v_cvt_pk_bf16_f32 v70, v28, v29
v_cvt_pk_bf16_f32 v71, v30, v31
mav 12,28,36
s_waitcnt lgkmcnt(1)
mav 0,32,40
buffer_load_dwordx4 v[148:151], v88, s[36:39], s49 offen sc1
mav 4,36,40
R 52,5120
mav 8,40,40
mav 12,44,40
mav 0,48,44
buffer_load_dwordx4 v[152:155], v88, s[36:39], s49 offen offset:1024 sc1
mav 4,52,44
R 56,6144
mav 8,56,44
mav 12,60,44
s_waitcnt lgkmcnt(1)
mav 0,64,48
buffer_load_dwordx4 v[156:159], v89, s[36:39], s49 offen sc1
mav 4,68,48
R 60,7168
mav 8,72,48
mav 12,76,48
mav 0,80,52
buffer_load_dwordx4 v[160:163], v89, s[36:39], s49 offen offset:1024 sc1
mav 4,84,52
R 32,8192
mav 8,88,52
s_add_u32 m0, s50, 0
mav 12,92,52
s_waitcnt lgkmcnt(1)
ds_write_b128 v94, v[64:67] offset:0
mav 0,96,56
D 83,0
mav 4,100,56
R 36,9216
mav 8,104,56
ds_write_b128 v95, v[68:71] offset:0
mav 12,108,56
ds_read_b128 v[64:67], v96 offset:0
mav 0,112,60
D 83,1024
mav 4,116,60
R 40,10240
mav 8,120,60
ds_read_b128 v[68:71], v96 offset:1024
mav 12,124,60
s_waitcnt lgkmcnt(4)
mav 0,128,32
R 44,11264
mav 4,132,32
mav 8,136,32
mav 12,140,32
mav 0,144,36
R 48,12288
mav 4,148,36
mav 8,152,36
mav 12,156,36
s_waitcnt lgkmcnt(1)
mav 0,160,40
R 52,13312
mav 4,164,40
mav 8,168,40
mav 12,172,40
mav 0,176,44
D 83,2048
mav 4,180,44
R 56,14336
mav 8,184,44
mav 12,188,44
s_waitcnt lgkmcnt(1)
mav 0,192,48
D 83,3072
mav 4,196,48
R 60,15360
mav 8,200,48
mav 12,204,48
mav 0,208,52
buffer_store_dwordx4 v[64:67], v91, s[44:47], s54 offen sc1
mav 4,212,52
mav 8,216,52
mav 12,220,52
s_waitcnt lgkmcnt(0)
mav 0,224,56
buffer_store_dwordx4 v[68:71], v92, s[44:47], s54 offen sc1
mav 4,228,56
mav 8,232,56
mav 12,236,56
mav 0,240,60
buffer_load_dwordx4 v[164:167], v90, s[36:39], s49 offen sc1
mav 4,244,60
mav 8,248,60
mav 12,252,60
s_waitcnt vmcnt(16)
mvv 0,116,124
buffer_load_dwordx4 v[168:171], v90, s[36:39], s49 offen offset:1024 sc1
mvv 4,120,124
s_waitcnt vmcnt(16)
mvv 8,116,128
s_sub_u32 s54, s54, 32768
mvv 12,120,128
s_waitcnt vmcnt(15)
mvv 0,116,132
mvv 4,120,132
s_waitcnt vmcnt(14)
mvv 8,116,136
s_waitcnt vmcnt(13)
s_add_u32 s49, s49, 313344
mvv 12,120,136
s_barrier
mvv 0,116,140
R 32,32768
mvv 4,120,140
s_waitcnt vmcnt(12)
mvv 8,116,144
mvv 12,120,144
s_waitcnt vmcnt(11)
mvv 0,116,148
buffer_load_dwordx4 v[172:175], v86, s[36:39], s49 offen sc1
mvv 4,120,148
s_waitcnt vmcnt(11)
R 36,33792
mvv 8,116,152
mvv 12,120,152
s_waitcnt vmcnt(10)
mvv 0,116,156
buffer_load_dwordx4 v[176:179], v86, s[36:39], s49 offen offset:1024 sc1
mvv 4,120,156
s_waitcnt vmcnt(10)
R 40,34816
mvv 8,116,160
mvv 12,120,160
s_waitcnt lgkmcnt(1)
v_mfma_f32_16x16x32_bf16 v[16:19], a[0:3], v[32:35], v[100:103]
buffer_load_dwordx4 v[180:183], v87, s[36:39], s49 offen sc1
v_mfma_f32_16x16x32_bf16 v[20:23], a[4:7], v[32:35], v[104:107]
R 44,35840
v_cvt_pk_bf16_f32 v64, v0, v1
v_mfma_f32_16x16x32_bf16 v[24:27], a[8:11], v[32:35], v[108:111]
v_cvt_pk_bf16_f32 v65, v2, v3
v_cvt_pk_bf16_f32 v66, v4, v5
v_mfma_f32_16x16x32_bf16 v[28:31], a[12:15], v[32:35], v[112:115]
v_cvt_pk_bf16_f32 v67, v6, v7
v_cvt_pk_bf16_f32 v68, v8, v9
mav 16,16,36
buffer_load_dwordx4 v[184:187], v87, s[36:39], s49 offen offset:1024 sc1
mav 20,20,36
R 48,36864
v_cvt_pk_bf16_f32 v69, v10, v11
mav 24,24,36
v_cvt_pk_bf16_f32 v70, v12, v13
v_cvt_pk_bf16_f32 v71, v14, v15
mav 28,28,36
s_waitcnt lgkmcnt(1)
mav 16,32,40
buffer_load_dwordx4 v[188:191], v88, s[36:39], s49 offen sc1
mav 20,36,40
R 52,37888
mav 24,40,40
mav 28,44,40
mav 16,48,44
buffer_load_dwordx4 v[192:195], v88, s[36:39], s49 offen offset:1024 sc1
mav 20,52,44
R 56,38912
mav 24,56,44
mav 28,60,44
s_waitcnt lgkmcnt(1)
mav 16,64,48
buffer_load_dwordx4 v[196:199], v89, s[36:39], s49 offen sc1
mav 20,68,48
R 60,39936
mav 24,72,48
mav 28,76,48
mav 16,80,52
buffer_load_dwordx4 v[200:203], v89, s[36:39], s49 offen offset:1024 sc1
mav 20,84,52
R 32,40960
mav 24,88,52
s_add_u32 m0, s50, 32768
mav 28,92,52
s_waitcnt lgkmcnt(1)
ds_write_b128 v94, v[64:67] offset:0
mav 16,96,56
D 83,0
mav 20,100,56
R 36,41984
mav 24,104,56
ds_write_b128 v95, v[68:71] offset:0
mav 28,108,56
ds_read_b128 v[64:67], v96 offset:0
mav 16,112,60
D 83,1024
mav 20,116,60
R 40,43008
mav 24,120,60
ds_read_b128 v[68:71], v96 offset:1024
mav 28,124,60
s_waitcnt lgkmcnt(4)
mav 16,128,32
R 44,44032
mav 20,132,32
mav 24,136,32
mav 28,140,32
mav 16,144,36
R 48,45056
mav 20,148,36
mav 24,152,36
mav 28,156,36
s_waitcnt lgkmcnt(1)
mav 16,160,40
s_mov_b32 s60, -12
.p2align 6
LR1_2:
mav 20,164,40
R 52,46080
mav 24,168,40
mav 28,172,40
mav 16,176,44
D 83,2048
mav 20,180,44
R 56,47104
mav 24,184,44
mav 28,188,44
s_waitcnt lgkmcnt(1)
mav 16,192,48
D 83,3072
mav 20,196,48
R 60,48128
mav 24,200,48
mav 28,204,48
mav 16,208,52
buffer_store_dwordx4 v[64:67], v91, s[44:47], s54 offen sc1
mav 20,212,52
mav 24,216,52
mav 28,220,52
s_waitcnt lgkmcnt(0)
mav 16,224,56
buffer_store_dwordx4 v[68:71], v92, s[44:47], s54 offen sc1
mav 20,228,56
mav 24,232,56
mav 28,236,56
mav 16,240,60
buffer_load_dwordx4 v[124:127], v90, s[36:39], s49 offen sc1
mav 20,244,60
mav 24,248,60
mav 28,252,60
s_waitcnt vmcnt(16)
mvv 16,116,164
buffer_load_dwordx4 v[128:131], v90, s[36:39], s49 offen offset:1024 sc1
mvv 20,120,164
s_waitcnt vmcnt(16)
mvv 24,116,168
s_xor_b32 s50, s50, 0x10000
s_sub_u32 s54, s54, 32768
mvv 28,120,168
s_waitcnt vmcnt(15)
mvv 16,116,172
mvv 20,120,172
s_waitcnt vmcnt(14)
v_xor_b32 v80, 0x10000, v80
mvv 24,116,176
s_waitcnt vmcnt(13)
s_add_u32 s49, s49, 313344
mvv 28,120,176
s_barrier
mvv 16,116,180
R 32,0
mvv 20,120,180
s_waitcnt vmcnt(12)
mvv 24,116,184
mvv 28,120,184
s_waitcnt vmcnt(11)
mvv 16,116,188
buffer_load_dwordx4 v[132:135], v86, s[36:39], s49 offen sc1
mvv 20,120,188
s_waitcnt vmcnt(11)
R 36,1024
mvv 24,116,192
mvv 28,120,192
s_waitcnt vmcnt(10)
mvv 16,116,196
buffer_load_dwordx4 v[136:139], v86, s[36:39], s49 offen offset:1024 sc1
mvv 20,120,196
s_waitcnt vmcnt(10)
R 40,2048
mvv 24,116,200
mvv 28,120,200
s_waitcnt lgkmcnt(1)
v_mfma_f32_16x16x32_bf16 v[0:3], a[0:3], v[32:35], v[100:103]
buffer_load_dwordx4 v[140:143], v87, s[36:39], s49 offen sc1
v_mfma_f32_16x16x32_bf16 v[4:7], a[4:7], v[32:35], v[104:107]
R 44,3072
v_cvt_pk_bf16_f32 v64, v16, v17
v_mfma_f32_16x16x32_bf16 v[8:11], a[8:11], v[32:35], v[108:111]
v_cvt_pk_bf16_f32 v65, v18, v19
v_cvt_pk_bf16_f32 v66, v20, v21
v_mfma_f32_16x16x32_bf16 v[12:15], a[12:15], v[32:35], v[112:115]
v_cvt_pk_bf16_f32 v67, v22, v23
v_cvt_pk_bf16_f32 v68, v24, v25
mav 0,16,36
buffer_load_dwordx4 v[144:147], v87, s[36:39], s49 offen offset:1024 sc1
mav 4,20,36
R 48,4096
v_cvt_pk_bf16_f32 v69, v26, v27
mav 8,24,36
v_cvt_pk_bf16_f32 v70, v28, v29
v_cvt_pk_bf16_f32 v71, v30, v31
mav 12,28,36
s_waitcnt lgkmcnt(1)
mav 0,32,40
buffer_load_dwordx4 v[148:151], v88, s[36:39], s49 offen sc1
mav 4,36,40
R 52,5120
mav 8,40,40
mav 12,44,40
mav 0,48,44
buffer_load_dwordx4 v[152:155], v88, s[36:39], s49 offen offset:1024 sc1
mav 4,52,44
R 56,6144
mav 8,56,44
mav 12,60,44
s_waitcnt lgkmcnt(1)
mav 0,64,48
buffer_load_dwordx4 v[156:159], v89, s[36:39], s49 offen sc1
mav 4,68,48
R 60,7168
mav 8,72,48
mav 12,76,48
mav 0,80,52
buffer_load_dwordx4 v[160:163], v89, s[36:39], s49 offen offset:1024 sc1
mav 4,84,52
R 32,8192
mav 8,88,52
s_add_u32 m0, s50, 0
mav 12,92,52
s_waitcnt lgkmcnt(1)
ds_write_b128 v94, v[64:67] offset:0
mav 0,96,56
D 83,0
mav 4,100,56
R 36,9216
mav 8,104,56
LsR10:
s_cbranch_execz LsR14
ds_write_b128 v95, v[68:71] offset:0
mav 12,108,56
ds_read_b128 v[64:67], v96 offset:0
mav 0,112,60
D 83,1024
mav 4,116,60
R 40,10240
mav 8,120,60
ds_read_b128 v[68:71], v96 offset:1024
mav 12,124,60
s_waitcnt lgkmcnt(4)
mav 0,128,32
R 44,11264
mav 4,132,32
mav 8,136,32
mav 12,140,32
mav 0,144,36
R 48,12288
mav 4,148,36
mav 8,152,36
mav 12,156,36
s_waitcnt lgkmcnt(1)
mav 0,160,40
R 52,13312
mav 4,164,40
mav 8,168,40
mav 12,172,40
mav 0,176,44
D 83,2048
mav 4,180,44
R 56,14336
mav 8,184,44
mav 12,188,44
s_waitcnt lgkmcnt(1)
mav 0,192,48
D 83,3072
mav 4,196,48
R 60,15360
mav 8,200,48
mav 12,204,48
mav 0,208,52
buffer_store_dwordx4 v[64:67], v91, s[44:47], s54 offen sc1
mav 4,212,52
mav 8,216,52
mav 12,220,52
s_waitcnt lgkmcnt(0)
mav 0,224,56
buffer_store_dwordx4 v[68:71], v92, s[44:47], s54 offen sc1
mav 4,228,56
mav 8,232,56
mav 12,236,56
mav 0,240,60
buffer_load_dwordx4 v[164:167], v90, s[36:39], s49 offen sc1
mav 4,244,60
mav 8,248,60
mav 12,252,60
s_waitcnt vmcnt(16)
mvv 0,116,124
buffer_load_dwordx4 v[168:171], v90, s[36:39], s49 offen offset:1024 sc1
mvv 4,120,124
s_waitcnt vmcnt(16)
mvv 8,116,128
s_sub_u32 s54, s54, 32768
mvv 12,120,128
s_waitcnt vmcnt(15)
mvv 0,116,132
mvv 4,120,132
s_waitcnt vmcnt(14)
mvv 8,116,136
s_waitcnt vmcnt(13)
s_add_u32 s49, s49, 313344
mvv 12,120,136
s_barrier
mvv 0,116,140
R 32,32768
mvv 4,120,140
s_waitcnt vmcnt(12)
mvv 8,116,144
mvv 12,120,144
s_waitcnt vmcnt(11)
mvv 0,116,148
buffer_load_dwordx4 v[172:175], v86, s[36:39], s49 offen sc1
mvv 4,120,148
s_waitcnt vmcnt(11)
R 36,33792
mvv 8,116,152
mvv 12,120,152
s_waitcnt vmcnt(10)
mvv 0,116,156
buffer_load_dwordx4 v[176:179], v86, s[36:39], s49 offen offset:1024 sc1
mvv 4,120,156
s_waitcnt vmcnt(10)
R 40,34816
mvv 8,116,160
mvv 12,120,160
s_waitcnt lgkmcnt(1)
v_mfma_f32_16x16x32_bf16 v[16:19], a[0:3], v[32:35], v[100:103]
buffer_load_dwordx4 v[180:183], v87, s[36:39], s49 offen sc1
v_mfma_f32_16x16x32_bf16 v[20:23], a[4:7], v[32:35], v[104:107]
R 44,35840
v_cvt_pk_bf16_f32 v64, v0, v1
v_mfma_f32_16x16x32_bf16 v[24:27], a[8:11], v[32:35], v[108:111]
v_cvt_pk_bf16_f32 v65, v2, v3
v_cvt_pk_bf16_f32 v66, v4, v5
v_mfma_f32_16x16x32_bf16 v[28:31], a[12:15], v[32:35], v[112:115]
v_cvt_pk_bf16_f32 v67, v6, v7
v_cvt_pk_bf16_f32 v68, v8, v9
mav 16,16,36
buffer_load_dwordx4 v[184:187], v87, s[36:39], s49 offen offset:1024 sc1
mav 20,20,36
R 48,36864
v_cvt_pk_bf16_f32 v69, v10, v11
mav 24,24,36
v_cvt_pk_bf16_f32 v70, v12, v13
v_cvt_pk_bf16_f32 v71, v14, v15
mav 28,28,36
s_waitcnt lgkmcnt(1)
mav 16,32,40
buffer_load_dwordx4 v[188:191], v88, s[36:39], s49 offen sc1
mav 20,36,40
R 52,37888
mav 24,40,40
mav 28,44,40
mav 16,48,44
buffer_load_dwordx4 v[192:195], v88, s[36:39], s49 offen offset:1024 sc1
mav 20,52,44
R 56,38912
mav 24,56,44
mav 28,60,44
s_waitcnt lgkmcnt(1)
mav 16,64,48
buffer_load_dwordx4 v[196:199], v89, s[36:39], s49 offen sc1
mav 20,68,48
R 60,39936
mav 24,72,48
mav 28,76,48
mav 16,80,52
buffer_load_dwordx4 v[200:203], v89, s[36:39], s49 offen offset:1024 sc1
mav 20,84,52
R 32,40960
mav 24,88,52
s_add_u32 m0, s50, 32768
mav 28,92,52
s_waitcnt lgkmcnt(1)
ds_write_b128 v94, v[64:67] offset:0
mav 16,96,56
D 83,0
mav 20,100,56
R 36,41984
mav 24,104,56
ds_write_b128 v95, v[68:71] offset:0
mav 28,108,56
ds_read_b128 v[64:67], v96 offset:0
mav 16,112,60
D 83,1024
mav 20,116,60
R 40,43008
mav 24,120,60
ds_read_b128 v[68:71], v96 offset:1024
mav 28,124,60
s_waitcnt lgkmcnt(4)
mav 16,128,32
R 44,44032
mav 20,132,32
mav 24,136,32
mav 28,140,32
mav 16,144,36
R 48,45056
mav 20,148,36
mav 24,152,36
mav 28,156,36
s_waitcnt lgkmcnt(1)
s_add_u32 s60, s60, 1
mav 16,160,40
s_cbranch_scc0 LR1_2
mav 20,164,40
R 52,46080
mav 24,168,40
mav 28,172,40
mav 16,176,44
D 83,2048
mav 20,180,44
R 56,47104
mav 24,184,44
mav 28,188,44
s_waitcnt lgkmcnt(1)
mav 16,192,48
D 83,3072
mav 20,196,48
R 60,48128
mav 24,200,48
mav 28,204,48
mav 16,208,52
buffer_store_dwordx4 v[64:67], v91, s[44:47], s54 offen sc1
mav 20,212,52
mav 24,216,52
mav 28,220,52
s_waitcnt lgkmcnt(0)
mav 16,224,56
buffer_store_dwordx4 v[68:71], v92, s[44:47], s54 offen sc1
mav 20,228,56
mav 24,232,56
mav 28,236,56
mav 16,240,60
buffer_load_dwordx4 v[124:127], v90, s[36:39], s49 offen sc1
mav 20,244,60
mav 24,248,60
mav 28,252,60
s_waitcnt vmcnt(16)
mvv 16,116,164
buffer_load_dwordx4 v[128:131], v90, s[36:39], s49 offen offset:1024 sc1
mvv 20,120,164
s_waitcnt vmcnt(16)
mvv 24,116,168
s_sub_u32 s54, s54, 32768
mvv 28,120,168
s_waitcnt vmcnt(15)
mvv 16,116,172
mvv 20,120,172
s_waitcnt vmcnt(14)
v_xor_b32 v80, 0x10000, v80
mvv 24,116,176
s_waitcnt vmcnt(13)
s_add_u32 s49, s49, 313344
mvv 28,120,176
s_barrier
mvv 16,116,180
R 32,0
mvv 20,120,180
s_waitcnt vmcnt(12)
mvv 24,116,184
mvv 28,120,184
s_waitcnt vmcnt(11)
mvv 16,116,188
buffer_load_dwordx4 v[132:135], v86, s[36:39], s49 offen sc1
mvv 20,120,188
s_waitcnt vmcnt(11)
R 36,1024
mvv 24,116,192
mvv 28,120,192
s_waitcnt vmcnt(10)
mvv 16,116,196
buffer_load_dwordx4 v[136:139], v86, s[36:39], s49 offen offset:1024 sc1
mvv 20,120,196
s_waitcnt vmcnt(10)
R 40,2048
mvv 24,116,200
mvv 28,120,200
s_waitcnt lgkmcnt(1)
v_mfma_f32_16x16x32_bf16 v[0:3], a[0:3], v[32:35], v[100:103]
buffer_load_dwordx4 v[140:143], v87, s[36:39], s49 offen sc1
v_mfma_f32_16x16x32_bf16 v[4:7], a[4:7], v[32:35], v[104:107]
R 44,3072
v_cvt_pk_bf16_f32 v64, v16, v17
v_mfma_f32_16x16x32_bf16 v[8:11], a[8:11], v[32:35], v[108:111]
v_cvt_pk_bf16_f32 v65, v18, v19
v_cvt_pk_bf16_f32 v66, v20, v21
v_mfma_f32_16x16x32_bf16 v[12:15], a[12:15], v[32:35], v[112:115]
v_cvt_pk_bf16_f32 v67, v22, v23
v_cvt_pk_bf16_f32 v68, v24, v25
mav 0,16,36
buffer_load_dwordx4 v[144:147], v87, s[36:39], s49 offen offset:1024 sc1
mav 4,20,36
R 48,4096
v_cvt_pk_bf16_f32 v69, v26, v27
mav 8,24,36
v_cvt_pk_bf16_f32 v70, v28, v29
v_cvt_pk_bf16_f32 v71, v30, v31
mav 12,28,36
s_waitcnt lgkmcnt(1)
mav 0,32,40
buffer_load_dwordx4 v[148:151], v88, s[36:39], s49 offen sc1
mav 4,36,40
R 52,5120
mav 8,40,40
mav 12,44,40
mav 0,48,44
buffer_load_dwordx4 v[152:155], v88, s[36:39], s49 offen offset:1024 sc1
mav 4,52,44
R 56,6144
mav 8,56,44
mav 12,60,44
s_waitcnt lgkmcnt(1)
mav 0,64,48
buffer_load_dwordx4 v[156:159], v89, s[36:39], s49 offen sc1
mav 4,68,48
R 60,7168
mav 8,72,48
mav 12,76,48
mav 0,80,52
buffer_load_dwordx4 v[160:163], v89, s[36:39], s49 offen offset:1024 sc1
mav 4,84,52
R 32,8192
mav 8,88,52
mav 12,92,52
s_waitcnt lgkmcnt(1)
ds_write_b128 v94, v[64:67] offset:0
mav 0,96,56
R 36,9216
mav 4,100,56
ds_write_b128 v95, v[68:71] offset:0
mav 8,104,56
ds_read_b128 v[64:67], v96 offset:0
mav 12,108,56
ds_read_b128 v[68:71], v96 offset:1024
mav 0,112,60
R 40,10240
mav 4,116,60
mav 8,120,60
mav 12,124,60
s_waitcnt lgkmcnt(3)
mav 0,128,32
R 44,11264
mav 4,132,32
mav 8,136,32
mav 12,140,32
mav 0,144,36
R 48,12288
mav 4,148,36
mav 8,152,36
mav 12,156,36
s_waitcnt lgkmcnt(1)
mav 0,160,40
R 52,13312
mav 4,164,40
mav 8,168,40
mav 12,172,40
mav 0,176,44
R 56,14336
mav 4,180,44
mav 8,184,44
mav 12,188,44
s_waitcnt lgkmcnt(1)
mav 0,192,48
R 60,15360
mav 4,196,48
mav 8,200,48
mav 12,204,48
mav 0,208,52
buffer_store_dwordx4 v[64:67], v91, s[44:47], s54 offen sc1
mav 4,212,52
mav 8,216,52
mav 12,220,52
s_waitcnt lgkmcnt(0)
mav 0,224,56
buffer_store_dwordx4 v[68:71], v92, s[44:47], s54 offen sc1
mav 4,228,56
mav 8,232,56
mav 12,236,56
mav 0,240,60
buffer_load_dwordx4 v[164:167], v90, s[36:39], s49 offen sc1
mav 4,244,60
mav 8,248,60
mav 12,252,60
s_waitcnt vmcnt(12)
mvv 0,116,124
buffer_load_dwordx4 v[168:171], v90, s[36:39], s49 offen offset:1024 sc1
mvv 4,120,124
s_waitcnt vmcnt(12)
mvv 8,116,128
s_sub_u32 s54, s54, 32768
mvv 12,120,128
s_waitcnt vmcnt(11)
mvv 0,116,132
mvv 4,120,132
s_waitcnt vmcnt(10)
mvv 8,116,136
s_waitcnt vmcnt(9)
s_add_u32 s49, s49, 313344
mvv 12,120,136
s_barrier
mvv 0,116,140
R 32,32768
mvv 4,120,140
s_waitcnt vmcnt(8)
mvv 8,116,144
mvv 12,120,144
s_waitcnt vmcnt(7)
mvv 0,116,148
buffer_load_dwordx4 v[172:175], v86, s[36:39], s49 offen sc1
mvv 4,120,148
s_waitcnt vmcnt(7)
R 36,33792
mvv 8,116,152
mvv 12,120,152
s_waitcnt vmcnt(6)
mvv 0,116,156
buffer_load_dwordx4 v[176:179], v86, s[36:39], s49 offen offset:1024 sc1
mvv 4,120,156
s_waitcnt vmcnt(6)
R 40,34816
mvv 8,116,160
mvv 12,120,160
s_waitcnt lgkmcnt(1)
v_mfma_f32_16x16x32_bf16 v[16:19], a[0:3], v[32:35], v[100:103]
buffer_load_dwordx4 v[180:183], v87, s[36:39], s49 offen sc1
v_mfma_f32_16x16x32_bf16 v[20:23], a[4:7], v[32:35], v[104:107]
R 44,35840
v_cvt_pk_bf16_f32 v64, v0, v1
v_mfma_f32_16x16x32_bf16 v[24:27], a[8:11], v[32:35], v[108:111]
v_cvt_pk_bf16_f32 v65, v2, v3
v_cvt_pk_bf16_f32 v66, v4, v5
v_mfma_f32_16x16x32_bf16 v[28:31], a[12:15], v[32:35], v[112:115]
v_cvt_pk_bf16_f32 v67, v6, v7
v_cvt_pk_bf16_f32 v68, v8, v9
mav 16,16,36
buffer_load_dwordx4 v[184:187], v87, s[36:39], s49 offen offset:1024 sc1
mav 20,20,36
R 48,36864
v_cvt_pk_bf16_f32 v69, v10, v11
mav 24,24,36
ds_write_b128 v94, v[64:67] offset:0
mav 28,28,36
s_waitcnt lgkmcnt(2)
v_cvt_pk_bf16_f32 v70, v12, v13
v_cvt_pk_bf16_f32 v71, v14, v15
mav 16,32,40
buffer_load_dwordx4 v[188:191], v88, s[36:39], s49 offen sc1
mav 20,36,40
R 52,37888
mav 24,40,40
ds_write_b128 v95, v[68:71] offset:0
mav 28,44,40
ds_read_b128 v[64:67], v96 offset:0
mav 16,48,44
buffer_load_dwordx4 v[192:195], v88, s[36:39], s49 offen offset:1024 sc1
mav 20,52,44
R 56,38912
mav 24,56,44
ds_read_b128 v[68:71], v96 offset:1024
mav 28,60,44
s_waitcnt lgkmcnt(4)
mav 16,64,48
buffer_load_dwordx4 v[196:199], v89, s[36:39], s49 offen sc1
mav 20,68,48
R 60,39936
mav 24,72,48
s_waitcnt lgkmcnt(3)
mav 28,76,48
mav 16,80,52
buffer_store_dwordx4 v[64:67], v91, s[44:47], s54 offen sc1
buffer_load_dwordx4 v[200:203], v89, s[36:39], s49 offen offset:1024 sc1
mav 20,84,52
s_waitcnt lgkmcnt(1)
R 32,40960
mav 24,88,52
mav 28,92,52
)"
R"(
mav 16,96,56
buffer_store_dwordx4 v[68:71], v92, s[44:47], s54 offen sc1
R 36,41984
mav 20,100,56
mav 24,104,56
mav 28,108,56
s_waitcnt lgkmcnt(1)
mav 16,112,60
R 40,43008
mav 20,116,60
mav 24,120,60
mav 28,124,60
mav 16,128,32
R 44,44032
mav 20,132,32
mav 24,136,32
mav 28,140,32
s_waitcnt lgkmcnt(1)
mav 16,144,36
R 48,45056
mav 20,148,36
mav 24,152,36
mav 28,156,36
mav 16,160,40
mav 20,164,40
R 52,46080
mav 24,168,40
mav 28,172,40
s_waitcnt lgkmcnt(1)
mav 16,176,44
R 56,47104
mav 20,180,44
mav 24,184,44
mav 28,188,44
mav 16,192,48
R 60,48128
mav 20,196,48
mav 24,200,48
mav 28,204,48
s_waitcnt lgkmcnt(1)
mav 16,208,52
mav 20,212,52
mav 24,216,52
mav 28,220,52
mav 16,224,56
mav 20,228,56
mav 24,232,56
mav 28,236,56
s_waitcnt lgkmcnt(0)
mav 16,240,60
mav 20,244,60
mav 24,248,60
mav 28,252,60
s_waitcnt vmcnt(11)
mvv 16,116,164
mvv 20,120,164
s_waitcnt vmcnt(10)
mvv 24,116,168
s_sub_u32 s54, s54, 32768
mvv 28,120,168
s_waitcnt vmcnt(9)
mvv 16,116,172
mvv 20,120,172
s_waitcnt vmcnt(8)
mvv 24,116,176
mvv 28,120,176
s_waitcnt vmcnt(7)
mvv 16,116,180
mvv 20,120,180
s_waitcnt vmcnt(6)
mvv 24,116,184
mvv 28,120,184
s_waitcnt vmcnt(5)
mvv 16,116,188
mvv 20,120,188
s_waitcnt vmcnt(4)
mvv 24,116,192
mvv 28,120,192
s_waitcnt vmcnt(3)
mvv 16,116,196
mvv 20,120,196
s_waitcnt vmcnt(1)
mvv 24,116,200
mvv 28,120,200
s_nop 3
v_cvt_pk_bf16_f32 v64, v16, v17
v_cvt_pk_bf16_f32 v65, v18, v19
v_cvt_pk_bf16_f32 v66, v20, v21
v_cvt_pk_bf16_f32 v67, v22, v23
ds_write_b128 v94, v[64:67] offset:0
v_cvt_pk_bf16_f32 v68, v24, v25
v_cvt_pk_bf16_f32 v69, v26, v27
v_cvt_pk_bf16_f32 v70, v28, v29
v_cvt_pk_bf16_f32 v71, v30, v31
ds_write_b128 v95, v[68:71] offset:0
ds_read_b128 v[64:67], v96 offset:0
ds_read_b128 v[68:71], v96 offset:1024
s_waitcnt lgkmcnt(0)
buffer_store_dwordx4 v[64:67], v91, s[44:47], s54 offen sc1
buffer_store_dwordx4 v[68:71], v92, s[44:47], s54 offen sc1
s_endpgm
LvR2:
LsR11:
s_cbranch_execz LsR15
s_add_u32 s49, s49, 313344
s_barrier
s_add_u32 m0, s50, 0
LsR12:
s_cbranch_execz LsR16
s_nop 0
D 83,0
D 83,1024
D 83,2048
D 83,3072
s_add_u32 s49, s49, 313344
s_barrier
s_add_u32 m0, s50, 32768
s_nop 0
D 83,0
D 83,1024
D 83,2048
D 83,3072
buffer_load_dwordx4 v[124:127], v90, s[36:39], s49 offen sc1
buffer_load_dwordx4 v[128:131], v90, s[36:39], s49 offen offset:1024 sc1
s_xor_b32 s50, s50, 0x10000
s_waitcnt vmcnt(6)
s_add_u32 s49, s49, 313344
s_barrier
R 32,0
buffer_load_dwordx4 v[132:135], v86, s[36:39], s49 offen sc1
R 36,1024
buffer_load_dwordx4 v[136:139], v86, s[36:39], s49 offen offset:1024 sc1
R 40,2048
buffer_load_dwordx4 v[140:143], v87, s[36:39], s49 offen sc1
s_waitcnt lgkmcnt(1)
v_mfma_f32_16x16x32_bf16 v[0:3], a[0:3], v[32:35], v[100:103]
R 44,3072
v_mfma_f32_16x16x32_bf16 v[4:7], a[4:7], v[32:35], v[104:107]
v_mfma_f32_16x16x32_bf16 v[8:11], a[8:11], v[32:35], v[108:111]
buffer_load_dwordx4 v[144:147], v87, s[36:39], s49 offen offset:1024 sc1
v_mfma_f32_16x16x32_bf16 v[12:15], a[12:15], v[32:35], v[112:115]
mav 0,16,36
R 48,4096
mav 4,20,36
mav 8,24,36
buffer_load_dwordx4 v[148:151], v88, s[36:39], s49 offen sc1
mav 12,28,36
s_waitcnt lgkmcnt(1)
mav 0,32,40
R 52,5120
mav 4,36,40
mav 8,40,40
buffer_load_dwordx4 v[152:155], v88, s[36:39], s49 offen offset:1024 sc1
mav 12,44,40
mav 0,48,44
R 56,6144
mav 4,52,44
mav 8,56,44
buffer_load_dwordx4 v[156:159], v89, s[36:39], s49 offen sc1
mav 12,60,44
s_waitcnt lgkmcnt(1)
mav 0,64,48
R 60,7168
mav 4,68,48
mav 8,72,48
buffer_load_dwordx4 v[160:163], v89, s[36:39], s49 offen offset:1024 sc1
mav 12,76,48
mav 0,80,52
R 32,8192
mav 4,84,52
mav 8,88,52
s_add_u32 m0, s50, 0
mav 12,92,52
s_waitcnt lgkmcnt(1)
mav 0,96,56
R 36,9216
mav 4,100,56
mav 8,104,56
D 83,0
mav 12,108,56
mav 0,112,60
R 40,10240
mav 4,116,60
mav 8,120,60
D 83,1024
mav 12,124,60
s_waitcnt lgkmcnt(1)
mav 0,128,32
R 44,11264
mav 4,132,32
mav 8,136,32
mav 12,140,32
mav 0,144,36
R 48,12288
mav 4,148,36
mav 8,152,36
mav 12,156,36
s_waitcnt lgkmcnt(1)
mav 0,160,40
R 52,13312
mav 4,164,40
mav 8,168,40
mav 12,172,40
mav 0,176,44
R 56,14336
mav 4,180,44
mav 8,184,44
D 83,2048
mav 12,188,44
s_waitcnt lgkmcnt(1)
mav 0,192,48
R 60,15360
mav 4,196,48
mav 8,200,48
D 83,3072
mav 12,204,48
mav 0,208,52
mav 4,212,52
mav 8,216,52
mav 12,220,52
s_waitcnt lgkmcnt(0)
mav 0,224,56
mav 4,228,56
mav 8,232,56
mav 12,236,56
mav 0,240,60
mav 4,244,60
mav 8,248,60
buffer_load_dwordx4 v[164:167], v90, s[36:39], s49 offen sc1
mav 12,252,60
s_waitcnt vmcnt(14)
mvv 0,116,124
mvv 4,120,124
s_waitcnt vmcnt(13)
mvv 8,116,128
buffer_load_dwordx4 v[168:171], v90, s[36:39], s49 offen offset:1024 sc1
mvv 12,120,128
s_waitcnt vmcnt(13)
mvv 0,116,132
mvv 4,120,132
s_waitcnt vmcnt(12)
mvv 8,116,136
s_waitcnt vmcnt(11)
s_add_u32 s49, s49, 313344
mvv 12,120,136
s_barrier
mvv 0,116,140
R 32,32768
mvv 4,120,140
s_waitcnt vmcnt(10)
mvv 8,116,144
buffer_load_dwordx4 v[172:175], v86, s[36:39], s49 offen sc1
mvv 12,120,144
s_waitcnt vmcnt(10)
mvv 0,116,148
R 36,33792
mvv 4,120,148
s_waitcnt vmcnt(9)
mvv 8,116,152
buffer_load_dwordx4 v[176:179], v86, s[36:39], s49 offen offset:1024 sc1
mvv 12,120,152
s_waitcnt vmcnt(9)
mvv 0,116,156
R 40,34816
mvv 4,120,156
s_waitcnt vmcnt(8)
mvv 8,116,160
buffer_load_dwordx4 v[180:183], v87, s[36:39], s49 offen sc1
mvv 12,120,160
s_waitcnt lgkmcnt(1)
v_mfma_f32_16x16x32_bf16 v[16:19], a[0:3], v[32:35], v[100:103]
R 44,35840
v_cvt_pk_bf16_f32 v64, v0, v1
v_mfma_f32_16x16x32_bf16 v[20:23], a[4:7], v[32:35], v[104:107]
v_cvt_pk_bf16_f32 v65, v2, v3
v_cvt_pk_bf16_f32 v66, v4, v5
v_mfma_f32_16x16x32_bf16 v[24:27], a[8:11], v[32:35], v[108:111]
buffer_load_dwordx4 v[184:187], v87, s[36:39], s49 offen offset:1024 sc1
v_mfma_f32_16x16x32_bf16 v[28:31], a[12:15], v[32:35], v[112:115]
v_cvt_pk_bf16_f32 v67, v6, v7
v_cvt_pk_bf16_f32 v68, v8, v9
mav 16,16,36
R 48,36864
v_cvt_pk_bf16_f32 v69, v10, v11
mav 20,20,36
v_cvt_pk_bf16_f32 v70, v12, v13
v_cvt_pk_bf16_f32 v71, v14, v15
mav 24,24,36
buffer_load_dwordx4 v[188:191], v88, s[36:39], s49 offen sc1
mav 28,28,36
s_waitcnt lgkmcnt(1)
v_mul_f32 v75, v0, v0
v_add_f32 v74, v1, v0
mav 16,32,40
R 52,37888
v_fmac_f32 v75, v1, v1
mav 20,36,40
v_add_f32 v74, v2, v74
v_fmac_f32 v75, v2, v2
mav 24,40,40
buffer_load_dwordx4 v[192:195], v88, s[36:39], s49 offen offset:1024 sc1
mav 28,44,40
v_add_f32 v74, v3, v74
v_fmac_f32 v75, v3, v3
mav 16,48,44
R 56,38912
v_add_f32 v74, v4, v74
mav 20,52,44
v_fmac_f32 v75, v4, v4
v_add_f32 v74, v5, v74
mav 24,56,44
buffer_load_dwordx4 v[196:199], v89, s[36:39], s49 offen sc1
mav 28,60,44
s_waitcnt lgkmcnt(1)
v_fmac_f32 v75, v5, v5
v_add_f32 v74, v6, v74
mav 16,64,48
R 60,39936
v_fmac_f32 v75, v6, v6
mav 20,68,48
v_add_f32 v74, v7, v74
v_fmac_f32 v75, v7, v7
mav 24,72,48
buffer_load_dwordx4 v[200:203], v89, s[36:39], s49 offen offset:1024 sc1
mav 28,76,48
v_add_f32 v74, v8, v74
v_fmac_f32 v75, v8, v8
mav 16,80,52
R 32,40960
v_add_f32 v74, v9, v74
mav 20,84,52
v_fmac_f32 v75, v9, v9
v_add_f32 v74, v10, v74
mav 24,88,52
s_add_u32 m0, s50, 32768
v_fmac_f32 v75, v10, v10
v_add_f32 v74, v11, v74
mav 28,92,52
s_waitcnt lgkmcnt(1)
v_fmac_f32 v75, v11, v11
v_add_f32 v74, v12, v74
mav 16,96,56
R 36,41984
v_fmac_f32 v75, v12, v12
mav 20,100,56
v_add_f32 v74, v13, v74
v_fmac_f32 v75, v13, v13
mav 24,104,56
D 83,0
mav 28,108,56
ds_write_b128 v94, v[64:67] offset:0
mav 16,112,60
R 40,43008
v_add_f32 v74, v14, v74
mav 20,116,60
ds_write_b128 v95, v[68:71] offset:0
mav 24,120,60
D 83,1024
mav 28,124,60
s_waitcnt lgkmcnt(3)
ds_read_b128 v[64:67], v96 offset:0
v_fmac_f32 v75, v14, v14
mav 16,128,32
R 44,44032
v_add_f32 v74, v15, v74
mav 20,132,32
ds_read_b128 v[68:71], v96 offset:1024
v_fmac_f32 v75, v15, v15
mav 24,136,32
mav 28,140,32
v_permlane32_swap_b32 v74, v75
mav 16,144,36
R 48,45056
v_add_f32 v74, v74, v75
mav 20,148,36
mav 24,152,36
mav 28,156,36
s_waitcnt lgkmcnt(1)
mav 16,160,40
mav 20,164,40
R 52,46080
mav 24,168,40
mav 28,172,40
mav 16,176,44
R 56,47104
mav 20,180,44
mav 24,184,44
D 83,2048
mav 28,188,44
s_waitcnt lgkmcnt(1)
mav 16,192,48
R 60,48128
mav 20,196,48
mav 24,200,48
D 83,3072
mav 28,204,48
mav 16,208,52
mav 20,212,52
mav 24,216,52
buffer_store_dwordx4 v[64:67], v91, s[36:39], s54 offen sc1
mav 28,220,52
s_waitcnt lgkmcnt(0)
mav 16,224,56
mav 20,228,56
mav 24,232,56
buffer_store_dwordx4 v[68:71], v92, s[36:39], s54 offen sc1
mav 28,236,56
mav 16,240,60
mav 20,244,60
mav 24,248,60
buffer_load_dwordx4 v[124:127], v90, s[36:39], s49 offen sc1
mav 28,252,60
s_waitcnt vmcnt(16)
mvv 16,116,164
mvv 20,120,164
s_waitcnt vmcnt(15)
mvv 24,116,168
buffer_load_dwordx4 v[128:131], v90, s[36:39], s49 offen offset:1024 sc1
mvv 28,120,168
s_waitcnt vmcnt(15)
s_xor_b32 s50, s50, 0x10000
s_sub_u32 s54, s54, 32768
mvv 16,116,172
mvv 20,120,172
s_waitcnt vmcnt(14)
v_xor_b32 v80, 0x10000, v80
mvv 24,116,176
s_waitcnt vmcnt(13)
s_add_u32 s49, s49, 313344
mvv 28,120,176
s_barrier
mvv 16,116,180
R 32,0
mvv 20,120,180
s_waitcnt vmcnt(12)
mvv 24,116,184
buffer_load_dwordx4 v[132:135], v86, s[36:39], s49 offen sc1
mvv 28,120,184
s_waitcnt vmcnt(12)
mvv 16,116,188
R 36,1024
mvv 20,120,188
s_waitcnt vmcnt(11)
mvv 24,116,192
buffer_load_dwordx4 v[136:139], v86, s[36:39], s49 offen offset:1024 sc1
mvv 28,120,192
s_waitcnt vmcnt(11)
mvv 16,116,196
R 40,2048
mvv 20,120,196
s_waitcnt vmcnt(10)
mvv 24,116,200
buffer_load_dwordx4 v[140:143], v87, s[36:39], s49 offen sc1
mvv 28,120,200
s_waitcnt lgkmcnt(1)
v_mfma_f32_16x16x32_bf16 v[0:3], a[0:3], v[32:35], v[100:103]
R 44,3072
v_cvt_pk_bf16_f32 v64, v16, v17
v_mfma_f32_16x16x32_bf16 v[4:7], a[4:7], v[32:35], v[104:107]
v_cvt_pk_bf16_f32 v65, v18, v19
v_cvt_pk_bf16_f32 v66, v20, v21
v_mfma_f32_16x16x32_bf16 v[8:11], a[8:11], v[32:35], v[108:111]
buffer_load_dwordx4 v[144:147], v87, s[36:39], s49 offen offset:1024 sc1
v_mfma_f32_16x16x32_bf16 v[12:15], a[12:15], v[32:35], v[112:115]
v_cvt_pk_bf16_f32 v67, v22, v23
v_cvt_pk_bf16_f32 v68, v24, v25
mav 0,16,36
R 48,4096
v_cvt_pk_bf16_f32 v69, v26, v27
mav 4,20,36
v_cvt_pk_bf16_f32 v70, v28, v29
v_cvt_pk_bf16_f32 v71, v30, v31
mav 8,24,36
buffer_load_dwordx4 v[148:151], v88, s[36:39], s49 offen sc1
mav 12,28,36
s_waitcnt lgkmcnt(1)
v_mul_f32 v75, v16, v16
v_fmac_f32 v75, v17, v17
mav 0,32,40
R 52,5120
v_fmac_f32 v75, v18, v18
mav 4,36,40
v_fmac_f32 v75, v19, v19
v_fmac_f32 v75, v20, v20
mav 8,40,40
buffer_load_dwordx4 v[152:155], v88, s[36:39], s49 offen offset:1024 sc1
mav 12,44,40
v_fmac_f32 v75, v21, v21
v_fmac_f32 v75, v22, v22
mav 0,48,44
R 56,6144
v_fmac_f32 v75, v23, v23
mav 4,52,44
v_fmac_f32 v75, v24, v24
v_fmac_f32 v75, v25, v25
mav 8,56,44
buffer_load_dwordx4 v[156:159], v89, s[36:39], s49 offen sc1
mav 12,60,44
s_waitcnt lgkmcnt(1)
v_fmac_f32 v75, v26, v26
v_fmac_f32 v75, v27, v27
mav 0,64,48
R 60,7168
v_fmac_f32 v75, v28, v28
mav 4,68,48
v_fmac_f32 v75, v29, v29
v_fmac_f32 v75, v30, v30
mav 8,72,48
buffer_load_dwordx4 v[160:163], v89, s[36:39], s49 offen offset:1024 sc1
mav 12,76,48
v_fmac_f32 v75, v31, v31
mav 0,80,52
R 32,8192
mav 4,84,52
mav 8,88,52
s_add_u32 m0, s50, 0
mav 12,92,52
s_waitcnt lgkmcnt(1)
mav 0,96,56
R 36,9216
mav 4,100,56
mav 8,104,56
D 83,0
mav 12,108,56
ds_write_b32 v97, v74 offset:0
mav 0,112,60
R 40,10240
v_add_f32 v74, v17, v16
mav 4,116,60
ds_write_b128 v94, v[64:67] offset:0
mav 8,120,60
D 83,1024
mav 12,124,60
s_waitcnt lgkmcnt(3)
ds_write_b128 v95, v[68:71] offset:0
mav 0,128,32
R 44,11264
v_add_f32 v74, v18, v74
mav 4,132,32
ds_read_b128 v[64:67], v96 offset:0
v_add_f32 v74, v19, v74
mav 8,136,32
ds_read_b128 v[68:71], v96 offset:1024
v_add_f32 v74, v20, v74
mav 12,140,32
v_add_f32 v74, v21, v74
v_add_f32 v74, v22, v74
mav 0,144,36
R 48,12288
v_add_f32 v74, v23, v74
mav 4,148,36
v_add_f32 v74, v24, v74
v_add_f32 v74, v25, v74
mav 8,152,36
v_add_f32 v74, v26, v74
v_add_f32 v74, v27, v74
mav 12,156,36
s_waitcnt lgkmcnt(2)
v_add_f32 v74, v28, v74
v_add_f32 v74, v29, v74
mav 0,160,40
R 52,13312
v_add_f32 v74, v30, v74
mav 4,164,40
v_add_f32 v74, v31, v74
v_add_u32 v72, 1, v72
mav 8,168,40
v_permlane32_swap_b32 v74, v75
mav 12,172,40
v_add_f32 v74, v74, v75
mav 0,176,44
R 56,14336
mav 4,180,44
mav 8,184,44
D 83,2048
mav 12,188,44
s_waitcnt lgkmcnt(1)
mav 0,192,48
R 60,15360
mav 4,196,48
mav 8,200,48
D 83,3072
mav 12,204,48
mav 0,208,52
mav 4,212,52
mav 8,216,52
buffer_store_dwordx4 v[64:67], v91, s[36:39], s54 offen sc1
mav 12,220,52
s_waitcnt lgkmcnt(0)
mav 0,224,56
mav 4,228,56
mav 8,232,56
buffer_store_dwordx4 v[68:71], v92, s[36:39], s54 offen sc1
mav 12,236,56
mav 0,240,60
mav 4,244,60
mav 8,248,60
buffer_load_dwordx4 v[164:167], v90, s[36:39], s49 offen sc1
mav 12,252,60
s_waitcnt vmcnt(16)
mvv 0,116,124
buffer_store_dword v72, v81, s[36:39], s52 offen sc1
mvv 4,120,124
s_waitcnt vmcnt(16)
mvv 8,116,128
buffer_load_dwordx4 v[168:171], v90, s[36:39], s49 offen offset:1024 sc1
mvv 12,120,128
s_waitcnt vmcnt(16)
s_sub_u32 s54, s54, 32768
mvv 0,116,132
mvv 4,120,132
s_waitcnt vmcnt(15)
mvv 8,116,136
s_waitcnt vmcnt(14)
s_add_u32 s49, s49, 313344
mvv 12,120,136
s_barrier
mvv 0,116,140
R 32,32768
mvv 4,120,140
s_waitcnt vmcnt(13)
mvv 8,116,144
buffer_load_dwordx4 v[172:175], v86, s[36:39], s49 offen sc1
mvv 12,120,144
s_waitcnt vmcnt(13)
mvv 0,116,148
R 36,33792
mvv 4,120,148
s_waitcnt vmcnt(12)
mvv 8,116,152
buffer_load_dwordx4 v[176:179], v86, s[36:39], s49 offen offset:1024 sc1
mvv 12,120,152
s_waitcnt vmcnt(12)
mvv 0,116,156
R 40,34816
mvv 4,120,156
s_waitcnt vmcnt(11)
mvv 8,116,160
buffer_load_dwordx4 v[180:183], v87, s[36:39], s49 offen sc1
mvv 12,120,160
s_waitcnt lgkmcnt(1)
v_mfma_f32_16x16x32_bf16 v[16:19], a[0:3], v[32:35], v[100:103]
R 44,35840
v_cvt_pk_bf16_f32 v64, v0, v1
v_mfma_f32_16x16x32_bf16 v[20:23], a[4:7], v[32:35], v[104:107]
v_cvt_pk_bf16_f32 v65, v2, v3
v_cvt_pk_bf16_f32 v66, v4, v5
v_mfma_f32_16x16x32_bf16 v[24:27], a[8:11], v[32:35], v[108:111]
buffer_load_dwordx4 v[184:187], v87, s[36:39], s49 offen offset:1024 sc1
v_mfma_f32_16x16x32_bf16 v[28:31], a[12:15], v[32:35], v[112:115]
v_cvt_pk_bf16_f32 v67, v6, v7
v_cvt_pk_bf16_f32 v68, v8, v9
mav 16,16,36
R 48,36864
v_cvt_pk_bf16_f32 v69, v10, v11
mav 20,20,36
v_cvt_pk_bf16_f32 v70, v12, v13
v_cvt_pk_bf16_f32 v71, v14, v15
mav 24,24,36
buffer_load_dwordx4 v[188:191], v88, s[36:39], s49 offen sc1
mav 28,28,36
s_waitcnt lgkmcnt(1)
v_mul_f32 v75, v0, v0
v_fmac_f32 v75, v1, v1
mav 16,32,40
R 52,37888
v_fmac_f32 v75, v2, v2
mav 20,36,40
v_fmac_f32 v75, v3, v3
v_fmac_f32 v75, v4, v4
mav 24,40,40
buffer_load_dwordx4 v[192:195], v88, s[36:39], s49 offen offset:1024 sc1
mav 28,44,40
v_fmac_f32 v75, v5, v5
v_fmac_f32 v75, v6, v6
mav 16,48,44
R 56,38912
v_fmac_f32 v75, v7, v7
mav 20,52,44
v_fmac_f32 v75, v8, v8
v_fmac_f32 v75, v9, v9
mav 24,56,44
buffer_load_dwordx4 v[196:199], v89, s[36:39], s49 offen sc1
mav 28,60,44
s_waitcnt lgkmcnt(1)
v_fmac_f32 v75, v10, v10
v_fmac_f32 v75, v11, v11
mav 16,64,48
R 60,39936
v_fmac_f32 v75, v12, v12
mav 20,68,48
v_fmac_f32 v75, v13, v13
v_fmac_f32 v75, v14, v14
mav 24,72,48
buffer_load_dwordx4 v[200:203], v89, s[36:39], s49 offen offset:1024 sc1
LsR13:
s_cbranch_execz LsR17
mav 28,76,48
v_fmac_f32 v75, v15, v15
mav 16,80,52
R 32,40960
mav 20,84,52
mav 24,88,52
s_add_u32 m0, s50, 32768
mav 28,92,52
s_waitcnt lgkmcnt(1)
mav 16,96,56
R 36,41984
mav 20,100,56
mav 24,104,56
D 83,0
mav 28,108,56
ds_write_b32 v97, v74 offset:1024
mav 16,112,60
R 40,43008
v_add_f32 v74, v1, v0
mav 20,116,60
ds_write_b128 v94, v[64:67] offset:0
mav 24,120,60
D 83,1024
mav 28,124,60
s_waitcnt lgkmcnt(3)
ds_write_b128 v95, v[68:71] offset:0
mav 16,128,32
R 44,44032
v_add_f32 v74, v2, v74
mav 20,132,32
ds_read_b128 v[64:67], v96 offset:0
v_add_f32 v74, v3, v74
mav 24,136,32
ds_read_b128 v[68:71], v96 offset:1024
v_add_f32 v74, v4, v74
mav 28,140,32
v_add_f32 v74, v5, v74
v_add_f32 v74, v6, v74
mav 16,144,36
R 48,45056
v_add_f32 v74, v7, v74
mav 20,148,36
v_add_f32 v74, v8, v74
v_add_f32 v74, v9, v74
mav 24,152,36
v_add_f32 v74, v10, v74
v_add_f32 v74, v11, v74
mav 28,156,36
s_waitcnt lgkmcnt(2)
v_add_f32 v74, v12, v74
mav 16,160,40
s_mov_b32 s60, -14
.p2align 6
LR2_1:
mav 20,164,40
R 52,46080
v_add_f32 v74, v13, v74
mav 24,168,40
v_add_f32 v74, v14, v74
v_add_f32 v74, v15, v74
mav 28,172,40
v_add_u32 v72, 1, v72
mav 16,176,44
R 56,47104
mav 20,180,44
v_permlane32_swap_b32 v74, v75
mav 24,184,44
D 83,2048
mav 28,188,44
s_waitcnt lgkmcnt(1)
v_add_f32 v74, v74, v75
mav 16,192,48
R 60,48128
mav 20,196,48
mav 24,200,48
D 83,3072
mav 28,204,48
mav 16,208,52
mav 20,212,52
mav 24,216,52
buffer_store_dwordx4 v[64:67], v91, s[36:39], s54 offen sc1
mav 28,220,52
s_waitcnt lgkmcnt(0)
mav 16,224,56
mav 20,228,56
mav 24,232,56
buffer_store_dwordx4 v[68:71], v92, s[36:39], s54 offen sc1
mav 28,236,56
mav 16,240,60
mav 20,244,60
mav 24,248,60
buffer_load_dwordx4 v[124:127], v90, s[36:39], s49 offen sc1
mav 28,252,60
s_waitcnt vmcnt(17)
mvv 16,116,164
buffer_store_dword v72, v81, s[36:39], s52 offen sc1
mvv 20,120,164
s_waitcnt vmcnt(16)
mvv 24,116,168
buffer_load_dwordx4 v[128:131], v90, s[36:39], s49 offen offset:1024 sc1
mvv 28,120,168
s_waitcnt vmcnt(16)
s_xor_b32 s50, s50, 0x10000
s_sub_u32 s54, s54, 32768
mvv 16,116,172
mvv 20,120,172
s_waitcnt vmcnt(15)
v_xor_b32 v80, 0x10000, v80
mvv 24,116,176
s_waitcnt vmcnt(14)
s_add_u32 s49, s49, 313344
mvv 28,120,176
s_barrier
mvv 16,116,180
R 32,0
mvv 20,120,180
s_waitcnt vmcnt(13)
mvv 24,116,184
buffer_load_dwordx4 v[132:135], v86, s[36:39], s49 offen sc1
mvv 28,120,184
s_waitcnt vmcnt(13)
mvv 16,116,188
R 36,1024
mvv 20,120,188
s_waitcnt vmcnt(12)
mvv 24,116,192
buffer_load_dwordx4 v[136:139], v86, s[36:39], s49 offen offset:1024 sc1
mvv 28,120,192
s_waitcnt vmcnt(12)
mvv 16,116,196
R 40,2048
mvv 20,120,196
s_waitcnt vmcnt(11)
mvv 24,116,200
buffer_load_dwordx4 v[140:143], v87, s[36:39], s49 offen sc1
mvv 28,120,200
s_waitcnt lgkmcnt(1)
v_mfma_f32_16x16x32_bf16 v[0:3], a[0:3], v[32:35], v[100:103]
R 44,3072
v_cvt_pk_bf16_f32 v64, v16, v17
v_mfma_f32_16x16x32_bf16 v[4:7], a[4:7], v[32:35], v[104:107]
v_cvt_pk_bf16_f32 v65, v18, v19
v_cvt_pk_bf16_f32 v66, v20, v21
v_mfma_f32_16x16x32_bf16 v[8:11], a[8:11], v[32:35], v[108:111]
buffer_load_dwordx4 v[144:147], v87, s[36:39], s49 offen offset:1024 sc1
v_mfma_f32_16x16x32_bf16 v[12:15], a[12:15], v[32:35], v[112:115]
v_cvt_pk_bf16_f32 v67, v22, v23
v_cvt_pk_bf16_f32 v68, v24, v25
mav 0,16,36
R 48,4096
v_cvt_pk_bf16_f32 v69, v26, v27
mav 4,20,36
v_cvt_pk_bf16_f32 v70, v28, v29
v_cvt_pk_bf16_f32 v71, v30, v31
mav 8,24,36
buffer_load_dwordx4 v[148:151], v88, s[36:39], s49 offen sc1
mav 12,28,36
s_waitcnt lgkmcnt(1)
v_mul_f32 v75, v16, v16
v_fmac_f32 v75, v17, v17
mav 0,32,40
R 52,5120
v_fmac_f32 v75, v18, v18
mav 4,36,40
v_fmac_f32 v75, v19, v19
v_fmac_f32 v75, v20, v20
mav 8,40,40
buffer_load_dwordx4 v[152:155], v88, s[36:39], s49 offen offset:1024 sc1
mav 12,44,40
v_fmac_f32 v75, v21, v21
v_fmac_f32 v75, v22, v22
mav 0,48,44
R 56,6144
v_fmac_f32 v75, v23, v23
mav 4,52,44
v_fmac_f32 v75, v24, v24
v_fmac_f32 v75, v25, v25
mav 8,56,44
buffer_load_dwordx4 v[156:159], v89, s[36:39], s49 offen sc1
mav 12,60,44
s_waitcnt lgkmcnt(1)
v_fmac_f32 v75, v26, v26
v_fmac_f32 v75, v27, v27
mav 0,64,48
R 60,7168
v_fmac_f32 v75, v28, v28
mav 4,68,48
v_fmac_f32 v75, v29, v29
v_fmac_f32 v75, v30, v30
mav 8,72,48
buffer_load_dwordx4 v[160:163], v89, s[36:39], s49 offen offset:1024 sc1
mav 12,76,48
v_fmac_f32 v75, v31, v31
mav 0,80,52
R 32,8192
mav 4,84,52
mav 8,88,52
s_add_u32 m0, s50, 0
mav 12,92,52
s_waitcnt lgkmcnt(1)
mav 0,96,56
R 36,9216
mav 4,100,56
mav 8,104,56
D 83,0
mav 12,108,56
ds_write_b32 v97, v74 offset:0
mav 0,112,60
R 40,10240
v_add_f32 v74, v17, v16
mav 4,116,60
ds_write_b128 v94, v[64:67] offset:0
mav 8,120,60
D 83,1024
mav 12,124,60
s_waitcnt lgkmcnt(3)
ds_write_b128 v95, v[68:71] offset:0
mav 0,128,32
R 44,11264
v_add_f32 v74, v18, v74
mav 4,132,32
ds_read_b128 v[64:67], v96 offset:0
v_add_f32 v74, v19, v74
mav 8,136,32
ds_read_b128 v[68:71], v96 offset:1024
v_add_f32 v74, v20, v74
mav 12,140,32
v_add_f32 v74, v21, v74
v_add_f32 v74, v22, v74
mav 0,144,36
R 48,12288
v_add_f32 v74, v23, v74
mav 4,148,36
v_add_f32 v74, v24, v74
v_add_f32 v74, v25, v74
mav 8,152,36
v_add_f32 v74, v26, v74
v_add_f32 v74, v27, v74
mav 12,156,36
s_waitcnt lgkmcnt(2)
v_add_f32 v74, v28, v74
v_add_f32 v74, v29, v74
mav 0,160,40
R 52,13312
v_add_f32 v74, v30, v74
mav 4,164,40
v_add_f32 v74, v31, v74
v_add_u32 v72, 1, v72
mav 8,168,40
v_permlane32_swap_b32 v74, v75
mav 12,172,40
v_add_f32 v74, v74, v75
mav 0,176,44
R 56,14336
mav 4,180,44
mav 8,184,44
D 83,2048
mav 12,188,44
s_waitcnt lgkmcnt(1)
mav 0,192,48
R 60,15360
mav 4,196,48
mav 8,200,48
D 83,3072
mav 12,204,48
mav 0,208,52
mav 4,212,52
mav 8,216,52
buffer_store_dwordx4 v[64:67], v91, s[36:39], s54 offen sc1
mav 12,220,52
s_waitcnt lgkmcnt(0)
mav 0,224,56
mav 4,228,56
mav 8,232,56
buffer_store_dwordx4 v[68:71], v92, s[36:39], s54 offen sc1
mav 12,236,56
mav 0,240,60
mav 4,244,60
mav 8,248,60
buffer_load_dwordx4 v[164:167], v90, s[36:39], s49 offen sc1
mav 12,252,60
s_waitcnt vmcnt(17)
mvv 0,116,124
buffer_store_dword v72, v81, s[36:39], s52 offen sc1
mvv 4,120,124
s_waitcnt vmcnt(16)
mvv 8,116,128
buffer_load_dwordx4 v[168:171], v90, s[36:39], s49 offen offset:1024 sc1
mvv 12,120,128
s_waitcnt vmcnt(16)
s_sub_u32 s54, s54, 32768
mvv 0,116,132
mvv 4,120,132
s_waitcnt vmcnt(15)
mvv 8,116,136
s_waitcnt vmcnt(14)
s_add_u32 s49, s49, 313344
mvv 12,120,136
s_barrier
mvv 0,116,140
R 32,32768
mvv 4,120,140
s_waitcnt vmcnt(13)
mvv 8,116,144
buffer_load_dwordx4 v[172:175], v86, s[36:39], s49 offen sc1
mvv 12,120,144
s_waitcnt vmcnt(13)
mvv 0,116,148
R 36,33792
mvv 4,120,148
s_waitcnt vmcnt(12)
mvv 8,116,152
buffer_load_dwordx4 v[176:179], v86, s[36:39], s49 offen offset:1024 sc1
mvv 12,120,152
s_waitcnt vmcnt(12)
mvv 0,116,156
R 40,34816
mvv 4,120,156
s_waitcnt vmcnt(11)
mvv 8,116,160
buffer_load_dwordx4 v[180:183], v87, s[36:39], s49 offen sc1
mvv 12,120,160
s_waitcnt lgkmcnt(1)
v_mfma_f32_16x16x32_bf16 v[16:19], a[0:3], v[32:35], v[100:103]
R 44,35840
v_cvt_pk_bf16_f32 v64, v0, v1
v_mfma_f32_16x16x32_bf16 v[20:23], a[4:7], v[32:35], v[104:107]
v_cvt_pk_bf16_f32 v65, v2, v3
v_cvt_pk_bf16_f32 v66, v4, v5
v_mfma_f32_16x16x32_bf16 v[24:27], a[8:11], v[32:35], v[108:111]
buffer_load_dwordx4 v[184:187], v87, s[36:39], s49 offen offset:1024 sc1
v_mfma_f32_16x16x32_bf16 v[28:31], a[12:15], v[32:35], v[112:115]
v_cvt_pk_bf16_f32 v67, v6, v7
v_cvt_pk_bf16_f32 v68, v8, v9
mav 16,16,36
R 48,36864
v_cvt_pk_bf16_f32 v69, v10, v11
mav 20,20,36
v_cvt_pk_bf16_f32 v70, v12, v13
v_cvt_pk_bf16_f32 v71, v14, v15
mav 24,24,36
buffer_load_dwordx4 v[188:191], v88, s[36:39], s49 offen sc1
mav 28,28,36
s_waitcnt lgkmcnt(1)
v_mul_f32 v75, v0, v0
v_fmac_f32 v75, v1, v1
mav 16,32,40
R 52,37888
v_fmac_f32 v75, v2, v2
mav 20,36,40
v_fmac_f32 v75, v3, v3
v_fmac_f32 v75, v4, v4
mav 24,40,40
buffer_load_dwordx4 v[192:195], v88, s[36:39], s49 offen offset:1024 sc1
mav 28,44,40
v_fmac_f32 v75, v5, v5
v_fmac_f32 v75, v6, v6
mav 16,48,44
R 56,38912
v_fmac_f32 v75, v7, v7
mav 20,52,44
v_fmac_f32 v75, v8, v8
v_fmac_f32 v75, v9, v9
mav 24,56,44
buffer_load_dwordx4 v[196:199], v89, s[36:39], s49 offen sc1
mav 28,60,44
s_waitcnt lgkmcnt(1)
v_fmac_f32 v75, v10, v10
v_fmac_f32 v75, v11, v11
mav 16,64,48
R 60,39936
v_fmac_f32 v75, v12, v12
mav 20,68,48
v_fmac_f32 v75, v13, v13
v_fmac_f32 v75, v14, v14
mav 24,72,48
buffer_load_dwordx4 v[200:203], v89, s[36:39], s49 offen offset:1024 sc1
mav 28,76,48
v_fmac_f32 v75, v15, v15
mav 16,80,52
R 32,40960
mav 20,84,52
mav 24,88,52
s_add_u32 m0, s50, 32768
mav 28,92,52
s_waitcnt lgkmcnt(1)
mav 16,96,56
R 36,41984
mav 20,100,56
mav 24,104,56
D 83,0
mav 28,108,56
ds_write_b32 v97, v74 offset:1024
mav 16,112,60
R 40,43008
v_add_f32 v74, v1, v0
mav 20,116,60
ds_write_b128 v94, v[64:67] offset:0
mav 24,120,60
D 83,1024
mav 28,124,60
s_waitcnt lgkmcnt(3)
ds_write_b128 v95, v[68:71] offset:0
mav 16,128,32
R 44,44032
v_add_f32 v74, v2, v74
mav 20,132,32
ds_read_b128 v[64:67], v96 offset:0
v_add_f32 v74, v3, v74
mav 24,136,32
ds_read_b128 v[68:71], v96 offset:1024
v_add_f32 v74, v4, v74
mav 28,140,32
v_add_f32 v74, v5, v74
v_add_f32 v74, v6, v74
mav 16,144,36
R 48,45056
v_add_f32 v74, v7, v74
mav 20,148,36
v_add_f32 v74, v8, v74
v_add_f32 v74, v9, v74
mav 24,152,36
v_add_f32 v74, v10, v74
v_add_f32 v74, v11, v74
mav 28,156,36
s_waitcnt lgkmcnt(2)
s_add_u32 s60, s60, 1
v_add_f32 v74, v12, v74
mav 16,160,40
s_cbranch_scc0 LR2_1
mav 20,164,40
R 52,46080
v_add_f32 v74, v13, v74
mav 24,168,40
v_add_f32 v74, v14, v74
v_add_f32 v74, v15, v74
mav 28,172,40
v_add_u32 v72, 1, v72
mav 16,176,44
R 56,47104
mav 20,180,44
v_permlane32_swap_b32 v74, v75
mav 24,184,44
D 83,2048
mav 28,188,44
s_waitcnt lgkmcnt(1)
v_add_f32 v74, v74, v75
mav 16,192,48
R 60,48128
mav 20,196,48
mav 24,200,48
D 83,3072
mav 28,204,48
mav 16,208,52
mav 20,212,52
mav 24,216,52
buffer_store_dwordx4 v[64:67], v91, s[36:39], s54 offen sc1
mav 28,220,52
s_waitcnt lgkmcnt(0)
mav 16,224,56
mav 20,228,56
mav 24,232,56
buffer_store_dwordx4 v[68:71], v92, s[36:39], s54 offen sc1
mav 28,236,56
mav 16,240,60
mav 20,244,60
mav 24,248,60
buffer_load_dwordx4 v[124:127], v90, s[36:39], s49 offen sc1
mav 28,252,60
s_waitcnt vmcnt(17)
mvv 16,116,164
buffer_store_dword v72, v81, s[36:39], s52 offen sc1
mvv 20,120,164
s_waitcnt vmcnt(16)
mvv 24,116,168
buffer_load_dwordx4 v[128:131], v90, s[36:39], s49 offen offset:1024 sc1
mvv 28,120,168
s_waitcnt vmcnt(16)
s_xor_b32 s50, s50, 0x10000
s_sub_u32 s54, s54, 32768
mvv 16,116,172
mvv 20,120,172
s_waitcnt vmcnt(15)
v_xor_b32 v80, 0x10000, v80
mvv 24,116,176
s_waitcnt vmcnt(14)
s_add_u32 s49, s49, 313344
mvv 28,120,176
s_barrier
mvv 16,116,180
R 32,0
mvv 20,120,180
s_waitcnt vmcnt(13)
mvv 24,116,184
buffer_load_dwordx4 v[132:135], v86, s[36:39], s49 offen sc1
mvv 28,120,184
s_waitcnt vmcnt(13)
mvv 16,116,188
R 36,1024
mvv 20,120,188
s_waitcnt vmcnt(12)
mvv 24,116,192
buffer_load_dwordx4 v[136:139], v86, s[36:39], s49 offen offset:1024 sc1
mvv 28,120,192
s_waitcnt vmcnt(12)
mvv 16,116,196
R 40,2048
mvv 20,120,196
s_waitcnt vmcnt(11)
mvv 24,116,200
buffer_load_dwordx4 v[140:143], v87, s[36:39], s49 offen sc1
mvv 28,120,200
s_waitcnt lgkmcnt(1)
v_mfma_f32_16x16x32_bf16 v[0:3], a[0:3], v[32:35], v[100:103]
R 44,3072
v_cvt_pk_bf16_f32 v64, v16, v17
v_mfma_f32_16x16x32_bf16 v[4:7], a[4:7], v[32:35], v[104:107]
v_cvt_pk_bf16_f32 v65, v18, v19
v_cvt_pk_bf16_f32 v66, v20, v21
v_mfma_f32_16x16x32_bf16 v[8:11], a[8:11], v[32:35], v[108:111]
buffer_load_dwordx4 v[144:147], v87, s[36:39], s49 offen offset:1024 sc1
v_mfma_f32_16x16x32_bf16 v[12:15], a[12:15], v[32:35], v[112:115]
v_cvt_pk_bf16_f32 v67, v22, v23
v_cvt_pk_bf16_f32 v68, v24, v25
mav 0,16,36
R 48,4096
v_cvt_pk_bf16_f32 v69, v26, v27
mav 4,20,36
v_cvt_pk_bf16_f32 v70, v28, v29
v_cvt_pk_bf16_f32 v71, v30, v31
mav 8,24,36
buffer_load_dwordx4 v[148:151], v88, s[36:39], s49 offen sc1
mav 12,28,36
s_waitcnt lgkmcnt(1)
v_mul_f32 v75, v16, v16
v_fmac_f32 v75, v17, v17
mav 0,32,40
R 52,5120
v_fmac_f32 v75, v18, v18
mav 4,36,40
v_fmac_f32 v75, v19, v19
v_fmac_f32 v75, v20, v20
mav 8,40,40
buffer_load_dwordx4 v[152:155], v88, s[36:39], s49 offen offset:1024 sc1
mav 12,44,40
v_fmac_f32 v75, v21, v21
v_fmac_f32 v75, v22, v22
mav 0,48,44
R 56,6144
v_fmac_f32 v75, v23, v23
mav 4,52,44
v_fmac_f32 v75, v24, v24
v_fmac_f32 v75, v25, v25
mav 8,56,44
buffer_load_dwordx4 v[156:159], v89, s[36:39], s49 offen sc1
mav 12,60,44
s_waitcnt lgkmcnt(1)
v_fmac_f32 v75, v26, v26
v_fmac_f32 v75, v27, v27
mav 0,64,48
R 60,7168
v_fmac_f32 v75, v28, v28
mav 4,68,48
v_fmac_f32 v75, v29, v29
v_fmac_f32 v75, v30, v30
mav 8,72,48
buffer_load_dwordx4 v[160:163], v89, s[36:39], s49 offen offset:1024 sc1
mav 12,76,48
v_fmac_f32 v75, v31, v31
mav 0,80,52
R 32,8192
mav 4,84,52
mav 8,88,52
s_add_u32 m0, s50, 0
mav 12,92,52
s_waitcnt lgkmcnt(1)
mav 0,96,56
R 36,9216
mav 4,100,56
mav 8,104,56
D 83,0
mav 12,108,56
ds_write_b32 v97, v74 offset:0
mav 0,112,60
R 40,10240
v_add_f32 v74, v17, v16
mav 4,116,60
ds_write_b128 v94, v[64:67] offset:0
mav 8,120,60
D 83,1024
mav 12,124,60
s_waitcnt lgkmcnt(3)
ds_write_b128 v95, v[68:71] offset:0
mav 0,128,32
LsR14:
s_cbranch_execz LsR18
R 44,11264
v_add_f32 v74, v18, v74
mav 4,132,32
ds_read_b128 v[64:67], v96 offset:0
v_add_f32 v74, v19, v74
mav 8,136,32
ds_read_b128 v[68:71], v96 offset:1024
v_add_f32 v74, v20, v74
mav 12,140,32
v_add_f32 v74, v21, v74
v_add_f32 v74, v22, v74
mav 0,144,36
R 48,12288
v_add_f32 v74, v23, v74
mav 4,148,36
v_add_f32 v74, v24, v74
v_add_f32 v74, v25, v74
mav 8,152,36
v_add_f32 v74, v26, v74
v_add_f32 v74, v27, v74
mav 12,156,36
s_waitcnt lgkmcnt(2)
v_add_f32 v74, v28, v74
v_add_f32 v74, v29, v74
mav 0,160,40
R 52,13312
v_add_f32 v74, v30, v74
mav 4,164,40
v_add_f32 v74, v31, v74
v_add_u32 v72, 1, v72
mav 8,168,40
v_permlane32_swap_b32 v74, v75
mav 12,172,40
v_add_f32 v74, v74, v75
mav 0,176,44
R 56,14336
mav 4,180,44
mav 8,184,44
D 83,2048
mav 12,188,44
s_waitcnt lgkmcnt(1)
mav 0,192,48
R 60,15360
mav 4,196,48
mav 8,200,48
D 83,3072
mav 12,204,48
mav 0,208,52
mav 4,212,52
mav 8,216,52
buffer_store_dwordx4 v[64:67], v91, s[36:39], s54 offen sc1
mav 12,220,52
s_waitcnt lgkmcnt(0)
mav 0,224,56
mav 4,228,56
mav 8,232,56
buffer_store_dwordx4 v[68:71], v92, s[36:39], s54 offen sc1
mav 12,236,56
mav 0,240,60
mav 4,244,60
mav 8,248,60
buffer_load_dwordx4 v[164:167], v90, s[36:39], s49 offen sc1
mav 12,252,60
s_waitcnt vmcnt(17)
mvv 0,116,124
buffer_store_dword v72, v81, s[36:39], s52 offen sc1
mvv 4,120,124
s_waitcnt vmcnt(16)
mvv 8,116,128
buffer_load_dwordx4 v[168:171], v90, s[36:39], s49 offen offset:1024 sc1
mvv 12,120,128
s_waitcnt vmcnt(16)
s_sub_u32 s54, s54, 3178496
mvv 0,116,132
mvv 4,120,132
s_waitcnt vmcnt(15)
mvv 8,116,136
s_waitcnt vmcnt(14)
s_add_u32 s49, s49, 313344
mvv 12,120,136
s_barrier
mvv 0,116,140
R 32,32768
mvv 4,120,140
s_waitcnt vmcnt(13)
mvv 8,116,144
buffer_load_dwordx4 v[172:175], v86, s[36:39], s49 offen sc1
mvv 12,120,144
s_waitcnt vmcnt(13)
mvv 0,116,148
R 36,33792
mvv 4,120,148
s_waitcnt vmcnt(12)
mvv 8,116,152
buffer_load_dwordx4 v[176:179], v86, s[36:39], s49 offen offset:1024 sc1
mvv 12,120,152
s_waitcnt vmcnt(12)
mvv 0,116,156
R 40,34816
mvv 4,120,156
s_waitcnt vmcnt(11)
mvv 8,116,160
buffer_load_dwordx4 v[180:183], v87, s[36:39], s49 offen sc1
mvv 12,120,160
s_waitcnt lgkmcnt(1)
v_mfma_f32_16x16x32_bf16 v[16:19], a[0:3], v[32:35], v[100:103]
R 44,35840
v_cvt_pk_bf16_f32 v64, v0, v1
v_mfma_f32_16x16x32_bf16 v[20:23], a[4:7], v[32:35], v[104:107]
v_cvt_pk_bf16_f32 v65, v2, v3
v_cvt_pk_bf16_f32 v66, v4, v5
v_mfma_f32_16x16x32_bf16 v[24:27], a[8:11], v[32:35], v[108:111]
buffer_load_dwordx4 v[184:187], v87, s[36:39], s49 offen offset:1024 sc1
v_mfma_f32_16x16x32_bf16 v[28:31], a[12:15], v[32:35], v[112:115]
v_cvt_pk_bf16_f32 v67, v6, v7
v_cvt_pk_bf16_f32 v68, v8, v9
mav 16,16,36
R 48,36864
v_cvt_pk_bf16_f32 v69, v10, v11
mav 20,20,36
v_cvt_pk_bf16_f32 v70, v12, v13
v_cvt_pk_bf16_f32 v71, v14, v15
mav 24,24,36
buffer_load_dwordx4 v[188:191], v88, s[36:39], s49 offen sc1
mav 28,28,36
s_waitcnt lgkmcnt(1)
mav 16,32,40
R 52,37888
mav 20,36,40
mav 24,40,40
buffer_load_dwordx4 v[192:195], v88, s[36:39], s49 offen offset:1024 sc1
mav 28,44,40
mav 16,48,44
R 56,38912
mav 20,52,44
mav 24,56,44
buffer_load_dwordx4 v[196:199], v89, s[36:39], s49 offen sc1
mav 28,60,44
s_waitcnt lgkmcnt(1)
mav 16,64,48
R 60,39936
mav 20,68,48
mav 24,72,48
buffer_load_dwordx4 v[200:203], v89, s[36:39], s49 offen offset:1024 sc1
mav 28,76,48
mav 16,80,52
R 32,40960
mav 20,84,52
mav 24,88,52
s_add_u32 m0, s50, 32768
mav 28,92,52
s_waitcnt lgkmcnt(1)
mav 16,96,56
R 36,41984
mav 20,100,56
mav 24,104,56
D 83,0
mav 28,108,56
ds_write_b32 v97, v74 offset:1024
mav 16,112,60
R 40,43008
mav 20,116,60
ds_write_b128 v94, v[64:67] offset:0
mav 24,120,60
D 83,1024
mav 28,124,60
s_waitcnt lgkmcnt(3)
ds_write_b128 v95, v[68:71] offset:0
mav 16,128,32
R 44,44032
mav 20,132,32
ds_read_b128 v[64:67], v96 offset:0
mav 24,136,32
ds_read_b128 v[68:71], v96 offset:1024
mav 28,140,32
mav 16,144,36
R 48,45056
mav 20,148,36
mav 24,152,36
mav 28,156,36
s_waitcnt lgkmcnt(2)
mav 16,160,40
mav 20,164,40
R 52,46080
v_add_u32 v72, 69, v72
mav 24,168,40
mav 28,172,40
mav 16,176,44
R 56,47104
mav 20,180,44
mav 24,184,44
D 83,2048
mav 28,188,44
s_waitcnt lgkmcnt(1)
mav 16,192,48
R 60,48128
mav 20,196,48
mav 24,200,48
D 83,3072
mav 28,204,48
mav 16,208,52
mav 20,212,52
mav 24,216,52
buffer_store_dwordx4 v[64:67], v91, s[44:47], s54 offen sc1
mav 28,220,52
s_waitcnt lgkmcnt(0)
mav 16,224,56
mav 20,228,56
mav 24,232,56
buffer_store_dwordx4 v[68:71], v92, s[44:47], s54 offen sc1
mav 28,236,56
mav 16,240,60
mav 20,244,60
mav 24,248,60
buffer_load_dwordx4 v[124:127], v90, s[36:39], s49 offen sc1
mav 28,252,60
s_waitcnt vmcnt(17)
mvv 16,116,164
buffer_store_dword v72, v81, s[36:39], s52 offen sc1
mvv 20,120,164
s_waitcnt vmcnt(16)
mvv 24,116,168
buffer_load_dwordx4 v[128:131], v90, s[36:39], s49 offen offset:1024 sc1
mvv 28,120,168
s_waitcnt vmcnt(16)
s_xor_b32 s50, s50, 0x10000
s_sub_u32 s54, s54, 32768
mvv 16,116,172
mvv 20,120,172
s_waitcnt vmcnt(15)
v_xor_b32 v80, 0x10000, v80
mvv 24,116,176
s_waitcnt vmcnt(14)
s_add_u32 s49, s49, 313344
mvv 28,120,176
s_barrier
mvv 16,116,180
R 32,0
mvv 20,120,180
s_waitcnt vmcnt(13)
mvv 24,116,184
buffer_load_dwordx4 v[132:135], v86, s[36:39], s49 offen sc1
mvv 28,120,184
s_waitcnt vmcnt(13)
mvv 16,116,188
R 36,1024
mvv 20,120,188
s_waitcnt vmcnt(12)
mvv 24,116,192
buffer_load_dwordx4 v[136:139], v86, s[36:39], s49 offen offset:1024 sc1
mvv 28,120,192
s_waitcnt vmcnt(12)
mvv 16,116,196
R 40,2048
mvv 20,120,196
s_waitcnt vmcnt(11)
mvv 24,116,200
buffer_load_dwordx4 v[140:143], v87, s[36:39], s49 offen sc1
mvv 28,120,200
s_waitcnt lgkmcnt(1)
v_mfma_f32_16x16x32_bf16 v[0:3], a[0:3], v[32:35], v[100:103]
R 44,3072
v_cvt_pk_bf16_f32 v64, v16, v17
v_mfma_f32_16x16x32_bf16 v[4:7], a[4:7], v[32:35], v[104:107]
v_cvt_pk_bf16_f32 v65, v18, v19
v_cvt_pk_bf16_f32 v66, v20, v21
v_mfma_f32_16x16x32_bf16 v[8:11], a[8:11], v[32:35], v[108:111]
buffer_load_dwordx4 v[144:147], v87, s[36:39], s49 offen offset:1024 sc1
v_mfma_f32_16x16x32_bf16 v[12:15], a[12:15], v[32:35], v[112:115]
v_cvt_pk_bf16_f32 v67, v22, v23
v_cvt_pk_bf16_f32 v68, v24, v25
mav 0,16,36
R 48,4096
v_cvt_pk_bf16_f32 v69, v26, v27
mav 4,20,36
v_cvt_pk_bf16_f32 v70, v28, v29
v_cvt_pk_bf16_f32 v71, v30, v31
mav 8,24,36
buffer_load_dwordx4 v[148:151], v88, s[36:39], s49 offen sc1
mav 12,28,36
s_waitcnt lgkmcnt(1)
mav 0,32,40
R 52,5120
mav 4,36,40
mav 8,40,40
buffer_load_dwordx4 v[152:155], v88, s[36:39], s49 offen offset:1024 sc1
mav 12,44,40
mav 0,48,44
R 56,6144
mav 4,52,44
mav 8,56,44
buffer_load_dwordx4 v[156:159], v89, s[36:39], s49 offen sc1
mav 12,60,44
s_waitcnt lgkmcnt(1)
mav 0,64,48
R 60,7168
mav 4,68,48
mav 8,72,48
buffer_load_dwordx4 v[160:163], v89, s[36:39], s49 offen offset:1024 sc1
mav 12,76,48
mav 0,80,52
R 32,8192
mav 4,84,52
mav 8,88,52
s_add_u32 m0, s50, 0
mav 12,92,52
s_waitcnt lgkmcnt(1)
mav 0,96,56
R 36,9216
mav 4,100,56
mav 8,104,56
D 83,0
mav 12,108,56
ds_write_b128 v94, v[64:67] offset:0
mav 0,112,60
R 40,10240
mav 4,116,60
ds_write_b128 v95, v[68:71] offset:0
mav 8,120,60
D 83,1024
mav 12,124,60
s_waitcnt lgkmcnt(3)
ds_read_b128 v[64:67], v96 offset:0
mav 0,128,32
R 44,11264
mav 4,132,32
ds_read_b128 v[68:71], v96 offset:1024
mav 8,136,32
mav 12,140,32
mav 0,144,36
R 48,12288
mav 4,148,36
mav 8,152,36
mav 12,156,36
s_waitcnt lgkmcnt(1)
mav 0,160,40
R 52,13312
mav 4,164,40
mav 8,168,40
mav 12,172,40
mav 0,176,44
R 56,14336
mav 4,180,44
mav 8,184,44
D 83,2048
mav 12,188,44
s_waitcnt lgkmcnt(1)
mav 0,192,48
R 60,15360
mav 4,196,48
mav 8,200,48
D 83,3072
mav 12,204,48
mav 0,208,52
mav 4,212,52
mav 8,216,52
buffer_store_dwordx4 v[64:67], v91, s[44:47], s54 offen sc1
mav 12,220,52
s_waitcnt lgkmcnt(0)
mav 0,224,56
mav 4,228,56
mav 8,232,56
buffer_store_dwordx4 v[68:71], v92, s[44:47], s54 offen sc1
mav 12,236,56
mav 0,240,60
mav 4,244,60
mav 8,248,60
buffer_load_dwordx4 v[164:167], v90, s[36:39], s49 offen sc1
mav 12,252,60
s_waitcnt vmcnt(17)
mvv 0,116,124
mvv 4,120,124
s_waitcnt vmcnt(15)
mvv 8,116,128
buffer_load_dwordx4 v[168:171], v90, s[36:39], s49 offen offset:1024 sc1
mvv 12,120,128
s_waitcnt vmcnt(15)
s_sub_u32 s54, s54, 32768
mvv 0,116,132
mvv 4,120,132
s_waitcnt vmcnt(14)
mvv 8,116,136
s_waitcnt vmcnt(13)
s_add_u32 s49, s49, 313344
mvv 12,120,136
s_barrier
mvv 0,116,140
R 32,32768
mvv 4,120,140
s_waitcnt vmcnt(12)
mvv 8,116,144
buffer_load_dwordx4 v[172:175], v86, s[36:39], s49 offen sc1
mvv 12,120,144
s_waitcnt vmcnt(12)
mvv 0,116,148
R 36,33792
mvv 4,120,148
s_waitcnt vmcnt(11)
mvv 8,116,152
buffer_load_dwordx4 v[176:179], v86, s[36:39], s49 offen offset:1024 sc1
mvv 12,120,152
s_waitcnt vmcnt(11)
mvv 0,116,156
R 40,34816
mvv 4,120,156
s_waitcnt vmcnt(10)
mvv 8,116,160
buffer_load_dwordx4 v[180:183], v87, s[36:39], s49 offen sc1
mvv 12,120,160
s_waitcnt lgkmcnt(1)
v_mfma_f32_16x16x32_bf16 v[16:19], a[0:3], v[32:35], v[100:103]
R 44,35840
v_cvt_pk_bf16_f32 v64, v0, v1
v_mfma_f32_16x16x32_bf16 v[20:23], a[4:7], v[32:35], v[104:107]
v_cvt_pk_bf16_f32 v65, v2, v3
v_cvt_pk_bf16_f32 v66, v4, v5
v_mfma_f32_16x16x32_bf16 v[24:27], a[8:11], v[32:35], v[108:111]
buffer_load_dwordx4 v[184:187], v87, s[36:39], s49 offen offset:1024 sc1
v_mfma_f32_16x16x32_bf16 v[28:31], a[12:15], v[32:35], v[112:115]
v_cvt_pk_bf16_f32 v67, v6, v7
v_cvt_pk_bf16_f32 v68, v8, v9
mav 16,16,36
R 48,36864
v_cvt_pk_bf16_f32 v69, v10, v11
mav 20,20,36
v_cvt_pk_bf16_f32 v70, v12, v13
v_cvt_pk_bf16_f32 v71, v14, v15
mav 24,24,36
buffer_load_dwordx4 v[188:191], v88, s[36:39], s49 offen sc1
mav 28,28,36
s_waitcnt lgkmcnt(1)
mav 16,32,40
R 52,37888
mav 20,36,40
mav 24,40,40
buffer_load_dwordx4 v[192:195], v88, s[36:39], s49 offen offset:1024 sc1
mav 28,44,40
mav 16,48,44
R 56,38912
mav 20,52,44
mav 24,56,44
buffer_load_dwordx4 v[196:199], v89, s[36:39], s49 offen sc1
mav 28,60,44
s_waitcnt lgkmcnt(1)
mav 16,64,48
R 60,39936
mav 20,68,48
mav 24,72,48
buffer_load_dwordx4 v[200:203], v89, s[36:39], s49 offen offset:1024 sc1
mav 28,76,48
mav 16,80,52
R 32,40960
mav 20,84,52
mav 24,88,52
s_add_u32 m0, s50, 32768
mav 28,92,52
s_waitcnt lgkmcnt(1)
mav 16,96,56
R 36,41984
mav 20,100,56
mav 24,104,56
D 83,0
mav 28,108,56
ds_write_b128 v94, v[64:67] offset:0
mav 16,112,60
R 40,43008
mav 20,116,60
ds_write_b128 v95, v[68:71] offset:0
mav 24,120,60
D 83,1024
mav 28,124,60
s_waitcnt lgkmcnt(3)
ds_read_b128 v[64:67], v96 offset:0
mav 16,128,32
R 44,44032
mav 20,132,32
ds_read_b128 v[68:71], v96 offset:1024
mav 24,136,32
mav 28,140,32
mav 16,144,36
R 48,45056
mav 20,148,36
mav 24,152,36
mav 28,156,36
s_waitcnt lgkmcnt(1)
mav 16,160,40
s_mov_b32 s60, -13
.p2align 6
LR2_2:
mav 20,164,40
R 52,46080
mav 24,168,40
mav 28,172,40
mav 16,176,44
R 56,47104
mav 20,180,44
mav 24,184,44
D 83,2048
mav 28,188,44
s_waitcnt lgkmcnt(1)
mav 16,192,48
R 60,48128
mav 20,196,48
mav 24,200,48
D 83,3072
mav 28,204,48
mav 16,208,52
mav 20,212,52
mav 24,216,52
buffer_store_dwordx4 v[64:67], v91, s[44:47], s54 offen sc1
mav 28,220,52
s_waitcnt lgkmcnt(0)
mav 16,224,56
mav 20,228,56
mav 24,232,56
buffer_store_dwordx4 v[68:71], v92, s[44:47], s54 offen sc1
mav 28,236,56
mav 16,240,60
mav 20,244,60
mav 24,248,60
buffer_load_dwordx4 v[124:127], v90, s[36:39], s49 offen sc1
mav 28,252,60
s_waitcnt vmcnt(16)
mvv 16,116,164
mvv 20,120,164
s_waitcnt vmcnt(15)
mvv 24,116,168
buffer_load_dwordx4 v[128:131], v90, s[36:39], s49 offen offset:1024 sc1
mvv 28,120,168
s_waitcnt vmcnt(15)
s_xor_b32 s50, s50, 0x10000
s_sub_u32 s54, s54, 32768
mvv 16,116,172
mvv 20,120,172
s_waitcnt vmcnt(14)
v_xor_b32 v80, 0x10000, v80
mvv 24,116,176
s_waitcnt vmcnt(13)
s_add_u32 s49, s49, 313344
mvv 28,120,176
s_barrier
mvv 16,116,180
R 32,0
mvv 20,120,180
s_waitcnt vmcnt(12)
mvv 24,116,184
buffer_load_dwordx4 v[132:135], v86, s[36:39], s49 offen sc1
mvv 28,120,184
s_waitcnt vmcnt(12)
mvv 16,116,188
LsR15:
s_cbranch_execz LsR19
R 36,1024
mvv 20,120,188
s_waitcnt vmcnt(11)
mvv 24,116,192
buffer_load_dwordx4 v[136:139], v86, s[36:39], s49 offen offset:1024 sc1
mvv 28,120,192
s_waitcnt vmcnt(11)
mvv 16,116,196
R 40,2048
mvv 20,120,196
s_waitcnt vmcnt(10)
mvv 24,116,200
buffer_load_dwordx4 v[140:143], v87, s[36:39], s49 offen sc1
mvv 28,120,200
s_waitcnt lgkmcnt(1)
v_mfma_f32_16x16x32_bf16 v[0:3], a[0:3], v[32:35], v[100:103]
R 44,3072
v_cvt_pk_bf16_f32 v64, v16, v17
v_mfma_f32_16x16x32_bf16 v[4:7], a[4:7], v[32:35], v[104:107]
v_cvt_pk_bf16_f32 v65, v18, v19
v_cvt_pk_bf16_f32 v66, v20, v21
v_mfma_f32_16x16x32_bf16 v[8:11], a[8:11], v[32:35], v[108:111]
buffer_load_dwordx4 v[144:147], v87, s[36:39], s49 offen offset:1024 sc1
v_mfma_f32_16x16x32_bf16 v[12:15], a[12:15], v[32:35], v[112:115]
v_cvt_pk_bf16_f32 v67, v22, v23
v_cvt_pk_bf16_f32 v68, v24, v25
mav 0,16,36
R 48,4096
v_cvt_pk_bf16_f32 v69, v26, v27
mav 4,20,36
v_cvt_pk_bf16_f32 v70, v28, v29
v_cvt_pk_bf16_f32 v71, v30, v31
mav 8,24,36
buffer_load_dwordx4 v[148:151], v88, s[36:39], s49 offen sc1
mav 12,28,36
s_waitcnt lgkmcnt(1)
mav 0,32,40
R 52,5120
mav 4,36,40
mav 8,40,40
buffer_load_dwordx4 v[152:155], v88, s[36:39], s49 offen offset:1024 sc1
mav 12,44,40
mav 0,48,44
R 56,6144
mav 4,52,44
mav 8,56,44
buffer_load_dwordx4 v[156:159], v89, s[36:39], s49 offen sc1
mav 12,60,44
s_waitcnt lgkmcnt(1)
mav 0,64,48
R 60,7168
mav 4,68,48
mav 8,72,48
buffer_load_dwordx4 v[160:163], v89, s[36:39], s49 offen offset:1024 sc1
mav 12,76,48
mav 0,80,52
R 32,8192
mav 4,84,52
mav 8,88,52
s_add_u32 m0, s50, 0
mav 12,92,52
s_waitcnt lgkmcnt(1)
mav 0,96,56
R 36,9216
mav 4,100,56
mav 8,104,56
D 83,0
mav 12,108,56
ds_write_b128 v94, v[64:67] offset:0
mav 0,112,60
R 40,10240
mav 4,116,60
ds_write_b128 v95, v[68:71] offset:0
mav 8,120,60
D 83,1024
mav 12,124,60
s_waitcnt lgkmcnt(3)
ds_read_b128 v[64:67], v96 offset:0
mav 0,128,32
R 44,11264
mav 4,132,32
ds_read_b128 v[68:71], v96 offset:1024
mav 8,136,32
mav 12,140,32
mav 0,144,36
R 48,12288
mav 4,148,36
mav 8,152,36
mav 12,156,36
s_waitcnt lgkmcnt(1)
mav 0,160,40
R 52,13312
mav 4,164,40
mav 8,168,40
mav 12,172,40
mav 0,176,44
R 56,14336
mav 4,180,44
mav 8,184,44
D 83,2048
mav 12,188,44
s_waitcnt lgkmcnt(1)
mav 0,192,48
R 60,15360
mav 4,196,48
mav 8,200,48
D 83,3072
mav 12,204,48
mav 0,208,52
mav 4,212,52
mav 8,216,52
buffer_store_dwordx4 v[64:67], v91, s[44:47], s54 offen sc1
mav 12,220,52
s_waitcnt lgkmcnt(0)
mav 0,224,56
mav 4,228,56
mav 8,232,56
buffer_store_dwordx4 v[68:71], v92, s[44:47], s54 offen sc1
mav 12,236,56
mav 0,240,60
mav 4,244,60
mav 8,248,60
buffer_load_dwordx4 v[164:167], v90, s[36:39], s49 offen sc1
mav 12,252,60
s_waitcnt vmcnt(16)
mvv 0,116,124
mvv 4,120,124
s_waitcnt vmcnt(15)
mvv 8,116,128
buffer_load_dwordx4 v[168:171], v90, s[36:39], s49 offen offset:1024 sc1
mvv 12,120,128
s_waitcnt vmcnt(15)
s_sub_u32 s54, s54, 32768
mvv 0,116,132
mvv 4,120,132
s_waitcnt vmcnt(14)
mvv 8,116,136
s_waitcnt vmcnt(13)
s_add_u32 s49, s49, 313344
mvv 12,120,136
s_barrier
mvv 0,116,140
R 32,32768
mvv 4,120,140
s_waitcnt vmcnt(12)
mvv 8,116,144
buffer_load_dwordx4 v[172:175], v86, s[36:39], s49 offen sc1
mvv 12,120,144
s_waitcnt vmcnt(12)
mvv 0,116,148
R 36,33792
mvv 4,120,148
s_waitcnt vmcnt(11)
mvv 8,116,152
buffer_load_dwordx4 v[176:179], v86, s[36:39], s49 offen offset:1024 sc1
)"
R"(
mvv 12,120,152
s_waitcnt vmcnt(11)
mvv 0,116,156
R 40,34816
mvv 4,120,156
s_waitcnt vmcnt(10)
mvv 8,116,160
buffer_load_dwordx4 v[180:183], v87, s[36:39], s49 offen sc1
mvv 12,120,160
s_waitcnt lgkmcnt(1)
v_mfma_f32_16x16x32_bf16 v[16:19], a[0:3], v[32:35], v[100:103]
R 44,35840
v_cvt_pk_bf16_f32 v64, v0, v1
v_mfma_f32_16x16x32_bf16 v[20:23], a[4:7], v[32:35], v[104:107]
v_cvt_pk_bf16_f32 v65, v2, v3
v_cvt_pk_bf16_f32 v66, v4, v5
v_mfma_f32_16x16x32_bf16 v[24:27], a[8:11], v[32:35], v[108:111]
buffer_load_dwordx4 v[184:187], v87, s[36:39], s49 offen offset:1024 sc1
v_mfma_f32_16x16x32_bf16 v[28:31], a[12:15], v[32:35], v[112:115]
v_cvt_pk_bf16_f32 v67, v6, v7
v_cvt_pk_bf16_f32 v68, v8, v9
mav 16,16,36
R 48,36864
v_cvt_pk_bf16_f32 v69, v10, v11
mav 20,20,36
v_cvt_pk_bf16_f32 v70, v12, v13
v_cvt_pk_bf16_f32 v71, v14, v15
mav 24,24,36
buffer_load_dwordx4 v[188:191], v88, s[36:39], s49 offen sc1
mav 28,28,36
s_waitcnt lgkmcnt(1)
mav 16,32,40
R 52,37888
mav 20,36,40
mav 24,40,40
buffer_load_dwordx4 v[192:195], v88, s[36:39], s49 offen offset:1024 sc1
mav 28,44,40
mav 16,48,44
R 56,38912
mav 20,52,44
mav 24,56,44
buffer_load_dwordx4 v[196:199], v89, s[36:39], s49 offen sc1
mav 28,60,44
s_waitcnt lgkmcnt(1)
mav 16,64,48
R 60,39936
mav 20,68,48
mav 24,72,48
buffer_load_dwordx4 v[200:203], v89, s[36:39], s49 offen offset:1024 sc1
mav 28,76,48
mav 16,80,52
R 32,40960
mav 20,84,52
mav 24,88,52
s_add_u32 m0, s50, 32768
mav 28,92,52
s_waitcnt lgkmcnt(1)
mav 16,96,56
R 36,41984
mav 20,100,56
mav 24,104,56
D 83,0
mav 28,108,56
ds_write_b128 v94, v[64:67] offset:0
mav 16,112,60
R 40,43008
mav 20,116,60
ds_write_b128 v95, v[68:71] offset:0
mav 24,120,60
D 83,1024
mav 28,124,60
s_waitcnt lgkmcnt(3)
ds_read_b128 v[64:67], v96 offset:0
mav 16,128,32
R 44,44032
mav 20,132,32
ds_read_b128 v[68:71], v96 offset:1024
mav 24,136,32
mav 28,140,32
mav 16,144,36
R 48,45056
mav 20,148,36
mav 24,152,36
mav 28,156,36
s_waitcnt lgkmcnt(1)
s_add_u32 s60, s60, 1
mav 16,160,40
s_cbranch_scc0 LR2_2
mav 20,164,40
R 52,46080
mav 24,168,40
mav 28,172,40
mav 16,176,44
R 56,47104
mav 20,180,44
mav 24,184,44
D 83,2048
mav 28,188,44
s_waitcnt lgkmcnt(1)
mav 16,192,48
R 60,48128
mav 20,196,48
mav 24,200,48
D 83,3072
mav 28,204,48
mav 16,208,52
mav 20,212,52
mav 24,216,52
buffer_store_dwordx4 v[64:67], v91, s[44:47], s54 offen sc1
mav 28,220,52
s_waitcnt lgkmcnt(0)
mav 16,224,56
mav 20,228,56
mav 24,232,56
buffer_store_dwordx4 v[68:71], v92, s[44:47], s54 offen sc1
mav 28,236,56
mav 16,240,60
mav 20,244,60
mav 24,248,60
buffer_load_dwordx4 v[124:127], v90, s[36:39], s49 offen sc1
mav 28,252,60
s_waitcnt vmcnt(16)
mvv 16,116,164
mvv 20,120,164
s_waitcnt vmcnt(15)
mvv 24,116,168
buffer_load_dwordx4 v[128:131], v90, s[36:39], s49 offen offset:1024 sc1
mvv 28,120,168
s_waitcnt vmcnt(15)
s_sub_u32 s54, s54, 32768
mvv 16,116,172
mvv 20,120,172
s_waitcnt vmcnt(14)
v_xor_b32 v80, 0x10000, v80
mvv 24,116,176
s_waitcnt vmcnt(13)
s_add_u32 s49, s49, 313344
mvv 28,120,176
s_barrier
mvv 16,116,180
R 32,0
mvv 20,120,180
s_waitcnt vmcnt(12)
mvv 24,116,184
buffer_load_dwordx4 v[132:135], v86, s[36:39], s49 offen sc1
mvv 28,120,184
s_waitcnt vmcnt(12)
mvv 16,116,188
R 36,1024
mvv 20,120,188
s_waitcnt vmcnt(11)
mvv 24,116,192
buffer_load_dwordx4 v[136:139], v86, s[36:39], s49 offen offset:1024 sc1
mvv 28,120,192
s_waitcnt vmcnt(11)
mvv 16,116,196
R 40,2048
mvv 20,120,196
s_waitcnt vmcnt(10)
mvv 24,116,200
buffer_load_dwordx4 v[140:143], v87, s[36:39], s49 offen sc1
mvv 28,120,200
s_waitcnt lgkmcnt(1)
v_mfma_f32_16x16x32_bf16 v[0:3], a[0:3], v[32:35], v[100:103]
R 44,3072
v_cvt_pk_bf16_f32 v64, v16, v17
v_mfma_f32_16x16x32_bf16 v[4:7], a[4:7], v[32:35], v[104:107]
v_cvt_pk_bf16_f32 v65, v18, v19
v_cvt_pk_bf16_f32 v66, v20, v21
v_mfma_f32_16x16x32_bf16 v[8:11], a[8:11], v[32:35], v[108:111]
buffer_load_dwordx4 v[144:147], v87, s[36:39], s49 offen offset:1024 sc1
v_mfma_f32_16x16x32_bf16 v[12:15], a[12:15], v[32:35], v[112:115]
v_cvt_pk_bf16_f32 v67, v22, v23
v_cvt_pk_bf16_f32 v68, v24, v25
mav 0,16,36
R 48,4096
v_cvt_pk_bf16_f32 v69, v26, v27
mav 4,20,36
v_cvt_pk_bf16_f32 v70, v28, v29
v_cvt_pk_bf16_f32 v71, v30, v31
mav 8,24,36
buffer_load_dwordx4 v[148:151], v88, s[36:39], s49 offen sc1
mav 12,28,36
s_waitcnt lgkmcnt(1)
mav 0,32,40
R 52,5120
mav 4,36,40
mav 8,40,40
buffer_load_dwordx4 v[152:155], v88, s[36:39], s49 offen offset:1024 sc1
mav 12,44,40
mav 0,48,44
R 56,6144
mav 4,52,44
mav 8,56,44
buffer_load_dwordx4 v[156:159], v89, s[36:39], s49 offen sc1
mav 12,60,44
s_waitcnt lgkmcnt(1)
mav 0,64,48
R 60,7168
mav 4,68,48
mav 8,72,48
buffer_load_dwordx4 v[160:163], v89, s[36:39], s49 offen offset:1024 sc1
mav 12,76,48
mav 0,80,52
R 32,8192
mav 4,84,52
mav 8,88,52
mav 12,92,52
s_waitcnt lgkmcnt(1)
mav 0,96,56
R 36,9216
mav 4,100,56
mav 8,104,56
mav 12,108,56
ds_write_b128 v94, v[64:67] offset:0
mav 0,112,60
R 40,10240
mav 4,116,60
ds_write_b128 v95, v[68:71] offset:0
mav 8,120,60
ds_read_b128 v[64:67], v96 offset:0
mav 12,124,60
s_waitcnt lgkmcnt(4)
ds_read_b128 v[68:71], v96 offset:1024
mav 0,128,32
R 44,11264
mav 4,132,32
mav 8,136,32
mav 12,140,32
mav 0,144,36
R 48,12288
mav 4,148,36
mav 8,152,36
mav 12,156,36
s_waitcnt lgkmcnt(1)
mav 0,160,40
R 52,13312
mav 4,164,40
mav 8,168,40
mav 12,172,40
mav 0,176,44
R 56,14336
mav 4,180,44
mav 8,184,44
mav 12,188,44
s_waitcnt lgkmcnt(1)
mav 0,192,48
R 60,15360
mav 4,196,48
mav 8,200,48
mav 12,204,48
mav 0,208,52
mav 4,212,52
mav 8,216,52
buffer_store_dwordx4 v[64:67], v91, s[44:47], s54 offen sc1
mav 12,220,52
s_waitcnt lgkmcnt(0)
mav 0,224,56
mav 4,228,56
mav 8,232,56
buffer_store_dwordx4 v[68:71], v92, s[44:47], s54 offen sc1
mav 12,236,56
mav 0,240,60
mav 4,244,60
mav 8,248,60
buffer_load_dwordx4 v[164:167], v90, s[36:39], s49 offen sc1
mav 12,252,60
s_waitcnt vmcnt(12)
mvv 0,116,124
mvv 4,120,124
s_waitcnt vmcnt(11)
mvv 8,116,128
buffer_load_dwordx4 v[168:171], v90, s[36:39], s49 offen offset:1024 sc1
mvv 12,120,128
s_waitcnt vmcnt(11)
s_sub_u32 s54, s54, 32768
mvv 0,116,132
mvv 4,120,132
s_waitcnt vmcnt(10)
mvv 8,116,136
s_waitcnt vmcnt(9)
s_add_u32 s49, s49, 313344
mvv 12,120,136
s_barrier
mvv 0,116,140
R 32,32768
mvv 4,120,140
s_waitcnt vmcnt(8)
mvv 8,116,144
buffer_load_dwordx4 v[172:175], v86, s[36:39], s49 offen sc1
mvv 12,120,144
s_waitcnt vmcnt(8)
mvv 0,116,148
R 36,33792
mvv 4,120,148
s_waitcnt vmcnt(7)
mvv 8,116,152
buffer_load_dwordx4 v[176:179], v86, s[36:39], s49 offen offset:1024 sc1
mvv 12,120,152
s_waitcnt vmcnt(7)
mvv 0,116,156
R 40,34816
mvv 4,120,156
s_waitcnt vmcnt(6)
mvv 8,116,160
buffer_load_dwordx4 v[180:183], v87, s[36:39], s49 offen sc1
mvv 12,120,160
s_waitcnt lgkmcnt(1)
v_mfma_f32_16x16x32_bf16 v[16:19], a[0:3], v[32:35], v[100:103]
R 44,35840
v_cvt_pk_bf16_f32 v64, v0, v1
v_mfma_f32_16x16x32_bf16 v[20:23], a[4:7], v[32:35], v[104:107]
v_cvt_pk_bf16_f32 v65, v2, v3
v_cvt_pk_bf16_f32 v66, v4, v5
v_mfma_f32_16x16x32_bf16 v[24:27], a[8:11], v[32:35], v[108:111]
buffer_load_dwordx4 v[184:187], v87, s[36:39], s49 offen offset:1024 sc1
v_mfma_f32_16x16x32_bf16 v[28:31], a[12:15], v[32:35], v[112:115]
v_cvt_pk_bf16_f32 v67, v6, v7
v_cvt_pk_bf16_f32 v68, v8, v9
mav 16,16,36
R 48,36864
v_cvt_pk_bf16_f32 v69, v10, v11
mav 20,20,36
ds_write_b128 v94, v[64:67] offset:0
mav 24,24,36
buffer_load_dwordx4 v[188:191], v88, s[36:39], s49 offen sc1
mav 28,28,36
s_waitcnt lgkmcnt(2)
v_cvt_pk_bf16_f32 v70, v12, v13
v_cvt_pk_bf16_f32 v71, v14, v15
mav 16,32,40
R 52,37888
mav 20,36,40
ds_write_b128 v95, v[68:71] offset:0
mav 24,40,40
buffer_load_dwordx4 v[192:195], v88, s[36:39], s49 offen offset:1024 sc1
mav 28,44,40
ds_read_b128 v[64:67], v96 offset:0
mav 16,48,44
R 56,38912
mav 20,52,44
ds_read_b128 v[68:71], v96 offset:1024
mav 24,56,44
buffer_load_dwordx4 v[196:199], v89, s[36:39], s49 offen sc1
mav 28,60,44
s_waitcnt lgkmcnt(3)
mav 16,64,48
R 60,39936
mav 20,68,48
mav 24,72,48
s_waitcnt lgkmcnt(2)
buffer_load_dwordx4 v[200:203], v89, s[36:39], s49 offen offset:1024 sc1
mav 28,76,48
mav 16,80,52
s_waitcnt lgkmcnt(1)
R 32,40960
mav 20,84,52
mav 24,88,52
buffer_store_dwordx4 v[64:67], v91, s[44:47], s54 offen sc1
mav 28,92,52
mav 16,96,56
R 36,41984
mav 20,100,56
mav 24,104,56
buffer_store_dwordx4 v[68:71], v92, s[44:47], s54 offen sc1
mav 28,108,56
s_waitcnt lgkmcnt(1)
mav 16,112,60
R 40,43008
mav 20,116,60
mav 24,120,60
mav 28,124,60
mav 16,128,32
R 44,44032
mav 20,132,32
mav 24,136,32
mav 28,140,32
s_waitcnt lgkmcnt(1)
mav 16,144,36
R 48,45056
mav 20,148,36
mav 24,152,36
mav 28,156,36
mav 16,160,40
mav 20,164,40
R 52,46080
mav 24,168,40
mav 28,172,40
s_waitcnt lgkmcnt(1)
mav 16,176,44
R 56,47104
mav 20,180,44
LsR16:
s_cbranch_execz LsR20
mav 24,184,44
mav 28,188,44
mav 16,192,48
R 60,48128
mav 20,196,48
mav 24,200,48
mav 28,204,48
s_waitcnt lgkmcnt(1)
mav 16,208,52
mav 20,212,52
mav 24,216,52
mav 28,220,52
mav 16,224,56
mav 20,228,56
mav 24,232,56
mav 28,236,56
s_waitcnt lgkmcnt(0)
mav 16,240,60
mav 20,244,60
mav 24,248,60
mav 28,252,60
s_waitcnt vmcnt(11)
mvv 16,116,164
mvv 20,120,164
s_waitcnt vmcnt(10)
mvv 24,116,168
s_sub_u32 s54, s54, 32768
mvv 28,120,168
s_waitcnt vmcnt(9)
mvv 16,116,172
mvv 20,120,172
s_waitcnt vmcnt(8)
mvv 24,116,176
mvv 28,120,176
s_waitcnt vmcnt(7)
mvv 16,116,180
mvv 20,120,180
s_waitcnt vmcnt(6)
mvv 24,116,184
mvv 28,120,184
s_waitcnt vmcnt(5)
mvv 16,116,188
mvv 20,120,188
s_waitcnt vmcnt(4)
mvv 24,116,192
mvv 28,120,192
s_waitcnt vmcnt(3)
mvv 16,116,196
mvv 20,120,196
s_waitcnt vmcnt(2)
mvv 24,116,200
mvv 28,120,200
s_nop 3
v_cvt_pk_bf16_f32 v64, v16, v17
v_cvt_pk_bf16_f32 v65, v18, v19
v_cvt_pk_bf16_f32 v66, v20, v21
v_cvt_pk_bf16_f32 v67, v22, v23
ds_write_b128 v94, v[64:67] offset:0
v_cvt_pk_bf16_f32 v68, v24, v25
v_cvt_pk_bf16_f32 v69, v26, v27
v_cvt_pk_bf16_f32 v70, v28, v29
v_cvt_pk_bf16_f32 v71, v30, v31
ds_write_b128 v95, v[68:71] offset:0
ds_read_b128 v[64:67], v96 offset:0
ds_read_b128 v[68:71], v96 offset:1024
s_waitcnt lgkmcnt(0)
buffer_store_dwordx4 v[64:67], v91, s[44:47], s54 offen sc1
buffer_store_dwordx4 v[68:71], v92, s[44:47], s54 offen sc1
s_endpgm
LvR3:
LsR17:
s_cbranch_execz LsR21
s_add_u32 s49, s49, 313344
s_barrier
s_add_u32 m0, s50, 0
s_nop 0
D 83,0
D 83,1024
D 83,2048
D 83,3072
s_add_u32 s49, s49, 313344
s_barrier
s_add_u32 m0, s50, 32768
s_nop 0
D 83,0
D 83,1024
D 83,2048
D 83,3072
buffer_load_dwordx4 v[124:127], v90, s[36:39], s49 offen sc1
buffer_load_dwordx4 v[128:131], v90, s[36:39], s49 offen offset:1024 sc1
s_xor_b32 s50, s50, 0x10000
s_waitcnt vmcnt(6)
s_add_u32 s49, s49, 313344
s_barrier
R 32,0
buffer_load_dwordx4 v[132:135], v86, s[36:39], s49 offen sc1
R 36,1024
buffer_load_dwordx4 v[136:139], v86, s[36:39], s49 offen offset:1024 sc1
R 40,2048
buffer_load_dwordx4 v[140:143], v87, s[36:39], s49 offen sc1
s_waitcnt lgkmcnt(1)
v_mfma_f32_16x16x32_bf16 v[0:3], a[0:3], v[32:35], v[100:103]
R 44,3072
v_mfma_f32_16x16x32_bf16 v[4:7], a[4:7], v[32:35], v[104:107]
v_mfma_f32_16x16x32_bf16 v[8:11], a[8:11], v[32:35], v[108:111]
v_mfma_f32_16x16x32_bf16 v[12:15], a[12:15], v[32:35], v[112:115]
buffer_load_dwordx4 v[144:147], v87, s[36:39], s49 offen offset:1024 sc1
mav 0,16,36
R 48,4096
mav 4,20,36
mav 8,24,36
mav 12,28,36
s_waitcnt lgkmcnt(1)
buffer_load_dwordx4 v[148:151], v88, s[36:39], s49 offen sc1
mav 0,32,40
R 52,5120
mav 4,36,40
mav 8,40,40
mav 12,44,40
buffer_load_dwordx4 v[152:155], v88, s[36:39], s49 offen offset:1024 sc1
mav 0,48,44
R 56,6144
mav 4,52,44
mav 8,56,44
mav 12,60,44
s_waitcnt lgkmcnt(1)
buffer_load_dwordx4 v[156:159], v89, s[36:39], s49 offen sc1
mav 0,64,48
R 60,7168
mav 4,68,48
mav 8,72,48
mav 12,76,48
buffer_load_dwordx4 v[160:163], v89, s[36:39], s49 offen offset:1024 sc1
mav 0,80,52
R 32,8192
mav 4,84,52
mav 8,88,52
s_add_u32 m0, s50, 0
mav 12,92,52
s_waitcnt lgkmcnt(1)
mav 0,96,56
R 36,9216
mav 4,100,56
mav 8,104,56
mav 12,108,56
D 83,0
mav 0,112,60
R 40,10240
mav 4,116,60
mav 8,120,60
mav 12,124,60
s_waitcnt lgkmcnt(1)
D 83,1024
mav 0,128,32
R 44,11264
mav 4,132,32
mav 8,136,32
mav 12,140,32
mav 0,144,36
R 48,12288
mav 4,148,36
mav 8,152,36
mav 12,156,36
s_waitcnt lgkmcnt(1)
mav 0,160,40
R 52,13312
mav 4,164,40
mav 8,168,40
mav 12,172,40
mav 0,176,44
R 56,14336
mav 4,180,44
mav 8,184,44
mav 12,188,44
s_waitcnt lgkmcnt(1)
D 83,2048
mav 0,192,48
R 60,15360
mav 4,196,48
mav 8,200,48
mav 12,204,48
D 83,3072
mav 0,208,52
mav 4,212,52
mav 8,216,52
mav 12,220,52
s_waitcnt lgkmcnt(0)
mav 0,224,56
mav 4,228,56
mav 8,232,56
mav 12,236,56
mav 0,240,60
mav 4,244,60
mav 8,248,60
mav 12,252,60
s_waitcnt vmcnt(13)
buffer_load_dwordx4 v[164:167], v90, s[36:39], s49 offen sc1
mvv 0,116,124
mvv 4,120,124
s_waitcnt vmcnt(13)
mvv 8,116,128
mvv 12,120,128
s_waitcnt vmcnt(12)
buffer_load_dwordx4 v[168:171], v90, s[36:39], s49 offen offset:1024 sc1
mvv 0,116,132
mvv 4,120,132
s_waitcnt vmcnt(12)
mvv 8,116,136
s_waitcnt vmcnt(11)
s_add_u32 s49, s49, 313344
mvv 12,120,136
s_barrier
mvv 0,116,140
R 32,32768
mvv 4,120,140
s_waitcnt vmcnt(10)
mvv 8,116,144
mvv 12,120,144
s_waitcnt vmcnt(9)
buffer_load_dwordx4 v[172:175], v86, s[36:39], s49 offen sc1
mvv 0,116,148
R 36,33792
mvv 4,120,148
s_waitcnt vmcnt(9)
mvv 8,116,152
mvv 12,120,152
s_waitcnt vmcnt(8)
buffer_load_dwordx4 v[176:179], v86, s[36:39], s49 offen offset:1024 sc1
mvv 0,116,156
R 40,34816
mvv 4,120,156
s_waitcnt vmcnt(8)
mvv 8,116,160
mvv 12,120,160
s_waitcnt lgkmcnt(1)
buffer_load_dwordx4 v[180:183], v87, s[36:39], s49 offen sc1
v_mfma_f32_16x16x32_bf16 v[16:19], a[0:3], v[32:35], v[100:103]
R 44,35840
v_cvt_pk_bf16_f32 v64, v0, v1
v_mfma_f32_16x16x32_bf16 v[20:23], a[4:7], v[32:35], v[104:107]
v_cvt_pk_bf16_f32 v65, v2, v3
v_cvt_pk_bf16_f32 v66, v4, v5
v_mfma_f32_16x16x32_bf16 v[24:27], a[8:11], v[32:35], v[108:111]
v_cvt_pk_bf16_f32 v67, v6, v7
v_cvt_pk_bf16_f32 v68, v8, v9
v_mfma_f32_16x16x32_bf16 v[28:31], a[12:15], v[32:35], v[112:115]
buffer_load_dwordx4 v[184:187], v87, s[36:39], s49 offen offset:1024 sc1
mav 16,16,36
R 48,36864
v_cvt_pk_bf16_f32 v69, v10, v11
mav 20,20,36
v_cvt_pk_bf16_f32 v70, v12, v13
v_cvt_pk_bf16_f32 v71, v14, v15
mav 24,24,36
v_mul_f32 v75, v0, v0
v_add_f32 v74, v1, v0
mav 28,28,36
s_waitcnt lgkmcnt(1)
buffer_load_dwordx4 v[188:191], v88, s[36:39], s49 offen sc1
mav 16,32,40
R 52,37888
v_fmac_f32 v75, v1, v1
mav 20,36,40
v_add_f32 v74, v2, v74
v_fmac_f32 v75, v2, v2
mav 24,40,40
v_add_f32 v74, v3, v74
v_fmac_f32 v75, v3, v3
mav 28,44,40
buffer_load_dwordx4 v[192:195], v88, s[36:39], s49 offen offset:1024 sc1
mav 16,48,44
R 56,38912
v_add_f32 v74, v4, v74
mav 20,52,44
v_fmac_f32 v75, v4, v4
v_add_f32 v74, v5, v74
mav 24,56,44
v_fmac_f32 v75, v5, v5
v_add_f32 v74, v6, v74
mav 28,60,44
s_waitcnt lgkmcnt(1)
buffer_load_dwordx4 v[196:199], v89, s[36:39], s49 offen sc1
mav 16,64,48
R 60,39936
v_fmac_f32 v75, v6, v6
mav 20,68,48
v_add_f32 v74, v7, v74
v_fmac_f32 v75, v7, v7
mav 24,72,48
v_add_f32 v74, v8, v74
v_fmac_f32 v75, v8, v8
mav 28,76,48
buffer_load_dwordx4 v[200:203], v89, s[36:39], s49 offen offset:1024 sc1
mav 16,80,52
R 32,40960
v_add_f32 v74, v9, v74
mav 20,84,52
v_fmac_f32 v75, v9, v9
v_add_f32 v74, v10, v74
mav 24,88,52
s_add_u32 m0, s50, 32768
v_fmac_f32 v75, v10, v10
v_add_f32 v74, v11, v74
mav 28,92,52
s_waitcnt lgkmcnt(1)
v_fmac_f32 v75, v11, v11
v_add_f32 v74, v12, v74
mav 16,96,56
R 36,41984
v_fmac_f32 v75, v12, v12
mav 20,100,56
v_add_f32 v74, v13, v74
v_fmac_f32 v75, v13, v13
mav 24,104,56
v_add_f32 v74, v14, v74
v_fmac_f32 v75, v14, v14
mav 28,108,56
D 83,0
mav 16,112,60
R 40,43008
v_add_f32 v74, v15, v74
mav 20,116,60
v_fmac_f32 v75, v15, v15
mav 24,120,60
mav 28,124,60
s_waitcnt lgkmcnt(1)
D 83,1024
mav 16,128,32
R 44,44032
mav 20,132,32
ds_write_b128 v94, v[64:67] offset:0
mav 24,136,32
ds_write_b128 v95, v[68:71] offset:0
mav 28,140,32
ds_read_b128 v[64:67], v96 offset:0
mav 16,144,36
R 48,45056
mav 20,148,36
ds_read_b128 v[68:71], v96 offset:1024
mav 24,152,36
v_permlane32_swap_b32 v74, v75
mav 28,156,36
s_waitcnt lgkmcnt(4)
v_add_f32 v74, v74, v75
mav 16,160,40
mav 20,164,40
R 52,46080
mav 24,168,40
mav 28,172,40
mav 16,176,44
R 56,47104
mav 20,180,44
mav 24,184,44
mav 28,188,44
s_waitcnt lgkmcnt(1)
D 83,2048
mav 16,192,48
R 60,48128
mav 20,196,48
mav 24,200,48
mav 28,204,48
D 83,3072
mav 16,208,52
mav 20,212,52
mav 24,216,52
mav 28,220,52
buffer_store_dwordx4 v[64:67], v91, s[36:39], s54 offen sc1
s_waitcnt lgkmcnt(0)
mav 16,224,56
mav 20,228,56
mav 24,232,56
mav 28,236,56
buffer_store_dwordx4 v[68:71], v92, s[36:39], s54 offen sc1
mav 16,240,60
mav 20,244,60
mav 24,248,60
mav 28,252,60
s_waitcnt vmcnt(15)
buffer_load_dwordx4 v[124:127], v90, s[36:39], s49 offen sc1
mvv 16,116,164
mvv 20,120,164
s_waitcnt vmcnt(15)
mvv 24,116,168
s_xor_b32 s50, s50, 0x10000
s_sub_u32 s54, s54, 32768
mvv 28,120,168
s_waitcnt vmcnt(14)
buffer_load_dwordx4 v[128:131], v90, s[36:39], s49 offen offset:1024 sc1
mvv 16,116,172
mvv 20,120,172
s_waitcnt vmcnt(14)
v_xor_b32 v80, 0x10000, v80
mvv 24,116,176
s_waitcnt vmcnt(13)
s_add_u32 s49, s49, 313344
mvv 28,120,176
s_barrier
mvv 16,116,180
R 32,0
mvv 20,120,180
s_waitcnt vmcnt(12)
mvv 24,116,184
mvv 28,120,184
s_waitcnt vmcnt(11)
buffer_load_dwordx4 v[132:135], v86, s[36:39], s49 offen sc1
mvv 16,116,188
R 36,1024
mvv 20,120,188
s_waitcnt vmcnt(11)
mvv 24,116,192
mvv 28,120,192
s_waitcnt vmcnt(10)
buffer_load_dwordx4 v[136:139], v86, s[36:39], s49 offen offset:1024 sc1
mvv 16,116,196
R 40,2048
mvv 20,120,196
s_waitcnt vmcnt(10)
mvv 24,116,200
mvv 28,120,200
s_waitcnt lgkmcnt(1)
buffer_load_dwordx4 v[140:143], v87, s[36:39], s49 offen sc1
v_mfma_f32_16x16x32_bf16 v[0:3], a[0:3], v[32:35], v[100:103]
R 44,3072
v_cvt_pk_bf16_f32 v64, v16, v17
v_mfma_f32_16x16x32_bf16 v[4:7], a[4:7], v[32:35], v[104:107]
v_cvt_pk_bf16_f32 v65, v18, v19
v_cvt_pk_bf16_f32 v66, v20, v21
v_mfma_f32_16x16x32_bf16 v[8:11], a[8:11], v[32:35], v[108:111]
v_cvt_pk_bf16_f32 v67, v22, v23
v_cvt_pk_bf16_f32 v68, v24, v25
v_mfma_f32_16x16x32_bf16 v[12:15], a[12:15], v[32:35], v[112:115]
buffer_load_dwordx4 v[144:147], v87, s[36:39], s49 offen offset:1024 sc1
mav 0,16,36
R 48,4096
v_cvt_pk_bf16_f32 v69, v26, v27
mav 4,20,36
v_cvt_pk_bf16_f32 v70, v28, v29
v_cvt_pk_bf16_f32 v71, v30, v31
mav 8,24,36
v_mul_f32 v75, v16, v16
v_fmac_f32 v75, v17, v17
mav 12,28,36
s_waitcnt lgkmcnt(1)
buffer_load_dwordx4 v[148:151], v88, s[36:39], s49 offen sc1
mav 0,32,40
R 52,5120
v_fmac_f32 v75, v18, v18
mav 4,36,40
v_fmac_f32 v75, v19, v19
v_fmac_f32 v75, v20, v20
mav 8,40,40
v_fmac_f32 v75, v21, v21
v_fmac_f32 v75, v22, v22
mav 12,44,40
buffer_load_dwordx4 v[152:155], v88, s[36:39], s49 offen offset:1024 sc1
mav 0,48,44
R 56,6144
v_fmac_f32 v75, v23, v23
mav 4,52,44
v_fmac_f32 v75, v24, v24
v_fmac_f32 v75, v25, v25
mav 8,56,44
v_fmac_f32 v75, v26, v26
v_fmac_f32 v75, v27, v27
mav 12,60,44
s_waitcnt lgkmcnt(1)
buffer_load_dwordx4 v[156:159], v89, s[36:39], s49 offen sc1
mav 0,64,48
R 60,7168
v_fmac_f32 v75, v28, v28
mav 4,68,48
v_fmac_f32 v75, v29, v29
v_fmac_f32 v75, v30, v30
mav 8,72,48
v_fmac_f32 v75, v31, v31
mav 12,76,48
buffer_load_dwordx4 v[160:163], v89, s[36:39], s49 offen offset:1024 sc1
mav 0,80,52
R 32,8192
mav 4,84,52
mav 8,88,52
s_add_u32 m0, s50, 0
mav 12,92,52
s_waitcnt lgkmcnt(1)
mav 0,96,56
R 36,9216
mav 4,100,56
mav 8,104,56
mav 12,108,56
D 83,0
mav 0,112,60
R 40,10240
mav 4,116,60
mav 8,120,60
ds_write_b32 v97, v74 offset:0
mav 12,124,60
s_waitcnt lgkmcnt(2)
D 83,1024
mav 0,128,32
R 44,11264
v_add_f32 v74, v17, v16
mav 4,132,32
ds_write_b128 v94, v[64:67] offset:0
mav 8,136,32
ds_write_b128 v95, v[68:71] offset:0
mav 12,140,32
ds_read_b128 v[64:67], v96 offset:0
v_add_f32 v74, v18, v74
mav 0,144,36
R 48,12288
v_add_f32 v74, v19, v74
mav 4,148,36
ds_read_b128 v[68:71], v96 offset:1024
v_add_f32 v74, v20, v74
mav 8,152,36
v_add_f32 v74, v21, v74
v_add_f32 v74, v22, v74
mav 12,156,36
s_waitcnt lgkmcnt(4)
v_add_f32 v74, v23, v74
v_add_f32 v74, v24, v74
mav 0,160,40
R 52,13312
v_add_f32 v74, v25, v74
mav 4,164,40
v_add_f32 v74, v26, v74
v_add_f32 v74, v27, v74
mav 8,168,40
v_add_f32 v74, v28, v74
v_add_f32 v74, v29, v74
mav 12,172,40
v_add_f32 v74, v30, v74
v_add_f32 v74, v31, v74
mav 0,176,44
R 56,14336
v_add_u32 v72, 1, v72
mav 4,180,44
v_permlane32_swap_b32 v74, v75
mav 8,184,44
v_add_f32 v74, v74, v75
mav 12,188,44
s_waitcnt lgkmcnt(1)
D 83,2048
mav 0,192,48
R 60,15360
mav 4,196,48
mav 8,200,48
mav 12,204,48
D 83,3072
mav 0,208,52
mav 4,212,52
mav 8,216,52
mav 12,220,52
buffer_store_dwordx4 v[64:67], v91, s[36:39], s54 offen sc1
s_waitcnt lgkmcnt(0)
mav 0,224,56
mav 4,228,56
mav 8,232,56
mav 12,236,56
buffer_store_dwordx4 v[68:71], v92, s[36:39], s54 offen sc1
mav 0,240,60
mav 4,244,60
mav 8,248,60
mav 12,252,60
s_waitcnt vmcnt(15)
buffer_load_dwordx4 v[164:167], v90, s[36:39], s49 offen sc1
mvv 0,116,124
buffer_store_dword v72, v81, s[36:39], s52 offen sc1
mvv 4,120,124
s_waitcnt vmcnt(16)
mvv 8,116,128
s_sub_u32 s54, s54, 32768
mvv 12,120,128
s_waitcnt vmcnt(15)
buffer_load_dwordx4 v[168:171], v90, s[36:39], s49 offen offset:1024 sc1
mvv 0,116,132
mvv 4,120,132
s_waitcnt vmcnt(15)
mvv 8,116,136
s_waitcnt vmcnt(14)
s_add_u32 s49, s49, 313344
mvv 12,120,136
s_barrier
mvv 0,116,140
R 32,32768
mvv 4,120,140
LsR18:
s_cbranch_execz LwR
s_waitcnt vmcnt(13)
mvv 8,116,144
mvv 12,120,144
s_waitcnt vmcnt(12)
buffer_load_dwordx4 v[172:175], v86, s[36:39], s49 offen sc1
mvv 0,116,148
R 36,33792
mvv 4,120,148
s_waitcnt vmcnt(12)
mvv 8,116,152
mvv 12,120,152
s_waitcnt vmcnt(11)
buffer_load_dwordx4 v[176:179], v86, s[36:39], s49 offen offset:1024 sc1
mvv 0,116,156
R 40,34816
mvv 4,120,156
s_waitcnt vmcnt(11)
mvv 8,116,160
mvv 12,120,160
s_waitcnt lgkmcnt(1)
buffer_load_dwordx4 v[180:183], v87, s[36:39], s49 offen sc1
v_mfma_f32_16x16x32_bf16 v[16:19], a[0:3], v[32:35], v[100:103]
R 44,35840
v_cvt_pk_bf16_f32 v64, v0, v1
v_mfma_f32_16x16x32_bf16 v[20:23], a[4:7], v[32:35], v[104:107]
v_cvt_pk_bf16_f32 v65, v2, v3
v_cvt_pk_bf16_f32 v66, v4, v5
v_mfma_f32_16x16x32_bf16 v[24:27], a[8:11], v[32:35], v[108:111]
v_cvt_pk_bf16_f32 v67, v6, v7
v_cvt_pk_bf16_f32 v68, v8, v9
v_mfma_f32_16x16x32_bf16 v[28:31], a[12:15], v[32:35], v[112:115]
buffer_load_dwordx4 v[184:187], v87, s[36:39], s49 offen offset:1024 sc1
mav 16,16,36
R 48,36864
v_cvt_pk_bf16_f32 v69, v10, v11
mav 20,20,36
v_cvt_pk_bf16_f32 v70, v12, v13
v_cvt_pk_bf16_f32 v71, v14, v15
mav 24,24,36
v_mul_f32 v75, v0, v0
v_fmac_f32 v75, v1, v1
mav 28,28,36
s_waitcnt lgkmcnt(1)
buffer_load_dwordx4 v[188:191], v88, s[36:39], s49 offen sc1
mav 16,32,40
R 52,37888
v_fmac_f32 v75, v2, v2
mav 20,36,40
v_fmac_f32 v75, v3, v3
v_fmac_f32 v75, v4, v4
mav 24,40,40
v_fmac_f32 v75, v5, v5
v_fmac_f32 v75, v6, v6
mav 28,44,40
buffer_load_dwordx4 v[192:195], v88, s[36:39], s49 offen offset:1024 sc1
mav 16,48,44
R 56,38912
v_fmac_f32 v75, v7, v7
mav 20,52,44
v_fmac_f32 v75, v8, v8
v_fmac_f32 v75, v9, v9
mav 24,56,44
v_fmac_f32 v75, v10, v10
v_fmac_f32 v75, v11, v11
mav 28,60,44
s_waitcnt lgkmcnt(1)
buffer_load_dwordx4 v[196:199], v89, s[36:39], s49 offen sc1
mav 16,64,48
R 60,39936
v_fmac_f32 v75, v12, v12
mav 20,68,48
v_fmac_f32 v75, v13, v13
v_fmac_f32 v75, v14, v14
mav 24,72,48
v_fmac_f32 v75, v15, v15
mav 28,76,48
buffer_load_dwordx4 v[200:203], v89, s[36:39], s49 offen offset:1024 sc1
mav 16,80,52
R 32,40960
mav 20,84,52
mav 24,88,52
s_add_u32 m0, s50, 32768
mav 28,92,52
s_waitcnt lgkmcnt(1)
mav 16,96,56
R 36,41984
mav 20,100,56
mav 24,104,56
mav 28,108,56
D 83,0
mav 16,112,60
R 40,43008
mav 20,116,60
mav 24,120,60
ds_write_b32 v97, v74 offset:1024
mav 28,124,60
s_waitcnt lgkmcnt(2)
D 83,1024
mav 16,128,32
R 44,44032
v_add_f32 v74, v1, v0
mav 20,132,32
ds_write_b128 v94, v[64:67] offset:0
mav 24,136,32
ds_write_b128 v95, v[68:71] offset:0
mav 28,140,32
ds_read_b128 v[64:67], v96 offset:0
v_add_f32 v74, v2, v74
mav 16,144,36
R 48,45056
v_add_f32 v74, v3, v74
mav 20,148,36
ds_read_b128 v[68:71], v96 offset:1024
v_add_f32 v74, v4, v74
mav 24,152,36
v_add_f32 v74, v5, v74
v_add_f32 v74, v6, v74
mav 28,156,36
s_waitcnt lgkmcnt(4)
v_add_f32 v74, v7, v74
mav 16,160,40
s_mov_b32 s60, -14
.p2align 6
LR3_1:
mav 20,164,40
R 52,46080
v_add_f32 v74, v8, v74
mav 24,168,40
v_add_f32 v74, v9, v74
v_add_f32 v74, v10, v74
mav 28,172,40
v_add_f32 v74, v11, v74
v_add_f32 v74, v12, v74
mav 16,176,44
R 56,47104
v_add_f32 v74, v13, v74
mav 20,180,44
v_add_f32 v74, v14, v74
v_add_f32 v74, v15, v74
mav 24,184,44
v_add_u32 v72, 1, v72
mav 28,188,44
s_waitcnt lgkmcnt(1)
D 83,2048
mav 16,192,48
R 60,48128
mav 20,196,48
v_permlane32_swap_b32 v74, v75
mav 24,200,48
v_add_f32 v74, v74, v75
mav 28,204,48
D 83,3072
mav 16,208,52
mav 20,212,52
mav 24,216,52
mav 28,220,52
buffer_store_dwordx4 v[64:67], v91, s[36:39], s54 offen sc1
s_waitcnt lgkmcnt(0)
mav 16,224,56
mav 20,228,56
mav 24,232,56
mav 28,236,56
buffer_store_dwordx4 v[68:71], v92, s[36:39], s54 offen sc1
mav 16,240,60
mav 20,244,60
mav 24,248,60
mav 28,252,60
s_waitcnt vmcnt(16)
buffer_load_dwordx4 v[124:127], v90, s[36:39], s49 offen sc1
mvv 16,116,164
buffer_store_dword v72, v81, s[36:39], s52 offen sc1
mvv 20,120,164
s_waitcnt vmcnt(16)
mvv 24,116,168
s_xor_b32 s50, s50, 0x10000
s_sub_u32 s54, s54, 32768
mvv 28,120,168
s_waitcnt vmcnt(15)
buffer_load_dwordx4 v[128:131], v90, s[36:39], s49 offen offset:1024 sc1
mvv 16,116,172
mvv 20,120,172
s_waitcnt vmcnt(15)
v_xor_b32 v80, 0x10000, v80
mvv 24,116,176
s_waitcnt vmcnt(14)
s_add_u32 s49, s49, 313344
mvv 28,120,176
s_barrier
mvv 16,116,180
R 32,0
mvv 20,120,180
s_waitcnt vmcnt(13)
mvv 24,116,184
mvv 28,120,184
s_waitcnt vmcnt(12)
buffer_load_dwordx4 v[132:135], v86, s[36:39], s49 offen sc1
mvv 16,116,188
R 36,1024
mvv 20,120,188
s_waitcnt vmcnt(12)
mvv 24,116,192
mvv 28,120,192
s_waitcnt vmcnt(11)
buffer_load_dwordx4 v[136:139], v86, s[36:39], s49 offen offset:1024 sc1
mvv 16,116,196
R 40,2048
mvv 20,120,196
s_waitcnt vmcnt(11)
mvv 24,116,200
mvv 28,120,200
s_waitcnt lgkmcnt(1)
buffer_load_dwordx4 v[140:143], v87, s[36:39], s49 offen sc1
v_mfma_f32_16x16x32_bf16 v[0:3], a[0:3], v[32:35], v[100:103]
R 44,3072
v_cvt_pk_bf16_f32 v64, v16, v17
v_mfma_f32_16x16x32_bf16 v[4:7], a[4:7], v[32:35], v[104:107]
v_cvt_pk_bf16_f32 v65, v18, v19
v_cvt_pk_bf16_f32 v66, v20, v21
v_mfma_f32_16x16x32_bf16 v[8:11], a[8:11], v[32:35], v[108:111]
v_cvt_pk_bf16_f32 v67, v22, v23
v_cvt_pk_bf16_f32 v68, v24, v25
v_mfma_f32_16x16x32_bf16 v[12:15], a[12:15], v[32:35], v[112:115]
buffer_load_dwordx4 v[144:147], v87, s[36:39], s49 offen offset:1024 sc1
mav 0,16,36
R 48,4096
v_cvt_pk_bf16_f32 v69, v26, v27
mav 4,20,36
v_cvt_pk_bf16_f32 v70, v28, v29
v_cvt_pk_bf16_f32 v71, v30, v31
mav 8,24,36
v_mul_f32 v75, v16, v16
v_fmac_f32 v75, v17, v17
mav 12,28,36
s_waitcnt lgkmcnt(1)
buffer_load_dwordx4 v[148:151], v88, s[36:39], s49 offen sc1
mav 0,32,40
R 52,5120
v_fmac_f32 v75, v18, v18
mav 4,36,40
v_fmac_f32 v75, v19, v19
v_fmac_f32 v75, v20, v20
mav 8,40,40
v_fmac_f32 v75, v21, v21
v_fmac_f32 v75, v22, v22
mav 12,44,40
buffer_load_dwordx4 v[152:155], v88, s[36:39], s49 offen offset:1024 sc1
mav 0,48,44
R 56,6144
v_fmac_f32 v75, v23, v23
mav 4,52,44
v_fmac_f32 v75, v24, v24
v_fmac_f32 v75, v25, v25
mav 8,56,44
v_fmac_f32 v75, v26, v26
v_fmac_f32 v75, v27, v27
mav 12,60,44
s_waitcnt lgkmcnt(1)
buffer_load_dwordx4 v[156:159], v89, s[36:39], s49 offen sc1
mav 0,64,48
R 60,7168
v_fmac_f32 v75, v28, v28
mav 4,68,48
v_fmac_f32 v75, v29, v29
v_fmac_f32 v75, v30, v30
mav 8,72,48
v_fmac_f32 v75, v31, v31
mav 12,76,48
buffer_load_dwordx4 v[160:163], v89, s[36:39], s49 offen offset:1024 sc1
mav 0,80,52
R 32,8192
mav 4,84,52
mav 8,88,52
s_add_u32 m0, s50, 0
mav 12,92,52
s_waitcnt lgkmcnt(1)
mav 0,96,56
R 36,9216
mav 4,100,56
mav 8,104,56
mav 12,108,56
D 83,0
mav 0,112,60
R 40,10240
mav 4,116,60
mav 8,120,60
ds_write_b32 v97, v74 offset:0
mav 12,124,60
s_waitcnt lgkmcnt(2)
D 83,1024
mav 0,128,32
R 44,11264
v_add_f32 v74, v17, v16
mav 4,132,32
ds_write_b128 v94, v[64:67] offset:0
mav 8,136,32
ds_write_b128 v95, v[68:71] offset:0
mav 12,140,32
ds_read_b128 v[64:67], v96 offset:0
v_add_f32 v74, v18, v74
mav 0,144,36
R 48,12288
v_add_f32 v74, v19, v74
mav 4,148,36
ds_read_b128 v[68:71], v96 offset:1024
v_add_f32 v74, v20, v74
mav 8,152,36
v_add_f32 v74, v21, v74
v_add_f32 v74, v22, v74
mav 12,156,36
s_waitcnt lgkmcnt(4)
v_add_f32 v74, v23, v74
v_add_f32 v74, v24, v74
mav 0,160,40
R 52,13312
v_add_f32 v74, v25, v74
mav 4,164,40
v_add_f32 v74, v26, v74
v_add_f32 v74, v27, v74
mav 8,168,40
v_add_f32 v74, v28, v74
v_add_f32 v74, v29, v74
mav 12,172,40
v_add_f32 v74, v30, v74
v_add_f32 v74, v31, v74
mav 0,176,44
R 56,14336
v_add_u32 v72, 1, v72
mav 4,180,44
v_permlane32_swap_b32 v74, v75
mav 8,184,44
v_add_f32 v74, v74, v75
mav 12,188,44
s_waitcnt lgkmcnt(1)
D 83,2048
mav 0,192,48
R 60,15360
mav 4,196,48
mav 8,200,48
mav 12,204,48
D 83,3072
mav 0,208,52
mav 4,212,52
mav 8,216,52
mav 12,220,52
buffer_store_dwordx4 v[64:67], v91, s[36:39], s54 offen sc1
s_waitcnt lgkmcnt(0)
mav 0,224,56
mav 4,228,56
mav 8,232,56
mav 12,236,56
buffer_store_dwordx4 v[68:71], v92, s[36:39], s54 offen sc1
mav 0,240,60
mav 4,244,60
mav 8,248,60
mav 12,252,60
s_waitcnt vmcnt(16)
buffer_load_dwordx4 v[164:167], v90, s[36:39], s49 offen sc1
mvv 0,116,124
buffer_store_dword v72, v81, s[36:39], s52 offen sc1
mvv 4,120,124
s_waitcnt vmcnt(16)
mvv 8,116,128
s_sub_u32 s54, s54, 32768
mvv 12,120,128
s_waitcnt vmcnt(15)
buffer_load_dwordx4 v[168:171], v90, s[36:39], s49 offen offset:1024 sc1
mvv 0,116,132
mvv 4,120,132
s_waitcnt vmcnt(15)
mvv 8,116,136
s_waitcnt vmcnt(14)
s_add_u32 s49, s49, 313344
mvv 12,120,136
s_barrier
mvv 0,116,140
R 32,32768
mvv 4,120,140
s_waitcnt vmcnt(13)
mvv 8,116,144
mvv 12,120,144
s_waitcnt vmcnt(12)
buffer_load_dwordx4 v[172:175], v86, s[36:39], s49 offen sc1
mvv 0,116,148
R 36,33792
mvv 4,120,148
s_waitcnt vmcnt(12)
mvv 8,116,152
mvv 12,120,152
s_waitcnt vmcnt(11)
buffer_load_dwordx4 v[176:179], v86, s[36:39], s49 offen offset:1024 sc1
mvv 0,116,156
R 40,34816
mvv 4,120,156
s_waitcnt vmcnt(11)
mvv 8,116,160
mvv 12,120,160
s_waitcnt lgkmcnt(1)
buffer_load_dwordx4 v[180:183], v87, s[36:39], s49 offen sc1
v_mfma_f32_16x16x32_bf16 v[16:19], a[0:3], v[32:35], v[100:103]
R 44,35840
v_cvt_pk_bf16_f32 v64, v0, v1
v_mfma_f32_16x16x32_bf16 v[20:23], a[4:7], v[32:35], v[104:107]
v_cvt_pk_bf16_f32 v65, v2, v3
v_cvt_pk_bf16_f32 v66, v4, v5
v_mfma_f32_16x16x32_bf16 v[24:27], a[8:11], v[32:35], v[108:111]
v_cvt_pk_bf16_f32 v67, v6, v7
v_cvt_pk_bf16_f32 v68, v8, v9
v_mfma_f32_16x16x32_bf16 v[28:31], a[12:15], v[32:35], v[112:115]
buffer_load_dwordx4 v[184:187], v87, s[36:39], s49 offen offset:1024 sc1
mav 16,16,36
R 48,36864
v_cvt_pk_bf16_f32 v69, v10, v11
mav 20,20,36
v_cvt_pk_bf16_f32 v70, v12, v13
v_cvt_pk_bf16_f32 v71, v14, v15
mav 24,24,36
v_mul_f32 v75, v0, v0
v_fmac_f32 v75, v1, v1
mav 28,28,36
s_waitcnt lgkmcnt(1)
buffer_load_dwordx4 v[188:191], v88, s[36:39], s49 offen sc1
mav 16,32,40
R 52,37888
v_fmac_f32 v75, v2, v2
mav 20,36,40
v_fmac_f32 v75, v3, v3
v_fmac_f32 v75, v4, v4
mav 24,40,40
v_fmac_f32 v75, v5, v5
v_fmac_f32 v75, v6, v6
mav 28,44,40
buffer_load_dwordx4 v[192:195], v88, s[36:39], s49 offen offset:1024 sc1
mav 16,48,44
R 56,38912
v_fmac_f32 v75, v7, v7
mav 20,52,44
v_fmac_f32 v75, v8, v8
v_fmac_f32 v75, v9, v9
mav 24,56,44
v_fmac_f32 v75, v10, v10
v_fmac_f32 v75, v11, v11
mav 28,60,44
s_waitcnt lgkmcnt(1)
buffer_load_dwordx4 v[196:199], v89, s[36:39], s49 offen sc1
mav 16,64,48
R 60,39936
v_fmac_f32 v75, v12, v12
mav 20,68,48
v_fmac_f32 v75, v13, v13
v_fmac_f32 v75, v14, v14
mav 24,72,48
v_fmac_f32 v75, v15, v15
mav 28,76,48
buffer_load_dwordx4 v[200:203], v89, s[36:39], s49 offen offset:1024 sc1
mav 16,80,52
R 32,40960
mav 20,84,52
mav 24,88,52
s_add_u32 m0, s50, 32768
mav 28,92,52
s_waitcnt lgkmcnt(1)
mav 16,96,56
R 36,41984
mav 20,100,56
mav 24,104,56
mav 28,108,56
D 83,0
mav 16,112,60
R 40,43008
mav 20,116,60
mav 24,120,60
ds_write_b32 v97, v74 offset:1024
mav 28,124,60
s_waitcnt lgkmcnt(2)
D 83,1024
mav 16,128,32
R 44,44032
v_add_f32 v74, v1, v0
mav 20,132,32
ds_write_b128 v94, v[64:67] offset:0
mav 24,136,32
ds_write_b128 v95, v[68:71] offset:0
mav 28,140,32
ds_read_b128 v[64:67], v96 offset:0
v_add_f32 v74, v2, v74
mav 16,144,36
R 48,45056
v_add_f32 v74, v3, v74
mav 20,148,36
ds_read_b128 v[68:71], v96 offset:1024
v_add_f32 v74, v4, v74
mav 24,152,36
v_add_f32 v74, v5, v74
v_add_f32 v74, v6, v74
mav 28,156,36
s_waitcnt lgkmcnt(4)
s_add_u32 s60, s60, 1
v_add_f32 v74, v7, v74
mav 16,160,40
s_cbranch_scc0 LR3_1
mav 20,164,40
R 52,46080
v_add_f32 v74, v8, v74
mav 24,168,40
v_add_f32 v74, v9, v74
v_add_f32 v74, v10, v74
mav 28,172,40
v_add_f32 v74, v11, v74
v_add_f32 v74, v12, v74
mav 16,176,44
R 56,47104
v_add_f32 v74, v13, v74
mav 20,180,44
v_add_f32 v74, v14, v74
v_add_f32 v74, v15, v74
mav 24,184,44
v_add_u32 v72, 1, v72
mav 28,188,44
s_waitcnt lgkmcnt(1)
D 83,2048
mav 16,192,48
R 60,48128
mav 20,196,48
v_permlane32_swap_b32 v74, v75
mav 24,200,48
v_add_f32 v74, v74, v75
mav 28,204,48
D 83,3072
mav 16,208,52
mav 20,212,52
mav 24,216,52
mav 28,220,52
buffer_store_dwordx4 v[64:67], v91, s[36:39], s54 offen sc1
s_waitcnt lgkmcnt(0)
mav 16,224,56
mav 20,228,56
mav 24,232,56
mav 28,236,56
buffer_store_dwordx4 v[68:71], v92, s[36:39], s54 offen sc1
mav 16,240,60
mav 20,244,60
mav 24,248,60
mav 28,252,60
s_waitcnt vmcnt(16)
buffer_load_dwordx4 v[124:127], v90, s[36:39], s49 offen sc1
mvv 16,116,164
buffer_store_dword v72, v81, s[36:39], s52 offen sc1
mvv 20,120,164
s_waitcnt vmcnt(16)
mvv 24,116,168
s_xor_b32 s50, s50, 0x10000
s_sub_u32 s54, s54, 32768
mvv 28,120,168
s_waitcnt vmcnt(15)
buffer_load_dwordx4 v[128:131], v90, s[36:39], s49 offen offset:1024 sc1
mvv 16,116,172
mvv 20,120,172
s_waitcnt vmcnt(15)
v_xor_b32 v80, 0x10000, v80
mvv 24,116,176
s_waitcnt vmcnt(14)
s_add_u32 s49, s49, 313344
mvv 28,120,176
s_barrier
mvv 16,116,180
R 32,0
mvv 20,120,180
s_waitcnt vmcnt(13)
mvv 24,116,184
mvv 28,120,184
s_waitcnt vmcnt(12)
buffer_load_dwordx4 v[132:135], v86, s[36:39], s49 offen sc1
mvv 16,116,188
R 36,1024
mvv 20,120,188
s_waitcnt vmcnt(12)
mvv 24,116,192
mvv 28,120,192
s_waitcnt vmcnt(11)
buffer_load_dwordx4 v[136:139], v86, s[36:39], s49 offen offset:1024 sc1
mvv 16,116,196
R 40,2048
mvv 20,120,196
s_waitcnt vmcnt(11)
mvv 24,116,200
mvv 28,120,200
s_waitcnt lgkmcnt(1)
buffer_load_dwordx4 v[140:143], v87, s[36:39], s49 offen sc1
v_mfma_f32_16x16x32_bf16 v[0:3], a[0:3], v[32:35], v[100:103]
R 44,3072
v_cvt_pk_bf16_f32 v64, v16, v17
LsR19:
s_cbranch_execz LwR
v_mfma_f32_16x16x32_bf16 v[4:7], a[4:7], v[32:35], v[104:107]
v_cvt_pk_bf16_f32 v65, v18, v19
v_cvt_pk_bf16_f32 v66, v20, v21
v_mfma_f32_16x16x32_bf16 v[8:11], a[8:11], v[32:35], v[108:111]
v_cvt_pk_bf16_f32 v67, v22, v23
v_cvt_pk_bf16_f32 v68, v24, v25
v_mfma_f32_16x16x32_bf16 v[12:15], a[12:15], v[32:35], v[112:115]
buffer_load_dwordx4 v[144:147], v87, s[36:39], s49 offen offset:1024 sc1
mav 0,16,36
R 48,4096
v_cvt_pk_bf16_f32 v69, v26, v27
mav 4,20,36
v_cvt_pk_bf16_f32 v70, v28, v29
v_cvt_pk_bf16_f32 v71, v30, v31
mav 8,24,36
v_mul_f32 v75, v16, v16
v_fmac_f32 v75, v17, v17
mav 12,28,36
s_waitcnt lgkmcnt(1)
buffer_load_dwordx4 v[148:151], v88, s[36:39], s49 offen sc1
mav 0,32,40
R 52,5120
v_fmac_f32 v75, v18, v18
mav 4,36,40
v_fmac_f32 v75, v19, v19
v_fmac_f32 v75, v20, v20
mav 8,40,40
v_fmac_f32 v75, v21, v21
v_fmac_f32 v75, v22, v22
mav 12,44,40
buffer_load_dwordx4 v[152:155], v88, s[36:39], s49 offen offset:1024 sc1
mav 0,48,44
R 56,6144
v_fmac_f32 v75, v23, v23
mav 4,52,44
v_fmac_f32 v75, v24, v24
v_fmac_f32 v75, v25, v25
mav 8,56,44
v_fmac_f32 v75, v26, v26
v_fmac_f32 v75, v27, v27
mav 12,60,44
s_waitcnt lgkmcnt(1)
buffer_load_dwordx4 v[156:159], v89, s[36:39], s49 offen sc1
mav 0,64,48
R 60,7168
v_fmac_f32 v75, v28, v28
mav 4,68,48
v_fmac_f32 v75, v29, v29
v_fmac_f32 v75, v30, v30
mav 8,72,48
v_fmac_f32 v75, v31, v31
mav 12,76,48
buffer_load_dwordx4 v[160:163], v89, s[36:39], s49 offen offset:1024 sc1
mav 0,80,52
R 32,8192
mav 4,84,52
mav 8,88,52
s_add_u32 m0, s50, 0
mav 12,92,52
s_waitcnt lgkmcnt(1)
mav 0,96,56
R 36,9216
mav 4,100,56
mav 8,104,56
mav 12,108,56
D 83,0
mav 0,112,60
R 40,10240
mav 4,116,60
mav 8,120,60
ds_write_b32 v97, v74 offset:0
mav 12,124,60
s_waitcnt lgkmcnt(2)
D 83,1024
mav 0,128,32
R 44,11264
v_add_f32 v74, v17, v16
mav 4,132,32
ds_write_b128 v94, v[64:67] offset:0
mav 8,136,32
ds_write_b128 v95, v[68:71] offset:0
mav 12,140,32
ds_read_b128 v[64:67], v96 offset:0
v_add_f32 v74, v18, v74
mav 0,144,36
R 48,12288
v_add_f32 v74, v19, v74
mav 4,148,36
ds_read_b128 v[68:71], v96 offset:1024
v_add_f32 v74, v20, v74
mav 8,152,36
v_add_f32 v74, v21, v74
v_add_f32 v74, v22, v74
mav 12,156,36
s_waitcnt lgkmcnt(4)
v_add_f32 v74, v23, v74
v_add_f32 v74, v24, v74
mav 0,160,40
R 52,13312
v_add_f32 v74, v25, v74
mav 4,164,40
v_add_f32 v74, v26, v74
v_add_f32 v74, v27, v74
mav 8,168,40
v_add_f32 v74, v28, v74
v_add_f32 v74, v29, v74
mav 12,172,40
v_add_f32 v74, v30, v74
v_add_f32 v74, v31, v74
mav 0,176,44
R 56,14336
v_add_u32 v72, 1, v72
mav 4,180,44
v_permlane32_swap_b32 v74, v75
mav 8,184,44
v_add_f32 v74, v74, v75
mav 12,188,44
s_waitcnt lgkmcnt(1)
D 83,2048
mav 0,192,48
R 60,15360
mav 4,196,48
mav 8,200,48
mav 12,204,48
D 83,3072
mav 0,208,52
mav 4,212,52
mav 8,216,52
mav 12,220,52
buffer_store_dwordx4 v[64:67], v91, s[36:39], s54 offen sc1
s_waitcnt lgkmcnt(0)
mav 0,224,56
mav 4,228,56
mav 8,232,56
mav 12,236,56
buffer_store_dwordx4 v[68:71], v92, s[36:39], s54 offen sc1
mav 0,240,60
mav 4,244,60
mav 8,248,60
mav 12,252,60
s_waitcnt vmcnt(16)
buffer_load_dwordx4 v[164:167], v90, s[36:39], s49 offen sc1
mvv 0,116,124
buffer_store_dword v72, v81, s[36:39], s52 offen sc1
mvv 4,120,124
s_waitcnt vmcnt(16)
mvv 8,116,128
s_sub_u32 s54, s54, 3178496
mvv 12,120,128
s_waitcnt vmcnt(15)
buffer_load_dwordx4 v[168:171], v90, s[36:39], s49 offen offset:1024 sc1
mvv 0,116,132
mvv 4,120,132
s_waitcnt vmcnt(15)
mvv 8,116,136
s_waitcnt vmcnt(14)
s_add_u32 s49, s49, 313344
mvv 12,120,136
s_barrier
mvv 0,116,140
R 32,32768
mvv 4,120,140
s_waitcnt vmcnt(13)
mvv 8,116,144
mvv 12,120,144
s_waitcnt vmcnt(12)
buffer_load_dwordx4 v[172:175], v86, s[36:39], s49 offen sc1
mvv 0,116,148
R 36,33792
mvv 4,120,148
s_waitcnt vmcnt(12)
mvv 8,116,152
mvv 12,120,152
s_waitcnt vmcnt(11)
buffer_load_dwordx4 v[176:179], v86, s[36:39], s49 offen offset:1024 sc1
mvv 0,116,156
R 40,34816
mvv 4,120,156
s_waitcnt vmcnt(11)
mvv 8,116,160
mvv 12,120,160
s_waitcnt lgkmcnt(1)
buffer_load_dwordx4 v[180:183], v87, s[36:39], s49 offen sc1
v_mfma_f32_16x16x32_bf16 v[16:19], a[0:3], v[32:35], v[100:103]
R 44,35840
v_cvt_pk_bf16_f32 v64, v0, v1
v_mfma_f32_16x16x32_bf16 v[20:23], a[4:7], v[32:35], v[104:107]
v_cvt_pk_bf16_f32 v65, v2, v3
v_cvt_pk_bf16_f32 v66, v4, v5
v_mfma_f32_16x16x32_bf16 v[24:27], a[8:11], v[32:35], v[108:111]
v_cvt_pk_bf16_f32 v67, v6, v7
v_cvt_pk_bf16_f32 v68, v8, v9
v_mfma_f32_16x16x32_bf16 v[28:31], a[12:15], v[32:35], v[112:115]
buffer_load_dwordx4 v[184:187], v87, s[36:39], s49 offen offset:1024 sc1
mav 16,16,36
R 48,36864
v_cvt_pk_bf16_f32 v69, v10, v11
mav 20,20,36
v_cvt_pk_bf16_f32 v70, v12, v13
v_cvt_pk_bf16_f32 v71, v14, v15
mav 24,24,36
mav 28,28,36
s_waitcnt lgkmcnt(1)
buffer_load_dwordx4 v[188:191], v88, s[36:39], s49 offen sc1
mav 16,32,40
R 52,37888
mav 20,36,40
mav 24,40,40
mav 28,44,40
buffer_load_dwordx4 v[192:195], v88, s[36:39], s49 offen offset:1024 sc1
mav 16,48,44
R 56,38912
mav 20,52,44
mav 24,56,44
mav 28,60,44
s_waitcnt lgkmcnt(1)
buffer_load_dwordx4 v[196:199], v89, s[36:39], s49 offen sc1
mav 16,64,48
R 60,39936
mav 20,68,48
mav 24,72,48
mav 28,76,48
buffer_load_dwordx4 v[200:203], v89, s[36:39], s49 offen offset:1024 sc1
mav 16,80,52
R 32,40960
mav 20,84,52
mav 24,88,52
s_add_u32 m0, s50, 32768
mav 28,92,52
s_waitcnt lgkmcnt(1)
mav 16,96,56
R 36,41984
mav 20,100,56
mav 24,104,56
mav 28,108,56
D 83,0
mav 16,112,60
R 40,43008
mav 20,116,60
mav 24,120,60
ds_write_b32 v97, v74 offset:1024
mav 28,124,60
s_waitcnt lgkmcnt(2)
D 83,1024
mav 16,128,32
R 44,44032
mav 20,132,32
ds_write_b128 v94, v[64:67] offset:0
mav 24,136,32
ds_write_b128 v95, v[68:71] offset:0
mav 28,140,32
ds_read_b128 v[64:67], v96 offset:0
mav 16,144,36
R 48,45056
mav 20,148,36
ds_read_b128 v[68:71], v96 offset:1024
mav 24,152,36
mav 28,156,36
s_waitcnt lgkmcnt(4)
mav 16,160,40
mav 20,164,40
R 52,46080
v_add_u32 v72, 69, v72
mav 24,168,40
mav 28,172,40
mav 16,176,44
R 56,47104
mav 20,180,44
mav 24,184,44
mav 28,188,44
s_waitcnt lgkmcnt(1)
D 83,2048
mav 16,192,48
R 60,48128
mav 20,196,48
mav 24,200,48
mav 28,204,48
D 83,3072
mav 16,208,52
mav 20,212,52
mav 24,216,52
mav 28,220,52
buffer_store_dwordx4 v[64:67], v91, s[44:47], s54 offen sc1
s_waitcnt lgkmcnt(0)
mav 16,224,56
mav 20,228,56
mav 24,232,56
mav 28,236,56
buffer_store_dwordx4 v[68:71], v92, s[44:47], s54 offen sc1
mav 16,240,60
mav 20,244,60
mav 24,248,60
mav 28,252,60
s_waitcnt vmcnt(16)
buffer_load_dwordx4 v[124:127], v90, s[36:39], s49 offen sc1
mvv 16,116,164
buffer_store_dword v72, v81, s[36:39], s52 offen sc1
mvv 20,120,164
s_waitcnt vmcnt(16)
mvv 24,116,168
s_xor_b32 s50, s50, 0x10000
s_sub_u32 s54, s54, 32768
mvv 28,120,168
s_waitcnt vmcnt(15)
buffer_load_dwordx4 v[128:131], v90, s[36:39], s49 offen offset:1024 sc1
mvv 16,116,172
mvv 20,120,172
s_waitcnt vmcnt(15)
v_xor_b32 v80, 0x10000, v80
mvv 24,116,176
s_waitcnt vmcnt(14)
s_add_u32 s49, s49, 313344
mvv 28,120,176
s_barrier
mvv 16,116,180
R 32,0
mvv 20,120,180
s_waitcnt vmcnt(13)
mvv 24,116,184
mvv 28,120,184
s_waitcnt vmcnt(12)
buffer_load_dwordx4 v[132:135], v86, s[36:39], s49 offen sc1
mvv 16,116,188
R 36,1024
mvv 20,120,188
s_waitcnt vmcnt(12)
mvv 24,116,192
mvv 28,120,192
s_waitcnt vmcnt(11)
buffer_load_dwordx4 v[136:139], v86, s[36:39], s49 offen offset:1024 sc1
mvv 16,116,196
R 40,2048
mvv 20,120,196
s_waitcnt vmcnt(11)
mvv 24,116,200
mvv 28,120,200
s_waitcnt lgkmcnt(1)
buffer_load_dwordx4 v[140:143], v87, s[36:39], s49 offen sc1
v_mfma_f32_16x16x32_bf16 v[0:3], a[0:3], v[32:35], v[100:103]
R 44,3072
v_cvt_pk_bf16_f32 v64, v16, v17
v_mfma_f32_16x16x32_bf16 v[4:7], a[4:7], v[32:35], v[104:107]
v_cvt_pk_bf16_f32 v65, v18, v19
v_cvt_pk_bf16_f32 v66, v20, v21
v_mfma_f32_16x16x32_bf16 v[8:11], a[8:11], v[32:35], v[108:111]
v_cvt_pk_bf16_f32 v67, v22, v23
v_cvt_pk_bf16_f32 v68, v24, v25
v_mfma_f32_16x16x32_bf16 v[12:15], a[12:15], v[32:35], v[112:115]
buffer_load_dwordx4 v[144:147], v87, s[36:39], s49 offen offset:1024 sc1
mav 0,16,36
R 48,4096
v_cvt_pk_bf16_f32 v69, v26, v27
mav 4,20,36
v_cvt_pk_bf16_f32 v70, v28, v29
v_cvt_pk_bf16_f32 v71, v30, v31
mav 8,24,36
mav 12,28,36
s_waitcnt lgkmcnt(1)
buffer_load_dwordx4 v[148:151], v88, s[36:39], s49 offen sc1
mav 0,32,40
R 52,5120
mav 4,36,40
mav 8,40,40
mav 12,44,40
buffer_load_dwordx4 v[152:155], v88, s[36:39], s49 offen offset:1024 sc1
mav 0,48,44
R 56,6144
mav 4,52,44
mav 8,56,44
mav 12,60,44
s_waitcnt lgkmcnt(1)
buffer_load_dwordx4 v[156:159], v89, s[36:39], s49 offen sc1
mav 0,64,48
R 60,7168
mav 4,68,48
mav 8,72,48
mav 12,76,48
buffer_load_dwordx4 v[160:163], v89, s[36:39], s49 offen offset:1024 sc1
mav 0,80,52
R 32,8192
mav 4,84,52
mav 8,88,52
s_add_u32 m0, s50, 0
mav 12,92,52
s_waitcnt lgkmcnt(1)
mav 0,96,56
R 36,9216
mav 4,100,56
mav 8,104,56
mav 12,108,56
D 83,0
mav 0,112,60
R 40,10240
mav 4,116,60
mav 8,120,60
mav 12,124,60
s_waitcnt lgkmcnt(1)
D 83,1024
mav 0,128,32
R 44,11264
mav 4,132,32
ds_write_b128 v94, v[64:67] offset:0
mav 8,136,32
ds_write_b128 v95, v[68:71] offset:0
mav 12,140,32
ds_read_b128 v[64:67], v96 offset:0
mav 0,144,36
R 48,12288
mav 4,148,36
ds_read_b128 v[68:71], v96 offset:1024
mav 8,152,36
mav 12,156,36
s_waitcnt lgkmcnt(4)
mav 0,160,40
R 52,13312
mav 4,164,40
mav 8,168,40
mav 12,172,40
mav 0,176,44
R 56,14336
mav 4,180,44
mav 8,184,44
mav 12,188,44
s_waitcnt lgkmcnt(1)
D 83,2048
)"
R"(
mav 0,192,48
R 60,15360
mav 4,196,48
mav 8,200,48
mav 12,204,48
D 83,3072
mav 0,208,52
mav 4,212,52
mav 8,216,52
mav 12,220,52
buffer_store_dwordx4 v[64:67], v91, s[44:47], s54 offen sc1
s_waitcnt lgkmcnt(0)
mav 0,224,56
mav 4,228,56
mav 8,232,56
mav 12,236,56
buffer_store_dwordx4 v[68:71], v92, s[44:47], s54 offen sc1
mav 0,240,60
mav 4,244,60
mav 8,248,60
mav 12,252,60
s_waitcnt vmcnt(16)
buffer_load_dwordx4 v[164:167], v90, s[36:39], s49 offen sc1
mvv 0,116,124
mvv 4,120,124
s_waitcnt vmcnt(15)
mvv 8,116,128
s_sub_u32 s54, s54, 32768
mvv 12,120,128
s_waitcnt vmcnt(14)
buffer_load_dwordx4 v[168:171], v90, s[36:39], s49 offen offset:1024 sc1
mvv 0,116,132
mvv 4,120,132
s_waitcnt vmcnt(14)
mvv 8,116,136
s_waitcnt vmcnt(13)
s_add_u32 s49, s49, 313344
mvv 12,120,136
s_barrier
mvv 0,116,140
R 32,32768
mvv 4,120,140
s_waitcnt vmcnt(12)
mvv 8,116,144
mvv 12,120,144
s_waitcnt vmcnt(11)
buffer_load_dwordx4 v[172:175], v86, s[36:39], s49 offen sc1
mvv 0,116,148
R 36,33792
mvv 4,120,148
s_waitcnt vmcnt(11)
mvv 8,116,152
mvv 12,120,152
s_waitcnt vmcnt(10)
buffer_load_dwordx4 v[176:179], v86, s[36:39], s49 offen offset:1024 sc1
mvv 0,116,156
R 40,34816
mvv 4,120,156
s_waitcnt vmcnt(10)
mvv 8,116,160
mvv 12,120,160
s_waitcnt lgkmcnt(1)
buffer_load_dwordx4 v[180:183], v87, s[36:39], s49 offen sc1
v_mfma_f32_16x16x32_bf16 v[16:19], a[0:3], v[32:35], v[100:103]
R 44,35840
v_cvt_pk_bf16_f32 v64, v0, v1
v_mfma_f32_16x16x32_bf16 v[20:23], a[4:7], v[32:35], v[104:107]
v_cvt_pk_bf16_f32 v65, v2, v3
v_cvt_pk_bf16_f32 v66, v4, v5
v_mfma_f32_16x16x32_bf16 v[24:27], a[8:11], v[32:35], v[108:111]
v_cvt_pk_bf16_f32 v67, v6, v7
v_cvt_pk_bf16_f32 v68, v8, v9
v_mfma_f32_16x16x32_bf16 v[28:31], a[12:15], v[32:35], v[112:115]
buffer_load_dwordx4 v[184:187], v87, s[36:39], s49 offen offset:1024 sc1
mav 16,16,36
R 48,36864
v_cvt_pk_bf16_f32 v69, v10, v11
mav 20,20,36
v_cvt_pk_bf16_f32 v70, v12, v13
v_cvt_pk_bf16_f32 v71, v14, v15
mav 24,24,36
mav 28,28,36
s_waitcnt lgkmcnt(1)
buffer_load_dwordx4 v[188:191], v88, s[36:39], s49 offen sc1
mav 16,32,40
R 52,37888
mav 20,36,40
mav 24,40,40
mav 28,44,40
buffer_load_dwordx4 v[192:195], v88, s[36:39], s49 offen offset:1024 sc1
mav 16,48,44
R 56,38912
mav 20,52,44
mav 24,56,44
mav 28,60,44
s_waitcnt lgkmcnt(1)
buffer_load_dwordx4 v[196:199], v89, s[36:39], s49 offen sc1
mav 16,64,48
R 60,39936
mav 20,68,48
mav 24,72,48
mav 28,76,48
buffer_load_dwordx4 v[200:203], v89, s[36:39], s49 offen offset:1024 sc1
mav 16,80,52
R 32,40960
mav 20,84,52
mav 24,88,52
s_add_u32 m0, s50, 32768
mav 28,92,52
s_waitcnt lgkmcnt(1)
mav 16,96,56
R 36,41984
mav 20,100,56
mav 24,104,56
mav 28,108,56
D 83,0
mav 16,112,60
R 40,43008
mav 20,116,60
mav 24,120,60
mav 28,124,60
s_waitcnt lgkmcnt(1)
D 83,1024
mav 16,128,32
R 44,44032
mav 20,132,32
ds_write_b128 v94, v[64:67] offset:0
mav 24,136,32
ds_write_b128 v95, v[68:71] offset:0
mav 28,140,32
ds_read_b128 v[64:67], v96 offset:0
mav 16,144,36
R 48,45056
mav 20,148,36
ds_read_b128 v[68:71], v96 offset:1024
LsR20:
s_cbranch_execz LwR
mav 24,152,36
mav 28,156,36
s_waitcnt lgkmcnt(4)
mav 16,160,40
s_mov_b32 s60, -13
.p2align 6
LR3_2:
mav 20,164,40
R 52,46080
mav 24,168,40
mav 28,172,40
mav 16,176,44
R 56,47104
mav 20,180,44
mav 24,184,44
mav 28,188,44
s_waitcnt lgkmcnt(1)
D 83,2048
mav 16,192,48
R 60,48128
mav 20,196,48
mav 24,200,48
mav 28,204,48
D 83,3072
mav 16,208,52
mav 20,212,52
mav 24,216,52
mav 28,220,52
buffer_store_dwordx4 v[64:67], v91, s[44:47], s54 offen sc1
s_waitcnt lgkmcnt(0)
mav 16,224,56
mav 20,228,56
mav 24,232,56
mav 28,236,56
buffer_store_dwordx4 v[68:71], v92, s[44:47], s54 offen sc1
mav 16,240,60
mav 20,244,60
mav 24,248,60
mav 28,252,60
s_waitcnt vmcnt(15)
buffer_load_dwordx4 v[124:127], v90, s[36:39], s49 offen sc1
mvv 16,116,164
mvv 20,120,164
s_waitcnt vmcnt(15)
mvv 24,116,168
s_xor_b32 s50, s50, 0x10000
s_sub_u32 s54, s54, 32768
mvv 28,120,168
s_waitcnt vmcnt(14)
buffer_load_dwordx4 v[128:131], v90, s[36:39], s49 offen offset:1024 sc1
mvv 16,116,172
mvv 20,120,172
s_waitcnt vmcnt(14)
v_xor_b32 v80, 0x10000, v80
mvv 24,116,176
s_waitcnt vmcnt(13)
s_add_u32 s49, s49, 313344
mvv 28,120,176
s_barrier
mvv 16,116,180
R 32,0
mvv 20,120,180
s_waitcnt vmcnt(12)
mvv 24,116,184
mvv 28,120,184
s_waitcnt vmcnt(11)
buffer_load_dwordx4 v[132:135], v86, s[36:39], s49 offen sc1
mvv 16,116,188
R 36,1024
mvv 20,120,188
s_waitcnt vmcnt(11)
mvv 24,116,192
mvv 28,120,192
s_waitcnt vmcnt(10)
buffer_load_dwordx4 v[136:139], v86, s[36:39], s49 offen offset:1024 sc1
mvv 16,116,196
R 40,2048
mvv 20,120,196
s_waitcnt vmcnt(10)
mvv 24,116,200
mvv 28,120,200
s_waitcnt lgkmcnt(1)
buffer_load_dwordx4 v[140:143], v87, s[36:39], s49 offen sc1
v_mfma_f32_16x16x32_bf16 v[0:3], a[0:3], v[32:35], v[100:103]
R 44,3072
v_cvt_pk_bf16_f32 v64, v16, v17
v_mfma_f32_16x16x32_bf16 v[4:7], a[4:7], v[32:35], v[104:107]
v_cvt_pk_bf16_f32 v65, v18, v19
v_cvt_pk_bf16_f32 v66, v20, v21
v_mfma_f32_16x16x32_bf16 v[8:11], a[8:11], v[32:35], v[108:111]
v_cvt_pk_bf16_f32 v67, v22, v23
v_cvt_pk_bf16_f32 v68, v24, v25
v_mfma_f32_16x16x32_bf16 v[12:15], a[12:15], v[32:35], v[112:115]
buffer_load_dwordx4 v[144:147], v87, s[36:39], s49 offen offset:1024 sc1
mav 0,16,36
R 48,4096
v_cvt_pk_bf16_f32 v69, v26, v27
mav 4,20,36
v_cvt_pk_bf16_f32 v70, v28, v29
v_cvt_pk_bf16_f32 v71, v30, v31
mav 8,24,36
mav 12,28,36
s_waitcnt lgkmcnt(1)
buffer_load_dwordx4 v[148:151], v88, s[36:39], s49 offen sc1
mav 0,32,40
R 52,5120
mav 4,36,40
mav 8,40,40
mav 12,44,40
buffer_load_dwordx4 v[152:155], v88, s[36:39], s49 offen offset:1024 sc1
mav 0,48,44
R 56,6144
mav 4,52,44
mav 8,56,44
mav 12,60,44
s_waitcnt lgkmcnt(1)
buffer_load_dwordx4 v[156:159], v89, s[36:39], s49 offen sc1
mav 0,64,48
R 60,7168
mav 4,68,48
mav 8,72,48
mav 12,76,48
buffer_load_dwordx4 v[160:163], v89, s[36:39], s49 offen offset:1024 sc1
mav 0,80,52
R 32,8192
mav 4,84,52
mav 8,88,52
s_add_u32 m0, s50, 0
mav 12,92,52
s_waitcnt lgkmcnt(1)
mav 0,96,56
R 36,9216
mav 4,100,56
mav 8,104,56
mav 12,108,56
D 83,0
mav 0,112,60
R 40,10240
mav 4,116,60
mav 8,120,60
mav 12,124,60
s_waitcnt lgkmcnt(1)
D 83,1024
mav 0,128,32
R 44,11264
mav 4,132,32
ds_write_b128 v94, v[64:67] offset:0
mav 8,136,32
ds_write_b128 v95, v[68:71] offset:0
mav 12,140,32
ds_read_b128 v[64:67], v96 offset:0
mav 0,144,36
R 48,12288
mav 4,148,36
ds_read_b128 v[68:71], v96 offset:1024
mav 8,152,36
mav 12,156,36
s_waitcnt lgkmcnt(4)
mav 0,160,40
R 52,13312
mav 4,164,40
mav 8,168,40
mav 12,172,40
mav 0,176,44
R 56,14336
mav 4,180,44
mav 8,184,44
mav 12,188,44
s_waitcnt lgkmcnt(1)
D 83,2048
mav 0,192,48
R 60,15360
mav 4,196,48
mav 8,200,48
mav 12,204,48
D 83,3072
mav 0,208,52
mav 4,212,52
mav 8,216,52
mav 12,220,52
buffer_store_dwordx4 v[64:67], v91, s[44:47], s54 offen sc1
s_waitcnt lgkmcnt(0)
mav 0,224,56
mav 4,228,56
mav 8,232,56
mav 12,236,56
buffer_store_dwordx4 v[68:71], v92, s[44:47], s54 offen sc1
mav 0,240,60
mav 4,244,60
mav 8,248,60
mav 12,252,60
s_waitcnt vmcnt(15)
buffer_load_dwordx4 v[164:167], v90, s[36:39], s49 offen sc1
mvv 0,116,124
mvv 4,120,124
s_waitcnt vmcnt(15)
mvv 8,116,128
s_sub_u32 s54, s54, 32768
mvv 12,120,128
s_waitcnt vmcnt(14)
buffer_load_dwordx4 v[168:171], v90, s[36:39], s49 offen offset:1024 sc1
mvv 0,116,132
mvv 4,120,132
s_waitcnt vmcnt(14)
mvv 8,116,136
s_waitcnt vmcnt(13)
s_add_u32 s49, s49, 313344
mvv 12,120,136
s_barrier
mvv 0,116,140
R 32,32768
mvv 4,120,140
s_waitcnt vmcnt(12)
mvv 8,116,144
mvv 12,120,144
s_waitcnt vmcnt(11)
buffer_load_dwordx4 v[172:175], v86, s[36:39], s49 offen sc1
mvv 0,116,148
R 36,33792
mvv 4,120,148
s_waitcnt vmcnt(11)
mvv 8,116,152
mvv 12,120,152
s_waitcnt vmcnt(10)
buffer_load_dwordx4 v[176:179], v86, s[36:39], s49 offen offset:1024 sc1
mvv 0,116,156
R 40,34816
mvv 4,120,156
s_waitcnt vmcnt(10)
mvv 8,116,160
mvv 12,120,160
s_waitcnt lgkmcnt(1)
buffer_load_dwordx4 v[180:183], v87, s[36:39], s49 offen sc1
v_mfma_f32_16x16x32_bf16 v[16:19], a[0:3], v[32:35], v[100:103]
R 44,35840
v_cvt_pk_bf16_f32 v64, v0, v1
v_mfma_f32_16x16x32_bf16 v[20:23], a[4:7], v[32:35], v[104:107]
v_cvt_pk_bf16_f32 v65, v2, v3
v_cvt_pk_bf16_f32 v66, v4, v5
v_mfma_f32_16x16x32_bf16 v[24:27], a[8:11], v[32:35], v[108:111]
v_cvt_pk_bf16_f32 v67, v6, v7
v_cvt_pk_bf16_f32 v68, v8, v9
v_mfma_f32_16x16x32_bf16 v[28:31], a[12:15], v[32:35], v[112:115]
buffer_load_dwordx4 v[184:187], v87, s[36:39], s49 offen offset:1024 sc1
mav 16,16,36
R 48,36864
v_cvt_pk_bf16_f32 v69, v10, v11
mav 20,20,36
v_cvt_pk_bf16_f32 v70, v12, v13
v_cvt_pk_bf16_f32 v71, v14, v15
mav 24,24,36
mav 28,28,36
s_waitcnt lgkmcnt(1)
buffer_load_dwordx4 v[188:191], v88, s[36:39], s49 offen sc1
mav 16,32,40
R 52,37888
mav 20,36,40
mav 24,40,40
mav 28,44,40
buffer_load_dwordx4 v[192:195], v88, s[36:39], s49 offen offset:1024 sc1
mav 16,48,44
R 56,38912
mav 20,52,44
mav 24,56,44
mav 28,60,44
s_waitcnt lgkmcnt(1)
buffer_load_dwordx4 v[196:199], v89, s[36:39], s49 offen sc1
mav 16,64,48
R 60,39936
mav 20,68,48
mav 24,72,48
mav 28,76,48
buffer_load_dwordx4 v[200:203], v89, s[36:39], s49 offen offset:1024 sc1
mav 16,80,52
R 32,40960
mav 20,84,52
mav 24,88,52
s_add_u32 m0, s50, 32768
mav 28,92,52
s_waitcnt lgkmcnt(1)
mav 16,96,56
R 36,41984
mav 20,100,56
mav 24,104,56
mav 28,108,56
D 83,0
mav 16,112,60
R 40,43008
mav 20,116,60
mav 24,120,60
mav 28,124,60
s_waitcnt lgkmcnt(1)
D 83,1024
mav 16,128,32
R 44,44032
mav 20,132,32
ds_write_b128 v94, v[64:67] offset:0
mav 24,136,32
ds_write_b128 v95, v[68:71] offset:0
mav 28,140,32
ds_read_b128 v[64:67], v96 offset:0
mav 16,144,36
R 48,45056
mav 20,148,36
ds_read_b128 v[68:71], v96 offset:1024
mav 24,152,36
mav 28,156,36
s_waitcnt lgkmcnt(4)
s_add_u32 s60, s60, 1
mav 16,160,40
s_cbranch_scc0 LR3_2
mav 20,164,40
R 52,46080
mav 24,168,40
mav 28,172,40
mav 16,176,44
R 56,47104
mav 20,180,44
mav 24,184,44
mav 28,188,44
s_waitcnt lgkmcnt(1)
D 83,2048
mav 16,192,48
R 60,48128
mav 20,196,48
mav 24,200,48
mav 28,204,48
D 83,3072
mav 16,208,52
mav 20,212,52
mav 24,216,52
mav 28,220,52
buffer_store_dwordx4 v[64:67], v91, s[44:47], s54 offen sc1
s_waitcnt lgkmcnt(0)
mav 16,224,56
mav 20,228,56
mav 24,232,56
mav 28,236,56
buffer_store_dwordx4 v[68:71], v92, s[44:47], s54 offen sc1
mav 16,240,60
mav 20,244,60
mav 24,248,60
mav 28,252,60
s_waitcnt vmcnt(15)
buffer_load_dwordx4 v[124:127], v90, s[36:39], s49 offen sc1
mvv 16,116,164
mvv 20,120,164
s_waitcnt vmcnt(15)
mvv 24,116,168
s_sub_u32 s54, s54, 32768
mvv 28,120,168
s_waitcnt vmcnt(14)
buffer_load_dwordx4 v[128:131], v90, s[36:39], s49 offen offset:1024 sc1
mvv 16,116,172
mvv 20,120,172
s_waitcnt vmcnt(14)
v_xor_b32 v80, 0x10000, v80
mvv 24,116,176
s_waitcnt vmcnt(13)
s_add_u32 s49, s49, 313344
mvv 28,120,176
s_barrier
mvv 16,116,180
R 32,0
mvv 20,120,180
s_waitcnt vmcnt(12)
mvv 24,116,184
mvv 28,120,184
s_waitcnt vmcnt(11)
buffer_load_dwordx4 v[132:135], v86, s[36:39], s49 offen sc1
mvv 16,116,188
R 36,1024
mvv 20,120,188
s_waitcnt vmcnt(11)
mvv 24,116,192
mvv 28,120,192
s_waitcnt vmcnt(10)
buffer_load_dwordx4 v[136:139], v86, s[36:39], s49 offen offset:1024 sc1
mvv 16,116,196
R 40,2048
mvv 20,120,196
s_waitcnt vmcnt(10)
mvv 24,116,200
mvv 28,120,200
s_waitcnt lgkmcnt(1)
buffer_load_dwordx4 v[140:143], v87, s[36:39], s49 offen sc1
v_mfma_f32_16x16x32_bf16 v[0:3], a[0:3], v[32:35], v[100:103]
R 44,3072
v_cvt_pk_bf16_f32 v64, v16, v17
v_mfma_f32_16x16x32_bf16 v[4:7], a[4:7], v[32:35], v[104:107]
v_cvt_pk_bf16_f32 v65, v18, v19
v_cvt_pk_bf16_f32 v66, v20, v21
v_mfma_f32_16x16x32_bf16 v[8:11], a[8:11], v[32:35], v[108:111]
v_cvt_pk_bf16_f32 v67, v22, v23
v_cvt_pk_bf16_f32 v68, v24, v25
v_mfma_f32_16x16x32_bf16 v[12:15], a[12:15], v[32:35], v[112:115]
buffer_load_dwordx4 v[144:147], v87, s[36:39], s49 offen offset:1024 sc1
mav 0,16,36
R 48,4096
v_cvt_pk_bf16_f32 v69, v26, v27
mav 4,20,36
v_cvt_pk_bf16_f32 v70, v28, v29
v_cvt_pk_bf16_f32 v71, v30, v31
mav 8,24,36
mav 12,28,36
s_waitcnt lgkmcnt(1)
buffer_load_dwordx4 v[148:151], v88, s[36:39], s49 offen sc1
mav 0,32,40
R 52,5120
mav 4,36,40
mav 8,40,40
mav 12,44,40
buffer_load_dwordx4 v[152:155], v88, s[36:39], s49 offen offset:1024 sc1
mav 0,48,44
R 56,6144
mav 4,52,44
mav 8,56,44
mav 12,60,44
s_waitcnt lgkmcnt(1)
buffer_load_dwordx4 v[156:159], v89, s[36:39], s49 offen sc1
mav 0,64,48
R 60,7168
mav 4,68,48
mav 8,72,48
mav 12,76,48
buffer_load_dwordx4 v[160:163], v89, s[36:39], s49 offen offset:1024 sc1
mav 0,80,52
R 32,8192
mav 4,84,52
mav 8,88,52
mav 12,92,52
s_waitcnt lgkmcnt(1)
mav 0,96,56
R 36,9216
mav 4,100,56
mav 8,104,56
mav 12,108,56
mav 0,112,60
R 40,10240
mav 4,116,60
mav 8,120,60
mav 12,124,60
s_waitcnt lgkmcnt(1)
ds_write_b128 v94, v[64:67] offset:0
mav 0,128,32
R 44,11264
mav 4,132,32
ds_write_b128 v95, v[68:71] offset:0
mav 8,136,32
ds_read_b128 v[64:67], v96 offset:0
mav 12,140,32
ds_read_b128 v[68:71], v96 offset:1024
mav 0,144,36
R 48,12288
mav 4,148,36
mav 8,152,36
mav 12,156,36
s_waitcnt lgkmcnt(3)
mav 0,160,40
R 52,13312
mav 4,164,40
mav 8,168,40
mav 12,172,40
mav 0,176,44
R 56,14336
mav 4,180,44
mav 8,184,44
mav 12,188,44
s_waitcnt lgkmcnt(1)
mav 0,192,48
R 60,15360
mav 4,196,48
mav 8,200,48
mav 12,204,48
mav 0,208,52
mav 4,212,52
mav 8,216,52
mav 12,220,52
buffer_store_dwordx4 v[64:67], v91, s[44:47], s54 offen sc1
s_waitcnt lgkmcnt(0)
mav 0,224,56
mav 4,228,56
mav 8,232,56
mav 12,236,56
buffer_store_dwordx4 v[68:71], v92, s[44:47], s54 offen sc1
mav 0,240,60
mav 4,244,60
mav 8,248,60
mav 12,252,60
s_waitcnt vmcnt(11)
buffer_load_dwordx4 v[164:167], v90, s[36:39], s49 offen sc1
mvv 0,116,124
mvv 4,120,124
s_waitcnt vmcnt(11)
mvv 8,116,128
s_sub_u32 s54, s54, 32768
mvv 12,120,128
s_waitcnt vmcnt(10)
buffer_load_dwordx4 v[168:171], v90, s[36:39], s49 offen offset:1024 sc1
mvv 0,116,132
mvv 4,120,132
s_waitcnt vmcnt(10)
mvv 8,116,136
s_waitcnt vmcnt(9)
s_add_u32 s49, s49, 313344
mvv 12,120,136
s_barrier
mvv 0,116,140
R 32,32768
mvv 4,120,140
s_waitcnt vmcnt(8)
mvv 8,116,144
mvv 12,120,144
s_waitcnt vmcnt(7)
buffer_load_dwordx4 v[172:175], v86, s[36:39], s49 offen sc1
mvv 0,116,148
R 36,33792
mvv 4,120,148
s_waitcnt vmcnt(7)
mvv 8,116,152
mvv 12,120,152
s_waitcnt vmcnt(6)
buffer_load_dwordx4 v[176:179], v86, s[36:39], s49 offen offset:1024 sc1
mvv 0,116,156
R 40,34816
mvv 4,120,156
s_waitcnt vmcnt(6)
mvv 8,116,160
mvv 12,120,160
s_waitcnt lgkmcnt(1)
buffer_load_dwordx4 v[180:183], v87, s[36:39], s49 offen sc1
v_mfma_f32_16x16x32_bf16 v[16:19], a[0:3], v[32:35], v[100:103]
R 44,35840
v_cvt_pk_bf16_f32 v64, v0, v1
v_mfma_f32_16x16x32_bf16 v[20:23], a[4:7], v[32:35], v[104:107]
v_cvt_pk_bf16_f32 v65, v2, v3
v_cvt_pk_bf16_f32 v66, v4, v5
v_mfma_f32_16x16x32_bf16 v[24:27], a[8:11], v[32:35], v[108:111]
v_cvt_pk_bf16_f32 v67, v6, v7
v_cvt_pk_bf16_f32 v68, v8, v9
v_mfma_f32_16x16x32_bf16 v[28:31], a[12:15], v[32:35], v[112:115]
buffer_load_dwordx4 v[184:187], v87, s[36:39], s49 offen offset:1024 sc1
mav 16,16,36
R 48,36864
v_cvt_pk_bf16_f32 v69, v10, v11
mav 20,20,36
ds_write_b128 v94, v[64:67] offset:0
mav 24,24,36
LsR21:
s_cbranch_execz LwR
v_cvt_pk_bf16_f32 v70, v12, v13
v_cvt_pk_bf16_f32 v71, v14, v15
mav 28,28,36
s_waitcnt lgkmcnt(2)
buffer_load_dwordx4 v[188:191], v88, s[36:39], s49 offen sc1
mav 16,32,40
R 52,37888
mav 20,36,40
ds_write_b128 v95, v[68:71] offset:0
mav 24,40,40
ds_read_b128 v[64:67], v96 offset:0
mav 28,44,40
buffer_load_dwordx4 v[192:195], v88, s[36:39], s49 offen offset:1024 sc1
mav 16,48,44
R 56,38912
mav 20,52,44
ds_read_b128 v[68:71], v96 offset:1024
mav 24,56,44
mav 28,60,44
s_waitcnt lgkmcnt(3)
buffer_load_dwordx4 v[196:199], v89, s[36:39], s49 offen sc1
mav 16,64,48
R 60,39936
mav 20,68,48
s_waitcnt lgkmcnt(3)
mav 24,72,48
mav 28,76,48
buffer_store_dwordx4 v[64:67], v91, s[44:47], s54 offen sc1
buffer_load_dwordx4 v[200:203], v89, s[36:39], s49 offen offset:1024 sc1
mav 16,80,52
s_waitcnt lgkmcnt(1)
R 32,40960
mav 20,84,52
mav 24,88,52
mav 28,92,52
buffer_store_dwordx4 v[68:71], v92, s[44:47], s54 offen sc1
mav 16,96,56
R 36,41984
mav 20,100,56
mav 24,104,56
mav 28,108,56
s_waitcnt lgkmcnt(1)
mav 16,112,60
R 40,43008
mav 20,116,60
mav 24,120,60
mav 28,124,60
mav 16,128,32
R 44,44032
mav 20,132,32
mav 24,136,32
mav 28,140,32
s_waitcnt lgkmcnt(1)
mav 16,144,36
R 48,45056
mav 20,148,36
mav 24,152,36
mav 28,156,36
mav 16,160,40
mav 20,164,40
R 52,46080
mav 24,168,40
mav 28,172,40
s_waitcnt lgkmcnt(1)
mav 16,176,44
R 56,47104
mav 20,180,44
mav 24,184,44
mav 28,188,44
mav 16,192,48
R 60,48128
mav 20,196,48
mav 24,200,48
mav 28,204,48
s_waitcnt lgkmcnt(1)
mav 16,208,52
mav 20,212,52
mav 24,216,52
mav 28,220,52
mav 16,224,56
mav 20,228,56
mav 24,232,56
mav 28,236,56
s_waitcnt lgkmcnt(0)
mav 16,240,60
mav 20,244,60
mav 24,248,60
mav 28,252,60
s_waitcnt vmcnt(11)
mvv 16,116,164
mvv 20,120,164
s_waitcnt vmcnt(10)
mvv 24,116,168
s_sub_u32 s54, s54, 32768
mvv 28,120,168
s_waitcnt vmcnt(9)
mvv 16,116,172
mvv 20,120,172
s_waitcnt vmcnt(8)
mvv 24,116,176
mvv 28,120,176
s_waitcnt vmcnt(7)
mvv 16,116,180
mvv 20,120,180
s_waitcnt vmcnt(6)
mvv 24,116,184
mvv 28,120,184
s_waitcnt vmcnt(5)
mvv 16,116,188
mvv 20,120,188
s_waitcnt vmcnt(4)
mvv 24,116,192
mvv 28,120,192
s_waitcnt vmcnt(3)
mvv 16,116,196
mvv 20,120,196
s_waitcnt vmcnt(1)
mvv 24,116,200
mvv 28,120,200
s_nop 3
v_cvt_pk_bf16_f32 v64, v16, v17
v_cvt_pk_bf16_f32 v65, v18, v19
v_cvt_pk_bf16_f32 v66, v20, v21
v_cvt_pk_bf16_f32 v67, v22, v23
ds_write_b128 v94, v[64:67] offset:0
v_cvt_pk_bf16_f32 v68, v24, v25
v_cvt_pk_bf16_f32 v69, v26, v27
v_cvt_pk_bf16_f32 v70, v28, v29
v_cvt_pk_bf16_f32 v71, v30, v31
ds_write_b128 v95, v[68:71] offset:0
ds_read_b128 v[64:67], v96 offset:0
ds_read_b128 v[68:71], v96 offset:1024
s_waitcnt lgkmcnt(0)
buffer_store_dwordx4 v[64:67], v91, s[44:47], s54 offen sc1
buffer_store_dwordx4 v[68:71], v92, s[44:47], s54 offen sc1
s_endpgm
Ct:
v_lshrrev_b32 v156, 5, v80
v_mul_u32_u24 v156, 96, v156
v_add_u32 v156, v80, v156
Dt:
s_mov_b32 s60, 4096
buffer_load_dwordx4 v[124:127], v156, s[36:39], s60 offen sc1
s_add_u32 s60, s60, 4096
buffer_load_dwordx4 v[128:131], v156, s[36:39], s60 offen sc1
s_add_u32 s60, s60, 4096
buffer_load_dwordx4 v[132:135], v156, s[36:39], s60 offen sc1
s_add_u32 s60, s60, 4096
buffer_load_dwordx4 v[136:139], v156, s[36:39], s60 offen sc1
s_add_u32 s60, s60, 4096
buffer_load_dwordx4 v[140:143], v156, s[36:39], s60 offen sc1
s_add_u32 s60, s60, 4096
buffer_load_dwordx4 v[144:147], v156, s[36:39], s60 offen sc1
s_add_u32 s60, s60, 4096
buffer_load_dwordx4 v[148:151], v156, s[36:39], s60 offen sc1
s_add_u32 s60, s60, 4096
buffer_load_dwordx4 v[152:155], v156, s[36:39], s60 offen sc1
s_add_u32 s60, s60, 4096
s_mov_b64 s[66:67], 0
s_waitcnt vmcnt(0)
v_sub_u32 v124, v124, v72
v_xor_b32 v125, v125, v73
v_cmp_lt_u64 vcc, s[62:63], v[124:125]
s_or_b64 s[66:67], s[66:67], vcc
v_sub_u32 v126, v126, v72
v_xor_b32 v127, v127, v73
v_cmp_lt_u64 vcc, s[62:63], v[126:127]
s_or_b64 s[66:67], s[66:67], vcc
v_sub_u32 v128, v128, v72
v_xor_b32 v129, v129, v73
v_cmp_lt_u64 vcc, s[62:63], v[128:129]
s_or_b64 s[66:67], s[66:67], vcc
v_sub_u32 v130, v130, v72
v_xor_b32 v131, v131, v73
v_cmp_lt_u64 vcc, s[62:63], v[130:131]
s_or_b64 s[66:67], s[66:67], vcc
v_sub_u32 v132, v132, v72
v_xor_b32 v133, v133, v73
v_cmp_lt_u64 vcc, s[62:63], v[132:133]
s_or_b64 s[66:67], s[66:67], vcc
v_sub_u32 v134, v134, v72
v_xor_b32 v135, v135, v73
v_cmp_lt_u64 vcc, s[62:63], v[134:135]
s_or_b64 s[66:67], s[66:67], vcc
v_sub_u32 v136, v136, v72
v_xor_b32 v137, v137, v73
v_cmp_lt_u64 vcc, s[62:63], v[136:137]
s_or_b64 s[66:67], s[66:67], vcc
v_sub_u32 v138, v138, v72
v_xor_b32 v139, v139, v73
v_cmp_lt_u64 vcc, s[62:63], v[138:139]
s_or_b64 s[66:67], s[66:67], vcc
v_sub_u32 v140, v140, v72
v_xor_b32 v141, v141, v73
v_cmp_lt_u64 vcc, s[62:63], v[140:141]
s_or_b64 s[66:67], s[66:67], vcc
v_sub_u32 v142, v142, v72
v_xor_b32 v143, v143, v73
v_cmp_lt_u64 vcc, s[62:63], v[142:143]
s_or_b64 s[66:67], s[66:67], vcc
v_sub_u32 v144, v144, v72
v_xor_b32 v145, v145, v73
v_cmp_lt_u64 vcc, s[62:63], v[144:145]
s_or_b64 s[66:67], s[66:67], vcc
v_sub_u32 v146, v146, v72
v_xor_b32 v147, v147, v73
v_cmp_lt_u64 vcc, s[62:63], v[146:147]
s_or_b64 s[66:67], s[66:67], vcc
v_sub_u32 v148, v148, v72
v_xor_b32 v149, v149, v73
v_cmp_lt_u64 vcc, s[62:63], v[148:149]
s_or_b64 s[66:67], s[66:67], vcc
v_sub_u32 v150, v150, v72
v_xor_b32 v151, v151, v73
v_cmp_lt_u64 vcc, s[62:63], v[150:151]
s_or_b64 s[66:67], s[66:67], vcc
v_sub_u32 v152, v152, v72
v_xor_b32 v153, v153, v73
v_cmp_lt_u64 vcc, s[62:63], v[152:153]
s_or_b64 s[66:67], s[66:67], vcc
v_sub_u32 v154, v154, v72
v_xor_b32 v155, v155, v73
v_cmp_lt_u64 vcc, s[62:63], v[154:155]
s_or_b64 s[66:67], s[66:67], vcc
s_cbranch_scc0 Et
s_cmp_eq_u32 s61, 0
s_cbranch_scc1 Et
s_sub_u32 s61, s61, 1
s_sleep 64
s_branch Dt
Et:
buffer_store_dword v73, v81, s[36:39], 0 offen sc1
s_branch Bt
Lfin:
.purgem mav
.purgem Ta
.purgem mva
.purgem mvv
.purgem Tv
.purgem R
.purgem D
.purgem D2
.purgem E
)"
    : "+{v[0:7]}"(v0), "+{v[80:95]}"(c0), "+{v[96:111]}"(c1), "+{v[112:127]}"(c2), "+{s[36:51]}"(s0), "+{s[52:59]}"(s1)
    :
    : "v8", "v9", "v10", "v11", "v12", "v13", "v14", "v15", "v16", "v17", "v18", "v19", "v20", "v21", "v22", "v23", "v24", "v25", "v26", "v27", "v28", "v29", "v30", "v31", "v32", "v33", "v34", "v35", "v36", "v37", "v38", "v39", "v40", "v41", "v42", "v43", "v44", "v45", "v46", "v47", "v48", "v49", "v50", "v51", "v52", "v53", "v54", "v55", "v56", "v57", "v58", "v59", "v60", "v61", "v62", "v63", "v64", "v65", "v66", "v67", "v68", "v69", "v70", "v71", "v72", "v73", "v74", "v75", "v76", "v77", "v78", "v79", "v128", "v129", "v130", "v131", "v132", "v133", "v134", "v135", "v136", "v137", "v138", "v139", "v140", "v141", "v142", "v143", "v144", "v145", "v146", "v147", "v148", "v149", "v150", "v151", "v152", "v153", "v154", "v155", "v156", "v157", "v158", "v159", "v160", "v161", "v162", "v163", "v164", "v165", "v166", "v167", "v168", "v169", "v170", "v171", "v172", "v173", "v174", "v175", "v176", "v177", "v178", "v179", "v180", "v181", "v182", "v183", "v184", "v185", "v186", "v187", "v188", "v189", "v190", "v191", "v192", "v193", "v194", "v195", "v196", "v197", "v198", "v199", "v200", "v201", "v202", "v203", "v204", "v205", "v206", "v207", "v208", "v209", "v210", "v211", "v212", "v213", "v214", "v215", "v216", "v217", "v218", "v219", "v220", "v221", "v222", "v223", "v224", "v225", "v226", "v227", "v228", "v229", "v230", "v231", "v232", "v233", "v234", "v235", "v236", "v237", "v238", "v239", "v240", "v241", "v242", "v243", "v244", "v245", "v246", "v247", "v248", "v249", "v250", "v251", "v252", "v253", "v254", "v255", "a0", "a1", "a2", "a3", "a4", "a5", "a6", "a7", "a8", "a9", "a10", "a11", "a12", "a13", "a14", "a15", "a16", "a17", "a18", "a19", "a20", "a21", "a22", "a23", "a24", "a25", "a26", "a27", "a28", "a29", "a30", "a31", "a32", "a33", "a34", "a35", "a36", "a37", "a38", "a39", "a40", "a41", "a42", "a43", "a44", "a45", "a46", "a47", "a48", "a49", "a50", "a51", "a52", "a53", "a54", "a55", "a56", "a57", "a58", "a59", "a60", "a61", "a62", "a63", "a64", "a65", "a66", "a67", "a68", "a69", "a70", "a71", "a72", "a73", "a74", "a75", "a76", "a77", "a78", "a79", "a80", "a81", "a82", "a83", "a84", "a85", "a86", "a87", "a88", "a89", "a90", "a91", "a92", "a93", "a94", "a95", "a96", "a97", "a98", "a99", "a100", "a101", "a102", "a103", "a104", "a105", "a106", "a107", "a108", "a109", "a110", "a111", "a112", "a113", "a114", "a115", "a116", "a117", "a118", "a119", "a120", "a121", "a122", "a123", "a124", "a125", "a126", "a127", "a128", "a129", "a130", "a131", "a132", "a133", "a134", "a135", "a136", "a137", "a138", "a139", "a140", "a141", "a142", "a143", "a144", "a145", "a146", "a147", "a148", "a149", "a150", "a151", "a152", "a153", "a154", "a155", "a156", "a157", "a158", "a159", "a160", "a161", "a162", "a163", "a164", "a165", "a166", "a167", "a168", "a169", "a170", "a171", "a172", "a173", "a174", "a175", "a176", "a177", "a178", "a179", "a180", "a181", "a182", "a183", "a184", "a185", "a186", "a187", "a188", "a189", "a190", "a191", "a192", "a193", "a194", "a195", "a196", "a197", "a198", "a199", "a200", "a201", "a202", "a203", "a204", "a205", "a206", "a207", "a208", "a209", "a210", "a211", "a212", "a213", "a214", "a215", "a216", "a217", "a218", "a219", "a220", "a221", "a222", "a223", "a224", "a225", "a226", "a227", "a228", "a229", "a230", "a231", "a232", "a233", "a234", "a235", "a236", "a237", "a238", "a239", "a240", "a241", "a242", "a243", "a244", "a245", "a246", "a247", "a248", "a249", "a250", "a251", "a252", "a253", "a254", "a255", "s60", "s61", "s62", "s63", "s64", "s65", "s66", "s67", "s68", "s69", "s70", "s71", "s72", "s73", "s74", "s75", "s76", "s77", "s78", "s79", "s80", "s81", "s82", "s83", "s84", "s85", "s86", "s87", "s88", "s89", "s90", "s91", "s92", "s93", "s94", "s95", "s96", "s97", "s98", "s99", "vcc", "scc", "memory");
}

extern "C" void kernel_launch(void* const* d_in, const int* in_sizes, int n_in, void* d_out, int out_size,
                              void* d_ws, size_t ws_size, hipStream_t stream) {
  Args m{};
  m.x = (const u16*)d_in[0]; m.wqkv = (const u16*)d_in[1]; m.wo = (const u16*)d_in[2]; m.w1 = (const u16*)d_in[4];
  m.b1 = (const u16*)d_in[5]; m.w2 = (const u16*)d_in[6]; m.b2 = (const u16*)d_in[7];
  m.out = (u16*)d_out; m.ws = (char*)d_ws;
  const unsigned tab[64] = {0x22022000u, 0x3814b010u, 0x26062404u, 0x781cf018u, 0x2a0a2808u, 0xb8943090u, 0x2e0e2c0cu, 0xf89c7098u, 0x62426040u, 0x3954b150u, 0x66466444u, 0x795cf158u, 0x6a4a6848u, 0xb9d431d0u, 0x6e4e6c4cu, 0xf9dc71d8u, 0xa282a080u, 0x3a15b211u, 0xa686a484u, 0x7a1df219u, 0xaa8aa888u, 0xba953291u, 0xae8eac8cu, 0xfa9d7299u, 0xe2c2e0c0u, 0x3b55b351u, 0xe6c6e4c4u, 0x7b5df359u, 0xeacae8c8u, 0xbbd533d1u, 0xeeceecccu, 0xfbdd73d9u, 0x23032101u, 0x7c16b412u, 0x27072505u, 0x3c1ef41au, 0x2b0b2909u, 0xfc963492u, 0x2f0f2d0du, 0xbc9e749au, 0x63436141u, 0x7d56b552u, 0x67476545u, 0x3d5ef55au, 0x6b4b6949u, 0xfdd635d2u, 0x6f4f6d4du, 0xbdde75dau, 0xa383a181u, 0x7e17b613u, 0xa787a585u, 0x3e1ff61bu, 0xab8ba989u, 0xfe973693u, 0xaf8fad8du, 0xbe9f769bu, 0xe3c3e1c1u, 0x7f57b753u, 0xe7c7e5c5u, 0x3f5ff75bu, 0xebcbe9c9u, 0xffd737d3u, 0xefcfedcdu, 0xbfdf77dbu};
  for (int i = 0; i < 64; ++i) m.tab[i] = tab[i];
  (void)hipFuncSetAttribute((const void*)df_kernel, hipFuncAttributeMaxDynamicSharedMemorySize, LDS_BYTES);
  hipLaunchKernelGGL(df_kernel, dim3(256), dim3(256), LDS_BYTES, stream, m);
}
```

```cpp
#include <hip/hip_runtime.h>
#include <stdint.h>

typedef __attribute__((ext_vector_type(16))) unsigned u32x16;
typedef __attribute__((ext_vector_type(8))) unsigned u32x8;
typedef __attribute__((ext_vector_type(4))) unsigned u32x4;
typedef __attribute__((ext_vector_type(2))) unsigned u32x2;
typedef __attribute__((ext_vector_type(2))) __bf16 bf2_t;
typedef unsigned short u16;
typedef __amdgpu_buffer_rsrc_t rsrc_t;
typedef __attribute__((address_space(3))) void lds_t;

constexpr unsigned FLAGS = 4096, RB = 8388608, REC = 313344, SX = 311296, WS_END = 88604672, DBG = 1048576;
constexpr int LDS_BYTES = 163840;
#define NEGB 0xff61b1e6u
#define FW(p, s, w) (FLAGS + (unsigned)(((p) * 64 + (s)) * 128 + (w) * 8))
#define blk0(s) (RB + (unsigned)(p * 64 + (s) - 2) * REC)
#define TA(seg, hf, sub) (unsigned)((seg) * 4096 + (8 * lg + (li >> 2)) * 128 + ((((hf) * 4 + (li & 3)) ^ ((li >> 1) & 4)) << 4) + (sub))
#define TO(seg, hf, half) (unsigned)((seg) * 4096 + (8 * lg + (li >> 2)) * 128 + ((((hf) * 4 + (half) * 2 + ((li & 3) >> 1)) ^ ((li >> 1) & 4)) << 4) + (li & 1) * 8)
#define BIAS(ptr, n, i) ((unsigned)__builtin_amdgcn_raw_buffer_load_b16(__builtin_amdgcn_make_buffer_rsrc((void*)(ptr), 0, (n) * 2, 0x00020000), (i) * 2, 0, 0) << 16)
#define XRD (unsigned)((li & 7) * 4128 + (li >> 3) * 2048 + lg * 16)

struct Args {
  const u16 *x, *wqkv, *wo, *w1, *w2;
  char* ws;
  u16* out;
  const u16 *b1, *b2;
  unsigned tab[64];
};

__device__ __forceinline__ float rows_sum(float v) {
  const auto a = __builtin_amdgcn_permlane16_swap(__float_as_uint(v), __float_as_uint(v), false, false);
  const float m = __uint_as_float(a[0]) + __uint_as_float(a[1]);
  const auto b = __builtin_amdgcn_permlane32_swap(__float_as_uint(m), __float_as_uint(m), false, false);
  return __uint_as_float(b[0]) + __uint_as_float(b[1]);
}

__device__ __forceinline__ const u16* arg_b2() {
  auto ka = (const __attribute__((address_space(4))) Args*)__builtin_amdgcn_kernarg_segment_ptr();
  asm volatile("" : "+s"(ka));
  return ka->b2;
}

__global__ __launch_bounds__(256, 1) void df_kernel(Args m) {
  const int w = __builtin_amdgcn_readfirstlane(threadIdx.x >> 6);
  const int bid = blockIdx.x, xc = bid & 7, j_ = bid >> 3;
  const int te_ = ((const __attribute__((address_space(4))) Args*)__builtin_amdgcn_kernarg_segment_ptr())->tab[(j_ & 28) * 2 + xc] >> ((j_ & 3) * 8), slot = te_ & 63, p = (te_ >> 6) & 3;
  {
    const unsigned tid = threadIdx.x;
    lds_t* const sink = (lds_t*)(size_t)(65536 + w * 1024);
    if (w & 1) {
      unsigned long long pc; unsigned o = tid < 112 ? (tid & 63) * 128 : (tid < 128 ? tid - 48 : tid & 63) * 4096 + j_ * 128;
      asm volatile("s_mov_b32 m0, %2\n\ts_getpc_b64 %0\nLbeg:\n\tv_min_u32 %1, Lfin - Lbeg + 60, %1\n\tglobal_load_lds_dword %1, %0" : "=&s"(pc), "+v"(o) : "s"(65536 + w * 1024) : "memory");
    }
    {
      const u32x4 rs = {(unsigned)(size_t)m.ws, (unsigned)((size_t)m.ws >> 32) & 0xffffu, WS_END, 0x00020000};
      asm volatile("s_nop 4\n\tbuffer_load_dword a255, off, %0, 0 sc1" :: "s"(rs) : "a255", "memory");
    }
    const int ts = slot, tp = p;
    if (ts >> 4 == 2) {
      const rsrc_t rT = __builtin_amdgcn_make_buffer_rsrc((void*)m.wqkv, 0, 1024 * 3072 * 2, 0x00020000);
      const rsrc_t rX = __builtin_amdgcn_make_buffer_rsrc((void*)m.x, 0, 2048 * 1024 * 2, 0x00020000);
      const unsigned t = ((slot & 3) * 4 + p) * 256 + tid;
#pragma unroll
      for (int i = 0; i < 3; ++i) {
        __builtin_amdgcn_raw_ptr_buffer_load_lds(rT, sink, 4, (t >> 2) * 2048 + (t & ((1 << 2) - 1)) * 128, (ts & 12) * 128 + i * (2048 * 3072 / 3), 0, 0);
        if (i < 1) __builtin_amdgcn_raw_ptr_buffer_load_lds(rX, sink, 4, (t >> 10) * 1048576 + (t & 1023) * 128, 28 * 32768, 0, 0);
      }
    }
    if (0 && ts >= 56) {
      const rsrc_t rU = __builtin_amdgcn_make_buffer_rsrc((void*)m.wo, 0, 1024 * 1024 * 2, 0x00020000);
#pragma unroll
      for (int i = 0; i < 4; ++i) __builtin_amdgcn_raw_ptr_buffer_load_lds(rU, sink, 4, ((ts - 56) * 256 + tid) * 128, ((tp * 1 * 2 + 4 + i) & 7) * 262144, 0, 0);
    }
    if (1 && (j_ & 3) == 3) {
      const rsrc_t rS = __builtin_amdgcn_make_buffer_rsrc((void*)m.ws, 0, WS_END, 0x00020000);
      const rsrc_t rO = __builtin_amdgcn_make_buffer_rsrc((void*)m.out, 0, 4 << 20, 0x00020000);
      const unsigned t = (j_ >> 2) * 256 + tid;
      if (1 & 1) __builtin_amdgcn_raw_ptr_buffer_load_lds(rS, sink, 4, t < 64 ? t << 21 : 0x80000000u, 0, 0, 16);
      if (1 & 2) __builtin_amdgcn_raw_ptr_buffer_load_lds(rS, sink, 4, t << 16, 0, 0, 16);
      if (1 & 4) __builtin_amdgcn_raw_ptr_buffer_load_lds(rS, sink, 4, (t >> 9) * (64 * REC) + (t & 511) * 4096, RB, 0, 16);
      if (1 & 3) __builtin_amdgcn_raw_ptr_buffer_load_lds(rO, sink, 4, t << (1 & 2 ? 16 : 21) & 0x7fffffffu, 0, 0, 16);
    }
    if (slot < 16) {
      const rsrc_t rW = __builtin_amdgcn_make_buffer_rsrc((void*)m.wqkv, 0, 1024 * 3072 * 2, 0x00020000);
      const rsrc_t rX = __builtin_amdgcn_make_buffer_rsrc((void*)m.x, 0, 2048 * 1024 * 2, 0x00020000);
      const unsigned ln = tid & 63, vw = (ln >> 3) * 6144 + (((ln & 7) ^ ((ln >> 2) & 4)) << 4), vx = (ln >> 4) * 2048 + (((ln & 15) ^ ((ln >> 4) * 4)) << 4);
#pragma unroll
      for (int g = 0; g < 1; ++g) {
#pragma unroll
        for (int i = 0; i < 6; ++i) {
          __builtin_amdgcn_raw_ptr_buffer_load_lds(rW, (lds_t*)(size_t)(36864 + (3 - g) * 24576 + i * 4096 + w * 1024), 16, vw, w * 8 * 6144 + slot * 128 + (2 * g + i / 3) * 32 * 6144 + i % 3 * 2048, 0, 0);
          if (!(g & 1) && (i == 1 || i == 3))
            __builtin_amdgcn_raw_ptr_buffer_load_lds(rX, (lds_t*)(size_t)(w * 8208 + 2048 + (i >> 1) * 4096 + g * 512), 16, vx, (p * 32 + 31 - (i >> 1)) * 32768 + w * 8192 + g * 128, 0, 0);
        }
      }
    }
  }
  const int si = slot >> 4 == 1 ? slot & 15 : -1;
  if (si >= 0) {
    int lane;
    asm volatile("v_mbcnt_lo_u32_b32 %0, -1, 0\n\tv_mbcnt_hi_u32_b32 %0, -1, %0" : "=v"(lane));
    const int lg = lane >> 4, li = lane & 15;
    const rsrc_t rS = __builtin_amdgcn_make_buffer_rsrc((void*)m.ws, 0, WS_END, 0x00020000);
    const rsrc_t rX = __builtin_amdgcn_make_buffer_rsrc((void*)m.x, 0, 2048 * 1024 * 2, 0x00020000);
    const bf2_t one2 = {(__bf16)1.0f, (__bf16)1.0f};
    const unsigned go = lg * 2048 + w * 512 + ((li ^ (lg * 4)) << 4), lb = 73728 + w * 16512, lo = lb + (li >> 2) * 2064 + (li & 3) * 256 + lg * 16;
#pragma unroll
    for (int i = 0; i < 16; ++i)
      __builtin_amdgcn_raw_ptr_buffer_load_lds(rX, (lds_t*)(size_t)(lb + (i >> 1) * 2064 + (i & 1) * 1024), 16, go + ((i >> 1) * 8192 + (i & 1) * 256), (p * 16 + 15 - si) * 65536, 0, 0);
    asm volatile("s_waitcnt vmcnt(0)" ::: "memory");
#pragma unroll
    for (int b = 0; b < 2; ++b) {
      float s = 0.f, q = 0.f;
#pragma unroll
      for (int k = 0; k < 8; ++k) {
        const u32x4 v = *(const __attribute__((address_space(3))) u32x4*)(size_t)(lo + b * 8256 + (k >> 2) * 1024 + (((k ^ li) & 3) << 6));
#pragma unroll
        for (int e = 0; e < 4; ++e) {
          const unsigned xe = v[e];
          const bf2_t x = __builtin_bit_cast(bf2_t, xe);
          s = __builtin_amdgcn_fdot2_f32_bf16(x, one2, s, false);
          q = __builtin_amdgcn_fdot2_f32_bf16(x, x, q, false);
        }
      }
      s = rows_sum(s); q = rows_sum(q);
      __builtin_amdgcn_raw_buffer_store_b64((u32x2){__float_as_uint(s), __float_as_uint(q)}, rS, lg ? 0x80000000u : w * 128 + li * 8 + SX, RB + (unsigned)(p * 64 + si * 2 + 1 - b) * REC, 16);
      if (0 && si == 1) {
        unsigned tg;
        asm volatile("v_accvgpr_read_b32 %0, a255" : "=v"(tg));
        tg += 0x9e3779b9u;
        __builtin_amdgcn_raw_buffer_store_b128((u32x4){__float_as_uint(s), tg, __float_as_uint(q), tg}, rS, lg ? 0x80000000u : w * 256 + li * 16, 196608 + p * 4096 + (1 - b) * 2048, 16);
      }
    }
  }
  const int lane = threadIdx.x & 63, lg = lane >> 4, li = lane & 15;
  const unsigned l16 = lane * 16;
  u32x16 c0 = 0, c1 = 0, c2 = 0, s0 = 0;
  u32x8 v0 = 0, s1 = 0;
  const u16 *wp, *op = m.out; unsigned wn, on = 4 << 20;
  const u16 *rQp = m.wqkv, *rOp = m.wo, *r1p = m.w1, *r2p = m.w2;
  constexpr unsigned rQn = 1024 * 3072 * 2, rOn = 1024 * 1024 * 2, r1n = 1024 * 3584 * 2, r2n = r1n;
  const int rot = p * 1, wr = (w + rot) & 3;
  if (slot < 16 && !(w & 1)) {
    s0[12] = 0 + (w >> 1); wp = rQp; wn = rQn;
    op = m.x - 512 * 1024; on = 5 << 20;
    v0[3] = (p * 32 + 62) * 32768 + (w * 4 + lg) * 2048 + ((li ^ (lg * 4)) << 4); v0[7] = 2048 + (li >> 2) * 8208 + (li & 3) * 256 + lg * 16;
    s0[13] = blk0(0) + 0u + (slot * 2 + (w >> 1)) * 1024 - (133120u + (w >> 1) * 2048);
    s0[15] = w * 8 * 6144 + slot * 128 + 393216;
    s1[2] = 262144u + (p * 32 + slot * 2 + (w >> 1)) * 4096 - (133120u + (w >> 1) * 2048);
    s1[3] = p ? 262144u + ((p - 1) * 32 + slot * 2 + (w >> 1)) * 4096 - (133120u + (w >> 1) * 2048) : 0x80000000u;
    s1[4] = p ? 0u : NEGB;
    v0[0] = (lane >> 3) * 6144 + (((lane & 7) ^ ((lane >> 2) & 4)) << 4) + 0u;
    v0[1] = (lane >> 3) * 6144 + (((lane & 7) ^ ((lane >> 2) & 4)) << 4) + 2048u;
    v0[2] = (lane >> 3) * 6144 + (((lane & 7) ^ ((lane >> 2) & 4)) << 4) + 4096u;
    v0[4] = TA(0, w >> 1, 0);
    v0[5] = TA(0, w >> 1, 8);
    v0[6] = TA(2, w >> 1, 0);
    c0[0] = 66048u + XRD;
    c0[1] = (66048u + XRD) ^ XRD;
    c0[2] = 133120u + (w >> 1) * 2048 + l16;
    c0[3] = 141312u + li * 8;
    c0[4] = 141312u + lg * 32;
    c0[6] = (((4 * lg + 0) ^ li) & 1) == 0 && (4 * lg + 0 <= li) ? 0u : NEGB;
    c0[7] = (((4 * lg + 1) ^ li) & 1) == 0 && (4 * lg + 1 <= li) ? 0u : NEGB;
    c0[8] = (((4 * lg + 2) ^ li) & 1) == 0 && (4 * lg + 2 <= li) ? 0u : NEGB;
    c0[9] = (((4 * lg + 3) ^ li) & 1) == 0 && (4 * lg + 3 <= li) ? 0u : NEGB;
    c0[10] = (((4 * lg + 0) ^ li) & 1) == 0 && (4 * lg + 0 > li) ? 0u : NEGB;
    c0[11] = (((4 * lg + 1) ^ li) & 1) == 0 && (4 * lg + 1 > li) ? 0u : NEGB;
    c0[12] = (((4 * lg + 2) ^ li) & 1) == 0 && (4 * lg + 2 > li) ? 0u : NEGB;
    c0[13] = (((4 * lg + 3) ^ li) & 1) == 0 && (4 * lg + 3 > li) ? 0u : NEGB;
    c0[14] = (4 * lg + 0 + (li & 1) <= li) ? ((li & 1) ? 0xffff0000u : 0xffffu) : 0u;
    c0[15] = (4 * lg + 2 + (li & 1) <= li) ? ((li & 1) ? 0xffff0000u : 0xffffu) : 0u;
    c1[0] = (4 * lg + 0 + (li & 1) > li) ? ((li & 1) ? 0xffff0000u : 0xffffu) : 0u;
    c1[1] = (4 * lg + 2 + (li & 1) > li) ? ((li & 1) ? 0xffff0000u : 0xffffu) : 0u;
    c1[2] = 133120u + (w >> 1) * 2048 + lane * 8;
  } else if (slot < 16) {
    s0[12] = 4 + (w >> 1); wp = rQp; wn = rQn;
    op = m.x - 512 * 1024; on = 5 << 20;
    v0[3] = (p * 32 + 62) * 32768 + (w * 4 + lg) * 2048 + ((li ^ (lg * 4)) << 4); v0[7] = 2048 + (li >> 2) * 8208 + (li & 3) * 256 + lg * 16;
    s0[13] = blk0(0);
    s0[14] = 66048 + (w >> 1) * 4128;
    s0[15] = w * 8 * 6144 + slot * 128 + 393216;
    s1[2] = (p * 32 + 61) * 32768;
    s1[3] = (p * 32 + 61) * 32768 + 14336;
    s1[4] = (p * 32 + 32) * 32768;
    s1[5] = (66048 + (w >> 1) * 4128) ^ ((w >> 1) * 4128);
    v0[0] = (lane >> 3) * 6144 + (((lane & 7) ^ ((lane >> 2) & 4)) << 4) + 0u;
    v0[1] = (lane >> 3) * 6144 + (((lane & 7) ^ ((lane >> 2) & 4)) << 4) + 2048u;
    v0[2] = (lane >> 3) * 6144 + (((lane & 7) ^ ((lane >> 2) & 4)) << 4) + 4096u;
    v0[4] = TA(1, w >> 1, 0);
    v0[5] = TA(1, w >> 1, 8);
    v0[6] = TA(2, w >> 1, 8);
    c0[0] = 66048u + XRD;
    c0[1] = (66048u + XRD) ^ XRD;
    c0[2] = lane ? 0x80000000u : 0u;
    c0[3] = 144384u + lane * 4;
    c0[4] = 133120u + (w >> 1) * 2048 + l16;
    c0[5] = 141312u + li * 8;
    c0[6] = 141312u + lg * 32;
    c0[7] = (0 + (w >> 1)) * 2048 + l16;
    c0[8] = (2 + (w >> 1)) * 2048 + l16;
    c0[9] = (4 + (w >> 1)) * 2048 + l16;
    c0[10] = (6 + (w >> 1)) * 2048 + l16;
    c0[11] = (lane >> 1) == 24 && p ? FW(p - 1, 48 + (slot & 7), 2) : FW(p, 60 + ((lane & 15) >> 2), lane & 3);
    c0[12] = lg == 1 ? FW(p, 16 + li, 0) : (lane >> 1) == 24 && p ? FW(p - 1, 48 + (slot & 7), 2) : FW(p, 60 + ((lane & 15) >> 2), lane & 3);
    c0[13] = (unsigned)-26;
    c0[14] = 937984u + lane * 8;
  } else if (slot >> 3 == 6) {
    s0[12] = 8 + (w); wp = rOp; wn = rOn;
    op = m.x - 512 * 1024; on = 5 << 20;
    s0[13] = blk0(-2);
    s0[14] = w * 8192;
    s0[15] = w * 8 * 2048 + (slot - 48) * 256 + rot * 524288;
    s1[2] = (p * 32 + 63) * 32768;
    s1[4] = (p * 32 + 32) * 32768;
    s1[5] = w * 8 * 2048 + (slot - 48) * 256 + 2097152u;
    v0[0] = (lane >> 3) * 2048 + (((lane & 7) ^ ((lane >> 2) & 4)) << 4) + 0u;
    v0[1] = (lane >> 3) * 2048 + (((lane & 7) ^ ((lane >> 2) & 4)) << 4) + 128u;
    v0[4] = TA(w >> 1, w & 1, 0);
    v0[5] = TA(w >> 1, w & 1, 8);
    c0[0] = l16;
    c0[1] = lane ? 0x80000000u : 0u;
    c0[2] = 144384u + lane * 4;
    c0[3] = 659456u + ((slot - 48) * 4 + w) * 1024 + l16;
    c0[4] = 940032u + wr * 8192 + l16;
    c0[5] = 944128u + wr * 8192 + l16;
    c0[6] = FW(p, (lane & 31) >> 1, (lane & 1) * 2);
    c0[7] = 1u;
    c0[8] = li * 2048 + ((slot - 48) * 4 + w) * 64 + lg * 16;
    c0[9] = 142336u + ((li * 2 + (lg >> 1)) * 8 + w * 2 + (lg & 1)) * 4;
    c0[10] = 142336u + l16;
    c0[11] = (lane & 1) ? 0x80000000u : 625664u + (slot - 48) * 128 + (lane >> 1) * 4;
  } else if (slot < 32) {
    s0[12] = 12 + (w); wp = r1p; wn = r1n;
    s0[13] = blk0(-2);
    s0[14] = w * 8192;
    s0[15] = w * 8 * 7168 + (slot - 16) * 384 + rot * 1835008;
    s1[5] = w * 8 * 7168 + (slot - 16) * 384 + 7340032u;
    v0[0] = (lane >> 3) * 7168 + (((lane & 7) ^ ((lane >> 2) & 4)) << 4) + 0u;
    v0[1] = (lane >> 3) * 7168 + (((lane & 7) ^ ((lane >> 2) & 4)) << 4) + 128u;
    v0[2] = (lane >> 3) * 7168 + (((lane & 7) ^ ((lane >> 2) & 4)) << 4) + 256u;
    v0[4] = TA(((w >> 1) * 3 + (w & 1)) >> 1, ((w >> 1) * 3 + (w & 1)) & 1, 0);
    v0[5] = TA(((w >> 1) * 3 + (w & 1)) >> 1, ((w >> 1) * 3 + (w & 1)) & 1, 8);
    v0[6] = TO(((w >> 1) * 3 + 2) >> 1, ((w >> 1) * 3 + 2) & 1, w & 1);
    c0[0] = l16;
    c0[1] = lane ? 0x80000000u : 0u;
    c0[2] = 144384u + lane * 4;
    c0[3] = 378880u + ((slot - 16) * 6 + (w >> 1) * 3 + (w & 1)) * 1024 + l16;
    c0[4] = !(lg & 1) ? 378880u + ((slot - 16) * 6 + (w >> 1) * 3 + 2) * 1024 + ((w & 1) * 2 + (lg >> 1)) * 256 + li * 16 : 0x80000000u;
    c0[5] = 972800u + wr * 8192 + l16;
    c0[6] = 976896u + wr * 8192 + l16;
    c0[7] = FW(p, 48 + ((lane & 31) >> 2), lane & 3);
    c0[8] = 1u;
    c0[9] = 141312u + li * 8;
    c0[10] = 939008u + lane * 8;
    c0[12] = BIAS(m.b1, 3584, ((slot - 16) * 6 + (w >> 1) * 3 + (w & 1)) * 32 + lg * 8 + 0);
    c0[13] = BIAS(m.b1, 3584, ((slot - 16) * 6 + (w >> 1) * 3 + (w & 1)) * 32 + lg * 8 + 1);
    c0[14] = BIAS(m.b1, 3584, ((slot - 16) * 6 + (w >> 1) * 3 + (w & 1)) * 32 + lg * 8 + 2);
    c0[15] = BIAS(m.b1, 3584, ((slot - 16) * 6 + (w >> 1) * 3 + (w & 1)) * 32 + lg * 8 + 3);
    c1[0] = BIAS(m.b1, 3584, ((slot - 16) * 6 + (w >> 1) * 3 + (w & 1)) * 32 + lg * 8 + 4);
    c1[1] = BIAS(m.b1, 3584, ((slot - 16) * 6 + (w >> 1) * 3 + (w & 1)) * 32 + lg * 8 + 5);
    c1[2] = BIAS(m.b1, 3584, ((slot - 16) * 6 + (w >> 1) * 3 + (w & 1)) * 32 + lg * 8 + 6);
    c1[3] = BIAS(m.b1, 3584, ((slot - 16) * 6 + (w >> 1) * 3 + (w & 1)) * 32 + lg * 8 + 7);
    c1[4] = BIAS(m.b1, 3584, ((slot - 16) * 6 + (w >> 1) * 3 + 2) * 32 + (w & 1) * 16 + lg * 4 + 0);
    c1[5] = BIAS(m.b1, 3584, ((slot - 16) * 6 + (w >> 1) * 3 + 2) * 32 + (w & 1) * 16 + lg * 4 + 1);
    c1[6] = BIAS(m.b1, 3584, ((slot - 16) * 6 + (w >> 1) * 3 + 2) * 32 + (w & 1) * 16 + lg * 4 + 2);
    c1[7] = BIAS(m.b1, 3584, ((slot - 16) * 6 + (w >> 1) * 3 + 2) * 32 + (w & 1) * 16 + lg * 4 + 3);
  } else if (slot >> 2 == 14) {
    s0[12] = 16 + (w); wp = r1p; wn = r1n;
    s0[13] = blk0(-2);
    s0[14] = w * 8192;
    s0[15] = w * 8 * 7168 + (slot - 32) * 256 + rot * 1835008;
    s1[5] = w * 8 * 7168 + (slot - 32) * 256 + 7340032u;
    v0[0] = (lane >> 3) * 7168 + (((lane & 7) ^ ((lane >> 2) & 4)) << 4) + 0u;
    v0[1] = (lane >> 3) * 7168 + (((lane & 7) ^ ((lane >> 2) & 4)) << 4) + 128u;
    v0[4] = TA(w >> 1, w & 1, 0);
    v0[5] = TA(w >> 1, w & 1, 8);
    c0[0] = l16;
    c0[1] = lane ? 0x80000000u : 0u;
    c0[2] = 144384u + lane * 4;
    c0[3] = 378880u + ((slot - 32) * 4 + w) * 1024 + l16;
    c0[4] = 972800u + wr * 8192 + l16;
    c0[5] = 976896u + wr * 8192 + l16;
    c0[6] = FW(p, 48 + ((lane & 31) >> 2), lane & 3);
    c0[7] = 1u;
    c0[8] = 141312u + li * 8;
    c0[9] = 939008u + lane * 8;
    c0[10] = BIAS(m.b1, 3584, ((slot - 32) * 4 + w) * 32 + lg * 8 + 0);
    c0[11] = BIAS(m.b1, 3584, ((slot - 32) * 4 + w) * 32 + lg * 8 + 1);
    c0[12] = BIAS(m.b1, 3584, ((slot - 32) * 4 + w) * 32 + lg * 8 + 2);
    c0[13] = BIAS(m.b1, 3584, ((slot - 32) * 4 + w) * 32 + lg * 8 + 3);
    c0[14] = BIAS(m.b1, 3584, ((slot - 32) * 4 + w) * 32 + lg * 8 + 4);
    c0[15] = BIAS(m.b1, 3584, ((slot - 32) * 4 + w) * 32 + lg * 8 + 5);
    c1[0] = BIAS(m.b1, 3584, ((slot - 32) * 4 + w) * 32 + lg * 8 + 6);
    c1[1] = BIAS(m.b1, 3584, ((slot - 32) * 4 + w) * 32 + lg * 8 + 7);
  } else if (slot < 48) {
    s0[12] = 20 + (w); wp = r2p; wn = r2n;
    s0[13] = blk0(-2);
    s0[14] = w * 6144;
    s0[15] = (((slot - 32) >> 2) * 768 + w * 8) * 2048 + (slot & 3) * 512 + rot * 393216;
    s1[5] = (((slot - 32) >> 2) * 768 + w * 8) * 2048 + (slot & 3) * 512 + 1572864u;
    v0[0] = (lane >> 3) * 2048 + (((lane & 7) ^ ((lane >> 2) & 4)) << 4) + 0u;
    v0[1] = (lane >> 3) * 2048 + (((lane & 7) ^ ((lane >> 2) & 4)) << 4) + 128u;
    v0[2] = (lane >> 3) * 2048 + (((lane & 7) ^ ((lane >> 2) & 4)) << 4) + 256u;
    v0[3] = (lane >> 3) * 2048 + (((lane & 7) ^ ((lane >> 2) & 4)) << 4) + 384u;
    v0[4] = TA(w, 0, 0);
    v0[5] = TA(w, 0, 8);
    v0[6] = TA(w, 1, 0);
    v0[7] = TA(w, 1, 8);
    c0[0] = l16;
    c0[1] = lane ? 0x80000000u : 0u;
    c0[2] = 144384u + lane * 4;
    c0[3] = 1005568u + ((slot - 32) >> 2) * 24576 + wr * 6144 + l16;
    c0[4] = 1009664u + ((slot - 32) >> 2) * 24576 + wr * 6144 + l16;
    c0[5] = FW(p, (lane >= 48 && slot >= 44 ? 56 : (slot - 16) & ~3) + (li >> 2), lane & 3);
    c0[6] = 1u;
    c0[7] = 493568u + (slot - 32) * 8192 + w * 2048 + l16;
  } else if (true) {
    s0[12] = 24 + (w); wp = r2p; wn = r2n;
    const u16* const b2p = arg_b2();
    s0[13] = blk0(-3);
    s0[14] = w * 4096;
    s0[15] = (3072 + w * 8) * 2048 + (slot - 60) * 512 + rot * 262144;
    s1[2] = (p * 32 + 31) * 32768 + 4194304;
    s1[5] = (3072 + w * 8) * 2048 + (slot - 60) * 512 + 1048576u;
    v0[0] = (lane >> 3) * 2048 + (((lane & 7) ^ ((lane >> 2) & 4)) << 4) + 0u;
    v0[1] = (lane >> 3) * 2048 + (((lane & 7) ^ ((lane >> 2) & 4)) << 4) + 128u;
    v0[2] = (lane >> 3) * 2048 + (((lane & 7) ^ ((lane >> 2) & 4)) << 4) + 256u;
    v0[3] = (lane >> 3) * 2048 + (((lane & 7) ^ ((lane >> 2) & 4)) << 4) + 384u;
    v0[4] = TA(w, 0, 0);
    v0[5] = TA(w, 0, 8);
    v0[6] = TA(w, 1, 0);
    v0[7] = TA(w, 1, 8);
    c0[0] = l16;
    c0[1] = lane ? 0x80000000u : 0u;
    c0[2] = 144384u + lane * 4;
    c0[3] = 1417216u + wr * 4096 + l16;
    c0[4] = FW(p, 32 + (lane & 12) + slot - 60, lane & 3);
    c0[5] = 1u;
    c0[6] = 806912u + (slot - 60) * 8192 + w * 2048 + l16;
    c0[7] = 839680u + (slot - 60) * 8192 + w * 2048 + l16;
    c0[8] = 872448u + (slot - 60) * 8192 + w * 2048 + l16;
    c0[9] = 905216u + (slot - 60) * 8192 + w * 2048 + l16;
    c0[10] = 972800u + (slot - 60) * 8192 + w * 2048 + l16;
    c0[11] = (lane >> 3) * 2048 + (lane & 7) * 16 + (slot - 60) * 512 + w * 128;
    c0[12] = ((lane >> 3) + 8) * 2048 + (lane & 7) * 16 + (slot - 60) * 512 + w * 128;
    c0[14] = 133120u + w * 2048 + li * 128 + (((0 + lg) ^ (li & 7)) << 4);
    c0[15] = 133120u + w * 2048 + li * 128 + (((4 + lg) ^ (li & 7)) << 4);
    c1[0] = 133120u + w * 2048 + (lane >> 3) * 128 + (((lane ^ (lane >> 3)) & 7) << 4);
    c1[1] = 142336u + ((li * 2 + (lg >> 1)) * 8 + w * 2 + (lg & 1)) * 4;
    c1[2] = 142336u + l16;
    c1[3] = (lane & 1) ? 0x80000000u : 10024960u + (slot - 60) * 128 + (lane >> 1) * 4;
    c1[4] = BIAS(b2p, 1024, (slot - 60) * 256 + w * 64 + 0 + lg * 8);
    c1[5] = BIAS(b2p, 1024, (slot - 60) * 256 + w * 64 + 1 + lg * 8);
    c1[6] = BIAS(b2p, 1024, (slot - 60) * 256 + w * 64 + 2 + lg * 8);
    c1[7] = BIAS(b2p, 1024, (slot - 60) * 256 + w * 64 + 3 + lg * 8);
    c1[8] = BIAS(b2p, 1024, (slot - 60) * 256 + w * 64 + 4 + lg * 8);
    c1[9] = BIAS(b2p, 1024, (slot - 60) * 256 + w * 64 + 5 + lg * 8);
    c1[10] = BIAS(b2p, 1024, (slot - 60) * 256 + w * 64 + 6 + lg * 8);
    c1[11] = BIAS(b2p, 1024, (slot - 60) * 256 + w * 64 + 7 + lg * 8);
    c1[12] = BIAS(b2p, 1024, (slot - 60) * 256 + w * 64 + 32 + lg * 8);
    c1[13] = BIAS(b2p, 1024, (slot - 60) * 256 + w * 64 + 33 + lg * 8);
    c1[14] = BIAS(b2p, 1024, (slot - 60) * 256 + w * 64 + 34 + lg * 8);
    c1[15] = BIAS(b2p, 1024, (slot - 60) * 256 + w * 64 + 35 + lg * 8);
    c2[0] = BIAS(b2p, 1024, (slot - 60) * 256 + w * 64 + 36 + lg * 8);
    c2[1] = BIAS(b2p, 1024, (slot - 60) * 256 + w * 64 + 37 + lg * 8);
    c2[2] = BIAS(b2p, 1024, (slot - 60) * 256 + w * 64 + 38 + lg * 8);
    c2[3] = BIAS(b2p, 1024, (slot - 60) * 256 + w * 64 + 39 + lg * 8);
    c2[4] = (lg == (li >> 2) && ((li & 3) >> 1) + 0 == 0) ? ((li & 1) ? 0x3f800000u : 0x3f80u) : 0u;
    c2[5] = (lg == (li >> 2) && ((li & 3) >> 1) + 0 == 1) ? ((li & 1) ? 0x3f800000u : 0x3f80u) : 0u;
    c2[6] = (lg == (li >> 2) && ((li & 3) >> 1) + 0 == 2) ? ((li & 1) ? 0x3f800000u : 0x3f80u) : 0u;
    c2[7] = (lg == (li >> 2) && ((li & 3) >> 1) + 0 == 3) ? ((li & 1) ? 0x3f800000u : 0x3f80u) : 0u;
    c2[8] = (lg == (li >> 2) && ((li & 3) >> 1) + 2 == 0) ? ((li & 1) ? 0x3f800000u : 0x3f80u) : 0u;
    c2[9] = (lg == (li >> 2) && ((li & 3) >> 1) + 2 == 1) ? ((li & 1) ? 0x3f800000u : 0x3f80u) : 0u;
    c2[10] = (lg == (li >> 2) && ((li & 3) >> 1) + 2 == 2) ? ((li & 1) ? 0x3f800000u : 0x3f80u) : 0u;
    c2[11] = (lg == (li >> 2) && ((li & 3) >> 1) + 2 == 3) ? ((li & 1) ? 0x3f800000u : 0x3f80u) : 0u;
  }
  s0[0] = (unsigned)(size_t)m.ws; s0[1] = (unsigned)((size_t)m.ws >> 32) & 0xffffu; s0[2] = WS_END; s0[3] = 0x00020000;
  s0[4] = (unsigned)(size_t)wp; s0[5] = (unsigned)((size_t)wp >> 32) & 0xffffu; s0[6] = wn; s0[7] = 0x00020000;
  s0[8] = (unsigned)(size_t)op; s0[9] = (unsigned)((size_t)op >> 32) & 0xffffu; s0[10] = on; s0[11] = 0x00020000;
  s1[0] = FW(p, slot, w); s1[1] = w * 1024; s1[7] = DBG + ((p * 64 + slot) * 4 + w) * 1024;
#pragma unroll
  for (int i = 0; i < 16; ++i) s0[i] = __builtin_amdgcn_readfirstlane(s0[i]);
#pragma unroll
  for (int i = 0; i < 8; ++i) s1[i] = __builtin_amdgcn_readfirstlane(s1[i]);
  asm volatile(
R"(
.macro mav d,a,b
v_mfma_f32_16x16x32_bf16 v[\d:\d+3], a[\a:\a+3], v[\b:\b+3], v[\d:\d+3]
.endm
.macro Ta d,a,o
ds_read_b64_tr_b16 a[\d:\d+1], v\a offset:\o
ds_read_b64_tr_b16 a[\d+2:\d+3], v\a offset:\o+512
.endm
.macro mva d,a,b
v_mfma_f32_16x16x32_bf16 v[\d:\d+3], v[\a:\a+3], a[\b:\b+3], v[\d:\d+3]
.endm
.macro mvv d,a,b
v_mfma_f32_16x16x32_bf16 v[\d:\d+3], v[\a:\a+3], v[\b:\b+3], v[\d:\d+3]
.endm
.macro Tv d,a,o
ds_read_b64_tr_b16 v[\d:\d+1], v\a offset:\o
ds_read_b64_tr_b16 v[\d+2:\d+3], v\a offset:\o+512
.endm
.macro R d,o
ds_read_b128 v[\d:\d+3], v80 offset:\o
.endm
.macro D v,o
buffer_load_dwordx4 v\v, s[36:39], s49 offen offset:\o sc1 lds
.endm
.macro D2 v,s,o
buffer_load_dwordx4 v\v, s[44:47], s\s offen offset:\o sc1 lds
.endm
.macro E v,o
s_add_u32 m0, s53, \o
s_nop 0
buffer_load_dwordx4 v\v, s[40:43], s51 offen lds
.endm
s_branch La
.p2align 12
La:
s_nop 4
s_mov_b32 s61, 200000
s_mov_b64 s[62:63], 255
s_mov_b32 s64, 0x3f803f80
s_mov_b32 s65, 0x3e8293ee
s_lshr_b32 s60, s48, 2
Lr0:
s_cmp_eq_u32 s60, 0
s_cbranch_scc0 Lr1
s_lshr_b32 s66, s53, 10
s_mul_i32 s66, s66, 8208
s_mov_b32 s67, 0x8000
s_mov_b64 exec, 0
s_cmp_eq_u32 s53, 0
s_cbranch_scc1 LsQ0
s_branch LsQ2
LwQA:
s_mov_b64 exec, -1
E 0,86016
E 1,90112
E 2,94208
s_add_u32 s51, s51, 196608
E 0,98304
E 1,102400
E 2,106496
s_add_u32 s51, s51, 196608
E 0,61440
E 1,65536
s_add_u32 m0, s66, 2816
s_nop 0
buffer_load_dwordx4 v3, s[44:47], s67 offen offset:256 lds
E 2,69632
s_add_u32 s51, s51, 196608
E 0,73728
s_add_u32 m0, s66, 6912
s_nop 0
buffer_load_dwordx4 v3, s[44:47], 0 offen offset:256 lds
E 1,77824
E 2,81920
s_add_u32 s51, s51, 196608
v_lshrrev_b32 v12, 2, v7
v_and_b32 v12, 0xc0, v12
v_xor_b32 v13, 0, v12
v_add_u32 v8, v7, v13
v_xor_b32 v13, 64, v12
v_add_u32 v9, v7, v13
v_xor_b32 v13, 128, v12
v_add_u32 v10, v7, v13
v_xor_b32 v13, 192, v12
v_add_u32 v11, v7, v13
v_mov_b32 v114, s64
v_mov_b32 v115, s64
v_mov_b32 v116, s64
v_mov_b32 v117, s64
s_mov_b32 s92, 0xaaaaaaaa
s_mov_b32 s93, s92
s_mov_b32 s94, 0xcccccccc
s_mov_b32 s95, s94
s_mov_b32 s96, 0xf0000f
s_mov_b32 s97, 0xf0000f00
s_waitcnt vmcnt(14)
s_barrier
v_add_u32 v4, 69632, v4
v_add_u32 v5, 69632, v5
v_add_u32 v6, 69632, v6
Ta 0,4,40960
Ta 4,5,40960
Ta 8,6,40960
Ta 12,4,53248
Ta 16,5,53248
Ta 20,6,53248
ds_read_b128 v[38:41], v8 offset:0
ds_read_b128 v[42:45], v9 offset:0
ds_read_b128 v[46:49], v8 offset:4096
ds_read_b128 v[50:53], v9 offset:4096
v_accvgpr_read_b32 v79, a255
v_mov_b32 v78, 0x100
v_add_u32 v79, 0x9e3779b9, v79
buffer_store_dwordx2 v[78:79], off, s[36:39], s52 sc1
E 0,36864
E 1,40960
E 2,45056
s_add_u32 s51, s51, 196608
E 0,49152
E 1,53248
E 2,57344
s_add_u32 s51, s51, 196608
s_waitcnt lgkmcnt(0)
s_waitcnt vmcnt(15)
s_barrier
E 0,110592
Ta 24,4,16384
v_mfma_f32_16x16x32_bf16 v[74:77], v[114:117], v[38:41], 0
v_mfma_f32_16x16x32_bf16 v[118:121], v[38:41], v[38:41], 0
Ta 28,5,16384
mvv 74,114,42
Ta 32,6,16384
E 1,114688
mvv 118,42,42
Ta 36,4,28672
v_mfma_f32_16x16x32_bf16 v[62:65], a[0:3], v[114:117], 0
v_mfma_f32_16x16x32_bf16 v[66:69], a[4:7], v[114:117], 0
Ta 40,5,28672
s_add_u32 m0, s66, 3584
s_nop 0
buffer_load_dwordx4 v3, s[44:47], s67 offen offset:512 lds
v_mfma_f32_16x16x32_bf16 v[70:73], v[114:117], a[8:11], 0
Ta 44,6,28672
v_mfma_f32_16x16x32_bf16 v[14:17], a[0:3], v[38:41], 0
ds_read_b128 v[54:57], v10 offset:0
v_mfma_f32_16x16x32_bf16 v[18:21], a[4:7], v[38:41], 0
E 2,118784
s_add_u32 s51, s51, 196608
v_mfma_f32_16x16x32_bf16 v[22:25], v[38:41], a[8:11], 0
ds_read_b128 v[58:61], v11 offset:0
v_mfma_f32_16x16x32_bf16 v[26:29], a[0:3], v[46:49], 0
ds_read_b128 v[100:103], v10 offset:4096
E 0,122880
v_mfma_f32_16x16x32_bf16 v[30:33], a[4:7], v[46:49], 0
ds_read_b128 v[104:107], v11 offset:4096
v_mfma_f32_16x16x32_bf16 v[34:37], v[46:49], a[8:11], 0
mav 62,12,114
s_add_u32 m0, s66, 7680
s_nop 0
buffer_load_dwordx4 v3, s[44:47], 0 offen offset:512 lds
mav 66,16,114
mva 70,114,20
mav 14,12,42
E 1,126976
mav 18,16,42
mva 22,42,20
mav 26,12,50
E 2,131072
s_add_u32 s51, s51, 196608
mav 30,16,50
mva 34,50,20
s_waitcnt lgkmcnt(0)
s_waitcnt vmcnt(15)
s_barrier
v_add_u32 v4, -49152, v4
v_add_u32 v5, -49152, v5
v_add_u32 v6, -49152, v6
E 0,86016
Ta 48,4,40960
mvv 74,114,54
mvv 118,54,54
Ta 52,5,40960
mvv 74,114,58
Ta 56,6,40960
mvv 118,58,58
E 1,90112
Ta 60,4,53248
mav 62,24,114
mav 66,28,114
Ta 64,5,53248
mva 70,114,32
Ta 68,6,53248
mav 14,24,54
E 2,94208
s_add_u32 s51, s51, 196608
ds_read_b128 v[38:41], v8 offset:1024
mav 18,28,54
mva 22,54,32
ds_read_b128 v[42:45], v9 offset:1024
mav 26,24,100
ds_read_b128 v[46:49], v8 offset:5120
E 0,98304
mav 30,28,100
ds_read_b128 v[50:53], v9 offset:5120
mva 34,100,32
mav 62,36,114
mav 66,40,114
E 1,102400
mva 70,114,44
mav 14,36,58
mav 18,40,58
mva 22,58,44
E 2,106496
s_add_u32 s51, s51, 196608
mav 26,36,104
mav 30,40,104
mva 34,104,44
s_waitcnt lgkmcnt(0)
s_waitcnt vmcnt(14)
s_barrier
E 0,61440
Ta 72,4,16384
mvv 74,114,38
mvv 118,38,38
Ta 76,5,16384
mvv 74,114,42
Ta 80,6,16384
E 1,65536
mvv 118,42,42
Ta 84,4,28672
mav 62,48,114
mav 66,52,114
Ta 88,5,28672
s_add_u32 m0, s66, 4352
s_nop 0
buffer_load_dwordx4 v3, s[44:47], s67 offen offset:768 lds
mva 70,114,56
Ta 92,6,28672
mav 14,48,38
ds_read_b128 v[54:57], v10 offset:1024
mav 18,52,38
E 2,69632
s_add_u32 s51, s51, 196608
mva 22,38,56
ds_read_b128 v[58:61], v11 offset:1024
mav 26,48,46
ds_read_b128 v[100:103], v10 offset:5120
E 0,73728
mav 30,52,46
ds_read_b128 v[104:107], v11 offset:5120
mva 34,46,56
mav 62,60,114
s_add_u32 m0, s66, 8448
s_nop 0
buffer_load_dwordx4 v3, s[44:47], 0 offen offset:768 lds
mav 66,64,114
mva 70,114,68
mav 14,60,42
E 1,77824
mav 18,64,42
mva 22,42,68
mav 26,60,50
E 2,81920
s_add_u32 s51, s51, 196608
mav 30,64,50
mva 34,50,68
s_waitcnt lgkmcnt(0)
s_waitcnt vmcnt(14)
s_barrier
v_add_u32 v4, 49152, v4
v_add_u32 v5, 49152, v5
v_add_u32 v6, 49152, v6
E 0,36864
Ta 96,4,40960
mvv 74,114,54
mvv 118,54,54
Ta 100,5,40960
mvv 74,114,58
Ta 104,6,40960
mvv 118,58,58
E 1,40960
Ta 108,4,53248
mav 62,72,114
mav 66,76,114
Ta 112,5,53248
mva 70,114,80
Ta 116,6,53248
mav 14,72,54
E 2,45056
s_add_u32 s51, s51, 196608
ds_read_b128 v[38:41], v8 offset:2048
mav 18,76,54
mva 22,54,80
ds_read_b128 v[42:45], v9 offset:2048
mav 26,72,100
ds_read_b128 v[46:49], v8 offset:6144
E 0,49152
mav 30,76,100
ds_read_b128 v[50:53], v9 offset:6144
mva 34,100,80
mav 62,84,114
mav 66,88,114
E 1,53248
mva 70,114,92
mav 14,84,58
mav 18,88,58
mva 22,58,92
E 2,57344
s_add_u32 s51, s51, 196608
mav 26,84,104
mav 30,88,104
mva 34,104,92
s_waitcnt lgkmcnt(0)
s_waitcnt vmcnt(14)
s_barrier
E 0,110592
Ta 120,4,16384
mvv 74,114,38
mvv 118,38,38
Ta 124,5,16384
mvv 74,114,42
Ta 128,6,16384
E 1,114688
mvv 118,42,42
Ta 132,4,28672
mav 62,96,114
mav 66,100,114
Ta 136,5,28672
s_add_u32 m0, s66, 1024
s_nop 0
buffer_load_dwordx4 v3, s[44:47], s67 offen offset:1024 lds
mva 70,114,104
Ta 140,6,28672
mav 14,96,38
ds_read_b128 v[54:57], v10 offset:2048
mav 18,100,38
E 2,118784
s_add_u32 s51, s51, 196608
mva 22,38,104
ds_read_b128 v[58:61], v11 offset:2048
mav 26,96,46
ds_read_b128 v[100:103], v10 offset:6144
E 0,122880
mav 30,100,46
ds_read_b128 v[104:107], v11 offset:6144
mva 34,46,104
mav 62,108,114
s_add_u32 m0, s66, 5120
s_nop 0
buffer_load_dwordx4 v3, s[44:47], 0 offen offset:1024 lds
mav 66,112,114
mva 70,114,116
mav 14,108,42
E 1,126976
mav 18,112,42
mva 22,42,116
mav 26,108,50
E 2,131072
s_add_u32 s51, s51, 196608
mav 30,112,50
mva 34,50,116
s_waitcnt lgkmcnt(0)
s_waitcnt vmcnt(14)
s_barrier
v_add_u32 v4, -49152, v4
v_add_u32 v5, -49152, v5
v_add_u32 v6, -49152, v6
E 0,86016
Ta 144,4,40960
mvv 74,114,54
mvv 118,54,54
Ta 148,5,40960
mvv 74,114,58
Ta 152,6,40960
mvv 118,58,58
E 1,90112
Ta 156,4,53248
mav 62,120,114
mav 66,124,114
Ta 160,5,53248
mva 70,114,128
Ta 164,6,53248
mav 14,120,54
E 2,94208
s_add_u32 s51, s51, 196608
ds_read_b128 v[38:41], v8 offset:3072
mav 18,124,54
mva 22,54,128
ds_read_b128 v[42:45], v9 offset:3072
mav 26,120,100
ds_read_b128 v[46:49], v8 offset:7168
E 0,98304
mav 30,124,100
ds_read_b128 v[50:53], v9 offset:7168
mva 34,100,128
mav 62,132,114
mav 66,136,114
E 1,102400
mva 70,114,140
mav 14,132,58
mav 18,136,58
mva 22,58,140
E 2,106496
s_add_u32 s51, s51, 196608
mav 26,132,104
mav 30,136,104
mva 34,104,140
s_waitcnt lgkmcnt(0)
s_waitcnt vmcnt(14)
s_barrier
E 0,61440
Ta 168,4,16384
mvv 74,114,38
mvv 118,38,38
Ta 172,5,16384
mvv 74,114,42
Ta 176,6,16384
E 1,65536
mvv 118,42,42
Ta 180,4,28672
mav 62,144,114
mav 66,148,114
Ta 184,5,28672
s_add_u32 m0, s66, 1792
s_nop 0
buffer_load_dwordx4 v3, s[44:47], s67 offen offset:1280 lds
mva 70,114,152
Ta 188,6,28672
mav 14,144,38
ds_read_b128 v[54:57], v10 offset:3072
mav 18,148,38
E 2,69632
s_add_u32 s51, s51, 196608
mva 22,38,152
ds_read_b128 v[58:61], v11 offset:3072
mav 26,144,46
ds_read_b128 v[100:103], v10 offset:7168
E 0,73728
mav 30,148,46
ds_read_b128 v[104:107], v11 offset:7168
mva 34,46,152
mav 62,156,114
LsQ0:
s_cbranch_execz LsQ4
s_add_u32 m0, s66, 5888
s_nop 0
buffer_load_dwordx4 v3, s[44:47], 0 offen offset:1280 lds
mav 66,160,114
mva 70,114,164
mav 14,156,42
E 1,77824
mav 18,160,42
mva 22,42,164
mav 26,156,50
E 2,81920
s_add_u32 s51, s51, 196608
mav 30,160,50
mva 34,50,164
s_waitcnt lgkmcnt(0)
s_waitcnt vmcnt(14)
s_barrier
v_add_u32 v4, 49152, v4
v_add_u32 v5, 49152, v5
v_add_u32 v6, 49152, v6
E 0,36864
Ta 192,4,40960
mvv 74,114,54
mvv 118,54,54
Ta 196,5,40960
mvv 74,114,58
Ta 200,6,40960
mvv 118,58,58
E 1,40960
Ta 204,4,53248
mav 62,168,114
mav 66,172,114
Ta 208,5,53248
mva 70,114,176
Ta 212,6,53248
mav 14,168,54
E 2,45056
s_add_u32 s51, s51, 196608
ds_read_b128 v[38:41], v8 offset:0
mav 18,172,54
mva 22,54,176
ds_read_b128 v[42:45], v9 offset:0
mav 26,168,100
ds_read_b128 v[46:49], v8 offset:4096
E 0,49152
mav 30,172,100
ds_read_b128 v[50:53], v9 offset:4096
mva 34,100,176
mav 62,180,114
mav 66,184,114
E 1,53248
mva 70,114,188
mav 14,180,58
mav 18,184,58
mva 22,58,188
E 2,57344
s_add_u32 s51, s51, 196608
mav 26,180,104
mav 30,184,104
mva 34,104,188
s_waitcnt lgkmcnt(0)
s_waitcnt vmcnt(14)
s_barrier
E 0,110592
Ta 216,4,16384
mvv 74,114,38
mvv 118,38,38
Ta 220,5,16384
mvv 74,114,42
Ta 224,6,16384
E 1,114688
mvv 118,42,42
Ta 228,4,28672
mav 62,192,114
mav 66,196,114
Ta 232,5,28672
s_add_u32 m0, s66, 2560
s_nop 0
buffer_load_dwordx4 v3, s[44:47], s67 offen offset:1536 lds
mva 70,114,200
Ta 236,6,28672
mav 14,192,38
ds_read_b128 v[54:57], v10 offset:0
mav 18,196,38
E 2,118784
s_add_u32 s51, s51, 196608
mva 22,38,200
ds_read_b128 v[58:61], v11 offset:0
mav 26,192,46
ds_read_b128 v[100:103], v10 offset:4096
E 0,122880
mav 30,196,46
ds_read_b128 v[104:107], v11 offset:4096
mva 34,46,200
mav 62,204,114
s_add_u32 m0, s66, 6656
s_nop 0
buffer_load_dwordx4 v3, s[44:47], 0 offen offset:1536 lds
mav 66,208,114
mva 70,114,212
mav 14,204,42
E 1,126976
mav 18,208,42
mva 22,42,212
mav 26,204,50
E 2,131072
s_add_u32 s51, s51, 196608
mav 30,208,50
mva 34,50,212
s_waitcnt lgkmcnt(0)
s_waitcnt vmcnt(14)
s_barrier
v_add_u32 v4, -49152, v4
v_add_u32 v5, -49152, v5
v_add_u32 v6, -49152, v6
E 0,86016
Ta 240,4,40960
mvv 74,114,54
mvv 118,54,54
Ta 244,5,40960
mvv 74,114,58
Ta 248,6,40960
mvv 118,58,58
E 1,90112
Ta 252,4,53248
mav 62,216,114
mav 66,220,114
Tv 128,5,53248
mva 70,114,224
Tv 132,6,53248
mav 14,216,54
E 2,94208
s_add_u32 s51, s51, 196608
ds_read_b128 v[38:41], v8 offset:1024
mav 18,220,54
mva 22,54,224
ds_read_b128 v[42:45], v9 offset:1024
mav 26,216,100
ds_read_b128 v[46:49], v8 offset:5120
E 0,98304
mav 30,220,100
ds_read_b128 v[50:53], v9 offset:5120
mva 34,100,224
mav 62,228,114
mav 66,232,114
E 1,102400
mva 70,114,236
mav 14,228,58
mav 18,232,58
mva 22,58,236
E 2,106496
s_add_u32 s51, s51, 196608
mav 26,228,104
mav 30,232,104
mva 34,104,236
s_waitcnt lgkmcnt(0)
s_waitcnt vmcnt(14)
s_barrier
E 0,61440
Tv 136,4,16384
mvv 74,114,38
mvv 118,38,38
Tv 140,5,16384
mvv 74,114,42
Tv 144,6,16384
E 1,65536
mvv 118,42,42
Tv 148,4,28672
mav 62,240,114
mav 66,244,114
Tv 152,5,28672
s_add_u32 m0, s66, 3328
s_nop 0
buffer_load_dwordx4 v3, s[44:47], s67 offen offset:1792 lds
mva 70,114,248
Tv 156,6,28672
mav 14,240,38
ds_read_b128 v[54:57], v10 offset:1024
mav 18,244,38
E 2,69632
s_add_u32 s51, s51, 196608
mva 22,38,248
ds_read_b128 v[58:61], v11 offset:1024
mav 26,240,46
ds_read_b128 v[100:103], v10 offset:5120
E 0,73728
mav 30,244,46
ds_read_b128 v[104:107], v11 offset:5120
mva 34,46,248
mav 62,252,114
s_add_u32 m0, s66, 7424
s_nop 0
buffer_load_dwordx4 v3, s[44:47], 0 offen offset:1792 lds
mvv 66,128,114
mvv 70,114,132
mav 14,252,42
E 1,77824
mvv 18,128,42
mvv 22,42,132
mav 26,252,50
E 2,81920
s_add_u32 s51, s51, 196608
mvv 30,128,50
mvv 34,50,132
s_waitcnt lgkmcnt(0)
s_waitcnt vmcnt(14)
s_barrier
v_add_u32 v4, 49152, v4
v_add_u32 v5, 49152, v5
v_add_u32 v6, 49152, v6
E 0,36864
Tv 160,4,40960
mvv 74,114,54
mvv 118,54,54
Tv 164,5,40960
mvv 74,114,58
Tv 168,6,40960
mvv 118,58,58
E 1,40960
Tv 172,4,53248
mvv 62,136,114
mvv 66,140,114
Tv 176,5,53248
mvv 70,114,144
Tv 180,6,53248
mvv 14,136,54
E 2,45056
s_add_u32 s51, s51, 196608
ds_read_b128 v[38:41], v8 offset:2048
mvv 18,140,54
mvv 22,54,144
ds_read_b128 v[42:45], v9 offset:2048
mvv 26,136,100
ds_read_b128 v[46:49], v8 offset:6144
E 0,49152
mvv 30,140,100
ds_read_b128 v[50:53], v9 offset:6144
mvv 34,100,144
mvv 62,148,114
mvv 66,152,114
E 1,53248
mvv 70,114,156
mvv 14,148,58
mvv 18,152,58
mvv 22,58,156
E 2,57344
s_add_u32 s51, s51, 196608
mvv 26,148,104
mvv 30,152,104
mvv 34,104,156
s_waitcnt lgkmcnt(0)
s_waitcnt vmcnt(14)
s_barrier
Tv 184,4,16384
mvv 74,114,38
mvv 118,38,38
Tv 188,5,16384
mvv 74,114,42
Tv 192,6,16384
mvv 118,42,42
Tv 196,4,28672
mvv 62,160,114
mvv 66,164,114
Tv 200,5,28672
mvv 70,114,168
Tv 204,6,28672
mvv 14,160,38
ds_read_b128 v[54:57], v10 offset:2048
mvv 18,164,38
mvv 22,38,168
ds_read_b128 v[58:61], v11 offset:2048
mvv 26,160,46
ds_read_b128 v[100:103], v10 offset:6144
mvv 30,164,46
ds_read_b128 v[104:107], v11 offset:6144
mvv 34,46,168
mvv 62,172,114
mvv 66,176,114
mvv 70,114,180
mvv 14,172,42
mvv 18,176,42
mvv 22,42,180
mvv 26,172,50
mvv 30,176,50
mvv 34,50,180
s_waitcnt lgkmcnt(0)
s_waitcnt vmcnt(6)
s_barrier
v_add_u32 v4, -49152, v4
v_add_u32 v5, -49152, v5
v_add_u32 v6, -49152, v6
Tv 208,4,40960
mvv 74,114,54
mvv 118,54,54
Tv 212,5,40960
mvv 74,114,58
Tv 216,6,40960
mvv 118,58,58
Tv 220,4,53248
mvv 62,184,114
mvv 66,188,114
Tv 224,5,53248
mvv 70,114,192
Tv 228,6,53248
mvv 14,184,54
ds_read_b128 v[38:41], v8 offset:3072
mvv 18,188,54
mvv 22,54,192
ds_read_b128 v[42:45], v9 offset:3072
mvv 26,184,100
ds_read_b128 v[46:49], v8 offset:7168
mvv 30,188,100
ds_read_b128 v[50:53], v9 offset:7168
mvv 34,100,192
mvv 62,196,114
mvv 66,200,114
mvv 70,114,204
mvv 14,196,58
mvv 18,200,58
mvv 22,58,204
mvv 26,196,104
mvv 30,200,104
mvv 34,104,204
s_waitcnt lgkmcnt(0)
s_waitcnt vmcnt(0)
s_barrier
Tv 232,4,16384
mvv 74,114,38
mvv 118,38,38
Tv 236,5,16384
mvv 74,114,42
Tv 240,6,16384
mvv 118,42,42
Tv 244,4,28672
mvv 62,208,114
mvv 66,212,114
Tv 248,5,28672
mvv 70,114,216
Tv 252,6,28672
mvv 14,208,38
ds_read_b128 v[54:57], v10 offset:3072
mvv 18,212,38
mvv 22,38,216
ds_read_b128 v[58:61], v11 offset:3072
mvv 26,208,46
ds_read_b128 v[100:103], v10 offset:7168
mvv 30,212,46
ds_read_b128 v[104:107], v11 offset:7168
mvv 34,46,216
mvv 62,220,114
mvv 66,224,114
mvv 70,114,228
mvv 14,220,42
mvv 18,224,42
mvv 22,42,228
mvv 26,220,50
mvv 30,224,50
mvv 34,50,228
s_waitcnt lgkmcnt(0)
mvv 74,114,54
mvv 118,54,54
mvv 74,114,58
mvv 118,58,58
mvv 62,232,114
mvv 66,236,114
mvv 70,114,240
mvv 14,232,54
mvv 18,236,54
mvv 22,54,240
mvv 26,232,100
mvv 30,236,100
mvv 34,100,240
v_cndmask_b32 v118, v118, v119, s[92:93]
mvv 62,244,114
v_cndmask_b32 v120, v120, v121, s[92:93]
mvv 66,248,114
v_mul_f32 v74, 0x3a800000, v74
v_cndmask_b32 v118, v118, v120, s[94:95]
mvv 70,114,252
v_mul_f32 v118, 0x3a800000, v118
mvv 14,244,58
v_fma_f32 v118, -v74, v74, v118
mvv 18,248,58
v_max_f32 v118, 0, v118
mvv 22,58,252
v_add_f32 v118, 0x3727c5ac, v118
mvv 26,244,104
v_rsq_f32 v118, v118
mvv 30,248,104
s_nop 0
v_mul_f32_e64 v119, -v74, v118
mvv 34,104,252
s_mov_b64 exec, s[96:97]
ds_write_b64 v83, v[118:119] offset:0
s_mov_b64 exec, -1
s_waitcnt lgkmcnt(0)
s_barrier
s_nop 7
v_mov_b32 v99, v70
s_and_b32 s60, s48, 3
s_cmp_eq_u32 s60, 1
s_cbranch_scc1 LvQA1
LvQA0:
LsQ1:
s_cbranch_execz LsQ5
ds_read_b64 v[122:123], v83 offset:0
ds_read_b128 v[70:73], v84 offset:0
ds_read_b128 v[74:77], v84 offset:16
s_add_u32 s49, s49, 313344
s_waitcnt lgkmcnt(0)
v_mul_f32 v71, v71, v99
v_fma_f32 v22, v22, v70, v71
v_mul_f32 v73, v73, v99
v_fma_f32 v23, v23, v72, v73
v_mul_f32 v75, v75, v99
v_fma_f32 v24, v24, v74, v75
v_mul_f32 v77, v77, v99
v_fma_f32 v25, v25, v76, v77
v_cvt_pk_bf16_f32 v8, v22, v23
v_cvt_pk_bf16_f32 v9, v24, v25
v_mul_f32 v124, v123, v62
v_fma_f32 v14, v14, v122, v124
v_mul_f32 v125, v123, v63
v_fma_f32 v15, v15, v122, v125
v_mul_f32 v124, v123, v64
v_fma_f32 v16, v16, v122, v124
v_mul_f32 v125, v123, v65
v_fma_f32 v17, v17, v122, v125
v_mul_f32 v124, v123, v66
v_fma_f32 v18, v18, v122, v124
v_mul_f32 v125, v123, v67
v_fma_f32 v19, v19, v122, v125
v_mul_f32 v124, v123, v68
v_fma_f32 v20, v20, v122, v124
v_mul_f32 v125, v123, v69
v_fma_f32 v21, v21, v122, v125
v_cvt_pk_bf16_f32 v0, v14, v15
v_cvt_pk_bf16_f32 v1, v16, v17
v_cvt_pk_bf16_f32 v2, v18, v19
v_cvt_pk_bf16_f32 v3, v20, v21
s_add_u32 s49, s49, 313344
s_barrier
ds_read_b128 v[100:103], v82 offset:0
ds_read_b64 v[108:109], v82 offset:1024
R 38,33024
R 42,33088
LsQ2:
s_cbranch_execz LsQ6
R 46,33152
R 50,33216
ds_read_b64 v[122:123], v83 offset:128
ds_read_b128 v[70:73], v84 offset:128
ds_read_b128 v[74:77], v84 offset:144
s_waitcnt lgkmcnt(0)
v_mfma_f32_16x16x32_bf16 v[14:17], a[0:3], v[38:41], 0
R 54,33280
v_mul_f32 v71, v71, v99
v_mfma_f32_16x16x32_bf16 v[18:21], a[4:7], v[38:41], 0
v_fma_f32 v34, v34, v70, v71
v_mul_f32 v73, v73, v99
v_mfma_f32_16x16x32_bf16 v[22:25], v[38:41], a[8:11], 0
v_fma_f32 v35, v35, v72, v73
v_mul_f32 v75, v75, v99
mav 14,12,42
R 58,33344
v_fma_f32 v36, v36, v74, v75
mav 18,16,42
v_mul_f32 v77, v77, v99
v_fma_f32 v37, v37, v76, v77
mva 22,42,20
v_cvt_pk_bf16_f32 v10, v34, v35
v_cvt_pk_bf16_f32 v11, v36, v37
mav 14,24,46
R 38,33408
v_mul_f32 v124, v123, v62
v_mfma_f32_16x16x32_bf16 v[70:73], v[100:103], v[0:3], v[86:89]
v_fma_f32 v26, v26, v122, v124
v_mul_f32 v125, v123, v63
mav 18,28,46
v_fma_f32 v27, v27, v122, v125
v_mul_f32 v124, v123, v64
mva 22,46,32
v_fma_f32 v28, v28, v122, v124
v_mul_f32 v125, v123, v65
mav 14,36,50
R 42,33472
v_fma_f32 v29, v29, v122, v125
mav 18,40,50
v_mul_f32 v124, v123, v66
v_fma_f32 v30, v30, v122, v124
mva 22,50,44
s_waitcnt lgkmcnt(2)
v_mul_f32 v125, v123, v67
v_fma_f32 v31, v31, v122, v125
mav 14,48,54
R 46,33536
v_mul_f32 v124, v123, v68
mav 18,52,54
v_fma_f32 v32, v32, v122, v124
v_mul_f32 v125, v123, v69
mva 22,54,56
v_fma_f32 v33, v33, v122, v125
mav 14,60,58
R 50,33600
mav 18,64,58
mva 22,58,68
s_waitcnt lgkmcnt(2)
mav 14,72,38
R 54,33664
mav 18,76,38
mva 22,38,80
buffer_store_dwordx4 v[100:103], v82, s[36:39], s54 offen sc1
mav 14,84,42
R 58,33728
mav 18,88,42
buffer_store_dwordx2 v[8:9], v98, s[36:39], s54 offen offset:1024 sc1
mva 22,42,92
s_waitcnt lgkmcnt(2)
mav 14,96,46
buffer_store_dwordx2 v[108:109], v98, s[36:39], s54 offen offset:1536 sc1
R 38,33792
mav 18,100,46
s_barrier
mva 22,46,104
ds_read_b128 v[104:107], v82 offset:4096
mav 14,108,50
ds_read_b64 v[110:111], v82 offset:5120
mav 18,112,50
R 42,33856
mva 22,50,116
s_waitcnt lgkmcnt(4)
mav 14,120,54
R 46,33920
mav 18,124,54
mva 22,54,128
mav 14,132,58
s_waitcnt lgkmcnt(2)
R 50,33984
v_mfma_f32_16x16x32_bf16 v[74:77], v[104:107], v[0:3], v[90:93]
v_cvt_pk_bf16_f32 v0, v26, v27
v_cvt_pk_bf16_f32 v1, v28, v29
mav 18,136,58
v_cvt_pk_bf16_f32 v2, v30, v31
v_cvt_pk_bf16_f32 v3, v32, v33
mva 22,58,140
mav 14,144,38
R 54,34048
v_max3_f32 v118, v70, v71, v74
mav 18,148,38
v_max3_f32 v119, v72, v73, v76
v_max_f32 v118, v118, v75
mva 22,38,152
s_waitcnt lgkmcnt(2)
v_max_f32 v119, v119, v77
v_max_f32 v120, v118, v119
mav 14,156,42
R 38,34112
v_max_f32 v121, v118, v119
mav 18,160,42
mva 22,42,164
v_permlane32_swap_b32 v120, v121
mav 14,168,46
R 42,34176
v_max_f32 v114, v120, v121
mav 18,172,46
v_max_f32 v115, v120, v121
mva 22,46,176
s_waitcnt lgkmcnt(2)
v_permlane16_swap_b32 v114, v115
mav 14,180,50
R 46,34240
v_max_f32 v120, v114, v115
mav 18,184,50
v_mul_f32 v120, s65, v120
v_fma_f32 v118, v118, s65, -v120
mva 22,50,188
v_fma_f32 v119, v119, s65, -v120
mav 14,192,54
R 50,34304
mav 18,196,54
v_exp_f32 v118, v118
mva 22,54,200
s_waitcnt lgkmcnt(2)
v_exp_f32 v119, v119
mav 14,204,38
R 54,34368
v_add_f32 v120, v118, v119
mav 18,208,38
v_add_f32 v121, v118, v119
mva 22,38,212
mav 14,216,42
R 38,34432
mav 18,220,42
v_permlane32_swap_b32 v120, v121
mva 22,42,224
s_waitcnt lgkmcnt(2)
v_add_f32 v114, v120, v121
v_add_f32 v115, v120, v121
mav 14,228,46
R 42,34496
mav 18,232,46
v_permlane16_swap_b32 v114, v115
mva 22,46,236
v_add_f32 v120, v114, v115
mav 14,240,50
R 46,34560
mav 18,244,50
v_rcp_f32 v120, v120
mva 22,50,248
s_waitcnt lgkmcnt(2)
v_mul_f32 v118, v118, v120
v_mul_f32 v119, v119, v120
mav 14,252,54
R 50,34624
v_cvt_pk_bf16_f32 v118, v118, v118
mvv 18,128,54
v_cvt_pk_bf16_f32 v119, v119, v119
v_and_b32 v114, v118, v94
mvv 22,54,132
v_and_b32 v115, v119, v95
v_and_b32 v116, v118, v96
mvv 14,136,38
R 54,34688
v_and_b32 v117, v119, v97
mvv 18,140,38
mvv 22,38,144
s_waitcnt lgkmcnt(2)
v_mfma_f32_16x16x32_bf16 v[70:73], v[8:11], v[114:117], 0
v_mfma_f32_16x16x32_bf16 v[74:77], v[108:111], v[114:117], 0
mvv 14,148,42
R 38,34752
mvv 18,152,42
mvv 22,42,156
mvv 14,160,46
R 42,34816
mvv 18,164,46
v_cvt_pk_bf16_f32 v114, v70, v71
v_cvt_pk_bf16_f32 v115, v72, v73
mvv 22,46,168
s_waitcnt lgkmcnt(2)
v_cvt_pk_bf16_f32 v116, v74, v75
v_cvt_pk_bf16_f32 v117, v76, v77
mvv 14,172,50
R 46,34880
mvv 18,176,50
buffer_store_dwordx4 v[114:117], v82, s[36:39], s49 offen sc1
s_add_u32 s49, s49, 313344
mvv 22,50,180
mvv 14,184,54
R 50,34944
mvv 18,188,54
mvv 22,54,192
s_waitcnt lgkmcnt(2)
mvv 14,196,38
R 54,35008
mvv 18,200,38
mvv 22,38,204
mvv 14,208,42
R 38,0
mvv 18,212,42
ds_read_b64 v[122:123], v83 offset:0
mvv 22,42,216
s_waitcnt lgkmcnt(3)
ds_read_b128 v[70:73], v84 offset:0
mvv 14,220,46
R 42,64
mvv 18,224,46
ds_read_b128 v[74:77], v84 offset:16
mvv 22,46,228
mvv 14,232,50
R 46,128
mvv 18,236,50
mvv 22,50,240
s_waitcnt lgkmcnt(3)
mvv 14,244,54
R 50,192
mvv 18,248,54
mvv 22,54,252
v_mfma_f32_16x16x32_bf16 v[26:29], a[0:3], v[38:41], 0
s_waitcnt lgkmcnt(1)
R 54,256
v_mul_f32 v71, v71, v99
v_mfma_f32_16x16x32_bf16 v[30:33], a[4:7], v[38:41], 0
v_mul_f32 v73, v73, v99
v_mul_f32 v75, v75, v99
v_mfma_f32_16x16x32_bf16 v[34:37], v[38:41], a[8:11], 0
v_fma_f32 v22, v22, v70, v71
v_fma_f32 v23, v23, v72, v73
mav 26,12,42
R 58,320
v_fma_f32 v24, v24, v74, v75
mav 30,16,42
v_mul_f32 v77, v77, v99
v_fma_f32 v25, v25, v76, v77
mva 34,42,20
v_cvt_pk_bf16_f32 v8, v22, v23
v_cvt_pk_bf16_f32 v9, v24, v25
mav 26,24,46
R 38,384
v_mul_f32 v124, v123, v62
v_mfma_f32_16x16x32_bf16 v[70:73], v[104:107], v[0:3], v[86:89]
v_fma_f32 v14, v14, v122, v124
v_mul_f32 v125, v123, v63
mav 30,28,46
v_fma_f32 v15, v15, v122, v125
v_mul_f32 v124, v123, v64
mva 34,46,32
s_waitcnt lgkmcnt(2)
v_fma_f32 v16, v16, v122, v124
v_mul_f32 v125, v123, v65
mav 26,36,50
R 42,448
v_fma_f32 v17, v17, v122, v125
mav 30,40,50
v_mul_f32 v124, v123, v66
v_fma_f32 v18, v18, v122, v124
mva 34,50,44
v_mul_f32 v125, v123, v67
v_fma_f32 v19, v19, v122, v125
mav 26,48,54
R 46,512
v_mul_f32 v124, v123, v68
mav 30,52,54
v_fma_f32 v20, v20, v122, v124
v_mul_f32 v125, v123, v69
mva 34,54,56
s_waitcnt lgkmcnt(2)
v_fma_f32 v21, v21, v122, v125
v_add_u32 v78, 1, v78
mav 26,60,58
R 50,576
mav 30,64,58
mva 34,58,68
mav 26,72,38
R 54,640
mav 30,76,38
mva 34,38,80
s_waitcnt lgkmcnt(2)
mav 26,84,42
R 58,704
mav 30,88,42
mva 34,42,92
mav 26,96,46
R 38,768
mav 30,100,46
s_barrier
mva 34,46,104
s_waitcnt lgkmcnt(2)
ds_read_b128 v[100:103], v82 offset:0
mav 26,108,50
ds_read_b64 v[108:109], v82 offset:1024
mav 30,112,50
R 42,832
mva 34,50,116
mav 26,120,54
s_waitcnt vmcnt(0)
buffer_store_dword v78, off, s[36:39], s52 sc1
mav 30,124,54
R 46,896
mva 34,54,128
s_waitcnt lgkmcnt(3)
v_mfma_f32_16x16x32_bf16 v[74:77], v[100:103], v[0:3], v[90:93]
v_cvt_pk_bf16_f32 v0, v14, v15
v_cvt_pk_bf16_f32 v1, v16, v17
mav 26,132,58
R 50,960
v_cvt_pk_bf16_f32 v2, v18, v19
mav 30,136,58
v_cvt_pk_bf16_f32 v3, v20, v21
mva 34,58,140
v_max3_f32 v118, v70, v71, v74
v_max3_f32 v119, v72, v73, v76
mav 26,144,38
R 54,1024
v_max_f32 v118, v118, v75
mav 30,148,38
v_max_f32 v119, v119, v77
v_max_f32 v120, v118, v119
mva 34,38,152
s_waitcnt lgkmcnt(2)
v_max_f32 v121, v118, v119
mav 26,156,42
R 38,1088
mav 30,160,42
v_permlane32_swap_b32 v120, v121
mva 34,42,164
v_max_f32 v114, v120, v121
v_max_f32 v115, v120, v121
mav 26,168,46
R 42,1152
mav 30,172,46
v_permlane16_swap_b32 v114, v115
mva 34,46,176
s_waitcnt lgkmcnt(2)
v_max_f32 v120, v114, v115
v_mul_f32 v120, s65, v120
mav 26,180,50
R 46,1216
v_fma_f32 v118, v118, s65, -v120
mav 30,184,50
v_fma_f32 v119, v119, s65, -v120
mva 34,50,188
v_exp_f32 v118, v118
mav 26,192,54
R 50,1280
mav 30,196,54
v_exp_f32 v119, v119
mva 34,54,200
s_waitcnt lgkmcnt(2)
v_add_f32 v120, v118, v119
v_add_f32 v121, v118, v119
mav 26,204,38
R 54,1344
mav 30,208,38
v_permlane32_swap_b32 v120, v121
mva 34,38,212
v_add_f32 v114, v120, v121
v_add_f32 v115, v120, v121
mav 26,216,42
R 38,1408
mav 30,220,42
v_permlane16_swap_b32 v114, v115
mva 34,42,224
s_waitcnt lgkmcnt(2)
v_add_f32 v120, v114, v115
mav 26,228,46
R 42,1472
mav 30,232,46
v_rcp_f32 v120, v120
mva 34,46,236
v_mul_f32 v118, v118, v120
v_mul_f32 v119, v119, v120
mav 26,240,50
R 46,1536
v_cvt_pk_bf16_f32 v118, v118, v118
mav 30,244,50
v_cvt_pk_bf16_f32 v119, v119, v119
v_and_b32 v114, v118, v96
mva 34,50,248
s_waitcnt lgkmcnt(2)
v_and_b32 v115, v119, v97
v_and_b32 v116, v118, v94
mav 26,252,54
R 50,1600
v_and_b32 v117, v119, v95
mvv 30,128,54
mvv 34,54,132
v_mfma_f32_16x16x32_bf16 v[70:73], v[8:11], v[114:117], 0
v_mfma_f32_16x16x32_bf16 v[74:77], v[108:111], v[114:117], 0
mvv 26,136,38
R 54,1664
mvv 30,140,38
mvv 34,38,144
s_waitcnt lgkmcnt(2)
mvv 26,148,42
R 38,1728
v_cvt_pk_bf16_f32 v114, v70, v71
mvv 30,152,42
v_cvt_pk_bf16_f32 v115, v72, v73
v_cvt_pk_bf16_f32 v116, v74, v75
mvv 34,42,156
v_cvt_pk_bf16_f32 v117, v76, v77
mvv 26,160,46
buffer_store_dwordx4 v[114:117], v82, s[36:39], s49 offen sc1
R 42,1792
s_add_u32 s49, s49, 313344
mvv 30,164,46
mvv 34,46,168
s_waitcnt lgkmcnt(2)
mvv 26,172,50
R 46,1856
mvv 30,176,50
mvv 34,50,180
mvv 26,184,54
R 50,1920
mvv 30,188,54
s_waitcnt lgkmcnt(2)
mvv 34,54,192
s_mov_b32 s60, -13
.p2align 6
LQA0_1:
mvv 26,196,38
R 54,1984
v_xor_b32 v80, v81, v80
mvv 30,200,38
mvv 34,38,204
mvv 26,208,42
R 38,33024
mvv 30,212,42
ds_read_b64 v[122:123], v83 offset:128
mvv 34,42,216
s_waitcnt lgkmcnt(3)
ds_read_b128 v[70:73], v84 offset:128
mvv 26,220,46
R 42,33088
mvv 30,224,46
ds_read_b128 v[74:77], v84 offset:144
mvv 34,46,228
mvv 26,232,50
R 46,33152
mvv 30,236,50
mvv 34,50,240
s_waitcnt lgkmcnt(3)
mvv 26,244,54
R 50,33216
mvv 30,248,54
mvv 34,54,252
v_mfma_f32_16x16x32_bf16 v[14:17], a[0:3], v[38:41], 0
s_waitcnt lgkmcnt(1)
R 54,33280
v_mul_f32 v71, v71, v99
v_mfma_f32_16x16x32_bf16 v[18:21], a[4:7], v[38:41], 0
v_mul_f32 v73, v73, v99
v_mul_f32 v75, v75, v99
v_mfma_f32_16x16x32_bf16 v[22:25], v[38:41], a[8:11], 0
v_fma_f32 v34, v34, v70, v71
v_fma_f32 v35, v35, v72, v73
mav 14,12,42
R 58,33344
v_fma_f32 v36, v36, v74, v75
mav 18,16,42
v_mul_f32 v77, v77, v99
v_fma_f32 v37, v37, v76, v77
mva 22,42,20
v_cvt_pk_bf16_f32 v10, v34, v35
v_cvt_pk_bf16_f32 v11, v36, v37
mav 14,24,46
R 38,33408
v_mul_f32 v124, v123, v62
v_mfma_f32_16x16x32_bf16 v[70:73], v[100:103], v[0:3], v[86:89]
v_fma_f32 v26, v26, v122, v124
v_mul_f32 v125, v123, v63
mav 18,28,46
v_fma_f32 v27, v27, v122, v125
v_mul_f32 v124, v123, v64
mva 22,46,32
s_waitcnt lgkmcnt(2)
v_fma_f32 v28, v28, v122, v124
v_mul_f32 v125, v123, v65
mav 14,36,50
R 42,33472
v_fma_f32 v29, v29, v122, v125
mav 18,40,50
v_mul_f32 v124, v123, v66
v_fma_f32 v30, v30, v122, v124
mva 22,50,44
v_mul_f32 v125, v123, v67
v_fma_f32 v31, v31, v122, v125
mav 14,48,54
R 46,33536
v_mul_f32 v124, v123, v68
mav 18,52,54
v_fma_f32 v32, v32, v122, v124
v_mul_f32 v125, v123, v69
mva 22,54,56
s_waitcnt lgkmcnt(2)
v_fma_f32 v33, v33, v122, v125
v_add_u32 v78, 1, v78
mav 14,60,58
R 50,33600
mav 18,64,58
mva 22,58,68
mav 14,72,38
R 54,33664
mav 18,76,38
mva 22,38,80
s_waitcnt lgkmcnt(2)
mav 14,84,42
R 58,33728
mav 18,88,42
mva 22,42,92
mav 14,96,46
R 38,33792
mav 18,100,46
s_barrier
mva 22,46,104
s_waitcnt lgkmcnt(2)
ds_read_b128 v[104:107], v82 offset:4096
mav 14,108,50
ds_read_b64 v[110:111], v82 offset:5120
mav 18,112,50
R 42,33856
mva 22,50,116
mav 14,120,54
s_waitcnt vmcnt(0)
buffer_store_dword v78, off, s[36:39], s52 sc1
mav 18,124,54
R 46,33920
mva 22,54,128
s_waitcnt lgkmcnt(3)
v_mfma_f32_16x16x32_bf16 v[74:77], v[104:107], v[0:3], v[90:93]
v_cvt_pk_bf16_f32 v0, v26, v27
v_cvt_pk_bf16_f32 v1, v28, v29
mav 14,132,58
R 50,33984
v_cvt_pk_bf16_f32 v2, v30, v31
mav 18,136,58
v_cvt_pk_bf16_f32 v3, v32, v33
mva 22,58,140
v_max3_f32 v118, v70, v71, v74
v_max3_f32 v119, v72, v73, v76
mav 14,144,38
R 54,34048
v_max_f32 v118, v118, v75
mav 18,148,38
v_max_f32 v119, v119, v77
v_max_f32 v120, v118, v119
mva 22,38,152
s_waitcnt lgkmcnt(2)
v_max_f32 v121, v118, v119
mav 14,156,42
R 38,34112
mav 18,160,42
v_permlane32_swap_b32 v120, v121
mva 22,42,164
v_max_f32 v114, v120, v121
v_max_f32 v115, v120, v121
mav 14,168,46
R 42,34176
mav 18,172,46
v_permlane16_swap_b32 v114, v115
mva 22,46,176
s_waitcnt lgkmcnt(2)
v_max_f32 v120, v114, v115
v_mul_f32 v120, s65, v120
mav 14,180,50
R 46,34240
LsQ3:
s_cbranch_execz LsQ7
v_fma_f32 v118, v118, s65, -v120
mav 18,184,50
v_fma_f32 v119, v119, s65, -v120
mva 22,50,188
v_exp_f32 v118, v118
mav 14,192,54
R 50,34304
mav 18,196,54
v_exp_f32 v119, v119
mva 22,54,200
s_waitcnt lgkmcnt(2)
v_add_f32 v120, v118, v119
v_add_f32 v121, v118, v119
mav 14,204,38
R 54,34368
mav 18,208,38
v_permlane32_swap_b32 v120, v121
mva 22,38,212
v_add_f32 v114, v120, v121
v_add_f32 v115, v120, v121
mav 14,216,42
R 38,34432
mav 18,220,42
v_permlane16_swap_b32 v114, v115
mva 22,42,224
s_waitcnt lgkmcnt(2)
v_add_f32 v120, v114, v115
mav 14,228,46
R 42,34496
mav 18,232,46
v_rcp_f32 v120, v120
mva 22,46,236
v_mul_f32 v118, v118, v120
v_mul_f32 v119, v119, v120
mav 14,240,50
R 46,34560
v_cvt_pk_bf16_f32 v118, v118, v118
mav 18,244,50
v_cvt_pk_bf16_f32 v119, v119, v119
v_and_b32 v114, v118, v94
mva 22,50,248
s_waitcnt lgkmcnt(2)
v_and_b32 v115, v119, v95
v_and_b32 v116, v118, v96
mav 14,252,54
R 50,34624
v_and_b32 v117, v119, v97
mvv 18,128,54
mvv 22,54,132
v_mfma_f32_16x16x32_bf16 v[70:73], v[8:11], v[114:117], 0
v_mfma_f32_16x16x32_bf16 v[74:77], v[108:111], v[114:117], 0
mvv 14,136,38
R 54,34688
mvv 18,140,38
mvv 22,38,144
s_waitcnt lgkmcnt(2)
mvv 14,148,42
R 38,34752
v_cvt_pk_bf16_f32 v114, v70, v71
mvv 18,152,42
v_cvt_pk_bf16_f32 v115, v72, v73
v_cvt_pk_bf16_f32 v116, v74, v75
mvv 22,42,156
v_cvt_pk_bf16_f32 v117, v76, v77
mvv 14,160,46
buffer_store_dwordx4 v[114:117], v82, s[36:39], s49 offen sc1
R 42,34816
s_add_u32 s49, s49, 313344
mvv 18,164,46
mvv 22,46,168
s_waitcnt lgkmcnt(2)
mvv 14,172,50
R 46,34880
mvv 18,176,50
mvv 22,50,180
mvv 14,184,54
R 50,34944
mvv 18,188,54
mvv 22,54,192
s_waitcnt lgkmcnt(2)
mvv 14,196,38
R 54,35008
mvv 18,200,38
mvv 22,38,204
mvv 14,208,42
R 38,0
mvv 18,212,42
ds_read_b64 v[122:123], v83 offset:0
mvv 22,42,216
s_waitcnt lgkmcnt(3)
ds_read_b128 v[70:73], v84 offset:0
mvv 14,220,46
R 42,64
mvv 18,224,46
ds_read_b128 v[74:77], v84 offset:16
mvv 22,46,228
mvv 14,232,50
R 46,128
mvv 18,236,50
mvv 22,50,240
s_waitcnt lgkmcnt(3)
mvv 14,244,54
R 50,192
mvv 18,248,54
mvv 22,54,252
v_mfma_f32_16x16x32_bf16 v[26:29], a[0:3], v[38:41], 0
s_waitcnt lgkmcnt(1)
R 54,256
v_mul_f32 v71, v71, v99
v_mfma_f32_16x16x32_bf16 v[30:33], a[4:7], v[38:41], 0
v_mul_f32 v73, v73, v99
v_mul_f32 v75, v75, v99
v_mfma_f32_16x16x32_bf16 v[34:37], v[38:41], a[8:11], 0
v_fma_f32 v22, v22, v70, v71
v_fma_f32 v23, v23, v72, v73
mav 26,12,42
R 58,320
v_fma_f32 v24, v24, v74, v75
mav 30,16,42
v_mul_f32 v77, v77, v99
v_fma_f32 v25, v25, v76, v77
mva 34,42,20
v_cvt_pk_bf16_f32 v8, v22, v23
v_cvt_pk_bf16_f32 v9, v24, v25
mav 26,24,46
R 38,384
v_mul_f32 v124, v123, v62
v_mfma_f32_16x16x32_bf16 v[70:73], v[104:107], v[0:3], v[86:89]
v_fma_f32 v14, v14, v122, v124
v_mul_f32 v125, v123, v63
mav 30,28,46
v_fma_f32 v15, v15, v122, v125
v_mul_f32 v124, v123, v64
mva 34,46,32
s_waitcnt lgkmcnt(2)
v_fma_f32 v16, v16, v122, v124
v_mul_f32 v125, v123, v65
mav 26,36,50
R 42,448
v_fma_f32 v17, v17, v122, v125
mav 30,40,50
v_mul_f32 v124, v123, v66
v_fma_f32 v18, v18, v122, v124
mva 34,50,44
v_mul_f32 v125, v123, v67
v_fma_f32 v19, v19, v122, v125
mav 26,48,54
R 46,512
v_mul_f32 v124, v123, v68
mav 30,52,54
v_fma_f32 v20, v20, v122, v124
v_mul_f32 v125, v123, v69
mva 34,54,56
s_waitcnt lgkmcnt(2)
v_fma_f32 v21, v21, v122, v125
v_add_u32 v78, 1, v78
mav 26,60,58
R 50,576
mav 30,64,58
mva 34,58,68
mav 26,72,38
R 54,640
mav 30,76,38
mva 34,38,80
s_waitcnt lgkmcnt(2)
mav 26,84,42
R 58,704
mav 30,88,42
mva 34,42,92
mav 26,96,46
R 38,768
mav 30,100,46
s_barrier
mva 34,46,104
s_waitcnt lgkmcnt(2)
ds_read_b128 v[100:103], v82 offset:0
mav 26,108,50
ds_read_b64 v[108:109], v82 offset:1024
mav 30,112,50
R 42,832
mva 34,50,116
mav 26,120,54
s_waitcnt vmcnt(0)
buffer_store_dword v78, off, s[36:39], s52 sc1
mav 30,124,54
R 46,896
mva 34,54,128
s_waitcnt lgkmcnt(3)
v_mfma_f32_16x16x32_bf16 v[74:77], v[100:103], v[0:3], v[90:93]
v_cvt_pk_bf16_f32 v0, v14, v15
v_cvt_pk_bf16_f32 v1, v16, v17
mav 26,132,58
R 50,960
v_cvt_pk_bf16_f32 v2, v18, v19
mav 30,136,58
v_cvt_pk_bf16_f32 v3, v20, v21
mva 34,58,140
v_max3_f32 v118, v70, v71, v74
v_max3_f32 v119, v72, v73, v76
mav 26,144,38
R 54,1024
v_max_f32 v118, v118, v75
mav 30,148,38
v_max_f32 v119, v119, v77
v_max_f32 v120, v118, v119
mva 34,38,152
s_waitcnt lgkmcnt(2)
v_max_f32 v121, v118, v119
mav 26,156,42
R 38,1088
mav 30,160,42
v_permlane32_swap_b32 v120, v121
mva 34,42,164
v_max_f32 v114, v120, v121
v_max_f32 v115, v120, v121
mav 26,168,46
R 42,1152
mav 30,172,46
v_permlane16_swap_b32 v114, v115
mva 34,46,176
s_waitcnt lgkmcnt(2)
v_max_f32 v120, v114, v115
v_mul_f32 v120, s65, v120
mav 26,180,50
R 46,1216
v_fma_f32 v118, v118, s65, -v120
mav 30,184,50
v_fma_f32 v119, v119, s65, -v120
mva 34,50,188
v_exp_f32 v118, v118
mav 26,192,54
R 50,1280
mav 30,196,54
v_exp_f32 v119, v119
mva 34,54,200
s_waitcnt lgkmcnt(2)
v_add_f32 v120, v118, v119
v_add_f32 v121, v118, v119
mav 26,204,38
R 54,1344
mav 30,208,38
v_permlane32_swap_b32 v120, v121
mva 34,38,212
v_add_f32 v114, v120, v121
v_add_f32 v115, v120, v121
mav 26,216,42
R 38,1408
mav 30,220,42
v_permlane16_swap_b32 v114, v115
mva 34,42,224
s_waitcnt lgkmcnt(2)
v_add_f32 v120, v114, v115
mav 26,228,46
R 42,1472
mav 30,232,46
v_rcp_f32 v120, v120
mva 34,46,236
v_mul_f32 v118, v118, v120
v_mul_f32 v119, v119, v120
mav 26,240,50
R 46,1536
v_cvt_pk_bf16_f32 v118, v118, v118
mav 30,244,50
v_cvt_pk_bf16_f32 v119, v119, v119
v_and_b32 v114, v118, v96
mva 34,50,248
s_waitcnt lgkmcnt(2)
v_and_b32 v115, v119, v97
v_and_b32 v116, v118, v94
mav 26,252,54
R 50,1600
v_and_b32 v117, v119, v95
mvv 30,128,54
mvv 34,54,132
v_mfma_f32_16x16x32_bf16 v[70:73], v[8:11], v[114:117], 0
v_mfma_f32_16x16x32_bf16 v[74:77], v[108:111], v[114:117], 0
mvv 26,136,38
R 54,1664
mvv 30,140,38
mvv 34,38,144
s_waitcnt lgkmcnt(2)
mvv 26,148,42
R 38,1728
v_cvt_pk_bf16_f32 v114, v70, v71
mvv 30,152,42
v_cvt_pk_bf16_f32 v115, v72, v73
v_cvt_pk_bf16_f32 v116, v74, v75
mvv 34,42,156
v_cvt_pk_bf16_f32 v117, v76, v77
mvv 26,160,46
buffer_store_dwordx4 v[114:117], v82, s[36:39], s49 offen sc1
R 42,1792
s_add_u32 s49, s49, 313344
mvv 30,164,46
mvv 34,46,168
s_waitcnt lgkmcnt(2)
mvv 26,172,50
R 46,1856
mvv 30,176,50
mvv 34,50,180
mvv 26,184,54
R 50,1920
mvv 30,188,54
s_waitcnt lgkmcnt(2)
s_add_u32 s60, s60, 1
mvv 34,54,192
s_cbranch_scc0 LQA0_1
mvv 26,196,38
R 54,1984
v_xor_b32 v80, v81, v80
mvv 30,200,38
mvv 34,38,204
mvv 26,208,42
R 38,33024
mvv 30,212,42
ds_read_b64 v[122:123], v83 offset:128
mvv 34,42,216
s_waitcnt lgkmcnt(3)
ds_read_b128 v[70:73], v84 offset:128
mvv 26,220,46
R 42,33088
mvv 30,224,46
ds_read_b128 v[74:77], v84 offset:144
mvv 34,46,228
mvv 26,232,50
R 46,33152
mvv 30,236,50
mvv 34,50,240
s_waitcnt lgkmcnt(3)
mvv 26,244,54
R 50,33216
mvv 30,248,54
mvv 34,54,252
v_mfma_f32_16x16x32_bf16 v[14:17], a[0:3], v[38:41], 0
s_waitcnt lgkmcnt(1)
R 54,33280
v_mul_f32 v71, v71, v99
v_mfma_f32_16x16x32_bf16 v[18:21], a[4:7], v[38:41], 0
v_mul_f32 v73, v73, v99
v_mul_f32 v75, v75, v99
v_mfma_f32_16x16x32_bf16 v[22:25], v[38:41], a[8:11], 0
v_fma_f32 v34, v34, v70, v71
v_fma_f32 v35, v35, v72, v73
mav 14,12,42
R 58,33344
v_fma_f32 v36, v36, v74, v75
mav 18,16,42
v_mul_f32 v77, v77, v99
v_fma_f32 v37, v37, v76, v77
mva 22,42,20
v_cvt_pk_bf16_f32 v10, v34, v35
v_cvt_pk_bf16_f32 v11, v36, v37
mav 14,24,46
R 38,33408
v_mul_f32 v124, v123, v62
v_mfma_f32_16x16x32_bf16 v[70:73], v[100:103], v[0:3], v[86:89]
v_fma_f32 v26, v26, v122, v124
v_mul_f32 v125, v123, v63
mav 18,28,46
v_fma_f32 v27, v27, v122, v125
v_mul_f32 v124, v123, v64
mva 22,46,32
s_waitcnt lgkmcnt(2)
v_fma_f32 v28, v28, v122, v124
v_mul_f32 v125, v123, v65
mav 14,36,50
R 42,33472
v_fma_f32 v29, v29, v122, v125
mav 18,40,50
v_mul_f32 v124, v123, v66
v_fma_f32 v30, v30, v122, v124
mva 22,50,44
v_mul_f32 v125, v123, v67
v_fma_f32 v31, v31, v122, v125
mav 14,48,54
R 46,33536
v_mul_f32 v124, v123, v68
mav 18,52,54
v_fma_f32 v32, v32, v122, v124
v_mul_f32 v125, v123, v69
mva 22,54,56
s_waitcnt lgkmcnt(2)
v_fma_f32 v33, v33, v122, v125
v_add_u32 v78, 1, v78
mav 14,60,58
R 50,33600
mav 18,64,58
mva 22,58,68
mav 14,72,38
R 54,33664
mav 18,76,38
mva 22,38,80
s_waitcnt lgkmcnt(2)
mav 14,84,42
R 58,33728
mav 18,88,42
mva 22,42,92
mav 14,96,46
R 38,33792
mav 18,100,46
s_barrier
mva 22,46,104
s_waitcnt lgkmcnt(2)
ds_read_b128 v[104:107], v82 offset:4096
mav 14,108,50
ds_read_b64 v[110:111], v82 offset:5120
mav 18,112,50
R 42,33856
mva 22,50,116
mav 14,120,54
s_waitcnt vmcnt(0)
buffer_store_dword v78, off, s[36:39], s52 sc1
mav 18,124,54
R 46,33920
mva 22,54,128
s_waitcnt lgkmcnt(3)
v_mfma_f32_16x16x32_bf16 v[74:77], v[104:107], v[0:3], v[90:93]
v_cvt_pk_bf16_f32 v0, v26, v27
v_cvt_pk_bf16_f32 v1, v28, v29
mav 14,132,58
R 50,33984
v_cvt_pk_bf16_f32 v2, v30, v31
mav 18,136,58
v_cvt_pk_bf16_f32 v3, v32, v33
mva 22,58,140
v_max3_f32 v118, v70, v71, v74
v_max3_f32 v119, v72, v73, v76
mav 14,144,38
R 54,34048
v_max_f32 v118, v118, v75
mav 18,148,38
v_max_f32 v119, v119, v77
v_max_f32 v120, v118, v119
mva 22,38,152
s_waitcnt lgkmcnt(2)
v_max_f32 v121, v118, v119
mav 14,156,42
R 38,34112
mav 18,160,42
v_permlane32_swap_b32 v120, v121
mva 22,42,164
v_max_f32 v114, v120, v121
v_max_f32 v115, v120, v121
mav 14,168,46
R 42,34176
mav 18,172,46
v_permlane16_swap_b32 v114, v115
mva 22,46,176
s_waitcnt lgkmcnt(2)
v_max_f32 v120, v114, v115
v_mul_f32 v120, s65, v120
mav 14,180,50
R 46,34240
v_fma_f32 v118, v118, s65, -v120
mav 18,184,50
v_fma_f32 v119, v119, s65, -v120
mva 22,50,188
v_exp_f32 v118, v118
mav 14,192,54
R 50,34304
mav 18,196,54
v_exp_f32 v119, v119
mva 22,54,200
s_waitcnt lgkmcnt(2)
v_add_f32 v120, v118, v119
v_add_f32 v121, v118, v119
mav 14,204,38
R 54,34368
mav 18,208,38
v_permlane32_swap_b32 v120, v121
mva 22,38,212
v_add_f32 v114, v120, v121
v_add_f32 v115, v120, v121
mav 14,216,42
R 38,34432
mav 18,220,42
v_permlane16_swap_b32 v114, v115
mva 22,42,224
s_waitcnt lgkmcnt(2)
v_add_f32 v120, v114, v115
mav 14,228,46
R 42,34496
mav 18,232,46
v_rcp_f32 v120, v120
mva 22,46,236
v_mul_f32 v118, v118, v120
v_mul_f32 v119, v119, v120
mav 14,240,50
R 46,34560
v_cvt_pk_bf16_f32 v118, v118, v118
mav 18,244,50
v_cvt_pk_bf16_f32 v119, v119, v119
v_and_b32 v114, v118, v94
mva 22,50,248
s_waitcnt lgkmcnt(2)
v_and_b32 v115, v119, v95
v_and_b32 v116, v118, v96
mav 14,252,54
R 50,34624
v_and_b32 v117, v119, v97
mvv 18,128,54
mvv 22,54,132
v_mfma_f32_16x16x32_bf16 v[70:73], v[8:11], v[114:117], 0
v_mfma_f32_16x16x32_bf16 v[74:77], v[108:111], v[114:117], 0
mvv 14,136,38
R 54,34688
mvv 18,140,38
mvv 22,38,144
s_waitcnt lgkmcnt(2)
mvv 14,148,42
R 38,34752
v_cvt_pk_bf16_f32 v114, v70, v71
mvv 18,152,42
v_cvt_pk_bf16_f32 v115, v72, v73
v_cvt_pk_bf16_f32 v116, v74, v75
mvv 22,42,156
v_cvt_pk_bf16_f32 v117, v76, v77
mvv 14,160,46
buffer_store_dwordx4 v[114:117], v82, s[36:39], s49 offen sc1
R 42,34816
s_add_u32 s49, s49, 313344
mvv 18,164,46
mvv 22,46,168
s_waitcnt lgkmcnt(2)
mvv 14,172,50
R 46,34880
mvv 18,176,50
mvv 22,50,180
)"
R"(
mvv 14,184,54
R 50,34944
mvv 18,188,54
mvv 22,54,192
s_waitcnt lgkmcnt(2)
mvv 14,196,38
R 54,35008
mvv 18,200,38
mvv 22,38,204
mvv 14,208,42
R 38,0
mvv 18,212,42
ds_read_b64 v[122:123], v83 offset:0
mvv 22,42,216
s_waitcnt lgkmcnt(3)
ds_read_b128 v[70:73], v84 offset:0
mvv 14,220,46
R 42,64
mvv 18,224,46
ds_read_b128 v[74:77], v84 offset:16
mvv 22,46,228
mvv 14,232,50
R 46,128
mvv 18,236,50
mvv 22,50,240
s_waitcnt lgkmcnt(3)
mvv 14,244,54
R 50,192
mvv 18,248,54
mvv 22,54,252
v_mfma_f32_16x16x32_bf16 v[26:29], a[0:3], v[38:41], 0
s_waitcnt lgkmcnt(1)
R 54,256
v_mul_f32 v71, v71, v99
v_mfma_f32_16x16x32_bf16 v[30:33], a[4:7], v[38:41], 0
v_mul_f32 v73, v73, v99
v_mul_f32 v75, v75, v99
v_mfma_f32_16x16x32_bf16 v[34:37], v[38:41], a[8:11], 0
v_fma_f32 v22, v22, v70, v71
v_fma_f32 v23, v23, v72, v73
mav 26,12,42
R 58,320
v_fma_f32 v24, v24, v74, v75
mav 30,16,42
v_mul_f32 v77, v77, v99
v_fma_f32 v25, v25, v76, v77
mva 34,42,20
v_cvt_pk_bf16_f32 v8, v22, v23
v_cvt_pk_bf16_f32 v9, v24, v25
mav 26,24,46
R 38,384
v_mul_f32 v124, v123, v62
v_mfma_f32_16x16x32_bf16 v[70:73], v[104:107], v[0:3], v[86:89]
v_fma_f32 v14, v14, v122, v124
v_mul_f32 v125, v123, v63
mav 30,28,46
v_fma_f32 v15, v15, v122, v125
v_mul_f32 v124, v123, v64
LsQ4:
s_cbranch_execz LsQ8
mva 34,46,32
s_waitcnt lgkmcnt(2)
v_fma_f32 v16, v16, v122, v124
v_mul_f32 v125, v123, v65
mav 26,36,50
R 42,448
v_fma_f32 v17, v17, v122, v125
mav 30,40,50
buffer_load_dwordx4 v[4:7], v82, s[36:39], s55 offen sc1
mva 34,50,44
v_mul_f32 v124, v123, v66
v_fma_f32 v18, v18, v122, v124
mav 26,48,54
buffer_load_dwordx2 v[12:13], v98, s[36:39], s55 offen offset:1024 sc1
mav 30,52,54
R 46,512
v_mul_f32 v125, v123, v67
mva 34,54,56
s_waitcnt lgkmcnt(2)
buffer_load_dwordx2 v[112:113], v98, s[36:39], s55 offen offset:1536 sc1
mav 26,60,58
R 50,576
v_fma_f32 v19, v19, v122, v125
mav 30,64,58
v_mul_f32 v124, v123, v68
v_fma_f32 v20, v20, v122, v124
mva 34,58,68
v_mul_f32 v125, v123, v69
v_fma_f32 v21, v21, v122, v125
mav 26,72,38
R 54,640
v_add_u32 v78, 1, v78
mav 30,76,38
mva 34,38,80
s_waitcnt lgkmcnt(2)
mav 26,84,42
R 58,704
mav 30,88,42
mva 34,42,92
mav 26,96,46
R 38,768
mav 30,100,46
s_barrier
mva 34,46,104
s_waitcnt lgkmcnt(2)
ds_read_b128 v[100:103], v82 offset:0
mav 26,108,50
ds_read_b64 v[108:109], v82 offset:1024
mav 30,112,50
R 42,832
mva 34,50,116
mav 26,120,54
s_waitcnt vmcnt(3)
buffer_store_dword v78, off, s[36:39], s52 sc1
mav 30,124,54
R 46,896
mva 34,54,128
s_waitcnt lgkmcnt(3)
v_mfma_f32_16x16x32_bf16 v[74:77], v[100:103], v[0:3], v[90:93]
v_cvt_pk_bf16_f32 v0, v14, v15
v_cvt_pk_bf16_f32 v1, v16, v17
mav 26,132,58
R 50,960
v_cvt_pk_bf16_f32 v2, v18, v19
mav 30,136,58
v_cvt_pk_bf16_f32 v3, v20, v21
mva 34,58,140
v_max3_f32 v118, v70, v71, v74
v_max3_f32 v119, v72, v73, v76
mav 26,144,38
R 54,1024
v_max_f32 v118, v118, v75
mav 30,148,38
v_max_f32 v119, v119, v77
v_max_f32 v120, v118, v119
mva 34,38,152
s_waitcnt lgkmcnt(2)
v_max_f32 v121, v118, v119
mav 26,156,42
R 38,1088
mav 30,160,42
v_permlane32_swap_b32 v120, v121
mva 34,42,164
v_max_f32 v114, v120, v121
v_max_f32 v115, v120, v121
mav 26,168,46
R 42,1152
mav 30,172,46
v_permlane16_swap_b32 v114, v115
mva 34,46,176
s_waitcnt lgkmcnt(2)
v_max_f32 v120, v114, v115
v_mul_f32 v120, s65, v120
mav 26,180,50
R 46,1216
v_fma_f32 v118, v118, s65, -v120
mav 30,184,50
v_fma_f32 v119, v119, s65, -v120
mva 34,50,188
v_exp_f32 v118, v118
mav 26,192,54
R 50,1280
mav 30,196,54
v_exp_f32 v119, v119
mva 34,54,200
s_waitcnt lgkmcnt(2)
v_add_f32 v120, v118, v119
v_add_f32 v121, v118, v119
mav 26,204,38
R 54,1344
mav 30,208,38
v_permlane32_swap_b32 v120, v121
mva 34,38,212
v_add_f32 v114, v120, v121
v_add_f32 v115, v120, v121
mav 26,216,42
R 38,1408
mav 30,220,42
v_permlane16_swap_b32 v114, v115
mva 34,42,224
s_waitcnt lgkmcnt(2)
v_add_f32 v120, v114, v115
mav 26,228,46
R 42,1472
mav 30,232,46
v_rcp_f32 v120, v120
mva 34,46,236
v_mul_f32 v118, v118, v120
v_mul_f32 v119, v119, v120
mav 26,240,50
R 46,1536
v_cvt_pk_bf16_f32 v118, v118, v118
mav 30,244,50
v_cvt_pk_bf16_f32 v119, v119, v119
v_and_b32 v114, v118, v96
mva 34,50,248
s_waitcnt lgkmcnt(2)
v_and_b32 v115, v119, v97
v_and_b32 v116, v118, v94
mav 26,252,54
R 50,1600
v_and_b32 v117, v119, v95
mvv 30,128,54
mvv 34,54,132
v_mfma_f32_16x16x32_bf16 v[70:73], v[8:11], v[114:117], 0
v_mfma_f32_16x16x32_bf16 v[74:77], v[108:111], v[114:117], 0
mvv 26,136,38
R 54,1664
mvv 30,140,38
mvv 34,38,144
s_waitcnt lgkmcnt(2)
mvv 26,148,42
R 38,1728
v_cvt_pk_bf16_f32 v114, v70, v71
mvv 30,152,42
v_cvt_pk_bf16_f32 v115, v72, v73
v_cvt_pk_bf16_f32 v116, v74, v75
mvv 34,42,156
v_cvt_pk_bf16_f32 v117, v76, v77
mvv 26,160,46
buffer_store_dwordx4 v[114:117], v82, s[36:39], s49 offen sc1
R 42,1792
s_add_u32 s49, s49, 313344
mvv 30,164,46
mvv 34,46,168
s_waitcnt lgkmcnt(2)
mvv 26,172,50
R 46,1856
mvv 30,176,50
mvv 34,50,180
mvv 26,184,54
R 50,1920
mvv 30,188,54
s_waitcnt lgkmcnt(2)
mvv 34,54,192
mvv 26,196,38
R 54,1984
v_xor_b32 v80, v81, v80
mvv 30,200,38
mvv 34,38,204
mvv 26,208,42
R 38,33024
mvv 30,212,42
ds_read_b64 v[122:123], v83 offset:128
mvv 34,42,216
s_waitcnt lgkmcnt(3)
ds_read_b128 v[70:73], v84 offset:128
mvv 26,220,46
R 42,33088
mvv 30,224,46
ds_read_b128 v[74:77], v84 offset:144
mvv 34,46,228
mvv 26,232,50
R 46,33152
mvv 30,236,50
mvv 34,50,240
s_waitcnt lgkmcnt(3)
mvv 26,244,54
R 50,33216
mvv 30,248,54
mvv 34,54,252
v_mfma_f32_16x16x32_bf16 v[14:17], a[0:3], v[38:41], 0
s_waitcnt lgkmcnt(1)
R 54,33280
v_mul_f32 v71, v71, v99
v_mfma_f32_16x16x32_bf16 v[18:21], a[4:7], v[38:41], 0
v_mul_f32 v73, v73, v99
v_mul_f32 v75, v75, v99
v_mfma_f32_16x16x32_bf16 v[22:25], v[38:41], a[8:11], 0
v_fma_f32 v34, v34, v70, v71
v_fma_f32 v35, v35, v72, v73
mav 14,12,42
R 58,33344
v_fma_f32 v36, v36, v74, v75
mav 18,16,42
v_mul_f32 v77, v77, v99
v_fma_f32 v37, v37, v76, v77
mva 22,42,20
v_cvt_pk_bf16_f32 v10, v34, v35
v_cvt_pk_bf16_f32 v11, v36, v37
mav 14,24,46
R 38,33408
v_mul_f32 v124, v123, v62
v_mfma_f32_16x16x32_bf16 v[70:73], v[100:103], v[0:3], v[86:89]
v_fma_f32 v26, v26, v122, v124
v_mul_f32 v125, v123, v63
mav 18,28,46
v_fma_f32 v27, v27, v122, v125
v_mul_f32 v124, v123, v64
mva 22,46,32
s_waitcnt lgkmcnt(2)
v_fma_f32 v28, v28, v122, v124
v_mul_f32 v125, v123, v65
mav 14,36,50
R 42,33472
v_fma_f32 v29, v29, v122, v125
mav 18,40,50
v_mul_f32 v124, v123, v66
v_fma_f32 v30, v30, v122, v124
mva 22,50,44
v_mul_f32 v125, v123, v67
v_fma_f32 v31, v31, v122, v125
mav 14,48,54
R 46,33536
v_mul_f32 v124, v123, v68
mav 18,52,54
v_fma_f32 v32, v32, v122, v124
v_mul_f32 v125, v123, v69
mva 22,54,56
s_waitcnt lgkmcnt(2)
v_fma_f32 v33, v33, v122, v125
v_add_u32 v78, 1, v78
mav 14,60,58
R 50,33600
mav 18,64,58
mva 22,58,68
mav 14,72,38
R 54,33664
mav 18,76,38
mva 22,38,80
s_waitcnt lgkmcnt(2)
mav 14,84,42
R 58,33728
mav 18,88,42
mva 22,42,92
mav 14,96,46
R 38,33792
mav 18,100,46
s_barrier
mva 22,46,104
s_waitcnt lgkmcnt(2)
ds_read_b128 v[104:107], v82 offset:4096
mav 14,108,50
ds_read_b64 v[110:111], v82 offset:5120
mav 18,112,50
R 42,33856
mva 22,50,116
mav 14,120,54
s_waitcnt vmcnt(0)
buffer_store_dword v78, off, s[36:39], s52 sc1
mav 18,124,54
R 46,33920
mva 22,54,128
s_waitcnt lgkmcnt(3)
v_mfma_f32_16x16x32_bf16 v[74:77], v[104:107], v[0:3], v[90:93]
v_cvt_pk_bf16_f32 v0, v26, v27
v_cvt_pk_bf16_f32 v1, v28, v29
mav 14,132,58
R 50,33984
v_cvt_pk_bf16_f32 v2, v30, v31
mav 18,136,58
v_cvt_pk_bf16_f32 v3, v32, v33
mva 22,58,140
v_max3_f32 v118, v70, v71, v74
v_max3_f32 v119, v72, v73, v76
mav 14,144,38
R 54,34048
v_max_f32 v118, v118, v75
mav 18,148,38
v_max_f32 v119, v119, v77
v_max_f32 v120, v118, v119
mva 22,38,152
s_waitcnt lgkmcnt(2)
v_max_f32 v121, v118, v119
mav 14,156,42
R 38,34112
mav 18,160,42
v_permlane32_swap_b32 v120, v121
mva 22,42,164
v_max_f32 v114, v120, v121
v_max_f32 v115, v120, v121
mav 14,168,46
R 42,34176
mav 18,172,46
v_permlane16_swap_b32 v114, v115
mva 22,46,176
s_waitcnt lgkmcnt(2)
v_max_f32 v120, v114, v115
v_mul_f32 v120, s65, v120
mav 14,180,50
R 46,34240
v_fma_f32 v118, v118, s65, -v120
mav 18,184,50
v_fma_f32 v119, v119, s65, -v120
mva 22,50,188
v_exp_f32 v118, v118
mav 14,192,54
R 50,34304
mav 18,196,54
v_exp_f32 v119, v119
mva 22,54,200
s_waitcnt lgkmcnt(2)
v_add_f32 v120, v118, v119
v_add_f32 v121, v118, v119
mav 14,204,38
R 54,34368
mav 18,208,38
v_permlane32_swap_b32 v120, v121
mva 22,38,212
v_add_f32 v114, v120, v121
v_add_f32 v115, v120, v121
mav 14,216,42
R 38,34432
mav 18,220,42
v_permlane16_swap_b32 v114, v115
mva 22,42,224
s_waitcnt lgkmcnt(2)
v_add_f32 v120, v114, v115
mav 14,228,46
R 42,34496
mav 18,232,46
v_rcp_f32 v120, v120
mva 22,46,236
v_mul_f32 v118, v118, v120
v_mul_f32 v119, v119, v120
mav 14,240,50
R 46,34560
v_cvt_pk_bf16_f32 v118, v118, v118
mav 18,244,50
v_cvt_pk_bf16_f32 v119, v119, v119
v_and_b32 v114, v118, v94
mva 22,50,248
s_waitcnt lgkmcnt(2)
v_and_b32 v115, v119, v95
v_and_b32 v116, v118, v96
mav 14,252,54
R 50,34624
v_and_b32 v117, v119, v97
mvv 18,128,54
mvv 22,54,132
v_mfma_f32_16x16x32_bf16 v[70:73], v[8:11], v[114:117], 0
v_mfma_f32_16x16x32_bf16 v[74:77], v[108:111], v[114:117], 0
mvv 14,136,38
R 54,34688
mvv 18,140,38
mvv 22,38,144
s_waitcnt lgkmcnt(2)
mvv 14,148,42
R 38,34752
v_cvt_pk_bf16_f32 v114, v70, v71
mvv 18,152,42
v_cvt_pk_bf16_f32 v115, v72, v73
v_cvt_pk_bf16_f32 v116, v74, v75
mvv 22,42,156
v_cvt_pk_bf16_f32 v117, v76, v77
mvv 14,160,46
buffer_store_dwordx4 v[114:117], v82, s[36:39], s49 offen sc1
R 42,34816
s_add_u32 s49, s49, 313344
mvv 18,164,46
mvv 22,46,168
s_waitcnt lgkmcnt(2)
mvv 14,172,50
R 46,34880
mvv 18,176,50
mvv 22,50,180
mvv 14,184,54
R 50,34944
mvv 18,188,54
mvv 22,54,192
s_waitcnt lgkmcnt(2)
mvv 14,196,38
R 54,35008
mvv 18,200,38
mvv 22,38,204
mvv 14,208,42
R 38,0
mvv 18,212,42
ds_read_b64 v[122:123], v83 offset:0
mvv 22,42,216
s_waitcnt lgkmcnt(3)
ds_read_b128 v[70:73], v84 offset:0
mvv 14,220,46
R 42,64
mvv 18,224,46
ds_read_b128 v[74:77], v84 offset:16
mvv 22,46,228
mvv 14,232,50
R 46,128
mvv 18,236,50
mvv 22,50,240
s_waitcnt lgkmcnt(3)
mvv 14,244,54
R 50,192
mvv 18,248,54
mvv 22,54,252
v_mfma_f32_16x16x32_bf16 v[26:29], a[0:3], v[38:41], 0
s_waitcnt lgkmcnt(1)
R 54,256
v_mul_f32 v71, v71, v99
v_mfma_f32_16x16x32_bf16 v[30:33], a[4:7], v[38:41], 0
v_mul_f32 v73, v73, v99
v_mul_f32 v75, v75, v99
v_mfma_f32_16x16x32_bf16 v[34:37], v[38:41], a[8:11], 0
v_fma_f32 v22, v22, v70, v71
v_fma_f32 v23, v23, v72, v73
mav 26,12,42
R 58,320
v_fma_f32 v24, v24, v74, v75
mav 30,16,42
v_mul_f32 v77, v77, v99
v_fma_f32 v25, v25, v76, v77
mva 34,42,20
v_cvt_pk_bf16_f32 v8, v22, v23
v_cvt_pk_bf16_f32 v9, v24, v25
mav 26,24,46
R 38,384
v_min_f32 v74, s56, v90
v_mfma_f32_16x16x32_bf16 v[70:73], v[104:107], v[0:3], v[86:89]
v_min_f32 v75, s56, v91
v_min_f32 v76, s56, v92
mav 30,28,46
v_min_f32 v77, s56, v93
v_mul_f32 v124, v123, v62
mva 34,46,32
s_waitcnt lgkmcnt(2)
v_fma_f32 v14, v14, v122, v124
v_mul_f32 v125, v123, v63
mvv 74,4,0
v_fma_f32 v15, v15, v122, v125
v_mul_f32 v124, v123, v64
mav 26,36,50
R 42,448
v_fma_f32 v16, v16, v122, v124
mav 30,40,50
v_mul_f32 v125, v123, v65
v_fma_f32 v17, v17, v122, v125
mva 34,50,44
v_max3_f32 v118, v70, v71, v74
v_max3_f32 v119, v72, v73, v76
mav 26,48,54
R 46,512
v_max_f32 v118, v118, v75
mav 30,52,54
v_max_f32 v119, v119, v77
v_max_f32 v120, v118, v119
mva 34,54,56
s_waitcnt lgkmcnt(2)
v_max_f32 v121, v118, v119
v_mul_f32 v124, v123, v66
mav 26,60,58
R 50,576
v_fma_f32 v18, v18, v122, v124
mav 30,64,58
v_permlane32_swap_b32 v120, v121
mva 34,58,68
v_max_f32 v114, v120, v121
v_max_f32 v115, v120, v121
mav 26,72,38
R 54,640
v_mul_f32 v125, v123, v67
mav 30,76,38
v_permlane16_swap_b32 v114, v115
mva 34,38,80
s_waitcnt lgkmcnt(2)
v_max_f32 v120, v114, v115
v_mul_f32 v120, s65, v120
mav 26,84,42
R 58,704
v_fma_f32 v118, v118, s65, -v120
mav 30,88,42
v_fma_f32 v119, v119, s65, -v120
v_fma_f32 v19, v19, v122, v125
mva 34,42,92
v_exp_f32 v118, v118
mav 26,96,46
R 38,768
v_mul_f32 v124, v123, v68
mav 30,100,46
s_barrier
mva 34,46,104
s_waitcnt lgkmcnt(2)
ds_read_b128 v[100:103], v82 offset:0
v_fma_f32 v20, v20, v122, v124
mav 26,108,50
ds_read_b64 v[108:109], v82 offset:1024
v_mul_f32 v125, v123, v69
mav 30,112,50
R 42,832
v_fma_f32 v21, v21, v122, v125
mva 34,50,116
v_exp_f32 v119, v119
mav 26,120,54
R 46,896
v_add_f32 v120, v118, v119
mav 30,124,54
v_add_f32 v121, v118, v119
v_cvt_pk_bf16_f32 v0, v14, v15
mva 34,54,128
s_waitcnt lgkmcnt(3)
v_permlane32_swap_b32 v120, v121
mav 26,132,58
R 50,960
v_add_f32 v114, v120, v121
mav 30,136,58
v_add_f32 v115, v120, v121
v_cvt_pk_bf16_f32 v1, v16, v17
mva 34,58,140
v_permlane16_swap_b32 v114, v115
mav 26,144,38
R 54,1024
v_add_f32 v120, v114, v115
mav 30,148,38
v_rcp_f32 v120, v120
mva 34,38,152
s_waitcnt lgkmcnt(2)
v_mul_f32 v118, v118, v120
v_mul_f32 v119, v119, v120
mav 26,156,42
R 38,1088
v_cvt_pk_bf16_f32 v118, v118, v118
mav 30,160,42
buffer_store_dwordx4 v[100:103], v82, s[36:39], s54 offen offset:2048 sc1
v_cvt_pk_bf16_f32 v119, v119, v119
v_and_b32 v114, v118, v94
v_and_b32 v115, v119, v95
mva 34,42,164
v_and_b32 v116, v118, v96
v_and_b32 v117, v119, v97
mav 26,168,46
buffer_store_dwordx2 v[8:9], v98, s[36:39], s54 offen offset:3072 sc1
R 42,1152
v_mfma_f32_16x16x32_bf16 v[70:73], v[10:13], v[114:117], 0
v_cvt_pk_bf16_f32 v2, v18, v19
v_cvt_pk_bf16_f32 v3, v20, v21
v_mfma_f32_16x16x32_bf16 v[74:77], v[110:113], v[114:117], 0
v_add_u32 v78, 1, v78
mav 30,172,46
s_waitcnt vmcnt(2)
mva 34,46,176
s_waitcnt lgkmcnt(2)
buffer_store_dword v78, off, s[36:39], s52 sc1
mav 26,180,50
R 46,1216
v_cvt_pk_bf16_f32 v114, v70, v71
mav 30,184,50
buffer_store_dwordx2 v[108:109], v98, s[36:39], s54 offen offset:3584 sc1
v_cvt_pk_bf16_f32 v115, v72, v73
mva 34,50,188
v_cvt_pk_bf16_f32 v116, v74, v75
v_cvt_pk_bf16_f32 v117, v76, v77
mav 26,192,54
buffer_store_dwordx4 v[114:117], v82, s[36:39], s49 offen sc1
R 50,1280
s_add_u32 s49, s49, 313344
mav 30,196,54
mva 34,54,200
s_waitcnt lgkmcnt(2)
mav 26,204,38
R 54,1344
mav 30,208,38
mva 34,38,212
LsQ5:
s_cbranch_execz LsQ9
mav 26,216,42
R 38,1408
mav 30,220,42
mva 34,42,224
s_waitcnt lgkmcnt(2)
mav 26,228,46
R 42,1472
mav 30,232,46
mva 34,46,236
mav 26,240,50
R 46,1536
mav 30,244,50
mva 34,50,248
s_waitcnt lgkmcnt(2)
mav 26,252,54
R 50,1600
mvv 30,128,54
mvv 34,54,132
mvv 26,136,38
R 54,1664
mvv 30,140,38
mvv 34,38,144
s_waitcnt lgkmcnt(2)
mvv 26,148,42
R 38,1728
mvv 30,152,42
mvv 34,42,156
mvv 26,160,46
R 42,1792
mvv 30,164,46
mvv 34,46,168
s_waitcnt lgkmcnt(2)
mvv 26,172,50
R 46,1856
mvv 30,176,50
mvv 34,50,180
mvv 26,184,54
R 50,1920
mvv 30,188,54
s_waitcnt lgkmcnt(2)
mvv 34,54,192
s_mov_b32 s60, -14
.p2align 6
LQA0_2:
mvv 26,196,38
R 54,1984
v_xor_b32 v80, v81, v80
mvv 30,200,38
mvv 34,38,204
mvv 26,208,42
R 38,33024
mvv 30,212,42
ds_read_b64 v[122:123], v83 offset:128
mvv 34,42,216
s_waitcnt lgkmcnt(3)
ds_read_b128 v[70:73], v84 offset:128
mvv 26,220,46
R 42,33088
mvv 30,224,46
ds_read_b128 v[74:77], v84 offset:144
mvv 34,46,228
mvv 26,232,50
R 46,33152
mvv 30,236,50
mvv 34,50,240
s_waitcnt lgkmcnt(3)
mvv 26,244,54
R 50,33216
mvv 30,248,54
mvv 34,54,252
v_mfma_f32_16x16x32_bf16 v[14:17], a[0:3], v[38:41], 0
s_waitcnt lgkmcnt(1)
R 54,33280
v_mul_f32 v71, v71, v99
v_mfma_f32_16x16x32_bf16 v[18:21], a[4:7], v[38:41], 0
v_mul_f32 v73, v73, v99
v_mul_f32 v75, v75, v99
v_mfma_f32_16x16x32_bf16 v[22:25], v[38:41], a[8:11], 0
v_fma_f32 v34, v34, v70, v71
v_fma_f32 v35, v35, v72, v73
mav 14,12,42
R 58,33344
v_fma_f32 v36, v36, v74, v75
mav 18,16,42
v_mul_f32 v77, v77, v99
v_fma_f32 v37, v37, v76, v77
mva 22,42,20
v_cvt_pk_bf16_f32 v10, v34, v35
v_cvt_pk_bf16_f32 v11, v36, v37
mav 14,24,46
R 38,33408
v_mul_f32 v124, v123, v62
v_mfma_f32_16x16x32_bf16 v[70:73], v[100:103], v[0:3], v[86:89]
v_fma_f32 v26, v26, v122, v124
v_mul_f32 v125, v123, v63
mav 18,28,46
v_fma_f32 v27, v27, v122, v125
v_mul_f32 v124, v123, v64
mva 22,46,32
s_waitcnt lgkmcnt(2)
v_fma_f32 v28, v28, v122, v124
v_mul_f32 v125, v123, v65
mav 14,36,50
R 42,33472
v_fma_f32 v29, v29, v122, v125
mav 18,40,50
v_mul_f32 v124, v123, v66
v_fma_f32 v30, v30, v122, v124
mva 22,50,44
v_mul_f32 v125, v123, v67
v_fma_f32 v31, v31, v122, v125
mav 14,48,54
R 46,33536
v_mul_f32 v124, v123, v68
mav 18,52,54
v_fma_f32 v32, v32, v122, v124
v_mul_f32 v125, v123, v69
mva 22,54,56
s_waitcnt lgkmcnt(2)
v_fma_f32 v33, v33, v122, v125
v_add_u32 v78, 1, v78
mav 14,60,58
R 50,33600
mav 18,64,58
mva 22,58,68
mav 14,72,38
R 54,33664
mav 18,76,38
mva 22,38,80
s_waitcnt lgkmcnt(2)
mav 14,84,42
R 58,33728
mav 18,88,42
mva 22,42,92
mav 14,96,46
R 38,33792
mav 18,100,46
s_barrier
mva 22,46,104
s_waitcnt lgkmcnt(2)
ds_read_b128 v[104:107], v82 offset:4096
mav 14,108,50
ds_read_b64 v[110:111], v82 offset:5120
mav 18,112,50
R 42,33856
mva 22,50,116
mav 14,120,54
s_waitcnt vmcnt(0)
buffer_store_dword v78, off, s[36:39], s52 sc1
mav 18,124,54
R 46,33920
mva 22,54,128
s_waitcnt lgkmcnt(3)
v_mfma_f32_16x16x32_bf16 v[74:77], v[104:107], v[0:3], v[90:93]
v_cvt_pk_bf16_f32 v0, v26, v27
v_cvt_pk_bf16_f32 v1, v28, v29
mav 14,132,58
R 50,33984
v_cvt_pk_bf16_f32 v2, v30, v31
mav 18,136,58
v_cvt_pk_bf16_f32 v3, v32, v33
mva 22,58,140
v_max3_f32 v118, v70, v71, v74
v_max3_f32 v119, v72, v73, v76
mav 14,144,38
R 54,34048
v_max_f32 v118, v118, v75
mav 18,148,38
v_max_f32 v119, v119, v77
v_max_f32 v120, v118, v119
mva 22,38,152
s_waitcnt lgkmcnt(2)
v_max_f32 v121, v118, v119
mav 14,156,42
R 38,34112
mav 18,160,42
v_permlane32_swap_b32 v120, v121
mva 22,42,164
v_max_f32 v114, v120, v121
v_max_f32 v115, v120, v121
mav 14,168,46
R 42,34176
mav 18,172,46
v_permlane16_swap_b32 v114, v115
mva 22,46,176
s_waitcnt lgkmcnt(2)
v_max_f32 v120, v114, v115
v_mul_f32 v120, s65, v120
mav 14,180,50
R 46,34240
v_fma_f32 v118, v118, s65, -v120
mav 18,184,50
v_fma_f32 v119, v119, s65, -v120
mva 22,50,188
v_exp_f32 v118, v118
mav 14,192,54
R 50,34304
mav 18,196,54
v_exp_f32 v119, v119
mva 22,54,200
s_waitcnt lgkmcnt(2)
v_add_f32 v120, v118, v119
v_add_f32 v121, v118, v119
mav 14,204,38
R 54,34368
mav 18,208,38
v_permlane32_swap_b32 v120, v121
mva 22,38,212
v_add_f32 v114, v120, v121
v_add_f32 v115, v120, v121
mav 14,216,42
R 38,34432
mav 18,220,42
v_permlane16_swap_b32 v114, v115
mva 22,42,224
s_waitcnt lgkmcnt(2)
v_add_f32 v120, v114, v115
mav 14,228,46
R 42,34496
mav 18,232,46
v_rcp_f32 v120, v120
mva 22,46,236
v_mul_f32 v118, v118, v120
v_mul_f32 v119, v119, v120
mav 14,240,50
R 46,34560
v_cvt_pk_bf16_f32 v118, v118, v118
mav 18,244,50
v_cvt_pk_bf16_f32 v119, v119, v119
v_and_b32 v114, v118, v94
mva 22,50,248
s_waitcnt lgkmcnt(2)
v_and_b32 v115, v119, v95
v_and_b32 v116, v118, v96
mav 14,252,54
R 50,34624
v_and_b32 v117, v119, v97
mvv 18,128,54
mvv 22,54,132
v_mfma_f32_16x16x32_bf16 v[70:73], v[8:11], v[114:117], 0
v_mfma_f32_16x16x32_bf16 v[74:77], v[108:111], v[114:117], 0
mvv 14,136,38
R 54,34688
mvv 18,140,38
mvv 22,38,144
s_waitcnt lgkmcnt(2)
mvv 14,148,42
R 38,34752
v_cvt_pk_bf16_f32 v114, v70, v71
mvv 18,152,42
v_cvt_pk_bf16_f32 v115, v72, v73
v_cvt_pk_bf16_f32 v116, v74, v75
mvv 22,42,156
v_cvt_pk_bf16_f32 v117, v76, v77
mvv 14,160,46
buffer_store_dwordx4 v[114:117], v82, s[36:39], s49 offen sc1
R 42,34816
s_add_u32 s49, s49, 313344
mvv 18,164,46
mvv 22,46,168
s_waitcnt lgkmcnt(2)
mvv 14,172,50
R 46,34880
mvv 18,176,50
mvv 22,50,180
mvv 14,184,54
R 50,34944
mvv 18,188,54
mvv 22,54,192
s_waitcnt lgkmcnt(2)
mvv 14,196,38
R 54,35008
mvv 18,200,38
mvv 22,38,204
mvv 14,208,42
R 38,0
mvv 18,212,42
ds_read_b64 v[122:123], v83 offset:0
mvv 22,42,216
s_waitcnt lgkmcnt(3)
ds_read_b128 v[70:73], v84 offset:0
mvv 14,220,46
R 42,64
mvv 18,224,46
ds_read_b128 v[74:77], v84 offset:16
mvv 22,46,228
mvv 14,232,50
R 46,128
mvv 18,236,50
mvv 22,50,240
s_waitcnt lgkmcnt(3)
mvv 14,244,54
R 50,192
mvv 18,248,54
mvv 22,54,252
v_mfma_f32_16x16x32_bf16 v[26:29], a[0:3], v[38:41], 0
s_waitcnt lgkmcnt(1)
R 54,256
v_mul_f32 v71, v71, v99
v_mfma_f32_16x16x32_bf16 v[30:33], a[4:7], v[38:41], 0
v_mul_f32 v73, v73, v99
v_mul_f32 v75, v75, v99
v_mfma_f32_16x16x32_bf16 v[34:37], v[38:41], a[8:11], 0
v_fma_f32 v22, v22, v70, v71
v_fma_f32 v23, v23, v72, v73
mav 26,12,42
R 58,320
v_fma_f32 v24, v24, v74, v75
mav 30,16,42
v_mul_f32 v77, v77, v99
v_fma_f32 v25, v25, v76, v77
mva 34,42,20
v_cvt_pk_bf16_f32 v8, v22, v23
v_cvt_pk_bf16_f32 v9, v24, v25
mav 26,24,46
R 38,384
v_mul_f32 v124, v123, v62
v_mfma_f32_16x16x32_bf16 v[70:73], v[104:107], v[0:3], v[86:89]
v_fma_f32 v14, v14, v122, v124
v_mul_f32 v125, v123, v63
mav 30,28,46
v_fma_f32 v15, v15, v122, v125
v_mul_f32 v124, v123, v64
mva 34,46,32
s_waitcnt lgkmcnt(2)
v_fma_f32 v16, v16, v122, v124
v_mul_f32 v125, v123, v65
mav 26,36,50
R 42,448
v_fma_f32 v17, v17, v122, v125
mav 30,40,50
v_mul_f32 v124, v123, v66
v_fma_f32 v18, v18, v122, v124
mva 34,50,44
v_mul_f32 v125, v123, v67
v_fma_f32 v19, v19, v122, v125
mav 26,48,54
R 46,512
v_mul_f32 v124, v123, v68
mav 30,52,54
v_fma_f32 v20, v20, v122, v124
v_mul_f32 v125, v123, v69
mva 34,54,56
s_waitcnt lgkmcnt(2)
v_fma_f32 v21, v21, v122, v125
v_add_u32 v78, 1, v78
mav 26,60,58
R 50,576
mav 30,64,58
mva 34,58,68
mav 26,72,38
R 54,640
mav 30,76,38
mva 34,38,80
s_waitcnt lgkmcnt(2)
mav 26,84,42
R 58,704
mav 30,88,42
mva 34,42,92
mav 26,96,46
R 38,768
mav 30,100,46
s_barrier
mva 34,46,104
s_waitcnt lgkmcnt(2)
ds_read_b128 v[100:103], v82 offset:0
mav 26,108,50
ds_read_b64 v[108:109], v82 offset:1024
mav 30,112,50
R 42,832
mva 34,50,116
mav 26,120,54
s_waitcnt vmcnt(0)
buffer_store_dword v78, off, s[36:39], s52 sc1
mav 30,124,54
R 46,896
mva 34,54,128
s_waitcnt lgkmcnt(3)
v_mfma_f32_16x16x32_bf16 v[74:77], v[100:103], v[0:3], v[90:93]
v_cvt_pk_bf16_f32 v0, v14, v15
v_cvt_pk_bf16_f32 v1, v16, v17
mav 26,132,58
R 50,960
v_cvt_pk_bf16_f32 v2, v18, v19
mav 30,136,58
v_cvt_pk_bf16_f32 v3, v20, v21
mva 34,58,140
v_max3_f32 v118, v70, v71, v74
v_max3_f32 v119, v72, v73, v76
mav 26,144,38
R 54,1024
v_max_f32 v118, v118, v75
mav 30,148,38
v_max_f32 v119, v119, v77
v_max_f32 v120, v118, v119
mva 34,38,152
s_waitcnt lgkmcnt(2)
v_max_f32 v121, v118, v119
mav 26,156,42
R 38,1088
mav 30,160,42
v_permlane32_swap_b32 v120, v121
mva 34,42,164
v_max_f32 v114, v120, v121
v_max_f32 v115, v120, v121
mav 26,168,46
R 42,1152
mav 30,172,46
v_permlane16_swap_b32 v114, v115
mva 34,46,176
s_waitcnt lgkmcnt(2)
v_max_f32 v120, v114, v115
v_mul_f32 v120, s65, v120
mav 26,180,50
R 46,1216
v_fma_f32 v118, v118, s65, -v120
mav 30,184,50
v_fma_f32 v119, v119, s65, -v120
mva 34,50,188
v_exp_f32 v118, v118
mav 26,192,54
R 50,1280
mav 30,196,54
v_exp_f32 v119, v119
mva 34,54,200
s_waitcnt lgkmcnt(2)
v_add_f32 v120, v118, v119
v_add_f32 v121, v118, v119
mav 26,204,38
R 54,1344
mav 30,208,38
v_permlane32_swap_b32 v120, v121
mva 34,38,212
v_add_f32 v114, v120, v121
v_add_f32 v115, v120, v121
mav 26,216,42
R 38,1408
mav 30,220,42
v_permlane16_swap_b32 v114, v115
mva 34,42,224
s_waitcnt lgkmcnt(2)
v_add_f32 v120, v114, v115
mav 26,228,46
R 42,1472
mav 30,232,46
v_rcp_f32 v120, v120
mva 34,46,236
v_mul_f32 v118, v118, v120
v_mul_f32 v119, v119, v120
mav 26,240,50
R 46,1536
v_cvt_pk_bf16_f32 v118, v118, v118
mav 30,244,50
v_cvt_pk_bf16_f32 v119, v119, v119
v_and_b32 v114, v118, v96
mva 34,50,248
s_waitcnt lgkmcnt(2)
v_and_b32 v115, v119, v97
v_and_b32 v116, v118, v94
mav 26,252,54
R 50,1600
v_and_b32 v117, v119, v95
mvv 30,128,54
mvv 34,54,132
v_mfma_f32_16x16x32_bf16 v[70:73], v[8:11], v[114:117], 0
v_mfma_f32_16x16x32_bf16 v[74:77], v[108:111], v[114:117], 0
mvv 26,136,38
R 54,1664
mvv 30,140,38
mvv 34,38,144
s_waitcnt lgkmcnt(2)
mvv 26,148,42
R 38,1728
v_cvt_pk_bf16_f32 v114, v70, v71
mvv 30,152,42
v_cvt_pk_bf16_f32 v115, v72, v73
v_cvt_pk_bf16_f32 v116, v74, v75
mvv 34,42,156
v_cvt_pk_bf16_f32 v117, v76, v77
mvv 26,160,46
buffer_store_dwordx4 v[114:117], v82, s[36:39], s49 offen sc1
R 42,1792
s_add_u32 s49, s49, 313344
mvv 30,164,46
mvv 34,46,168
s_waitcnt lgkmcnt(2)
mvv 26,172,50
R 46,1856
mvv 30,176,50
mvv 34,50,180
mvv 26,184,54
R 50,1920
mvv 30,188,54
s_waitcnt lgkmcnt(2)
s_add_u32 s60, s60, 1
mvv 34,54,192
s_cbranch_scc0 LQA0_2
mvv 26,196,38
R 54,1984
v_xor_b32 v80, v81, v80
mvv 30,200,38
mvv 34,38,204
mvv 26,208,42
R 38,33024
mvv 30,212,42
ds_read_b64 v[122:123], v83 offset:128
mvv 34,42,216
s_waitcnt lgkmcnt(3)
ds_read_b128 v[70:73], v84 offset:128
mvv 26,220,46
R 42,33088
mvv 30,224,46
ds_read_b128 v[74:77], v84 offset:144
mvv 34,46,228
mvv 26,232,50
R 46,33152
mvv 30,236,50
mvv 34,50,240
s_waitcnt lgkmcnt(3)
mvv 26,244,54
R 50,33216
mvv 30,248,54
mvv 34,54,252
v_mfma_f32_16x16x32_bf16 v[14:17], a[0:3], v[38:41], 0
s_waitcnt lgkmcnt(1)
R 54,33280
v_mul_f32 v71, v71, v99
v_mfma_f32_16x16x32_bf16 v[18:21], a[4:7], v[38:41], 0
v_mul_f32 v73, v73, v99
v_mul_f32 v75, v75, v99
v_mfma_f32_16x16x32_bf16 v[22:25], v[38:41], a[8:11], 0
v_fma_f32 v34, v34, v70, v71
v_fma_f32 v35, v35, v72, v73
mav 14,12,42
R 58,33344
v_fma_f32 v36, v36, v74, v75
mav 18,16,42
v_mul_f32 v77, v77, v99
v_fma_f32 v37, v37, v76, v77
mva 22,42,20
v_cvt_pk_bf16_f32 v10, v34, v35
v_cvt_pk_bf16_f32 v11, v36, v37
mav 14,24,46
R 38,33408
v_mul_f32 v124, v123, v62
v_mfma_f32_16x16x32_bf16 v[70:73], v[100:103], v[0:3], v[86:89]
v_fma_f32 v26, v26, v122, v124
v_mul_f32 v125, v123, v63
mav 18,28,46
v_fma_f32 v27, v27, v122, v125
v_mul_f32 v124, v123, v64
mva 22,46,32
s_waitcnt lgkmcnt(2)
v_fma_f32 v28, v28, v122, v124
v_mul_f32 v125, v123, v65
mav 14,36,50
R 42,33472
v_fma_f32 v29, v29, v122, v125
mav 18,40,50
v_mul_f32 v124, v123, v66
v_fma_f32 v30, v30, v122, v124
mva 22,50,44
v_mul_f32 v125, v123, v67
v_fma_f32 v31, v31, v122, v125
mav 14,48,54
R 46,33536
v_mul_f32 v124, v123, v68
mav 18,52,54
v_fma_f32 v32, v32, v122, v124
v_mul_f32 v125, v123, v69
mva 22,54,56
s_waitcnt lgkmcnt(2)
v_fma_f32 v33, v33, v122, v125
v_add_u32 v78, 1, v78
mav 14,60,58
R 50,33600
mav 18,64,58
mva 22,58,68
mav 14,72,38
R 54,33664
mav 18,76,38
mva 22,38,80
s_waitcnt lgkmcnt(2)
mav 14,84,42
R 58,33728
LsQ6:
s_cbranch_execz LsQ10
mav 18,88,42
mva 22,42,92
mav 14,96,46
R 38,33792
mav 18,100,46
s_barrier
mva 22,46,104
s_waitcnt lgkmcnt(2)
ds_read_b128 v[104:107], v82 offset:4096
mav 14,108,50
ds_read_b64 v[110:111], v82 offset:5120
mav 18,112,50
R 42,33856
mva 22,50,116
mav 14,120,54
s_waitcnt vmcnt(0)
buffer_store_dword v78, off, s[36:39], s52 sc1
mav 18,124,54
R 46,33920
mva 22,54,128
s_waitcnt lgkmcnt(3)
v_mfma_f32_16x16x32_bf16 v[74:77], v[104:107], v[0:3], v[90:93]
v_cvt_pk_bf16_f32 v0, v26, v27
v_cvt_pk_bf16_f32 v1, v28, v29
mav 14,132,58
R 50,33984
v_cvt_pk_bf16_f32 v2, v30, v31
mav 18,136,58
v_cvt_pk_bf16_f32 v3, v32, v33
mva 22,58,140
v_max3_f32 v118, v70, v71, v74
v_max3_f32 v119, v72, v73, v76
mav 14,144,38
R 54,34048
v_max_f32 v118, v118, v75
mav 18,148,38
v_max_f32 v119, v119, v77
v_max_f32 v120, v118, v119
mva 22,38,152
s_waitcnt lgkmcnt(2)
v_max_f32 v121, v118, v119
mav 14,156,42
R 38,34112
mav 18,160,42
v_permlane32_swap_b32 v120, v121
mva 22,42,164
v_max_f32 v114, v120, v121
v_max_f32 v115, v120, v121
mav 14,168,46
R 42,34176
mav 18,172,46
v_permlane16_swap_b32 v114, v115
mva 22,46,176
s_waitcnt lgkmcnt(2)
v_max_f32 v120, v114, v115
v_mul_f32 v120, s65, v120
mav 14,180,50
R 46,34240
v_fma_f32 v118, v118, s65, -v120
mav 18,184,50
v_fma_f32 v119, v119, s65, -v120
mva 22,50,188
v_exp_f32 v118, v118
mav 14,192,54
R 50,34304
mav 18,196,54
v_exp_f32 v119, v119
mva 22,54,200
s_waitcnt lgkmcnt(2)
v_add_f32 v120, v118, v119
v_add_f32 v121, v118, v119
mav 14,204,38
R 54,34368
mav 18,208,38
v_permlane32_swap_b32 v120, v121
mva 22,38,212
v_add_f32 v114, v120, v121
v_add_f32 v115, v120, v121
mav 14,216,42
R 38,34432
mav 18,220,42
v_permlane16_swap_b32 v114, v115
mva 22,42,224
s_waitcnt lgkmcnt(2)
v_add_f32 v120, v114, v115
mav 14,228,46
R 42,34496
mav 18,232,46
v_rcp_f32 v120, v120
mva 22,46,236
v_mul_f32 v118, v118, v120
v_mul_f32 v119, v119, v120
mav 14,240,50
R 46,34560
v_cvt_pk_bf16_f32 v118, v118, v118
mav 18,244,50
v_cvt_pk_bf16_f32 v119, v119, v119
v_and_b32 v114, v118, v94
mva 22,50,248
s_waitcnt lgkmcnt(2)
v_and_b32 v115, v119, v95
v_and_b32 v116, v118, v96
mav 14,252,54
R 50,34624
v_and_b32 v117, v119, v97
mvv 18,128,54
mvv 22,54,132
v_mfma_f32_16x16x32_bf16 v[70:73], v[8:11], v[114:117], 0
v_mfma_f32_16x16x32_bf16 v[74:77], v[108:111], v[114:117], 0
mvv 14,136,38
R 54,34688
mvv 18,140,38
mvv 22,38,144
s_waitcnt lgkmcnt(2)
mvv 14,148,42
R 38,34752
v_cvt_pk_bf16_f32 v114, v70, v71
mvv 18,152,42
v_cvt_pk_bf16_f32 v115, v72, v73
v_cvt_pk_bf16_f32 v116, v74, v75
mvv 22,42,156
v_cvt_pk_bf16_f32 v117, v76, v77
mvv 14,160,46
buffer_store_dwordx4 v[114:117], v82, s[36:39], s49 offen sc1
R 42,34816
s_add_u32 s49, s49, 313344
mvv 18,164,46
mvv 22,46,168
s_waitcnt lgkmcnt(2)
mvv 14,172,50
R 46,34880
mvv 18,176,50
mvv 22,50,180
mvv 14,184,54
R 50,34944
mvv 18,188,54
mvv 22,54,192
s_waitcnt lgkmcnt(2)
mvv 14,196,38
R 54,35008
mvv 18,200,38
mvv 22,38,204
mvv 14,208,42
R 38,0
mvv 18,212,42
ds_read_b64 v[122:123], v83 offset:0
mvv 22,42,216
s_waitcnt lgkmcnt(3)
ds_read_b128 v[70:73], v84 offset:0
mvv 14,220,46
R 42,64
mvv 18,224,46
ds_read_b128 v[74:77], v84 offset:16
mvv 22,46,228
mvv 14,232,50
R 46,128
mvv 18,236,50
mvv 22,50,240
s_waitcnt lgkmcnt(3)
mvv 14,244,54
R 50,192
mvv 18,248,54
mvv 22,54,252
v_mfma_f32_16x16x32_bf16 v[26:29], a[0:3], v[38:41], 0
s_waitcnt lgkmcnt(1)
R 54,256
v_mul_f32 v71, v71, v99
v_mfma_f32_16x16x32_bf16 v[30:33], a[4:7], v[38:41], 0
v_mul_f32 v73, v73, v99
v_mul_f32 v75, v75, v99
v_mfma_f32_16x16x32_bf16 v[34:37], v[38:41], a[8:11], 0
v_fma_f32 v22, v22, v70, v71
v_fma_f32 v23, v23, v72, v73
mav 26,12,42
R 58,320
v_fma_f32 v24, v24, v74, v75
mav 30,16,42
v_mul_f32 v77, v77, v99
v_fma_f32 v25, v25, v76, v77
mva 34,42,20
v_cvt_pk_bf16_f32 v8, v22, v23
v_cvt_pk_bf16_f32 v9, v24, v25
mav 26,24,46
R 38,384
v_mul_f32 v124, v123, v62
v_mfma_f32_16x16x32_bf16 v[70:73], v[104:107], v[0:3], v[86:89]
v_fma_f32 v14, v14, v122, v124
v_mul_f32 v125, v123, v63
mav 30,28,46
v_fma_f32 v15, v15, v122, v125
v_mul_f32 v124, v123, v64
mva 34,46,32
s_waitcnt lgkmcnt(2)
v_fma_f32 v16, v16, v122, v124
v_mul_f32 v125, v123, v65
mav 26,36,50
R 42,448
v_fma_f32 v17, v17, v122, v125
mav 30,40,50
buffer_load_dwordx4 v[4:7], v82, s[36:39], s55 offen offset:2048 sc1
mva 34,50,44
v_mul_f32 v124, v123, v66
v_fma_f32 v18, v18, v122, v124
mav 26,48,54
buffer_load_dwordx2 v[12:13], v98, s[36:39], s55 offen offset:3072 sc1
mav 30,52,54
R 46,512
v_mul_f32 v125, v123, v67
mva 34,54,56
s_waitcnt lgkmcnt(2)
buffer_load_dwordx2 v[112:113], v98, s[36:39], s55 offen offset:3584 sc1
mav 26,60,58
R 50,576
v_fma_f32 v19, v19, v122, v125
mav 30,64,58
v_mul_f32 v124, v123, v68
v_fma_f32 v20, v20, v122, v124
mva 34,58,68
v_mul_f32 v125, v123, v69
v_fma_f32 v21, v21, v122, v125
mav 26,72,38
R 54,640
v_add_u32 v78, 1, v78
mav 30,76,38
mva 34,38,80
s_waitcnt lgkmcnt(2)
mav 26,84,42
R 58,704
mav 30,88,42
mva 34,42,92
mav 26,96,46
R 38,768
mav 30,100,46
s_barrier
mva 34,46,104
s_waitcnt lgkmcnt(2)
ds_read_b128 v[100:103], v82 offset:0
mav 26,108,50
ds_read_b64 v[108:109], v82 offset:1024
mav 30,112,50
R 42,832
mva 34,50,116
mav 26,120,54
s_waitcnt vmcnt(3)
buffer_store_dword v78, off, s[36:39], s52 sc1
mav 30,124,54
R 46,896
mva 34,54,128
s_waitcnt lgkmcnt(3)
v_mfma_f32_16x16x32_bf16 v[74:77], v[100:103], v[0:3], v[90:93]
v_cvt_pk_bf16_f32 v0, v14, v15
v_cvt_pk_bf16_f32 v1, v16, v17
mav 26,132,58
R 50,960
v_cvt_pk_bf16_f32 v2, v18, v19
mav 30,136,58
v_cvt_pk_bf16_f32 v3, v20, v21
mva 34,58,140
v_max3_f32 v118, v70, v71, v74
v_max3_f32 v119, v72, v73, v76
mav 26,144,38
R 54,1024
v_max_f32 v118, v118, v75
mav 30,148,38
v_max_f32 v119, v119, v77
v_max_f32 v120, v118, v119
mva 34,38,152
s_waitcnt lgkmcnt(2)
v_max_f32 v121, v118, v119
mav 26,156,42
R 38,1088
mav 30,160,42
v_permlane32_swap_b32 v120, v121
mva 34,42,164
v_max_f32 v114, v120, v121
v_max_f32 v115, v120, v121
mav 26,168,46
R 42,1152
mav 30,172,46
v_permlane16_swap_b32 v114, v115
mva 34,46,176
s_waitcnt lgkmcnt(2)
v_max_f32 v120, v114, v115
v_mul_f32 v120, s65, v120
mav 26,180,50
R 46,1216
v_fma_f32 v118, v118, s65, -v120
mav 30,184,50
v_fma_f32 v119, v119, s65, -v120
mva 34,50,188
v_exp_f32 v118, v118
mav 26,192,54
R 50,1280
mav 30,196,54
v_exp_f32 v119, v119
mva 34,54,200
s_waitcnt lgkmcnt(2)
v_add_f32 v120, v118, v119
v_add_f32 v121, v118, v119
mav 26,204,38
R 54,1344
mav 30,208,38
v_permlane32_swap_b32 v120, v121
mva 34,38,212
v_add_f32 v114, v120, v121
v_add_f32 v115, v120, v121
mav 26,216,42
R 38,1408
mav 30,220,42
v_permlane16_swap_b32 v114, v115
mva 34,42,224
s_waitcnt lgkmcnt(2)
v_add_f32 v120, v114, v115
mav 26,228,46
R 42,1472
mav 30,232,46
v_rcp_f32 v120, v120
mva 34,46,236
v_mul_f32 v118, v118, v120
v_mul_f32 v119, v119, v120
mav 26,240,50
R 46,1536
v_cvt_pk_bf16_f32 v118, v118, v118
mav 30,244,50
v_cvt_pk_bf16_f32 v119, v119, v119
v_and_b32 v114, v118, v96
mva 34,50,248
s_waitcnt lgkmcnt(2)
v_and_b32 v115, v119, v97
v_and_b32 v116, v118, v94
mav 26,252,54
R 50,1600
v_and_b32 v117, v119, v95
mvv 30,128,54
mvv 34,54,132
v_mfma_f32_16x16x32_bf16 v[70:73], v[8:11], v[114:117], 0
v_mfma_f32_16x16x32_bf16 v[74:77], v[108:111], v[114:117], 0
mvv 26,136,38
R 54,1664
mvv 30,140,38
mvv 34,38,144
s_waitcnt lgkmcnt(2)
mvv 26,148,42
R 38,1728
v_cvt_pk_bf16_f32 v114, v70, v71
mvv 30,152,42
v_cvt_pk_bf16_f32 v115, v72, v73
v_cvt_pk_bf16_f32 v116, v74, v75
mvv 34,42,156
v_cvt_pk_bf16_f32 v117, v76, v77
mvv 26,160,46
buffer_store_dwordx4 v[114:117], v82, s[36:39], s49 offen sc1
R 42,1792
s_add_u32 s49, s49, 313344
mvv 30,164,46
mvv 34,46,168
s_waitcnt lgkmcnt(2)
mvv 26,172,50
R 46,1856
mvv 30,176,50
mvv 34,50,180
mvv 26,184,54
R 50,1920
mvv 30,188,54
s_waitcnt lgkmcnt(2)
mvv 34,54,192
mvv 26,196,38
R 54,1984
mvv 30,200,38
mvv 34,38,204
mvv 26,208,42
mvv 30,212,42
ds_read_b64 v[122:123], v83 offset:128
mvv 34,42,216
s_waitcnt lgkmcnt(2)
ds_read_b128 v[70:73], v84 offset:128
mvv 26,220,46
ds_read_b128 v[74:77], v84 offset:144
mvv 30,224,46
mvv 34,46,228
mvv 26,232,50
mvv 30,236,50
mvv 34,50,240
s_waitcnt lgkmcnt(1)
mvv 26,244,54
mvv 30,248,54
mvv 34,54,252
v_mul_f32 v71, v71, v99
s_nop 6
v_fma_f32 v34, v34, v70, v71
v_mul_f32 v73, v73, v99
v_fma_f32 v35, v35, v72, v73
s_waitcnt lgkmcnt(0)
v_mul_f32 v75, v75, v99
v_fma_f32 v36, v36, v74, v75
v_mul_f32 v77, v77, v99
v_fma_f32 v37, v37, v76, v77
v_cvt_pk_bf16_f32 v10, v34, v35
v_cvt_pk_bf16_f32 v11, v36, v37
s_nop 1
v_mfma_f32_16x16x32_bf16 v[70:73], v[100:103], v[0:3], v[86:89]
v_mul_f32 v124, v123, v62
v_fma_f32 v26, v26, v122, v124
v_mul_f32 v125, v123, v63
v_fma_f32 v27, v27, v122, v125
v_mul_f32 v124, v123, v64
v_fma_f32 v28, v28, v122, v124
v_mul_f32 v125, v123, v65
v_fma_f32 v29, v29, v122, v125
v_mul_f32 v124, v123, v66
v_fma_f32 v30, v30, v122, v124
v_mul_f32 v125, v123, v67
v_fma_f32 v31, v31, v122, v125
v_mul_f32 v124, v123, v68
v_fma_f32 v32, v32, v122, v124
v_mul_f32 v125, v123, v69
v_fma_f32 v33, v33, v122, v125
s_barrier
ds_read_b128 v[104:107], v82 offset:4096
ds_read_b64 v[110:111], v82 offset:5120
s_waitcnt lgkmcnt(0)
v_mfma_f32_16x16x32_bf16 v[74:77], v[104:107], v[0:3], v[90:93]
s_nop 7
v_max3_f32 v118, v70, v71, v74
v_max3_f32 v119, v72, v73, v76
v_max_f32 v118, v118, v75
v_max_f32 v119, v119, v77
v_max_f32 v120, v118, v119
v_max_f32 v121, v118, v119
s_nop 1
v_permlane32_swap_b32 v120, v121
v_max_f32 v114, v120, v121
v_max_f32 v115, v120, v121
s_nop 1
v_permlane16_swap_b32 v114, v115
v_max_f32 v120, v114, v115
v_mul_f32 v120, s65, v120
v_fma_f32 v118, v118, s65, -v120
v_fma_f32 v119, v119, s65, -v120
v_exp_f32 v118, v118
v_exp_f32 v119, v119
s_nop 0
v_add_f32 v120, v118, v119
v_add_f32 v121, v118, v119
s_nop 1
v_permlane32_swap_b32 v120, v121
v_add_f32 v114, v120, v121
v_add_f32 v115, v120, v121
s_nop 1
v_permlane16_swap_b32 v114, v115
v_add_f32 v120, v114, v115
v_rcp_f32 v120, v120
s_nop 0
v_mul_f32 v118, v118, v120
v_mul_f32 v119, v119, v120
v_cvt_pk_bf16_f32 v118, v118, v118
v_cvt_pk_bf16_f32 v119, v119, v119
v_and_b32 v114, v118, v94
v_and_b32 v115, v119, v95
v_and_b32 v116, v118, v96
v_and_b32 v117, v119, v97
s_nop 1
v_mfma_f32_16x16x32_bf16 v[70:73], v[8:11], v[114:117], 0
v_mfma_f32_16x16x32_bf16 v[74:77], v[108:111], v[114:117], 0
s_nop 6
v_cvt_pk_bf16_f32 v114, v70, v71
v_cvt_pk_bf16_f32 v115, v72, v73
v_cvt_pk_bf16_f32 v116, v74, v75
v_cvt_pk_bf16_f32 v117, v76, v77
buffer_store_dwordx4 v[114:117], v82, s[36:39], s49 offen sc1
s_add_u32 s49, s49, 313344
v_cvt_pk_bf16_f32 v0, v26, v27
v_cvt_pk_bf16_f32 v1, v28, v29
v_cvt_pk_bf16_f32 v2, v30, v31
v_cvt_pk_bf16_f32 v3, v32, v33
v_add_u32 v78, 1, v78
s_waitcnt vmcnt(1)
buffer_store_dword v78, off, s[36:39], s52 sc1
v_mfma_f32_16x16x32_bf16 v[70:73], v[104:107], v[0:3], v[86:89]
v_min_f32 v74, s56, v90
v_min_f32 v75, s56, v91
v_min_f32 v76, s56, v92
v_min_f32 v77, s56, v93
s_nop 1
mvv 74,4,0
s_nop 7
v_max3_f32 v118, v70, v71, v74
v_max3_f32 v119, v72, v73, v76
v_max_f32 v118, v118, v75
v_max_f32 v119, v119, v77
v_max_f32 v120, v118, v119
v_max_f32 v121, v118, v119
s_nop 1
v_permlane32_swap_b32 v120, v121
v_max_f32 v114, v120, v121
v_max_f32 v115, v120, v121
s_nop 1
v_permlane16_swap_b32 v114, v115
v_max_f32 v120, v114, v115
v_mul_f32 v120, s65, v120
v_fma_f32 v118, v118, s65, -v120
v_fma_f32 v119, v119, s65, -v120
v_exp_f32 v118, v118
v_exp_f32 v119, v119
s_nop 0
v_add_f32 v120, v118, v119
v_add_f32 v121, v118, v119
s_nop 1
v_permlane32_swap_b32 v120, v121
v_add_f32 v114, v120, v121
v_add_f32 v115, v120, v121
s_nop 1
v_permlane16_swap_b32 v114, v115
v_add_f32 v120, v114, v115
v_rcp_f32 v120, v120
s_nop 0
v_mul_f32 v118, v118, v120
v_mul_f32 v119, v119, v120
v_cvt_pk_bf16_f32 v118, v118, v118
v_cvt_pk_bf16_f32 v119, v119, v119
v_and_b32 v114, v118, v94
v_and_b32 v115, v119, v95
v_and_b32 v116, v118, v96
v_and_b32 v117, v119, v97
s_nop 1
v_mfma_f32_16x16x32_bf16 v[70:73], v[10:13], v[114:117], 0
v_mfma_f32_16x16x32_bf16 v[74:77], v[110:113], v[114:117], 0
s_nop 6
v_cvt_pk_bf16_f32 v114, v70, v71
v_cvt_pk_bf16_f32 v115, v72, v73
v_cvt_pk_bf16_f32 v116, v74, v75
v_cvt_pk_bf16_f32 v117, v76, v77
buffer_store_dwordx4 v[114:117], v82, s[36:39], s49 offen sc1
v_add_u32 v78, 1, v78
s_waitcnt vmcnt(2)
buffer_store_dword v78, off, s[36:39], s52 sc1
v_add_u32 v78, 37, v78
s_waitcnt vmcnt(1)
buffer_store_dword v78, off, s[36:39], s52 sc1
s_endpgm
LvQA1:
LsQ7:
s_cbranch_execz LsQ11
ds_read_b64 v[122:123], v83 offset:0
ds_read_b128 v[70:73], v84 offset:0
ds_read_b128 v[74:77], v84 offset:16
s_add_u32 s49, s49, 313344
s_waitcnt lgkmcnt(0)
v_mul_f32 v71, v71, v99
v_fma_f32 v22, v22, v70, v71
v_mul_f32 v73, v73, v99
v_fma_f32 v23, v23, v72, v73
v_mul_f32 v75, v75, v99
v_fma_f32 v24, v24, v74, v75
v_mul_f32 v77, v77, v99
v_fma_f32 v25, v25, v76, v77
v_cvt_pk_bf16_f32 v8, v22, v23
v_cvt_pk_bf16_f32 v9, v24, v25
v_mul_f32 v124, v123, v62
v_fma_f32 v14, v14, v122, v124
v_mul_f32 v125, v123, v63
v_fma_f32 v15, v15, v122, v125
v_mul_f32 v124, v123, v64
v_fma_f32 v16, v16, v122, v124
v_mul_f32 v125, v123, v65
v_fma_f32 v17, v17, v122, v125
v_mul_f32 v124, v123, v66
v_fma_f32 v18, v18, v122, v124
v_mul_f32 v125, v123, v67
v_fma_f32 v19, v19, v122, v125
v_mul_f32 v124, v123, v68
v_fma_f32 v20, v20, v122, v124
v_mul_f32 v125, v123, v69
v_fma_f32 v21, v21, v122, v125
v_cvt_pk_bf16_f32 v0, v14, v15
v_cvt_pk_bf16_f32 v1, v16, v17
v_cvt_pk_bf16_f32 v2, v18, v19
v_cvt_pk_bf16_f32 v3, v20, v21
s_add_u32 s49, s49, 313344
s_barrier
ds_read_b128 v[100:103], v82 offset:0
ds_read_b64 v[108:109], v82 offset:1024
R 38,33024
R 42,33088
R 46,33152
R 50,33216
ds_read_b64 v[122:123], v83 offset:128
ds_read_b128 v[70:73], v84 offset:128
ds_read_b128 v[74:77], v84 offset:144
s_waitcnt lgkmcnt(0)
v_mfma_f32_16x16x32_bf16 v[14:17], a[0:3], v[38:41], 0
R 54,33280
v_mul_f32 v71, v71, v99
v_mfma_f32_16x16x32_bf16 v[18:21], a[4:7], v[38:41], 0
v_fma_f32 v34, v34, v70, v71
v_mul_f32 v73, v73, v99
v_mfma_f32_16x16x32_bf16 v[22:25], v[38:41], a[8:11], 0
v_fma_f32 v35, v35, v72, v73
v_mul_f32 v75, v75, v99
mav 14,12,42
R 58,33344
v_fma_f32 v36, v36, v74, v75
mav 18,16,42
v_mul_f32 v77, v77, v99
v_fma_f32 v37, v37, v76, v77
mva 22,42,20
v_cvt_pk_bf16_f32 v10, v34, v35
v_cvt_pk_bf16_f32 v11, v36, v37
mav 14,24,46
R 38,33408
v_mul_f32 v124, v123, v62
v_mfma_f32_16x16x32_bf16 v[70:73], v[100:103], v[0:3], v[86:89]
v_fma_f32 v26, v26, v122, v124
v_mul_f32 v125, v123, v63
mav 18,28,46
v_fma_f32 v27, v27, v122, v125
v_mul_f32 v124, v123, v64
mva 22,46,32
v_fma_f32 v28, v28, v122, v124
v_mul_f32 v125, v123, v65
mav 14,36,50
R 42,33472
v_fma_f32 v29, v29, v122, v125
LsQ8:
s_cbranch_execz LsQ12
mav 18,40,50
v_mul_f32 v124, v123, v66
v_fma_f32 v30, v30, v122, v124
mva 22,50,44
s_waitcnt lgkmcnt(2)
v_mul_f32 v125, v123, v67
v_fma_f32 v31, v31, v122, v125
mav 14,48,54
R 46,33536
v_mul_f32 v124, v123, v68
mav 18,52,54
v_fma_f32 v32, v32, v122, v124
v_mul_f32 v125, v123, v69
mva 22,54,56
v_fma_f32 v33, v33, v122, v125
mav 14,60,58
R 50,33600
mav 18,64,58
mva 22,58,68
s_waitcnt lgkmcnt(2)
mav 14,72,38
R 54,33664
mav 18,76,38
buffer_store_dwordx4 v[100:103], v82, s[36:39], s54 offen sc1
mva 22,38,80
mav 14,84,42
buffer_store_dwordx2 v[8:9], v98, s[36:39], s54 offen offset:1024 sc1
R 58,33728
mav 18,88,42
mva 22,42,92
s_waitcnt lgkmcnt(2)
buffer_store_dwordx2 v[108:109], v98, s[36:39], s54 offen offset:1536 sc1
mav 14,96,46
R 38,33792
mav 18,100,46
s_barrier
mva 22,46,104
ds_read_b128 v[104:107], v82 offset:4096
mav 14,108,50
ds_read_b64 v[110:111], v82 offset:5120
mav 18,112,50
R 42,33856
mva 22,50,116
s_waitcnt lgkmcnt(4)
mav 14,120,54
R 46,33920
mav 18,124,54
mva 22,54,128
mav 14,132,58
s_waitcnt lgkmcnt(2)
R 50,33984
v_mfma_f32_16x16x32_bf16 v[74:77], v[104:107], v[0:3], v[90:93]
v_cvt_pk_bf16_f32 v0, v26, v27
v_cvt_pk_bf16_f32 v1, v28, v29
mav 18,136,58
v_cvt_pk_bf16_f32 v2, v30, v31
v_cvt_pk_bf16_f32 v3, v32, v33
mva 22,58,140
mav 14,144,38
R 54,34048
v_max3_f32 v118, v70, v71, v74
mav 18,148,38
v_max3_f32 v119, v72, v73, v76
v_max_f32 v118, v118, v75
mva 22,38,152
s_waitcnt lgkmcnt(2)
v_max_f32 v119, v119, v77
v_max_f32 v120, v118, v119
mav 14,156,42
R 38,34112
v_max_f32 v121, v118, v119
mav 18,160,42
mva 22,42,164
v_permlane32_swap_b32 v120, v121
mav 14,168,46
R 42,34176
v_max_f32 v114, v120, v121
mav 18,172,46
v_max_f32 v115, v120, v121
mva 22,46,176
s_waitcnt lgkmcnt(2)
v_permlane16_swap_b32 v114, v115
mav 14,180,50
R 46,34240
v_max_f32 v120, v114, v115
mav 18,184,50
v_mul_f32 v120, s65, v120
v_fma_f32 v118, v118, s65, -v120
mva 22,50,188
v_fma_f32 v119, v119, s65, -v120
mav 14,192,54
R 50,34304
mav 18,196,54
v_exp_f32 v118, v118
mva 22,54,200
s_waitcnt lgkmcnt(2)
v_exp_f32 v119, v119
mav 14,204,38
R 54,34368
v_add_f32 v120, v118, v119
mav 18,208,38
v_add_f32 v121, v118, v119
mva 22,38,212
mav 14,216,42
R 38,34432
mav 18,220,42
v_permlane32_swap_b32 v120, v121
mva 22,42,224
s_waitcnt lgkmcnt(2)
v_add_f32 v114, v120, v121
v_add_f32 v115, v120, v121
mav 14,228,46
R 42,34496
mav 18,232,46
v_permlane16_swap_b32 v114, v115
mva 22,46,236
v_add_f32 v120, v114, v115
mav 14,240,50
R 46,34560
mav 18,244,50
v_rcp_f32 v120, v120
mva 22,50,248
s_waitcnt lgkmcnt(2)
v_mul_f32 v118, v118, v120
v_mul_f32 v119, v119, v120
mav 14,252,54
R 50,34624
v_cvt_pk_bf16_f32 v118, v118, v118
mvv 18,128,54
v_cvt_pk_bf16_f32 v119, v119, v119
v_and_b32 v114, v118, v94
mvv 22,54,132
v_and_b32 v115, v119, v95
v_and_b32 v116, v118, v96
)"
R"(
mvv 14,136,38
R 54,34688
v_and_b32 v117, v119, v97
mvv 18,140,38
mvv 22,38,144
s_waitcnt lgkmcnt(2)
v_mfma_f32_16x16x32_bf16 v[70:73], v[8:11], v[114:117], 0
v_mfma_f32_16x16x32_bf16 v[74:77], v[108:111], v[114:117], 0
mvv 14,148,42
R 38,34752
mvv 18,152,42
mvv 22,42,156
mvv 14,160,46
R 42,34816
mvv 18,164,46
v_cvt_pk_bf16_f32 v114, v70, v71
v_cvt_pk_bf16_f32 v115, v72, v73
mvv 22,46,168
s_waitcnt lgkmcnt(2)
v_cvt_pk_bf16_f32 v116, v74, v75
v_cvt_pk_bf16_f32 v117, v76, v77
mvv 14,172,50
buffer_store_dwordx4 v[114:117], v82, s[36:39], s49 offen sc1
R 46,34880
s_add_u32 s49, s49, 313344
mvv 18,176,50
mvv 22,50,180
mvv 14,184,54
R 50,34944
mvv 18,188,54
mvv 22,54,192
s_waitcnt lgkmcnt(2)
mvv 14,196,38
R 54,35008
mvv 18,200,38
mvv 22,38,204
mvv 14,208,42
R 38,0
mvv 18,212,42
ds_read_b64 v[122:123], v83 offset:0
mvv 22,42,216
s_waitcnt lgkmcnt(3)
ds_read_b128 v[70:73], v84 offset:0
mvv 14,220,46
R 42,64
mvv 18,224,46
ds_read_b128 v[74:77], v84 offset:16
mvv 22,46,228
mvv 14,232,50
R 46,128
mvv 18,236,50
mvv 22,50,240
s_waitcnt lgkmcnt(3)
mvv 14,244,54
R 50,192
mvv 18,248,54
mvv 22,54,252
v_mfma_f32_16x16x32_bf16 v[26:29], a[0:3], v[38:41], 0
s_waitcnt lgkmcnt(1)
R 54,256
v_mul_f32 v71, v71, v99
v_mfma_f32_16x16x32_bf16 v[30:33], a[4:7], v[38:41], 0
v_mul_f32 v73, v73, v99
v_mul_f32 v75, v75, v99
v_mfma_f32_16x16x32_bf16 v[34:37], v[38:41], a[8:11], 0
v_fma_f32 v22, v22, v70, v71
v_fma_f32 v23, v23, v72, v73
mav 26,12,42
R 58,320
v_fma_f32 v24, v24, v74, v75
mav 30,16,42
v_mul_f32 v77, v77, v99
v_fma_f32 v25, v25, v76, v77
mva 34,42,20
v_cvt_pk_bf16_f32 v8, v22, v23
v_cvt_pk_bf16_f32 v9, v24, v25
mav 26,24,46
R 38,384
v_mul_f32 v124, v123, v62
v_mfma_f32_16x16x32_bf16 v[70:73], v[104:107], v[0:3], v[86:89]
v_fma_f32 v14, v14, v122, v124
v_mul_f32 v125, v123, v63
mav 30,28,46
v_fma_f32 v15, v15, v122, v125
v_mul_f32 v124, v123, v64
mva 34,46,32
s_waitcnt lgkmcnt(2)
v_fma_f32 v16, v16, v122, v124
v_mul_f32 v125, v123, v65
mav 26,36,50
R 42,448
v_fma_f32 v17, v17, v122, v125
mav 30,40,50
v_mul_f32 v124, v123, v66
v_fma_f32 v18, v18, v122, v124
mva 34,50,44
v_mul_f32 v125, v123, v67
v_fma_f32 v19, v19, v122, v125
mav 26,48,54
R 46,512
v_mul_f32 v124, v123, v68
mav 30,52,54
v_fma_f32 v20, v20, v122, v124
v_mul_f32 v125, v123, v69
mva 34,54,56
s_waitcnt lgkmcnt(2)
v_fma_f32 v21, v21, v122, v125
v_add_u32 v78, 1, v78
mav 26,60,58
R 50,576
mav 30,64,58
mva 34,58,68
mav 26,72,38
R 54,640
mav 30,76,38
mva 34,38,80
s_waitcnt lgkmcnt(2)
mav 26,84,42
R 58,704
mav 30,88,42
mva 34,42,92
mav 26,96,46
R 38,768
mav 30,100,46
s_barrier
mva 34,46,104
s_waitcnt lgkmcnt(2)
ds_read_b128 v[100:103], v82 offset:0
mav 26,108,50
ds_read_b64 v[108:109], v82 offset:1024
mav 30,112,50
R 42,832
mva 34,50,116
mav 26,120,54
s_waitcnt vmcnt(0)
R 46,896
mav 30,124,54
buffer_store_dword v78, off, s[36:39], s52 sc1
mva 34,54,128
s_waitcnt lgkmcnt(3)
v_mfma_f32_16x16x32_bf16 v[74:77], v[100:103], v[0:3], v[90:93]
v_cvt_pk_bf16_f32 v0, v14, v15
v_cvt_pk_bf16_f32 v1, v16, v17
mav 26,132,58
R 50,960
v_cvt_pk_bf16_f32 v2, v18, v19
mav 30,136,58
v_cvt_pk_bf16_f32 v3, v20, v21
mva 34,58,140
v_max3_f32 v118, v70, v71, v74
v_max3_f32 v119, v72, v73, v76
mav 26,144,38
R 54,1024
v_max_f32 v118, v118, v75
mav 30,148,38
v_max_f32 v119, v119, v77
v_max_f32 v120, v118, v119
mva 34,38,152
s_waitcnt lgkmcnt(2)
v_max_f32 v121, v118, v119
mav 26,156,42
R 38,1088
mav 30,160,42
v_permlane32_swap_b32 v120, v121
mva 34,42,164
v_max_f32 v114, v120, v121
v_max_f32 v115, v120, v121
mav 26,168,46
R 42,1152
mav 30,172,46
v_permlane16_swap_b32 v114, v115
mva 34,46,176
s_waitcnt lgkmcnt(2)
v_max_f32 v120, v114, v115
v_mul_f32 v120, s65, v120
mav 26,180,50
R 46,1216
v_fma_f32 v118, v118, s65, -v120
mav 30,184,50
v_fma_f32 v119, v119, s65, -v120
mva 34,50,188
v_exp_f32 v118, v118
mav 26,192,54
R 50,1280
mav 30,196,54
v_exp_f32 v119, v119
mva 34,54,200
s_waitcnt lgkmcnt(2)
v_add_f32 v120, v118, v119
v_add_f32 v121, v118, v119
mav 26,204,38
R 54,1344
mav 30,208,38
v_permlane32_swap_b32 v120, v121
mva 34,38,212
v_add_f32 v114, v120, v121
v_add_f32 v115, v120, v121
mav 26,216,42
R 38,1408
mav 30,220,42
v_permlane16_swap_b32 v114, v115
mva 34,42,224
s_waitcnt lgkmcnt(2)
v_add_f32 v120, v114, v115
mav 26,228,46
R 42,1472
mav 30,232,46
v_rcp_f32 v120, v120
mva 34,46,236
v_mul_f32 v118, v118, v120
v_mul_f32 v119, v119, v120
mav 26,240,50
R 46,1536
v_cvt_pk_bf16_f32 v118, v118, v118
mav 30,244,50
v_cvt_pk_bf16_f32 v119, v119, v119
v_and_b32 v114, v118, v96
mva 34,50,248
s_waitcnt lgkmcnt(2)
v_and_b32 v115, v119, v97
v_and_b32 v116, v118, v94
mav 26,252,54
R 50,1600
v_and_b32 v117, v119, v95
mvv 30,128,54
mvv 34,54,132
v_mfma_f32_16x16x32_bf16 v[70:73], v[8:11], v[114:117], 0
v_mfma_f32_16x16x32_bf16 v[74:77], v[108:111], v[114:117], 0
mvv 26,136,38
R 54,1664
mvv 30,140,38
mvv 34,38,144
s_waitcnt lgkmcnt(2)
mvv 26,148,42
R 38,1728
v_cvt_pk_bf16_f32 v114, v70, v71
mvv 30,152,42
v_cvt_pk_bf16_f32 v115, v72, v73
v_cvt_pk_bf16_f32 v116, v74, v75
mvv 34,42,156
v_cvt_pk_bf16_f32 v117, v76, v77
mvv 26,160,46
R 42,1792
mvv 30,164,46
buffer_store_dwordx4 v[114:117], v82, s[36:39], s49 offen sc1
s_add_u32 s49, s49, 313344
mvv 34,46,168
s_waitcnt lgkmcnt(2)
mvv 26,172,50
R 46,1856
mvv 30,176,50
mvv 34,50,180
mvv 26,184,54
R 50,1920
mvv 30,188,54
s_waitcnt lgkmcnt(2)
mvv 34,54,192
s_mov_b32 s60, -13
.p2align 6
LQA1_1:
mvv 26,196,38
R 54,1984
v_xor_b32 v80, v81, v80
mvv 30,200,38
mvv 34,38,204
mvv 26,208,42
R 38,33024
mvv 30,212,42
ds_read_b64 v[122:123], v83 offset:128
mvv 34,42,216
s_waitcnt lgkmcnt(3)
ds_read_b128 v[70:73], v84 offset:128
mvv 26,220,46
R 42,33088
mvv 30,224,46
ds_read_b128 v[74:77], v84 offset:144
mvv 34,46,228
mvv 26,232,50
R 46,33152
mvv 30,236,50
mvv 34,50,240
s_waitcnt lgkmcnt(3)
mvv 26,244,54
R 50,33216
mvv 30,248,54
mvv 34,54,252
v_mfma_f32_16x16x32_bf16 v[14:17], a[0:3], v[38:41], 0
s_waitcnt lgkmcnt(1)
R 54,33280
v_mul_f32 v71, v71, v99
v_mfma_f32_16x16x32_bf16 v[18:21], a[4:7], v[38:41], 0
v_mul_f32 v73, v73, v99
v_mul_f32 v75, v75, v99
v_mfma_f32_16x16x32_bf16 v[22:25], v[38:41], a[8:11], 0
v_fma_f32 v34, v34, v70, v71
v_fma_f32 v35, v35, v72, v73
mav 14,12,42
R 58,33344
v_fma_f32 v36, v36, v74, v75
mav 18,16,42
v_mul_f32 v77, v77, v99
v_fma_f32 v37, v37, v76, v77
mva 22,42,20
v_cvt_pk_bf16_f32 v10, v34, v35
v_cvt_pk_bf16_f32 v11, v36, v37
mav 14,24,46
R 38,33408
v_mul_f32 v124, v123, v62
v_mfma_f32_16x16x32_bf16 v[70:73], v[100:103], v[0:3], v[86:89]
v_fma_f32 v26, v26, v122, v124
v_mul_f32 v125, v123, v63
mav 18,28,46
v_fma_f32 v27, v27, v122, v125
v_mul_f32 v124, v123, v64
mva 22,46,32
s_waitcnt lgkmcnt(2)
v_fma_f32 v28, v28, v122, v124
v_mul_f32 v125, v123, v65
mav 14,36,50
R 42,33472
v_fma_f32 v29, v29, v122, v125
mav 18,40,50
v_mul_f32 v124, v123, v66
v_fma_f32 v30, v30, v122, v124
mva 22,50,44
v_mul_f32 v125, v123, v67
v_fma_f32 v31, v31, v122, v125
mav 14,48,54
R 46,33536
v_mul_f32 v124, v123, v68
mav 18,52,54
v_fma_f32 v32, v32, v122, v124
v_mul_f32 v125, v123, v69
mva 22,54,56
s_waitcnt lgkmcnt(2)
v_fma_f32 v33, v33, v122, v125
v_add_u32 v78, 1, v78
mav 14,60,58
R 50,33600
mav 18,64,58
mva 22,58,68
mav 14,72,38
R 54,33664
mav 18,76,38
mva 22,38,80
s_waitcnt lgkmcnt(2)
mav 14,84,42
R 58,33728
mav 18,88,42
mva 22,42,92
mav 14,96,46
R 38,33792
mav 18,100,46
s_barrier
mva 22,46,104
s_waitcnt lgkmcnt(2)
ds_read_b128 v[104:107], v82 offset:4096
mav 14,108,50
ds_read_b64 v[110:111], v82 offset:5120
mav 18,112,50
R 42,33856
mva 22,50,116
mav 14,120,54
s_waitcnt vmcnt(0)
R 46,33920
mav 18,124,54
buffer_store_dword v78, off, s[36:39], s52 sc1
mva 22,54,128
s_waitcnt lgkmcnt(3)
v_mfma_f32_16x16x32_bf16 v[74:77], v[104:107], v[0:3], v[90:93]
v_cvt_pk_bf16_f32 v0, v26, v27
v_cvt_pk_bf16_f32 v1, v28, v29
mav 14,132,58
R 50,33984
v_cvt_pk_bf16_f32 v2, v30, v31
mav 18,136,58
v_cvt_pk_bf16_f32 v3, v32, v33
mva 22,58,140
v_max3_f32 v118, v70, v71, v74
v_max3_f32 v119, v72, v73, v76
mav 14,144,38
R 54,34048
v_max_f32 v118, v118, v75
mav 18,148,38
v_max_f32 v119, v119, v77
v_max_f32 v120, v118, v119
mva 22,38,152
s_waitcnt lgkmcnt(2)
v_max_f32 v121, v118, v119
mav 14,156,42
R 38,34112
mav 18,160,42
v_permlane32_swap_b32 v120, v121
mva 22,42,164
v_max_f32 v114, v120, v121
v_max_f32 v115, v120, v121
mav 14,168,46
R 42,34176
mav 18,172,46
v_permlane16_swap_b32 v114, v115
mva 22,46,176
s_waitcnt lgkmcnt(2)
v_max_f32 v120, v114, v115
v_mul_f32 v120, s65, v120
mav 14,180,50
R 46,34240
v_fma_f32 v118, v118, s65, -v120
mav 18,184,50
v_fma_f32 v119, v119, s65, -v120
mva 22,50,188
v_exp_f32 v118, v118
mav 14,192,54
R 50,34304
mav 18,196,54
v_exp_f32 v119, v119
mva 22,54,200
s_waitcnt lgkmcnt(2)
v_add_f32 v120, v118, v119
v_add_f32 v121, v118, v119
mav 14,204,38
R 54,34368
mav 18,208,38
v_permlane32_swap_b32 v120, v121
mva 22,38,212
v_add_f32 v114, v120, v121
v_add_f32 v115, v120, v121
mav 14,216,42
R 38,34432
mav 18,220,42
v_permlane16_swap_b32 v114, v115
mva 22,42,224
s_waitcnt lgkmcnt(2)
v_add_f32 v120, v114, v115
mav 14,228,46
R 42,34496
mav 18,232,46
v_rcp_f32 v120, v120
mva 22,46,236
v_mul_f32 v118, v118, v120
v_mul_f32 v119, v119, v120
mav 14,240,50
R 46,34560
v_cvt_pk_bf16_f32 v118, v118, v118
mav 18,244,50
v_cvt_pk_bf16_f32 v119, v119, v119
v_and_b32 v114, v118, v94
mva 22,50,248
s_waitcnt lgkmcnt(2)
v_and_b32 v115, v119, v95
v_and_b32 v116, v118, v96
mav 14,252,54
R 50,34624
v_and_b32 v117, v119, v97
mvv 18,128,54
mvv 22,54,132
v_mfma_f32_16x16x32_bf16 v[70:73], v[8:11], v[114:117], 0
LsQ9:
s_cbranch_execz LsQ13
v_mfma_f32_16x16x32_bf16 v[74:77], v[108:111], v[114:117], 0
mvv 14,136,38
R 54,34688
mvv 18,140,38
mvv 22,38,144
s_waitcnt lgkmcnt(2)
mvv 14,148,42
R 38,34752
v_cvt_pk_bf16_f32 v114, v70, v71
mvv 18,152,42
v_cvt_pk_bf16_f32 v115, v72, v73
v_cvt_pk_bf16_f32 v116, v74, v75
mvv 22,42,156
v_cvt_pk_bf16_f32 v117, v76, v77
mvv 14,160,46
R 42,34816
mvv 18,164,46
buffer_store_dwordx4 v[114:117], v82, s[36:39], s49 offen sc1
s_add_u32 s49, s49, 313344
mvv 22,46,168
s_waitcnt lgkmcnt(2)
mvv 14,172,50
R 46,34880
mvv 18,176,50
mvv 22,50,180
mvv 14,184,54
R 50,34944
mvv 18,188,54
mvv 22,54,192
s_waitcnt lgkmcnt(2)
mvv 14,196,38
R 54,35008
mvv 18,200,38
mvv 22,38,204
mvv 14,208,42
R 38,0
mvv 18,212,42
ds_read_b64 v[122:123], v83 offset:0
mvv 22,42,216
s_waitcnt lgkmcnt(3)
ds_read_b128 v[70:73], v84 offset:0
mvv 14,220,46
R 42,64
mvv 18,224,46
ds_read_b128 v[74:77], v84 offset:16
mvv 22,46,228
mvv 14,232,50
R 46,128
mvv 18,236,50
mvv 22,50,240
s_waitcnt lgkmcnt(3)
mvv 14,244,54
R 50,192
mvv 18,248,54
mvv 22,54,252
v_mfma_f32_16x16x32_bf16 v[26:29], a[0:3], v[38:41], 0
s_waitcnt lgkmcnt(1)
R 54,256
v_mul_f32 v71, v71, v99
v_mfma_f32_16x16x32_bf16 v[30:33], a[4:7], v[38:41], 0
v_mul_f32 v73, v73, v99
v_mul_f32 v75, v75, v99
v_mfma_f32_16x16x32_bf16 v[34:37], v[38:41], a[8:11], 0
v_fma_f32 v22, v22, v70, v71
v_fma_f32 v23, v23, v72, v73
mav 26,12,42
R 58,320
v_fma_f32 v24, v24, v74, v75
mav 30,16,42
v_mul_f32 v77, v77, v99
v_fma_f32 v25, v25, v76, v77
mva 34,42,20
v_cvt_pk_bf16_f32 v8, v22, v23
v_cvt_pk_bf16_f32 v9, v24, v25
mav 26,24,46
R 38,384
v_mul_f32 v124, v123, v62
v_mfma_f32_16x16x32_bf16 v[70:73], v[104:107], v[0:3], v[86:89]
v_fma_f32 v14, v14, v122, v124
v_mul_f32 v125, v123, v63
mav 30,28,46
v_fma_f32 v15, v15, v122, v125
v_mul_f32 v124, v123, v64
mva 34,46,32
s_waitcnt lgkmcnt(2)
v_fma_f32 v16, v16, v122, v124
v_mul_f32 v125, v123, v65
mav 26,36,50
R 42,448
v_fma_f32 v17, v17, v122, v125
mav 30,40,50
v_mul_f32 v124, v123, v66
v_fma_f32 v18, v18, v122, v124
mva 34,50,44
v_mul_f32 v125, v123, v67
v_fma_f32 v19, v19, v122, v125
mav 26,48,54
R 46,512
v_mul_f32 v124, v123, v68
mav 30,52,54
v_fma_f32 v20, v20, v122, v124
v_mul_f32 v125, v123, v69
mva 34,54,56
s_waitcnt lgkmcnt(2)
v_fma_f32 v21, v21, v122, v125
v_add_u32 v78, 1, v78
mav 26,60,58
R 50,576
mav 30,64,58
mva 34,58,68
mav 26,72,38
R 54,640
mav 30,76,38
mva 34,38,80
s_waitcnt lgkmcnt(2)
mav 26,84,42
R 58,704
mav 30,88,42
mva 34,42,92
mav 26,96,46
R 38,768
mav 30,100,46
s_barrier
mva 34,46,104
s_waitcnt lgkmcnt(2)
ds_read_b128 v[100:103], v82 offset:0
mav 26,108,50
ds_read_b64 v[108:109], v82 offset:1024
mav 30,112,50
R 42,832
mva 34,50,116
mav 26,120,54
s_waitcnt vmcnt(0)
R 46,896
mav 30,124,54
buffer_store_dword v78, off, s[36:39], s52 sc1
mva 34,54,128
s_waitcnt lgkmcnt(3)
v_mfma_f32_16x16x32_bf16 v[74:77], v[100:103], v[0:3], v[90:93]
v_cvt_pk_bf16_f32 v0, v14, v15
v_cvt_pk_bf16_f32 v1, v16, v17
mav 26,132,58
R 50,960
v_cvt_pk_bf16_f32 v2, v18, v19
mav 30,136,58
v_cvt_pk_bf16_f32 v3, v20, v21
mva 34,58,140
v_max3_f32 v118, v70, v71, v74
v_max3_f32 v119, v72, v73, v76
mav 26,144,38
R 54,1024
v_max_f32 v118, v118, v75
mav 30,148,38
v_max_f32 v119, v119, v77
v_max_f32 v120, v118, v119
mva 34,38,152
s_waitcnt lgkmcnt(2)
v_max_f32 v121, v118, v119
mav 26,156,42
R 38,1088
mav 30,160,42
v_permlane32_swap_b32 v120, v121
mva 34,42,164
v_max_f32 v114, v120, v121
v_max_f32 v115, v120, v121
mav 26,168,46
R 42,1152
mav 30,172,46
v_permlane16_swap_b32 v114, v115
mva 34,46,176
s_waitcnt lgkmcnt(2)
v_max_f32 v120, v114, v115
v_mul_f32 v120, s65, v120
mav 26,180,50
R 46,1216
v_fma_f32 v118, v118, s65, -v120
mav 30,184,50
v_fma_f32 v119, v119, s65, -v120
mva 34,50,188
v_exp_f32 v118, v118
mav 26,192,54
R 50,1280
mav 30,196,54
v_exp_f32 v119, v119
mva 34,54,200
s_waitcnt lgkmcnt(2)
v_add_f32 v120, v118, v119
v_add_f32 v121, v118, v119
mav 26,204,38
R 54,1344
mav 30,208,38
v_permlane32_swap_b32 v120, v121
mva 34,38,212
v_add_f32 v114, v120, v121
v_add_f32 v115, v120, v121
mav 26,216,42
R 38,1408
mav 30,220,42
v_permlane16_swap_b32 v114, v115
mva 34,42,224
s_waitcnt lgkmcnt(2)
v_add_f32 v120, v114, v115
mav 26,228,46
R 42,1472
mav 30,232,46
v_rcp_f32 v120, v120
mva 34,46,236
v_mul_f32 v118, v118, v120
v_mul_f32 v119, v119, v120
mav 26,240,50
R 46,1536
v_cvt_pk_bf16_f32 v118, v118, v118
mav 30,244,50
v_cvt_pk_bf16_f32 v119, v119, v119
v_and_b32 v114, v118, v96
mva 34,50,248
s_waitcnt lgkmcnt(2)
v_and_b32 v115, v119, v97
v_and_b32 v116, v118, v94
mav 26,252,54
R 50,1600
v_and_b32 v117, v119, v95
mvv 30,128,54
mvv 34,54,132
v_mfma_f32_16x16x32_bf16 v[70:73], v[8:11], v[114:117], 0
v_mfma_f32_16x16x32_bf16 v[74:77], v[108:111], v[114:117], 0
mvv 26,136,38
R 54,1664
mvv 30,140,38
mvv 34,38,144
s_waitcnt lgkmcnt(2)
mvv 26,148,42
R 38,1728
v_cvt_pk_bf16_f32 v114, v70, v71
mvv 30,152,42
v_cvt_pk_bf16_f32 v115, v72, v73
v_cvt_pk_bf16_f32 v116, v74, v75
mvv 34,42,156
v_cvt_pk_bf16_f32 v117, v76, v77
mvv 26,160,46
R 42,1792
mvv 30,164,46
buffer_store_dwordx4 v[114:117], v82, s[36:39], s49 offen sc1
s_add_u32 s49, s49, 313344
mvv 34,46,168
s_waitcnt lgkmcnt(2)
mvv 26,172,50
R 46,1856
mvv 30,176,50
mvv 34,50,180
mvv 26,184,54
R 50,1920
mvv 30,188,54
s_waitcnt lgkmcnt(2)
s_add_u32 s60, s60, 1
mvv 34,54,192
s_cbranch_scc0 LQA1_1
mvv 26,196,38
R 54,1984
v_xor_b32 v80, v81, v80
mvv 30,200,38
mvv 34,38,204
mvv 26,208,42
R 38,33024
mvv 30,212,42
ds_read_b64 v[122:123], v83 offset:128
mvv 34,42,216
s_waitcnt lgkmcnt(3)
ds_read_b128 v[70:73], v84 offset:128
mvv 26,220,46
R 42,33088
mvv 30,224,46
ds_read_b128 v[74:77], v84 offset:144
mvv 34,46,228
mvv 26,232,50
R 46,33152
mvv 30,236,50
mvv 34,50,240
s_waitcnt lgkmcnt(3)
mvv 26,244,54
R 50,33216
mvv 30,248,54
mvv 34,54,252
v_mfma_f32_16x16x32_bf16 v[14:17], a[0:3], v[38:41], 0
s_waitcnt lgkmcnt(1)
R 54,33280
v_mul_f32 v71, v71, v99
v_mfma_f32_16x16x32_bf16 v[18:21], a[4:7], v[38:41], 0
v_mul_f32 v73, v73, v99
v_mul_f32 v75, v75, v99
v_mfma_f32_16x16x32_bf16 v[22:25], v[38:41], a[8:11], 0
v_fma_f32 v34, v34, v70, v71
v_fma_f32 v35, v35, v72, v73
mav 14,12,42
R 58,33344
v_fma_f32 v36, v36, v74, v75
mav 18,16,42
v_mul_f32 v77, v77, v99
v_fma_f32 v37, v37, v76, v77
mva 22,42,20
v_cvt_pk_bf16_f32 v10, v34, v35
v_cvt_pk_bf16_f32 v11, v36, v37
mav 14,24,46
R 38,33408
v_mul_f32 v124, v123, v62
v_mfma_f32_16x16x32_bf16 v[70:73], v[100:103], v[0:3], v[86:89]
v_fma_f32 v26, v26, v122, v124
v_mul_f32 v125, v123, v63
mav 18,28,46
v_fma_f32 v27, v27, v122, v125
v_mul_f32 v124, v123, v64
mva 22,46,32
s_waitcnt lgkmcnt(2)
v_fma_f32 v28, v28, v122, v124
v_mul_f32 v125, v123, v65
mav 14,36,50
R 42,33472
v_fma_f32 v29, v29, v122, v125
mav 18,40,50
v_mul_f32 v124, v123, v66
v_fma_f32 v30, v30, v122, v124
mva 22,50,44
v_mul_f32 v125, v123, v67
v_fma_f32 v31, v31, v122, v125
mav 14,48,54
R 46,33536
v_mul_f32 v124, v123, v68
mav 18,52,54
v_fma_f32 v32, v32, v122, v124
v_mul_f32 v125, v123, v69
mva 22,54,56
s_waitcnt lgkmcnt(2)
v_fma_f32 v33, v33, v122, v125
v_add_u32 v78, 1, v78
mav 14,60,58
R 50,33600
mav 18,64,58
mva 22,58,68
mav 14,72,38
R 54,33664
mav 18,76,38
mva 22,38,80
s_waitcnt lgkmcnt(2)
mav 14,84,42
R 58,33728
mav 18,88,42
mva 22,42,92
mav 14,96,46
R 38,33792
mav 18,100,46
s_barrier
mva 22,46,104
s_waitcnt lgkmcnt(2)
ds_read_b128 v[104:107], v82 offset:4096
mav 14,108,50
ds_read_b64 v[110:111], v82 offset:5120
mav 18,112,50
R 42,33856
mva 22,50,116
mav 14,120,54
s_waitcnt vmcnt(0)
R 46,33920
mav 18,124,54
buffer_store_dword v78, off, s[36:39], s52 sc1
mva 22,54,128
s_waitcnt lgkmcnt(3)
v_mfma_f32_16x16x32_bf16 v[74:77], v[104:107], v[0:3], v[90:93]
v_cvt_pk_bf16_f32 v0, v26, v27
v_cvt_pk_bf16_f32 v1, v28, v29
mav 14,132,58
R 50,33984
v_cvt_pk_bf16_f32 v2, v30, v31
mav 18,136,58
v_cvt_pk_bf16_f32 v3, v32, v33
mva 22,58,140
v_max3_f32 v118, v70, v71, v74
v_max3_f32 v119, v72, v73, v76
mav 14,144,38
R 54,34048
v_max_f32 v118, v118, v75
mav 18,148,38
v_max_f32 v119, v119, v77
v_max_f32 v120, v118, v119
mva 22,38,152
s_waitcnt lgkmcnt(2)
v_max_f32 v121, v118, v119
mav 14,156,42
R 38,34112
mav 18,160,42
v_permlane32_swap_b32 v120, v121
mva 22,42,164
v_max_f32 v114, v120, v121
v_max_f32 v115, v120, v121
mav 14,168,46
R 42,34176
mav 18,172,46
v_permlane16_swap_b32 v114, v115
mva 22,46,176
s_waitcnt lgkmcnt(2)
v_max_f32 v120, v114, v115
v_mul_f32 v120, s65, v120
mav 14,180,50
R 46,34240
v_fma_f32 v118, v118, s65, -v120
mav 18,184,50
v_fma_f32 v119, v119, s65, -v120
mva 22,50,188
v_exp_f32 v118, v118
mav 14,192,54
R 50,34304
mav 18,196,54
v_exp_f32 v119, v119
mva 22,54,200
s_waitcnt lgkmcnt(2)
v_add_f32 v120, v118, v119
v_add_f32 v121, v118, v119
mav 14,204,38
R 54,34368
mav 18,208,38
v_permlane32_swap_b32 v120, v121
mva 22,38,212
v_add_f32 v114, v120, v121
v_add_f32 v115, v120, v121
mav 14,216,42
R 38,34432
mav 18,220,42
v_permlane16_swap_b32 v114, v115
mva 22,42,224
s_waitcnt lgkmcnt(2)
v_add_f32 v120, v114, v115
mav 14,228,46
R 42,34496
mav 18,232,46
v_rcp_f32 v120, v120
mva 22,46,236
v_mul_f32 v118, v118, v120
v_mul_f32 v119, v119, v120
mav 14,240,50
R 46,34560
v_cvt_pk_bf16_f32 v118, v118, v118
mav 18,244,50
v_cvt_pk_bf16_f32 v119, v119, v119
v_and_b32 v114, v118, v94
mva 22,50,248
s_waitcnt lgkmcnt(2)
v_and_b32 v115, v119, v95
v_and_b32 v116, v118, v96
mav 14,252,54
R 50,34624
v_and_b32 v117, v119, v97
mvv 18,128,54
mvv 22,54,132
v_mfma_f32_16x16x32_bf16 v[70:73], v[8:11], v[114:117], 0
v_mfma_f32_16x16x32_bf16 v[74:77], v[108:111], v[114:117], 0
mvv 14,136,38
R 54,34688
mvv 18,140,38
mvv 22,38,144
s_waitcnt lgkmcnt(2)
mvv 14,148,42
R 38,34752
v_cvt_pk_bf16_f32 v114, v70, v71
mvv 18,152,42
v_cvt_pk_bf16_f32 v115, v72, v73
v_cvt_pk_bf16_f32 v116, v74, v75
mvv 22,42,156
v_cvt_pk_bf16_f32 v117, v76, v77
mvv 14,160,46
R 42,34816
mvv 18,164,46
buffer_store_dwordx4 v[114:117], v82, s[36:39], s49 offen sc1
s_add_u32 s49, s49, 313344
mvv 22,46,168
s_waitcnt lgkmcnt(2)
mvv 14,172,50
R 46,34880
mvv 18,176,50
mvv 22,50,180
mvv 14,184,54
R 50,34944
mvv 18,188,54
mvv 22,54,192
s_waitcnt lgkmcnt(2)
mvv 14,196,38
R 54,35008
mvv 18,200,38
mvv 22,38,204
mvv 14,208,42
R 38,0
mvv 18,212,42
ds_read_b64 v[122:123], v83 offset:0
mvv 22,42,216
s_waitcnt lgkmcnt(3)
ds_read_b128 v[70:73], v84 offset:0
mvv 14,220,46
R 42,64
mvv 18,224,46
ds_read_b128 v[74:77], v84 offset:16
mvv 22,46,228
mvv 14,232,50
R 46,128
mvv 18,236,50
mvv 22,50,240
s_waitcnt lgkmcnt(3)
mvv 14,244,54
R 50,192
mvv 18,248,54
mvv 22,54,252
v_mfma_f32_16x16x32_bf16 v[26:29], a[0:3], v[38:41], 0
s_waitcnt lgkmcnt(1)
R 54,256
v_mul_f32 v71, v71, v99
v_mfma_f32_16x16x32_bf16 v[30:33], a[4:7], v[38:41], 0
v_mul_f32 v73, v73, v99
v_mul_f32 v75, v75, v99
v_mfma_f32_16x16x32_bf16 v[34:37], v[38:41], a[8:11], 0
v_fma_f32 v22, v22, v70, v71
v_fma_f32 v23, v23, v72, v73
mav 26,12,42
R 58,320
v_fma_f32 v24, v24, v74, v75
mav 30,16,42
v_mul_f32 v77, v77, v99
v_fma_f32 v25, v25, v76, v77
mva 34,42,20
v_cvt_pk_bf16_f32 v8, v22, v23
v_cvt_pk_bf16_f32 v9, v24, v25
mav 26,24,46
R 38,384
v_mul_f32 v124, v123, v62
v_mfma_f32_16x16x32_bf16 v[70:73], v[104:107], v[0:3], v[86:89]
v_fma_f32 v14, v14, v122, v124
v_mul_f32 v125, v123, v63
mav 30,28,46
v_fma_f32 v15, v15, v122, v125
v_mul_f32 v124, v123, v64
mva 34,46,32
s_waitcnt lgkmcnt(2)
v_fma_f32 v16, v16, v122, v124
v_mul_f32 v125, v123, v65
mav 26,36,50
R 42,448
v_fma_f32 v17, v17, v122, v125
mav 30,40,50
v_mul_f32 v124, v123, v66
v_fma_f32 v18, v18, v122, v124
mva 34,50,44
buffer_load_dwordx4 v[4:7], v82, s[36:39], s55 offen sc1
mav 26,48,54
R 46,512
v_mul_f32 v125, v123, v67
mav 30,52,54
buffer_load_dwordx2 v[12:13], v98, s[36:39], s55 offen offset:1024 sc1
mva 34,54,56
s_waitcnt lgkmcnt(2)
v_fma_f32 v19, v19, v122, v125
v_mul_f32 v124, v123, v68
mav 26,60,58
buffer_load_dwordx2 v[112:113], v98, s[36:39], s55 offen offset:1536 sc1
mav 30,64,58
R 50,576
v_fma_f32 v20, v20, v122, v124
mva 34,58,68
v_mul_f32 v125, v123, v69
v_fma_f32 v21, v21, v122, v125
mav 26,72,38
R 54,640
v_add_u32 v78, 1, v78
mav 30,76,38
mva 34,38,80
s_waitcnt lgkmcnt(2)
mav 26,84,42
R 58,704
mav 30,88,42
mva 34,42,92
mav 26,96,46
R 38,768
mav 30,100,46
s_barrier
mva 34,46,104
s_waitcnt lgkmcnt(2)
ds_read_b128 v[100:103], v82 offset:0
LsQ10:
s_cbranch_execz LsQ14
mav 26,108,50
ds_read_b64 v[108:109], v82 offset:1024
mav 30,112,50
R 42,832
mva 34,50,116
mav 26,120,54
s_waitcnt vmcnt(3)
R 46,896
mav 30,124,54
buffer_store_dword v78, off, s[36:39], s52 sc1
mva 34,54,128
s_waitcnt lgkmcnt(3)
v_mfma_f32_16x16x32_bf16 v[74:77], v[100:103], v[0:3], v[90:93]
v_cvt_pk_bf16_f32 v0, v14, v15
v_cvt_pk_bf16_f32 v1, v16, v17
mav 26,132,58
R 50,960
v_cvt_pk_bf16_f32 v2, v18, v19
mav 30,136,58
v_cvt_pk_bf16_f32 v3, v20, v21
mva 34,58,140
v_max3_f32 v118, v70, v71, v74
v_max3_f32 v119, v72, v73, v76
mav 26,144,38
R 54,1024
v_max_f32 v118, v118, v75
mav 30,148,38
v_max_f32 v119, v119, v77
v_max_f32 v120, v118, v119
mva 34,38,152
s_waitcnt lgkmcnt(2)
v_max_f32 v121, v118, v119
mav 26,156,42
R 38,1088
mav 30,160,42
v_permlane32_swap_b32 v120, v121
mva 34,42,164
v_max_f32 v114, v120, v121
v_max_f32 v115, v120, v121
mav 26,168,46
R 42,1152
mav 30,172,46
v_permlane16_swap_b32 v114, v115
mva 34,46,176
s_waitcnt lgkmcnt(2)
v_max_f32 v120, v114, v115
v_mul_f32 v120, s65, v120
mav 26,180,50
R 46,1216
v_fma_f32 v118, v118, s65, -v120
mav 30,184,50
v_fma_f32 v119, v119, s65, -v120
mva 34,50,188
v_exp_f32 v118, v118
mav 26,192,54
R 50,1280
mav 30,196,54
v_exp_f32 v119, v119
mva 34,54,200
s_waitcnt lgkmcnt(2)
v_add_f32 v120, v118, v119
v_add_f32 v121, v118, v119
mav 26,204,38
R 54,1344
mav 30,208,38
v_permlane32_swap_b32 v120, v121
mva 34,38,212
v_add_f32 v114, v120, v121
v_add_f32 v115, v120, v121
mav 26,216,42
R 38,1408
mav 30,220,42
v_permlane16_swap_b32 v114, v115
mva 34,42,224
s_waitcnt lgkmcnt(2)
v_add_f32 v120, v114, v115
mav 26,228,46
R 42,1472
mav 30,232,46
v_rcp_f32 v120, v120
mva 34,46,236
v_mul_f32 v118, v118, v120
v_mul_f32 v119, v119, v120
mav 26,240,50
R 46,1536
v_cvt_pk_bf16_f32 v118, v118, v118
mav 30,244,50
v_cvt_pk_bf16_f32 v119, v119, v119
v_and_b32 v114, v118, v96
mva 34,50,248
s_waitcnt lgkmcnt(2)
v_and_b32 v115, v119, v97
v_and_b32 v116, v118, v94
mav 26,252,54
R 50,1600
v_and_b32 v117, v119, v95
mvv 30,128,54
mvv 34,54,132
v_mfma_f32_16x16x32_bf16 v[70:73], v[8:11], v[114:117], 0
v_mfma_f32_16x16x32_bf16 v[74:77], v[108:111], v[114:117], 0
mvv 26,136,38
R 54,1664
mvv 30,140,38
mvv 34,38,144
s_waitcnt lgkmcnt(2)
mvv 26,148,42
R 38,1728
v_cvt_pk_bf16_f32 v114, v70, v71
mvv 30,152,42
v_cvt_pk_bf16_f32 v115, v72, v73
v_cvt_pk_bf16_f32 v116, v74, v75
mvv 34,42,156
v_cvt_pk_bf16_f32 v117, v76, v77
mvv 26,160,46
R 42,1792
mvv 30,164,46
buffer_store_dwordx4 v[114:117], v82, s[36:39], s49 offen sc1
s_add_u32 s49, s49, 313344
mvv 34,46,168
s_waitcnt lgkmcnt(2)
mvv 26,172,50
R 46,1856
mvv 30,176,50
mvv 34,50,180
mvv 26,184,54
R 50,1920
mvv 30,188,54
s_waitcnt lgkmcnt(2)
mvv 34,54,192
mvv 26,196,38
R 54,1984
v_xor_b32 v80, v81, v80
mvv 30,200,38
mvv 34,38,204
mvv 26,208,42
R 38,33024
mvv 30,212,42
ds_read_b64 v[122:123], v83 offset:128
mvv 34,42,216
s_waitcnt lgkmcnt(3)
ds_read_b128 v[70:73], v84 offset:128
mvv 26,220,46
R 42,33088
mvv 30,224,46
ds_read_b128 v[74:77], v84 offset:144
mvv 34,46,228
mvv 26,232,50
R 46,33152
mvv 30,236,50
mvv 34,50,240
s_waitcnt lgkmcnt(3)
mvv 26,244,54
R 50,33216
mvv 30,248,54
mvv 34,54,252
v_mfma_f32_16x16x32_bf16 v[14:17], a[0:3], v[38:41], 0
s_waitcnt lgkmcnt(1)
R 54,33280
v_mul_f32 v71, v71, v99
v_mfma_f32_16x16x32_bf16 v[18:21], a[4:7], v[38:41], 0
v_mul_f32 v73, v73, v99
v_mul_f32 v75, v75, v99
v_mfma_f32_16x16x32_bf16 v[22:25], v[38:41], a[8:11], 0
v_fma_f32 v34, v34, v70, v71
v_fma_f32 v35, v35, v72, v73
mav 14,12,42
R 58,33344
v_fma_f32 v36, v36, v74, v75
mav 18,16,42
v_mul_f32 v77, v77, v99
v_fma_f32 v37, v37, v76, v77
mva 22,42,20
v_cvt_pk_bf16_f32 v10, v34, v35
v_cvt_pk_bf16_f32 v11, v36, v37
mav 14,24,46
R 38,33408
v_mul_f32 v124, v123, v62
v_mfma_f32_16x16x32_bf16 v[70:73], v[100:103], v[0:3], v[86:89]
v_fma_f32 v26, v26, v122, v124
v_mul_f32 v125, v123, v63
mav 18,28,46
v_fma_f32 v27, v27, v122, v125
v_mul_f32 v124, v123, v64
mva 22,46,32
s_waitcnt lgkmcnt(2)
v_fma_f32 v28, v28, v122, v124
v_mul_f32 v125, v123, v65
mav 14,36,50
R 42,33472
v_fma_f32 v29, v29, v122, v125
mav 18,40,50
v_mul_f32 v124, v123, v66
v_fma_f32 v30, v30, v122, v124
mva 22,50,44
v_mul_f32 v125, v123, v67
v_fma_f32 v31, v31, v122, v125
mav 14,48,54
R 46,33536
v_mul_f32 v124, v123, v68
mav 18,52,54
v_fma_f32 v32, v32, v122, v124
v_mul_f32 v125, v123, v69
mva 22,54,56
s_waitcnt lgkmcnt(2)
v_fma_f32 v33, v33, v122, v125
v_add_u32 v78, 1, v78
mav 14,60,58
R 50,33600
mav 18,64,58
mva 22,58,68
mav 14,72,38
R 54,33664
mav 18,76,38
mva 22,38,80
s_waitcnt lgkmcnt(2)
mav 14,84,42
R 58,33728
mav 18,88,42
mva 22,42,92
mav 14,96,46
R 38,33792
mav 18,100,46
s_barrier
mva 22,46,104
s_waitcnt lgkmcnt(2)
ds_read_b128 v[104:107], v82 offset:4096
mav 14,108,50
ds_read_b64 v[110:111], v82 offset:5120
mav 18,112,50
R 42,33856
mva 22,50,116
mav 14,120,54
s_waitcnt vmcnt(0)
R 46,33920
mav 18,124,54
buffer_store_dword v78, off, s[36:39], s52 sc1
mva 22,54,128
s_waitcnt lgkmcnt(3)
v_mfma_f32_16x16x32_bf16 v[74:77], v[104:107], v[0:3], v[90:93]
v_cvt_pk_bf16_f32 v0, v26, v27
v_cvt_pk_bf16_f32 v1, v28, v29
mav 14,132,58
R 50,33984
v_cvt_pk_bf16_f32 v2, v30, v31
mav 18,136,58
v_cvt_pk_bf16_f32 v3, v32, v33
mva 22,58,140
v_max3_f32 v118, v70, v71, v74
v_max3_f32 v119, v72, v73, v76
mav 14,144,38
R 54,34048
v_max_f32 v118, v118, v75
mav 18,148,38
v_max_f32 v119, v119, v77
v_max_f32 v120, v118, v119
mva 22,38,152
s_waitcnt lgkmcnt(2)
v_max_f32 v121, v118, v119
mav 14,156,42
R 38,34112
mav 18,160,42
v_permlane32_swap_b32 v120, v121
mva 22,42,164
v_max_f32 v114, v120, v121
v_max_f32 v115, v120, v121
mav 14,168,46
R 42,34176
mav 18,172,46
v_permlane16_swap_b32 v114, v115
mva 22,46,176
s_waitcnt lgkmcnt(2)
v_max_f32 v120, v114, v115
v_mul_f32 v120, s65, v120
mav 14,180,50
R 46,34240
v_fma_f32 v118, v118, s65, -v120
mav 18,184,50
v_fma_f32 v119, v119, s65, -v120
mva 22,50,188
v_exp_f32 v118, v118
mav 14,192,54
R 50,34304
mav 18,196,54
v_exp_f32 v119, v119
mva 22,54,200
s_waitcnt lgkmcnt(2)
v_add_f32 v120, v118, v119
v_add_f32 v121, v118, v119
mav 14,204,38
R 54,34368
mav 18,208,38
v_permlane32_swap_b32 v120, v121
mva 22,38,212
v_add_f32 v114, v120, v121
v_add_f32 v115, v120, v121
mav 14,216,42
R 38,34432
mav 18,220,42
v_permlane16_swap_b32 v114, v115
mva 22,42,224
s_waitcnt lgkmcnt(2)
v_add_f32 v120, v114, v115
mav 14,228,46
R 42,34496
mav 18,232,46
v_rcp_f32 v120, v120
mva 22,46,236
v_mul_f32 v118, v118, v120
v_mul_f32 v119, v119, v120
mav 14,240,50
R 46,34560
v_cvt_pk_bf16_f32 v118, v118, v118
mav 18,244,50
v_cvt_pk_bf16_f32 v119, v119, v119
v_and_b32 v114, v118, v94
mva 22,50,248
s_waitcnt lgkmcnt(2)
v_and_b32 v115, v119, v95
v_and_b32 v116, v118, v96
mav 14,252,54
R 50,34624
v_and_b32 v117, v119, v97
mvv 18,128,54
mvv 22,54,132
v_mfma_f32_16x16x32_bf16 v[70:73], v[8:11], v[114:117], 0
v_mfma_f32_16x16x32_bf16 v[74:77], v[108:111], v[114:117], 0
mvv 14,136,38
R 54,34688
mvv 18,140,38
mvv 22,38,144
s_waitcnt lgkmcnt(2)
mvv 14,148,42
R 38,34752
v_cvt_pk_bf16_f32 v114, v70, v71
mvv 18,152,42
v_cvt_pk_bf16_f32 v115, v72, v73
v_cvt_pk_bf16_f32 v116, v74, v75
mvv 22,42,156
v_cvt_pk_bf16_f32 v117, v76, v77
mvv 14,160,46
R 42,34816
mvv 18,164,46
buffer_store_dwordx4 v[114:117], v82, s[36:39], s49 offen sc1
s_add_u32 s49, s49, 313344
mvv 22,46,168
s_waitcnt lgkmcnt(2)
mvv 14,172,50
R 46,34880
mvv 18,176,50
mvv 22,50,180
mvv 14,184,54
R 50,34944
mvv 18,188,54
mvv 22,54,192
s_waitcnt lgkmcnt(2)
mvv 14,196,38
R 54,35008
mvv 18,200,38
mvv 22,38,204
mvv 14,208,42
R 38,0
mvv 18,212,42
ds_read_b64 v[122:123], v83 offset:0
mvv 22,42,216
s_waitcnt lgkmcnt(3)
ds_read_b128 v[70:73], v84 offset:0
mvv 14,220,46
R 42,64
mvv 18,224,46
ds_read_b128 v[74:77], v84 offset:16
mvv 22,46,228
mvv 14,232,50
R 46,128
mvv 18,236,50
mvv 22,50,240
s_waitcnt lgkmcnt(3)
mvv 14,244,54
R 50,192
mvv 18,248,54
mvv 22,54,252
v_mfma_f32_16x16x32_bf16 v[26:29], a[0:3], v[38:41], 0
s_waitcnt lgkmcnt(1)
R 54,256
v_mul_f32 v71, v71, v99
v_mfma_f32_16x16x32_bf16 v[30:33], a[4:7], v[38:41], 0
v_mul_f32 v73, v73, v99
v_mul_f32 v75, v75, v99
v_mfma_f32_16x16x32_bf16 v[34:37], v[38:41], a[8:11], 0
v_fma_f32 v22, v22, v70, v71
v_fma_f32 v23, v23, v72, v73
mav 26,12,42
R 58,320
v_fma_f32 v24, v24, v74, v75
mav 30,16,42
v_mul_f32 v77, v77, v99
v_fma_f32 v25, v25, v76, v77
mva 34,42,20
v_cvt_pk_bf16_f32 v8, v22, v23
v_cvt_pk_bf16_f32 v9, v24, v25
mav 26,24,46
R 38,384
v_min_f32 v74, s56, v90
v_mfma_f32_16x16x32_bf16 v[70:73], v[104:107], v[0:3], v[86:89]
v_min_f32 v75, s56, v91
v_min_f32 v76, s56, v92
mav 30,28,46
v_min_f32 v77, s56, v93
v_mul_f32 v124, v123, v62
mva 34,46,32
s_waitcnt lgkmcnt(2)
v_fma_f32 v14, v14, v122, v124
v_mul_f32 v125, v123, v63
mvv 74,4,0
v_fma_f32 v15, v15, v122, v125
v_mul_f32 v124, v123, v64
mav 26,36,50
R 42,448
v_fma_f32 v16, v16, v122, v124
mav 30,40,50
v_mul_f32 v125, v123, v65
v_fma_f32 v17, v17, v122, v125
mva 34,50,44
v_max3_f32 v118, v70, v71, v74
v_max3_f32 v119, v72, v73, v76
mav 26,48,54
R 46,512
v_max_f32 v118, v118, v75
mav 30,52,54
v_max_f32 v119, v119, v77
v_max_f32 v120, v118, v119
mva 34,54,56
s_waitcnt lgkmcnt(2)
v_max_f32 v121, v118, v119
v_mul_f32 v124, v123, v66
mav 26,60,58
R 50,576
v_fma_f32 v18, v18, v122, v124
mav 30,64,58
v_permlane32_swap_b32 v120, v121
mva 34,58,68
v_max_f32 v114, v120, v121
v_max_f32 v115, v120, v121
mav 26,72,38
R 54,640
v_mul_f32 v125, v123, v67
mav 30,76,38
v_permlane16_swap_b32 v114, v115
mva 34,38,80
s_waitcnt lgkmcnt(2)
v_max_f32 v120, v114, v115
v_mul_f32 v120, s65, v120
mav 26,84,42
R 58,704
v_fma_f32 v118, v118, s65, -v120
mav 30,88,42
v_fma_f32 v119, v119, s65, -v120
v_fma_f32 v19, v19, v122, v125
mva 34,42,92
v_exp_f32 v118, v118
mav 26,96,46
R 38,768
v_mul_f32 v124, v123, v68
mav 30,100,46
s_barrier
mva 34,46,104
s_waitcnt lgkmcnt(2)
ds_read_b128 v[100:103], v82 offset:0
v_fma_f32 v20, v20, v122, v124
mav 26,108,50
ds_read_b64 v[108:109], v82 offset:1024
v_mul_f32 v125, v123, v69
mav 30,112,50
R 42,832
v_fma_f32 v21, v21, v122, v125
mva 34,50,116
v_exp_f32 v119, v119
mav 26,120,54
R 46,896
v_add_f32 v120, v118, v119
mav 30,124,54
v_add_f32 v121, v118, v119
v_cvt_pk_bf16_f32 v0, v14, v15
mva 34,54,128
s_waitcnt lgkmcnt(3)
v_permlane32_swap_b32 v120, v121
mav 26,132,58
R 50,960
v_add_f32 v114, v120, v121
mav 30,136,58
v_add_f32 v115, v120, v121
v_cvt_pk_bf16_f32 v1, v16, v17
mva 34,58,140
v_permlane16_swap_b32 v114, v115
mav 26,144,38
R 54,1024
v_add_f32 v120, v114, v115
mav 30,148,38
v_rcp_f32 v120, v120
mva 34,38,152
s_waitcnt lgkmcnt(2)
v_mul_f32 v118, v118, v120
v_mul_f32 v119, v119, v120
mav 26,156,42
R 38,1088
v_cvt_pk_bf16_f32 v118, v118, v118
mav 30,160,42
v_cvt_pk_bf16_f32 v119, v119, v119
v_and_b32 v114, v118, v94
mva 34,42,164
buffer_store_dwordx4 v[100:103], v82, s[36:39], s54 offen offset:2048 sc1
v_and_b32 v115, v119, v95
v_and_b32 v116, v118, v96
v_and_b32 v117, v119, v97
mav 26,168,46
R 42,1152
v_cvt_pk_bf16_f32 v2, v18, v19
v_mfma_f32_16x16x32_bf16 v[70:73], v[10:13], v[114:117], 0
v_cvt_pk_bf16_f32 v3, v20, v21
v_add_u32 v78, 1, v78
v_mfma_f32_16x16x32_bf16 v[74:77], v[110:113], v[114:117], 0
s_waitcnt vmcnt(1)
mav 30,172,46
buffer_store_dword v78, off, s[36:39], s52 sc1
mva 34,46,176
s_waitcnt lgkmcnt(2)
v_cvt_pk_bf16_f32 v114, v70, v71
v_cvt_pk_bf16_f32 v115, v72, v73
mav 26,180,50
buffer_store_dwordx2 v[8:9], v98, s[36:39], s54 offen offset:3072 sc1
R 46,1216
mav 30,184,50
v_cvt_pk_bf16_f32 v116, v74, v75
v_cvt_pk_bf16_f32 v117, v76, v77
mva 34,50,188
buffer_store_dwordx4 v[114:117], v82, s[36:39], s49 offen sc1
s_add_u32 s49, s49, 313344
mav 26,192,54
R 50,1280
mav 30,196,54
buffer_store_dwordx2 v[108:109], v98, s[36:39], s54 offen offset:3584 sc1
mva 34,54,200
s_waitcnt lgkmcnt(2)
mav 26,204,38
R 54,1344
mav 30,208,38
mva 34,38,212
mav 26,216,42
R 38,1408
mav 30,220,42
mva 34,42,224
s_waitcnt lgkmcnt(2)
mav 26,228,46
R 42,1472
mav 30,232,46
mva 34,46,236
mav 26,240,50
R 46,1536
mav 30,244,50
mva 34,50,248
s_waitcnt lgkmcnt(2)
mav 26,252,54
R 50,1600
mvv 30,128,54
mvv 34,54,132
mvv 26,136,38
R 54,1664
mvv 30,140,38
mvv 34,38,144
s_waitcnt lgkmcnt(2)
mvv 26,148,42
R 38,1728
mvv 30,152,42
mvv 34,42,156
mvv 26,160,46
R 42,1792
mvv 30,164,46
mvv 34,46,168
s_waitcnt lgkmcnt(2)
mvv 26,172,50
R 46,1856
mvv 30,176,50
mvv 34,50,180
mvv 26,184,54
R 50,1920
mvv 30,188,54
s_waitcnt lgkmcnt(2)
mvv 34,54,192
mvv 26,196,38
R 54,1984
LsQ11:
s_cbranch_execz LsQ15
v_xor_b32 v80, v81, v80
mvv 30,200,38
mvv 34,38,204
mvv 26,208,42
R 38,33024
mvv 30,212,42
ds_read_b64 v[122:123], v83 offset:128
mvv 34,42,216
s_waitcnt lgkmcnt(3)
ds_read_b128 v[70:73], v84 offset:128
mvv 26,220,46
R 42,33088
mvv 30,224,46
ds_read_b128 v[74:77], v84 offset:144
mvv 34,46,228
mvv 26,232,50
R 46,33152
mvv 30,236,50
mvv 34,50,240
s_waitcnt lgkmcnt(3)
mvv 26,244,54
R 50,33216
mvv 30,248,54
mvv 34,54,252
v_mfma_f32_16x16x32_bf16 v[14:17], a[0:3], v[38:41], 0
s_waitcnt lgkmcnt(1)
R 54,33280
v_mul_f32 v71, v71, v99
v_mfma_f32_16x16x32_bf16 v[18:21], a[4:7], v[38:41], 0
v_mul_f32 v73, v73, v99
v_mul_f32 v75, v75, v99
v_mfma_f32_16x16x32_bf16 v[22:25], v[38:41], a[8:11], 0
v_fma_f32 v34, v34, v70, v71
v_fma_f32 v35, v35, v72, v73
mav 14,12,42
R 58,33344
v_fma_f32 v36, v36, v74, v75
mav 18,16,42
v_mul_f32 v77, v77, v99
v_fma_f32 v37, v37, v76, v77
mva 22,42,20
v_cvt_pk_bf16_f32 v10, v34, v35
v_cvt_pk_bf16_f32 v11, v36, v37
mav 14,24,46
R 38,33408
v_mul_f32 v124, v123, v62
v_mfma_f32_16x16x32_bf16 v[70:73], v[100:103], v[0:3], v[86:89]
v_fma_f32 v26, v26, v122, v124
v_mul_f32 v125, v123, v63
mav 18,28,46
v_fma_f32 v27, v27, v122, v125
v_mul_f32 v124, v123, v64
mva 22,46,32
s_waitcnt lgkmcnt(2)
v_fma_f32 v28, v28, v122, v124
v_mul_f32 v125, v123, v65
mav 14,36,50
R 42,33472
v_fma_f32 v29, v29, v122, v125
mav 18,40,50
v_mul_f32 v124, v123, v66
v_fma_f32 v30, v30, v122, v124
mva 22,50,44
v_mul_f32 v125, v123, v67
v_fma_f32 v31, v31, v122, v125
mav 14,48,54
R 46,33536
v_mul_f32 v124, v123, v68
mav 18,52,54
v_fma_f32 v32, v32, v122, v124
v_mul_f32 v125, v123, v69
mva 22,54,56
s_waitcnt lgkmcnt(2)
v_fma_f32 v33, v33, v122, v125
v_add_u32 v78, 1, v78
mav 14,60,58
R 50,33600
mav 18,64,58
mva 22,58,68
mav 14,72,38
R 54,33664
mav 18,76,38
mva 22,38,80
s_waitcnt lgkmcnt(2)
mav 14,84,42
R 58,33728
mav 18,88,42
mva 22,42,92
mav 14,96,46
R 38,33792
mav 18,100,46
s_barrier
mva 22,46,104
s_waitcnt lgkmcnt(2)
ds_read_b128 v[104:107], v82 offset:4096
mav 14,108,50
ds_read_b64 v[110:111], v82 offset:5120
mav 18,112,50
R 42,33856
mva 22,50,116
mav 14,120,54
s_waitcnt vmcnt(1)
R 46,33920
mav 18,124,54
buffer_store_dword v78, off, s[36:39], s52 sc1
mva 22,54,128
s_waitcnt lgkmcnt(3)
v_mfma_f32_16x16x32_bf16 v[74:77], v[104:107], v[0:3], v[90:93]
v_cvt_pk_bf16_f32 v0, v26, v27
v_cvt_pk_bf16_f32 v1, v28, v29
mav 14,132,58
R 50,33984
v_cvt_pk_bf16_f32 v2, v30, v31
mav 18,136,58
v_cvt_pk_bf16_f32 v3, v32, v33
mva 22,58,140
v_max3_f32 v118, v70, v71, v74
v_max3_f32 v119, v72, v73, v76
mav 14,144,38
R 54,34048
v_max_f32 v118, v118, v75
mav 18,148,38
v_max_f32 v119, v119, v77
v_max_f32 v120, v118, v119
mva 22,38,152
s_waitcnt lgkmcnt(2)
v_max_f32 v121, v118, v119
mav 14,156,42
R 38,34112
mav 18,160,42
v_permlane32_swap_b32 v120, v121
mva 22,42,164
v_max_f32 v114, v120, v121
v_max_f32 v115, v120, v121
mav 14,168,46
R 42,34176
mav 18,172,46
v_permlane16_swap_b32 v114, v115
mva 22,46,176
s_waitcnt lgkmcnt(2)
v_max_f32 v120, v114, v115
v_mul_f32 v120, s65, v120
mav 14,180,50
R 46,34240
v_fma_f32 v118, v118, s65, -v120
mav 18,184,50
v_fma_f32 v119, v119, s65, -v120
mva 22,50,188
v_exp_f32 v118, v118
mav 14,192,54
R 50,34304
mav 18,196,54
v_exp_f32 v119, v119
mva 22,54,200
s_waitcnt lgkmcnt(2)
v_add_f32 v120, v118, v119
v_add_f32 v121, v118, v119
mav 14,204,38
R 54,34368
mav 18,208,38
v_permlane32_swap_b32 v120, v121
mva 22,38,212
v_add_f32 v114, v120, v121
v_add_f32 v115, v120, v121
mav 14,216,42
R 38,34432
mav 18,220,42
v_permlane16_swap_b32 v114, v115
mva 22,42,224
s_waitcnt lgkmcnt(2)
v_add_f32 v120, v114, v115
mav 14,228,46
R 42,34496
mav 18,232,46
v_rcp_f32 v120, v120
mva 22,46,236
v_mul_f32 v118, v118, v120
v_mul_f32 v119, v119, v120
mav 14,240,50
R 46,34560
v_cvt_pk_bf16_f32 v118, v118, v118
mav 18,244,50
v_cvt_pk_bf16_f32 v119, v119, v119
v_and_b32 v114, v118, v94
mva 22,50,248
s_waitcnt lgkmcnt(2)
v_and_b32 v115, v119, v95
v_and_b32 v116, v118, v96
mav 14,252,54
R 50,34624
v_and_b32 v117, v119, v97
mvv 18,128,54
mvv 22,54,132
v_mfma_f32_16x16x32_bf16 v[70:73], v[8:11], v[114:117], 0
v_mfma_f32_16x16x32_bf16 v[74:77], v[108:111], v[114:117], 0
mvv 14,136,38
R 54,34688
mvv 18,140,38
mvv 22,38,144
s_waitcnt lgkmcnt(2)
mvv 14,148,42
R 38,34752
v_cvt_pk_bf16_f32 v114, v70, v71
mvv 18,152,42
v_cvt_pk_bf16_f32 v115, v72, v73
v_cvt_pk_bf16_f32 v116, v74, v75
mvv 22,42,156
v_cvt_pk_bf16_f32 v117, v76, v77
mvv 14,160,46
R 42,34816
mvv 18,164,46
buffer_store_dwordx4 v[114:117], v82, s[36:39], s49 offen sc1
s_add_u32 s49, s49, 313344
mvv 22,46,168
s_waitcnt lgkmcnt(2)
mvv 14,172,50
R 46,34880
mvv 18,176,50
mvv 22,50,180
mvv 14,184,54
R 50,34944
mvv 18,188,54
mvv 22,54,192
s_waitcnt lgkmcnt(2)
mvv 14,196,38
R 54,35008
mvv 18,200,38
mvv 22,38,204
mvv 14,208,42
R 38,0
mvv 18,212,42
ds_read_b64 v[122:123], v83 offset:0
mvv 22,42,216
s_waitcnt lgkmcnt(3)
ds_read_b128 v[70:73], v84 offset:0
mvv 14,220,46
R 42,64
mvv 18,224,46
ds_read_b128 v[74:77], v84 offset:16
mvv 22,46,228
mvv 14,232,50
R 46,128
mvv 18,236,50
mvv 22,50,240
s_waitcnt lgkmcnt(3)
mvv 14,244,54
R 50,192
mvv 18,248,54
mvv 22,54,252
v_mfma_f32_16x16x32_bf16 v[26:29], a[0:3], v[38:41], 0
s_waitcnt lgkmcnt(1)
R 54,256
v_mul_f32 v71, v71, v99
v_mfma_f32_16x16x32_bf16 v[30:33], a[4:7], v[38:41], 0
v_mul_f32 v73, v73, v99
v_mul_f32 v75, v75, v99
v_mfma_f32_16x16x32_bf16 v[34:37], v[38:41], a[8:11], 0
v_fma_f32 v22, v22, v70, v71
v_fma_f32 v23, v23, v72, v73
mav 26,12,42
R 58,320
v_fma_f32 v24, v24, v74, v75
mav 30,16,42
v_mul_f32 v77, v77, v99
v_fma_f32 v25, v25, v76, v77
mva 34,42,20
v_cvt_pk_bf16_f32 v8, v22, v23
v_cvt_pk_bf16_f32 v9, v24, v25
mav 26,24,46
R 38,384
v_mul_f32 v124, v123, v62
v_mfma_f32_16x16x32_bf16 v[70:73], v[104:107], v[0:3], v[86:89]
v_fma_f32 v14, v14, v122, v124
v_mul_f32 v125, v123, v63
mav 30,28,46
v_fma_f32 v15, v15, v122, v125
v_mul_f32 v124, v123, v64
mva 34,46,32
s_waitcnt lgkmcnt(2)
v_fma_f32 v16, v16, v122, v124
v_mul_f32 v125, v123, v65
mav 26,36,50
R 42,448
v_fma_f32 v17, v17, v122, v125
mav 30,40,50
v_mul_f32 v124, v123, v66
v_fma_f32 v18, v18, v122, v124
mva 34,50,44
v_mul_f32 v125, v123, v67
v_fma_f32 v19, v19, v122, v125
mav 26,48,54
R 46,512
v_mul_f32 v124, v123, v68
mav 30,52,54
v_fma_f32 v20, v20, v122, v124
v_mul_f32 v125, v123, v69
mva 34,54,56
s_waitcnt lgkmcnt(2)
v_fma_f32 v21, v21, v122, v125
v_add_u32 v78, 1, v78
mav 26,60,58
R 50,576
mav 30,64,58
mva 34,58,68
mav 26,72,38
R 54,640
mav 30,76,38
mva 34,38,80
s_waitcnt lgkmcnt(2)
mav 26,84,42
R 58,704
mav 30,88,42
mva 34,42,92
mav 26,96,46
R 38,768
mav 30,100,46
s_barrier
mva 34,46,104
s_waitcnt lgkmcnt(2)
ds_read_b128 v[100:103], v82 offset:0
mav 26,108,50
ds_read_b64 v[108:109], v82 offset:1024
mav 30,112,50
R 42,832
mva 34,50,116
mav 26,120,54
s_waitcnt vmcnt(0)
R 46,896
mav 30,124,54
buffer_store_dword v78, off, s[36:39], s52 sc1
mva 34,54,128
s_waitcnt lgkmcnt(3)
v_mfma_f32_16x16x32_bf16 v[74:77], v[100:103], v[0:3], v[90:93]
v_cvt_pk_bf16_f32 v0, v14, v15
v_cvt_pk_bf16_f32 v1, v16, v17
mav 26,132,58
R 50,960
v_cvt_pk_bf16_f32 v2, v18, v19
mav 30,136,58
v_cvt_pk_bf16_f32 v3, v20, v21
mva 34,58,140
v_max3_f32 v118, v70, v71, v74
v_max3_f32 v119, v72, v73, v76
mav 26,144,38
R 54,1024
v_max_f32 v118, v118, v75
mav 30,148,38
v_max_f32 v119, v119, v77
v_max_f32 v120, v118, v119
mva 34,38,152
s_waitcnt lgkmcnt(2)
v_max_f32 v121, v118, v119
mav 26,156,42
R 38,1088
mav 30,160,42
v_permlane32_swap_b32 v120, v121
mva 34,42,164
v_max_f32 v114, v120, v121
v_max_f32 v115, v120, v121
mav 26,168,46
R 42,1152
mav 30,172,46
v_permlane16_swap_b32 v114, v115
mva 34,46,176
s_waitcnt lgkmcnt(2)
v_max_f32 v120, v114, v115
v_mul_f32 v120, s65, v120
)"
R"(
mav 26,180,50
R 46,1216
v_fma_f32 v118, v118, s65, -v120
mav 30,184,50
v_fma_f32 v119, v119, s65, -v120
mva 34,50,188
v_exp_f32 v118, v118
mav 26,192,54
R 50,1280
mav 30,196,54
v_exp_f32 v119, v119
mva 34,54,200
s_waitcnt lgkmcnt(2)
v_add_f32 v120, v118, v119
v_add_f32 v121, v118, v119
mav 26,204,38
R 54,1344
mav 30,208,38
v_permlane32_swap_b32 v120, v121
mva 34,38,212
v_add_f32 v114, v120, v121
v_add_f32 v115, v120, v121
mav 26,216,42
R 38,1408
mav 30,220,42
v_permlane16_swap_b32 v114, v115
mva 34,42,224
s_waitcnt lgkmcnt(2)
v_add_f32 v120, v114, v115
mav 26,228,46
R 42,1472
mav 30,232,46
v_rcp_f32 v120, v120
mva 34,46,236
v_mul_f32 v118, v118, v120
v_mul_f32 v119, v119, v120
mav 26,240,50
R 46,1536
v_cvt_pk_bf16_f32 v118, v118, v118
mav 30,244,50
v_cvt_pk_bf16_f32 v119, v119, v119
v_and_b32 v114, v118, v96
mva 34,50,248
s_waitcnt lgkmcnt(2)
v_and_b32 v115, v119, v97
v_and_b32 v116, v118, v94
mav 26,252,54
R 50,1600
v_and_b32 v117, v119, v95
mvv 30,128,54
mvv 34,54,132
v_mfma_f32_16x16x32_bf16 v[70:73], v[8:11], v[114:117], 0
v_mfma_f32_16x16x32_bf16 v[74:77], v[108:111], v[114:117], 0
mvv 26,136,38
R 54,1664
mvv 30,140,38
mvv 34,38,144
s_waitcnt lgkmcnt(2)
mvv 26,148,42
R 38,1728
v_cvt_pk_bf16_f32 v114, v70, v71
mvv 30,152,42
v_cvt_pk_bf16_f32 v115, v72, v73
v_cvt_pk_bf16_f32 v116, v74, v75
mvv 34,42,156
v_cvt_pk_bf16_f32 v117, v76, v77
mvv 26,160,46
R 42,1792
mvv 30,164,46
buffer_store_dwordx4 v[114:117], v82, s[36:39], s49 offen sc1
s_add_u32 s49, s49, 313344
mvv 34,46,168
s_waitcnt lgkmcnt(2)
mvv 26,172,50
R 46,1856
mvv 30,176,50
mvv 34,50,180
mvv 26,184,54
R 50,1920
mvv 30,188,54
s_waitcnt lgkmcnt(2)
mvv 34,54,192
s_mov_b32 s60, -13
.p2align 6
LQA1_2:
mvv 26,196,38
R 54,1984
v_xor_b32 v80, v81, v80
mvv 30,200,38
mvv 34,38,204
mvv 26,208,42
R 38,33024
mvv 30,212,42
ds_read_b64 v[122:123], v83 offset:128
mvv 34,42,216
s_waitcnt lgkmcnt(3)
ds_read_b128 v[70:73], v84 offset:128
mvv 26,220,46
R 42,33088
mvv 30,224,46
ds_read_b128 v[74:77], v84 offset:144
mvv 34,46,228
mvv 26,232,50
R 46,33152
mvv 30,236,50
mvv 34,50,240
s_waitcnt lgkmcnt(3)
mvv 26,244,54
R 50,33216
mvv 30,248,54
mvv 34,54,252
v_mfma_f32_16x16x32_bf16 v[14:17], a[0:3], v[38:41], 0
s_waitcnt lgkmcnt(1)
R 54,33280
v_mul_f32 v71, v71, v99
v_mfma_f32_16x16x32_bf16 v[18:21], a[4:7], v[38:41], 0
v_mul_f32 v73, v73, v99
v_mul_f32 v75, v75, v99
v_mfma_f32_16x16x32_bf16 v[22:25], v[38:41], a[8:11], 0
v_fma_f32 v34, v34, v70, v71
v_fma_f32 v35, v35, v72, v73
mav 14,12,42
R 58,33344
v_fma_f32 v36, v36, v74, v75
mav 18,16,42
v_mul_f32 v77, v77, v99
v_fma_f32 v37, v37, v76, v77
mva 22,42,20
v_cvt_pk_bf16_f32 v10, v34, v35
v_cvt_pk_bf16_f32 v11, v36, v37
mav 14,24,46
R 38,33408
v_mul_f32 v124, v123, v62
v_mfma_f32_16x16x32_bf16 v[70:73], v[100:103], v[0:3], v[86:89]
v_fma_f32 v26, v26, v122, v124
v_mul_f32 v125, v123, v63
mav 18,28,46
v_fma_f32 v27, v27, v122, v125
v_mul_f32 v124, v123, v64
mva 22,46,32
s_waitcnt lgkmcnt(2)
v_fma_f32 v28, v28, v122, v124
v_mul_f32 v125, v123, v65
mav 14,36,50
R 42,33472
v_fma_f32 v29, v29, v122, v125
mav 18,40,50
v_mul_f32 v124, v123, v66
v_fma_f32 v30, v30, v122, v124
mva 22,50,44
v_mul_f32 v125, v123, v67
v_fma_f32 v31, v31, v122, v125
mav 14,48,54
R 46,33536
v_mul_f32 v124, v123, v68
mav 18,52,54
v_fma_f32 v32, v32, v122, v124
v_mul_f32 v125, v123, v69
mva 22,54,56
s_waitcnt lgkmcnt(2)
v_fma_f32 v33, v33, v122, v125
v_add_u32 v78, 1, v78
mav 14,60,58
R 50,33600
mav 18,64,58
mva 22,58,68
mav 14,72,38
R 54,33664
mav 18,76,38
mva 22,38,80
s_waitcnt lgkmcnt(2)
mav 14,84,42
R 58,33728
mav 18,88,42
mva 22,42,92
mav 14,96,46
R 38,33792
mav 18,100,46
s_barrier
mva 22,46,104
s_waitcnt lgkmcnt(2)
ds_read_b128 v[104:107], v82 offset:4096
mav 14,108,50
ds_read_b64 v[110:111], v82 offset:5120
mav 18,112,50
R 42,33856
mva 22,50,116
mav 14,120,54
s_waitcnt vmcnt(0)
R 46,33920
mav 18,124,54
buffer_store_dword v78, off, s[36:39], s52 sc1
mva 22,54,128
s_waitcnt lgkmcnt(3)
v_mfma_f32_16x16x32_bf16 v[74:77], v[104:107], v[0:3], v[90:93]
v_cvt_pk_bf16_f32 v0, v26, v27
v_cvt_pk_bf16_f32 v1, v28, v29
mav 14,132,58
R 50,33984
v_cvt_pk_bf16_f32 v2, v30, v31
mav 18,136,58
v_cvt_pk_bf16_f32 v3, v32, v33
mva 22,58,140
v_max3_f32 v118, v70, v71, v74
v_max3_f32 v119, v72, v73, v76
mav 14,144,38
R 54,34048
v_max_f32 v118, v118, v75
mav 18,148,38
v_max_f32 v119, v119, v77
LsQ12:
s_cbranch_execz LsQ16
v_max_f32 v120, v118, v119
mva 22,38,152
s_waitcnt lgkmcnt(2)
v_max_f32 v121, v118, v119
mav 14,156,42
R 38,34112
mav 18,160,42
v_permlane32_swap_b32 v120, v121
mva 22,42,164
v_max_f32 v114, v120, v121
v_max_f32 v115, v120, v121
mav 14,168,46
R 42,34176
mav 18,172,46
v_permlane16_swap_b32 v114, v115
mva 22,46,176
s_waitcnt lgkmcnt(2)
v_max_f32 v120, v114, v115
v_mul_f32 v120, s65, v120
mav 14,180,50
R 46,34240
v_fma_f32 v118, v118, s65, -v120
mav 18,184,50
v_fma_f32 v119, v119, s65, -v120
mva 22,50,188
v_exp_f32 v118, v118
mav 14,192,54
R 50,34304
mav 18,196,54
v_exp_f32 v119, v119
mva 22,54,200
s_waitcnt lgkmcnt(2)
v_add_f32 v120, v118, v119
v_add_f32 v121, v118, v119
mav 14,204,38
R 54,34368
mav 18,208,38
v_permlane32_swap_b32 v120, v121
mva 22,38,212
v_add_f32 v114, v120, v121
v_add_f32 v115, v120, v121
mav 14,216,42
R 38,34432
mav 18,220,42
v_permlane16_swap_b32 v114, v115
mva 22,42,224
s_waitcnt lgkmcnt(2)
v_add_f32 v120, v114, v115
mav 14,228,46
R 42,34496
mav 18,232,46
v_rcp_f32 v120, v120
mva 22,46,236
v_mul_f32 v118, v118, v120
v_mul_f32 v119, v119, v120
mav 14,240,50
R 46,34560
v_cvt_pk_bf16_f32 v118, v118, v118
mav 18,244,50
v_cvt_pk_bf16_f32 v119, v119, v119
v_and_b32 v114, v118, v94
mva 22,50,248
s_waitcnt lgkmcnt(2)
v_and_b32 v115, v119, v95
v_and_b32 v116, v118, v96
mav 14,252,54
R 50,34624
v_and_b32 v117, v119, v97
mvv 18,128,54
mvv 22,54,132
v_mfma_f32_16x16x32_bf16 v[70:73], v[8:11], v[114:117], 0
v_mfma_f32_16x16x32_bf16 v[74:77], v[108:111], v[114:117], 0
mvv 14,136,38
R 54,34688
mvv 18,140,38
mvv 22,38,144
s_waitcnt lgkmcnt(2)
mvv 14,148,42
R 38,34752
v_cvt_pk_bf16_f32 v114, v70, v71
mvv 18,152,42
v_cvt_pk_bf16_f32 v115, v72, v73
v_cvt_pk_bf16_f32 v116, v74, v75
mvv 22,42,156
v_cvt_pk_bf16_f32 v117, v76, v77
mvv 14,160,46
R 42,34816
mvv 18,164,46
buffer_store_dwordx4 v[114:117], v82, s[36:39], s49 offen sc1
s_add_u32 s49, s49, 313344
mvv 22,46,168
s_waitcnt lgkmcnt(2)
mvv 14,172,50
R 46,34880
mvv 18,176,50
mvv 22,50,180
mvv 14,184,54
R 50,34944
mvv 18,188,54
mvv 22,54,192
s_waitcnt lgkmcnt(2)
mvv 14,196,38
R 54,35008
mvv 18,200,38
mvv 22,38,204
mvv 14,208,42
R 38,0
mvv 18,212,42
ds_read_b64 v[122:123], v83 offset:0
mvv 22,42,216
s_waitcnt lgkmcnt(3)
ds_read_b128 v[70:73], v84 offset:0
mvv 14,220,46
R 42,64
mvv 18,224,46
ds_read_b128 v[74:77], v84 offset:16
mvv 22,46,228
mvv 14,232,50
R 46,128
mvv 18,236,50
mvv 22,50,240
s_waitcnt lgkmcnt(3)
mvv 14,244,54
R 50,192
mvv 18,248,54
mvv 22,54,252
v_mfma_f32_16x16x32_bf16 v[26:29], a[0:3], v[38:41], 0
s_waitcnt lgkmcnt(1)
R 54,256
v_mul_f32 v71, v71, v99
v_mfma_f32_16x16x32_bf16 v[30:33], a[4:7], v[38:41], 0
v_mul_f32 v73, v73, v99
v_mul_f32 v75, v75, v99
v_mfma_f32_16x16x32_bf16 v[34:37], v[38:41], a[8:11], 0
v_fma_f32 v22, v22, v70, v71
v_fma_f32 v23, v23, v72, v73
mav 26,12,42
R 58,320
v_fma_f32 v24, v24, v74, v75
mav 30,16,42
v_mul_f32 v77, v77, v99
v_fma_f32 v25, v25, v76, v77
mva 34,42,20
v_cvt_pk_bf16_f32 v8, v22, v23
v_cvt_pk_bf16_f32 v9, v24, v25
mav 26,24,46
R 38,384
v_mul_f32 v124, v123, v62
v_mfma_f32_16x16x32_bf16 v[70:73], v[104:107], v[0:3], v[86:89]
v_fma_f32 v14, v14, v122, v124
v_mul_f32 v125, v123, v63
mav 30,28,46
v_fma_f32 v15, v15, v122, v125
v_mul_f32 v124, v123, v64
mva 34,46,32
s_waitcnt lgkmcnt(2)
v_fma_f32 v16, v16, v122, v124
v_mul_f32 v125, v123, v65
mav 26,36,50
R 42,448
v_fma_f32 v17, v17, v122, v125
mav 30,40,50
v_mul_f32 v124, v123, v66
v_fma_f32 v18, v18, v122, v124
mva 34,50,44
v_mul_f32 v125, v123, v67
v_fma_f32 v19, v19, v122, v125
mav 26,48,54
R 46,512
v_mul_f32 v124, v123, v68
mav 30,52,54
v_fma_f32 v20, v20, v122, v124
v_mul_f32 v125, v123, v69
mva 34,54,56
s_waitcnt lgkmcnt(2)
v_fma_f32 v21, v21, v122, v125
v_add_u32 v78, 1, v78
mav 26,60,58
R 50,576
mav 30,64,58
mva 34,58,68
mav 26,72,38
R 54,640
mav 30,76,38
mva 34,38,80
s_waitcnt lgkmcnt(2)
mav 26,84,42
R 58,704
mav 30,88,42
mva 34,42,92
mav 26,96,46
R 38,768
mav 30,100,46
s_barrier
mva 34,46,104
s_waitcnt lgkmcnt(2)
ds_read_b128 v[100:103], v82 offset:0
mav 26,108,50
ds_read_b64 v[108:109], v82 offset:1024
mav 30,112,50
R 42,832
mva 34,50,116
mav 26,120,54
s_waitcnt vmcnt(0)
R 46,896
mav 30,124,54
buffer_store_dword v78, off, s[36:39], s52 sc1
mva 34,54,128
s_waitcnt lgkmcnt(3)
v_mfma_f32_16x16x32_bf16 v[74:77], v[100:103], v[0:3], v[90:93]
v_cvt_pk_bf16_f32 v0, v14, v15
v_cvt_pk_bf16_f32 v1, v16, v17
mav 26,132,58
R 50,960
v_cvt_pk_bf16_f32 v2, v18, v19
mav 30,136,58
v_cvt_pk_bf16_f32 v3, v20, v21
mva 34,58,140
v_max3_f32 v118, v70, v71, v74
v_max3_f32 v119, v72, v73, v76
mav 26,144,38
R 54,1024
v_max_f32 v118, v118, v75
mav 30,148,38
v_max_f32 v119, v119, v77
v_max_f32 v120, v118, v119
mva 34,38,152
s_waitcnt lgkmcnt(2)
v_max_f32 v121, v118, v119
mav 26,156,42
R 38,1088
mav 30,160,42
v_permlane32_swap_b32 v120, v121
mva 34,42,164
v_max_f32 v114, v120, v121
v_max_f32 v115, v120, v121
mav 26,168,46
R 42,1152
mav 30,172,46
v_permlane16_swap_b32 v114, v115
mva 34,46,176
s_waitcnt lgkmcnt(2)
v_max_f32 v120, v114, v115
v_mul_f32 v120, s65, v120
mav 26,180,50
R 46,1216
v_fma_f32 v118, v118, s65, -v120
mav 30,184,50
v_fma_f32 v119, v119, s65, -v120
mva 34,50,188
v_exp_f32 v118, v118
mav 26,192,54
R 50,1280
mav 30,196,54
v_exp_f32 v119, v119
mva 34,54,200
s_waitcnt lgkmcnt(2)
v_add_f32 v120, v118, v119
v_add_f32 v121, v118, v119
mav 26,204,38
R 54,1344
mav 30,208,38
v_permlane32_swap_b32 v120, v121
mva 34,38,212
v_add_f32 v114, v120, v121
v_add_f32 v115, v120, v121
mav 26,216,42
R 38,1408
mav 30,220,42
v_permlane16_swap_b32 v114, v115
mva 34,42,224
s_waitcnt lgkmcnt(2)
v_add_f32 v120, v114, v115
mav 26,228,46
R 42,1472
mav 30,232,46
v_rcp_f32 v120, v120
mva 34,46,236
v_mul_f32 v118, v118, v120
v_mul_f32 v119, v119, v120
mav 26,240,50
R 46,1536
v_cvt_pk_bf16_f32 v118, v118, v118
mav 30,244,50
v_cvt_pk_bf16_f32 v119, v119, v119
v_and_b32 v114, v118, v96
mva 34,50,248
s_waitcnt lgkmcnt(2)
v_and_b32 v115, v119, v97
v_and_b32 v116, v118, v94
mav 26,252,54
R 50,1600
v_and_b32 v117, v119, v95
mvv 30,128,54
mvv 34,54,132
v_mfma_f32_16x16x32_bf16 v[70:73], v[8:11], v[114:117], 0
v_mfma_f32_16x16x32_bf16 v[74:77], v[108:111], v[114:117], 0
mvv 26,136,38
R 54,1664
mvv 30,140,38
mvv 34,38,144
s_waitcnt lgkmcnt(2)
mvv 26,148,42
R 38,1728
v_cvt_pk_bf16_f32 v114, v70, v71
mvv 30,152,42
v_cvt_pk_bf16_f32 v115, v72, v73
v_cvt_pk_bf16_f32 v116, v74, v75
mvv 34,42,156
v_cvt_pk_bf16_f32 v117, v76, v77
mvv 26,160,46
R 42,1792
mvv 30,164,46
buffer_store_dwordx4 v[114:117], v82, s[36:39], s49 offen sc1
s_add_u32 s49, s49, 313344
mvv 34,46,168
s_waitcnt lgkmcnt(2)
mvv 26,172,50
R 46,1856
mvv 30,176,50
mvv 34,50,180
mvv 26,184,54
R 50,1920
mvv 30,188,54
s_waitcnt lgkmcnt(2)
s_add_u32 s60, s60, 1
mvv 34,54,192
s_cbranch_scc0 LQA1_2
mvv 26,196,38
R 54,1984
v_xor_b32 v80, v81, v80
mvv 30,200,38
mvv 34,38,204
mvv 26,208,42
R 38,33024
mvv 30,212,42
ds_read_b64 v[122:123], v83 offset:128
mvv 34,42,216
s_waitcnt lgkmcnt(3)
ds_read_b128 v[70:73], v84 offset:128
mvv 26,220,46
R 42,33088
mvv 30,224,46
ds_read_b128 v[74:77], v84 offset:144
mvv 34,46,228
mvv 26,232,50
R 46,33152
mvv 30,236,50
mvv 34,50,240
s_waitcnt lgkmcnt(3)
mvv 26,244,54
R 50,33216
mvv 30,248,54
mvv 34,54,252
v_mfma_f32_16x16x32_bf16 v[14:17], a[0:3], v[38:41], 0
s_waitcnt lgkmcnt(1)
R 54,33280
v_mul_f32 v71, v71, v99
v_mfma_f32_16x16x32_bf16 v[18:21], a[4:7], v[38:41], 0
v_mul_f32 v73, v73, v99
v_mul_f32 v75, v75, v99
v_mfma_f32_16x16x32_bf16 v[22:25], v[38:41], a[8:11], 0
v_fma_f32 v34, v34, v70, v71
v_fma_f32 v35, v35, v72, v73
mav 14,12,42
R 58,33344
v_fma_f32 v36, v36, v74, v75
mav 18,16,42
v_mul_f32 v77, v77, v99
v_fma_f32 v37, v37, v76, v77
mva 22,42,20
v_cvt_pk_bf16_f32 v10, v34, v35
v_cvt_pk_bf16_f32 v11, v36, v37
mav 14,24,46
R 38,33408
v_mul_f32 v124, v123, v62
v_mfma_f32_16x16x32_bf16 v[70:73], v[100:103], v[0:3], v[86:89]
v_fma_f32 v26, v26, v122, v124
v_mul_f32 v125, v123, v63
mav 18,28,46
v_fma_f32 v27, v27, v122, v125
v_mul_f32 v124, v123, v64
mva 22,46,32
s_waitcnt lgkmcnt(2)
v_fma_f32 v28, v28, v122, v124
v_mul_f32 v125, v123, v65
mav 14,36,50
R 42,33472
v_fma_f32 v29, v29, v122, v125
mav 18,40,50
v_mul_f32 v124, v123, v66
v_fma_f32 v30, v30, v122, v124
mva 22,50,44
v_mul_f32 v125, v123, v67
v_fma_f32 v31, v31, v122, v125
mav 14,48,54
R 46,33536
v_mul_f32 v124, v123, v68
mav 18,52,54
v_fma_f32 v32, v32, v122, v124
v_mul_f32 v125, v123, v69
mva 22,54,56
s_waitcnt lgkmcnt(2)
v_fma_f32 v33, v33, v122, v125
v_add_u32 v78, 1, v78
mav 14,60,58
R 50,33600
mav 18,64,58
mva 22,58,68
mav 14,72,38
R 54,33664
mav 18,76,38
mva 22,38,80
s_waitcnt lgkmcnt(2)
mav 14,84,42
R 58,33728
mav 18,88,42
mva 22,42,92
mav 14,96,46
R 38,33792
mav 18,100,46
s_barrier
mva 22,46,104
s_waitcnt lgkmcnt(2)
ds_read_b128 v[104:107], v82 offset:4096
mav 14,108,50
ds_read_b64 v[110:111], v82 offset:5120
mav 18,112,50
R 42,33856
mva 22,50,116
mav 14,120,54
s_waitcnt vmcnt(0)
R 46,33920
mav 18,124,54
buffer_store_dword v78, off, s[36:39], s52 sc1
mva 22,54,128
s_waitcnt lgkmcnt(3)
v_mfma_f32_16x16x32_bf16 v[74:77], v[104:107], v[0:3], v[90:93]
v_cvt_pk_bf16_f32 v0, v26, v27
v_cvt_pk_bf16_f32 v1, v28, v29
mav 14,132,58
R 50,33984
v_cvt_pk_bf16_f32 v2, v30, v31
mav 18,136,58
v_cvt_pk_bf16_f32 v3, v32, v33
mva 22,58,140
v_max3_f32 v118, v70, v71, v74
v_max3_f32 v119, v72, v73, v76
mav 14,144,38
R 54,34048
v_max_f32 v118, v118, v75
mav 18,148,38
v_max_f32 v119, v119, v77
v_max_f32 v120, v118, v119
mva 22,38,152
s_waitcnt lgkmcnt(2)
v_max_f32 v121, v118, v119
mav 14,156,42
R 38,34112
mav 18,160,42
v_permlane32_swap_b32 v120, v121
mva 22,42,164
v_max_f32 v114, v120, v121
v_max_f32 v115, v120, v121
mav 14,168,46
R 42,34176
mav 18,172,46
v_permlane16_swap_b32 v114, v115
mva 22,46,176
s_waitcnt lgkmcnt(2)
v_max_f32 v120, v114, v115
v_mul_f32 v120, s65, v120
mav 14,180,50
R 46,34240
v_fma_f32 v118, v118, s65, -v120
mav 18,184,50
v_fma_f32 v119, v119, s65, -v120
mva 22,50,188
v_exp_f32 v118, v118
mav 14,192,54
R 50,34304
mav 18,196,54
v_exp_f32 v119, v119
mva 22,54,200
s_waitcnt lgkmcnt(2)
v_add_f32 v120, v118, v119
v_add_f32 v121, v118, v119
mav 14,204,38
R 54,34368
mav 18,208,38
v_permlane32_swap_b32 v120, v121
mva 22,38,212
v_add_f32 v114, v120, v121
v_add_f32 v115, v120, v121
mav 14,216,42
R 38,34432
mav 18,220,42
v_permlane16_swap_b32 v114, v115
mva 22,42,224
s_waitcnt lgkmcnt(2)
v_add_f32 v120, v114, v115
mav 14,228,46
R 42,34496
mav 18,232,46
v_rcp_f32 v120, v120
mva 22,46,236
v_mul_f32 v118, v118, v120
v_mul_f32 v119, v119, v120
mav 14,240,50
R 46,34560
v_cvt_pk_bf16_f32 v118, v118, v118
mav 18,244,50
v_cvt_pk_bf16_f32 v119, v119, v119
v_and_b32 v114, v118, v94
mva 22,50,248
s_waitcnt lgkmcnt(2)
v_and_b32 v115, v119, v95
v_and_b32 v116, v118, v96
mav 14,252,54
R 50,34624
v_and_b32 v117, v119, v97
mvv 18,128,54
mvv 22,54,132
v_mfma_f32_16x16x32_bf16 v[70:73], v[8:11], v[114:117], 0
v_mfma_f32_16x16x32_bf16 v[74:77], v[108:111], v[114:117], 0
mvv 14,136,38
R 54,34688
mvv 18,140,38
mvv 22,38,144
s_waitcnt lgkmcnt(2)
mvv 14,148,42
R 38,34752
v_cvt_pk_bf16_f32 v114, v70, v71
mvv 18,152,42
v_cvt_pk_bf16_f32 v115, v72, v73
v_cvt_pk_bf16_f32 v116, v74, v75
mvv 22,42,156
v_cvt_pk_bf16_f32 v117, v76, v77
mvv 14,160,46
R 42,34816
mvv 18,164,46
buffer_store_dwordx4 v[114:117], v82, s[36:39], s49 offen sc1
s_add_u32 s49, s49, 313344
mvv 22,46,168
s_waitcnt lgkmcnt(2)
mvv 14,172,50
R 46,34880
mvv 18,176,50
mvv 22,50,180
mvv 14,184,54
R 50,34944
mvv 18,188,54
mvv 22,54,192
s_waitcnt lgkmcnt(2)
mvv 14,196,38
R 54,35008
mvv 18,200,38
mvv 22,38,204
mvv 14,208,42
R 38,0
mvv 18,212,42
ds_read_b64 v[122:123], v83 offset:0
mvv 22,42,216
s_waitcnt lgkmcnt(3)
ds_read_b128 v[70:73], v84 offset:0
mvv 14,220,46
R 42,64
mvv 18,224,46
ds_read_b128 v[74:77], v84 offset:16
mvv 22,46,228
mvv 14,232,50
R 46,128
mvv 18,236,50
mvv 22,50,240
s_waitcnt lgkmcnt(3)
mvv 14,244,54
R 50,192
mvv 18,248,54
mvv 22,54,252
v_mfma_f32_16x16x32_bf16 v[26:29], a[0:3], v[38:41], 0
s_waitcnt lgkmcnt(1)
R 54,256
v_mul_f32 v71, v71, v99
v_mfma_f32_16x16x32_bf16 v[30:33], a[4:7], v[38:41], 0
v_mul_f32 v73, v73, v99
v_mul_f32 v75, v75, v99
v_mfma_f32_16x16x32_bf16 v[34:37], v[38:41], a[8:11], 0
v_fma_f32 v22, v22, v70, v71
v_fma_f32 v23, v23, v72, v73
LsQ13:
s_cbranch_execz LsQ17
mav 26,12,42
R 58,320
v_fma_f32 v24, v24, v74, v75
mav 30,16,42
v_mul_f32 v77, v77, v99
v_fma_f32 v25, v25, v76, v77
mva 34,42,20
v_cvt_pk_bf16_f32 v8, v22, v23
v_cvt_pk_bf16_f32 v9, v24, v25
mav 26,24,46
R 38,384
v_mul_f32 v124, v123, v62
v_mfma_f32_16x16x32_bf16 v[70:73], v[104:107], v[0:3], v[86:89]
v_fma_f32 v14, v14, v122, v124
v_mul_f32 v125, v123, v63
mav 30,28,46
v_fma_f32 v15, v15, v122, v125
v_mul_f32 v124, v123, v64
mva 34,46,32
s_waitcnt lgkmcnt(2)
v_fma_f32 v16, v16, v122, v124
v_mul_f32 v125, v123, v65
mav 26,36,50
R 42,448
v_fma_f32 v17, v17, v122, v125
mav 30,40,50
v_mul_f32 v124, v123, v66
v_fma_f32 v18, v18, v122, v124
mva 34,50,44
buffer_load_dwordx4 v[4:7], v82, s[36:39], s55 offen offset:2048 sc1
mav 26,48,54
R 46,512
v_mul_f32 v125, v123, v67
mav 30,52,54
buffer_load_dwordx2 v[12:13], v98, s[36:39], s55 offen offset:3072 sc1
mva 34,54,56
s_waitcnt lgkmcnt(2)
v_fma_f32 v19, v19, v122, v125
v_mul_f32 v124, v123, v68
mav 26,60,58
buffer_load_dwordx2 v[112:113], v98, s[36:39], s55 offen offset:3584 sc1
mav 30,64,58
R 50,576
v_fma_f32 v20, v20, v122, v124
mva 34,58,68
v_mul_f32 v125, v123, v69
v_fma_f32 v21, v21, v122, v125
mav 26,72,38
R 54,640
v_add_u32 v78, 1, v78
mav 30,76,38
mva 34,38,80
s_waitcnt lgkmcnt(2)
mav 26,84,42
R 58,704
mav 30,88,42
mva 34,42,92
mav 26,96,46
R 38,768
mav 30,100,46
s_barrier
mva 34,46,104
s_waitcnt lgkmcnt(2)
ds_read_b128 v[100:103], v82 offset:0
mav 26,108,50
ds_read_b64 v[108:109], v82 offset:1024
mav 30,112,50
R 42,832
mva 34,50,116
mav 26,120,54
s_waitcnt vmcnt(3)
R 46,896
mav 30,124,54
buffer_store_dword v78, off, s[36:39], s52 sc1
mva 34,54,128
s_waitcnt lgkmcnt(3)
v_mfma_f32_16x16x32_bf16 v[74:77], v[100:103], v[0:3], v[90:93]
v_cvt_pk_bf16_f32 v0, v14, v15
v_cvt_pk_bf16_f32 v1, v16, v17
mav 26,132,58
R 50,960
v_cvt_pk_bf16_f32 v2, v18, v19
mav 30,136,58
v_cvt_pk_bf16_f32 v3, v20, v21
mva 34,58,140
v_max3_f32 v118, v70, v71, v74
v_max3_f32 v119, v72, v73, v76
mav 26,144,38
R 54,1024
v_max_f32 v118, v118, v75
mav 30,148,38
v_max_f32 v119, v119, v77
v_max_f32 v120, v118, v119
mva 34,38,152
s_waitcnt lgkmcnt(2)
v_max_f32 v121, v118, v119
mav 26,156,42
R 38,1088
mav 30,160,42
v_permlane32_swap_b32 v120, v121
mva 34,42,164
v_max_f32 v114, v120, v121
v_max_f32 v115, v120, v121
mav 26,168,46
R 42,1152
mav 30,172,46
v_permlane16_swap_b32 v114, v115
mva 34,46,176
s_waitcnt lgkmcnt(2)
v_max_f32 v120, v114, v115
v_mul_f32 v120, s65, v120
mav 26,180,50
R 46,1216
v_fma_f32 v118, v118, s65, -v120
mav 30,184,50
v_fma_f32 v119, v119, s65, -v120
mva 34,50,188
v_exp_f32 v118, v118
mav 26,192,54
R 50,1280
mav 30,196,54
v_exp_f32 v119, v119
mva 34,54,200
s_waitcnt lgkmcnt(2)
v_add_f32 v120, v118, v119
v_add_f32 v121, v118, v119
mav 26,204,38
R 54,1344
mav 30,208,38
v_permlane32_swap_b32 v120, v121
mva 34,38,212
v_add_f32 v114, v120, v121
v_add_f32 v115, v120, v121
mav 26,216,42
R 38,1408
mav 30,220,42
v_permlane16_swap_b32 v114, v115
mva 34,42,224
s_waitcnt lgkmcnt(2)
v_add_f32 v120, v114, v115
mav 26,228,46
R 42,1472
mav 30,232,46
v_rcp_f32 v120, v120
mva 34,46,236
v_mul_f32 v118, v118, v120
v_mul_f32 v119, v119, v120
mav 26,240,50
R 46,1536
v_cvt_pk_bf16_f32 v118, v118, v118
mav 30,244,50
v_cvt_pk_bf16_f32 v119, v119, v119
v_and_b32 v114, v118, v96
mva 34,50,248
s_waitcnt lgkmcnt(2)
v_and_b32 v115, v119, v97
v_and_b32 v116, v118, v94
mav 26,252,54
R 50,1600
v_and_b32 v117, v119, v95
mvv 30,128,54
mvv 34,54,132
v_mfma_f32_16x16x32_bf16 v[70:73], v[8:11], v[114:117], 0
v_mfma_f32_16x16x32_bf16 v[74:77], v[108:111], v[114:117], 0
mvv 26,136,38
R 54,1664
mvv 30,140,38
mvv 34,38,144
s_waitcnt lgkmcnt(2)
mvv 26,148,42
R 38,1728
v_cvt_pk_bf16_f32 v114, v70, v71
mvv 30,152,42
v_cvt_pk_bf16_f32 v115, v72, v73
v_cvt_pk_bf16_f32 v116, v74, v75
mvv 34,42,156
v_cvt_pk_bf16_f32 v117, v76, v77
mvv 26,160,46
R 42,1792
mvv 30,164,46
buffer_store_dwordx4 v[114:117], v82, s[36:39], s49 offen sc1
s_add_u32 s49, s49, 313344
mvv 34,46,168
s_waitcnt lgkmcnt(2)
mvv 26,172,50
R 46,1856
mvv 30,176,50
mvv 34,50,180
mvv 26,184,54
R 50,1920
mvv 30,188,54
s_waitcnt lgkmcnt(2)
mvv 34,54,192
mvv 26,196,38
R 54,1984
mvv 30,200,38
mvv 34,38,204
mvv 26,208,42
mvv 30,212,42
ds_read_b64 v[122:123], v83 offset:128
mvv 34,42,216
s_waitcnt lgkmcnt(2)
ds_read_b128 v[70:73], v84 offset:128
mvv 26,220,46
ds_read_b128 v[74:77], v84 offset:144
mvv 30,224,46
mvv 34,46,228
mvv 26,232,50
mvv 30,236,50
mvv 34,50,240
s_waitcnt lgkmcnt(1)
mvv 26,244,54
mvv 30,248,54
mvv 34,54,252
v_mul_f32 v71, v71, v99
s_nop 6
v_fma_f32 v34, v34, v70, v71
v_mul_f32 v73, v73, v99
v_fma_f32 v35, v35, v72, v73
s_waitcnt lgkmcnt(0)
v_mul_f32 v75, v75, v99
v_fma_f32 v36, v36, v74, v75
v_mul_f32 v77, v77, v99
v_fma_f32 v37, v37, v76, v77
v_cvt_pk_bf16_f32 v10, v34, v35
v_cvt_pk_bf16_f32 v11, v36, v37
s_nop 1
v_mfma_f32_16x16x32_bf16 v[70:73], v[100:103], v[0:3], v[86:89]
v_mul_f32 v124, v123, v62
v_fma_f32 v26, v26, v122, v124
v_mul_f32 v125, v123, v63
v_fma_f32 v27, v27, v122, v125
v_mul_f32 v124, v123, v64
v_fma_f32 v28, v28, v122, v124
v_mul_f32 v125, v123, v65
v_fma_f32 v29, v29, v122, v125
v_mul_f32 v124, v123, v66
v_fma_f32 v30, v30, v122, v124
v_mul_f32 v125, v123, v67
v_fma_f32 v31, v31, v122, v125
v_mul_f32 v124, v123, v68
v_fma_f32 v32, v32, v122, v124
v_mul_f32 v125, v123, v69
v_fma_f32 v33, v33, v122, v125
s_barrier
ds_read_b128 v[104:107], v82 offset:4096
ds_read_b64 v[110:111], v82 offset:5120
s_waitcnt lgkmcnt(0)
v_mfma_f32_16x16x32_bf16 v[74:77], v[104:107], v[0:3], v[90:93]
s_nop 7
v_max3_f32 v118, v70, v71, v74
v_max3_f32 v119, v72, v73, v76
v_max_f32 v118, v118, v75
v_max_f32 v119, v119, v77
v_max_f32 v120, v118, v119
v_max_f32 v121, v118, v119
s_nop 1
v_permlane32_swap_b32 v120, v121
v_max_f32 v114, v120, v121
v_max_f32 v115, v120, v121
s_nop 1
v_permlane16_swap_b32 v114, v115
v_max_f32 v120, v114, v115
v_mul_f32 v120, s65, v120
v_fma_f32 v118, v118, s65, -v120
v_fma_f32 v119, v119, s65, -v120
v_exp_f32 v118, v118
v_exp_f32 v119, v119
s_nop 0
v_add_f32 v120, v118, v119
v_add_f32 v121, v118, v119
s_nop 1
v_permlane32_swap_b32 v120, v121
v_add_f32 v114, v120, v121
v_add_f32 v115, v120, v121
s_nop 1
v_permlane16_swap_b32 v114, v115
v_add_f32 v120, v114, v115
v_rcp_f32 v120, v120
s_nop 0
v_mul_f32 v118, v118, v120
v_mul_f32 v119, v119, v120
v_cvt_pk_bf16_f32 v118, v118, v118
v_cvt_pk_bf16_f32 v119, v119, v119
v_and_b32 v114, v118, v94
v_and_b32 v115, v119, v95
v_and_b32 v116, v118, v96
v_and_b32 v117, v119, v97
s_nop 1
v_mfma_f32_16x16x32_bf16 v[70:73], v[8:11], v[114:117], 0
v_mfma_f32_16x16x32_bf16 v[74:77], v[108:111], v[114:117], 0
s_nop 6
v_cvt_pk_bf16_f32 v114, v70, v71
v_cvt_pk_bf16_f32 v115, v72, v73
v_cvt_pk_bf16_f32 v116, v74, v75
v_cvt_pk_bf16_f32 v117, v76, v77
buffer_store_dwordx4 v[114:117], v82, s[36:39], s49 offen sc1
s_add_u32 s49, s49, 313344
v_cvt_pk_bf16_f32 v0, v26, v27
v_cvt_pk_bf16_f32 v1, v28, v29
v_cvt_pk_bf16_f32 v2, v30, v31
v_cvt_pk_bf16_f32 v3, v32, v33
v_add_u32 v78, 1, v78
s_waitcnt vmcnt(1)
buffer_store_dword v78, off, s[36:39], s52 sc1
v_mfma_f32_16x16x32_bf16 v[70:73], v[104:107], v[0:3], v[86:89]
v_min_f32 v74, s56, v90
v_min_f32 v75, s56, v91
v_min_f32 v76, s56, v92
v_min_f32 v77, s56, v93
s_nop 1
mvv 74,4,0
s_nop 7
v_max3_f32 v118, v70, v71, v74
v_max3_f32 v119, v72, v73, v76
v_max_f32 v118, v118, v75
v_max_f32 v119, v119, v77
v_max_f32 v120, v118, v119
v_max_f32 v121, v118, v119
s_nop 1
v_permlane32_swap_b32 v120, v121
v_max_f32 v114, v120, v121
v_max_f32 v115, v120, v121
s_nop 1
v_permlane16_swap_b32 v114, v115
v_max_f32 v120, v114, v115
v_mul_f32 v120, s65, v120
v_fma_f32 v118, v118, s65, -v120
v_fma_f32 v119, v119, s65, -v120
v_exp_f32 v118, v118
v_exp_f32 v119, v119
s_nop 0
v_add_f32 v120, v118, v119
v_add_f32 v121, v118, v119
s_nop 1
v_permlane32_swap_b32 v120, v121
v_add_f32 v114, v120, v121
v_add_f32 v115, v120, v121
s_nop 1
v_permlane16_swap_b32 v114, v115
v_add_f32 v120, v114, v115
v_rcp_f32 v120, v120
s_nop 0
v_mul_f32 v118, v118, v120
v_mul_f32 v119, v119, v120
v_cvt_pk_bf16_f32 v118, v118, v118
v_cvt_pk_bf16_f32 v119, v119, v119
v_and_b32 v114, v118, v94
v_and_b32 v115, v119, v95
v_and_b32 v116, v118, v96
v_and_b32 v117, v119, v97
s_nop 1
v_mfma_f32_16x16x32_bf16 v[70:73], v[10:13], v[114:117], 0
v_mfma_f32_16x16x32_bf16 v[74:77], v[110:113], v[114:117], 0
s_nop 6
v_cvt_pk_bf16_f32 v114, v70, v71
v_cvt_pk_bf16_f32 v115, v72, v73
v_cvt_pk_bf16_f32 v116, v74, v75
v_cvt_pk_bf16_f32 v117, v76, v77
buffer_store_dwordx4 v[114:117], v82, s[36:39], s49 offen sc1
v_add_u32 v78, 1, v78
s_waitcnt vmcnt(2)
buffer_store_dword v78, off, s[36:39], s52 sc1
v_add_u32 v78, 37, v78
s_waitcnt vmcnt(1)
buffer_store_dword v78, off, s[36:39], s52 sc1
s_endpgm
Lr1:
s_cmp_eq_u32 s60, 1
s_cbranch_scc0 Lr2
s_lshr_b32 s66, s53, 10
s_mul_i32 s66, s66, 8208
s_mov_b32 s67, 0x8000
s_mov_b64 exec, 0
s_cmp_eq_u32 s53, 1024
s_cbranch_scc1 LsQ1
s_branch LsQ3
LwQB:
s_mov_b64 exec, -1
E 0,86016
E 1,90112
E 2,94208
s_add_u32 s51, s51, 196608
E 0,98304
E 1,102400
E 2,106496
s_add_u32 s51, s51, 196608
E 0,61440
E 1,65536
s_add_u32 m0, s66, 2816
s_nop 0
buffer_load_dwordx4 v3, s[44:47], s67 offen offset:256 lds
E 2,69632
s_add_u32 s51, s51, 196608
E 0,73728
s_add_u32 m0, s66, 6912
s_nop 0
buffer_load_dwordx4 v3, s[44:47], 0 offen offset:256 lds
E 1,77824
E 2,81920
s_add_u32 s51, s51, 196608
v_lshrrev_b32 v12, 2, v7
v_and_b32 v12, 0xc0, v12
v_xor_b32 v13, 0, v12
v_add_u32 v8, v7, v13
v_xor_b32 v13, 64, v12
v_add_u32 v9, v7, v13
v_xor_b32 v13, 128, v12
v_add_u32 v10, v7, v13
v_xor_b32 v13, 192, v12
v_add_u32 v11, v7, v13
v_mov_b32 v112, s64
v_mov_b32 v113, s64
v_mov_b32 v114, s64
v_mov_b32 v115, s64
s_mov_b32 s92, 0xaaaaaaaa
s_mov_b32 s93, s92
s_mov_b32 s94, 0xcccccccc
s_mov_b32 s95, s94
s_mov_b32 s96, 0xf0000f
s_mov_b32 s97, 0xf0000f00
s_waitcnt vmcnt(14)
s_barrier
v_add_u32 v4, 69632, v4
v_add_u32 v5, 69632, v5
v_add_u32 v6, 69632, v6
Ta 0,4,40960
Ta 4,5,40960
Ta 8,6,40960
Ta 12,4,53248
Ta 16,5,53248
Ta 20,6,53248
ds_read_b128 v[46:49], v8 offset:0
ds_read_b128 v[50:53], v9 offset:0
ds_read_b128 v[54:57], v8 offset:4096
ds_read_b128 v[58:61], v9 offset:4096
v_accvgpr_read_b32 v117, a255
v_mov_b32 v116, 0x100
v_add_u32 v117, 0x9e3779b9, v117
buffer_store_dwordx2 v[116:117], v82, s[36:39], s52 offen sc1
v_add_u32 v93, v93, v116
E 0,36864
E 1,40960
E 2,45056
s_add_u32 s51, s51, 196608
E 0,49152
E 1,53248
E 2,57344
s_add_u32 s51, s51, 196608
s_waitcnt lgkmcnt(0)
s_waitcnt vmcnt(15)
s_barrier
E 0,110592
Ta 24,4,16384
v_mfma_f32_16x16x32_bf16 v[108:111], v[112:115], v[54:57], 0
v_mfma_f32_16x16x32_bf16 v[18:21], v[54:57], v[54:57], 0
Ta 28,5,16384
mvv 108,112,58
Ta 32,6,16384
E 1,114688
mvv 18,58,58
Ta 36,4,28672
v_mfma_f32_16x16x32_bf16 v[96:99], a[0:3], v[112:115], 0
v_mfma_f32_16x16x32_bf16 v[100:103], a[4:7], v[112:115], 0
Ta 40,5,28672
s_add_u32 m0, s66, 3584
s_nop 0
buffer_load_dwordx4 v3, s[44:47], s67 offen offset:512 lds
v_mfma_f32_16x16x32_bf16 v[104:107], v[112:115], a[8:11], 0
Ta 44,6,28672
v_mfma_f32_16x16x32_bf16 v[22:25], a[0:3], v[46:49], 0
ds_read_b128 v[62:65], v10 offset:0
v_mfma_f32_16x16x32_bf16 v[26:29], a[4:7], v[46:49], 0
E 2,118784
s_add_u32 s51, s51, 196608
v_mfma_f32_16x16x32_bf16 v[30:33], v[46:49], a[8:11], 0
ds_read_b128 v[66:69], v11 offset:0
v_mfma_f32_16x16x32_bf16 v[34:37], a[0:3], v[54:57], 0
ds_read_b128 v[70:73], v10 offset:4096
E 0,122880
v_mfma_f32_16x16x32_bf16 v[38:41], a[4:7], v[54:57], 0
ds_read_b128 v[74:77], v11 offset:4096
v_mfma_f32_16x16x32_bf16 v[42:45], v[54:57], a[8:11], 0
mav 96,12,112
s_add_u32 m0, s66, 7680
s_nop 0
buffer_load_dwordx4 v3, s[44:47], 0 offen offset:512 lds
mav 100,16,112
mva 104,112,20
mav 22,12,50
E 1,126976
mav 26,16,50
mva 30,50,20
mav 34,12,58
E 2,131072
s_add_u32 s51, s51, 196608
mav 38,16,58
mva 42,58,20
s_waitcnt lgkmcnt(0)
s_waitcnt vmcnt(15)
s_barrier
v_add_u32 v4, -49152, v4
v_add_u32 v5, -49152, v5
v_add_u32 v6, -49152, v6
E 0,86016
Ta 48,4,40960
mvv 108,112,70
mvv 18,70,70
Ta 52,5,40960
mvv 108,112,74
Ta 56,6,40960
mvv 18,74,74
E 1,90112
Ta 60,4,53248
mav 96,24,112
mav 100,28,112
Ta 64,5,53248
mva 104,112,32
Ta 68,6,53248
mav 22,24,62
E 2,94208
s_add_u32 s51, s51, 196608
ds_read_b128 v[46:49], v8 offset:1024
mav 26,28,62
mva 30,62,32
ds_read_b128 v[50:53], v9 offset:1024
mav 34,24,70
ds_read_b128 v[54:57], v8 offset:5120
E 0,98304
mav 38,28,70
ds_read_b128 v[58:61], v9 offset:5120
mva 42,70,32
mav 96,36,112
mav 100,40,112
E 1,102400
mva 104,112,44
mav 22,36,66
mav 26,40,66
mva 30,66,44
E 2,106496
s_add_u32 s51, s51, 196608
mav 34,36,74
mav 38,40,74
LsQ14:
s_cbranch_execz LsQ18
mva 42,74,44
s_waitcnt lgkmcnt(0)
s_waitcnt vmcnt(14)
s_barrier
E 0,61440
Ta 72,4,16384
mvv 108,112,54
mvv 18,54,54
Ta 76,5,16384
mvv 108,112,58
Ta 80,6,16384
E 1,65536
mvv 18,58,58
Ta 84,4,28672
mav 96,48,112
mav 100,52,112
Ta 88,5,28672
s_add_u32 m0, s66, 4352
s_nop 0
buffer_load_dwordx4 v3, s[44:47], s67 offen offset:768 lds
mva 104,112,56
Ta 92,6,28672
mav 22,48,46
ds_read_b128 v[62:65], v10 offset:1024
mav 26,52,46
E 2,69632
s_add_u32 s51, s51, 196608
mva 30,46,56
ds_read_b128 v[66:69], v11 offset:1024
mav 34,48,54
ds_read_b128 v[70:73], v10 offset:5120
E 0,73728
mav 38,52,54
ds_read_b128 v[74:77], v11 offset:5120
mva 42,54,56
mav 96,60,112
s_add_u32 m0, s66, 8448
s_nop 0
buffer_load_dwordx4 v3, s[44:47], 0 offen offset:768 lds
mav 100,64,112
mva 104,112,68
mav 22,60,50
E 1,77824
mav 26,64,50
mva 30,50,68
mav 34,60,58
E 2,81920
s_add_u32 s51, s51, 196608
mav 38,64,58
mva 42,58,68
s_waitcnt lgkmcnt(0)
s_waitcnt vmcnt(14)
s_barrier
v_add_u32 v4, 49152, v4
v_add_u32 v5, 49152, v5
v_add_u32 v6, 49152, v6
E 0,36864
Ta 96,4,40960
mvv 108,112,70
mvv 18,70,70
Ta 100,5,40960
mvv 108,112,74
Ta 104,6,40960
mvv 18,74,74
E 1,40960
Ta 108,4,53248
mav 96,72,112
mav 100,76,112
Ta 112,5,53248
mva 104,112,80
Ta 116,6,53248
mav 22,72,62
E 2,45056
s_add_u32 s51, s51, 196608
ds_read_b128 v[46:49], v8 offset:2048
mav 26,76,62
mva 30,62,80
ds_read_b128 v[50:53], v9 offset:2048
mav 34,72,70
ds_read_b128 v[54:57], v8 offset:6144
E 0,49152
mav 38,76,70
ds_read_b128 v[58:61], v9 offset:6144
mva 42,70,80
mav 96,84,112
mav 100,88,112
E 1,53248
mva 104,112,92
mav 22,84,66
mav 26,88,66
mva 30,66,92
E 2,57344
s_add_u32 s51, s51, 196608
mav 34,84,74
mav 38,88,74
mva 42,74,92
s_waitcnt lgkmcnt(0)
s_waitcnt vmcnt(14)
s_barrier
E 0,110592
Ta 120,4,16384
mvv 108,112,54
mvv 18,54,54
Ta 124,5,16384
mvv 108,112,58
Ta 128,6,16384
E 1,114688
mvv 18,58,58
Ta 132,4,28672
mav 96,96,112
mav 100,100,112
Ta 136,5,28672
s_add_u32 m0, s66, 1024
s_nop 0
buffer_load_dwordx4 v3, s[44:47], s67 offen offset:1024 lds
mva 104,112,104
Ta 140,6,28672
mav 22,96,46
ds_read_b128 v[62:65], v10 offset:2048
mav 26,100,46
E 2,118784
s_add_u32 s51, s51, 196608
mva 30,46,104
ds_read_b128 v[66:69], v11 offset:2048
mav 34,96,54
ds_read_b128 v[70:73], v10 offset:6144
E 0,122880
mav 38,100,54
ds_read_b128 v[74:77], v11 offset:6144
mva 42,54,104
mav 96,108,112
s_add_u32 m0, s66, 5120
s_nop 0
buffer_load_dwordx4 v3, s[44:47], 0 offen offset:1024 lds
mav 100,112,112
mva 104,112,116
mav 22,108,50
E 1,126976
mav 26,112,50
mva 30,50,116
mav 34,108,58
E 2,131072
s_add_u32 s51, s51, 196608
mav 38,112,58
mva 42,58,116
s_waitcnt lgkmcnt(0)
s_waitcnt vmcnt(14)
s_barrier
v_add_u32 v4, -49152, v4
v_add_u32 v5, -49152, v5
v_add_u32 v6, -49152, v6
E 0,86016
Ta 144,4,40960
mvv 108,112,70
mvv 18,70,70
Ta 148,5,40960
mvv 108,112,74
Ta 152,6,40960
mvv 18,74,74
E 1,90112
Ta 156,4,53248
mav 96,120,112
mav 100,124,112
Ta 160,5,53248
mva 104,112,128
Ta 164,6,53248
mav 22,120,62
E 2,94208
s_add_u32 s51, s51, 196608
ds_read_b128 v[46:49], v8 offset:3072
mav 26,124,62
mva 30,62,128
ds_read_b128 v[50:53], v9 offset:3072
mav 34,120,70
ds_read_b128 v[54:57], v8 offset:7168
E 0,98304
mav 38,124,70
ds_read_b128 v[58:61], v9 offset:7168
mva 42,70,128
mav 96,132,112
mav 100,136,112
E 1,102400
mva 104,112,140
mav 22,132,66
mav 26,136,66
mva 30,66,140
E 2,106496
s_add_u32 s51, s51, 196608
mav 34,132,74
mav 38,136,74
mva 42,74,140
s_waitcnt lgkmcnt(0)
s_waitcnt vmcnt(14)
s_barrier
E 0,61440
Ta 168,4,16384
mvv 108,112,54
mvv 18,54,54
Ta 172,5,16384
mvv 108,112,58
Ta 176,6,16384
E 1,65536
mvv 18,58,58
Ta 180,4,28672
mav 96,144,112
mav 100,148,112
Ta 184,5,28672
s_add_u32 m0, s66, 1792
s_nop 0
buffer_load_dwordx4 v3, s[44:47], s67 offen offset:1280 lds
mva 104,112,152
Ta 188,6,28672
mav 22,144,46
ds_read_b128 v[62:65], v10 offset:3072
mav 26,148,46
E 2,69632
s_add_u32 s51, s51, 196608
mva 30,46,152
ds_read_b128 v[66:69], v11 offset:3072
mav 34,144,54
ds_read_b128 v[70:73], v10 offset:7168
E 0,73728
mav 38,148,54
ds_read_b128 v[74:77], v11 offset:7168
mva 42,54,152
mav 96,156,112
s_add_u32 m0, s66, 5888
s_nop 0
buffer_load_dwordx4 v3, s[44:47], 0 offen offset:1280 lds
mav 100,160,112
mva 104,112,164
mav 22,156,50
E 1,77824
mav 26,160,50
mva 30,50,164
mav 34,156,58
E 2,81920
s_add_u32 s51, s51, 196608
mav 38,160,58
mva 42,58,164
s_waitcnt lgkmcnt(0)
s_waitcnt vmcnt(14)
s_barrier
v_add_u32 v4, 49152, v4
v_add_u32 v5, 49152, v5
v_add_u32 v6, 49152, v6
E 0,36864
Ta 192,4,40960
mvv 108,112,70
mvv 18,70,70
Ta 196,5,40960
mvv 108,112,74
Ta 200,6,40960
mvv 18,74,74
E 1,40960
Ta 204,4,53248
mav 96,168,112
mav 100,172,112
Ta 208,5,53248
mva 104,112,176
Ta 212,6,53248
mav 22,168,62
E 2,45056
s_add_u32 s51, s51, 196608
ds_read_b128 v[46:49], v8 offset:0
mav 26,172,62
mva 30,62,176
ds_read_b128 v[50:53], v9 offset:0
mav 34,168,70
ds_read_b128 v[54:57], v8 offset:4096
E 0,49152
mav 38,172,70
ds_read_b128 v[58:61], v9 offset:4096
mva 42,70,176
mav 96,180,112
mav 100,184,112
E 1,53248
mva 104,112,188
mav 22,180,66
mav 26,184,66
mva 30,66,188
E 2,57344
s_add_u32 s51, s51, 196608
mav 34,180,74
mav 38,184,74
mva 42,74,188
s_waitcnt lgkmcnt(0)
s_waitcnt vmcnt(14)
s_barrier
E 0,110592
Ta 216,4,16384
mvv 108,112,54
mvv 18,54,54
Ta 220,5,16384
mvv 108,112,58
Ta 224,6,16384
E 1,114688
mvv 18,58,58
Ta 228,4,28672
mav 96,192,112
mav 100,196,112
Ta 232,5,28672
s_add_u32 m0, s66, 2560
s_nop 0
buffer_load_dwordx4 v3, s[44:47], s67 offen offset:1536 lds
mva 104,112,200
Ta 236,6,28672
mav 22,192,46
ds_read_b128 v[62:65], v10 offset:0
mav 26,196,46
E 2,118784
s_add_u32 s51, s51, 196608
mva 30,46,200
ds_read_b128 v[66:69], v11 offset:0
mav 34,192,54
ds_read_b128 v[70:73], v10 offset:4096
E 0,122880
mav 38,196,54
ds_read_b128 v[74:77], v11 offset:4096
mva 42,54,200
mav 96,204,112
s_add_u32 m0, s66, 6656
s_nop 0
buffer_load_dwordx4 v3, s[44:47], 0 offen offset:1536 lds
mav 100,208,112
mva 104,112,212
mav 22,204,50
E 1,126976
mav 26,208,50
mva 30,50,212
mav 34,204,58
E 2,131072
s_add_u32 s51, s51, 196608
mav 38,208,58
mva 42,58,212
s_waitcnt lgkmcnt(0)
s_waitcnt vmcnt(14)
s_barrier
v_add_u32 v4, -49152, v4
v_add_u32 v5, -49152, v5
v_add_u32 v6, -49152, v6
E 0,86016
Ta 240,4,40960
mvv 108,112,70
mvv 18,70,70
Ta 244,5,40960
mvv 108,112,74
Ta 248,6,40960
mvv 18,74,74
E 1,90112
Ta 252,4,53248
mav 96,216,112
mav 100,220,112
Tv 128,5,53248
mva 104,112,224
Tv 132,6,53248
mav 22,216,62
E 2,94208
s_add_u32 s51, s51, 196608
ds_read_b128 v[46:49], v8 offset:1024
mav 26,220,62
mva 30,62,224
ds_read_b128 v[50:53], v9 offset:1024
mav 34,216,70
ds_read_b128 v[54:57], v8 offset:5120
E 0,98304
mav 38,220,70
ds_read_b128 v[58:61], v9 offset:5120
mva 42,70,224
mav 96,228,112
mav 100,232,112
E 1,102400
mva 104,112,236
mav 22,228,66
mav 26,232,66
mva 30,66,236
E 2,106496
s_add_u32 s51, s51, 196608
mav 34,228,74
mav 38,232,74
mva 42,74,236
s_waitcnt lgkmcnt(0)
s_waitcnt vmcnt(14)
s_barrier
E 0,61440
Tv 136,4,16384
mvv 108,112,54
mvv 18,54,54
Tv 140,5,16384
mvv 108,112,58
Tv 144,6,16384
E 1,65536
mvv 18,58,58
Tv 148,4,28672
mav 96,240,112
mav 100,244,112
Tv 152,5,28672
s_add_u32 m0, s66, 3328
s_nop 0
buffer_load_dwordx4 v3, s[44:47], s67 offen offset:1792 lds
mva 104,112,248
Tv 156,6,28672
LsQ15:
s_cbranch_execz LsQ19
mav 22,240,46
ds_read_b128 v[62:65], v10 offset:1024
mav 26,244,46
E 2,69632
s_add_u32 s51, s51, 196608
mva 30,46,248
ds_read_b128 v[66:69], v11 offset:1024
mav 34,240,54
ds_read_b128 v[70:73], v10 offset:5120
E 0,73728
mav 38,244,54
ds_read_b128 v[74:77], v11 offset:5120
mva 42,54,248
mav 96,252,112
s_add_u32 m0, s66, 7424
s_nop 0
buffer_load_dwordx4 v3, s[44:47], 0 offen offset:1792 lds
mvv 100,128,112
mvv 104,112,132
mav 22,252,50
E 1,77824
mvv 26,128,50
mvv 30,50,132
mav 34,252,58
E 2,81920
s_add_u32 s51, s51, 196608
mvv 38,128,58
mvv 42,58,132
s_waitcnt lgkmcnt(0)
s_waitcnt vmcnt(14)
s_barrier
v_add_u32 v4, 49152, v4
v_add_u32 v5, 49152, v5
v_add_u32 v6, 49152, v6
E 0,36864
Tv 160,4,40960
mvv 108,112,70
mvv 18,70,70
Tv 164,5,40960
mvv 108,112,74
Tv 168,6,40960
mvv 18,74,74
E 1,40960
Tv 172,4,53248
mvv 96,136,112
mvv 100,140,112
Tv 176,5,53248
mvv 104,112,144
E 2,45056
s_add_u32 s51, s51, 196608
Tv 180,6,53248
mvv 22,136,62
ds_read_b128 v[46:49], v8 offset:2048
mvv 26,140,62
mvv 30,62,144
ds_read_b128 v[50:53], v9 offset:2048
E 0,49152
mvv 34,136,70
ds_read_b128 v[54:57], v8 offset:6144
mvv 38,140,70
ds_read_b128 v[58:61], v9 offset:6144
mvv 42,70,144
E 1,53248
mvv 96,148,112
mvv 100,152,112
mvv 104,112,156
E 2,57344
s_add_u32 s51, s51, 196608
mvv 22,148,66
mvv 26,152,66
mvv 30,66,156
buffer_load_dwordx2 v[122:123], v92, s[36:39], 0 offen sc1
mvv 34,148,74
mvv 38,152,74
mvv 42,74,156
s_waitcnt lgkmcnt(0)
s_waitcnt vmcnt(15)
s_barrier
s_add_u32 m0, s50, 49536
s_nop 0
D2 89,54,0
Tv 184,4,16384
mvv 108,112,54
mvv 18,54,54
Tv 188,5,16384
mvv 108,112,58
Tv 192,6,16384
D2 89,54,1024
mvv 18,58,58
Tv 196,4,28672
mvv 96,160,112
mvv 100,164,112
Tv 200,5,28672
D2 89,55,2048
mvv 104,112,168
Tv 204,6,28672
mvv 22,160,46
ds_read_b128 v[62:65], v10 offset:2048
mvv 26,164,46
D2 89,55,3072
mvv 30,46,168
ds_read_b128 v[66:69], v11 offset:2048
mvv 34,160,54
ds_read_b128 v[70:73], v10 offset:6144
s_add_u32 m0, s50, 57792
s_nop 0
D2 90,54,0
mvv 38,164,54
ds_read_b128 v[74:77], v11 offset:6144
mvv 42,54,168
mvv 96,172,112
D2 90,54,1024
mvv 100,176,112
mvv 104,112,180
mvv 22,172,50
D2 90,55,2048
mvv 26,176,50
mvv 30,50,180
mvv 34,172,58
D2 90,55,3072
mvv 38,176,58
mvv 42,58,180
s_waitcnt lgkmcnt(0)
s_waitcnt vmcnt(15)
s_barrier
v_add_u32 v4, -49152, v4
v_add_u32 v5, -49152, v5
v_add_u32 v6, -49152, v6
s_add_u32 m0, s50, 33024
s_nop 0
D2 87,54,0
Tv 208,4,40960
mvv 108,112,70
mvv 18,70,70
Tv 212,5,40960
mvv 108,112,74
Tv 216,6,40960
D2 87,54,1024
mvv 18,74,74
Tv 220,4,53248
mvv 96,184,112
mvv 100,188,112
Tv 224,5,53248
D2 87,55,2048
mvv 104,112,192
Tv 228,6,53248
mvv 22,184,62
ds_read_b128 v[46:49], v8 offset:3072
mvv 26,188,62
D2 87,55,3072
mvv 30,62,192
ds_read_b128 v[50:53], v9 offset:3072
mvv 34,184,70
ds_read_b128 v[54:57], v8 offset:7168
s_add_u32 m0, s50, 41280
s_nop 0
D2 88,54,0
mvv 38,188,70
ds_read_b128 v[58:61], v9 offset:7168
mvv 42,70,192
mvv 96,196,112
D2 88,54,1024
mvv 100,200,112
mvv 104,112,204
mvv 22,196,66
D2 88,55,2048
mvv 26,200,66
mvv 30,66,204
mvv 34,196,74
D2 88,55,3072
mvv 38,200,74
mvv 42,74,204
s_waitcnt lgkmcnt(0)
s_waitcnt vmcnt(17)
s_barrier
Tv 232,4,16384
Tv 236,5,16384
Tv 240,6,16384
Tv 244,4,28672
Tv 248,5,28672
Tv 252,6,28672
ds_read_b128 v[62:65], v10 offset:3072
ds_read_b128 v[66:69], v11 offset:3072
ds_read_b128 v[70:73], v10 offset:7168
ds_read_b128 v[74:77], v11 offset:7168
s_lshr_b32 s60, s53, 11
s_mul_i32 s60, s60, 313344
s_add_u32 s60, s60, 626688
s_add_u32 s60, s60, s49
s_sub_u32 s54, s54, 0x8000
s_sub_u32 s55, s55, 0x8000
s_waitcnt vmcnt(16)
ds_write_b32 v83, v122
v_sub_u32 v122, v122, v93
v_xor_b32 v123, v123, v117
v_cmp_lt_u64 vcc, s[62:63], v[122:123]
s_cbranch_vccnz Cq
Bq:
v_add_u32 v93, 1, v93
buffer_load_dwordx2 v[14:15], v94, s[36:39], s60 offen sc1
mvv 108,112,54
mvv 18,54,54
mvv 108,112,58
s_add_u32 m0, s50, 0
s_nop 0
D2 87,54,0
mvv 18,58,58
mvv 96,208,112
D2 87,54,1024
mvv 100,212,112
mvv 104,112,216
mvv 22,208,46
D2 87,55,2048
mvv 26,212,46
mvv 30,46,216
D2 87,55,3072
mvv 34,208,54
mvv 38,212,54
mvv 42,54,216
s_add_u32 m0, s50, 8256
s_nop 0
D2 88,54,0
mvv 96,220,112
mvv 100,224,112
D2 88,54,1024
mvv 104,112,228
mvv 22,220,50
mvv 26,224,50
D2 88,55,2048
mvv 30,50,228
mvv 34,220,58
D2 88,55,3072
mvv 38,224,58
mvv 42,58,228
s_waitcnt lgkmcnt(0)
s_add_u32 m0, s50, 16512
s_nop 0
D2 89,54,0
mvv 108,112,70
mvv 18,70,70
mvv 108,112,74
D2 89,54,1024
mvv 18,74,74
mvv 96,232,112
mvv 100,236,112
D2 89,55,2048
mvv 104,112,240
mvv 22,232,62
)"
R"(
mvv 26,236,62
D2 89,55,3072
mvv 30,62,240
mvv 34,232,70
s_add_u32 m0, s50, 24768
s_nop 0
D2 90,54,0
mvv 38,236,70
mvv 42,70,240
v_cndmask_b32 v18, v18, v19, s[92:93]
mvv 96,244,112
v_cndmask_b32 v20, v20, v21, s[92:93]
D2 90,54,1024
mvv 100,248,112
v_mul_f32 v108, 0x3a800000, v108
v_cndmask_b32 v18, v18, v20, s[94:95]
mvv 104,112,252
v_mul_f32 v18, 0x3a800000, v18
mvv 22,244,66
D2 90,55,2048
v_fma_f32 v18, -v108, v108, v18
mvv 26,248,66
v_max_f32 v18, 0, v18
mvv 30,66,252
v_add_f32 v18, 0x3727c5ac, v18
mvv 34,244,74
D2 90,55,3072
v_rsq_f32 v18, v18
mvv 38,248,74
s_nop 0
v_mul_f32_e64 v19, -v108, v18
mvv 42,74,252
s_mov_b64 exec, s[96:97]
ds_write_b64 v85, v[18:19] offset:128
s_mov_b64 exec, -1
s_waitcnt lgkmcnt(0)
s_barrier
s_nop 7
v_mov_b32 v95, v104
s_add_u32 s40, s36, 4194304
s_addc_u32 s41, s37, 0
s_and_b32 s60, s48, 3
s_cmp_eq_u32 s60, 1
s_cbranch_scc1 LvQB1
LvQB0:
LsQ16:
s_cbranch_execz LsQ20
ds_read_b64 v[118:119], v85 offset:0
ds_read_b128 v[104:107], v86 offset:0
ds_read_b128 v[108:111], v86 offset:16
s_waitcnt lgkmcnt(0)
v_mul_f32 v120, v119, v96
v_fma_f32 v22, v22, v118, v120
v_mul_f32 v121, v119, v97
v_fma_f32 v23, v23, v118, v121
v_mul_f32 v120, v119, v98
v_fma_f32 v24, v24, v118, v120
v_mul_f32 v121, v119, v99
v_fma_f32 v25, v25, v118, v121
v_mul_f32 v120, v119, v100
v_fma_f32 v26, v26, v118, v120
v_mul_f32 v121, v119, v101
v_fma_f32 v27, v27, v118, v121
v_mul_f32 v120, v119, v102
v_fma_f32 v28, v28, v118, v120
v_mul_f32 v121, v119, v103
v_fma_f32 v29, v29, v118, v121
v_cvt_pk_bf16_f32 v112, v22, v23
v_cvt_pk_bf16_f32 v113, v24, v25
v_cvt_pk_bf16_f32 v114, v26, v27
v_cvt_pk_bf16_f32 v115, v28, v29
ds_write_b128 v84, v[112:115] offset:0
v_mul_f32 v105, v105, v95
v_fma_f32 v30, v30, v104, v105
v_mul_f32 v107, v107, v95
v_fma_f32 v31, v31, v106, v107
v_mul_f32 v109, v109, v95
v_fma_f32 v32, v32, v108, v109
v_mul_f32 v111, v111, v95
v_fma_f32 v33, v33, v110, v111
v_cvt_pk_bf16_f32 v78, v30, v31
v_cvt_pk_bf16_f32 v79, v32, v33
ds_write_b64 v84, v[78:79] offset:1024
s_xor_b32 s50, s50, s57
s_waitcnt vmcnt(17) lgkmcnt(0)
s_barrier
R 46,33024
R 50,33088
R 54,33152
R 58,33216
s_waitcnt vmcnt(16)
v_permlane32_swap_b32 v14, v15
v_add_f32 v16, v14, v15
v_add_f32 v17, v14, v15
s_nop 1
v_permlane16_swap_b32 v16, v17
v_add_f32 v14, v16, v17
v_add_f32 v15, v16, v17
s_nop 1
v_permlane32_swap_b32 v14, v15
v_mul_f32 v16, 0x3a800000, v14
v_mul_f32 v17, 0x3a800000, v15
v_fma_f32 v17, -v16, v16, v17
v_max_f32 v17, 0, v17
v_add_f32 v17, 0x3727c5ac, v17
v_rsq_f32 v14, v17
s_nop 0
v_mul_f32_e64 v15, -v16, v14
ds_write_b64 v85, v[14:15] offset:0
ds_read_b64 v[118:119], v85 offset:128
ds_read_b128 v[104:107], v86 offset:128
ds_read_b128 v[108:111], v86 offset:144
s_waitcnt lgkmcnt(0)
v_mfma_f32_16x16x32_bf16 v[22:25], a[0:3], v[46:49], 0
R 62,33280
v_mul_f32 v120, v119, v96
v_mfma_f32_16x16x32_bf16 v[26:29], a[4:7], v[46:49], 0
v_fma_f32 v34, v34, v118, v120
v_mul_f32 v121, v119, v97
v_mfma_f32_16x16x32_bf16 v[30:33], v[46:49], a[8:11], 0
v_fma_f32 v35, v35, v118, v121
v_mul_f32 v120, v119, v98
mav 22,12,50
R 66,33344
v_fma_f32 v36, v36, v118, v120
mav 26,16,50
v_mul_f32 v121, v119, v99
v_fma_f32 v37, v37, v118, v121
mva 30,50,20
v_mul_f32 v120, v119, v100
v_fma_f32 v38, v38, v118, v120
mav 22,24,54
R 70,33408
v_mul_f32 v121, v119, v101
mav 26,28,54
v_fma_f32 v39, v39, v118, v121
v_mul_f32 v120, v119, v102
mva 30,54,32
v_fma_f32 v40, v40, v118, v120
v_mul_f32 v121, v119, v103
mav 22,36,58
R 74,33472
v_fma_f32 v41, v41, v118, v121
mav 26,40,58
v_cvt_pk_bf16_f32 v112, v34, v35
v_cvt_pk_bf16_f32 v113, v36, v37
mva 30,58,44
s_waitcnt lgkmcnt(2)
v_cvt_pk_bf16_f32 v114, v38, v39
v_cvt_pk_bf16_f32 v115, v40, v41
mav 22,48,62
R 46,33536
v_mul_f32 v105, v105, v95
mav 26,52,62
ds_write_b128 v84, v[112:115] offset:4096
mva 30,62,56
v_fma_f32 v42, v42, v104, v105
v_mul_f32 v107, v107, v95
mav 22,60,66
R 50,33600
v_fma_f32 v43, v43, v106, v107
mav 26,64,66
v_mul_f32 v109, v109, v95
v_fma_f32 v44, v44, v108, v109
mva 30,66,68
s_waitcnt lgkmcnt(3)
v_mul_f32 v111, v111, v95
v_fma_f32 v45, v45, v110, v111
mav 22,72,70
R 54,33664
v_cvt_pk_bf16_f32 v78, v42, v43
mav 26,76,70
v_cvt_pk_bf16_f32 v79, v44, v45
mva 30,70,80
ds_write_b64 v84, v[78:79] offset:5120
mav 22,84,74
R 58,33728
mav 26,88,74
mva 30,74,92
s_waitcnt lgkmcnt(3)
mav 22,96,46
s_waitcnt vmcnt(0) lgkmcnt(1)
R 62,33792
s_mov_b32 m0, 144384
mav 26,100,46
s_barrier
mva 30,46,104
buffer_load_dword v91, s[36:39], 0 offen sc1 lds
mav 22,108,50
s_sub_u32 s54, s54, 0x8000
R 66,33856
s_add_u32 m0, s50, 33024
mav 26,112,50
s_cmp_lt_u32 s54, s56
s_cselect_b64 s[44:45], s[40:41], s[44:45]
mva 30,50,116
mav 22,120,54
D2 87,54,0
mav 26,124,54
s_add_u32 s55, s54, 14336
R 70,33920
mva 30,54,128
s_waitcnt lgkmcnt(2)
D2 87,54,1024
mav 22,132,58
R 74,33984
mav 26,136,58
D2 87,55,2048
mva 30,58,140
mav 22,144,62
D2 87,55,3072
mav 26,148,62
s_add_u32 m0, s50, 41280
R 46,34048
mva 30,62,152
s_waitcnt lgkmcnt(2)
D2 88,54,0
mav 22,156,66
R 50,34112
mav 26,160,66
D2 88,54,1024
mva 30,66,164
mav 22,168,70
D2 88,55,2048
mav 26,172,70
R 54,34176
mva 30,70,176
s_waitcnt lgkmcnt(2)
D2 88,55,3072
mav 22,180,74
s_add_u32 m0, s50, 49536
R 58,34240
mav 26,184,74
D2 89,54,0
mva 30,74,188
mav 22,192,46
D2 89,54,1024
mav 26,196,46
R 62,34304
mva 30,46,200
s_waitcnt lgkmcnt(2)
D2 89,55,2048
mav 22,204,50
R 66,34368
mav 26,208,50
D2 89,55,3072
mva 30,50,212
s_add_u32 m0, s50, 57792
mav 22,216,54
D2 90,54,0
mav 26,220,54
R 70,34432
mva 30,54,224
s_waitcnt lgkmcnt(2)
D2 90,54,1024
mav 22,228,58
R 74,34496
mav 26,232,58
D2 90,55,2048
mva 30,58,236
s_sub_u32 s54, s54, 0x8000
mav 22,240,62
D2 90,55,3072
mav 26,244,62
s_add_u32 m0, s50, 0
R 46,34560
mva 30,62,248
s_waitcnt lgkmcnt(2)
D2 87,54,0
mav 22,252,66
R 50,34624
mvv 26,128,66
D2 87,54,1024
mvv 30,66,132
s_add_u32 s55, s54, 14336
mvv 22,136,70
D2 87,55,2048
LsQ17:
s_cbranch_execz LsQ21
mvv 26,140,70
R 54,34688
mvv 30,70,144
s_waitcnt lgkmcnt(2)
D2 87,55,3072
mvv 22,148,74
s_add_u32 m0, s50, 8256
R 58,34752
mvv 26,152,74
D2 88,54,0
mvv 30,74,156
mvv 22,160,46
D2 88,54,1024
mvv 26,164,46
R 62,34816
mvv 30,46,168
s_waitcnt lgkmcnt(2)
D2 88,55,2048
mvv 22,172,50
R 66,34880
mvv 26,176,50
D2 88,55,3072
mvv 30,50,180
s_add_u32 m0, s50, 16512
mvv 22,184,54
D2 89,54,0
mvv 26,188,54
R 70,34944
mvv 30,54,192
s_waitcnt lgkmcnt(2)
D2 89,54,1024
mvv 22,196,58
R 74,35008
mvv 26,200,58
D2 89,55,2048
mvv 30,58,204
mvv 22,208,62
D2 89,55,3072
mvv 26,212,62
s_add_u32 m0, s50, 24768
R 46,0
mvv 30,62,216
s_waitcnt lgkmcnt(2)
D2 90,54,0
mvv 22,220,66
ds_read_b32 v122, v83 offset:0
mvv 26,224,66
D2 90,54,1024
mvv 30,66,228
R 50,64
mvv 22,232,70
D2 90,55,2048
mvv 26,236,70
R 54,128
mvv 30,70,240
s_waitcnt lgkmcnt(3)
D2 90,55,3072
mvv 22,244,74
R 58,192
mvv 26,248,74
s_waitcnt lgkmcnt(3)
v_cmp_gt_i32 vcc, v93, v122
mvv 30,74,252
ds_read_b64 v[118:119], v85 offset:0
v_mfma_f32_16x16x32_bf16 v[34:37], a[0:3], v[46:49], 0
s_cbranch_vccnz CQB0_1
BQB0_1:
v_mfma_f32_16x16x32_bf16 v[38:41], a[4:7], v[46:49], 0
R 62,256
v_mfma_f32_16x16x32_bf16 v[42:45], v[46:49], a[8:11], 0
s_waitcnt lgkmcnt(3)
ds_read_b128 v[104:107], v86 offset:0
mav 34,12,50
R 66,320
mav 38,16,50
ds_read_b128 v[108:111], v86 offset:16
mva 42,50,20
mav 34,24,54
s_waitcnt lgkmcnt(4)
R 70,384
v_mul_f32 v120, v119, v96
mav 38,28,54
v_fma_f32 v22, v22, v118, v120
v_mul_f32 v121, v119, v97
mva 42,54,32
v_fma_f32 v23, v23, v118, v121
v_mul_f32 v120, v119, v98
mav 34,36,58
s_waitcnt lgkmcnt(2)
R 74,448
v_fma_f32 v24, v24, v118, v120
mav 38,40,58
v_mul_f32 v121, v119, v99
v_fma_f32 v25, v25, v118, v121
mva 42,58,44
s_waitcnt lgkmcnt(2)
v_mul_f32 v120, v119, v100
v_fma_f32 v26, v26, v118, v120
mav 34,48,62
R 46,512
v_mul_f32 v121, v119, v101
mav 38,52,62
v_fma_f32 v27, v27, v118, v121
v_mul_f32 v120, v119, v102
mva 42,62,56
v_fma_f32 v28, v28, v118, v120
v_mul_f32 v121, v119, v103
mav 34,60,66
R 50,576
v_fma_f32 v29, v29, v118, v121
mav 38,64,66
v_cvt_pk_bf16_f32 v112, v22, v23
v_cvt_pk_bf16_f32 v113, v24, v25
mva 42,66,68
s_waitcnt lgkmcnt(2)
v_cvt_pk_bf16_f32 v114, v26, v27
v_cvt_pk_bf16_f32 v115, v28, v29
mav 34,72,70
R 54,640
v_mul_f32 v105, v105, v95
mav 38,76,70
ds_write_b128 v84, v[112:115] offset:0
mva 42,70,80
s_xor_b32 s50, s50, s57
v_fma_f32 v30, v30, v104, v105
v_mul_f32 v107, v107, v95
mav 34,84,74
R 58,704
v_fma_f32 v31, v31, v106, v107
mav 38,88,74
v_mul_f32 v109, v109, v95
v_fma_f32 v32, v32, v108, v109
mva 42,74,92
s_waitcnt lgkmcnt(3)
v_mul_f32 v111, v111, v95
v_fma_f32 v33, v33, v110, v111
mav 34,96,46
s_mov_b32 m0, 144384
R 62,768
v_cvt_pk_bf16_f32 v78, v30, v31
mav 38,100,46
s_sub_u32 s54, s54, 0x8000
v_cvt_pk_bf16_f32 v79, v32, v33
mva 42,46,104
buffer_load_dword v91, s[36:39], 0 offen sc1 lds
mav 34,108,50
s_add_u32 m0, s50, 33024
R 66,832
mav 38,112,50
ds_write_b64 v84, v[78:79] offset:1024
mva 42,50,116
s_waitcnt lgkmcnt(3)
s_cmp_lt_u32 s54, s56
s_cselect_b64 s[44:45], s[40:41], s[44:45]
mav 34,120,54
R 70,896
mav 38,124,54
s_waitcnt vmcnt(17) lgkmcnt(1)
s_add_u32 s55, s54, 14336
mva 42,54,128
s_barrier
mav 34,132,58
R 74,960
mav 38,136,58
D2 87,54,0
mva 42,58,140
mav 34,144,62
D2 87,54,1024
mav 38,148,62
R 46,1024
mva 42,62,152
D2 87,55,2048
mav 34,156,66
R 50,1088
mav 38,160,66
D2 87,55,3072
mva 42,66,164
s_waitcnt lgkmcnt(2)
s_add_u32 m0, s50, 41280
v_add_u32 v93, 1, v93
mav 34,168,70
D2 88,54,0
mav 38,172,70
R 54,1152
mva 42,70,176
D2 88,54,1024
mav 34,180,74
R 58,1216
mav 38,184,74
D2 88,55,2048
mva 42,74,188
s_waitcnt lgkmcnt(2)
mav 34,192,46
D2 88,55,3072
mav 38,196,46
s_add_u32 m0, s50, 49536
R 62,1280
mva 42,46,200
D2 89,54,0
mav 34,204,50
R 66,1344
mav 38,208,50
D2 89,54,1024
mva 42,50,212
s_waitcnt lgkmcnt(2)
mav 34,216,54
D2 89,55,2048
mav 38,220,54
R 70,1408
mva 42,54,224
D2 89,55,3072
mav 34,228,58
s_add_u32 m0, s50, 57792
R 74,1472
mav 38,232,58
D2 90,54,0
mva 42,58,236
s_waitcnt lgkmcnt(2)
mav 34,240,62
D2 90,54,1024
mav 38,244,62
R 46,1536
mva 42,62,248
D2 90,55,2048
mav 34,252,66
R 50,1600
mvv 38,128,66
D2 90,55,3072
mvv 42,66,132
s_waitcnt lgkmcnt(2)
mvv 34,136,70
R 54,1664
mvv 38,140,70
mvv 42,70,144
mvv 34,148,74
R 58,1728
mvv 38,152,74
mvv 42,74,156
s_waitcnt lgkmcnt(2)
mvv 34,160,46
R 62,1792
mvv 38,164,46
mvv 42,46,168
mvv 34,172,50
R 66,1856
mvv 38,176,50
mvv 42,50,180
s_waitcnt lgkmcnt(2)
mvv 34,184,54
R 70,1920
mvv 38,188,54
mvv 42,54,192
mvv 34,196,58
R 74,1984
v_xor_b32 v80, v81, v80
mvv 38,200,58
mvv 42,58,204
s_waitcnt lgkmcnt(2)
mvv 34,208,62
R 46,33024
mvv 38,212,62
ds_read_b32 v122, v83 offset:0
mvv 42,62,216
ds_read_b64 v[118:119], v85 offset:128
mvv 34,220,66
R 50,33088
mvv 38,224,66
ds_read_b128 v[104:107], v86 offset:128
mvv 42,66,228
s_waitcnt lgkmcnt(5)
ds_read_b128 v[108:111], v86 offset:144
mvv 34,232,70
R 54,33152
mvv 38,236,70
mvv 42,70,240
s_waitcnt lgkmcnt(4)
v_cmp_gt_i32 vcc, v93, v122
mvv 34,244,74
R 58,33216
mvv 38,248,74
mvv 42,74,252
s_cbranch_vccnz CQB0_2
BQB0_2:
v_mfma_f32_16x16x32_bf16 v[22:25], a[0:3], v[46:49], 0
R 62,33280
v_mul_f32 v120, v119, v96
v_mfma_f32_16x16x32_bf16 v[26:29], a[4:7], v[46:49], 0
s_waitcnt lgkmcnt(2)
v_fma_f32 v34, v34, v118, v120
v_mul_f32 v121, v119, v97
v_mfma_f32_16x16x32_bf16 v[30:33], v[46:49], a[8:11], 0
v_fma_f32 v35, v35, v118, v121
v_mul_f32 v120, v119, v98
mav 22,12,50
R 66,33344
v_fma_f32 v36, v36, v118, v120
mav 26,16,50
v_mul_f32 v121, v119, v99
v_fma_f32 v37, v37, v118, v121
mva 30,50,20
v_mul_f32 v120, v119, v100
v_fma_f32 v38, v38, v118, v120
mav 22,24,54
R 70,33408
v_mul_f32 v121, v119, v101
mav 26,28,54
v_fma_f32 v39, v39, v118, v121
v_mul_f32 v120, v119, v102
mva 30,54,32
s_waitcnt lgkmcnt(2)
v_fma_f32 v40, v40, v118, v120
v_mul_f32 v121, v119, v103
mav 22,36,58
R 74,33472
v_fma_f32 v41, v41, v118, v121
mav 26,40,58
v_cvt_pk_bf16_f32 v112, v34, v35
v_cvt_pk_bf16_f32 v113, v36, v37
mva 30,58,44
v_cvt_pk_bf16_f32 v114, v38, v39
v_cvt_pk_bf16_f32 v115, v40, v41
mav 22,48,62
R 46,33536
v_mul_f32 v105, v105, v95
mav 26,52,62
ds_write_b128 v84, v[112:115] offset:4096
mva 30,62,56
s_waitcnt lgkmcnt(3)
v_fma_f32 v42, v42, v104, v105
v_mul_f32 v107, v107, v95
mav 22,60,66
R 50,33600
v_fma_f32 v43, v43, v106, v107
mav 26,64,66
v_mul_f32 v109, v109, v95
v_fma_f32 v44, v44, v108, v109
mva 30,66,68
v_mul_f32 v111, v111, v95
v_fma_f32 v45, v45, v110, v111
mav 22,72,70
R 54,33664
v_cvt_pk_bf16_f32 v78, v42, v43
mav 26,76,70
v_cvt_pk_bf16_f32 v79, v44, v45
mva 30,70,80
s_waitcnt lgkmcnt(2)
ds_write_b64 v84, v[78:79] offset:5120
mav 22,84,74
R 58,33728
mav 26,88,74
mva 30,74,92
mav 22,96,46
s_waitcnt vmcnt(17) lgkmcnt(1)
R 62,33792
s_mov_b32 m0, 144384
mav 26,100,46
s_barrier
mva 30,46,104
buffer_load_dword v91, s[36:39], 0 offen sc1 lds
mav 22,108,50
s_sub_u32 s54, s54, 0x8000
R 66,33856
s_add_u32 m0, s50, 0
mav 26,112,50
mva 30,50,116
mav 22,120,54
D2 87,54,0
mav 26,124,54
s_add_u32 s55, s54, 14336
R 70,33920
mva 30,54,128
s_waitcnt lgkmcnt(2)
D2 87,54,1024
mav 22,132,58
R 74,33984
mav 26,136,58
D2 87,55,2048
mva 30,58,140
mav 22,144,62
D2 87,55,3072
mav 26,148,62
s_add_u32 m0, s50, 8256
R 46,34048
mva 30,62,152
s_waitcnt lgkmcnt(2)
D2 88,54,0
mav 22,156,66
R 50,34112
mav 26,160,66
D2 88,54,1024
mva 30,66,164
v_add_u32 v93, 1, v93
mav 22,168,70
D2 88,55,2048
mav 26,172,70
R 54,34176
mva 30,70,176
s_waitcnt lgkmcnt(2)
D2 88,55,3072
mav 22,180,74
s_add_u32 m0, s50, 16512
R 58,34240
mav 26,184,74
D2 89,54,0
mva 30,74,188
mav 22,192,46
D2 89,54,1024
mav 26,196,46
R 62,34304
mva 30,46,200
s_waitcnt lgkmcnt(2)
D2 89,55,2048
mav 22,204,50
R 66,34368
mav 26,208,50
D2 89,55,3072
mva 30,50,212
s_add_u32 m0, s50, 24768
mav 22,216,54
D2 90,54,0
mav 26,220,54
R 70,34432
mva 30,54,224
s_waitcnt lgkmcnt(2)
D2 90,54,1024
mav 22,228,58
R 74,34496
mav 26,232,58
D2 90,55,2048
mva 30,58,236
mav 22,240,62
D2 90,55,3072
mav 26,244,62
R 46,34560
mva 30,62,248
s_waitcnt lgkmcnt(2)
mav 22,252,66
R 50,34624
mvv 26,128,66
mvv 30,66,132
mvv 22,136,70
R 54,34688
mvv 26,140,70
mvv 30,70,144
s_waitcnt lgkmcnt(2)
mvv 22,148,74
R 58,34752
mvv 26,152,74
mvv 30,74,156
mvv 22,160,46
R 62,34816
mvv 26,164,46
mvv 30,46,168
s_waitcnt lgkmcnt(2)
mvv 22,172,50
R 66,34880
mvv 26,176,50
mvv 30,50,180
mvv 22,184,54
R 70,34944
mvv 26,188,54
mvv 30,54,192
s_waitcnt lgkmcnt(2)
mvv 22,196,58
R 74,35008
mvv 26,200,58
mvv 30,58,204
mvv 22,208,62
R 46,0
mvv 26,212,62
ds_read_b32 v122, v83 offset:0
mvv 30,62,216
s_waitcnt lgkmcnt(3)
ds_read_b64 v[118:119], v85 offset:0
mvv 22,220,66
R 50,64
mvv 26,224,66
ds_read_b128 v[104:107], v86 offset:0
mvv 30,66,228
ds_read_b128 v[108:111], v86 offset:16
mvv 22,232,70
R 54,128
mvv 26,236,70
mvv 30,70,240
s_waitcnt lgkmcnt(4)
v_cmp_gt_i32 vcc, v93, v122
mvv 22,244,74
R 58,192
mvv 26,248,74
mvv 30,74,252
s_cbranch_vccnz CQB0_3
BQB0_3:
v_mfma_f32_16x16x32_bf16 v[34:37], a[0:3], v[46:49], 0
R 62,256
v_mul_f32 v120, v119, v96
v_mfma_f32_16x16x32_bf16 v[38:41], a[4:7], v[46:49], 0
s_waitcnt lgkmcnt(2)
v_fma_f32 v22, v22, v118, v120
v_mul_f32 v121, v119, v97
v_mfma_f32_16x16x32_bf16 v[42:45], v[46:49], a[8:11], 0
v_fma_f32 v23, v23, v118, v121
v_mul_f32 v120, v119, v98
mav 34,12,50
R 66,320
v_fma_f32 v24, v24, v118, v120
mav 38,16,50
v_mul_f32 v121, v119, v99
v_fma_f32 v25, v25, v118, v121
mva 42,50,20
v_mul_f32 v120, v119, v100
v_fma_f32 v26, v26, v118, v120
mav 34,24,54
R 70,384
v_mul_f32 v121, v119, v101
mav 38,28,54
v_fma_f32 v27, v27, v118, v121
v_mul_f32 v120, v119, v102
mva 42,54,32
s_waitcnt lgkmcnt(2)
v_fma_f32 v28, v28, v118, v120
v_mul_f32 v121, v119, v103
mav 34,36,58
R 74,448
v_fma_f32 v29, v29, v118, v121
mav 38,40,58
v_cvt_pk_bf16_f32 v112, v22, v23
v_cvt_pk_bf16_f32 v113, v24, v25
mva 42,58,44
v_cvt_pk_bf16_f32 v114, v26, v27
v_cvt_pk_bf16_f32 v115, v28, v29
mav 34,48,62
R 46,512
v_mul_f32 v105, v105, v95
mav 38,52,62
ds_write_b128 v84, v[112:115] offset:0
mva 42,62,56
s_waitcnt lgkmcnt(3)
v_fma_f32 v30, v30, v104, v105
v_mul_f32 v107, v107, v95
mav 34,60,66
R 50,576
v_fma_f32 v31, v31, v106, v107
mav 38,64,66
v_mul_f32 v109, v109, v95
v_fma_f32 v32, v32, v108, v109
mva 42,66,68
v_mul_f32 v111, v111, v95
v_fma_f32 v33, v33, v110, v111
mav 34,72,70
R 54,640
v_cvt_pk_bf16_f32 v78, v30, v31
mav 38,76,70
v_cvt_pk_bf16_f32 v79, v32, v33
mva 42,70,80
s_waitcnt lgkmcnt(2)
ds_write_b64 v84, v[78:79] offset:1024
mav 34,84,74
s_xor_b32 s50, s50, s57
R 58,704
mav 38,88,74
mva 42,74,92
mav 34,96,46
LsQ18:
s_cbranch_execz LsQ22
s_waitcnt vmcnt(17) lgkmcnt(1)
R 62,768
s_mov_b32 m0, 144384
mav 38,100,46
s_barrier
mva 42,46,104
buffer_load_dword v91, s[36:39], 0 offen sc1 lds
mav 34,108,50
s_sub_u32 s54, s54, 0x8000
R 66,832
s_add_u32 m0, s50, 33024
mav 38,112,50
s_cmp_lt_u32 s54, s56
s_cselect_b64 s[44:45], s[40:41], s[44:45]
mva 42,50,116
mav 34,120,54
D2 87,54,0
mav 38,124,54
s_add_u32 s55, s54, 14336
R 70,896
mva 42,54,128
s_waitcnt lgkmcnt(2)
D2 87,54,1024
mav 34,132,58
R 74,960
mav 38,136,58
D2 87,55,2048
mva 42,58,140
mav 34,144,62
D2 87,55,3072
mav 38,148,62
s_add_u32 m0, s50, 41280
R 46,1024
mva 42,62,152
s_waitcnt lgkmcnt(2)
D2 88,54,0
mav 34,156,66
R 50,1088
mav 38,160,66
D2 88,54,1024
mva 42,66,164
v_add_u32 v93, 1, v93
mav 34,168,70
D2 88,55,2048
mav 38,172,70
R 54,1152
mva 42,70,176
s_waitcnt lgkmcnt(2)
D2 88,55,3072
mav 34,180,74
s_add_u32 m0, s50, 49536
R 58,1216
mav 38,184,74
D2 89,54,0
mva 42,74,188
mav 34,192,46
D2 89,54,1024
mav 38,196,46
R 62,1280
mva 42,46,200
s_waitcnt lgkmcnt(2)
D2 89,55,2048
mav 34,204,50
R 66,1344
mav 38,208,50
D2 89,55,3072
mva 42,50,212
s_add_u32 m0, s50, 57792
mav 34,216,54
D2 90,54,0
mav 38,220,54
R 70,1408
mva 42,54,224
s_waitcnt lgkmcnt(2)
D2 90,54,1024
mav 34,228,58
R 74,1472
mav 38,232,58
D2 90,55,2048
mva 42,58,236
mav 34,240,62
D2 90,55,3072
mav 38,244,62
R 46,1536
mva 42,62,248
s_waitcnt lgkmcnt(2)
mav 34,252,66
R 50,1600
mvv 38,128,66
mvv 42,66,132
mvv 34,136,70
R 54,1664
mvv 38,140,70
mvv 42,70,144
s_waitcnt lgkmcnt(2)
mvv 34,148,74
R 58,1728
mvv 38,152,74
mvv 42,74,156
mvv 34,160,46
R 62,1792
mvv 38,164,46
mvv 42,46,168
s_waitcnt lgkmcnt(2)
mvv 34,172,50
R 66,1856
mvv 38,176,50
s_mov_b32 s60, -27
.p2align 6
LQB0_4:
mvv 42,50,180
mvv 34,184,54
R 70,1920
mvv 38,188,54
mvv 42,54,192
s_waitcnt lgkmcnt(2)
mvv 34,196,58
R 74,1984
v_xor_b32 v80, v81, v80
mvv 38,200,58
mvv 42,58,204
mvv 34,208,62
R 46,33024
mvv 38,212,62
ds_read_b32 v122, v83 offset:0
mvv 42,62,216
s_waitcnt lgkmcnt(3)
ds_read_b64 v[118:119], v85 offset:128
mvv 34,220,66
R 50,33088
mvv 38,224,66
ds_read_b128 v[104:107], v86 offset:128
mvv 42,66,228
ds_read_b128 v[108:111], v86 offset:144
mvv 34,232,70
R 54,33152
mvv 38,236,70
mvv 42,70,240
s_waitcnt lgkmcnt(4)
v_cmp_gt_i32 vcc, v93, v122
mvv 34,244,74
R 58,33216
mvv 38,248,74
mvv 42,74,252
s_cbranch_vccnz CQB0_5
BQB0_5:
v_mfma_f32_16x16x32_bf16 v[22:25], a[0:3], v[46:49], 0
R 62,33280
v_mul_f32 v120, v119, v96
v_mfma_f32_16x16x32_bf16 v[26:29], a[4:7], v[46:49], 0
s_waitcnt lgkmcnt(2)
v_fma_f32 v34, v34, v118, v120
v_mul_f32 v121, v119, v97
v_mfma_f32_16x16x32_bf16 v[30:33], v[46:49], a[8:11], 0
v_fma_f32 v35, v35, v118, v121
v_mul_f32 v120, v119, v98
mav 22,12,50
R 66,33344
v_fma_f32 v36, v36, v118, v120
mav 26,16,50
v_mul_f32 v121, v119, v99
v_fma_f32 v37, v37, v118, v121
mva 30,50,20
v_mul_f32 v120, v119, v100
v_fma_f32 v38, v38, v118, v120
mav 22,24,54
R 70,33408
v_mul_f32 v121, v119, v101
mav 26,28,54
v_fma_f32 v39, v39, v118, v121
v_mul_f32 v120, v119, v102
mva 30,54,32
s_waitcnt lgkmcnt(2)
v_fma_f32 v40, v40, v118, v120
v_mul_f32 v121, v119, v103
mav 22,36,58
R 74,33472
v_fma_f32 v41, v41, v118, v121
mav 26,40,58
v_cvt_pk_bf16_f32 v112, v34, v35
v_cvt_pk_bf16_f32 v113, v36, v37
mva 30,58,44
v_cvt_pk_bf16_f32 v114, v38, v39
v_cvt_pk_bf16_f32 v115, v40, v41
mav 22,48,62
R 46,33536
v_mul_f32 v105, v105, v95
mav 26,52,62
ds_write_b128 v84, v[112:115] offset:4096
mva 30,62,56
s_waitcnt lgkmcnt(3)
v_fma_f32 v42, v42, v104, v105
v_mul_f32 v107, v107, v95
mav 22,60,66
R 50,33600
v_fma_f32 v43, v43, v106, v107
mav 26,64,66
v_mul_f32 v109, v109, v95
v_fma_f32 v44, v44, v108, v109
mva 30,66,68
v_mul_f32 v111, v111, v95
v_fma_f32 v45, v45, v110, v111
mav 22,72,70
R 54,33664
v_cvt_pk_bf16_f32 v78, v42, v43
mav 26,76,70
v_cvt_pk_bf16_f32 v79, v44, v45
mva 30,70,80
s_waitcnt lgkmcnt(2)
ds_write_b64 v84, v[78:79] offset:5120
mav 22,84,74
R 58,33728
mav 26,88,74
mva 30,74,92
mav 22,96,46
s_waitcnt vmcnt(17) lgkmcnt(1)
R 62,33792
s_mov_b32 m0, 144384
mav 26,100,46
s_barrier
mva 30,46,104
buffer_load_dword v91, s[36:39], 0 offen sc1 lds
mav 22,108,50
s_sub_u32 s54, s54, 0x8000
R 66,33856
s_add_u32 m0, s50, 0
mav 26,112,50
mva 30,50,116
mav 22,120,54
D2 87,54,0
mav 26,124,54
s_add_u32 s55, s54, 14336
R 70,33920
mva 30,54,128
s_waitcnt lgkmcnt(2)
D2 87,54,1024
mav 22,132,58
R 74,33984
mav 26,136,58
D2 87,55,2048
mva 30,58,140
mav 22,144,62
D2 87,55,3072
mav 26,148,62
s_add_u32 m0, s50, 8256
R 46,34048
mva 30,62,152
s_waitcnt lgkmcnt(2)
D2 88,54,0
mav 22,156,66
R 50,34112
mav 26,160,66
D2 88,54,1024
mva 30,66,164
v_add_u32 v93, 1, v93
mav 22,168,70
D2 88,55,2048
mav 26,172,70
R 54,34176
mva 30,70,176
s_waitcnt lgkmcnt(2)
D2 88,55,3072
mav 22,180,74
s_add_u32 m0, s50, 16512
R 58,34240
mav 26,184,74
D2 89,54,0
mva 30,74,188
mav 22,192,46
D2 89,54,1024
mav 26,196,46
R 62,34304
mva 30,46,200
s_waitcnt lgkmcnt(2)
D2 89,55,2048
mav 22,204,50
R 66,34368
mav 26,208,50
D2 89,55,3072
mva 30,50,212
s_add_u32 m0, s50, 24768
mav 22,216,54
D2 90,54,0
mav 26,220,54
R 70,34432
mva 30,54,224
s_waitcnt lgkmcnt(2)
D2 90,54,1024
mav 22,228,58
R 74,34496
mav 26,232,58
D2 90,55,2048
mva 30,58,236
mav 22,240,62
D2 90,55,3072
mav 26,244,62
R 46,34560
mva 30,62,248
s_waitcnt lgkmcnt(2)
mav 22,252,66
R 50,34624
mvv 26,128,66
mvv 30,66,132
mvv 22,136,70
R 54,34688
mvv 26,140,70
mvv 30,70,144
s_waitcnt lgkmcnt(2)
mvv 22,148,74
R 58,34752
mvv 26,152,74
mvv 30,74,156
mvv 22,160,46
R 62,34816
mvv 26,164,46
mvv 30,46,168
s_waitcnt lgkmcnt(2)
mvv 22,172,50
R 66,34880
mvv 26,176,50
mvv 30,50,180
mvv 22,184,54
R 70,34944
mvv 26,188,54
mvv 30,54,192
s_waitcnt lgkmcnt(2)
mvv 22,196,58
R 74,35008
mvv 26,200,58
mvv 30,58,204
mvv 22,208,62
R 46,0
mvv 26,212,62
ds_read_b32 v122, v83 offset:0
mvv 30,62,216
s_waitcnt lgkmcnt(3)
ds_read_b64 v[118:119], v85 offset:0
mvv 22,220,66
R 50,64
mvv 26,224,66
ds_read_b128 v[104:107], v86 offset:0
mvv 30,66,228
ds_read_b128 v[108:111], v86 offset:16
mvv 22,232,70
R 54,128
mvv 26,236,70
mvv 30,70,240
s_waitcnt lgkmcnt(4)
v_cmp_gt_i32 vcc, v93, v122
mvv 22,244,74
R 58,192
mvv 26,248,74
mvv 30,74,252
s_cbranch_vccnz CQB0_6
BQB0_6:
v_mfma_f32_16x16x32_bf16 v[34:37], a[0:3], v[46:49], 0
R 62,256
v_mul_f32 v120, v119, v96
v_mfma_f32_16x16x32_bf16 v[38:41], a[4:7], v[46:49], 0
s_waitcnt lgkmcnt(2)
v_fma_f32 v22, v22, v118, v120
v_mul_f32 v121, v119, v97
v_mfma_f32_16x16x32_bf16 v[42:45], v[46:49], a[8:11], 0
v_fma_f32 v23, v23, v118, v121
v_mul_f32 v120, v119, v98
mav 34,12,50
R 66,320
v_fma_f32 v24, v24, v118, v120
mav 38,16,50
v_mul_f32 v121, v119, v99
v_fma_f32 v25, v25, v118, v121
mva 42,50,20
v_mul_f32 v120, v119, v100
v_fma_f32 v26, v26, v118, v120
mav 34,24,54
R 70,384
v_mul_f32 v121, v119, v101
mav 38,28,54
v_fma_f32 v27, v27, v118, v121
v_mul_f32 v120, v119, v102
mva 42,54,32
s_waitcnt lgkmcnt(2)
v_fma_f32 v28, v28, v118, v120
v_mul_f32 v121, v119, v103
mav 34,36,58
R 74,448
v_fma_f32 v29, v29, v118, v121
mav 38,40,58
v_cvt_pk_bf16_f32 v112, v22, v23
v_cvt_pk_bf16_f32 v113, v24, v25
mva 42,58,44
v_cvt_pk_bf16_f32 v114, v26, v27
v_cvt_pk_bf16_f32 v115, v28, v29
mav 34,48,62
R 46,512
v_mul_f32 v105, v105, v95
mav 38,52,62
ds_write_b128 v84, v[112:115] offset:0
mva 42,62,56
s_waitcnt lgkmcnt(3)
v_fma_f32 v30, v30, v104, v105
v_mul_f32 v107, v107, v95
mav 34,60,66
R 50,576
v_fma_f32 v31, v31, v106, v107
mav 38,64,66
v_mul_f32 v109, v109, v95
v_fma_f32 v32, v32, v108, v109
mva 42,66,68
v_mul_f32 v111, v111, v95
v_fma_f32 v33, v33, v110, v111
mav 34,72,70
R 54,640
v_cvt_pk_bf16_f32 v78, v30, v31
mav 38,76,70
v_cvt_pk_bf16_f32 v79, v32, v33
mva 42,70,80
s_waitcnt lgkmcnt(2)
ds_write_b64 v84, v[78:79] offset:1024
mav 34,84,74
s_xor_b32 s50, s50, s57
R 58,704
mav 38,88,74
mva 42,74,92
mav 34,96,46
s_waitcnt vmcnt(17) lgkmcnt(1)
R 62,768
s_mov_b32 m0, 144384
mav 38,100,46
s_barrier
mva 42,46,104
buffer_load_dword v91, s[36:39], 0 offen sc1 lds
mav 34,108,50
s_sub_u32 s54, s54, 0x8000
R 66,832
s_add_u32 m0, s50, 33024
mav 38,112,50
s_cmp_lt_u32 s54, s56
s_cselect_b64 s[44:45], s[40:41], s[44:45]
mva 42,50,116
mav 34,120,54
D2 87,54,0
mav 38,124,54
s_add_u32 s55, s54, 14336
R 70,896
mva 42,54,128
s_waitcnt lgkmcnt(2)
D2 87,54,1024
mav 34,132,58
R 74,960
mav 38,136,58
D2 87,55,2048
mva 42,58,140
mav 34,144,62
D2 87,55,3072
mav 38,148,62
s_add_u32 m0, s50, 41280
R 46,1024
mva 42,62,152
s_waitcnt lgkmcnt(2)
D2 88,54,0
mav 34,156,66
R 50,1088
mav 38,160,66
D2 88,54,1024
mva 42,66,164
v_add_u32 v93, 1, v93
mav 34,168,70
D2 88,55,2048
mav 38,172,70
R 54,1152
mva 42,70,176
s_waitcnt lgkmcnt(2)
D2 88,55,3072
mav 34,180,74
s_add_u32 m0, s50, 49536
R 58,1216
mav 38,184,74
D2 89,54,0
mva 42,74,188
mav 34,192,46
D2 89,54,1024
mav 38,196,46
R 62,1280
mva 42,46,200
s_waitcnt lgkmcnt(2)
D2 89,55,2048
mav 34,204,50
R 66,1344
mav 38,208,50
D2 89,55,3072
mva 42,50,212
s_add_u32 m0, s50, 57792
mav 34,216,54
D2 90,54,0
mav 38,220,54
R 70,1408
mva 42,54,224
s_waitcnt lgkmcnt(2)
D2 90,54,1024
mav 34,228,58
R 74,1472
mav 38,232,58
D2 90,55,2048
mva 42,58,236
mav 34,240,62
D2 90,55,3072
mav 38,244,62
R 46,1536
mva 42,62,248
s_waitcnt lgkmcnt(2)
mav 34,252,66
R 50,1600
mvv 38,128,66
mvv 42,66,132
mvv 34,136,70
R 54,1664
mvv 38,140,70
mvv 42,70,144
s_waitcnt lgkmcnt(2)
mvv 34,148,74
R 58,1728
mvv 38,152,74
mvv 42,74,156
mvv 34,160,46
R 62,1792
mvv 38,164,46
mvv 42,46,168
s_waitcnt lgkmcnt(2)
mvv 34,172,50
s_add_u32 s60, s60, 1
R 66,1856
mvv 38,176,50
s_cbranch_scc0 LQB0_4
mvv 42,50,180
mvv 34,184,54
R 70,1920
mvv 38,188,54
mvv 42,54,192
s_waitcnt lgkmcnt(2)
mvv 34,196,58
R 74,1984
v_xor_b32 v80, v81, v80
mvv 38,200,58
mvv 42,58,204
mvv 34,208,62
R 46,33024
mvv 38,212,62
ds_read_b32 v122, v83 offset:0
mvv 42,62,216
s_waitcnt lgkmcnt(3)
ds_read_b64 v[118:119], v85 offset:128
mvv 34,220,66
R 50,33088
mvv 38,224,66
ds_read_b128 v[104:107], v86 offset:128
mvv 42,66,228
LsQ19:
s_cbranch_execz LsQ23
ds_read_b128 v[108:111], v86 offset:144
mvv 34,232,70
R 54,33152
mvv 38,236,70
mvv 42,70,240
s_waitcnt lgkmcnt(4)
v_cmp_gt_i32 vcc, v93, v122
mvv 34,244,74
R 58,33216
mvv 38,248,74
mvv 42,74,252
s_cbranch_vccnz CQB0_7
BQB0_7:
v_mfma_f32_16x16x32_bf16 v[22:25], a[0:3], v[46:49], 0
R 62,33280
v_mul_f32 v120, v119, v96
v_mfma_f32_16x16x32_bf16 v[26:29], a[4:7], v[46:49], 0
s_waitcnt lgkmcnt(2)
v_fma_f32 v34, v34, v118, v120
v_mul_f32 v121, v119, v97
v_mfma_f32_16x16x32_bf16 v[30:33], v[46:49], a[8:11], 0
v_fma_f32 v35, v35, v118, v121
v_mul_f32 v120, v119, v98
mav 22,12,50
R 66,33344
v_fma_f32 v36, v36, v118, v120
mav 26,16,50
v_mul_f32 v121, v119, v99
v_fma_f32 v37, v37, v118, v121
mva 30,50,20
v_mul_f32 v120, v119, v100
v_fma_f32 v38, v38, v118, v120
mav 22,24,54
R 70,33408
v_mul_f32 v121, v119, v101
mav 26,28,54
v_fma_f32 v39, v39, v118, v121
v_mul_f32 v120, v119, v102
mva 30,54,32
s_waitcnt lgkmcnt(2)
v_fma_f32 v40, v40, v118, v120
v_mul_f32 v121, v119, v103
mav 22,36,58
R 74,33472
v_fma_f32 v41, v41, v118, v121
mav 26,40,58
v_cvt_pk_bf16_f32 v112, v34, v35
v_cvt_pk_bf16_f32 v113, v36, v37
mva 30,58,44
v_cvt_pk_bf16_f32 v114, v38, v39
v_cvt_pk_bf16_f32 v115, v40, v41
mav 22,48,62
R 46,33536
v_mul_f32 v105, v105, v95
mav 26,52,62
ds_write_b128 v84, v[112:115] offset:4096
mva 30,62,56
s_waitcnt lgkmcnt(3)
v_fma_f32 v42, v42, v104, v105
v_mul_f32 v107, v107, v95
mav 22,60,66
R 50,33600
v_fma_f32 v43, v43, v106, v107
mav 26,64,66
v_mul_f32 v109, v109, v95
v_fma_f32 v44, v44, v108, v109
mva 30,66,68
v_mul_f32 v111, v111, v95
v_fma_f32 v45, v45, v110, v111
mav 22,72,70
R 54,33664
v_cvt_pk_bf16_f32 v78, v42, v43
mav 26,76,70
v_cvt_pk_bf16_f32 v79, v44, v45
mva 30,70,80
s_waitcnt lgkmcnt(2)
ds_write_b64 v84, v[78:79] offset:5120
mav 22,84,74
R 58,33728
mav 26,88,74
mva 30,74,92
mav 22,96,46
s_waitcnt vmcnt(17) lgkmcnt(1)
R 62,33792
s_mov_b32 m0, 144384
mav 26,100,46
s_barrier
mva 30,46,104
buffer_load_dword v91, s[36:39], 0 offen sc1 lds
mav 22,108,50
s_sub_u32 s54, s54, 0x8000
R 66,33856
s_add_u32 m0, s50, 0
mav 26,112,50
mva 30,50,116
mav 22,120,54
D2 87,54,0
mav 26,124,54
s_add_u32 s55, s54, 14336
R 70,33920
mva 30,54,128
s_waitcnt lgkmcnt(2)
D2 87,54,1024
mav 22,132,58
R 74,33984
mav 26,136,58
D2 87,55,2048
mva 30,58,140
mav 22,144,62
D2 87,55,3072
mav 26,148,62
s_add_u32 m0, s50, 8256
R 46,34048
mva 30,62,152
s_waitcnt lgkmcnt(2)
D2 88,54,0
mav 22,156,66
R 50,34112
mav 26,160,66
D2 88,54,1024
mva 30,66,164
v_add_u32 v93, 1, v93
mav 22,168,70
D2 88,55,2048
mav 26,172,70
R 54,34176
mva 30,70,176
s_waitcnt lgkmcnt(2)
D2 88,55,3072
mav 22,180,74
s_add_u32 m0, s50, 16512
R 58,34240
mav 26,184,74
D2 89,54,0
mva 30,74,188
mav 22,192,46
D2 89,54,1024
mav 26,196,46
R 62,34304
mva 30,46,200
s_waitcnt lgkmcnt(2)
D2 89,55,2048
mav 22,204,50
R 66,34368
mav 26,208,50
D2 89,55,3072
mva 30,50,212
s_add_u32 m0, s50, 24768
mav 22,216,54
D2 90,54,0
mav 26,220,54
R 70,34432
mva 30,54,224
s_waitcnt lgkmcnt(2)
D2 90,54,1024
mav 22,228,58
R 74,34496
mav 26,232,58
D2 90,55,2048
mva 30,58,236
mav 22,240,62
D2 90,55,3072
mav 26,244,62
R 46,34560
mva 30,62,248
s_waitcnt lgkmcnt(2)
mav 22,252,66
R 50,34624
mvv 26,128,66
mvv 30,66,132
mvv 22,136,70
R 54,34688
mvv 26,140,70
mvv 30,70,144
s_waitcnt lgkmcnt(2)
mvv 22,148,74
R 58,34752
mvv 26,152,74
mvv 30,74,156
mvv 22,160,46
R 62,34816
mvv 26,164,46
mvv 30,46,168
s_waitcnt lgkmcnt(2)
mvv 22,172,50
R 66,34880
mvv 26,176,50
mvv 30,50,180
mvv 22,184,54
R 70,34944
mvv 26,188,54
mvv 30,54,192
s_waitcnt lgkmcnt(2)
mvv 22,196,58
R 74,35008
mvv 26,200,58
mvv 30,58,204
mvv 22,208,62
R 46,0
mvv 26,212,62
ds_read_b32 v122, v83 offset:0
mvv 30,62,216
s_waitcnt lgkmcnt(3)
ds_read_b64 v[118:119], v85 offset:0
mvv 22,220,66
R 50,64
mvv 26,224,66
ds_read_b128 v[104:107], v86 offset:0
mvv 30,66,228
ds_read_b128 v[108:111], v86 offset:16
mvv 22,232,70
R 54,128
mvv 26,236,70
mvv 30,70,240
s_waitcnt lgkmcnt(4)
v_cmp_gt_i32 vcc, v93, v122
mvv 22,244,74
R 58,192
mvv 26,248,74
mvv 30,74,252
s_cbranch_vccnz CQB0_8
BQB0_8:
v_mfma_f32_16x16x32_bf16 v[34:37], a[0:3], v[46:49], 0
R 62,256
v_mul_f32 v120, v119, v96
v_mfma_f32_16x16x32_bf16 v[38:41], a[4:7], v[46:49], 0
s_waitcnt lgkmcnt(2)
v_fma_f32 v22, v22, v118, v120
v_mul_f32 v121, v119, v97
v_mfma_f32_16x16x32_bf16 v[42:45], v[46:49], a[8:11], 0
v_fma_f32 v23, v23, v118, v121
v_mul_f32 v120, v119, v98
mav 34,12,50
R 66,320
v_fma_f32 v24, v24, v118, v120
mav 38,16,50
v_mul_f32 v121, v119, v99
v_fma_f32 v25, v25, v118, v121
mva 42,50,20
v_mul_f32 v120, v119, v100
v_fma_f32 v26, v26, v118, v120
mav 34,24,54
R 70,384
v_mul_f32 v121, v119, v101
mav 38,28,54
v_fma_f32 v27, v27, v118, v121
v_mul_f32 v120, v119, v102
mva 42,54,32
s_waitcnt lgkmcnt(2)
v_fma_f32 v28, v28, v118, v120
v_mul_f32 v121, v119, v103
mav 34,36,58
R 74,448
v_fma_f32 v29, v29, v118, v121
mav 38,40,58
v_cvt_pk_bf16_f32 v112, v22, v23
v_cvt_pk_bf16_f32 v113, v24, v25
mva 42,58,44
v_cvt_pk_bf16_f32 v114, v26, v27
v_cvt_pk_bf16_f32 v115, v28, v29
mav 34,48,62
R 46,512
v_mul_f32 v105, v105, v95
mav 38,52,62
ds_write_b128 v84, v[112:115] offset:0
mva 42,62,56
s_waitcnt lgkmcnt(3)
v_fma_f32 v30, v30, v104, v105
v_mul_f32 v107, v107, v95
mav 34,60,66
R 50,576
v_fma_f32 v31, v31, v106, v107
mav 38,64,66
v_mul_f32 v109, v109, v95
v_fma_f32 v32, v32, v108, v109
mva 42,66,68
v_mul_f32 v111, v111, v95
v_fma_f32 v33, v33, v110, v111
mav 34,72,70
R 54,640
v_cvt_pk_bf16_f32 v78, v30, v31
mav 38,76,70
v_cvt_pk_bf16_f32 v79, v32, v33
mva 42,70,80
s_waitcnt lgkmcnt(2)
ds_write_b64 v84, v[78:79] offset:1024
mav 34,84,74
R 58,704
mav 38,88,74
mva 42,74,92
mav 34,96,46
s_waitcnt vmcnt(17) lgkmcnt(1)
R 62,768
mav 38,100,46
s_barrier
mva 42,46,104
mav 34,108,50
R 66,832
mav 38,112,50
mva 42,50,116
mav 34,120,54
R 70,896
mav 38,124,54
mva 42,54,128
s_waitcnt lgkmcnt(2)
mav 34,132,58
R 74,960
mav 38,136,58
mva 42,58,140
mav 34,144,62
R 46,1024
mav 38,148,62
mva 42,62,152
s_waitcnt lgkmcnt(2)
mav 34,156,66
R 50,1088
mav 38,160,66
mva 42,66,164
mav 34,168,70
R 54,1152
mav 38,172,70
mva 42,70,176
s_waitcnt lgkmcnt(2)
mav 34,180,74
R 58,1216
mav 38,184,74
mva 42,74,188
mav 34,192,46
R 62,1280
mav 38,196,46
mva 42,46,200
s_waitcnt lgkmcnt(2)
mav 34,204,50
R 66,1344
mav 38,208,50
mva 42,50,212
mav 34,216,54
R 70,1408
mav 38,220,54
mva 42,54,224
s_waitcnt lgkmcnt(2)
mav 34,228,58
R 74,1472
mav 38,232,58
mva 42,58,236
mav 34,240,62
R 46,1536
mav 38,244,62
mva 42,62,248
s_waitcnt lgkmcnt(2)
mav 34,252,66
R 50,1600
mvv 38,128,66
mvv 42,66,132
mvv 34,136,70
R 54,1664
mvv 38,140,70
mvv 42,70,144
s_waitcnt lgkmcnt(2)
mvv 34,148,74
R 58,1728
mvv 38,152,74
mvv 42,74,156
mvv 34,160,46
R 62,1792
mvv 38,164,46
mvv 42,46,168
s_waitcnt lgkmcnt(2)
mvv 34,172,50
R 66,1856
mvv 38,176,50
mvv 42,50,180
mvv 34,184,54
R 70,1920
mvv 38,188,54
mvv 42,54,192
s_waitcnt lgkmcnt(2)
mvv 34,196,58
R 74,1984
v_xor_b32 v80, v81, v80
mvv 38,200,58
mvv 42,58,204
mvv 34,208,62
R 46,33024
mvv 38,212,62
ds_read_b64 v[118:119], v85 offset:128
mvv 42,62,216
s_waitcnt lgkmcnt(3)
ds_read_b128 v[104:107], v86 offset:128
mvv 34,220,66
R 50,33088
mvv 38,224,66
ds_read_b128 v[108:111], v86 offset:144
mvv 42,66,228
mvv 34,232,70
R 54,33152
mvv 38,236,70
mvv 42,70,240
s_waitcnt lgkmcnt(3)
mvv 34,244,74
R 58,33216
mvv 38,248,74
mvv 42,74,252
v_mfma_f32_16x16x32_bf16 v[22:25], a[0:3], v[46:49], 0
R 62,33280
v_mul_f32 v120, v119, v96
v_mfma_f32_16x16x32_bf16 v[26:29], a[4:7], v[46:49], 0
s_waitcnt lgkmcnt(2)
v_fma_f32 v34, v34, v118, v120
v_mul_f32 v121, v119, v97
v_mfma_f32_16x16x32_bf16 v[30:33], v[46:49], a[8:11], 0
v_fma_f32 v35, v35, v118, v121
v_mul_f32 v120, v119, v98
mav 22,12,50
R 66,33344
v_fma_f32 v36, v36, v118, v120
mav 26,16,50
v_mul_f32 v121, v119, v99
v_fma_f32 v37, v37, v118, v121
mva 30,50,20
v_mul_f32 v120, v119, v100
v_fma_f32 v38, v38, v118, v120
mav 22,24,54
R 70,33408
v_mul_f32 v121, v119, v101
mav 26,28,54
v_fma_f32 v39, v39, v118, v121
v_mul_f32 v120, v119, v102
mva 30,54,32
s_waitcnt lgkmcnt(2)
v_fma_f32 v40, v40, v118, v120
v_mul_f32 v121, v119, v103
mav 22,36,58
R 74,33472
v_fma_f32 v41, v41, v118, v121
mav 26,40,58
v_cvt_pk_bf16_f32 v112, v34, v35
v_cvt_pk_bf16_f32 v113, v36, v37
mva 30,58,44
v_cvt_pk_bf16_f32 v114, v38, v39
v_cvt_pk_bf16_f32 v115, v40, v41
mav 22,48,62
R 46,33536
v_mul_f32 v105, v105, v95
mav 26,52,62
ds_write_b128 v84, v[112:115] offset:4096
mva 30,62,56
s_waitcnt lgkmcnt(3)
v_fma_f32 v42, v42, v104, v105
v_mul_f32 v107, v107, v95
mav 22,60,66
R 50,33600
v_fma_f32 v43, v43, v106, v107
mav 26,64,66
v_mul_f32 v109, v109, v95
v_fma_f32 v44, v44, v108, v109
mva 30,66,68
v_mul_f32 v111, v111, v95
v_fma_f32 v45, v45, v110, v111
mav 22,72,70
R 54,33664
v_cvt_pk_bf16_f32 v78, v42, v43
mav 26,76,70
v_cvt_pk_bf16_f32 v79, v44, v45
mva 30,70,80
s_waitcnt lgkmcnt(2)
ds_write_b64 v84, v[78:79] offset:5120
mav 22,84,74
R 58,33728
mav 26,88,74
mva 30,74,92
mav 22,96,46
s_waitcnt vmcnt(0) lgkmcnt(1)
R 62,33792
mav 26,100,46
s_barrier
mva 30,46,104
mav 22,108,50
R 66,33856
mav 26,112,50
mva 30,50,116
mav 22,120,54
R 70,33920
mav 26,124,54
mva 30,54,128
s_waitcnt lgkmcnt(2)
mav 22,132,58
R 74,33984
mav 26,136,58
mva 30,58,140
mav 22,144,62
R 46,34048
mav 26,148,62
mva 30,62,152
s_waitcnt lgkmcnt(2)
mav 22,156,66
R 50,34112
mav 26,160,66
mva 30,66,164
mav 22,168,70
R 54,34176
mav 26,172,70
mva 30,70,176
s_waitcnt lgkmcnt(2)
mav 22,180,74
R 58,34240
mav 26,184,74
mva 30,74,188
mav 22,192,46
R 62,34304
mav 26,196,46
mva 30,46,200
s_waitcnt lgkmcnt(2)
mav 22,204,50
R 66,34368
mav 26,208,50
mva 30,50,212
mav 22,216,54
R 70,34432
mav 26,220,54
mva 30,54,224
s_waitcnt lgkmcnt(2)
mav 22,228,58
R 74,34496
mav 26,232,58
mva 30,58,236
mav 22,240,62
R 46,34560
mav 26,244,62
mva 30,62,248
s_waitcnt lgkmcnt(2)
mav 22,252,66
R 50,34624
mvv 26,128,66
mvv 30,66,132
mvv 22,136,70
R 54,34688
mvv 26,140,70
mvv 30,70,144
s_waitcnt lgkmcnt(2)
mvv 22,148,74
R 58,34752
mvv 26,152,74
mvv 30,74,156
mvv 22,160,46
R 62,34816
mvv 26,164,46
mvv 30,46,168
s_waitcnt lgkmcnt(2)
mvv 22,172,50
R 66,34880
mvv 26,176,50
mvv 30,50,180
mvv 22,184,54
R 70,34944
mvv 26,188,54
mvv 30,54,192
s_waitcnt lgkmcnt(2)
mvv 22,196,58
R 74,35008
LsQ20:
s_cbranch_execz LsQ24
mvv 26,200,58
mvv 30,58,204
mvv 22,208,62
R 46,0
mvv 26,212,62
ds_read_b64 v[118:119], v85 offset:0
mvv 30,62,216
s_waitcnt lgkmcnt(3)
ds_read_b128 v[104:107], v86 offset:0
mvv 22,220,66
R 50,64
mvv 26,224,66
ds_read_b128 v[108:111], v86 offset:16
mvv 30,66,228
mvv 22,232,70
R 54,128
mvv 26,236,70
mvv 30,70,240
s_waitcnt lgkmcnt(3)
mvv 22,244,74
R 58,192
mvv 26,248,74
mvv 30,74,252
v_mfma_f32_16x16x32_bf16 v[34:37], a[0:3], v[46:49], 0
R 62,256
v_mul_f32 v120, v119, v96
v_mfma_f32_16x16x32_bf16 v[38:41], a[4:7], v[46:49], 0
s_waitcnt lgkmcnt(2)
v_fma_f32 v22, v22, v118, v120
v_mul_f32 v121, v119, v97
v_mfma_f32_16x16x32_bf16 v[42:45], v[46:49], a[8:11], 0
v_fma_f32 v23, v23, v118, v121
v_mul_f32 v120, v119, v98
mav 34,12,50
R 66,320
v_fma_f32 v24, v24, v118, v120
)"
R"(
mav 38,16,50
v_mul_f32 v121, v119, v99
v_fma_f32 v25, v25, v118, v121
mva 42,50,20
v_mul_f32 v120, v119, v100
v_fma_f32 v26, v26, v118, v120
mav 34,24,54
R 70,384
v_mul_f32 v121, v119, v101
mav 38,28,54
v_fma_f32 v27, v27, v118, v121
v_mul_f32 v120, v119, v102
mva 42,54,32
s_waitcnt lgkmcnt(2)
v_fma_f32 v28, v28, v118, v120
v_mul_f32 v121, v119, v103
mav 34,36,58
R 74,448
v_fma_f32 v29, v29, v118, v121
mav 38,40,58
v_cvt_pk_bf16_f32 v112, v22, v23
v_cvt_pk_bf16_f32 v113, v24, v25
mva 42,58,44
v_cvt_pk_bf16_f32 v114, v26, v27
v_cvt_pk_bf16_f32 v115, v28, v29
mav 34,48,62
R 46,512
v_mul_f32 v105, v105, v95
mav 38,52,62
ds_write_b128 v84, v[112:115] offset:0
mva 42,62,56
s_waitcnt lgkmcnt(3)
v_fma_f32 v30, v30, v104, v105
v_mul_f32 v107, v107, v95
mav 34,60,66
R 50,576
v_fma_f32 v31, v31, v106, v107
mav 38,64,66
v_mul_f32 v109, v109, v95
v_fma_f32 v32, v32, v108, v109
mva 42,66,68
v_mul_f32 v111, v111, v95
v_fma_f32 v33, v33, v110, v111
mav 34,72,70
R 54,640
v_cvt_pk_bf16_f32 v78, v30, v31
mav 38,76,70
v_cvt_pk_bf16_f32 v79, v32, v33
mva 42,70,80
s_waitcnt lgkmcnt(2)
ds_write_b64 v84, v[78:79] offset:1024
mav 34,84,74
R 58,704
mav 38,88,74
mva 42,74,92
mav 34,96,46
s_waitcnt lgkmcnt(1)
R 62,768
mav 38,100,46
s_barrier
mva 42,46,104
mav 34,108,50
R 66,832
mav 38,112,50
mva 42,50,116
mav 34,120,54
R 70,896
mav 38,124,54
mva 42,54,128
s_waitcnt lgkmcnt(2)
mav 34,132,58
R 74,960
mav 38,136,58
mva 42,58,140
mav 34,144,62
R 46,1024
mav 38,148,62
mva 42,62,152
s_waitcnt lgkmcnt(2)
mav 34,156,66
R 50,1088
mav 38,160,66
mva 42,66,164
mav 34,168,70
R 54,1152
mav 38,172,70
mva 42,70,176
s_waitcnt lgkmcnt(2)
mav 34,180,74
R 58,1216
mav 38,184,74
mva 42,74,188
mav 34,192,46
R 62,1280
mav 38,196,46
mva 42,46,200
s_waitcnt lgkmcnt(2)
mav 34,204,50
R 66,1344
mav 38,208,50
mva 42,50,212
mav 34,216,54
R 70,1408
mav 38,220,54
mva 42,54,224
s_waitcnt lgkmcnt(2)
mav 34,228,58
R 74,1472
mav 38,232,58
mva 42,58,236
mav 34,240,62
R 46,1536
mav 38,244,62
mva 42,62,248
s_waitcnt lgkmcnt(2)
mav 34,252,66
R 50,1600
mvv 38,128,66
mvv 42,66,132
mvv 34,136,70
R 54,1664
mvv 38,140,70
mvv 42,70,144
s_waitcnt lgkmcnt(2)
mvv 34,148,74
R 58,1728
mvv 38,152,74
mvv 42,74,156
mvv 34,160,46
R 62,1792
mvv 38,164,46
mvv 42,46,168
s_waitcnt lgkmcnt(2)
mvv 34,172,50
R 66,1856
mvv 38,176,50
mvv 42,50,180
mvv 34,184,54
R 70,1920
mvv 38,188,54
mvv 42,54,192
s_waitcnt lgkmcnt(2)
mvv 34,196,58
R 74,1984
mvv 38,200,58
mvv 42,58,204
mvv 34,208,62
mvv 38,212,62
ds_read_b64 v[118:119], v85 offset:128
mvv 42,62,216
s_waitcnt lgkmcnt(2)
ds_read_b128 v[104:107], v86 offset:128
mvv 34,220,66
ds_read_b128 v[108:111], v86 offset:144
mvv 38,224,66
mvv 42,66,228
mvv 34,232,70
mvv 38,236,70
mvv 42,70,240
s_waitcnt lgkmcnt(1)
mvv 34,244,74
mvv 38,248,74
mvv 42,74,252
v_mul_f32 v120, v119, v96
s_nop 4
v_fma_f32 v34, v34, v118, v120
v_mul_f32 v121, v119, v97
v_fma_f32 v35, v35, v118, v121
v_mul_f32 v120, v119, v98
v_fma_f32 v36, v36, v118, v120
v_mul_f32 v121, v119, v99
v_fma_f32 v37, v37, v118, v121
v_mul_f32 v120, v119, v100
v_fma_f32 v38, v38, v118, v120
v_mul_f32 v121, v119, v101
v_fma_f32 v39, v39, v118, v121
v_mul_f32 v120, v119, v102
v_fma_f32 v40, v40, v118, v120
v_mul_f32 v121, v119, v103
v_fma_f32 v41, v41, v118, v121
v_cvt_pk_bf16_f32 v112, v34, v35
v_cvt_pk_bf16_f32 v113, v36, v37
v_cvt_pk_bf16_f32 v114, v38, v39
v_cvt_pk_bf16_f32 v115, v40, v41
ds_write_b128 v84, v[112:115] offset:4096
v_mul_f32 v105, v105, v95
v_fma_f32 v42, v42, v104, v105
v_mul_f32 v107, v107, v95
v_fma_f32 v43, v43, v106, v107
s_waitcnt lgkmcnt(1)
v_mul_f32 v109, v109, v95
v_fma_f32 v44, v44, v108, v109
v_mul_f32 v111, v111, v95
v_fma_f32 v45, v45, v110, v111
v_cvt_pk_bf16_f32 v78, v42, v43
v_cvt_pk_bf16_f32 v79, v44, v45
ds_write_b64 v84, v[78:79] offset:5120
s_waitcnt lgkmcnt(0)
s_barrier
s_endpgm
CQB0_1:
s_mov_b32 s58, m0
s_mov_b32 m0, 144384
DQB0_1:
s_cmp_eq_u32 s61, 0
s_cbranch_scc1 EQB0_1
s_sub_u32 s61, s61, 1
buffer_load_dword v91, s[36:39], 0 offen sc1 lds
s_sleep 1
ds_read_b32 v122, v83
s_waitcnt lgkmcnt(0)
v_cmp_gt_i32 vcc, v93, v122
s_cbranch_vccnz DQB0_1
EQB0_1:
s_mov_b32 m0, s58
s_branch BQB0_1
CQB0_2:
s_mov_b32 s58, m0
s_mov_b32 m0, 144384
DQB0_2:
s_cmp_eq_u32 s61, 0
s_cbranch_scc1 EQB0_2
s_sub_u32 s61, s61, 1
buffer_load_dword v91, s[36:39], 0 offen sc1 lds
s_sleep 1
ds_read_b32 v122, v83
s_waitcnt lgkmcnt(0)
v_cmp_gt_i32 vcc, v93, v122
s_cbranch_vccnz DQB0_2
EQB0_2:
s_mov_b32 m0, s58
s_branch BQB0_2
CQB0_3:
s_mov_b32 s58, m0
s_mov_b32 m0, 144384
DQB0_3:
s_cmp_eq_u32 s61, 0
s_cbranch_scc1 EQB0_3
s_sub_u32 s61, s61, 1
buffer_load_dword v91, s[36:39], 0 offen sc1 lds
s_sleep 1
ds_read_b32 v122, v83
s_waitcnt lgkmcnt(0)
v_cmp_gt_i32 vcc, v93, v122
s_cbranch_vccnz DQB0_3
EQB0_3:
s_mov_b32 m0, s58
s_branch BQB0_3
CQB0_5:
s_mov_b32 s58, m0
s_mov_b32 m0, 144384
DQB0_5:
s_cmp_eq_u32 s61, 0
s_cbranch_scc1 EQB0_5
s_sub_u32 s61, s61, 1
buffer_load_dword v91, s[36:39], 0 offen sc1 lds
s_sleep 1
ds_read_b32 v122, v83
s_waitcnt lgkmcnt(0)
v_cmp_gt_i32 vcc, v93, v122
s_cbranch_vccnz DQB0_5
EQB0_5:
s_mov_b32 m0, s58
s_branch BQB0_5
CQB0_6:
s_mov_b32 s58, m0
s_mov_b32 m0, 144384
DQB0_6:
s_cmp_eq_u32 s61, 0
s_cbranch_scc1 EQB0_6
s_sub_u32 s61, s61, 1
buffer_load_dword v91, s[36:39], 0 offen sc1 lds
s_sleep 1
ds_read_b32 v122, v83
s_waitcnt lgkmcnt(0)
v_cmp_gt_i32 vcc, v93, v122
s_cbranch_vccnz DQB0_6
EQB0_6:
s_mov_b32 m0, s58
s_branch BQB0_6
CQB0_7:
s_mov_b32 s58, m0
s_mov_b32 m0, 144384
DQB0_7:
s_cmp_eq_u32 s61, 0
s_cbranch_scc1 EQB0_7
s_sub_u32 s61, s61, 1
buffer_load_dword v91, s[36:39], 0 offen sc1 lds
s_sleep 1
ds_read_b32 v122, v83
s_waitcnt lgkmcnt(0)
v_cmp_gt_i32 vcc, v93, v122
s_cbranch_vccnz DQB0_7
EQB0_7:
s_mov_b32 m0, s58
s_branch BQB0_7
CQB0_8:
s_mov_b32 s58, m0
s_mov_b32 m0, 144384
DQB0_8:
s_cmp_eq_u32 s61, 0
s_cbranch_scc1 EQB0_8
s_sub_u32 s61, s61, 1
buffer_load_dword v91, s[36:39], 0 offen sc1 lds
s_sleep 1
ds_read_b32 v122, v83
s_waitcnt lgkmcnt(0)
v_cmp_gt_i32 vcc, v93, v122
s_cbranch_vccnz DQB0_8
EQB0_8:
s_mov_b32 m0, s58
s_branch BQB0_8
LvQB1:
LsQ21:
s_cbranch_execz LsQ25
ds_read_b64 v[118:119], v85 offset:0
ds_read_b128 v[104:107], v86 offset:0
ds_read_b128 v[108:111], v86 offset:16
s_add_u32 s49, s49, 313344
s_waitcnt lgkmcnt(0)
v_mul_f32 v120, v119, v96
v_fma_f32 v22, v22, v118, v120
v_mul_f32 v121, v119, v97
v_fma_f32 v23, v23, v118, v121
v_mul_f32 v120, v119, v98
v_fma_f32 v24, v24, v118, v120
v_mul_f32 v121, v119, v99
v_fma_f32 v25, v25, v118, v121
v_mul_f32 v120, v119, v100
v_fma_f32 v26, v26, v118, v120
v_mul_f32 v121, v119, v101
v_fma_f32 v27, v27, v118, v121
v_mul_f32 v120, v119, v102
v_fma_f32 v28, v28, v118, v120
v_mul_f32 v121, v119, v103
v_fma_f32 v29, v29, v118, v121
v_cvt_pk_bf16_f32 v112, v22, v23
v_cvt_pk_bf16_f32 v113, v24, v25
v_cvt_pk_bf16_f32 v114, v26, v27
v_cvt_pk_bf16_f32 v115, v28, v29
ds_write_b128 v84, v[112:115] offset:0
v_mul_f32 v105, v105, v95
v_fma_f32 v30, v30, v104, v105
v_mul_f32 v107, v107, v95
v_fma_f32 v31, v31, v106, v107
v_mul_f32 v109, v109, v95
v_fma_f32 v32, v32, v108, v109
v_mul_f32 v111, v111, v95
v_fma_f32 v33, v33, v110, v111
v_cvt_pk_bf16_f32 v78, v30, v31
v_cvt_pk_bf16_f32 v79, v32, v33
ds_write_b64 v84, v[78:79] offset:1024
s_xor_b32 s50, s50, s57
s_waitcnt vmcnt(17) lgkmcnt(0)
s_add_u32 s49, s49, 313344
s_barrier
R 46,33024
R 50,33088
R 54,33152
R 58,33216
s_waitcnt vmcnt(16)
v_permlane32_swap_b32 v14, v15
v_add_f32 v16, v14, v15
v_add_f32 v17, v14, v15
s_nop 1
v_permlane16_swap_b32 v16, v17
v_add_f32 v14, v16, v17
v_add_f32 v15, v16, v17
s_nop 1
v_permlane32_swap_b32 v14, v15
v_mul_f32 v16, 0x3a800000, v14
v_mul_f32 v17, 0x3a800000, v15
v_fma_f32 v17, -v16, v16, v17
v_max_f32 v17, 0, v17
v_add_f32 v17, 0x3727c5ac, v17
v_rsq_f32 v14, v17
s_nop 0
v_mul_f32_e64 v15, -v16, v14
ds_read_b64 v[118:119], v85 offset:128
ds_read_b128 v[104:107], v86 offset:128
ds_read_b128 v[108:111], v86 offset:144
s_waitcnt lgkmcnt(0)
v_mfma_f32_16x16x32_bf16 v[22:25], a[0:3], v[46:49], 0
R 62,33280
v_mul_f32 v120, v119, v96
v_mfma_f32_16x16x32_bf16 v[26:29], a[4:7], v[46:49], 0
v_fma_f32 v34, v34, v118, v120
v_mul_f32 v121, v119, v97
v_mfma_f32_16x16x32_bf16 v[30:33], v[46:49], a[8:11], 0
v_fma_f32 v35, v35, v118, v121
v_mul_f32 v120, v119, v98
mav 22,12,50
R 66,33344
v_fma_f32 v36, v36, v118, v120
mav 26,16,50
v_mul_f32 v121, v119, v99
v_fma_f32 v37, v37, v118, v121
mva 30,50,20
v_mul_f32 v120, v119, v100
v_fma_f32 v38, v38, v118, v120
mav 22,24,54
R 70,33408
v_mul_f32 v121, v119, v101
mav 26,28,54
v_fma_f32 v39, v39, v118, v121
v_mul_f32 v120, v119, v102
mva 30,54,32
v_fma_f32 v40, v40, v118, v120
v_mul_f32 v121, v119, v103
mav 22,36,58
R 74,33472
v_fma_f32 v41, v41, v118, v121
mav 26,40,58
v_cvt_pk_bf16_f32 v112, v34, v35
v_cvt_pk_bf16_f32 v113, v36, v37
mva 30,58,44
s_waitcnt lgkmcnt(2)
v_cvt_pk_bf16_f32 v114, v38, v39
v_cvt_pk_bf16_f32 v115, v40, v41
mav 22,48,62
R 46,33536
v_mul_f32 v105, v105, v95
mav 26,52,62
ds_write_b128 v84, v[112:115] offset:4096
mva 30,62,56
v_fma_f32 v42, v42, v104, v105
v_mul_f32 v107, v107, v95
mav 22,60,66
R 50,33600
v_fma_f32 v43, v43, v106, v107
mav 26,64,66
v_mul_f32 v109, v109, v95
v_fma_f32 v44, v44, v108, v109
mva 30,66,68
s_waitcnt lgkmcnt(3)
v_mul_f32 v111, v111, v95
v_fma_f32 v45, v45, v110, v111
mav 22,72,70
R 54,33664
v_cvt_pk_bf16_f32 v78, v42, v43
mav 26,76,70
v_cvt_pk_bf16_f32 v79, v44, v45
mva 30,70,80
ds_write_b64 v84, v[78:79] offset:5120
mav 22,84,74
R 58,33728
mav 26,88,74
mva 30,74,92
s_waitcnt lgkmcnt(3)
mav 22,96,46
s_waitcnt vmcnt(0) lgkmcnt(1)
R 62,33792
s_add_u32 s49, s49, 313344
mav 26,100,46
s_barrier
mva 30,46,104
ds_write_b64 v85, v[14:15] offset:128
mav 22,108,50
s_sub_u32 s54, s54, 0x8000
R 66,33856
s_add_u32 m0, s50, 33024
mav 26,112,50
s_cmp_lt_u32 s54, s56
s_cselect_b64 s[44:45], s[40:41], s[44:45]
mva 30,50,116
D2 87,54,0
mav 22,120,54
R 70,33920
mav 26,124,54
D2 87,54,1024
mva 30,54,128
s_waitcnt lgkmcnt(2)
s_add_u32 s55, s54, 14336
mav 22,132,58
D2 87,55,2048
mav 26,136,58
R 74,33984
mva 30,58,140
D2 87,55,3072
mav 22,144,62
s_add_u32 m0, s50, 41280
R 46,34048
mav 26,148,62
D2 88,54,0
mva 30,62,152
s_waitcnt lgkmcnt(2)
mav 22,156,66
D2 88,54,1024
mav 26,160,66
R 50,34112
mva 30,66,164
D2 88,55,2048
mav 22,168,70
R 54,34176
mav 26,172,70
D2 88,55,3072
mva 30,70,176
s_waitcnt lgkmcnt(2)
s_add_u32 m0, s50, 49536
mav 22,180,74
D2 89,54,0
mav 26,184,74
R 58,34240
mva 30,74,188
D2 89,54,1024
mav 22,192,46
R 62,34304
mav 26,196,46
D2 89,55,2048
mva 30,46,200
s_waitcnt lgkmcnt(2)
mav 22,204,50
D2 89,55,3072
mav 26,208,50
s_add_u32 m0, s50, 57792
R 66,34368
mva 30,50,212
D2 90,54,0
mav 22,216,54
R 70,34432
mav 26,220,54
D2 90,54,1024
mva 30,54,224
s_waitcnt lgkmcnt(2)
mav 22,228,58
D2 90,55,2048
mav 26,232,58
R 74,34496
mva 30,58,236
D2 90,55,3072
mav 22,240,62
s_sub_u32 s54, s54, 0x8000
R 46,34560
s_add_u32 m0, s50, 0
mav 26,244,62
mva 30,62,248
s_waitcnt lgkmcnt(2)
mav 22,252,66
D2 87,54,0
mvv 26,128,66
s_add_u32 s55, s54, 14336
R 50,34624
mvv 30,66,132
D2 87,54,1024
mvv 22,136,70
R 54,34688
mvv 26,140,70
D2 87,55,2048
mvv 30,70,144
s_waitcnt lgkmcnt(2)
mvv 22,148,74
D2 87,55,3072
mvv 26,152,74
s_add_u32 m0, s50, 8256
R 58,34752
mvv 30,74,156
D2 88,54,0
mvv 22,160,46
R 62,34816
mvv 26,164,46
D2 88,54,1024
mvv 30,46,168
s_waitcnt lgkmcnt(2)
mvv 22,172,50
D2 88,55,2048
mvv 26,176,50
R 66,34880
mvv 30,50,180
D2 88,55,3072
mvv 22,184,54
s_add_u32 m0, s50, 16512
R 70,34944
mvv 26,188,54
D2 89,54,0
mvv 30,54,192
s_waitcnt lgkmcnt(2)
mvv 22,196,58
D2 89,54,1024
mvv 26,200,58
R 74,35008
mvv 30,58,204
D2 89,55,2048
mvv 22,208,62
R 46,0
mvv 26,212,62
D2 89,55,3072
mvv 30,62,216
s_waitcnt lgkmcnt(2)
LsQ22:
s_cbranch_execz LwQA
ds_read_b64 v[118:119], v85 offset:0
s_add_u32 m0, s50, 24768
mvv 22,220,66
D2 90,54,0
mvv 26,224,66
R 50,64
mvv 30,66,228
D2 90,54,1024
mvv 22,232,70
R 54,128
mvv 26,236,70
D2 90,55,2048
mvv 30,70,240
s_waitcnt lgkmcnt(2)
ds_read_b128 v[104:107], v86 offset:0
mvv 22,244,74
D2 90,55,3072
mvv 26,248,74
R 58,192
mvv 30,74,252
ds_read_b128 v[108:111], v86 offset:16
v_mfma_f32_16x16x32_bf16 v[34:37], a[0:3], v[46:49], 0
R 62,256
v_mul_f32 v120, v119, v96
v_mfma_f32_16x16x32_bf16 v[38:41], a[4:7], v[46:49], 0
v_fma_f32 v22, v22, v118, v120
v_mul_f32 v121, v119, v97
v_mfma_f32_16x16x32_bf16 v[42:45], v[46:49], a[8:11], 0
s_waitcnt lgkmcnt(3)
v_fma_f32 v23, v23, v118, v121
v_mul_f32 v120, v119, v98
mav 34,12,50
R 66,320
v_fma_f32 v24, v24, v118, v120
mav 38,16,50
v_mul_f32 v121, v119, v99
v_fma_f32 v25, v25, v118, v121
mva 42,50,20
v_mul_f32 v120, v119, v100
v_fma_f32 v26, v26, v118, v120
mav 34,24,54
s_waitcnt lgkmcnt(1)
R 70,384
v_mul_f32 v121, v119, v101
mav 38,28,54
v_fma_f32 v27, v27, v118, v121
v_mul_f32 v120, v119, v102
mva 42,54,32
v_fma_f32 v28, v28, v118, v120
v_mul_f32 v121, v119, v103
mav 34,36,58
R 74,448
v_fma_f32 v29, v29, v118, v121
mav 38,40,58
v_cvt_pk_bf16_f32 v112, v22, v23
v_cvt_pk_bf16_f32 v113, v24, v25
mva 42,58,44
v_cvt_pk_bf16_f32 v114, v26, v27
v_cvt_pk_bf16_f32 v115, v28, v29
mav 34,48,62
R 46,512
v_mul_f32 v105, v105, v95
mav 38,52,62
ds_write_b128 v84, v[112:115] offset:0
mva 42,62,56
s_waitcnt lgkmcnt(3)
v_fma_f32 v30, v30, v104, v105
v_mul_f32 v107, v107, v95
mav 34,60,66
R 50,576
v_fma_f32 v31, v31, v106, v107
mav 38,64,66
v_mul_f32 v109, v109, v95
v_fma_f32 v32, v32, v108, v109
mva 42,66,68
v_mul_f32 v111, v111, v95
v_fma_f32 v33, v33, v110, v111
mav 34,72,70
R 54,640
v_cvt_pk_bf16_f32 v78, v30, v31
mav 38,76,70
v_cvt_pk_bf16_f32 v79, v32, v33
mva 42,70,80
s_waitcnt lgkmcnt(2)
ds_write_b64 v84, v[78:79] offset:1024
mav 34,84,74
s_xor_b32 s50, s50, s57
R 58,704
mav 38,88,74
mva 42,74,92
mav 34,96,46
s_waitcnt vmcnt(16) lgkmcnt(1)
R 62,768
s_add_u32 s49, s49, 313344
mav 38,100,46
s_barrier
mva 42,46,104
s_sub_u32 s54, s54, 0x8000
s_cmp_lt_u32 s54, s56
s_cselect_b64 s[44:45], s[40:41], s[44:45]
mav 34,108,50
buffer_load_dwordx2 v[14:15], v94, s[36:39], s49 offen sc1
mav 38,112,50
s_add_u32 m0, s50, 33024
R 66,832
mva 42,50,116
D2 87,54,0
mav 34,120,54
R 70,896
mav 38,124,54
D2 87,54,1024
mva 42,54,128
s_waitcnt lgkmcnt(2)
s_add_u32 s55, s54, 14336
mav 34,132,58
D2 87,55,2048
mav 38,136,58
R 74,960
mva 42,58,140
D2 87,55,3072
mav 34,144,62
s_add_u32 m0, s50, 41280
R 46,1024
mav 38,148,62
D2 88,54,0
mva 42,62,152
s_waitcnt lgkmcnt(2)
mav 34,156,66
D2 88,54,1024
mav 38,160,66
R 50,1088
mva 42,66,164
D2 88,55,2048
mav 34,168,70
R 54,1152
mav 38,172,70
D2 88,55,3072
mva 42,70,176
s_waitcnt lgkmcnt(2)
s_add_u32 m0, s50, 49536
mav 34,180,74
D2 89,54,0
mav 38,184,74
R 58,1216
mva 42,74,188
D2 89,54,1024
mav 34,192,46
R 62,1280
mav 38,196,46
D2 89,55,2048
mva 42,46,200
s_waitcnt lgkmcnt(2)
mav 34,204,50
D2 89,55,3072
mav 38,208,50
s_add_u32 m0, s50, 57792
R 66,1344
mva 42,50,212
D2 90,54,0
mav 34,216,54
R 70,1408
mav 38,220,54
D2 90,54,1024
mva 42,54,224
s_waitcnt lgkmcnt(2)
mav 34,228,58
D2 90,55,2048
mav 38,232,58
R 74,1472
mva 42,58,236
D2 90,55,3072
mav 34,240,62
R 46,1536
mav 38,244,62
mva 42,62,248
s_waitcnt lgkmcnt(2)
mav 34,252,66
R 50,1600
mvv 38,128,66
mvv 42,66,132
mvv 34,136,70
R 54,1664
mvv 38,140,70
mvv 42,70,144
s_waitcnt lgkmcnt(2)
mvv 34,148,74
R 58,1728
mvv 38,152,74
mvv 42,74,156
mvv 34,160,46
R 62,1792
mvv 38,164,46
mvv 42,46,168
s_waitcnt vmcnt(16) lgkmcnt(2)
v_permlane32_swap_b32 v14, v15
mvv 34,172,50
R 66,1856
v_add_f32 v16, v14, v15
mvv 38,176,50
mvv 42,50,180
v_add_f32 v17, v14, v15
mvv 34,184,54
R 70,1920
mvv 38,188,54
v_permlane16_swap_b32 v16, v17
mvv 42,54,192
s_waitcnt lgkmcnt(2)
v_add_f32 v14, v16, v17
v_add_f32 v15, v16, v17
mvv 34,196,58
R 74,1984
v_xor_b32 v80, v81, v80
mvv 38,200,58
v_permlane32_swap_b32 v14, v15
mvv 42,58,204
v_mul_f32 v16, 0x3a800000, v14
v_mul_f32 v17, 0x3a800000, v15
mvv 34,208,62
R 46,33024
v_fma_f32 v17, -v16, v16, v17
mvv 38,212,62
ds_read_b64 v[118:119], v85 offset:128
v_max_f32 v17, 0, v17
mvv 42,62,216
s_waitcnt lgkmcnt(3)
ds_read_b128 v[104:107], v86 offset:128
v_add_f32 v17, 0x3727c5ac, v17
mvv 34,220,66
R 50,33088
mvv 38,224,66
ds_read_b128 v[108:111], v86 offset:144
mvv 42,66,228
v_rsq_f32 v14, v17
mvv 34,232,70
R 54,33152
v_mul_f32_e64 v15, -v16, v14
mvv 38,236,70
mvv 42,70,240
s_waitcnt lgkmcnt(3)
mvv 34,244,74
R 58,33216
mvv 38,248,74
mvv 42,74,252
v_mfma_f32_16x16x32_bf16 v[22:25], a[0:3], v[46:49], 0
R 62,33280
v_mul_f32 v120, v119, v96
v_mfma_f32_16x16x32_bf16 v[26:29], a[4:7], v[46:49], 0
s_waitcnt lgkmcnt(2)
v_fma_f32 v34, v34, v118, v120
v_mul_f32 v121, v119, v97
v_mfma_f32_16x16x32_bf16 v[30:33], v[46:49], a[8:11], 0
v_fma_f32 v35, v35, v118, v121
v_mul_f32 v120, v119, v98
mav 22,12,50
R 66,33344
v_fma_f32 v36, v36, v118, v120
mav 26,16,50
v_mul_f32 v121, v119, v99
v_fma_f32 v37, v37, v118, v121
mva 30,50,20
v_mul_f32 v120, v119, v100
v_fma_f32 v38, v38, v118, v120
mav 22,24,54
R 70,33408
v_mul_f32 v121, v119, v101
mav 26,28,54
v_fma_f32 v39, v39, v118, v121
v_mul_f32 v120, v119, v102
mva 30,54,32
s_waitcnt lgkmcnt(2)
v_fma_f32 v40, v40, v118, v120
v_mul_f32 v121, v119, v103
mav 22,36,58
R 74,33472
v_fma_f32 v41, v41, v118, v121
mav 26,40,58
v_cvt_pk_bf16_f32 v112, v34, v35
v_cvt_pk_bf16_f32 v113, v36, v37
mva 30,58,44
v_cvt_pk_bf16_f32 v114, v38, v39
v_cvt_pk_bf16_f32 v115, v40, v41
mav 22,48,62
R 46,33536
v_mul_f32 v105, v105, v95
mav 26,52,62
ds_write_b128 v84, v[112:115] offset:4096
mva 30,62,56
s_waitcnt lgkmcnt(3)
v_fma_f32 v42, v42, v104, v105
v_mul_f32 v107, v107, v95
mav 22,60,66
R 50,33600
v_fma_f32 v43, v43, v106, v107
mav 26,64,66
v_mul_f32 v109, v109, v95
v_fma_f32 v44, v44, v108, v109
mva 30,66,68
v_mul_f32 v111, v111, v95
v_fma_f32 v45, v45, v110, v111
mav 22,72,70
R 54,33664
v_cvt_pk_bf16_f32 v78, v42, v43
mav 26,76,70
v_cvt_pk_bf16_f32 v79, v44, v45
mva 30,70,80
s_waitcnt lgkmcnt(2)
ds_write_b64 v84, v[78:79] offset:5120
mav 22,84,74
R 58,33728
mav 26,88,74
ds_write_b64 v85, v[14:15] offset:0
mva 30,74,92
mav 22,96,46
s_add_u32 s49, s49, 313344
R 62,33792
mav 26,100,46
s_waitcnt lgkmcnt(1)
mva 30,46,104
s_barrier
mav 22,108,50
buffer_load_dwordx2 v[14:15], v94, s[36:39], s49 offen sc1
mav 26,112,50
s_sub_u32 s54, s54, 0x8000
R 66,33856
s_add_u32 m0, s50, 0
mva 30,50,116
D2 87,54,0
mav 22,120,54
R 70,33920
mav 26,124,54
D2 87,54,1024
mva 30,54,128
s_add_u32 s55, s54, 14336
mav 22,132,58
D2 87,55,2048
mav 26,136,58
R 74,33984
mva 30,58,140
s_waitcnt lgkmcnt(2)
D2 87,55,3072
mav 22,144,62
s_add_u32 m0, s50, 8256
R 46,34048
mav 26,148,62
D2 88,54,0
mva 30,62,152
mav 22,156,66
D2 88,54,1024
mav 26,160,66
R 50,34112
mva 30,66,164
s_waitcnt lgkmcnt(2)
D2 88,55,2048
mav 22,168,70
R 54,34176
mav 26,172,70
D2 88,55,3072
mva 30,70,176
s_add_u32 m0, s50, 16512
mav 22,180,74
D2 89,54,0
mav 26,184,74
R 58,34240
mva 30,74,188
s_waitcnt lgkmcnt(2)
D2 89,54,1024
mav 22,192,46
R 62,34304
mav 26,196,46
D2 89,55,2048
mva 30,46,200
mav 22,204,50
D2 89,55,3072
mav 26,208,50
s_add_u32 m0, s50, 24768
R 66,34368
mva 30,50,212
s_waitcnt lgkmcnt(2)
D2 90,54,0
mav 22,216,54
R 70,34432
mav 26,220,54
D2 90,54,1024
mva 30,54,224
mav 22,228,58
D2 90,55,2048
mav 26,232,58
R 74,34496
mva 30,58,236
s_waitcnt lgkmcnt(2)
D2 90,55,3072
mav 22,240,62
R 46,34560
mav 26,244,62
mva 30,62,248
mav 22,252,66
R 50,34624
mvv 26,128,66
mvv 30,66,132
s_waitcnt lgkmcnt(2)
mvv 22,136,70
R 54,34688
mvv 26,140,70
mvv 30,70,144
mvv 22,148,74
R 58,34752
mvv 26,152,74
mvv 30,74,156
s_waitcnt lgkmcnt(2)
mvv 22,160,46
R 62,34816
mvv 26,164,46
mvv 30,46,168
s_waitcnt vmcnt(16)
v_permlane32_swap_b32 v14, v15
mvv 22,172,50
R 66,34880
v_add_f32 v16, v14, v15
mvv 26,176,50
v_add_f32 v17, v14, v15
mvv 30,50,180
s_waitcnt lgkmcnt(2)
v_permlane16_swap_b32 v16, v17
mvv 22,184,54
R 70,34944
v_add_f32 v14, v16, v17
mvv 26,188,54
v_add_f32 v15, v16, v17
mvv 30,54,192
mvv 22,196,58
R 74,35008
mvv 26,200,58
v_permlane32_swap_b32 v14, v15
mvv 30,58,204
s_waitcnt lgkmcnt(2)
v_mul_f32 v16, 0x3a800000, v14
v_mul_f32 v17, 0x3a800000, v15
mvv 22,208,62
R 46,0
v_fma_f32 v17, -v16, v16, v17
mvv 26,212,62
ds_read_b64 v[118:119], v85 offset:0
v_max_f32 v17, 0, v17
mvv 30,62,216
ds_read_b128 v[104:107], v86 offset:0
v_add_f32 v17, 0x3727c5ac, v17
mvv 22,220,66
R 50,64
mvv 26,224,66
ds_read_b128 v[108:111], v86 offset:16
mvv 30,66,228
s_waitcnt lgkmcnt(5)
v_rsq_f32 v14, v17
mvv 22,232,70
R 54,128
v_mul_f32_e64 v15, -v16, v14
mvv 26,236,70
mvv 30,70,240
mvv 22,244,74
R 58,192
mvv 26,248,74
mvv 30,74,252
s_waitcnt lgkmcnt(2)
v_mfma_f32_16x16x32_bf16 v[34:37], a[0:3], v[46:49], 0
R 62,256
v_mul_f32 v120, v119, v96
v_mfma_f32_16x16x32_bf16 v[38:41], a[4:7], v[46:49], 0
v_fma_f32 v22, v22, v118, v120
v_mul_f32 v121, v119, v97
v_mfma_f32_16x16x32_bf16 v[42:45], v[46:49], a[8:11], 0
v_fma_f32 v23, v23, v118, v121
v_mul_f32 v120, v119, v98
mav 34,12,50
R 66,320
v_fma_f32 v24, v24, v118, v120
mav 38,16,50
v_mul_f32 v121, v119, v99
v_fma_f32 v25, v25, v118, v121
mva 42,50,20
s_waitcnt lgkmcnt(2)
v_mul_f32 v120, v119, v100
v_fma_f32 v26, v26, v118, v120
mav 34,24,54
R 70,384
v_mul_f32 v121, v119, v101
mav 38,28,54
v_fma_f32 v27, v27, v118, v121
v_mul_f32 v120, v119, v102
mva 42,54,32
v_fma_f32 v28, v28, v118, v120
v_mul_f32 v121, v119, v103
mav 34,36,58
R 74,448
v_fma_f32 v29, v29, v118, v121
mav 38,40,58
v_cvt_pk_bf16_f32 v112, v22, v23
v_cvt_pk_bf16_f32 v113, v24, v25
mva 42,58,44
s_waitcnt lgkmcnt(2)
v_cvt_pk_bf16_f32 v114, v26, v27
v_cvt_pk_bf16_f32 v115, v28, v29
mav 34,48,62
R 46,512
v_mul_f32 v105, v105, v95
mav 38,52,62
ds_write_b128 v84, v[112:115] offset:0
mva 42,62,56
v_fma_f32 v30, v30, v104, v105
v_mul_f32 v107, v107, v95
mav 34,60,66
R 50,576
v_fma_f32 v31, v31, v106, v107
mav 38,64,66
v_mul_f32 v109, v109, v95
v_fma_f32 v32, v32, v108, v109
mva 42,66,68
s_waitcnt lgkmcnt(3)
v_mul_f32 v111, v111, v95
v_fma_f32 v33, v33, v110, v111
mav 34,72,70
R 54,640
v_cvt_pk_bf16_f32 v78, v30, v31
mav 38,76,70
v_cvt_pk_bf16_f32 v79, v32, v33
mva 42,70,80
ds_write_b64 v84, v[78:79] offset:1024
mav 34,84,74
s_xor_b32 s50, s50, s57
R 58,704
mav 38,88,74
ds_write_b64 v85, v[14:15] offset:128
mva 42,74,92
s_waitcnt lgkmcnt(4)
mav 34,96,46
s_add_u32 s49, s49, 313344
R 62,768
mav 38,100,46
s_waitcnt lgkmcnt(1)
s_sub_u32 s54, s54, 0x8000
mva 42,46,104
s_barrier
mav 34,108,50
buffer_load_dwordx2 v[14:15], v94, s[36:39], s49 offen sc1
mav 38,112,50
s_add_u32 m0, s50, 33024
R 66,832
mva 42,50,116
s_cmp_lt_u32 s54, s56
s_cselect_b64 s[44:45], s[40:41], s[44:45]
mav 34,120,54
R 70,896
mav 38,124,54
D2 87,54,0
mva 42,54,128
s_add_u32 s55, s54, 14336
mav 34,132,58
D2 87,54,1024
mav 38,136,58
R 74,960
mva 42,58,140
s_waitcnt lgkmcnt(2)
D2 87,55,2048
mav 34,144,62
R 46,1024
LsQ23:
s_cbranch_execz LwQB
mav 38,148,62
D2 87,55,3072
mva 42,62,152
s_add_u32 m0, s50, 41280
mav 34,156,66
D2 88,54,0
mav 38,160,66
R 50,1088
mva 42,66,164
s_waitcnt lgkmcnt(2)
D2 88,54,1024
mav 34,168,70
R 54,1152
mav 38,172,70
D2 88,55,2048
mva 42,70,176
mav 34,180,74
D2 88,55,3072
mav 38,184,74
s_add_u32 m0, s50, 49536
R 58,1216
mva 42,74,188
s_waitcnt lgkmcnt(2)
D2 89,54,0
mav 34,192,46
R 62,1280
mav 38,196,46
D2 89,54,1024
mva 42,46,200
mav 34,204,50
D2 89,55,2048
mav 38,208,50
R 66,1344
mva 42,50,212
s_waitcnt lgkmcnt(2)
D2 89,55,3072
mav 34,216,54
s_add_u32 m0, s50, 57792
R 70,1408
mav 38,220,54
D2 90,54,0
mva 42,54,224
mav 34,228,58
D2 90,54,1024
mav 38,232,58
R 74,1472
mva 42,58,236
s_waitcnt lgkmcnt(2)
D2 90,55,2048
mav 34,240,62
R 46,1536
mav 38,244,62
D2 90,55,3072
mva 42,62,248
mav 34,252,66
R 50,1600
mvv 38,128,66
mvv 42,66,132
s_waitcnt lgkmcnt(2)
mvv 34,136,70
R 54,1664
mvv 38,140,70
mvv 42,70,144
mvv 34,148,74
R 58,1728
mvv 38,152,74
mvv 42,74,156
s_waitcnt lgkmcnt(2)
mvv 34,160,46
R 62,1792
mvv 38,164,46
mvv 42,46,168
s_waitcnt vmcnt(16)
v_permlane32_swap_b32 v14, v15
mvv 34,172,50
R 66,1856
v_add_f32 v16, v14, v15
mvv 38,176,50
s_mov_b32 s60, -27
.p2align 6
LQB1_1:
mvv 42,50,180
s_waitcnt lgkmcnt(2)
v_add_f32 v17, v14, v15
mvv 34,184,54
R 70,1920
mvv 38,188,54
v_permlane16_swap_b32 v16, v17
mvv 42,54,192
v_add_f32 v14, v16, v17
v_add_f32 v15, v16, v17
mvv 34,196,58
R 74,1984
v_xor_b32 v80, v81, v80
mvv 38,200,58
v_permlane32_swap_b32 v14, v15
mvv 42,58,204
s_waitcnt lgkmcnt(2)
v_mul_f32 v16, 0x3a800000, v14
v_mul_f32 v17, 0x3a800000, v15
mvv 34,208,62
R 46,33024
v_fma_f32 v17, -v16, v16, v17
mvv 38,212,62
ds_read_b64 v[118:119], v85 offset:128
v_max_f32 v17, 0, v17
mvv 42,62,216
ds_read_b128 v[104:107], v86 offset:128
v_add_f32 v17, 0x3727c5ac, v17
mvv 34,220,66
R 50,33088
mvv 38,224,66
ds_read_b128 v[108:111], v86 offset:144
mvv 42,66,228
s_waitcnt lgkmcnt(5)
v_rsq_f32 v14, v17
mvv 34,232,70
R 54,33152
v_mul_f32_e64 v15, -v16, v14
mvv 38,236,70
mvv 42,70,240
mvv 34,244,74
R 58,33216
mvv 38,248,74
mvv 42,74,252
s_waitcnt lgkmcnt(2)
v_mfma_f32_16x16x32_bf16 v[22:25], a[0:3], v[46:49], 0
R 62,33280
v_mul_f32 v120, v119, v96
v_mfma_f32_16x16x32_bf16 v[26:29], a[4:7], v[46:49], 0
v_fma_f32 v34, v34, v118, v120
v_mul_f32 v121, v119, v97
v_mfma_f32_16x16x32_bf16 v[30:33], v[46:49], a[8:11], 0
v_fma_f32 v35, v35, v118, v121
v_mul_f32 v120, v119, v98
mav 22,12,50
R 66,33344
v_fma_f32 v36, v36, v118, v120
mav 26,16,50
v_mul_f32 v121, v119, v99
v_fma_f32 v37, v37, v118, v121
mva 30,50,20
s_waitcnt lgkmcnt(2)
v_mul_f32 v120, v119, v100
v_fma_f32 v38, v38, v118, v120
mav 22,24,54
R 70,33408
v_mul_f32 v121, v119, v101
mav 26,28,54
v_fma_f32 v39, v39, v118, v121
v_mul_f32 v120, v119, v102
mva 30,54,32
v_fma_f32 v40, v40, v118, v120
v_mul_f32 v121, v119, v103
mav 22,36,58
R 74,33472
v_fma_f32 v41, v41, v118, v121
mav 26,40,58
v_cvt_pk_bf16_f32 v112, v34, v35
v_cvt_pk_bf16_f32 v113, v36, v37
mva 30,58,44
s_waitcnt lgkmcnt(2)
v_cvt_pk_bf16_f32 v114, v38, v39
v_cvt_pk_bf16_f32 v115, v40, v41
mav 22,48,62
R 46,33536
v_mul_f32 v105, v105, v95
mav 26,52,62
ds_write_b128 v84, v[112:115] offset:4096
mva 30,62,56
v_fma_f32 v42, v42, v104, v105
v_mul_f32 v107, v107, v95
mav 22,60,66
R 50,33600
v_fma_f32 v43, v43, v106, v107
mav 26,64,66
v_mul_f32 v109, v109, v95
v_fma_f32 v44, v44, v108, v109
mva 30,66,68
s_waitcnt lgkmcnt(3)
v_mul_f32 v111, v111, v95
v_fma_f32 v45, v45, v110, v111
mav 22,72,70
R 54,33664
v_cvt_pk_bf16_f32 v78, v42, v43
mav 26,76,70
v_cvt_pk_bf16_f32 v79, v44, v45
mva 30,70,80
ds_write_b64 v84, v[78:79] offset:5120
mav 22,84,74
R 58,33728
mav 26,88,74
ds_write_b64 v85, v[14:15] offset:0
mva 30,74,92
s_waitcnt lgkmcnt(4)
mav 22,96,46
s_add_u32 s49, s49, 313344
R 62,33792
mav 26,100,46
s_waitcnt lgkmcnt(1)
mva 30,46,104
s_barrier
mav 22,108,50
buffer_load_dwordx2 v[14:15], v94, s[36:39], s49 offen sc1
mav 26,112,50
s_sub_u32 s54, s54, 0x8000
R 66,33856
s_add_u32 m0, s50, 0
mva 30,50,116
D2 87,54,0
mav 22,120,54
R 70,33920
mav 26,124,54
D2 87,54,1024
mva 30,54,128
s_add_u32 s55, s54, 14336
mav 22,132,58
D2 87,55,2048
mav 26,136,58
R 74,33984
mva 30,58,140
s_waitcnt lgkmcnt(2)
D2 87,55,3072
mav 22,144,62
s_add_u32 m0, s50, 8256
R 46,34048
mav 26,148,62
D2 88,54,0
mva 30,62,152
mav 22,156,66
D2 88,54,1024
mav 26,160,66
R 50,34112
mva 30,66,164
s_waitcnt lgkmcnt(2)
D2 88,55,2048
mav 22,168,70
R 54,34176
mav 26,172,70
D2 88,55,3072
mva 30,70,176
s_add_u32 m0, s50, 16512
mav 22,180,74
D2 89,54,0
mav 26,184,74
R 58,34240
mva 30,74,188
s_waitcnt lgkmcnt(2)
D2 89,54,1024
mav 22,192,46
R 62,34304
mav 26,196,46
D2 89,55,2048
mva 30,46,200
mav 22,204,50
D2 89,55,3072
mav 26,208,50
s_add_u32 m0, s50, 24768
R 66,34368
mva 30,50,212
s_waitcnt lgkmcnt(2)
D2 90,54,0
mav 22,216,54
R 70,34432
mav 26,220,54
D2 90,54,1024
mva 30,54,224
mav 22,228,58
D2 90,55,2048
mav 26,232,58
R 74,34496
mva 30,58,236
s_waitcnt lgkmcnt(2)
D2 90,55,3072
mav 22,240,62
R 46,34560
mav 26,244,62
mva 30,62,248
mav 22,252,66
R 50,34624
mvv 26,128,66
mvv 30,66,132
s_waitcnt lgkmcnt(2)
mvv 22,136,70
R 54,34688
mvv 26,140,70
mvv 30,70,144
mvv 22,148,74
R 58,34752
mvv 26,152,74
mvv 30,74,156
s_waitcnt lgkmcnt(2)
mvv 22,160,46
R 62,34816
mvv 26,164,46
mvv 30,46,168
s_waitcnt vmcnt(16)
v_permlane32_swap_b32 v14, v15
mvv 22,172,50
R 66,34880
v_add_f32 v16, v14, v15
mvv 26,176,50
v_add_f32 v17, v14, v15
mvv 30,50,180
s_waitcnt lgkmcnt(2)
v_permlane16_swap_b32 v16, v17
mvv 22,184,54
R 70,34944
v_add_f32 v14, v16, v17
mvv 26,188,54
v_add_f32 v15, v16, v17
mvv 30,54,192
mvv 22,196,58
R 74,35008
mvv 26,200,58
v_permlane32_swap_b32 v14, v15
mvv 30,58,204
s_waitcnt lgkmcnt(2)
v_mul_f32 v16, 0x3a800000, v14
v_mul_f32 v17, 0x3a800000, v15
mvv 22,208,62
R 46,0
v_fma_f32 v17, -v16, v16, v17
mvv 26,212,62
ds_read_b64 v[118:119], v85 offset:0
v_max_f32 v17, 0, v17
mvv 30,62,216
ds_read_b128 v[104:107], v86 offset:0
v_add_f32 v17, 0x3727c5ac, v17
mvv 22,220,66
R 50,64
mvv 26,224,66
ds_read_b128 v[108:111], v86 offset:16
mvv 30,66,228
s_waitcnt lgkmcnt(5)
v_rsq_f32 v14, v17
mvv 22,232,70
R 54,128
v_mul_f32_e64 v15, -v16, v14
mvv 26,236,70
mvv 30,70,240
mvv 22,244,74
R 58,192
mvv 26,248,74
mvv 30,74,252
s_waitcnt lgkmcnt(2)
v_mfma_f32_16x16x32_bf16 v[34:37], a[0:3], v[46:49], 0
R 62,256
v_mul_f32 v120, v119, v96
v_mfma_f32_16x16x32_bf16 v[38:41], a[4:7], v[46:49], 0
v_fma_f32 v22, v22, v118, v120
v_mul_f32 v121, v119, v97
v_mfma_f32_16x16x32_bf16 v[42:45], v[46:49], a[8:11], 0
v_fma_f32 v23, v23, v118, v121
v_mul_f32 v120, v119, v98
mav 34,12,50
R 66,320
v_fma_f32 v24, v24, v118, v120
mav 38,16,50
v_mul_f32 v121, v119, v99
v_fma_f32 v25, v25, v118, v121
mva 42,50,20
s_waitcnt lgkmcnt(2)
v_mul_f32 v120, v119, v100
v_fma_f32 v26, v26, v118, v120
mav 34,24,54
R 70,384
v_mul_f32 v121, v119, v101
mav 38,28,54
v_fma_f32 v27, v27, v118, v121
v_mul_f32 v120, v119, v102
mva 42,54,32
v_fma_f32 v28, v28, v118, v120
v_mul_f32 v121, v119, v103
mav 34,36,58
R 74,448
v_fma_f32 v29, v29, v118, v121
mav 38,40,58
v_cvt_pk_bf16_f32 v112, v22, v23
v_cvt_pk_bf16_f32 v113, v24, v25
mva 42,58,44
s_waitcnt lgkmcnt(2)
v_cvt_pk_bf16_f32 v114, v26, v27
v_cvt_pk_bf16_f32 v115, v28, v29
mav 34,48,62
R 46,512
v_mul_f32 v105, v105, v95
mav 38,52,62
ds_write_b128 v84, v[112:115] offset:0
mva 42,62,56
v_fma_f32 v30, v30, v104, v105
v_mul_f32 v107, v107, v95
mav 34,60,66
R 50,576
v_fma_f32 v31, v31, v106, v107
mav 38,64,66
v_mul_f32 v109, v109, v95
v_fma_f32 v32, v32, v108, v109
mva 42,66,68
s_waitcnt lgkmcnt(3)
v_mul_f32 v111, v111, v95
v_fma_f32 v33, v33, v110, v111
mav 34,72,70
R 54,640
v_cvt_pk_bf16_f32 v78, v30, v31
mav 38,76,70
v_cvt_pk_bf16_f32 v79, v32, v33
mva 42,70,80
ds_write_b64 v84, v[78:79] offset:1024
mav 34,84,74
s_xor_b32 s50, s50, s57
R 58,704
mav 38,88,74
ds_write_b64 v85, v[14:15] offset:128
mva 42,74,92
s_waitcnt lgkmcnt(4)
mav 34,96,46
s_add_u32 s49, s49, 313344
R 62,768
mav 38,100,46
s_waitcnt lgkmcnt(1)
s_sub_u32 s54, s54, 0x8000
mva 42,46,104
s_barrier
mav 34,108,50
buffer_load_dwordx2 v[14:15], v94, s[36:39], s49 offen sc1
mav 38,112,50
s_add_u32 m0, s50, 33024
R 66,832
mva 42,50,116
s_cmp_lt_u32 s54, s56
s_cselect_b64 s[44:45], s[40:41], s[44:45]
mav 34,120,54
R 70,896
mav 38,124,54
D2 87,54,0
mva 42,54,128
s_add_u32 s55, s54, 14336
mav 34,132,58
D2 87,54,1024
mav 38,136,58
R 74,960
mva 42,58,140
s_waitcnt lgkmcnt(2)
D2 87,55,2048
mav 34,144,62
R 46,1024
mav 38,148,62
D2 87,55,3072
mva 42,62,152
s_add_u32 m0, s50, 41280
mav 34,156,66
D2 88,54,0
mav 38,160,66
R 50,1088
mva 42,66,164
s_waitcnt lgkmcnt(2)
D2 88,54,1024
mav 34,168,70
R 54,1152
mav 38,172,70
D2 88,55,2048
mva 42,70,176
mav 34,180,74
D2 88,55,3072
mav 38,184,74
s_add_u32 m0, s50, 49536
R 58,1216
mva 42,74,188
s_waitcnt lgkmcnt(2)
D2 89,54,0
mav 34,192,46
R 62,1280
mav 38,196,46
D2 89,54,1024
mva 42,46,200
mav 34,204,50
D2 89,55,2048
mav 38,208,50
R 66,1344
mva 42,50,212
s_waitcnt lgkmcnt(2)
D2 89,55,3072
mav 34,216,54
s_add_u32 m0, s50, 57792
R 70,1408
mav 38,220,54
D2 90,54,0
mva 42,54,224
mav 34,228,58
D2 90,54,1024
mav 38,232,58
R 74,1472
mva 42,58,236
s_waitcnt lgkmcnt(2)
D2 90,55,2048
mav 34,240,62
R 46,1536
mav 38,244,62
D2 90,55,3072
mva 42,62,248
mav 34,252,66
R 50,1600
mvv 38,128,66
mvv 42,66,132
s_waitcnt lgkmcnt(2)
mvv 34,136,70
R 54,1664
mvv 38,140,70
mvv 42,70,144
mvv 34,148,74
R 58,1728
mvv 38,152,74
mvv 42,74,156
s_waitcnt lgkmcnt(2)
mvv 34,160,46
R 62,1792
mvv 38,164,46
mvv 42,46,168
s_waitcnt vmcnt(16)
v_permlane32_swap_b32 v14, v15
mvv 34,172,50
s_add_u32 s60, s60, 1
R 66,1856
v_add_f32 v16, v14, v15
mvv 38,176,50
s_cbranch_scc0 LQB1_1
mvv 42,50,180
s_waitcnt lgkmcnt(2)
v_add_f32 v17, v14, v15
mvv 34,184,54
R 70,1920
mvv 38,188,54
v_permlane16_swap_b32 v16, v17
mvv 42,54,192
v_add_f32 v14, v16, v17
v_add_f32 v15, v16, v17
mvv 34,196,58
R 74,1984
v_xor_b32 v80, v81, v80
mvv 38,200,58
v_permlane32_swap_b32 v14, v15
mvv 42,58,204
s_waitcnt lgkmcnt(2)
v_mul_f32 v16, 0x3a800000, v14
v_mul_f32 v17, 0x3a800000, v15
mvv 34,208,62
R 46,33024
v_fma_f32 v17, -v16, v16, v17
mvv 38,212,62
ds_read_b64 v[118:119], v85 offset:128
v_max_f32 v17, 0, v17
mvv 42,62,216
ds_read_b128 v[104:107], v86 offset:128
v_add_f32 v17, 0x3727c5ac, v17
mvv 34,220,66
R 50,33088
mvv 38,224,66
ds_read_b128 v[108:111], v86 offset:144
mvv 42,66,228
LsQ24:
s_cbranch_execz LwQA
s_waitcnt lgkmcnt(5)
v_rsq_f32 v14, v17
mvv 34,232,70
R 54,33152
v_mul_f32_e64 v15, -v16, v14
mvv 38,236,70
mvv 42,70,240
mvv 34,244,74
R 58,33216
mvv 38,248,74
mvv 42,74,252
s_waitcnt lgkmcnt(2)
v_mfma_f32_16x16x32_bf16 v[22:25], a[0:3], v[46:49], 0
R 62,33280
v_mul_f32 v120, v119, v96
v_mfma_f32_16x16x32_bf16 v[26:29], a[4:7], v[46:49], 0
v_fma_f32 v34, v34, v118, v120
v_mul_f32 v121, v119, v97
v_mfma_f32_16x16x32_bf16 v[30:33], v[46:49], a[8:11], 0
v_fma_f32 v35, v35, v118, v121
v_mul_f32 v120, v119, v98
mav 22,12,50
R 66,33344
v_fma_f32 v36, v36, v118, v120
mav 26,16,50
v_mul_f32 v121, v119, v99
v_fma_f32 v37, v37, v118, v121
mva 30,50,20
s_waitcnt lgkmcnt(2)
v_mul_f32 v120, v119, v100
v_fma_f32 v38, v38, v118, v120
mav 22,24,54
R 70,33408
v_mul_f32 v121, v119, v101
mav 26,28,54
v_fma_f32 v39, v39, v118, v121
v_mul_f32 v120, v119, v102
mva 30,54,32
v_fma_f32 v40, v40, v118, v120
v_mul_f32 v121, v119, v103
mav 22,36,58
R 74,33472
v_fma_f32 v41, v41, v118, v121
mav 26,40,58
v_cvt_pk_bf16_f32 v112, v34, v35
v_cvt_pk_bf16_f32 v113, v36, v37
mva 30,58,44
s_waitcnt lgkmcnt(2)
v_cvt_pk_bf16_f32 v114, v38, v39
v_cvt_pk_bf16_f32 v115, v40, v41
mav 22,48,62
R 46,33536
v_mul_f32 v105, v105, v95
mav 26,52,62
ds_write_b128 v84, v[112:115] offset:4096
mva 30,62,56
v_fma_f32 v42, v42, v104, v105
v_mul_f32 v107, v107, v95
mav 22,60,66
R 50,33600
v_fma_f32 v43, v43, v106, v107
mav 26,64,66
v_mul_f32 v109, v109, v95
v_fma_f32 v44, v44, v108, v109
mva 30,66,68
s_waitcnt lgkmcnt(3)
v_mul_f32 v111, v111, v95
v_fma_f32 v45, v45, v110, v111
mav 22,72,70
R 54,33664
v_cvt_pk_bf16_f32 v78, v42, v43
mav 26,76,70
v_cvt_pk_bf16_f32 v79, v44, v45
mva 30,70,80
ds_write_b64 v84, v[78:79] offset:5120
mav 22,84,74
R 58,33728
mav 26,88,74
ds_write_b64 v85, v[14:15] offset:0
mva 30,74,92
s_waitcnt lgkmcnt(4)
mav 22,96,46
s_add_u32 s49, s49, 313344
R 62,33792
mav 26,100,46
s_waitcnt lgkmcnt(1)
mva 30,46,104
s_barrier
mav 22,108,50
buffer_load_dwordx2 v[14:15], v94, s[36:39], s49 offen sc1
mav 26,112,50
s_sub_u32 s54, s54, 0x8000
R 66,33856
s_add_u32 m0, s50, 0
mva 30,50,116
D2 87,54,0
mav 22,120,54
R 70,33920
mav 26,124,54
D2 87,54,1024
mva 30,54,128
s_add_u32 s55, s54, 14336
mav 22,132,58
D2 87,55,2048
mav 26,136,58
R 74,33984
mva 30,58,140
s_waitcnt lgkmcnt(2)
D2 87,55,3072
mav 22,144,62
s_add_u32 m0, s50, 8256
R 46,34048
mav 26,148,62
D2 88,54,0
mva 30,62,152
mav 22,156,66
D2 88,54,1024
mav 26,160,66
R 50,34112
mva 30,66,164
s_waitcnt lgkmcnt(2)
D2 88,55,2048
mav 22,168,70
R 54,34176
mav 26,172,70
D2 88,55,3072
mva 30,70,176
s_add_u32 m0, s50, 16512
mav 22,180,74
D2 89,54,0
mav 26,184,74
R 58,34240
mva 30,74,188
s_waitcnt lgkmcnt(2)
D2 89,54,1024
mav 22,192,46
R 62,34304
mav 26,196,46
D2 89,55,2048
mva 30,46,200
mav 22,204,50
D2 89,55,3072
mav 26,208,50
s_add_u32 m0, s50, 24768
R 66,34368
mva 30,50,212
s_waitcnt lgkmcnt(2)
D2 90,54,0
mav 22,216,54
R 70,34432
mav 26,220,54
D2 90,54,1024
mva 30,54,224
mav 22,228,58
D2 90,55,2048
mav 26,232,58
R 74,34496
mva 30,58,236
s_waitcnt lgkmcnt(2)
D2 90,55,3072
mav 22,240,62
R 46,34560
mav 26,244,62
mva 30,62,248
mav 22,252,66
R 50,34624
mvv 26,128,66
mvv 30,66,132
s_waitcnt lgkmcnt(2)
mvv 22,136,70
R 54,34688
mvv 26,140,70
mvv 30,70,144
mvv 22,148,74
R 58,34752
mvv 26,152,74
mvv 30,74,156
s_waitcnt lgkmcnt(2)
mvv 22,160,46
R 62,34816
mvv 26,164,46
mvv 30,46,168
s_waitcnt vmcnt(16)
v_permlane32_swap_b32 v14, v15
mvv 22,172,50
R 66,34880
v_add_f32 v16, v14, v15
mvv 26,176,50
v_add_f32 v17, v14, v15
mvv 30,50,180
s_waitcnt lgkmcnt(2)
v_permlane16_swap_b32 v16, v17
mvv 22,184,54
R 70,34944
v_add_f32 v14, v16, v17
mvv 26,188,54
v_add_f32 v15, v16, v17
mvv 30,54,192
mvv 22,196,58
R 74,35008
mvv 26,200,58
v_permlane32_swap_b32 v14, v15
mvv 30,58,204
s_waitcnt lgkmcnt(2)
v_mul_f32 v16, 0x3a800000, v14
v_mul_f32 v17, 0x3a800000, v15
mvv 22,208,62
R 46,0
v_fma_f32 v17, -v16, v16, v17
mvv 26,212,62
ds_read_b64 v[118:119], v85 offset:0
v_max_f32 v17, 0, v17
mvv 30,62,216
ds_read_b128 v[104:107], v86 offset:0
v_add_f32 v17, 0x3727c5ac, v17
mvv 22,220,66
R 50,64
mvv 26,224,66
ds_read_b128 v[108:111], v86 offset:16
mvv 30,66,228
s_waitcnt lgkmcnt(5)
v_rsq_f32 v14, v17
mvv 22,232,70
R 54,128
v_mul_f32_e64 v15, -v16, v14
mvv 26,236,70
mvv 30,70,240
mvv 22,244,74
R 58,192
mvv 26,248,74
mvv 30,74,252
s_waitcnt lgkmcnt(2)
v_mfma_f32_16x16x32_bf16 v[34:37], a[0:3], v[46:49], 0
R 62,256
v_mul_f32 v120, v119, v96
v_mfma_f32_16x16x32_bf16 v[38:41], a[4:7], v[46:49], 0
v_fma_f32 v22, v22, v118, v120
v_mul_f32 v121, v119, v97
v_mfma_f32_16x16x32_bf16 v[42:45], v[46:49], a[8:11], 0
v_fma_f32 v23, v23, v118, v121
v_mul_f32 v120, v119, v98
mav 34,12,50
R 66,320
v_fma_f32 v24, v24, v118, v120
mav 38,16,50
v_mul_f32 v121, v119, v99
v_fma_f32 v25, v25, v118, v121
mva 42,50,20
s_waitcnt lgkmcnt(2)
v_mul_f32 v120, v119, v100
v_fma_f32 v26, v26, v118, v120
mav 34,24,54
R 70,384
v_mul_f32 v121, v119, v101
mav 38,28,54
v_fma_f32 v27, v27, v118, v121
v_mul_f32 v120, v119, v102
mva 42,54,32
v_fma_f32 v28, v28, v118, v120
v_mul_f32 v121, v119, v103
mav 34,36,58
R 74,448
v_fma_f32 v29, v29, v118, v121
mav 38,40,58
v_cvt_pk_bf16_f32 v112, v22, v23
v_cvt_pk_bf16_f32 v113, v24, v25
mva 42,58,44
s_waitcnt lgkmcnt(2)
v_cvt_pk_bf16_f32 v114, v26, v27
v_cvt_pk_bf16_f32 v115, v28, v29
mav 34,48,62
R 46,512
v_mul_f32 v105, v105, v95
mav 38,52,62
ds_write_b128 v84, v[112:115] offset:0
mva 42,62,56
v_fma_f32 v30, v30, v104, v105
v_mul_f32 v107, v107, v95
mav 34,60,66
R 50,576
v_fma_f32 v31, v31, v106, v107
mav 38,64,66
)"
R"(
v_mul_f32 v109, v109, v95
v_fma_f32 v32, v32, v108, v109
mva 42,66,68
s_waitcnt lgkmcnt(3)
v_mul_f32 v111, v111, v95
v_fma_f32 v33, v33, v110, v111
mav 34,72,70
R 54,640
v_cvt_pk_bf16_f32 v78, v30, v31
mav 38,76,70
v_cvt_pk_bf16_f32 v79, v32, v33
mva 42,70,80
ds_write_b64 v84, v[78:79] offset:1024
mav 34,84,74
R 58,704
mav 38,88,74
ds_write_b64 v85, v[14:15] offset:128
mva 42,74,92
s_waitcnt lgkmcnt(4)
mav 34,96,46
s_add_u32 s49, s49, 313344
R 62,768
mav 38,100,46
s_waitcnt lgkmcnt(1)
mva 42,46,104
s_barrier
mav 34,108,50
buffer_load_dwordx2 v[14:15], v94, s[36:39], s49 offen sc1
mav 38,112,50
R 66,832
mva 42,50,116
mav 34,120,54
R 70,896
mav 38,124,54
mva 42,54,128
mav 34,132,58
R 74,960
mav 38,136,58
mva 42,58,140
s_waitcnt lgkmcnt(2)
mav 34,144,62
R 46,1024
mav 38,148,62
mva 42,62,152
mav 34,156,66
R 50,1088
mav 38,160,66
mva 42,66,164
s_waitcnt lgkmcnt(2)
mav 34,168,70
R 54,1152
mav 38,172,70
mva 42,70,176
mav 34,180,74
R 58,1216
mav 38,184,74
mva 42,74,188
s_waitcnt lgkmcnt(2)
mav 34,192,46
R 62,1280
mav 38,196,46
mva 42,46,200
mav 34,204,50
R 66,1344
mav 38,208,50
mva 42,50,212
s_waitcnt lgkmcnt(2)
mav 34,216,54
R 70,1408
mav 38,220,54
mva 42,54,224
mav 34,228,58
R 74,1472
mav 38,232,58
mva 42,58,236
s_waitcnt lgkmcnt(2)
mav 34,240,62
R 46,1536
mav 38,244,62
mva 42,62,248
mav 34,252,66
R 50,1600
mvv 38,128,66
mvv 42,66,132
s_waitcnt lgkmcnt(2)
mvv 34,136,70
R 54,1664
mvv 38,140,70
mvv 42,70,144
mvv 34,148,74
R 58,1728
mvv 38,152,74
mvv 42,74,156
s_waitcnt lgkmcnt(2)
mvv 34,160,46
R 62,1792
mvv 38,164,46
mvv 42,46,168
s_waitcnt vmcnt(0)
v_permlane32_swap_b32 v14, v15
mvv 34,172,50
R 66,1856
v_add_f32 v16, v14, v15
mvv 38,176,50
mvv 42,50,180
s_waitcnt lgkmcnt(2)
v_add_f32 v17, v14, v15
mvv 34,184,54
R 70,1920
mvv 38,188,54
v_permlane16_swap_b32 v16, v17
mvv 42,54,192
v_add_f32 v14, v16, v17
v_add_f32 v15, v16, v17
mvv 34,196,58
R 74,1984
v_xor_b32 v80, v81, v80
mvv 38,200,58
v_permlane32_swap_b32 v14, v15
mvv 42,58,204
s_waitcnt lgkmcnt(2)
v_mul_f32 v16, 0x3a800000, v14
v_mul_f32 v17, 0x3a800000, v15
mvv 34,208,62
R 46,33024
v_fma_f32 v17, -v16, v16, v17
mvv 38,212,62
ds_read_b64 v[118:119], v85 offset:128
v_max_f32 v17, 0, v17
mvv 42,62,216
ds_read_b128 v[104:107], v86 offset:128
v_add_f32 v17, 0x3727c5ac, v17
mvv 34,220,66
R 50,33088
mvv 38,224,66
ds_read_b128 v[108:111], v86 offset:144
mvv 42,66,228
s_waitcnt lgkmcnt(5)
v_rsq_f32 v14, v17
mvv 34,232,70
R 54,33152
v_mul_f32_e64 v15, -v16, v14
mvv 38,236,70
mvv 42,70,240
mvv 34,244,74
R 58,33216
mvv 38,248,74
mvv 42,74,252
s_waitcnt lgkmcnt(2)
v_mfma_f32_16x16x32_bf16 v[22:25], a[0:3], v[46:49], 0
R 62,33280
v_mul_f32 v120, v119, v96
v_mfma_f32_16x16x32_bf16 v[26:29], a[4:7], v[46:49], 0
v_fma_f32 v34, v34, v118, v120
v_mul_f32 v121, v119, v97
v_mfma_f32_16x16x32_bf16 v[30:33], v[46:49], a[8:11], 0
v_fma_f32 v35, v35, v118, v121
v_mul_f32 v120, v119, v98
mav 22,12,50
R 66,33344
v_fma_f32 v36, v36, v118, v120
mav 26,16,50
v_mul_f32 v121, v119, v99
v_fma_f32 v37, v37, v118, v121
mva 30,50,20
s_waitcnt lgkmcnt(2)
v_mul_f32 v120, v119, v100
v_fma_f32 v38, v38, v118, v120
mav 22,24,54
R 70,33408
v_mul_f32 v121, v119, v101
mav 26,28,54
v_fma_f32 v39, v39, v118, v121
v_mul_f32 v120, v119, v102
mva 30,54,32
v_fma_f32 v40, v40, v118, v120
v_mul_f32 v121, v119, v103
mav 22,36,58
R 74,33472
v_fma_f32 v41, v41, v118, v121
mav 26,40,58
v_cvt_pk_bf16_f32 v112, v34, v35
v_cvt_pk_bf16_f32 v113, v36, v37
mva 30,58,44
s_waitcnt lgkmcnt(2)
v_cvt_pk_bf16_f32 v114, v38, v39
v_cvt_pk_bf16_f32 v115, v40, v41
mav 22,48,62
R 46,33536
v_mul_f32 v105, v105, v95
mav 26,52,62
ds_write_b128 v84, v[112:115] offset:4096
mva 30,62,56
v_fma_f32 v42, v42, v104, v105
v_mul_f32 v107, v107, v95
mav 22,60,66
R 50,33600
v_fma_f32 v43, v43, v106, v107
mav 26,64,66
v_mul_f32 v109, v109, v95
v_fma_f32 v44, v44, v108, v109
mva 30,66,68
s_waitcnt lgkmcnt(3)
v_mul_f32 v111, v111, v95
v_fma_f32 v45, v45, v110, v111
mav 22,72,70
R 54,33664
v_cvt_pk_bf16_f32 v78, v42, v43
mav 26,76,70
v_cvt_pk_bf16_f32 v79, v44, v45
mva 30,70,80
ds_write_b64 v84, v[78:79] offset:5120
mav 22,84,74
R 58,33728
mav 26,88,74
ds_write_b64 v85, v[14:15] offset:0
mva 30,74,92
s_waitcnt lgkmcnt(4)
mav 22,96,46
s_add_u32 s49, s49, 313344
R 62,33792
mav 26,100,46
s_waitcnt lgkmcnt(1)
mva 30,46,104
s_barrier
mav 22,108,50
buffer_load_dwordx2 v[14:15], v94, s[36:39], s49 offen sc1
mav 26,112,50
R 66,33856
mva 30,50,116
mav 22,120,54
R 70,33920
mav 26,124,54
mva 30,54,128
mav 22,132,58
R 74,33984
mav 26,136,58
mva 30,58,140
s_waitcnt lgkmcnt(2)
mav 22,144,62
R 46,34048
mav 26,148,62
mva 30,62,152
mav 22,156,66
R 50,34112
mav 26,160,66
mva 30,66,164
s_waitcnt lgkmcnt(2)
mav 22,168,70
R 54,34176
mav 26,172,70
mva 30,70,176
mav 22,180,74
R 58,34240
mav 26,184,74
mva 30,74,188
s_waitcnt lgkmcnt(2)
mav 22,192,46
R 62,34304
mav 26,196,46
mva 30,46,200
mav 22,204,50
R 66,34368
mav 26,208,50
mva 30,50,212
s_waitcnt lgkmcnt(2)
mav 22,216,54
R 70,34432
mav 26,220,54
mva 30,54,224
mav 22,228,58
R 74,34496
mav 26,232,58
mva 30,58,236
s_waitcnt lgkmcnt(2)
mav 22,240,62
R 46,34560
mav 26,244,62
mva 30,62,248
mav 22,252,66
R 50,34624
mvv 26,128,66
mvv 30,66,132
LsQ25:
s_cbranch_execz LwQB
s_waitcnt lgkmcnt(2)
mvv 22,136,70
R 54,34688
mvv 26,140,70
mvv 30,70,144
mvv 22,148,74
R 58,34752
mvv 26,152,74
mvv 30,74,156
s_waitcnt lgkmcnt(2)
mvv 22,160,46
R 62,34816
mvv 26,164,46
mvv 30,46,168
s_waitcnt vmcnt(0)
v_permlane32_swap_b32 v14, v15
mvv 22,172,50
R 66,34880
v_add_f32 v16, v14, v15
mvv 26,176,50
v_add_f32 v17, v14, v15
mvv 30,50,180
s_waitcnt lgkmcnt(2)
v_permlane16_swap_b32 v16, v17
mvv 22,184,54
R 70,34944
v_add_f32 v14, v16, v17
mvv 26,188,54
v_add_f32 v15, v16, v17
mvv 30,54,192
mvv 22,196,58
R 74,35008
mvv 26,200,58
v_permlane32_swap_b32 v14, v15
mvv 30,58,204
s_waitcnt lgkmcnt(2)
v_mul_f32 v16, 0x3a800000, v14
v_mul_f32 v17, 0x3a800000, v15
mvv 22,208,62
R 46,0
v_fma_f32 v17, -v16, v16, v17
mvv 26,212,62
ds_read_b64 v[118:119], v85 offset:0
v_max_f32 v17, 0, v17
mvv 30,62,216
ds_read_b128 v[104:107], v86 offset:0
v_add_f32 v17, 0x3727c5ac, v17
mvv 22,220,66
R 50,64
mvv 26,224,66
ds_read_b128 v[108:111], v86 offset:16
mvv 30,66,228
s_waitcnt lgkmcnt(5)
v_rsq_f32 v14, v17
mvv 22,232,70
R 54,128
v_mul_f32_e64 v15, -v16, v14
mvv 26,236,70
mvv 30,70,240
mvv 22,244,74
R 58,192
mvv 26,248,74
mvv 30,74,252
s_waitcnt lgkmcnt(2)
v_mfma_f32_16x16x32_bf16 v[34:37], a[0:3], v[46:49], 0
R 62,256
v_mul_f32 v120, v119, v96
v_mfma_f32_16x16x32_bf16 v[38:41], a[4:7], v[46:49], 0
v_fma_f32 v22, v22, v118, v120
v_mul_f32 v121, v119, v97
v_mfma_f32_16x16x32_bf16 v[42:45], v[46:49], a[8:11], 0
v_fma_f32 v23, v23, v118, v121
v_mul_f32 v120, v119, v98
mav 34,12,50
R 66,320
v_fma_f32 v24, v24, v118, v120
mav 38,16,50
v_mul_f32 v121, v119, v99
v_fma_f32 v25, v25, v118, v121
mva 42,50,20
s_waitcnt lgkmcnt(2)
v_mul_f32 v120, v119, v100
v_fma_f32 v26, v26, v118, v120
mav 34,24,54
R 70,384
v_mul_f32 v121, v119, v101
mav 38,28,54
v_fma_f32 v27, v27, v118, v121
v_mul_f32 v120, v119, v102
mva 42,54,32
v_fma_f32 v28, v28, v118, v120
v_mul_f32 v121, v119, v103
mav 34,36,58
R 74,448
v_fma_f32 v29, v29, v118, v121
mav 38,40,58
v_cvt_pk_bf16_f32 v112, v22, v23
v_cvt_pk_bf16_f32 v113, v24, v25
mva 42,58,44
s_waitcnt lgkmcnt(2)
v_cvt_pk_bf16_f32 v114, v26, v27
v_cvt_pk_bf16_f32 v115, v28, v29
mav 34,48,62
R 46,512
v_mul_f32 v105, v105, v95
mav 38,52,62
ds_write_b128 v84, v[112:115] offset:0
mva 42,62,56
v_fma_f32 v30, v30, v104, v105
v_mul_f32 v107, v107, v95
mav 34,60,66
R 50,576
v_fma_f32 v31, v31, v106, v107
mav 38,64,66
v_mul_f32 v109, v109, v95
v_fma_f32 v32, v32, v108, v109
mva 42,66,68
s_waitcnt lgkmcnt(3)
v_mul_f32 v111, v111, v95
v_fma_f32 v33, v33, v110, v111
mav 34,72,70
R 54,640
v_cvt_pk_bf16_f32 v78, v30, v31
mav 38,76,70
v_cvt_pk_bf16_f32 v79, v32, v33
mva 42,70,80
ds_write_b64 v84, v[78:79] offset:1024
mav 34,84,74
R 58,704
mav 38,88,74
ds_write_b64 v85, v[14:15] offset:128
mva 42,74,92
s_waitcnt lgkmcnt(4)
mav 34,96,46
R 62,768
mav 38,100,46
s_waitcnt lgkmcnt(1)
mva 42,46,104
s_barrier
mav 34,108,50
R 66,832
mav 38,112,50
mva 42,50,116
mav 34,120,54
R 70,896
mav 38,124,54
mva 42,54,128
mav 34,132,58
R 74,960
mav 38,136,58
mva 42,58,140
s_waitcnt lgkmcnt(2)
mav 34,144,62
R 46,1024
mav 38,148,62
mva 42,62,152
mav 34,156,66
R 50,1088
mav 38,160,66
mva 42,66,164
s_waitcnt lgkmcnt(2)
mav 34,168,70
R 54,1152
mav 38,172,70
mva 42,70,176
mav 34,180,74
R 58,1216
mav 38,184,74
mva 42,74,188
s_waitcnt lgkmcnt(2)
mav 34,192,46
R 62,1280
mav 38,196,46
mva 42,46,200
mav 34,204,50
R 66,1344
mav 38,208,50
mva 42,50,212
s_waitcnt lgkmcnt(2)
mav 34,216,54
R 70,1408
mav 38,220,54
mva 42,54,224
mav 34,228,58
R 74,1472
mav 38,232,58
mva 42,58,236
s_waitcnt lgkmcnt(2)
mav 34,240,62
R 46,1536
mav 38,244,62
mva 42,62,248
mav 34,252,66
R 50,1600
mvv 38,128,66
mvv 42,66,132
s_waitcnt lgkmcnt(2)
mvv 34,136,70
R 54,1664
mvv 38,140,70
mvv 42,70,144
mvv 34,148,74
R 58,1728
mvv 38,152,74
mvv 42,74,156
s_waitcnt lgkmcnt(2)
mvv 34,160,46
R 62,1792
mvv 38,164,46
mvv 42,46,168
mvv 34,172,50
R 66,1856
mvv 38,176,50
mvv 42,50,180
s_waitcnt lgkmcnt(2)
mvv 34,184,54
R 70,1920
mvv 38,188,54
mvv 42,54,192
mvv 34,196,58
R 74,1984
mvv 38,200,58
mvv 42,58,204
s_waitcnt lgkmcnt(2)
mvv 34,208,62
mvv 38,212,62
ds_read_b64 v[118:119], v85 offset:128
mvv 42,62,216
ds_read_b128 v[104:107], v86 offset:128
mvv 34,220,66
ds_read_b128 v[108:111], v86 offset:144
mvv 38,224,66
mvv 42,66,228
s_waitcnt lgkmcnt(3)
mvv 34,232,70
mvv 38,236,70
mvv 42,70,240
mvv 34,244,74
mvv 38,248,74
mvv 42,74,252
s_waitcnt lgkmcnt(0)
v_mul_f32 v120, v119, v96
s_nop 3
v_fma_f32 v34, v34, v118, v120
v_mul_f32 v121, v119, v97
v_fma_f32 v35, v35, v118, v121
v_mul_f32 v120, v119, v98
v_fma_f32 v36, v36, v118, v120
v_mul_f32 v121, v119, v99
v_fma_f32 v37, v37, v118, v121
v_mul_f32 v120, v119, v100
v_fma_f32 v38, v38, v118, v120
v_mul_f32 v121, v119, v101
v_fma_f32 v39, v39, v118, v121
v_mul_f32 v120, v119, v102
v_fma_f32 v40, v40, v118, v120
v_mul_f32 v121, v119, v103
v_fma_f32 v41, v41, v118, v121
v_cvt_pk_bf16_f32 v112, v34, v35
v_cvt_pk_bf16_f32 v113, v36, v37
v_cvt_pk_bf16_f32 v114, v38, v39
v_cvt_pk_bf16_f32 v115, v40, v41
ds_write_b128 v84, v[112:115] offset:4096
v_mul_f32 v105, v105, v95
v_fma_f32 v42, v42, v104, v105
v_mul_f32 v107, v107, v95
v_fma_f32 v43, v43, v106, v107
v_mul_f32 v109, v109, v95
v_fma_f32 v44, v44, v108, v109
v_mul_f32 v111, v111, v95
v_fma_f32 v45, v45, v110, v111
v_cvt_pk_bf16_f32 v78, v42, v43
v_cvt_pk_bf16_f32 v79, v44, v45
ds_write_b64 v84, v[78:79] offset:5120
s_waitcnt lgkmcnt(0)
s_barrier
s_endpgm
Cq:
s_cmp_eq_u32 s61, 0
s_cbranch_scc1 Bq
s_sub_u32 s61, s61, 1
s_sleep 2
buffer_load_dwordx2 v[122:123], v92, s[36:39], 0 offen sc1
s_waitcnt vmcnt(0)
ds_write_b32 v83, v122
v_sub_u32 v122, v122, v93
v_xor_b32 v123, v123, v117
v_cmp_lt_u64 vcc, s[62:63], v[122:123]
s_cbranch_vccnz Cq
s_branch Bq
Lr2:
s_cmp_eq_u32 s60, 2
s_cbranch_scc0 Lr3
s_waitcnt vmcnt(0)
v_accvgpr_read_b32 v57, a255
v_mov_b32 v56, 0x100
v_add_u32 v57, 0x9e3779b9, v57
buffer_store_dwordx2 v[56:57], v81, s[36:39], s52 offen sc1
v_add_u32 v87, v87, v56
s_sleep 32
s_sleep 32
E 0,65536
E 1,69632
s_add_u32 s51, s51, 65536
E 0,73728
E 1,77824
s_add_u32 s51, s51, 65536
E 0,81920
E 1,86016
s_add_u32 s51, s51, 65536
E 0,90112
E 1,94208
s_add_u32 s51, s51, 65536
s_mov_b64 exec, 0
s_cmp_eq_u32 s53, 0
s_cbranch_scc1 LsO0
s_cmp_eq_u32 s53, 1024
s_cbranch_scc1 LsO1
s_cmp_eq_u32 s53, 2048
s_cbranch_scc1 LsO2
s_branch LsO3
LwO:
s_mov_b64 exec, -1
s_waitcnt vmcnt(4)
s_barrier
v_add_u32 v4, 65536, v4
v_add_u32 v5, 65536, v5
Ta 0,4,0
Ta 4,5,0
Ta 8,4,8192
Ta 12,5,8192
E 0,98304
E 1,102400
s_add_u32 s51, s51, 65536
E 0,106496
E 1,110592
s_add_u32 s51, s51, 65536
E 0,114688
E 1,118784
s_add_u32 s51, s51, 65536
E 0,122880
E 1,126976
s_add_u32 s51, s51, 65536
s_cmp_ge_u32 s51, s57
s_cselect_b32 s60, 2097152, 0
s_sub_u32 s51, s51, s60
E 0,131072
E 1,135168
s_add_u32 s51, s51, 65536
E 0,139264
E 1,143360
s_add_u32 s51, s51, 65536
E 0,147456
E 1,151552
s_add_u32 s51, s51, 65536
E 0,155648
E 1,159744
s_add_u32 s51, s51, 65536
s_waitcnt lgkmcnt(0)
s_waitcnt vmcnt(16)
s_barrier
E 0,65536
Ta 16,4,16384
Ta 20,5,16384
E 1,69632
s_add_u32 s51, s51, 65536
Ta 24,4,24576
Ta 28,5,24576
E 0,73728
E 1,77824
s_add_u32 s51, s51, 65536
s_waitcnt lgkmcnt(0)
s_waitcnt vmcnt(16)
s_barrier
E 0,81920
Ta 32,4,32768
Ta 36,5,32768
E 1,86016
s_add_u32 s51, s51, 65536
Ta 40,4,40960
Ta 44,5,40960
E 0,90112
E 1,94208
s_add_u32 s51, s51, 65536
s_cmp_ge_u32 s51, s57
s_cselect_b32 s60, 2097152, 0
s_sub_u32 s51, s51, s60
s_waitcnt lgkmcnt(0)
s_waitcnt vmcnt(16)
s_barrier
E 0,98304
Ta 48,4,49152
Ta 52,5,49152
E 1,102400
s_add_u32 s51, s51, 65536
Ta 56,4,57344
Ta 60,5,57344
E 0,106496
E 1,110592
s_add_u32 s51, s51, 65536
s_waitcnt lgkmcnt(0)
s_waitcnt vmcnt(16)
s_barrier
v_add_u32 v4, 65536, v4
v_add_u32 v5, 65536, v5
E 0,114688
Ta 64,4,0
Ta 68,5,0
E 1,118784
s_add_u32 s51, s51, 65536
Ta 72,4,8192
Ta 76,5,8192
E 0,122880
E 1,126976
s_add_u32 s51, s51, 65536
s_waitcnt lgkmcnt(0)
s_waitcnt vmcnt(16)
s_barrier
E 0,131072
Ta 80,4,16384
Ta 84,5,16384
E 1,135168
s_add_u32 s51, s51, 65536
Ta 88,4,24576
Ta 92,5,24576
E 0,139264
E 1,143360
s_add_u32 s51, s51, 65536
s_waitcnt lgkmcnt(0)
s_waitcnt vmcnt(16)
s_barrier
v_add_u32 v4, -65536, v4
v_add_u32 v5, -65536, v5
E 0,147456
Ta 96,4,0
Ta 100,5,0
E 1,151552
s_add_u32 s51, s51, 65536
Ta 104,4,8192
Ta 108,5,8192
E 0,155648
E 1,159744
s_add_u32 s51, s51, 65536
s_cmp_ge_u32 s51, s57
s_cselect_b32 s60, 2097152, 0
s_sub_u32 s51, s51, s60
s_waitcnt lgkmcnt(0)
s_waitcnt vmcnt(16)
s_barrier
E 0,65536
Ta 112,4,16384
Ta 116,5,16384
E 1,69632
s_add_u32 s51, s51, 65536
Ta 120,4,24576
Ta 124,5,24576
E 0,73728
E 1,77824
s_add_u32 s51, s51, 65536
s_waitcnt lgkmcnt(0)
s_waitcnt vmcnt(16)
s_barrier
LsO0:
s_cbranch_execz LsO4
E 0,81920
Ta 128,4,32768
Ta 132,5,32768
E 1,86016
s_add_u32 s51, s51, 65536
Ta 136,4,40960
Ta 140,5,40960
E 0,90112
E 1,94208
s_add_u32 s51, s51, 65536
s_waitcnt lgkmcnt(0)
s_waitcnt vmcnt(16)
s_barrier
E 0,98304
Ta 144,4,49152
Ta 148,5,49152
E 1,102400
s_add_u32 s51, s51, 65536
Ta 152,4,57344
Ta 156,5,57344
E 0,106496
E 1,110592
s_add_u32 s51, s51, 65536
s_waitcnt lgkmcnt(0)
s_waitcnt vmcnt(16)
s_barrier
v_add_u32 v4, 65536, v4
v_add_u32 v5, 65536, v5
E 0,114688
Ta 160,4,0
Ta 164,5,0
E 1,118784
s_add_u32 s51, s51, 65536
Ta 168,4,8192
Ta 172,5,8192
E 0,122880
E 1,126976
s_add_u32 s51, s51, 65536
s_cmp_ge_u32 s51, s57
s_cselect_b32 s60, 2097152, 0
s_sub_u32 s51, s51, s60
s_waitcnt lgkmcnt(0)
s_waitcnt vmcnt(16)
s_barrier
Ta 176,4,16384
Ta 180,5,16384
Ta 184,4,24576
Ta 188,5,24576
s_waitcnt lgkmcnt(0)
s_waitcnt vmcnt(12)
s_barrier
v_add_u32 v4, -65536, v4
v_add_u32 v5, -65536, v5
Ta 192,4,0
Ta 196,5,0
Ta 200,4,8192
Ta 204,5,8192
s_waitcnt lgkmcnt(0)
s_waitcnt vmcnt(8)
s_barrier
Ta 208,4,16384
Ta 212,5,16384
Ta 216,4,24576
Ta 220,5,24576
s_waitcnt lgkmcnt(0)
s_waitcnt vmcnt(4)
s_barrier
Ta 224,4,32768
Ta 228,5,32768
Ta 232,4,40960
Ta 236,5,40960
s_waitcnt lgkmcnt(0)
s_waitcnt vmcnt(0)
s_barrier
Ta 240,4,49152
Ta 244,5,49152
Ta 248,4,57344
Ta 252,5,57344
s_waitcnt lgkmcnt(0)
s_waitcnt vmcnt(0)
s_barrier
s_nop 7
s_add_u32 s40, s36, 4194304
s_addc_u32 s41, s37, 0
s_and_b32 s60, s48, 3
s_cmp_eq_u32 s60, 1
s_cbranch_scc1 LvO1
s_cmp_eq_u32 s60, 2
s_cbranch_scc1 LvO2
s_cmp_eq_u32 s60, 3
s_cbranch_scc1 LvO3
LvO0:
LsO1:
s_cbranch_execz LsO5
buffer_load_dwordx2 v[60:61], v86, s[36:39], 0 offen sc1
s_waitcnt vmcnt(0)
ds_write_b32 v82, v60
v_sub_u32 v60, v60, v87
v_xor_b32 v61, v61, v57
v_cmp_lt_u64 vcc, s[62:63], v[60:61]
s_waitcnt lgkmcnt(0)
s_cbranch_vccnz CO0_1
BO0_1:
s_add_u32 s49, s49, 313344
s_barrier
buffer_load_dwordx4 v[48:51], v88, s[44:47], s54 offen sc1
s_add_u32 m0, s50, 0
s_nop 0
D 84,0
D 84,1024
D 84,2048
D 84,3072
s_add_u32 m0, s50, 4096
s_nop 0
D 85,0
D 85,1024
D 85,2048
D 85,3072
s_mov_b32 m0, 144384
s_nop 0
buffer_load_dword v86, s[36:39], 0 offen sc1 lds
s_waitcnt vmcnt(9)
v_lshlrev_b32 v0, 16, v48
v_and_b32 v1, 0xffff0000, v48
v_lshlrev_b32 v2, 16, v49
v_and_b32 v3, 0xffff0000, v49
v_lshlrev_b32 v4, 16, v50
v_and_b32 v5, 0xffff0000, v50
v_lshlrev_b32 v6, 16, v51
v_and_b32 v7, 0xffff0000, v51
s_sub_u32 s54, s54, 0x8000
buffer_load_dwordx4 v[48:51], v88, s[44:47], s54 offen sc1
buffer_load_dword v86, s[36:39], 0 offen sc1 lds
v_add_u32 v87, 1, v87
ds_read_b32 v60, v82 offset:0
s_waitcnt lgkmcnt(0)
v_cmp_gt_i32 vcc, v87, v60
s_waitcnt vmcnt(3)
s_add_u32 s49, s49, 313344
s_cbranch_vccnz CO0_2
BO0_2:
s_barrier
s_add_u32 m0, s50, 32768
R 16,0
D 84,0
R 20,1024
R 24,2048
D 84,1024
R 28,3072
R 32,4096
D 84,2048
s_waitcnt lgkmcnt(3)
mav 0,0,16
R 36,5120
mav 4,4,16
mav 0,8,20
R 40,6144
mav 4,12,20
s_waitcnt lgkmcnt(3)
D 84,3072
mav 0,16,24
R 44,7168
mav 4,20,24
s_add_u32 m0, s50, 36864
mav 0,24,28
R 16,8192
mav 4,28,28
s_waitcnt lgkmcnt(3)
D 85,0
mav 0,32,32
R 20,9216
mav 4,36,32
mav 0,40,36
R 24,10240
mav 4,44,36
s_waitcnt lgkmcnt(3)
D 85,1024
mav 0,48,40
R 28,11264
mav 4,52,40
mav 0,56,44
R 32,12288
mav 4,60,44
s_waitcnt lgkmcnt(3)
D 85,2048
mav 0,64,16
R 36,13312
mav 4,68,16
mav 0,72,20
R 40,14336
mav 4,76,20
s_waitcnt lgkmcnt(3)
D 85,3072
mav 0,80,24
R 44,15360
mav 4,84,24
mav 0,88,28
R 16,16384
mav 4,92,28
s_waitcnt lgkmcnt(3)
mav 0,96,32
R 20,17408
mav 4,100,32
mav 0,104,36
s_mov_b32 m0, 144384
R 24,18432
mav 4,108,36
s_waitcnt lgkmcnt(3)
mav 0,112,40
R 28,19456
mav 4,116,40
mav 0,120,44
s_waitcnt vmcnt(9)
R 32,20480
v_lshlrev_b32 v8, 16, v48
mav 4,124,44
s_waitcnt lgkmcnt(3)
buffer_load_dword v86, s[36:39], 0 offen sc1 lds
mav 0,128,16
R 36,21504
v_and_b32 v9, 0xffff0000, v48
mav 4,132,16
v_lshlrev_b32 v10, 16, v49
v_and_b32 v11, 0xffff0000, v49
mav 0,136,20
R 40,22528
v_lshlrev_b32 v12, 16, v50
mav 4,140,20
s_waitcnt lgkmcnt(3)
v_and_b32 v13, 0xffff0000, v50
v_lshlrev_b32 v14, 16, v51
mav 0,144,24
R 44,23552
v_and_b32 v15, 0xffff0000, v51
mav 4,148,24
s_sub_u32 s54, s54, 0x8000
mav 0,152,28
R 16,24576
mav 4,156,28
s_waitcnt lgkmcnt(3)
s_cmp_lt_u32 s54, s56
s_cselect_b64 s[44:45], s[40:41], s[44:45]
mav 0,160,32
R 20,25600
v_add_u32 v87, 1, v87
mav 4,164,32
mav 0,168,36
R 24,26624
mav 4,172,36
s_waitcnt lgkmcnt(3)
buffer_load_dwordx4 v[48:51], v88, s[44:47], s54 offen sc1
mav 0,176,40
R 28,27648
mav 4,180,40
mav 0,184,44
R 32,28672
mav 4,188,44
s_waitcnt lgkmcnt(3)
buffer_load_dword v86, s[36:39], 0 offen sc1 lds
mav 0,192,16
R 36,29696
mav 4,196,16
mav 0,200,20
R 40,30720
mav 4,204,20
s_waitcnt lgkmcnt(3)
mav 0,208,24
R 44,31744
mav 4,212,24
mav 0,216,28
mav 4,220,28
s_waitcnt lgkmcnt(2)
mav 0,224,32
mav 4,228,32
mav 0,232,36
mav 4,236,36
s_waitcnt lgkmcnt(0)
mav 0,240,40
mav 4,244,40
mav 0,248,44
mav 4,252,44
ds_read_b32 v60, v82 offset:0
s_nop 5
v_cvt_pk_bf16_f32 v52, v0, v1
v_cvt_pk_bf16_f32 v53, v2, v3
v_cvt_pk_bf16_f32 v54, v4, v5
v_cvt_pk_bf16_f32 v55, v6, v7
buffer_store_dwordx4 v[52:55], v83, s[36:39], s49 offen sc1
v_mul_f32 v59, v0, v0
v_add_f32 v58, v1, v0
v_fmac_f32 v59, v1, v1
v_add_f32 v58, v2, v58
v_fmac_f32 v59, v2, v2
v_add_f32 v58, v3, v58
v_fmac_f32 v59, v3, v3
v_add_f32 v58, v4, v58
v_fmac_f32 v59, v4, v4
v_add_f32 v58, v5, v58
v_fmac_f32 v59, v5, v5
v_add_f32 v58, v6, v58
v_fmac_f32 v59, v6, v6
v_add_f32 v58, v7, v58
v_fmac_f32 v59, v7, v7
s_nop 1
v_permlane32_swap_b32 v58, v59
v_add_f32 v58, v58, v59
ds_write_b32 v89, v58 offset:0
s_xor_b32 s50, s50, 0x10000
s_waitcnt lgkmcnt(1)
v_cmp_gt_i32 vcc, v87, v60
s_waitcnt vmcnt(4) lgkmcnt(0)
s_add_u32 s49, s49, 313344
s_cbranch_vccnz CO0_3
BO0_3:
s_barrier
s_add_u32 m0, s50, 0
R 16,32768
D 84,0
R 20,33792
R 24,34816
D 84,1024
R 28,35840
R 32,36864
D 84,2048
s_waitcnt lgkmcnt(3)
mav 8,0,16
R 36,37888
mav 12,4,16
mav 8,8,20
R 40,38912
mav 12,12,20
s_waitcnt lgkmcnt(3)
D 84,3072
mav 8,16,24
R 44,39936
mav 12,20,24
s_add_u32 m0, s50, 4096
mav 8,24,28
R 16,40960
mav 12,28,28
s_waitcnt lgkmcnt(3)
D 85,0
mav 8,32,32
R 20,41984
mav 12,36,32
mav 8,40,36
R 24,43008
mav 12,44,36
s_waitcnt lgkmcnt(3)
D 85,1024
mav 8,48,40
R 28,44032
v_add_u32 v56, 1, v56
mav 12,52,40
mav 8,56,44
R 32,45056
mav 12,60,44
s_waitcnt lgkmcnt(3)
D 85,2048
mav 8,64,16
R 36,46080
mav 12,68,16
mav 8,72,20
R 40,47104
mav 12,76,20
s_waitcnt lgkmcnt(3)
D 85,3072
mav 8,80,24
R 44,48128
mav 12,84,24
mav 8,88,28
R 16,49152
mav 12,92,28
s_waitcnt lgkmcnt(3)
mav 8,96,32
R 20,50176
mav 12,100,32
mav 8,104,36
s_mov_b32 m0, 144384
R 24,51200
mav 12,108,36
s_waitcnt lgkmcnt(3)
mav 8,112,40
R 28,52224
mav 12,116,40
mav 8,120,44
s_waitcnt vmcnt(10)
R 32,53248
v_lshlrev_b32 v0, 16, v48
mav 12,124,44
s_waitcnt lgkmcnt(3)
buffer_load_dword v86, s[36:39], 0 offen sc1 lds
mav 8,128,16
R 36,54272
v_and_b32 v1, 0xffff0000, v48
mav 12,132,16
v_lshlrev_b32 v2, 16, v49
v_and_b32 v3, 0xffff0000, v49
mav 8,136,20
R 40,55296
v_lshlrev_b32 v4, 16, v50
mav 12,140,20
s_waitcnt lgkmcnt(3)
v_and_b32 v5, 0xffff0000, v50
v_lshlrev_b32 v6, 16, v51
mav 8,144,24
s_waitcnt vmcnt(9)
R 44,56320
v_and_b32 v7, 0xffff0000, v51
mav 12,148,24
mav 8,152,28
s_sub_u32 s54, s54, 0x8000
R 16,57344
mav 12,156,28
s_waitcnt lgkmcnt(3)
buffer_store_dword v56, v81, s[36:39], s52 offen sc1
mav 8,160,32
R 20,58368
v_add_u32 v87, 1, v87
mav 12,164,32
mav 8,168,36
R 24,59392
mav 12,172,36
s_waitcnt lgkmcnt(3)
buffer_load_dwordx4 v[48:51], v88, s[44:47], s54 offen sc1
mav 8,176,40
R 28,60416
mav 12,180,40
mav 8,184,44
R 32,61440
mav 12,188,44
s_waitcnt lgkmcnt(3)
buffer_load_dword v86, s[36:39], 0 offen sc1 lds
mav 8,192,16
R 36,62464
mav 12,196,16
mav 8,200,20
R 40,63488
mav 12,204,20
s_waitcnt lgkmcnt(3)
mav 8,208,24
R 44,64512
mav 12,212,24
mav 8,216,28
mav 12,220,28
s_waitcnt lgkmcnt(2)
mav 8,224,32
mav 12,228,32
mav 8,232,36
s_mov_b32 s60, -30
.p2align 6
LO0_4:
mav 12,236,36
s_waitcnt lgkmcnt(0)
mav 8,240,40
mav 12,244,40
mav 8,248,44
mav 12,252,44
ds_read_b32 v60, v82 offset:0
s_nop 5
v_cvt_pk_bf16_f32 v52, v8, v9
v_cvt_pk_bf16_f32 v53, v10, v11
v_cvt_pk_bf16_f32 v54, v12, v13
v_cvt_pk_bf16_f32 v55, v14, v15
buffer_store_dwordx4 v[52:55], v83, s[36:39], s49 offen sc1
v_mul_f32 v59, v8, v8
v_add_f32 v58, v9, v8
v_fmac_f32 v59, v9, v9
v_add_f32 v58, v10, v58
v_fmac_f32 v59, v10, v10
v_add_f32 v58, v11, v58
v_fmac_f32 v59, v11, v11
v_add_f32 v58, v12, v58
v_fmac_f32 v59, v12, v12
v_add_f32 v58, v13, v58
v_fmac_f32 v59, v13, v13
v_add_f32 v58, v14, v58
v_fmac_f32 v59, v14, v14
v_add_f32 v58, v15, v58
v_fmac_f32 v59, v15, v15
s_nop 1
v_permlane32_swap_b32 v58, v59
v_add_f32 v58, v58, v59
ds_write_b32 v89, v58 offset:1024
s_waitcnt lgkmcnt(1)
v_cmp_gt_i32 vcc, v87, v60
v_xor_b32 v80, 0x10000, v80
s_waitcnt vmcnt(5) lgkmcnt(0)
s_add_u32 s49, s49, 313344
s_cbranch_vccnz CO0_5
BO0_5:
s_barrier
s_add_u32 m0, s50, 32768
R 16,0
D 84,0
R 20,1024
R 24,2048
D 84,1024
R 28,3072
R 32,4096
D 84,2048
s_waitcnt lgkmcnt(3)
mav 0,0,16
R 36,5120
mav 4,4,16
mav 0,8,20
R 40,6144
mav 4,12,20
s_waitcnt lgkmcnt(3)
D 84,3072
mav 0,16,24
R 44,7168
mav 4,20,24
s_add_u32 m0, s50, 36864
mav 0,24,28
R 16,8192
mav 4,28,28
s_waitcnt lgkmcnt(3)
D 85,0
mav 0,32,32
R 20,9216
mav 4,36,32
mav 0,40,36
R 24,10240
mav 4,44,36
s_waitcnt lgkmcnt(3)
D 85,1024
mav 0,48,40
R 28,11264
v_add_u32 v56, 1, v56
mav 4,52,40
mav 0,56,44
R 32,12288
mav 4,60,44
s_waitcnt lgkmcnt(3)
D 85,2048
mav 0,64,16
R 36,13312
mav 4,68,16
mav 0,72,20
R 40,14336
mav 4,76,20
s_waitcnt lgkmcnt(3)
D 85,3072
mav 0,80,24
R 44,15360
mav 4,84,24
mav 0,88,28
R 16,16384
LsO2:
s_cbranch_execz LsO6
mav 4,92,28
s_waitcnt lgkmcnt(3)
mav 0,96,32
R 20,17408
mav 4,100,32
mav 0,104,36
s_mov_b32 m0, 144384
R 24,18432
mav 4,108,36
s_waitcnt lgkmcnt(3)
mav 0,112,40
R 28,19456
mav 4,116,40
mav 0,120,44
s_waitcnt vmcnt(10)
R 32,20480
v_lshlrev_b32 v8, 16, v48
mav 4,124,44
s_waitcnt lgkmcnt(3)
buffer_load_dword v86, s[36:39], 0 offen sc1 lds
mav 0,128,16
R 36,21504
v_and_b32 v9, 0xffff0000, v48
mav 4,132,16
v_lshlrev_b32 v10, 16, v49
v_and_b32 v11, 0xffff0000, v49
mav 0,136,20
R 40,22528
v_lshlrev_b32 v12, 16, v50
mav 4,140,20
s_waitcnt lgkmcnt(3)
v_and_b32 v13, 0xffff0000, v50
v_lshlrev_b32 v14, 16, v51
mav 0,144,24
s_waitcnt vmcnt(9)
R 44,23552
v_and_b32 v15, 0xffff0000, v51
mav 4,148,24
s_sub_u32 s54, s54, 0x8000
mav 0,152,28
R 16,24576
mav 4,156,28
s_waitcnt lgkmcnt(3)
buffer_store_dword v56, v81, s[36:39], s52 offen sc1
mav 0,160,32
R 20,25600
v_add_u32 v87, 1, v87
mav 4,164,32
s_cmp_lt_u32 s54, s56
s_cselect_b64 s[44:45], s[40:41], s[44:45]
mav 0,168,36
R 24,26624
mav 4,172,36
s_waitcnt lgkmcnt(3)
buffer_load_dwordx4 v[48:51], v88, s[44:47], s54 offen sc1
mav 0,176,40
R 28,27648
mav 4,180,40
mav 0,184,44
R 32,28672
mav 4,188,44
s_waitcnt lgkmcnt(3)
buffer_load_dword v86, s[36:39], 0 offen sc1 lds
mav 0,192,16
R 36,29696
mav 4,196,16
mav 0,200,20
R 40,30720
mav 4,204,20
s_waitcnt lgkmcnt(3)
mav 0,208,24
R 44,31744
mav 4,212,24
mav 0,216,28
mav 4,220,28
s_waitcnt lgkmcnt(2)
mav 0,224,32
mav 4,228,32
mav 0,232,36
mav 4,236,36
s_waitcnt lgkmcnt(0)
mav 0,240,40
mav 4,244,40
mav 0,248,44
mav 4,252,44
ds_read_b32 v60, v82 offset:0
s_nop 5
v_cvt_pk_bf16_f32 v52, v0, v1
v_cvt_pk_bf16_f32 v53, v2, v3
v_cvt_pk_bf16_f32 v54, v4, v5
v_cvt_pk_bf16_f32 v55, v6, v7
buffer_store_dwordx4 v[52:55], v83, s[36:39], s49 offen sc1
v_mul_f32 v59, v0, v0
v_add_f32 v58, v1, v0
v_fmac_f32 v59, v1, v1
v_add_f32 v58, v2, v58
v_fmac_f32 v59, v2, v2
v_add_f32 v58, v3, v58
v_fmac_f32 v59, v3, v3
v_add_f32 v58, v4, v58
v_fmac_f32 v59, v4, v4
v_add_f32 v58, v5, v58
v_fmac_f32 v59, v5, v5
v_add_f32 v58, v6, v58
v_fmac_f32 v59, v6, v6
v_add_f32 v58, v7, v58
v_fmac_f32 v59, v7, v7
s_nop 1
v_permlane32_swap_b32 v58, v59
v_add_f32 v58, v58, v59
ds_write_b32 v89, v58 offset:0
s_xor_b32 s50, s50, 0x10000
s_waitcnt lgkmcnt(1)
v_cmp_gt_i32 vcc, v87, v60
s_waitcnt vmcnt(5) lgkmcnt(0)
s_add_u32 s49, s49, 313344
s_cbranch_vccnz CO0_6
BO0_6:
s_barrier
s_add_u32 m0, s50, 0
R 16,32768
D 84,0
R 20,33792
R 24,34816
D 84,1024
R 28,35840
R 32,36864
D 84,2048
s_waitcnt lgkmcnt(3)
mav 8,0,16
R 36,37888
mav 12,4,16
mav 8,8,20
R 40,38912
mav 12,12,20
s_waitcnt lgkmcnt(3)
D 84,3072
mav 8,16,24
R 44,39936
mav 12,20,24
s_add_u32 m0, s50, 4096
mav 8,24,28
R 16,40960
mav 12,28,28
s_waitcnt lgkmcnt(3)
D 85,0
mav 8,32,32
R 20,41984
mav 12,36,32
mav 8,40,36
R 24,43008
mav 12,44,36
s_waitcnt lgkmcnt(3)
D 85,1024
mav 8,48,40
R 28,44032
v_add_u32 v56, 1, v56
mav 12,52,40
mav 8,56,44
R 32,45056
mav 12,60,44
s_waitcnt lgkmcnt(3)
D 85,2048
mav 8,64,16
R 36,46080
mav 12,68,16
mav 8,72,20
R 40,47104
mav 12,76,20
s_waitcnt lgkmcnt(3)
D 85,3072
mav 8,80,24
R 44,48128
mav 12,84,24
mav 8,88,28
R 16,49152
mav 12,92,28
s_waitcnt lgkmcnt(3)
mav 8,96,32
R 20,50176
mav 12,100,32
mav 8,104,36
s_mov_b32 m0, 144384
R 24,51200
mav 12,108,36
s_waitcnt lgkmcnt(3)
mav 8,112,40
R 28,52224
mav 12,116,40
mav 8,120,44
s_waitcnt vmcnt(10)
R 32,53248
v_lshlrev_b32 v0, 16, v48
mav 12,124,44
s_waitcnt lgkmcnt(3)
buffer_load_dword v86, s[36:39], 0 offen sc1 lds
mav 8,128,16
R 36,54272
v_and_b32 v1, 0xffff0000, v48
mav 12,132,16
v_lshlrev_b32 v2, 16, v49
v_and_b32 v3, 0xffff0000, v49
mav 8,136,20
R 40,55296
v_lshlrev_b32 v4, 16, v50
mav 12,140,20
s_waitcnt lgkmcnt(3)
v_and_b32 v5, 0xffff0000, v50
v_lshlrev_b32 v6, 16, v51
mav 8,144,24
s_waitcnt vmcnt(9)
R 44,56320
v_and_b32 v7, 0xffff0000, v51
mav 12,148,24
mav 8,152,28
s_sub_u32 s54, s54, 0x8000
R 16,57344
mav 12,156,28
s_waitcnt lgkmcnt(3)
buffer_store_dword v56, v81, s[36:39], s52 offen sc1
mav 8,160,32
R 20,58368
v_add_u32 v87, 1, v87
mav 12,164,32
mav 8,168,36
R 24,59392
mav 12,172,36
s_waitcnt lgkmcnt(3)
buffer_load_dwordx4 v[48:51], v88, s[44:47], s54 offen sc1
mav 8,176,40
R 28,60416
mav 12,180,40
mav 8,184,44
R 32,61440
mav 12,188,44
s_waitcnt lgkmcnt(3)
buffer_load_dword v86, s[36:39], 0 offen sc1 lds
mav 8,192,16
R 36,62464
mav 12,196,16
mav 8,200,20
R 40,63488
mav 12,204,20
s_waitcnt lgkmcnt(3)
mav 8,208,24
R 44,64512
mav 12,212,24
mav 8,216,28
mav 12,220,28
s_waitcnt lgkmcnt(2)
mav 8,224,32
mav 12,228,32
s_add_u32 s60, s60, 1
mav 8,232,36
s_cbranch_scc0 LO0_4
mav 12,236,36
s_waitcnt lgkmcnt(0)
mav 8,240,40
mav 12,244,40
mav 8,248,44
mav 12,252,44
ds_read_b32 v60, v82 offset:0
s_nop 5
v_cvt_pk_bf16_f32 v52, v8, v9
v_cvt_pk_bf16_f32 v53, v10, v11
v_cvt_pk_bf16_f32 v54, v12, v13
v_cvt_pk_bf16_f32 v55, v14, v15
buffer_store_dwordx4 v[52:55], v83, s[36:39], s49 offen sc1
v_mul_f32 v59, v8, v8
v_add_f32 v58, v9, v8
v_fmac_f32 v59, v9, v9
v_add_f32 v58, v10, v58
v_fmac_f32 v59, v10, v10
v_add_f32 v58, v11, v58
v_fmac_f32 v59, v11, v11
v_add_f32 v58, v12, v58
v_fmac_f32 v59, v12, v12
v_add_f32 v58, v13, v58
v_fmac_f32 v59, v13, v13
v_add_f32 v58, v14, v58
v_fmac_f32 v59, v14, v14
v_add_f32 v58, v15, v58
v_fmac_f32 v59, v15, v15
s_nop 1
v_permlane32_swap_b32 v58, v59
v_add_f32 v58, v58, v59
ds_write_b32 v89, v58 offset:1024
s_waitcnt lgkmcnt(1)
v_cmp_gt_i32 vcc, v87, v60
v_xor_b32 v80, 0x10000, v80
s_waitcnt vmcnt(5) lgkmcnt(0)
s_add_u32 s49, s49, 313344
s_cbranch_vccnz CO0_7
BO0_7:
s_barrier
s_add_u32 m0, s50, 32768
R 16,0
D 84,0
R 20,1024
R 24,2048
D 84,1024
R 28,3072
R 32,4096
D 84,2048
s_waitcnt lgkmcnt(3)
mav 0,0,16
R 36,5120
mav 4,4,16
mav 0,8,20
R 40,6144
mav 4,12,20
s_waitcnt lgkmcnt(3)
D 84,3072
mav 0,16,24
R 44,7168
mav 4,20,24
s_add_u32 m0, s50, 36864
mav 0,24,28
R 16,8192
mav 4,28,28
s_waitcnt lgkmcnt(3)
D 85,0
mav 0,32,32
R 20,9216
mav 4,36,32
mav 0,40,36
R 24,10240
mav 4,44,36
s_waitcnt lgkmcnt(3)
D 85,1024
mav 0,48,40
R 28,11264
v_add_u32 v56, 1, v56
mav 4,52,40
mav 0,56,44
R 32,12288
mav 4,60,44
s_waitcnt lgkmcnt(3)
D 85,2048
mav 0,64,16
R 36,13312
mav 4,68,16
mav 0,72,20
R 40,14336
mav 4,76,20
s_waitcnt lgkmcnt(3)
D 85,3072
mav 0,80,24
R 44,15360
mav 4,84,24
mav 0,88,28
R 16,16384
mav 4,92,28
s_waitcnt lgkmcnt(3)
mav 0,96,32
R 20,17408
mav 4,100,32
mav 0,104,36
R 24,18432
mav 4,108,36
s_waitcnt lgkmcnt(3)
mav 0,112,40
R 28,19456
mav 4,116,40
mav 0,120,44
s_waitcnt vmcnt(10)
R 32,20480
v_lshlrev_b32 v8, 16, v48
mav 4,124,44
s_waitcnt lgkmcnt(3)
v_and_b32 v9, 0xffff0000, v48
v_lshlrev_b32 v10, 16, v49
mav 0,128,16
R 36,21504
v_and_b32 v11, 0xffff0000, v49
mav 4,132,16
v_lshlrev_b32 v12, 16, v50
v_and_b32 v13, 0xffff0000, v50
mav 0,136,20
R 40,22528
v_lshlrev_b32 v14, 16, v51
mav 4,140,20
s_waitcnt lgkmcnt(3)
v_and_b32 v15, 0xffff0000, v51
mav 0,144,24
s_waitcnt vmcnt(8)
R 44,23552
mav 4,148,24
mav 0,152,28
R 16,24576
mav 4,156,28
s_waitcnt lgkmcnt(3)
buffer_store_dword v56, v81, s[36:39], s52 offen sc1
mav 0,160,32
R 20,25600
mav 4,164,32
mav 0,168,36
R 24,26624
mav 4,172,36
s_waitcnt lgkmcnt(3)
mav 0,176,40
R 28,27648
mav 4,180,40
mav 0,184,44
R 32,28672
mav 4,188,44
s_waitcnt lgkmcnt(3)
mav 0,192,16
R 36,29696
mav 4,196,16
mav 0,200,20
R 40,30720
mav 4,204,20
s_waitcnt lgkmcnt(3)
mav 0,208,24
R 44,31744
mav 4,212,24
mav 0,216,28
mav 4,220,28
s_waitcnt lgkmcnt(2)
mav 0,224,32
mav 4,228,32
mav 0,232,36
mav 4,236,36
s_waitcnt lgkmcnt(0)
mav 0,240,40
mav 4,244,40
mav 0,248,44
mav 4,252,44
s_nop 6
v_cvt_pk_bf16_f32 v52, v0, v1
v_cvt_pk_bf16_f32 v53, v2, v3
v_cvt_pk_bf16_f32 v54, v4, v5
v_cvt_pk_bf16_f32 v55, v6, v7
buffer_store_dwordx4 v[52:55], v83, s[36:39], s49 offen sc1
v_mul_f32 v59, v0, v0
v_add_f32 v58, v1, v0
v_fmac_f32 v59, v1, v1
v_add_f32 v58, v2, v58
v_fmac_f32 v59, v2, v2
v_add_f32 v58, v3, v58
v_fmac_f32 v59, v3, v3
v_add_f32 v58, v4, v58
v_fmac_f32 v59, v4, v4
v_add_f32 v58, v5, v58
v_fmac_f32 v59, v5, v5
v_add_f32 v58, v6, v58
v_fmac_f32 v59, v6, v6
v_add_f32 v58, v7, v58
v_fmac_f32 v59, v7, v7
s_nop 1
v_permlane32_swap_b32 v58, v59
v_add_f32 v58, v58, v59
ds_write_b32 v89, v58 offset:0
s_waitcnt vmcnt(2) lgkmcnt(0)
s_add_u32 s49, s49, 313344
s_barrier
R 16,32768
R 20,33792
R 24,34816
R 28,35840
R 32,36864
s_waitcnt lgkmcnt(3)
mav 8,0,16
R 36,37888
mav 12,4,16
mav 8,8,20
R 40,38912
mav 12,12,20
s_waitcnt lgkmcnt(3)
mav 8,16,24
R 44,39936
mav 12,20,24
mav 8,24,28
R 16,40960
mav 12,28,28
s_waitcnt lgkmcnt(3)
mav 8,32,32
R 20,41984
mav 12,36,32
mav 8,40,36
R 24,43008
mav 12,44,36
s_waitcnt lgkmcnt(3)
mav 8,48,40
R 28,44032
v_add_u32 v56, 1, v56
mav 12,52,40
mav 8,56,44
R 32,45056
mav 12,60,44
s_waitcnt lgkmcnt(3)
mav 8,64,16
R 36,46080
mav 12,68,16
mav 8,72,20
R 40,47104
mav 12,76,20
s_waitcnt lgkmcnt(3)
mav 8,80,24
R 44,48128
mav 12,84,24
mav 8,88,28
R 16,49152
mav 12,92,28
s_waitcnt lgkmcnt(3)
mav 8,96,32
R 20,50176
mav 12,100,32
mav 8,104,36
R 24,51200
mav 12,108,36
s_waitcnt lgkmcnt(3)
mav 8,112,40
R 28,52224
mav 12,116,40
mav 8,120,44
R 32,53248
mav 12,124,44
s_waitcnt lgkmcnt(3)
mav 8,128,16
R 36,54272
mav 12,132,16
mav 8,136,20
R 40,55296
mav 12,140,20
s_waitcnt lgkmcnt(3)
mav 8,144,24
s_waitcnt vmcnt(0)
R 44,56320
mav 12,148,24
mav 8,152,28
R 16,57344
mav 12,156,28
s_waitcnt lgkmcnt(3)
buffer_store_dword v56, v81, s[36:39], s52 offen sc1
mav 8,160,32
R 20,58368
mav 12,164,32
mav 8,168,36
R 24,59392
mav 12,172,36
s_waitcnt lgkmcnt(3)
mav 8,176,40
R 28,60416
mav 12,180,40
mav 8,184,44
R 32,61440
mav 12,188,44
s_waitcnt lgkmcnt(3)
mav 8,192,16
R 36,62464
mav 12,196,16
mav 8,200,20
R 40,63488
mav 12,204,20
s_waitcnt lgkmcnt(3)
mav 8,208,24
R 44,64512
mav 12,212,24
mav 8,216,28
mav 12,220,28
s_waitcnt lgkmcnt(2)
mav 8,224,32
mav 12,228,32
mav 8,232,36
mav 12,236,36
s_waitcnt lgkmcnt(0)
mav 8,240,40
mav 12,244,40
mav 8,248,44
mav 12,252,44
s_nop 6
v_cvt_pk_bf16_f32 v52, v8, v9
v_cvt_pk_bf16_f32 v53, v10, v11
v_cvt_pk_bf16_f32 v54, v12, v13
v_cvt_pk_bf16_f32 v55, v14, v15
buffer_store_dwordx4 v[52:55], v83, s[36:39], s49 offen sc1
v_mul_f32 v59, v8, v8
v_add_f32 v58, v9, v8
v_fmac_f32 v59, v9, v9
v_add_f32 v58, v10, v58
v_fmac_f32 v59, v10, v10
v_add_f32 v58, v11, v58
v_fmac_f32 v59, v11, v11
v_add_f32 v58, v12, v58
v_fmac_f32 v59, v12, v12
v_add_f32 v58, v13, v58
v_fmac_f32 v59, v13, v13
v_add_f32 v58, v14, v58
v_fmac_f32 v59, v14, v14
v_add_f32 v58, v15, v58
v_fmac_f32 v59, v15, v15
s_nop 1
v_permlane32_swap_b32 v58, v59
v_add_f32 v58, v58, v59
ds_write_b32 v89, v58 offset:1024
s_waitcnt lgkmcnt(0)
s_add_u32 s49, s49, 313344
s_barrier
v_add_u32 v56, 37, v56
s_waitcnt vmcnt(0)
buffer_store_dword v56, v81, s[36:39], s52 offen sc1
LsO3:
s_cbranch_execz LsO7
s_endpgm
CO0_1:
s_cmp_eq_u32 s61, 0
s_cbranch_scc1 BO0_1
s_sub_u32 s61, s61, 1
s_sleep 8
buffer_load_dwordx2 v[60:61], v86, s[36:39], 0 offen sc1
s_waitcnt vmcnt(0)
ds_write_b32 v82, v60
v_sub_u32 v60, v60, v87
v_xor_b32 v61, v61, v57
v_cmp_lt_u64 vcc, s[62:63], v[60:61]
s_waitcnt lgkmcnt(0)
s_cbranch_vccnz CO0_1
s_branch BO0_1
CO0_2:
s_mov_b32 s58, m0
s_mov_b32 m0, 144384
DO0_2:
s_cmp_eq_u32 s61, 0
s_cbranch_scc1 EO0_2
s_sub_u32 s61, s61, 1
buffer_load_dword v86, s[36:39], 0 offen sc1 lds
s_sleep 1
ds_read_b32 v60, v82
s_waitcnt lgkmcnt(0)
v_cmp_gt_i32 vcc, v87, v60
s_cbranch_vccnz DO0_2
EO0_2:
s_mov_b32 m0, s58
s_branch BO0_2
CO0_3:
s_mov_b32 s58, m0
s_mov_b32 m0, 144384
DO0_3:
s_cmp_eq_u32 s61, 0
s_cbranch_scc1 EO0_3
s_sub_u32 s61, s61, 1
buffer_load_dword v86, s[36:39], 0 offen sc1 lds
s_sleep 1
ds_read_b32 v60, v82
s_waitcnt lgkmcnt(0)
v_cmp_gt_i32 vcc, v87, v60
s_cbranch_vccnz DO0_3
EO0_3:
s_mov_b32 m0, s58
s_branch BO0_3
CO0_5:
s_mov_b32 s58, m0
s_mov_b32 m0, 144384
DO0_5:
s_cmp_eq_u32 s61, 0
s_cbranch_scc1 EO0_5
s_sub_u32 s61, s61, 1
buffer_load_dword v86, s[36:39], 0 offen sc1 lds
s_sleep 1
ds_read_b32 v60, v82
s_waitcnt lgkmcnt(0)
v_cmp_gt_i32 vcc, v87, v60
s_cbranch_vccnz DO0_5
EO0_5:
s_mov_b32 m0, s58
s_branch BO0_5
CO0_6:
s_mov_b32 s58, m0
s_mov_b32 m0, 144384
DO0_6:
s_cmp_eq_u32 s61, 0
s_cbranch_scc1 EO0_6
s_sub_u32 s61, s61, 1
buffer_load_dword v86, s[36:39], 0 offen sc1 lds
s_sleep 1
ds_read_b32 v60, v82
s_waitcnt lgkmcnt(0)
v_cmp_gt_i32 vcc, v87, v60
s_cbranch_vccnz DO0_6
EO0_6:
s_mov_b32 m0, s58
s_branch BO0_6
CO0_7:
s_mov_b32 s58, m0
s_mov_b32 m0, 144384
DO0_7:
s_cmp_eq_u32 s61, 0
s_cbranch_scc1 EO0_7
s_sub_u32 s61, s61, 1
buffer_load_dword v86, s[36:39], 0 offen sc1 lds
s_sleep 1
ds_read_b32 v60, v82
s_waitcnt lgkmcnt(0)
v_cmp_gt_i32 vcc, v87, v60
s_cbranch_vccnz DO0_7
EO0_7:
s_mov_b32 m0, s58
s_branch BO0_7
)"
R"(
LvO1:
LsO4:
s_cbranch_execz LsO8
s_add_u32 s49, s49, 313344
s_barrier
buffer_load_dwordx4 v[48:51], v88, s[44:47], s54 offen sc1
s_add_u32 m0, s50, 0
s_nop 0
D 84,0
D 84,1024
D 84,2048
D 84,3072
s_add_u32 m0, s50, 4096
s_nop 0
D 85,0
D 85,1024
D 85,2048
D 85,3072
s_waitcnt vmcnt(8)
v_lshlrev_b32 v0, 16, v48
v_and_b32 v1, 0xffff0000, v48
v_lshlrev_b32 v2, 16, v49
v_and_b32 v3, 0xffff0000, v49
v_lshlrev_b32 v4, 16, v50
v_and_b32 v5, 0xffff0000, v50
v_lshlrev_b32 v6, 16, v51
v_and_b32 v7, 0xffff0000, v51
s_sub_u32 s54, s54, 0x8000
buffer_load_dwordx4 v[48:51], v88, s[44:47], s54 offen sc1
s_waitcnt vmcnt(1)
s_add_u32 s49, s49, 313344
s_barrier
s_add_u32 m0, s50, 32768
R 16,0
D 84,0
R 20,1024
R 24,2048
D 84,1024
R 28,3072
R 32,4096
D 84,2048
s_waitcnt lgkmcnt(3)
mav 0,0,16
R 36,5120
mav 4,4,16
mav 0,8,20
R 40,6144
mav 4,12,20
s_waitcnt lgkmcnt(3)
mav 0,16,24
R 44,7168
mav 4,20,24
mav 0,24,28
D 84,3072
mav 4,28,28
s_waitcnt lgkmcnt(2)
R 16,8192
s_add_u32 m0, s50, 36864
mav 0,32,32
R 20,9216
mav 4,36,32
mav 0,40,36
D 85,0
mav 4,44,36
s_waitcnt lgkmcnt(2)
R 24,10240
mav 0,48,40
R 28,11264
mav 4,52,40
mav 0,56,44
D 85,1024
mav 4,60,44
s_waitcnt lgkmcnt(2)
R 32,12288
mav 0,64,16
R 36,13312
mav 4,68,16
mav 0,72,20
D 85,2048
mav 4,76,20
s_waitcnt lgkmcnt(2)
R 40,14336
mav 0,80,24
R 44,15360
mav 4,84,24
mav 0,88,28
D 85,3072
mav 4,92,28
s_waitcnt lgkmcnt(2)
R 16,16384
mav 0,96,32
R 20,17408
mav 4,100,32
mav 0,104,36
R 24,18432
mav 4,108,36
s_waitcnt lgkmcnt(3)
mav 0,112,40
R 28,19456
mav 4,116,40
mav 0,120,44
s_waitcnt vmcnt(8)
R 32,20480
v_lshlrev_b32 v8, 16, v48
mav 4,124,44
s_waitcnt lgkmcnt(3)
v_and_b32 v9, 0xffff0000, v48
v_lshlrev_b32 v10, 16, v49
mav 0,128,16
R 36,21504
v_and_b32 v11, 0xffff0000, v49
mav 4,132,16
v_lshlrev_b32 v12, 16, v50
v_and_b32 v13, 0xffff0000, v50
mav 0,136,20
R 40,22528
v_lshlrev_b32 v14, 16, v51
mav 4,140,20
s_waitcnt lgkmcnt(3)
v_and_b32 v15, 0xffff0000, v51
mav 0,144,24
R 44,23552
mav 4,148,24
s_sub_u32 s54, s54, 0x8000
mav 0,152,28
R 16,24576
mav 4,156,28
s_waitcnt lgkmcnt(3)
s_cmp_lt_u32 s54, s56
s_cselect_b64 s[44:45], s[40:41], s[44:45]
mav 0,160,32
R 20,25600
mav 4,164,32
mav 0,168,36
buffer_load_dwordx4 v[48:51], v88, s[44:47], s54 offen sc1
mav 4,172,36
s_waitcnt lgkmcnt(2)
R 24,26624
mav 0,176,40
R 28,27648
mav 4,180,40
mav 0,184,44
R 32,28672
mav 4,188,44
s_waitcnt lgkmcnt(3)
mav 0,192,16
R 36,29696
mav 4,196,16
mav 0,200,20
R 40,30720
mav 4,204,20
s_waitcnt lgkmcnt(3)
mav 0,208,24
R 44,31744
mav 4,212,24
mav 0,216,28
mav 4,220,28
s_waitcnt lgkmcnt(2)
mav 0,224,32
mav 4,228,32
mav 0,232,36
mav 4,236,36
s_waitcnt lgkmcnt(0)
mav 0,240,40
mav 4,244,40
mav 0,248,44
mav 4,252,44
s_nop 6
v_cvt_pk_bf16_f32 v52, v0, v1
v_cvt_pk_bf16_f32 v53, v2, v3
v_cvt_pk_bf16_f32 v54, v4, v5
v_cvt_pk_bf16_f32 v55, v6, v7
buffer_store_dwordx4 v[52:55], v83, s[36:39], s49 offen sc1
v_mul_f32 v59, v0, v0
v_add_f32 v58, v1, v0
v_fmac_f32 v59, v1, v1
v_add_f32 v58, v2, v58
v_fmac_f32 v59, v2, v2
v_add_f32 v58, v3, v58
v_fmac_f32 v59, v3, v3
v_add_f32 v58, v4, v58
v_fmac_f32 v59, v4, v4
v_add_f32 v58, v5, v58
v_fmac_f32 v59, v5, v5
v_add_f32 v58, v6, v58
v_fmac_f32 v59, v6, v6
v_add_f32 v58, v7, v58
v_fmac_f32 v59, v7, v7
s_nop 1
v_permlane32_swap_b32 v58, v59
v_add_f32 v58, v58, v59
ds_write_b32 v89, v58 offset:0
s_xor_b32 s50, s50, 0x10000
s_waitcnt vmcnt(2) lgkmcnt(0)
s_add_u32 s49, s49, 313344
s_barrier
s_add_u32 m0, s50, 0
R 16,32768
ds_read_b128 v[62:65], v90 offset:0
D 84,0
R 20,33792
R 24,34816
D 84,1024
R 28,35840
s_waitcnt lgkmcnt(3)
v_add_f32 v62, v62, v63
v_add_f32 v64, v64, v65
v_add_f32 v62, v62, v64
s_nop 1
v_add_f32_dpp v63, v62, v62 quad_perm:[1,0,3,2] row_mask:0xf bank_mask:0xf
buffer_store_dword v63, v91, s[36:39], s49 offen sc1
R 32,36864
D 84,2048
mav 8,0,16
R 36,37888
mav 12,4,16
s_waitcnt lgkmcnt(3)
mav 8,8,20
R 40,38912
mav 12,12,20
mav 8,16,24
R 44,39936
mav 12,20,24
s_waitcnt lgkmcnt(3)
mav 8,24,28
D 84,3072
mav 12,28,28
s_add_u32 m0, s50, 4096
R 16,40960
mav 8,32,32
R 20,41984
mav 12,36,32
s_waitcnt lgkmcnt(3)
mav 8,40,36
D 85,0
mav 12,44,36
R 24,43008
mav 8,48,40
R 28,44032
v_add_u32 v56, 1, v56
mav 12,52,40
s_waitcnt lgkmcnt(3)
mav 8,56,44
D 85,1024
mav 12,60,44
R 32,45056
mav 8,64,16
R 36,46080
mav 12,68,16
s_waitcnt lgkmcnt(3)
mav 8,72,20
D 85,2048
mav 12,76,20
R 40,47104
mav 8,80,24
R 44,48128
mav 12,84,24
s_waitcnt lgkmcnt(3)
mav 8,88,28
D 85,3072
mav 12,92,28
R 16,49152
mav 8,96,32
R 20,50176
mav 12,100,32
s_waitcnt lgkmcnt(3)
mav 8,104,36
R 24,51200
mav 12,108,36
mav 8,112,40
R 28,52224
mav 12,116,40
s_waitcnt lgkmcnt(3)
mav 8,120,44
s_waitcnt vmcnt(10)
R 32,53248
v_lshlrev_b32 v0, 16, v48
mav 12,124,44
v_and_b32 v1, 0xffff0000, v48
v_lshlrev_b32 v2, 16, v49
mav 8,128,16
R 36,54272
v_and_b32 v3, 0xffff0000, v49
mav 12,132,16
s_waitcnt lgkmcnt(3)
v_lshlrev_b32 v4, 16, v50
v_and_b32 v5, 0xffff0000, v50
mav 8,136,20
R 40,55296
v_lshlrev_b32 v6, 16, v51
mav 12,140,20
v_and_b32 v7, 0xffff0000, v51
mav 8,144,24
s_waitcnt vmcnt(9)
R 44,56320
mav 12,148,24
s_waitcnt lgkmcnt(3)
mav 8,152,28
buffer_store_dword v56, v81, s[36:39], s52 offen sc1
mav 12,156,28
s_sub_u32 s54, s54, 0x8000
R 16,57344
mav 8,160,32
R 20,58368
mav 12,164,32
s_waitcnt lgkmcnt(3)
mav 8,168,36
buffer_load_dwordx4 v[48:51], v88, s[44:47], s54 offen sc1
mav 12,172,36
R 24,59392
mav 8,176,40
R 28,60416
mav 12,180,40
s_waitcnt lgkmcnt(3)
mav 8,184,44
R 32,61440
mav 12,188,44
mav 8,192,16
R 36,62464
mav 12,196,16
s_waitcnt lgkmcnt(3)
mav 8,200,20
R 40,63488
mav 12,204,20
mav 8,208,24
R 44,64512
mav 12,212,24
s_waitcnt lgkmcnt(3)
mav 8,216,28
mav 12,220,28
mav 8,224,32
mav 12,228,32
s_waitcnt lgkmcnt(1)
mav 8,232,36
s_mov_b32 s60, -30
.p2align 6
LO1_1:
mav 12,236,36
mav 8,240,40
mav 12,244,40
s_waitcnt lgkmcnt(0)
mav 8,248,44
mav 12,252,44
s_nop 6
v_cvt_pk_bf16_f32 v52, v8, v9
v_cvt_pk_bf16_f32 v53, v10, v11
v_cvt_pk_bf16_f32 v54, v12, v13
v_cvt_pk_bf16_f32 v55, v14, v15
buffer_store_dwordx4 v[52:55], v83, s[36:39], s49 offen sc1
v_mul_f32 v59, v8, v8
v_add_f32 v58, v9, v8
v_fmac_f32 v59, v9, v9
v_add_f32 v58, v10, v58
v_fmac_f32 v59, v10, v10
v_add_f32 v58, v11, v58
v_fmac_f32 v59, v11, v11
v_add_f32 v58, v12, v58
v_fmac_f32 v59, v12, v12
v_add_f32 v58, v13, v58
v_fmac_f32 v59, v13, v13
v_add_f32 v58, v14, v58
v_fmac_f32 v59, v14, v14
v_add_f32 v58, v15, v58
v_fmac_f32 v59, v15, v15
s_nop 1
v_permlane32_swap_b32 v58, v59
v_add_f32 v58, v58, v59
ds_write_b32 v89, v58 offset:1024
v_xor_b32 v80, 0x10000, v80
s_waitcnt vmcnt(3) lgkmcnt(0)
s_add_u32 s49, s49, 313344
s_barrier
s_add_u32 m0, s50, 32768
R 16,0
ds_read_b128 v[62:65], v90 offset:1024
D 84,0
R 20,1024
R 24,2048
D 84,1024
R 28,3072
s_waitcnt lgkmcnt(3)
v_add_f32 v62, v62, v63
v_add_f32 v64, v64, v65
v_add_f32 v62, v62, v64
s_nop 1
v_add_f32_dpp v63, v62, v62 quad_perm:[1,0,3,2] row_mask:0xf bank_mask:0xf
buffer_store_dword v63, v91, s[36:39], s49 offen sc1
R 32,4096
D 84,2048
mav 0,0,16
R 36,5120
mav 4,4,16
s_waitcnt lgkmcnt(3)
mav 0,8,20
R 40,6144
mav 4,12,20
mav 0,16,24
R 44,7168
mav 4,20,24
s_waitcnt lgkmcnt(3)
mav 0,24,28
D 84,3072
mav 4,28,28
s_add_u32 m0, s50, 36864
R 16,8192
mav 0,32,32
R 20,9216
mav 4,36,32
s_waitcnt lgkmcnt(3)
mav 0,40,36
D 85,0
mav 4,44,36
R 24,10240
mav 0,48,40
R 28,11264
v_add_u32 v56, 1, v56
mav 4,52,40
s_waitcnt lgkmcnt(3)
mav 0,56,44
D 85,1024
mav 4,60,44
R 32,12288
mav 0,64,16
R 36,13312
mav 4,68,16
s_waitcnt lgkmcnt(3)
mav 0,72,20
D 85,2048
mav 4,76,20
R 40,14336
mav 0,80,24
R 44,15360
mav 4,84,24
s_waitcnt lgkmcnt(3)
mav 0,88,28
D 85,3072
mav 4,92,28
R 16,16384
mav 0,96,32
R 20,17408
mav 4,100,32
s_waitcnt lgkmcnt(3)
mav 0,104,36
R 24,18432
mav 4,108,36
mav 0,112,40
R 28,19456
mav 4,116,40
s_waitcnt lgkmcnt(3)
mav 0,120,44
s_waitcnt vmcnt(10)
R 32,20480
v_lshlrev_b32 v8, 16, v48
mav 4,124,44
v_and_b32 v9, 0xffff0000, v48
v_lshlrev_b32 v10, 16, v49
mav 0,128,16
R 36,21504
v_and_b32 v11, 0xffff0000, v49
mav 4,132,16
s_waitcnt lgkmcnt(3)
v_lshlrev_b32 v12, 16, v50
v_and_b32 v13, 0xffff0000, v50
mav 0,136,20
R 40,22528
v_lshlrev_b32 v14, 16, v51
mav 4,140,20
v_and_b32 v15, 0xffff0000, v51
mav 0,144,24
s_waitcnt vmcnt(9)
R 44,23552
mav 4,148,24
s_waitcnt lgkmcnt(3)
s_sub_u32 s54, s54, 0x8000
mav 0,152,28
buffer_store_dword v56, v81, s[36:39], s52 offen sc1
mav 4,156,28
R 16,24576
mav 0,160,32
R 20,25600
mav 4,164,32
s_waitcnt lgkmcnt(3)
s_cmp_lt_u32 s54, s56
s_cselect_b64 s[44:45], s[40:41], s[44:45]
mav 0,168,36
buffer_load_dwordx4 v[48:51], v88, s[44:47], s54 offen sc1
mav 4,172,36
R 24,26624
mav 0,176,40
R 28,27648
mav 4,180,40
s_waitcnt lgkmcnt(3)
mav 0,184,44
R 32,28672
mav 4,188,44
mav 0,192,16
R 36,29696
mav 4,196,16
s_waitcnt lgkmcnt(3)
mav 0,200,20
R 40,30720
mav 4,204,20
mav 0,208,24
R 44,31744
mav 4,212,24
s_waitcnt lgkmcnt(3)
mav 0,216,28
mav 4,220,28
mav 0,224,32
mav 4,228,32
s_waitcnt lgkmcnt(1)
mav 0,232,36
mav 4,236,36
mav 0,240,40
mav 4,244,40
s_waitcnt lgkmcnt(0)
mav 0,248,44
mav 4,252,44
s_nop 6
v_cvt_pk_bf16_f32 v52, v0, v1
v_cvt_pk_bf16_f32 v53, v2, v3
v_cvt_pk_bf16_f32 v54, v4, v5
v_cvt_pk_bf16_f32 v55, v6, v7
buffer_store_dwordx4 v[52:55], v83, s[36:39], s49 offen sc1
v_mul_f32 v59, v0, v0
v_add_f32 v58, v1, v0
v_fmac_f32 v59, v1, v1
v_add_f32 v58, v2, v58
v_fmac_f32 v59, v2, v2
v_add_f32 v58, v3, v58
v_fmac_f32 v59, v3, v3
v_add_f32 v58, v4, v58
v_fmac_f32 v59, v4, v4
v_add_f32 v58, v5, v58
v_fmac_f32 v59, v5, v5
v_add_f32 v58, v6, v58
v_fmac_f32 v59, v6, v6
v_add_f32 v58, v7, v58
v_fmac_f32 v59, v7, v7
LsO5:
s_cbranch_execz LsO9
s_nop 1
v_permlane32_swap_b32 v58, v59
v_add_f32 v58, v58, v59
ds_write_b32 v89, v58 offset:0
s_xor_b32 s50, s50, 0x10000
s_waitcnt vmcnt(3) lgkmcnt(0)
s_add_u32 s49, s49, 313344
s_barrier
s_add_u32 m0, s50, 0
R 16,32768
ds_read_b128 v[62:65], v90 offset:0
D 84,0
R 20,33792
R 24,34816
D 84,1024
R 28,35840
s_waitcnt lgkmcnt(3)
v_add_f32 v62, v62, v63
v_add_f32 v64, v64, v65
v_add_f32 v62, v62, v64
s_nop 1
v_add_f32_dpp v63, v62, v62 quad_perm:[1,0,3,2] row_mask:0xf bank_mask:0xf
buffer_store_dword v63, v91, s[36:39], s49 offen sc1
R 32,36864
D 84,2048
mav 8,0,16
R 36,37888
mav 12,4,16
s_waitcnt lgkmcnt(3)
mav 8,8,20
R 40,38912
mav 12,12,20
mav 8,16,24
R 44,39936
mav 12,20,24
s_waitcnt lgkmcnt(3)
mav 8,24,28
D 84,3072
mav 12,28,28
s_add_u32 m0, s50, 4096
R 16,40960
mav 8,32,32
R 20,41984
mav 12,36,32
s_waitcnt lgkmcnt(3)
mav 8,40,36
D 85,0
mav 12,44,36
R 24,43008
mav 8,48,40
R 28,44032
v_add_u32 v56, 1, v56
mav 12,52,40
s_waitcnt lgkmcnt(3)
mav 8,56,44
D 85,1024
mav 12,60,44
R 32,45056
mav 8,64,16
R 36,46080
mav 12,68,16
s_waitcnt lgkmcnt(3)
mav 8,72,20
D 85,2048
mav 12,76,20
R 40,47104
mav 8,80,24
R 44,48128
mav 12,84,24
s_waitcnt lgkmcnt(3)
mav 8,88,28
D 85,3072
mav 12,92,28
R 16,49152
mav 8,96,32
R 20,50176
mav 12,100,32
s_waitcnt lgkmcnt(3)
mav 8,104,36
R 24,51200
mav 12,108,36
mav 8,112,40
R 28,52224
mav 12,116,40
s_waitcnt lgkmcnt(3)
mav 8,120,44
s_waitcnt vmcnt(10)
R 32,53248
v_lshlrev_b32 v0, 16, v48
mav 12,124,44
v_and_b32 v1, 0xffff0000, v48
v_lshlrev_b32 v2, 16, v49
mav 8,128,16
R 36,54272
v_and_b32 v3, 0xffff0000, v49
mav 12,132,16
s_waitcnt lgkmcnt(3)
v_lshlrev_b32 v4, 16, v50
v_and_b32 v5, 0xffff0000, v50
mav 8,136,20
R 40,55296
v_lshlrev_b32 v6, 16, v51
mav 12,140,20
v_and_b32 v7, 0xffff0000, v51
mav 8,144,24
s_waitcnt vmcnt(9)
R 44,56320
mav 12,148,24
s_waitcnt lgkmcnt(3)
mav 8,152,28
buffer_store_dword v56, v81, s[36:39], s52 offen sc1
mav 12,156,28
s_sub_u32 s54, s54, 0x8000
R 16,57344
mav 8,160,32
R 20,58368
mav 12,164,32
s_waitcnt lgkmcnt(3)
mav 8,168,36
buffer_load_dwordx4 v[48:51], v88, s[44:47], s54 offen sc1
mav 12,172,36
R 24,59392
mav 8,176,40
R 28,60416
mav 12,180,40
s_waitcnt lgkmcnt(3)
mav 8,184,44
R 32,61440
mav 12,188,44
mav 8,192,16
R 36,62464
mav 12,196,16
s_waitcnt lgkmcnt(3)
mav 8,200,20
R 40,63488
mav 12,204,20
mav 8,208,24
R 44,64512
mav 12,212,24
s_waitcnt lgkmcnt(3)
mav 8,216,28
mav 12,220,28
mav 8,224,32
mav 12,228,32
s_waitcnt lgkmcnt(1)
s_add_u32 s60, s60, 1
mav 8,232,36
s_cbranch_scc0 LO1_1
mav 12,236,36
mav 8,240,40
mav 12,244,40
s_waitcnt lgkmcnt(0)
mav 8,248,44
mav 12,252,44
s_nop 6
v_cvt_pk_bf16_f32 v52, v8, v9
v_cvt_pk_bf16_f32 v53, v10, v11
v_cvt_pk_bf16_f32 v54, v12, v13
v_cvt_pk_bf16_f32 v55, v14, v15
buffer_store_dwordx4 v[52:55], v83, s[36:39], s49 offen sc1
v_mul_f32 v59, v8, v8
v_add_f32 v58, v9, v8
v_fmac_f32 v59, v9, v9
v_add_f32 v58, v10, v58
v_fmac_f32 v59, v10, v10
v_add_f32 v58, v11, v58
v_fmac_f32 v59, v11, v11
v_add_f32 v58, v12, v58
v_fmac_f32 v59, v12, v12
v_add_f32 v58, v13, v58
v_fmac_f32 v59, v13, v13
v_add_f32 v58, v14, v58
v_fmac_f32 v59, v14, v14
v_add_f32 v58, v15, v58
v_fmac_f32 v59, v15, v15
s_nop 1
v_permlane32_swap_b32 v58, v59
v_add_f32 v58, v58, v59
ds_write_b32 v89, v58 offset:1024
v_xor_b32 v80, 0x10000, v80
s_waitcnt vmcnt(3) lgkmcnt(0)
s_add_u32 s49, s49, 313344
s_barrier
s_add_u32 m0, s50, 32768
R 16,0
ds_read_b128 v[62:65], v90 offset:1024
D 84,0
R 20,1024
R 24,2048
D 84,1024
R 28,3072
s_waitcnt lgkmcnt(3)
v_add_f32 v62, v62, v63
v_add_f32 v64, v64, v65
v_add_f32 v62, v62, v64
s_nop 1
v_add_f32_dpp v63, v62, v62 quad_perm:[1,0,3,2] row_mask:0xf bank_mask:0xf
buffer_store_dword v63, v91, s[36:39], s49 offen sc1
R 32,4096
D 84,2048
mav 0,0,16
R 36,5120
mav 4,4,16
s_waitcnt lgkmcnt(3)
mav 0,8,20
R 40,6144
mav 4,12,20
mav 0,16,24
R 44,7168
mav 4,20,24
s_waitcnt lgkmcnt(3)
mav 0,24,28
D 84,3072
mav 4,28,28
s_add_u32 m0, s50, 36864
R 16,8192
mav 0,32,32
R 20,9216
mav 4,36,32
s_waitcnt lgkmcnt(3)
mav 0,40,36
D 85,0
mav 4,44,36
R 24,10240
mav 0,48,40
R 28,11264
v_add_u32 v56, 1, v56
mav 4,52,40
s_waitcnt lgkmcnt(3)
mav 0,56,44
D 85,1024
mav 4,60,44
R 32,12288
mav 0,64,16
R 36,13312
mav 4,68,16
s_waitcnt lgkmcnt(3)
mav 0,72,20
D 85,2048
mav 4,76,20
R 40,14336
mav 0,80,24
R 44,15360
mav 4,84,24
s_waitcnt lgkmcnt(3)
mav 0,88,28
D 85,3072
mav 4,92,28
R 16,16384
mav 0,96,32
R 20,17408
mav 4,100,32
s_waitcnt lgkmcnt(3)
mav 0,104,36
R 24,18432
mav 4,108,36
mav 0,112,40
R 28,19456
mav 4,116,40
s_waitcnt lgkmcnt(3)
mav 0,120,44
s_waitcnt vmcnt(10)
R 32,20480
v_lshlrev_b32 v8, 16, v48
mav 4,124,44
v_and_b32 v9, 0xffff0000, v48
v_lshlrev_b32 v10, 16, v49
mav 0,128,16
R 36,21504
v_and_b32 v11, 0xffff0000, v49
mav 4,132,16
s_waitcnt lgkmcnt(3)
v_lshlrev_b32 v12, 16, v50
v_and_b32 v13, 0xffff0000, v50
mav 0,136,20
R 40,22528
v_lshlrev_b32 v14, 16, v51
mav 4,140,20
v_and_b32 v15, 0xffff0000, v51
mav 0,144,24
s_waitcnt vmcnt(9)
R 44,23552
mav 4,148,24
s_waitcnt lgkmcnt(3)
mav 0,152,28
buffer_store_dword v56, v81, s[36:39], s52 offen sc1
mav 4,156,28
R 16,24576
mav 0,160,32
R 20,25600
mav 4,164,32
s_waitcnt lgkmcnt(3)
mav 0,168,36
R 24,26624
mav 4,172,36
mav 0,176,40
R 28,27648
mav 4,180,40
s_waitcnt lgkmcnt(3)
mav 0,184,44
R 32,28672
mav 4,188,44
mav 0,192,16
R 36,29696
mav 4,196,16
s_waitcnt lgkmcnt(3)
mav 0,200,20
R 40,30720
mav 4,204,20
mav 0,208,24
R 44,31744
mav 4,212,24
s_waitcnt lgkmcnt(3)
mav 0,216,28
mav 4,220,28
mav 0,224,32
mav 4,228,32
s_waitcnt lgkmcnt(1)
mav 0,232,36
mav 4,236,36
mav 0,240,40
mav 4,244,40
s_waitcnt lgkmcnt(0)
mav 0,248,44
mav 4,252,44
s_nop 6
v_cvt_pk_bf16_f32 v52, v0, v1
v_cvt_pk_bf16_f32 v53, v2, v3
v_cvt_pk_bf16_f32 v54, v4, v5
v_cvt_pk_bf16_f32 v55, v6, v7
buffer_store_dwordx4 v[52:55], v83, s[36:39], s49 offen sc1
v_mul_f32 v59, v0, v0
v_add_f32 v58, v1, v0
v_fmac_f32 v59, v1, v1
v_add_f32 v58, v2, v58
v_fmac_f32 v59, v2, v2
v_add_f32 v58, v3, v58
v_fmac_f32 v59, v3, v3
v_add_f32 v58, v4, v58
v_fmac_f32 v59, v4, v4
v_add_f32 v58, v5, v58
v_fmac_f32 v59, v5, v5
v_add_f32 v58, v6, v58
v_fmac_f32 v59, v6, v6
v_add_f32 v58, v7, v58
v_fmac_f32 v59, v7, v7
s_nop 1
v_permlane32_swap_b32 v58, v59
v_add_f32 v58, v58, v59
ds_write_b32 v89, v58 offset:0
s_waitcnt vmcnt(2) lgkmcnt(0)
s_add_u32 s49, s49, 313344
s_barrier
R 16,32768
ds_read_b128 v[62:65], v90 offset:0
R 20,33792
R 24,34816
R 28,35840
s_waitcnt lgkmcnt(3)
v_add_f32 v62, v62, v63
v_add_f32 v64, v64, v65
v_add_f32 v62, v62, v64
s_nop 1
v_add_f32_dpp v63, v62, v62 quad_perm:[1,0,3,2] row_mask:0xf bank_mask:0xf
buffer_store_dword v63, v91, s[36:39], s49 offen sc1
R 32,36864
mav 8,0,16
R 36,37888
mav 12,4,16
s_waitcnt lgkmcnt(3)
mav 8,8,20
R 40,38912
mav 12,12,20
mav 8,16,24
R 44,39936
mav 12,20,24
s_waitcnt lgkmcnt(3)
mav 8,24,28
R 16,40960
mav 12,28,28
mav 8,32,32
R 20,41984
mav 12,36,32
s_waitcnt lgkmcnt(3)
mav 8,40,36
R 24,43008
mav 12,44,36
mav 8,48,40
R 28,44032
v_add_u32 v56, 1, v56
mav 12,52,40
s_waitcnt lgkmcnt(3)
mav 8,56,44
R 32,45056
mav 12,60,44
mav 8,64,16
R 36,46080
mav 12,68,16
s_waitcnt lgkmcnt(3)
mav 8,72,20
R 40,47104
mav 12,76,20
mav 8,80,24
R 44,48128
mav 12,84,24
s_waitcnt lgkmcnt(3)
mav 8,88,28
R 16,49152
mav 12,92,28
mav 8,96,32
R 20,50176
mav 12,100,32
s_waitcnt lgkmcnt(3)
mav 8,104,36
R 24,51200
mav 12,108,36
mav 8,112,40
R 28,52224
mav 12,116,40
s_waitcnt lgkmcnt(3)
mav 8,120,44
R 32,53248
mav 12,124,44
mav 8,128,16
R 36,54272
mav 12,132,16
s_waitcnt lgkmcnt(3)
mav 8,136,20
R 40,55296
mav 12,140,20
mav 8,144,24
s_waitcnt vmcnt(1)
R 44,56320
mav 12,148,24
s_waitcnt lgkmcnt(3)
mav 8,152,28
buffer_store_dword v56, v81, s[36:39], s52 offen sc1
mav 12,156,28
R 16,57344
mav 8,160,32
R 20,58368
mav 12,164,32
s_waitcnt lgkmcnt(3)
mav 8,168,36
R 24,59392
mav 12,172,36
mav 8,176,40
R 28,60416
mav 12,180,40
s_waitcnt lgkmcnt(3)
mav 8,184,44
R 32,61440
mav 12,188,44
mav 8,192,16
R 36,62464
mav 12,196,16
s_waitcnt lgkmcnt(3)
mav 8,200,20
R 40,63488
mav 12,204,20
mav 8,208,24
R 44,64512
mav 12,212,24
s_waitcnt lgkmcnt(3)
mav 8,216,28
mav 12,220,28
mav 8,224,32
mav 12,228,32
s_waitcnt lgkmcnt(1)
mav 8,232,36
mav 12,236,36
mav 8,240,40
mav 12,244,40
s_waitcnt lgkmcnt(0)
mav 8,248,44
mav 12,252,44
s_nop 6
v_cvt_pk_bf16_f32 v52, v8, v9
v_cvt_pk_bf16_f32 v53, v10, v11
v_cvt_pk_bf16_f32 v54, v12, v13
v_cvt_pk_bf16_f32 v55, v14, v15
buffer_store_dwordx4 v[52:55], v83, s[36:39], s49 offen sc1
v_mul_f32 v59, v8, v8
v_add_f32 v58, v9, v8
v_fmac_f32 v59, v9, v9
v_add_f32 v58, v10, v58
v_fmac_f32 v59, v10, v10
v_add_f32 v58, v11, v58
v_fmac_f32 v59, v11, v11
v_add_f32 v58, v12, v58
v_fmac_f32 v59, v12, v12
v_add_f32 v58, v13, v58
v_fmac_f32 v59, v13, v13
v_add_f32 v58, v14, v58
v_fmac_f32 v59, v14, v14
v_add_f32 v58, v15, v58
v_fmac_f32 v59, v15, v15
s_nop 1
v_permlane32_swap_b32 v58, v59
v_add_f32 v58, v58, v59
ds_write_b32 v89, v58 offset:1024
s_waitcnt lgkmcnt(0)
s_add_u32 s49, s49, 313344
s_barrier
ds_read_b128 v[62:65], v90 offset:1024
s_waitcnt lgkmcnt(0)
v_add_f32 v62, v62, v63
v_add_f32 v64, v64, v65
v_add_f32 v62, v62, v64
s_nop 1
v_add_f32_dpp v63, v62, v62 quad_perm:[1,0,3,2] row_mask:0xf bank_mask:0xf
buffer_store_dword v63, v91, s[36:39], s49 offen sc1
v_add_u32 v56, 37, v56
s_waitcnt vmcnt(0)
buffer_store_dword v56, v81, s[36:39], s52 offen sc1
s_endpgm
LvO2:
LsO6:
s_cbranch_execz LsO10
s_add_u32 s49, s49, 313344
s_barrier
buffer_load_dwordx4 v[48:51], v88, s[44:47], s54 offen sc1
s_add_u32 m0, s50, 0
s_nop 0
D 84,0
D 84,1024
D 84,2048
D 84,3072
s_add_u32 m0, s50, 4096
s_nop 0
D 85,0
D 85,1024
D 85,2048
D 85,3072
s_waitcnt vmcnt(8)
v_lshlrev_b32 v0, 16, v48
v_and_b32 v1, 0xffff0000, v48
v_lshlrev_b32 v2, 16, v49
v_and_b32 v3, 0xffff0000, v49
v_lshlrev_b32 v4, 16, v50
v_and_b32 v5, 0xffff0000, v50
v_lshlrev_b32 v6, 16, v51
v_and_b32 v7, 0xffff0000, v51
s_sub_u32 s54, s54, 0x8000
buffer_load_dwordx4 v[48:51], v88, s[44:47], s54 offen sc1
s_waitcnt vmcnt(1)
s_add_u32 s49, s49, 313344
s_barrier
s_add_u32 m0, s50, 32768
R 16,0
D 84,0
R 20,1024
R 24,2048
D 84,1024
R 28,3072
R 32,4096
D 84,2048
s_waitcnt lgkmcnt(3)
mav 0,0,16
R 36,5120
mav 4,4,16
mav 0,8,20
R 40,6144
mav 4,12,20
s_waitcnt lgkmcnt(3)
mav 0,16,24
D 84,3072
mav 4,20,24
s_add_u32 m0, s50, 36864
R 44,7168
mav 0,24,28
R 16,8192
mav 4,28,28
s_waitcnt lgkmcnt(3)
mav 0,32,32
D 85,0
mav 4,36,32
R 20,9216
mav 0,40,36
R 24,10240
mav 4,44,36
s_waitcnt lgkmcnt(3)
mav 0,48,40
D 85,1024
mav 4,52,40
R 28,11264
mav 0,56,44
R 32,12288
mav 4,60,44
s_waitcnt lgkmcnt(3)
mav 0,64,16
D 85,2048
mav 4,68,16
R 36,13312
mav 0,72,20
R 40,14336
mav 4,76,20
s_waitcnt lgkmcnt(3)
mav 0,80,24
D 85,3072
mav 4,84,24
R 44,15360
LsO7:
s_cbranch_execz LsO11
mav 0,88,28
R 16,16384
mav 4,92,28
s_waitcnt lgkmcnt(3)
mav 0,96,32
R 20,17408
mav 4,100,32
mav 0,104,36
R 24,18432
mav 4,108,36
s_waitcnt lgkmcnt(3)
mav 0,112,40
R 28,19456
mav 4,116,40
mav 0,120,44
s_waitcnt vmcnt(8)
R 32,20480
v_lshlrev_b32 v8, 16, v48
mav 4,124,44
s_waitcnt lgkmcnt(3)
v_and_b32 v9, 0xffff0000, v48
v_lshlrev_b32 v10, 16, v49
mav 0,128,16
R 36,21504
v_and_b32 v11, 0xffff0000, v49
mav 4,132,16
v_lshlrev_b32 v12, 16, v50
v_and_b32 v13, 0xffff0000, v50
mav 0,136,20
R 40,22528
v_lshlrev_b32 v14, 16, v51
mav 4,140,20
s_waitcnt lgkmcnt(3)
v_and_b32 v15, 0xffff0000, v51
mav 0,144,24
R 44,23552
mav 4,148,24
s_sub_u32 s54, s54, 0x8000
mav 0,152,28
R 16,24576
mav 4,156,28
s_waitcnt lgkmcnt(3)
s_cmp_lt_u32 s54, s56
s_cselect_b64 s[44:45], s[40:41], s[44:45]
mav 0,160,32
buffer_load_dwordx4 v[48:51], v88, s[44:47], s54 offen sc1
mav 4,164,32
R 20,25600
mav 0,168,36
R 24,26624
mav 4,172,36
s_waitcnt lgkmcnt(3)
mav 0,176,40
R 28,27648
mav 4,180,40
mav 0,184,44
R 32,28672
mav 4,188,44
s_waitcnt lgkmcnt(3)
mav 0,192,16
R 36,29696
mav 4,196,16
mav 0,200,20
R 40,30720
mav 4,204,20
s_waitcnt lgkmcnt(3)
mav 0,208,24
R 44,31744
mav 4,212,24
mav 0,216,28
mav 4,220,28
s_waitcnt lgkmcnt(2)
mav 0,224,32
mav 4,228,32
mav 0,232,36
mav 4,236,36
s_waitcnt lgkmcnt(0)
mav 0,240,40
mav 4,244,40
mav 0,248,44
mav 4,252,44
s_nop 6
v_cvt_pk_bf16_f32 v52, v0, v1
v_cvt_pk_bf16_f32 v53, v2, v3
v_cvt_pk_bf16_f32 v54, v4, v5
v_cvt_pk_bf16_f32 v55, v6, v7
buffer_store_dwordx4 v[52:55], v83, s[36:39], s49 offen sc1
v_mul_f32 v59, v0, v0
v_add_f32 v58, v1, v0
v_fmac_f32 v59, v1, v1
v_add_f32 v58, v2, v58
v_fmac_f32 v59, v2, v2
v_add_f32 v58, v3, v58
v_fmac_f32 v59, v3, v3
v_add_f32 v58, v4, v58
v_fmac_f32 v59, v4, v4
v_add_f32 v58, v5, v58
v_fmac_f32 v59, v5, v5
v_add_f32 v58, v6, v58
v_fmac_f32 v59, v6, v6
v_add_f32 v58, v7, v58
v_fmac_f32 v59, v7, v7
s_nop 1
v_permlane32_swap_b32 v58, v59
v_add_f32 v58, v58, v59
ds_write_b32 v89, v58 offset:0
s_xor_b32 s50, s50, 0x10000
s_waitcnt vmcnt(2) lgkmcnt(0)
s_add_u32 s49, s49, 313344
s_barrier
s_add_u32 m0, s50, 0
R 16,32768
D 84,0
R 20,33792
R 24,34816
D 84,1024
R 28,35840
R 32,36864
D 84,2048
s_waitcnt lgkmcnt(3)
mav 8,0,16
R 36,37888
mav 12,4,16
mav 8,8,20
R 40,38912
mav 12,12,20
s_waitcnt lgkmcnt(3)
mav 8,16,24
D 84,3072
mav 12,20,24
s_add_u32 m0, s50, 4096
R 44,39936
mav 8,24,28
R 16,40960
mav 12,28,28
s_waitcnt lgkmcnt(3)
mav 8,32,32
D 85,0
mav 12,36,32
R 20,41984
mav 8,40,36
R 24,43008
mav 12,44,36
s_waitcnt lgkmcnt(3)
mav 8,48,40
D 85,1024
mav 12,52,40
R 28,44032
v_add_u32 v56, 1, v56
mav 8,56,44
R 32,45056
mav 12,60,44
s_waitcnt lgkmcnt(3)
mav 8,64,16
D 85,2048
mav 12,68,16
R 36,46080
mav 8,72,20
R 40,47104
mav 12,76,20
s_waitcnt lgkmcnt(3)
mav 8,80,24
D 85,3072
mav 12,84,24
R 44,48128
mav 8,88,28
R 16,49152
mav 12,92,28
s_waitcnt lgkmcnt(3)
mav 8,96,32
R 20,50176
mav 12,100,32
mav 8,104,36
R 24,51200
mav 12,108,36
s_waitcnt lgkmcnt(3)
mav 8,112,40
R 28,52224
mav 12,116,40
mav 8,120,44
s_waitcnt vmcnt(9)
R 32,53248
v_lshlrev_b32 v0, 16, v48
mav 12,124,44
s_waitcnt lgkmcnt(3)
v_and_b32 v1, 0xffff0000, v48
v_lshlrev_b32 v2, 16, v49
mav 8,128,16
R 36,54272
v_and_b32 v3, 0xffff0000, v49
mav 12,132,16
v_lshlrev_b32 v4, 16, v50
v_and_b32 v5, 0xffff0000, v50
mav 8,136,20
R 40,55296
v_lshlrev_b32 v6, 16, v51
mav 12,140,20
s_waitcnt lgkmcnt(3)
v_and_b32 v7, 0xffff0000, v51
mav 8,144,24
s_waitcnt vmcnt(8)
buffer_store_dword v56, v81, s[36:39], s52 offen sc1
mav 12,148,24
R 44,56320
mav 8,152,28
s_sub_u32 s54, s54, 0x8000
R 16,57344
mav 12,156,28
s_waitcnt lgkmcnt(3)
mav 8,160,32
buffer_load_dwordx4 v[48:51], v88, s[44:47], s54 offen sc1
mav 12,164,32
R 20,58368
mav 8,168,36
R 24,59392
mav 12,172,36
s_waitcnt lgkmcnt(3)
mav 8,176,40
R 28,60416
mav 12,180,40
mav 8,184,44
R 32,61440
mav 12,188,44
s_waitcnt lgkmcnt(3)
mav 8,192,16
R 36,62464
mav 12,196,16
mav 8,200,20
R 40,63488
mav 12,204,20
s_waitcnt lgkmcnt(3)
mav 8,208,24
R 44,64512
mav 12,212,24
mav 8,216,28
mav 12,220,28
s_waitcnt lgkmcnt(2)
mav 8,224,32
mav 12,228,32
mav 8,232,36
s_mov_b32 s60, -30
.p2align 6
LO2_1:
mav 12,236,36
s_waitcnt lgkmcnt(0)
mav 8,240,40
mav 12,244,40
mav 8,248,44
mav 12,252,44
s_nop 6
v_cvt_pk_bf16_f32 v52, v8, v9
v_cvt_pk_bf16_f32 v53, v10, v11
v_cvt_pk_bf16_f32 v54, v12, v13
v_cvt_pk_bf16_f32 v55, v14, v15
buffer_store_dwordx4 v[52:55], v83, s[36:39], s49 offen sc1
v_mul_f32 v59, v8, v8
v_add_f32 v58, v9, v8
v_fmac_f32 v59, v9, v9
v_add_f32 v58, v10, v58
v_fmac_f32 v59, v10, v10
v_add_f32 v58, v11, v58
v_fmac_f32 v59, v11, v11
v_add_f32 v58, v12, v58
v_fmac_f32 v59, v12, v12
v_add_f32 v58, v13, v58
v_fmac_f32 v59, v13, v13
v_add_f32 v58, v14, v58
v_fmac_f32 v59, v14, v14
v_add_f32 v58, v15, v58
v_fmac_f32 v59, v15, v15
s_nop 1
v_permlane32_swap_b32 v58, v59
v_add_f32 v58, v58, v59
ds_write_b32 v89, v58 offset:1024
v_xor_b32 v80, 0x10000, v80
s_waitcnt vmcnt(3) lgkmcnt(0)
s_add_u32 s49, s49, 313344
s_barrier
s_add_u32 m0, s50, 32768
R 16,0
D 84,0
R 20,1024
R 24,2048
D 84,1024
R 28,3072
R 32,4096
D 84,2048
s_waitcnt lgkmcnt(3)
mav 0,0,16
R 36,5120
mav 4,4,16
mav 0,8,20
R 40,6144
mav 4,12,20
s_waitcnt lgkmcnt(3)
mav 0,16,24
D 84,3072
mav 4,20,24
s_add_u32 m0, s50, 36864
R 44,7168
mav 0,24,28
R 16,8192
mav 4,28,28
s_waitcnt lgkmcnt(3)
mav 0,32,32
D 85,0
mav 4,36,32
R 20,9216
mav 0,40,36
R 24,10240
mav 4,44,36
s_waitcnt lgkmcnt(3)
mav 0,48,40
D 85,1024
mav 4,52,40
R 28,11264
v_add_u32 v56, 1, v56
mav 0,56,44
R 32,12288
mav 4,60,44
s_waitcnt lgkmcnt(3)
mav 0,64,16
D 85,2048
mav 4,68,16
R 36,13312
mav 0,72,20
R 40,14336
mav 4,76,20
s_waitcnt lgkmcnt(3)
mav 0,80,24
D 85,3072
mav 4,84,24
R 44,15360
mav 0,88,28
R 16,16384
mav 4,92,28
s_waitcnt lgkmcnt(3)
mav 0,96,32
R 20,17408
mav 4,100,32
mav 0,104,36
R 24,18432
mav 4,108,36
s_waitcnt lgkmcnt(3)
mav 0,112,40
R 28,19456
mav 4,116,40
mav 0,120,44
s_waitcnt vmcnt(9)
R 32,20480
v_lshlrev_b32 v8, 16, v48
mav 4,124,44
s_waitcnt lgkmcnt(3)
v_and_b32 v9, 0xffff0000, v48
v_lshlrev_b32 v10, 16, v49
mav 0,128,16
R 36,21504
v_and_b32 v11, 0xffff0000, v49
mav 4,132,16
v_lshlrev_b32 v12, 16, v50
v_and_b32 v13, 0xffff0000, v50
mav 0,136,20
R 40,22528
v_lshlrev_b32 v14, 16, v51
mav 4,140,20
s_waitcnt lgkmcnt(3)
v_and_b32 v15, 0xffff0000, v51
mav 0,144,24
s_waitcnt vmcnt(8)
buffer_store_dword v56, v81, s[36:39], s52 offen sc1
mav 4,148,24
s_sub_u32 s54, s54, 0x8000
R 44,23552
mav 0,152,28
R 16,24576
mav 4,156,28
s_waitcnt lgkmcnt(3)
s_cmp_lt_u32 s54, s56
s_cselect_b64 s[44:45], s[40:41], s[44:45]
mav 0,160,32
buffer_load_dwordx4 v[48:51], v88, s[44:47], s54 offen sc1
mav 4,164,32
R 20,25600
mav 0,168,36
R 24,26624
mav 4,172,36
s_waitcnt lgkmcnt(3)
mav 0,176,40
R 28,27648
mav 4,180,40
mav 0,184,44
R 32,28672
mav 4,188,44
s_waitcnt lgkmcnt(3)
mav 0,192,16
R 36,29696
mav 4,196,16
mav 0,200,20
R 40,30720
mav 4,204,20
s_waitcnt lgkmcnt(3)
mav 0,208,24
R 44,31744
mav 4,212,24
mav 0,216,28
mav 4,220,28
s_waitcnt lgkmcnt(2)
mav 0,224,32
mav 4,228,32
mav 0,232,36
mav 4,236,36
s_waitcnt lgkmcnt(0)
mav 0,240,40
mav 4,244,40
mav 0,248,44
mav 4,252,44
s_nop 6
v_cvt_pk_bf16_f32 v52, v0, v1
v_cvt_pk_bf16_f32 v53, v2, v3
v_cvt_pk_bf16_f32 v54, v4, v5
v_cvt_pk_bf16_f32 v55, v6, v7
buffer_store_dwordx4 v[52:55], v83, s[36:39], s49 offen sc1
v_mul_f32 v59, v0, v0
v_add_f32 v58, v1, v0
v_fmac_f32 v59, v1, v1
v_add_f32 v58, v2, v58
v_fmac_f32 v59, v2, v2
v_add_f32 v58, v3, v58
v_fmac_f32 v59, v3, v3
v_add_f32 v58, v4, v58
v_fmac_f32 v59, v4, v4
v_add_f32 v58, v5, v58
v_fmac_f32 v59, v5, v5
v_add_f32 v58, v6, v58
v_fmac_f32 v59, v6, v6
v_add_f32 v58, v7, v58
v_fmac_f32 v59, v7, v7
s_nop 1
v_permlane32_swap_b32 v58, v59
v_add_f32 v58, v58, v59
ds_write_b32 v89, v58 offset:0
s_xor_b32 s50, s50, 0x10000
s_waitcnt vmcnt(3) lgkmcnt(0)
s_add_u32 s49, s49, 313344
s_barrier
s_add_u32 m0, s50, 0
R 16,32768
D 84,0
R 20,33792
R 24,34816
D 84,1024
R 28,35840
R 32,36864
D 84,2048
s_waitcnt lgkmcnt(3)
mav 8,0,16
R 36,37888
mav 12,4,16
mav 8,8,20
R 40,38912
mav 12,12,20
s_waitcnt lgkmcnt(3)
mav 8,16,24
D 84,3072
mav 12,20,24
s_add_u32 m0, s50, 4096
R 44,39936
mav 8,24,28
R 16,40960
mav 12,28,28
s_waitcnt lgkmcnt(3)
mav 8,32,32
D 85,0
mav 12,36,32
R 20,41984
mav 8,40,36
R 24,43008
mav 12,44,36
s_waitcnt lgkmcnt(3)
mav 8,48,40
D 85,1024
mav 12,52,40
R 28,44032
v_add_u32 v56, 1, v56
mav 8,56,44
R 32,45056
mav 12,60,44
s_waitcnt lgkmcnt(3)
mav 8,64,16
D 85,2048
mav 12,68,16
R 36,46080
mav 8,72,20
R 40,47104
mav 12,76,20
s_waitcnt lgkmcnt(3)
mav 8,80,24
D 85,3072
mav 12,84,24
R 44,48128
mav 8,88,28
R 16,49152
mav 12,92,28
s_waitcnt lgkmcnt(3)
mav 8,96,32
R 20,50176
mav 12,100,32
mav 8,104,36
R 24,51200
mav 12,108,36
s_waitcnt lgkmcnt(3)
mav 8,112,40
R 28,52224
mav 12,116,40
mav 8,120,44
s_waitcnt vmcnt(9)
R 32,53248
v_lshlrev_b32 v0, 16, v48
mav 12,124,44
s_waitcnt lgkmcnt(3)
v_and_b32 v1, 0xffff0000, v48
v_lshlrev_b32 v2, 16, v49
mav 8,128,16
R 36,54272
v_and_b32 v3, 0xffff0000, v49
mav 12,132,16
v_lshlrev_b32 v4, 16, v50
v_and_b32 v5, 0xffff0000, v50
mav 8,136,20
R 40,55296
v_lshlrev_b32 v6, 16, v51
mav 12,140,20
s_waitcnt lgkmcnt(3)
v_and_b32 v7, 0xffff0000, v51
mav 8,144,24
s_waitcnt vmcnt(8)
buffer_store_dword v56, v81, s[36:39], s52 offen sc1
mav 12,148,24
R 44,56320
mav 8,152,28
s_sub_u32 s54, s54, 0x8000
R 16,57344
mav 12,156,28
s_waitcnt lgkmcnt(3)
mav 8,160,32
buffer_load_dwordx4 v[48:51], v88, s[44:47], s54 offen sc1
mav 12,164,32
R 20,58368
mav 8,168,36
R 24,59392
mav 12,172,36
s_waitcnt lgkmcnt(3)
mav 8,176,40
R 28,60416
mav 12,180,40
mav 8,184,44
R 32,61440
mav 12,188,44
s_waitcnt lgkmcnt(3)
mav 8,192,16
R 36,62464
mav 12,196,16
mav 8,200,20
R 40,63488
mav 12,204,20
s_waitcnt lgkmcnt(3)
mav 8,208,24
R 44,64512
mav 12,212,24
mav 8,216,28
mav 12,220,28
s_waitcnt lgkmcnt(2)
mav 8,224,32
mav 12,228,32
s_add_u32 s60, s60, 1
mav 8,232,36
s_cbranch_scc0 LO2_1
mav 12,236,36
s_waitcnt lgkmcnt(0)
mav 8,240,40
mav 12,244,40
mav 8,248,44
mav 12,252,44
s_nop 6
v_cvt_pk_bf16_f32 v52, v8, v9
v_cvt_pk_bf16_f32 v53, v10, v11
v_cvt_pk_bf16_f32 v54, v12, v13
LsO8:
s_cbranch_execz LwO
v_cvt_pk_bf16_f32 v55, v14, v15
buffer_store_dwordx4 v[52:55], v83, s[36:39], s49 offen sc1
v_mul_f32 v59, v8, v8
v_add_f32 v58, v9, v8
v_fmac_f32 v59, v9, v9
v_add_f32 v58, v10, v58
v_fmac_f32 v59, v10, v10
v_add_f32 v58, v11, v58
v_fmac_f32 v59, v11, v11
v_add_f32 v58, v12, v58
v_fmac_f32 v59, v12, v12
v_add_f32 v58, v13, v58
v_fmac_f32 v59, v13, v13
v_add_f32 v58, v14, v58
v_fmac_f32 v59, v14, v14
v_add_f32 v58, v15, v58
v_fmac_f32 v59, v15, v15
s_nop 1
v_permlane32_swap_b32 v58, v59
v_add_f32 v58, v58, v59
ds_write_b32 v89, v58 offset:1024
v_xor_b32 v80, 0x10000, v80
s_waitcnt vmcnt(3) lgkmcnt(0)
s_add_u32 s49, s49, 313344
s_barrier
s_add_u32 m0, s50, 32768
R 16,0
D 84,0
R 20,1024
R 24,2048
D 84,1024
R 28,3072
R 32,4096
D 84,2048
s_waitcnt lgkmcnt(3)
mav 0,0,16
R 36,5120
mav 4,4,16
mav 0,8,20
R 40,6144
mav 4,12,20
s_waitcnt lgkmcnt(3)
mav 0,16,24
D 84,3072
mav 4,20,24
s_add_u32 m0, s50, 36864
R 44,7168
mav 0,24,28
R 16,8192
mav 4,28,28
s_waitcnt lgkmcnt(3)
mav 0,32,32
D 85,0
mav 4,36,32
R 20,9216
mav 0,40,36
R 24,10240
mav 4,44,36
s_waitcnt lgkmcnt(3)
mav 0,48,40
D 85,1024
mav 4,52,40
R 28,11264
v_add_u32 v56, 1, v56
mav 0,56,44
R 32,12288
mav 4,60,44
s_waitcnt lgkmcnt(3)
mav 0,64,16
D 85,2048
mav 4,68,16
R 36,13312
mav 0,72,20
R 40,14336
mav 4,76,20
s_waitcnt lgkmcnt(3)
mav 0,80,24
D 85,3072
mav 4,84,24
R 44,15360
mav 0,88,28
R 16,16384
mav 4,92,28
s_waitcnt lgkmcnt(3)
mav 0,96,32
R 20,17408
mav 4,100,32
mav 0,104,36
R 24,18432
mav 4,108,36
s_waitcnt lgkmcnt(3)
mav 0,112,40
R 28,19456
mav 4,116,40
mav 0,120,44
s_waitcnt vmcnt(9)
R 32,20480
v_lshlrev_b32 v8, 16, v48
mav 4,124,44
s_waitcnt lgkmcnt(3)
v_and_b32 v9, 0xffff0000, v48
v_lshlrev_b32 v10, 16, v49
mav 0,128,16
R 36,21504
v_and_b32 v11, 0xffff0000, v49
mav 4,132,16
v_lshlrev_b32 v12, 16, v50
v_and_b32 v13, 0xffff0000, v50
mav 0,136,20
R 40,22528
v_lshlrev_b32 v14, 16, v51
mav 4,140,20
s_waitcnt lgkmcnt(3)
v_and_b32 v15, 0xffff0000, v51
mav 0,144,24
s_waitcnt vmcnt(8)
buffer_store_dword v56, v81, s[36:39], s52 offen sc1
mav 4,148,24
R 44,23552
mav 0,152,28
R 16,24576
mav 4,156,28
s_waitcnt lgkmcnt(3)
mav 0,160,32
R 20,25600
mav 4,164,32
mav 0,168,36
R 24,26624
mav 4,172,36
s_waitcnt lgkmcnt(3)
mav 0,176,40
R 28,27648
mav 4,180,40
mav 0,184,44
R 32,28672
mav 4,188,44
s_waitcnt lgkmcnt(3)
mav 0,192,16
R 36,29696
mav 4,196,16
mav 0,200,20
R 40,30720
mav 4,204,20
s_waitcnt lgkmcnt(3)
mav 0,208,24
R 44,31744
mav 4,212,24
mav 0,216,28
mav 4,220,28
s_waitcnt lgkmcnt(2)
mav 0,224,32
mav 4,228,32
mav 0,232,36
mav 4,236,36
s_waitcnt lgkmcnt(0)
mav 0,240,40
mav 4,244,40
mav 0,248,44
mav 4,252,44
s_nop 6
v_cvt_pk_bf16_f32 v52, v0, v1
v_cvt_pk_bf16_f32 v53, v2, v3
v_cvt_pk_bf16_f32 v54, v4, v5
v_cvt_pk_bf16_f32 v55, v6, v7
buffer_store_dwordx4 v[52:55], v83, s[36:39], s49 offen sc1
v_mul_f32 v59, v0, v0
v_add_f32 v58, v1, v0
v_fmac_f32 v59, v1, v1
v_add_f32 v58, v2, v58
v_fmac_f32 v59, v2, v2
v_add_f32 v58, v3, v58
v_fmac_f32 v59, v3, v3
v_add_f32 v58, v4, v58
v_fmac_f32 v59, v4, v4
v_add_f32 v58, v5, v58
v_fmac_f32 v59, v5, v5
v_add_f32 v58, v6, v58
v_fmac_f32 v59, v6, v6
v_add_f32 v58, v7, v58
v_fmac_f32 v59, v7, v7
s_nop 1
v_permlane32_swap_b32 v58, v59
v_add_f32 v58, v58, v59
ds_write_b32 v89, v58 offset:0
s_waitcnt vmcnt(2) lgkmcnt(0)
s_add_u32 s49, s49, 313344
s_barrier
R 16,32768
R 20,33792
R 24,34816
R 28,35840
R 32,36864
s_waitcnt lgkmcnt(3)
mav 8,0,16
R 36,37888
mav 12,4,16
mav 8,8,20
R 40,38912
mav 12,12,20
s_waitcnt lgkmcnt(3)
mav 8,16,24
R 44,39936
mav 12,20,24
mav 8,24,28
R 16,40960
mav 12,28,28
s_waitcnt lgkmcnt(3)
mav 8,32,32
R 20,41984
mav 12,36,32
mav 8,40,36
R 24,43008
mav 12,44,36
s_waitcnt lgkmcnt(3)
mav 8,48,40
R 28,44032
v_add_u32 v56, 1, v56
mav 12,52,40
mav 8,56,44
R 32,45056
mav 12,60,44
s_waitcnt lgkmcnt(3)
mav 8,64,16
R 36,46080
mav 12,68,16
mav 8,72,20
R 40,47104
mav 12,76,20
s_waitcnt lgkmcnt(3)
mav 8,80,24
R 44,48128
mav 12,84,24
mav 8,88,28
R 16,49152
mav 12,92,28
s_waitcnt lgkmcnt(3)
mav 8,96,32
R 20,50176
mav 12,100,32
mav 8,104,36
R 24,51200
mav 12,108,36
s_waitcnt lgkmcnt(3)
mav 8,112,40
R 28,52224
mav 12,116,40
mav 8,120,44
R 32,53248
mav 12,124,44
s_waitcnt lgkmcnt(3)
mav 8,128,16
R 36,54272
mav 12,132,16
mav 8,136,20
R 40,55296
mav 12,140,20
s_waitcnt lgkmcnt(3)
mav 8,144,24
s_waitcnt vmcnt(0)
buffer_store_dword v56, v81, s[36:39], s52 offen sc1
mav 12,148,24
R 44,56320
mav 8,152,28
R 16,57344
mav 12,156,28
s_waitcnt lgkmcnt(3)
)"
R"(
mav 8,160,32
R 20,58368
mav 12,164,32
mav 8,168,36
R 24,59392
mav 12,172,36
s_waitcnt lgkmcnt(3)
mav 8,176,40
R 28,60416
mav 12,180,40
mav 8,184,44
R 32,61440
mav 12,188,44
s_waitcnt lgkmcnt(3)
mav 8,192,16
R 36,62464
mav 12,196,16
mav 8,200,20
R 40,63488
mav 12,204,20
s_waitcnt lgkmcnt(3)
mav 8,208,24
R 44,64512
mav 12,212,24
mav 8,216,28
mav 12,220,28
s_waitcnt lgkmcnt(2)
mav 8,224,32
mav 12,228,32
mav 8,232,36
mav 12,236,36
s_waitcnt lgkmcnt(0)
mav 8,240,40
mav 12,244,40
mav 8,248,44
mav 12,252,44
s_nop 6
v_cvt_pk_bf16_f32 v52, v8, v9
v_cvt_pk_bf16_f32 v53, v10, v11
v_cvt_pk_bf16_f32 v54, v12, v13
v_cvt_pk_bf16_f32 v55, v14, v15
buffer_store_dwordx4 v[52:55], v83, s[36:39], s49 offen sc1
v_mul_f32 v59, v8, v8
v_add_f32 v58, v9, v8
v_fmac_f32 v59, v9, v9
v_add_f32 v58, v10, v58
v_fmac_f32 v59, v10, v10
v_add_f32 v58, v11, v58
v_fmac_f32 v59, v11, v11
v_add_f32 v58, v12, v58
v_fmac_f32 v59, v12, v12
v_add_f32 v58, v13, v58
v_fmac_f32 v59, v13, v13
v_add_f32 v58, v14, v58
v_fmac_f32 v59, v14, v14
v_add_f32 v58, v15, v58
v_fmac_f32 v59, v15, v15
s_nop 1
v_permlane32_swap_b32 v58, v59
v_add_f32 v58, v58, v59
ds_write_b32 v89, v58 offset:1024
s_waitcnt lgkmcnt(0)
s_add_u32 s49, s49, 313344
s_barrier
v_add_u32 v56, 37, v56
s_waitcnt vmcnt(0)
buffer_store_dword v56, v81, s[36:39], s52 offen sc1
s_endpgm
LvO3:
LsO9:
s_cbranch_execz LwO
s_add_u32 s49, s49, 313344
s_barrier
buffer_load_dwordx4 v[48:51], v88, s[44:47], s54 offen sc1
s_add_u32 m0, s50, 0
s_nop 0
D 84,0
D 84,1024
D 84,2048
D 84,3072
s_add_u32 m0, s50, 4096
s_nop 0
D 85,0
D 85,1024
D 85,2048
D 85,3072
s_waitcnt vmcnt(8)
v_lshlrev_b32 v0, 16, v48
v_and_b32 v1, 0xffff0000, v48
v_lshlrev_b32 v2, 16, v49
v_and_b32 v3, 0xffff0000, v49
v_lshlrev_b32 v4, 16, v50
v_and_b32 v5, 0xffff0000, v50
v_lshlrev_b32 v6, 16, v51
v_and_b32 v7, 0xffff0000, v51
s_sub_u32 s54, s54, 0x8000
buffer_load_dwordx4 v[48:51], v88, s[44:47], s54 offen sc1
s_waitcnt vmcnt(1)
s_add_u32 s49, s49, 313344
s_barrier
s_add_u32 m0, s50, 32768
R 16,0
D 84,0
R 20,1024
R 24,2048
D 84,1024
R 28,3072
R 32,4096
D 84,2048
s_waitcnt lgkmcnt(3)
mav 0,0,16
R 36,5120
mav 4,4,16
mav 0,8,20
R 40,6144
mav 4,12,20
s_waitcnt lgkmcnt(3)
mav 0,16,24
R 44,7168
mav 4,20,24
D 84,3072
mav 0,24,28
s_add_u32 m0, s50, 36864
R 16,8192
mav 4,28,28
s_waitcnt lgkmcnt(3)
mav 0,32,32
R 20,9216
mav 4,36,32
D 85,0
mav 0,40,36
R 24,10240
mav 4,44,36
s_waitcnt lgkmcnt(3)
mav 0,48,40
R 28,11264
mav 4,52,40
D 85,1024
mav 0,56,44
R 32,12288
mav 4,60,44
s_waitcnt lgkmcnt(3)
mav 0,64,16
R 36,13312
mav 4,68,16
D 85,2048
mav 0,72,20
R 40,14336
mav 4,76,20
s_waitcnt lgkmcnt(3)
mav 0,80,24
R 44,15360
mav 4,84,24
D 85,3072
mav 0,88,28
R 16,16384
mav 4,92,28
s_waitcnt lgkmcnt(3)
mav 0,96,32
R 20,17408
mav 4,100,32
mav 0,104,36
R 24,18432
mav 4,108,36
s_waitcnt lgkmcnt(3)
mav 0,112,40
R 28,19456
mav 4,116,40
mav 0,120,44
s_waitcnt vmcnt(8)
R 32,20480
v_lshlrev_b32 v8, 16, v48
mav 4,124,44
s_waitcnt lgkmcnt(3)
v_and_b32 v9, 0xffff0000, v48
v_lshlrev_b32 v10, 16, v49
mav 0,128,16
R 36,21504
v_and_b32 v11, 0xffff0000, v49
mav 4,132,16
v_lshlrev_b32 v12, 16, v50
v_and_b32 v13, 0xffff0000, v50
mav 0,136,20
R 40,22528
v_lshlrev_b32 v14, 16, v51
mav 4,140,20
s_waitcnt lgkmcnt(3)
v_and_b32 v15, 0xffff0000, v51
mav 0,144,24
R 44,23552
mav 4,148,24
s_sub_u32 s54, s54, 0x8000
mav 0,152,28
R 16,24576
mav 4,156,28
s_waitcnt lgkmcnt(3)
s_cmp_lt_u32 s54, s56
s_cselect_b64 s[44:45], s[40:41], s[44:45]
mav 0,160,32
R 20,25600
mav 4,164,32
buffer_load_dwordx4 v[48:51], v88, s[44:47], s54 offen sc1
mav 0,168,36
R 24,26624
mav 4,172,36
s_waitcnt lgkmcnt(3)
mav 0,176,40
R 28,27648
mav 4,180,40
mav 0,184,44
R 32,28672
mav 4,188,44
s_waitcnt lgkmcnt(3)
mav 0,192,16
R 36,29696
mav 4,196,16
mav 0,200,20
R 40,30720
mav 4,204,20
s_waitcnt lgkmcnt(3)
mav 0,208,24
R 44,31744
mav 4,212,24
mav 0,216,28
mav 4,220,28
s_waitcnt lgkmcnt(2)
mav 0,224,32
mav 4,228,32
mav 0,232,36
mav 4,236,36
s_waitcnt lgkmcnt(0)
mav 0,240,40
mav 4,244,40
mav 0,248,44
mav 4,252,44
s_nop 6
v_cvt_pk_bf16_f32 v52, v0, v1
v_cvt_pk_bf16_f32 v53, v2, v3
v_cvt_pk_bf16_f32 v54, v4, v5
v_cvt_pk_bf16_f32 v55, v6, v7
buffer_store_dwordx4 v[52:55], v83, s[36:39], s49 offen sc1
v_mul_f32 v59, v0, v0
v_add_f32 v58, v1, v0
v_fmac_f32 v59, v1, v1
v_add_f32 v58, v2, v58
v_fmac_f32 v59, v2, v2
v_add_f32 v58, v3, v58
v_fmac_f32 v59, v3, v3
v_add_f32 v58, v4, v58
v_fmac_f32 v59, v4, v4
v_add_f32 v58, v5, v58
v_fmac_f32 v59, v5, v5
v_add_f32 v58, v6, v58
v_fmac_f32 v59, v6, v6
v_add_f32 v58, v7, v58
v_fmac_f32 v59, v7, v7
s_nop 1
v_permlane32_swap_b32 v58, v59
v_add_f32 v58, v58, v59
ds_write_b32 v89, v58 offset:0
s_xor_b32 s50, s50, 0x10000
s_waitcnt vmcnt(2) lgkmcnt(0)
s_add_u32 s49, s49, 313344
s_barrier
s_add_u32 m0, s50, 0
R 16,32768
D 84,0
R 20,33792
R 24,34816
D 84,1024
R 28,35840
R 32,36864
D 84,2048
s_waitcnt lgkmcnt(3)
mav 8,0,16
R 36,37888
mav 12,4,16
mav 8,8,20
R 40,38912
mav 12,12,20
s_waitcnt lgkmcnt(3)
mav 8,16,24
R 44,39936
mav 12,20,24
D 84,3072
mav 8,24,28
s_add_u32 m0, s50, 4096
R 16,40960
mav 12,28,28
s_waitcnt lgkmcnt(3)
mav 8,32,32
R 20,41984
mav 12,36,32
D 85,0
mav 8,40,36
R 24,43008
mav 12,44,36
s_waitcnt lgkmcnt(3)
mav 8,48,40
R 28,44032
v_add_u32 v56, 1, v56
mav 12,52,40
D 85,1024
mav 8,56,44
R 32,45056
mav 12,60,44
s_waitcnt lgkmcnt(3)
mav 8,64,16
R 36,46080
mav 12,68,16
D 85,2048
mav 8,72,20
R 40,47104
mav 12,76,20
s_waitcnt lgkmcnt(3)
mav 8,80,24
R 44,48128
mav 12,84,24
D 85,3072
mav 8,88,28
R 16,49152
mav 12,92,28
s_waitcnt lgkmcnt(3)
mav 8,96,32
R 20,50176
mav 12,100,32
mav 8,104,36
R 24,51200
mav 12,108,36
s_waitcnt lgkmcnt(3)
mav 8,112,40
R 28,52224
mav 12,116,40
LsO10:
s_cbranch_execz LwO
mav 8,120,44
s_waitcnt vmcnt(9)
R 32,53248
v_lshlrev_b32 v0, 16, v48
mav 12,124,44
s_waitcnt lgkmcnt(3)
v_and_b32 v1, 0xffff0000, v48
v_lshlrev_b32 v2, 16, v49
mav 8,128,16
R 36,54272
v_and_b32 v3, 0xffff0000, v49
mav 12,132,16
v_lshlrev_b32 v4, 16, v50
v_and_b32 v5, 0xffff0000, v50
mav 8,136,20
R 40,55296
v_lshlrev_b32 v6, 16, v51
mav 12,140,20
s_waitcnt lgkmcnt(3)
v_and_b32 v7, 0xffff0000, v51
mav 8,144,24
s_waitcnt vmcnt(8)
R 44,56320
mav 12,148,24
buffer_store_dword v56, v81, s[36:39], s52 offen sc1
mav 8,152,28
s_sub_u32 s54, s54, 0x8000
R 16,57344
mav 12,156,28
s_waitcnt lgkmcnt(3)
mav 8,160,32
R 20,58368
mav 12,164,32
buffer_load_dwordx4 v[48:51], v88, s[44:47], s54 offen sc1
mav 8,168,36
R 24,59392
mav 12,172,36
s_waitcnt lgkmcnt(3)
mav 8,176,40
R 28,60416
mav 12,180,40
mav 8,184,44
R 32,61440
mav 12,188,44
s_waitcnt lgkmcnt(3)
mav 8,192,16
R 36,62464
mav 12,196,16
mav 8,200,20
R 40,63488
mav 12,204,20
s_waitcnt lgkmcnt(3)
mav 8,208,24
R 44,64512
mav 12,212,24
mav 8,216,28
mav 12,220,28
s_waitcnt lgkmcnt(2)
mav 8,224,32
mav 12,228,32
mav 8,232,36
s_mov_b32 s60, -30
.p2align 6
LO3_1:
mav 12,236,36
s_waitcnt lgkmcnt(0)
mav 8,240,40
mav 12,244,40
mav 8,248,44
mav 12,252,44
s_nop 6
v_cvt_pk_bf16_f32 v52, v8, v9
v_cvt_pk_bf16_f32 v53, v10, v11
v_cvt_pk_bf16_f32 v54, v12, v13
v_cvt_pk_bf16_f32 v55, v14, v15
buffer_store_dwordx4 v[52:55], v83, s[36:39], s49 offen sc1
v_mul_f32 v59, v8, v8
v_add_f32 v58, v9, v8
v_fmac_f32 v59, v9, v9
v_add_f32 v58, v10, v58
v_fmac_f32 v59, v10, v10
v_add_f32 v58, v11, v58
v_fmac_f32 v59, v11, v11
v_add_f32 v58, v12, v58
v_fmac_f32 v59, v12, v12
v_add_f32 v58, v13, v58
v_fmac_f32 v59, v13, v13
v_add_f32 v58, v14, v58
v_fmac_f32 v59, v14, v14
v_add_f32 v58, v15, v58
v_fmac_f32 v59, v15, v15
s_nop 1
v_permlane32_swap_b32 v58, v59
v_add_f32 v58, v58, v59
ds_write_b32 v89, v58 offset:1024
v_xor_b32 v80, 0x10000, v80
s_waitcnt vmcnt(3) lgkmcnt(0)
s_add_u32 s49, s49, 313344
s_barrier
s_add_u32 m0, s50, 32768
R 16,0
D 84,0
R 20,1024
R 24,2048
D 84,1024
R 28,3072
R 32,4096
D 84,2048
s_waitcnt lgkmcnt(3)
mav 0,0,16
R 36,5120
mav 4,4,16
mav 0,8,20
R 40,6144
mav 4,12,20
s_waitcnt lgkmcnt(3)
mav 0,16,24
R 44,7168
mav 4,20,24
D 84,3072
mav 0,24,28
s_add_u32 m0, s50, 36864
R 16,8192
mav 4,28,28
s_waitcnt lgkmcnt(3)
mav 0,32,32
R 20,9216
mav 4,36,32
D 85,0
mav 0,40,36
R 24,10240
mav 4,44,36
s_waitcnt lgkmcnt(3)
mav 0,48,40
R 28,11264
v_add_u32 v56, 1, v56
mav 4,52,40
D 85,1024
mav 0,56,44
R 32,12288
mav 4,60,44
s_waitcnt lgkmcnt(3)
mav 0,64,16
R 36,13312
mav 4,68,16
D 85,2048
mav 0,72,20
R 40,14336
mav 4,76,20
s_waitcnt lgkmcnt(3)
mav 0,80,24
R 44,15360
mav 4,84,24
D 85,3072
mav 0,88,28
R 16,16384
mav 4,92,28
s_waitcnt lgkmcnt(3)
mav 0,96,32
R 20,17408
mav 4,100,32
mav 0,104,36
R 24,18432
mav 4,108,36
s_waitcnt lgkmcnt(3)
mav 0,112,40
R 28,19456
mav 4,116,40
mav 0,120,44
s_waitcnt vmcnt(9)
R 32,20480
v_lshlrev_b32 v8, 16, v48
mav 4,124,44
s_waitcnt lgkmcnt(3)
v_and_b32 v9, 0xffff0000, v48
v_lshlrev_b32 v10, 16, v49
mav 0,128,16
R 36,21504
v_and_b32 v11, 0xffff0000, v49
mav 4,132,16
v_lshlrev_b32 v12, 16, v50
v_and_b32 v13, 0xffff0000, v50
mav 0,136,20
R 40,22528
v_lshlrev_b32 v14, 16, v51
mav 4,140,20
s_waitcnt lgkmcnt(3)
v_and_b32 v15, 0xffff0000, v51
mav 0,144,24
s_waitcnt vmcnt(8)
R 44,23552
mav 4,148,24
buffer_store_dword v56, v81, s[36:39], s52 offen sc1
mav 0,152,28
s_sub_u32 s54, s54, 0x8000
R 16,24576
mav 4,156,28
s_waitcnt lgkmcnt(3)
s_cmp_lt_u32 s54, s56
s_cselect_b64 s[44:45], s[40:41], s[44:45]
mav 0,160,32
R 20,25600
mav 4,164,32
buffer_load_dwordx4 v[48:51], v88, s[44:47], s54 offen sc1
mav 0,168,36
R 24,26624
mav 4,172,36
s_waitcnt lgkmcnt(3)
mav 0,176,40
R 28,27648
mav 4,180,40
mav 0,184,44
R 32,28672
mav 4,188,44
s_waitcnt lgkmcnt(3)
mav 0,192,16
R 36,29696
mav 4,196,16
mav 0,200,20
R 40,30720
mav 4,204,20
s_waitcnt lgkmcnt(3)
mav 0,208,24
R 44,31744
mav 4,212,24
mav 0,216,28
mav 4,220,28
s_waitcnt lgkmcnt(2)
mav 0,224,32
mav 4,228,32
mav 0,232,36
mav 4,236,36
s_waitcnt lgkmcnt(0)
mav 0,240,40
mav 4,244,40
mav 0,248,44
mav 4,252,44
s_nop 6
v_cvt_pk_bf16_f32 v52, v0, v1
v_cvt_pk_bf16_f32 v53, v2, v3
v_cvt_pk_bf16_f32 v54, v4, v5
v_cvt_pk_bf16_f32 v55, v6, v7
buffer_store_dwordx4 v[52:55], v83, s[36:39], s49 offen sc1
v_mul_f32 v59, v0, v0
v_add_f32 v58, v1, v0
v_fmac_f32 v59, v1, v1
v_add_f32 v58, v2, v58
v_fmac_f32 v59, v2, v2
v_add_f32 v58, v3, v58
v_fmac_f32 v59, v3, v3
v_add_f32 v58, v4, v58
v_fmac_f32 v59, v4, v4
v_add_f32 v58, v5, v58
v_fmac_f32 v59, v5, v5
v_add_f32 v58, v6, v58
v_fmac_f32 v59, v6, v6
v_add_f32 v58, v7, v58
v_fmac_f32 v59, v7, v7
s_nop 1
v_permlane32_swap_b32 v58, v59
v_add_f32 v58, v58, v59
ds_write_b32 v89, v58 offset:0
s_xor_b32 s50, s50, 0x10000
s_waitcnt vmcnt(3) lgkmcnt(0)
s_add_u32 s49, s49, 313344
s_barrier
s_add_u32 m0, s50, 0
R 16,32768
D 84,0
R 20,33792
R 24,34816
D 84,1024
R 28,35840
R 32,36864
D 84,2048
s_waitcnt lgkmcnt(3)
mav 8,0,16
R 36,37888
mav 12,4,16
mav 8,8,20
R 40,38912
mav 12,12,20
s_waitcnt lgkmcnt(3)
mav 8,16,24
R 44,39936
mav 12,20,24
D 84,3072
mav 8,24,28
s_add_u32 m0, s50, 4096
R 16,40960
mav 12,28,28
s_waitcnt lgkmcnt(3)
mav 8,32,32
R 20,41984
mav 12,36,32
D 85,0
mav 8,40,36
R 24,43008
mav 12,44,36
s_waitcnt lgkmcnt(3)
mav 8,48,40
R 28,44032
v_add_u32 v56, 1, v56
mav 12,52,40
D 85,1024
mav 8,56,44
R 32,45056
mav 12,60,44
s_waitcnt lgkmcnt(3)
mav 8,64,16
R 36,46080
mav 12,68,16
D 85,2048
mav 8,72,20
R 40,47104
mav 12,76,20
s_waitcnt lgkmcnt(3)
mav 8,80,24
R 44,48128
mav 12,84,24
D 85,3072
mav 8,88,28
R 16,49152
mav 12,92,28
s_waitcnt lgkmcnt(3)
mav 8,96,32
R 20,50176
mav 12,100,32
mav 8,104,36
R 24,51200
mav 12,108,36
s_waitcnt lgkmcnt(3)
mav 8,112,40
R 28,52224
mav 12,116,40
mav 8,120,44
s_waitcnt vmcnt(9)
R 32,53248
v_lshlrev_b32 v0, 16, v48
mav 12,124,44
s_waitcnt lgkmcnt(3)
v_and_b32 v1, 0xffff0000, v48
v_lshlrev_b32 v2, 16, v49
mav 8,128,16
R 36,54272
v_and_b32 v3, 0xffff0000, v49
mav 12,132,16
v_lshlrev_b32 v4, 16, v50
v_and_b32 v5, 0xffff0000, v50
mav 8,136,20
R 40,55296
v_lshlrev_b32 v6, 16, v51
mav 12,140,20
s_waitcnt lgkmcnt(3)
v_and_b32 v7, 0xffff0000, v51
mav 8,144,24
s_waitcnt vmcnt(8)
R 44,56320
mav 12,148,24
buffer_store_dword v56, v81, s[36:39], s52 offen sc1
mav 8,152,28
s_sub_u32 s54, s54, 0x8000
R 16,57344
mav 12,156,28
s_waitcnt lgkmcnt(3)
mav 8,160,32
R 20,58368
mav 12,164,32
buffer_load_dwordx4 v[48:51], v88, s[44:47], s54 offen sc1
mav 8,168,36
R 24,59392
mav 12,172,36
s_waitcnt lgkmcnt(3)
mav 8,176,40
R 28,60416
mav 12,180,40
mav 8,184,44
R 32,61440
mav 12,188,44
s_waitcnt lgkmcnt(3)
mav 8,192,16
R 36,62464
mav 12,196,16
mav 8,200,20
R 40,63488
mav 12,204,20
s_waitcnt lgkmcnt(3)
mav 8,208,24
R 44,64512
mav 12,212,24
mav 8,216,28
mav 12,220,28
s_waitcnt lgkmcnt(2)
mav 8,224,32
mav 12,228,32
s_add_u32 s60, s60, 1
mav 8,232,36
s_cbranch_scc0 LO3_1
mav 12,236,36
s_waitcnt lgkmcnt(0)
mav 8,240,40
mav 12,244,40
mav 8,248,44
mav 12,252,44
s_nop 6
v_cvt_pk_bf16_f32 v52, v8, v9
v_cvt_pk_bf16_f32 v53, v10, v11
v_cvt_pk_bf16_f32 v54, v12, v13
v_cvt_pk_bf16_f32 v55, v14, v15
buffer_store_dwordx4 v[52:55], v83, s[36:39], s49 offen sc1
v_mul_f32 v59, v8, v8
v_add_f32 v58, v9, v8
v_fmac_f32 v59, v9, v9
v_add_f32 v58, v10, v58
v_fmac_f32 v59, v10, v10
v_add_f32 v58, v11, v58
v_fmac_f32 v59, v11, v11
v_add_f32 v58, v12, v58
v_fmac_f32 v59, v12, v12
v_add_f32 v58, v13, v58
v_fmac_f32 v59, v13, v13
v_add_f32 v58, v14, v58
v_fmac_f32 v59, v14, v14
v_add_f32 v58, v15, v58
v_fmac_f32 v59, v15, v15
s_nop 1
v_permlane32_swap_b32 v58, v59
v_add_f32 v58, v58, v59
ds_write_b32 v89, v58 offset:1024
v_xor_b32 v80, 0x10000, v80
s_waitcnt vmcnt(3) lgkmcnt(0)
s_add_u32 s49, s49, 313344
s_barrier
s_add_u32 m0, s50, 32768
R 16,0
D 84,0
R 20,1024
R 24,2048
D 84,1024
R 28,3072
R 32,4096
D 84,2048
s_waitcnt lgkmcnt(3)
mav 0,0,16
R 36,5120
mav 4,4,16
mav 0,8,20
R 40,6144
mav 4,12,20
s_waitcnt lgkmcnt(3)
mav 0,16,24
R 44,7168
mav 4,20,24
D 84,3072
mav 0,24,28
s_add_u32 m0, s50, 36864
R 16,8192
mav 4,28,28
s_waitcnt lgkmcnt(3)
mav 0,32,32
R 20,9216
mav 4,36,32
D 85,0
mav 0,40,36
R 24,10240
mav 4,44,36
s_waitcnt lgkmcnt(3)
mav 0,48,40
R 28,11264
v_add_u32 v56, 1, v56
mav 4,52,40
D 85,1024
mav 0,56,44
R 32,12288
mav 4,60,44
s_waitcnt lgkmcnt(3)
mav 0,64,16
R 36,13312
mav 4,68,16
D 85,2048
mav 0,72,20
R 40,14336
mav 4,76,20
s_waitcnt lgkmcnt(3)
mav 0,80,24
R 44,15360
mav 4,84,24
D 85,3072
mav 0,88,28
R 16,16384
mav 4,92,28
s_waitcnt lgkmcnt(3)
mav 0,96,32
R 20,17408
mav 4,100,32
mav 0,104,36
R 24,18432
mav 4,108,36
s_waitcnt lgkmcnt(3)
mav 0,112,40
R 28,19456
mav 4,116,40
mav 0,120,44
s_waitcnt vmcnt(9)
R 32,20480
v_lshlrev_b32 v8, 16, v48
mav 4,124,44
s_waitcnt lgkmcnt(3)
v_and_b32 v9, 0xffff0000, v48
v_lshlrev_b32 v10, 16, v49
mav 0,128,16
R 36,21504
v_and_b32 v11, 0xffff0000, v49
mav 4,132,16
v_lshlrev_b32 v12, 16, v50
v_and_b32 v13, 0xffff0000, v50
mav 0,136,20
R 40,22528
v_lshlrev_b32 v14, 16, v51
mav 4,140,20
s_waitcnt lgkmcnt(3)
v_and_b32 v15, 0xffff0000, v51
mav 0,144,24
s_waitcnt vmcnt(8)
R 44,23552
mav 4,148,24
buffer_store_dword v56, v81, s[36:39], s52 offen sc1
mav 0,152,28
R 16,24576
mav 4,156,28
s_waitcnt lgkmcnt(3)
mav 0,160,32
R 20,25600
mav 4,164,32
mav 0,168,36
R 24,26624
mav 4,172,36
s_waitcnt lgkmcnt(3)
mav 0,176,40
R 28,27648
mav 4,180,40
mav 0,184,44
R 32,28672
mav 4,188,44
s_waitcnt lgkmcnt(3)
mav 0,192,16
R 36,29696
mav 4,196,16
mav 0,200,20
R 40,30720
mav 4,204,20
s_waitcnt lgkmcnt(3)
mav 0,208,24
R 44,31744
mav 4,212,24
mav 0,216,28
mav 4,220,28
s_waitcnt lgkmcnt(2)
mav 0,224,32
mav 4,228,32
mav 0,232,36
mav 4,236,36
s_waitcnt lgkmcnt(0)
mav 0,240,40
mav 4,244,40
mav 0,248,44
mav 4,252,44
s_nop 6
v_cvt_pk_bf16_f32 v52, v0, v1
v_cvt_pk_bf16_f32 v53, v2, v3
v_cvt_pk_bf16_f32 v54, v4, v5
v_cvt_pk_bf16_f32 v55, v6, v7
buffer_store_dwordx4 v[52:55], v83, s[36:39], s49 offen sc1
v_mul_f32 v59, v0, v0
v_add_f32 v58, v1, v0
v_fmac_f32 v59, v1, v1
v_add_f32 v58, v2, v58
v_fmac_f32 v59, v2, v2
v_add_f32 v58, v3, v58
v_fmac_f32 v59, v3, v3
v_add_f32 v58, v4, v58
v_fmac_f32 v59, v4, v4
v_add_f32 v58, v5, v58
v_fmac_f32 v59, v5, v5
v_add_f32 v58, v6, v58
v_fmac_f32 v59, v6, v6
v_add_f32 v58, v7, v58
v_fmac_f32 v59, v7, v7
s_nop 1
v_permlane32_swap_b32 v58, v59
v_add_f32 v58, v58, v59
ds_write_b32 v89, v58 offset:0
s_waitcnt vmcnt(2) lgkmcnt(0)
s_add_u32 s49, s49, 313344
s_barrier
R 16,32768
R 20,33792
R 24,34816
R 28,35840
R 32,36864
LsO11:
s_cbranch_execz LwO
s_waitcnt lgkmcnt(3)
mav 8,0,16
R 36,37888
mav 12,4,16
mav 8,8,20
R 40,38912
mav 12,12,20
s_waitcnt lgkmcnt(3)
mav 8,16,24
R 44,39936
mav 12,20,24
mav 8,24,28
R 16,40960
mav 12,28,28
s_waitcnt lgkmcnt(3)
mav 8,32,32
R 20,41984
mav 12,36,32
mav 8,40,36
R 24,43008
mav 12,44,36
s_waitcnt lgkmcnt(3)
mav 8,48,40
R 28,44032
v_add_u32 v56, 1, v56
mav 12,52,40
mav 8,56,44
R 32,45056
mav 12,60,44
s_waitcnt lgkmcnt(3)
mav 8,64,16
R 36,46080
mav 12,68,16
mav 8,72,20
R 40,47104
mav 12,76,20
s_waitcnt lgkmcnt(3)
mav 8,80,24
R 44,48128
mav 12,84,24
mav 8,88,28
R 16,49152
mav 12,92,28
s_waitcnt lgkmcnt(3)
mav 8,96,32
R 20,50176
mav 12,100,32
mav 8,104,36
R 24,51200
mav 12,108,36
s_waitcnt lgkmcnt(3)
mav 8,112,40
R 28,52224
mav 12,116,40
mav 8,120,44
R 32,53248
mav 12,124,44
s_waitcnt lgkmcnt(3)
mav 8,128,16
R 36,54272
mav 12,132,16
mav 8,136,20
R 40,55296
mav 12,140,20
s_waitcnt lgkmcnt(3)
mav 8,144,24
s_waitcnt vmcnt(0)
R 44,56320
mav 12,148,24
buffer_store_dword v56, v81, s[36:39], s52 offen sc1
mav 8,152,28
R 16,57344
mav 12,156,28
s_waitcnt lgkmcnt(3)
mav 8,160,32
R 20,58368
mav 12,164,32
mav 8,168,36
R 24,59392
mav 12,172,36
s_waitcnt lgkmcnt(3)
mav 8,176,40
R 28,60416
mav 12,180,40
mav 8,184,44
R 32,61440
mav 12,188,44
s_waitcnt lgkmcnt(3)
mav 8,192,16
R 36,62464
mav 12,196,16
mav 8,200,20
R 40,63488
mav 12,204,20
s_waitcnt lgkmcnt(3)
mav 8,208,24
R 44,64512
mav 12,212,24
mav 8,216,28
mav 12,220,28
s_waitcnt lgkmcnt(2)
mav 8,224,32
mav 12,228,32
mav 8,232,36
mav 12,236,36
s_waitcnt lgkmcnt(0)
mav 8,240,40
mav 12,244,40
mav 8,248,44
mav 12,252,44
s_nop 6
v_cvt_pk_bf16_f32 v52, v8, v9
v_cvt_pk_bf16_f32 v53, v10, v11
v_cvt_pk_bf16_f32 v54, v12, v13
v_cvt_pk_bf16_f32 v55, v14, v15
buffer_store_dwordx4 v[52:55], v83, s[36:39], s49 offen sc1
v_mul_f32 v59, v8, v8
v_add_f32 v58, v9, v8
v_fmac_f32 v59, v9, v9
v_add_f32 v58, v10, v58
v_fmac_f32 v59, v10, v10
v_add_f32 v58, v11, v58
v_fmac_f32 v59, v11, v11
v_add_f32 v58, v12, v58
v_fmac_f32 v59, v12, v12
v_add_f32 v58, v13, v58
v_fmac_f32 v59, v13, v13
v_add_f32 v58, v14, v58
v_fmac_f32 v59, v14, v14
v_add_f32 v58, v15, v58
v_fmac_f32 v59, v15, v15
s_nop 1
v_permlane32_swap_b32 v58, v59
v_add_f32 v58, v58, v59
ds_write_b32 v89, v58 offset:1024
s_waitcnt lgkmcnt(0)
s_add_u32 s49, s49, 313344
s_barrier
v_add_u32 v56, 37, v56
s_waitcnt vmcnt(0)
buffer_store_dword v56, v81, s[36:39], s52 offen sc1
s_endpgm
Lr3:
s_cmp_eq_u32 s60, 3
s_cbranch_scc0 Lr4
s_waitcnt vmcnt(0)
s_barrier
v_accvgpr_read_b32 v77, a255
v_mov_b32 v76, 0x100
v_add_u32 v77, 0x9e3779b9, v77
buffer_store_dwordx2 v[76:77], v81, s[36:39], s52 offen sc1
v_add_u32 v88, v88, v76
E 0,65536
E 1,69632
E 2,73728
s_add_u32 s51, s51, 229376
E 0,77824
E 1,81920
E 2,86016
s_add_u32 s51, s51, 229376
E 0,90112
E 1,94208
E 2,98304
s_add_u32 s51, s51, 229376
E 0,102400
E 1,106496
E 2,110592
s_add_u32 s51, s51, 229376
s_mov_b64 exec, 0
s_cmp_eq_u32 s53, 0
s_cbranch_scc1 LsF0
s_cmp_eq_u32 s53, 1024
s_cbranch_scc1 LsF1
s_cmp_eq_u32 s53, 2048
s_cbranch_scc1 LsF2
s_branch LsF3
LwF:
s_mov_b64 exec, -1
v_mov_b32 v24, s64
v_mov_b32 v25, s64
v_mov_b32 v26, s64
v_mov_b32 v27, s64
s_waitcnt vmcnt(6)
s_barrier
v_add_u32 v4, 65536, v4
v_add_u32 v5, 65536, v5
v_add_u32 v6, 65536, v6
Ta 0,4,0
Ta 4,5,0
Ta 8,6,0
Ta 12,4,12288
Ta 16,5,12288
Ta 20,6,12288
E 0,114688
E 1,118784
E 2,122880
s_add_u32 s51, s51, 229376
E 0,126976
E 1,131072
E 2,135168
s_add_u32 s51, s51, 229376
E 0,139264
E 1,143360
E 2,147456
s_add_u32 s51, s51, 229376
E 0,151552
E 1,155648
E 2,159744
s_add_u32 s51, s51, 229376
s_cmp_ge_u32 s51, s57
s_cselect_b32 s60, 7340032, 0
s_sub_u32 s51, s51, s60
s_waitcnt lgkmcnt(0)
s_waitcnt vmcnt(12)
s_barrier
E 0,65536
Ta 24,4,24576
v_mfma_f32_16x16x32_bf16 v[56:59], a[0:3], v[24:27], 0
Ta 28,5,24576
E 1,69632
v_mfma_f32_16x16x32_bf16 v[60:63], a[4:7], v[24:27], 0
Ta 32,6,24576
Ta 36,4,36864
E 2,73728
s_add_u32 s51, s51, 229376
v_mfma_f32_16x16x32_bf16 v[64:67], a[8:11], v[24:27], 0
Ta 40,5,36864
E 0,77824
Ta 44,6,36864
mav 56,12,24
E 1,81920
mav 60,16,24
E 2,86016
s_add_u32 s51, s51, 229376
mav 64,20,24
s_waitcnt lgkmcnt(0)
s_waitcnt vmcnt(12)
s_barrier
v_add_u32 v4, 49152, v4
v_add_u32 v5, 49152, v5
v_add_u32 v6, 49152, v6
E 0,90112
Ta 48,4,0
mav 56,24,24
Ta 52,5,0
E 1,94208
mav 60,28,24
Ta 56,6,0
Ta 60,4,12288
E 2,98304
s_add_u32 s51, s51, 229376
mav 64,32,24
Ta 64,5,12288
E 0,102400
Ta 68,6,12288
mav 56,36,24
E 1,106496
mav 60,40,24
E 2,110592
s_add_u32 s51, s51, 229376
mav 64,44,24
s_waitcnt lgkmcnt(0)
s_waitcnt vmcnt(12)
s_barrier
E 0,114688
Ta 72,4,24576
mav 56,48,24
Ta 76,5,24576
E 1,118784
mav 60,52,24
Ta 80,6,24576
Ta 84,4,36864
E 2,122880
s_add_u32 s51, s51, 229376
mav 64,56,24
Ta 88,5,36864
E 0,126976
Ta 92,6,36864
mav 56,60,24
E 1,131072
mav 60,64,24
E 2,135168
s_add_u32 s51, s51, 229376
mav 64,68,24
s_waitcnt lgkmcnt(0)
s_waitcnt vmcnt(12)
s_barrier
v_add_u32 v4, -49152, v4
v_add_u32 v5, -49152, v5
v_add_u32 v6, -49152, v6
E 0,139264
Ta 96,4,0
mav 56,72,24
Ta 100,5,0
E 1,143360
mav 60,76,24
Ta 104,6,0
Ta 108,4,12288
E 2,147456
s_add_u32 s51, s51, 229376
mav 64,80,24
Ta 112,5,12288
E 0,151552
Ta 116,6,12288
mav 56,84,24
E 1,155648
mav 60,88,24
E 2,159744
s_add_u32 s51, s51, 229376
s_cmp_ge_u32 s51, s57
s_cselect_b32 s60, 7340032, 0
s_sub_u32 s51, s51, s60
mav 64,92,24
s_waitcnt lgkmcnt(0)
s_waitcnt vmcnt(12)
s_barrier
E 0,65536
Ta 120,4,24576
mav 56,96,24
Ta 124,5,24576
E 1,69632
mav 60,100,24
Ta 128,6,24576
Ta 132,4,36864
E 2,73728
s_add_u32 s51, s51, 229376
mav 64,104,24
Ta 136,5,36864
E 0,77824
Ta 140,6,36864
mav 56,108,24
E 1,81920
mav 60,112,24
E 2,86016
s_add_u32 s51, s51, 229376
mav 64,116,24
s_waitcnt lgkmcnt(0)
s_waitcnt vmcnt(12)
s_barrier
v_add_u32 v4, 49152, v4
v_add_u32 v5, 49152, v5
v_add_u32 v6, 49152, v6
E 0,90112
Ta 144,4,0
mav 56,120,24
Ta 148,5,0
E 1,94208
mav 60,124,24
Ta 152,6,0
Ta 156,4,12288
E 2,98304
s_add_u32 s51, s51, 229376
mav 64,128,24
Ta 160,5,12288
E 0,102400
Ta 164,6,12288
mav 56,132,24
E 1,106496
mav 60,136,24
E 2,110592
s_add_u32 s51, s51, 229376
mav 64,140,24
s_waitcnt lgkmcnt(0)
s_waitcnt vmcnt(12)
s_barrier
E 0,114688
Ta 168,4,24576
mav 56,144,24
Ta 172,5,24576
E 1,118784
mav 60,148,24
Ta 176,6,24576
Ta 180,4,36864
E 2,122880
s_add_u32 s51, s51, 229376
mav 64,152,24
Ta 184,5,36864
E 0,126976
Ta 188,6,36864
mav 56,156,24
E 1,131072
mav 60,160,24
E 2,135168
s_add_u32 s51, s51, 229376
mav 64,164,24
s_waitcnt lgkmcnt(0)
s_waitcnt vmcnt(12)
s_barrier
v_add_u32 v4, -49152, v4
v_add_u32 v5, -49152, v5
v_add_u32 v6, -49152, v6
E 0,139264
Ta 192,4,0
mav 56,168,24
Ta 196,5,0
E 1,143360
mav 60,172,24
Ta 200,6,0
Ta 204,4,12288
E 2,147456
s_add_u32 s51, s51, 229376
mav 64,176,24
LsF0:
s_cbranch_execz LsF4
Ta 208,5,12288
E 0,151552
Ta 212,6,12288
mav 56,180,24
E 1,155648
mav 60,184,24
E 2,159744
s_add_u32 s51, s51, 229376
s_cmp_ge_u32 s51, s57
s_cselect_b32 s60, 7340032, 0
s_sub_u32 s51, s51, s60
mav 64,188,24
s_waitcnt lgkmcnt(0)
s_waitcnt vmcnt(12)
s_barrier
E 0,65536
Ta 216,4,24576
mav 56,192,24
Ta 220,5,24576
E 1,69632
mav 60,196,24
Ta 224,6,24576
Ta 228,4,36864
E 2,73728
s_add_u32 s51, s51, 229376
mav 64,200,24
Ta 232,5,36864
E 0,77824
Ta 236,6,36864
mav 56,204,24
E 1,81920
mav 60,208,24
E 2,86016
s_add_u32 s51, s51, 229376
mav 64,212,24
s_waitcnt lgkmcnt(0)
s_waitcnt vmcnt(12)
s_barrier
v_add_u32 v4, 49152, v4
v_add_u32 v5, 49152, v5
v_add_u32 v6, 49152, v6
E 0,90112
Ta 240,4,0
mav 56,216,24
Ta 244,5,0
E 1,94208
mav 60,220,24
Ta 248,6,0
Ta 252,4,12288
E 2,98304
s_add_u32 s51, s51, 229376
mav 64,224,24
Tv 128,5,12288
E 0,102400
Tv 132,6,12288
mav 56,228,24
E 1,106496
mav 60,232,24
E 2,110592
s_add_u32 s51, s51, 229376
mav 64,236,24
s_waitcnt lgkmcnt(0)
s_waitcnt vmcnt(12)
s_barrier
E 0,114688
Tv 136,4,24576
mav 56,240,24
Tv 140,5,24576
E 1,118784
mav 60,244,24
Tv 144,6,24576
Tv 148,4,36864
E 2,122880
s_add_u32 s51, s51, 229376
mav 64,248,24
Tv 152,5,36864
E 0,126976
Tv 156,6,36864
mav 56,252,24
E 1,131072
mvv 60,128,24
E 2,135168
s_add_u32 s51, s51, 229376
mvv 64,132,24
s_waitcnt lgkmcnt(0)
s_waitcnt vmcnt(12)
s_barrier
v_add_u32 v4, -49152, v4
v_add_u32 v5, -49152, v5
v_add_u32 v6, -49152, v6
E 0,139264
Tv 160,4,0
mvv 56,136,24
Tv 164,5,0
E 1,143360
mvv 60,140,24
Tv 168,6,0
Tv 172,4,12288
E 2,147456
s_add_u32 s51, s51, 229376
mvv 64,144,24
Tv 176,5,12288
E 0,151552
Tv 180,6,12288
mvv 56,148,24
E 1,155648
mvv 60,152,24
E 2,159744
s_add_u32 s51, s51, 229376
s_cmp_ge_u32 s51, s57
s_cselect_b32 s60, 7340032, 0
s_sub_u32 s51, s51, s60
mvv 64,156,24
s_waitcnt lgkmcnt(0)
s_waitcnt vmcnt(12)
s_barrier
Tv 184,4,24576
mvv 56,160,24
Tv 188,5,24576
mvv 60,164,24
Tv 192,6,24576
Tv 196,4,36864
mvv 64,168,24
Tv 200,5,36864
Tv 204,6,36864
mvv 56,172,24
mvv 60,176,24
mvv 64,180,24
s_waitcnt lgkmcnt(0)
s_waitcnt vmcnt(6)
s_barrier
v_add_u32 v4, 49152, v4
v_add_u32 v5, 49152, v5
v_add_u32 v6, 49152, v6
Tv 208,4,0
mvv 56,184,24
Tv 212,5,0
mvv 60,188,24
Tv 216,6,0
Tv 220,4,12288
mvv 64,192,24
Tv 224,5,12288
Tv 228,6,12288
mvv 56,196,24
mvv 60,200,24
mvv 64,204,24
s_waitcnt lgkmcnt(0)
s_waitcnt vmcnt(0)
s_barrier
Tv 232,4,24576
mvv 56,208,24
Tv 236,5,24576
mvv 60,212,24
Tv 240,6,24576
Tv 244,4,36864
mvv 64,216,24
Tv 248,5,36864
Tv 252,6,36864
mvv 56,220,24
mvv 60,224,24
mvv 64,228,24
s_waitcnt lgkmcnt(0)
mvv 56,232,24
mvv 60,236,24
mvv 64,240,24
mvv 56,244,24
mvv 60,248,24
mvv 64,252,24
s_waitcnt vmcnt(0)
s_barrier
s_nop 7
s_and_b32 s60, s48, 3
s_cmp_eq_u32 s60, 1
s_cbranch_scc1 LvF1
s_cmp_eq_u32 s60, 2
s_cbranch_scc1 LvF2
s_cmp_eq_u32 s60, 3
s_cbranch_scc1 LvF3
LvF0:
LsF1:
s_cbranch_execz LsF5
buffer_load_dwordx2 v[106:107], v87, s[36:39], 0 offen sc1
s_waitcnt vmcnt(0)
ds_write_b32 v82, v106
v_sub_u32 v106, v106, v88
v_xor_b32 v107, v107, v77
v_cmp_lt_u64 vcc, s[62:63], v[106:107]
s_waitcnt lgkmcnt(0)
s_cbranch_vccnz CF0_1
BF0_1:
s_add_u32 s49, s49, 313344
s_barrier
s_add_u32 m0, s50, 0
s_nop 0
D 85,0
D 85,1024
D 85,2048
D 85,3072
s_add_u32 m0, s50, 4096
s_nop 0
D 86,0
D 86,1024
D 86,2048
D 86,3072
s_mov_b32 m0, 144384
s_nop 0
buffer_load_dword v87, s[36:39], 0 offen sc1 lds
v_add_u32 v88, 1, v88
ds_read_b32 v106, v82 offset:0
s_waitcnt lgkmcnt(0)
v_cmp_gt_i32 vcc, v88, v106
s_waitcnt vmcnt(1)
s_add_u32 s49, s49, 313344
s_cbranch_vccnz CF0_2
BF0_2:
s_barrier
s_add_u32 m0, s50, 32768
R 24,0
D 85,0
R 28,1024
D 85,1024
R 32,2048
R 36,3072
D 85,2048
s_waitcnt lgkmcnt(2)
v_mfma_f32_16x16x32_bf16 v[0:3], a[0:3], v[24:27], 0
R 40,4096
v_mfma_f32_16x16x32_bf16 v[4:7], a[4:7], v[24:27], 0
D 85,3072
v_mfma_f32_16x16x32_bf16 v[8:11], a[8:11], v[24:27], 0
mav 0,12,28
s_add_u32 m0, s50, 36864
R 44,5120
mav 4,16,28
mav 8,20,28
s_waitcnt lgkmcnt(2)
D 86,0
mav 0,24,32
R 48,6144
mav 4,28,32
mav 8,32,32
mav 0,36,36
D 86,1024
mav 4,40,36
R 52,7168
mav 8,44,36
s_waitcnt lgkmcnt(2)
mav 0,48,40
R 24,8192
mav 4,52,40
D 86,2048
mav 8,56,40
mav 0,60,44
R 28,9216
mav 4,64,44
mav 8,68,44
s_waitcnt lgkmcnt(2)
D 86,3072
mav 0,72,48
R 32,10240
mav 4,76,48
mav 8,80,48
mav 0,84,52
R 36,11264
mav 4,88,52
mav 8,92,52
s_waitcnt lgkmcnt(2)
mav 0,96,24
R 40,12288
mav 4,100,24
mav 8,104,24
mav 0,108,28
R 44,13312
mav 4,112,28
mav 8,116,28
s_waitcnt lgkmcnt(2)
mav 0,120,32
R 48,14336
mav 4,124,32
mav 8,128,32
mav 0,132,36
R 52,15360
mav 4,136,36
s_mov_b32 m0, 144384
mav 8,140,36
s_waitcnt lgkmcnt(2)
mav 0,144,40
R 24,16384
mav 4,148,40
buffer_load_dword v87, s[36:39], 0 offen sc1 lds
mav 8,152,40
mav 0,156,44
R 28,17408
mav 4,160,44
v_add_u32 v88, 1, v88
mav 8,164,44
s_waitcnt lgkmcnt(2)
mav 0,168,48
R 32,18432
mav 4,172,48
mav 8,176,48
mav 0,180,52
R 36,19456
mav 4,184,52
mav 8,188,52
s_waitcnt lgkmcnt(2)
mav 0,192,24
R 40,20480
mav 4,196,24
mav 8,200,24
mav 0,204,28
R 44,21504
mav 4,208,28
mav 8,212,28
s_waitcnt lgkmcnt(2)
mav 0,216,32
R 48,22528
mav 4,220,32
mav 8,224,32
mav 0,228,36
R 52,23552
mav 4,232,36
mav 8,236,36
s_waitcnt lgkmcnt(2)
mav 0,240,40
R 24,24576
mav 4,244,40
mav 8,248,40
mav 0,252,44
R 28,25600
mvv 4,128,44
mvv 8,132,44
s_waitcnt lgkmcnt(2)
mvv 0,136,48
R 32,26624
mvv 4,140,48
mvv 8,144,48
mvv 0,148,52
R 36,27648
mvv 4,152,52
mvv 8,156,52
s_waitcnt lgkmcnt(2)
mvv 0,160,24
R 40,28672
mvv 4,164,24
ds_read_b32 v106, v82 offset:0
mvv 8,168,24
mvv 0,172,28
R 44,29696
mvv 4,176,28
mvv 8,180,28
s_waitcnt lgkmcnt(3)
mvv 0,184,32
s_xor_b32 s50, s50, 0x10000
R 48,30720
mvv 4,188,32
mvv 8,192,32
s_waitcnt lgkmcnt(2)
v_cmp_gt_i32 vcc, v88, v106
mvv 0,196,36
R 52,31744
mvv 4,200,36
s_waitcnt vmcnt(1)
s_add_u32 s49, s49, 313344
mvv 8,204,36
s_cbranch_vccnz CF0_3
BF0_3:
s_barrier
mvv 0,208,40
s_add_u32 m0, s50, 0
R 24,32768
mvv 4,212,40
D 85,0
mvv 8,216,40
s_waitcnt lgkmcnt(2)
ds_read_b64 v[78:79], v89 offset:0
mvv 0,220,44
R 28,33792
mvv 4,224,44
mvv 8,228,44
D 85,1024
mvv 0,232,48
R 32,34816
mvv 4,236,48
mvv 8,240,48
s_waitcnt lgkmcnt(2)
mvv 0,244,52
D 85,2048
mvv 4,248,52
R 36,35840
mvv 8,252,52
v_mfma_f32_16x16x32_bf16 v[12:15], a[0:3], v[24:27], 0
R 40,36864
v_fma_f32 v104, v79, v56, v92
v_mfma_f32_16x16x32_bf16 v[16:19], a[4:7], v[24:27], 0
D 85,3072
v_mfma_f32_16x16x32_bf16 v[20:23], a[8:11], v[24:27], 0
s_waitcnt lgkmcnt(2)
v_fma_f32 v0, v0, v78, v104
v_fma_f32 v105, v79, v57, v93
mav 12,12,28
s_add_u32 m0, s50, 4096
R 44,37888
v_fma_f32 v1, v1, v78, v105
mav 16,16,28
v_mul_f32_e64 v0, v0, |v0|
v_mul_f32_e64 v1, v1, |v1|
mav 20,20,28
D 86,0
mav 12,24,32
R 48,38912
v_cvt_pk_bf16_f32 v0, v0, v1
mav 16,28,32
v_pk_max_i16 v68, v0, 0
v_fma_f32 v104, v79, v58, v94
mav 20,32,32
s_waitcnt lgkmcnt(2)
v_fma_f32 v2, v2, v78, v104
v_fma_f32 v105, v79, v59, v95
mav 12,36,36
D 86,1024
mav 16,40,36
R 52,39936
v_fma_f32 v3, v3, v78, v105
mav 20,44,36
v_mul_f32_e64 v2, v2, |v2|
v_mul_f32_e64 v3, v3, |v3|
mav 12,48,40
R 24,40960
v_cvt_pk_bf16_f32 v2, v2, v3
mav 16,52,40
D 86,2048
mav 20,56,40
s_waitcnt lgkmcnt(2)
v_pk_max_i16 v69, v2, 0
v_fma_f32 v104, v79, v60, v96
mav 12,60,44
R 28,41984
v_fma_f32 v4, v4, v78, v104
mav 16,64,44
v_fma_f32 v105, v79, v61, v97
v_fma_f32 v5, v5, v78, v105
mav 20,68,44
D 86,3072
mav 12,72,48
R 32,43008
v_mul_f32_e64 v4, v4, |v4|
mav 16,76,48
v_mul_f32_e64 v5, v5, |v5|
v_cvt_pk_bf16_f32 v4, v4, v5
mav 20,80,48
s_waitcnt lgkmcnt(2)
v_pk_max_i16 v70, v4, 0
v_fma_f32 v104, v79, v62, v98
mav 12,84,52
R 36,44032
v_fma_f32 v6, v6, v78, v104
mav 16,88,52
v_fma_f32 v105, v79, v63, v99
v_fma_f32 v7, v7, v78, v105
mav 20,92,52
v_mul_f32_e64 v6, v6, |v6|
v_mul_f32_e64 v7, v7, |v7|
mav 12,96,24
R 40,45056
v_cvt_pk_bf16_f32 v6, v6, v7
mav 16,100,24
v_pk_max_i16 v71, v6, 0
v_fma_f32 v104, v79, v64, v100
mav 20,104,24
s_waitcnt lgkmcnt(2)
v_fma_f32 v8, v8, v78, v104
v_fma_f32 v105, v79, v65, v101
LsF2:
s_cbranch_execz LsF6
mav 12,108,28
R 44,46080
v_fma_f32 v9, v9, v78, v105
mav 16,112,28
v_mul_f32_e64 v8, v8, |v8|
v_mul_f32_e64 v9, v9, |v9|
mav 20,116,28
buffer_store_dwordx4 v[68:71], v83, s[36:39], s49 offen sc1
v_cvt_pk_bf16_f32 v8, v8, v9
v_pk_max_i16 v72, v8, 0
v_pk_max_i16 v74, v8, 0
mav 12,120,32
R 48,47104
v_fma_f32 v104, v79, v66, v102
mav 16,124,32
v_fma_f32 v10, v10, v78, v104
v_fma_f32 v105, v79, v67, v103
mav 20,128,32
s_waitcnt lgkmcnt(2)
v_fma_f32 v11, v11, v78, v105
v_mul_f32_e64 v10, v10, |v10|
mav 12,132,36
R 52,48128
v_mul_f32_e64 v11, v11, |v11|
mav 16,136,36
s_mov_b32 m0, 144384
v_cvt_pk_bf16_f32 v10, v10, v11
v_pk_max_i16 v73, v10, 0
mav 20,140,36
v_pk_max_i16 v75, v10, 0
mav 12,144,40
R 24,49152
mav 16,148,40
buffer_load_dword v87, s[36:39], 0 offen sc1 lds
mav 20,152,40
s_waitcnt lgkmcnt(2)
v_permlane16_swap_b32 v72, v74
mav 12,156,44
R 28,50176
mav 16,160,44
v_permlane16_swap_b32 v73, v75
mav 20,164,44
buffer_store_dwordx4 v[72:75], v84, s[36:39], s49 offen sc1
v_add_u32 v88, 1, v88
mav 12,168,48
R 32,51200
mav 16,172,48
mav 20,176,48
s_waitcnt lgkmcnt(2)
mav 12,180,52
R 36,52224
mav 16,184,52
mav 20,188,52
mav 12,192,24
R 40,53248
mav 16,196,24
mav 20,200,24
s_waitcnt lgkmcnt(2)
mav 12,204,28
R 44,54272
mav 16,208,28
mav 20,212,28
mav 12,216,32
R 48,55296
mav 16,220,32
mav 20,224,32
s_waitcnt lgkmcnt(2)
mav 12,228,36
R 52,56320
mav 16,232,36
mav 20,236,36
mav 12,240,40
R 24,57344
mav 16,244,40
mav 20,248,40
s_waitcnt lgkmcnt(2)
mav 12,252,44
R 28,58368
mvv 16,128,44
mvv 20,132,44
mvv 12,136,48
R 32,59392
mvv 16,140,48
mvv 20,144,48
s_waitcnt lgkmcnt(2)
mvv 12,148,52
R 36,60416
mvv 16,152,52
mvv 20,156,52
mvv 12,160,24
s_mov_b32 s60, -30
.p2align 6
LF0_4:
mvv 16,164,24
R 40,61440
mvv 20,168,24
s_waitcnt lgkmcnt(2)
ds_read_b32 v106, v82 offset:0
mvv 12,172,28
R 44,62464
mvv 16,176,28
v_add_u32 v76, 1, v76
mvv 20,180,28
mvv 12,184,32
R 48,63488
mvv 16,188,32
mvv 20,192,32
s_waitcnt lgkmcnt(2)
v_cmp_gt_i32 vcc, v88, v106
mvv 12,196,36
R 52,64512
v_xor_b32 v80, 0x10000, v80
mvv 16,200,36
s_waitcnt vmcnt(3)
s_add_u32 s49, s49, 313344
mvv 20,204,36
s_cbranch_vccnz CF0_5
BF0_5:
s_barrier
mvv 12,208,40
s_add_u32 m0, s50, 32768
R 24,0
mvv 16,212,40
D 85,0
mvv 20,216,40
s_waitcnt lgkmcnt(2)
ds_read_b64 v[78:79], v89 offset:128
mvv 12,220,44
R 28,1024
mvv 16,224,44
mvv 20,228,44
D 85,1024
mvv 12,232,48
R 32,2048
mvv 16,236,48
mvv 20,240,48
s_waitcnt lgkmcnt(2)
mvv 12,244,52
D 85,2048
mvv 16,248,52
R 36,3072
mvv 20,252,52
v_mfma_f32_16x16x32_bf16 v[0:3], a[0:3], v[24:27], 0
R 40,4096
v_fma_f32 v104, v79, v56, v92
v_mfma_f32_16x16x32_bf16 v[4:7], a[4:7], v[24:27], 0
D 85,3072
v_mfma_f32_16x16x32_bf16 v[8:11], a[8:11], v[24:27], 0
s_waitcnt lgkmcnt(2)
v_fma_f32 v12, v12, v78, v104
v_fma_f32 v105, v79, v57, v93
mav 0,12,28
s_add_u32 m0, s50, 36864
R 44,5120
v_fma_f32 v13, v13, v78, v105
mav 4,16,28
s_waitcnt vmcnt(4)
v_mul_f32_e64 v12, v12, |v12|
v_mul_f32_e64 v13, v13, |v13|
mav 8,20,28
buffer_store_dword v76, v81, s[36:39], s52 offen sc1
mav 0,24,32
R 48,6144
v_cvt_pk_bf16_f32 v12, v12, v13
mav 4,28,32
v_pk_max_i16 v68, v12, 0
v_fma_f32 v104, v79, v58, v94
mav 8,32,32
s_waitcnt lgkmcnt(2)
v_fma_f32 v14, v14, v78, v104
v_fma_f32 v105, v79, v59, v95
mav 0,36,36
D 86,0
mav 4,40,36
R 52,7168
v_fma_f32 v15, v15, v78, v105
mav 8,44,36
v_mul_f32_e64 v14, v14, |v14|
v_mul_f32_e64 v15, v15, |v15|
mav 0,48,40
R 24,8192
v_cvt_pk_bf16_f32 v14, v14, v15
mav 4,52,40
D 86,1024
)"
R"(
mav 8,56,40
s_waitcnt lgkmcnt(2)
v_pk_max_i16 v69, v14, 0
v_fma_f32 v104, v79, v60, v96
mav 0,60,44
R 28,9216
v_fma_f32 v16, v16, v78, v104
mav 4,64,44
v_fma_f32 v105, v79, v61, v97
v_fma_f32 v17, v17, v78, v105
mav 8,68,44
D 86,2048
mav 0,72,48
R 32,10240
v_mul_f32_e64 v16, v16, |v16|
mav 4,76,48
v_mul_f32_e64 v17, v17, |v17|
v_cvt_pk_bf16_f32 v16, v16, v17
mav 8,80,48
s_waitcnt lgkmcnt(2)
v_pk_max_i16 v70, v16, 0
v_fma_f32 v104, v79, v62, v98
mav 0,84,52
D 86,3072
mav 4,88,52
R 36,11264
v_fma_f32 v18, v18, v78, v104
mav 8,92,52
v_fma_f32 v105, v79, v63, v99
v_fma_f32 v19, v19, v78, v105
mav 0,96,24
R 40,12288
v_mul_f32_e64 v18, v18, |v18|
mav 4,100,24
v_mul_f32_e64 v19, v19, |v19|
v_cvt_pk_bf16_f32 v18, v18, v19
mav 8,104,24
s_waitcnt lgkmcnt(2)
v_pk_max_i16 v71, v18, 0
v_fma_f32 v104, v79, v64, v100
mav 0,108,28
R 44,13312
v_fma_f32 v20, v20, v78, v104
mav 4,112,28
v_fma_f32 v105, v79, v65, v101
v_fma_f32 v21, v21, v78, v105
mav 8,116,28
buffer_store_dwordx4 v[68:71], v83, s[36:39], s49 offen sc1
v_mul_f32_e64 v20, v20, |v20|
v_mul_f32_e64 v21, v21, |v21|
v_cvt_pk_bf16_f32 v20, v20, v21
mav 0,120,32
R 48,14336
v_pk_max_i16 v72, v20, 0
mav 4,124,32
v_pk_max_i16 v74, v20, 0
v_fma_f32 v104, v79, v66, v102
mav 8,128,32
s_waitcnt lgkmcnt(2)
v_fma_f32 v22, v22, v78, v104
v_fma_f32 v105, v79, v67, v103
mav 0,132,36
R 52,15360
v_fma_f32 v23, v23, v78, v105
mav 4,136,36
s_mov_b32 m0, 144384
v_mul_f32_e64 v22, v22, |v22|
v_mul_f32_e64 v23, v23, |v23|
mav 8,140,36
v_cvt_pk_bf16_f32 v22, v22, v23
v_pk_max_i16 v73, v22, 0
mav 0,144,40
R 24,16384
v_pk_max_i16 v75, v22, 0
mav 4,148,40
buffer_load_dword v87, s[36:39], 0 offen sc1 lds
mav 8,152,40
s_waitcnt lgkmcnt(2)
v_permlane16_swap_b32 v72, v74
mav 0,156,44
R 28,17408
mav 4,160,44
v_permlane16_swap_b32 v73, v75
mav 8,164,44
buffer_store_dwordx4 v[72:75], v84, s[36:39], s49 offen sc1
v_add_u32 v88, 1, v88
mav 0,168,48
R 32,18432
mav 4,172,48
mav 8,176,48
s_waitcnt lgkmcnt(2)
mav 0,180,52
R 36,19456
mav 4,184,52
mav 8,188,52
mav 0,192,24
R 40,20480
mav 4,196,24
mav 8,200,24
s_waitcnt lgkmcnt(2)
mav 0,204,28
R 44,21504
mav 4,208,28
mav 8,212,28
mav 0,216,32
R 48,22528
mav 4,220,32
mav 8,224,32
s_waitcnt lgkmcnt(2)
mav 0,228,36
R 52,23552
mav 4,232,36
mav 8,236,36
mav 0,240,40
R 24,24576
mav 4,244,40
mav 8,248,40
s_waitcnt lgkmcnt(2)
mav 0,252,44
R 28,25600
mvv 4,128,44
mvv 8,132,44
mvv 0,136,48
R 32,26624
mvv 4,140,48
mvv 8,144,48
s_waitcnt lgkmcnt(2)
mvv 0,148,52
R 36,27648
mvv 4,152,52
mvv 8,156,52
mvv 0,160,24
R 40,28672
mvv 4,164,24
ds_read_b32 v106, v82 offset:0
mvv 8,168,24
s_waitcnt lgkmcnt(3)
mvv 0,172,28
R 44,29696
mvv 4,176,28
v_add_u32 v76, 1, v76
mvv 8,180,28
mvv 0,184,32
s_xor_b32 s50, s50, 0x10000
R 48,30720
mvv 4,188,32
mvv 8,192,32
s_waitcnt lgkmcnt(2)
v_cmp_gt_i32 vcc, v88, v106
mvv 0,196,36
R 52,31744
mvv 4,200,36
s_waitcnt vmcnt(3)
s_add_u32 s49, s49, 313344
mvv 8,204,36
s_cbranch_vccnz CF0_6
BF0_6:
s_barrier
mvv 0,208,40
s_add_u32 m0, s50, 0
R 24,32768
mvv 4,212,40
D 85,0
mvv 8,216,40
s_waitcnt lgkmcnt(2)
ds_read_b64 v[78:79], v89 offset:0
mvv 0,220,44
R 28,33792
mvv 4,224,44
mvv 8,228,44
D 85,1024
mvv 0,232,48
R 32,34816
mvv 4,236,48
mvv 8,240,48
s_waitcnt lgkmcnt(2)
mvv 0,244,52
D 85,2048
mvv 4,248,52
R 36,35840
mvv 8,252,52
v_mfma_f32_16x16x32_bf16 v[12:15], a[0:3], v[24:27], 0
R 40,36864
v_fma_f32 v104, v79, v56, v92
v_mfma_f32_16x16x32_bf16 v[16:19], a[4:7], v[24:27], 0
D 85,3072
v_mfma_f32_16x16x32_bf16 v[20:23], a[8:11], v[24:27], 0
s_waitcnt lgkmcnt(2)
v_fma_f32 v0, v0, v78, v104
v_fma_f32 v105, v79, v57, v93
mav 12,12,28
s_add_u32 m0, s50, 4096
R 44,37888
v_fma_f32 v1, v1, v78, v105
mav 16,16,28
s_waitcnt vmcnt(4)
v_mul_f32_e64 v0, v0, |v0|
v_mul_f32_e64 v1, v1, |v1|
mav 20,20,28
buffer_store_dword v76, v81, s[36:39], s52 offen sc1
mav 12,24,32
R 48,38912
v_cvt_pk_bf16_f32 v0, v0, v1
mav 16,28,32
v_pk_max_i16 v68, v0, 0
v_fma_f32 v104, v79, v58, v94
mav 20,32,32
s_waitcnt lgkmcnt(2)
v_fma_f32 v2, v2, v78, v104
v_fma_f32 v105, v79, v59, v95
mav 12,36,36
D 86,0
mav 16,40,36
R 52,39936
v_fma_f32 v3, v3, v78, v105
mav 20,44,36
v_mul_f32_e64 v2, v2, |v2|
v_mul_f32_e64 v3, v3, |v3|
mav 12,48,40
R 24,40960
v_cvt_pk_bf16_f32 v2, v2, v3
mav 16,52,40
D 86,1024
mav 20,56,40
s_waitcnt lgkmcnt(2)
v_pk_max_i16 v69, v2, 0
v_fma_f32 v104, v79, v60, v96
mav 12,60,44
R 28,41984
v_fma_f32 v4, v4, v78, v104
mav 16,64,44
v_fma_f32 v105, v79, v61, v97
v_fma_f32 v5, v5, v78, v105
mav 20,68,44
D 86,2048
mav 12,72,48
R 32,43008
v_mul_f32_e64 v4, v4, |v4|
mav 16,76,48
v_mul_f32_e64 v5, v5, |v5|
v_cvt_pk_bf16_f32 v4, v4, v5
mav 20,80,48
s_waitcnt lgkmcnt(2)
v_pk_max_i16 v70, v4, 0
v_fma_f32 v104, v79, v62, v98
mav 12,84,52
D 86,3072
mav 16,88,52
R 36,44032
v_fma_f32 v6, v6, v78, v104
mav 20,92,52
v_fma_f32 v105, v79, v63, v99
v_fma_f32 v7, v7, v78, v105
mav 12,96,24
R 40,45056
v_mul_f32_e64 v6, v6, |v6|
mav 16,100,24
v_mul_f32_e64 v7, v7, |v7|
v_cvt_pk_bf16_f32 v6, v6, v7
mav 20,104,24
s_waitcnt lgkmcnt(2)
v_pk_max_i16 v71, v6, 0
v_fma_f32 v104, v79, v64, v100
mav 12,108,28
R 44,46080
v_fma_f32 v8, v8, v78, v104
mav 16,112,28
v_fma_f32 v105, v79, v65, v101
v_fma_f32 v9, v9, v78, v105
mav 20,116,28
buffer_store_dwordx4 v[68:71], v83, s[36:39], s49 offen sc1
v_mul_f32_e64 v8, v8, |v8|
v_mul_f32_e64 v9, v9, |v9|
v_cvt_pk_bf16_f32 v8, v8, v9
mav 12,120,32
R 48,47104
v_pk_max_i16 v72, v8, 0
mav 16,124,32
v_pk_max_i16 v74, v8, 0
v_fma_f32 v104, v79, v66, v102
mav 20,128,32
s_waitcnt lgkmcnt(2)
v_fma_f32 v10, v10, v78, v104
v_fma_f32 v105, v79, v67, v103
mav 12,132,36
R 52,48128
v_fma_f32 v11, v11, v78, v105
mav 16,136,36
s_mov_b32 m0, 144384
v_mul_f32_e64 v10, v10, |v10|
v_mul_f32_e64 v11, v11, |v11|
mav 20,140,36
v_cvt_pk_bf16_f32 v10, v10, v11
v_pk_max_i16 v73, v10, 0
mav 12,144,40
R 24,49152
v_pk_max_i16 v75, v10, 0
mav 16,148,40
buffer_load_dword v87, s[36:39], 0 offen sc1 lds
mav 20,152,40
s_waitcnt lgkmcnt(2)
v_permlane16_swap_b32 v72, v74
mav 12,156,44
R 28,50176
mav 16,160,44
v_permlane16_swap_b32 v73, v75
mav 20,164,44
buffer_store_dwordx4 v[72:75], v84, s[36:39], s49 offen sc1
v_add_u32 v88, 1, v88
mav 12,168,48
R 32,51200
mav 16,172,48
mav 20,176,48
s_waitcnt lgkmcnt(2)
mav 12,180,52
R 36,52224
mav 16,184,52
mav 20,188,52
mav 12,192,24
R 40,53248
mav 16,196,24
mav 20,200,24
s_waitcnt lgkmcnt(2)
mav 12,204,28
R 44,54272
mav 16,208,28
mav 20,212,28
mav 12,216,32
R 48,55296
mav 16,220,32
mav 20,224,32
s_waitcnt lgkmcnt(2)
mav 12,228,36
R 52,56320
mav 16,232,36
mav 20,236,36
mav 12,240,40
R 24,57344
mav 16,244,40
mav 20,248,40
s_waitcnt lgkmcnt(2)
mav 12,252,44
R 28,58368
mvv 16,128,44
mvv 20,132,44
mvv 12,136,48
R 32,59392
mvv 16,140,48
mvv 20,144,48
s_waitcnt lgkmcnt(2)
mvv 12,148,52
R 36,60416
mvv 16,152,52
mvv 20,156,52
s_add_u32 s60, s60, 1
mvv 12,160,24
s_cbranch_scc0 LF0_4
mvv 16,164,24
R 40,61440
mvv 20,168,24
s_waitcnt lgkmcnt(2)
ds_read_b32 v106, v82 offset:0
LsF3:
s_cbranch_execz LsF7
mvv 12,172,28
R 44,62464
mvv 16,176,28
v_add_u32 v76, 1, v76
mvv 20,180,28
mvv 12,184,32
R 48,63488
mvv 16,188,32
mvv 20,192,32
s_waitcnt lgkmcnt(2)
v_cmp_gt_i32 vcc, v88, v106
mvv 12,196,36
R 52,64512
v_xor_b32 v80, 0x10000, v80
mvv 16,200,36
s_waitcnt vmcnt(3)
s_add_u32 s49, s49, 313344
mvv 20,204,36
s_cbranch_vccnz CF0_7
BF0_7:
s_barrier
mvv 12,208,40
s_add_u32 m0, s50, 32768
R 24,0
mvv 16,212,40
D 85,0
mvv 20,216,40
s_waitcnt lgkmcnt(2)
ds_read_b64 v[78:79], v89 offset:128
mvv 12,220,44
R 28,1024
mvv 16,224,44
mvv 20,228,44
D 85,1024
mvv 12,232,48
R 32,2048
mvv 16,236,48
mvv 20,240,48
s_waitcnt lgkmcnt(2)
mvv 12,244,52
D 85,2048
mvv 16,248,52
R 36,3072
mvv 20,252,52
v_mfma_f32_16x16x32_bf16 v[0:3], a[0:3], v[24:27], 0
R 40,4096
v_fma_f32 v104, v79, v56, v92
v_mfma_f32_16x16x32_bf16 v[4:7], a[4:7], v[24:27], 0
D 85,3072
v_mfma_f32_16x16x32_bf16 v[8:11], a[8:11], v[24:27], 0
s_waitcnt lgkmcnt(2)
v_fma_f32 v12, v12, v78, v104
v_fma_f32 v105, v79, v57, v93
mav 0,12,28
s_add_u32 m0, s50, 36864
R 44,5120
v_fma_f32 v13, v13, v78, v105
mav 4,16,28
s_waitcnt vmcnt(4)
v_mul_f32_e64 v12, v12, |v12|
v_mul_f32_e64 v13, v13, |v13|
mav 8,20,28
buffer_store_dword v76, v81, s[36:39], s52 offen sc1
mav 0,24,32
R 48,6144
v_cvt_pk_bf16_f32 v12, v12, v13
mav 4,28,32
v_pk_max_i16 v68, v12, 0
v_fma_f32 v104, v79, v58, v94
mav 8,32,32
s_waitcnt lgkmcnt(2)
v_fma_f32 v14, v14, v78, v104
v_fma_f32 v105, v79, v59, v95
mav 0,36,36
D 86,0
mav 4,40,36
R 52,7168
v_fma_f32 v15, v15, v78, v105
mav 8,44,36
v_mul_f32_e64 v14, v14, |v14|
v_mul_f32_e64 v15, v15, |v15|
mav 0,48,40
R 24,8192
v_cvt_pk_bf16_f32 v14, v14, v15
mav 4,52,40
D 86,1024
mav 8,56,40
s_waitcnt lgkmcnt(2)
v_pk_max_i16 v69, v14, 0
v_fma_f32 v104, v79, v60, v96
mav 0,60,44
R 28,9216
v_fma_f32 v16, v16, v78, v104
mav 4,64,44
v_fma_f32 v105, v79, v61, v97
v_fma_f32 v17, v17, v78, v105
mav 8,68,44
D 86,2048
mav 0,72,48
R 32,10240
v_mul_f32_e64 v16, v16, |v16|
mav 4,76,48
v_mul_f32_e64 v17, v17, |v17|
v_cvt_pk_bf16_f32 v16, v16, v17
mav 8,80,48
s_waitcnt lgkmcnt(2)
v_pk_max_i16 v70, v16, 0
v_fma_f32 v104, v79, v62, v98
mav 0,84,52
D 86,3072
mav 4,88,52
R 36,11264
v_fma_f32 v18, v18, v78, v104
mav 8,92,52
v_fma_f32 v105, v79, v63, v99
v_fma_f32 v19, v19, v78, v105
mav 0,96,24
R 40,12288
v_mul_f32_e64 v18, v18, |v18|
mav 4,100,24
v_mul_f32_e64 v19, v19, |v19|
v_cvt_pk_bf16_f32 v18, v18, v19
mav 8,104,24
s_waitcnt lgkmcnt(2)
v_pk_max_i16 v71, v18, 0
v_fma_f32 v104, v79, v64, v100
mav 0,108,28
R 44,13312
v_fma_f32 v20, v20, v78, v104
mav 4,112,28
v_fma_f32 v105, v79, v65, v101
v_fma_f32 v21, v21, v78, v105
mav 8,116,28
buffer_store_dwordx4 v[68:71], v83, s[36:39], s49 offen sc1
v_mul_f32_e64 v20, v20, |v20|
v_mul_f32_e64 v21, v21, |v21|
v_cvt_pk_bf16_f32 v20, v20, v21
mav 0,120,32
R 48,14336
v_pk_max_i16 v72, v20, 0
mav 4,124,32
v_pk_max_i16 v74, v20, 0
v_fma_f32 v104, v79, v66, v102
mav 8,128,32
s_waitcnt lgkmcnt(2)
v_fma_f32 v22, v22, v78, v104
v_fma_f32 v105, v79, v67, v103
mav 0,132,36
R 52,15360
v_fma_f32 v23, v23, v78, v105
mav 4,136,36
s_mov_b32 m0, 144384
v_mul_f32_e64 v22, v22, |v22|
v_mul_f32_e64 v23, v23, |v23|
mav 8,140,36
v_cvt_pk_bf16_f32 v22, v22, v23
v_pk_max_i16 v73, v22, 0
mav 0,144,40
R 24,16384
v_pk_max_i16 v75, v22, 0
mav 4,148,40
buffer_load_dword v87, s[36:39], 0 offen sc1 lds
mav 8,152,40
s_waitcnt lgkmcnt(2)
v_permlane16_swap_b32 v72, v74
mav 0,156,44
R 28,17408
mav 4,160,44
v_permlane16_swap_b32 v73, v75
mav 8,164,44
buffer_store_dwordx4 v[72:75], v84, s[36:39], s49 offen sc1
v_add_u32 v88, 1, v88
mav 0,168,48
R 32,18432
mav 4,172,48
mav 8,176,48
s_waitcnt lgkmcnt(2)
mav 0,180,52
R 36,19456
mav 4,184,52
mav 8,188,52
mav 0,192,24
R 40,20480
mav 4,196,24
mav 8,200,24
s_waitcnt lgkmcnt(2)
mav 0,204,28
R 44,21504
mav 4,208,28
mav 8,212,28
mav 0,216,32
R 48,22528
mav 4,220,32
mav 8,224,32
s_waitcnt lgkmcnt(2)
mav 0,228,36
R 52,23552
mav 4,232,36
mav 8,236,36
mav 0,240,40
R 24,24576
mav 4,244,40
mav 8,248,40
s_waitcnt lgkmcnt(2)
mav 0,252,44
R 28,25600
mvv 4,128,44
mvv 8,132,44
mvv 0,136,48
R 32,26624
mvv 4,140,48
mvv 8,144,48
s_waitcnt lgkmcnt(2)
mvv 0,148,52
R 36,27648
mvv 4,152,52
mvv 8,156,52
mvv 0,160,24
R 40,28672
mvv 4,164,24
ds_read_b32 v106, v82 offset:0
mvv 8,168,24
s_waitcnt lgkmcnt(3)
mvv 0,172,28
R 44,29696
mvv 4,176,28
v_add_u32 v76, 1, v76
mvv 8,180,28
mvv 0,184,32
R 48,30720
mvv 4,188,32
mvv 8,192,32
s_waitcnt lgkmcnt(2)
v_cmp_gt_i32 vcc, v88, v106
mvv 0,196,36
R 52,31744
mvv 4,200,36
s_waitcnt vmcnt(3)
s_add_u32 s49, s49, 313344
mvv 8,204,36
s_cbranch_vccnz CF0_8
BF0_8:
s_barrier
mvv 0,208,40
R 24,32768
mvv 4,212,40
ds_read_b64 v[78:79], v89 offset:0
mvv 8,216,40
s_waitcnt lgkmcnt(3)
mvv 0,220,44
R 28,33792
mvv 4,224,44
mvv 8,228,44
mvv 0,232,48
R 32,34816
mvv 4,236,48
mvv 8,240,48
s_waitcnt lgkmcnt(2)
mvv 0,244,52
R 36,35840
mvv 4,248,52
mvv 8,252,52
v_mfma_f32_16x16x32_bf16 v[12:15], a[0:3], v[24:27], 0
R 40,36864
v_fma_f32 v104, v79, v56, v92
v_mfma_f32_16x16x32_bf16 v[16:19], a[4:7], v[24:27], 0
v_fma_f32 v105, v79, v57, v93
v_fma_f32 v0, v0, v78, v104
v_mfma_f32_16x16x32_bf16 v[20:23], a[8:11], v[24:27], 0
s_waitcnt lgkmcnt(2)
v_fma_f32 v1, v1, v78, v105
v_mul_f32_e64 v0, v0, |v0|
mav 12,12,28
R 44,37888
v_mul_f32_e64 v1, v1, |v1|
mav 16,16,28
s_waitcnt vmcnt(0)
v_cvt_pk_bf16_f32 v0, v0, v1
v_pk_max_i16 v68, v0, 0
mav 20,20,28
buffer_store_dword v76, v81, s[36:39], s52 offen sc1
mav 12,24,32
R 48,38912
v_fma_f32 v104, v79, v58, v94
mav 16,28,32
v_fma_f32 v2, v2, v78, v104
v_fma_f32 v105, v79, v59, v95
mav 20,32,32
s_waitcnt lgkmcnt(2)
v_fma_f32 v3, v3, v78, v105
v_mul_f32_e64 v2, v2, |v2|
mav 12,36,36
R 52,39936
v_mul_f32_e64 v3, v3, |v3|
mav 16,40,36
v_cvt_pk_bf16_f32 v2, v2, v3
v_pk_max_i16 v69, v2, 0
mav 20,44,36
v_fma_f32 v104, v79, v60, v96
v_fma_f32 v4, v4, v78, v104
mav 12,48,40
R 24,40960
v_fma_f32 v105, v79, v61, v97
mav 16,52,40
v_fma_f32 v5, v5, v78, v105
v_mul_f32_e64 v4, v4, |v4|
mav 20,56,40
s_waitcnt lgkmcnt(2)
v_mul_f32_e64 v5, v5, |v5|
v_cvt_pk_bf16_f32 v4, v4, v5
mav 12,60,44
R 28,41984
v_pk_max_i16 v70, v4, 0
mav 16,64,44
v_fma_f32 v104, v79, v62, v98
v_fma_f32 v6, v6, v78, v104
mav 20,68,44
v_fma_f32 v105, v79, v63, v99
v_fma_f32 v7, v7, v78, v105
mav 12,72,48
R 32,43008
v_mul_f32_e64 v6, v6, |v6|
mav 16,76,48
v_mul_f32_e64 v7, v7, |v7|
v_cvt_pk_bf16_f32 v6, v6, v7
mav 20,80,48
s_waitcnt lgkmcnt(2)
v_pk_max_i16 v71, v6, 0
v_fma_f32 v104, v79, v64, v100
mav 12,84,52
buffer_store_dwordx4 v[68:71], v83, s[36:39], s49 offen sc1
R 36,44032
v_fma_f32 v8, v8, v78, v104
mav 16,88,52
v_fma_f32 v105, v79, v65, v101
v_fma_f32 v9, v9, v78, v105
mav 20,92,52
v_mul_f32_e64 v8, v8, |v8|
v_mul_f32_e64 v9, v9, |v9|
mav 12,96,24
R 40,45056
v_cvt_pk_bf16_f32 v8, v8, v9
mav 16,100,24
v_pk_max_i16 v72, v8, 0
v_pk_max_i16 v74, v8, 0
mav 20,104,24
s_waitcnt lgkmcnt(2)
v_fma_f32 v104, v79, v66, v102
v_fma_f32 v10, v10, v78, v104
mav 12,108,28
R 44,46080
v_fma_f32 v105, v79, v67, v103
mav 16,112,28
v_fma_f32 v11, v11, v78, v105
v_mul_f32_e64 v10, v10, |v10|
mav 20,116,28
v_mul_f32_e64 v11, v11, |v11|
v_cvt_pk_bf16_f32 v10, v10, v11
mav 12,120,32
R 48,47104
v_pk_max_i16 v73, v10, 0
mav 16,124,32
v_pk_max_i16 v75, v10, 0
mav 20,128,32
s_waitcnt lgkmcnt(2)
v_permlane16_swap_b32 v72, v74
mav 12,132,36
R 52,48128
mav 16,136,36
v_permlane16_swap_b32 v73, v75
mav 20,140,36
mav 12,144,40
R 24,49152
mav 16,148,40
buffer_store_dwordx4 v[72:75], v84, s[36:39], s49 offen sc1
mav 20,152,40
s_waitcnt lgkmcnt(2)
mav 12,156,44
R 28,50176
mav 16,160,44
mav 20,164,44
mav 12,168,48
R 32,51200
mav 16,172,48
mav 20,176,48
s_waitcnt lgkmcnt(2)
mav 12,180,52
R 36,52224
mav 16,184,52
mav 20,188,52
mav 12,192,24
R 40,53248
mav 16,196,24
mav 20,200,24
s_waitcnt lgkmcnt(2)
mav 12,204,28
R 44,54272
mav 16,208,28
mav 20,212,28
mav 12,216,32
R 48,55296
mav 16,220,32
mav 20,224,32
s_waitcnt lgkmcnt(2)
mav 12,228,36
R 52,56320
mav 16,232,36
mav 20,236,36
mav 12,240,40
R 24,57344
mav 16,244,40
mav 20,248,40
s_waitcnt lgkmcnt(2)
mav 12,252,44
R 28,58368
mvv 16,128,44
mvv 20,132,44
mvv 12,136,48
R 32,59392
mvv 16,140,48
mvv 20,144,48
s_waitcnt lgkmcnt(2)
mvv 12,148,52
R 36,60416
mvv 16,152,52
mvv 20,156,52
mvv 12,160,24
mvv 16,164,24
R 40,61440
mvv 20,168,24
s_waitcnt lgkmcnt(2)
mvv 12,172,28
R 44,62464
mvv 16,176,28
v_add_u32 v76, 1, v76
mvv 20,180,28
mvv 12,184,32
R 48,63488
mvv 16,188,32
mvv 20,192,32
s_waitcnt lgkmcnt(2)
mvv 12,196,36
R 52,64512
mvv 16,200,36
s_add_u32 s49, s49, 313344
mvv 20,204,36
s_barrier
mvv 12,208,40
mvv 16,212,40
ds_read_b64 v[78:79], v89 offset:128
mvv 20,216,40
s_waitcnt lgkmcnt(2)
mvv 12,220,44
mvv 16,224,44
mvv 20,228,44
mvv 12,232,48
mvv 16,236,48
mvv 20,240,48
s_waitcnt lgkmcnt(0)
mvv 12,244,52
mvv 16,248,52
mvv 20,252,52
v_fma_f32 v104, v79, v56, v92
s_nop 4
v_fma_f32 v12, v12, v78, v104
v_fma_f32 v105, v79, v57, v93
v_fma_f32 v13, v13, v78, v105
v_mul_f32_e64 v12, v12, |v12|
v_mul_f32_e64 v13, v13, |v13|
v_cvt_pk_bf16_f32 v12, v12, v13
v_pk_max_i16 v68, v12, 0
v_fma_f32 v104, v79, v58, v94
v_fma_f32 v14, v14, v78, v104
v_fma_f32 v105, v79, v59, v95
v_fma_f32 v15, v15, v78, v105
v_mul_f32_e64 v14, v14, |v14|
v_mul_f32_e64 v15, v15, |v15|
v_cvt_pk_bf16_f32 v14, v14, v15
v_pk_max_i16 v69, v14, 0
v_fma_f32 v104, v79, v60, v96
v_fma_f32 v16, v16, v78, v104
v_fma_f32 v105, v79, v61, v97
v_fma_f32 v17, v17, v78, v105
v_mul_f32_e64 v16, v16, |v16|
v_mul_f32_e64 v17, v17, |v17|
v_cvt_pk_bf16_f32 v16, v16, v17
v_pk_max_i16 v70, v16, 0
v_fma_f32 v104, v79, v62, v98
v_fma_f32 v18, v18, v78, v104
v_fma_f32 v105, v79, v63, v99
v_fma_f32 v19, v19, v78, v105
v_mul_f32_e64 v18, v18, |v18|
v_mul_f32_e64 v19, v19, |v19|
v_cvt_pk_bf16_f32 v18, v18, v19
v_pk_max_i16 v71, v18, 0
buffer_store_dwordx4 v[68:71], v83, s[36:39], s49 offen sc1
v_fma_f32 v104, v79, v64, v100
v_fma_f32 v20, v20, v78, v104
v_fma_f32 v105, v79, v65, v101
v_fma_f32 v21, v21, v78, v105
v_mul_f32_e64 v20, v20, |v20|
v_mul_f32_e64 v21, v21, |v21|
v_cvt_pk_bf16_f32 v20, v20, v21
v_pk_max_i16 v72, v20, 0
v_pk_max_i16 v74, v20, 0
v_fma_f32 v104, v79, v66, v102
v_fma_f32 v22, v22, v78, v104
v_fma_f32 v105, v79, v67, v103
v_fma_f32 v23, v23, v78, v105
v_mul_f32_e64 v22, v22, |v22|
v_mul_f32_e64 v23, v23, |v23|
v_cvt_pk_bf16_f32 v22, v22, v23
v_pk_max_i16 v73, v22, 0
v_pk_max_i16 v75, v22, 0
v_permlane16_swap_b32 v72, v74
s_nop 0
v_permlane16_swap_b32 v73, v75
buffer_store_dwordx4 v[72:75], v84, s[36:39], s49 offen sc1
s_waitcnt vmcnt(2)
buffer_store_dword v76, v81, s[36:39], s52 offen sc1
v_add_u32 v76, 37, v76
s_waitcnt vmcnt(1)
buffer_store_dword v76, v81, s[36:39], s52 offen sc1
s_endpgm
CF0_1:
s_cmp_eq_u32 s61, 0
s_cbranch_scc1 BF0_1
s_sub_u32 s61, s61, 1
s_sleep 8
buffer_load_dwordx2 v[106:107], v87, s[36:39], 0 offen sc1
s_waitcnt vmcnt(0)
ds_write_b32 v82, v106
v_sub_u32 v106, v106, v88
v_xor_b32 v107, v107, v77
v_cmp_lt_u64 vcc, s[62:63], v[106:107]
s_waitcnt lgkmcnt(0)
s_cbranch_vccnz CF0_1
s_branch BF0_1
CF0_2:
s_mov_b32 s58, m0
s_mov_b32 m0, 144384
DF0_2:
s_cmp_eq_u32 s61, 0
s_cbranch_scc1 EF0_2
s_sub_u32 s61, s61, 1
buffer_load_dword v87, s[36:39], 0 offen sc1 lds
s_sleep 1
ds_read_b32 v106, v82
s_waitcnt lgkmcnt(0)
v_cmp_gt_i32 vcc, v88, v106
s_cbranch_vccnz DF0_2
EF0_2:
s_mov_b32 m0, s58
s_branch BF0_2
CF0_3:
s_mov_b32 s58, m0
s_mov_b32 m0, 144384
DF0_3:
s_cmp_eq_u32 s61, 0
s_cbranch_scc1 EF0_3
s_sub_u32 s61, s61, 1
buffer_load_dword v87, s[36:39], 0 offen sc1 lds
s_sleep 1
ds_read_b32 v106, v82
LsF4:
s_cbranch_execz LsF8
s_waitcnt lgkmcnt(0)
v_cmp_gt_i32 vcc, v88, v106
s_cbranch_vccnz DF0_3
EF0_3:
s_mov_b32 m0, s58
s_branch BF0_3
CF0_5:
s_mov_b32 s58, m0
s_mov_b32 m0, 144384
DF0_5:
s_cmp_eq_u32 s61, 0
s_cbranch_scc1 EF0_5
s_sub_u32 s61, s61, 1
buffer_load_dword v87, s[36:39], 0 offen sc1 lds
s_sleep 1
ds_read_b32 v106, v82
s_waitcnt lgkmcnt(0)
v_cmp_gt_i32 vcc, v88, v106
s_cbranch_vccnz DF0_5
EF0_5:
s_mov_b32 m0, s58
s_branch BF0_5
CF0_6:
s_mov_b32 s58, m0
s_mov_b32 m0, 144384
DF0_6:
s_cmp_eq_u32 s61, 0
s_cbranch_scc1 EF0_6
s_sub_u32 s61, s61, 1
buffer_load_dword v87, s[36:39], 0 offen sc1 lds
s_sleep 1
ds_read_b32 v106, v82
s_waitcnt lgkmcnt(0)
v_cmp_gt_i32 vcc, v88, v106
s_cbranch_vccnz DF0_6
EF0_6:
s_mov_b32 m0, s58
s_branch BF0_6
CF0_7:
s_mov_b32 s58, m0
s_mov_b32 m0, 144384
DF0_7:
s_cmp_eq_u32 s61, 0
s_cbranch_scc1 EF0_7
s_sub_u32 s61, s61, 1
buffer_load_dword v87, s[36:39], 0 offen sc1 lds
s_sleep 1
ds_read_b32 v106, v82
s_waitcnt lgkmcnt(0)
v_cmp_gt_i32 vcc, v88, v106
s_cbranch_vccnz DF0_7
EF0_7:
s_mov_b32 m0, s58
s_branch BF0_7
CF0_8:
s_mov_b32 s58, m0
s_mov_b32 m0, 144384
DF0_8:
s_cmp_eq_u32 s61, 0
s_cbranch_scc1 EF0_8
s_sub_u32 s61, s61, 1
buffer_load_dword v87, s[36:39], 0 offen sc1 lds
s_sleep 1
ds_read_b32 v106, v82
s_waitcnt lgkmcnt(0)
v_cmp_gt_i32 vcc, v88, v106
s_cbranch_vccnz DF0_8
EF0_8:
s_mov_b32 m0, s58
s_branch BF0_8
LvF1:
LsF5:
s_cbranch_execz LsF9
s_add_u32 s49, s49, 313344
s_barrier
s_add_u32 m0, s50, 0
s_nop 0
D 85,0
D 85,1024
D 85,2048
D 85,3072
s_add_u32 m0, s50, 4096
s_nop 0
D 86,0
D 86,1024
D 86,2048
D 86,3072
s_waitcnt vmcnt(0)
s_add_u32 s49, s49, 313344
s_barrier
s_add_u32 m0, s50, 32768
R 24,0
buffer_load_dwordx2 v[108:109], v90, s[36:39], s49 offen sc1
buffer_load_dwordx2 v[110:111], v90, s[36:39], s49 offen offset:512 sc1
D 85,0
R 28,1024
D 85,1024
R 32,2048
R 36,3072
D 85,2048
s_waitcnt lgkmcnt(2)
v_mfma_f32_16x16x32_bf16 v[0:3], a[0:3], v[24:27], 0
R 40,4096
v_mfma_f32_16x16x32_bf16 v[4:7], a[4:7], v[24:27], 0
v_mfma_f32_16x16x32_bf16 v[8:11], a[8:11], v[24:27], 0
mav 0,12,28
R 44,5120
mav 4,16,28
D 85,3072
mav 8,20,28
s_waitcnt lgkmcnt(2)
s_add_u32 m0, s50, 36864
mav 0,24,32
R 48,6144
mav 4,28,32
mav 8,32,32
D 86,0
mav 0,36,36
R 52,7168
mav 4,40,36
mav 8,44,36
s_waitcnt lgkmcnt(2)
mav 0,48,40
D 86,1024
mav 4,52,40
R 24,8192
mav 8,56,40
mav 0,60,44
R 28,9216
mav 4,64,44
D 86,2048
mav 8,68,44
s_waitcnt lgkmcnt(2)
mav 0,72,48
R 32,10240
mav 4,76,48
mav 8,80,48
D 86,3072
mav 0,84,52
R 36,11264
mav 4,88,52
mav 8,92,52
s_waitcnt lgkmcnt(2)
mav 0,96,24
R 40,12288
mav 4,100,24
mav 8,104,24
mav 0,108,28
R 44,13312
mav 4,112,28
mav 8,116,28
s_waitcnt lgkmcnt(2)
mav 0,120,32
R 48,14336
mav 4,124,32
mav 8,128,32
mav 0,132,36
R 52,15360
mav 4,136,36
mav 8,140,36
s_waitcnt lgkmcnt(2)
mav 0,144,40
R 24,16384
mav 4,148,40
mav 8,152,40
mav 0,156,44
R 28,17408
mav 4,160,44
mav 8,164,44
s_waitcnt lgkmcnt(2)
mav 0,168,48
R 32,18432
mav 4,172,48
mav 8,176,48
mav 0,180,52
R 36,19456
mav 4,184,52
mav 8,188,52
s_waitcnt lgkmcnt(2)
mav 0,192,24
R 40,20480
mav 4,196,24
mav 8,200,24
s_waitcnt vmcnt(8)
v_add_f32 v108, v108, v110
v_add_f32 v109, v109, v111
mav 0,204,28
R 44,21504
mav 4,208,28
v_permlane32_swap_b32 v108, v109
mav 8,212,28
s_waitcnt lgkmcnt(2)
v_add_f32 v110, v108, v109
v_add_f32 v111, v108, v109
mav 0,216,32
R 48,22528
mav 4,220,32
v_permlane16_swap_b32 v110, v111
mav 8,224,32
v_add_f32 v108, v110, v111
v_add_f32 v109, v110, v111
mav 0,228,36
R 52,23552
mav 4,232,36
v_permlane32_swap_b32 v108, v109
mav 8,236,36
s_waitcnt lgkmcnt(2)
v_mul_f32 v110, 0x3a800000, v108
v_mul_f32 v111, 0x3a800000, v109
mav 0,240,40
R 24,24576
v_fma_f32 v111, -v110, v110, v111
mav 4,244,40
v_max_f32 v111, 0, v111
v_add_f32 v111, 0x3727c5ac, v111
mav 8,248,40
v_rsq_f32 v108, v111
mav 0,252,44
R 28,25600
v_mul_f32_e64 v109, -v110, v108
mvv 4,128,44
mvv 8,132,44
s_waitcnt lgkmcnt(2)
mvv 0,136,48
R 32,26624
mvv 4,140,48
mvv 8,144,48
mvv 0,148,52
R 36,27648
mvv 4,152,52
mvv 8,156,52
s_waitcnt lgkmcnt(2)
mvv 0,160,24
R 40,28672
mvv 4,164,24
mvv 8,168,24
mvv 0,172,28
R 44,29696
mvv 4,176,28
mvv 8,180,28
s_waitcnt lgkmcnt(2)
ds_write_b64 v89, v[108:109] offset:0
mvv 0,184,32
s_xor_b32 s50, s50, 0x10000
R 48,30720
mvv 4,188,32
mvv 8,192,32
mvv 0,196,36
R 52,31744
mvv 4,200,36
s_waitcnt vmcnt(0) lgkmcnt(2)
s_add_u32 s49, s49, 313344
mvv 8,204,36
s_barrier
mvv 0,208,40
buffer_load_dwordx2 v[108:109], v90, s[36:39], s49 offen sc1
mvv 4,212,40
s_add_u32 m0, s50, 0
R 24,32768
mvv 8,216,40
ds_read_b64 v[78:79], v89 offset:0
mvv 0,220,44
R 28,33792
mvv 4,224,44
buffer_load_dwordx2 v[110:111], v90, s[36:39], s49 offen offset:512 sc1
mvv 8,228,44
s_waitcnt lgkmcnt(3)
mvv 0,232,48
R 32,34816
mvv 4,236,48
mvv 8,240,48
D 85,0
mvv 0,244,52
R 36,35840
mvv 4,248,52
mvv 8,252,52
s_waitcnt lgkmcnt(2)
v_mfma_f32_16x16x32_bf16 v[12:15], a[0:3], v[24:27], 0
D 85,1024
v_mfma_f32_16x16x32_bf16 v[16:19], a[4:7], v[24:27], 0
R 40,36864
v_fma_f32 v104, v79, v56, v92
v_mfma_f32_16x16x32_bf16 v[20:23], a[8:11], v[24:27], 0
v_fma_f32 v0, v0, v78, v104
v_fma_f32 v105, v79, v57, v93
mav 12,12,28
R 44,37888
v_fma_f32 v1, v1, v78, v105
mav 16,16,28
D 85,2048
mav 20,20,28
s_waitcnt lgkmcnt(2)
v_mul_f32_e64 v0, v0, |v0|
v_mul_f32_e64 v1, v1, |v1|
mav 12,24,32
R 48,38912
v_cvt_pk_bf16_f32 v0, v0, v1
mav 16,28,32
v_pk_max_i16 v68, v0, 0
v_fma_f32 v104, v79, v58, v94
mav 20,32,32
D 85,3072
mav 12,36,36
s_add_u32 m0, s50, 4096
R 52,39936
v_fma_f32 v2, v2, v78, v104
mav 16,40,36
v_fma_f32 v105, v79, v59, v95
v_fma_f32 v3, v3, v78, v105
mav 20,44,36
s_waitcnt lgkmcnt(2)
v_mul_f32_e64 v2, v2, |v2|
v_mul_f32_e64 v3, v3, |v3|
mav 12,48,40
D 86,0
mav 16,52,40
R 24,40960
v_cvt_pk_bf16_f32 v2, v2, v3
mav 20,56,40
v_pk_max_i16 v69, v2, 0
v_fma_f32 v104, v79, v60, v96
mav 12,60,44
R 28,41984
v_fma_f32 v4, v4, v78, v104
mav 16,64,44
D 86,1024
mav 20,68,44
s_waitcnt lgkmcnt(2)
v_fma_f32 v105, v79, v61, v97
v_fma_f32 v5, v5, v78, v105
mav 12,72,48
R 32,43008
v_mul_f32_e64 v4, v4, |v4|
mav 16,76,48
v_mul_f32_e64 v5, v5, |v5|
v_cvt_pk_bf16_f32 v4, v4, v5
mav 20,80,48
D 86,2048
mav 12,84,52
R 36,44032
v_pk_max_i16 v70, v4, 0
mav 16,88,52
v_fma_f32 v104, v79, v62, v98
v_fma_f32 v6, v6, v78, v104
mav 20,92,52
s_waitcnt lgkmcnt(2)
v_fma_f32 v105, v79, v63, v99
v_fma_f32 v7, v7, v78, v105
mav 12,96,24
D 86,3072
mav 16,100,24
R 40,45056
v_mul_f32_e64 v6, v6, |v6|
mav 20,104,24
v_mul_f32_e64 v7, v7, |v7|
v_cvt_pk_bf16_f32 v6, v6, v7
mav 12,108,28
R 44,46080
v_pk_max_i16 v71, v6, 0
mav 16,112,28
buffer_store_dwordx4 v[68:71], v83, s[36:39], s49 offen sc1
v_fma_f32 v104, v79, v64, v100
v_fma_f32 v8, v8, v78, v104
v_fma_f32 v105, v79, v65, v101
mav 20,116,28
s_waitcnt lgkmcnt(2)
v_fma_f32 v9, v9, v78, v105
v_mul_f32_e64 v8, v8, |v8|
mav 12,120,32
R 48,47104
v_mul_f32_e64 v9, v9, |v9|
mav 16,124,32
v_cvt_pk_bf16_f32 v8, v8, v9
v_pk_max_i16 v72, v8, 0
mav 20,128,32
v_pk_max_i16 v74, v8, 0
v_fma_f32 v104, v79, v66, v102
mav 12,132,36
R 52,48128
v_fma_f32 v10, v10, v78, v104
mav 16,136,36
v_fma_f32 v105, v79, v67, v103
v_fma_f32 v11, v11, v78, v105
mav 20,140,36
s_waitcnt lgkmcnt(2)
v_mul_f32_e64 v10, v10, |v10|
v_mul_f32_e64 v11, v11, |v11|
mav 12,144,40
R 24,49152
v_cvt_pk_bf16_f32 v10, v10, v11
mav 16,148,40
v_pk_max_i16 v73, v10, 0
v_pk_max_i16 v75, v10, 0
mav 20,152,40
v_permlane16_swap_b32 v72, v74
mav 12,156,44
R 28,50176
mav 16,160,44
v_permlane16_swap_b32 v73, v75
mav 20,164,44
s_waitcnt lgkmcnt(2)
mav 12,168,48
R 32,51200
mav 16,172,48
mav 20,176,48
buffer_store_dwordx4 v[72:75], v84, s[36:39], s49 offen sc1
mav 12,180,52
R 36,52224
mav 16,184,52
mav 20,188,52
s_waitcnt lgkmcnt(2)
mav 12,192,24
R 40,53248
mav 16,196,24
mav 20,200,24
s_waitcnt vmcnt(10)
v_add_f32 v108, v108, v110
v_add_f32 v109, v109, v111
mav 12,204,28
R 44,54272
mav 16,208,28
v_permlane32_swap_b32 v108, v109
mav 20,212,28
s_waitcnt lgkmcnt(2)
v_add_f32 v110, v108, v109
v_add_f32 v111, v108, v109
mav 12,216,32
R 48,55296
mav 16,220,32
v_permlane16_swap_b32 v110, v111
mav 20,224,32
v_add_f32 v108, v110, v111
v_add_f32 v109, v110, v111
mav 12,228,36
R 52,56320
mav 16,232,36
v_permlane32_swap_b32 v108, v109
mav 20,236,36
s_waitcnt lgkmcnt(2)
v_mul_f32 v110, 0x3a800000, v108
v_mul_f32 v111, 0x3a800000, v109
mav 12,240,40
R 24,57344
v_fma_f32 v111, -v110, v110, v111
mav 16,244,40
v_max_f32 v111, 0, v111
v_add_f32 v111, 0x3727c5ac, v111
mav 20,248,40
v_rsq_f32 v108, v111
mav 12,252,44
R 28,58368
v_mul_f32_e64 v109, -v110, v108
mvv 16,128,44
mvv 20,132,44
s_waitcnt lgkmcnt(2)
mvv 12,136,48
R 32,59392
mvv 16,140,48
mvv 20,144,48
mvv 12,148,52
R 36,60416
mvv 16,152,52
mvv 20,156,52
s_waitcnt lgkmcnt(2)
mvv 12,160,24
mvv 16,164,24
R 40,61440
mvv 20,168,24
mvv 12,172,28
R 44,62464
mvv 16,176,28
v_add_u32 v76, 1, v76
mvv 20,180,28
s_waitcnt lgkmcnt(2)
ds_write_b64 v89, v[108:109] offset:128
mvv 12,184,32
R 48,63488
mvv 16,188,32
mvv 20,192,32
mvv 12,196,36
R 52,64512
v_xor_b32 v80, 0x10000, v80
mvv 16,200,36
s_waitcnt vmcnt(2) lgkmcnt(2)
s_add_u32 s49, s49, 313344
mvv 20,204,36
s_barrier
mvv 12,208,40
buffer_load_dwordx2 v[108:109], v90, s[36:39], s49 offen sc1
mvv 16,212,40
s_add_u32 m0, s50, 32768
R 24,0
mvv 20,216,40
ds_read_b64 v[78:79], v89 offset:128
mvv 12,220,44
R 28,1024
mvv 16,224,44
buffer_load_dwordx2 v[110:111], v90, s[36:39], s49 offen offset:512 sc1
mvv 20,228,44
s_waitcnt lgkmcnt(3)
mvv 12,232,48
R 32,2048
mvv 16,236,48
mvv 20,240,48
D 85,0
mvv 12,244,52
R 36,3072
mvv 16,248,52
mvv 20,252,52
s_waitcnt lgkmcnt(2)
v_mfma_f32_16x16x32_bf16 v[0:3], a[0:3], v[24:27], 0
D 85,1024
v_mfma_f32_16x16x32_bf16 v[4:7], a[4:7], v[24:27], 0
R 40,4096
v_fma_f32 v104, v79, v56, v92
v_mfma_f32_16x16x32_bf16 v[8:11], a[8:11], v[24:27], 0
v_fma_f32 v12, v12, v78, v104
v_fma_f32 v105, v79, v57, v93
mav 0,12,28
R 44,5120
v_fma_f32 v13, v13, v78, v105
mav 4,16,28
s_waitcnt vmcnt(4)
buffer_store_dword v76, v81, s[36:39], s52 offen sc1
mav 8,20,28
s_waitcnt lgkmcnt(2)
v_mul_f32_e64 v12, v12, |v12|
v_mul_f32_e64 v13, v13, |v13|
mav 0,24,32
R 48,6144
v_cvt_pk_bf16_f32 v12, v12, v13
mav 4,28,32
v_pk_max_i16 v68, v12, 0
v_fma_f32 v104, v79, v58, v94
mav 8,32,32
D 85,2048
mav 0,36,36
R 52,7168
v_fma_f32 v14, v14, v78, v104
mav 4,40,36
v_fma_f32 v105, v79, v59, v95
v_fma_f32 v15, v15, v78, v105
mav 8,44,36
s_waitcnt lgkmcnt(2)
v_mul_f32_e64 v14, v14, |v14|
v_mul_f32_e64 v15, v15, |v15|
mav 0,48,40
D 85,3072
mav 4,52,40
s_add_u32 m0, s50, 36864
R 24,8192
v_cvt_pk_bf16_f32 v14, v14, v15
mav 8,56,40
v_pk_max_i16 v69, v14, 0
v_fma_f32 v104, v79, v60, v96
mav 0,60,44
R 28,9216
v_fma_f32 v16, v16, v78, v104
mav 4,64,44
D 86,0
mav 8,68,44
s_waitcnt lgkmcnt(2)
v_fma_f32 v105, v79, v61, v97
v_fma_f32 v17, v17, v78, v105
mav 0,72,48
R 32,10240
v_mul_f32_e64 v16, v16, |v16|
mav 4,76,48
v_mul_f32_e64 v17, v17, |v17|
v_cvt_pk_bf16_f32 v16, v16, v17
mav 8,80,48
D 86,1024
mav 0,84,52
R 36,11264
v_pk_max_i16 v70, v16, 0
mav 4,88,52
v_fma_f32 v104, v79, v62, v98
v_fma_f32 v18, v18, v78, v104
mav 8,92,52
s_waitcnt lgkmcnt(2)
v_fma_f32 v105, v79, v63, v99
v_fma_f32 v19, v19, v78, v105
mav 0,96,24
D 86,2048
mav 4,100,24
R 40,12288
LsF6:
s_cbranch_execz LsF10
v_mul_f32_e64 v18, v18, |v18|
mav 8,104,24
v_mul_f32_e64 v19, v19, |v19|
v_cvt_pk_bf16_f32 v18, v18, v19
mav 0,108,28
R 44,13312
v_pk_max_i16 v71, v18, 0
mav 4,112,28
buffer_store_dwordx4 v[68:71], v83, s[36:39], s49 offen sc1
D 86,3072
mav 8,116,28
s_waitcnt lgkmcnt(2)
v_fma_f32 v104, v79, v64, v100
v_fma_f32 v20, v20, v78, v104
mav 0,120,32
R 48,14336
v_fma_f32 v105, v79, v65, v101
mav 4,124,32
v_fma_f32 v21, v21, v78, v105
v_mul_f32_e64 v20, v20, |v20|
mav 8,128,32
v_mul_f32_e64 v21, v21, |v21|
v_cvt_pk_bf16_f32 v20, v20, v21
mav 0,132,36
R 52,15360
v_pk_max_i16 v72, v20, 0
mav 4,136,36
v_pk_max_i16 v74, v20, 0
v_fma_f32 v104, v79, v66, v102
mav 8,140,36
s_waitcnt lgkmcnt(2)
v_fma_f32 v22, v22, v78, v104
v_fma_f32 v105, v79, v67, v103
mav 0,144,40
R 24,16384
v_fma_f32 v23, v23, v78, v105
mav 4,148,40
v_mul_f32_e64 v22, v22, |v22|
v_mul_f32_e64 v23, v23, |v23|
mav 8,152,40
v_cvt_pk_bf16_f32 v22, v22, v23
v_pk_max_i16 v73, v22, 0
mav 0,156,44
R 28,17408
v_pk_max_i16 v75, v22, 0
mav 4,160,44
v_permlane16_swap_b32 v72, v74
mav 8,164,44
s_waitcnt lgkmcnt(2)
v_permlane16_swap_b32 v73, v75
mav 0,168,48
R 32,18432
mav 4,172,48
mav 8,176,48
buffer_store_dwordx4 v[72:75], v84, s[36:39], s49 offen sc1
mav 0,180,52
R 36,19456
mav 4,184,52
mav 8,188,52
s_waitcnt lgkmcnt(2)
mav 0,192,24
R 40,20480
mav 4,196,24
mav 8,200,24
s_waitcnt vmcnt(11)
v_add_f32 v108, v108, v110
v_add_f32 v109, v109, v111
mav 0,204,28
R 44,21504
mav 4,208,28
v_permlane32_swap_b32 v108, v109
mav 8,212,28
s_waitcnt lgkmcnt(2)
v_add_f32 v110, v108, v109
v_add_f32 v111, v108, v109
mav 0,216,32
R 48,22528
mav 4,220,32
v_permlane16_swap_b32 v110, v111
mav 8,224,32
v_add_f32 v108, v110, v111
v_add_f32 v109, v110, v111
mav 0,228,36
R 52,23552
mav 4,232,36
v_permlane32_swap_b32 v108, v109
mav 8,236,36
s_waitcnt lgkmcnt(2)
v_mul_f32 v110, 0x3a800000, v108
v_mul_f32 v111, 0x3a800000, v109
mav 0,240,40
R 24,24576
v_fma_f32 v111, -v110, v110, v111
mav 4,244,40
v_max_f32 v111, 0, v111
v_add_f32 v111, 0x3727c5ac, v111
mav 8,248,40
v_rsq_f32 v108, v111
mav 0,252,44
R 28,25600
v_mul_f32_e64 v109, -v110, v108
mvv 4,128,44
mvv 8,132,44
s_waitcnt lgkmcnt(2)
mvv 0,136,48
R 32,26624
mvv 4,140,48
mvv 8,144,48
mvv 0,148,52
R 36,27648
mvv 4,152,52
mvv 8,156,52
s_waitcnt lgkmcnt(2)
mvv 0,160,24
R 40,28672
mvv 4,164,24
mvv 8,168,24
mvv 0,172,28
R 44,29696
mvv 4,176,28
v_add_u32 v76, 1, v76
mvv 8,180,28
s_waitcnt lgkmcnt(2)
ds_write_b64 v89, v[108:109] offset:0
mvv 0,184,32
s_xor_b32 s50, s50, 0x10000
R 48,30720
mvv 4,188,32
mvv 8,192,32
mvv 0,196,36
R 52,31744
mvv 4,200,36
s_waitcnt vmcnt(1) lgkmcnt(2)
s_add_u32 s49, s49, 313344
mvv 8,204,36
s_barrier
mvv 0,208,40
buffer_load_dwordx2 v[108:109], v90, s[36:39], s49 offen sc1
mvv 4,212,40
s_add_u32 m0, s50, 0
R 24,32768
mvv 8,216,40
ds_read_b64 v[78:79], v89 offset:0
mvv 0,220,44
R 28,33792
mvv 4,224,44
buffer_load_dwordx2 v[110:111], v90, s[36:39], s49 offen offset:512 sc1
mvv 8,228,44
s_waitcnt lgkmcnt(3)
mvv 0,232,48
R 32,34816
mvv 4,236,48
mvv 8,240,48
D 85,0
mvv 0,244,52
R 36,35840
mvv 4,248,52
mvv 8,252,52
s_waitcnt lgkmcnt(2)
v_mfma_f32_16x16x32_bf16 v[12:15], a[0:3], v[24:27], 0
D 85,1024
v_mfma_f32_16x16x32_bf16 v[16:19], a[4:7], v[24:27], 0
R 40,36864
v_fma_f32 v104, v79, v56, v92
v_mfma_f32_16x16x32_bf16 v[20:23], a[8:11], v[24:27], 0
v_fma_f32 v0, v0, v78, v104
v_fma_f32 v105, v79, v57, v93
mav 12,12,28
R 44,37888
v_fma_f32 v1, v1, v78, v105
mav 16,16,28
s_waitcnt vmcnt(4)
buffer_store_dword v76, v81, s[36:39], s52 offen sc1
mav 20,20,28
s_waitcnt lgkmcnt(2)
v_mul_f32_e64 v0, v0, |v0|
v_mul_f32_e64 v1, v1, |v1|
mav 12,24,32
R 48,38912
v_cvt_pk_bf16_f32 v0, v0, v1
mav 16,28,32
v_pk_max_i16 v68, v0, 0
v_fma_f32 v104, v79, v58, v94
mav 20,32,32
D 85,2048
mav 12,36,36
R 52,39936
v_fma_f32 v2, v2, v78, v104
mav 16,40,36
v_fma_f32 v105, v79, v59, v95
v_fma_f32 v3, v3, v78, v105
mav 20,44,36
s_waitcnt lgkmcnt(2)
v_mul_f32_e64 v2, v2, |v2|
v_mul_f32_e64 v3, v3, |v3|
mav 12,48,40
D 85,3072
mav 16,52,40
s_add_u32 m0, s50, 4096
R 24,40960
v_cvt_pk_bf16_f32 v2, v2, v3
mav 20,56,40
v_pk_max_i16 v69, v2, 0
v_fma_f32 v104, v79, v60, v96
mav 12,60,44
R 28,41984
v_fma_f32 v4, v4, v78, v104
mav 16,64,44
D 86,0
mav 20,68,44
s_waitcnt lgkmcnt(2)
v_fma_f32 v105, v79, v61, v97
v_fma_f32 v5, v5, v78, v105
mav 12,72,48
R 32,43008
v_mul_f32_e64 v4, v4, |v4|
mav 16,76,48
v_mul_f32_e64 v5, v5, |v5|
v_cvt_pk_bf16_f32 v4, v4, v5
mav 20,80,48
D 86,1024
mav 12,84,52
R 36,44032
v_pk_max_i16 v70, v4, 0
mav 16,88,52
v_fma_f32 v104, v79, v62, v98
v_fma_f32 v6, v6, v78, v104
mav 20,92,52
s_waitcnt lgkmcnt(2)
v_fma_f32 v105, v79, v63, v99
v_fma_f32 v7, v7, v78, v105
mav 12,96,24
D 86,2048
mav 16,100,24
R 40,45056
v_mul_f32_e64 v6, v6, |v6|
mav 20,104,24
v_mul_f32_e64 v7, v7, |v7|
v_cvt_pk_bf16_f32 v6, v6, v7
mav 12,108,28
R 44,46080
v_pk_max_i16 v71, v6, 0
mav 16,112,28
buffer_store_dwordx4 v[68:71], v83, s[36:39], s49 offen sc1
D 86,3072
mav 20,116,28
s_waitcnt lgkmcnt(2)
v_fma_f32 v104, v79, v64, v100
v_fma_f32 v8, v8, v78, v104
mav 12,120,32
R 48,47104
v_fma_f32 v105, v79, v65, v101
mav 16,124,32
v_fma_f32 v9, v9, v78, v105
v_mul_f32_e64 v8, v8, |v8|
mav 20,128,32
v_mul_f32_e64 v9, v9, |v9|
v_cvt_pk_bf16_f32 v8, v8, v9
mav 12,132,36
R 52,48128
v_pk_max_i16 v72, v8, 0
mav 16,136,36
v_pk_max_i16 v74, v8, 0
v_fma_f32 v104, v79, v66, v102
mav 20,140,36
s_waitcnt lgkmcnt(2)
v_fma_f32 v10, v10, v78, v104
v_fma_f32 v105, v79, v67, v103
mav 12,144,40
R 24,49152
v_fma_f32 v11, v11, v78, v105
mav 16,148,40
v_mul_f32_e64 v10, v10, |v10|
v_mul_f32_e64 v11, v11, |v11|
mav 20,152,40
v_cvt_pk_bf16_f32 v10, v10, v11
v_pk_max_i16 v73, v10, 0
mav 12,156,44
R 28,50176
v_pk_max_i16 v75, v10, 0
mav 16,160,44
v_permlane16_swap_b32 v72, v74
mav 20,164,44
s_waitcnt lgkmcnt(2)
v_permlane16_swap_b32 v73, v75
mav 12,168,48
R 32,51200
mav 16,172,48
mav 20,176,48
buffer_store_dwordx4 v[72:75], v84, s[36:39], s49 offen sc1
mav 12,180,52
R 36,52224
mav 16,184,52
mav 20,188,52
s_waitcnt lgkmcnt(2)
mav 12,192,24
R 40,53248
mav 16,196,24
mav 20,200,24
s_waitcnt vmcnt(11)
v_add_f32 v108, v108, v110
v_add_f32 v109, v109, v111
mav 12,204,28
R 44,54272
mav 16,208,28
v_permlane32_swap_b32 v108, v109
mav 20,212,28
s_waitcnt lgkmcnt(2)
v_add_f32 v110, v108, v109
v_add_f32 v111, v108, v109
mav 12,216,32
R 48,55296
mav 16,220,32
v_permlane16_swap_b32 v110, v111
mav 20,224,32
v_add_f32 v108, v110, v111
v_add_f32 v109, v110, v111
mav 12,228,36
R 52,56320
mav 16,232,36
v_permlane32_swap_b32 v108, v109
mav 20,236,36
s_waitcnt lgkmcnt(2)
v_mul_f32 v110, 0x3a800000, v108
v_mul_f32 v111, 0x3a800000, v109
mav 12,240,40
R 24,57344
v_fma_f32 v111, -v110, v110, v111
mav 16,244,40
v_max_f32 v111, 0, v111
v_add_f32 v111, 0x3727c5ac, v111
mav 20,248,40
v_rsq_f32 v108, v111
mav 12,252,44
R 28,58368
v_mul_f32_e64 v109, -v110, v108
mvv 16,128,44
mvv 20,132,44
s_waitcnt lgkmcnt(2)
mvv 12,136,48
R 32,59392
mvv 16,140,48
mvv 20,144,48
mvv 12,148,52
R 36,60416
mvv 16,152,52
mvv 20,156,52
s_waitcnt lgkmcnt(2)
mvv 12,160,24
s_mov_b32 s60, -29
.p2align 6
LF1_1:
mvv 16,164,24
R 40,61440
mvv 20,168,24
mvv 12,172,28
R 44,62464
mvv 16,176,28
v_add_u32 v76, 1, v76
mvv 20,180,28
s_waitcnt lgkmcnt(2)
ds_write_b64 v89, v[108:109] offset:128
mvv 12,184,32
R 48,63488
mvv 16,188,32
mvv 20,192,32
mvv 12,196,36
R 52,64512
v_xor_b32 v80, 0x10000, v80
mvv 16,200,36
s_waitcnt vmcnt(1) lgkmcnt(2)
s_add_u32 s49, s49, 313344
mvv 20,204,36
s_barrier
mvv 12,208,40
buffer_load_dwordx2 v[108:109], v90, s[36:39], s49 offen sc1
mvv 16,212,40
s_add_u32 m0, s50, 32768
R 24,0
mvv 20,216,40
ds_read_b64 v[78:79], v89 offset:128
mvv 12,220,44
R 28,1024
mvv 16,224,44
buffer_load_dwordx2 v[110:111], v90, s[36:39], s49 offen offset:512 sc1
mvv 20,228,44
s_waitcnt lgkmcnt(3)
mvv 12,232,48
R 32,2048
mvv 16,236,48
mvv 20,240,48
D 85,0
mvv 12,244,52
R 36,3072
mvv 16,248,52
mvv 20,252,52
s_waitcnt lgkmcnt(2)
v_mfma_f32_16x16x32_bf16 v[0:3], a[0:3], v[24:27], 0
D 85,1024
v_mfma_f32_16x16x32_bf16 v[4:7], a[4:7], v[24:27], 0
R 40,4096
v_fma_f32 v104, v79, v56, v92
v_mfma_f32_16x16x32_bf16 v[8:11], a[8:11], v[24:27], 0
v_fma_f32 v12, v12, v78, v104
v_fma_f32 v105, v79, v57, v93
mav 0,12,28
R 44,5120
v_fma_f32 v13, v13, v78, v105
mav 4,16,28
s_waitcnt vmcnt(4)
buffer_store_dword v76, v81, s[36:39], s52 offen sc1
mav 8,20,28
s_waitcnt lgkmcnt(2)
v_mul_f32_e64 v12, v12, |v12|
v_mul_f32_e64 v13, v13, |v13|
mav 0,24,32
R 48,6144
v_cvt_pk_bf16_f32 v12, v12, v13
mav 4,28,32
v_pk_max_i16 v68, v12, 0
v_fma_f32 v104, v79, v58, v94
mav 8,32,32
D 85,2048
mav 0,36,36
R 52,7168
v_fma_f32 v14, v14, v78, v104
mav 4,40,36
v_fma_f32 v105, v79, v59, v95
v_fma_f32 v15, v15, v78, v105
mav 8,44,36
s_waitcnt lgkmcnt(2)
v_mul_f32_e64 v14, v14, |v14|
v_mul_f32_e64 v15, v15, |v15|
mav 0,48,40
D 85,3072
mav 4,52,40
s_add_u32 m0, s50, 36864
R 24,8192
v_cvt_pk_bf16_f32 v14, v14, v15
mav 8,56,40
v_pk_max_i16 v69, v14, 0
v_fma_f32 v104, v79, v60, v96
mav 0,60,44
R 28,9216
v_fma_f32 v16, v16, v78, v104
mav 4,64,44
D 86,0
mav 8,68,44
s_waitcnt lgkmcnt(2)
v_fma_f32 v105, v79, v61, v97
v_fma_f32 v17, v17, v78, v105
mav 0,72,48
R 32,10240
v_mul_f32_e64 v16, v16, |v16|
mav 4,76,48
v_mul_f32_e64 v17, v17, |v17|
v_cvt_pk_bf16_f32 v16, v16, v17
mav 8,80,48
D 86,1024
mav 0,84,52
R 36,11264
v_pk_max_i16 v70, v16, 0
mav 4,88,52
v_fma_f32 v104, v79, v62, v98
v_fma_f32 v18, v18, v78, v104
mav 8,92,52
s_waitcnt lgkmcnt(2)
v_fma_f32 v105, v79, v63, v99
v_fma_f32 v19, v19, v78, v105
mav 0,96,24
D 86,2048
mav 4,100,24
R 40,12288
v_mul_f32_e64 v18, v18, |v18|
mav 8,104,24
v_mul_f32_e64 v19, v19, |v19|
v_cvt_pk_bf16_f32 v18, v18, v19
mav 0,108,28
R 44,13312
v_pk_max_i16 v71, v18, 0
mav 4,112,28
buffer_store_dwordx4 v[68:71], v83, s[36:39], s49 offen sc1
)"
R"(
D 86,3072
mav 8,116,28
s_waitcnt lgkmcnt(2)
v_fma_f32 v104, v79, v64, v100
v_fma_f32 v20, v20, v78, v104
mav 0,120,32
R 48,14336
v_fma_f32 v105, v79, v65, v101
mav 4,124,32
v_fma_f32 v21, v21, v78, v105
v_mul_f32_e64 v20, v20, |v20|
mav 8,128,32
v_mul_f32_e64 v21, v21, |v21|
v_cvt_pk_bf16_f32 v20, v20, v21
mav 0,132,36
R 52,15360
v_pk_max_i16 v72, v20, 0
mav 4,136,36
v_pk_max_i16 v74, v20, 0
v_fma_f32 v104, v79, v66, v102
mav 8,140,36
s_waitcnt lgkmcnt(2)
v_fma_f32 v22, v22, v78, v104
v_fma_f32 v105, v79, v67, v103
mav 0,144,40
R 24,16384
v_fma_f32 v23, v23, v78, v105
mav 4,148,40
v_mul_f32_e64 v22, v22, |v22|
v_mul_f32_e64 v23, v23, |v23|
mav 8,152,40
v_cvt_pk_bf16_f32 v22, v22, v23
v_pk_max_i16 v73, v22, 0
mav 0,156,44
R 28,17408
v_pk_max_i16 v75, v22, 0
mav 4,160,44
v_permlane16_swap_b32 v72, v74
mav 8,164,44
s_waitcnt lgkmcnt(2)
v_permlane16_swap_b32 v73, v75
mav 0,168,48
R 32,18432
mav 4,172,48
mav 8,176,48
buffer_store_dwordx4 v[72:75], v84, s[36:39], s49 offen sc1
mav 0,180,52
R 36,19456
mav 4,184,52
mav 8,188,52
s_waitcnt lgkmcnt(2)
mav 0,192,24
R 40,20480
mav 4,196,24
mav 8,200,24
s_waitcnt vmcnt(11)
v_add_f32 v108, v108, v110
v_add_f32 v109, v109, v111
mav 0,204,28
R 44,21504
mav 4,208,28
v_permlane32_swap_b32 v108, v109
mav 8,212,28
s_waitcnt lgkmcnt(2)
v_add_f32 v110, v108, v109
v_add_f32 v111, v108, v109
LsF7:
s_cbranch_execz LsF11
mav 0,216,32
R 48,22528
mav 4,220,32
v_permlane16_swap_b32 v110, v111
mav 8,224,32
v_add_f32 v108, v110, v111
v_add_f32 v109, v110, v111
mav 0,228,36
R 52,23552
mav 4,232,36
v_permlane32_swap_b32 v108, v109
mav 8,236,36
s_waitcnt lgkmcnt(2)
v_mul_f32 v110, 0x3a800000, v108
v_mul_f32 v111, 0x3a800000, v109
mav 0,240,40
R 24,24576
v_fma_f32 v111, -v110, v110, v111
mav 4,244,40
v_max_f32 v111, 0, v111
v_add_f32 v111, 0x3727c5ac, v111
mav 8,248,40
v_rsq_f32 v108, v111
mav 0,252,44
R 28,25600
v_mul_f32_e64 v109, -v110, v108
mvv 4,128,44
mvv 8,132,44
s_waitcnt lgkmcnt(2)
mvv 0,136,48
R 32,26624
mvv 4,140,48
mvv 8,144,48
mvv 0,148,52
R 36,27648
mvv 4,152,52
mvv 8,156,52
s_waitcnt lgkmcnt(2)
mvv 0,160,24
R 40,28672
mvv 4,164,24
mvv 8,168,24
mvv 0,172,28
R 44,29696
mvv 4,176,28
v_add_u32 v76, 1, v76
mvv 8,180,28
s_waitcnt lgkmcnt(2)
ds_write_b64 v89, v[108:109] offset:0
mvv 0,184,32
s_xor_b32 s50, s50, 0x10000
R 48,30720
mvv 4,188,32
mvv 8,192,32
mvv 0,196,36
R 52,31744
mvv 4,200,36
s_waitcnt vmcnt(1) lgkmcnt(2)
s_add_u32 s49, s49, 313344
mvv 8,204,36
s_barrier
mvv 0,208,40
buffer_load_dwordx2 v[108:109], v90, s[36:39], s49 offen sc1
mvv 4,212,40
s_add_u32 m0, s50, 0
R 24,32768
mvv 8,216,40
ds_read_b64 v[78:79], v89 offset:0
mvv 0,220,44
R 28,33792
mvv 4,224,44
buffer_load_dwordx2 v[110:111], v90, s[36:39], s49 offen offset:512 sc1
mvv 8,228,44
s_waitcnt lgkmcnt(3)
mvv 0,232,48
R 32,34816
mvv 4,236,48
mvv 8,240,48
D 85,0
mvv 0,244,52
R 36,35840
mvv 4,248,52
mvv 8,252,52
s_waitcnt lgkmcnt(2)
v_mfma_f32_16x16x32_bf16 v[12:15], a[0:3], v[24:27], 0
D 85,1024
v_mfma_f32_16x16x32_bf16 v[16:19], a[4:7], v[24:27], 0
R 40,36864
v_fma_f32 v104, v79, v56, v92
v_mfma_f32_16x16x32_bf16 v[20:23], a[8:11], v[24:27], 0
v_fma_f32 v0, v0, v78, v104
v_fma_f32 v105, v79, v57, v93
mav 12,12,28
R 44,37888
v_fma_f32 v1, v1, v78, v105
mav 16,16,28
s_waitcnt vmcnt(4)
buffer_store_dword v76, v81, s[36:39], s52 offen sc1
mav 20,20,28
s_waitcnt lgkmcnt(2)
v_mul_f32_e64 v0, v0, |v0|
v_mul_f32_e64 v1, v1, |v1|
mav 12,24,32
R 48,38912
v_cvt_pk_bf16_f32 v0, v0, v1
mav 16,28,32
v_pk_max_i16 v68, v0, 0
v_fma_f32 v104, v79, v58, v94
mav 20,32,32
D 85,2048
mav 12,36,36
R 52,39936
v_fma_f32 v2, v2, v78, v104
mav 16,40,36
v_fma_f32 v105, v79, v59, v95
v_fma_f32 v3, v3, v78, v105
mav 20,44,36
s_waitcnt lgkmcnt(2)
v_mul_f32_e64 v2, v2, |v2|
v_mul_f32_e64 v3, v3, |v3|
mav 12,48,40
D 85,3072
mav 16,52,40
s_add_u32 m0, s50, 4096
R 24,40960
v_cvt_pk_bf16_f32 v2, v2, v3
mav 20,56,40
v_pk_max_i16 v69, v2, 0
v_fma_f32 v104, v79, v60, v96
mav 12,60,44
R 28,41984
v_fma_f32 v4, v4, v78, v104
mav 16,64,44
D 86,0
mav 20,68,44
s_waitcnt lgkmcnt(2)
v_fma_f32 v105, v79, v61, v97
v_fma_f32 v5, v5, v78, v105
mav 12,72,48
R 32,43008
v_mul_f32_e64 v4, v4, |v4|
mav 16,76,48
v_mul_f32_e64 v5, v5, |v5|
v_cvt_pk_bf16_f32 v4, v4, v5
mav 20,80,48
D 86,1024
mav 12,84,52
R 36,44032
v_pk_max_i16 v70, v4, 0
mav 16,88,52
v_fma_f32 v104, v79, v62, v98
v_fma_f32 v6, v6, v78, v104
mav 20,92,52
s_waitcnt lgkmcnt(2)
v_fma_f32 v105, v79, v63, v99
v_fma_f32 v7, v7, v78, v105
mav 12,96,24
D 86,2048
mav 16,100,24
R 40,45056
v_mul_f32_e64 v6, v6, |v6|
mav 20,104,24
v_mul_f32_e64 v7, v7, |v7|
v_cvt_pk_bf16_f32 v6, v6, v7
mav 12,108,28
R 44,46080
v_pk_max_i16 v71, v6, 0
mav 16,112,28
buffer_store_dwordx4 v[68:71], v83, s[36:39], s49 offen sc1
D 86,3072
mav 20,116,28
s_waitcnt lgkmcnt(2)
v_fma_f32 v104, v79, v64, v100
v_fma_f32 v8, v8, v78, v104
mav 12,120,32
R 48,47104
v_fma_f32 v105, v79, v65, v101
mav 16,124,32
v_fma_f32 v9, v9, v78, v105
v_mul_f32_e64 v8, v8, |v8|
mav 20,128,32
v_mul_f32_e64 v9, v9, |v9|
v_cvt_pk_bf16_f32 v8, v8, v9
mav 12,132,36
R 52,48128
v_pk_max_i16 v72, v8, 0
mav 16,136,36
v_pk_max_i16 v74, v8, 0
v_fma_f32 v104, v79, v66, v102
mav 20,140,36
s_waitcnt lgkmcnt(2)
v_fma_f32 v10, v10, v78, v104
v_fma_f32 v105, v79, v67, v103
mav 12,144,40
R 24,49152
v_fma_f32 v11, v11, v78, v105
mav 16,148,40
v_mul_f32_e64 v10, v10, |v10|
v_mul_f32_e64 v11, v11, |v11|
mav 20,152,40
v_cvt_pk_bf16_f32 v10, v10, v11
v_pk_max_i16 v73, v10, 0
mav 12,156,44
R 28,50176
v_pk_max_i16 v75, v10, 0
mav 16,160,44
v_permlane16_swap_b32 v72, v74
mav 20,164,44
s_waitcnt lgkmcnt(2)
v_permlane16_swap_b32 v73, v75
mav 12,168,48
R 32,51200
mav 16,172,48
mav 20,176,48
buffer_store_dwordx4 v[72:75], v84, s[36:39], s49 offen sc1
mav 12,180,52
R 36,52224
mav 16,184,52
mav 20,188,52
s_waitcnt lgkmcnt(2)
mav 12,192,24
R 40,53248
mav 16,196,24
mav 20,200,24
s_waitcnt vmcnt(11)
v_add_f32 v108, v108, v110
v_add_f32 v109, v109, v111
mav 12,204,28
R 44,54272
mav 16,208,28
v_permlane32_swap_b32 v108, v109
mav 20,212,28
s_waitcnt lgkmcnt(2)
v_add_f32 v110, v108, v109
v_add_f32 v111, v108, v109
mav 12,216,32
R 48,55296
mav 16,220,32
v_permlane16_swap_b32 v110, v111
mav 20,224,32
v_add_f32 v108, v110, v111
v_add_f32 v109, v110, v111
mav 12,228,36
R 52,56320
mav 16,232,36
v_permlane32_swap_b32 v108, v109
mav 20,236,36
s_waitcnt lgkmcnt(2)
v_mul_f32 v110, 0x3a800000, v108
v_mul_f32 v111, 0x3a800000, v109
mav 12,240,40
R 24,57344
v_fma_f32 v111, -v110, v110, v111
mav 16,244,40
v_max_f32 v111, 0, v111
v_add_f32 v111, 0x3727c5ac, v111
mav 20,248,40
v_rsq_f32 v108, v111
mav 12,252,44
R 28,58368
v_mul_f32_e64 v109, -v110, v108
mvv 16,128,44
mvv 20,132,44
s_waitcnt lgkmcnt(2)
mvv 12,136,48
R 32,59392
mvv 16,140,48
mvv 20,144,48
mvv 12,148,52
R 36,60416
mvv 16,152,52
mvv 20,156,52
s_waitcnt lgkmcnt(2)
s_add_u32 s60, s60, 1
mvv 12,160,24
s_cbranch_scc0 LF1_1
mvv 16,164,24
R 40,61440
mvv 20,168,24
mvv 12,172,28
R 44,62464
mvv 16,176,28
v_add_u32 v76, 1, v76
mvv 20,180,28
s_waitcnt lgkmcnt(2)
ds_write_b64 v89, v[108:109] offset:128
mvv 12,184,32
R 48,63488
mvv 16,188,32
mvv 20,192,32
mvv 12,196,36
R 52,64512
v_xor_b32 v80, 0x10000, v80
mvv 16,200,36
s_waitcnt vmcnt(1) lgkmcnt(2)
s_add_u32 s49, s49, 313344
mvv 20,204,36
s_barrier
mvv 12,208,40
buffer_load_dwordx2 v[108:109], v90, s[36:39], s49 offen sc1
mvv 16,212,40
s_add_u32 m0, s50, 32768
R 24,0
mvv 20,216,40
ds_read_b64 v[78:79], v89 offset:128
mvv 12,220,44
R 28,1024
mvv 16,224,44
buffer_load_dwordx2 v[110:111], v90, s[36:39], s49 offen offset:512 sc1
mvv 20,228,44
s_waitcnt lgkmcnt(3)
mvv 12,232,48
R 32,2048
mvv 16,236,48
mvv 20,240,48
D 85,0
mvv 12,244,52
R 36,3072
mvv 16,248,52
mvv 20,252,52
s_waitcnt lgkmcnt(2)
v_mfma_f32_16x16x32_bf16 v[0:3], a[0:3], v[24:27], 0
D 85,1024
v_mfma_f32_16x16x32_bf16 v[4:7], a[4:7], v[24:27], 0
R 40,4096
v_fma_f32 v104, v79, v56, v92
v_mfma_f32_16x16x32_bf16 v[8:11], a[8:11], v[24:27], 0
v_fma_f32 v12, v12, v78, v104
v_fma_f32 v105, v79, v57, v93
mav 0,12,28
R 44,5120
v_fma_f32 v13, v13, v78, v105
mav 4,16,28
s_waitcnt vmcnt(4)
buffer_store_dword v76, v81, s[36:39], s52 offen sc1
mav 8,20,28
s_waitcnt lgkmcnt(2)
v_mul_f32_e64 v12, v12, |v12|
v_mul_f32_e64 v13, v13, |v13|
mav 0,24,32
R 48,6144
v_cvt_pk_bf16_f32 v12, v12, v13
mav 4,28,32
v_pk_max_i16 v68, v12, 0
v_fma_f32 v104, v79, v58, v94
mav 8,32,32
D 85,2048
mav 0,36,36
R 52,7168
v_fma_f32 v14, v14, v78, v104
mav 4,40,36
v_fma_f32 v105, v79, v59, v95
v_fma_f32 v15, v15, v78, v105
mav 8,44,36
s_waitcnt lgkmcnt(2)
v_mul_f32_e64 v14, v14, |v14|
v_mul_f32_e64 v15, v15, |v15|
mav 0,48,40
D 85,3072
mav 4,52,40
s_add_u32 m0, s50, 36864
R 24,8192
v_cvt_pk_bf16_f32 v14, v14, v15
mav 8,56,40
v_pk_max_i16 v69, v14, 0
v_fma_f32 v104, v79, v60, v96
mav 0,60,44
R 28,9216
v_fma_f32 v16, v16, v78, v104
mav 4,64,44
D 86,0
mav 8,68,44
s_waitcnt lgkmcnt(2)
v_fma_f32 v105, v79, v61, v97
v_fma_f32 v17, v17, v78, v105
mav 0,72,48
R 32,10240
v_mul_f32_e64 v16, v16, |v16|
mav 4,76,48
v_mul_f32_e64 v17, v17, |v17|
v_cvt_pk_bf16_f32 v16, v16, v17
mav 8,80,48
D 86,1024
mav 0,84,52
R 36,11264
v_pk_max_i16 v70, v16, 0
mav 4,88,52
v_fma_f32 v104, v79, v62, v98
v_fma_f32 v18, v18, v78, v104
mav 8,92,52
s_waitcnt lgkmcnt(2)
v_fma_f32 v105, v79, v63, v99
v_fma_f32 v19, v19, v78, v105
mav 0,96,24
D 86,2048
mav 4,100,24
R 40,12288
v_mul_f32_e64 v18, v18, |v18|
mav 8,104,24
v_mul_f32_e64 v19, v19, |v19|
v_cvt_pk_bf16_f32 v18, v18, v19
mav 0,108,28
R 44,13312
v_pk_max_i16 v71, v18, 0
mav 4,112,28
buffer_store_dwordx4 v[68:71], v83, s[36:39], s49 offen sc1
D 86,3072
mav 8,116,28
s_waitcnt lgkmcnt(2)
v_fma_f32 v104, v79, v64, v100
v_fma_f32 v20, v20, v78, v104
mav 0,120,32
R 48,14336
v_fma_f32 v105, v79, v65, v101
mav 4,124,32
v_fma_f32 v21, v21, v78, v105
v_mul_f32_e64 v20, v20, |v20|
mav 8,128,32
v_mul_f32_e64 v21, v21, |v21|
v_cvt_pk_bf16_f32 v20, v20, v21
mav 0,132,36
R 52,15360
v_pk_max_i16 v72, v20, 0
mav 4,136,36
v_pk_max_i16 v74, v20, 0
v_fma_f32 v104, v79, v66, v102
mav 8,140,36
s_waitcnt lgkmcnt(2)
v_fma_f32 v22, v22, v78, v104
v_fma_f32 v105, v79, v67, v103
mav 0,144,40
R 24,16384
v_fma_f32 v23, v23, v78, v105
mav 4,148,40
v_mul_f32_e64 v22, v22, |v22|
v_mul_f32_e64 v23, v23, |v23|
mav 8,152,40
v_cvt_pk_bf16_f32 v22, v22, v23
v_pk_max_i16 v73, v22, 0
mav 0,156,44
R 28,17408
v_pk_max_i16 v75, v22, 0
mav 4,160,44
v_permlane16_swap_b32 v72, v74
mav 8,164,44
s_waitcnt lgkmcnt(2)
v_permlane16_swap_b32 v73, v75
mav 0,168,48
R 32,18432
mav 4,172,48
mav 8,176,48
buffer_store_dwordx4 v[72:75], v84, s[36:39], s49 offen sc1
mav 0,180,52
R 36,19456
mav 4,184,52
mav 8,188,52
s_waitcnt lgkmcnt(2)
mav 0,192,24
R 40,20480
mav 4,196,24
mav 8,200,24
s_waitcnt vmcnt(11)
v_add_f32 v108, v108, v110
v_add_f32 v109, v109, v111
mav 0,204,28
R 44,21504
mav 4,208,28
v_permlane32_swap_b32 v108, v109
mav 8,212,28
s_waitcnt lgkmcnt(2)
v_add_f32 v110, v108, v109
v_add_f32 v111, v108, v109
mav 0,216,32
R 48,22528
mav 4,220,32
v_permlane16_swap_b32 v110, v111
mav 8,224,32
v_add_f32 v108, v110, v111
v_add_f32 v109, v110, v111
mav 0,228,36
R 52,23552
mav 4,232,36
v_permlane32_swap_b32 v108, v109
mav 8,236,36
s_waitcnt lgkmcnt(2)
v_mul_f32 v110, 0x3a800000, v108
v_mul_f32 v111, 0x3a800000, v109
mav 0,240,40
R 24,24576
v_fma_f32 v111, -v110, v110, v111
mav 4,244,40
v_max_f32 v111, 0, v111
v_add_f32 v111, 0x3727c5ac, v111
mav 8,248,40
v_rsq_f32 v108, v111
mav 0,252,44
R 28,25600
v_mul_f32_e64 v109, -v110, v108
mvv 4,128,44
mvv 8,132,44
s_waitcnt lgkmcnt(2)
mvv 0,136,48
R 32,26624
mvv 4,140,48
mvv 8,144,48
mvv 0,148,52
R 36,27648
mvv 4,152,52
mvv 8,156,52
s_waitcnt lgkmcnt(2)
mvv 0,160,24
R 40,28672
mvv 4,164,24
mvv 8,168,24
mvv 0,172,28
R 44,29696
mvv 4,176,28
v_add_u32 v76, 1, v76
mvv 8,180,28
s_waitcnt lgkmcnt(2)
ds_write_b64 v89, v[108:109] offset:0
mvv 0,184,32
R 48,30720
mvv 4,188,32
mvv 8,192,32
mvv 0,196,36
R 52,31744
mvv 4,200,36
s_waitcnt vmcnt(1) lgkmcnt(2)
s_add_u32 s49, s49, 313344
mvv 8,204,36
s_barrier
mvv 0,208,40
buffer_load_dwordx2 v[108:109], v90, s[36:39], s49 offen sc1
mvv 4,212,40
R 24,32768
mvv 8,216,40
ds_read_b64 v[78:79], v89 offset:0
mvv 0,220,44
R 28,33792
mvv 4,224,44
buffer_load_dwordx2 v[110:111], v90, s[36:39], s49 offen offset:512 sc1
mvv 8,228,44
s_waitcnt lgkmcnt(3)
mvv 0,232,48
R 32,34816
mvv 4,236,48
mvv 8,240,48
mvv 0,244,52
R 36,35840
mvv 4,248,52
mvv 8,252,52
s_waitcnt lgkmcnt(2)
v_mfma_f32_16x16x32_bf16 v[12:15], a[0:3], v[24:27], 0
LsF8:
s_cbranch_execz LsF12
R 40,36864
v_fma_f32 v104, v79, v56, v92
v_mfma_f32_16x16x32_bf16 v[16:19], a[4:7], v[24:27], 0
v_fma_f32 v0, v0, v78, v104
v_fma_f32 v105, v79, v57, v93
v_mfma_f32_16x16x32_bf16 v[20:23], a[8:11], v[24:27], 0
v_fma_f32 v1, v1, v78, v105
v_mul_f32_e64 v0, v0, |v0|
mav 12,12,28
R 44,37888
v_mul_f32_e64 v1, v1, |v1|
mav 16,16,28
s_waitcnt vmcnt(2)
buffer_store_dword v76, v81, s[36:39], s52 offen sc1
mav 20,20,28
s_waitcnt lgkmcnt(2)
v_cvt_pk_bf16_f32 v0, v0, v1
v_pk_max_i16 v68, v0, 0
mav 12,24,32
R 48,38912
v_fma_f32 v104, v79, v58, v94
mav 16,28,32
v_fma_f32 v2, v2, v78, v104
v_fma_f32 v105, v79, v59, v95
mav 20,32,32
v_fma_f32 v3, v3, v78, v105
v_mul_f32_e64 v2, v2, |v2|
mav 12,36,36
R 52,39936
v_mul_f32_e64 v3, v3, |v3|
mav 16,40,36
v_cvt_pk_bf16_f32 v2, v2, v3
v_pk_max_i16 v69, v2, 0
mav 20,44,36
s_waitcnt lgkmcnt(2)
v_fma_f32 v104, v79, v60, v96
v_fma_f32 v4, v4, v78, v104
mav 12,48,40
R 24,40960
v_fma_f32 v105, v79, v61, v97
mav 16,52,40
v_fma_f32 v5, v5, v78, v105
v_mul_f32_e64 v4, v4, |v4|
mav 20,56,40
v_mul_f32_e64 v5, v5, |v5|
v_cvt_pk_bf16_f32 v4, v4, v5
mav 12,60,44
R 28,41984
v_pk_max_i16 v70, v4, 0
mav 16,64,44
v_fma_f32 v104, v79, v62, v98
v_fma_f32 v6, v6, v78, v104
mav 20,68,44
s_waitcnt lgkmcnt(2)
v_fma_f32 v105, v79, v63, v99
v_fma_f32 v7, v7, v78, v105
mav 12,72,48
R 32,43008
v_mul_f32_e64 v6, v6, |v6|
mav 16,76,48
v_mul_f32_e64 v7, v7, |v7|
v_cvt_pk_bf16_f32 v6, v6, v7
mav 20,80,48
v_pk_max_i16 v71, v6, 0
v_fma_f32 v104, v79, v64, v100
mav 12,84,52
R 36,44032
v_fma_f32 v8, v8, v78, v104
mav 16,88,52
v_fma_f32 v105, v79, v65, v101
v_fma_f32 v9, v9, v78, v105
mav 20,92,52
s_waitcnt lgkmcnt(2)
v_mul_f32_e64 v8, v8, |v8|
v_mul_f32_e64 v9, v9, |v9|
mav 12,96,24
buffer_store_dwordx4 v[68:71], v83, s[36:39], s49 offen sc1
R 40,45056
v_cvt_pk_bf16_f32 v8, v8, v9
mav 16,100,24
v_pk_max_i16 v72, v8, 0
v_pk_max_i16 v74, v8, 0
mav 20,104,24
v_fma_f32 v104, v79, v66, v102
v_fma_f32 v10, v10, v78, v104
mav 12,108,28
R 44,46080
v_fma_f32 v105, v79, v67, v103
mav 16,112,28
v_fma_f32 v11, v11, v78, v105
v_mul_f32_e64 v10, v10, |v10|
mav 20,116,28
s_waitcnt lgkmcnt(2)
v_mul_f32_e64 v11, v11, |v11|
v_cvt_pk_bf16_f32 v10, v10, v11
mav 12,120,32
R 48,47104
v_pk_max_i16 v73, v10, 0
mav 16,124,32
v_pk_max_i16 v75, v10, 0
mav 20,128,32
v_permlane16_swap_b32 v72, v74
mav 12,132,36
R 52,48128
mav 16,136,36
v_permlane16_swap_b32 v73, v75
mav 20,140,36
s_waitcnt lgkmcnt(2)
mav 12,144,40
buffer_store_dwordx4 v[72:75], v84, s[36:39], s49 offen sc1
R 24,49152
mav 16,148,40
mav 20,152,40
mav 12,156,44
R 28,50176
mav 16,160,44
mav 20,164,44
s_waitcnt lgkmcnt(2)
mav 12,168,48
R 32,51200
mav 16,172,48
mav 20,176,48
mav 12,180,52
R 36,52224
mav 16,184,52
mav 20,188,52
s_waitcnt lgkmcnt(2)
mav 12,192,24
R 40,53248
mav 16,196,24
mav 20,200,24
s_waitcnt vmcnt(3)
v_add_f32 v108, v108, v110
v_add_f32 v109, v109, v111
mav 12,204,28
R 44,54272
mav 16,208,28
v_permlane32_swap_b32 v108, v109
mav 20,212,28
s_waitcnt lgkmcnt(2)
v_add_f32 v110, v108, v109
v_add_f32 v111, v108, v109
mav 12,216,32
R 48,55296
mav 16,220,32
v_permlane16_swap_b32 v110, v111
mav 20,224,32
v_add_f32 v108, v110, v111
v_add_f32 v109, v110, v111
mav 12,228,36
R 52,56320
mav 16,232,36
v_permlane32_swap_b32 v108, v109
mav 20,236,36
s_waitcnt lgkmcnt(2)
v_mul_f32 v110, 0x3a800000, v108
v_mul_f32 v111, 0x3a800000, v109
mav 12,240,40
R 24,57344
v_fma_f32 v111, -v110, v110, v111
mav 16,244,40
v_max_f32 v111, 0, v111
v_add_f32 v111, 0x3727c5ac, v111
mav 20,248,40
v_rsq_f32 v108, v111
mav 12,252,44
R 28,58368
v_mul_f32_e64 v109, -v110, v108
mvv 16,128,44
mvv 20,132,44
s_waitcnt lgkmcnt(2)
mvv 12,136,48
R 32,59392
mvv 16,140,48
mvv 20,144,48
mvv 12,148,52
R 36,60416
mvv 16,152,52
mvv 20,156,52
s_waitcnt lgkmcnt(2)
mvv 12,160,24
mvv 16,164,24
R 40,61440
mvv 20,168,24
mvv 12,172,28
R 44,62464
mvv 16,176,28
v_add_u32 v76, 1, v76
mvv 20,180,28
s_waitcnt lgkmcnt(2)
ds_write_b64 v89, v[108:109] offset:128
mvv 12,184,32
R 48,63488
mvv 16,188,32
mvv 20,192,32
mvv 12,196,36
R 52,64512
mvv 16,200,36
s_waitcnt lgkmcnt(2)
s_add_u32 s49, s49, 313344
mvv 20,204,36
s_barrier
mvv 12,208,40
mvv 16,212,40
ds_read_b64 v[78:79], v89 offset:128
mvv 20,216,40
mvv 12,220,44
mvv 16,224,44
mvv 20,228,44
s_waitcnt lgkmcnt(1)
mvv 12,232,48
mvv 16,236,48
mvv 20,240,48
mvv 12,244,52
mvv 16,248,52
mvv 20,252,52
s_waitcnt lgkmcnt(0)
v_fma_f32 v104, v79, v56, v92
s_nop 3
v_fma_f32 v12, v12, v78, v104
v_fma_f32 v105, v79, v57, v93
v_fma_f32 v13, v13, v78, v105
v_mul_f32_e64 v12, v12, |v12|
v_mul_f32_e64 v13, v13, |v13|
v_cvt_pk_bf16_f32 v12, v12, v13
v_pk_max_i16 v68, v12, 0
v_fma_f32 v104, v79, v58, v94
v_fma_f32 v14, v14, v78, v104
v_fma_f32 v105, v79, v59, v95
v_fma_f32 v15, v15, v78, v105
v_mul_f32_e64 v14, v14, |v14|
v_mul_f32_e64 v15, v15, |v15|
v_cvt_pk_bf16_f32 v14, v14, v15
v_pk_max_i16 v69, v14, 0
v_fma_f32 v104, v79, v60, v96
v_fma_f32 v16, v16, v78, v104
v_fma_f32 v105, v79, v61, v97
v_fma_f32 v17, v17, v78, v105
v_mul_f32_e64 v16, v16, |v16|
v_mul_f32_e64 v17, v17, |v17|
v_cvt_pk_bf16_f32 v16, v16, v17
v_pk_max_i16 v70, v16, 0
v_fma_f32 v104, v79, v62, v98
v_fma_f32 v18, v18, v78, v104
v_fma_f32 v105, v79, v63, v99
v_fma_f32 v19, v19, v78, v105
v_mul_f32_e64 v18, v18, |v18|
v_mul_f32_e64 v19, v19, |v19|
v_cvt_pk_bf16_f32 v18, v18, v19
v_pk_max_i16 v71, v18, 0
buffer_store_dwordx4 v[68:71], v83, s[36:39], s49 offen sc1
v_fma_f32 v104, v79, v64, v100
v_fma_f32 v20, v20, v78, v104
v_fma_f32 v105, v79, v65, v101
v_fma_f32 v21, v21, v78, v105
v_mul_f32_e64 v20, v20, |v20|
v_mul_f32_e64 v21, v21, |v21|
v_cvt_pk_bf16_f32 v20, v20, v21
v_pk_max_i16 v72, v20, 0
v_pk_max_i16 v74, v20, 0
v_fma_f32 v104, v79, v66, v102
v_fma_f32 v22, v22, v78, v104
v_fma_f32 v105, v79, v67, v103
v_fma_f32 v23, v23, v78, v105
v_mul_f32_e64 v22, v22, |v22|
v_mul_f32_e64 v23, v23, |v23|
v_cvt_pk_bf16_f32 v22, v22, v23
v_pk_max_i16 v73, v22, 0
v_pk_max_i16 v75, v22, 0
v_permlane16_swap_b32 v72, v74
s_nop 0
v_permlane16_swap_b32 v73, v75
buffer_store_dwordx4 v[72:75], v84, s[36:39], s49 offen sc1
s_waitcnt vmcnt(2)
buffer_store_dword v76, v81, s[36:39], s52 offen sc1
v_add_u32 v76, 37, v76
s_waitcnt vmcnt(1)
buffer_store_dword v76, v81, s[36:39], s52 offen sc1
s_endpgm
LvF2:
LsF9:
s_cbranch_execz LsF13
s_add_u32 s49, s49, 313344
s_barrier
s_add_u32 m0, s50, 0
s_nop 0
D 85,0
D 85,1024
D 85,2048
D 85,3072
s_add_u32 m0, s50, 4096
s_nop 0
D 86,0
D 86,1024
D 86,2048
D 86,3072
s_waitcnt vmcnt(0)
s_add_u32 s49, s49, 313344
s_barrier
s_add_u32 m0, s50, 32768
R 24,0
D 85,0
R 28,1024
D 85,1024
R 32,2048
R 36,3072
D 85,2048
s_waitcnt lgkmcnt(2)
v_mfma_f32_16x16x32_bf16 v[0:3], a[0:3], v[24:27], 0
R 40,4096
v_mfma_f32_16x16x32_bf16 v[4:7], a[4:7], v[24:27], 0
v_mfma_f32_16x16x32_bf16 v[8:11], a[8:11], v[24:27], 0
D 85,3072
mav 0,12,28
s_add_u32 m0, s50, 36864
R 44,5120
mav 4,16,28
mav 8,20,28
s_waitcnt lgkmcnt(2)
mav 0,24,32
D 86,0
mav 4,28,32
R 48,6144
mav 8,32,32
mav 0,36,36
R 52,7168
mav 4,40,36
D 86,1024
mav 8,44,36
s_waitcnt lgkmcnt(2)
mav 0,48,40
R 24,8192
mav 4,52,40
mav 8,56,40
D 86,2048
mav 0,60,44
R 28,9216
mav 4,64,44
mav 8,68,44
s_waitcnt lgkmcnt(2)
mav 0,72,48
D 86,3072
mav 4,76,48
R 32,10240
mav 8,80,48
mav 0,84,52
R 36,11264
mav 4,88,52
mav 8,92,52
s_waitcnt lgkmcnt(2)
mav 0,96,24
R 40,12288
mav 4,100,24
mav 8,104,24
mav 0,108,28
R 44,13312
mav 4,112,28
mav 8,116,28
s_waitcnt lgkmcnt(2)
mav 0,120,32
R 48,14336
mav 4,124,32
mav 8,128,32
mav 0,132,36
R 52,15360
mav 4,136,36
mav 8,140,36
s_waitcnt lgkmcnt(2)
mav 0,144,40
R 24,16384
mav 4,148,40
mav 8,152,40
mav 0,156,44
R 28,17408
mav 4,160,44
mav 8,164,44
s_waitcnt lgkmcnt(2)
mav 0,168,48
R 32,18432
mav 4,172,48
mav 8,176,48
mav 0,180,52
R 36,19456
mav 4,184,52
mav 8,188,52
s_waitcnt lgkmcnt(2)
mav 0,192,24
R 40,20480
mav 4,196,24
mav 8,200,24
mav 0,204,28
R 44,21504
mav 4,208,28
mav 8,212,28
s_waitcnt lgkmcnt(2)
mav 0,216,32
R 48,22528
mav 4,220,32
mav 8,224,32
mav 0,228,36
R 52,23552
mav 4,232,36
mav 8,236,36
s_waitcnt lgkmcnt(2)
mav 0,240,40
R 24,24576
mav 4,244,40
mav 8,248,40
mav 0,252,44
R 28,25600
mvv 4,128,44
mvv 8,132,44
s_waitcnt lgkmcnt(2)
mvv 0,136,48
R 32,26624
mvv 4,140,48
mvv 8,144,48
mvv 0,148,52
R 36,27648
mvv 4,152,52
mvv 8,156,52
s_waitcnt lgkmcnt(2)
mvv 0,160,24
R 40,28672
mvv 4,164,24
mvv 8,168,24
mvv 0,172,28
R 44,29696
mvv 4,176,28
mvv 8,180,28
s_waitcnt lgkmcnt(2)
mvv 0,184,32
s_xor_b32 s50, s50, 0x10000
R 48,30720
mvv 4,188,32
mvv 8,192,32
mvv 0,196,36
R 52,31744
mvv 4,200,36
s_waitcnt vmcnt(0) lgkmcnt(2)
s_add_u32 s49, s49, 313344
mvv 8,204,36
s_barrier
mvv 0,208,40
s_add_u32 m0, s50, 0
R 24,32768
mvv 4,212,40
ds_read_b64 v[78:79], v89 offset:0
mvv 8,216,40
D 85,0
mvv 0,220,44
R 28,33792
mvv 4,224,44
mvv 8,228,44
s_waitcnt lgkmcnt(3)
mvv 0,232,48
D 85,1024
mvv 4,236,48
R 32,34816
mvv 8,240,48
mvv 0,244,52
R 36,35840
mvv 4,248,52
D 85,2048
mvv 8,252,52
s_waitcnt lgkmcnt(2)
v_mfma_f32_16x16x32_bf16 v[12:15], a[0:3], v[24:27], 0
R 40,36864
v_fma_f32 v104, v79, v56, v92
v_mfma_f32_16x16x32_bf16 v[16:19], a[4:7], v[24:27], 0
v_fma_f32 v0, v0, v78, v104
v_fma_f32 v105, v79, v57, v93
v_mfma_f32_16x16x32_bf16 v[20:23], a[8:11], v[24:27], 0
D 85,3072
mav 12,12,28
s_add_u32 m0, s50, 4096
R 44,37888
v_fma_f32 v1, v1, v78, v105
mav 16,16,28
v_mul_f32_e64 v0, v0, |v0|
v_mul_f32_e64 v1, v1, |v1|
mav 20,20,28
s_waitcnt lgkmcnt(2)
v_cvt_pk_bf16_f32 v0, v0, v1
v_pk_max_i16 v68, v0, 0
mav 12,24,32
D 86,0
mav 16,28,32
R 48,38912
v_fma_f32 v104, v79, v58, v94
mav 20,32,32
v_fma_f32 v2, v2, v78, v104
v_fma_f32 v105, v79, v59, v95
mav 12,36,36
R 52,39936
v_fma_f32 v3, v3, v78, v105
mav 16,40,36
D 86,1024
mav 20,44,36
s_waitcnt lgkmcnt(2)
v_mul_f32_e64 v2, v2, |v2|
v_mul_f32_e64 v3, v3, |v3|
mav 12,48,40
R 24,40960
v_cvt_pk_bf16_f32 v2, v2, v3
mav 16,52,40
v_pk_max_i16 v69, v2, 0
v_fma_f32 v104, v79, v60, v96
mav 20,56,40
D 86,2048
mav 12,60,44
R 28,41984
v_fma_f32 v4, v4, v78, v104
mav 16,64,44
v_fma_f32 v105, v79, v61, v97
v_fma_f32 v5, v5, v78, v105
mav 20,68,44
s_waitcnt lgkmcnt(2)
v_mul_f32_e64 v4, v4, |v4|
v_mul_f32_e64 v5, v5, |v5|
mav 12,72,48
D 86,3072
mav 16,76,48
R 32,43008
v_cvt_pk_bf16_f32 v4, v4, v5
mav 20,80,48
v_pk_max_i16 v70, v4, 0
v_fma_f32 v104, v79, v62, v98
mav 12,84,52
R 36,44032
v_fma_f32 v6, v6, v78, v104
mav 16,88,52
v_fma_f32 v105, v79, v63, v99
v_fma_f32 v7, v7, v78, v105
mav 20,92,52
s_waitcnt lgkmcnt(2)
v_mul_f32_e64 v6, v6, |v6|
v_mul_f32_e64 v7, v7, |v7|
mav 12,96,24
R 40,45056
v_cvt_pk_bf16_f32 v6, v6, v7
mav 16,100,24
v_pk_max_i16 v71, v6, 0
v_fma_f32 v104, v79, v64, v100
mav 20,104,24
buffer_store_dwordx4 v[68:71], v83, s[36:39], s49 offen sc1
v_fma_f32 v8, v8, v78, v104
v_fma_f32 v105, v79, v65, v101
v_fma_f32 v9, v9, v78, v105
LsF10:
s_cbranch_execz LsF14
mav 12,108,28
R 44,46080
v_mul_f32_e64 v8, v8, |v8|
mav 16,112,28
v_mul_f32_e64 v9, v9, |v9|
v_cvt_pk_bf16_f32 v8, v8, v9
mav 20,116,28
s_waitcnt lgkmcnt(2)
v_pk_max_i16 v72, v8, 0
v_pk_max_i16 v74, v8, 0
mav 12,120,32
R 48,47104
v_fma_f32 v104, v79, v66, v102
mav 16,124,32
v_fma_f32 v10, v10, v78, v104
v_fma_f32 v105, v79, v67, v103
mav 20,128,32
v_fma_f32 v11, v11, v78, v105
v_mul_f32_e64 v10, v10, |v10|
mav 12,132,36
R 52,48128
v_mul_f32_e64 v11, v11, |v11|
mav 16,136,36
v_cvt_pk_bf16_f32 v10, v10, v11
v_pk_max_i16 v73, v10, 0
mav 20,140,36
s_waitcnt lgkmcnt(2)
v_pk_max_i16 v75, v10, 0
mav 12,144,40
R 24,49152
mav 16,148,40
v_permlane16_swap_b32 v72, v74
mav 20,152,40
v_permlane16_swap_b32 v73, v75
mav 12,156,44
R 28,50176
mav 16,160,44
mav 20,164,44
s_waitcnt lgkmcnt(2)
mav 12,168,48
buffer_store_dwordx4 v[72:75], v84, s[36:39], s49 offen sc1
R 32,51200
mav 16,172,48
mav 20,176,48
mav 12,180,52
R 36,52224
mav 16,184,52
mav 20,188,52
s_waitcnt lgkmcnt(2)
mav 12,192,24
R 40,53248
mav 16,196,24
mav 20,200,24
mav 12,204,28
R 44,54272
mav 16,208,28
mav 20,212,28
s_waitcnt lgkmcnt(2)
mav 12,216,32
R 48,55296
mav 16,220,32
mav 20,224,32
mav 12,228,36
R 52,56320
mav 16,232,36
mav 20,236,36
s_waitcnt lgkmcnt(2)
mav 12,240,40
R 24,57344
mav 16,244,40
mav 20,248,40
mav 12,252,44
R 28,58368
mvv 16,128,44
mvv 20,132,44
s_waitcnt lgkmcnt(2)
mvv 12,136,48
R 32,59392
mvv 16,140,48
mvv 20,144,48
mvv 12,148,52
R 36,60416
mvv 16,152,52
mvv 20,156,52
s_waitcnt lgkmcnt(2)
mvv 12,160,24
s_mov_b32 s60, -30
.p2align 6
LF2_1:
mvv 16,164,24
R 40,61440
mvv 20,168,24
mvv 12,172,28
R 44,62464
mvv 16,176,28
v_add_u32 v76, 1, v76
mvv 20,180,28
s_waitcnt lgkmcnt(2)
mvv 12,184,32
R 48,63488
mvv 16,188,32
mvv 20,192,32
mvv 12,196,36
R 52,64512
v_xor_b32 v80, 0x10000, v80
mvv 16,200,36
s_waitcnt vmcnt(2) lgkmcnt(2)
s_add_u32 s49, s49, 313344
mvv 20,204,36
s_barrier
mvv 12,208,40
s_add_u32 m0, s50, 32768
R 24,0
mvv 16,212,40
ds_read_b64 v[78:79], v89 offset:128
mvv 20,216,40
D 85,0
mvv 12,220,44
R 28,1024
mvv 16,224,44
mvv 20,228,44
s_waitcnt lgkmcnt(3)
mvv 12,232,48
D 85,1024
mvv 16,236,48
R 32,2048
mvv 20,240,48
mvv 12,244,52
R 36,3072
mvv 16,248,52
D 85,2048
mvv 20,252,52
s_waitcnt lgkmcnt(2)
v_mfma_f32_16x16x32_bf16 v[0:3], a[0:3], v[24:27], 0
R 40,4096
v_fma_f32 v104, v79, v56, v92
v_mfma_f32_16x16x32_bf16 v[4:7], a[4:7], v[24:27], 0
v_fma_f32 v12, v12, v78, v104
v_fma_f32 v105, v79, v57, v93
v_mfma_f32_16x16x32_bf16 v[8:11], a[8:11], v[24:27], 0
D 85,3072
mav 0,12,28
s_add_u32 m0, s50, 36864
R 44,5120
v_fma_f32 v13, v13, v78, v105
mav 4,16,28
s_waitcnt vmcnt(4)
v_mul_f32_e64 v12, v12, |v12|
v_mul_f32_e64 v13, v13, |v13|
mav 8,20,28
s_waitcnt lgkmcnt(2)
v_cvt_pk_bf16_f32 v12, v12, v13
v_pk_max_i16 v68, v12, 0
mav 0,24,32
buffer_store_dword v76, v81, s[36:39], s52 offen sc1
mav 4,28,32
R 48,6144
v_fma_f32 v104, v79, v58, v94
mav 8,32,32
v_fma_f32 v14, v14, v78, v104
v_fma_f32 v105, v79, v59, v95
mav 0,36,36
R 52,7168
v_fma_f32 v15, v15, v78, v105
mav 4,40,36
D 86,0
mav 8,44,36
s_waitcnt lgkmcnt(2)
v_mul_f32_e64 v14, v14, |v14|
v_mul_f32_e64 v15, v15, |v15|
mav 0,48,40
R 24,8192
v_cvt_pk_bf16_f32 v14, v14, v15
mav 4,52,40
v_pk_max_i16 v69, v14, 0
v_fma_f32 v104, v79, v60, v96
mav 8,56,40
D 86,1024
mav 0,60,44
R 28,9216
v_fma_f32 v16, v16, v78, v104
mav 4,64,44
v_fma_f32 v105, v79, v61, v97
v_fma_f32 v17, v17, v78, v105
mav 8,68,44
s_waitcnt lgkmcnt(2)
v_mul_f32_e64 v16, v16, |v16|
v_mul_f32_e64 v17, v17, |v17|
mav 0,72,48
D 86,2048
mav 4,76,48
R 32,10240
v_cvt_pk_bf16_f32 v16, v16, v17
mav 8,80,48
v_pk_max_i16 v70, v16, 0
v_fma_f32 v104, v79, v62, v98
mav 0,84,52
R 36,11264
v_fma_f32 v18, v18, v78, v104
mav 4,88,52
D 86,3072
mav 8,92,52
s_waitcnt lgkmcnt(2)
v_fma_f32 v105, v79, v63, v99
v_fma_f32 v19, v19, v78, v105
mav 0,96,24
R 40,12288
v_mul_f32_e64 v18, v18, |v18|
mav 4,100,24
v_mul_f32_e64 v19, v19, |v19|
v_cvt_pk_bf16_f32 v18, v18, v19
mav 8,104,24
v_pk_max_i16 v71, v18, 0
v_fma_f32 v104, v79, v64, v100
mav 0,108,28
R 44,13312
v_fma_f32 v20, v20, v78, v104
mav 4,112,28
v_fma_f32 v105, v79, v65, v101
v_fma_f32 v21, v21, v78, v105
mav 8,116,28
s_waitcnt lgkmcnt(2)
v_mul_f32_e64 v20, v20, |v20|
v_mul_f32_e64 v21, v21, |v21|
mav 0,120,32
buffer_store_dwordx4 v[68:71], v83, s[36:39], s49 offen sc1
R 48,14336
v_cvt_pk_bf16_f32 v20, v20, v21
mav 4,124,32
v_pk_max_i16 v72, v20, 0
v_pk_max_i16 v74, v20, 0
mav 8,128,32
v_fma_f32 v104, v79, v66, v102
v_fma_f32 v22, v22, v78, v104
mav 0,132,36
R 52,15360
v_fma_f32 v105, v79, v67, v103
mav 4,136,36
v_fma_f32 v23, v23, v78, v105
v_mul_f32_e64 v22, v22, |v22|
mav 8,140,36
s_waitcnt lgkmcnt(2)
v_mul_f32_e64 v23, v23, |v23|
v_cvt_pk_bf16_f32 v22, v22, v23
mav 0,144,40
R 24,16384
v_pk_max_i16 v73, v22, 0
mav 4,148,40
v_pk_max_i16 v75, v22, 0
mav 8,152,40
v_permlane16_swap_b32 v72, v74
mav 0,156,44
R 28,17408
mav 4,160,44
v_permlane16_swap_b32 v73, v75
mav 8,164,44
s_waitcnt lgkmcnt(2)
mav 0,168,48
buffer_store_dwordx4 v[72:75], v84, s[36:39], s49 offen sc1
R 32,18432
mav 4,172,48
mav 8,176,48
mav 0,180,52
R 36,19456
mav 4,184,52
mav 8,188,52
s_waitcnt lgkmcnt(2)
mav 0,192,24
R 40,20480
mav 4,196,24
mav 8,200,24
mav 0,204,28
R 44,21504
mav 4,208,28
mav 8,212,28
s_waitcnt lgkmcnt(2)
mav 0,216,32
R 48,22528
mav 4,220,32
mav 8,224,32
mav 0,228,36
R 52,23552
mav 4,232,36
mav 8,236,36
s_waitcnt lgkmcnt(2)
mav 0,240,40
R 24,24576
mav 4,244,40
mav 8,248,40
mav 0,252,44
R 28,25600
mvv 4,128,44
mvv 8,132,44
s_waitcnt lgkmcnt(2)
mvv 0,136,48
R 32,26624
mvv 4,140,48
mvv 8,144,48
mvv 0,148,52
R 36,27648
mvv 4,152,52
mvv 8,156,52
s_waitcnt lgkmcnt(2)
mvv 0,160,24
R 40,28672
mvv 4,164,24
mvv 8,168,24
mvv 0,172,28
R 44,29696
mvv 4,176,28
v_add_u32 v76, 1, v76
mvv 8,180,28
s_waitcnt lgkmcnt(2)
mvv 0,184,32
s_xor_b32 s50, s50, 0x10000
R 48,30720
mvv 4,188,32
mvv 8,192,32
mvv 0,196,36
R 52,31744
mvv 4,200,36
s_waitcnt vmcnt(2) lgkmcnt(2)
s_add_u32 s49, s49, 313344
mvv 8,204,36
s_barrier
mvv 0,208,40
s_add_u32 m0, s50, 0
R 24,32768
mvv 4,212,40
ds_read_b64 v[78:79], v89 offset:0
mvv 8,216,40
D 85,0
mvv 0,220,44
R 28,33792
mvv 4,224,44
mvv 8,228,44
s_waitcnt lgkmcnt(3)
mvv 0,232,48
D 85,1024
mvv 4,236,48
R 32,34816
mvv 8,240,48
mvv 0,244,52
R 36,35840
mvv 4,248,52
D 85,2048
mvv 8,252,52
s_waitcnt lgkmcnt(2)
v_mfma_f32_16x16x32_bf16 v[12:15], a[0:3], v[24:27], 0
R 40,36864
v_fma_f32 v104, v79, v56, v92
v_mfma_f32_16x16x32_bf16 v[16:19], a[4:7], v[24:27], 0
v_fma_f32 v0, v0, v78, v104
v_fma_f32 v105, v79, v57, v93
v_mfma_f32_16x16x32_bf16 v[20:23], a[8:11], v[24:27], 0
D 85,3072
mav 12,12,28
s_add_u32 m0, s50, 4096
R 44,37888
v_fma_f32 v1, v1, v78, v105
mav 16,16,28
s_waitcnt vmcnt(4)
v_mul_f32_e64 v0, v0, |v0|
v_mul_f32_e64 v1, v1, |v1|
mav 20,20,28
s_waitcnt lgkmcnt(2)
v_cvt_pk_bf16_f32 v0, v0, v1
v_pk_max_i16 v68, v0, 0
mav 12,24,32
buffer_store_dword v76, v81, s[36:39], s52 offen sc1
mav 16,28,32
R 48,38912
v_fma_f32 v104, v79, v58, v94
mav 20,32,32
v_fma_f32 v2, v2, v78, v104
v_fma_f32 v105, v79, v59, v95
mav 12,36,36
R 52,39936
v_fma_f32 v3, v3, v78, v105
mav 16,40,36
D 86,0
mav 20,44,36
s_waitcnt lgkmcnt(2)
v_mul_f32_e64 v2, v2, |v2|
v_mul_f32_e64 v3, v3, |v3|
mav 12,48,40
R 24,40960
v_cvt_pk_bf16_f32 v2, v2, v3
mav 16,52,40
v_pk_max_i16 v69, v2, 0
v_fma_f32 v104, v79, v60, v96
mav 20,56,40
D 86,1024
mav 12,60,44
R 28,41984
v_fma_f32 v4, v4, v78, v104
mav 16,64,44
v_fma_f32 v105, v79, v61, v97
v_fma_f32 v5, v5, v78, v105
mav 20,68,44
s_waitcnt lgkmcnt(2)
v_mul_f32_e64 v4, v4, |v4|
v_mul_f32_e64 v5, v5, |v5|
mav 12,72,48
D 86,2048
mav 16,76,48
R 32,43008
v_cvt_pk_bf16_f32 v4, v4, v5
mav 20,80,48
v_pk_max_i16 v70, v4, 0
v_fma_f32 v104, v79, v62, v98
mav 12,84,52
R 36,44032
v_fma_f32 v6, v6, v78, v104
mav 16,88,52
D 86,3072
mav 20,92,52
s_waitcnt lgkmcnt(2)
v_fma_f32 v105, v79, v63, v99
v_fma_f32 v7, v7, v78, v105
mav 12,96,24
R 40,45056
v_mul_f32_e64 v6, v6, |v6|
mav 16,100,24
v_mul_f32_e64 v7, v7, |v7|
v_cvt_pk_bf16_f32 v6, v6, v7
mav 20,104,24
v_pk_max_i16 v71, v6, 0
v_fma_f32 v104, v79, v64, v100
mav 12,108,28
R 44,46080
v_fma_f32 v8, v8, v78, v104
mav 16,112,28
v_fma_f32 v105, v79, v65, v101
v_fma_f32 v9, v9, v78, v105
mav 20,116,28
s_waitcnt lgkmcnt(2)
v_mul_f32_e64 v8, v8, |v8|
v_mul_f32_e64 v9, v9, |v9|
mav 12,120,32
buffer_store_dwordx4 v[68:71], v83, s[36:39], s49 offen sc1
R 48,47104
v_cvt_pk_bf16_f32 v8, v8, v9
mav 16,124,32
v_pk_max_i16 v72, v8, 0
v_pk_max_i16 v74, v8, 0
mav 20,128,32
v_fma_f32 v104, v79, v66, v102
v_fma_f32 v10, v10, v78, v104
mav 12,132,36
R 52,48128
v_fma_f32 v105, v79, v67, v103
mav 16,136,36
v_fma_f32 v11, v11, v78, v105
v_mul_f32_e64 v10, v10, |v10|
mav 20,140,36
s_waitcnt lgkmcnt(2)
v_mul_f32_e64 v11, v11, |v11|
v_cvt_pk_bf16_f32 v10, v10, v11
mav 12,144,40
R 24,49152
v_pk_max_i16 v73, v10, 0
mav 16,148,40
v_pk_max_i16 v75, v10, 0
mav 20,152,40
v_permlane16_swap_b32 v72, v74
mav 12,156,44
R 28,50176
mav 16,160,44
v_permlane16_swap_b32 v73, v75
mav 20,164,44
s_waitcnt lgkmcnt(2)
mav 12,168,48
buffer_store_dwordx4 v[72:75], v84, s[36:39], s49 offen sc1
R 32,51200
mav 16,172,48
mav 20,176,48
mav 12,180,52
R 36,52224
mav 16,184,52
mav 20,188,52
s_waitcnt lgkmcnt(2)
mav 12,192,24
R 40,53248
mav 16,196,24
mav 20,200,24
mav 12,204,28
R 44,54272
mav 16,208,28
mav 20,212,28
s_waitcnt lgkmcnt(2)
mav 12,216,32
R 48,55296
mav 16,220,32
mav 20,224,32
mav 12,228,36
R 52,56320
mav 16,232,36
mav 20,236,36
s_waitcnt lgkmcnt(2)
mav 12,240,40
R 24,57344
mav 16,244,40
mav 20,248,40
mav 12,252,44
R 28,58368
mvv 16,128,44
mvv 20,132,44
s_waitcnt lgkmcnt(2)
mvv 12,136,48
R 32,59392
mvv 16,140,48
mvv 20,144,48
mvv 12,148,52
R 36,60416
mvv 16,152,52
mvv 20,156,52
s_waitcnt lgkmcnt(2)
s_add_u32 s60, s60, 1
mvv 12,160,24
s_cbranch_scc0 LF2_1
mvv 16,164,24
R 40,61440
mvv 20,168,24
mvv 12,172,28
R 44,62464
mvv 16,176,28
v_add_u32 v76, 1, v76
mvv 20,180,28
s_waitcnt lgkmcnt(2)
mvv 12,184,32
R 48,63488
mvv 16,188,32
mvv 20,192,32
mvv 12,196,36
R 52,64512
v_xor_b32 v80, 0x10000, v80
LsF11:
s_cbranch_execz LsF15
mvv 16,200,36
s_waitcnt vmcnt(2) lgkmcnt(2)
s_add_u32 s49, s49, 313344
mvv 20,204,36
s_barrier
mvv 12,208,40
s_add_u32 m0, s50, 32768
R 24,0
mvv 16,212,40
ds_read_b64 v[78:79], v89 offset:128
mvv 20,216,40
D 85,0
mvv 12,220,44
R 28,1024
mvv 16,224,44
mvv 20,228,44
s_waitcnt lgkmcnt(3)
mvv 12,232,48
D 85,1024
mvv 16,236,48
R 32,2048
mvv 20,240,48
mvv 12,244,52
R 36,3072
mvv 16,248,52
D 85,2048
mvv 20,252,52
s_waitcnt lgkmcnt(2)
v_mfma_f32_16x16x32_bf16 v[0:3], a[0:3], v[24:27], 0
R 40,4096
v_fma_f32 v104, v79, v56, v92
v_mfma_f32_16x16x32_bf16 v[4:7], a[4:7], v[24:27], 0
v_fma_f32 v12, v12, v78, v104
v_fma_f32 v105, v79, v57, v93
v_mfma_f32_16x16x32_bf16 v[8:11], a[8:11], v[24:27], 0
D 85,3072
mav 0,12,28
s_add_u32 m0, s50, 36864
R 44,5120
v_fma_f32 v13, v13, v78, v105
mav 4,16,28
s_waitcnt vmcnt(4)
v_mul_f32_e64 v12, v12, |v12|
v_mul_f32_e64 v13, v13, |v13|
mav 8,20,28
s_waitcnt lgkmcnt(2)
v_cvt_pk_bf16_f32 v12, v12, v13
v_pk_max_i16 v68, v12, 0
mav 0,24,32
buffer_store_dword v76, v81, s[36:39], s52 offen sc1
mav 4,28,32
R 48,6144
v_fma_f32 v104, v79, v58, v94
mav 8,32,32
v_fma_f32 v14, v14, v78, v104
v_fma_f32 v105, v79, v59, v95
mav 0,36,36
R 52,7168
v_fma_f32 v15, v15, v78, v105
mav 4,40,36
D 86,0
mav 8,44,36
s_waitcnt lgkmcnt(2)
v_mul_f32_e64 v14, v14, |v14|
v_mul_f32_e64 v15, v15, |v15|
mav 0,48,40
R 24,8192
v_cvt_pk_bf16_f32 v14, v14, v15
mav 4,52,40
v_pk_max_i16 v69, v14, 0
v_fma_f32 v104, v79, v60, v96
mav 8,56,40
D 86,1024
mav 0,60,44
R 28,9216
v_fma_f32 v16, v16, v78, v104
mav 4,64,44
v_fma_f32 v105, v79, v61, v97
v_fma_f32 v17, v17, v78, v105
mav 8,68,44
s_waitcnt lgkmcnt(2)
v_mul_f32_e64 v16, v16, |v16|
v_mul_f32_e64 v17, v17, |v17|
mav 0,72,48
D 86,2048
mav 4,76,48
R 32,10240
v_cvt_pk_bf16_f32 v16, v16, v17
mav 8,80,48
v_pk_max_i16 v70, v16, 0
v_fma_f32 v104, v79, v62, v98
mav 0,84,52
R 36,11264
v_fma_f32 v18, v18, v78, v104
mav 4,88,52
D 86,3072
mav 8,92,52
s_waitcnt lgkmcnt(2)
v_fma_f32 v105, v79, v63, v99
v_fma_f32 v19, v19, v78, v105
mav 0,96,24
R 40,12288
v_mul_f32_e64 v18, v18, |v18|
mav 4,100,24
v_mul_f32_e64 v19, v19, |v19|
v_cvt_pk_bf16_f32 v18, v18, v19
mav 8,104,24
v_pk_max_i16 v71, v18, 0
v_fma_f32 v104, v79, v64, v100
mav 0,108,28
R 44,13312
v_fma_f32 v20, v20, v78, v104
mav 4,112,28
v_fma_f32 v105, v79, v65, v101
v_fma_f32 v21, v21, v78, v105
mav 8,116,28
s_waitcnt lgkmcnt(2)
v_mul_f32_e64 v20, v20, |v20|
v_mul_f32_e64 v21, v21, |v21|
mav 0,120,32
buffer_store_dwordx4 v[68:71], v83, s[36:39], s49 offen sc1
R 48,14336
v_cvt_pk_bf16_f32 v20, v20, v21
mav 4,124,32
v_pk_max_i16 v72, v20, 0
v_pk_max_i16 v74, v20, 0
mav 8,128,32
v_fma_f32 v104, v79, v66, v102
v_fma_f32 v22, v22, v78, v104
mav 0,132,36
R 52,15360
v_fma_f32 v105, v79, v67, v103
mav 4,136,36
v_fma_f32 v23, v23, v78, v105
v_mul_f32_e64 v22, v22, |v22|
mav 8,140,36
s_waitcnt lgkmcnt(2)
v_mul_f32_e64 v23, v23, |v23|
v_cvt_pk_bf16_f32 v22, v22, v23
mav 0,144,40
R 24,16384
v_pk_max_i16 v73, v22, 0
mav 4,148,40
v_pk_max_i16 v75, v22, 0
mav 8,152,40
v_permlane16_swap_b32 v72, v74
mav 0,156,44
R 28,17408
mav 4,160,44
v_permlane16_swap_b32 v73, v75
mav 8,164,44
s_waitcnt lgkmcnt(2)
mav 0,168,48
buffer_store_dwordx4 v[72:75], v84, s[36:39], s49 offen sc1
R 32,18432
mav 4,172,48
mav 8,176,48
mav 0,180,52
R 36,19456
mav 4,184,52
mav 8,188,52
s_waitcnt lgkmcnt(2)
mav 0,192,24
R 40,20480
mav 4,196,24
mav 8,200,24
mav 0,204,28
R 44,21504
mav 4,208,28
mav 8,212,28
s_waitcnt lgkmcnt(2)
mav 0,216,32
R 48,22528
mav 4,220,32
mav 8,224,32
mav 0,228,36
R 52,23552
mav 4,232,36
mav 8,236,36
s_waitcnt lgkmcnt(2)
mav 0,240,40
R 24,24576
mav 4,244,40
mav 8,248,40
mav 0,252,44
R 28,25600
mvv 4,128,44
mvv 8,132,44
s_waitcnt lgkmcnt(2)
mvv 0,136,48
R 32,26624
mvv 4,140,48
mvv 8,144,48
mvv 0,148,52
R 36,27648
mvv 4,152,52
mvv 8,156,52
s_waitcnt lgkmcnt(2)
mvv 0,160,24
R 40,28672
mvv 4,164,24
mvv 8,168,24
mvv 0,172,28
R 44,29696
mvv 4,176,28
v_add_u32 v76, 1, v76
mvv 8,180,28
s_waitcnt lgkmcnt(2)
mvv 0,184,32
R 48,30720
mvv 4,188,32
mvv 8,192,32
mvv 0,196,36
R 52,31744
mvv 4,200,36
s_waitcnt vmcnt(2) lgkmcnt(2)
s_add_u32 s49, s49, 313344
mvv 8,204,36
s_barrier
mvv 0,208,40
R 24,32768
mvv 4,212,40
ds_read_b64 v[78:79], v89 offset:0
mvv 8,216,40
mvv 0,220,44
R 28,33792
mvv 4,224,44
mvv 8,228,44
s_waitcnt lgkmcnt(3)
mvv 0,232,48
R 32,34816
mvv 4,236,48
mvv 8,240,48
mvv 0,244,52
R 36,35840
mvv 4,248,52
mvv 8,252,52
s_waitcnt lgkmcnt(2)
v_mfma_f32_16x16x32_bf16 v[12:15], a[0:3], v[24:27], 0
R 40,36864
v_fma_f32 v104, v79, v56, v92
v_mfma_f32_16x16x32_bf16 v[16:19], a[4:7], v[24:27], 0
v_fma_f32 v0, v0, v78, v104
v_fma_f32 v105, v79, v57, v93
v_mfma_f32_16x16x32_bf16 v[20:23], a[8:11], v[24:27], 0
v_fma_f32 v1, v1, v78, v105
v_mul_f32_e64 v0, v0, |v0|
mav 12,12,28
R 44,37888
v_mul_f32_e64 v1, v1, |v1|
mav 16,16,28
s_waitcnt vmcnt(0)
v_cvt_pk_bf16_f32 v0, v0, v1
v_pk_max_i16 v68, v0, 0
mav 20,20,28
s_waitcnt lgkmcnt(2)
v_fma_f32 v104, v79, v58, v94
v_fma_f32 v2, v2, v78, v104
mav 12,24,32
buffer_store_dword v76, v81, s[36:39], s52 offen sc1
mav 16,28,32
R 48,38912
v_fma_f32 v105, v79, v59, v95
mav 20,32,32
v_fma_f32 v3, v3, v78, v105
v_mul_f32_e64 v2, v2, |v2|
mav 12,36,36
R 52,39936
v_mul_f32_e64 v3, v3, |v3|
mav 16,40,36
v_cvt_pk_bf16_f32 v2, v2, v3
v_pk_max_i16 v69, v2, 0
mav 20,44,36
s_waitcnt lgkmcnt(2)
v_fma_f32 v104, v79, v60, v96
v_fma_f32 v4, v4, v78, v104
mav 12,48,40
R 24,40960
v_fma_f32 v105, v79, v61, v97
mav 16,52,40
v_fma_f32 v5, v5, v78, v105
v_mul_f32_e64 v4, v4, |v4|
mav 20,56,40
v_mul_f32_e64 v5, v5, |v5|
v_cvt_pk_bf16_f32 v4, v4, v5
mav 12,60,44
)"
R"(
R 28,41984
v_pk_max_i16 v70, v4, 0
mav 16,64,44
v_fma_f32 v104, v79, v62, v98
v_fma_f32 v6, v6, v78, v104
mav 20,68,44
s_waitcnt lgkmcnt(2)
v_fma_f32 v105, v79, v63, v99
v_fma_f32 v7, v7, v78, v105
mav 12,72,48
R 32,43008
v_mul_f32_e64 v6, v6, |v6|
mav 16,76,48
v_mul_f32_e64 v7, v7, |v7|
v_cvt_pk_bf16_f32 v6, v6, v7
mav 20,80,48
v_pk_max_i16 v71, v6, 0
v_fma_f32 v104, v79, v64, v100
mav 12,84,52
R 36,44032
v_fma_f32 v8, v8, v78, v104
mav 16,88,52
buffer_store_dwordx4 v[68:71], v83, s[36:39], s49 offen sc1
v_fma_f32 v105, v79, v65, v101
v_fma_f32 v9, v9, v78, v105
v_mul_f32_e64 v8, v8, |v8|
mav 20,92,52
s_waitcnt lgkmcnt(2)
v_mul_f32_e64 v9, v9, |v9|
v_cvt_pk_bf16_f32 v8, v8, v9
mav 12,96,24
R 40,45056
v_pk_max_i16 v72, v8, 0
mav 16,100,24
v_pk_max_i16 v74, v8, 0
v_fma_f32 v104, v79, v66, v102
mav 20,104,24
v_fma_f32 v10, v10, v78, v104
v_fma_f32 v105, v79, v67, v103
mav 12,108,28
R 44,46080
v_fma_f32 v11, v11, v78, v105
mav 16,112,28
v_mul_f32_e64 v10, v10, |v10|
v_mul_f32_e64 v11, v11, |v11|
mav 20,116,28
s_waitcnt lgkmcnt(2)
v_cvt_pk_bf16_f32 v10, v10, v11
v_pk_max_i16 v73, v10, 0
mav 12,120,32
R 48,47104
v_pk_max_i16 v75, v10, 0
mav 16,124,32
v_permlane16_swap_b32 v72, v74
mav 20,128,32
v_permlane16_swap_b32 v73, v75
mav 12,132,36
R 52,48128
mav 16,136,36
buffer_store_dwordx4 v[72:75], v84, s[36:39], s49 offen sc1
mav 20,140,36
s_waitcnt lgkmcnt(2)
mav 12,144,40
R 24,49152
mav 16,148,40
mav 20,152,40
mav 12,156,44
R 28,50176
mav 16,160,44
mav 20,164,44
s_waitcnt lgkmcnt(2)
mav 12,168,48
R 32,51200
mav 16,172,48
mav 20,176,48
mav 12,180,52
R 36,52224
mav 16,184,52
mav 20,188,52
s_waitcnt lgkmcnt(2)
mav 12,192,24
R 40,53248
mav 16,196,24
mav 20,200,24
mav 12,204,28
R 44,54272
mav 16,208,28
mav 20,212,28
s_waitcnt lgkmcnt(2)
mav 12,216,32
R 48,55296
mav 16,220,32
mav 20,224,32
mav 12,228,36
R 52,56320
mav 16,232,36
mav 20,236,36
s_waitcnt lgkmcnt(2)
mav 12,240,40
R 24,57344
mav 16,244,40
mav 20,248,40
mav 12,252,44
R 28,58368
mvv 16,128,44
mvv 20,132,44
s_waitcnt lgkmcnt(2)
mvv 12,136,48
R 32,59392
mvv 16,140,48
mvv 20,144,48
mvv 12,148,52
R 36,60416
mvv 16,152,52
mvv 20,156,52
s_waitcnt lgkmcnt(2)
mvv 12,160,24
mvv 16,164,24
R 40,61440
mvv 20,168,24
mvv 12,172,28
R 44,62464
mvv 16,176,28
v_add_u32 v76, 1, v76
mvv 20,180,28
s_waitcnt lgkmcnt(2)
mvv 12,184,32
R 48,63488
mvv 16,188,32
mvv 20,192,32
mvv 12,196,36
R 52,64512
mvv 16,200,36
s_waitcnt lgkmcnt(2)
s_add_u32 s49, s49, 313344
mvv 20,204,36
s_barrier
mvv 12,208,40
mvv 16,212,40
ds_read_b64 v[78:79], v89 offset:128
mvv 20,216,40
mvv 12,220,44
mvv 16,224,44
mvv 20,228,44
s_waitcnt lgkmcnt(1)
mvv 12,232,48
mvv 16,236,48
mvv 20,240,48
mvv 12,244,52
mvv 16,248,52
mvv 20,252,52
s_waitcnt lgkmcnt(0)
v_fma_f32 v104, v79, v56, v92
s_nop 3
v_fma_f32 v12, v12, v78, v104
v_fma_f32 v105, v79, v57, v93
v_fma_f32 v13, v13, v78, v105
v_mul_f32_e64 v12, v12, |v12|
v_mul_f32_e64 v13, v13, |v13|
v_cvt_pk_bf16_f32 v12, v12, v13
v_pk_max_i16 v68, v12, 0
v_fma_f32 v104, v79, v58, v94
v_fma_f32 v14, v14, v78, v104
v_fma_f32 v105, v79, v59, v95
v_fma_f32 v15, v15, v78, v105
v_mul_f32_e64 v14, v14, |v14|
v_mul_f32_e64 v15, v15, |v15|
v_cvt_pk_bf16_f32 v14, v14, v15
v_pk_max_i16 v69, v14, 0
v_fma_f32 v104, v79, v60, v96
v_fma_f32 v16, v16, v78, v104
v_fma_f32 v105, v79, v61, v97
v_fma_f32 v17, v17, v78, v105
v_mul_f32_e64 v16, v16, |v16|
v_mul_f32_e64 v17, v17, |v17|
v_cvt_pk_bf16_f32 v16, v16, v17
v_pk_max_i16 v70, v16, 0
v_fma_f32 v104, v79, v62, v98
v_fma_f32 v18, v18, v78, v104
v_fma_f32 v105, v79, v63, v99
v_fma_f32 v19, v19, v78, v105
v_mul_f32_e64 v18, v18, |v18|
v_mul_f32_e64 v19, v19, |v19|
v_cvt_pk_bf16_f32 v18, v18, v19
v_pk_max_i16 v71, v18, 0
buffer_store_dwordx4 v[68:71], v83, s[36:39], s49 offen sc1
v_fma_f32 v104, v79, v64, v100
v_fma_f32 v20, v20, v78, v104
v_fma_f32 v105, v79, v65, v101
v_fma_f32 v21, v21, v78, v105
v_mul_f32_e64 v20, v20, |v20|
v_mul_f32_e64 v21, v21, |v21|
v_cvt_pk_bf16_f32 v20, v20, v21
v_pk_max_i16 v72, v20, 0
v_pk_max_i16 v74, v20, 0
v_fma_f32 v104, v79, v66, v102
v_fma_f32 v22, v22, v78, v104
v_fma_f32 v105, v79, v67, v103
v_fma_f32 v23, v23, v78, v105
v_mul_f32_e64 v22, v22, |v22|
v_mul_f32_e64 v23, v23, |v23|
v_cvt_pk_bf16_f32 v22, v22, v23
v_pk_max_i16 v73, v22, 0
v_pk_max_i16 v75, v22, 0
v_permlane16_swap_b32 v72, v74
s_nop 0
v_permlane16_swap_b32 v73, v75
buffer_store_dwordx4 v[72:75], v84, s[36:39], s49 offen sc1
s_waitcnt vmcnt(2)
buffer_store_dword v76, v81, s[36:39], s52 offen sc1
v_add_u32 v76, 37, v76
s_waitcnt vmcnt(1)
buffer_store_dword v76, v81, s[36:39], s52 offen sc1
s_endpgm
LvF3:
LsF12:
s_cbranch_execz LwF
s_add_u32 s49, s49, 313344
s_barrier
s_add_u32 m0, s50, 0
s_nop 0
D 85,0
D 85,1024
D 85,2048
D 85,3072
s_add_u32 m0, s50, 4096
s_nop 0
D 86,0
D 86,1024
D 86,2048
D 86,3072
s_waitcnt vmcnt(0)
s_add_u32 s49, s49, 313344
s_barrier
s_add_u32 m0, s50, 32768
R 24,0
D 85,0
R 28,1024
D 85,1024
R 32,2048
R 36,3072
D 85,2048
s_waitcnt lgkmcnt(2)
v_mfma_f32_16x16x32_bf16 v[0:3], a[0:3], v[24:27], 0
R 40,4096
v_mfma_f32_16x16x32_bf16 v[4:7], a[4:7], v[24:27], 0
v_mfma_f32_16x16x32_bf16 v[8:11], a[8:11], v[24:27], 0
mav 0,12,28
D 85,3072
mav 4,16,28
s_add_u32 m0, s50, 36864
R 44,5120
mav 8,20,28
s_waitcnt lgkmcnt(2)
mav 0,24,32
R 48,6144
mav 4,28,32
D 86,0
mav 8,32,32
LsF13:
s_cbranch_execz LwF
mav 0,36,36
R 52,7168
mav 4,40,36
mav 8,44,36
s_waitcnt lgkmcnt(2)
D 86,1024
mav 0,48,40
R 24,8192
mav 4,52,40
mav 8,56,40
mav 0,60,44
D 86,2048
mav 4,64,44
R 28,9216
mav 8,68,44
s_waitcnt lgkmcnt(2)
mav 0,72,48
R 32,10240
mav 4,76,48
D 86,3072
mav 8,80,48
mav 0,84,52
R 36,11264
mav 4,88,52
mav 8,92,52
s_waitcnt lgkmcnt(2)
mav 0,96,24
R 40,12288
mav 4,100,24
mav 8,104,24
mav 0,108,28
R 44,13312
mav 4,112,28
mav 8,116,28
s_waitcnt lgkmcnt(2)
mav 0,120,32
R 48,14336
mav 4,124,32
mav 8,128,32
mav 0,132,36
R 52,15360
mav 4,136,36
mav 8,140,36
s_waitcnt lgkmcnt(2)
mav 0,144,40
R 24,16384
mav 4,148,40
mav 8,152,40
mav 0,156,44
R 28,17408
mav 4,160,44
mav 8,164,44
s_waitcnt lgkmcnt(2)
mav 0,168,48
R 32,18432
mav 4,172,48
mav 8,176,48
mav 0,180,52
R 36,19456
mav 4,184,52
mav 8,188,52
s_waitcnt lgkmcnt(2)
mav 0,192,24
R 40,20480
mav 4,196,24
mav 8,200,24
mav 0,204,28
R 44,21504
mav 4,208,28
mav 8,212,28
s_waitcnt lgkmcnt(2)
mav 0,216,32
R 48,22528
mav 4,220,32
mav 8,224,32
mav 0,228,36
R 52,23552
mav 4,232,36
mav 8,236,36
s_waitcnt lgkmcnt(2)
mav 0,240,40
R 24,24576
mav 4,244,40
mav 8,248,40
mav 0,252,44
R 28,25600
mvv 4,128,44
mvv 8,132,44
s_waitcnt lgkmcnt(2)
mvv 0,136,48
R 32,26624
mvv 4,140,48
mvv 8,144,48
mvv 0,148,52
R 36,27648
mvv 4,152,52
mvv 8,156,52
s_waitcnt lgkmcnt(2)
mvv 0,160,24
R 40,28672
mvv 4,164,24
mvv 8,168,24
mvv 0,172,28
R 44,29696
mvv 4,176,28
mvv 8,180,28
s_waitcnt lgkmcnt(2)
mvv 0,184,32
s_xor_b32 s50, s50, 0x10000
R 48,30720
mvv 4,188,32
mvv 8,192,32
mvv 0,196,36
R 52,31744
mvv 4,200,36
s_waitcnt vmcnt(0) lgkmcnt(2)
s_add_u32 s49, s49, 313344
mvv 8,204,36
s_barrier
mvv 0,208,40
s_add_u32 m0, s50, 0
R 24,32768
mvv 4,212,40
ds_read_b64 v[78:79], v89 offset:0
mvv 8,216,40
mvv 0,220,44
D 85,0
mvv 4,224,44
R 28,33792
mvv 8,228,44
s_waitcnt lgkmcnt(3)
mvv 0,232,48
R 32,34816
mvv 4,236,48
D 85,1024
mvv 8,240,48
mvv 0,244,52
R 36,35840
mvv 4,248,52
mvv 8,252,52
s_waitcnt lgkmcnt(2)
D 85,2048
v_mfma_f32_16x16x32_bf16 v[12:15], a[0:3], v[24:27], 0
R 40,36864
v_fma_f32 v104, v79, v56, v92
v_mfma_f32_16x16x32_bf16 v[16:19], a[4:7], v[24:27], 0
v_fma_f32 v0, v0, v78, v104
v_fma_f32 v105, v79, v57, v93
v_mfma_f32_16x16x32_bf16 v[20:23], a[8:11], v[24:27], 0
v_fma_f32 v1, v1, v78, v105
v_mul_f32_e64 v0, v0, |v0|
mav 12,12,28
D 85,3072
mav 16,16,28
s_add_u32 m0, s50, 4096
R 44,37888
v_mul_f32_e64 v1, v1, |v1|
mav 20,20,28
s_waitcnt lgkmcnt(2)
v_cvt_pk_bf16_f32 v0, v0, v1
v_pk_max_i16 v68, v0, 0
mav 12,24,32
R 48,38912
v_fma_f32 v104, v79, v58, v94
mav 16,28,32
D 86,0
mav 20,32,32
v_fma_f32 v2, v2, v78, v104
v_fma_f32 v105, v79, v59, v95
mav 12,36,36
R 52,39936
v_fma_f32 v3, v3, v78, v105
mav 16,40,36
v_mul_f32_e64 v2, v2, |v2|
v_mul_f32_e64 v3, v3, |v3|
mav 20,44,36
s_waitcnt lgkmcnt(2)
D 86,1024
mav 12,48,40
R 24,40960
v_cvt_pk_bf16_f32 v2, v2, v3
mav 16,52,40
v_pk_max_i16 v69, v2, 0
v_fma_f32 v104, v79, v60, v96
mav 20,56,40
v_fma_f32 v4, v4, v78, v104
v_fma_f32 v105, v79, v61, v97
mav 12,60,44
D 86,2048
mav 16,64,44
R 28,41984
v_fma_f32 v5, v5, v78, v105
mav 20,68,44
s_waitcnt lgkmcnt(2)
v_mul_f32_e64 v4, v4, |v4|
v_mul_f32_e64 v5, v5, |v5|
mav 12,72,48
R 32,43008
v_cvt_pk_bf16_f32 v4, v4, v5
mav 16,76,48
D 86,3072
mav 20,80,48
v_pk_max_i16 v70, v4, 0
v_fma_f32 v104, v79, v62, v98
mav 12,84,52
R 36,44032
v_fma_f32 v6, v6, v78, v104
mav 16,88,52
v_fma_f32 v105, v79, v63, v99
v_fma_f32 v7, v7, v78, v105
mav 20,92,52
s_waitcnt lgkmcnt(2)
v_mul_f32_e64 v6, v6, |v6|
v_mul_f32_e64 v7, v7, |v7|
mav 12,96,24
R 40,45056
v_cvt_pk_bf16_f32 v6, v6, v7
mav 16,100,24
v_pk_max_i16 v71, v6, 0
v_fma_f32 v104, v79, v64, v100
mav 20,104,24
v_fma_f32 v8, v8, v78, v104
v_fma_f32 v105, v79, v65, v101
mav 12,108,28
buffer_store_dwordx4 v[68:71], v83, s[36:39], s49 offen sc1
R 44,46080
v_fma_f32 v9, v9, v78, v105
mav 16,112,28
v_mul_f32_e64 v8, v8, |v8|
v_mul_f32_e64 v9, v9, |v9|
mav 20,116,28
s_waitcnt lgkmcnt(2)
v_cvt_pk_bf16_f32 v8, v8, v9
v_pk_max_i16 v72, v8, 0
mav 12,120,32
R 48,47104
v_pk_max_i16 v74, v8, 0
mav 16,124,32
v_fma_f32 v104, v79, v66, v102
v_fma_f32 v10, v10, v78, v104
mav 20,128,32
v_fma_f32 v105, v79, v67, v103
v_fma_f32 v11, v11, v78, v105
mav 12,132,36
R 52,48128
v_mul_f32_e64 v10, v10, |v10|
mav 16,136,36
v_mul_f32_e64 v11, v11, |v11|
v_cvt_pk_bf16_f32 v10, v10, v11
mav 20,140,36
s_waitcnt lgkmcnt(2)
v_pk_max_i16 v73, v10, 0
v_pk_max_i16 v75, v10, 0
mav 12,144,40
R 24,49152
mav 16,148,40
v_permlane16_swap_b32 v72, v74
mav 20,152,40
v_permlane16_swap_b32 v73, v75
mav 12,156,44
buffer_store_dwordx4 v[72:75], v84, s[36:39], s49 offen sc1
R 28,50176
mav 16,160,44
mav 20,164,44
s_waitcnt lgkmcnt(2)
mav 12,168,48
R 32,51200
mav 16,172,48
mav 20,176,48
mav 12,180,52
R 36,52224
mav 16,184,52
mav 20,188,52
s_waitcnt lgkmcnt(2)
mav 12,192,24
R 40,53248
mav 16,196,24
mav 20,200,24
mav 12,204,28
R 44,54272
mav 16,208,28
mav 20,212,28
s_waitcnt lgkmcnt(2)
mav 12,216,32
R 48,55296
mav 16,220,32
mav 20,224,32
mav 12,228,36
R 52,56320
mav 16,232,36
mav 20,236,36
s_waitcnt lgkmcnt(2)
mav 12,240,40
R 24,57344
mav 16,244,40
mav 20,248,40
mav 12,252,44
R 28,58368
mvv 16,128,44
mvv 20,132,44
s_waitcnt lgkmcnt(2)
mvv 12,136,48
R 32,59392
mvv 16,140,48
mvv 20,144,48
mvv 12,148,52
R 36,60416
mvv 16,152,52
mvv 20,156,52
s_waitcnt lgkmcnt(2)
mvv 12,160,24
s_mov_b32 s60, -30
.p2align 6
LF3_1:
mvv 16,164,24
R 40,61440
mvv 20,168,24
mvv 12,172,28
R 44,62464
mvv 16,176,28
v_add_u32 v76, 1, v76
mvv 20,180,28
s_waitcnt lgkmcnt(2)
mvv 12,184,32
R 48,63488
mvv 16,188,32
mvv 20,192,32
mvv 12,196,36
R 52,64512
v_xor_b32 v80, 0x10000, v80
mvv 16,200,36
s_waitcnt vmcnt(2) lgkmcnt(2)
s_add_u32 s49, s49, 313344
mvv 20,204,36
s_barrier
mvv 12,208,40
s_add_u32 m0, s50, 32768
R 24,0
mvv 16,212,40
ds_read_b64 v[78:79], v89 offset:128
mvv 20,216,40
mvv 12,220,44
D 85,0
mvv 16,224,44
R 28,1024
mvv 20,228,44
s_waitcnt lgkmcnt(3)
mvv 12,232,48
R 32,2048
mvv 16,236,48
D 85,1024
mvv 20,240,48
mvv 12,244,52
R 36,3072
mvv 16,248,52
mvv 20,252,52
s_waitcnt lgkmcnt(2)
D 85,2048
v_mfma_f32_16x16x32_bf16 v[0:3], a[0:3], v[24:27], 0
R 40,4096
v_fma_f32 v104, v79, v56, v92
v_mfma_f32_16x16x32_bf16 v[4:7], a[4:7], v[24:27], 0
v_fma_f32 v12, v12, v78, v104
v_fma_f32 v105, v79, v57, v93
v_mfma_f32_16x16x32_bf16 v[8:11], a[8:11], v[24:27], 0
v_fma_f32 v13, v13, v78, v105
v_mul_f32_e64 v12, v12, |v12|
mav 0,12,28
D 85,3072
mav 4,16,28
s_waitcnt vmcnt(4)
R 44,5120
s_add_u32 m0, s50, 36864
mav 8,20,28
s_waitcnt lgkmcnt(2)
v_mul_f32_e64 v13, v13, |v13|
v_cvt_pk_bf16_f32 v12, v12, v13
mav 0,24,32
R 48,6144
v_pk_max_i16 v68, v12, 0
mav 4,28,32
buffer_store_dword v76, v81, s[36:39], s52 offen sc1
mav 8,32,32
v_fma_f32 v104, v79, v58, v94
v_fma_f32 v14, v14, v78, v104
mav 0,36,36
R 52,7168
v_fma_f32 v105, v79, v59, v95
mav 4,40,36
v_fma_f32 v15, v15, v78, v105
v_mul_f32_e64 v14, v14, |v14|
mav 8,44,36
s_waitcnt lgkmcnt(2)
D 86,0
mav 0,48,40
R 24,8192
v_mul_f32_e64 v15, v15, |v15|
mav 4,52,40
v_cvt_pk_bf16_f32 v14, v14, v15
v_pk_max_i16 v69, v14, 0
mav 8,56,40
v_fma_f32 v104, v79, v60, v96
v_fma_f32 v16, v16, v78, v104
mav 0,60,44
D 86,1024
mav 4,64,44
R 28,9216
v_fma_f32 v105, v79, v61, v97
mav 8,68,44
s_waitcnt lgkmcnt(2)
v_fma_f32 v17, v17, v78, v105
v_mul_f32_e64 v16, v16, |v16|
mav 0,72,48
R 32,10240
v_mul_f32_e64 v17, v17, |v17|
mav 4,76,48
D 86,2048
mav 8,80,48
v_cvt_pk_bf16_f32 v16, v16, v17
v_pk_max_i16 v70, v16, 0
mav 0,84,52
R 36,11264
v_fma_f32 v104, v79, v62, v98
mav 4,88,52
v_fma_f32 v18, v18, v78, v104
v_fma_f32 v105, v79, v63, v99
mav 8,92,52
s_waitcnt lgkmcnt(2)
D 86,3072
mav 0,96,24
R 40,12288
v_fma_f32 v19, v19, v78, v105
mav 4,100,24
v_mul_f32_e64 v18, v18, |v18|
v_mul_f32_e64 v19, v19, |v19|
mav 8,104,24
v_cvt_pk_bf16_f32 v18, v18, v19
v_pk_max_i16 v71, v18, 0
mav 0,108,28
buffer_store_dwordx4 v[68:71], v83, s[36:39], s49 offen sc1
R 44,13312
v_fma_f32 v104, v79, v64, v100
mav 4,112,28
v_fma_f32 v20, v20, v78, v104
v_fma_f32 v105, v79, v65, v101
mav 8,116,28
s_waitcnt lgkmcnt(2)
v_fma_f32 v21, v21, v78, v105
v_mul_f32_e64 v20, v20, |v20|
mav 0,120,32
R 48,14336
v_mul_f32_e64 v21, v21, |v21|
mav 4,124,32
v_cvt_pk_bf16_f32 v20, v20, v21
v_pk_max_i16 v72, v20, 0
mav 8,128,32
v_pk_max_i16 v74, v20, 0
v_fma_f32 v104, v79, v66, v102
mav 0,132,36
R 52,15360
v_fma_f32 v22, v22, v78, v104
mav 4,136,36
v_fma_f32 v105, v79, v67, v103
v_fma_f32 v23, v23, v78, v105
mav 8,140,36
s_waitcnt lgkmcnt(2)
v_mul_f32_e64 v22, v22, |v22|
v_mul_f32_e64 v23, v23, |v23|
mav 0,144,40
R 24,16384
v_cvt_pk_bf16_f32 v22, v22, v23
mav 4,148,40
v_pk_max_i16 v73, v22, 0
v_pk_max_i16 v75, v22, 0
mav 8,152,40
v_permlane16_swap_b32 v72, v74
mav 0,156,44
R 28,17408
mav 4,160,44
v_permlane16_swap_b32 v73, v75
mav 8,164,44
s_waitcnt lgkmcnt(2)
mav 0,168,48
R 32,18432
mav 4,172,48
buffer_store_dwordx4 v[72:75], v84, s[36:39], s49 offen sc1
mav 8,176,48
mav 0,180,52
R 36,19456
mav 4,184,52
mav 8,188,52
s_waitcnt lgkmcnt(2)
mav 0,192,24
R 40,20480
mav 4,196,24
mav 8,200,24
mav 0,204,28
R 44,21504
mav 4,208,28
mav 8,212,28
s_waitcnt lgkmcnt(2)
mav 0,216,32
R 48,22528
mav 4,220,32
mav 8,224,32
mav 0,228,36
R 52,23552
mav 4,232,36
mav 8,236,36
s_waitcnt lgkmcnt(2)
mav 0,240,40
R 24,24576
mav 4,244,40
mav 8,248,40
mav 0,252,44
R 28,25600
mvv 4,128,44
mvv 8,132,44
s_waitcnt lgkmcnt(2)
mvv 0,136,48
R 32,26624
mvv 4,140,48
mvv 8,144,48
mvv 0,148,52
R 36,27648
mvv 4,152,52
mvv 8,156,52
s_waitcnt lgkmcnt(2)
mvv 0,160,24
R 40,28672
mvv 4,164,24
mvv 8,168,24
mvv 0,172,28
LsF14:
s_cbranch_execz LwF
R 44,29696
mvv 4,176,28
v_add_u32 v76, 1, v76
mvv 8,180,28
s_waitcnt lgkmcnt(2)
mvv 0,184,32
s_xor_b32 s50, s50, 0x10000
R 48,30720
mvv 4,188,32
mvv 8,192,32
mvv 0,196,36
R 52,31744
mvv 4,200,36
s_waitcnt vmcnt(2) lgkmcnt(2)
s_add_u32 s49, s49, 313344
mvv 8,204,36
s_barrier
mvv 0,208,40
s_add_u32 m0, s50, 0
R 24,32768
mvv 4,212,40
ds_read_b64 v[78:79], v89 offset:0
mvv 8,216,40
mvv 0,220,44
D 85,0
mvv 4,224,44
R 28,33792
mvv 8,228,44
s_waitcnt lgkmcnt(3)
mvv 0,232,48
R 32,34816
mvv 4,236,48
D 85,1024
mvv 8,240,48
mvv 0,244,52
R 36,35840
mvv 4,248,52
mvv 8,252,52
s_waitcnt lgkmcnt(2)
D 85,2048
v_mfma_f32_16x16x32_bf16 v[12:15], a[0:3], v[24:27], 0
R 40,36864
v_fma_f32 v104, v79, v56, v92
v_mfma_f32_16x16x32_bf16 v[16:19], a[4:7], v[24:27], 0
v_fma_f32 v0, v0, v78, v104
v_fma_f32 v105, v79, v57, v93
v_mfma_f32_16x16x32_bf16 v[20:23], a[8:11], v[24:27], 0
v_fma_f32 v1, v1, v78, v105
v_mul_f32_e64 v0, v0, |v0|
mav 12,12,28
D 85,3072
mav 16,16,28
s_waitcnt vmcnt(4)
R 44,37888
s_add_u32 m0, s50, 4096
mav 20,20,28
s_waitcnt lgkmcnt(2)
v_mul_f32_e64 v1, v1, |v1|
v_cvt_pk_bf16_f32 v0, v0, v1
mav 12,24,32
R 48,38912
v_pk_max_i16 v68, v0, 0
mav 16,28,32
buffer_store_dword v76, v81, s[36:39], s52 offen sc1
mav 20,32,32
v_fma_f32 v104, v79, v58, v94
v_fma_f32 v2, v2, v78, v104
mav 12,36,36
R 52,39936
v_fma_f32 v105, v79, v59, v95
mav 16,40,36
v_fma_f32 v3, v3, v78, v105
v_mul_f32_e64 v2, v2, |v2|
mav 20,44,36
s_waitcnt lgkmcnt(2)
D 86,0
mav 12,48,40
R 24,40960
v_mul_f32_e64 v3, v3, |v3|
mav 16,52,40
v_cvt_pk_bf16_f32 v2, v2, v3
v_pk_max_i16 v69, v2, 0
mav 20,56,40
v_fma_f32 v104, v79, v60, v96
v_fma_f32 v4, v4, v78, v104
mav 12,60,44
D 86,1024
mav 16,64,44
R 28,41984
v_fma_f32 v105, v79, v61, v97
mav 20,68,44
s_waitcnt lgkmcnt(2)
v_fma_f32 v5, v5, v78, v105
v_mul_f32_e64 v4, v4, |v4|
mav 12,72,48
R 32,43008
v_mul_f32_e64 v5, v5, |v5|
mav 16,76,48
D 86,2048
mav 20,80,48
v_cvt_pk_bf16_f32 v4, v4, v5
v_pk_max_i16 v70, v4, 0
mav 12,84,52
R 36,44032
v_fma_f32 v104, v79, v62, v98
mav 16,88,52
v_fma_f32 v6, v6, v78, v104
v_fma_f32 v105, v79, v63, v99
mav 20,92,52
s_waitcnt lgkmcnt(2)
D 86,3072
mav 12,96,24
R 40,45056
v_fma_f32 v7, v7, v78, v105
mav 16,100,24
v_mul_f32_e64 v6, v6, |v6|
v_mul_f32_e64 v7, v7, |v7|
mav 20,104,24
v_cvt_pk_bf16_f32 v6, v6, v7
v_pk_max_i16 v71, v6, 0
mav 12,108,28
buffer_store_dwordx4 v[68:71], v83, s[36:39], s49 offen sc1
R 44,46080
v_fma_f32 v104, v79, v64, v100
mav 16,112,28
v_fma_f32 v8, v8, v78, v104
v_fma_f32 v105, v79, v65, v101
mav 20,116,28
s_waitcnt lgkmcnt(2)
v_fma_f32 v9, v9, v78, v105
v_mul_f32_e64 v8, v8, |v8|
mav 12,120,32
R 48,47104
v_mul_f32_e64 v9, v9, |v9|
mav 16,124,32
v_cvt_pk_bf16_f32 v8, v8, v9
v_pk_max_i16 v72, v8, 0
mav 20,128,32
v_pk_max_i16 v74, v8, 0
v_fma_f32 v104, v79, v66, v102
mav 12,132,36
R 52,48128
v_fma_f32 v10, v10, v78, v104
mav 16,136,36
v_fma_f32 v105, v79, v67, v103
v_fma_f32 v11, v11, v78, v105
mav 20,140,36
s_waitcnt lgkmcnt(2)
v_mul_f32_e64 v10, v10, |v10|
v_mul_f32_e64 v11, v11, |v11|
mav 12,144,40
R 24,49152
v_cvt_pk_bf16_f32 v10, v10, v11
mav 16,148,40
v_pk_max_i16 v73, v10, 0
v_pk_max_i16 v75, v10, 0
mav 20,152,40
v_permlane16_swap_b32 v72, v74
mav 12,156,44
R 28,50176
mav 16,160,44
v_permlane16_swap_b32 v73, v75
mav 20,164,44
s_waitcnt lgkmcnt(2)
mav 12,168,48
R 32,51200
mav 16,172,48
buffer_store_dwordx4 v[72:75], v84, s[36:39], s49 offen sc1
mav 20,176,48
mav 12,180,52
R 36,52224
mav 16,184,52
mav 20,188,52
s_waitcnt lgkmcnt(2)
mav 12,192,24
R 40,53248
mav 16,196,24
mav 20,200,24
mav 12,204,28
R 44,54272
mav 16,208,28
mav 20,212,28
s_waitcnt lgkmcnt(2)
mav 12,216,32
R 48,55296
mav 16,220,32
mav 20,224,32
mav 12,228,36
R 52,56320
mav 16,232,36
mav 20,236,36
s_waitcnt lgkmcnt(2)
mav 12,240,40
R 24,57344
mav 16,244,40
mav 20,248,40
mav 12,252,44
R 28,58368
mvv 16,128,44
mvv 20,132,44
s_waitcnt lgkmcnt(2)
mvv 12,136,48
R 32,59392
mvv 16,140,48
mvv 20,144,48
mvv 12,148,52
R 36,60416
mvv 16,152,52
mvv 20,156,52
s_waitcnt lgkmcnt(2)
s_add_u32 s60, s60, 1
mvv 12,160,24
s_cbranch_scc0 LF3_1
mvv 16,164,24
R 40,61440
mvv 20,168,24
mvv 12,172,28
R 44,62464
mvv 16,176,28
v_add_u32 v76, 1, v76
mvv 20,180,28
s_waitcnt lgkmcnt(2)
mvv 12,184,32
R 48,63488
mvv 16,188,32
mvv 20,192,32
mvv 12,196,36
R 52,64512
v_xor_b32 v80, 0x10000, v80
mvv 16,200,36
s_waitcnt vmcnt(2) lgkmcnt(2)
s_add_u32 s49, s49, 313344
mvv 20,204,36
s_barrier
mvv 12,208,40
s_add_u32 m0, s50, 32768
R 24,0
mvv 16,212,40
ds_read_b64 v[78:79], v89 offset:128
mvv 20,216,40
mvv 12,220,44
D 85,0
mvv 16,224,44
R 28,1024
mvv 20,228,44
s_waitcnt lgkmcnt(3)
mvv 12,232,48
R 32,2048
mvv 16,236,48
D 85,1024
mvv 20,240,48
mvv 12,244,52
R 36,3072
mvv 16,248,52
mvv 20,252,52
s_waitcnt lgkmcnt(2)
D 85,2048
v_mfma_f32_16x16x32_bf16 v[0:3], a[0:3], v[24:27], 0
R 40,4096
v_fma_f32 v104, v79, v56, v92
v_mfma_f32_16x16x32_bf16 v[4:7], a[4:7], v[24:27], 0
v_fma_f32 v12, v12, v78, v104
v_fma_f32 v105, v79, v57, v93
v_mfma_f32_16x16x32_bf16 v[8:11], a[8:11], v[24:27], 0
v_fma_f32 v13, v13, v78, v105
v_mul_f32_e64 v12, v12, |v12|
mav 0,12,28
D 85,3072
mav 4,16,28
s_waitcnt vmcnt(4)
R 44,5120
s_add_u32 m0, s50, 36864
mav 8,20,28
s_waitcnt lgkmcnt(2)
v_mul_f32_e64 v13, v13, |v13|
v_cvt_pk_bf16_f32 v12, v12, v13
mav 0,24,32
R 48,6144
v_pk_max_i16 v68, v12, 0
mav 4,28,32
buffer_store_dword v76, v81, s[36:39], s52 offen sc1
mav 8,32,32
v_fma_f32 v104, v79, v58, v94
v_fma_f32 v14, v14, v78, v104
mav 0,36,36
R 52,7168
v_fma_f32 v105, v79, v59, v95
mav 4,40,36
v_fma_f32 v15, v15, v78, v105
v_mul_f32_e64 v14, v14, |v14|
mav 8,44,36
s_waitcnt lgkmcnt(2)
D 86,0
mav 0,48,40
R 24,8192
v_mul_f32_e64 v15, v15, |v15|
mav 4,52,40
v_cvt_pk_bf16_f32 v14, v14, v15
v_pk_max_i16 v69, v14, 0
mav 8,56,40
v_fma_f32 v104, v79, v60, v96
v_fma_f32 v16, v16, v78, v104
mav 0,60,44
D 86,1024
mav 4,64,44
R 28,9216
v_fma_f32 v105, v79, v61, v97
mav 8,68,44
s_waitcnt lgkmcnt(2)
v_fma_f32 v17, v17, v78, v105
v_mul_f32_e64 v16, v16, |v16|
mav 0,72,48
R 32,10240
v_mul_f32_e64 v17, v17, |v17|
mav 4,76,48
D 86,2048
mav 8,80,48
v_cvt_pk_bf16_f32 v16, v16, v17
v_pk_max_i16 v70, v16, 0
mav 0,84,52
R 36,11264
v_fma_f32 v104, v79, v62, v98
mav 4,88,52
v_fma_f32 v18, v18, v78, v104
v_fma_f32 v105, v79, v63, v99
mav 8,92,52
s_waitcnt lgkmcnt(2)
D 86,3072
mav 0,96,24
R 40,12288
v_fma_f32 v19, v19, v78, v105
mav 4,100,24
v_mul_f32_e64 v18, v18, |v18|
v_mul_f32_e64 v19, v19, |v19|
mav 8,104,24
v_cvt_pk_bf16_f32 v18, v18, v19
v_pk_max_i16 v71, v18, 0
mav 0,108,28
buffer_store_dwordx4 v[68:71], v83, s[36:39], s49 offen sc1
R 44,13312
v_fma_f32 v104, v79, v64, v100
mav 4,112,28
v_fma_f32 v20, v20, v78, v104
v_fma_f32 v105, v79, v65, v101
mav 8,116,28
s_waitcnt lgkmcnt(2)
v_fma_f32 v21, v21, v78, v105
v_mul_f32_e64 v20, v20, |v20|
mav 0,120,32
R 48,14336
v_mul_f32_e64 v21, v21, |v21|
mav 4,124,32
v_cvt_pk_bf16_f32 v20, v20, v21
v_pk_max_i16 v72, v20, 0
mav 8,128,32
v_pk_max_i16 v74, v20, 0
v_fma_f32 v104, v79, v66, v102
mav 0,132,36
R 52,15360
v_fma_f32 v22, v22, v78, v104
mav 4,136,36
v_fma_f32 v105, v79, v67, v103
v_fma_f32 v23, v23, v78, v105
mav 8,140,36
s_waitcnt lgkmcnt(2)
v_mul_f32_e64 v22, v22, |v22|
v_mul_f32_e64 v23, v23, |v23|
mav 0,144,40
R 24,16384
v_cvt_pk_bf16_f32 v22, v22, v23
mav 4,148,40
v_pk_max_i16 v73, v22, 0
v_pk_max_i16 v75, v22, 0
mav 8,152,40
v_permlane16_swap_b32 v72, v74
mav 0,156,44
R 28,17408
mav 4,160,44
v_permlane16_swap_b32 v73, v75
mav 8,164,44
s_waitcnt lgkmcnt(2)
mav 0,168,48
R 32,18432
mav 4,172,48
buffer_store_dwordx4 v[72:75], v84, s[36:39], s49 offen sc1
mav 8,176,48
mav 0,180,52
R 36,19456
mav 4,184,52
mav 8,188,52
s_waitcnt lgkmcnt(2)
mav 0,192,24
R 40,20480
mav 4,196,24
mav 8,200,24
mav 0,204,28
R 44,21504
mav 4,208,28
mav 8,212,28
s_waitcnt lgkmcnt(2)
mav 0,216,32
R 48,22528
mav 4,220,32
mav 8,224,32
mav 0,228,36
R 52,23552
mav 4,232,36
mav 8,236,36
s_waitcnt lgkmcnt(2)
mav 0,240,40
R 24,24576
mav 4,244,40
mav 8,248,40
mav 0,252,44
R 28,25600
mvv 4,128,44
mvv 8,132,44
s_waitcnt lgkmcnt(2)
mvv 0,136,48
R 32,26624
mvv 4,140,48
mvv 8,144,48
mvv 0,148,52
R 36,27648
mvv 4,152,52
mvv 8,156,52
s_waitcnt lgkmcnt(2)
mvv 0,160,24
R 40,28672
mvv 4,164,24
mvv 8,168,24
mvv 0,172,28
R 44,29696
mvv 4,176,28
v_add_u32 v76, 1, v76
mvv 8,180,28
s_waitcnt lgkmcnt(2)
mvv 0,184,32
R 48,30720
mvv 4,188,32
mvv 8,192,32
mvv 0,196,36
R 52,31744
mvv 4,200,36
s_waitcnt vmcnt(2) lgkmcnt(2)
s_add_u32 s49, s49, 313344
mvv 8,204,36
s_barrier
mvv 0,208,40
R 24,32768
mvv 4,212,40
ds_read_b64 v[78:79], v89 offset:0
mvv 8,216,40
mvv 0,220,44
R 28,33792
mvv 4,224,44
mvv 8,228,44
s_waitcnt lgkmcnt(3)
mvv 0,232,48
R 32,34816
mvv 4,236,48
mvv 8,240,48
mvv 0,244,52
R 36,35840
mvv 4,248,52
mvv 8,252,52
s_waitcnt lgkmcnt(2)
v_mfma_f32_16x16x32_bf16 v[12:15], a[0:3], v[24:27], 0
R 40,36864
v_fma_f32 v104, v79, v56, v92
v_mfma_f32_16x16x32_bf16 v[16:19], a[4:7], v[24:27], 0
v_fma_f32 v0, v0, v78, v104
v_fma_f32 v105, v79, v57, v93
v_mfma_f32_16x16x32_bf16 v[20:23], a[8:11], v[24:27], 0
v_fma_f32 v1, v1, v78, v105
v_mul_f32_e64 v0, v0, |v0|
mav 12,12,28
R 44,37888
v_mul_f32_e64 v1, v1, |v1|
mav 16,16,28
s_waitcnt vmcnt(0)
v_cvt_pk_bf16_f32 v0, v0, v1
v_pk_max_i16 v68, v0, 0
mav 20,20,28
s_waitcnt lgkmcnt(2)
v_fma_f32 v104, v79, v58, v94
v_fma_f32 v2, v2, v78, v104
mav 12,24,32
R 48,38912
v_fma_f32 v105, v79, v59, v95
mav 16,28,32
buffer_store_dword v76, v81, s[36:39], s52 offen sc1
mav 20,32,32
v_fma_f32 v3, v3, v78, v105
v_mul_f32_e64 v2, v2, |v2|
mav 12,36,36
R 52,39936
v_mul_f32_e64 v3, v3, |v3|
mav 16,40,36
v_cvt_pk_bf16_f32 v2, v2, v3
v_pk_max_i16 v69, v2, 0
mav 20,44,36
s_waitcnt lgkmcnt(2)
v_fma_f32 v104, v79, v60, v96
v_fma_f32 v4, v4, v78, v104
mav 12,48,40
R 24,40960
v_fma_f32 v105, v79, v61, v97
mav 16,52,40
v_fma_f32 v5, v5, v78, v105
v_mul_f32_e64 v4, v4, |v4|
mav 20,56,40
v_mul_f32_e64 v5, v5, |v5|
v_cvt_pk_bf16_f32 v4, v4, v5
mav 12,60,44
R 28,41984
v_pk_max_i16 v70, v4, 0
mav 16,64,44
v_fma_f32 v104, v79, v62, v98
v_fma_f32 v6, v6, v78, v104
mav 20,68,44
s_waitcnt lgkmcnt(2)
v_fma_f32 v105, v79, v63, v99
v_fma_f32 v7, v7, v78, v105
mav 12,72,48
R 32,43008
v_mul_f32_e64 v6, v6, |v6|
mav 16,76,48
v_mul_f32_e64 v7, v7, |v7|
v_cvt_pk_bf16_f32 v6, v6, v7
mav 20,80,48
v_pk_max_i16 v71, v6, 0
v_fma_f32 v104, v79, v64, v100
mav 12,84,52
R 36,44032
v_fma_f32 v8, v8, v78, v104
mav 16,88,52
v_fma_f32 v105, v79, v65, v101
v_fma_f32 v9, v9, v78, v105
LsF15:
s_cbranch_execz LwF
mav 20,92,52
buffer_store_dwordx4 v[68:71], v83, s[36:39], s49 offen sc1
s_waitcnt lgkmcnt(2)
v_mul_f32_e64 v8, v8, |v8|
v_mul_f32_e64 v9, v9, |v9|
mav 12,96,24
R 40,45056
v_cvt_pk_bf16_f32 v8, v8, v9
mav 16,100,24
v_pk_max_i16 v72, v8, 0
v_pk_max_i16 v74, v8, 0
mav 20,104,24
v_fma_f32 v104, v79, v66, v102
v_fma_f32 v10, v10, v78, v104
mav 12,108,28
R 44,46080
v_fma_f32 v105, v79, v67, v103
mav 16,112,28
v_fma_f32 v11, v11, v78, v105
v_mul_f32_e64 v10, v10, |v10|
mav 20,116,28
s_waitcnt lgkmcnt(2)
v_mul_f32_e64 v11, v11, |v11|
v_cvt_pk_bf16_f32 v10, v10, v11
mav 12,120,32
R 48,47104
v_pk_max_i16 v73, v10, 0
mav 16,124,32
v_pk_max_i16 v75, v10, 0
mav 20,128,32
v_permlane16_swap_b32 v72, v74
mav 12,132,36
R 52,48128
mav 16,136,36
v_permlane16_swap_b32 v73, v75
mav 20,140,36
buffer_store_dwordx4 v[72:75], v84, s[36:39], s49 offen sc1
s_waitcnt lgkmcnt(2)
mav 12,144,40
R 24,49152
mav 16,148,40
mav 20,152,40
mav 12,156,44
R 28,50176
mav 16,160,44
mav 20,164,44
s_waitcnt lgkmcnt(2)
mav 12,168,48
R 32,51200
mav 16,172,48
mav 20,176,48
mav 12,180,52
R 36,52224
mav 16,184,52
mav 20,188,52
s_waitcnt lgkmcnt(2)
mav 12,192,24
R 40,53248
mav 16,196,24
mav 20,200,24
mav 12,204,28
R 44,54272
mav 16,208,28
mav 20,212,28
s_waitcnt lgkmcnt(2)
mav 12,216,32
R 48,55296
mav 16,220,32
mav 20,224,32
mav 12,228,36
R 52,56320
mav 16,232,36
mav 20,236,36
s_waitcnt lgkmcnt(2)
mav 12,240,40
R 24,57344
mav 16,244,40
mav 20,248,40
mav 12,252,44
R 28,58368
mvv 16,128,44
mvv 20,132,44
s_waitcnt lgkmcnt(2)
mvv 12,136,48
R 32,59392
mvv 16,140,48
mvv 20,144,48
mvv 12,148,52
R 36,60416
mvv 16,152,52
mvv 20,156,52
s_waitcnt lgkmcnt(2)
mvv 12,160,24
mvv 16,164,24
R 40,61440
mvv 20,168,24
mvv 12,172,28
R 44,62464
mvv 16,176,28
v_add_u32 v76, 1, v76
mvv 20,180,28
s_waitcnt lgkmcnt(2)
mvv 12,184,32
R 48,63488
mvv 16,188,32
mvv 20,192,32
mvv 12,196,36
R 52,64512
mvv 16,200,36
s_waitcnt lgkmcnt(2)
s_add_u32 s49, s49, 313344
mvv 20,204,36
s_barrier
mvv 12,208,40
mvv 16,212,40
ds_read_b64 v[78:79], v89 offset:128
mvv 20,216,40
mvv 12,220,44
mvv 16,224,44
mvv 20,228,44
s_waitcnt lgkmcnt(1)
mvv 12,232,48
mvv 16,236,48
mvv 20,240,48
mvv 12,244,52
mvv 16,248,52
mvv 20,252,52
s_waitcnt lgkmcnt(0)
v_fma_f32 v104, v79, v56, v92
s_nop 3
v_fma_f32 v12, v12, v78, v104
v_fma_f32 v105, v79, v57, v93
v_fma_f32 v13, v13, v78, v105
v_mul_f32_e64 v12, v12, |v12|
v_mul_f32_e64 v13, v13, |v13|
v_cvt_pk_bf16_f32 v12, v12, v13
v_pk_max_i16 v68, v12, 0
v_fma_f32 v104, v79, v58, v94
v_fma_f32 v14, v14, v78, v104
v_fma_f32 v105, v79, v59, v95
v_fma_f32 v15, v15, v78, v105
v_mul_f32_e64 v14, v14, |v14|
v_mul_f32_e64 v15, v15, |v15|
v_cvt_pk_bf16_f32 v14, v14, v15
v_pk_max_i16 v69, v14, 0
v_fma_f32 v104, v79, v60, v96
v_fma_f32 v16, v16, v78, v104
v_fma_f32 v105, v79, v61, v97
v_fma_f32 v17, v17, v78, v105
v_mul_f32_e64 v16, v16, |v16|
v_mul_f32_e64 v17, v17, |v17|
v_cvt_pk_bf16_f32 v16, v16, v17
v_pk_max_i16 v70, v16, 0
v_fma_f32 v104, v79, v62, v98
v_fma_f32 v18, v18, v78, v104
v_fma_f32 v105, v79, v63, v99
v_fma_f32 v19, v19, v78, v105
v_mul_f32_e64 v18, v18, |v18|
v_mul_f32_e64 v19, v19, |v19|
v_cvt_pk_bf16_f32 v18, v18, v19
v_pk_max_i16 v71, v18, 0
buffer_store_dwordx4 v[68:71], v83, s[36:39], s49 offen sc1
v_fma_f32 v104, v79, v64, v100
v_fma_f32 v20, v20, v78, v104
v_fma_f32 v105, v79, v65, v101
v_fma_f32 v21, v21, v78, v105
v_mul_f32_e64 v20, v20, |v20|
v_mul_f32_e64 v21, v21, |v21|
v_cvt_pk_bf16_f32 v20, v20, v21
v_pk_max_i16 v72, v20, 0
v_pk_max_i16 v74, v20, 0
v_fma_f32 v104, v79, v66, v102
v_fma_f32 v22, v22, v78, v104
v_fma_f32 v105, v79, v67, v103
v_fma_f32 v23, v23, v78, v105
v_mul_f32_e64 v22, v22, |v22|
v_mul_f32_e64 v23, v23, |v23|
v_cvt_pk_bf16_f32 v22, v22, v23
v_pk_max_i16 v73, v22, 0
v_pk_max_i16 v75, v22, 0
v_permlane16_swap_b32 v72, v74
s_nop 0
v_permlane16_swap_b32 v73, v75
buffer_store_dwordx4 v[72:75], v84, s[36:39], s49 offen sc1
s_waitcnt vmcnt(2)
buffer_store_dword v76, v81, s[36:39], s52 offen sc1
v_add_u32 v76, 37, v76
s_waitcnt vmcnt(1)
buffer_store_dword v76, v81, s[36:39], s52 offen sc1
s_endpgm
Lr4:
s_cmp_eq_u32 s60, 4
s_cbranch_scc0 Lr5
s_waitcnt vmcnt(0)
v_accvgpr_read_b32 v65, a255
v_mov_b32 v64, 0x100
v_add_u32 v65, 0x9e3779b9, v65
buffer_store_dwordx2 v[64:65], v81, s[36:39], s52 offen sc1
v_add_u32 v87, v87, v64
s_sleep 32
s_sleep 32
s_sleep 32
E 0,65536
E 1,69632
s_add_u32 s51, s51, 229376
E 0,73728
E 1,77824
s_add_u32 s51, s51, 229376
E 0,81920
E 1,86016
s_add_u32 s51, s51, 229376
E 0,90112
E 1,94208
s_add_u32 s51, s51, 229376
s_mov_b64 exec, 0
s_cmp_eq_u32 s53, 0
s_cbranch_scc1 LsE0
s_cmp_eq_u32 s53, 1024
s_cbranch_scc1 LsE1
s_cmp_eq_u32 s53, 2048
s_cbranch_scc1 LsE2
s_branch LsE3
LwE:
s_mov_b64 exec, -1
v_mov_b32 v16, s64
v_mov_b32 v17, s64
v_mov_b32 v18, s64
v_mov_b32 v19, s64
s_waitcnt vmcnt(4)
s_barrier
v_add_u32 v4, 65536, v4
v_add_u32 v5, 65536, v5
Ta 0,4,0
Ta 4,5,0
Ta 8,4,8192
Ta 12,5,8192
E 0,98304
E 1,102400
s_add_u32 s51, s51, 229376
E 0,106496
E 1,110592
s_add_u32 s51, s51, 229376
E 0,114688
E 1,118784
s_add_u32 s51, s51, 229376
E 0,122880
E 1,126976
s_add_u32 s51, s51, 229376
s_cmp_ge_u32 s51, s57
s_cselect_b32 s60, 7340032, 0
s_sub_u32 s51, s51, s60
E 0,131072
E 1,135168
s_add_u32 s51, s51, 229376
E 0,139264
E 1,143360
s_add_u32 s51, s51, 229376
E 0,147456
E 1,151552
s_add_u32 s51, s51, 229376
E 0,155648
E 1,159744
s_add_u32 s51, s51, 229376
s_waitcnt lgkmcnt(0)
s_waitcnt vmcnt(16)
s_barrier
E 0,65536
Ta 16,4,16384
v_mfma_f32_16x16x32_bf16 v[48:51], a[0:3], v[16:19], 0
Ta 20,5,16384
E 1,69632
s_add_u32 s51, s51, 229376
v_mfma_f32_16x16x32_bf16 v[52:55], a[4:7], v[16:19], 0
Ta 24,4,24576
Ta 28,5,24576
E 0,73728
mav 48,8,16
E 1,77824
s_add_u32 s51, s51, 229376
mav 52,12,16
s_waitcnt lgkmcnt(0)
s_waitcnt vmcnt(16)
s_barrier
E 0,81920
Ta 32,4,32768
mav 48,16,16
Ta 36,5,32768
E 1,86016
s_add_u32 s51, s51, 229376
mav 52,20,16
Ta 40,4,40960
Ta 44,5,40960
E 0,90112
mav 48,24,16
E 1,94208
s_add_u32 s51, s51, 229376
s_cmp_ge_u32 s51, s57
s_cselect_b32 s60, 7340032, 0
s_sub_u32 s51, s51, s60
mav 52,28,16
s_waitcnt lgkmcnt(0)
s_waitcnt vmcnt(16)
s_barrier
E 0,98304
Ta 48,4,49152
mav 48,32,16
Ta 52,5,49152
E 1,102400
s_add_u32 s51, s51, 229376
mav 52,36,16
Ta 56,4,57344
Ta 60,5,57344
E 0,106496
mav 48,40,16
E 1,110592
s_add_u32 s51, s51, 229376
mav 52,44,16
s_waitcnt lgkmcnt(0)
s_waitcnt vmcnt(16)
s_barrier
v_add_u32 v4, 65536, v4
v_add_u32 v5, 65536, v5
E 0,114688
Ta 64,4,0
mav 48,48,16
Ta 68,5,0
E 1,118784
s_add_u32 s51, s51, 229376
mav 52,52,16
Ta 72,4,8192
Ta 76,5,8192
E 0,122880
mav 48,56,16
E 1,126976
s_add_u32 s51, s51, 229376
mav 52,60,16
s_waitcnt lgkmcnt(0)
s_waitcnt vmcnt(16)
s_barrier
E 0,131072
Ta 80,4,16384
mav 48,64,16
Ta 84,5,16384
E 1,135168
s_add_u32 s51, s51, 229376
mav 52,68,16
Ta 88,4,24576
Ta 92,5,24576
E 0,139264
mav 48,72,16
E 1,143360
s_add_u32 s51, s51, 229376
mav 52,76,16
s_waitcnt lgkmcnt(0)
s_waitcnt vmcnt(16)
s_barrier
v_add_u32 v4, -65536, v4
v_add_u32 v5, -65536, v5
E 0,147456
Ta 96,4,0
mav 48,80,16
Ta 100,5,0
E 1,151552
s_add_u32 s51, s51, 229376
mav 52,84,16
Ta 104,4,8192
Ta 108,5,8192
E 0,155648
mav 48,88,16
E 1,159744
s_add_u32 s51, s51, 229376
s_cmp_ge_u32 s51, s57
s_cselect_b32 s60, 7340032, 0
s_sub_u32 s51, s51, s60
mav 52,92,16
s_waitcnt lgkmcnt(0)
s_waitcnt vmcnt(16)
s_barrier
E 0,65536
Ta 112,4,16384
mav 48,96,16
Ta 116,5,16384
E 1,69632
s_add_u32 s51, s51, 229376
mav 52,100,16
Ta 120,4,24576
Ta 124,5,24576
E 0,73728
mav 48,104,16
E 1,77824
s_add_u32 s51, s51, 229376
mav 52,108,16
s_waitcnt lgkmcnt(0)
s_waitcnt vmcnt(16)
s_barrier
E 0,81920
Ta 128,4,32768
mav 48,112,16
Ta 132,5,32768
E 1,86016
s_add_u32 s51, s51, 229376
mav 52,116,16
Ta 136,4,40960
Ta 140,5,40960
E 0,90112
mav 48,120,16
E 1,94208
s_add_u32 s51, s51, 229376
mav 52,124,16
s_waitcnt lgkmcnt(0)
s_waitcnt vmcnt(16)
s_barrier
E 0,98304
Ta 144,4,49152
mav 48,128,16
Ta 148,5,49152
E 1,102400
s_add_u32 s51, s51, 229376
mav 52,132,16
Ta 152,4,57344
Ta 156,5,57344
E 0,106496
mav 48,136,16
E 1,110592
LsE0:
s_cbranch_execz LsE4
s_add_u32 s51, s51, 229376
mav 52,140,16
s_waitcnt lgkmcnt(0)
s_waitcnt vmcnt(16)
s_barrier
v_add_u32 v4, 65536, v4
v_add_u32 v5, 65536, v5
E 0,114688
Ta 160,4,0
mav 48,144,16
Ta 164,5,0
E 1,118784
s_add_u32 s51, s51, 229376
mav 52,148,16
Ta 168,4,8192
Ta 172,5,8192
E 0,122880
mav 48,152,16
E 1,126976
s_add_u32 s51, s51, 229376
s_cmp_ge_u32 s51, s57
s_cselect_b32 s60, 7340032, 0
s_sub_u32 s51, s51, s60
mav 52,156,16
s_waitcnt lgkmcnt(0)
s_waitcnt vmcnt(16)
s_barrier
Ta 176,4,16384
mav 48,160,16
Ta 180,5,16384
mav 52,164,16
Ta 184,4,24576
Ta 188,5,24576
mav 48,168,16
mav 52,172,16
s_waitcnt lgkmcnt(0)
s_waitcnt vmcnt(12)
s_barrier
v_add_u32 v4, -65536, v4
v_add_u32 v5, -65536, v5
Ta 192,4,0
mav 48,176,16
Ta 196,5,0
mav 52,180,16
Ta 200,4,8192
Ta 204,5,8192
mav 48,184,16
mav 52,188,16
s_waitcnt lgkmcnt(0)
s_waitcnt vmcnt(8)
s_barrier
Ta 208,4,16384
mav 48,192,16
Ta 212,5,16384
mav 52,196,16
Ta 216,4,24576
Ta 220,5,24576
mav 48,200,16
mav 52,204,16
s_waitcnt lgkmcnt(0)
s_waitcnt vmcnt(4)
s_barrier
Ta 224,4,32768
mav 48,208,16
Ta 228,5,32768
mav 52,212,16
Ta 232,4,40960
Ta 236,5,40960
mav 48,216,16
mav 52,220,16
s_waitcnt lgkmcnt(0)
s_waitcnt vmcnt(0)
s_barrier
Ta 240,4,49152
mav 48,224,16
Ta 244,5,49152
mav 52,228,16
Ta 248,4,57344
Ta 252,5,57344
mav 48,232,16
mav 52,236,16
s_waitcnt lgkmcnt(0)
mav 48,240,16
mav 52,244,16
mav 48,248,16
mav 52,252,16
s_waitcnt vmcnt(0)
s_barrier
s_nop 7
s_and_b32 s60, s48, 3
s_cmp_eq_u32 s60, 1
s_cbranch_scc1 LvE1
s_cmp_eq_u32 s60, 2
s_cbranch_scc1 LvE2
s_cmp_eq_u32 s60, 3
s_cbranch_scc1 LvE3
LvE0:
LsE1:
s_cbranch_execz LsE5
buffer_load_dwordx2 v[70:71], v86, s[36:39], 0 offen sc1
s_waitcnt vmcnt(0)
ds_write_b32 v82, v70
v_sub_u32 v70, v70, v87
v_xor_b32 v71, v71, v65
v_cmp_lt_u64 vcc, s[62:63], v[70:71]
s_waitcnt lgkmcnt(0)
s_cbranch_vccnz CE0_1
BE0_1:
s_add_u32 s49, s49, 313344
s_barrier
s_add_u32 m0, s50, 0
s_nop 0
D 84,0
D 84,1024
D 84,2048
D 84,3072
s_add_u32 m0, s50, 4096
s_nop 0
D 85,0
D 85,1024
D 85,2048
D 85,3072
s_mov_b32 m0, 144384
s_nop 0
buffer_load_dword v86, s[36:39], 0 offen sc1 lds
v_add_u32 v87, 1, v87
ds_read_b32 v70, v82 offset:0
s_waitcnt lgkmcnt(0)
v_cmp_gt_i32 vcc, v87, v70
s_waitcnt vmcnt(1)
s_add_u32 s49, s49, 313344
s_cbranch_vccnz CE0_2
BE0_2:
s_barrier
s_add_u32 m0, s50, 32768
R 16,0
D 84,0
R 20,1024
R 24,2048
D 84,1024
R 28,3072
R 32,4096
D 84,2048
s_waitcnt lgkmcnt(3)
v_mfma_f32_16x16x32_bf16 v[0:3], a[0:3], v[16:19], 0
R 36,5120
v_mfma_f32_16x16x32_bf16 v[4:7], a[4:7], v[16:19], 0
mav 0,8,20
R 40,6144
mav 4,12,20
s_waitcnt lgkmcnt(3)
D 84,3072
mav 0,16,24
R 44,7168
mav 4,20,24
s_add_u32 m0, s50, 36864
mav 0,24,28
R 16,8192
mav 4,28,28
s_waitcnt lgkmcnt(3)
D 85,0
mav 0,32,32
R 20,9216
mav 4,36,32
mav 0,40,36
R 24,10240
mav 4,44,36
s_waitcnt lgkmcnt(3)
D 85,1024
mav 0,48,40
R 28,11264
mav 4,52,40
mav 0,56,44
R 32,12288
mav 4,60,44
s_waitcnt lgkmcnt(3)
D 85,2048
mav 0,64,16
R 36,13312
mav 4,68,16
mav 0,72,20
R 40,14336
mav 4,76,20
s_waitcnt lgkmcnt(3)
D 85,3072
mav 0,80,24
R 44,15360
mav 4,84,24
mav 0,88,28
R 16,16384
mav 4,92,28
s_waitcnt lgkmcnt(3)
mav 0,96,32
R 20,17408
mav 4,100,32
mav 0,104,36
R 24,18432
mav 4,108,36
s_waitcnt lgkmcnt(3)
mav 0,112,40
s_mov_b32 m0, 144384
R 28,19456
mav 4,116,40
mav 0,120,44
R 32,20480
mav 4,124,44
s_waitcnt lgkmcnt(3)
buffer_load_dword v86, s[36:39], 0 offen sc1 lds
mav 0,128,16
R 36,21504
mav 4,132,16
mav 0,136,20
R 40,22528
mav 4,140,20
s_waitcnt lgkmcnt(3)
mav 0,144,24
R 44,23552
mav 4,148,24
mav 0,152,28
R 16,24576
mav 4,156,28
s_waitcnt lgkmcnt(3)
mav 0,160,32
R 20,25600
v_add_u32 v87, 1, v87
mav 4,164,32
mav 0,168,36
R 24,26624
mav 4,172,36
s_waitcnt lgkmcnt(3)
mav 0,176,40
R 28,27648
mav 4,180,40
mav 0,184,44
R 32,28672
mav 4,188,44
s_waitcnt lgkmcnt(3)
)"
R"(
mav 0,192,16
R 36,29696
mav 4,196,16
mav 0,200,20
R 40,30720
mav 4,204,20
s_waitcnt lgkmcnt(3)
mav 0,208,24
R 44,31744
mav 4,212,24
mav 0,216,28
mav 4,220,28
s_waitcnt lgkmcnt(2)
mav 0,224,32
mav 4,228,32
mav 0,232,36
mav 4,236,36
s_waitcnt lgkmcnt(0)
mav 0,240,40
mav 4,244,40
mav 0,248,44
mav 4,252,44
ds_read_b32 v70, v82 offset:0
s_xor_b32 s50, s50, 0x10000
s_waitcnt lgkmcnt(0)
v_cmp_gt_i32 vcc, v87, v70
s_waitcnt vmcnt(1)
s_add_u32 s49, s49, 313344
s_cbranch_vccnz CE0_3
BE0_3:
s_barrier
s_add_u32 m0, s50, 0
R 16,32768
D 84,0
ds_read_b64 v[66:67], v88 offset:0
R 20,33792
R 24,34816
D 84,1024
R 28,35840
R 32,36864
D 84,2048
s_waitcnt lgkmcnt(3)
v_mfma_f32_16x16x32_bf16 v[8:11], a[0:3], v[16:19], 0
R 36,37888
v_fma_f32 v68, v67, v48, v90
v_mfma_f32_16x16x32_bf16 v[12:15], a[4:7], v[16:19], 0
v_fma_f32 v0, v0, v66, v68
v_fma_f32 v69, v67, v49, v91
mav 8,8,20
R 40,38912
v_fma_f32 v1, v1, v66, v69
mav 12,12,20
s_waitcnt lgkmcnt(3)
D 84,3072
mav 8,16,24
R 44,39936
v_mul_f32_e64 v0, v0, |v0|
mav 12,20,24
s_add_u32 m0, s50, 4096
v_mul_f32_e64 v1, v1, |v1|
v_cvt_pk_bf16_f32 v0, v0, v1
mav 8,24,28
R 16,40960
v_pk_max_i16 v56, v0, 0
mav 12,28,28
s_waitcnt lgkmcnt(3)
D 85,0
mav 8,32,32
R 20,41984
v_fma_f32 v68, v67, v50, v92
mav 12,36,32
v_fma_f32 v2, v2, v66, v68
v_fma_f32 v69, v67, v51, v93
mav 8,40,36
R 24,43008
v_fma_f32 v3, v3, v66, v69
mav 12,44,36
s_waitcnt lgkmcnt(3)
D 85,1024
mav 8,48,40
R 28,44032
v_mul_f32_e64 v2, v2, |v2|
mav 12,52,40
v_mul_f32_e64 v3, v3, |v3|
v_cvt_pk_bf16_f32 v2, v2, v3
mav 8,56,44
R 32,45056
v_pk_max_i16 v57, v2, 0
mav 12,60,44
s_waitcnt lgkmcnt(3)
D 85,2048
mav 8,64,16
R 36,46080
v_fma_f32 v68, v67, v52, v94
mav 12,68,16
v_fma_f32 v4, v4, v66, v68
v_fma_f32 v69, v67, v53, v95
mav 8,72,20
R 40,47104
v_fma_f32 v5, v5, v66, v69
mav 12,76,20
s_waitcnt lgkmcnt(3)
D 85,3072
mav 8,80,24
R 44,48128
v_mul_f32_e64 v4, v4, |v4|
mav 12,84,24
v_mul_f32_e64 v5, v5, |v5|
v_cvt_pk_bf16_f32 v4, v4, v5
mav 8,88,28
R 16,49152
v_pk_max_i16 v58, v4, 0
mav 12,92,28
s_waitcnt lgkmcnt(3)
v_fma_f32 v68, v67, v54, v96
v_fma_f32 v6, v6, v66, v68
mav 8,96,32
R 20,50176
v_fma_f32 v69, v67, v55, v97
mav 12,100,32
v_fma_f32 v7, v7, v66, v69
v_mul_f32_e64 v6, v6, |v6|
mav 8,104,36
R 24,51200
v_mul_f32_e64 v7, v7, |v7|
mav 12,108,36
s_waitcnt lgkmcnt(3)
v_cvt_pk_bf16_f32 v6, v6, v7
v_pk_max_i16 v59, v6, 0
mav 8,112,40
s_mov_b32 m0, 144384
R 28,52224
mav 12,116,40
mav 8,120,44
R 32,53248
mav 12,124,44
buffer_store_dwordx4 v[56:59], v83, s[36:39], s49 offen sc1
s_waitcnt lgkmcnt(3)
mav 8,128,16
R 36,54272
mav 12,132,16
mav 8,136,20
R 40,55296
mav 12,140,20
s_waitcnt lgkmcnt(3)
buffer_load_dword v86, s[36:39], 0 offen sc1 lds
mav 8,144,24
R 44,56320
mav 12,148,24
mav 8,152,28
R 16,57344
mav 12,156,28
s_waitcnt lgkmcnt(3)
mav 8,160,32
R 20,58368
v_add_u32 v87, 1, v87
mav 12,164,32
mav 8,168,36
R 24,59392
mav 12,172,36
s_waitcnt lgkmcnt(3)
mav 8,176,40
R 28,60416
mav 12,180,40
mav 8,184,44
R 32,61440
mav 12,188,44
s_waitcnt lgkmcnt(3)
mav 8,192,16
R 36,62464
mav 12,196,16
mav 8,200,20
R 40,63488
mav 12,204,20
s_waitcnt lgkmcnt(3)
mav 8,208,24
R 44,64512
mav 12,212,24
mav 8,216,28
mav 12,220,28
s_waitcnt lgkmcnt(2)
mav 8,224,32
v_add_u32 v64, 1, v64
mav 12,228,32
mav 8,232,36
s_mov_b32 s60, -30
.p2align 6
LE0_4:
mav 12,236,36
s_waitcnt lgkmcnt(0)
mav 8,240,40
mav 12,244,40
mav 8,248,44
mav 12,252,44
ds_read_b32 v70, v82 offset:0
s_waitcnt lgkmcnt(0)
v_cmp_gt_i32 vcc, v87, v70
v_xor_b32 v80, 0x10000, v80
s_waitcnt vmcnt(2)
s_add_u32 s49, s49, 313344
s_cbranch_vccnz CE0_5
BE0_5:
s_barrier
s_add_u32 m0, s50, 32768
R 16,0
D 84,0
ds_read_b64 v[66:67], v88 offset:128
R 20,1024
R 24,2048
D 84,1024
R 28,3072
s_waitcnt vmcnt(3)
buffer_store_dword v64, v81, s[36:39], s52 offen sc1
R 32,4096
D 84,2048
s_waitcnt lgkmcnt(3)
v_mfma_f32_16x16x32_bf16 v[0:3], a[0:3], v[16:19], 0
R 36,5120
v_fma_f32 v68, v67, v48, v90
v_mfma_f32_16x16x32_bf16 v[4:7], a[4:7], v[16:19], 0
v_fma_f32 v8, v8, v66, v68
v_fma_f32 v69, v67, v49, v91
mav 0,8,20
R 40,6144
v_fma_f32 v9, v9, v66, v69
mav 4,12,20
s_waitcnt lgkmcnt(3)
D 84,3072
mav 0,16,24
R 44,7168
v_mul_f32_e64 v8, v8, |v8|
mav 4,20,24
s_add_u32 m0, s50, 36864
v_mul_f32_e64 v9, v9, |v9|
v_cvt_pk_bf16_f32 v8, v8, v9
mav 0,24,28
R 16,8192
v_pk_max_i16 v56, v8, 0
mav 4,28,28
s_waitcnt lgkmcnt(3)
D 85,0
mav 0,32,32
R 20,9216
v_fma_f32 v68, v67, v50, v92
mav 4,36,32
v_fma_f32 v10, v10, v66, v68
v_fma_f32 v69, v67, v51, v93
mav 0,40,36
R 24,10240
v_fma_f32 v11, v11, v66, v69
mav 4,44,36
s_waitcnt lgkmcnt(3)
D 85,1024
mav 0,48,40
R 28,11264
v_mul_f32_e64 v10, v10, |v10|
mav 4,52,40
v_mul_f32_e64 v11, v11, |v11|
v_cvt_pk_bf16_f32 v10, v10, v11
mav 0,56,44
R 32,12288
v_pk_max_i16 v57, v10, 0
mav 4,60,44
s_waitcnt lgkmcnt(3)
D 85,2048
mav 0,64,16
R 36,13312
v_fma_f32 v68, v67, v52, v94
mav 4,68,16
v_fma_f32 v12, v12, v66, v68
v_fma_f32 v69, v67, v53, v95
mav 0,72,20
R 40,14336
v_fma_f32 v13, v13, v66, v69
mav 4,76,20
s_waitcnt lgkmcnt(3)
D 85,3072
mav 0,80,24
R 44,15360
v_mul_f32_e64 v12, v12, |v12|
mav 4,84,24
v_mul_f32_e64 v13, v13, |v13|
v_cvt_pk_bf16_f32 v12, v12, v13
mav 0,88,28
R 16,16384
v_pk_max_i16 v58, v12, 0
mav 4,92,28
s_waitcnt lgkmcnt(3)
v_fma_f32 v68, v67, v54, v96
v_fma_f32 v14, v14, v66, v68
mav 0,96,32
R 20,17408
v_fma_f32 v69, v67, v55, v97
mav 4,100,32
v_fma_f32 v15, v15, v66, v69
v_mul_f32_e64 v14, v14, |v14|
mav 0,104,36
R 24,18432
v_mul_f32_e64 v15, v15, |v15|
mav 4,108,36
LsE2:
s_cbranch_execz LsE6
s_waitcnt lgkmcnt(3)
v_cvt_pk_bf16_f32 v14, v14, v15
v_pk_max_i16 v59, v14, 0
mav 0,112,40
s_mov_b32 m0, 144384
R 28,19456
mav 4,116,40
mav 0,120,44
R 32,20480
mav 4,124,44
buffer_store_dwordx4 v[56:59], v83, s[36:39], s49 offen sc1
s_waitcnt lgkmcnt(3)
mav 0,128,16
R 36,21504
mav 4,132,16
mav 0,136,20
R 40,22528
mav 4,140,20
s_waitcnt lgkmcnt(3)
buffer_load_dword v86, s[36:39], 0 offen sc1 lds
mav 0,144,24
R 44,23552
mav 4,148,24
mav 0,152,28
R 16,24576
mav 4,156,28
s_waitcnt lgkmcnt(3)
mav 0,160,32
R 20,25600
v_add_u32 v87, 1, v87
mav 4,164,32
mav 0,168,36
R 24,26624
mav 4,172,36
s_waitcnt lgkmcnt(3)
mav 0,176,40
R 28,27648
mav 4,180,40
mav 0,184,44
R 32,28672
mav 4,188,44
s_waitcnt lgkmcnt(3)
mav 0,192,16
R 36,29696
mav 4,196,16
mav 0,200,20
R 40,30720
mav 4,204,20
s_waitcnt lgkmcnt(3)
mav 0,208,24
R 44,31744
mav 4,212,24
mav 0,216,28
mav 4,220,28
s_waitcnt lgkmcnt(2)
mav 0,224,32
v_add_u32 v64, 1, v64
mav 4,228,32
mav 0,232,36
mav 4,236,36
s_waitcnt lgkmcnt(0)
mav 0,240,40
mav 4,244,40
mav 0,248,44
mav 4,252,44
ds_read_b32 v70, v82 offset:0
s_xor_b32 s50, s50, 0x10000
s_waitcnt lgkmcnt(0)
v_cmp_gt_i32 vcc, v87, v70
s_waitcnt vmcnt(2)
s_add_u32 s49, s49, 313344
s_cbranch_vccnz CE0_6
BE0_6:
s_barrier
s_add_u32 m0, s50, 0
R 16,32768
D 84,0
ds_read_b64 v[66:67], v88 offset:0
R 20,33792
R 24,34816
D 84,1024
R 28,35840
s_waitcnt vmcnt(3)
buffer_store_dword v64, v81, s[36:39], s52 offen sc1
R 32,36864
D 84,2048
s_waitcnt lgkmcnt(3)
v_mfma_f32_16x16x32_bf16 v[8:11], a[0:3], v[16:19], 0
R 36,37888
v_fma_f32 v68, v67, v48, v90
v_mfma_f32_16x16x32_bf16 v[12:15], a[4:7], v[16:19], 0
v_fma_f32 v0, v0, v66, v68
v_fma_f32 v69, v67, v49, v91
mav 8,8,20
R 40,38912
v_fma_f32 v1, v1, v66, v69
mav 12,12,20
s_waitcnt lgkmcnt(3)
D 84,3072
mav 8,16,24
R 44,39936
v_mul_f32_e64 v0, v0, |v0|
mav 12,20,24
s_add_u32 m0, s50, 4096
v_mul_f32_e64 v1, v1, |v1|
v_cvt_pk_bf16_f32 v0, v0, v1
mav 8,24,28
R 16,40960
v_pk_max_i16 v56, v0, 0
mav 12,28,28
s_waitcnt lgkmcnt(3)
D 85,0
mav 8,32,32
R 20,41984
v_fma_f32 v68, v67, v50, v92
mav 12,36,32
v_fma_f32 v2, v2, v66, v68
v_fma_f32 v69, v67, v51, v93
mav 8,40,36
R 24,43008
v_fma_f32 v3, v3, v66, v69
mav 12,44,36
s_waitcnt lgkmcnt(3)
D 85,1024
mav 8,48,40
R 28,44032
v_mul_f32_e64 v2, v2, |v2|
mav 12,52,40
v_mul_f32_e64 v3, v3, |v3|
v_cvt_pk_bf16_f32 v2, v2, v3
mav 8,56,44
R 32,45056
v_pk_max_i16 v57, v2, 0
mav 12,60,44
s_waitcnt lgkmcnt(3)
D 85,2048
mav 8,64,16
R 36,46080
v_fma_f32 v68, v67, v52, v94
mav 12,68,16
v_fma_f32 v4, v4, v66, v68
v_fma_f32 v69, v67, v53, v95
mav 8,72,20
R 40,47104
v_fma_f32 v5, v5, v66, v69
mav 12,76,20
s_waitcnt lgkmcnt(3)
D 85,3072
mav 8,80,24
R 44,48128
v_mul_f32_e64 v4, v4, |v4|
mav 12,84,24
v_mul_f32_e64 v5, v5, |v5|
v_cvt_pk_bf16_f32 v4, v4, v5
mav 8,88,28
R 16,49152
v_pk_max_i16 v58, v4, 0
mav 12,92,28
s_waitcnt lgkmcnt(3)
v_fma_f32 v68, v67, v54, v96
v_fma_f32 v6, v6, v66, v68
mav 8,96,32
R 20,50176
v_fma_f32 v69, v67, v55, v97
mav 12,100,32
v_fma_f32 v7, v7, v66, v69
v_mul_f32_e64 v6, v6, |v6|
mav 8,104,36
R 24,51200
v_mul_f32_e64 v7, v7, |v7|
mav 12,108,36
s_waitcnt lgkmcnt(3)
v_cvt_pk_bf16_f32 v6, v6, v7
v_pk_max_i16 v59, v6, 0
mav 8,112,40
s_mov_b32 m0, 144384
R 28,52224
mav 12,116,40
mav 8,120,44
R 32,53248
mav 12,124,44
buffer_store_dwordx4 v[56:59], v83, s[36:39], s49 offen sc1
s_waitcnt lgkmcnt(3)
mav 8,128,16
R 36,54272
mav 12,132,16
mav 8,136,20
R 40,55296
mav 12,140,20
s_waitcnt lgkmcnt(3)
buffer_load_dword v86, s[36:39], 0 offen sc1 lds
mav 8,144,24
R 44,56320
mav 12,148,24
mav 8,152,28
R 16,57344
mav 12,156,28
s_waitcnt lgkmcnt(3)
mav 8,160,32
R 20,58368
v_add_u32 v87, 1, v87
mav 12,164,32
mav 8,168,36
R 24,59392
mav 12,172,36
s_waitcnt lgkmcnt(3)
mav 8,176,40
R 28,60416
mav 12,180,40
mav 8,184,44
R 32,61440
mav 12,188,44
s_waitcnt lgkmcnt(3)
mav 8,192,16
R 36,62464
mav 12,196,16
mav 8,200,20
R 40,63488
mav 12,204,20
s_waitcnt lgkmcnt(3)
mav 8,208,24
R 44,64512
mav 12,212,24
mav 8,216,28
mav 12,220,28
s_waitcnt lgkmcnt(2)
mav 8,224,32
v_add_u32 v64, 1, v64
mav 12,228,32
s_add_u32 s60, s60, 1
mav 8,232,36
s_cbranch_scc0 LE0_4
mav 12,236,36
s_waitcnt lgkmcnt(0)
mav 8,240,40
mav 12,244,40
mav 8,248,44
mav 12,252,44
ds_read_b32 v70, v82 offset:0
s_waitcnt lgkmcnt(0)
v_cmp_gt_i32 vcc, v87, v70
v_xor_b32 v80, 0x10000, v80
s_waitcnt vmcnt(2)
s_add_u32 s49, s49, 313344
s_cbranch_vccnz CE0_7
BE0_7:
s_barrier
s_add_u32 m0, s50, 32768
R 16,0
D 84,0
ds_read_b64 v[66:67], v88 offset:128
R 20,1024
R 24,2048
D 84,1024
R 28,3072
s_waitcnt vmcnt(3)
buffer_store_dword v64, v81, s[36:39], s52 offen sc1
R 32,4096
D 84,2048
s_waitcnt lgkmcnt(3)
v_mfma_f32_16x16x32_bf16 v[0:3], a[0:3], v[16:19], 0
R 36,5120
v_fma_f32 v68, v67, v48, v90
v_mfma_f32_16x16x32_bf16 v[4:7], a[4:7], v[16:19], 0
v_fma_f32 v8, v8, v66, v68
v_fma_f32 v69, v67, v49, v91
mav 0,8,20
R 40,6144
v_fma_f32 v9, v9, v66, v69
mav 4,12,20
s_waitcnt lgkmcnt(3)
D 84,3072
mav 0,16,24
R 44,7168
v_mul_f32_e64 v8, v8, |v8|
mav 4,20,24
s_add_u32 m0, s50, 36864
v_mul_f32_e64 v9, v9, |v9|
v_cvt_pk_bf16_f32 v8, v8, v9
mav 0,24,28
R 16,8192
v_pk_max_i16 v56, v8, 0
mav 4,28,28
s_waitcnt lgkmcnt(3)
D 85,0
mav 0,32,32
R 20,9216
v_fma_f32 v68, v67, v50, v92
mav 4,36,32
v_fma_f32 v10, v10, v66, v68
v_fma_f32 v69, v67, v51, v93
mav 0,40,36
R 24,10240
v_fma_f32 v11, v11, v66, v69
mav 4,44,36
s_waitcnt lgkmcnt(3)
D 85,1024
mav 0,48,40
R 28,11264
v_mul_f32_e64 v10, v10, |v10|
mav 4,52,40
v_mul_f32_e64 v11, v11, |v11|
v_cvt_pk_bf16_f32 v10, v10, v11
mav 0,56,44
R 32,12288
v_pk_max_i16 v57, v10, 0
mav 4,60,44
s_waitcnt lgkmcnt(3)
D 85,2048
mav 0,64,16
R 36,13312
v_fma_f32 v68, v67, v52, v94
mav 4,68,16
v_fma_f32 v12, v12, v66, v68
v_fma_f32 v69, v67, v53, v95
mav 0,72,20
R 40,14336
v_fma_f32 v13, v13, v66, v69
mav 4,76,20
s_waitcnt lgkmcnt(3)
D 85,3072
mav 0,80,24
R 44,15360
v_mul_f32_e64 v12, v12, |v12|
mav 4,84,24
v_mul_f32_e64 v13, v13, |v13|
v_cvt_pk_bf16_f32 v12, v12, v13
mav 0,88,28
R 16,16384
v_pk_max_i16 v58, v12, 0
mav 4,92,28
s_waitcnt lgkmcnt(3)
v_fma_f32 v68, v67, v54, v96
v_fma_f32 v14, v14, v66, v68
mav 0,96,32
R 20,17408
v_fma_f32 v69, v67, v55, v97
mav 4,100,32
v_fma_f32 v15, v15, v66, v69
v_mul_f32_e64 v14, v14, |v14|
mav 0,104,36
R 24,18432
v_mul_f32_e64 v15, v15, |v15|
mav 4,108,36
s_waitcnt lgkmcnt(3)
v_cvt_pk_bf16_f32 v14, v14, v15
v_pk_max_i16 v59, v14, 0
mav 0,112,40
s_mov_b32 m0, 144384
R 28,19456
mav 4,116,40
mav 0,120,44
R 32,20480
mav 4,124,44
buffer_store_dwordx4 v[56:59], v83, s[36:39], s49 offen sc1
s_waitcnt lgkmcnt(3)
mav 0,128,16
R 36,21504
mav 4,132,16
mav 0,136,20
R 40,22528
mav 4,140,20
s_waitcnt lgkmcnt(3)
buffer_load_dword v86, s[36:39], 0 offen sc1 lds
mav 0,144,24
R 44,23552
mav 4,148,24
mav 0,152,28
R 16,24576
mav 4,156,28
s_waitcnt lgkmcnt(3)
mav 0,160,32
R 20,25600
v_add_u32 v87, 1, v87
mav 4,164,32
mav 0,168,36
R 24,26624
mav 4,172,36
s_waitcnt lgkmcnt(3)
mav 0,176,40
R 28,27648
mav 4,180,40
mav 0,184,44
R 32,28672
mav 4,188,44
s_waitcnt lgkmcnt(3)
mav 0,192,16
R 36,29696
mav 4,196,16
mav 0,200,20
R 40,30720
mav 4,204,20
s_waitcnt lgkmcnt(3)
mav 0,208,24
R 44,31744
mav 4,212,24
mav 0,216,28
mav 4,220,28
s_waitcnt lgkmcnt(2)
mav 0,224,32
v_add_u32 v64, 1, v64
mav 4,228,32
mav 0,232,36
mav 4,236,36
s_waitcnt lgkmcnt(0)
mav 0,240,40
mav 4,244,40
mav 0,248,44
mav 4,252,44
ds_read_b32 v70, v82 offset:0
s_waitcnt lgkmcnt(0)
v_cmp_gt_i32 vcc, v87, v70
s_waitcnt vmcnt(2)
s_add_u32 s49, s49, 313344
s_cbranch_vccnz CE0_8
BE0_8:
s_barrier
R 16,32768
ds_read_b64 v[66:67], v88 offset:0
R 20,33792
R 24,34816
R 28,35840
s_waitcnt vmcnt(1)
buffer_store_dword v64, v81, s[36:39], s52 offen sc1
R 32,36864
s_waitcnt lgkmcnt(3)
v_mfma_f32_16x16x32_bf16 v[8:11], a[0:3], v[16:19], 0
R 36,37888
v_fma_f32 v68, v67, v48, v90
v_mfma_f32_16x16x32_bf16 v[12:15], a[4:7], v[16:19], 0
v_fma_f32 v0, v0, v66, v68
v_fma_f32 v69, v67, v49, v91
mav 8,8,20
R 40,38912
v_fma_f32 v1, v1, v66, v69
mav 12,12,20
s_waitcnt lgkmcnt(3)
v_mul_f32_e64 v0, v0, |v0|
v_mul_f32_e64 v1, v1, |v1|
mav 8,16,24
R 44,39936
v_cvt_pk_bf16_f32 v0, v0, v1
mav 12,20,24
v_pk_max_i16 v56, v0, 0
v_fma_f32 v68, v67, v50, v92
mav 8,24,28
R 16,40960
v_fma_f32 v2, v2, v66, v68
mav 12,28,28
s_waitcnt lgkmcnt(3)
v_fma_f32 v69, v67, v51, v93
v_fma_f32 v3, v3, v66, v69
mav 8,32,32
R 20,41984
v_mul_f32_e64 v2, v2, |v2|
mav 12,36,32
v_mul_f32_e64 v3, v3, |v3|
v_cvt_pk_bf16_f32 v2, v2, v3
mav 8,40,36
R 24,43008
v_pk_max_i16 v57, v2, 0
mav 12,44,36
s_waitcnt lgkmcnt(3)
v_fma_f32 v68, v67, v52, v94
v_fma_f32 v4, v4, v66, v68
mav 8,48,40
R 28,44032
v_fma_f32 v69, v67, v53, v95
mav 12,52,40
v_fma_f32 v5, v5, v66, v69
v_mul_f32_e64 v4, v4, |v4|
mav 8,56,44
R 32,45056
v_mul_f32_e64 v5, v5, |v5|
mav 12,60,44
s_waitcnt lgkmcnt(3)
v_cvt_pk_bf16_f32 v4, v4, v5
v_pk_max_i16 v58, v4, 0
mav 8,64,16
R 36,46080
v_fma_f32 v68, v67, v54, v96
mav 12,68,16
v_fma_f32 v6, v6, v66, v68
v_fma_f32 v69, v67, v55, v97
mav 8,72,20
R 40,47104
v_fma_f32 v7, v7, v66, v69
mav 12,76,20
s_waitcnt lgkmcnt(3)
v_mul_f32_e64 v6, v6, |v6|
v_mul_f32_e64 v7, v7, |v7|
mav 8,80,24
R 44,48128
v_cvt_pk_bf16_f32 v6, v6, v7
mav 12,84,24
v_pk_max_i16 v59, v6, 0
mav 8,88,28
R 16,49152
mav 12,92,28
buffer_store_dwordx4 v[56:59], v83, s[36:39], s49 offen sc1
s_waitcnt lgkmcnt(3)
mav 8,96,32
R 20,50176
mav 12,100,32
mav 8,104,36
R 24,51200
mav 12,108,36
s_waitcnt lgkmcnt(3)
mav 8,112,40
R 28,52224
mav 12,116,40
mav 8,120,44
R 32,53248
mav 12,124,44
s_waitcnt lgkmcnt(3)
mav 8,128,16
R 36,54272
mav 12,132,16
mav 8,136,20
R 40,55296
mav 12,140,20
s_waitcnt lgkmcnt(3)
mav 8,144,24
R 44,56320
mav 12,148,24
mav 8,152,28
R 16,57344
mav 12,156,28
s_waitcnt lgkmcnt(3)
mav 8,160,32
R 20,58368
mav 12,164,32
mav 8,168,36
R 24,59392
mav 12,172,36
s_waitcnt lgkmcnt(3)
mav 8,176,40
R 28,60416
mav 12,180,40
mav 8,184,44
R 32,61440
mav 12,188,44
s_waitcnt lgkmcnt(3)
mav 8,192,16
R 36,62464
mav 12,196,16
mav 8,200,20
R 40,63488
mav 12,204,20
s_waitcnt lgkmcnt(3)
mav 8,208,24
R 44,64512
mav 12,212,24
mav 8,216,28
mav 12,220,28
s_waitcnt lgkmcnt(2)
LsE3:
s_cbranch_execz LsE7
mav 8,224,32
v_add_u32 v64, 1, v64
mav 12,228,32
mav 8,232,36
mav 12,236,36
s_waitcnt lgkmcnt(0)
mav 8,240,40
mav 12,244,40
mav 8,248,44
mav 12,252,44
s_add_u32 s49, s49, 313344
s_barrier
ds_read_b64 v[66:67], v88 offset:128
s_waitcnt vmcnt(0)
buffer_store_dword v64, v81, s[36:39], s52 offen sc1
s_waitcnt lgkmcnt(0)
v_fma_f32 v68, v67, v48, v90
v_fma_f32 v8, v8, v66, v68
v_fma_f32 v69, v67, v49, v91
v_fma_f32 v9, v9, v66, v69
v_mul_f32_e64 v8, v8, |v8|
v_mul_f32_e64 v9, v9, |v9|
v_cvt_pk_bf16_f32 v8, v8, v9
v_pk_max_i16 v56, v8, 0
v_fma_f32 v68, v67, v50, v92
v_fma_f32 v10, v10, v66, v68
v_fma_f32 v69, v67, v51, v93
v_fma_f32 v11, v11, v66, v69
v_mul_f32_e64 v10, v10, |v10|
v_mul_f32_e64 v11, v11, |v11|
v_cvt_pk_bf16_f32 v10, v10, v11
v_pk_max_i16 v57, v10, 0
v_fma_f32 v68, v67, v52, v94
v_fma_f32 v12, v12, v66, v68
v_fma_f32 v69, v67, v53, v95
v_fma_f32 v13, v13, v66, v69
v_mul_f32_e64 v12, v12, |v12|
v_mul_f32_e64 v13, v13, |v13|
v_cvt_pk_bf16_f32 v12, v12, v13
v_pk_max_i16 v58, v12, 0
v_fma_f32 v68, v67, v54, v96
v_fma_f32 v14, v14, v66, v68
v_fma_f32 v69, v67, v55, v97
v_fma_f32 v15, v15, v66, v69
v_mul_f32_e64 v14, v14, |v14|
v_mul_f32_e64 v15, v15, |v15|
v_cvt_pk_bf16_f32 v14, v14, v15
v_pk_max_i16 v59, v14, 0
buffer_store_dwordx4 v[56:59], v83, s[36:39], s49 offen sc1
v_add_u32 v64, 37, v64
s_waitcnt vmcnt(0)
buffer_store_dword v64, v81, s[36:39], s52 offen sc1
s_endpgm
CE0_1:
s_cmp_eq_u32 s61, 0
s_cbranch_scc1 BE0_1
s_sub_u32 s61, s61, 1
s_sleep 8
buffer_load_dwordx2 v[70:71], v86, s[36:39], 0 offen sc1
s_waitcnt vmcnt(0)
ds_write_b32 v82, v70
v_sub_u32 v70, v70, v87
v_xor_b32 v71, v71, v65
v_cmp_lt_u64 vcc, s[62:63], v[70:71]
s_waitcnt lgkmcnt(0)
s_cbranch_vccnz CE0_1
s_branch BE0_1
CE0_2:
s_mov_b32 s58, m0
s_mov_b32 m0, 144384
DE0_2:
s_cmp_eq_u32 s61, 0
s_cbranch_scc1 EE0_2
s_sub_u32 s61, s61, 1
buffer_load_dword v86, s[36:39], 0 offen sc1 lds
s_sleep 1
ds_read_b32 v70, v82
s_waitcnt lgkmcnt(0)
v_cmp_gt_i32 vcc, v87, v70
s_cbranch_vccnz DE0_2
EE0_2:
s_mov_b32 m0, s58
s_branch BE0_2
CE0_3:
s_mov_b32 s58, m0
s_mov_b32 m0, 144384
DE0_3:
s_cmp_eq_u32 s61, 0
s_cbranch_scc1 EE0_3
s_sub_u32 s61, s61, 1
buffer_load_dword v86, s[36:39], 0 offen sc1 lds
s_sleep 1
ds_read_b32 v70, v82
s_waitcnt lgkmcnt(0)
v_cmp_gt_i32 vcc, v87, v70
s_cbranch_vccnz DE0_3
EE0_3:
s_mov_b32 m0, s58
s_branch BE0_3
CE0_5:
s_mov_b32 s58, m0
s_mov_b32 m0, 144384
DE0_5:
s_cmp_eq_u32 s61, 0
s_cbranch_scc1 EE0_5
s_sub_u32 s61, s61, 1
buffer_load_dword v86, s[36:39], 0 offen sc1 lds
s_sleep 1
ds_read_b32 v70, v82
s_waitcnt lgkmcnt(0)
v_cmp_gt_i32 vcc, v87, v70
s_cbranch_vccnz DE0_5
EE0_5:
s_mov_b32 m0, s58
s_branch BE0_5
CE0_6:
s_mov_b32 s58, m0
s_mov_b32 m0, 144384
DE0_6:
s_cmp_eq_u32 s61, 0
s_cbranch_scc1 EE0_6
s_sub_u32 s61, s61, 1
buffer_load_dword v86, s[36:39], 0 offen sc1 lds
s_sleep 1
ds_read_b32 v70, v82
s_waitcnt lgkmcnt(0)
v_cmp_gt_i32 vcc, v87, v70
s_cbranch_vccnz DE0_6
EE0_6:
s_mov_b32 m0, s58
s_branch BE0_6
CE0_7:
s_mov_b32 s58, m0
s_mov_b32 m0, 144384
DE0_7:
s_cmp_eq_u32 s61, 0
s_cbranch_scc1 EE0_7
s_sub_u32 s61, s61, 1
buffer_load_dword v86, s[36:39], 0 offen sc1 lds
s_sleep 1
ds_read_b32 v70, v82
s_waitcnt lgkmcnt(0)
v_cmp_gt_i32 vcc, v87, v70
s_cbranch_vccnz DE0_7
EE0_7:
s_mov_b32 m0, s58
s_branch BE0_7
CE0_8:
s_mov_b32 s58, m0
s_mov_b32 m0, 144384
DE0_8:
s_cmp_eq_u32 s61, 0
s_cbranch_scc1 EE0_8
s_sub_u32 s61, s61, 1
buffer_load_dword v86, s[36:39], 0 offen sc1 lds
s_sleep 1
ds_read_b32 v70, v82
s_waitcnt lgkmcnt(0)
v_cmp_gt_i32 vcc, v87, v70
s_cbranch_vccnz DE0_8
EE0_8:
s_mov_b32 m0, s58
s_branch BE0_8
LvE1:
LsE4:
s_cbranch_execz LsE8
s_add_u32 s49, s49, 313344
s_barrier
s_add_u32 m0, s50, 0
s_nop 0
D 84,0
D 84,1024
D 84,2048
D 84,3072
s_add_u32 m0, s50, 4096
s_nop 0
D 85,0
D 85,1024
D 85,2048
D 85,3072
s_waitcnt vmcnt(0)
s_add_u32 s49, s49, 313344
s_barrier
s_add_u32 m0, s50, 32768
R 16,0
buffer_load_dwordx2 v[72:73], v89, s[36:39], s49 offen sc1
buffer_load_dwordx2 v[74:75], v89, s[36:39], s49 offen offset:512 sc1
D 84,0
R 20,1024
R 24,2048
D 84,1024
R 28,3072
R 32,4096
D 84,2048
s_waitcnt lgkmcnt(3)
v_mfma_f32_16x16x32_bf16 v[0:3], a[0:3], v[16:19], 0
R 36,5120
v_mfma_f32_16x16x32_bf16 v[4:7], a[4:7], v[16:19], 0
mav 0,8,20
R 40,6144
mav 4,12,20
s_waitcnt lgkmcnt(3)
mav 0,16,24
R 44,7168
mav 4,20,24
mav 0,24,28
D 84,3072
mav 4,28,28
s_waitcnt lgkmcnt(2)
R 16,8192
s_add_u32 m0, s50, 36864
mav 0,32,32
R 20,9216
mav 4,36,32
mav 0,40,36
D 85,0
mav 4,44,36
s_waitcnt lgkmcnt(2)
R 24,10240
mav 0,48,40
R 28,11264
mav 4,52,40
mav 0,56,44
D 85,1024
mav 4,60,44
s_waitcnt lgkmcnt(2)
R 32,12288
mav 0,64,16
R 36,13312
mav 4,68,16
mav 0,72,20
D 85,2048
mav 4,76,20
s_waitcnt lgkmcnt(2)
R 40,14336
mav 0,80,24
R 44,15360
mav 4,84,24
mav 0,88,28
D 85,3072
mav 4,92,28
s_waitcnt lgkmcnt(2)
R 16,16384
mav 0,96,32
R 20,17408
mav 4,100,32
mav 0,104,36
R 24,18432
mav 4,108,36
s_waitcnt lgkmcnt(3)
mav 0,112,40
R 28,19456
mav 4,116,40
mav 0,120,44
R 32,20480
mav 4,124,44
s_waitcnt lgkmcnt(3)
mav 0,128,16
R 36,21504
mav 4,132,16
mav 0,136,20
R 40,22528
mav 4,140,20
s_waitcnt vmcnt(8) lgkmcnt(3)
v_add_f32 v72, v72, v74
v_add_f32 v73, v73, v75
mav 0,144,24
R 44,23552
mav 4,148,24
v_permlane32_swap_b32 v72, v73
mav 0,152,28
R 16,24576
v_add_f32 v74, v72, v73
mav 4,156,28
s_waitcnt lgkmcnt(3)
v_add_f32 v75, v72, v73
mav 0,160,32
R 20,25600
mav 4,164,32
v_permlane16_swap_b32 v74, v75
mav 0,168,36
R 24,26624
v_add_f32 v72, v74, v75
mav 4,172,36
s_waitcnt lgkmcnt(3)
v_add_f32 v73, v74, v75
mav 0,176,40
R 28,27648
mav 4,180,40
v_permlane32_swap_b32 v72, v73
mav 0,184,44
R 32,28672
v_mul_f32 v74, 0x3a800000, v72
mav 4,188,44
s_waitcnt lgkmcnt(3)
v_mul_f32 v75, 0x3a800000, v73
v_fma_f32 v75, -v74, v74, v75
mav 0,192,16
R 36,29696
v_max_f32 v75, 0, v75
mav 4,196,16
v_add_f32 v75, 0x3727c5ac, v75
mav 0,200,20
R 40,30720
mav 4,204,20
s_waitcnt lgkmcnt(3)
v_rsq_f32 v72, v75
mav 0,208,24
R 44,31744
v_mul_f32_e64 v73, -v74, v72
mav 4,212,24
mav 0,216,28
mav 4,220,28
s_waitcnt lgkmcnt(2)
mav 0,224,32
mav 4,228,32
mav 0,232,36
mav 4,236,36
s_waitcnt lgkmcnt(0)
mav 0,240,40
mav 4,244,40
mav 0,248,44
mav 4,252,44
ds_write_b64 v88, v[72:73] offset:0
s_xor_b32 s50, s50, 0x10000
s_waitcnt vmcnt(0) lgkmcnt(0)
s_add_u32 s49, s49, 313344
s_barrier
s_add_u32 m0, s50, 0
R 16,32768
buffer_load_dwordx2 v[72:73], v89, s[36:39], s49 offen sc1
buffer_load_dwordx2 v[74:75], v89, s[36:39], s49 offen offset:512 sc1
D 84,0
ds_read_b64 v[66:67], v88 offset:0
R 20,33792
R 24,34816
D 84,1024
R 28,35840
R 32,36864
D 84,2048
s_waitcnt lgkmcnt(3)
v_mfma_f32_16x16x32_bf16 v[8:11], a[0:3], v[16:19], 0
R 36,37888
v_fma_f32 v68, v67, v48, v90
v_mfma_f32_16x16x32_bf16 v[12:15], a[4:7], v[16:19], 0
v_fma_f32 v0, v0, v66, v68
v_fma_f32 v69, v67, v49, v91
mav 8,8,20
R 40,38912
v_fma_f32 v1, v1, v66, v69
mav 12,12,20
s_waitcnt lgkmcnt(3)
v_mul_f32_e64 v0, v0, |v0|
v_mul_f32_e64 v1, v1, |v1|
mav 8,16,24
R 44,39936
v_cvt_pk_bf16_f32 v0, v0, v1
mav 12,20,24
v_pk_max_i16 v56, v0, 0
v_fma_f32 v68, v67, v50, v92
mav 8,24,28
D 84,3072
mav 12,28,28
s_waitcnt lgkmcnt(2)
R 16,40960
s_add_u32 m0, s50, 4096
mav 8,32,32
R 20,41984
v_fma_f32 v2, v2, v66, v68
mav 12,36,32
v_fma_f32 v69, v67, v51, v93
v_fma_f32 v3, v3, v66, v69
mav 8,40,36
D 85,0
mav 12,44,36
s_waitcnt lgkmcnt(2)
R 24,43008
v_mul_f32_e64 v2, v2, |v2|
mav 8,48,40
R 28,44032
v_mul_f32_e64 v3, v3, |v3|
mav 12,52,40
v_cvt_pk_bf16_f32 v2, v2, v3
v_pk_max_i16 v57, v2, 0
mav 8,56,44
D 85,1024
mav 12,60,44
s_waitcnt lgkmcnt(2)
R 32,45056
v_fma_f32 v68, v67, v52, v94
mav 8,64,16
R 36,46080
v_fma_f32 v4, v4, v66, v68
mav 12,68,16
v_fma_f32 v69, v67, v53, v95
v_fma_f32 v5, v5, v66, v69
mav 8,72,20
D 85,2048
mav 12,76,20
s_waitcnt lgkmcnt(2)
R 40,47104
v_mul_f32_e64 v4, v4, |v4|
mav 8,80,24
R 44,48128
v_mul_f32_e64 v5, v5, |v5|
mav 12,84,24
v_cvt_pk_bf16_f32 v4, v4, v5
v_pk_max_i16 v58, v4, 0
mav 8,88,28
D 85,3072
mav 12,92,28
s_waitcnt lgkmcnt(2)
R 16,49152
v_fma_f32 v68, v67, v54, v96
mav 8,96,32
R 20,50176
v_fma_f32 v6, v6, v66, v68
mav 12,100,32
v_fma_f32 v69, v67, v55, v97
v_fma_f32 v7, v7, v66, v69
mav 8,104,36
R 24,51200
v_mul_f32_e64 v6, v6, |v6|
mav 12,108,36
s_waitcnt lgkmcnt(3)
v_mul_f32_e64 v7, v7, |v7|
v_cvt_pk_bf16_f32 v6, v6, v7
mav 8,112,40
R 28,52224
v_pk_max_i16 v59, v6, 0
mav 12,116,40
mav 8,120,44
buffer_store_dwordx4 v[56:59], v83, s[36:39], s49 offen sc1
R 32,53248
mav 12,124,44
s_waitcnt lgkmcnt(3)
mav 8,128,16
R 36,54272
mav 12,132,16
mav 8,136,20
R 40,55296
mav 12,140,20
s_waitcnt vmcnt(9) lgkmcnt(3)
v_add_f32 v72, v72, v74
v_add_f32 v73, v73, v75
mav 8,144,24
R 44,56320
mav 12,148,24
v_permlane32_swap_b32 v72, v73
mav 8,152,28
R 16,57344
v_add_f32 v74, v72, v73
mav 12,156,28
s_waitcnt lgkmcnt(3)
v_add_f32 v75, v72, v73
mav 8,160,32
R 20,58368
mav 12,164,32
v_permlane16_swap_b32 v74, v75
mav 8,168,36
R 24,59392
v_add_f32 v72, v74, v75
mav 12,172,36
s_waitcnt lgkmcnt(3)
v_add_f32 v73, v74, v75
mav 8,176,40
R 28,60416
mav 12,180,40
v_permlane32_swap_b32 v72, v73
mav 8,184,44
R 32,61440
v_mul_f32 v74, 0x3a800000, v72
mav 12,188,44
s_waitcnt lgkmcnt(3)
v_mul_f32 v75, 0x3a800000, v73
v_fma_f32 v75, -v74, v74, v75
mav 8,192,16
R 36,62464
v_max_f32 v75, 0, v75
mav 12,196,16
v_add_f32 v75, 0x3727c5ac, v75
mav 8,200,20
R 40,63488
mav 12,204,20
s_waitcnt lgkmcnt(3)
v_rsq_f32 v72, v75
mav 8,208,24
R 44,64512
v_mul_f32_e64 v73, -v74, v72
mav 12,212,24
mav 8,216,28
mav 12,220,28
s_waitcnt lgkmcnt(2)
mav 8,224,32
v_add_u32 v64, 1, v64
mav 12,228,32
mav 8,232,36
s_mov_b32 s60, -30
.p2align 6
LE1_1:
mav 12,236,36
s_waitcnt lgkmcnt(0)
mav 8,240,40
mav 12,244,40
mav 8,248,44
mav 12,252,44
ds_write_b64 v88, v[72:73] offset:128
v_xor_b32 v80, 0x10000, v80
s_waitcnt vmcnt(1) lgkmcnt(0)
s_add_u32 s49, s49, 313344
s_barrier
s_add_u32 m0, s50, 32768
R 16,0
buffer_load_dwordx2 v[72:73], v89, s[36:39], s49 offen sc1
buffer_load_dwordx2 v[74:75], v89, s[36:39], s49 offen offset:512 sc1
D 84,0
ds_read_b64 v[66:67], v88 offset:128
R 20,1024
R 24,2048
D 84,1024
R 28,3072
s_waitcnt vmcnt(4)
buffer_store_dword v64, v81, s[36:39], s52 offen sc1
R 32,4096
D 84,2048
s_waitcnt lgkmcnt(3)
v_mfma_f32_16x16x32_bf16 v[0:3], a[0:3], v[16:19], 0
R 36,5120
v_fma_f32 v68, v67, v48, v90
v_mfma_f32_16x16x32_bf16 v[4:7], a[4:7], v[16:19], 0
v_fma_f32 v8, v8, v66, v68
v_fma_f32 v69, v67, v49, v91
mav 0,8,20
R 40,6144
v_fma_f32 v9, v9, v66, v69
mav 4,12,20
s_waitcnt lgkmcnt(3)
v_mul_f32_e64 v8, v8, |v8|
v_mul_f32_e64 v9, v9, |v9|
mav 0,16,24
R 44,7168
v_cvt_pk_bf16_f32 v8, v8, v9
mav 4,20,24
v_pk_max_i16 v56, v8, 0
v_fma_f32 v68, v67, v50, v92
mav 0,24,28
D 84,3072
mav 4,28,28
s_waitcnt lgkmcnt(2)
R 16,8192
s_add_u32 m0, s50, 36864
mav 0,32,32
R 20,9216
v_fma_f32 v10, v10, v66, v68
mav 4,36,32
v_fma_f32 v69, v67, v51, v93
v_fma_f32 v11, v11, v66, v69
mav 0,40,36
D 85,0
mav 4,44,36
s_waitcnt lgkmcnt(2)
R 24,10240
v_mul_f32_e64 v10, v10, |v10|
mav 0,48,40
R 28,11264
v_mul_f32_e64 v11, v11, |v11|
mav 4,52,40
v_cvt_pk_bf16_f32 v10, v10, v11
v_pk_max_i16 v57, v10, 0
mav 0,56,44
D 85,1024
mav 4,60,44
s_waitcnt lgkmcnt(2)
R 32,12288
v_fma_f32 v68, v67, v52, v94
mav 0,64,16
R 36,13312
v_fma_f32 v12, v12, v66, v68
mav 4,68,16
v_fma_f32 v69, v67, v53, v95
v_fma_f32 v13, v13, v66, v69
mav 0,72,20
D 85,2048
mav 4,76,20
s_waitcnt lgkmcnt(2)
R 40,14336
v_mul_f32_e64 v12, v12, |v12|
mav 0,80,24
R 44,15360
v_mul_f32_e64 v13, v13, |v13|
mav 4,84,24
v_cvt_pk_bf16_f32 v12, v12, v13
v_pk_max_i16 v58, v12, 0
mav 0,88,28
D 85,3072
mav 4,92,28
s_waitcnt lgkmcnt(2)
R 16,16384
v_fma_f32 v68, v67, v54, v96
mav 0,96,32
R 20,17408
v_fma_f32 v14, v14, v66, v68
mav 4,100,32
v_fma_f32 v69, v67, v55, v97
v_fma_f32 v15, v15, v66, v69
mav 0,104,36
R 24,18432
v_mul_f32_e64 v14, v14, |v14|
mav 4,108,36
s_waitcnt lgkmcnt(3)
LsE5:
s_cbranch_execz LsE9
v_mul_f32_e64 v15, v15, |v15|
v_cvt_pk_bf16_f32 v14, v14, v15
mav 0,112,40
R 28,19456
v_pk_max_i16 v59, v14, 0
mav 4,116,40
mav 0,120,44
buffer_store_dwordx4 v[56:59], v83, s[36:39], s49 offen sc1
R 32,20480
mav 4,124,44
s_waitcnt lgkmcnt(3)
mav 0,128,16
R 36,21504
mav 4,132,16
mav 0,136,20
R 40,22528
mav 4,140,20
s_waitcnt vmcnt(10) lgkmcnt(3)
v_add_f32 v72, v72, v74
v_add_f32 v73, v73, v75
mav 0,144,24
R 44,23552
mav 4,148,24
v_permlane32_swap_b32 v72, v73
mav 0,152,28
R 16,24576
v_add_f32 v74, v72, v73
mav 4,156,28
s_waitcnt lgkmcnt(3)
v_add_f32 v75, v72, v73
mav 0,160,32
R 20,25600
mav 4,164,32
v_permlane16_swap_b32 v74, v75
mav 0,168,36
R 24,26624
v_add_f32 v72, v74, v75
mav 4,172,36
s_waitcnt lgkmcnt(3)
v_add_f32 v73, v74, v75
mav 0,176,40
R 28,27648
mav 4,180,40
v_permlane32_swap_b32 v72, v73
mav 0,184,44
R 32,28672
v_mul_f32 v74, 0x3a800000, v72
mav 4,188,44
s_waitcnt lgkmcnt(3)
v_mul_f32 v75, 0x3a800000, v73
v_fma_f32 v75, -v74, v74, v75
mav 0,192,16
R 36,29696
v_max_f32 v75, 0, v75
mav 4,196,16
v_add_f32 v75, 0x3727c5ac, v75
mav 0,200,20
R 40,30720
mav 4,204,20
s_waitcnt lgkmcnt(3)
v_rsq_f32 v72, v75
mav 0,208,24
R 44,31744
v_mul_f32_e64 v73, -v74, v72
mav 4,212,24
mav 0,216,28
mav 4,220,28
s_waitcnt lgkmcnt(2)
mav 0,224,32
v_add_u32 v64, 1, v64
mav 4,228,32
mav 0,232,36
mav 4,236,36
s_waitcnt lgkmcnt(0)
mav 0,240,40
mav 4,244,40
mav 0,248,44
mav 4,252,44
ds_write_b64 v88, v[72:73] offset:0
s_xor_b32 s50, s50, 0x10000
s_waitcnt vmcnt(1) lgkmcnt(0)
s_add_u32 s49, s49, 313344
s_barrier
s_add_u32 m0, s50, 0
R 16,32768
buffer_load_dwordx2 v[72:73], v89, s[36:39], s49 offen sc1
buffer_load_dwordx2 v[74:75], v89, s[36:39], s49 offen offset:512 sc1
D 84,0
ds_read_b64 v[66:67], v88 offset:0
R 20,33792
R 24,34816
D 84,1024
R 28,35840
s_waitcnt vmcnt(4)
buffer_store_dword v64, v81, s[36:39], s52 offen sc1
R 32,36864
D 84,2048
s_waitcnt lgkmcnt(3)
v_mfma_f32_16x16x32_bf16 v[8:11], a[0:3], v[16:19], 0
R 36,37888
v_fma_f32 v68, v67, v48, v90
v_mfma_f32_16x16x32_bf16 v[12:15], a[4:7], v[16:19], 0
v_fma_f32 v0, v0, v66, v68
v_fma_f32 v69, v67, v49, v91
mav 8,8,20
R 40,38912
v_fma_f32 v1, v1, v66, v69
mav 12,12,20
s_waitcnt lgkmcnt(3)
v_mul_f32_e64 v0, v0, |v0|
v_mul_f32_e64 v1, v1, |v1|
mav 8,16,24
R 44,39936
v_cvt_pk_bf16_f32 v0, v0, v1
mav 12,20,24
v_pk_max_i16 v56, v0, 0
v_fma_f32 v68, v67, v50, v92
mav 8,24,28
D 84,3072
mav 12,28,28
s_waitcnt lgkmcnt(2)
R 16,40960
s_add_u32 m0, s50, 4096
mav 8,32,32
R 20,41984
v_fma_f32 v2, v2, v66, v68
mav 12,36,32
v_fma_f32 v69, v67, v51, v93
v_fma_f32 v3, v3, v66, v69
mav 8,40,36
D 85,0
mav 12,44,36
s_waitcnt lgkmcnt(2)
R 24,43008
v_mul_f32_e64 v2, v2, |v2|
mav 8,48,40
R 28,44032
v_mul_f32_e64 v3, v3, |v3|
mav 12,52,40
v_cvt_pk_bf16_f32 v2, v2, v3
v_pk_max_i16 v57, v2, 0
mav 8,56,44
D 85,1024
mav 12,60,44
s_waitcnt lgkmcnt(2)
R 32,45056
v_fma_f32 v68, v67, v52, v94
mav 8,64,16
R 36,46080
v_fma_f32 v4, v4, v66, v68
mav 12,68,16
v_fma_f32 v69, v67, v53, v95
v_fma_f32 v5, v5, v66, v69
mav 8,72,20
D 85,2048
mav 12,76,20
s_waitcnt lgkmcnt(2)
R 40,47104
v_mul_f32_e64 v4, v4, |v4|
mav 8,80,24
R 44,48128
v_mul_f32_e64 v5, v5, |v5|
mav 12,84,24
v_cvt_pk_bf16_f32 v4, v4, v5
v_pk_max_i16 v58, v4, 0
mav 8,88,28
D 85,3072
mav 12,92,28
s_waitcnt lgkmcnt(2)
R 16,49152
v_fma_f32 v68, v67, v54, v96
mav 8,96,32
R 20,50176
v_fma_f32 v6, v6, v66, v68
mav 12,100,32
v_fma_f32 v69, v67, v55, v97
v_fma_f32 v7, v7, v66, v69
mav 8,104,36
R 24,51200
v_mul_f32_e64 v6, v6, |v6|
mav 12,108,36
s_waitcnt lgkmcnt(3)
v_mul_f32_e64 v7, v7, |v7|
v_cvt_pk_bf16_f32 v6, v6, v7
mav 8,112,40
R 28,52224
v_pk_max_i16 v59, v6, 0
mav 12,116,40
mav 8,120,44
buffer_store_dwordx4 v[56:59], v83, s[36:39], s49 offen sc1
R 32,53248
mav 12,124,44
s_waitcnt lgkmcnt(3)
mav 8,128,16
R 36,54272
mav 12,132,16
mav 8,136,20
R 40,55296
mav 12,140,20
s_waitcnt vmcnt(10) lgkmcnt(3)
v_add_f32 v72, v72, v74
v_add_f32 v73, v73, v75
mav 8,144,24
R 44,56320
mav 12,148,24
v_permlane32_swap_b32 v72, v73
mav 8,152,28
R 16,57344
v_add_f32 v74, v72, v73
mav 12,156,28
s_waitcnt lgkmcnt(3)
v_add_f32 v75, v72, v73
mav 8,160,32
R 20,58368
mav 12,164,32
v_permlane16_swap_b32 v74, v75
mav 8,168,36
R 24,59392
v_add_f32 v72, v74, v75
mav 12,172,36
s_waitcnt lgkmcnt(3)
v_add_f32 v73, v74, v75
mav 8,176,40
R 28,60416
mav 12,180,40
v_permlane32_swap_b32 v72, v73
mav 8,184,44
R 32,61440
v_mul_f32 v74, 0x3a800000, v72
mav 12,188,44
s_waitcnt lgkmcnt(3)
v_mul_f32 v75, 0x3a800000, v73
v_fma_f32 v75, -v74, v74, v75
mav 8,192,16
R 36,62464
v_max_f32 v75, 0, v75
mav 12,196,16
v_add_f32 v75, 0x3727c5ac, v75
mav 8,200,20
R 40,63488
mav 12,204,20
s_waitcnt lgkmcnt(3)
v_rsq_f32 v72, v75
mav 8,208,24
R 44,64512
v_mul_f32_e64 v73, -v74, v72
mav 12,212,24
mav 8,216,28
mav 12,220,28
s_waitcnt lgkmcnt(2)
mav 8,224,32
v_add_u32 v64, 1, v64
mav 12,228,32
s_add_u32 s60, s60, 1
mav 8,232,36
s_cbranch_scc0 LE1_1
mav 12,236,36
s_waitcnt lgkmcnt(0)
mav 8,240,40
mav 12,244,40
mav 8,248,44
mav 12,252,44
ds_write_b64 v88, v[72:73] offset:128
v_xor_b32 v80, 0x10000, v80
s_waitcnt vmcnt(1) lgkmcnt(0)
s_add_u32 s49, s49, 313344
s_barrier
s_add_u32 m0, s50, 32768
R 16,0
buffer_load_dwordx2 v[72:73], v89, s[36:39], s49 offen sc1
buffer_load_dwordx2 v[74:75], v89, s[36:39], s49 offen offset:512 sc1
D 84,0
ds_read_b64 v[66:67], v88 offset:128
R 20,1024
R 24,2048
D 84,1024
R 28,3072
s_waitcnt vmcnt(4)
buffer_store_dword v64, v81, s[36:39], s52 offen sc1
R 32,4096
D 84,2048
s_waitcnt lgkmcnt(3)
v_mfma_f32_16x16x32_bf16 v[0:3], a[0:3], v[16:19], 0
R 36,5120
v_fma_f32 v68, v67, v48, v90
v_mfma_f32_16x16x32_bf16 v[4:7], a[4:7], v[16:19], 0
v_fma_f32 v8, v8, v66, v68
v_fma_f32 v69, v67, v49, v91
mav 0,8,20
R 40,6144
v_fma_f32 v9, v9, v66, v69
mav 4,12,20
s_waitcnt lgkmcnt(3)
v_mul_f32_e64 v8, v8, |v8|
v_mul_f32_e64 v9, v9, |v9|
mav 0,16,24
R 44,7168
v_cvt_pk_bf16_f32 v8, v8, v9
mav 4,20,24
v_pk_max_i16 v56, v8, 0
v_fma_f32 v68, v67, v50, v92
mav 0,24,28
D 84,3072
mav 4,28,28
s_waitcnt lgkmcnt(2)
R 16,8192
s_add_u32 m0, s50, 36864
mav 0,32,32
R 20,9216
v_fma_f32 v10, v10, v66, v68
mav 4,36,32
v_fma_f32 v69, v67, v51, v93
v_fma_f32 v11, v11, v66, v69
mav 0,40,36
D 85,0
mav 4,44,36
s_waitcnt lgkmcnt(2)
R 24,10240
v_mul_f32_e64 v10, v10, |v10|
mav 0,48,40
R 28,11264
v_mul_f32_e64 v11, v11, |v11|
mav 4,52,40
v_cvt_pk_bf16_f32 v10, v10, v11
v_pk_max_i16 v57, v10, 0
mav 0,56,44
D 85,1024
mav 4,60,44
s_waitcnt lgkmcnt(2)
R 32,12288
v_fma_f32 v68, v67, v52, v94
mav 0,64,16
R 36,13312
v_fma_f32 v12, v12, v66, v68
mav 4,68,16
v_fma_f32 v69, v67, v53, v95
v_fma_f32 v13, v13, v66, v69
mav 0,72,20
D 85,2048
mav 4,76,20
s_waitcnt lgkmcnt(2)
R 40,14336
v_mul_f32_e64 v12, v12, |v12|
mav 0,80,24
R 44,15360
v_mul_f32_e64 v13, v13, |v13|
mav 4,84,24
v_cvt_pk_bf16_f32 v12, v12, v13
v_pk_max_i16 v58, v12, 0
mav 0,88,28
D 85,3072
mav 4,92,28
s_waitcnt lgkmcnt(2)
R 16,16384
v_fma_f32 v68, v67, v54, v96
mav 0,96,32
R 20,17408
v_fma_f32 v14, v14, v66, v68
mav 4,100,32
v_fma_f32 v69, v67, v55, v97
v_fma_f32 v15, v15, v66, v69
mav 0,104,36
R 24,18432
v_mul_f32_e64 v14, v14, |v14|
mav 4,108,36
s_waitcnt lgkmcnt(3)
v_mul_f32_e64 v15, v15, |v15|
v_cvt_pk_bf16_f32 v14, v14, v15
mav 0,112,40
R 28,19456
v_pk_max_i16 v59, v14, 0
mav 4,116,40
mav 0,120,44
buffer_store_dwordx4 v[56:59], v83, s[36:39], s49 offen sc1
R 32,20480
mav 4,124,44
s_waitcnt lgkmcnt(3)
mav 0,128,16
R 36,21504
mav 4,132,16
mav 0,136,20
R 40,22528
mav 4,140,20
s_waitcnt vmcnt(10) lgkmcnt(3)
v_add_f32 v72, v72, v74
v_add_f32 v73, v73, v75
mav 0,144,24
R 44,23552
mav 4,148,24
v_permlane32_swap_b32 v72, v73
mav 0,152,28
R 16,24576
v_add_f32 v74, v72, v73
mav 4,156,28
s_waitcnt lgkmcnt(3)
v_add_f32 v75, v72, v73
mav 0,160,32
R 20,25600
mav 4,164,32
v_permlane16_swap_b32 v74, v75
mav 0,168,36
R 24,26624
v_add_f32 v72, v74, v75
mav 4,172,36
s_waitcnt lgkmcnt(3)
v_add_f32 v73, v74, v75
mav 0,176,40
R 28,27648
mav 4,180,40
v_permlane32_swap_b32 v72, v73
mav 0,184,44
R 32,28672
v_mul_f32 v74, 0x3a800000, v72
mav 4,188,44
s_waitcnt lgkmcnt(3)
v_mul_f32 v75, 0x3a800000, v73
v_fma_f32 v75, -v74, v74, v75
mav 0,192,16
R 36,29696
v_max_f32 v75, 0, v75
mav 4,196,16
v_add_f32 v75, 0x3727c5ac, v75
mav 0,200,20
R 40,30720
mav 4,204,20
s_waitcnt lgkmcnt(3)
v_rsq_f32 v72, v75
mav 0,208,24
R 44,31744
v_mul_f32_e64 v73, -v74, v72
mav 4,212,24
mav 0,216,28
mav 4,220,28
s_waitcnt lgkmcnt(2)
mav 0,224,32
v_add_u32 v64, 1, v64
mav 4,228,32
mav 0,232,36
mav 4,236,36
s_waitcnt lgkmcnt(0)
mav 0,240,40
mav 4,244,40
mav 0,248,44
mav 4,252,44
ds_write_b64 v88, v[72:73] offset:0
s_waitcnt vmcnt(1) lgkmcnt(0)
s_add_u32 s49, s49, 313344
s_barrier
R 16,32768
buffer_load_dwordx2 v[72:73], v89, s[36:39], s49 offen sc1
buffer_load_dwordx2 v[74:75], v89, s[36:39], s49 offen offset:512 sc1
ds_read_b64 v[66:67], v88 offset:0
R 20,33792
R 24,34816
R 28,35840
s_waitcnt vmcnt(2)
buffer_store_dword v64, v81, s[36:39], s52 offen sc1
R 32,36864
s_waitcnt lgkmcnt(3)
v_mfma_f32_16x16x32_bf16 v[8:11], a[0:3], v[16:19], 0
R 36,37888
v_fma_f32 v68, v67, v48, v90
v_mfma_f32_16x16x32_bf16 v[12:15], a[4:7], v[16:19], 0
v_fma_f32 v0, v0, v66, v68
v_fma_f32 v69, v67, v49, v91
mav 8,8,20
R 40,38912
v_fma_f32 v1, v1, v66, v69
mav 12,12,20
s_waitcnt lgkmcnt(3)
v_mul_f32_e64 v0, v0, |v0|
v_mul_f32_e64 v1, v1, |v1|
mav 8,16,24
R 44,39936
v_cvt_pk_bf16_f32 v0, v0, v1
mav 12,20,24
v_pk_max_i16 v56, v0, 0
v_fma_f32 v68, v67, v50, v92
mav 8,24,28
R 16,40960
v_fma_f32 v2, v2, v66, v68
mav 12,28,28
s_waitcnt lgkmcnt(3)
v_fma_f32 v69, v67, v51, v93
v_fma_f32 v3, v3, v66, v69
mav 8,32,32
R 20,41984
v_mul_f32_e64 v2, v2, |v2|
mav 12,36,32
v_mul_f32_e64 v3, v3, |v3|
v_cvt_pk_bf16_f32 v2, v2, v3
mav 8,40,36
R 24,43008
v_pk_max_i16 v57, v2, 0
mav 12,44,36
s_waitcnt lgkmcnt(3)
v_fma_f32 v68, v67, v52, v94
v_fma_f32 v4, v4, v66, v68
mav 8,48,40
R 28,44032
v_fma_f32 v69, v67, v53, v95
mav 12,52,40
v_fma_f32 v5, v5, v66, v69
v_mul_f32_e64 v4, v4, |v4|
mav 8,56,44
R 32,45056
v_mul_f32_e64 v5, v5, |v5|
mav 12,60,44
s_waitcnt lgkmcnt(3)
v_cvt_pk_bf16_f32 v4, v4, v5
v_pk_max_i16 v58, v4, 0
mav 8,64,16
R 36,46080
v_fma_f32 v68, v67, v54, v96
mav 12,68,16
v_fma_f32 v6, v6, v66, v68
v_fma_f32 v69, v67, v55, v97
mav 8,72,20
R 40,47104
v_fma_f32 v7, v7, v66, v69
mav 12,76,20
s_waitcnt lgkmcnt(3)
v_mul_f32_e64 v6, v6, |v6|
v_mul_f32_e64 v7, v7, |v7|
mav 8,80,24
R 44,48128
v_cvt_pk_bf16_f32 v6, v6, v7
mav 12,84,24
)"
R"(
v_pk_max_i16 v59, v6, 0
mav 8,88,28
buffer_store_dwordx4 v[56:59], v83, s[36:39], s49 offen sc1
R 16,49152
mav 12,92,28
s_waitcnt lgkmcnt(3)
mav 8,96,32
R 20,50176
mav 12,100,32
mav 8,104,36
R 24,51200
mav 12,108,36
s_waitcnt lgkmcnt(3)
mav 8,112,40
R 28,52224
mav 12,116,40
mav 8,120,44
R 32,53248
mav 12,124,44
s_waitcnt lgkmcnt(3)
mav 8,128,16
R 36,54272
mav 12,132,16
mav 8,136,20
R 40,55296
mav 12,140,20
s_waitcnt vmcnt(2) lgkmcnt(3)
v_add_f32 v72, v72, v74
v_add_f32 v73, v73, v75
mav 8,144,24
R 44,56320
mav 12,148,24
v_permlane32_swap_b32 v72, v73
mav 8,152,28
LsE6:
s_cbranch_execz LsE10
R 16,57344
v_add_f32 v74, v72, v73
mav 12,156,28
s_waitcnt lgkmcnt(3)
v_add_f32 v75, v72, v73
mav 8,160,32
R 20,58368
mav 12,164,32
v_permlane16_swap_b32 v74, v75
mav 8,168,36
R 24,59392
v_add_f32 v72, v74, v75
mav 12,172,36
s_waitcnt lgkmcnt(3)
v_add_f32 v73, v74, v75
mav 8,176,40
R 28,60416
mav 12,180,40
v_permlane32_swap_b32 v72, v73
mav 8,184,44
R 32,61440
v_mul_f32 v74, 0x3a800000, v72
mav 12,188,44
s_waitcnt lgkmcnt(3)
v_mul_f32 v75, 0x3a800000, v73
v_fma_f32 v75, -v74, v74, v75
mav 8,192,16
R 36,62464
v_max_f32 v75, 0, v75
mav 12,196,16
v_add_f32 v75, 0x3727c5ac, v75
mav 8,200,20
R 40,63488
mav 12,204,20
s_waitcnt lgkmcnt(3)
v_rsq_f32 v72, v75
mav 8,208,24
R 44,64512
v_mul_f32_e64 v73, -v74, v72
mav 12,212,24
mav 8,216,28
mav 12,220,28
s_waitcnt lgkmcnt(2)
mav 8,224,32
v_add_u32 v64, 1, v64
mav 12,228,32
mav 8,232,36
mav 12,236,36
s_waitcnt lgkmcnt(0)
mav 8,240,40
mav 12,244,40
mav 8,248,44
mav 12,252,44
ds_write_b64 v88, v[72:73] offset:128
s_waitcnt lgkmcnt(0)
s_add_u32 s49, s49, 313344
s_barrier
ds_read_b64 v[66:67], v88 offset:128
s_waitcnt vmcnt(0)
buffer_store_dword v64, v81, s[36:39], s52 offen sc1
s_waitcnt lgkmcnt(0)
v_fma_f32 v68, v67, v48, v90
v_fma_f32 v8, v8, v66, v68
v_fma_f32 v69, v67, v49, v91
v_fma_f32 v9, v9, v66, v69
v_mul_f32_e64 v8, v8, |v8|
v_mul_f32_e64 v9, v9, |v9|
v_cvt_pk_bf16_f32 v8, v8, v9
v_pk_max_i16 v56, v8, 0
v_fma_f32 v68, v67, v50, v92
v_fma_f32 v10, v10, v66, v68
v_fma_f32 v69, v67, v51, v93
v_fma_f32 v11, v11, v66, v69
v_mul_f32_e64 v10, v10, |v10|
v_mul_f32_e64 v11, v11, |v11|
v_cvt_pk_bf16_f32 v10, v10, v11
v_pk_max_i16 v57, v10, 0
v_fma_f32 v68, v67, v52, v94
v_fma_f32 v12, v12, v66, v68
v_fma_f32 v69, v67, v53, v95
v_fma_f32 v13, v13, v66, v69
v_mul_f32_e64 v12, v12, |v12|
v_mul_f32_e64 v13, v13, |v13|
v_cvt_pk_bf16_f32 v12, v12, v13
v_pk_max_i16 v58, v12, 0
v_fma_f32 v68, v67, v54, v96
v_fma_f32 v14, v14, v66, v68
v_fma_f32 v69, v67, v55, v97
v_fma_f32 v15, v15, v66, v69
v_mul_f32_e64 v14, v14, |v14|
v_mul_f32_e64 v15, v15, |v15|
v_cvt_pk_bf16_f32 v14, v14, v15
v_pk_max_i16 v59, v14, 0
buffer_store_dwordx4 v[56:59], v83, s[36:39], s49 offen sc1
v_add_u32 v64, 37, v64
s_waitcnt vmcnt(0)
buffer_store_dword v64, v81, s[36:39], s52 offen sc1
s_endpgm
LvE2:
LsE7:
s_cbranch_execz LsE11
s_add_u32 s49, s49, 313344
s_barrier
s_add_u32 m0, s50, 0
s_nop 0
D 84,0
D 84,1024
D 84,2048
D 84,3072
s_add_u32 m0, s50, 4096
s_nop 0
D 85,0
D 85,1024
D 85,2048
D 85,3072
s_waitcnt vmcnt(0)
s_add_u32 s49, s49, 313344
s_barrier
s_add_u32 m0, s50, 32768
R 16,0
D 84,0
R 20,1024
R 24,2048
D 84,1024
R 28,3072
R 32,4096
D 84,2048
s_waitcnt lgkmcnt(3)
v_mfma_f32_16x16x32_bf16 v[0:3], a[0:3], v[16:19], 0
R 36,5120
v_mfma_f32_16x16x32_bf16 v[4:7], a[4:7], v[16:19], 0
mav 0,8,20
R 40,6144
mav 4,12,20
s_waitcnt lgkmcnt(3)
mav 0,16,24
D 84,3072
mav 4,20,24
s_add_u32 m0, s50, 36864
R 44,7168
mav 0,24,28
R 16,8192
mav 4,28,28
s_waitcnt lgkmcnt(3)
mav 0,32,32
D 85,0
mav 4,36,32
R 20,9216
mav 0,40,36
R 24,10240
mav 4,44,36
s_waitcnt lgkmcnt(3)
mav 0,48,40
D 85,1024
mav 4,52,40
R 28,11264
mav 0,56,44
R 32,12288
mav 4,60,44
s_waitcnt lgkmcnt(3)
mav 0,64,16
D 85,2048
mav 4,68,16
R 36,13312
mav 0,72,20
R 40,14336
mav 4,76,20
s_waitcnt lgkmcnt(3)
mav 0,80,24
D 85,3072
mav 4,84,24
R 44,15360
mav 0,88,28
R 16,16384
mav 4,92,28
s_waitcnt lgkmcnt(3)
mav 0,96,32
R 20,17408
mav 4,100,32
mav 0,104,36
R 24,18432
mav 4,108,36
s_waitcnt lgkmcnt(3)
mav 0,112,40
R 28,19456
mav 4,116,40
mav 0,120,44
R 32,20480
mav 4,124,44
s_waitcnt lgkmcnt(3)
mav 0,128,16
R 36,21504
mav 4,132,16
mav 0,136,20
R 40,22528
mav 4,140,20
s_waitcnt lgkmcnt(3)
mav 0,144,24
R 44,23552
mav 4,148,24
mav 0,152,28
R 16,24576
mav 4,156,28
s_waitcnt lgkmcnt(3)
mav 0,160,32
R 20,25600
mav 4,164,32
mav 0,168,36
R 24,26624
mav 4,172,36
s_waitcnt lgkmcnt(3)
mav 0,176,40
R 28,27648
mav 4,180,40
mav 0,184,44
R 32,28672
mav 4,188,44
s_waitcnt lgkmcnt(3)
mav 0,192,16
R 36,29696
mav 4,196,16
mav 0,200,20
R 40,30720
mav 4,204,20
s_waitcnt lgkmcnt(3)
mav 0,208,24
R 44,31744
mav 4,212,24
mav 0,216,28
mav 4,220,28
s_waitcnt lgkmcnt(2)
mav 0,224,32
mav 4,228,32
mav 0,232,36
mav 4,236,36
s_waitcnt lgkmcnt(0)
mav 0,240,40
mav 4,244,40
mav 0,248,44
mav 4,252,44
s_xor_b32 s50, s50, 0x10000
s_waitcnt vmcnt(0)
s_add_u32 s49, s49, 313344
s_barrier
s_add_u32 m0, s50, 0
R 16,32768
D 84,0
ds_read_b64 v[66:67], v88 offset:0
R 20,33792
R 24,34816
D 84,1024
R 28,35840
R 32,36864
D 84,2048
s_waitcnt lgkmcnt(3)
v_mfma_f32_16x16x32_bf16 v[8:11], a[0:3], v[16:19], 0
R 36,37888
v_fma_f32 v68, v67, v48, v90
v_mfma_f32_16x16x32_bf16 v[12:15], a[4:7], v[16:19], 0
v_fma_f32 v0, v0, v66, v68
v_fma_f32 v69, v67, v49, v91
mav 8,8,20
R 40,38912
v_fma_f32 v1, v1, v66, v69
mav 12,12,20
s_waitcnt lgkmcnt(3)
v_mul_f32_e64 v0, v0, |v0|
v_mul_f32_e64 v1, v1, |v1|
mav 8,16,24
D 84,3072
mav 12,20,24
s_add_u32 m0, s50, 4096
R 44,39936
v_cvt_pk_bf16_f32 v0, v0, v1
mav 8,24,28
R 16,40960
v_pk_max_i16 v56, v0, 0
mav 12,28,28
s_waitcnt lgkmcnt(3)
v_fma_f32 v68, v67, v50, v92
v_fma_f32 v2, v2, v66, v68
mav 8,32,32
D 85,0
mav 12,36,32
R 20,41984
v_fma_f32 v69, v67, v51, v93
mav 8,40,36
R 24,43008
v_fma_f32 v3, v3, v66, v69
mav 12,44,36
s_waitcnt lgkmcnt(3)
v_mul_f32_e64 v2, v2, |v2|
v_mul_f32_e64 v3, v3, |v3|
mav 8,48,40
D 85,1024
mav 12,52,40
R 28,44032
v_cvt_pk_bf16_f32 v2, v2, v3
mav 8,56,44
R 32,45056
v_pk_max_i16 v57, v2, 0
mav 12,60,44
s_waitcnt lgkmcnt(3)
v_fma_f32 v68, v67, v52, v94
v_fma_f32 v4, v4, v66, v68
mav 8,64,16
D 85,2048
mav 12,68,16
R 36,46080
v_fma_f32 v69, v67, v53, v95
mav 8,72,20
R 40,47104
v_fma_f32 v5, v5, v66, v69
mav 12,76,20
s_waitcnt lgkmcnt(3)
v_mul_f32_e64 v4, v4, |v4|
v_mul_f32_e64 v5, v5, |v5|
mav 8,80,24
D 85,3072
mav 12,84,24
R 44,48128
v_cvt_pk_bf16_f32 v4, v4, v5
mav 8,88,28
R 16,49152
v_pk_max_i16 v58, v4, 0
mav 12,92,28
s_waitcnt lgkmcnt(3)
v_fma_f32 v68, v67, v54, v96
v_fma_f32 v6, v6, v66, v68
mav 8,96,32
R 20,50176
v_fma_f32 v69, v67, v55, v97
mav 12,100,32
v_fma_f32 v7, v7, v66, v69
v_mul_f32_e64 v6, v6, |v6|
mav 8,104,36
R 24,51200
v_mul_f32_e64 v7, v7, |v7|
mav 12,108,36
s_waitcnt lgkmcnt(3)
v_cvt_pk_bf16_f32 v6, v6, v7
v_pk_max_i16 v59, v6, 0
mav 8,112,40
buffer_store_dwordx4 v[56:59], v83, s[36:39], s49 offen sc1
R 28,52224
mav 12,116,40
mav 8,120,44
R 32,53248
mav 12,124,44
s_waitcnt lgkmcnt(3)
mav 8,128,16
R 36,54272
mav 12,132,16
mav 8,136,20
R 40,55296
mav 12,140,20
s_waitcnt lgkmcnt(3)
mav 8,144,24
R 44,56320
mav 12,148,24
mav 8,152,28
R 16,57344
mav 12,156,28
s_waitcnt lgkmcnt(3)
mav 8,160,32
R 20,58368
mav 12,164,32
mav 8,168,36
R 24,59392
mav 12,172,36
s_waitcnt lgkmcnt(3)
mav 8,176,40
R 28,60416
mav 12,180,40
mav 8,184,44
R 32,61440
mav 12,188,44
s_waitcnt lgkmcnt(3)
mav 8,192,16
R 36,62464
mav 12,196,16
mav 8,200,20
R 40,63488
mav 12,204,20
s_waitcnt lgkmcnt(3)
mav 8,208,24
R 44,64512
mav 12,212,24
mav 8,216,28
mav 12,220,28
s_waitcnt lgkmcnt(2)
mav 8,224,32
v_add_u32 v64, 1, v64
mav 12,228,32
mav 8,232,36
s_mov_b32 s60, -30
.p2align 6
LE2_1:
mav 12,236,36
s_waitcnt lgkmcnt(0)
mav 8,240,40
mav 12,244,40
mav 8,248,44
mav 12,252,44
v_xor_b32 v80, 0x10000, v80
s_waitcnt vmcnt(1)
s_add_u32 s49, s49, 313344
s_barrier
s_add_u32 m0, s50, 32768
R 16,0
D 84,0
ds_read_b64 v[66:67], v88 offset:128
R 20,1024
R 24,2048
D 84,1024
R 28,3072
s_waitcnt vmcnt(2)
buffer_store_dword v64, v81, s[36:39], s52 offen sc1
R 32,4096
D 84,2048
s_waitcnt lgkmcnt(3)
v_mfma_f32_16x16x32_bf16 v[0:3], a[0:3], v[16:19], 0
R 36,5120
v_fma_f32 v68, v67, v48, v90
v_mfma_f32_16x16x32_bf16 v[4:7], a[4:7], v[16:19], 0
v_fma_f32 v8, v8, v66, v68
v_fma_f32 v69, v67, v49, v91
mav 0,8,20
R 40,6144
v_fma_f32 v9, v9, v66, v69
mav 4,12,20
s_waitcnt lgkmcnt(3)
v_mul_f32_e64 v8, v8, |v8|
v_mul_f32_e64 v9, v9, |v9|
mav 0,16,24
D 84,3072
mav 4,20,24
s_add_u32 m0, s50, 36864
R 44,7168
v_cvt_pk_bf16_f32 v8, v8, v9
mav 0,24,28
R 16,8192
v_pk_max_i16 v56, v8, 0
mav 4,28,28
s_waitcnt lgkmcnt(3)
v_fma_f32 v68, v67, v50, v92
v_fma_f32 v10, v10, v66, v68
mav 0,32,32
D 85,0
mav 4,36,32
R 20,9216
v_fma_f32 v69, v67, v51, v93
mav 0,40,36
R 24,10240
v_fma_f32 v11, v11, v66, v69
mav 4,44,36
s_waitcnt lgkmcnt(3)
v_mul_f32_e64 v10, v10, |v10|
v_mul_f32_e64 v11, v11, |v11|
mav 0,48,40
D 85,1024
mav 4,52,40
R 28,11264
v_cvt_pk_bf16_f32 v10, v10, v11
mav 0,56,44
R 32,12288
v_pk_max_i16 v57, v10, 0
mav 4,60,44
s_waitcnt lgkmcnt(3)
v_fma_f32 v68, v67, v52, v94
v_fma_f32 v12, v12, v66, v68
mav 0,64,16
D 85,2048
mav 4,68,16
R 36,13312
v_fma_f32 v69, v67, v53, v95
mav 0,72,20
R 40,14336
v_fma_f32 v13, v13, v66, v69
mav 4,76,20
s_waitcnt lgkmcnt(3)
v_mul_f32_e64 v12, v12, |v12|
v_mul_f32_e64 v13, v13, |v13|
mav 0,80,24
D 85,3072
mav 4,84,24
R 44,15360
v_cvt_pk_bf16_f32 v12, v12, v13
mav 0,88,28
R 16,16384
v_pk_max_i16 v58, v12, 0
mav 4,92,28
s_waitcnt lgkmcnt(3)
v_fma_f32 v68, v67, v54, v96
v_fma_f32 v14, v14, v66, v68
mav 0,96,32
R 20,17408
v_fma_f32 v69, v67, v55, v97
mav 4,100,32
v_fma_f32 v15, v15, v66, v69
v_mul_f32_e64 v14, v14, |v14|
mav 0,104,36
R 24,18432
v_mul_f32_e64 v15, v15, |v15|
mav 4,108,36
s_waitcnt lgkmcnt(3)
v_cvt_pk_bf16_f32 v14, v14, v15
v_pk_max_i16 v59, v14, 0
mav 0,112,40
buffer_store_dwordx4 v[56:59], v83, s[36:39], s49 offen sc1
R 28,19456
mav 4,116,40
mav 0,120,44
R 32,20480
mav 4,124,44
s_waitcnt lgkmcnt(3)
mav 0,128,16
R 36,21504
mav 4,132,16
mav 0,136,20
R 40,22528
mav 4,140,20
s_waitcnt lgkmcnt(3)
mav 0,144,24
R 44,23552
mav 4,148,24
mav 0,152,28
R 16,24576
mav 4,156,28
s_waitcnt lgkmcnt(3)
mav 0,160,32
R 20,25600
mav 4,164,32
mav 0,168,36
R 24,26624
mav 4,172,36
s_waitcnt lgkmcnt(3)
mav 0,176,40
R 28,27648
mav 4,180,40
mav 0,184,44
R 32,28672
mav 4,188,44
s_waitcnt lgkmcnt(3)
mav 0,192,16
R 36,29696
mav 4,196,16
mav 0,200,20
R 40,30720
mav 4,204,20
s_waitcnt lgkmcnt(3)
LsE8:
s_cbranch_execz LwE
mav 0,208,24
R 44,31744
mav 4,212,24
mav 0,216,28
mav 4,220,28
s_waitcnt lgkmcnt(2)
mav 0,224,32
v_add_u32 v64, 1, v64
mav 4,228,32
mav 0,232,36
mav 4,236,36
s_waitcnt lgkmcnt(0)
mav 0,240,40
mav 4,244,40
mav 0,248,44
mav 4,252,44
s_xor_b32 s50, s50, 0x10000
s_waitcnt vmcnt(1)
s_add_u32 s49, s49, 313344
s_barrier
s_add_u32 m0, s50, 0
R 16,32768
D 84,0
ds_read_b64 v[66:67], v88 offset:0
R 20,33792
R 24,34816
D 84,1024
R 28,35840
s_waitcnt vmcnt(2)
buffer_store_dword v64, v81, s[36:39], s52 offen sc1
R 32,36864
D 84,2048
s_waitcnt lgkmcnt(3)
v_mfma_f32_16x16x32_bf16 v[8:11], a[0:3], v[16:19], 0
R 36,37888
v_fma_f32 v68, v67, v48, v90
v_mfma_f32_16x16x32_bf16 v[12:15], a[4:7], v[16:19], 0
v_fma_f32 v0, v0, v66, v68
v_fma_f32 v69, v67, v49, v91
mav 8,8,20
R 40,38912
v_fma_f32 v1, v1, v66, v69
mav 12,12,20
s_waitcnt lgkmcnt(3)
v_mul_f32_e64 v0, v0, |v0|
v_mul_f32_e64 v1, v1, |v1|
mav 8,16,24
D 84,3072
mav 12,20,24
s_add_u32 m0, s50, 4096
R 44,39936
v_cvt_pk_bf16_f32 v0, v0, v1
mav 8,24,28
R 16,40960
v_pk_max_i16 v56, v0, 0
mav 12,28,28
s_waitcnt lgkmcnt(3)
v_fma_f32 v68, v67, v50, v92
v_fma_f32 v2, v2, v66, v68
mav 8,32,32
D 85,0
mav 12,36,32
R 20,41984
v_fma_f32 v69, v67, v51, v93
mav 8,40,36
R 24,43008
v_fma_f32 v3, v3, v66, v69
mav 12,44,36
s_waitcnt lgkmcnt(3)
v_mul_f32_e64 v2, v2, |v2|
v_mul_f32_e64 v3, v3, |v3|
mav 8,48,40
D 85,1024
mav 12,52,40
R 28,44032
v_cvt_pk_bf16_f32 v2, v2, v3
mav 8,56,44
R 32,45056
v_pk_max_i16 v57, v2, 0
mav 12,60,44
s_waitcnt lgkmcnt(3)
v_fma_f32 v68, v67, v52, v94
v_fma_f32 v4, v4, v66, v68
mav 8,64,16
D 85,2048
mav 12,68,16
R 36,46080
v_fma_f32 v69, v67, v53, v95
mav 8,72,20
R 40,47104
v_fma_f32 v5, v5, v66, v69
mav 12,76,20
s_waitcnt lgkmcnt(3)
v_mul_f32_e64 v4, v4, |v4|
v_mul_f32_e64 v5, v5, |v5|
mav 8,80,24
D 85,3072
mav 12,84,24
R 44,48128
v_cvt_pk_bf16_f32 v4, v4, v5
mav 8,88,28
R 16,49152
v_pk_max_i16 v58, v4, 0
mav 12,92,28
s_waitcnt lgkmcnt(3)
v_fma_f32 v68, v67, v54, v96
v_fma_f32 v6, v6, v66, v68
mav 8,96,32
R 20,50176
v_fma_f32 v69, v67, v55, v97
mav 12,100,32
v_fma_f32 v7, v7, v66, v69
v_mul_f32_e64 v6, v6, |v6|
mav 8,104,36
R 24,51200
v_mul_f32_e64 v7, v7, |v7|
mav 12,108,36
s_waitcnt lgkmcnt(3)
v_cvt_pk_bf16_f32 v6, v6, v7
v_pk_max_i16 v59, v6, 0
mav 8,112,40
buffer_store_dwordx4 v[56:59], v83, s[36:39], s49 offen sc1
R 28,52224
mav 12,116,40
mav 8,120,44
R 32,53248
mav 12,124,44
s_waitcnt lgkmcnt(3)
mav 8,128,16
R 36,54272
mav 12,132,16
mav 8,136,20
R 40,55296
mav 12,140,20
s_waitcnt lgkmcnt(3)
mav 8,144,24
R 44,56320
mav 12,148,24
mav 8,152,28
R 16,57344
mav 12,156,28
s_waitcnt lgkmcnt(3)
mav 8,160,32
R 20,58368
mav 12,164,32
mav 8,168,36
R 24,59392
mav 12,172,36
s_waitcnt lgkmcnt(3)
mav 8,176,40
R 28,60416
mav 12,180,40
mav 8,184,44
R 32,61440
mav 12,188,44
s_waitcnt lgkmcnt(3)
mav 8,192,16
R 36,62464
mav 12,196,16
mav 8,200,20
R 40,63488
mav 12,204,20
s_waitcnt lgkmcnt(3)
mav 8,208,24
R 44,64512
mav 12,212,24
mav 8,216,28
mav 12,220,28
s_waitcnt lgkmcnt(2)
mav 8,224,32
v_add_u32 v64, 1, v64
mav 12,228,32
s_add_u32 s60, s60, 1
mav 8,232,36
s_cbranch_scc0 LE2_1
mav 12,236,36
s_waitcnt lgkmcnt(0)
mav 8,240,40
mav 12,244,40
mav 8,248,44
mav 12,252,44
v_xor_b32 v80, 0x10000, v80
s_waitcnt vmcnt(1)
s_add_u32 s49, s49, 313344
s_barrier
s_add_u32 m0, s50, 32768
R 16,0
D 84,0
ds_read_b64 v[66:67], v88 offset:128
R 20,1024
R 24,2048
D 84,1024
R 28,3072
s_waitcnt vmcnt(2)
buffer_store_dword v64, v81, s[36:39], s52 offen sc1
R 32,4096
D 84,2048
s_waitcnt lgkmcnt(3)
v_mfma_f32_16x16x32_bf16 v[0:3], a[0:3], v[16:19], 0
R 36,5120
v_fma_f32 v68, v67, v48, v90
v_mfma_f32_16x16x32_bf16 v[4:7], a[4:7], v[16:19], 0
v_fma_f32 v8, v8, v66, v68
v_fma_f32 v69, v67, v49, v91
mav 0,8,20
R 40,6144
v_fma_f32 v9, v9, v66, v69
mav 4,12,20
s_waitcnt lgkmcnt(3)
v_mul_f32_e64 v8, v8, |v8|
v_mul_f32_e64 v9, v9, |v9|
mav 0,16,24
D 84,3072
mav 4,20,24
s_add_u32 m0, s50, 36864
R 44,7168
v_cvt_pk_bf16_f32 v8, v8, v9
mav 0,24,28
R 16,8192
v_pk_max_i16 v56, v8, 0
mav 4,28,28
s_waitcnt lgkmcnt(3)
v_fma_f32 v68, v67, v50, v92
v_fma_f32 v10, v10, v66, v68
mav 0,32,32
D 85,0
mav 4,36,32
R 20,9216
v_fma_f32 v69, v67, v51, v93
mav 0,40,36
R 24,10240
v_fma_f32 v11, v11, v66, v69
mav 4,44,36
s_waitcnt lgkmcnt(3)
v_mul_f32_e64 v10, v10, |v10|
v_mul_f32_e64 v11, v11, |v11|
mav 0,48,40
D 85,1024
mav 4,52,40
R 28,11264
v_cvt_pk_bf16_f32 v10, v10, v11
mav 0,56,44
R 32,12288
v_pk_max_i16 v57, v10, 0
mav 4,60,44
s_waitcnt lgkmcnt(3)
v_fma_f32 v68, v67, v52, v94
v_fma_f32 v12, v12, v66, v68
mav 0,64,16
D 85,2048
mav 4,68,16
R 36,13312
v_fma_f32 v69, v67, v53, v95
mav 0,72,20
R 40,14336
v_fma_f32 v13, v13, v66, v69
mav 4,76,20
s_waitcnt lgkmcnt(3)
v_mul_f32_e64 v12, v12, |v12|
v_mul_f32_e64 v13, v13, |v13|
mav 0,80,24
D 85,3072
mav 4,84,24
R 44,15360
v_cvt_pk_bf16_f32 v12, v12, v13
mav 0,88,28
R 16,16384
v_pk_max_i16 v58, v12, 0
mav 4,92,28
s_waitcnt lgkmcnt(3)
v_fma_f32 v68, v67, v54, v96
v_fma_f32 v14, v14, v66, v68
mav 0,96,32
R 20,17408
v_fma_f32 v69, v67, v55, v97
mav 4,100,32
v_fma_f32 v15, v15, v66, v69
v_mul_f32_e64 v14, v14, |v14|
mav 0,104,36
R 24,18432
v_mul_f32_e64 v15, v15, |v15|
mav 4,108,36
s_waitcnt lgkmcnt(3)
v_cvt_pk_bf16_f32 v14, v14, v15
v_pk_max_i16 v59, v14, 0
mav 0,112,40
buffer_store_dwordx4 v[56:59], v83, s[36:39], s49 offen sc1
R 28,19456
mav 4,116,40
mav 0,120,44
R 32,20480
mav 4,124,44
s_waitcnt lgkmcnt(3)
mav 0,128,16
R 36,21504
mav 4,132,16
mav 0,136,20
R 40,22528
mav 4,140,20
s_waitcnt lgkmcnt(3)
mav 0,144,24
R 44,23552
mav 4,148,24
mav 0,152,28
R 16,24576
mav 4,156,28
s_waitcnt lgkmcnt(3)
mav 0,160,32
R 20,25600
mav 4,164,32
mav 0,168,36
R 24,26624
mav 4,172,36
s_waitcnt lgkmcnt(3)
mav 0,176,40
R 28,27648
mav 4,180,40
mav 0,184,44
R 32,28672
mav 4,188,44
s_waitcnt lgkmcnt(3)
mav 0,192,16
R 36,29696
mav 4,196,16
mav 0,200,20
R 40,30720
mav 4,204,20
s_waitcnt lgkmcnt(3)
mav 0,208,24
R 44,31744
mav 4,212,24
mav 0,216,28
mav 4,220,28
s_waitcnt lgkmcnt(2)
mav 0,224,32
v_add_u32 v64, 1, v64
mav 4,228,32
mav 0,232,36
mav 4,236,36
s_waitcnt lgkmcnt(0)
mav 0,240,40
mav 4,244,40
mav 0,248,44
mav 4,252,44
s_waitcnt vmcnt(1)
s_add_u32 s49, s49, 313344
s_barrier
R 16,32768
ds_read_b64 v[66:67], v88 offset:0
R 20,33792
R 24,34816
R 28,35840
s_waitcnt vmcnt(0)
buffer_store_dword v64, v81, s[36:39], s52 offen sc1
R 32,36864
s_waitcnt lgkmcnt(3)
v_mfma_f32_16x16x32_bf16 v[8:11], a[0:3], v[16:19], 0
R 36,37888
v_fma_f32 v68, v67, v48, v90
v_mfma_f32_16x16x32_bf16 v[12:15], a[4:7], v[16:19], 0
v_fma_f32 v0, v0, v66, v68
v_fma_f32 v69, v67, v49, v91
mav 8,8,20
R 40,38912
v_fma_f32 v1, v1, v66, v69
mav 12,12,20
s_waitcnt lgkmcnt(3)
v_mul_f32_e64 v0, v0, |v0|
v_mul_f32_e64 v1, v1, |v1|
mav 8,16,24
R 44,39936
v_cvt_pk_bf16_f32 v0, v0, v1
mav 12,20,24
v_pk_max_i16 v56, v0, 0
v_fma_f32 v68, v67, v50, v92
mav 8,24,28
R 16,40960
v_fma_f32 v2, v2, v66, v68
mav 12,28,28
s_waitcnt lgkmcnt(3)
v_fma_f32 v69, v67, v51, v93
v_fma_f32 v3, v3, v66, v69
mav 8,32,32
R 20,41984
v_mul_f32_e64 v2, v2, |v2|
mav 12,36,32
v_mul_f32_e64 v3, v3, |v3|
v_cvt_pk_bf16_f32 v2, v2, v3
mav 8,40,36
R 24,43008
v_pk_max_i16 v57, v2, 0
mav 12,44,36
s_waitcnt lgkmcnt(3)
v_fma_f32 v68, v67, v52, v94
v_fma_f32 v4, v4, v66, v68
mav 8,48,40
R 28,44032
v_fma_f32 v69, v67, v53, v95
mav 12,52,40
v_fma_f32 v5, v5, v66, v69
v_mul_f32_e64 v4, v4, |v4|
mav 8,56,44
R 32,45056
v_mul_f32_e64 v5, v5, |v5|
mav 12,60,44
s_waitcnt lgkmcnt(3)
v_cvt_pk_bf16_f32 v4, v4, v5
v_pk_max_i16 v58, v4, 0
mav 8,64,16
R 36,46080
v_fma_f32 v68, v67, v54, v96
mav 12,68,16
v_fma_f32 v6, v6, v66, v68
v_fma_f32 v69, v67, v55, v97
mav 8,72,20
R 40,47104
v_fma_f32 v7, v7, v66, v69
mav 12,76,20
s_waitcnt lgkmcnt(3)
v_mul_f32_e64 v6, v6, |v6|
v_mul_f32_e64 v7, v7, |v7|
mav 8,80,24
R 44,48128
v_cvt_pk_bf16_f32 v6, v6, v7
mav 12,84,24
v_pk_max_i16 v59, v6, 0
mav 8,88,28
R 16,49152
mav 12,92,28
s_waitcnt lgkmcnt(3)
mav 8,96,32
buffer_store_dwordx4 v[56:59], v83, s[36:39], s49 offen sc1
R 20,50176
mav 12,100,32
mav 8,104,36
R 24,51200
mav 12,108,36
s_waitcnt lgkmcnt(3)
mav 8,112,40
R 28,52224
mav 12,116,40
mav 8,120,44
R 32,53248
mav 12,124,44
s_waitcnt lgkmcnt(3)
mav 8,128,16
R 36,54272
mav 12,132,16
mav 8,136,20
R 40,55296
mav 12,140,20
s_waitcnt lgkmcnt(3)
mav 8,144,24
R 44,56320
mav 12,148,24
mav 8,152,28
R 16,57344
mav 12,156,28
s_waitcnt lgkmcnt(3)
mav 8,160,32
R 20,58368
mav 12,164,32
mav 8,168,36
R 24,59392
mav 12,172,36
s_waitcnt lgkmcnt(3)
mav 8,176,40
R 28,60416
mav 12,180,40
mav 8,184,44
R 32,61440
mav 12,188,44
s_waitcnt lgkmcnt(3)
mav 8,192,16
R 36,62464
mav 12,196,16
mav 8,200,20
R 40,63488
mav 12,204,20
s_waitcnt lgkmcnt(3)
mav 8,208,24
R 44,64512
mav 12,212,24
mav 8,216,28
mav 12,220,28
s_waitcnt lgkmcnt(2)
mav 8,224,32
v_add_u32 v64, 1, v64
mav 12,228,32
mav 8,232,36
mav 12,236,36
s_waitcnt lgkmcnt(0)
mav 8,240,40
mav 12,244,40
mav 8,248,44
mav 12,252,44
s_add_u32 s49, s49, 313344
s_barrier
ds_read_b64 v[66:67], v88 offset:128
s_waitcnt vmcnt(0)
buffer_store_dword v64, v81, s[36:39], s52 offen sc1
s_waitcnt lgkmcnt(0)
v_fma_f32 v68, v67, v48, v90
v_fma_f32 v8, v8, v66, v68
v_fma_f32 v69, v67, v49, v91
v_fma_f32 v9, v9, v66, v69
v_mul_f32_e64 v8, v8, |v8|
v_mul_f32_e64 v9, v9, |v9|
v_cvt_pk_bf16_f32 v8, v8, v9
v_pk_max_i16 v56, v8, 0
v_fma_f32 v68, v67, v50, v92
v_fma_f32 v10, v10, v66, v68
v_fma_f32 v69, v67, v51, v93
v_fma_f32 v11, v11, v66, v69
v_mul_f32_e64 v10, v10, |v10|
v_mul_f32_e64 v11, v11, |v11|
v_cvt_pk_bf16_f32 v10, v10, v11
v_pk_max_i16 v57, v10, 0
v_fma_f32 v68, v67, v52, v94
v_fma_f32 v12, v12, v66, v68
v_fma_f32 v69, v67, v53, v95
v_fma_f32 v13, v13, v66, v69
v_mul_f32_e64 v12, v12, |v12|
v_mul_f32_e64 v13, v13, |v13|
v_cvt_pk_bf16_f32 v12, v12, v13
v_pk_max_i16 v58, v12, 0
v_fma_f32 v68, v67, v54, v96
v_fma_f32 v14, v14, v66, v68
v_fma_f32 v69, v67, v55, v97
v_fma_f32 v15, v15, v66, v69
v_mul_f32_e64 v14, v14, |v14|
v_mul_f32_e64 v15, v15, |v15|
v_cvt_pk_bf16_f32 v14, v14, v15
v_pk_max_i16 v59, v14, 0
buffer_store_dwordx4 v[56:59], v83, s[36:39], s49 offen sc1
v_add_u32 v64, 37, v64
s_waitcnt vmcnt(0)
buffer_store_dword v64, v81, s[36:39], s52 offen sc1
s_endpgm
LvE3:
LsE9:
s_cbranch_execz LwE
s_add_u32 s49, s49, 313344
s_barrier
s_add_u32 m0, s50, 0
s_nop 0
D 84,0
D 84,1024
D 84,2048
D 84,3072
s_add_u32 m0, s50, 4096
s_nop 0
LsE10:
s_cbranch_execz LwE
D 85,0
D 85,1024
D 85,2048
D 85,3072
s_waitcnt vmcnt(0)
s_add_u32 s49, s49, 313344
s_barrier
s_add_u32 m0, s50, 32768
R 16,0
D 84,0
R 20,1024
R 24,2048
D 84,1024
R 28,3072
R 32,4096
D 84,2048
s_waitcnt lgkmcnt(3)
v_mfma_f32_16x16x32_bf16 v[0:3], a[0:3], v[16:19], 0
R 36,5120
v_mfma_f32_16x16x32_bf16 v[4:7], a[4:7], v[16:19], 0
mav 0,8,20
R 40,6144
mav 4,12,20
s_waitcnt lgkmcnt(3)
mav 0,16,24
R 44,7168
mav 4,20,24
D 84,3072
mav 0,24,28
s_add_u32 m0, s50, 36864
R 16,8192
mav 4,28,28
s_waitcnt lgkmcnt(3)
mav 0,32,32
R 20,9216
mav 4,36,32
D 85,0
mav 0,40,36
R 24,10240
mav 4,44,36
s_waitcnt lgkmcnt(3)
mav 0,48,40
R 28,11264
mav 4,52,40
D 85,1024
mav 0,56,44
R 32,12288
mav 4,60,44
s_waitcnt lgkmcnt(3)
mav 0,64,16
R 36,13312
mav 4,68,16
D 85,2048
mav 0,72,20
R 40,14336
mav 4,76,20
s_waitcnt lgkmcnt(3)
mav 0,80,24
R 44,15360
mav 4,84,24
D 85,3072
mav 0,88,28
R 16,16384
mav 4,92,28
s_waitcnt lgkmcnt(3)
mav 0,96,32
R 20,17408
mav 4,100,32
mav 0,104,36
R 24,18432
mav 4,108,36
s_waitcnt lgkmcnt(3)
mav 0,112,40
R 28,19456
mav 4,116,40
mav 0,120,44
R 32,20480
mav 4,124,44
s_waitcnt lgkmcnt(3)
mav 0,128,16
R 36,21504
mav 4,132,16
mav 0,136,20
R 40,22528
mav 4,140,20
s_waitcnt lgkmcnt(3)
mav 0,144,24
R 44,23552
mav 4,148,24
mav 0,152,28
R 16,24576
mav 4,156,28
s_waitcnt lgkmcnt(3)
mav 0,160,32
R 20,25600
mav 4,164,32
mav 0,168,36
R 24,26624
mav 4,172,36
s_waitcnt lgkmcnt(3)
mav 0,176,40
R 28,27648
mav 4,180,40
mav 0,184,44
R 32,28672
mav 4,188,44
s_waitcnt lgkmcnt(3)
mav 0,192,16
R 36,29696
mav 4,196,16
mav 0,200,20
R 40,30720
mav 4,204,20
s_waitcnt lgkmcnt(3)
mav 0,208,24
R 44,31744
mav 4,212,24
mav 0,216,28
mav 4,220,28
s_waitcnt lgkmcnt(2)
mav 0,224,32
mav 4,228,32
mav 0,232,36
mav 4,236,36
s_waitcnt lgkmcnt(0)
mav 0,240,40
mav 4,244,40
mav 0,248,44
mav 4,252,44
s_xor_b32 s50, s50, 0x10000
s_waitcnt vmcnt(0)
s_add_u32 s49, s49, 313344
s_barrier
s_add_u32 m0, s50, 0
R 16,32768
D 84,0
ds_read_b64 v[66:67], v88 offset:0
R 20,33792
R 24,34816
D 84,1024
R 28,35840
R 32,36864
D 84,2048
s_waitcnt lgkmcnt(3)
v_mfma_f32_16x16x32_bf16 v[8:11], a[0:3], v[16:19], 0
R 36,37888
v_fma_f32 v68, v67, v48, v90
v_mfma_f32_16x16x32_bf16 v[12:15], a[4:7], v[16:19], 0
v_fma_f32 v0, v0, v66, v68
v_fma_f32 v69, v67, v49, v91
mav 8,8,20
R 40,38912
v_fma_f32 v1, v1, v66, v69
mav 12,12,20
s_waitcnt lgkmcnt(3)
v_mul_f32_e64 v0, v0, |v0|
v_mul_f32_e64 v1, v1, |v1|
mav 8,16,24
R 44,39936
v_cvt_pk_bf16_f32 v0, v0, v1
mav 12,20,24
D 84,3072
mav 8,24,28
s_add_u32 m0, s50, 4096
R 16,40960
v_pk_max_i16 v56, v0, 0
mav 12,28,28
s_waitcnt lgkmcnt(3)
v_fma_f32 v68, v67, v50, v92
v_fma_f32 v2, v2, v66, v68
mav 8,32,32
R 20,41984
v_fma_f32 v69, v67, v51, v93
mav 12,36,32
D 85,0
mav 8,40,36
R 24,43008
v_fma_f32 v3, v3, v66, v69
mav 12,44,36
s_waitcnt lgkmcnt(3)
v_mul_f32_e64 v2, v2, |v2|
v_mul_f32_e64 v3, v3, |v3|
mav 8,48,40
R 28,44032
v_cvt_pk_bf16_f32 v2, v2, v3
mav 12,52,40
D 85,1024
mav 8,56,44
R 32,45056
v_pk_max_i16 v57, v2, 0
mav 12,60,44
s_waitcnt lgkmcnt(3)
v_fma_f32 v68, v67, v52, v94
v_fma_f32 v4, v4, v66, v68
mav 8,64,16
R 36,46080
v_fma_f32 v69, v67, v53, v95
mav 12,68,16
D 85,2048
mav 8,72,20
R 40,47104
v_fma_f32 v5, v5, v66, v69
mav 12,76,20
s_waitcnt lgkmcnt(3)
v_mul_f32_e64 v4, v4, |v4|
v_mul_f32_e64 v5, v5, |v5|
mav 8,80,24
R 44,48128
v_cvt_pk_bf16_f32 v4, v4, v5
mav 12,84,24
D 85,3072
mav 8,88,28
R 16,49152
v_pk_max_i16 v58, v4, 0
mav 12,92,28
s_waitcnt lgkmcnt(3)
v_fma_f32 v68, v67, v54, v96
v_fma_f32 v6, v6, v66, v68
mav 8,96,32
R 20,50176
v_fma_f32 v69, v67, v55, v97
mav 12,100,32
v_fma_f32 v7, v7, v66, v69
v_mul_f32_e64 v6, v6, |v6|
mav 8,104,36
R 24,51200
v_mul_f32_e64 v7, v7, |v7|
mav 12,108,36
s_waitcnt lgkmcnt(3)
v_cvt_pk_bf16_f32 v6, v6, v7
v_pk_max_i16 v59, v6, 0
mav 8,112,40
R 28,52224
mav 12,116,40
buffer_store_dwordx4 v[56:59], v83, s[36:39], s49 offen sc1
mav 8,120,44
R 32,53248
mav 12,124,44
s_waitcnt lgkmcnt(3)
mav 8,128,16
R 36,54272
mav 12,132,16
mav 8,136,20
R 40,55296
mav 12,140,20
s_waitcnt lgkmcnt(3)
mav 8,144,24
R 44,56320
mav 12,148,24
mav 8,152,28
R 16,57344
mav 12,156,28
s_waitcnt lgkmcnt(3)
mav 8,160,32
R 20,58368
mav 12,164,32
mav 8,168,36
R 24,59392
mav 12,172,36
s_waitcnt lgkmcnt(3)
mav 8,176,40
R 28,60416
mav 12,180,40
mav 8,184,44
R 32,61440
mav 12,188,44
s_waitcnt lgkmcnt(3)
mav 8,192,16
R 36,62464
mav 12,196,16
mav 8,200,20
R 40,63488
mav 12,204,20
s_waitcnt lgkmcnt(3)
mav 8,208,24
R 44,64512
mav 12,212,24
mav 8,216,28
mav 12,220,28
s_waitcnt lgkmcnt(2)
mav 8,224,32
v_add_u32 v64, 1, v64
mav 12,228,32
mav 8,232,36
s_mov_b32 s60, -30
.p2align 6
LE3_1:
mav 12,236,36
s_waitcnt lgkmcnt(0)
mav 8,240,40
mav 12,244,40
mav 8,248,44
mav 12,252,44
v_xor_b32 v80, 0x10000, v80
s_waitcnt vmcnt(1)
s_add_u32 s49, s49, 313344
s_barrier
s_add_u32 m0, s50, 32768
R 16,0
D 84,0
ds_read_b64 v[66:67], v88 offset:128
R 20,1024
R 24,2048
D 84,1024
R 28,3072
s_waitcnt vmcnt(2)
buffer_store_dword v64, v81, s[36:39], s52 offen sc1
R 32,4096
D 84,2048
s_waitcnt lgkmcnt(3)
v_mfma_f32_16x16x32_bf16 v[0:3], a[0:3], v[16:19], 0
R 36,5120
v_fma_f32 v68, v67, v48, v90
v_mfma_f32_16x16x32_bf16 v[4:7], a[4:7], v[16:19], 0
v_fma_f32 v8, v8, v66, v68
v_fma_f32 v69, v67, v49, v91
mav 0,8,20
R 40,6144
v_fma_f32 v9, v9, v66, v69
mav 4,12,20
s_waitcnt lgkmcnt(3)
v_mul_f32_e64 v8, v8, |v8|
v_mul_f32_e64 v9, v9, |v9|
mav 0,16,24
R 44,7168
v_cvt_pk_bf16_f32 v8, v8, v9
mav 4,20,24
D 84,3072
mav 0,24,28
s_add_u32 m0, s50, 36864
R 16,8192
v_pk_max_i16 v56, v8, 0
mav 4,28,28
s_waitcnt lgkmcnt(3)
v_fma_f32 v68, v67, v50, v92
v_fma_f32 v10, v10, v66, v68
mav 0,32,32
R 20,9216
v_fma_f32 v69, v67, v51, v93
mav 4,36,32
D 85,0
mav 0,40,36
R 24,10240
v_fma_f32 v11, v11, v66, v69
mav 4,44,36
s_waitcnt lgkmcnt(3)
v_mul_f32_e64 v10, v10, |v10|
v_mul_f32_e64 v11, v11, |v11|
mav 0,48,40
R 28,11264
v_cvt_pk_bf16_f32 v10, v10, v11
mav 4,52,40
D 85,1024
mav 0,56,44
R 32,12288
v_pk_max_i16 v57, v10, 0
mav 4,60,44
s_waitcnt lgkmcnt(3)
v_fma_f32 v68, v67, v52, v94
v_fma_f32 v12, v12, v66, v68
mav 0,64,16
R 36,13312
v_fma_f32 v69, v67, v53, v95
mav 4,68,16
D 85,2048
mav 0,72,20
R 40,14336
v_fma_f32 v13, v13, v66, v69
mav 4,76,20
s_waitcnt lgkmcnt(3)
v_mul_f32_e64 v12, v12, |v12|
v_mul_f32_e64 v13, v13, |v13|
mav 0,80,24
R 44,15360
v_cvt_pk_bf16_f32 v12, v12, v13
mav 4,84,24
D 85,3072
mav 0,88,28
R 16,16384
v_pk_max_i16 v58, v12, 0
mav 4,92,28
s_waitcnt lgkmcnt(3)
v_fma_f32 v68, v67, v54, v96
v_fma_f32 v14, v14, v66, v68
mav 0,96,32
R 20,17408
v_fma_f32 v69, v67, v55, v97
mav 4,100,32
v_fma_f32 v15, v15, v66, v69
v_mul_f32_e64 v14, v14, |v14|
mav 0,104,36
R 24,18432
v_mul_f32_e64 v15, v15, |v15|
mav 4,108,36
s_waitcnt lgkmcnt(3)
v_cvt_pk_bf16_f32 v14, v14, v15
v_pk_max_i16 v59, v14, 0
mav 0,112,40
R 28,19456
mav 4,116,40
buffer_store_dwordx4 v[56:59], v83, s[36:39], s49 offen sc1
mav 0,120,44
R 32,20480
mav 4,124,44
s_waitcnt lgkmcnt(3)
mav 0,128,16
R 36,21504
mav 4,132,16
mav 0,136,20
R 40,22528
mav 4,140,20
s_waitcnt lgkmcnt(3)
mav 0,144,24
R 44,23552
mav 4,148,24
mav 0,152,28
R 16,24576
mav 4,156,28
s_waitcnt lgkmcnt(3)
mav 0,160,32
R 20,25600
mav 4,164,32
mav 0,168,36
R 24,26624
mav 4,172,36
s_waitcnt lgkmcnt(3)
mav 0,176,40
R 28,27648
mav 4,180,40
mav 0,184,44
R 32,28672
mav 4,188,44
s_waitcnt lgkmcnt(3)
mav 0,192,16
R 36,29696
mav 4,196,16
mav 0,200,20
R 40,30720
mav 4,204,20
s_waitcnt lgkmcnt(3)
mav 0,208,24
R 44,31744
mav 4,212,24
mav 0,216,28
mav 4,220,28
s_waitcnt lgkmcnt(2)
mav 0,224,32
v_add_u32 v64, 1, v64
mav 4,228,32
mav 0,232,36
mav 4,236,36
s_waitcnt lgkmcnt(0)
mav 0,240,40
mav 4,244,40
mav 0,248,44
mav 4,252,44
s_xor_b32 s50, s50, 0x10000
s_waitcnt vmcnt(1)
s_add_u32 s49, s49, 313344
s_barrier
s_add_u32 m0, s50, 0
R 16,32768
D 84,0
ds_read_b64 v[66:67], v88 offset:0
R 20,33792
R 24,34816
D 84,1024
R 28,35840
s_waitcnt vmcnt(2)
buffer_store_dword v64, v81, s[36:39], s52 offen sc1
R 32,36864
D 84,2048
s_waitcnt lgkmcnt(3)
v_mfma_f32_16x16x32_bf16 v[8:11], a[0:3], v[16:19], 0
R 36,37888
v_fma_f32 v68, v67, v48, v90
v_mfma_f32_16x16x32_bf16 v[12:15], a[4:7], v[16:19], 0
v_fma_f32 v0, v0, v66, v68
v_fma_f32 v69, v67, v49, v91
mav 8,8,20
R 40,38912
v_fma_f32 v1, v1, v66, v69
mav 12,12,20
s_waitcnt lgkmcnt(3)
v_mul_f32_e64 v0, v0, |v0|
v_mul_f32_e64 v1, v1, |v1|
mav 8,16,24
R 44,39936
v_cvt_pk_bf16_f32 v0, v0, v1
mav 12,20,24
D 84,3072
mav 8,24,28
s_add_u32 m0, s50, 4096
R 16,40960
v_pk_max_i16 v56, v0, 0
mav 12,28,28
s_waitcnt lgkmcnt(3)
v_fma_f32 v68, v67, v50, v92
v_fma_f32 v2, v2, v66, v68
mav 8,32,32
R 20,41984
v_fma_f32 v69, v67, v51, v93
mav 12,36,32
D 85,0
mav 8,40,36
R 24,43008
v_fma_f32 v3, v3, v66, v69
mav 12,44,36
s_waitcnt lgkmcnt(3)
v_mul_f32_e64 v2, v2, |v2|
v_mul_f32_e64 v3, v3, |v3|
mav 8,48,40
R 28,44032
v_cvt_pk_bf16_f32 v2, v2, v3
mav 12,52,40
D 85,1024
mav 8,56,44
R 32,45056
v_pk_max_i16 v57, v2, 0
mav 12,60,44
s_waitcnt lgkmcnt(3)
v_fma_f32 v68, v67, v52, v94
v_fma_f32 v4, v4, v66, v68
mav 8,64,16
R 36,46080
v_fma_f32 v69, v67, v53, v95
mav 12,68,16
D 85,2048
mav 8,72,20
R 40,47104
v_fma_f32 v5, v5, v66, v69
mav 12,76,20
s_waitcnt lgkmcnt(3)
v_mul_f32_e64 v4, v4, |v4|
v_mul_f32_e64 v5, v5, |v5|
mav 8,80,24
R 44,48128
v_cvt_pk_bf16_f32 v4, v4, v5
mav 12,84,24
D 85,3072
mav 8,88,28
R 16,49152
v_pk_max_i16 v58, v4, 0
LsE11:
s_cbranch_execz LwE
mav 12,92,28
s_waitcnt lgkmcnt(3)
v_fma_f32 v68, v67, v54, v96
v_fma_f32 v6, v6, v66, v68
mav 8,96,32
R 20,50176
v_fma_f32 v69, v67, v55, v97
mav 12,100,32
v_fma_f32 v7, v7, v66, v69
v_mul_f32_e64 v6, v6, |v6|
mav 8,104,36
R 24,51200
v_mul_f32_e64 v7, v7, |v7|
mav 12,108,36
s_waitcnt lgkmcnt(3)
v_cvt_pk_bf16_f32 v6, v6, v7
v_pk_max_i16 v59, v6, 0
mav 8,112,40
R 28,52224
mav 12,116,40
buffer_store_dwordx4 v[56:59], v83, s[36:39], s49 offen sc1
mav 8,120,44
R 32,53248
mav 12,124,44
s_waitcnt lgkmcnt(3)
mav 8,128,16
R 36,54272
mav 12,132,16
mav 8,136,20
R 40,55296
mav 12,140,20
s_waitcnt lgkmcnt(3)
mav 8,144,24
R 44,56320
mav 12,148,24
mav 8,152,28
R 16,57344
mav 12,156,28
s_waitcnt lgkmcnt(3)
mav 8,160,32
R 20,58368
mav 12,164,32
mav 8,168,36
R 24,59392
mav 12,172,36
s_waitcnt lgkmcnt(3)
mav 8,176,40
R 28,60416
mav 12,180,40
mav 8,184,44
R 32,61440
mav 12,188,44
s_waitcnt lgkmcnt(3)
mav 8,192,16
R 36,62464
mav 12,196,16
mav 8,200,20
R 40,63488
mav 12,204,20
s_waitcnt lgkmcnt(3)
mav 8,208,24
R 44,64512
mav 12,212,24
mav 8,216,28
mav 12,220,28
s_waitcnt lgkmcnt(2)
mav 8,224,32
v_add_u32 v64, 1, v64
mav 12,228,32
s_add_u32 s60, s60, 1
mav 8,232,36
s_cbranch_scc0 LE3_1
mav 12,236,36
s_waitcnt lgkmcnt(0)
mav 8,240,40
mav 12,244,40
mav 8,248,44
mav 12,252,44
v_xor_b32 v80, 0x10000, v80
s_waitcnt vmcnt(1)
s_add_u32 s49, s49, 313344
s_barrier
s_add_u32 m0, s50, 32768
R 16,0
D 84,0
ds_read_b64 v[66:67], v88 offset:128
R 20,1024
R 24,2048
D 84,1024
R 28,3072
s_waitcnt vmcnt(2)
buffer_store_dword v64, v81, s[36:39], s52 offen sc1
R 32,4096
D 84,2048
s_waitcnt lgkmcnt(3)
v_mfma_f32_16x16x32_bf16 v[0:3], a[0:3], v[16:19], 0
R 36,5120
v_fma_f32 v68, v67, v48, v90
v_mfma_f32_16x16x32_bf16 v[4:7], a[4:7], v[16:19], 0
v_fma_f32 v8, v8, v66, v68
v_fma_f32 v69, v67, v49, v91
mav 0,8,20
R 40,6144
v_fma_f32 v9, v9, v66, v69
mav 4,12,20
s_waitcnt lgkmcnt(3)
v_mul_f32_e64 v8, v8, |v8|
v_mul_f32_e64 v9, v9, |v9|
mav 0,16,24
R 44,7168
v_cvt_pk_bf16_f32 v8, v8, v9
mav 4,20,24
D 84,3072
mav 0,24,28
s_add_u32 m0, s50, 36864
R 16,8192
v_pk_max_i16 v56, v8, 0
mav 4,28,28
s_waitcnt lgkmcnt(3)
v_fma_f32 v68, v67, v50, v92
v_fma_f32 v10, v10, v66, v68
mav 0,32,32
R 20,9216
v_fma_f32 v69, v67, v51, v93
mav 4,36,32
D 85,0
mav 0,40,36
R 24,10240
v_fma_f32 v11, v11, v66, v69
mav 4,44,36
s_waitcnt lgkmcnt(3)
v_mul_f32_e64 v10, v10, |v10|
v_mul_f32_e64 v11, v11, |v11|
mav 0,48,40
R 28,11264
v_cvt_pk_bf16_f32 v10, v10, v11
mav 4,52,40
D 85,1024
mav 0,56,44
R 32,12288
v_pk_max_i16 v57, v10, 0
mav 4,60,44
s_waitcnt lgkmcnt(3)
v_fma_f32 v68, v67, v52, v94
v_fma_f32 v12, v12, v66, v68
mav 0,64,16
R 36,13312
v_fma_f32 v69, v67, v53, v95
mav 4,68,16
D 85,2048
mav 0,72,20
R 40,14336
v_fma_f32 v13, v13, v66, v69
mav 4,76,20
s_waitcnt lgkmcnt(3)
v_mul_f32_e64 v12, v12, |v12|
v_mul_f32_e64 v13, v13, |v13|
mav 0,80,24
R 44,15360
v_cvt_pk_bf16_f32 v12, v12, v13
mav 4,84,24
D 85,3072
mav 0,88,28
R 16,16384
v_pk_max_i16 v58, v12, 0
mav 4,92,28
s_waitcnt lgkmcnt(3)
v_fma_f32 v68, v67, v54, v96
v_fma_f32 v14, v14, v66, v68
mav 0,96,32
R 20,17408
v_fma_f32 v69, v67, v55, v97
mav 4,100,32
v_fma_f32 v15, v15, v66, v69
v_mul_f32_e64 v14, v14, |v14|
mav 0,104,36
R 24,18432
v_mul_f32_e64 v15, v15, |v15|
mav 4,108,36
s_waitcnt lgkmcnt(3)
v_cvt_pk_bf16_f32 v14, v14, v15
v_pk_max_i16 v59, v14, 0
mav 0,112,40
R 28,19456
mav 4,116,40
buffer_store_dwordx4 v[56:59], v83, s[36:39], s49 offen sc1
mav 0,120,44
R 32,20480
mav 4,124,44
s_waitcnt lgkmcnt(3)
mav 0,128,16
R 36,21504
mav 4,132,16
mav 0,136,20
R 40,22528
mav 4,140,20
s_waitcnt lgkmcnt(3)
mav 0,144,24
R 44,23552
mav 4,148,24
mav 0,152,28
R 16,24576
mav 4,156,28
s_waitcnt lgkmcnt(3)
mav 0,160,32
R 20,25600
mav 4,164,32
mav 0,168,36
R 24,26624
mav 4,172,36
s_waitcnt lgkmcnt(3)
mav 0,176,40
R 28,27648
mav 4,180,40
mav 0,184,44
R 32,28672
mav 4,188,44
s_waitcnt lgkmcnt(3)
mav 0,192,16
R 36,29696
mav 4,196,16
mav 0,200,20
R 40,30720
mav 4,204,20
s_waitcnt lgkmcnt(3)
mav 0,208,24
R 44,31744
mav 4,212,24
mav 0,216,28
mav 4,220,28
s_waitcnt lgkmcnt(2)
mav 0,224,32
v_add_u32 v64, 1, v64
mav 4,228,32
mav 0,232,36
mav 4,236,36
s_waitcnt lgkmcnt(0)
mav 0,240,40
mav 4,244,40
mav 0,248,44
mav 4,252,44
s_waitcnt vmcnt(1)
s_add_u32 s49, s49, 313344
s_barrier
R 16,32768
ds_read_b64 v[66:67], v88 offset:0
R 20,33792
R 24,34816
R 28,35840
s_waitcnt vmcnt(0)
buffer_store_dword v64, v81, s[36:39], s52 offen sc1
R 32,36864
s_waitcnt lgkmcnt(3)
v_mfma_f32_16x16x32_bf16 v[8:11], a[0:3], v[16:19], 0
R 36,37888
v_fma_f32 v68, v67, v48, v90
v_mfma_f32_16x16x32_bf16 v[12:15], a[4:7], v[16:19], 0
v_fma_f32 v0, v0, v66, v68
v_fma_f32 v69, v67, v49, v91
mav 8,8,20
R 40,38912
v_fma_f32 v1, v1, v66, v69
mav 12,12,20
s_waitcnt lgkmcnt(3)
v_mul_f32_e64 v0, v0, |v0|
v_mul_f32_e64 v1, v1, |v1|
mav 8,16,24
R 44,39936
v_cvt_pk_bf16_f32 v0, v0, v1
mav 12,20,24
v_pk_max_i16 v56, v0, 0
v_fma_f32 v68, v67, v50, v92
mav 8,24,28
R 16,40960
v_fma_f32 v2, v2, v66, v68
mav 12,28,28
s_waitcnt lgkmcnt(3)
v_fma_f32 v69, v67, v51, v93
v_fma_f32 v3, v3, v66, v69
mav 8,32,32
R 20,41984
v_mul_f32_e64 v2, v2, |v2|
mav 12,36,32
v_mul_f32_e64 v3, v3, |v3|
v_cvt_pk_bf16_f32 v2, v2, v3
mav 8,40,36
R 24,43008
v_pk_max_i16 v57, v2, 0
mav 12,44,36
s_waitcnt lgkmcnt(3)
v_fma_f32 v68, v67, v52, v94
v_fma_f32 v4, v4, v66, v68
mav 8,48,40
R 28,44032
v_fma_f32 v69, v67, v53, v95
mav 12,52,40
v_fma_f32 v5, v5, v66, v69
v_mul_f32_e64 v4, v4, |v4|
mav 8,56,44
R 32,45056
v_mul_f32_e64 v5, v5, |v5|
mav 12,60,44
s_waitcnt lgkmcnt(3)
v_cvt_pk_bf16_f32 v4, v4, v5
v_pk_max_i16 v58, v4, 0
mav 8,64,16
R 36,46080
v_fma_f32 v68, v67, v54, v96
mav 12,68,16
v_fma_f32 v6, v6, v66, v68
v_fma_f32 v69, v67, v55, v97
mav 8,72,20
)"
R"(
R 40,47104
v_fma_f32 v7, v7, v66, v69
mav 12,76,20
s_waitcnt lgkmcnt(3)
v_mul_f32_e64 v6, v6, |v6|
v_mul_f32_e64 v7, v7, |v7|
mav 8,80,24
R 44,48128
v_cvt_pk_bf16_f32 v6, v6, v7
mav 12,84,24
v_pk_max_i16 v59, v6, 0
mav 8,88,28
R 16,49152
mav 12,92,28
s_waitcnt lgkmcnt(3)
mav 8,96,32
R 20,50176
mav 12,100,32
buffer_store_dwordx4 v[56:59], v83, s[36:39], s49 offen sc1
mav 8,104,36
R 24,51200
mav 12,108,36
s_waitcnt lgkmcnt(3)
mav 8,112,40
R 28,52224
mav 12,116,40
mav 8,120,44
R 32,53248
mav 12,124,44
s_waitcnt lgkmcnt(3)
mav 8,128,16
R 36,54272
mav 12,132,16
mav 8,136,20
R 40,55296
mav 12,140,20
s_waitcnt lgkmcnt(3)
mav 8,144,24
R 44,56320
mav 12,148,24
mav 8,152,28
R 16,57344
mav 12,156,28
s_waitcnt lgkmcnt(3)
mav 8,160,32
R 20,58368
mav 12,164,32
mav 8,168,36
R 24,59392
mav 12,172,36
s_waitcnt lgkmcnt(3)
mav 8,176,40
R 28,60416
mav 12,180,40
mav 8,184,44
R 32,61440
mav 12,188,44
s_waitcnt lgkmcnt(3)
mav 8,192,16
R 36,62464
mav 12,196,16
mav 8,200,20
R 40,63488
mav 12,204,20
s_waitcnt lgkmcnt(3)
mav 8,208,24
R 44,64512
mav 12,212,24
mav 8,216,28
mav 12,220,28
s_waitcnt lgkmcnt(2)
mav 8,224,32
v_add_u32 v64, 1, v64
mav 12,228,32
mav 8,232,36
mav 12,236,36
s_waitcnt lgkmcnt(0)
mav 8,240,40
mav 12,244,40
mav 8,248,44
mav 12,252,44
s_add_u32 s49, s49, 313344
s_barrier
ds_read_b64 v[66:67], v88 offset:128
s_waitcnt vmcnt(0)
buffer_store_dword v64, v81, s[36:39], s52 offen sc1
s_waitcnt lgkmcnt(0)
v_fma_f32 v68, v67, v48, v90
v_fma_f32 v8, v8, v66, v68
v_fma_f32 v69, v67, v49, v91
v_fma_f32 v9, v9, v66, v69
v_mul_f32_e64 v8, v8, |v8|
v_mul_f32_e64 v9, v9, |v9|
v_cvt_pk_bf16_f32 v8, v8, v9
v_pk_max_i16 v56, v8, 0
v_fma_f32 v68, v67, v50, v92
v_fma_f32 v10, v10, v66, v68
v_fma_f32 v69, v67, v51, v93
v_fma_f32 v11, v11, v66, v69
v_mul_f32_e64 v10, v10, |v10|
v_mul_f32_e64 v11, v11, |v11|
v_cvt_pk_bf16_f32 v10, v10, v11
v_pk_max_i16 v57, v10, 0
v_fma_f32 v68, v67, v52, v94
v_fma_f32 v12, v12, v66, v68
v_fma_f32 v69, v67, v53, v95
v_fma_f32 v13, v13, v66, v69
v_mul_f32_e64 v12, v12, |v12|
v_mul_f32_e64 v13, v13, |v13|
v_cvt_pk_bf16_f32 v12, v12, v13
v_pk_max_i16 v58, v12, 0
v_fma_f32 v68, v67, v54, v96
v_fma_f32 v14, v14, v66, v68
v_fma_f32 v69, v67, v55, v97
v_fma_f32 v15, v15, v66, v69
v_mul_f32_e64 v14, v14, |v14|
v_mul_f32_e64 v15, v15, |v15|
v_cvt_pk_bf16_f32 v14, v14, v15
v_pk_max_i16 v59, v14, 0
buffer_store_dwordx4 v[56:59], v83, s[36:39], s49 offen sc1
v_add_u32 v64, 37, v64
s_waitcnt vmcnt(0)
buffer_store_dword v64, v81, s[36:39], s52 offen sc1
s_endpgm
Lr5:
s_cmp_eq_u32 s60, 5
s_cbranch_scc0 Lr6
s_waitcnt vmcnt(0)
v_accvgpr_read_b32 v73, a255
v_mov_b32 v72, 0x100
v_add_u32 v73, 0x9e3779b9, v73
buffer_store_dwordx2 v[72:73], v81, s[36:39], s52 offen sc1
v_add_u32 v86, v86, v72
s_sleep 32
s_sleep 32
s_sleep 32
s_sleep 32
s_sleep 32
s_sleep 32
E 0,65536
E 1,69632
E 2,73728
E 3,77824
s_add_u32 s51, s51, 65536
E 0,81920
E 1,86016
E 2,90112
E 3,94208
s_add_u32 s51, s51, 65536
E 0,98304
E 1,102400
E 2,106496
E 3,110592
s_add_u32 s51, s51, 65536
E 0,114688
E 1,118784
E 2,122880
E 3,126976
s_add_u32 s51, s51, 65536
s_mov_b64 exec, 0
s_cmp_eq_u32 s53, 0
s_cbranch_scc1 LsG0
s_cmp_eq_u32 s53, 1024
s_cbranch_scc1 LsG1
s_cmp_eq_u32 s53, 2048
s_cbranch_scc1 LsG2
s_branch LsG3
LwG:
s_mov_b64 exec, -1
s_waitcnt vmcnt(8)
s_barrier
v_add_u32 v4, 65536, v4
v_add_u32 v5, 65536, v5
v_add_u32 v6, 65536, v6
v_add_u32 v7, 65536, v7
Ta 0,4,0
Ta 4,5,0
Ta 8,6,0
Ta 12,7,0
Ta 16,4,16384
Ta 20,5,16384
Ta 24,6,16384
Ta 28,7,16384
E 0,131072
E 1,135168
E 2,139264
E 3,143360
s_add_u32 s51, s51, 65536
E 0,147456
E 1,151552
E 2,155648
E 3,159744
s_add_u32 s51, s51, 65536
s_cmp_ge_u32 s51, s57
s_cselect_b32 s60, 1572864, 0
s_sub_u32 s51, s51, s60
s_waitcnt lgkmcnt(0)
s_waitcnt vmcnt(8)
LsG0:
s_cbranch_execz LsG4
s_barrier
E 0,65536
Ta 32,4,32768
Ta 36,5,32768
E 1,69632
Ta 40,6,32768
Ta 44,7,32768
E 2,73728
Ta 48,4,49152
E 3,77824
s_add_u32 s51, s51, 65536
Ta 52,5,49152
Ta 56,6,49152
E 0,81920
Ta 60,7,49152
E 1,86016
E 2,90112
E 3,94208
s_add_u32 s51, s51, 65536
s_waitcnt lgkmcnt(0)
s_waitcnt vmcnt(8)
s_barrier
v_add_u32 v4, 65536, v4
v_add_u32 v5, 65536, v5
v_add_u32 v6, 65536, v6
v_add_u32 v7, 65536, v7
E 0,98304
Ta 64,4,0
Ta 68,5,0
E 1,102400
Ta 72,6,0
Ta 76,7,0
E 2,106496
Ta 80,4,16384
E 3,110592
s_add_u32 s51, s51, 65536
Ta 84,5,16384
Ta 88,6,16384
E 0,114688
Ta 92,7,16384
E 1,118784
E 2,122880
E 3,126976
s_add_u32 s51, s51, 65536
s_waitcnt lgkmcnt(0)
s_waitcnt vmcnt(8)
s_barrier
v_add_u32 v4, -65536, v4
v_add_u32 v5, -65536, v5
v_add_u32 v6, -65536, v6
v_add_u32 v7, -65536, v7
E 0,131072
Ta 96,4,0
Ta 100,5,0
E 1,135168
Ta 104,6,0
Ta 108,7,0
E 2,139264
Ta 112,4,16384
E 3,143360
s_add_u32 s51, s51, 65536
Ta 116,5,16384
Ta 120,6,16384
E 0,147456
Ta 124,7,16384
E 1,151552
E 2,155648
E 3,159744
s_add_u32 s51, s51, 65536
s_cmp_ge_u32 s51, s57
s_cselect_b32 s60, 1572864, 0
s_sub_u32 s51, s51, s60
s_waitcnt lgkmcnt(0)
s_waitcnt vmcnt(8)
s_barrier
E 0,65536
Ta 128,4,32768
Ta 132,5,32768
E 1,69632
Ta 136,6,32768
Ta 140,7,32768
E 2,73728
Ta 144,4,49152
E 3,77824
s_add_u32 s51, s51, 65536
Ta 148,5,49152
Ta 152,6,49152
E 0,81920
Ta 156,7,49152
E 1,86016
E 2,90112
E 3,94208
s_add_u32 s51, s51, 65536
s_waitcnt lgkmcnt(0)
s_waitcnt vmcnt(8)
s_barrier
v_add_u32 v4, 65536, v4
v_add_u32 v5, 65536, v5
v_add_u32 v6, 65536, v6
v_add_u32 v7, 65536, v7
E 0,98304
Ta 160,4,0
Ta 164,5,0
E 1,102400
Ta 168,6,0
Ta 172,7,0
E 2,106496
Ta 176,4,16384
E 3,110592
s_add_u32 s51, s51, 65536
Ta 180,5,16384
Ta 184,6,16384
E 0,114688
Ta 188,7,16384
E 1,118784
E 2,122880
E 3,126976
s_add_u32 s51, s51, 65536
s_waitcnt lgkmcnt(0)
s_waitcnt vmcnt(8)
s_barrier
v_add_u32 v4, -65536, v4
v_add_u32 v5, -65536, v5
v_add_u32 v6, -65536, v6
v_add_u32 v7, -65536, v7
E 0,131072
Ta 192,4,0
Ta 196,5,0
E 1,135168
Ta 200,6,0
Ta 204,7,0
E 2,139264
Ta 208,4,16384
E 3,143360
s_add_u32 s51, s51, 65536
Ta 212,5,16384
Ta 216,6,16384
E 0,147456
Ta 220,7,16384
E 1,151552
E 2,155648
E 3,159744
s_add_u32 s51, s51, 65536
s_cmp_ge_u32 s51, s57
s_cselect_b32 s60, 1572864, 0
s_sub_u32 s51, s51, s60
s_waitcnt lgkmcnt(0)
s_waitcnt vmcnt(8)
s_barrier
E 0,65536
Ta 224,4,32768
Ta 228,5,32768
E 1,69632
Ta 232,6,32768
Ta 236,7,32768
E 2,73728
Ta 240,4,49152
E 3,77824
s_add_u32 s51, s51, 65536
Ta 244,5,49152
Ta 248,6,49152
E 0,81920
Ta 252,7,49152
E 1,86016
E 2,90112
E 3,94208
s_add_u32 s51, s51, 65536
s_waitcnt lgkmcnt(0)
s_waitcnt vmcnt(8)
s_barrier
v_add_u32 v4, 65536, v4
v_add_u32 v5, 65536, v5
v_add_u32 v6, 65536, v6
v_add_u32 v7, 65536, v7
E 0,98304
Tv 128,4,0
Tv 132,5,0
E 1,102400
Tv 136,6,0
Tv 140,7,0
E 2,106496
Tv 144,4,16384
E 3,110592
s_add_u32 s51, s51, 65536
Tv 148,5,16384
Tv 152,6,16384
E 0,114688
Tv 156,7,16384
E 1,118784
E 2,122880
E 3,126976
s_add_u32 s51, s51, 65536
s_waitcnt lgkmcnt(0)
s_waitcnt vmcnt(8)
s_barrier
v_add_u32 v4, -65536, v4
v_add_u32 v5, -65536, v5
v_add_u32 v6, -65536, v6
v_add_u32 v7, -65536, v7
E 0,131072
Tv 160,4,0
Tv 164,5,0
E 1,135168
Tv 168,6,0
Tv 172,7,0
E 2,139264
Tv 176,4,16384
E 3,143360
s_add_u32 s51, s51, 65536
Tv 180,5,16384
Tv 184,6,16384
E 0,147456
Tv 188,7,16384
E 1,151552
E 2,155648
E 3,159744
s_add_u32 s51, s51, 65536
s_cmp_ge_u32 s51, s57
s_cselect_b32 s60, 1572864, 0
s_sub_u32 s51, s51, s60
s_waitcnt lgkmcnt(0)
s_waitcnt vmcnt(8)
s_barrier
Tv 192,4,32768
Tv 196,5,32768
Tv 200,6,32768
Tv 204,7,32768
Tv 208,4,49152
Tv 212,5,49152
Tv 216,6,49152
Tv 220,7,49152
s_waitcnt lgkmcnt(0)
s_waitcnt vmcnt(0)
s_barrier
v_add_u32 v4, 65536, v4
v_add_u32 v5, 65536, v5
v_add_u32 v6, 65536, v6
v_add_u32 v7, 65536, v7
Tv 224,4,0
Tv 228,5,0
Tv 232,6,0
Tv 236,7,0
Tv 240,4,16384
Tv 244,5,16384
Tv 248,6,16384
Tv 252,7,16384
s_waitcnt lgkmcnt(0)
s_waitcnt vmcnt(0)
s_barrier
s_nop 7
s_and_b32 s60, s48, 3
s_cmp_eq_u32 s60, 1
s_cbranch_scc1 LvG1
s_cmp_eq_u32 s60, 2
s_cbranch_scc1 LvG2
s_cmp_eq_u32 s60, 3
s_cbranch_scc1 LvG3
LvG0:
LsG1:
s_cbranch_execz LsG5
v_add_u32 v72, 1, v72
buffer_load_dwordx2 v[74:75], v85, s[36:39], 0 offen sc1
s_waitcnt vmcnt(0)
ds_write_b32 v82, v74
v_sub_u32 v74, v74, v86
v_xor_b32 v75, v75, v73
v_cmp_lt_u64 vcc, s[62:63], v[74:75]
s_waitcnt lgkmcnt(0)
s_cbranch_vccnz CG0_1
BG0_1:
s_add_u32 s49, s49, 313344
s_barrier
s_add_u32 m0, s50, 0
buffer_store_dword v72, v81, s[36:39], s52 offen sc1
D 83,0
D 83,1024
D 83,2048
D 83,3072
s_add_u32 m0, s50, 4096
s_nop 0
D 84,0
D 84,1024
s_mov_b32 m0, 144384
s_nop 0
buffer_load_dword v85, s[36:39], 0 offen sc1 lds
v_add_u32 v86, 1, v86
v_add_u32 v72, 1, v72
ds_read_b32 v74, v82 offset:0
s_waitcnt lgkmcnt(0)
v_cmp_gt_i32 vcc, v86, v74
s_waitcnt vmcnt(1)
s_add_u32 s49, s49, 313344
s_cbranch_vccnz CG0_2
BG0_2:
s_barrier
s_add_u32 m0, s50, 32768
R 32,0
buffer_store_dword v72, v81, s[36:39], s52 offen sc1
D 83,0
R 36,1024
D 83,1024
R 40,2048
D 83,2048
s_waitcnt lgkmcnt(1)
v_mfma_f32_16x16x32_bf16 v[0:3], a[0:3], v[32:35], 0
R 44,3072
v_mfma_f32_16x16x32_bf16 v[4:7], a[4:7], v[32:35], 0
D 83,3072
v_mfma_f32_16x16x32_bf16 v[8:11], a[8:11], v[32:35], 0
v_mfma_f32_16x16x32_bf16 v[12:15], a[12:15], v[32:35], 0
s_add_u32 m0, s50, 36864
mav 0,16,36
R 48,4096
mav 4,20,36
D 84,0
mav 8,24,36
mav 12,28,36
s_waitcnt lgkmcnt(1)
mav 0,32,40
R 52,5120
mav 4,36,40
D 84,1024
mav 8,40,40
mav 12,44,40
mav 0,48,44
R 56,6144
mav 4,52,44
mav 8,56,44
mav 12,60,44
s_waitcnt lgkmcnt(1)
mav 0,64,48
R 60,7168
mav 4,68,48
mav 8,72,48
mav 12,76,48
mav 0,80,52
R 32,8192
mav 4,84,52
mav 8,88,52
mav 12,92,52
s_waitcnt lgkmcnt(1)
mav 0,96,56
R 36,9216
mav 4,100,56
mav 8,104,56
mav 12,108,56
mav 0,112,60
R 40,10240
mav 4,116,60
mav 8,120,60
mav 12,124,60
s_waitcnt lgkmcnt(1)
mav 0,128,32
R 44,11264
mav 4,132,32
mav 8,136,32
LsG2:
s_cbranch_execz LsG6
s_mov_b32 m0, 144384
mav 12,140,32
mav 0,144,36
R 48,12288
mav 4,148,36
buffer_load_dword v85, s[36:39], 0 offen sc1 lds
mav 8,152,36
mav 12,156,36
s_waitcnt lgkmcnt(1)
mav 0,160,40
R 52,13312
v_add_u32 v86, 1, v86
mav 4,164,40
mav 8,168,40
mav 12,172,40
mav 0,176,44
R 56,14336
mav 4,180,44
mav 8,184,44
mav 12,188,44
s_waitcnt lgkmcnt(1)
mav 0,192,48
R 60,15360
mav 4,196,48
mav 8,200,48
mav 12,204,48
mav 0,208,52
R 32,16384
mav 4,212,52
mav 8,216,52
mav 12,220,52
s_waitcnt lgkmcnt(1)
mav 0,224,56
R 36,17408
mav 4,228,56
mav 8,232,56
mav 12,236,56
mav 0,240,60
R 40,18432
mav 4,244,60
mav 8,248,60
mav 12,252,60
s_waitcnt lgkmcnt(1)
mvv 0,128,32
R 44,19456
mvv 4,132,32
mvv 8,136,32
mvv 12,140,32
mvv 0,144,36
R 48,20480
mvv 4,148,36
mvv 8,152,36
mvv 12,156,36
s_waitcnt lgkmcnt(1)
mvv 0,160,40
R 52,21504
mvv 4,164,40
ds_read_b32 v74, v82 offset:0
mvv 8,168,40
mvv 12,172,40
mvv 0,176,44
R 56,22528
mvv 4,180,44
mvv 8,184,44
s_xor_b32 s50, s50, 0x10000
mvv 12,188,44
s_waitcnt lgkmcnt(1)
mvv 0,192,48
R 60,23552
v_cmp_gt_i32 vcc, v86, v74
mvv 4,196,48
mvv 8,200,48
s_waitcnt vmcnt(1)
s_add_u32 s49, s49, 313344
mvv 12,204,48
s_cbranch_vccnz CG0_3
BG0_3:
s_barrier
mvv 0,208,52
s_add_u32 m0, s50, 0
R 32,32768
mvv 4,212,52
D 83,0
mvv 8,216,52
mvv 12,220,52
s_waitcnt lgkmcnt(1)
mvv 0,224,56
R 36,33792
mvv 4,228,56
D 83,1024
mvv 8,232,56
mvv 12,236,56
mvv 0,240,60
R 40,34816
mvv 4,244,60
D 83,2048
mvv 8,248,60
mvv 12,252,60
s_waitcnt lgkmcnt(1)
v_mfma_f32_16x16x32_bf16 v[16:19], a[0:3], v[32:35], 0
R 44,35840
v_cvt_pk_bf16_f32 v64, v0, v1
v_mfma_f32_16x16x32_bf16 v[20:23], a[4:7], v[32:35], 0
D 83,3072
v_mfma_f32_16x16x32_bf16 v[24:27], a[8:11], v[32:35], 0
v_cvt_pk_bf16_f32 v65, v2, v3
v_cvt_pk_bf16_f32 v66, v4, v5
v_mfma_f32_16x16x32_bf16 v[28:31], a[12:15], v[32:35], 0
s_add_u32 m0, s50, 4096
v_cvt_pk_bf16_f32 v67, v6, v7
v_cvt_pk_bf16_f32 v68, v8, v9
mav 16,16,36
R 48,36864
v_cvt_pk_bf16_f32 v69, v10, v11
mav 20,20,36
buffer_store_dwordx4 v[64:67], v87, s[36:39], s49 offen sc1
D 84,0
mav 24,24,36
v_cvt_pk_bf16_f32 v70, v12, v13
v_cvt_pk_bf16_f32 v71, v14, v15
mav 28,28,36
s_waitcnt lgkmcnt(1)
mav 16,32,40
R 52,37888
mav 20,36,40
buffer_store_dwordx4 v[68:71], v87, s[36:39], s49 offen offset:1024 sc1
D 84,1024
mav 24,40,40
mav 28,44,40
mav 16,48,44
R 56,38912
mav 20,52,44
mav 24,56,44
mav 28,60,44
s_waitcnt lgkmcnt(1)
mav 16,64,48
R 60,39936
mav 20,68,48
mav 24,72,48
mav 28,76,48
mav 16,80,52
R 32,40960
mav 20,84,52
mav 24,88,52
mav 28,92,52
s_waitcnt lgkmcnt(1)
mav 16,96,56
R 36,41984
mav 20,100,56
mav 24,104,56
mav 28,108,56
mav 16,112,60
R 40,43008
mav 20,116,60
mav 24,120,60
mav 28,124,60
s_waitcnt lgkmcnt(1)
mav 16,128,32
R 44,44032
mav 20,132,32
mav 24,136,32
s_mov_b32 m0, 144384
mav 28,140,32
mav 16,144,36
R 48,45056
v_add_u32 v72, 1, v72
mav 20,148,36
buffer_load_dword v85, s[36:39], 0 offen sc1 lds
mav 24,152,36
mav 28,156,36
s_waitcnt lgkmcnt(1)
mav 16,160,40
R 52,46080
v_add_u32 v86, 1, v86
mav 20,164,40
mav 24,168,40
mav 28,172,40
mav 16,176,44
R 56,47104
mav 20,180,44
mav 24,184,44
mav 28,188,44
s_waitcnt lgkmcnt(1)
mav 16,192,48
R 60,48128
mav 20,196,48
mav 24,200,48
s_mov_b32 s60, -30
.p2align 6
LG0_4:
mav 28,204,48
mav 16,208,52
R 32,49152
mav 20,212,52
mav 24,216,52
mav 28,220,52
s_waitcnt lgkmcnt(1)
mav 16,224,56
R 36,50176
mav 20,228,56
mav 24,232,56
mav 28,236,56
mav 16,240,60
s_waitcnt vmcnt(2)
R 40,51200
mav 20,244,60
buffer_store_dword v72, v81, s[36:39], s52 offen sc1
mav 24,248,60
mav 28,252,60
s_waitcnt lgkmcnt(1)
mvv 16,128,32
R 44,52224
mvv 20,132,32
mvv 24,136,32
mvv 28,140,32
mvv 16,144,36
R 48,53248
mvv 20,148,36
mvv 24,152,36
mvv 28,156,36
s_waitcnt lgkmcnt(1)
mvv 16,160,40
R 52,54272
mvv 20,164,40
ds_read_b32 v74, v82 offset:0
mvv 24,168,40
mvv 28,172,40
mvv 16,176,44
R 56,55296
mvv 20,180,44
mvv 24,184,44
mvv 28,188,44
s_waitcnt lgkmcnt(1)
mvv 16,192,48
R 60,56320
v_cmp_gt_i32 vcc, v86, v74
mvv 20,196,48
v_xor_b32 v80, 0x10000, v80
mvv 24,200,48
s_waitcnt vmcnt(2)
s_add_u32 s49, s49, 313344
mvv 28,204,48
s_cbranch_vccnz CG0_5
BG0_5:
s_barrier
mvv 16,208,52
s_add_u32 m0, s50, 32768
R 32,0
mvv 20,212,52
D 83,0
mvv 24,216,52
mvv 28,220,52
s_waitcnt lgkmcnt(1)
mvv 16,224,56
R 36,1024
mvv 20,228,56
D 83,1024
mvv 24,232,56
mvv 28,236,56
mvv 16,240,60
R 40,2048
mvv 20,244,60
D 83,2048
mvv 24,248,60
mvv 28,252,60
s_waitcnt lgkmcnt(1)
v_mfma_f32_16x16x32_bf16 v[0:3], a[0:3], v[32:35], 0
R 44,3072
v_cvt_pk_bf16_f32 v64, v16, v17
v_mfma_f32_16x16x32_bf16 v[4:7], a[4:7], v[32:35], 0
D 83,3072
v_mfma_f32_16x16x32_bf16 v[8:11], a[8:11], v[32:35], 0
v_cvt_pk_bf16_f32 v65, v18, v19
v_cvt_pk_bf16_f32 v66, v20, v21
v_mfma_f32_16x16x32_bf16 v[12:15], a[12:15], v[32:35], 0
s_add_u32 m0, s50, 36864
v_cvt_pk_bf16_f32 v67, v22, v23
v_cvt_pk_bf16_f32 v68, v24, v25
mav 0,16,36
R 48,4096
v_cvt_pk_bf16_f32 v69, v26, v27
mav 4,20,36
buffer_store_dwordx4 v[64:67], v87, s[36:39], s49 offen sc1
D 84,0
mav 8,24,36
v_cvt_pk_bf16_f32 v70, v28, v29
v_cvt_pk_bf16_f32 v71, v30, v31
mav 12,28,36
s_waitcnt lgkmcnt(1)
mav 0,32,40
R 52,5120
mav 4,36,40
buffer_store_dwordx4 v[68:71], v87, s[36:39], s49 offen offset:1024 sc1
D 84,1024
mav 8,40,40
mav 12,44,40
mav 0,48,44
R 56,6144
mav 4,52,44
mav 8,56,44
mav 12,60,44
s_waitcnt lgkmcnt(1)
mav 0,64,48
R 60,7168
mav 4,68,48
mav 8,72,48
mav 12,76,48
mav 0,80,52
R 32,8192
mav 4,84,52
mav 8,88,52
mav 12,92,52
s_waitcnt lgkmcnt(1)
mav 0,96,56
R 36,9216
mav 4,100,56
mav 8,104,56
mav 12,108,56
mav 0,112,60
R 40,10240
mav 4,116,60
mav 8,120,60
mav 12,124,60
s_waitcnt lgkmcnt(1)
mav 0,128,32
R 44,11264
mav 4,132,32
mav 8,136,32
s_mov_b32 m0, 144384
mav 12,140,32
mav 0,144,36
R 48,12288
v_add_u32 v72, 1, v72
mav 4,148,36
buffer_load_dword v85, s[36:39], 0 offen sc1 lds
mav 8,152,36
mav 12,156,36
s_waitcnt lgkmcnt(1)
mav 0,160,40
R 52,13312
v_add_u32 v86, 1, v86
mav 4,164,40
mav 8,168,40
mav 12,172,40
mav 0,176,44
R 56,14336
mav 4,180,44
mav 8,184,44
mav 12,188,44
s_waitcnt lgkmcnt(1)
mav 0,192,48
R 60,15360
mav 4,196,48
mav 8,200,48
mav 12,204,48
mav 0,208,52
R 32,16384
mav 4,212,52
mav 8,216,52
mav 12,220,52
s_waitcnt lgkmcnt(1)
mav 0,224,56
R 36,17408
mav 4,228,56
mav 8,232,56
mav 12,236,56
mav 0,240,60
s_waitcnt vmcnt(2)
R 40,18432
mav 4,244,60
buffer_store_dword v72, v81, s[36:39], s52 offen sc1
mav 8,248,60
mav 12,252,60
s_waitcnt lgkmcnt(1)
mvv 0,128,32
R 44,19456
mvv 4,132,32
mvv 8,136,32
mvv 12,140,32
mvv 0,144,36
R 48,20480
mvv 4,148,36
mvv 8,152,36
mvv 12,156,36
s_waitcnt lgkmcnt(1)
mvv 0,160,40
R 52,21504
mvv 4,164,40
ds_read_b32 v74, v82 offset:0
mvv 8,168,40
mvv 12,172,40
mvv 0,176,44
R 56,22528
mvv 4,180,44
mvv 8,184,44
s_xor_b32 s50, s50, 0x10000
mvv 12,188,44
s_waitcnt lgkmcnt(1)
mvv 0,192,48
R 60,23552
v_cmp_gt_i32 vcc, v86, v74
mvv 4,196,48
mvv 8,200,48
s_waitcnt vmcnt(2)
s_add_u32 s49, s49, 313344
mvv 12,204,48
s_cbranch_vccnz CG0_6
BG0_6:
s_barrier
mvv 0,208,52
s_add_u32 m0, s50, 0
R 32,32768
mvv 4,212,52
D 83,0
mvv 8,216,52
mvv 12,220,52
s_waitcnt lgkmcnt(1)
mvv 0,224,56
R 36,33792
mvv 4,228,56
D 83,1024
mvv 8,232,56
mvv 12,236,56
mvv 0,240,60
R 40,34816
mvv 4,244,60
D 83,2048
mvv 8,248,60
mvv 12,252,60
s_waitcnt lgkmcnt(1)
v_mfma_f32_16x16x32_bf16 v[16:19], a[0:3], v[32:35], 0
R 44,35840
v_cvt_pk_bf16_f32 v64, v0, v1
v_mfma_f32_16x16x32_bf16 v[20:23], a[4:7], v[32:35], 0
D 83,3072
v_mfma_f32_16x16x32_bf16 v[24:27], a[8:11], v[32:35], 0
v_cvt_pk_bf16_f32 v65, v2, v3
v_cvt_pk_bf16_f32 v66, v4, v5
v_mfma_f32_16x16x32_bf16 v[28:31], a[12:15], v[32:35], 0
s_add_u32 m0, s50, 4096
v_cvt_pk_bf16_f32 v67, v6, v7
v_cvt_pk_bf16_f32 v68, v8, v9
mav 16,16,36
R 48,36864
v_cvt_pk_bf16_f32 v69, v10, v11
mav 20,20,36
buffer_store_dwordx4 v[64:67], v87, s[36:39], s49 offen sc1
D 84,0
mav 24,24,36
v_cvt_pk_bf16_f32 v70, v12, v13
v_cvt_pk_bf16_f32 v71, v14, v15
mav 28,28,36
s_waitcnt lgkmcnt(1)
mav 16,32,40
R 52,37888
mav 20,36,40
buffer_store_dwordx4 v[68:71], v87, s[36:39], s49 offen offset:1024 sc1
D 84,1024
mav 24,40,40
mav 28,44,40
mav 16,48,44
R 56,38912
mav 20,52,44
mav 24,56,44
mav 28,60,44
s_waitcnt lgkmcnt(1)
mav 16,64,48
R 60,39936
mav 20,68,48
mav 24,72,48
mav 28,76,48
mav 16,80,52
R 32,40960
mav 20,84,52
mav 24,88,52
mav 28,92,52
s_waitcnt lgkmcnt(1)
mav 16,96,56
R 36,41984
mav 20,100,56
mav 24,104,56
mav 28,108,56
mav 16,112,60
R 40,43008
mav 20,116,60
mav 24,120,60
mav 28,124,60
s_waitcnt lgkmcnt(1)
mav 16,128,32
R 44,44032
mav 20,132,32
mav 24,136,32
s_mov_b32 m0, 144384
mav 28,140,32
mav 16,144,36
R 48,45056
v_add_u32 v72, 1, v72
mav 20,148,36
buffer_load_dword v85, s[36:39], 0 offen sc1 lds
mav 24,152,36
mav 28,156,36
s_waitcnt lgkmcnt(1)
mav 16,160,40
R 52,46080
v_add_u32 v86, 1, v86
mav 20,164,40
mav 24,168,40
mav 28,172,40
mav 16,176,44
R 56,47104
mav 20,180,44
mav 24,184,44
mav 28,188,44
s_waitcnt lgkmcnt(1)
mav 16,192,48
R 60,48128
mav 20,196,48
s_add_u32 s60, s60, 1
mav 24,200,48
s_cbranch_scc0 LG0_4
mav 28,204,48
mav 16,208,52
R 32,49152
mav 20,212,52
mav 24,216,52
mav 28,220,52
s_waitcnt lgkmcnt(1)
mav 16,224,56
R 36,50176
mav 20,228,56
mav 24,232,56
mav 28,236,56
mav 16,240,60
s_waitcnt vmcnt(2)
R 40,51200
mav 20,244,60
buffer_store_dword v72, v81, s[36:39], s52 offen sc1
mav 24,248,60
mav 28,252,60
s_waitcnt lgkmcnt(1)
mvv 16,128,32
R 44,52224
LsG3:
s_cbranch_execz LsG7
mvv 20,132,32
mvv 24,136,32
mvv 28,140,32
mvv 16,144,36
R 48,53248
mvv 20,148,36
mvv 24,152,36
mvv 28,156,36
s_waitcnt lgkmcnt(1)
mvv 16,160,40
R 52,54272
mvv 20,164,40
ds_read_b32 v74, v82 offset:0
mvv 24,168,40
mvv 28,172,40
mvv 16,176,44
R 56,55296
mvv 20,180,44
mvv 24,184,44
mvv 28,188,44
s_waitcnt lgkmcnt(1)
mvv 16,192,48
R 60,56320
v_cmp_gt_i32 vcc, v86, v74
mvv 20,196,48
v_xor_b32 v80, 0x10000, v80
mvv 24,200,48
s_waitcnt vmcnt(2)
s_add_u32 s49, s49, 313344
mvv 28,204,48
s_cbranch_vccnz CG0_7
BG0_7:
s_barrier
mvv 16,208,52
s_add_u32 m0, s50, 32768
R 32,0
mvv 20,212,52
D 83,0
mvv 24,216,52
mvv 28,220,52
s_waitcnt lgkmcnt(1)
mvv 16,224,56
R 36,1024
mvv 20,228,56
D 83,1024
mvv 24,232,56
mvv 28,236,56
mvv 16,240,60
R 40,2048
mvv 20,244,60
D 83,2048
mvv 24,248,60
mvv 28,252,60
s_waitcnt lgkmcnt(1)
v_mfma_f32_16x16x32_bf16 v[0:3], a[0:3], v[32:35], 0
R 44,3072
v_cvt_pk_bf16_f32 v64, v16, v17
v_mfma_f32_16x16x32_bf16 v[4:7], a[4:7], v[32:35], 0
D 83,3072
v_mfma_f32_16x16x32_bf16 v[8:11], a[8:11], v[32:35], 0
v_cvt_pk_bf16_f32 v65, v18, v19
v_cvt_pk_bf16_f32 v66, v20, v21
v_mfma_f32_16x16x32_bf16 v[12:15], a[12:15], v[32:35], 0
s_add_u32 m0, s50, 36864
v_cvt_pk_bf16_f32 v67, v22, v23
v_cvt_pk_bf16_f32 v68, v24, v25
mav 0,16,36
R 48,4096
v_cvt_pk_bf16_f32 v69, v26, v27
mav 4,20,36
buffer_store_dwordx4 v[64:67], v87, s[36:39], s49 offen sc1
D 84,0
mav 8,24,36
v_cvt_pk_bf16_f32 v70, v28, v29
v_cvt_pk_bf16_f32 v71, v30, v31
mav 12,28,36
s_waitcnt lgkmcnt(1)
mav 0,32,40
R 52,5120
mav 4,36,40
buffer_store_dwordx4 v[68:71], v87, s[36:39], s49 offen offset:1024 sc1
D 84,1024
mav 8,40,40
mav 12,44,40
mav 0,48,44
R 56,6144
mav 4,52,44
mav 8,56,44
mav 12,60,44
s_waitcnt lgkmcnt(1)
mav 0,64,48
R 60,7168
mav 4,68,48
mav 8,72,48
mav 12,76,48
mav 0,80,52
R 32,8192
mav 4,84,52
mav 8,88,52
mav 12,92,52
s_waitcnt lgkmcnt(1)
mav 0,96,56
R 36,9216
mav 4,100,56
mav 8,104,56
mav 12,108,56
mav 0,112,60
R 40,10240
mav 4,116,60
mav 8,120,60
mav 12,124,60
s_waitcnt lgkmcnt(1)
mav 0,128,32
R 44,11264
mav 4,132,32
mav 8,136,32
mav 12,140,32
mav 0,144,36
R 48,12288
v_add_u32 v72, 1, v72
mav 4,148,36
mav 8,152,36
mav 12,156,36
s_waitcnt lgkmcnt(1)
mav 0,160,40
R 52,13312
mav 4,164,40
mav 8,168,40
mav 12,172,40
mav 0,176,44
R 56,14336
mav 4,180,44
mav 8,184,44
mav 12,188,44
s_waitcnt lgkmcnt(1)
mav 0,192,48
R 60,15360
mav 4,196,48
mav 8,200,48
mav 12,204,48
mav 0,208,52
R 32,16384
mav 4,212,52
mav 8,216,52
mav 12,220,52
s_waitcnt lgkmcnt(1)
mav 0,224,56
R 36,17408
mav 4,228,56
mav 8,232,56
mav 12,236,56
mav 0,240,60
s_waitcnt vmcnt(1)
R 40,18432
mav 4,244,60
buffer_store_dword v72, v81, s[36:39], s52 offen sc1
mav 8,248,60
mav 12,252,60
s_waitcnt lgkmcnt(1)
mvv 0,128,32
R 44,19456
mvv 4,132,32
mvv 8,136,32
mvv 12,140,32
mvv 0,144,36
R 48,20480
mvv 4,148,36
mvv 8,152,36
mvv 12,156,36
s_waitcnt lgkmcnt(1)
mvv 0,160,40
R 52,21504
mvv 4,164,40
mvv 8,168,40
mvv 12,172,40
mvv 0,176,44
R 56,22528
mvv 4,180,44
mvv 8,184,44
mvv 12,188,44
s_waitcnt lgkmcnt(1)
mvv 0,192,48
R 60,23552
mvv 4,196,48
mvv 8,200,48
s_waitcnt vmcnt(1)
s_add_u32 s49, s49, 313344
mvv 12,204,48
s_barrier
mvv 0,208,52
R 32,32768
mvv 4,212,52
mvv 8,216,52
mvv 12,220,52
s_waitcnt lgkmcnt(1)
mvv 0,224,56
R 36,33792
mvv 4,228,56
mvv 8,232,56
mvv 12,236,56
mvv 0,240,60
R 40,34816
mvv 4,244,60
mvv 8,248,60
mvv 12,252,60
s_waitcnt lgkmcnt(1)
v_mfma_f32_16x16x32_bf16 v[16:19], a[0:3], v[32:35], 0
R 44,35840
v_mfma_f32_16x16x32_bf16 v[20:23], a[4:7], v[32:35], 0
v_cvt_pk_bf16_f32 v64, v0, v1
v_cvt_pk_bf16_f32 v65, v2, v3
v_mfma_f32_16x16x32_bf16 v[24:27], a[8:11], v[32:35], 0
v_cvt_pk_bf16_f32 v66, v4, v5
v_cvt_pk_bf16_f32 v67, v6, v7
v_mfma_f32_16x16x32_bf16 v[28:31], a[12:15], v[32:35], 0
v_cvt_pk_bf16_f32 v68, v8, v9
v_cvt_pk_bf16_f32 v69, v10, v11
mav 16,16,36
R 48,36864
v_cvt_pk_bf16_f32 v70, v12, v13
mav 20,20,36
buffer_store_dwordx4 v[64:67], v87, s[36:39], s49 offen sc1
v_cvt_pk_bf16_f32 v71, v14, v15
mav 24,24,36
mav 28,28,36
s_waitcnt lgkmcnt(1)
mav 16,32,40
R 52,37888
mav 20,36,40
buffer_store_dwordx4 v[68:71], v87, s[36:39], s49 offen offset:1024 sc1
mav 24,40,40
mav 28,44,40
mav 16,48,44
R 56,38912
mav 20,52,44
mav 24,56,44
mav 28,60,44
s_waitcnt lgkmcnt(1)
mav 16,64,48
R 60,39936
mav 20,68,48
mav 24,72,48
mav 28,76,48
mav 16,80,52
R 32,40960
mav 20,84,52
mav 24,88,52
mav 28,92,52
s_waitcnt lgkmcnt(1)
mav 16,96,56
R 36,41984
mav 20,100,56
mav 24,104,56
mav 28,108,56
mav 16,112,60
R 40,43008
mav 20,116,60
mav 24,120,60
mav 28,124,60
s_waitcnt lgkmcnt(1)
mav 16,128,32
R 44,44032
mav 20,132,32
mav 24,136,32
mav 28,140,32
mav 16,144,36
R 48,45056
v_add_u32 v72, 1, v72
mav 20,148,36
mav 24,152,36
mav 28,156,36
s_waitcnt lgkmcnt(1)
mav 16,160,40
R 52,46080
mav 20,164,40
mav 24,168,40
mav 28,172,40
mav 16,176,44
R 56,47104
mav 20,180,44
mav 24,184,44
mav 28,188,44
s_waitcnt lgkmcnt(1)
mav 16,192,48
R 60,48128
mav 20,196,48
mav 24,200,48
mav 28,204,48
mav 16,208,52
R 32,49152
mav 20,212,52
mav 24,216,52
mav 28,220,52
s_waitcnt lgkmcnt(1)
mav 16,224,56
R 36,50176
mav 20,228,56
mav 24,232,56
mav 28,236,56
mav 16,240,60
s_waitcnt vmcnt(0)
R 40,51200
mav 20,244,60
buffer_store_dword v72, v81, s[36:39], s52 offen sc1
mav 24,248,60
mav 28,252,60
s_waitcnt lgkmcnt(1)
mvv 16,128,32
R 44,52224
mvv 20,132,32
mvv 24,136,32
mvv 28,140,32
mvv 16,144,36
R 48,53248
mvv 20,148,36
mvv 24,152,36
mvv 28,156,36
s_waitcnt lgkmcnt(1)
mvv 16,160,40
R 52,54272
mvv 20,164,40
mvv 24,168,40
mvv 28,172,40
mvv 16,176,44
R 56,55296
mvv 20,180,44
mvv 24,184,44
mvv 28,188,44
s_waitcnt lgkmcnt(1)
mvv 16,192,48
R 60,56320
mvv 20,196,48
mvv 24,200,48
s_add_u32 s49, s49, 313344
mvv 28,204,48
mvv 16,208,52
mvv 20,212,52
mvv 24,216,52
mvv 28,220,52
s_waitcnt lgkmcnt(0)
mvv 16,224,56
mvv 20,228,56
mvv 24,232,56
mvv 28,236,56
mvv 16,240,60
mvv 20,244,60
mvv 24,248,60
mvv 28,252,60
s_nop 4
v_cvt_pk_bf16_f32 v64, v16, v17
v_cvt_pk_bf16_f32 v65, v18, v19
v_cvt_pk_bf16_f32 v66, v20, v21
v_cvt_pk_bf16_f32 v67, v22, v23
buffer_store_dwordx4 v[64:67], v87, s[36:39], s49 offen sc1
v_cvt_pk_bf16_f32 v68, v24, v25
v_cvt_pk_bf16_f32 v69, v26, v27
v_cvt_pk_bf16_f32 v70, v28, v29
v_cvt_pk_bf16_f32 v71, v30, v31
buffer_store_dwordx4 v[68:71], v87, s[36:39], s49 offen offset:1024 sc1
v_add_u32 v72, 35, v72
s_waitcnt vmcnt(0)
buffer_store_dword v72, v81, s[36:39], s52 offen sc1
s_endpgm
CG0_1:
s_cmp_eq_u32 s61, 0
s_cbranch_scc1 BG0_1
s_sub_u32 s61, s61, 1
s_sleep 8
buffer_load_dwordx2 v[74:75], v85, s[36:39], 0 offen sc1
s_waitcnt vmcnt(0)
ds_write_b32 v82, v74
v_sub_u32 v74, v74, v86
v_xor_b32 v75, v75, v73
v_cmp_lt_u64 vcc, s[62:63], v[74:75]
s_waitcnt lgkmcnt(0)
s_cbranch_vccnz CG0_1
s_branch BG0_1
CG0_2:
s_mov_b32 s58, m0
s_mov_b32 m0, 144384
DG0_2:
s_cmp_eq_u32 s61, 0
s_cbranch_scc1 EG0_2
s_sub_u32 s61, s61, 1
buffer_load_dword v85, s[36:39], 0 offen sc1 lds
s_sleep 1
ds_read_b32 v74, v82
s_waitcnt lgkmcnt(0)
v_cmp_gt_i32 vcc, v86, v74
s_cbranch_vccnz DG0_2
EG0_2:
s_mov_b32 m0, s58
s_branch BG0_2
CG0_3:
s_mov_b32 s58, m0
s_mov_b32 m0, 144384
DG0_3:
s_cmp_eq_u32 s61, 0
s_cbranch_scc1 EG0_3
s_sub_u32 s61, s61, 1
buffer_load_dword v85, s[36:39], 0 offen sc1 lds
s_sleep 1
ds_read_b32 v74, v82
s_waitcnt lgkmcnt(0)
v_cmp_gt_i32 vcc, v86, v74
s_cbranch_vccnz DG0_3
EG0_3:
s_mov_b32 m0, s58
s_branch BG0_3
CG0_5:
s_mov_b32 s58, m0
s_mov_b32 m0, 144384
DG0_5:
s_cmp_eq_u32 s61, 0
s_cbranch_scc1 EG0_5
s_sub_u32 s61, s61, 1
buffer_load_dword v85, s[36:39], 0 offen sc1 lds
s_sleep 1
ds_read_b32 v74, v82
s_waitcnt lgkmcnt(0)
v_cmp_gt_i32 vcc, v86, v74
s_cbranch_vccnz DG0_5
EG0_5:
s_mov_b32 m0, s58
s_branch BG0_5
CG0_6:
s_mov_b32 s58, m0
s_mov_b32 m0, 144384
DG0_6:
s_cmp_eq_u32 s61, 0
s_cbranch_scc1 EG0_6
s_sub_u32 s61, s61, 1
buffer_load_dword v85, s[36:39], 0 offen sc1 lds
s_sleep 1
ds_read_b32 v74, v82
s_waitcnt lgkmcnt(0)
v_cmp_gt_i32 vcc, v86, v74
s_cbranch_vccnz DG0_6
EG0_6:
s_mov_b32 m0, s58
s_branch BG0_6
CG0_7:
s_mov_b32 s58, m0
s_mov_b32 m0, 144384
DG0_7:
s_cmp_eq_u32 s61, 0
s_cbranch_scc1 EG0_7
s_sub_u32 s61, s61, 1
buffer_load_dword v85, s[36:39], 0 offen sc1 lds
s_sleep 1
ds_read_b32 v74, v82
s_waitcnt lgkmcnt(0)
v_cmp_gt_i32 vcc, v86, v74
s_cbranch_vccnz DG0_7
EG0_7:
s_mov_b32 m0, s58
s_branch BG0_7
LvG1:
LsG4:
s_cbranch_execz LsG8
v_add_u32 v72, 1, v72
s_add_u32 s49, s49, 313344
s_barrier
s_add_u32 m0, s50, 0
buffer_store_dword v72, v81, s[36:39], s52 offen sc1
D 83,0
D 83,1024
D 83,2048
D 83,3072
s_add_u32 m0, s50, 4096
s_nop 0
D 84,0
D 84,1024
v_add_u32 v72, 1, v72
s_waitcnt vmcnt(0)
s_add_u32 s49, s49, 313344
s_barrier
s_add_u32 m0, s50, 32768
R 32,0
buffer_store_dword v72, v81, s[36:39], s52 offen sc1
D 83,0
R 36,1024
D 83,1024
R 40,2048
D 83,2048
s_waitcnt lgkmcnt(1)
v_mfma_f32_16x16x32_bf16 v[0:3], a[0:3], v[32:35], 0
R 44,3072
v_mfma_f32_16x16x32_bf16 v[4:7], a[4:7], v[32:35], 0
v_mfma_f32_16x16x32_bf16 v[8:11], a[8:11], v[32:35], 0
v_mfma_f32_16x16x32_bf16 v[12:15], a[12:15], v[32:35], 0
mav 0,16,36
D 83,3072
mav 4,20,36
s_add_u32 m0, s50, 36864
R 48,4096
mav 8,24,36
mav 12,28,36
s_waitcnt lgkmcnt(1)
mav 0,32,40
D 84,0
mav 4,36,40
R 52,5120
mav 8,40,40
mav 12,44,40
mav 0,48,44
D 84,1024
mav 4,52,44
R 56,6144
mav 8,56,44
mav 12,60,44
s_waitcnt lgkmcnt(1)
mav 0,64,48
R 60,7168
mav 4,68,48
mav 8,72,48
mav 12,76,48
mav 0,80,52
R 32,8192
mav 4,84,52
mav 8,88,52
mav 12,92,52
s_waitcnt lgkmcnt(1)
mav 0,96,56
R 36,9216
mav 4,100,56
mav 8,104,56
mav 12,108,56
mav 0,112,60
R 40,10240
mav 4,116,60
mav 8,120,60
mav 12,124,60
s_waitcnt lgkmcnt(1)
mav 0,128,32
R 44,11264
mav 4,132,32
mav 8,136,32
mav 12,140,32
mav 0,144,36
R 48,12288
mav 4,148,36
mav 8,152,36
mav 12,156,36
s_waitcnt lgkmcnt(1)
mav 0,160,40
R 52,13312
mav 4,164,40
mav 8,168,40
mav 12,172,40
mav 0,176,44
R 56,14336
mav 4,180,44
mav 8,184,44
mav 12,188,44
s_waitcnt lgkmcnt(1)
mav 0,192,48
R 60,15360
mav 4,196,48
mav 8,200,48
mav 12,204,48
mav 0,208,52
R 32,16384
mav 4,212,52
mav 8,216,52
mav 12,220,52
s_waitcnt lgkmcnt(1)
mav 0,224,56
R 36,17408
mav 4,228,56
mav 8,232,56
mav 12,236,56
mav 0,240,60
R 40,18432
mav 4,244,60
mav 8,248,60
mav 12,252,60
s_waitcnt lgkmcnt(1)
mvv 0,128,32
R 44,19456
mvv 4,132,32
mvv 8,136,32
mvv 12,140,32
mvv 0,144,36
R 48,20480
mvv 4,148,36
mvv 8,152,36
mvv 12,156,36
s_waitcnt lgkmcnt(1)
mvv 0,160,40
R 52,21504
LsG5:
s_cbranch_execz LsG9
mvv 4,164,40
mvv 8,168,40
mvv 12,172,40
mvv 0,176,44
R 56,22528
mvv 4,180,44
mvv 8,184,44
s_xor_b32 s50, s50, 0x10000
mvv 12,188,44
s_waitcnt lgkmcnt(1)
mvv 0,192,48
R 60,23552
mvv 4,196,48
mvv 8,200,48
s_waitcnt vmcnt(0)
s_add_u32 s49, s49, 313344
mvv 12,204,48
s_barrier
mvv 0,208,52
s_add_u32 m0, s50, 0
R 32,32768
mvv 4,212,52
mvv 8,216,52
mvv 12,220,52
s_waitcnt lgkmcnt(1)
mvv 0,224,56
D 83,0
mvv 4,228,56
R 36,33792
mvv 8,232,56
mvv 12,236,56
mvv 0,240,60
D 83,1024
mvv 4,244,60
R 40,34816
mvv 8,248,60
mvv 12,252,60
s_waitcnt lgkmcnt(1)
v_mfma_f32_16x16x32_bf16 v[16:19], a[0:3], v[32:35], 0
D 83,2048
v_mfma_f32_16x16x32_bf16 v[20:23], a[4:7], v[32:35], 0
R 44,35840
v_cvt_pk_bf16_f32 v64, v0, v1
v_mfma_f32_16x16x32_bf16 v[24:27], a[8:11], v[32:35], 0
v_cvt_pk_bf16_f32 v65, v2, v3
v_cvt_pk_bf16_f32 v66, v4, v5
v_mfma_f32_16x16x32_bf16 v[28:31], a[12:15], v[32:35], 0
v_cvt_pk_bf16_f32 v67, v6, v7
v_cvt_pk_bf16_f32 v68, v8, v9
mav 16,16,36
buffer_store_dwordx4 v[64:67], v87, s[36:39], s49 offen sc1
D 83,3072
mav 20,20,36
s_add_u32 m0, s50, 4096
R 48,36864
v_cvt_pk_bf16_f32 v69, v10, v11
mav 24,24,36
v_cvt_pk_bf16_f32 v70, v12, v13
v_cvt_pk_bf16_f32 v71, v14, v15
mav 28,28,36
s_waitcnt lgkmcnt(1)
mav 16,32,40
buffer_store_dwordx4 v[68:71], v87, s[36:39], s49 offen offset:1024 sc1
D 84,0
mav 20,36,40
R 52,37888
mav 24,40,40
mav 28,44,40
mav 16,48,44
D 84,1024
mav 20,52,44
R 56,38912
mav 24,56,44
mav 28,60,44
s_waitcnt lgkmcnt(1)
mav 16,64,48
R 60,39936
mav 20,68,48
mav 24,72,48
mav 28,76,48
mav 16,80,52
R 32,40960
mav 20,84,52
mav 24,88,52
mav 28,92,52
s_waitcnt lgkmcnt(1)
mav 16,96,56
R 36,41984
mav 20,100,56
mav 24,104,56
mav 28,108,56
mav 16,112,60
R 40,43008
mav 20,116,60
mav 24,120,60
mav 28,124,60
s_waitcnt lgkmcnt(1)
mav 16,128,32
R 44,44032
mav 20,132,32
mav 24,136,32
mav 28,140,32
mav 16,144,36
R 48,45056
v_add_u32 v72, 1, v72
mav 20,148,36
mav 24,152,36
mav 28,156,36
s_waitcnt lgkmcnt(1)
mav 16,160,40
R 52,46080
mav 20,164,40
mav 24,168,40
mav 28,172,40
mav 16,176,44
R 56,47104
mav 20,180,44
mav 24,184,44
mav 28,188,44
s_waitcnt lgkmcnt(1)
mav 16,192,48
R 60,48128
mav 20,196,48
mav 24,200,48
s_mov_b32 s60, -30
.p2align 6
LG1_1:
mav 28,204,48
mav 16,208,52
R 32,49152
mav 20,212,52
mav 24,216,52
mav 28,220,52
s_waitcnt lgkmcnt(1)
mav 16,224,56
R 36,50176
mav 20,228,56
mav 24,232,56
mav 28,236,56
mav 16,240,60
s_waitcnt vmcnt(2)
buffer_store_dword v72, v81, s[36:39], s52 offen sc1
mav 20,244,60
R 40,51200
mav 24,248,60
mav 28,252,60
s_waitcnt lgkmcnt(1)
mvv 16,128,32
R 44,52224
mvv 20,132,32
mvv 24,136,32
mvv 28,140,32
mvv 16,144,36
R 48,53248
mvv 20,148,36
mvv 24,152,36
mvv 28,156,36
s_waitcnt lgkmcnt(1)
mvv 16,160,40
R 52,54272
mvv 20,164,40
mvv 24,168,40
mvv 28,172,40
mvv 16,176,44
R 56,55296
mvv 20,180,44
mvv 24,184,44
mvv 28,188,44
s_waitcnt lgkmcnt(1)
mvv 16,192,48
R 60,56320
mvv 20,196,48
v_xor_b32 v80, 0x10000, v80
mvv 24,200,48
s_waitcnt vmcnt(1)
s_add_u32 s49, s49, 313344
mvv 28,204,48
s_barrier
mvv 16,208,52
s_add_u32 m0, s50, 32768
R 32,0
mvv 20,212,52
mvv 24,216,52
mvv 28,220,52
s_waitcnt lgkmcnt(1)
mvv 16,224,56
D 83,0
mvv 20,228,56
R 36,1024
mvv 24,232,56
mvv 28,236,56
mvv 16,240,60
D 83,1024
mvv 20,244,60
R 40,2048
mvv 24,248,60
mvv 28,252,60
s_waitcnt lgkmcnt(1)
v_mfma_f32_16x16x32_bf16 v[0:3], a[0:3], v[32:35], 0
D 83,2048
v_mfma_f32_16x16x32_bf16 v[4:7], a[4:7], v[32:35], 0
R 44,3072
v_cvt_pk_bf16_f32 v64, v16, v17
v_mfma_f32_16x16x32_bf16 v[8:11], a[8:11], v[32:35], 0
v_cvt_pk_bf16_f32 v65, v18, v19
v_cvt_pk_bf16_f32 v66, v20, v21
v_mfma_f32_16x16x32_bf16 v[12:15], a[12:15], v[32:35], 0
v_cvt_pk_bf16_f32 v67, v22, v23
v_cvt_pk_bf16_f32 v68, v24, v25
mav 0,16,36
buffer_store_dwordx4 v[64:67], v87, s[36:39], s49 offen sc1
D 83,3072
mav 4,20,36
s_add_u32 m0, s50, 36864
R 48,4096
v_cvt_pk_bf16_f32 v69, v26, v27
mav 8,24,36
v_cvt_pk_bf16_f32 v70, v28, v29
v_cvt_pk_bf16_f32 v71, v30, v31
mav 12,28,36
s_waitcnt lgkmcnt(1)
mav 0,32,40
buffer_store_dwordx4 v[68:71], v87, s[36:39], s49 offen offset:1024 sc1
D 84,0
mav 4,36,40
R 52,5120
mav 8,40,40
mav 12,44,40
mav 0,48,44
D 84,1024
mav 4,52,44
R 56,6144
mav 8,56,44
mav 12,60,44
s_waitcnt lgkmcnt(1)
mav 0,64,48
R 60,7168
mav 4,68,48
mav 8,72,48
mav 12,76,48
mav 0,80,52
R 32,8192
mav 4,84,52
mav 8,88,52
mav 12,92,52
s_waitcnt lgkmcnt(1)
mav 0,96,56
R 36,9216
mav 4,100,56
mav 8,104,56
mav 12,108,56
mav 0,112,60
R 40,10240
mav 4,116,60
mav 8,120,60
mav 12,124,60
s_waitcnt lgkmcnt(1)
mav 0,128,32
R 44,11264
mav 4,132,32
mav 8,136,32
mav 12,140,32
mav 0,144,36
R 48,12288
v_add_u32 v72, 1, v72
mav 4,148,36
mav 8,152,36
mav 12,156,36
s_waitcnt lgkmcnt(1)
mav 0,160,40
R 52,13312
mav 4,164,40
mav 8,168,40
mav 12,172,40
mav 0,176,44
R 56,14336
mav 4,180,44
mav 8,184,44
mav 12,188,44
s_waitcnt lgkmcnt(1)
mav 0,192,48
R 60,15360
mav 4,196,48
mav 8,200,48
mav 12,204,48
mav 0,208,52
R 32,16384
mav 4,212,52
mav 8,216,52
mav 12,220,52
s_waitcnt lgkmcnt(1)
mav 0,224,56
R 36,17408
mav 4,228,56
mav 8,232,56
mav 12,236,56
mav 0,240,60
s_waitcnt vmcnt(2)
buffer_store_dword v72, v81, s[36:39], s52 offen sc1
mav 4,244,60
R 40,18432
mav 8,248,60
mav 12,252,60
s_waitcnt lgkmcnt(1)
mvv 0,128,32
)"
R"(
R 44,19456
mvv 4,132,32
mvv 8,136,32
mvv 12,140,32
mvv 0,144,36
R 48,20480
mvv 4,148,36
mvv 8,152,36
mvv 12,156,36
s_waitcnt lgkmcnt(1)
mvv 0,160,40
R 52,21504
mvv 4,164,40
mvv 8,168,40
mvv 12,172,40
mvv 0,176,44
R 56,22528
mvv 4,180,44
mvv 8,184,44
s_xor_b32 s50, s50, 0x10000
mvv 12,188,44
s_waitcnt lgkmcnt(1)
mvv 0,192,48
R 60,23552
mvv 4,196,48
mvv 8,200,48
s_waitcnt vmcnt(1)
s_add_u32 s49, s49, 313344
mvv 12,204,48
s_barrier
mvv 0,208,52
s_add_u32 m0, s50, 0
R 32,32768
mvv 4,212,52
mvv 8,216,52
mvv 12,220,52
s_waitcnt lgkmcnt(1)
mvv 0,224,56
D 83,0
mvv 4,228,56
R 36,33792
mvv 8,232,56
mvv 12,236,56
mvv 0,240,60
D 83,1024
mvv 4,244,60
R 40,34816
mvv 8,248,60
mvv 12,252,60
s_waitcnt lgkmcnt(1)
v_mfma_f32_16x16x32_bf16 v[16:19], a[0:3], v[32:35], 0
D 83,2048
v_mfma_f32_16x16x32_bf16 v[20:23], a[4:7], v[32:35], 0
R 44,35840
v_cvt_pk_bf16_f32 v64, v0, v1
v_mfma_f32_16x16x32_bf16 v[24:27], a[8:11], v[32:35], 0
v_cvt_pk_bf16_f32 v65, v2, v3
v_cvt_pk_bf16_f32 v66, v4, v5
v_mfma_f32_16x16x32_bf16 v[28:31], a[12:15], v[32:35], 0
v_cvt_pk_bf16_f32 v67, v6, v7
v_cvt_pk_bf16_f32 v68, v8, v9
mav 16,16,36
buffer_store_dwordx4 v[64:67], v87, s[36:39], s49 offen sc1
D 83,3072
mav 20,20,36
s_add_u32 m0, s50, 4096
R 48,36864
v_cvt_pk_bf16_f32 v69, v10, v11
mav 24,24,36
v_cvt_pk_bf16_f32 v70, v12, v13
v_cvt_pk_bf16_f32 v71, v14, v15
mav 28,28,36
s_waitcnt lgkmcnt(1)
mav 16,32,40
buffer_store_dwordx4 v[68:71], v87, s[36:39], s49 offen offset:1024 sc1
D 84,0
mav 20,36,40
R 52,37888
mav 24,40,40
mav 28,44,40
mav 16,48,44
D 84,1024
mav 20,52,44
R 56,38912
mav 24,56,44
mav 28,60,44
s_waitcnt lgkmcnt(1)
mav 16,64,48
R 60,39936
mav 20,68,48
mav 24,72,48
mav 28,76,48
mav 16,80,52
R 32,40960
mav 20,84,52
mav 24,88,52
mav 28,92,52
s_waitcnt lgkmcnt(1)
mav 16,96,56
R 36,41984
mav 20,100,56
mav 24,104,56
mav 28,108,56
mav 16,112,60
R 40,43008
mav 20,116,60
mav 24,120,60
mav 28,124,60
s_waitcnt lgkmcnt(1)
mav 16,128,32
R 44,44032
mav 20,132,32
mav 24,136,32
mav 28,140,32
mav 16,144,36
R 48,45056
v_add_u32 v72, 1, v72
mav 20,148,36
mav 24,152,36
mav 28,156,36
s_waitcnt lgkmcnt(1)
mav 16,160,40
R 52,46080
mav 20,164,40
mav 24,168,40
mav 28,172,40
mav 16,176,44
R 56,47104
mav 20,180,44
mav 24,184,44
mav 28,188,44
s_waitcnt lgkmcnt(1)
mav 16,192,48
R 60,48128
mav 20,196,48
s_add_u32 s60, s60, 1
mav 24,200,48
s_cbranch_scc0 LG1_1
mav 28,204,48
mav 16,208,52
R 32,49152
mav 20,212,52
mav 24,216,52
mav 28,220,52
s_waitcnt lgkmcnt(1)
mav 16,224,56
R 36,50176
mav 20,228,56
mav 24,232,56
mav 28,236,56
mav 16,240,60
s_waitcnt vmcnt(2)
buffer_store_dword v72, v81, s[36:39], s52 offen sc1
mav 20,244,60
R 40,51200
mav 24,248,60
mav 28,252,60
s_waitcnt lgkmcnt(1)
mvv 16,128,32
R 44,52224
mvv 20,132,32
mvv 24,136,32
mvv 28,140,32
mvv 16,144,36
R 48,53248
mvv 20,148,36
mvv 24,152,36
mvv 28,156,36
s_waitcnt lgkmcnt(1)
mvv 16,160,40
R 52,54272
mvv 20,164,40
mvv 24,168,40
mvv 28,172,40
mvv 16,176,44
R 56,55296
mvv 20,180,44
mvv 24,184,44
mvv 28,188,44
s_waitcnt lgkmcnt(1)
mvv 16,192,48
R 60,56320
mvv 20,196,48
v_xor_b32 v80, 0x10000, v80
mvv 24,200,48
s_waitcnt vmcnt(1)
s_add_u32 s49, s49, 313344
mvv 28,204,48
s_barrier
mvv 16,208,52
s_add_u32 m0, s50, 32768
R 32,0
mvv 20,212,52
mvv 24,216,52
mvv 28,220,52
s_waitcnt lgkmcnt(1)
mvv 16,224,56
D 83,0
mvv 20,228,56
R 36,1024
mvv 24,232,56
mvv 28,236,56
mvv 16,240,60
D 83,1024
mvv 20,244,60
R 40,2048
mvv 24,248,60
mvv 28,252,60
s_waitcnt lgkmcnt(1)
v_mfma_f32_16x16x32_bf16 v[0:3], a[0:3], v[32:35], 0
D 83,2048
v_mfma_f32_16x16x32_bf16 v[4:7], a[4:7], v[32:35], 0
R 44,3072
v_cvt_pk_bf16_f32 v64, v16, v17
v_mfma_f32_16x16x32_bf16 v[8:11], a[8:11], v[32:35], 0
v_cvt_pk_bf16_f32 v65, v18, v19
v_cvt_pk_bf16_f32 v66, v20, v21
v_mfma_f32_16x16x32_bf16 v[12:15], a[12:15], v[32:35], 0
v_cvt_pk_bf16_f32 v67, v22, v23
v_cvt_pk_bf16_f32 v68, v24, v25
mav 0,16,36
buffer_store_dwordx4 v[64:67], v87, s[36:39], s49 offen sc1
D 83,3072
mav 4,20,36
s_add_u32 m0, s50, 36864
R 48,4096
v_cvt_pk_bf16_f32 v69, v26, v27
mav 8,24,36
v_cvt_pk_bf16_f32 v70, v28, v29
v_cvt_pk_bf16_f32 v71, v30, v31
mav 12,28,36
s_waitcnt lgkmcnt(1)
mav 0,32,40
buffer_store_dwordx4 v[68:71], v87, s[36:39], s49 offen offset:1024 sc1
D 84,0
mav 4,36,40
R 52,5120
LsG6:
s_cbranch_execz LsG10
mav 8,40,40
mav 12,44,40
mav 0,48,44
D 84,1024
mav 4,52,44
R 56,6144
mav 8,56,44
mav 12,60,44
s_waitcnt lgkmcnt(1)
mav 0,64,48
R 60,7168
mav 4,68,48
mav 8,72,48
mav 12,76,48
mav 0,80,52
R 32,8192
mav 4,84,52
mav 8,88,52
mav 12,92,52
s_waitcnt lgkmcnt(1)
mav 0,96,56
R 36,9216
mav 4,100,56
mav 8,104,56
mav 12,108,56
mav 0,112,60
R 40,10240
mav 4,116,60
mav 8,120,60
mav 12,124,60
s_waitcnt lgkmcnt(1)
mav 0,128,32
R 44,11264
mav 4,132,32
mav 8,136,32
mav 12,140,32
mav 0,144,36
R 48,12288
v_add_u32 v72, 1, v72
mav 4,148,36
mav 8,152,36
mav 12,156,36
s_waitcnt lgkmcnt(1)
mav 0,160,40
R 52,13312
mav 4,164,40
mav 8,168,40
mav 12,172,40
mav 0,176,44
R 56,14336
mav 4,180,44
mav 8,184,44
mav 12,188,44
s_waitcnt lgkmcnt(1)
mav 0,192,48
R 60,15360
mav 4,196,48
mav 8,200,48
mav 12,204,48
mav 0,208,52
R 32,16384
mav 4,212,52
mav 8,216,52
mav 12,220,52
s_waitcnt lgkmcnt(1)
mav 0,224,56
R 36,17408
mav 4,228,56
mav 8,232,56
mav 12,236,56
mav 0,240,60
s_waitcnt vmcnt(2)
buffer_store_dword v72, v81, s[36:39], s52 offen sc1
mav 4,244,60
R 40,18432
mav 8,248,60
mav 12,252,60
s_waitcnt lgkmcnt(1)
mvv 0,128,32
R 44,19456
mvv 4,132,32
mvv 8,136,32
mvv 12,140,32
mvv 0,144,36
R 48,20480
mvv 4,148,36
mvv 8,152,36
mvv 12,156,36
s_waitcnt lgkmcnt(1)
mvv 0,160,40
R 52,21504
mvv 4,164,40
mvv 8,168,40
mvv 12,172,40
mvv 0,176,44
R 56,22528
mvv 4,180,44
mvv 8,184,44
mvv 12,188,44
s_waitcnt lgkmcnt(1)
mvv 0,192,48
R 60,23552
mvv 4,196,48
mvv 8,200,48
s_waitcnt vmcnt(1)
s_add_u32 s49, s49, 313344
mvv 12,204,48
s_barrier
mvv 0,208,52
R 32,32768
mvv 4,212,52
mvv 8,216,52
mvv 12,220,52
s_waitcnt lgkmcnt(1)
mvv 0,224,56
R 36,33792
mvv 4,228,56
mvv 8,232,56
mvv 12,236,56
mvv 0,240,60
R 40,34816
mvv 4,244,60
mvv 8,248,60
mvv 12,252,60
s_waitcnt lgkmcnt(1)
v_mfma_f32_16x16x32_bf16 v[16:19], a[0:3], v[32:35], 0
R 44,35840
v_mfma_f32_16x16x32_bf16 v[20:23], a[4:7], v[32:35], 0
v_cvt_pk_bf16_f32 v64, v0, v1
v_cvt_pk_bf16_f32 v65, v2, v3
v_mfma_f32_16x16x32_bf16 v[24:27], a[8:11], v[32:35], 0
v_cvt_pk_bf16_f32 v66, v4, v5
v_cvt_pk_bf16_f32 v67, v6, v7
v_mfma_f32_16x16x32_bf16 v[28:31], a[12:15], v[32:35], 0
v_cvt_pk_bf16_f32 v68, v8, v9
v_cvt_pk_bf16_f32 v69, v10, v11
mav 16,16,36
buffer_store_dwordx4 v[64:67], v87, s[36:39], s49 offen sc1
R 48,36864
v_cvt_pk_bf16_f32 v70, v12, v13
mav 20,20,36
v_cvt_pk_bf16_f32 v71, v14, v15
mav 24,24,36
mav 28,28,36
s_waitcnt lgkmcnt(1)
mav 16,32,40
buffer_store_dwordx4 v[68:71], v87, s[36:39], s49 offen offset:1024 sc1
R 52,37888
mav 20,36,40
mav 24,40,40
mav 28,44,40
mav 16,48,44
R 56,38912
mav 20,52,44
mav 24,56,44
mav 28,60,44
s_waitcnt lgkmcnt(1)
mav 16,64,48
R 60,39936
mav 20,68,48
mav 24,72,48
mav 28,76,48
mav 16,80,52
R 32,40960
mav 20,84,52
mav 24,88,52
mav 28,92,52
s_waitcnt lgkmcnt(1)
mav 16,96,56
R 36,41984
mav 20,100,56
mav 24,104,56
mav 28,108,56
mav 16,112,60
R 40,43008
mav 20,116,60
mav 24,120,60
mav 28,124,60
s_waitcnt lgkmcnt(1)
mav 16,128,32
R 44,44032
mav 20,132,32
mav 24,136,32
mav 28,140,32
mav 16,144,36
R 48,45056
v_add_u32 v72, 1, v72
mav 20,148,36
mav 24,152,36
mav 28,156,36
s_waitcnt lgkmcnt(1)
mav 16,160,40
R 52,46080
mav 20,164,40
mav 24,168,40
mav 28,172,40
mav 16,176,44
R 56,47104
mav 20,180,44
mav 24,184,44
mav 28,188,44
s_waitcnt lgkmcnt(1)
mav 16,192,48
R 60,48128
mav 20,196,48
mav 24,200,48
mav 28,204,48
mav 16,208,52
R 32,49152
mav 20,212,52
mav 24,216,52
mav 28,220,52
s_waitcnt lgkmcnt(1)
mav 16,224,56
R 36,50176
mav 20,228,56
mav 24,232,56
mav 28,236,56
mav 16,240,60
s_waitcnt vmcnt(0)
buffer_store_dword v72, v81, s[36:39], s52 offen sc1
mav 20,244,60
R 40,51200
mav 24,248,60
mav 28,252,60
s_waitcnt lgkmcnt(1)
mvv 16,128,32
R 44,52224
mvv 20,132,32
mvv 24,136,32
mvv 28,140,32
mvv 16,144,36
R 48,53248
mvv 20,148,36
mvv 24,152,36
mvv 28,156,36
s_waitcnt lgkmcnt(1)
mvv 16,160,40
R 52,54272
mvv 20,164,40
mvv 24,168,40
mvv 28,172,40
mvv 16,176,44
R 56,55296
mvv 20,180,44
mvv 24,184,44
mvv 28,188,44
s_waitcnt lgkmcnt(1)
mvv 16,192,48
R 60,56320
mvv 20,196,48
mvv 24,200,48
s_add_u32 s49, s49, 313344
mvv 28,204,48
mvv 16,208,52
mvv 20,212,52
mvv 24,216,52
mvv 28,220,52
s_waitcnt lgkmcnt(0)
mvv 16,224,56
mvv 20,228,56
mvv 24,232,56
mvv 28,236,56
mvv 16,240,60
mvv 20,244,60
mvv 24,248,60
mvv 28,252,60
s_nop 4
v_cvt_pk_bf16_f32 v64, v16, v17
v_cvt_pk_bf16_f32 v65, v18, v19
v_cvt_pk_bf16_f32 v66, v20, v21
v_cvt_pk_bf16_f32 v67, v22, v23
buffer_store_dwordx4 v[64:67], v87, s[36:39], s49 offen sc1
v_cvt_pk_bf16_f32 v68, v24, v25
v_cvt_pk_bf16_f32 v69, v26, v27
v_cvt_pk_bf16_f32 v70, v28, v29
v_cvt_pk_bf16_f32 v71, v30, v31
buffer_store_dwordx4 v[68:71], v87, s[36:39], s49 offen offset:1024 sc1
v_add_u32 v72, 35, v72
s_waitcnt vmcnt(0)
buffer_store_dword v72, v81, s[36:39], s52 offen sc1
s_endpgm
LvG2:
LsG7:
s_cbranch_execz LsG11
v_add_u32 v72, 1, v72
s_add_u32 s49, s49, 313344
s_barrier
s_add_u32 m0, s50, 0
buffer_store_dword v72, v81, s[36:39], s52 offen sc1
D 83,0
D 83,1024
D 83,2048
D 83,3072
s_add_u32 m0, s50, 4096
s_nop 0
D 84,0
D 84,1024
v_add_u32 v72, 1, v72
s_waitcnt vmcnt(0)
s_add_u32 s49, s49, 313344
s_barrier
s_add_u32 m0, s50, 32768
R 32,0
buffer_store_dword v72, v81, s[36:39], s52 offen sc1
D 83,0
R 36,1024
D 83,1024
R 40,2048
D 83,2048
s_waitcnt lgkmcnt(1)
v_mfma_f32_16x16x32_bf16 v[0:3], a[0:3], v[32:35], 0
R 44,3072
v_mfma_f32_16x16x32_bf16 v[4:7], a[4:7], v[32:35], 0
v_mfma_f32_16x16x32_bf16 v[8:11], a[8:11], v[32:35], 0
D 83,3072
v_mfma_f32_16x16x32_bf16 v[12:15], a[12:15], v[32:35], 0
s_add_u32 m0, s50, 36864
mav 0,16,36
R 48,4096
mav 4,20,36
mav 8,24,36
D 84,0
mav 12,28,36
s_waitcnt lgkmcnt(1)
mav 0,32,40
R 52,5120
mav 4,36,40
mav 8,40,40
D 84,1024
mav 12,44,40
mav 0,48,44
R 56,6144
mav 4,52,44
mav 8,56,44
mav 12,60,44
s_waitcnt lgkmcnt(1)
mav 0,64,48
R 60,7168
mav 4,68,48
mav 8,72,48
mav 12,76,48
mav 0,80,52
R 32,8192
mav 4,84,52
mav 8,88,52
mav 12,92,52
s_waitcnt lgkmcnt(1)
mav 0,96,56
R 36,9216
mav 4,100,56
mav 8,104,56
mav 12,108,56
mav 0,112,60
R 40,10240
mav 4,116,60
mav 8,120,60
mav 12,124,60
s_waitcnt lgkmcnt(1)
mav 0,128,32
R 44,11264
mav 4,132,32
mav 8,136,32
mav 12,140,32
mav 0,144,36
R 48,12288
mav 4,148,36
mav 8,152,36
mav 12,156,36
s_waitcnt lgkmcnt(1)
mav 0,160,40
R 52,13312
mav 4,164,40
mav 8,168,40
mav 12,172,40
mav 0,176,44
R 56,14336
mav 4,180,44
mav 8,184,44
mav 12,188,44
s_waitcnt lgkmcnt(1)
mav 0,192,48
R 60,15360
mav 4,196,48
mav 8,200,48
mav 12,204,48
mav 0,208,52
R 32,16384
mav 4,212,52
mav 8,216,52
mav 12,220,52
s_waitcnt lgkmcnt(1)
mav 0,224,56
R 36,17408
mav 4,228,56
mav 8,232,56
mav 12,236,56
mav 0,240,60
R 40,18432
mav 4,244,60
mav 8,248,60
mav 12,252,60
s_waitcnt lgkmcnt(1)
mvv 0,128,32
R 44,19456
mvv 4,132,32
mvv 8,136,32
mvv 12,140,32
mvv 0,144,36
R 48,20480
mvv 4,148,36
mvv 8,152,36
mvv 12,156,36
s_waitcnt lgkmcnt(1)
mvv 0,160,40
R 52,21504
mvv 4,164,40
mvv 8,168,40
mvv 12,172,40
mvv 0,176,44
R 56,22528
mvv 4,180,44
mvv 8,184,44
s_xor_b32 s50, s50, 0x10000
mvv 12,188,44
s_waitcnt lgkmcnt(1)
mvv 0,192,48
R 60,23552
mvv 4,196,48
mvv 8,200,48
s_waitcnt vmcnt(0)
s_add_u32 s49, s49, 313344
mvv 12,204,48
s_barrier
mvv 0,208,52
s_add_u32 m0, s50, 0
R 32,32768
mvv 4,212,52
mvv 8,216,52
D 83,0
mvv 12,220,52
s_waitcnt lgkmcnt(1)
mvv 0,224,56
R 36,33792
mvv 4,228,56
mvv 8,232,56
D 83,1024
mvv 12,236,56
mvv 0,240,60
R 40,34816
mvv 4,244,60
mvv 8,248,60
D 83,2048
mvv 12,252,60
s_waitcnt lgkmcnt(1)
v_mfma_f32_16x16x32_bf16 v[16:19], a[0:3], v[32:35], 0
R 44,35840
v_cvt_pk_bf16_f32 v64, v0, v1
v_mfma_f32_16x16x32_bf16 v[20:23], a[4:7], v[32:35], 0
v_cvt_pk_bf16_f32 v65, v2, v3
v_cvt_pk_bf16_f32 v66, v4, v5
v_mfma_f32_16x16x32_bf16 v[24:27], a[8:11], v[32:35], 0
D 83,3072
v_mfma_f32_16x16x32_bf16 v[28:31], a[12:15], v[32:35], 0
s_add_u32 m0, s50, 4096
v_cvt_pk_bf16_f32 v67, v6, v7
v_cvt_pk_bf16_f32 v68, v8, v9
mav 16,16,36
R 48,36864
v_cvt_pk_bf16_f32 v69, v10, v11
mav 20,20,36
v_cvt_pk_bf16_f32 v70, v12, v13
v_cvt_pk_bf16_f32 v71, v14, v15
mav 24,24,36
buffer_store_dwordx4 v[64:67], v87, s[36:39], s49 offen sc1
D 84,0
mav 28,28,36
s_waitcnt lgkmcnt(1)
mav 16,32,40
R 52,37888
mav 20,36,40
mav 24,40,40
buffer_store_dwordx4 v[68:71], v87, s[36:39], s49 offen offset:1024 sc1
D 84,1024
mav 28,44,40
mav 16,48,44
R 56,38912
mav 20,52,44
mav 24,56,44
mav 28,60,44
s_waitcnt lgkmcnt(1)
mav 16,64,48
R 60,39936
mav 20,68,48
mav 24,72,48
mav 28,76,48
mav 16,80,52
R 32,40960
mav 20,84,52
mav 24,88,52
mav 28,92,52
s_waitcnt lgkmcnt(1)
mav 16,96,56
R 36,41984
mav 20,100,56
mav 24,104,56
mav 28,108,56
mav 16,112,60
R 40,43008
mav 20,116,60
mav 24,120,60
mav 28,124,60
s_waitcnt lgkmcnt(1)
mav 16,128,32
R 44,44032
mav 20,132,32
mav 24,136,32
mav 28,140,32
mav 16,144,36
R 48,45056
v_add_u32 v72, 1, v72
mav 20,148,36
mav 24,152,36
mav 28,156,36
s_waitcnt lgkmcnt(1)
mav 16,160,40
R 52,46080
mav 20,164,40
mav 24,168,40
mav 28,172,40
mav 16,176,44
R 56,47104
mav 20,180,44
mav 24,184,44
mav 28,188,44
s_waitcnt lgkmcnt(1)
mav 16,192,48
R 60,48128
mav 20,196,48
mav 24,200,48
s_mov_b32 s60, -30
.p2align 6
LG2_1:
LsG8:
s_cbranch_execz LsG12
mav 28,204,48
mav 16,208,52
R 32,49152
mav 20,212,52
mav 24,216,52
mav 28,220,52
s_waitcnt lgkmcnt(1)
mav 16,224,56
R 36,50176
mav 20,228,56
mav 24,232,56
mav 28,236,56
mav 16,240,60
s_waitcnt vmcnt(1)
R 40,51200
mav 20,244,60
mav 24,248,60
buffer_store_dword v72, v81, s[36:39], s52 offen sc1
mav 28,252,60
s_waitcnt lgkmcnt(1)
mvv 16,128,32
R 44,52224
mvv 20,132,32
mvv 24,136,32
mvv 28,140,32
mvv 16,144,36
R 48,53248
mvv 20,148,36
mvv 24,152,36
mvv 28,156,36
s_waitcnt lgkmcnt(1)
mvv 16,160,40
R 52,54272
mvv 20,164,40
mvv 24,168,40
mvv 28,172,40
mvv 16,176,44
R 56,55296
mvv 20,180,44
mvv 24,184,44
mvv 28,188,44
s_waitcnt lgkmcnt(1)
mvv 16,192,48
R 60,56320
mvv 20,196,48
v_xor_b32 v80, 0x10000, v80
mvv 24,200,48
s_waitcnt vmcnt(1)
s_add_u32 s49, s49, 313344
mvv 28,204,48
s_barrier
mvv 16,208,52
s_add_u32 m0, s50, 32768
R 32,0
mvv 20,212,52
mvv 24,216,52
D 83,0
mvv 28,220,52
s_waitcnt lgkmcnt(1)
mvv 16,224,56
R 36,1024
mvv 20,228,56
mvv 24,232,56
D 83,1024
mvv 28,236,56
mvv 16,240,60
R 40,2048
mvv 20,244,60
mvv 24,248,60
D 83,2048
mvv 28,252,60
s_waitcnt lgkmcnt(1)
v_mfma_f32_16x16x32_bf16 v[0:3], a[0:3], v[32:35], 0
R 44,3072
v_cvt_pk_bf16_f32 v64, v16, v17
v_mfma_f32_16x16x32_bf16 v[4:7], a[4:7], v[32:35], 0
v_cvt_pk_bf16_f32 v65, v18, v19
v_cvt_pk_bf16_f32 v66, v20, v21
v_mfma_f32_16x16x32_bf16 v[8:11], a[8:11], v[32:35], 0
D 83,3072
v_mfma_f32_16x16x32_bf16 v[12:15], a[12:15], v[32:35], 0
s_add_u32 m0, s50, 36864
v_cvt_pk_bf16_f32 v67, v22, v23
v_cvt_pk_bf16_f32 v68, v24, v25
mav 0,16,36
R 48,4096
v_cvt_pk_bf16_f32 v69, v26, v27
mav 4,20,36
v_cvt_pk_bf16_f32 v70, v28, v29
v_cvt_pk_bf16_f32 v71, v30, v31
mav 8,24,36
buffer_store_dwordx4 v[64:67], v87, s[36:39], s49 offen sc1
D 84,0
mav 12,28,36
s_waitcnt lgkmcnt(1)
mav 0,32,40
R 52,5120
mav 4,36,40
mav 8,40,40
buffer_store_dwordx4 v[68:71], v87, s[36:39], s49 offen offset:1024 sc1
D 84,1024
mav 12,44,40
mav 0,48,44
R 56,6144
mav 4,52,44
mav 8,56,44
mav 12,60,44
s_waitcnt lgkmcnt(1)
mav 0,64,48
R 60,7168
mav 4,68,48
mav 8,72,48
mav 12,76,48
mav 0,80,52
R 32,8192
mav 4,84,52
mav 8,88,52
mav 12,92,52
s_waitcnt lgkmcnt(1)
mav 0,96,56
R 36,9216
mav 4,100,56
mav 8,104,56
mav 12,108,56
mav 0,112,60
R 40,10240
mav 4,116,60
mav 8,120,60
mav 12,124,60
s_waitcnt lgkmcnt(1)
mav 0,128,32
R 44,11264
mav 4,132,32
mav 8,136,32
mav 12,140,32
mav 0,144,36
R 48,12288
v_add_u32 v72, 1, v72
mav 4,148,36
mav 8,152,36
mav 12,156,36
s_waitcnt lgkmcnt(1)
mav 0,160,40
R 52,13312
mav 4,164,40
mav 8,168,40
mav 12,172,40
mav 0,176,44
R 56,14336
mav 4,180,44
mav 8,184,44
mav 12,188,44
s_waitcnt lgkmcnt(1)
mav 0,192,48
R 60,15360
mav 4,196,48
mav 8,200,48
mav 12,204,48
mav 0,208,52
R 32,16384
mav 4,212,52
mav 8,216,52
mav 12,220,52
s_waitcnt lgkmcnt(1)
mav 0,224,56
R 36,17408
mav 4,228,56
mav 8,232,56
mav 12,236,56
mav 0,240,60
s_waitcnt vmcnt(1)
R 40,18432
mav 4,244,60
mav 8,248,60
buffer_store_dword v72, v81, s[36:39], s52 offen sc1
mav 12,252,60
s_waitcnt lgkmcnt(1)
mvv 0,128,32
R 44,19456
mvv 4,132,32
mvv 8,136,32
mvv 12,140,32
mvv 0,144,36
R 48,20480
mvv 4,148,36
mvv 8,152,36
mvv 12,156,36
s_waitcnt lgkmcnt(1)
mvv 0,160,40
R 52,21504
mvv 4,164,40
mvv 8,168,40
mvv 12,172,40
mvv 0,176,44
R 56,22528
mvv 4,180,44
mvv 8,184,44
s_xor_b32 s50, s50, 0x10000
mvv 12,188,44
s_waitcnt lgkmcnt(1)
mvv 0,192,48
R 60,23552
mvv 4,196,48
mvv 8,200,48
s_waitcnt vmcnt(1)
s_add_u32 s49, s49, 313344
mvv 12,204,48
s_barrier
mvv 0,208,52
s_add_u32 m0, s50, 0
R 32,32768
mvv 4,212,52
mvv 8,216,52
D 83,0
mvv 12,220,52
s_waitcnt lgkmcnt(1)
mvv 0,224,56
R 36,33792
mvv 4,228,56
mvv 8,232,56
D 83,1024
mvv 12,236,56
mvv 0,240,60
R 40,34816
mvv 4,244,60
mvv 8,248,60
D 83,2048
mvv 12,252,60
s_waitcnt lgkmcnt(1)
v_mfma_f32_16x16x32_bf16 v[16:19], a[0:3], v[32:35], 0
R 44,35840
v_cvt_pk_bf16_f32 v64, v0, v1
v_mfma_f32_16x16x32_bf16 v[20:23], a[4:7], v[32:35], 0
v_cvt_pk_bf16_f32 v65, v2, v3
v_cvt_pk_bf16_f32 v66, v4, v5
v_mfma_f32_16x16x32_bf16 v[24:27], a[8:11], v[32:35], 0
D 83,3072
v_mfma_f32_16x16x32_bf16 v[28:31], a[12:15], v[32:35], 0
s_add_u32 m0, s50, 4096
v_cvt_pk_bf16_f32 v67, v6, v7
v_cvt_pk_bf16_f32 v68, v8, v9
mav 16,16,36
R 48,36864
v_cvt_pk_bf16_f32 v69, v10, v11
mav 20,20,36
v_cvt_pk_bf16_f32 v70, v12, v13
v_cvt_pk_bf16_f32 v71, v14, v15
mav 24,24,36
buffer_store_dwordx4 v[64:67], v87, s[36:39], s49 offen sc1
D 84,0
mav 28,28,36
s_waitcnt lgkmcnt(1)
mav 16,32,40
R 52,37888
mav 20,36,40
mav 24,40,40
buffer_store_dwordx4 v[68:71], v87, s[36:39], s49 offen offset:1024 sc1
D 84,1024
mav 28,44,40
mav 16,48,44
R 56,38912
mav 20,52,44
mav 24,56,44
mav 28,60,44
s_waitcnt lgkmcnt(1)
mav 16,64,48
R 60,39936
mav 20,68,48
mav 24,72,48
mav 28,76,48
mav 16,80,52
R 32,40960
mav 20,84,52
mav 24,88,52
mav 28,92,52
s_waitcnt lgkmcnt(1)
mav 16,96,56
R 36,41984
mav 20,100,56
mav 24,104,56
mav 28,108,56
mav 16,112,60
R 40,43008
mav 20,116,60
mav 24,120,60
mav 28,124,60
s_waitcnt lgkmcnt(1)
mav 16,128,32
R 44,44032
mav 20,132,32
mav 24,136,32
mav 28,140,32
mav 16,144,36
R 48,45056
v_add_u32 v72, 1, v72
mav 20,148,36
mav 24,152,36
mav 28,156,36
s_waitcnt lgkmcnt(1)
mav 16,160,40
R 52,46080
mav 20,164,40
mav 24,168,40
mav 28,172,40
mav 16,176,44
R 56,47104
mav 20,180,44
mav 24,184,44
mav 28,188,44
s_waitcnt lgkmcnt(1)
mav 16,192,48
R 60,48128
mav 20,196,48
s_add_u32 s60, s60, 1
mav 24,200,48
s_cbranch_scc0 LG2_1
mav 28,204,48
mav 16,208,52
R 32,49152
mav 20,212,52
mav 24,216,52
mav 28,220,52
s_waitcnt lgkmcnt(1)
mav 16,224,56
R 36,50176
mav 20,228,56
mav 24,232,56
mav 28,236,56
mav 16,240,60
s_waitcnt vmcnt(1)
R 40,51200
mav 20,244,60
mav 24,248,60
buffer_store_dword v72, v81, s[36:39], s52 offen sc1
mav 28,252,60
s_waitcnt lgkmcnt(1)
mvv 16,128,32
R 44,52224
mvv 20,132,32
mvv 24,136,32
mvv 28,140,32
mvv 16,144,36
R 48,53248
mvv 20,148,36
mvv 24,152,36
mvv 28,156,36
s_waitcnt lgkmcnt(1)
mvv 16,160,40
R 52,54272
mvv 20,164,40
mvv 24,168,40
mvv 28,172,40
mvv 16,176,44
R 56,55296
mvv 20,180,44
mvv 24,184,44
mvv 28,188,44
s_waitcnt lgkmcnt(1)
mvv 16,192,48
R 60,56320
mvv 20,196,48
v_xor_b32 v80, 0x10000, v80
mvv 24,200,48
s_waitcnt vmcnt(1)
s_add_u32 s49, s49, 313344
mvv 28,204,48
s_barrier
mvv 16,208,52
s_add_u32 m0, s50, 32768
R 32,0
mvv 20,212,52
mvv 24,216,52
D 83,0
mvv 28,220,52
s_waitcnt lgkmcnt(1)
mvv 16,224,56
R 36,1024
mvv 20,228,56
mvv 24,232,56
D 83,1024
mvv 28,236,56
mvv 16,240,60
R 40,2048
mvv 20,244,60
mvv 24,248,60
D 83,2048
mvv 28,252,60
s_waitcnt lgkmcnt(1)
v_mfma_f32_16x16x32_bf16 v[0:3], a[0:3], v[32:35], 0
R 44,3072
v_cvt_pk_bf16_f32 v64, v16, v17
v_mfma_f32_16x16x32_bf16 v[4:7], a[4:7], v[32:35], 0
v_cvt_pk_bf16_f32 v65, v18, v19
v_cvt_pk_bf16_f32 v66, v20, v21
v_mfma_f32_16x16x32_bf16 v[8:11], a[8:11], v[32:35], 0
D 83,3072
v_mfma_f32_16x16x32_bf16 v[12:15], a[12:15], v[32:35], 0
s_add_u32 m0, s50, 36864
v_cvt_pk_bf16_f32 v67, v22, v23
v_cvt_pk_bf16_f32 v68, v24, v25
mav 0,16,36
R 48,4096
v_cvt_pk_bf16_f32 v69, v26, v27
mav 4,20,36
v_cvt_pk_bf16_f32 v70, v28, v29
v_cvt_pk_bf16_f32 v71, v30, v31
mav 8,24,36
buffer_store_dwordx4 v[64:67], v87, s[36:39], s49 offen sc1
D 84,0
mav 12,28,36
s_waitcnt lgkmcnt(1)
mav 0,32,40
R 52,5120
mav 4,36,40
mav 8,40,40
buffer_store_dwordx4 v[68:71], v87, s[36:39], s49 offen offset:1024 sc1
D 84,1024
mav 12,44,40
mav 0,48,44
R 56,6144
mav 4,52,44
mav 8,56,44
mav 12,60,44
s_waitcnt lgkmcnt(1)
mav 0,64,48
R 60,7168
mav 4,68,48
mav 8,72,48
mav 12,76,48
mav 0,80,52
R 32,8192
mav 4,84,52
mav 8,88,52
mav 12,92,52
s_waitcnt lgkmcnt(1)
mav 0,96,56
R 36,9216
mav 4,100,56
mav 8,104,56
mav 12,108,56
mav 0,112,60
R 40,10240
mav 4,116,60
mav 8,120,60
mav 12,124,60
s_waitcnt lgkmcnt(1)
mav 0,128,32
R 44,11264
mav 4,132,32
mav 8,136,32
mav 12,140,32
mav 0,144,36
R 48,12288
v_add_u32 v72, 1, v72
mav 4,148,36
mav 8,152,36
mav 12,156,36
s_waitcnt lgkmcnt(1)
mav 0,160,40
R 52,13312
mav 4,164,40
mav 8,168,40
mav 12,172,40
mav 0,176,44
R 56,14336
mav 4,180,44
mav 8,184,44
mav 12,188,44
s_waitcnt lgkmcnt(1)
mav 0,192,48
R 60,15360
mav 4,196,48
mav 8,200,48
mav 12,204,48
mav 0,208,52
R 32,16384
mav 4,212,52
mav 8,216,52
mav 12,220,52
s_waitcnt lgkmcnt(1)
mav 0,224,56
R 36,17408
mav 4,228,56
mav 8,232,56
mav 12,236,56
mav 0,240,60
s_waitcnt vmcnt(1)
R 40,18432
mav 4,244,60
mav 8,248,60
buffer_store_dword v72, v81, s[36:39], s52 offen sc1
mav 12,252,60
s_waitcnt lgkmcnt(1)
mvv 0,128,32
R 44,19456
mvv 4,132,32
mvv 8,136,32
mvv 12,140,32
mvv 0,144,36
R 48,20480
mvv 4,148,36
mvv 8,152,36
mvv 12,156,36
s_waitcnt lgkmcnt(1)
mvv 0,160,40
R 52,21504
mvv 4,164,40
mvv 8,168,40
mvv 12,172,40
mvv 0,176,44
R 56,22528
mvv 4,180,44
mvv 8,184,44
mvv 12,188,44
s_waitcnt lgkmcnt(1)
mvv 0,192,48
R 60,23552
mvv 4,196,48
mvv 8,200,48
s_waitcnt vmcnt(1)
s_add_u32 s49, s49, 313344
mvv 12,204,48
s_barrier
mvv 0,208,52
R 32,32768
mvv 4,212,52
mvv 8,216,52
mvv 12,220,52
s_waitcnt lgkmcnt(1)
mvv 0,224,56
R 36,33792
mvv 4,228,56
mvv 8,232,56
mvv 12,236,56
mvv 0,240,60
R 40,34816
mvv 4,244,60
mvv 8,248,60
mvv 12,252,60
s_waitcnt lgkmcnt(1)
v_mfma_f32_16x16x32_bf16 v[16:19], a[0:3], v[32:35], 0
R 44,35840
LsG9:
s_cbranch_execz LwG
v_mfma_f32_16x16x32_bf16 v[20:23], a[4:7], v[32:35], 0
v_cvt_pk_bf16_f32 v64, v0, v1
v_cvt_pk_bf16_f32 v65, v2, v3
v_mfma_f32_16x16x32_bf16 v[24:27], a[8:11], v[32:35], 0
v_cvt_pk_bf16_f32 v66, v4, v5
v_cvt_pk_bf16_f32 v67, v6, v7
v_mfma_f32_16x16x32_bf16 v[28:31], a[12:15], v[32:35], 0
v_cvt_pk_bf16_f32 v68, v8, v9
v_cvt_pk_bf16_f32 v69, v10, v11
mav 16,16,36
R 48,36864
v_cvt_pk_bf16_f32 v70, v12, v13
mav 20,20,36
v_cvt_pk_bf16_f32 v71, v14, v15
mav 24,24,36
buffer_store_dwordx4 v[64:67], v87, s[36:39], s49 offen sc1
mav 28,28,36
s_waitcnt lgkmcnt(1)
mav 16,32,40
R 52,37888
mav 20,36,40
mav 24,40,40
buffer_store_dwordx4 v[68:71], v87, s[36:39], s49 offen offset:1024 sc1
mav 28,44,40
mav 16,48,44
R 56,38912
mav 20,52,44
mav 24,56,44
mav 28,60,44
s_waitcnt lgkmcnt(1)
mav 16,64,48
R 60,39936
mav 20,68,48
mav 24,72,48
mav 28,76,48
mav 16,80,52
R 32,40960
mav 20,84,52
mav 24,88,52
mav 28,92,52
s_waitcnt lgkmcnt(1)
mav 16,96,56
R 36,41984
mav 20,100,56
mav 24,104,56
mav 28,108,56
mav 16,112,60
R 40,43008
mav 20,116,60
mav 24,120,60
mav 28,124,60
s_waitcnt lgkmcnt(1)
mav 16,128,32
R 44,44032
mav 20,132,32
mav 24,136,32
mav 28,140,32
mav 16,144,36
R 48,45056
v_add_u32 v72, 1, v72
mav 20,148,36
mav 24,152,36
mav 28,156,36
s_waitcnt lgkmcnt(1)
mav 16,160,40
R 52,46080
mav 20,164,40
mav 24,168,40
mav 28,172,40
mav 16,176,44
R 56,47104
mav 20,180,44
mav 24,184,44
mav 28,188,44
s_waitcnt lgkmcnt(1)
mav 16,192,48
R 60,48128
mav 20,196,48
mav 24,200,48
mav 28,204,48
mav 16,208,52
R 32,49152
mav 20,212,52
mav 24,216,52
mav 28,220,52
s_waitcnt lgkmcnt(1)
mav 16,224,56
R 36,50176
mav 20,228,56
mav 24,232,56
mav 28,236,56
mav 16,240,60
s_waitcnt vmcnt(0)
R 40,51200
mav 20,244,60
mav 24,248,60
buffer_store_dword v72, v81, s[36:39], s52 offen sc1
mav 28,252,60
s_waitcnt lgkmcnt(1)
mvv 16,128,32
R 44,52224
mvv 20,132,32
mvv 24,136,32
mvv 28,140,32
mvv 16,144,36
R 48,53248
mvv 20,148,36
mvv 24,152,36
mvv 28,156,36
s_waitcnt lgkmcnt(1)
mvv 16,160,40
R 52,54272
mvv 20,164,40
mvv 24,168,40
mvv 28,172,40
mvv 16,176,44
R 56,55296
mvv 20,180,44
mvv 24,184,44
mvv 28,188,44
s_waitcnt lgkmcnt(1)
mvv 16,192,48
R 60,56320
mvv 20,196,48
mvv 24,200,48
s_add_u32 s49, s49, 313344
mvv 28,204,48
mvv 16,208,52
mvv 20,212,52
mvv 24,216,52
mvv 28,220,52
s_waitcnt lgkmcnt(0)
mvv 16,224,56
mvv 20,228,56
mvv 24,232,56
mvv 28,236,56
mvv 16,240,60
mvv 20,244,60
mvv 24,248,60
mvv 28,252,60
s_nop 4
v_cvt_pk_bf16_f32 v64, v16, v17
v_cvt_pk_bf16_f32 v65, v18, v19
v_cvt_pk_bf16_f32 v66, v20, v21
v_cvt_pk_bf16_f32 v67, v22, v23
buffer_store_dwordx4 v[64:67], v87, s[36:39], s49 offen sc1
v_cvt_pk_bf16_f32 v68, v24, v25
v_cvt_pk_bf16_f32 v69, v26, v27
v_cvt_pk_bf16_f32 v70, v28, v29
v_cvt_pk_bf16_f32 v71, v30, v31
buffer_store_dwordx4 v[68:71], v87, s[36:39], s49 offen offset:1024 sc1
v_add_u32 v72, 35, v72
s_waitcnt vmcnt(0)
buffer_store_dword v72, v81, s[36:39], s52 offen sc1
s_endpgm
LvG3:
LsG10:
s_cbranch_execz LwG
v_add_u32 v72, 1, v72
s_add_u32 s49, s49, 313344
s_barrier
s_add_u32 m0, s50, 0
buffer_store_dword v72, v81, s[36:39], s52 offen sc1
D 83,0
D 83,1024
D 83,2048
D 83,3072
s_add_u32 m0, s50, 4096
s_nop 0
D 84,0
D 84,1024
v_add_u32 v72, 1, v72
s_waitcnt vmcnt(0)
s_add_u32 s49, s49, 313344
s_barrier
s_add_u32 m0, s50, 32768
R 32,0
buffer_store_dword v72, v81, s[36:39], s52 offen sc1
D 83,0
R 36,1024
D 83,1024
R 40,2048
D 83,2048
s_waitcnt lgkmcnt(1)
v_mfma_f32_16x16x32_bf16 v[0:3], a[0:3], v[32:35], 0
R 44,3072
v_mfma_f32_16x16x32_bf16 v[4:7], a[4:7], v[32:35], 0
v_mfma_f32_16x16x32_bf16 v[8:11], a[8:11], v[32:35], 0
v_mfma_f32_16x16x32_bf16 v[12:15], a[12:15], v[32:35], 0
D 83,3072
mav 0,16,36
s_add_u32 m0, s50, 36864
R 48,4096
mav 4,20,36
mav 8,24,36
mav 12,28,36
s_waitcnt lgkmcnt(1)
D 84,0
mav 0,32,40
R 52,5120
mav 4,36,40
mav 8,40,40
mav 12,44,40
D 84,1024
mav 0,48,44
R 56,6144
mav 4,52,44
mav 8,56,44
mav 12,60,44
s_waitcnt lgkmcnt(1)
mav 0,64,48
R 60,7168
mav 4,68,48
mav 8,72,48
mav 12,76,48
mav 0,80,52
R 32,8192
mav 4,84,52
mav 8,88,52
mav 12,92,52
s_waitcnt lgkmcnt(1)
mav 0,96,56
R 36,9216
mav 4,100,56
mav 8,104,56
mav 12,108,56
mav 0,112,60
R 40,10240
mav 4,116,60
mav 8,120,60
mav 12,124,60
s_waitcnt lgkmcnt(1)
mav 0,128,32
R 44,11264
mav 4,132,32
mav 8,136,32
mav 12,140,32
mav 0,144,36
R 48,12288
mav 4,148,36
mav 8,152,36
mav 12,156,36
s_waitcnt lgkmcnt(1)
mav 0,160,40
R 52,13312
mav 4,164,40
mav 8,168,40
mav 12,172,40
mav 0,176,44
R 56,14336
mav 4,180,44
mav 8,184,44
mav 12,188,44
s_waitcnt lgkmcnt(1)
mav 0,192,48
R 60,15360
mav 4,196,48
mav 8,200,48
mav 12,204,48
mav 0,208,52
R 32,16384
mav 4,212,52
mav 8,216,52
mav 12,220,52
s_waitcnt lgkmcnt(1)
mav 0,224,56
R 36,17408
mav 4,228,56
mav 8,232,56
mav 12,236,56
mav 0,240,60
R 40,18432
mav 4,244,60
mav 8,248,60
mav 12,252,60
s_waitcnt lgkmcnt(1)
mvv 0,128,32
R 44,19456
mvv 4,132,32
mvv 8,136,32
mvv 12,140,32
mvv 0,144,36
R 48,20480
mvv 4,148,36
mvv 8,152,36
mvv 12,156,36
s_waitcnt lgkmcnt(1)
mvv 0,160,40
R 52,21504
mvv 4,164,40
mvv 8,168,40
mvv 12,172,40
mvv 0,176,44
R 56,22528
mvv 4,180,44
mvv 8,184,44
s_xor_b32 s50, s50, 0x10000
mvv 12,188,44
s_waitcnt lgkmcnt(1)
mvv 0,192,48
R 60,23552
mvv 4,196,48
mvv 8,200,48
s_waitcnt vmcnt(0)
s_add_u32 s49, s49, 313344
mvv 12,204,48
s_barrier
mvv 0,208,52
s_add_u32 m0, s50, 0
R 32,32768
mvv 4,212,52
mvv 8,216,52
mvv 12,220,52
s_waitcnt lgkmcnt(1)
D 83,0
mvv 0,224,56
R 36,33792
mvv 4,228,56
mvv 8,232,56
mvv 12,236,56
D 83,1024
mvv 0,240,60
R 40,34816
mvv 4,244,60
mvv 8,248,60
mvv 12,252,60
s_waitcnt lgkmcnt(1)
D 83,2048
v_mfma_f32_16x16x32_bf16 v[16:19], a[0:3], v[32:35], 0
R 44,35840
v_cvt_pk_bf16_f32 v64, v0, v1
v_mfma_f32_16x16x32_bf16 v[20:23], a[4:7], v[32:35], 0
v_cvt_pk_bf16_f32 v65, v2, v3
v_cvt_pk_bf16_f32 v66, v4, v5
v_mfma_f32_16x16x32_bf16 v[24:27], a[8:11], v[32:35], 0
v_cvt_pk_bf16_f32 v67, v6, v7
v_cvt_pk_bf16_f32 v68, v8, v9
v_mfma_f32_16x16x32_bf16 v[28:31], a[12:15], v[32:35], 0
buffer_store_dwordx4 v[64:67], v87, s[36:39], s49 offen sc1
D 83,3072
mav 16,16,36
s_add_u32 m0, s50, 4096
R 48,36864
v_cvt_pk_bf16_f32 v69, v10, v11
mav 20,20,36
v_cvt_pk_bf16_f32 v70, v12, v13
v_cvt_pk_bf16_f32 v71, v14, v15
mav 24,24,36
mav 28,28,36
buffer_store_dwordx4 v[68:71], v87, s[36:39], s49 offen offset:1024 sc1
s_waitcnt lgkmcnt(1)
mav 16,32,40
R 52,37888
mav 20,36,40
mav 24,40,40
mav 28,44,40
D 84,0
mav 16,48,44
R 56,38912
mav 20,52,44
mav 24,56,44
mav 28,60,44
s_waitcnt lgkmcnt(1)
D 84,1024
mav 16,64,48
R 60,39936
mav 20,68,48
mav 24,72,48
mav 28,76,48
mav 16,80,52
R 32,40960
mav 20,84,52
mav 24,88,52
mav 28,92,52
s_waitcnt lgkmcnt(1)
mav 16,96,56
R 36,41984
mav 20,100,56
mav 24,104,56
mav 28,108,56
mav 16,112,60
R 40,43008
mav 20,116,60
mav 24,120,60
mav 28,124,60
s_waitcnt lgkmcnt(1)
mav 16,128,32
R 44,44032
mav 20,132,32
mav 24,136,32
mav 28,140,32
mav 16,144,36
R 48,45056
v_add_u32 v72, 1, v72
mav 20,148,36
mav 24,152,36
mav 28,156,36
s_waitcnt lgkmcnt(1)
mav 16,160,40
R 52,46080
mav 20,164,40
mav 24,168,40
mav 28,172,40
mav 16,176,44
R 56,47104
mav 20,180,44
mav 24,184,44
mav 28,188,44
s_waitcnt lgkmcnt(1)
mav 16,192,48
R 60,48128
mav 20,196,48
mav 24,200,48
s_mov_b32 s60, -30
.p2align 6
LG3_1:
mav 28,204,48
mav 16,208,52
R 32,49152
mav 20,212,52
mav 24,216,52
mav 28,220,52
s_waitcnt lgkmcnt(1)
mav 16,224,56
R 36,50176
mav 20,228,56
mav 24,232,56
mav 28,236,56
mav 16,240,60
s_waitcnt vmcnt(2)
R 40,51200
mav 20,244,60
mav 24,248,60
mav 28,252,60
s_waitcnt lgkmcnt(1)
buffer_store_dword v72, v81, s[36:39], s52 offen sc1
mvv 16,128,32
R 44,52224
mvv 20,132,32
mvv 24,136,32
mvv 28,140,32
mvv 16,144,36
R 48,53248
mvv 20,148,36
mvv 24,152,36
mvv 28,156,36
s_waitcnt lgkmcnt(1)
mvv 16,160,40
R 52,54272
mvv 20,164,40
mvv 24,168,40
mvv 28,172,40
mvv 16,176,44
R 56,55296
mvv 20,180,44
mvv 24,184,44
mvv 28,188,44
s_waitcnt lgkmcnt(1)
mvv 16,192,48
R 60,56320
mvv 20,196,48
v_xor_b32 v80, 0x10000, v80
mvv 24,200,48
s_waitcnt vmcnt(1)
s_add_u32 s49, s49, 313344
mvv 28,204,48
s_barrier
mvv 16,208,52
s_add_u32 m0, s50, 32768
R 32,0
mvv 20,212,52
mvv 24,216,52
mvv 28,220,52
s_waitcnt lgkmcnt(1)
D 83,0
mvv 16,224,56
R 36,1024
mvv 20,228,56
mvv 24,232,56
mvv 28,236,56
D 83,1024
mvv 16,240,60
R 40,2048
mvv 20,244,60
mvv 24,248,60
mvv 28,252,60
s_waitcnt lgkmcnt(1)
D 83,2048
v_mfma_f32_16x16x32_bf16 v[0:3], a[0:3], v[32:35], 0
R 44,3072
v_cvt_pk_bf16_f32 v64, v16, v17
v_mfma_f32_16x16x32_bf16 v[4:7], a[4:7], v[32:35], 0
v_cvt_pk_bf16_f32 v65, v18, v19
v_cvt_pk_bf16_f32 v66, v20, v21
v_mfma_f32_16x16x32_bf16 v[8:11], a[8:11], v[32:35], 0
v_cvt_pk_bf16_f32 v67, v22, v23
v_cvt_pk_bf16_f32 v68, v24, v25
v_mfma_f32_16x16x32_bf16 v[12:15], a[12:15], v[32:35], 0
buffer_store_dwordx4 v[64:67], v87, s[36:39], s49 offen sc1
D 83,3072
mav 0,16,36
s_add_u32 m0, s50, 36864
R 48,4096
v_cvt_pk_bf16_f32 v69, v26, v27
mav 4,20,36
v_cvt_pk_bf16_f32 v70, v28, v29
v_cvt_pk_bf16_f32 v71, v30, v31
mav 8,24,36
mav 12,28,36
buffer_store_dwordx4 v[68:71], v87, s[36:39], s49 offen offset:1024 sc1
s_waitcnt lgkmcnt(1)
mav 0,32,40
R 52,5120
mav 4,36,40
mav 8,40,40
mav 12,44,40
D 84,0
mav 0,48,44
R 56,6144
mav 4,52,44
mav 8,56,44
mav 12,60,44
s_waitcnt lgkmcnt(1)
D 84,1024
mav 0,64,48
R 60,7168
mav 4,68,48
mav 8,72,48
mav 12,76,48
mav 0,80,52
R 32,8192
mav 4,84,52
mav 8,88,52
mav 12,92,52
s_waitcnt lgkmcnt(1)
mav 0,96,56
R 36,9216
mav 4,100,56
mav 8,104,56
mav 12,108,56
mav 0,112,60
R 40,10240
LsG11:
s_cbranch_execz LwG
mav 4,116,60
mav 8,120,60
mav 12,124,60
s_waitcnt lgkmcnt(1)
mav 0,128,32
R 44,11264
mav 4,132,32
mav 8,136,32
mav 12,140,32
mav 0,144,36
R 48,12288
v_add_u32 v72, 1, v72
mav 4,148,36
mav 8,152,36
mav 12,156,36
s_waitcnt lgkmcnt(1)
mav 0,160,40
R 52,13312
mav 4,164,40
mav 8,168,40
mav 12,172,40
mav 0,176,44
R 56,14336
mav 4,180,44
mav 8,184,44
mav 12,188,44
s_waitcnt lgkmcnt(1)
mav 0,192,48
R 60,15360
mav 4,196,48
mav 8,200,48
mav 12,204,48
mav 0,208,52
R 32,16384
mav 4,212,52
mav 8,216,52
mav 12,220,52
s_waitcnt lgkmcnt(1)
mav 0,224,56
R 36,17408
mav 4,228,56
mav 8,232,56
mav 12,236,56
mav 0,240,60
s_waitcnt vmcnt(2)
R 40,18432
mav 4,244,60
mav 8,248,60
mav 12,252,60
s_waitcnt lgkmcnt(1)
buffer_store_dword v72, v81, s[36:39], s52 offen sc1
mvv 0,128,32
R 44,19456
mvv 4,132,32
mvv 8,136,32
mvv 12,140,32
mvv 0,144,36
R 48,20480
mvv 4,148,36
mvv 8,152,36
mvv 12,156,36
s_waitcnt lgkmcnt(1)
mvv 0,160,40
R 52,21504
mvv 4,164,40
mvv 8,168,40
mvv 12,172,40
mvv 0,176,44
R 56,22528
mvv 4,180,44
mvv 8,184,44
s_xor_b32 s50, s50, 0x10000
mvv 12,188,44
s_waitcnt lgkmcnt(1)
mvv 0,192,48
R 60,23552
mvv 4,196,48
mvv 8,200,48
s_waitcnt vmcnt(1)
s_add_u32 s49, s49, 313344
mvv 12,204,48
s_barrier
mvv 0,208,52
s_add_u32 m0, s50, 0
R 32,32768
mvv 4,212,52
mvv 8,216,52
mvv 12,220,52
s_waitcnt lgkmcnt(1)
D 83,0
mvv 0,224,56
R 36,33792
mvv 4,228,56
mvv 8,232,56
mvv 12,236,56
D 83,1024
mvv 0,240,60
R 40,34816
mvv 4,244,60
mvv 8,248,60
mvv 12,252,60
s_waitcnt lgkmcnt(1)
D 83,2048
v_mfma_f32_16x16x32_bf16 v[16:19], a[0:3], v[32:35], 0
R 44,35840
v_cvt_pk_bf16_f32 v64, v0, v1
v_mfma_f32_16x16x32_bf16 v[20:23], a[4:7], v[32:35], 0
v_cvt_pk_bf16_f32 v65, v2, v3
v_cvt_pk_bf16_f32 v66, v4, v5
v_mfma_f32_16x16x32_bf16 v[24:27], a[8:11], v[32:35], 0
v_cvt_pk_bf16_f32 v67, v6, v7
v_cvt_pk_bf16_f32 v68, v8, v9
v_mfma_f32_16x16x32_bf16 v[28:31], a[12:15], v[32:35], 0
buffer_store_dwordx4 v[64:67], v87, s[36:39], s49 offen sc1
D 83,3072
mav 16,16,36
s_add_u32 m0, s50, 4096
R 48,36864
v_cvt_pk_bf16_f32 v69, v10, v11
mav 20,20,36
v_cvt_pk_bf16_f32 v70, v12, v13
v_cvt_pk_bf16_f32 v71, v14, v15
mav 24,24,36
mav 28,28,36
buffer_store_dwordx4 v[68:71], v87, s[36:39], s49 offen offset:1024 sc1
s_waitcnt lgkmcnt(1)
mav 16,32,40
R 52,37888
)"
R"(
mav 20,36,40
mav 24,40,40
mav 28,44,40
D 84,0
mav 16,48,44
R 56,38912
mav 20,52,44
mav 24,56,44
mav 28,60,44
s_waitcnt lgkmcnt(1)
D 84,1024
mav 16,64,48
R 60,39936
mav 20,68,48
mav 24,72,48
mav 28,76,48
mav 16,80,52
R 32,40960
mav 20,84,52
mav 24,88,52
mav 28,92,52
s_waitcnt lgkmcnt(1)
mav 16,96,56
R 36,41984
mav 20,100,56
mav 24,104,56
mav 28,108,56
mav 16,112,60
R 40,43008
mav 20,116,60
mav 24,120,60
mav 28,124,60
s_waitcnt lgkmcnt(1)
mav 16,128,32
R 44,44032
mav 20,132,32
mav 24,136,32
mav 28,140,32
mav 16,144,36
R 48,45056
v_add_u32 v72, 1, v72
mav 20,148,36
mav 24,152,36
mav 28,156,36
s_waitcnt lgkmcnt(1)
mav 16,160,40
R 52,46080
mav 20,164,40
mav 24,168,40
mav 28,172,40
mav 16,176,44
R 56,47104
mav 20,180,44
mav 24,184,44
mav 28,188,44
s_waitcnt lgkmcnt(1)
mav 16,192,48
R 60,48128
mav 20,196,48
s_add_u32 s60, s60, 1
mav 24,200,48
s_cbranch_scc0 LG3_1
mav 28,204,48
mav 16,208,52
R 32,49152
mav 20,212,52
mav 24,216,52
mav 28,220,52
s_waitcnt lgkmcnt(1)
mav 16,224,56
R 36,50176
mav 20,228,56
mav 24,232,56
mav 28,236,56
mav 16,240,60
s_waitcnt vmcnt(2)
R 40,51200
mav 20,244,60
mav 24,248,60
mav 28,252,60
s_waitcnt lgkmcnt(1)
buffer_store_dword v72, v81, s[36:39], s52 offen sc1
mvv 16,128,32
R 44,52224
mvv 20,132,32
mvv 24,136,32
mvv 28,140,32
mvv 16,144,36
R 48,53248
mvv 20,148,36
mvv 24,152,36
mvv 28,156,36
s_waitcnt lgkmcnt(1)
mvv 16,160,40
R 52,54272
mvv 20,164,40
mvv 24,168,40
mvv 28,172,40
mvv 16,176,44
R 56,55296
mvv 20,180,44
mvv 24,184,44
mvv 28,188,44
s_waitcnt lgkmcnt(1)
mvv 16,192,48
R 60,56320
mvv 20,196,48
v_xor_b32 v80, 0x10000, v80
mvv 24,200,48
s_waitcnt vmcnt(1)
s_add_u32 s49, s49, 313344
mvv 28,204,48
s_barrier
mvv 16,208,52
s_add_u32 m0, s50, 32768
R 32,0
mvv 20,212,52
mvv 24,216,52
mvv 28,220,52
s_waitcnt lgkmcnt(1)
D 83,0
mvv 16,224,56
R 36,1024
mvv 20,228,56
mvv 24,232,56
mvv 28,236,56
D 83,1024
mvv 16,240,60
R 40,2048
mvv 20,244,60
mvv 24,248,60
mvv 28,252,60
s_waitcnt lgkmcnt(1)
D 83,2048
v_mfma_f32_16x16x32_bf16 v[0:3], a[0:3], v[32:35], 0
R 44,3072
v_cvt_pk_bf16_f32 v64, v16, v17
v_mfma_f32_16x16x32_bf16 v[4:7], a[4:7], v[32:35], 0
v_cvt_pk_bf16_f32 v65, v18, v19
v_cvt_pk_bf16_f32 v66, v20, v21
v_mfma_f32_16x16x32_bf16 v[8:11], a[8:11], v[32:35], 0
v_cvt_pk_bf16_f32 v67, v22, v23
v_cvt_pk_bf16_f32 v68, v24, v25
v_mfma_f32_16x16x32_bf16 v[12:15], a[12:15], v[32:35], 0
buffer_store_dwordx4 v[64:67], v87, s[36:39], s49 offen sc1
D 83,3072
mav 0,16,36
s_add_u32 m0, s50, 36864
R 48,4096
v_cvt_pk_bf16_f32 v69, v26, v27
mav 4,20,36
v_cvt_pk_bf16_f32 v70, v28, v29
v_cvt_pk_bf16_f32 v71, v30, v31
mav 8,24,36
mav 12,28,36
buffer_store_dwordx4 v[68:71], v87, s[36:39], s49 offen offset:1024 sc1
s_waitcnt lgkmcnt(1)
mav 0,32,40
R 52,5120
mav 4,36,40
mav 8,40,40
mav 12,44,40
D 84,0
mav 0,48,44
R 56,6144
mav 4,52,44
mav 8,56,44
mav 12,60,44
s_waitcnt lgkmcnt(1)
D 84,1024
mav 0,64,48
R 60,7168
mav 4,68,48
mav 8,72,48
mav 12,76,48
mav 0,80,52
R 32,8192
mav 4,84,52
mav 8,88,52
mav 12,92,52
s_waitcnt lgkmcnt(1)
mav 0,96,56
R 36,9216
mav 4,100,56
mav 8,104,56
mav 12,108,56
mav 0,112,60
R 40,10240
mav 4,116,60
mav 8,120,60
mav 12,124,60
s_waitcnt lgkmcnt(1)
mav 0,128,32
R 44,11264
mav 4,132,32
mav 8,136,32
mav 12,140,32
mav 0,144,36
R 48,12288
v_add_u32 v72, 1, v72
mav 4,148,36
mav 8,152,36
mav 12,156,36
s_waitcnt lgkmcnt(1)
mav 0,160,40
R 52,13312
mav 4,164,40
mav 8,168,40
mav 12,172,40
mav 0,176,44
R 56,14336
mav 4,180,44
mav 8,184,44
mav 12,188,44
s_waitcnt lgkmcnt(1)
mav 0,192,48
R 60,15360
mav 4,196,48
mav 8,200,48
mav 12,204,48
mav 0,208,52
R 32,16384
mav 4,212,52
mav 8,216,52
mav 12,220,52
s_waitcnt lgkmcnt(1)
mav 0,224,56
R 36,17408
mav 4,228,56
mav 8,232,56
mav 12,236,56
mav 0,240,60
s_waitcnt vmcnt(2)
R 40,18432
mav 4,244,60
mav 8,248,60
mav 12,252,60
s_waitcnt lgkmcnt(1)
buffer_store_dword v72, v81, s[36:39], s52 offen sc1
mvv 0,128,32
R 44,19456
mvv 4,132,32
mvv 8,136,32
mvv 12,140,32
mvv 0,144,36
R 48,20480
mvv 4,148,36
mvv 8,152,36
mvv 12,156,36
s_waitcnt lgkmcnt(1)
mvv 0,160,40
R 52,21504
mvv 4,164,40
mvv 8,168,40
mvv 12,172,40
mvv 0,176,44
R 56,22528
mvv 4,180,44
mvv 8,184,44
mvv 12,188,44
s_waitcnt lgkmcnt(1)
mvv 0,192,48
R 60,23552
mvv 4,196,48
mvv 8,200,48
s_waitcnt vmcnt(1)
s_add_u32 s49, s49, 313344
mvv 12,204,48
s_barrier
mvv 0,208,52
R 32,32768
mvv 4,212,52
mvv 8,216,52
mvv 12,220,52
s_waitcnt lgkmcnt(1)
mvv 0,224,56
R 36,33792
mvv 4,228,56
mvv 8,232,56
mvv 12,236,56
mvv 0,240,60
R 40,34816
mvv 4,244,60
mvv 8,248,60
mvv 12,252,60
s_waitcnt lgkmcnt(1)
v_mfma_f32_16x16x32_bf16 v[16:19], a[0:3], v[32:35], 0
R 44,35840
v_mfma_f32_16x16x32_bf16 v[20:23], a[4:7], v[32:35], 0
v_cvt_pk_bf16_f32 v64, v0, v1
v_cvt_pk_bf16_f32 v65, v2, v3
v_mfma_f32_16x16x32_bf16 v[24:27], a[8:11], v[32:35], 0
v_cvt_pk_bf16_f32 v66, v4, v5
v_cvt_pk_bf16_f32 v67, v6, v7
v_mfma_f32_16x16x32_bf16 v[28:31], a[12:15], v[32:35], 0
buffer_store_dwordx4 v[64:67], v87, s[36:39], s49 offen sc1
v_cvt_pk_bf16_f32 v68, v8, v9
v_cvt_pk_bf16_f32 v69, v10, v11
v_cvt_pk_bf16_f32 v70, v12, v13
mav 16,16,36
R 48,36864
v_cvt_pk_bf16_f32 v71, v14, v15
mav 20,20,36
mav 24,24,36
mav 28,28,36
buffer_store_dwordx4 v[68:71], v87, s[36:39], s49 offen offset:1024 sc1
s_waitcnt lgkmcnt(1)
mav 16,32,40
R 52,37888
mav 20,36,40
mav 24,40,40
mav 28,44,40
mav 16,48,44
R 56,38912
mav 20,52,44
mav 24,56,44
mav 28,60,44
s_waitcnt lgkmcnt(1)
mav 16,64,48
R 60,39936
mav 20,68,48
mav 24,72,48
mav 28,76,48
mav 16,80,52
R 32,40960
mav 20,84,52
mav 24,88,52
mav 28,92,52
s_waitcnt lgkmcnt(1)
mav 16,96,56
R 36,41984
mav 20,100,56
mav 24,104,56
mav 28,108,56
mav 16,112,60
R 40,43008
mav 20,116,60
mav 24,120,60
mav 28,124,60
s_waitcnt lgkmcnt(1)
mav 16,128,32
R 44,44032
mav 20,132,32
mav 24,136,32
mav 28,140,32
mav 16,144,36
R 48,45056
v_add_u32 v72, 1, v72
mav 20,148,36
mav 24,152,36
mav 28,156,36
s_waitcnt lgkmcnt(1)
mav 16,160,40
R 52,46080
mav 20,164,40
mav 24,168,40
mav 28,172,40
mav 16,176,44
R 56,47104
mav 20,180,44
mav 24,184,44
mav 28,188,44
s_waitcnt lgkmcnt(1)
mav 16,192,48
R 60,48128
mav 20,196,48
mav 24,200,48
mav 28,204,48
mav 16,208,52
R 32,49152
mav 20,212,52
mav 24,216,52
mav 28,220,52
s_waitcnt lgkmcnt(1)
mav 16,224,56
R 36,50176
mav 20,228,56
mav 24,232,56
mav 28,236,56
mav 16,240,60
s_waitcnt vmcnt(0)
R 40,51200
mav 20,244,60
mav 24,248,60
mav 28,252,60
s_waitcnt lgkmcnt(1)
buffer_store_dword v72, v81, s[36:39], s52 offen sc1
mvv 16,128,32
R 44,52224
mvv 20,132,32
mvv 24,136,32
mvv 28,140,32
mvv 16,144,36
R 48,53248
mvv 20,148,36
mvv 24,152,36
mvv 28,156,36
s_waitcnt lgkmcnt(1)
mvv 16,160,40
R 52,54272
mvv 20,164,40
mvv 24,168,40
mvv 28,172,40
mvv 16,176,44
R 56,55296
mvv 20,180,44
mvv 24,184,44
mvv 28,188,44
s_waitcnt lgkmcnt(1)
mvv 16,192,48
R 60,56320
mvv 20,196,48
mvv 24,200,48
s_add_u32 s49, s49, 313344
LsG12:
s_cbranch_execz LwG
mvv 28,204,48
mvv 16,208,52
mvv 20,212,52
mvv 24,216,52
mvv 28,220,52
s_waitcnt lgkmcnt(0)
mvv 16,224,56
mvv 20,228,56
mvv 24,232,56
mvv 28,236,56
mvv 16,240,60
mvv 20,244,60
mvv 24,248,60
mvv 28,252,60
s_nop 4
v_cvt_pk_bf16_f32 v64, v16, v17
v_cvt_pk_bf16_f32 v65, v18, v19
v_cvt_pk_bf16_f32 v66, v20, v21
v_cvt_pk_bf16_f32 v67, v22, v23
buffer_store_dwordx4 v[64:67], v87, s[36:39], s49 offen sc1
v_cvt_pk_bf16_f32 v68, v24, v25
v_cvt_pk_bf16_f32 v69, v26, v27
v_cvt_pk_bf16_f32 v70, v28, v29
v_cvt_pk_bf16_f32 v71, v30, v31
buffer_store_dwordx4 v[68:71], v87, s[36:39], s49 offen offset:1024 sc1
v_add_u32 v72, 35, v72
s_waitcnt vmcnt(0)
buffer_store_dword v72, v81, s[36:39], s52 offen sc1
s_endpgm
Lr6:
s_waitcnt vmcnt(0)
v_accvgpr_read_b32 v73, a255
v_mov_b32 v72, 0x100
v_add_u32 v73, 0x9e3779b9, v73
buffer_store_dwordx2 v[72:73], v81, s[36:39], s52 offen sc1
v_add_u32 v85, v85, v72
s_sleep 32
s_sleep 32
s_sleep 32
s_sleep 32
E 0,65536
E 1,69632
E 2,73728
E 3,77824
s_add_u32 s51, s51, 65536
E 0,81920
E 1,86016
E 2,90112
E 3,94208
s_add_u32 s51, s51, 65536
E 0,98304
E 1,102400
E 2,106496
E 3,110592
s_add_u32 s51, s51, 65536
E 0,114688
E 1,118784
E 2,122880
E 3,126976
s_add_u32 s51, s51, 65536
s_cmp_ge_u32 s51, s57
s_cselect_b32 s60, 1048576, 0
s_sub_u32 s51, s51, s60
s_mov_b64 exec, 0
s_cmp_eq_u32 s53, 0
s_cbranch_scc1 LsR0
s_cmp_eq_u32 s53, 1024
s_cbranch_scc1 LsR1
s_cmp_eq_u32 s53, 2048
s_cbranch_scc1 LsR2
s_branch LsR3
LwR:
s_mov_b64 exec, -1
s_waitcnt vmcnt(8)
s_barrier
v_add_u32 v4, 65536, v4
v_add_u32 v5, 65536, v5
v_add_u32 v6, 65536, v6
v_add_u32 v7, 65536, v7
Ta 0,4,0
Ta 4,5,0
Ta 8,6,0
Ta 12,7,0
Ta 16,4,16384
Ta 20,5,16384
Ta 24,6,16384
Ta 28,7,16384
E 0,131072
E 1,135168
E 2,139264
E 3,143360
s_add_u32 s51, s51, 65536
E 0,147456
E 1,151552
E 2,155648
E 3,159744
s_add_u32 s51, s51, 65536
s_waitcnt lgkmcnt(0)
s_waitcnt vmcnt(8)
s_barrier
E 0,65536
Ta 32,4,32768
Ta 36,5,32768
E 1,69632
Ta 40,6,32768
Ta 44,7,32768
E 2,73728
Ta 48,4,49152
E 3,77824
s_add_u32 s51, s51, 65536
Ta 52,5,49152
Ta 56,6,49152
E 0,81920
Ta 60,7,49152
E 1,86016
E 2,90112
E 3,94208
s_add_u32 s51, s51, 65536
s_cmp_ge_u32 s51, s57
s_cselect_b32 s60, 1048576, 0
s_sub_u32 s51, s51, s60
s_waitcnt lgkmcnt(0)
s_waitcnt vmcnt(8)
s_barrier
v_add_u32 v4, 65536, v4
v_add_u32 v5, 65536, v5
v_add_u32 v6, 65536, v6
v_add_u32 v7, 65536, v7
E 0,98304
Ta 64,4,0
Ta 68,5,0
E 1,102400
Ta 72,6,0
Ta 76,7,0
E 2,106496
Ta 80,4,16384
E 3,110592
s_add_u32 s51, s51, 65536
Ta 84,5,16384
Ta 88,6,16384
E 0,114688
Ta 92,7,16384
E 1,118784
E 2,122880
E 3,126976
s_add_u32 s51, s51, 65536
s_waitcnt lgkmcnt(0)
s_waitcnt vmcnt(8)
s_barrier
v_add_u32 v4, -65536, v4
v_add_u32 v5, -65536, v5
v_add_u32 v6, -65536, v6
v_add_u32 v7, -65536, v7
E 0,131072
Ta 96,4,0
Ta 100,5,0
E 1,135168
Ta 104,6,0
Ta 108,7,0
E 2,139264
Ta 112,4,16384
E 3,143360
s_add_u32 s51, s51, 65536
Ta 116,5,16384
Ta 120,6,16384
E 0,147456
Ta 124,7,16384
E 1,151552
E 2,155648
E 3,159744
s_add_u32 s51, s51, 65536
s_cmp_ge_u32 s51, s57
s_cselect_b32 s60, 1048576, 0
s_sub_u32 s51, s51, s60
s_waitcnt lgkmcnt(0)
s_waitcnt vmcnt(8)
s_barrier
E 0,65536
Ta 128,4,32768
Ta 132,5,32768
E 1,69632
Ta 136,6,32768
Ta 140,7,32768
E 2,73728
Ta 144,4,49152
E 3,77824
s_add_u32 s51, s51, 65536
Ta 148,5,49152
Ta 152,6,49152
E 0,81920
Ta 156,7,49152
E 1,86016
E 2,90112
E 3,94208
s_add_u32 s51, s51, 65536
s_waitcnt lgkmcnt(0)
s_waitcnt vmcnt(8)
s_barrier
v_add_u32 v4, 65536, v4
v_add_u32 v5, 65536, v5
v_add_u32 v6, 65536, v6
v_add_u32 v7, 65536, v7
E 0,98304
Ta 160,4,0
Ta 164,5,0
E 1,102400
Ta 168,6,0
Ta 172,7,0
E 2,106496
Ta 176,4,16384
E 3,110592
s_add_u32 s51, s51, 65536
Ta 180,5,16384
Ta 184,6,16384
E 0,114688
Ta 188,7,16384
E 1,118784
E 2,122880
E 3,126976
s_add_u32 s51, s51, 65536
s_cmp_ge_u32 s51, s57
s_cselect_b32 s60, 1048576, 0
s_sub_u32 s51, s51, s60
s_waitcnt lgkmcnt(0)
s_waitcnt vmcnt(8)
s_barrier
v_add_u32 v4, -65536, v4
v_add_u32 v5, -65536, v5
v_add_u32 v6, -65536, v6
v_add_u32 v7, -65536, v7
Ta 192,4,0
Ta 196,5,0
Ta 200,6,0
Ta 204,7,0
Ta 208,4,16384
Ta 212,5,16384
Ta 216,6,16384
Ta 220,7,16384
s_waitcnt lgkmcnt(0)
s_waitcnt vmcnt(0)
s_barrier
Ta 224,4,32768
Ta 228,5,32768
Ta 232,6,32768
Ta 236,7,32768
Ta 240,4,49152
Ta 244,5,49152
Ta 248,6,49152
Ta 252,7,49152
s_waitcnt lgkmcnt(0)
s_waitcnt vmcnt(0)
s_barrier
s_nop 7
s_cmp_eq_u32 s52, 36760
s_cbranch_scc1 Ct
Bt:
s_and_b32 s60, s48, 3
s_cmp_eq_u32 s60, 1
s_cbranch_scc1 LvR1
s_cmp_eq_u32 s60, 2
s_cbranch_scc1 LvR2
s_cmp_eq_u32 s60, 3
s_cbranch_scc1 LvR3
LvR0:
LsR0:
s_cbranch_execz LsR4
buffer_load_dwordx2 v[76:77], v84, s[36:39], 0 offen sc1
s_waitcnt vmcnt(0)
ds_write_b32 v82, v76
v_sub_u32 v76, v76, v85
v_xor_b32 v77, v77, v73
v_cmp_lt_u64 vcc, s[62:63], v[76:77]
s_waitcnt lgkmcnt(0)
s_cbranch_vccnz CR0_1
BR0_1:
s_add_u32 s49, s49, 313344
s_barrier
s_mov_b32 m0, 144384
s_nop 0
buffer_load_dword v84, s[36:39], 0 offen sc1 lds
s_add_u32 m0, s50, 0
s_nop 0
D 83,0
D 83,1024
v_add_u32 v85, 1, v85
D 83,2048
D 83,3072
ds_read_b32 v76, v82 offset:0
s_waitcnt lgkmcnt(0)
v_cmp_gt_i32 vcc, v85, v76
s_add_u32 s49, s49, 313344
s_cbranch_vccnz CR0_2
BR0_2:
s_barrier
s_mov_b32 m0, 144384
s_nop 0
buffer_load_dword v84, s[36:39], 0 offen sc1 lds
s_add_u32 m0, s50, 32768
s_nop 0
D 83,0
D 83,1024
v_add_u32 v85, 1, v85
D 83,2048
D 83,3072
buffer_load_dwordx4 v[124:127], v90, s[36:39], s49 offen sc1
ds_read_b32 v76, v82 offset:0
buffer_load_dwordx4 v[128:131], v90, s[36:39], s49 offen offset:1024 sc1
s_xor_b32 s50, s50, 0x10000
s_waitcnt lgkmcnt(0)
v_cmp_gt_i32 vcc, v85, v76
s_waitcnt vmcnt(7)
s_add_u32 s49, s49, 313344
s_cbranch_vccnz CR0_3
BR0_3:
s_barrier
R 32,0
buffer_load_dwordx4 v[132:135], v86, s[36:39], s49 offen sc1
R 36,1024
buffer_load_dwordx4 v[136:139], v86, s[36:39], s49 offen offset:1024 sc1
R 40,2048
buffer_load_dwordx4 v[140:143], v87, s[36:39], s49 offen sc1
s_waitcnt lgkmcnt(1)
v_mfma_f32_16x16x32_bf16 v[0:3], a[0:3], v[32:35], v[100:103]
R 44,3072
v_mfma_f32_16x16x32_bf16 v[4:7], a[4:7], v[32:35], v[104:107]
buffer_load_dwordx4 v[144:147], v87, s[36:39], s49 offen offset:1024 sc1
v_mfma_f32_16x16x32_bf16 v[8:11], a[8:11], v[32:35], v[108:111]
v_mfma_f32_16x16x32_bf16 v[12:15], a[12:15], v[32:35], v[112:115]
mav 0,16,36
R 48,4096
mav 4,20,36
buffer_load_dwordx4 v[148:151], v88, s[36:39], s49 offen sc1
mav 8,24,36
mav 12,28,36
s_waitcnt lgkmcnt(1)
mav 0,32,40
R 52,5120
mav 4,36,40
buffer_load_dwordx4 v[152:155], v88, s[36:39], s49 offen offset:1024 sc1
mav 8,40,40
mav 12,44,40
mav 0,48,44
R 56,6144
mav 4,52,44
buffer_load_dwordx4 v[156:159], v89, s[36:39], s49 offen sc1
mav 8,56,44
mav 12,60,44
s_waitcnt lgkmcnt(1)
mav 0,64,48
R 60,7168
mav 4,68,48
buffer_load_dwordx4 v[160:163], v89, s[36:39], s49 offen offset:1024 sc1
mav 8,72,48
mav 12,76,48
s_mov_b32 m0, 144384
mav 0,80,52
R 32,8192
mav 4,84,52
mav 8,88,52
buffer_load_dword v84, s[36:39], 0 offen sc1 lds
mav 12,92,52
s_waitcnt lgkmcnt(1)
s_add_u32 m0, s50, 0
mav 0,96,56
R 36,9216
mav 4,100,56
D 83,0
mav 8,104,56
mav 12,108,56
mav 0,112,60
R 40,10240
mav 4,116,60
D 83,1024
mav 8,120,60
mav 12,124,60
s_waitcnt lgkmcnt(1)
mav 0,128,32
R 44,11264
mav 4,132,32
mav 8,136,32
mav 12,140,32
mav 0,144,36
R 48,12288
mav 4,148,36
mav 8,152,36
mav 12,156,36
s_waitcnt lgkmcnt(1)
mav 0,160,40
R 52,13312
v_add_u32 v85, 1, v85
mav 4,164,40
mav 8,168,40
mav 12,172,40
mav 0,176,44
R 56,14336
mav 4,180,44
D 83,2048
mav 8,184,44
mav 12,188,44
s_waitcnt lgkmcnt(1)
mav 0,192,48
R 60,15360
mav 4,196,48
D 83,3072
mav 8,200,48
mav 12,204,48
LsR1:
s_cbranch_execz LsR5
mav 0,208,52
mav 4,212,52
mav 8,216,52
mav 12,220,52
s_waitcnt lgkmcnt(0)
mav 0,224,56
mav 4,228,56
mav 8,232,56
mav 12,236,56
mav 0,240,60
mav 4,244,60
buffer_load_dwordx4 v[164:167], v90, s[36:39], s49 offen sc1
mav 8,248,60
ds_read_b32 v76, v82 offset:0
mav 12,252,60
s_waitcnt vmcnt(15)
mvv 0,116,124
mvv 4,120,124
s_waitcnt vmcnt(14)
buffer_load_dwordx4 v[168:171], v90, s[36:39], s49 offen offset:1024 sc1
mvv 8,116,128
mvv 12,120,128
s_waitcnt vmcnt(14)
mvv 0,116,132
mvv 4,120,132
s_waitcnt vmcnt(13) lgkmcnt(0)
v_cmp_gt_i32 vcc, v85, v76
mvv 8,116,136
s_waitcnt vmcnt(12)
s_add_u32 s49, s49, 313344
mvv 12,120,136
s_cbranch_vccnz CR0_4
BR0_4:
s_barrier
mvv 0,116,140
R 32,32768
mvv 4,120,140
s_waitcnt vmcnt(11)
buffer_load_dwordx4 v[172:175], v86, s[36:39], s49 offen sc1
mvv 8,116,144
mvv 12,120,144
s_waitcnt vmcnt(11)
mvv 0,116,148
R 36,33792
mvv 4,120,148
s_waitcnt vmcnt(10)
buffer_load_dwordx4 v[176:179], v86, s[36:39], s49 offen offset:1024 sc1
mvv 8,116,152
mvv 12,120,152
s_waitcnt vmcnt(10)
mvv 0,116,156
R 40,34816
mvv 4,120,156
s_waitcnt vmcnt(9)
buffer_load_dwordx4 v[180:183], v87, s[36:39], s49 offen sc1
mvv 8,116,160
mvv 12,120,160
s_waitcnt lgkmcnt(1)
v_mfma_f32_16x16x32_bf16 v[16:19], a[0:3], v[32:35], v[100:103]
R 44,35840
v_cvt_pk_bf16_f32 v64, v0, v1
v_mfma_f32_16x16x32_bf16 v[20:23], a[4:7], v[32:35], v[104:107]
buffer_load_dwordx4 v[184:187], v87, s[36:39], s49 offen offset:1024 sc1
v_mfma_f32_16x16x32_bf16 v[24:27], a[8:11], v[32:35], v[108:111]
v_cvt_pk_bf16_f32 v65, v2, v3
v_cvt_pk_bf16_f32 v66, v4, v5
v_mfma_f32_16x16x32_bf16 v[28:31], a[12:15], v[32:35], v[112:115]
v_cvt_pk_bf16_f32 v67, v6, v7
v_cvt_pk_bf16_f32 v68, v8, v9
mav 16,16,36
R 48,36864
v_cvt_pk_bf16_f32 v69, v10, v11
mav 20,20,36
buffer_load_dwordx4 v[188:191], v88, s[36:39], s49 offen sc1
mav 24,24,36
v_cvt_pk_bf16_f32 v70, v12, v13
v_cvt_pk_bf16_f32 v71, v14, v15
mav 28,28,36
s_waitcnt lgkmcnt(1)
v_mul_f32 v75, v0, v0
v_add_f32 v74, v1, v0
mav 16,32,40
R 52,37888
v_fmac_f32 v75, v1, v1
mav 20,36,40
buffer_load_dwordx4 v[192:195], v88, s[36:39], s49 offen offset:1024 sc1
mav 24,40,40
v_add_f32 v74, v2, v74
v_fmac_f32 v75, v2, v2
mav 28,44,40
v_add_f32 v74, v3, v74
v_fmac_f32 v75, v3, v3
mav 16,48,44
R 56,38912
v_add_f32 v74, v4, v74
mav 20,52,44
buffer_load_dwordx4 v[196:199], v89, s[36:39], s49 offen sc1
mav 24,56,44
v_fmac_f32 v75, v4, v4
v_add_f32 v74, v5, v74
mav 28,60,44
s_waitcnt lgkmcnt(1)
v_fmac_f32 v75, v5, v5
v_add_f32 v74, v6, v74
mav 16,64,48
R 60,39936
v_fmac_f32 v75, v6, v6
mav 20,68,48
buffer_load_dwordx4 v[200:203], v89, s[36:39], s49 offen offset:1024 sc1
mav 24,72,48
v_add_f32 v74, v7, v74
v_fmac_f32 v75, v7, v7
mav 28,76,48
ds_write_b128 v94, v[64:67] offset:0
mav 16,80,52
s_mov_b32 m0, 144384
R 32,40960
v_add_f32 v74, v8, v74
mav 20,84,52
ds_write_b128 v95, v[68:71] offset:0
mav 24,88,52
buffer_load_dword v84, s[36:39], 0 offen sc1 lds
mav 28,92,52
s_waitcnt lgkmcnt(3)
ds_read_b128 v[64:67], v96 offset:0
s_add_u32 m0, s50, 32768
mav 16,96,56
R 36,41984
v_fmac_f32 v75, v8, v8
mav 20,100,56
D 83,0
mav 24,104,56
ds_read_b128 v[68:71], v96 offset:1024
v_add_f32 v74, v9, v74
mav 28,108,56
v_fmac_f32 v75, v9, v9
v_add_f32 v74, v10, v74
mav 16,112,60
R 40,43008
v_fmac_f32 v75, v10, v10
mav 20,116,60
D 83,1024
mav 24,120,60
v_add_f32 v74, v11, v74
v_fmac_f32 v75, v11, v11
mav 28,124,60
s_waitcnt lgkmcnt(2)
v_add_f32 v74, v12, v74
v_fmac_f32 v75, v12, v12
mav 16,128,32
R 44,44032
v_add_f32 v74, v13, v74
mav 20,132,32
v_fmac_f32 v75, v13, v13
v_add_f32 v74, v14, v74
mav 24,136,32
v_fmac_f32 v75, v14, v14
v_add_f32 v74, v15, v74
mav 28,140,32
v_fmac_f32 v75, v15, v15
mav 16,144,36
R 48,45056
mav 20,148,36
v_permlane32_swap_b32 v74, v75
mav 24,152,36
v_add_f32 v74, v74, v75
mav 28,156,36
s_waitcnt lgkmcnt(1)
mav 16,160,40
mav 20,164,40
R 52,46080
v_add_u32 v85, 1, v85
mav 24,168,40
mav 28,172,40
mav 16,176,44
R 56,47104
mav 20,180,44
D 83,2048
mav 24,184,44
mav 28,188,44
s_waitcnt lgkmcnt(1)
mav 16,192,48
R 60,48128
mav 20,196,48
D 83,3072
mav 24,200,48
mav 28,204,48
mav 16,208,52
mav 20,212,52
buffer_store_dwordx4 v[64:67], v91, s[36:39], s54 offen sc1
mav 24,216,52
mav 28,220,52
s_waitcnt lgkmcnt(0)
mav 16,224,56
mav 20,228,56
buffer_store_dwordx4 v[68:71], v92, s[36:39], s54 offen sc1
mav 24,232,56
mav 28,236,56
mav 16,240,60
mav 20,244,60
buffer_load_dwordx4 v[124:127], v90, s[36:39], s49 offen sc1
mav 24,248,60
ds_read_b32 v76, v82 offset:0
mav 28,252,60
s_waitcnt vmcnt(17)
mvv 16,116,164
mvv 20,120,164
s_waitcnt vmcnt(16)
buffer_load_dwordx4 v[128:131], v90, s[36:39], s49 offen offset:1024 sc1
mvv 24,116,168
s_xor_b32 s50, s50, 0x10000
s_sub_u32 s54, s54, 32768
mvv 28,120,168
s_waitcnt vmcnt(16)
mvv 16,116,172
mvv 20,120,172
s_waitcnt vmcnt(15) lgkmcnt(0)
v_cmp_gt_i32 vcc, v85, v76
v_xor_b32 v80, 0x10000, v80
mvv 24,116,176
s_waitcnt vmcnt(14)
s_add_u32 s49, s49, 313344
mvv 28,120,176
s_cbranch_vccnz CR0_5
BR0_5:
s_barrier
mvv 16,116,180
R 32,0
mvv 20,120,180
s_waitcnt vmcnt(13)
buffer_load_dwordx4 v[132:135], v86, s[36:39], s49 offen sc1
mvv 24,116,184
mvv 28,120,184
s_waitcnt vmcnt(13)
mvv 16,116,188
R 36,1024
mvv 20,120,188
s_waitcnt vmcnt(12)
buffer_load_dwordx4 v[136:139], v86, s[36:39], s49 offen offset:1024 sc1
mvv 24,116,192
mvv 28,120,192
s_waitcnt vmcnt(12)
mvv 16,116,196
R 40,2048
mvv 20,120,196
s_waitcnt vmcnt(11)
buffer_load_dwordx4 v[140:143], v87, s[36:39], s49 offen sc1
mvv 24,116,200
mvv 28,120,200
s_waitcnt lgkmcnt(1)
v_mfma_f32_16x16x32_bf16 v[0:3], a[0:3], v[32:35], v[100:103]
R 44,3072
v_cvt_pk_bf16_f32 v64, v16, v17
v_mfma_f32_16x16x32_bf16 v[4:7], a[4:7], v[32:35], v[104:107]
buffer_load_dwordx4 v[144:147], v87, s[36:39], s49 offen offset:1024 sc1
v_mfma_f32_16x16x32_bf16 v[8:11], a[8:11], v[32:35], v[108:111]
v_cvt_pk_bf16_f32 v65, v18, v19
v_cvt_pk_bf16_f32 v66, v20, v21
v_mfma_f32_16x16x32_bf16 v[12:15], a[12:15], v[32:35], v[112:115]
v_cvt_pk_bf16_f32 v67, v22, v23
v_cvt_pk_bf16_f32 v68, v24, v25
mav 0,16,36
R 48,4096
v_cvt_pk_bf16_f32 v69, v26, v27
mav 4,20,36
buffer_load_dwordx4 v[148:151], v88, s[36:39], s49 offen sc1
mav 8,24,36
v_cvt_pk_bf16_f32 v70, v28, v29
v_cvt_pk_bf16_f32 v71, v30, v31
mav 12,28,36
s_waitcnt lgkmcnt(1)
v_mul_f32 v75, v16, v16
v_fmac_f32 v75, v17, v17
mav 0,32,40
R 52,5120
v_fmac_f32 v75, v18, v18
mav 4,36,40
buffer_load_dwordx4 v[152:155], v88, s[36:39], s49 offen offset:1024 sc1
mav 8,40,40
v_fmac_f32 v75, v19, v19
v_fmac_f32 v75, v20, v20
mav 12,44,40
v_fmac_f32 v75, v21, v21
v_fmac_f32 v75, v22, v22
mav 0,48,44
R 56,6144
v_fmac_f32 v75, v23, v23
mav 4,52,44
buffer_load_dwordx4 v[156:159], v89, s[36:39], s49 offen sc1
mav 8,56,44
v_fmac_f32 v75, v24, v24
v_fmac_f32 v75, v25, v25
mav 12,60,44
s_waitcnt lgkmcnt(1)
v_fmac_f32 v75, v26, v26
v_fmac_f32 v75, v27, v27
mav 0,64,48
R 60,7168
v_fmac_f32 v75, v28, v28
mav 4,68,48
buffer_load_dwordx4 v[160:163], v89, s[36:39], s49 offen offset:1024 sc1
mav 8,72,48
ds_write_b32 v97, v74 offset:0
mav 12,76,48
ds_write_b128 v94, v[64:67] offset:0
mav 0,80,52
s_mov_b32 m0, 144384
R 32,8192
v_add_f32 v74, v17, v16
mav 4,84,52
ds_write_b128 v95, v[68:71] offset:0
mav 8,88,52
buffer_load_dword v84, s[36:39], 0 offen sc1 lds
mav 12,92,52
s_waitcnt lgkmcnt(4)
ds_read_b128 v[64:67], v96 offset:0
s_add_u32 m0, s50, 0
mav 0,96,56
R 36,9216
v_add_f32 v74, v18, v74
mav 4,100,56
D 83,0
mav 8,104,56
ds_read_b128 v[68:71], v96 offset:1024
v_add_f32 v74, v19, v74
mav 12,108,56
v_add_f32 v74, v20, v74
v_add_f32 v74, v21, v74
mav 0,112,60
R 40,10240
v_add_f32 v74, v22, v74
mav 4,116,60
D 83,1024
mav 8,120,60
v_add_f32 v74, v23, v74
v_add_f32 v74, v24, v74
mav 12,124,60
s_waitcnt lgkmcnt(2)
v_add_f32 v74, v25, v74
v_add_f32 v74, v26, v74
mav 0,128,32
R 44,11264
v_add_f32 v74, v27, v74
mav 4,132,32
v_add_f32 v74, v28, v74
v_add_f32 v74, v29, v74
mav 8,136,32
v_fmac_f32 v75, v29, v29
v_add_f32 v74, v30, v74
mav 12,140,32
v_fmac_f32 v75, v30, v30
v_add_f32 v74, v31, v74
mav 0,144,36
R 48,12288
v_fmac_f32 v75, v31, v31
mav 4,148,36
mav 8,152,36
v_permlane32_swap_b32 v74, v75
mav 12,156,36
s_waitcnt lgkmcnt(1)
v_add_f32 v74, v74, v75
mav 0,160,40
R 52,13312
v_add_u32 v85, 1, v85
mav 4,164,40
v_add_u32 v72, 1, v72
mav 8,168,40
mav 12,172,40
mav 0,176,44
R 56,14336
mav 4,180,44
D 83,2048
mav 8,184,44
mav 12,188,44
s_waitcnt lgkmcnt(1)
mav 0,192,48
R 60,15360
mav 4,196,48
D 83,3072
mav 8,200,48
mav 12,204,48
mav 0,208,52
mav 4,212,52
buffer_store_dwordx4 v[64:67], v91, s[36:39], s54 offen sc1
mav 8,216,52
mav 12,220,52
s_waitcnt lgkmcnt(0)
mav 0,224,56
mav 4,228,56
buffer_store_dwordx4 v[68:71], v92, s[36:39], s54 offen sc1
mav 8,232,56
mav 12,236,56
mav 0,240,60
mav 4,244,60
buffer_load_dwordx4 v[164:167], v90, s[36:39], s49 offen sc1
mav 8,248,60
ds_read_b32 v76, v82 offset:0
mav 12,252,60
s_waitcnt vmcnt(17)
mvv 0,116,124
buffer_store_dword v72, v81, s[36:39], s52 offen sc1
mvv 4,120,124
s_waitcnt vmcnt(17)
buffer_load_dwordx4 v[168:171], v90, s[36:39], s49 offen offset:1024 sc1
mvv 8,116,128
s_sub_u32 s54, s54, 32768
mvv 12,120,128
s_waitcnt vmcnt(17)
mvv 0,116,132
mvv 4,120,132
s_waitcnt vmcnt(16) lgkmcnt(0)
v_cmp_gt_i32 vcc, v85, v76
mvv 8,116,136
s_waitcnt vmcnt(15)
s_add_u32 s49, s49, 313344
mvv 12,120,136
s_cbranch_vccnz CR0_6
BR0_6:
s_barrier
mvv 0,116,140
R 32,32768
mvv 4,120,140
s_waitcnt vmcnt(14)
buffer_load_dwordx4 v[172:175], v86, s[36:39], s49 offen sc1
mvv 8,116,144
mvv 12,120,144
s_waitcnt vmcnt(14)
mvv 0,116,148
R 36,33792
mvv 4,120,148
s_waitcnt vmcnt(13)
buffer_load_dwordx4 v[176:179], v86, s[36:39], s49 offen offset:1024 sc1
mvv 8,116,152
mvv 12,120,152
s_waitcnt vmcnt(13)
mvv 0,116,156
R 40,34816
mvv 4,120,156
s_waitcnt vmcnt(12)
buffer_load_dwordx4 v[180:183], v87, s[36:39], s49 offen sc1
mvv 8,116,160
mvv 12,120,160
s_waitcnt lgkmcnt(1)
v_mfma_f32_16x16x32_bf16 v[16:19], a[0:3], v[32:35], v[100:103]
R 44,35840
v_cvt_pk_bf16_f32 v64, v0, v1
v_mfma_f32_16x16x32_bf16 v[20:23], a[4:7], v[32:35], v[104:107]
buffer_load_dwordx4 v[184:187], v87, s[36:39], s49 offen offset:1024 sc1
v_mfma_f32_16x16x32_bf16 v[24:27], a[8:11], v[32:35], v[108:111]
v_cvt_pk_bf16_f32 v65, v2, v3
v_cvt_pk_bf16_f32 v66, v4, v5
v_mfma_f32_16x16x32_bf16 v[28:31], a[12:15], v[32:35], v[112:115]
v_cvt_pk_bf16_f32 v67, v6, v7
v_cvt_pk_bf16_f32 v68, v8, v9
mav 16,16,36
R 48,36864
v_cvt_pk_bf16_f32 v69, v10, v11
mav 20,20,36
buffer_load_dwordx4 v[188:191], v88, s[36:39], s49 offen sc1
mav 24,24,36
v_cvt_pk_bf16_f32 v70, v12, v13
v_cvt_pk_bf16_f32 v71, v14, v15
mav 28,28,36
s_waitcnt lgkmcnt(1)
v_mul_f32 v75, v0, v0
v_fmac_f32 v75, v1, v1
mav 16,32,40
R 52,37888
v_fmac_f32 v75, v2, v2
mav 20,36,40
buffer_load_dwordx4 v[192:195], v88, s[36:39], s49 offen offset:1024 sc1
mav 24,40,40
v_fmac_f32 v75, v3, v3
v_fmac_f32 v75, v4, v4
mav 28,44,40
v_fmac_f32 v75, v5, v5
v_fmac_f32 v75, v6, v6
mav 16,48,44
R 56,38912
v_fmac_f32 v75, v7, v7
mav 20,52,44
buffer_load_dwordx4 v[196:199], v89, s[36:39], s49 offen sc1
mav 24,56,44
v_fmac_f32 v75, v8, v8
v_fmac_f32 v75, v9, v9
mav 28,60,44
s_waitcnt lgkmcnt(1)
v_fmac_f32 v75, v10, v10
v_fmac_f32 v75, v11, v11
mav 16,64,48
R 60,39936
v_fmac_f32 v75, v12, v12
mav 20,68,48
buffer_load_dwordx4 v[200:203], v89, s[36:39], s49 offen offset:1024 sc1
mav 24,72,48
ds_write_b32 v97, v74 offset:1024
mav 28,76,48
ds_write_b128 v94, v[64:67] offset:0
mav 16,80,52
s_mov_b32 m0, 144384
R 32,40960
v_add_f32 v74, v1, v0
mav 20,84,52
ds_write_b128 v95, v[68:71] offset:0
mav 24,88,52
buffer_load_dword v84, s[36:39], 0 offen sc1 lds
mav 28,92,52
s_waitcnt lgkmcnt(4)
ds_read_b128 v[64:67], v96 offset:0
s_add_u32 m0, s50, 32768
mav 16,96,56
R 36,41984
v_add_f32 v74, v2, v74
mav 20,100,56
D 83,0
mav 24,104,56
ds_read_b128 v[68:71], v96 offset:1024
v_add_f32 v74, v3, v74
mav 28,108,56
v_add_f32 v74, v4, v74
v_add_f32 v74, v5, v74
mav 16,112,60
R 40,43008
v_add_f32 v74, v6, v74
mav 20,116,60
D 83,1024
mav 24,120,60
v_add_f32 v74, v7, v74
v_add_f32 v74, v8, v74
mav 28,124,60
s_waitcnt lgkmcnt(2)
v_add_f32 v74, v9, v74
v_add_f32 v74, v10, v74
mav 16,128,32
R 44,44032
v_add_f32 v74, v11, v74
mav 20,132,32
v_add_f32 v74, v12, v74
v_add_f32 v74, v13, v74
mav 24,136,32
v_fmac_f32 v75, v13, v13
v_add_f32 v74, v14, v74
mav 28,140,32
v_fmac_f32 v75, v14, v14
v_add_f32 v74, v15, v74
mav 16,144,36
R 48,45056
v_fmac_f32 v75, v15, v15
mav 20,148,36
mav 24,152,36
v_permlane32_swap_b32 v74, v75
mav 28,156,36
s_waitcnt lgkmcnt(1)
v_add_f32 v74, v74, v75
mav 16,160,40
s_mov_b32 s60, -14
.p2align 6
LR0_7:
mav 20,164,40
R 52,46080
v_add_u32 v85, 1, v85
mav 24,168,40
v_add_u32 v72, 1, v72
mav 28,172,40
mav 16,176,44
R 56,47104
mav 20,180,44
D 83,2048
mav 24,184,44
mav 28,188,44
s_waitcnt lgkmcnt(1)
mav 16,192,48
R 60,48128
mav 20,196,48
D 83,3072
mav 24,200,48
mav 28,204,48
mav 16,208,52
mav 20,212,52
buffer_store_dwordx4 v[64:67], v91, s[36:39], s54 offen sc1
mav 24,216,52
mav 28,220,52
s_waitcnt lgkmcnt(0)
mav 16,224,56
mav 20,228,56
buffer_store_dwordx4 v[68:71], v92, s[36:39], s54 offen sc1
mav 24,232,56
mav 28,236,56
mav 16,240,60
mav 20,244,60
buffer_load_dwordx4 v[124:127], v90, s[36:39], s49 offen sc1
mav 24,248,60
ds_read_b32 v76, v82 offset:0
mav 28,252,60
s_waitcnt vmcnt(18)
mvv 16,116,164
buffer_store_dword v72, v81, s[36:39], s52 offen sc1
mvv 20,120,164
s_waitcnt vmcnt(17)
buffer_load_dwordx4 v[128:131], v90, s[36:39], s49 offen offset:1024 sc1
mvv 24,116,168
LsR2:
s_cbranch_execz LsR6
s_xor_b32 s50, s50, 0x10000
s_sub_u32 s54, s54, 32768
mvv 28,120,168
s_waitcnt vmcnt(17)
mvv 16,116,172
mvv 20,120,172
s_waitcnt vmcnt(16) lgkmcnt(0)
v_cmp_gt_i32 vcc, v85, v76
v_xor_b32 v80, 0x10000, v80
mvv 24,116,176
s_waitcnt vmcnt(15)
s_add_u32 s49, s49, 313344
mvv 28,120,176
s_cbranch_vccnz CR0_8
BR0_8:
s_barrier
mvv 16,116,180
R 32,0
mvv 20,120,180
s_waitcnt vmcnt(14)
buffer_load_dwordx4 v[132:135], v86, s[36:39], s49 offen sc1
mvv 24,116,184
mvv 28,120,184
s_waitcnt vmcnt(14)
mvv 16,116,188
R 36,1024
mvv 20,120,188
s_waitcnt vmcnt(13)
buffer_load_dwordx4 v[136:139], v86, s[36:39], s49 offen offset:1024 sc1
mvv 24,116,192
mvv 28,120,192
s_waitcnt vmcnt(13)
mvv 16,116,196
R 40,2048
mvv 20,120,196
s_waitcnt vmcnt(12)
buffer_load_dwordx4 v[140:143], v87, s[36:39], s49 offen sc1
mvv 24,116,200
mvv 28,120,200
s_waitcnt lgkmcnt(1)
v_mfma_f32_16x16x32_bf16 v[0:3], a[0:3], v[32:35], v[100:103]
R 44,3072
v_cvt_pk_bf16_f32 v64, v16, v17
v_mfma_f32_16x16x32_bf16 v[4:7], a[4:7], v[32:35], v[104:107]
buffer_load_dwordx4 v[144:147], v87, s[36:39], s49 offen offset:1024 sc1
v_mfma_f32_16x16x32_bf16 v[8:11], a[8:11], v[32:35], v[108:111]
v_cvt_pk_bf16_f32 v65, v18, v19
v_cvt_pk_bf16_f32 v66, v20, v21
v_mfma_f32_16x16x32_bf16 v[12:15], a[12:15], v[32:35], v[112:115]
v_cvt_pk_bf16_f32 v67, v22, v23
v_cvt_pk_bf16_f32 v68, v24, v25
mav 0,16,36
R 48,4096
v_cvt_pk_bf16_f32 v69, v26, v27
mav 4,20,36
buffer_load_dwordx4 v[148:151], v88, s[36:39], s49 offen sc1
mav 8,24,36
v_cvt_pk_bf16_f32 v70, v28, v29
v_cvt_pk_bf16_f32 v71, v30, v31
mav 12,28,36
s_waitcnt lgkmcnt(1)
v_mul_f32 v75, v16, v16
v_fmac_f32 v75, v17, v17
mav 0,32,40
R 52,5120
v_fmac_f32 v75, v18, v18
mav 4,36,40
buffer_load_dwordx4 v[152:155], v88, s[36:39], s49 offen offset:1024 sc1
mav 8,40,40
v_fmac_f32 v75, v19, v19
v_fmac_f32 v75, v20, v20
mav 12,44,40
v_fmac_f32 v75, v21, v21
v_fmac_f32 v75, v22, v22
mav 0,48,44
R 56,6144
v_fmac_f32 v75, v23, v23
mav 4,52,44
buffer_load_dwordx4 v[156:159], v89, s[36:39], s49 offen sc1
mav 8,56,44
v_fmac_f32 v75, v24, v24
v_fmac_f32 v75, v25, v25
mav 12,60,44
s_waitcnt lgkmcnt(1)
v_fmac_f32 v75, v26, v26
v_fmac_f32 v75, v27, v27
mav 0,64,48
R 60,7168
v_fmac_f32 v75, v28, v28
mav 4,68,48
buffer_load_dwordx4 v[160:163], v89, s[36:39], s49 offen offset:1024 sc1
mav 8,72,48
ds_write_b32 v97, v74 offset:0
mav 12,76,48
ds_write_b128 v94, v[64:67] offset:0
mav 0,80,52
s_mov_b32 m0, 144384
R 32,8192
v_add_f32 v74, v17, v16
mav 4,84,52
ds_write_b128 v95, v[68:71] offset:0
mav 8,88,52
buffer_load_dword v84, s[36:39], 0 offen sc1 lds
mav 12,92,52
s_waitcnt lgkmcnt(4)
ds_read_b128 v[64:67], v96 offset:0
s_add_u32 m0, s50, 0
mav 0,96,56
R 36,9216
v_add_f32 v74, v18, v74
mav 4,100,56
D 83,0
mav 8,104,56
ds_read_b128 v[68:71], v96 offset:1024
v_add_f32 v74, v19, v74
mav 12,108,56
v_add_f32 v74, v20, v74
v_add_f32 v74, v21, v74
mav 0,112,60
R 40,10240
v_add_f32 v74, v22, v74
mav 4,116,60
D 83,1024
mav 8,120,60
v_add_f32 v74, v23, v74
v_add_f32 v74, v24, v74
mav 12,124,60
s_waitcnt lgkmcnt(2)
v_add_f32 v74, v25, v74
v_add_f32 v74, v26, v74
mav 0,128,32
R 44,11264
v_add_f32 v74, v27, v74
mav 4,132,32
v_add_f32 v74, v28, v74
v_add_f32 v74, v29, v74
mav 8,136,32
v_fmac_f32 v75, v29, v29
v_add_f32 v74, v30, v74
mav 12,140,32
v_fmac_f32 v75, v30, v30
v_add_f32 v74, v31, v74
mav 0,144,36
R 48,12288
v_fmac_f32 v75, v31, v31
mav 4,148,36
mav 8,152,36
v_permlane32_swap_b32 v74, v75
mav 12,156,36
s_waitcnt lgkmcnt(1)
v_add_f32 v74, v74, v75
mav 0,160,40
R 52,13312
v_add_u32 v85, 1, v85
mav 4,164,40
v_add_u32 v72, 1, v72
mav 8,168,40
mav 12,172,40
mav 0,176,44
R 56,14336
mav 4,180,44
D 83,2048
mav 8,184,44
mav 12,188,44
s_waitcnt lgkmcnt(1)
mav 0,192,48
R 60,15360
mav 4,196,48
D 83,3072
mav 8,200,48
mav 12,204,48
mav 0,208,52
mav 4,212,52
buffer_store_dwordx4 v[64:67], v91, s[36:39], s54 offen sc1
mav 8,216,52
mav 12,220,52
s_waitcnt lgkmcnt(0)
mav 0,224,56
mav 4,228,56
buffer_store_dwordx4 v[68:71], v92, s[36:39], s54 offen sc1
mav 8,232,56
mav 12,236,56
mav 0,240,60
mav 4,244,60
buffer_load_dwordx4 v[164:167], v90, s[36:39], s49 offen sc1
mav 8,248,60
ds_read_b32 v76, v82 offset:0
mav 12,252,60
s_waitcnt vmcnt(18)
mvv 0,116,124
buffer_store_dword v72, v81, s[36:39], s52 offen sc1
mvv 4,120,124
s_waitcnt vmcnt(17)
buffer_load_dwordx4 v[168:171], v90, s[36:39], s49 offen offset:1024 sc1
mvv 8,116,128
s_sub_u32 s54, s54, 32768
mvv 12,120,128
s_waitcnt vmcnt(17)
mvv 0,116,132
mvv 4,120,132
s_waitcnt vmcnt(16) lgkmcnt(0)
v_cmp_gt_i32 vcc, v85, v76
mvv 8,116,136
s_waitcnt vmcnt(15)
s_add_u32 s49, s49, 313344
mvv 12,120,136
s_cbranch_vccnz CR0_9
BR0_9:
s_barrier
mvv 0,116,140
R 32,32768
mvv 4,120,140
s_waitcnt vmcnt(14)
buffer_load_dwordx4 v[172:175], v86, s[36:39], s49 offen sc1
mvv 8,116,144
mvv 12,120,144
s_waitcnt vmcnt(14)
mvv 0,116,148
R 36,33792
mvv 4,120,148
s_waitcnt vmcnt(13)
buffer_load_dwordx4 v[176:179], v86, s[36:39], s49 offen offset:1024 sc1
mvv 8,116,152
mvv 12,120,152
s_waitcnt vmcnt(13)
mvv 0,116,156
R 40,34816
mvv 4,120,156
s_waitcnt vmcnt(12)
buffer_load_dwordx4 v[180:183], v87, s[36:39], s49 offen sc1
mvv 8,116,160
mvv 12,120,160
s_waitcnt lgkmcnt(1)
v_mfma_f32_16x16x32_bf16 v[16:19], a[0:3], v[32:35], v[100:103]
R 44,35840
v_cvt_pk_bf16_f32 v64, v0, v1
v_mfma_f32_16x16x32_bf16 v[20:23], a[4:7], v[32:35], v[104:107]
buffer_load_dwordx4 v[184:187], v87, s[36:39], s49 offen offset:1024 sc1
v_mfma_f32_16x16x32_bf16 v[24:27], a[8:11], v[32:35], v[108:111]
v_cvt_pk_bf16_f32 v65, v2, v3
v_cvt_pk_bf16_f32 v66, v4, v5
v_mfma_f32_16x16x32_bf16 v[28:31], a[12:15], v[32:35], v[112:115]
v_cvt_pk_bf16_f32 v67, v6, v7
v_cvt_pk_bf16_f32 v68, v8, v9
mav 16,16,36
R 48,36864
v_cvt_pk_bf16_f32 v69, v10, v11
mav 20,20,36
buffer_load_dwordx4 v[188:191], v88, s[36:39], s49 offen sc1
mav 24,24,36
v_cvt_pk_bf16_f32 v70, v12, v13
v_cvt_pk_bf16_f32 v71, v14, v15
mav 28,28,36
s_waitcnt lgkmcnt(1)
v_mul_f32 v75, v0, v0
v_fmac_f32 v75, v1, v1
mav 16,32,40
R 52,37888
v_fmac_f32 v75, v2, v2
mav 20,36,40
buffer_load_dwordx4 v[192:195], v88, s[36:39], s49 offen offset:1024 sc1
mav 24,40,40
v_fmac_f32 v75, v3, v3
v_fmac_f32 v75, v4, v4
mav 28,44,40
v_fmac_f32 v75, v5, v5
v_fmac_f32 v75, v6, v6
mav 16,48,44
R 56,38912
v_fmac_f32 v75, v7, v7
mav 20,52,44
buffer_load_dwordx4 v[196:199], v89, s[36:39], s49 offen sc1
mav 24,56,44
v_fmac_f32 v75, v8, v8
v_fmac_f32 v75, v9, v9
mav 28,60,44
s_waitcnt lgkmcnt(1)
v_fmac_f32 v75, v10, v10
v_fmac_f32 v75, v11, v11
mav 16,64,48
R 60,39936
v_fmac_f32 v75, v12, v12
mav 20,68,48
buffer_load_dwordx4 v[200:203], v89, s[36:39], s49 offen offset:1024 sc1
mav 24,72,48
ds_write_b32 v97, v74 offset:1024
mav 28,76,48
ds_write_b128 v94, v[64:67] offset:0
mav 16,80,52
s_mov_b32 m0, 144384
R 32,40960
v_add_f32 v74, v1, v0
mav 20,84,52
ds_write_b128 v95, v[68:71] offset:0
mav 24,88,52
buffer_load_dword v84, s[36:39], 0 offen sc1 lds
mav 28,92,52
s_waitcnt lgkmcnt(4)
ds_read_b128 v[64:67], v96 offset:0
s_add_u32 m0, s50, 32768
mav 16,96,56
R 36,41984
v_add_f32 v74, v2, v74
mav 20,100,56
D 83,0
mav 24,104,56
ds_read_b128 v[68:71], v96 offset:1024
v_add_f32 v74, v3, v74
mav 28,108,56
v_add_f32 v74, v4, v74
v_add_f32 v74, v5, v74
mav 16,112,60
R 40,43008
v_add_f32 v74, v6, v74
mav 20,116,60
D 83,1024
mav 24,120,60
v_add_f32 v74, v7, v74
v_add_f32 v74, v8, v74
mav 28,124,60
s_waitcnt lgkmcnt(2)
v_add_f32 v74, v9, v74
v_add_f32 v74, v10, v74
mav 16,128,32
R 44,44032
v_add_f32 v74, v11, v74
mav 20,132,32
v_add_f32 v74, v12, v74
v_add_f32 v74, v13, v74
mav 24,136,32
v_fmac_f32 v75, v13, v13
v_add_f32 v74, v14, v74
mav 28,140,32
v_fmac_f32 v75, v14, v14
v_add_f32 v74, v15, v74
mav 16,144,36
R 48,45056
v_fmac_f32 v75, v15, v15
mav 20,148,36
mav 24,152,36
v_permlane32_swap_b32 v74, v75
mav 28,156,36
s_waitcnt lgkmcnt(1)
s_add_u32 s60, s60, 1
v_add_f32 v74, v74, v75
mav 16,160,40
s_cbranch_scc0 LR0_7
mav 20,164,40
R 52,46080
v_add_u32 v85, 1, v85
mav 24,168,40
v_add_u32 v72, 1, v72
mav 28,172,40
mav 16,176,44
R 56,47104
mav 20,180,44
D 83,2048
mav 24,184,44
mav 28,188,44
s_waitcnt lgkmcnt(1)
mav 16,192,48
R 60,48128
mav 20,196,48
D 83,3072
mav 24,200,48
mav 28,204,48
mav 16,208,52
mav 20,212,52
buffer_store_dwordx4 v[64:67], v91, s[36:39], s54 offen sc1
mav 24,216,52
mav 28,220,52
s_waitcnt lgkmcnt(0)
mav 16,224,56
mav 20,228,56
buffer_store_dwordx4 v[68:71], v92, s[36:39], s54 offen sc1
mav 24,232,56
mav 28,236,56
mav 16,240,60
mav 20,244,60
buffer_load_dwordx4 v[124:127], v90, s[36:39], s49 offen sc1
mav 24,248,60
ds_read_b32 v76, v82 offset:0
mav 28,252,60
s_waitcnt vmcnt(18)
mvv 16,116,164
buffer_store_dword v72, v81, s[36:39], s52 offen sc1
mvv 20,120,164
s_waitcnt vmcnt(17)
buffer_load_dwordx4 v[128:131], v90, s[36:39], s49 offen offset:1024 sc1
mvv 24,116,168
s_xor_b32 s50, s50, 0x10000
s_sub_u32 s54, s54, 32768
mvv 28,120,168
s_waitcnt vmcnt(17)
mvv 16,116,172
mvv 20,120,172
s_waitcnt vmcnt(16) lgkmcnt(0)
v_cmp_gt_i32 vcc, v85, v76
v_xor_b32 v80, 0x10000, v80
mvv 24,116,176
s_waitcnt vmcnt(15)
s_add_u32 s49, s49, 313344
mvv 28,120,176
s_cbranch_vccnz CR0_10
BR0_10:
s_barrier
mvv 16,116,180
R 32,0
mvv 20,120,180
s_waitcnt vmcnt(14)
buffer_load_dwordx4 v[132:135], v86, s[36:39], s49 offen sc1
mvv 24,116,184
mvv 28,120,184
s_waitcnt vmcnt(14)
mvv 16,116,188
R 36,1024
mvv 20,120,188
s_waitcnt vmcnt(13)
buffer_load_dwordx4 v[136:139], v86, s[36:39], s49 offen offset:1024 sc1
mvv 24,116,192
mvv 28,120,192
s_waitcnt vmcnt(13)
mvv 16,116,196
R 40,2048
mvv 20,120,196
s_waitcnt vmcnt(12)
buffer_load_dwordx4 v[140:143], v87, s[36:39], s49 offen sc1
mvv 24,116,200
mvv 28,120,200
s_waitcnt lgkmcnt(1)
v_mfma_f32_16x16x32_bf16 v[0:3], a[0:3], v[32:35], v[100:103]
R 44,3072
v_cvt_pk_bf16_f32 v64, v16, v17
v_mfma_f32_16x16x32_bf16 v[4:7], a[4:7], v[32:35], v[104:107]
buffer_load_dwordx4 v[144:147], v87, s[36:39], s49 offen offset:1024 sc1
v_mfma_f32_16x16x32_bf16 v[8:11], a[8:11], v[32:35], v[108:111]
v_cvt_pk_bf16_f32 v65, v18, v19
v_cvt_pk_bf16_f32 v66, v20, v21
v_mfma_f32_16x16x32_bf16 v[12:15], a[12:15], v[32:35], v[112:115]
v_cvt_pk_bf16_f32 v67, v22, v23
v_cvt_pk_bf16_f32 v68, v24, v25
mav 0,16,36
R 48,4096
v_cvt_pk_bf16_f32 v69, v26, v27
mav 4,20,36
buffer_load_dwordx4 v[148:151], v88, s[36:39], s49 offen sc1
mav 8,24,36
v_cvt_pk_bf16_f32 v70, v28, v29
v_cvt_pk_bf16_f32 v71, v30, v31
mav 12,28,36
s_waitcnt lgkmcnt(1)
v_mul_f32 v75, v16, v16
v_fmac_f32 v75, v17, v17
mav 0,32,40
R 52,5120
v_fmac_f32 v75, v18, v18
mav 4,36,40
buffer_load_dwordx4 v[152:155], v88, s[36:39], s49 offen offset:1024 sc1
mav 8,40,40
v_fmac_f32 v75, v19, v19
v_fmac_f32 v75, v20, v20
mav 12,44,40
v_fmac_f32 v75, v21, v21
v_fmac_f32 v75, v22, v22
mav 0,48,44
R 56,6144
v_fmac_f32 v75, v23, v23
mav 4,52,44
buffer_load_dwordx4 v[156:159], v89, s[36:39], s49 offen sc1
mav 8,56,44
v_fmac_f32 v75, v24, v24
v_fmac_f32 v75, v25, v25
mav 12,60,44
s_waitcnt lgkmcnt(1)
v_fmac_f32 v75, v26, v26
v_fmac_f32 v75, v27, v27
mav 0,64,48
R 60,7168
v_fmac_f32 v75, v28, v28
mav 4,68,48
buffer_load_dwordx4 v[160:163], v89, s[36:39], s49 offen offset:1024 sc1
mav 8,72,48
ds_write_b32 v97, v74 offset:0
mav 12,76,48
ds_write_b128 v94, v[64:67] offset:0
mav 0,80,52
s_mov_b32 m0, 144384
R 32,8192
v_add_f32 v74, v17, v16
mav 4,84,52
ds_write_b128 v95, v[68:71] offset:0
mav 8,88,52
buffer_load_dword v84, s[36:39], 0 offen sc1 lds
mav 12,92,52
s_waitcnt lgkmcnt(4)
ds_read_b128 v[64:67], v96 offset:0
s_add_u32 m0, s50, 0
mav 0,96,56
R 36,9216
v_add_f32 v74, v18, v74
mav 4,100,56
D 83,0
mav 8,104,56
ds_read_b128 v[68:71], v96 offset:1024
v_add_f32 v74, v19, v74
mav 12,108,56
v_add_f32 v74, v20, v74
v_add_f32 v74, v21, v74
mav 0,112,60
R 40,10240
v_add_f32 v74, v22, v74
mav 4,116,60
D 83,1024
mav 8,120,60
v_add_f32 v74, v23, v74
v_add_f32 v74, v24, v74
mav 12,124,60
s_waitcnt lgkmcnt(2)
v_add_f32 v74, v25, v74
v_add_f32 v74, v26, v74
mav 0,128,32
R 44,11264
v_add_f32 v74, v27, v74
mav 4,132,32
v_add_f32 v74, v28, v74
v_add_f32 v74, v29, v74
mav 8,136,32
v_fmac_f32 v75, v29, v29
v_add_f32 v74, v30, v74
mav 12,140,32
v_fmac_f32 v75, v30, v30
v_add_f32 v74, v31, v74
mav 0,144,36
R 48,12288
v_fmac_f32 v75, v31, v31
mav 4,148,36
mav 8,152,36
v_permlane32_swap_b32 v74, v75
mav 12,156,36
s_waitcnt lgkmcnt(1)
v_add_f32 v74, v74, v75
mav 0,160,40
R 52,13312
v_add_u32 v85, 1, v85
mav 4,164,40
v_add_u32 v72, 1, v72
mav 8,168,40
mav 12,172,40
mav 0,176,44
R 56,14336
mav 4,180,44
D 83,2048
mav 8,184,44
mav 12,188,44
s_waitcnt lgkmcnt(1)
mav 0,192,48
R 60,15360
mav 4,196,48
D 83,3072
mav 8,200,48
mav 12,204,48
mav 0,208,52
mav 4,212,52
buffer_store_dwordx4 v[64:67], v91, s[36:39], s54 offen sc1
mav 8,216,52
mav 12,220,52
s_waitcnt lgkmcnt(0)
mav 0,224,56
mav 4,228,56
buffer_store_dwordx4 v[68:71], v92, s[36:39], s54 offen sc1
mav 8,232,56
mav 12,236,56
mav 0,240,60
)"
R"(
mav 4,244,60
buffer_load_dwordx4 v[164:167], v90, s[36:39], s49 offen sc1
mav 8,248,60
ds_read_b32 v76, v82 offset:0
mav 12,252,60
s_waitcnt vmcnt(18)
mvv 0,116,124
buffer_store_dword v72, v81, s[36:39], s52 offen sc1
mvv 4,120,124
s_waitcnt vmcnt(17)
buffer_load_dwordx4 v[168:171], v90, s[36:39], s49 offen offset:1024 sc1
mvv 8,116,128
s_sub_u32 s54, s54, 3178496
mvv 12,120,128
s_waitcnt vmcnt(17)
mvv 0,116,132
mvv 4,120,132
s_waitcnt vmcnt(16) lgkmcnt(0)
v_cmp_gt_i32 vcc, v85, v76
mvv 8,116,136
s_waitcnt vmcnt(15)
s_add_u32 s49, s49, 313344
mvv 12,120,136
s_cbranch_vccnz CR0_11
BR0_11:
s_barrier
mvv 0,116,140
R 32,32768
mvv 4,120,140
s_waitcnt vmcnt(14)
buffer_load_dwordx4 v[172:175], v86, s[36:39], s49 offen sc1
mvv 8,116,144
LsR3:
s_cbranch_execz LsR7
mvv 12,120,144
s_waitcnt vmcnt(14)
mvv 0,116,148
R 36,33792
mvv 4,120,148
s_waitcnt vmcnt(13)
buffer_load_dwordx4 v[176:179], v86, s[36:39], s49 offen offset:1024 sc1
mvv 8,116,152
mvv 12,120,152
s_waitcnt vmcnt(13)
mvv 0,116,156
R 40,34816
mvv 4,120,156
s_waitcnt vmcnt(12)
buffer_load_dwordx4 v[180:183], v87, s[36:39], s49 offen sc1
mvv 8,116,160
mvv 12,120,160
s_waitcnt lgkmcnt(1)
v_mfma_f32_16x16x32_bf16 v[16:19], a[0:3], v[32:35], v[100:103]
R 44,35840
v_cvt_pk_bf16_f32 v64, v0, v1
v_mfma_f32_16x16x32_bf16 v[20:23], a[4:7], v[32:35], v[104:107]
buffer_load_dwordx4 v[184:187], v87, s[36:39], s49 offen offset:1024 sc1
v_mfma_f32_16x16x32_bf16 v[24:27], a[8:11], v[32:35], v[108:111]
v_cvt_pk_bf16_f32 v65, v2, v3
v_cvt_pk_bf16_f32 v66, v4, v5
v_mfma_f32_16x16x32_bf16 v[28:31], a[12:15], v[32:35], v[112:115]
v_cvt_pk_bf16_f32 v67, v6, v7
v_cvt_pk_bf16_f32 v68, v8, v9
mav 16,16,36
R 48,36864
v_cvt_pk_bf16_f32 v69, v10, v11
mav 20,20,36
buffer_load_dwordx4 v[188:191], v88, s[36:39], s49 offen sc1
mav 24,24,36
v_cvt_pk_bf16_f32 v70, v12, v13
v_cvt_pk_bf16_f32 v71, v14, v15
mav 28,28,36
s_waitcnt lgkmcnt(1)
mav 16,32,40
R 52,37888
mav 20,36,40
buffer_load_dwordx4 v[192:195], v88, s[36:39], s49 offen offset:1024 sc1
mav 24,40,40
mav 28,44,40
mav 16,48,44
R 56,38912
mav 20,52,44
buffer_load_dwordx4 v[196:199], v89, s[36:39], s49 offen sc1
mav 24,56,44
mav 28,60,44
s_waitcnt lgkmcnt(1)
mav 16,64,48
R 60,39936
mav 20,68,48
buffer_load_dwordx4 v[200:203], v89, s[36:39], s49 offen offset:1024 sc1
mav 24,72,48
ds_write_b32 v97, v74 offset:1024
mav 28,76,48
ds_write_b128 v94, v[64:67] offset:0
mav 16,80,52
s_mov_b32 m0, 144384
R 32,40960
mav 20,84,52
ds_write_b128 v95, v[68:71] offset:0
mav 24,88,52
buffer_load_dword v84, s[36:39], 0 offen sc1 lds
mav 28,92,52
s_waitcnt lgkmcnt(4)
ds_read_b128 v[64:67], v96 offset:0
s_add_u32 m0, s50, 32768
mav 16,96,56
R 36,41984
mav 20,100,56
D 83,0
mav 24,104,56
ds_read_b128 v[68:71], v96 offset:1024
mav 28,108,56
mav 16,112,60
R 40,43008
mav 20,116,60
D 83,1024
mav 24,120,60
mav 28,124,60
s_waitcnt lgkmcnt(2)
mav 16,128,32
R 44,44032
mav 20,132,32
mav 24,136,32
mav 28,140,32
mav 16,144,36
R 48,45056
mav 20,148,36
mav 24,152,36
mav 28,156,36
s_waitcnt lgkmcnt(1)
mav 16,160,40
mav 20,164,40
R 52,46080
v_add_u32 v85, 1, v85
mav 24,168,40
v_add_u32 v72, 69, v72
mav 28,172,40
mav 16,176,44
R 56,47104
mav 20,180,44
D 83,2048
mav 24,184,44
mav 28,188,44
s_waitcnt lgkmcnt(1)
mav 16,192,48
R 60,48128
mav 20,196,48
D 83,3072
mav 24,200,48
mav 28,204,48
mav 16,208,52
mav 20,212,52
buffer_store_dwordx4 v[64:67], v91, s[44:47], s54 offen sc1
mav 24,216,52
mav 28,220,52
s_waitcnt lgkmcnt(0)
mav 16,224,56
mav 20,228,56
buffer_store_dwordx4 v[68:71], v92, s[44:47], s54 offen sc1
mav 24,232,56
mav 28,236,56
mav 16,240,60
mav 20,244,60
buffer_load_dwordx4 v[124:127], v90, s[36:39], s49 offen sc1
mav 24,248,60
ds_read_b32 v76, v82 offset:0
mav 28,252,60
s_waitcnt vmcnt(18)
mvv 16,116,164
buffer_store_dword v72, v81, s[36:39], s52 offen sc1
mvv 20,120,164
s_waitcnt vmcnt(17)
buffer_load_dwordx4 v[128:131], v90, s[36:39], s49 offen offset:1024 sc1
mvv 24,116,168
s_xor_b32 s50, s50, 0x10000
s_sub_u32 s54, s54, 32768
mvv 28,120,168
s_waitcnt vmcnt(17)
mvv 16,116,172
mvv 20,120,172
s_waitcnt vmcnt(16) lgkmcnt(0)
v_cmp_gt_i32 vcc, v85, v76
v_xor_b32 v80, 0x10000, v80
mvv 24,116,176
s_waitcnt vmcnt(15)
s_add_u32 s49, s49, 313344
mvv 28,120,176
s_cbranch_vccnz CR0_12
BR0_12:
s_barrier
mvv 16,116,180
R 32,0
mvv 20,120,180
s_waitcnt vmcnt(14)
buffer_load_dwordx4 v[132:135], v86, s[36:39], s49 offen sc1
mvv 24,116,184
mvv 28,120,184
s_waitcnt vmcnt(14)
mvv 16,116,188
R 36,1024
mvv 20,120,188
s_waitcnt vmcnt(13)
buffer_load_dwordx4 v[136:139], v86, s[36:39], s49 offen offset:1024 sc1
mvv 24,116,192
mvv 28,120,192
s_waitcnt vmcnt(13)
mvv 16,116,196
R 40,2048
mvv 20,120,196
s_waitcnt vmcnt(12)
buffer_load_dwordx4 v[140:143], v87, s[36:39], s49 offen sc1
mvv 24,116,200
mvv 28,120,200
s_waitcnt lgkmcnt(1)
v_mfma_f32_16x16x32_bf16 v[0:3], a[0:3], v[32:35], v[100:103]
R 44,3072
v_cvt_pk_bf16_f32 v64, v16, v17
v_mfma_f32_16x16x32_bf16 v[4:7], a[4:7], v[32:35], v[104:107]
buffer_load_dwordx4 v[144:147], v87, s[36:39], s49 offen offset:1024 sc1
v_mfma_f32_16x16x32_bf16 v[8:11], a[8:11], v[32:35], v[108:111]
v_cvt_pk_bf16_f32 v65, v18, v19
v_cvt_pk_bf16_f32 v66, v20, v21
v_mfma_f32_16x16x32_bf16 v[12:15], a[12:15], v[32:35], v[112:115]
v_cvt_pk_bf16_f32 v67, v22, v23
v_cvt_pk_bf16_f32 v68, v24, v25
mav 0,16,36
R 48,4096
v_cvt_pk_bf16_f32 v69, v26, v27
mav 4,20,36
buffer_load_dwordx4 v[148:151], v88, s[36:39], s49 offen sc1
mav 8,24,36
v_cvt_pk_bf16_f32 v70, v28, v29
v_cvt_pk_bf16_f32 v71, v30, v31
mav 12,28,36
s_waitcnt lgkmcnt(1)
mav 0,32,40
R 52,5120
mav 4,36,40
buffer_load_dwordx4 v[152:155], v88, s[36:39], s49 offen offset:1024 sc1
mav 8,40,40
mav 12,44,40
mav 0,48,44
R 56,6144
mav 4,52,44
buffer_load_dwordx4 v[156:159], v89, s[36:39], s49 offen sc1
mav 8,56,44
mav 12,60,44
s_waitcnt lgkmcnt(1)
mav 0,64,48
R 60,7168
mav 4,68,48
buffer_load_dwordx4 v[160:163], v89, s[36:39], s49 offen offset:1024 sc1
mav 8,72,48
mav 12,76,48
ds_write_b128 v94, v[64:67] offset:0
mav 0,80,52
s_mov_b32 m0, 144384
R 32,8192
mav 4,84,52
ds_write_b128 v95, v[68:71] offset:0
mav 8,88,52
buffer_load_dword v84, s[36:39], 0 offen sc1 lds
mav 12,92,52
s_waitcnt lgkmcnt(3)
ds_read_b128 v[64:67], v96 offset:0
s_add_u32 m0, s50, 0
mav 0,96,56
R 36,9216
mav 4,100,56
D 83,0
mav 8,104,56
ds_read_b128 v[68:71], v96 offset:1024
mav 12,108,56
mav 0,112,60
R 40,10240
mav 4,116,60
D 83,1024
mav 8,120,60
mav 12,124,60
s_waitcnt lgkmcnt(2)
mav 0,128,32
R 44,11264
mav 4,132,32
mav 8,136,32
mav 12,140,32
mav 0,144,36
R 48,12288
mav 4,148,36
mav 8,152,36
mav 12,156,36
s_waitcnt lgkmcnt(1)
mav 0,160,40
R 52,13312
v_add_u32 v85, 1, v85
mav 4,164,40
mav 8,168,40
mav 12,172,40
mav 0,176,44
R 56,14336
mav 4,180,44
D 83,2048
mav 8,184,44
mav 12,188,44
s_waitcnt lgkmcnt(1)
mav 0,192,48
R 60,15360
mav 4,196,48
D 83,3072
mav 8,200,48
mav 12,204,48
mav 0,208,52
mav 4,212,52
buffer_store_dwordx4 v[64:67], v91, s[44:47], s54 offen sc1
mav 8,216,52
mav 12,220,52
s_waitcnt lgkmcnt(0)
mav 0,224,56
mav 4,228,56
buffer_store_dwordx4 v[68:71], v92, s[44:47], s54 offen sc1
mav 8,232,56
mav 12,236,56
mav 0,240,60
mav 4,244,60
buffer_load_dwordx4 v[164:167], v90, s[36:39], s49 offen sc1
mav 8,248,60
ds_read_b32 v76, v82 offset:0
mav 12,252,60
s_waitcnt vmcnt(18)
mvv 0,116,124
mvv 4,120,124
s_waitcnt vmcnt(16)
buffer_load_dwordx4 v[168:171], v90, s[36:39], s49 offen offset:1024 sc1
mvv 8,116,128
s_sub_u32 s54, s54, 32768
mvv 12,120,128
s_waitcnt vmcnt(16)
mvv 0,116,132
mvv 4,120,132
s_waitcnt vmcnt(15) lgkmcnt(0)
v_cmp_gt_i32 vcc, v85, v76
mvv 8,116,136
s_waitcnt vmcnt(14)
s_add_u32 s49, s49, 313344
mvv 12,120,136
s_cbranch_vccnz CR0_13
BR0_13:
s_barrier
mvv 0,116,140
R 32,32768
mvv 4,120,140
s_waitcnt vmcnt(13)
buffer_load_dwordx4 v[172:175], v86, s[36:39], s49 offen sc1
mvv 8,116,144
mvv 12,120,144
s_waitcnt vmcnt(13)
mvv 0,116,148
R 36,33792
mvv 4,120,148
s_waitcnt vmcnt(12)
buffer_load_dwordx4 v[176:179], v86, s[36:39], s49 offen offset:1024 sc1
mvv 8,116,152
mvv 12,120,152
s_waitcnt vmcnt(12)
mvv 0,116,156
R 40,34816
mvv 4,120,156
s_waitcnt vmcnt(11)
buffer_load_dwordx4 v[180:183], v87, s[36:39], s49 offen sc1
mvv 8,116,160
mvv 12,120,160
s_waitcnt lgkmcnt(1)
v_mfma_f32_16x16x32_bf16 v[16:19], a[0:3], v[32:35], v[100:103]
R 44,35840
v_cvt_pk_bf16_f32 v64, v0, v1
v_mfma_f32_16x16x32_bf16 v[20:23], a[4:7], v[32:35], v[104:107]
buffer_load_dwordx4 v[184:187], v87, s[36:39], s49 offen offset:1024 sc1
v_mfma_f32_16x16x32_bf16 v[24:27], a[8:11], v[32:35], v[108:111]
v_cvt_pk_bf16_f32 v65, v2, v3
v_cvt_pk_bf16_f32 v66, v4, v5
v_mfma_f32_16x16x32_bf16 v[28:31], a[12:15], v[32:35], v[112:115]
v_cvt_pk_bf16_f32 v67, v6, v7
v_cvt_pk_bf16_f32 v68, v8, v9
mav 16,16,36
R 48,36864
v_cvt_pk_bf16_f32 v69, v10, v11
mav 20,20,36
buffer_load_dwordx4 v[188:191], v88, s[36:39], s49 offen sc1
mav 24,24,36
v_cvt_pk_bf16_f32 v70, v12, v13
v_cvt_pk_bf16_f32 v71, v14, v15
mav 28,28,36
s_waitcnt lgkmcnt(1)
mav 16,32,40
R 52,37888
mav 20,36,40
buffer_load_dwordx4 v[192:195], v88, s[36:39], s49 offen offset:1024 sc1
mav 24,40,40
mav 28,44,40
mav 16,48,44
R 56,38912
mav 20,52,44
buffer_load_dwordx4 v[196:199], v89, s[36:39], s49 offen sc1
mav 24,56,44
mav 28,60,44
s_waitcnt lgkmcnt(1)
mav 16,64,48
R 60,39936
mav 20,68,48
buffer_load_dwordx4 v[200:203], v89, s[36:39], s49 offen offset:1024 sc1
mav 24,72,48
mav 28,76,48
ds_write_b128 v94, v[64:67] offset:0
mav 16,80,52
s_mov_b32 m0, 144384
R 32,40960
mav 20,84,52
ds_write_b128 v95, v[68:71] offset:0
mav 24,88,52
buffer_load_dword v84, s[36:39], 0 offen sc1 lds
mav 28,92,52
s_waitcnt lgkmcnt(3)
ds_read_b128 v[64:67], v96 offset:0
s_add_u32 m0, s50, 32768
mav 16,96,56
R 36,41984
mav 20,100,56
D 83,0
mav 24,104,56
ds_read_b128 v[68:71], v96 offset:1024
mav 28,108,56
mav 16,112,60
R 40,43008
mav 20,116,60
D 83,1024
mav 24,120,60
mav 28,124,60
s_waitcnt lgkmcnt(2)
mav 16,128,32
R 44,44032
mav 20,132,32
mav 24,136,32
mav 28,140,32
mav 16,144,36
R 48,45056
mav 20,148,36
mav 24,152,36
mav 28,156,36
s_waitcnt lgkmcnt(1)
mav 16,160,40
s_mov_b32 s60, -13
.p2align 6
LR0_14:
mav 20,164,40
R 52,46080
v_add_u32 v85, 1, v85
mav 24,168,40
mav 28,172,40
mav 16,176,44
R 56,47104
mav 20,180,44
D 83,2048
mav 24,184,44
mav 28,188,44
s_waitcnt lgkmcnt(1)
mav 16,192,48
R 60,48128
mav 20,196,48
D 83,3072
mav 24,200,48
mav 28,204,48
mav 16,208,52
mav 20,212,52
buffer_store_dwordx4 v[64:67], v91, s[44:47], s54 offen sc1
mav 24,216,52
mav 28,220,52
s_waitcnt lgkmcnt(0)
mav 16,224,56
mav 20,228,56
buffer_store_dwordx4 v[68:71], v92, s[44:47], s54 offen sc1
mav 24,232,56
mav 28,236,56
mav 16,240,60
mav 20,244,60
buffer_load_dwordx4 v[124:127], v90, s[36:39], s49 offen sc1
mav 24,248,60
ds_read_b32 v76, v82 offset:0
mav 28,252,60
s_waitcnt vmcnt(17)
mvv 16,116,164
mvv 20,120,164
s_waitcnt vmcnt(16)
buffer_load_dwordx4 v[128:131], v90, s[36:39], s49 offen offset:1024 sc1
mvv 24,116,168
s_xor_b32 s50, s50, 0x10000
s_sub_u32 s54, s54, 32768
mvv 28,120,168
s_waitcnt vmcnt(16)
mvv 16,116,172
mvv 20,120,172
s_waitcnt vmcnt(15) lgkmcnt(0)
v_cmp_gt_i32 vcc, v85, v76
v_xor_b32 v80, 0x10000, v80
mvv 24,116,176
s_waitcnt vmcnt(14)
s_add_u32 s49, s49, 313344
mvv 28,120,176
s_cbranch_vccnz CR0_15
BR0_15:
s_barrier
mvv 16,116,180
R 32,0
mvv 20,120,180
s_waitcnt vmcnt(13)
buffer_load_dwordx4 v[132:135], v86, s[36:39], s49 offen sc1
mvv 24,116,184
mvv 28,120,184
s_waitcnt vmcnt(13)
mvv 16,116,188
R 36,1024
mvv 20,120,188
s_waitcnt vmcnt(12)
buffer_load_dwordx4 v[136:139], v86, s[36:39], s49 offen offset:1024 sc1
mvv 24,116,192
mvv 28,120,192
s_waitcnt vmcnt(12)
mvv 16,116,196
R 40,2048
mvv 20,120,196
s_waitcnt vmcnt(11)
buffer_load_dwordx4 v[140:143], v87, s[36:39], s49 offen sc1
mvv 24,116,200
mvv 28,120,200
s_waitcnt lgkmcnt(1)
v_mfma_f32_16x16x32_bf16 v[0:3], a[0:3], v[32:35], v[100:103]
R 44,3072
v_cvt_pk_bf16_f32 v64, v16, v17
v_mfma_f32_16x16x32_bf16 v[4:7], a[4:7], v[32:35], v[104:107]
buffer_load_dwordx4 v[144:147], v87, s[36:39], s49 offen offset:1024 sc1
v_mfma_f32_16x16x32_bf16 v[8:11], a[8:11], v[32:35], v[108:111]
v_cvt_pk_bf16_f32 v65, v18, v19
v_cvt_pk_bf16_f32 v66, v20, v21
v_mfma_f32_16x16x32_bf16 v[12:15], a[12:15], v[32:35], v[112:115]
v_cvt_pk_bf16_f32 v67, v22, v23
v_cvt_pk_bf16_f32 v68, v24, v25
mav 0,16,36
R 48,4096
v_cvt_pk_bf16_f32 v69, v26, v27
mav 4,20,36
buffer_load_dwordx4 v[148:151], v88, s[36:39], s49 offen sc1
mav 8,24,36
v_cvt_pk_bf16_f32 v70, v28, v29
v_cvt_pk_bf16_f32 v71, v30, v31
mav 12,28,36
s_waitcnt lgkmcnt(1)
mav 0,32,40
R 52,5120
mav 4,36,40
buffer_load_dwordx4 v[152:155], v88, s[36:39], s49 offen offset:1024 sc1
mav 8,40,40
mav 12,44,40
mav 0,48,44
R 56,6144
mav 4,52,44
buffer_load_dwordx4 v[156:159], v89, s[36:39], s49 offen sc1
mav 8,56,44
mav 12,60,44
s_waitcnt lgkmcnt(1)
mav 0,64,48
R 60,7168
mav 4,68,48
buffer_load_dwordx4 v[160:163], v89, s[36:39], s49 offen offset:1024 sc1
mav 8,72,48
mav 12,76,48
ds_write_b128 v94, v[64:67] offset:0
mav 0,80,52
s_mov_b32 m0, 144384
R 32,8192
mav 4,84,52
ds_write_b128 v95, v[68:71] offset:0
mav 8,88,52
buffer_load_dword v84, s[36:39], 0 offen sc1 lds
mav 12,92,52
s_waitcnt lgkmcnt(3)
LsR4:
s_cbranch_execz LsR8
ds_read_b128 v[64:67], v96 offset:0
s_add_u32 m0, s50, 0
mav 0,96,56
R 36,9216
mav 4,100,56
D 83,0
mav 8,104,56
ds_read_b128 v[68:71], v96 offset:1024
mav 12,108,56
mav 0,112,60
R 40,10240
mav 4,116,60
D 83,1024
mav 8,120,60
mav 12,124,60
s_waitcnt lgkmcnt(2)
mav 0,128,32
R 44,11264
mav 4,132,32
mav 8,136,32
mav 12,140,32
mav 0,144,36
R 48,12288
mav 4,148,36
mav 8,152,36
mav 12,156,36
s_waitcnt lgkmcnt(1)
mav 0,160,40
R 52,13312
v_add_u32 v85, 1, v85
mav 4,164,40
mav 8,168,40
mav 12,172,40
mav 0,176,44
R 56,14336
mav 4,180,44
D 83,2048
mav 8,184,44
mav 12,188,44
s_waitcnt lgkmcnt(1)
mav 0,192,48
R 60,15360
mav 4,196,48
D 83,3072
mav 8,200,48
mav 12,204,48
mav 0,208,52
mav 4,212,52
buffer_store_dwordx4 v[64:67], v91, s[44:47], s54 offen sc1
mav 8,216,52
mav 12,220,52
s_waitcnt lgkmcnt(0)
mav 0,224,56
mav 4,228,56
buffer_store_dwordx4 v[68:71], v92, s[44:47], s54 offen sc1
mav 8,232,56
mav 12,236,56
mav 0,240,60
mav 4,244,60
buffer_load_dwordx4 v[164:167], v90, s[36:39], s49 offen sc1
mav 8,248,60
ds_read_b32 v76, v82 offset:0
mav 12,252,60
s_waitcnt vmcnt(17)
mvv 0,116,124
mvv 4,120,124
s_waitcnt vmcnt(16)
buffer_load_dwordx4 v[168:171], v90, s[36:39], s49 offen offset:1024 sc1
mvv 8,116,128
s_sub_u32 s54, s54, 32768
mvv 12,120,128
s_waitcnt vmcnt(16)
mvv 0,116,132
mvv 4,120,132
s_waitcnt vmcnt(15) lgkmcnt(0)
v_cmp_gt_i32 vcc, v85, v76
mvv 8,116,136
s_waitcnt vmcnt(14)
s_add_u32 s49, s49, 313344
mvv 12,120,136
s_cbranch_vccnz CR0_16
BR0_16:
s_barrier
mvv 0,116,140
R 32,32768
mvv 4,120,140
s_waitcnt vmcnt(13)
buffer_load_dwordx4 v[172:175], v86, s[36:39], s49 offen sc1
mvv 8,116,144
mvv 12,120,144
s_waitcnt vmcnt(13)
mvv 0,116,148
R 36,33792
mvv 4,120,148
s_waitcnt vmcnt(12)
buffer_load_dwordx4 v[176:179], v86, s[36:39], s49 offen offset:1024 sc1
mvv 8,116,152
mvv 12,120,152
s_waitcnt vmcnt(12)
mvv 0,116,156
R 40,34816
mvv 4,120,156
s_waitcnt vmcnt(11)
buffer_load_dwordx4 v[180:183], v87, s[36:39], s49 offen sc1
mvv 8,116,160
mvv 12,120,160
s_waitcnt lgkmcnt(1)
v_mfma_f32_16x16x32_bf16 v[16:19], a[0:3], v[32:35], v[100:103]
R 44,35840
v_cvt_pk_bf16_f32 v64, v0, v1
v_mfma_f32_16x16x32_bf16 v[20:23], a[4:7], v[32:35], v[104:107]
buffer_load_dwordx4 v[184:187], v87, s[36:39], s49 offen offset:1024 sc1
v_mfma_f32_16x16x32_bf16 v[24:27], a[8:11], v[32:35], v[108:111]
v_cvt_pk_bf16_f32 v65, v2, v3
v_cvt_pk_bf16_f32 v66, v4, v5
v_mfma_f32_16x16x32_bf16 v[28:31], a[12:15], v[32:35], v[112:115]
v_cvt_pk_bf16_f32 v67, v6, v7
v_cvt_pk_bf16_f32 v68, v8, v9
mav 16,16,36
R 48,36864
v_cvt_pk_bf16_f32 v69, v10, v11
mav 20,20,36
buffer_load_dwordx4 v[188:191], v88, s[36:39], s49 offen sc1
mav 24,24,36
v_cvt_pk_bf16_f32 v70, v12, v13
v_cvt_pk_bf16_f32 v71, v14, v15
mav 28,28,36
s_waitcnt lgkmcnt(1)
mav 16,32,40
R 52,37888
mav 20,36,40
buffer_load_dwordx4 v[192:195], v88, s[36:39], s49 offen offset:1024 sc1
mav 24,40,40
mav 28,44,40
mav 16,48,44
R 56,38912
mav 20,52,44
buffer_load_dwordx4 v[196:199], v89, s[36:39], s49 offen sc1
mav 24,56,44
mav 28,60,44
s_waitcnt lgkmcnt(1)
mav 16,64,48
R 60,39936
mav 20,68,48
buffer_load_dwordx4 v[200:203], v89, s[36:39], s49 offen offset:1024 sc1
mav 24,72,48
mav 28,76,48
ds_write_b128 v94, v[64:67] offset:0
mav 16,80,52
s_mov_b32 m0, 144384
R 32,40960
mav 20,84,52
ds_write_b128 v95, v[68:71] offset:0
mav 24,88,52
buffer_load_dword v84, s[36:39], 0 offen sc1 lds
mav 28,92,52
s_waitcnt lgkmcnt(3)
ds_read_b128 v[64:67], v96 offset:0
s_add_u32 m0, s50, 32768
mav 16,96,56
R 36,41984
mav 20,100,56
D 83,0
mav 24,104,56
ds_read_b128 v[68:71], v96 offset:1024
mav 28,108,56
mav 16,112,60
R 40,43008
mav 20,116,60
D 83,1024
mav 24,120,60
mav 28,124,60
s_waitcnt lgkmcnt(2)
mav 16,128,32
R 44,44032
mav 20,132,32
mav 24,136,32
mav 28,140,32
mav 16,144,36
R 48,45056
mav 20,148,36
mav 24,152,36
mav 28,156,36
s_waitcnt lgkmcnt(1)
s_add_u32 s60, s60, 1
mav 16,160,40
s_cbranch_scc0 LR0_14
mav 20,164,40
R 52,46080
v_add_u32 v85, 1, v85
mav 24,168,40
mav 28,172,40
mav 16,176,44
R 56,47104
mav 20,180,44
D 83,2048
mav 24,184,44
mav 28,188,44
s_waitcnt lgkmcnt(1)
mav 16,192,48
R 60,48128
mav 20,196,48
D 83,3072
mav 24,200,48
mav 28,204,48
mav 16,208,52
mav 20,212,52
buffer_store_dwordx4 v[64:67], v91, s[44:47], s54 offen sc1
mav 24,216,52
mav 28,220,52
s_waitcnt lgkmcnt(0)
mav 16,224,56
mav 20,228,56
buffer_store_dwordx4 v[68:71], v92, s[44:47], s54 offen sc1
mav 24,232,56
mav 28,236,56
mav 16,240,60
mav 20,244,60
buffer_load_dwordx4 v[124:127], v90, s[36:39], s49 offen sc1
mav 24,248,60
ds_read_b32 v76, v82 offset:0
mav 28,252,60
s_waitcnt vmcnt(17)
mvv 16,116,164
mvv 20,120,164
s_waitcnt vmcnt(16)
buffer_load_dwordx4 v[128:131], v90, s[36:39], s49 offen offset:1024 sc1
mvv 24,116,168
s_sub_u32 s54, s54, 32768
mvv 28,120,168
s_waitcnt vmcnt(16)
mvv 16,116,172
mvv 20,120,172
s_waitcnt vmcnt(15) lgkmcnt(0)
v_cmp_gt_i32 vcc, v85, v76
v_xor_b32 v80, 0x10000, v80
mvv 24,116,176
s_waitcnt vmcnt(14)
s_add_u32 s49, s49, 313344
mvv 28,120,176
s_cbranch_vccnz CR0_17
BR0_17:
s_barrier
mvv 16,116,180
R 32,0
mvv 20,120,180
s_waitcnt vmcnt(13)
buffer_load_dwordx4 v[132:135], v86, s[36:39], s49 offen sc1
mvv 24,116,184
mvv 28,120,184
s_waitcnt vmcnt(13)
mvv 16,116,188
R 36,1024
mvv 20,120,188
s_waitcnt vmcnt(12)
buffer_load_dwordx4 v[136:139], v86, s[36:39], s49 offen offset:1024 sc1
mvv 24,116,192
mvv 28,120,192
s_waitcnt vmcnt(12)
mvv 16,116,196
R 40,2048
mvv 20,120,196
s_waitcnt vmcnt(11)
buffer_load_dwordx4 v[140:143], v87, s[36:39], s49 offen sc1
mvv 24,116,200
mvv 28,120,200
s_waitcnt lgkmcnt(1)
v_mfma_f32_16x16x32_bf16 v[0:3], a[0:3], v[32:35], v[100:103]
R 44,3072
v_cvt_pk_bf16_f32 v64, v16, v17
v_mfma_f32_16x16x32_bf16 v[4:7], a[4:7], v[32:35], v[104:107]
buffer_load_dwordx4 v[144:147], v87, s[36:39], s49 offen offset:1024 sc1
v_mfma_f32_16x16x32_bf16 v[8:11], a[8:11], v[32:35], v[108:111]
v_cvt_pk_bf16_f32 v65, v18, v19
v_cvt_pk_bf16_f32 v66, v20, v21
v_mfma_f32_16x16x32_bf16 v[12:15], a[12:15], v[32:35], v[112:115]
v_cvt_pk_bf16_f32 v67, v22, v23
v_cvt_pk_bf16_f32 v68, v24, v25
mav 0,16,36
R 48,4096
v_cvt_pk_bf16_f32 v69, v26, v27
mav 4,20,36
buffer_load_dwordx4 v[148:151], v88, s[36:39], s49 offen sc1
mav 8,24,36
v_cvt_pk_bf16_f32 v70, v28, v29
v_cvt_pk_bf16_f32 v71, v30, v31
mav 12,28,36
s_waitcnt lgkmcnt(1)
mav 0,32,40
R 52,5120
mav 4,36,40
buffer_load_dwordx4 v[152:155], v88, s[36:39], s49 offen offset:1024 sc1
mav 8,40,40
mav 12,44,40
mav 0,48,44
R 56,6144
mav 4,52,44
buffer_load_dwordx4 v[156:159], v89, s[36:39], s49 offen sc1
mav 8,56,44
mav 12,60,44
s_waitcnt lgkmcnt(1)
mav 0,64,48
R 60,7168
mav 4,68,48
buffer_load_dwordx4 v[160:163], v89, s[36:39], s49 offen offset:1024 sc1
mav 8,72,48
mav 12,76,48
ds_write_b128 v94, v[64:67] offset:0
mav 0,80,52
s_mov_b32 m0, 144384
R 32,8192
mav 4,84,52
ds_write_b128 v95, v[68:71] offset:0
mav 8,88,52
buffer_load_dword v84, s[36:39], 0 offen sc1 lds
mav 12,92,52
s_waitcnt lgkmcnt(3)
ds_read_b128 v[64:67], v96 offset:0
mav 0,96,56
R 36,9216
mav 4,100,56
ds_read_b128 v[68:71], v96 offset:1024
mav 8,104,56
mav 12,108,56
mav 0,112,60
R 40,10240
mav 4,116,60
mav 8,120,60
mav 12,124,60
s_waitcnt lgkmcnt(1)
mav 0,128,32
R 44,11264
mav 4,132,32
mav 8,136,32
mav 12,140,32
mav 0,144,36
R 48,12288
mav 4,148,36
mav 8,152,36
mav 12,156,36
s_waitcnt lgkmcnt(1)
mav 0,160,40
R 52,13312
v_add_u32 v85, 1, v85
mav 4,164,40
mav 8,168,40
mav 12,172,40
mav 0,176,44
R 56,14336
mav 4,180,44
mav 8,184,44
mav 12,188,44
s_waitcnt lgkmcnt(1)
mav 0,192,48
R 60,15360
mav 4,196,48
mav 8,200,48
mav 12,204,48
mav 0,208,52
mav 4,212,52
buffer_store_dwordx4 v[64:67], v91, s[44:47], s54 offen sc1
mav 8,216,52
mav 12,220,52
s_waitcnt lgkmcnt(0)
mav 0,224,56
mav 4,228,56
buffer_store_dwordx4 v[68:71], v92, s[44:47], s54 offen sc1
mav 8,232,56
mav 12,236,56
mav 0,240,60
mav 4,244,60
buffer_load_dwordx4 v[164:167], v90, s[36:39], s49 offen sc1
mav 8,248,60
ds_read_b32 v76, v82 offset:0
mav 12,252,60
s_waitcnt vmcnt(13)
mvv 0,116,124
mvv 4,120,124
s_waitcnt vmcnt(12)
buffer_load_dwordx4 v[168:171], v90, s[36:39], s49 offen offset:1024 sc1
mvv 8,116,128
s_sub_u32 s54, s54, 32768
mvv 12,120,128
s_waitcnt vmcnt(12)
mvv 0,116,132
mvv 4,120,132
s_waitcnt vmcnt(11) lgkmcnt(0)
v_cmp_gt_i32 vcc, v85, v76
mvv 8,116,136
s_waitcnt vmcnt(10)
s_add_u32 s49, s49, 313344
mvv 12,120,136
s_cbranch_vccnz CR0_18
BR0_18:
s_barrier
mvv 0,116,140
R 32,32768
mvv 4,120,140
s_waitcnt vmcnt(9)
buffer_load_dwordx4 v[172:175], v86, s[36:39], s49 offen sc1
mvv 8,116,144
mvv 12,120,144
s_waitcnt vmcnt(9)
mvv 0,116,148
R 36,33792
mvv 4,120,148
s_waitcnt vmcnt(8)
buffer_load_dwordx4 v[176:179], v86, s[36:39], s49 offen offset:1024 sc1
mvv 8,116,152
mvv 12,120,152
s_waitcnt vmcnt(8)
mvv 0,116,156
R 40,34816
mvv 4,120,156
s_waitcnt vmcnt(7)
buffer_load_dwordx4 v[180:183], v87, s[36:39], s49 offen sc1
mvv 8,116,160
mvv 12,120,160
s_waitcnt lgkmcnt(1)
v_mfma_f32_16x16x32_bf16 v[16:19], a[0:3], v[32:35], v[100:103]
R 44,35840
v_cvt_pk_bf16_f32 v64, v0, v1
v_mfma_f32_16x16x32_bf16 v[20:23], a[4:7], v[32:35], v[104:107]
buffer_load_dwordx4 v[184:187], v87, s[36:39], s49 offen offset:1024 sc1
v_mfma_f32_16x16x32_bf16 v[24:27], a[8:11], v[32:35], v[108:111]
v_cvt_pk_bf16_f32 v65, v2, v3
v_cvt_pk_bf16_f32 v66, v4, v5
v_mfma_f32_16x16x32_bf16 v[28:31], a[12:15], v[32:35], v[112:115]
v_cvt_pk_bf16_f32 v67, v6, v7
v_cvt_pk_bf16_f32 v68, v8, v9
mav 16,16,36
R 48,36864
v_cvt_pk_bf16_f32 v69, v10, v11
mav 20,20,36
buffer_load_dwordx4 v[188:191], v88, s[36:39], s49 offen sc1
mav 24,24,36
ds_write_b128 v94, v[64:67] offset:0
mav 28,28,36
s_waitcnt lgkmcnt(2)
v_cvt_pk_bf16_f32 v70, v12, v13
v_cvt_pk_bf16_f32 v71, v14, v15
mav 16,32,40
R 52,37888
mav 20,36,40
buffer_load_dwordx4 v[192:195], v88, s[36:39], s49 offen offset:1024 sc1
mav 24,40,40
ds_write_b128 v95, v[68:71] offset:0
mav 28,44,40
ds_read_b128 v[64:67], v96 offset:0
mav 16,48,44
R 56,38912
mav 20,52,44
buffer_load_dwordx4 v[196:199], v89, s[36:39], s49 offen sc1
mav 24,56,44
ds_read_b128 v[68:71], v96 offset:1024
mav 28,60,44
s_waitcnt lgkmcnt(4)
mav 16,64,48
R 60,39936
mav 20,68,48
buffer_load_dwordx4 v[200:203], v89, s[36:39], s49 offen offset:1024 sc1
mav 24,72,48
s_waitcnt lgkmcnt(2)
mav 28,76,48
mav 16,80,52
R 32,40960
mav 20,84,52
buffer_store_dwordx4 v[64:67], v91, s[44:47], s54 offen sc1
s_waitcnt lgkmcnt(2)
mav 24,88,52
mav 28,92,52
mav 16,96,56
R 36,41984
mav 20,100,56
buffer_store_dwordx4 v[68:71], v92, s[44:47], s54 offen sc1
mav 24,104,56
mav 28,108,56
s_waitcnt lgkmcnt(1)
mav 16,112,60
R 40,43008
mav 20,116,60
mav 24,120,60
mav 28,124,60
mav 16,128,32
R 44,44032
mav 20,132,32
mav 24,136,32
mav 28,140,32
s_waitcnt lgkmcnt(1)
mav 16,144,36
R 48,45056
mav 20,148,36
mav 24,152,36
mav 28,156,36
mav 16,160,40
mav 20,164,40
R 52,46080
mav 24,168,40
mav 28,172,40
s_waitcnt lgkmcnt(1)
mav 16,176,44
R 56,47104
mav 20,180,44
mav 24,184,44
mav 28,188,44
mav 16,192,48
R 60,48128
mav 20,196,48
mav 24,200,48
mav 28,204,48
s_waitcnt lgkmcnt(1)
mav 16,208,52
mav 20,212,52
mav 24,216,52
mav 28,220,52
mav 16,224,56
mav 20,228,56
mav 24,232,56
mav 28,236,56
s_waitcnt lgkmcnt(0)
mav 16,240,60
mav 20,244,60
mav 24,248,60
mav 28,252,60
s_waitcnt vmcnt(11)
mvv 16,116,164
mvv 20,120,164
s_waitcnt vmcnt(10)
mvv 24,116,168
s_sub_u32 s54, s54, 32768
mvv 28,120,168
s_waitcnt vmcnt(9)
mvv 16,116,172
mvv 20,120,172
s_waitcnt vmcnt(8)
mvv 24,116,176
mvv 28,120,176
s_waitcnt vmcnt(7)
mvv 16,116,180
mvv 20,120,180
s_waitcnt vmcnt(6)
mvv 24,116,184
mvv 28,120,184
s_waitcnt vmcnt(5)
mvv 16,116,188
mvv 20,120,188
s_waitcnt vmcnt(4)
mvv 24,116,192
mvv 28,120,192
s_waitcnt vmcnt(3)
mvv 16,116,196
mvv 20,120,196
s_waitcnt vmcnt(2)
mvv 24,116,200
mvv 28,120,200
s_nop 3
v_cvt_pk_bf16_f32 v64, v16, v17
v_cvt_pk_bf16_f32 v65, v18, v19
LsR5:
s_cbranch_execz LsR9
v_cvt_pk_bf16_f32 v66, v20, v21
v_cvt_pk_bf16_f32 v67, v22, v23
ds_write_b128 v94, v[64:67] offset:0
v_cvt_pk_bf16_f32 v68, v24, v25
v_cvt_pk_bf16_f32 v69, v26, v27
v_cvt_pk_bf16_f32 v70, v28, v29
v_cvt_pk_bf16_f32 v71, v30, v31
ds_write_b128 v95, v[68:71] offset:0
ds_read_b128 v[64:67], v96 offset:0
ds_read_b128 v[68:71], v96 offset:1024
s_waitcnt lgkmcnt(0)
buffer_store_dwordx4 v[64:67], v91, s[44:47], s54 offen sc1
buffer_store_dwordx4 v[68:71], v92, s[44:47], s54 offen sc1
s_endpgm
CR0_1:
s_cmp_eq_u32 s61, 0
s_cbranch_scc1 BR0_1
s_sub_u32 s61, s61, 1
s_sleep 8
buffer_load_dwordx2 v[76:77], v84, s[36:39], 0 offen sc1
s_waitcnt vmcnt(0)
ds_write_b32 v82, v76
v_sub_u32 v76, v76, v85
v_xor_b32 v77, v77, v73
v_cmp_lt_u64 vcc, s[62:63], v[76:77]
s_waitcnt lgkmcnt(0)
s_cbranch_vccnz CR0_1
s_branch BR0_1
CR0_2:
s_mov_b32 s58, m0
s_mov_b32 m0, 144384
DR0_2:
s_cmp_eq_u32 s61, 0
s_cbranch_scc1 ER0_2
s_sub_u32 s61, s61, 1
buffer_load_dword v84, s[36:39], 0 offen sc1 lds
s_sleep 1
ds_read_b32 v76, v82
s_waitcnt lgkmcnt(0)
v_cmp_gt_i32 vcc, v85, v76
s_cbranch_vccnz DR0_2
ER0_2:
s_mov_b32 m0, s58
s_branch BR0_2
CR0_3:
s_mov_b32 s58, m0
s_mov_b32 m0, 144384
DR0_3:
s_cmp_eq_u32 s61, 0
s_cbranch_scc1 ER0_3
s_sub_u32 s61, s61, 1
buffer_load_dword v84, s[36:39], 0 offen sc1 lds
s_sleep 1
ds_read_b32 v76, v82
s_waitcnt lgkmcnt(0)
v_cmp_gt_i32 vcc, v85, v76
s_cbranch_vccnz DR0_3
ER0_3:
s_mov_b32 m0, s58
s_branch BR0_3
CR0_4:
s_mov_b32 s58, m0
s_mov_b32 m0, 144384
DR0_4:
s_cmp_eq_u32 s61, 0
s_cbranch_scc1 ER0_4
s_sub_u32 s61, s61, 1
buffer_load_dword v84, s[36:39], 0 offen sc1 lds
s_sleep 1
ds_read_b32 v76, v82
s_waitcnt lgkmcnt(0)
v_cmp_gt_i32 vcc, v85, v76
s_cbranch_vccnz DR0_4
ER0_4:
s_mov_b32 m0, s58
s_branch BR0_4
CR0_5:
s_mov_b32 s58, m0
s_mov_b32 m0, 144384
DR0_5:
s_cmp_eq_u32 s61, 0
s_cbranch_scc1 ER0_5
s_sub_u32 s61, s61, 1
buffer_load_dword v84, s[36:39], 0 offen sc1 lds
s_sleep 1
ds_read_b32 v76, v82
s_waitcnt lgkmcnt(0)
v_cmp_gt_i32 vcc, v85, v76
s_cbranch_vccnz DR0_5
ER0_5:
s_mov_b32 m0, s58
s_branch BR0_5
CR0_6:
s_mov_b32 s58, m0
s_mov_b32 m0, 144384
DR0_6:
s_cmp_eq_u32 s61, 0
s_cbranch_scc1 ER0_6
s_sub_u32 s61, s61, 1
buffer_load_dword v84, s[36:39], 0 offen sc1 lds
s_sleep 1
ds_read_b32 v76, v82
s_waitcnt lgkmcnt(0)
v_cmp_gt_i32 vcc, v85, v76
s_cbranch_vccnz DR0_6
ER0_6:
s_mov_b32 m0, s58
s_branch BR0_6
CR0_8:
s_mov_b32 s58, m0
s_mov_b32 m0, 144384
DR0_8:
s_cmp_eq_u32 s61, 0
s_cbranch_scc1 ER0_8
s_sub_u32 s61, s61, 1
buffer_load_dword v84, s[36:39], 0 offen sc1 lds
s_sleep 1
ds_read_b32 v76, v82
s_waitcnt lgkmcnt(0)
v_cmp_gt_i32 vcc, v85, v76
s_cbranch_vccnz DR0_8
ER0_8:
s_mov_b32 m0, s58
s_branch BR0_8
CR0_9:
s_mov_b32 s58, m0
s_mov_b32 m0, 144384
DR0_9:
s_cmp_eq_u32 s61, 0
s_cbranch_scc1 ER0_9
s_sub_u32 s61, s61, 1
buffer_load_dword v84, s[36:39], 0 offen sc1 lds
s_sleep 1
ds_read_b32 v76, v82
s_waitcnt lgkmcnt(0)
v_cmp_gt_i32 vcc, v85, v76
s_cbranch_vccnz DR0_9
ER0_9:
s_mov_b32 m0, s58
s_branch BR0_9
CR0_10:
s_mov_b32 s58, m0
s_mov_b32 m0, 144384
DR0_10:
s_cmp_eq_u32 s61, 0
s_cbranch_scc1 ER0_10
s_sub_u32 s61, s61, 1
buffer_load_dword v84, s[36:39], 0 offen sc1 lds
s_sleep 1
ds_read_b32 v76, v82
s_waitcnt lgkmcnt(0)
v_cmp_gt_i32 vcc, v85, v76
s_cbranch_vccnz DR0_10
ER0_10:
s_mov_b32 m0, s58
s_branch BR0_10
CR0_11:
s_mov_b32 s58, m0
s_mov_b32 m0, 144384
DR0_11:
s_cmp_eq_u32 s61, 0
s_cbranch_scc1 ER0_11
s_sub_u32 s61, s61, 1
buffer_load_dword v84, s[36:39], 0 offen sc1 lds
s_sleep 1
ds_read_b32 v76, v82
s_waitcnt lgkmcnt(0)
v_cmp_gt_i32 vcc, v85, v76
s_cbranch_vccnz DR0_11
ER0_11:
s_mov_b32 m0, s58
s_branch BR0_11
CR0_12:
s_mov_b32 s58, m0
s_mov_b32 m0, 144384
DR0_12:
s_cmp_eq_u32 s61, 0
s_cbranch_scc1 ER0_12
s_sub_u32 s61, s61, 1
buffer_load_dword v84, s[36:39], 0 offen sc1 lds
s_sleep 1
ds_read_b32 v76, v82
s_waitcnt lgkmcnt(0)
v_cmp_gt_i32 vcc, v85, v76
s_cbranch_vccnz DR0_12
ER0_12:
s_mov_b32 m0, s58
s_branch BR0_12
CR0_13:
s_mov_b32 s58, m0
s_mov_b32 m0, 144384
DR0_13:
s_cmp_eq_u32 s61, 0
s_cbranch_scc1 ER0_13
s_sub_u32 s61, s61, 1
buffer_load_dword v84, s[36:39], 0 offen sc1 lds
s_sleep 1
ds_read_b32 v76, v82
s_waitcnt lgkmcnt(0)
v_cmp_gt_i32 vcc, v85, v76
s_cbranch_vccnz DR0_13
ER0_13:
s_mov_b32 m0, s58
s_branch BR0_13
CR0_15:
s_mov_b32 s58, m0
s_mov_b32 m0, 144384
DR0_15:
s_cmp_eq_u32 s61, 0
s_cbranch_scc1 ER0_15
s_sub_u32 s61, s61, 1
buffer_load_dword v84, s[36:39], 0 offen sc1 lds
s_sleep 1
ds_read_b32 v76, v82
s_waitcnt lgkmcnt(0)
v_cmp_gt_i32 vcc, v85, v76
s_cbranch_vccnz DR0_15
ER0_15:
s_mov_b32 m0, s58
s_branch BR0_15
CR0_16:
s_mov_b32 s58, m0
s_mov_b32 m0, 144384
DR0_16:
s_cmp_eq_u32 s61, 0
s_cbranch_scc1 ER0_16
s_sub_u32 s61, s61, 1
buffer_load_dword v84, s[36:39], 0 offen sc1 lds
s_sleep 1
ds_read_b32 v76, v82
s_waitcnt lgkmcnt(0)
v_cmp_gt_i32 vcc, v85, v76
s_cbranch_vccnz DR0_16
ER0_16:
s_mov_b32 m0, s58
s_branch BR0_16
CR0_17:
s_mov_b32 s58, m0
s_mov_b32 m0, 144384
DR0_17:
s_cmp_eq_u32 s61, 0
s_cbranch_scc1 ER0_17
s_sub_u32 s61, s61, 1
buffer_load_dword v84, s[36:39], 0 offen sc1 lds
s_sleep 1
ds_read_b32 v76, v82
s_waitcnt lgkmcnt(0)
v_cmp_gt_i32 vcc, v85, v76
s_cbranch_vccnz DR0_17
ER0_17:
s_mov_b32 m0, s58
s_branch BR0_17
CR0_18:
s_mov_b32 s58, m0
s_mov_b32 m0, 144384
DR0_18:
s_cmp_eq_u32 s61, 0
s_cbranch_scc1 ER0_18
s_sub_u32 s61, s61, 1
buffer_load_dword v84, s[36:39], 0 offen sc1 lds
s_sleep 1
ds_read_b32 v76, v82
s_waitcnt lgkmcnt(0)
v_cmp_gt_i32 vcc, v85, v76
s_cbranch_vccnz DR0_18
ER0_18:
s_mov_b32 m0, s58
s_branch BR0_18
LvR1:
LsR6:
s_cbranch_execz LsR10
s_add_u32 s49, s49, 313344
s_barrier
s_add_u32 m0, s50, 0
s_nop 0
D 83,0
D 83,1024
D 83,2048
D 83,3072
s_add_u32 s49, s49, 313344
s_barrier
s_add_u32 m0, s50, 32768
s_nop 0
D 83,0
D 83,1024
D 83,2048
D 83,3072
buffer_load_dwordx4 v[124:127], v90, s[36:39], s49 offen sc1
buffer_load_dwordx4 v[128:131], v90, s[36:39], s49 offen offset:1024 sc1
s_xor_b32 s50, s50, 0x10000
s_waitcnt vmcnt(6)
s_add_u32 s49, s49, 313344
s_barrier
R 32,0
buffer_load_dwordx4 v[132:135], v86, s[36:39], s49 offen sc1
R 36,1024
buffer_load_dwordx4 v[136:139], v86, s[36:39], s49 offen offset:1024 sc1
R 40,2048
buffer_load_dwordx4 v[140:143], v87, s[36:39], s49 offen sc1
s_waitcnt lgkmcnt(1)
v_mfma_f32_16x16x32_bf16 v[0:3], a[0:3], v[32:35], v[100:103]
R 44,3072
v_mfma_f32_16x16x32_bf16 v[4:7], a[4:7], v[32:35], v[104:107]
v_mfma_f32_16x16x32_bf16 v[8:11], a[8:11], v[32:35], v[108:111]
v_mfma_f32_16x16x32_bf16 v[12:15], a[12:15], v[32:35], v[112:115]
mav 0,16,36
buffer_load_dwordx4 v[144:147], v87, s[36:39], s49 offen offset:1024 sc1
mav 4,20,36
R 48,4096
mav 8,24,36
mav 12,28,36
s_waitcnt lgkmcnt(1)
mav 0,32,40
buffer_load_dwordx4 v[148:151], v88, s[36:39], s49 offen sc1
mav 4,36,40
R 52,5120
mav 8,40,40
mav 12,44,40
mav 0,48,44
buffer_load_dwordx4 v[152:155], v88, s[36:39], s49 offen offset:1024 sc1
mav 4,52,44
R 56,6144
mav 8,56,44
mav 12,60,44
s_waitcnt lgkmcnt(1)
mav 0,64,48
buffer_load_dwordx4 v[156:159], v89, s[36:39], s49 offen sc1
mav 4,68,48
R 60,7168
mav 8,72,48
mav 12,76,48
mav 0,80,52
buffer_load_dwordx4 v[160:163], v89, s[36:39], s49 offen offset:1024 sc1
mav 4,84,52
R 32,8192
mav 8,88,52
s_add_u32 m0, s50, 0
mav 12,92,52
s_waitcnt lgkmcnt(1)
mav 0,96,56
D 83,0
mav 4,100,56
R 36,9216
mav 8,104,56
mav 12,108,56
mav 0,112,60
D 83,1024
mav 4,116,60
R 40,10240
mav 8,120,60
mav 12,124,60
s_waitcnt lgkmcnt(1)
mav 0,128,32
R 44,11264
mav 4,132,32
mav 8,136,32
mav 12,140,32
mav 0,144,36
R 48,12288
mav 4,148,36
mav 8,152,36
mav 12,156,36
s_waitcnt lgkmcnt(1)
mav 0,160,40
R 52,13312
mav 4,164,40
mav 8,168,40
mav 12,172,40
mav 0,176,44
D 83,2048
mav 4,180,44
R 56,14336
mav 8,184,44
mav 12,188,44
s_waitcnt lgkmcnt(1)
mav 0,192,48
D 83,3072
mav 4,196,48
R 60,15360
mav 8,200,48
mav 12,204,48
mav 0,208,52
mav 4,212,52
mav 8,216,52
mav 12,220,52
s_waitcnt lgkmcnt(0)
mav 0,224,56
mav 4,228,56
mav 8,232,56
mav 12,236,56
mav 0,240,60
buffer_load_dwordx4 v[164:167], v90, s[36:39], s49 offen sc1
mav 4,244,60
mav 8,248,60
mav 12,252,60
s_waitcnt vmcnt(14)
mvv 0,116,124
buffer_load_dwordx4 v[168:171], v90, s[36:39], s49 offen offset:1024 sc1
mvv 4,120,124
s_waitcnt vmcnt(14)
mvv 8,116,128
mvv 12,120,128
s_waitcnt vmcnt(13)
mvv 0,116,132
mvv 4,120,132
s_waitcnt vmcnt(12)
mvv 8,116,136
s_waitcnt vmcnt(11)
s_add_u32 s49, s49, 313344
mvv 12,120,136
s_barrier
mvv 0,116,140
R 32,32768
mvv 4,120,140
s_waitcnt vmcnt(10)
mvv 8,116,144
mvv 12,120,144
s_waitcnt vmcnt(9)
mvv 0,116,148
buffer_load_dwordx4 v[172:175], v86, s[36:39], s49 offen sc1
mvv 4,120,148
s_waitcnt vmcnt(9)
R 36,33792
mvv 8,116,152
mvv 12,120,152
s_waitcnt vmcnt(8)
mvv 0,116,156
buffer_load_dwordx4 v[176:179], v86, s[36:39], s49 offen offset:1024 sc1
mvv 4,120,156
s_waitcnt vmcnt(8)
R 40,34816
mvv 8,116,160
mvv 12,120,160
s_waitcnt lgkmcnt(1)
v_mfma_f32_16x16x32_bf16 v[16:19], a[0:3], v[32:35], v[100:103]
buffer_load_dwordx4 v[180:183], v87, s[36:39], s49 offen sc1
v_mfma_f32_16x16x32_bf16 v[20:23], a[4:7], v[32:35], v[104:107]
R 44,35840
v_cvt_pk_bf16_f32 v64, v0, v1
v_mfma_f32_16x16x32_bf16 v[24:27], a[8:11], v[32:35], v[108:111]
v_cvt_pk_bf16_f32 v65, v2, v3
v_cvt_pk_bf16_f32 v66, v4, v5
v_mfma_f32_16x16x32_bf16 v[28:31], a[12:15], v[32:35], v[112:115]
v_cvt_pk_bf16_f32 v67, v6, v7
v_cvt_pk_bf16_f32 v68, v8, v9
mav 16,16,36
buffer_load_dwordx4 v[184:187], v87, s[36:39], s49 offen offset:1024 sc1
mav 20,20,36
R 48,36864
v_cvt_pk_bf16_f32 v69, v10, v11
mav 24,24,36
v_cvt_pk_bf16_f32 v70, v12, v13
v_cvt_pk_bf16_f32 v71, v14, v15
mav 28,28,36
s_waitcnt lgkmcnt(1)
v_mul_f32 v75, v0, v0
v_add_f32 v74, v1, v0
mav 16,32,40
buffer_load_dwordx4 v[188:191], v88, s[36:39], s49 offen sc1
mav 20,36,40
R 52,37888
v_fmac_f32 v75, v1, v1
mav 24,40,40
v_add_f32 v74, v2, v74
v_fmac_f32 v75, v2, v2
mav 28,44,40
v_add_f32 v74, v3, v74
v_fmac_f32 v75, v3, v3
mav 16,48,44
buffer_load_dwordx4 v[192:195], v88, s[36:39], s49 offen offset:1024 sc1
mav 20,52,44
R 56,38912
v_add_f32 v74, v4, v74
mav 24,56,44
v_fmac_f32 v75, v4, v4
v_add_f32 v74, v5, v74
mav 28,60,44
s_waitcnt lgkmcnt(1)
v_fmac_f32 v75, v5, v5
v_add_f32 v74, v6, v74
mav 16,64,48
buffer_load_dwordx4 v[196:199], v89, s[36:39], s49 offen sc1
mav 20,68,48
R 60,39936
v_fmac_f32 v75, v6, v6
mav 24,72,48
v_add_f32 v74, v7, v74
v_fmac_f32 v75, v7, v7
mav 28,76,48
v_add_f32 v74, v8, v74
v_fmac_f32 v75, v8, v8
mav 16,80,52
buffer_load_dwordx4 v[200:203], v89, s[36:39], s49 offen offset:1024 sc1
mav 20,84,52
R 32,40960
v_add_f32 v74, v9, v74
mav 24,88,52
s_add_u32 m0, s50, 32768
v_fmac_f32 v75, v9, v9
v_add_f32 v74, v10, v74
mav 28,92,52
s_waitcnt lgkmcnt(1)
ds_write_b128 v94, v[64:67] offset:0
mav 16,96,56
D 83,0
mav 20,100,56
R 36,41984
v_fmac_f32 v75, v10, v10
mav 24,104,56
ds_write_b128 v95, v[68:71] offset:0
mav 28,108,56
ds_read_b128 v[64:67], v96 offset:0
v_add_f32 v74, v11, v74
mav 16,112,60
D 83,1024
mav 20,116,60
R 40,43008
v_fmac_f32 v75, v11, v11
mav 24,120,60
ds_read_b128 v[68:71], v96 offset:1024
v_add_f32 v74, v12, v74
mav 28,124,60
s_waitcnt lgkmcnt(4)
v_fmac_f32 v75, v12, v12
v_add_f32 v74, v13, v74
mav 16,128,32
R 44,44032
v_fmac_f32 v75, v13, v13
mav 20,132,32
v_add_f32 v74, v14, v74
v_fmac_f32 v75, v14, v14
mav 24,136,32
v_add_f32 v74, v15, v74
v_fmac_f32 v75, v15, v15
mav 28,140,32
mav 16,144,36
R 48,45056
mav 20,148,36
v_permlane32_swap_b32 v74, v75
mav 24,152,36
v_add_f32 v74, v74, v75
mav 28,156,36
s_waitcnt lgkmcnt(1)
mav 16,160,40
mav 20,164,40
R 52,46080
mav 24,168,40
mav 28,172,40
mav 16,176,44
D 83,2048
mav 20,180,44
R 56,47104
mav 24,184,44
mav 28,188,44
s_waitcnt lgkmcnt(1)
mav 16,192,48
D 83,3072
mav 20,196,48
R 60,48128
mav 24,200,48
mav 28,204,48
mav 16,208,52
buffer_store_dwordx4 v[64:67], v91, s[36:39], s54 offen sc1
mav 20,212,52
mav 24,216,52
mav 28,220,52
s_waitcnt lgkmcnt(0)
mav 16,224,56
buffer_store_dwordx4 v[68:71], v92, s[36:39], s54 offen sc1
mav 20,228,56
mav 24,232,56
mav 28,236,56
mav 16,240,60
buffer_load_dwordx4 v[124:127], v90, s[36:39], s49 offen sc1
mav 20,244,60
mav 24,248,60
mav 28,252,60
s_waitcnt vmcnt(16)
mvv 16,116,164
buffer_load_dwordx4 v[128:131], v90, s[36:39], s49 offen offset:1024 sc1
mvv 20,120,164
s_waitcnt vmcnt(16)
mvv 24,116,168
s_xor_b32 s50, s50, 0x10000
s_sub_u32 s54, s54, 32768
mvv 28,120,168
s_waitcnt vmcnt(15)
mvv 16,116,172
mvv 20,120,172
s_waitcnt vmcnt(14)
v_xor_b32 v80, 0x10000, v80
mvv 24,116,176
s_waitcnt vmcnt(13)
s_add_u32 s49, s49, 313344
mvv 28,120,176
s_barrier
mvv 16,116,180
R 32,0
mvv 20,120,180
s_waitcnt vmcnt(12)
mvv 24,116,184
mvv 28,120,184
s_waitcnt vmcnt(11)
mvv 16,116,188
buffer_load_dwordx4 v[132:135], v86, s[36:39], s49 offen sc1
mvv 20,120,188
s_waitcnt vmcnt(11)
R 36,1024
mvv 24,116,192
mvv 28,120,192
s_waitcnt vmcnt(10)
mvv 16,116,196
buffer_load_dwordx4 v[136:139], v86, s[36:39], s49 offen offset:1024 sc1
mvv 20,120,196
s_waitcnt vmcnt(10)
R 40,2048
mvv 24,116,200
mvv 28,120,200
s_waitcnt lgkmcnt(1)
v_mfma_f32_16x16x32_bf16 v[0:3], a[0:3], v[32:35], v[100:103]
buffer_load_dwordx4 v[140:143], v87, s[36:39], s49 offen sc1
v_mfma_f32_16x16x32_bf16 v[4:7], a[4:7], v[32:35], v[104:107]
R 44,3072
v_cvt_pk_bf16_f32 v64, v16, v17
v_mfma_f32_16x16x32_bf16 v[8:11], a[8:11], v[32:35], v[108:111]
v_cvt_pk_bf16_f32 v65, v18, v19
v_cvt_pk_bf16_f32 v66, v20, v21
v_mfma_f32_16x16x32_bf16 v[12:15], a[12:15], v[32:35], v[112:115]
v_cvt_pk_bf16_f32 v67, v22, v23
v_cvt_pk_bf16_f32 v68, v24, v25
mav 0,16,36
buffer_load_dwordx4 v[144:147], v87, s[36:39], s49 offen offset:1024 sc1
mav 4,20,36
R 48,4096
v_cvt_pk_bf16_f32 v69, v26, v27
mav 8,24,36
v_cvt_pk_bf16_f32 v70, v28, v29
v_cvt_pk_bf16_f32 v71, v30, v31
mav 12,28,36
s_waitcnt lgkmcnt(1)
v_mul_f32 v75, v16, v16
v_fmac_f32 v75, v17, v17
mav 0,32,40
buffer_load_dwordx4 v[148:151], v88, s[36:39], s49 offen sc1
mav 4,36,40
R 52,5120
v_fmac_f32 v75, v18, v18
mav 8,40,40
v_fmac_f32 v75, v19, v19
v_fmac_f32 v75, v20, v20
mav 12,44,40
v_fmac_f32 v75, v21, v21
v_fmac_f32 v75, v22, v22
mav 0,48,44
buffer_load_dwordx4 v[152:155], v88, s[36:39], s49 offen offset:1024 sc1
mav 4,52,44
R 56,6144
v_fmac_f32 v75, v23, v23
mav 8,56,44
v_fmac_f32 v75, v24, v24
v_fmac_f32 v75, v25, v25
mav 12,60,44
s_waitcnt lgkmcnt(1)
v_fmac_f32 v75, v26, v26
v_fmac_f32 v75, v27, v27
mav 0,64,48
buffer_load_dwordx4 v[156:159], v89, s[36:39], s49 offen sc1
mav 4,68,48
R 60,7168
v_fmac_f32 v75, v28, v28
mav 8,72,48
v_fmac_f32 v75, v29, v29
v_fmac_f32 v75, v30, v30
mav 12,76,48
v_fmac_f32 v75, v31, v31
mav 0,80,52
buffer_load_dwordx4 v[160:163], v89, s[36:39], s49 offen offset:1024 sc1
mav 4,84,52
R 32,8192
mav 8,88,52
ds_write_b32 v97, v74 offset:0
mav 12,92,52
s_waitcnt lgkmcnt(2)
ds_write_b128 v94, v[64:67] offset:0
mav 0,96,56
s_add_u32 m0, s50, 0
R 36,9216
v_add_f32 v74, v17, v16
mav 4,100,56
ds_write_b128 v95, v[68:71] offset:0
mav 8,104,56
ds_read_b128 v[64:67], v96 offset:0
LsR7:
s_cbranch_execz LsR11
v_add_f32 v74, v18, v74
mav 12,108,56
ds_read_b128 v[68:71], v96 offset:1024
v_add_f32 v74, v19, v74
mav 0,112,60
D 83,0
mav 4,116,60
R 40,10240
v_add_f32 v74, v20, v74
mav 8,120,60
v_add_f32 v74, v21, v74
v_add_f32 v74, v22, v74
mav 12,124,60
s_waitcnt lgkmcnt(3)
v_add_f32 v74, v23, v74
v_add_f32 v74, v24, v74
mav 0,128,32
D 83,1024
mav 4,132,32
R 44,11264
v_add_f32 v74, v25, v74
mav 8,136,32
v_add_f32 v74, v26, v74
v_add_f32 v74, v27, v74
mav 12,140,32
v_add_f32 v74, v28, v74
v_add_f32 v74, v29, v74
mav 0,144,36
R 48,12288
v_add_f32 v74, v30, v74
mav 4,148,36
v_add_f32 v74, v31, v74
mav 8,152,36
mav 12,156,36
s_waitcnt lgkmcnt(1)
v_permlane32_swap_b32 v74, v75
mav 0,160,40
R 52,13312
v_add_f32 v74, v74, v75
mav 4,164,40
v_add_u32 v72, 1, v72
mav 8,168,40
mav 12,172,40
mav 0,176,44
D 83,2048
mav 4,180,44
R 56,14336
mav 8,184,44
mav 12,188,44
s_waitcnt lgkmcnt(1)
mav 0,192,48
D 83,3072
mav 4,196,48
R 60,15360
mav 8,200,48
mav 12,204,48
mav 0,208,52
buffer_store_dwordx4 v[64:67], v91, s[36:39], s54 offen sc1
mav 4,212,52
mav 8,216,52
mav 12,220,52
s_waitcnt lgkmcnt(0)
mav 0,224,56
buffer_store_dwordx4 v[68:71], v92, s[36:39], s54 offen sc1
mav 4,228,56
mav 8,232,56
mav 12,236,56
mav 0,240,60
buffer_load_dwordx4 v[164:167], v90, s[36:39], s49 offen sc1
mav 4,244,60
mav 8,248,60
mav 12,252,60
s_waitcnt vmcnt(16)
mvv 0,116,124
buffer_load_dwordx4 v[168:171], v90, s[36:39], s49 offen offset:1024 sc1
mvv 4,120,124
s_waitcnt vmcnt(16)
buffer_store_dword v72, v81, s[36:39], s52 offen sc1
mvv 8,116,128
s_sub_u32 s54, s54, 32768
mvv 12,120,128
s_waitcnt vmcnt(16)
mvv 0,116,132
mvv 4,120,132
s_waitcnt vmcnt(15)
mvv 8,116,136
s_waitcnt vmcnt(14)
s_add_u32 s49, s49, 313344
mvv 12,120,136
s_barrier
mvv 0,116,140
R 32,32768
mvv 4,120,140
s_waitcnt vmcnt(13)
ds_read_b128 v[204:207], v98 offset:0
mvv 8,116,144
mvv 12,120,144
s_waitcnt vmcnt(12)
mvv 0,116,148
buffer_load_dwordx4 v[172:175], v86, s[36:39], s49 offen sc1
mvv 4,120,148
s_waitcnt vmcnt(12)
R 36,33792
mvv 8,116,152
mvv 12,120,152
s_waitcnt vmcnt(11)
mvv 0,116,156
s_waitcnt lgkmcnt(1)
buffer_load_dwordx4 v[176:179], v86, s[36:39], s49 offen offset:1024 sc1
mvv 4,120,156
s_waitcnt vmcnt(11)
R 40,34816
v_add_f32 v204, v204, v205
mvv 8,116,160
v_add_f32 v206, v206, v207
v_add_f32 v204, v204, v206
mvv 12,120,160
v_mfma_f32_16x16x32_bf16 v[16:19], a[0:3], v[32:35], v[100:103]
buffer_load_dwordx4 v[180:183], v87, s[36:39], s49 offen sc1
v_mfma_f32_16x16x32_bf16 v[20:23], a[4:7], v[32:35], v[104:107]
R 44,35840
v_add_f32_dpp v205, v204, v204 quad_perm:[1,0,3,2] row_mask:0xf bank_mask:0xf
v_mfma_f32_16x16x32_bf16 v[24:27], a[8:11], v[32:35], v[108:111]
buffer_store_dword v205, v99, s[36:39], s49 offen sc1
v_mfma_f32_16x16x32_bf16 v[28:31], a[12:15], v[32:35], v[112:115]
s_waitcnt lgkmcnt(1)
v_cvt_pk_bf16_f32 v64, v0, v1
v_cvt_pk_bf16_f32 v65, v2, v3
mav 16,16,36
buffer_load_dwordx4 v[184:187], v87, s[36:39], s49 offen offset:1024 sc1
mav 20,20,36
R 48,36864
v_cvt_pk_bf16_f32 v66, v4, v5
mav 24,24,36
v_cvt_pk_bf16_f32 v67, v6, v7
v_cvt_pk_bf16_f32 v68, v8, v9
mav 28,28,36
v_cvt_pk_bf16_f32 v69, v10, v11
v_cvt_pk_bf16_f32 v70, v12, v13
mav 16,32,40
buffer_load_dwordx4 v[188:191], v88, s[36:39], s49 offen sc1
mav 20,36,40
R 52,37888
v_cvt_pk_bf16_f32 v71, v14, v15
mav 24,40,40
v_mul_f32 v75, v0, v0
v_fmac_f32 v75, v1, v1
mav 28,44,40
s_waitcnt lgkmcnt(1)
v_fmac_f32 v75, v2, v2
v_fmac_f32 v75, v3, v3
mav 16,48,44
buffer_load_dwordx4 v[192:195], v88, s[36:39], s49 offen offset:1024 sc1
mav 20,52,44
R 56,38912
v_fmac_f32 v75, v4, v4
mav 24,56,44
v_fmac_f32 v75, v5, v5
v_fmac_f32 v75, v6, v6
mav 28,60,44
v_fmac_f32 v75, v7, v7
)"
R"(
v_fmac_f32 v75, v8, v8
mav 16,64,48
buffer_load_dwordx4 v[196:199], v89, s[36:39], s49 offen sc1
mav 20,68,48
R 60,39936
v_fmac_f32 v75, v9, v9
mav 24,72,48
v_fmac_f32 v75, v10, v10
v_fmac_f32 v75, v11, v11
mav 28,76,48
s_waitcnt lgkmcnt(1)
v_fmac_f32 v75, v12, v12
v_fmac_f32 v75, v13, v13
mav 16,80,52
buffer_load_dwordx4 v[200:203], v89, s[36:39], s49 offen offset:1024 sc1
mav 20,84,52
R 32,40960
v_fmac_f32 v75, v14, v14
mav 24,88,52
ds_write_b32 v97, v74 offset:1024
mav 28,92,52
ds_write_b128 v94, v[64:67] offset:0
mav 16,96,56
s_add_u32 m0, s50, 32768
R 36,41984
v_add_f32 v74, v1, v0
mav 20,100,56
ds_write_b128 v95, v[68:71] offset:0
mav 24,104,56
ds_read_b128 v[64:67], v96 offset:0
v_add_f32 v74, v2, v74
mav 28,108,56
s_waitcnt lgkmcnt(5)
ds_read_b128 v[68:71], v96 offset:1024
v_add_f32 v74, v3, v74
mav 16,112,60
D 83,0
mav 20,116,60
R 40,43008
v_add_f32 v74, v4, v74
mav 24,120,60
v_add_f32 v74, v5, v74
v_add_f32 v74, v6, v74
mav 28,124,60
v_add_f32 v74, v7, v74
v_add_f32 v74, v8, v74
mav 16,128,32
D 83,1024
mav 20,132,32
R 44,44032
v_add_f32 v74, v9, v74
mav 24,136,32
v_add_f32 v74, v10, v74
v_add_f32 v74, v11, v74
mav 28,140,32
s_waitcnt lgkmcnt(1)
v_add_f32 v74, v12, v74
v_add_f32 v74, v13, v74
mav 16,144,36
R 48,45056
v_add_f32 v74, v14, v74
mav 20,148,36
v_add_f32 v74, v15, v74
v_fmac_f32 v75, v15, v15
mav 24,152,36
mav 28,156,36
v_permlane32_swap_b32 v74, v75
mav 16,160,40
s_mov_b32 s60, -14
.p2align 6
LR1_1:
mav 20,164,40
R 52,46080
v_add_f32 v74, v74, v75
mav 24,168,40
v_add_u32 v72, 1, v72
mav 28,172,40
s_waitcnt lgkmcnt(1)
mav 16,176,44
D 83,2048
mav 20,180,44
R 56,47104
mav 24,184,44
mav 28,188,44
mav 16,192,48
D 83,3072
mav 20,196,48
R 60,48128
mav 24,200,48
mav 28,204,48
s_waitcnt lgkmcnt(1)
mav 16,208,52
buffer_store_dwordx4 v[64:67], v91, s[36:39], s54 offen sc1
mav 20,212,52
mav 24,216,52
mav 28,220,52
mav 16,224,56
buffer_store_dwordx4 v[68:71], v92, s[36:39], s54 offen sc1
mav 20,228,56
mav 24,232,56
mav 28,236,56
s_waitcnt lgkmcnt(0)
mav 16,240,60
buffer_load_dwordx4 v[124:127], v90, s[36:39], s49 offen sc1
mav 20,244,60
mav 24,248,60
mav 28,252,60
s_waitcnt vmcnt(18)
mvv 16,116,164
s_waitcnt vmcnt(12)
buffer_load_dwordx4 v[128:131], v90, s[36:39], s49 offen offset:1024 sc1
mvv 20,120,164
buffer_store_dword v72, v81, s[36:39], s52 offen sc1
mvv 24,116,168
s_xor_b32 s50, s50, 0x10000
s_sub_u32 s54, s54, 32768
mvv 28,120,168
mvv 16,116,172
mvv 20,120,172
v_xor_b32 v80, 0x10000, v80
mvv 24,116,176
s_add_u32 s49, s49, 313344
mvv 28,120,176
s_barrier
mvv 16,116,180
R 32,0
mvv 20,120,180
s_waitcnt vmcnt(13)
ds_read_b128 v[204:207], v98 offset:1024
mvv 24,116,184
mvv 28,120,184
s_waitcnt vmcnt(12)
mvv 16,116,188
buffer_load_dwordx4 v[132:135], v86, s[36:39], s49 offen sc1
mvv 20,120,188
s_waitcnt vmcnt(12)
R 36,1024
mvv 24,116,192
mvv 28,120,192
s_waitcnt vmcnt(11)
mvv 16,116,196
s_waitcnt lgkmcnt(1)
buffer_load_dwordx4 v[136:139], v86, s[36:39], s49 offen offset:1024 sc1
mvv 20,120,196
s_waitcnt vmcnt(11)
R 40,2048
v_add_f32 v204, v204, v205
mvv 24,116,200
v_add_f32 v206, v206, v207
v_add_f32 v204, v204, v206
mvv 28,120,200
v_mfma_f32_16x16x32_bf16 v[0:3], a[0:3], v[32:35], v[100:103]
buffer_load_dwordx4 v[140:143], v87, s[36:39], s49 offen sc1
v_mfma_f32_16x16x32_bf16 v[4:7], a[4:7], v[32:35], v[104:107]
R 44,3072
v_add_f32_dpp v205, v204, v204 quad_perm:[1,0,3,2] row_mask:0xf bank_mask:0xf
v_mfma_f32_16x16x32_bf16 v[8:11], a[8:11], v[32:35], v[108:111]
buffer_store_dword v205, v99, s[36:39], s49 offen sc1
v_mfma_f32_16x16x32_bf16 v[12:15], a[12:15], v[32:35], v[112:115]
s_waitcnt lgkmcnt(1)
v_cvt_pk_bf16_f32 v64, v16, v17
v_cvt_pk_bf16_f32 v65, v18, v19
mav 0,16,36
buffer_load_dwordx4 v[144:147], v87, s[36:39], s49 offen offset:1024 sc1
mav 4,20,36
R 48,4096
v_cvt_pk_bf16_f32 v66, v20, v21
mav 8,24,36
v_cvt_pk_bf16_f32 v67, v22, v23
v_cvt_pk_bf16_f32 v68, v24, v25
mav 12,28,36
v_cvt_pk_bf16_f32 v69, v26, v27
v_cvt_pk_bf16_f32 v70, v28, v29
mav 0,32,40
buffer_load_dwordx4 v[148:151], v88, s[36:39], s49 offen sc1
mav 4,36,40
R 52,5120
v_cvt_pk_bf16_f32 v71, v30, v31
mav 8,40,40
v_mul_f32 v75, v16, v16
v_fmac_f32 v75, v17, v17
mav 12,44,40
s_waitcnt lgkmcnt(1)
v_fmac_f32 v75, v18, v18
v_fmac_f32 v75, v19, v19
mav 0,48,44
buffer_load_dwordx4 v[152:155], v88, s[36:39], s49 offen offset:1024 sc1
mav 4,52,44
R 56,6144
v_fmac_f32 v75, v20, v20
mav 8,56,44
v_fmac_f32 v75, v21, v21
v_fmac_f32 v75, v22, v22
mav 12,60,44
v_fmac_f32 v75, v23, v23
v_fmac_f32 v75, v24, v24
mav 0,64,48
buffer_load_dwordx4 v[156:159], v89, s[36:39], s49 offen sc1
mav 4,68,48
R 60,7168
v_fmac_f32 v75, v25, v25
mav 8,72,48
v_fmac_f32 v75, v26, v26
v_fmac_f32 v75, v27, v27
mav 12,76,48
s_waitcnt lgkmcnt(1)
v_fmac_f32 v75, v28, v28
v_fmac_f32 v75, v29, v29
mav 0,80,52
buffer_load_dwordx4 v[160:163], v89, s[36:39], s49 offen offset:1024 sc1
mav 4,84,52
R 32,8192
v_fmac_f32 v75, v30, v30
mav 8,88,52
ds_write_b32 v97, v74 offset:0
mav 12,92,52
ds_write_b128 v94, v[64:67] offset:0
mav 0,96,56
s_add_u32 m0, s50, 0
R 36,9216
v_add_f32 v74, v17, v16
mav 4,100,56
ds_write_b128 v95, v[68:71] offset:0
mav 8,104,56
ds_read_b128 v[64:67], v96 offset:0
v_add_f32 v74, v18, v74
mav 12,108,56
s_waitcnt lgkmcnt(5)
ds_read_b128 v[68:71], v96 offset:1024
v_add_f32 v74, v19, v74
mav 0,112,60
D 83,0
mav 4,116,60
R 40,10240
v_add_f32 v74, v20, v74
mav 8,120,60
v_add_f32 v74, v21, v74
v_add_f32 v74, v22, v74
mav 12,124,60
v_add_f32 v74, v23, v74
v_add_f32 v74, v24, v74
mav 0,128,32
D 83,1024
mav 4,132,32
R 44,11264
v_add_f32 v74, v25, v74
mav 8,136,32
v_add_f32 v74, v26, v74
v_add_f32 v74, v27, v74
mav 12,140,32
s_waitcnt lgkmcnt(1)
v_add_f32 v74, v28, v74
v_add_f32 v74, v29, v74
mav 0,144,36
R 48,12288
v_add_f32 v74, v30, v74
mav 4,148,36
v_add_f32 v74, v31, v74
v_fmac_f32 v75, v31, v31
mav 8,152,36
mav 12,156,36
v_permlane32_swap_b32 v74, v75
mav 0,160,40
R 52,13312
v_add_f32 v74, v74, v75
mav 4,164,40
v_add_u32 v72, 1, v72
mav 8,168,40
mav 12,172,40
s_waitcnt lgkmcnt(1)
mav 0,176,44
D 83,2048
mav 4,180,44
R 56,14336
mav 8,184,44
mav 12,188,44
mav 0,192,48
D 83,3072
mav 4,196,48
R 60,15360
mav 8,200,48
mav 12,204,48
s_waitcnt lgkmcnt(1)
mav 0,208,52
buffer_store_dwordx4 v[64:67], v91, s[36:39], s54 offen sc1
mav 4,212,52
mav 8,216,52
mav 12,220,52
mav 0,224,56
buffer_store_dwordx4 v[68:71], v92, s[36:39], s54 offen sc1
mav 4,228,56
mav 8,232,56
mav 12,236,56
s_waitcnt lgkmcnt(0)
mav 0,240,60
buffer_load_dwordx4 v[164:167], v90, s[36:39], s49 offen sc1
mav 4,244,60
mav 8,248,60
mav 12,252,60
s_waitcnt vmcnt(18)
mvv 0,116,124
s_waitcnt vmcnt(12)
buffer_load_dwordx4 v[168:171], v90, s[36:39], s49 offen offset:1024 sc1
mvv 4,120,124
buffer_store_dword v72, v81, s[36:39], s52 offen sc1
mvv 8,116,128
s_sub_u32 s54, s54, 32768
mvv 12,120,128
mvv 0,116,132
mvv 4,120,132
mvv 8,116,136
s_add_u32 s49, s49, 313344
mvv 12,120,136
s_barrier
mvv 0,116,140
R 32,32768
mvv 4,120,140
s_waitcnt vmcnt(13)
ds_read_b128 v[204:207], v98 offset:0
mvv 8,116,144
mvv 12,120,144
s_waitcnt vmcnt(12)
mvv 0,116,148
buffer_load_dwordx4 v[172:175], v86, s[36:39], s49 offen sc1
mvv 4,120,148
s_waitcnt vmcnt(12)
R 36,33792
mvv 8,116,152
mvv 12,120,152
s_waitcnt vmcnt(11)
mvv 0,116,156
s_waitcnt lgkmcnt(1)
buffer_load_dwordx4 v[176:179], v86, s[36:39], s49 offen offset:1024 sc1
mvv 4,120,156
s_waitcnt vmcnt(11)
R 40,34816
v_add_f32 v204, v204, v205
mvv 8,116,160
v_add_f32 v206, v206, v207
v_add_f32 v204, v204, v206
mvv 12,120,160
v_mfma_f32_16x16x32_bf16 v[16:19], a[0:3], v[32:35], v[100:103]
buffer_load_dwordx4 v[180:183], v87, s[36:39], s49 offen sc1
v_mfma_f32_16x16x32_bf16 v[20:23], a[4:7], v[32:35], v[104:107]
R 44,35840
v_add_f32_dpp v205, v204, v204 quad_perm:[1,0,3,2] row_mask:0xf bank_mask:0xf
v_mfma_f32_16x16x32_bf16 v[24:27], a[8:11], v[32:35], v[108:111]
buffer_store_dword v205, v99, s[36:39], s49 offen sc1
v_mfma_f32_16x16x32_bf16 v[28:31], a[12:15], v[32:35], v[112:115]
s_waitcnt lgkmcnt(1)
v_cvt_pk_bf16_f32 v64, v0, v1
v_cvt_pk_bf16_f32 v65, v2, v3
mav 16,16,36
buffer_load_dwordx4 v[184:187], v87, s[36:39], s49 offen offset:1024 sc1
mav 20,20,36
R 48,36864
v_cvt_pk_bf16_f32 v66, v4, v5
mav 24,24,36
v_cvt_pk_bf16_f32 v67, v6, v7
v_cvt_pk_bf16_f32 v68, v8, v9
mav 28,28,36
v_cvt_pk_bf16_f32 v69, v10, v11
v_cvt_pk_bf16_f32 v70, v12, v13
mav 16,32,40
buffer_load_dwordx4 v[188:191], v88, s[36:39], s49 offen sc1
mav 20,36,40
R 52,37888
v_cvt_pk_bf16_f32 v71, v14, v15
mav 24,40,40
v_mul_f32 v75, v0, v0
v_fmac_f32 v75, v1, v1
mav 28,44,40
s_waitcnt lgkmcnt(1)
v_fmac_f32 v75, v2, v2
v_fmac_f32 v75, v3, v3
mav 16,48,44
buffer_load_dwordx4 v[192:195], v88, s[36:39], s49 offen offset:1024 sc1
mav 20,52,44
R 56,38912
v_fmac_f32 v75, v4, v4
mav 24,56,44
v_fmac_f32 v75, v5, v5
v_fmac_f32 v75, v6, v6
mav 28,60,44
v_fmac_f32 v75, v7, v7
v_fmac_f32 v75, v8, v8
mav 16,64,48
buffer_load_dwordx4 v[196:199], v89, s[36:39], s49 offen sc1
mav 20,68,48
R 60,39936
v_fmac_f32 v75, v9, v9
mav 24,72,48
v_fmac_f32 v75, v10, v10
v_fmac_f32 v75, v11, v11
mav 28,76,48
s_waitcnt lgkmcnt(1)
v_fmac_f32 v75, v12, v12
v_fmac_f32 v75, v13, v13
mav 16,80,52
buffer_load_dwordx4 v[200:203], v89, s[36:39], s49 offen offset:1024 sc1
mav 20,84,52
R 32,40960
v_fmac_f32 v75, v14, v14
mav 24,88,52
ds_write_b32 v97, v74 offset:1024
mav 28,92,52
ds_write_b128 v94, v[64:67] offset:0
mav 16,96,56
s_add_u32 m0, s50, 32768
R 36,41984
v_add_f32 v74, v1, v0
mav 20,100,56
ds_write_b128 v95, v[68:71] offset:0
mav 24,104,56
ds_read_b128 v[64:67], v96 offset:0
v_add_f32 v74, v2, v74
mav 28,108,56
s_waitcnt lgkmcnt(5)
ds_read_b128 v[68:71], v96 offset:1024
v_add_f32 v74, v3, v74
mav 16,112,60
D 83,0
mav 20,116,60
R 40,43008
v_add_f32 v74, v4, v74
mav 24,120,60
v_add_f32 v74, v5, v74
v_add_f32 v74, v6, v74
mav 28,124,60
v_add_f32 v74, v7, v74
v_add_f32 v74, v8, v74
mav 16,128,32
D 83,1024
LsR8:
s_cbranch_execz LsR12
mav 20,132,32
R 44,44032
v_add_f32 v74, v9, v74
mav 24,136,32
v_add_f32 v74, v10, v74
v_add_f32 v74, v11, v74
mav 28,140,32
s_waitcnt lgkmcnt(1)
v_add_f32 v74, v12, v74
v_add_f32 v74, v13, v74
mav 16,144,36
R 48,45056
v_add_f32 v74, v14, v74
mav 20,148,36
v_add_f32 v74, v15, v74
v_fmac_f32 v75, v15, v15
mav 24,152,36
mav 28,156,36
s_add_u32 s60, s60, 1
v_permlane32_swap_b32 v74, v75
mav 16,160,40
s_cbranch_scc0 LR1_1
mav 20,164,40
R 52,46080
v_add_f32 v74, v74, v75
mav 24,168,40
v_add_u32 v72, 1, v72
mav 28,172,40
s_waitcnt lgkmcnt(1)
mav 16,176,44
D 83,2048
mav 20,180,44
R 56,47104
mav 24,184,44
mav 28,188,44
mav 16,192,48
D 83,3072
mav 20,196,48
R 60,48128
mav 24,200,48
mav 28,204,48
s_waitcnt lgkmcnt(1)
mav 16,208,52
buffer_store_dwordx4 v[64:67], v91, s[36:39], s54 offen sc1
mav 20,212,52
mav 24,216,52
mav 28,220,52
mav 16,224,56
buffer_store_dwordx4 v[68:71], v92, s[36:39], s54 offen sc1
mav 20,228,56
mav 24,232,56
mav 28,236,56
s_waitcnt lgkmcnt(0)
mav 16,240,60
buffer_load_dwordx4 v[124:127], v90, s[36:39], s49 offen sc1
mav 20,244,60
mav 24,248,60
mav 28,252,60
s_waitcnt vmcnt(18)
mvv 16,116,164
s_waitcnt vmcnt(12)
buffer_load_dwordx4 v[128:131], v90, s[36:39], s49 offen offset:1024 sc1
mvv 20,120,164
buffer_store_dword v72, v81, s[36:39], s52 offen sc1
mvv 24,116,168
s_xor_b32 s50, s50, 0x10000
s_sub_u32 s54, s54, 32768
mvv 28,120,168
mvv 16,116,172
mvv 20,120,172
v_xor_b32 v80, 0x10000, v80
mvv 24,116,176
s_add_u32 s49, s49, 313344
mvv 28,120,176
s_barrier
mvv 16,116,180
R 32,0
mvv 20,120,180
s_waitcnt vmcnt(13)
ds_read_b128 v[204:207], v98 offset:1024
mvv 24,116,184
mvv 28,120,184
s_waitcnt vmcnt(12)
mvv 16,116,188
buffer_load_dwordx4 v[132:135], v86, s[36:39], s49 offen sc1
mvv 20,120,188
s_waitcnt vmcnt(12)
R 36,1024
mvv 24,116,192
mvv 28,120,192
s_waitcnt vmcnt(11)
mvv 16,116,196
s_waitcnt lgkmcnt(1)
buffer_load_dwordx4 v[136:139], v86, s[36:39], s49 offen offset:1024 sc1
mvv 20,120,196
s_waitcnt vmcnt(11)
R 40,2048
v_add_f32 v204, v204, v205
mvv 24,116,200
v_add_f32 v206, v206, v207
v_add_f32 v204, v204, v206
mvv 28,120,200
v_mfma_f32_16x16x32_bf16 v[0:3], a[0:3], v[32:35], v[100:103]
buffer_load_dwordx4 v[140:143], v87, s[36:39], s49 offen sc1
v_mfma_f32_16x16x32_bf16 v[4:7], a[4:7], v[32:35], v[104:107]
R 44,3072
v_add_f32_dpp v205, v204, v204 quad_perm:[1,0,3,2] row_mask:0xf bank_mask:0xf
v_mfma_f32_16x16x32_bf16 v[8:11], a[8:11], v[32:35], v[108:111]
buffer_store_dword v205, v99, s[36:39], s49 offen sc1
v_mfma_f32_16x16x32_bf16 v[12:15], a[12:15], v[32:35], v[112:115]
s_waitcnt lgkmcnt(1)
v_cvt_pk_bf16_f32 v64, v16, v17
v_cvt_pk_bf16_f32 v65, v18, v19
mav 0,16,36
buffer_load_dwordx4 v[144:147], v87, s[36:39], s49 offen offset:1024 sc1
mav 4,20,36
R 48,4096
v_cvt_pk_bf16_f32 v66, v20, v21
mav 8,24,36
v_cvt_pk_bf16_f32 v67, v22, v23
v_cvt_pk_bf16_f32 v68, v24, v25
mav 12,28,36
v_cvt_pk_bf16_f32 v69, v26, v27
v_cvt_pk_bf16_f32 v70, v28, v29
mav 0,32,40
buffer_load_dwordx4 v[148:151], v88, s[36:39], s49 offen sc1
mav 4,36,40
R 52,5120
v_cvt_pk_bf16_f32 v71, v30, v31
mav 8,40,40
v_mul_f32 v75, v16, v16
v_fmac_f32 v75, v17, v17
mav 12,44,40
s_waitcnt lgkmcnt(1)
v_fmac_f32 v75, v18, v18
v_fmac_f32 v75, v19, v19
mav 0,48,44
buffer_load_dwordx4 v[152:155], v88, s[36:39], s49 offen offset:1024 sc1
mav 4,52,44
R 56,6144
v_fmac_f32 v75, v20, v20
mav 8,56,44
v_fmac_f32 v75, v21, v21
v_fmac_f32 v75, v22, v22
mav 12,60,44
v_fmac_f32 v75, v23, v23
v_fmac_f32 v75, v24, v24
mav 0,64,48
buffer_load_dwordx4 v[156:159], v89, s[36:39], s49 offen sc1
mav 4,68,48
R 60,7168
v_fmac_f32 v75, v25, v25
mav 8,72,48
v_fmac_f32 v75, v26, v26
v_fmac_f32 v75, v27, v27
mav 12,76,48
s_waitcnt lgkmcnt(1)
v_fmac_f32 v75, v28, v28
v_fmac_f32 v75, v29, v29
mav 0,80,52
buffer_load_dwordx4 v[160:163], v89, s[36:39], s49 offen offset:1024 sc1
mav 4,84,52
R 32,8192
v_fmac_f32 v75, v30, v30
mav 8,88,52
ds_write_b32 v97, v74 offset:0
mav 12,92,52
ds_write_b128 v94, v[64:67] offset:0
mav 0,96,56
s_add_u32 m0, s50, 0
R 36,9216
v_add_f32 v74, v17, v16
mav 4,100,56
ds_write_b128 v95, v[68:71] offset:0
mav 8,104,56
ds_read_b128 v[64:67], v96 offset:0
v_add_f32 v74, v18, v74
mav 12,108,56
s_waitcnt lgkmcnt(5)
ds_read_b128 v[68:71], v96 offset:1024
v_add_f32 v74, v19, v74
mav 0,112,60
D 83,0
mav 4,116,60
R 40,10240
v_add_f32 v74, v20, v74
mav 8,120,60
v_add_f32 v74, v21, v74
v_add_f32 v74, v22, v74
mav 12,124,60
v_add_f32 v74, v23, v74
v_add_f32 v74, v24, v74
mav 0,128,32
D 83,1024
mav 4,132,32
R 44,11264
v_add_f32 v74, v25, v74
mav 8,136,32
v_add_f32 v74, v26, v74
v_add_f32 v74, v27, v74
mav 12,140,32
s_waitcnt lgkmcnt(1)
v_add_f32 v74, v28, v74
v_add_f32 v74, v29, v74
mav 0,144,36
R 48,12288
v_add_f32 v74, v30, v74
mav 4,148,36
v_add_f32 v74, v31, v74
v_fmac_f32 v75, v31, v31
mav 8,152,36
mav 12,156,36
v_permlane32_swap_b32 v74, v75
mav 0,160,40
R 52,13312
v_add_f32 v74, v74, v75
mav 4,164,40
v_add_u32 v72, 1, v72
mav 8,168,40
mav 12,172,40
s_waitcnt lgkmcnt(1)
mav 0,176,44
D 83,2048
mav 4,180,44
R 56,14336
mav 8,184,44
mav 12,188,44
mav 0,192,48
D 83,3072
mav 4,196,48
R 60,15360
mav 8,200,48
mav 12,204,48
s_waitcnt lgkmcnt(1)
mav 0,208,52
buffer_store_dwordx4 v[64:67], v91, s[36:39], s54 offen sc1
mav 4,212,52
mav 8,216,52
mav 12,220,52
mav 0,224,56
buffer_store_dwordx4 v[68:71], v92, s[36:39], s54 offen sc1
mav 4,228,56
mav 8,232,56
mav 12,236,56
s_waitcnt lgkmcnt(0)
mav 0,240,60
buffer_load_dwordx4 v[164:167], v90, s[36:39], s49 offen sc1
mav 4,244,60
mav 8,248,60
mav 12,252,60
s_waitcnt vmcnt(18)
mvv 0,116,124
s_waitcnt vmcnt(12)
buffer_load_dwordx4 v[168:171], v90, s[36:39], s49 offen offset:1024 sc1
mvv 4,120,124
buffer_store_dword v72, v81, s[36:39], s52 offen sc1
mvv 8,116,128
s_sub_u32 s54, s54, 3178496
mvv 12,120,128
mvv 0,116,132
mvv 4,120,132
mvv 8,116,136
s_add_u32 s49, s49, 313344
mvv 12,120,136
s_barrier
mvv 0,116,140
R 32,32768
mvv 4,120,140
s_waitcnt vmcnt(13)
ds_read_b128 v[204:207], v98 offset:0
mvv 8,116,144
mvv 12,120,144
s_waitcnt vmcnt(12)
mvv 0,116,148
buffer_load_dwordx4 v[172:175], v86, s[36:39], s49 offen sc1
mvv 4,120,148
s_waitcnt vmcnt(12)
R 36,33792
mvv 8,116,152
mvv 12,120,152
s_waitcnt vmcnt(11)
mvv 0,116,156
s_waitcnt lgkmcnt(1)
buffer_load_dwordx4 v[176:179], v86, s[36:39], s49 offen offset:1024 sc1
mvv 4,120,156
s_waitcnt vmcnt(11)
R 40,34816
v_add_f32 v204, v204, v205
mvv 8,116,160
v_add_f32 v206, v206, v207
v_add_f32 v204, v204, v206
mvv 12,120,160
v_mfma_f32_16x16x32_bf16 v[16:19], a[0:3], v[32:35], v[100:103]
buffer_load_dwordx4 v[180:183], v87, s[36:39], s49 offen sc1
v_mfma_f32_16x16x32_bf16 v[20:23], a[4:7], v[32:35], v[104:107]
R 44,35840
v_add_f32_dpp v205, v204, v204 quad_perm:[1,0,3,2] row_mask:0xf bank_mask:0xf
v_mfma_f32_16x16x32_bf16 v[24:27], a[8:11], v[32:35], v[108:111]
buffer_store_dword v205, v99, s[36:39], s49 offen sc1
v_mfma_f32_16x16x32_bf16 v[28:31], a[12:15], v[32:35], v[112:115]
s_waitcnt lgkmcnt(1)
v_cvt_pk_bf16_f32 v64, v0, v1
v_cvt_pk_bf16_f32 v65, v2, v3
mav 16,16,36
buffer_load_dwordx4 v[184:187], v87, s[36:39], s49 offen offset:1024 sc1
mav 20,20,36
R 48,36864
v_cvt_pk_bf16_f32 v66, v4, v5
mav 24,24,36
v_cvt_pk_bf16_f32 v67, v6, v7
v_cvt_pk_bf16_f32 v68, v8, v9
mav 28,28,36
v_cvt_pk_bf16_f32 v69, v10, v11
v_cvt_pk_bf16_f32 v70, v12, v13
mav 16,32,40
buffer_load_dwordx4 v[188:191], v88, s[36:39], s49 offen sc1
mav 20,36,40
R 52,37888
v_cvt_pk_bf16_f32 v71, v14, v15
mav 24,40,40
mav 28,44,40
s_waitcnt lgkmcnt(1)
mav 16,48,44
buffer_load_dwordx4 v[192:195], v88, s[36:39], s49 offen offset:1024 sc1
mav 20,52,44
R 56,38912
mav 24,56,44
mav 28,60,44
mav 16,64,48
buffer_load_dwordx4 v[196:199], v89, s[36:39], s49 offen sc1
mav 20,68,48
R 60,39936
mav 24,72,48
mav 28,76,48
s_waitcnt lgkmcnt(1)
mav 16,80,52
buffer_load_dwordx4 v[200:203], v89, s[36:39], s49 offen offset:1024 sc1
mav 20,84,52
R 32,40960
mav 24,88,52
ds_write_b32 v97, v74 offset:1024
mav 28,92,52
ds_write_b128 v94, v[64:67] offset:0
mav 16,96,56
s_add_u32 m0, s50, 32768
R 36,41984
mav 20,100,56
ds_write_b128 v95, v[68:71] offset:0
mav 24,104,56
ds_read_b128 v[64:67], v96 offset:0
mav 28,108,56
s_waitcnt lgkmcnt(5)
ds_read_b128 v[68:71], v96 offset:1024
mav 16,112,60
D 83,0
mav 20,116,60
R 40,43008
mav 24,120,60
mav 28,124,60
mav 16,128,32
D 83,1024
mav 20,132,32
R 44,44032
mav 24,136,32
mav 28,140,32
s_waitcnt lgkmcnt(1)
mav 16,144,36
R 48,45056
mav 20,148,36
mav 24,152,36
mav 28,156,36
mav 16,160,40
mav 20,164,40
R 52,46080
v_add_u32 v72, 1, v72
mav 24,168,40
mav 28,172,40
s_waitcnt lgkmcnt(1)
mav 16,176,44
D 83,2048
mav 20,180,44
R 56,47104
mav 24,184,44
mav 28,188,44
mav 16,192,48
D 83,3072
mav 20,196,48
R 60,48128
mav 24,200,48
mav 28,204,48
s_waitcnt lgkmcnt(1)
mav 16,208,52
buffer_store_dwordx4 v[64:67], v91, s[44:47], s54 offen sc1
mav 20,212,52
mav 24,216,52
mav 28,220,52
mav 16,224,56
buffer_store_dwordx4 v[68:71], v92, s[44:47], s54 offen sc1
mav 20,228,56
mav 24,232,56
mav 28,236,56
s_waitcnt lgkmcnt(0)
mav 16,240,60
buffer_load_dwordx4 v[124:127], v90, s[36:39], s49 offen sc1
mav 20,244,60
mav 24,248,60
mav 28,252,60
s_waitcnt vmcnt(18)
mvv 16,116,164
s_waitcnt vmcnt(12)
buffer_load_dwordx4 v[128:131], v90, s[36:39], s49 offen offset:1024 sc1
mvv 20,120,164
buffer_store_dword v72, v81, s[36:39], s52 offen sc1
mvv 24,116,168
s_xor_b32 s50, s50, 0x10000
s_sub_u32 s54, s54, 32768
mvv 28,120,168
mvv 16,116,172
mvv 20,120,172
v_xor_b32 v80, 0x10000, v80
mvv 24,116,176
s_add_u32 s49, s49, 313344
mvv 28,120,176
s_barrier
mvv 16,116,180
R 32,0
mvv 20,120,180
s_waitcnt vmcnt(13)
ds_read_b128 v[204:207], v98 offset:1024
mvv 24,116,184
mvv 28,120,184
s_waitcnt vmcnt(12)
mvv 16,116,188
buffer_load_dwordx4 v[132:135], v86, s[36:39], s49 offen sc1
mvv 20,120,188
s_waitcnt vmcnt(12)
R 36,1024
mvv 24,116,192
mvv 28,120,192
s_waitcnt vmcnt(11)
mvv 16,116,196
s_waitcnt lgkmcnt(1)
buffer_load_dwordx4 v[136:139], v86, s[36:39], s49 offen offset:1024 sc1
mvv 20,120,196
s_waitcnt vmcnt(11)
R 40,2048
v_add_f32 v204, v204, v205
mvv 24,116,200
v_add_f32 v206, v206, v207
v_add_f32 v204, v204, v206
mvv 28,120,200
v_mfma_f32_16x16x32_bf16 v[0:3], a[0:3], v[32:35], v[100:103]
buffer_load_dwordx4 v[140:143], v87, s[36:39], s49 offen sc1
v_mfma_f32_16x16x32_bf16 v[4:7], a[4:7], v[32:35], v[104:107]
R 44,3072
v_add_f32_dpp v205, v204, v204 quad_perm:[1,0,3,2] row_mask:0xf bank_mask:0xf
v_mfma_f32_16x16x32_bf16 v[8:11], a[8:11], v[32:35], v[108:111]
buffer_store_dword v205, v99, s[36:39], s49 offen sc1
v_mfma_f32_16x16x32_bf16 v[12:15], a[12:15], v[32:35], v[112:115]
s_waitcnt lgkmcnt(1)
v_cvt_pk_bf16_f32 v64, v16, v17
v_cvt_pk_bf16_f32 v65, v18, v19
mav 0,16,36
buffer_load_dwordx4 v[144:147], v87, s[36:39], s49 offen offset:1024 sc1
mav 4,20,36
R 48,4096
v_cvt_pk_bf16_f32 v66, v20, v21
mav 8,24,36
v_cvt_pk_bf16_f32 v67, v22, v23
v_cvt_pk_bf16_f32 v68, v24, v25
mav 12,28,36
v_cvt_pk_bf16_f32 v69, v26, v27
v_cvt_pk_bf16_f32 v70, v28, v29
mav 0,32,40
buffer_load_dwordx4 v[148:151], v88, s[36:39], s49 offen sc1
mav 4,36,40
R 52,5120
v_cvt_pk_bf16_f32 v71, v30, v31
mav 8,40,40
mav 12,44,40
s_waitcnt lgkmcnt(1)
mav 0,48,44
buffer_load_dwordx4 v[152:155], v88, s[36:39], s49 offen offset:1024 sc1
mav 4,52,44
R 56,6144
mav 8,56,44
mav 12,60,44
mav 0,64,48
buffer_load_dwordx4 v[156:159], v89, s[36:39], s49 offen sc1
mav 4,68,48
R 60,7168
mav 8,72,48
mav 12,76,48
s_waitcnt lgkmcnt(1)
mav 0,80,52
buffer_load_dwordx4 v[160:163], v89, s[36:39], s49 offen offset:1024 sc1
mav 4,84,52
R 32,8192
mav 8,88,52
s_add_u32 m0, s50, 0
mav 12,92,52
ds_write_b128 v94, v[64:67] offset:0
mav 0,96,56
D 83,0
mav 4,100,56
R 36,9216
mav 8,104,56
ds_write_b128 v95, v[68:71] offset:0
mav 12,108,56
s_waitcnt lgkmcnt(3)
ds_read_b128 v[64:67], v96 offset:0
mav 0,112,60
D 83,1024
mav 4,116,60
R 40,10240
mav 8,120,60
ds_read_b128 v[68:71], v96 offset:1024
mav 12,124,60
mav 0,128,32
R 44,11264
mav 4,132,32
mav 8,136,32
mav 12,140,32
s_waitcnt lgkmcnt(2)
mav 0,144,36
R 48,12288
mav 4,148,36
mav 8,152,36
mav 12,156,36
mav 0,160,40
R 52,13312
v_add_u32 v72, 68, v72
mav 4,164,40
mav 8,168,40
mav 12,172,40
s_waitcnt lgkmcnt(1)
mav 0,176,44
D 83,2048
mav 4,180,44
R 56,14336
mav 8,184,44
mav 12,188,44
mav 0,192,48
D 83,3072
mav 4,196,48
R 60,15360
mav 8,200,48
mav 12,204,48
s_waitcnt lgkmcnt(1)
mav 0,208,52
buffer_store_dwordx4 v[64:67], v91, s[44:47], s54 offen sc1
mav 4,212,52
mav 8,216,52
mav 12,220,52
mav 0,224,56
buffer_store_dwordx4 v[68:71], v92, s[44:47], s54 offen sc1
mav 4,228,56
mav 8,232,56
mav 12,236,56
s_waitcnt lgkmcnt(0)
mav 0,240,60
buffer_load_dwordx4 v[164:167], v90, s[36:39], s49 offen sc1
mav 4,244,60
mav 8,248,60
mav 12,252,60
s_waitcnt vmcnt(18)
mvv 0,116,124
s_waitcnt vmcnt(12)
buffer_load_dwordx4 v[168:171], v90, s[36:39], s49 offen offset:1024 sc1
LsR9:
s_cbranch_execz LsR13
mvv 4,120,124
buffer_store_dword v72, v81, s[36:39], s52 offen sc1
mvv 8,116,128
s_sub_u32 s54, s54, 32768
mvv 12,120,128
mvv 0,116,132
mvv 4,120,132
mvv 8,116,136
s_add_u32 s49, s49, 313344
mvv 12,120,136
s_barrier
mvv 0,116,140
R 32,32768
mvv 4,120,140
s_waitcnt vmcnt(13)
mvv 8,116,144
mvv 12,120,144
s_waitcnt vmcnt(12)
mvv 0,116,148
buffer_load_dwordx4 v[172:175], v86, s[36:39], s49 offen sc1
mvv 4,120,148
s_waitcnt vmcnt(12)
R 36,33792
mvv 8,116,152
mvv 12,120,152
s_waitcnt vmcnt(11)
mvv 0,116,156
buffer_load_dwordx4 v[176:179], v86, s[36:39], s49 offen offset:1024 sc1
mvv 4,120,156
s_waitcnt vmcnt(11)
R 40,34816
mvv 8,116,160
mvv 12,120,160
s_waitcnt lgkmcnt(1)
v_mfma_f32_16x16x32_bf16 v[16:19], a[0:3], v[32:35], v[100:103]
buffer_load_dwordx4 v[180:183], v87, s[36:39], s49 offen sc1
v_mfma_f32_16x16x32_bf16 v[20:23], a[4:7], v[32:35], v[104:107]
R 44,35840
v_cvt_pk_bf16_f32 v64, v0, v1
v_mfma_f32_16x16x32_bf16 v[24:27], a[8:11], v[32:35], v[108:111]
v_cvt_pk_bf16_f32 v65, v2, v3
v_cvt_pk_bf16_f32 v66, v4, v5
v_mfma_f32_16x16x32_bf16 v[28:31], a[12:15], v[32:35], v[112:115]
v_cvt_pk_bf16_f32 v67, v6, v7
v_cvt_pk_bf16_f32 v68, v8, v9
mav 16,16,36
buffer_load_dwordx4 v[184:187], v87, s[36:39], s49 offen offset:1024 sc1
mav 20,20,36
R 48,36864
v_cvt_pk_bf16_f32 v69, v10, v11
mav 24,24,36
v_cvt_pk_bf16_f32 v70, v12, v13
v_cvt_pk_bf16_f32 v71, v14, v15
mav 28,28,36
s_waitcnt lgkmcnt(1)
mav 16,32,40
buffer_load_dwordx4 v[188:191], v88, s[36:39], s49 offen sc1
mav 20,36,40
R 52,37888
mav 24,40,40
mav 28,44,40
mav 16,48,44
buffer_load_dwordx4 v[192:195], v88, s[36:39], s49 offen offset:1024 sc1
mav 20,52,44
R 56,38912
mav 24,56,44
mav 28,60,44
s_waitcnt lgkmcnt(1)
mav 16,64,48
buffer_load_dwordx4 v[196:199], v89, s[36:39], s49 offen sc1
mav 20,68,48
R 60,39936
mav 24,72,48
mav 28,76,48
mav 16,80,52
buffer_load_dwordx4 v[200:203], v89, s[36:39], s49 offen offset:1024 sc1
mav 20,84,52
R 32,40960
mav 24,88,52
s_add_u32 m0, s50, 32768
mav 28,92,52
s_waitcnt lgkmcnt(1)
ds_write_b128 v94, v[64:67] offset:0
mav 16,96,56
D 83,0
mav 20,100,56
R 36,41984
mav 24,104,56
ds_write_b128 v95, v[68:71] offset:0
mav 28,108,56
ds_read_b128 v[64:67], v96 offset:0
mav 16,112,60
D 83,1024
mav 20,116,60
R 40,43008
mav 24,120,60
ds_read_b128 v[68:71], v96 offset:1024
mav 28,124,60
s_waitcnt lgkmcnt(4)
mav 16,128,32
R 44,44032
mav 20,132,32
mav 24,136,32
mav 28,140,32
mav 16,144,36
R 48,45056
mav 20,148,36
mav 24,152,36
mav 28,156,36
s_waitcnt lgkmcnt(1)
mav 16,160,40
mav 20,164,40
R 52,46080
mav 24,168,40
mav 28,172,40
mav 16,176,44
D 83,2048
mav 20,180,44
R 56,47104
mav 24,184,44
mav 28,188,44
s_waitcnt lgkmcnt(1)
mav 16,192,48
D 83,3072
mav 20,196,48
R 60,48128
mav 24,200,48
mav 28,204,48
mav 16,208,52
buffer_store_dwordx4 v[64:67], v91, s[44:47], s54 offen sc1
mav 20,212,52
mav 24,216,52
mav 28,220,52
s_waitcnt lgkmcnt(0)
mav 16,224,56
buffer_store_dwordx4 v[68:71], v92, s[44:47], s54 offen sc1
mav 20,228,56
mav 24,232,56
mav 28,236,56
mav 16,240,60
buffer_load_dwordx4 v[124:127], v90, s[36:39], s49 offen sc1
mav 20,244,60
mav 24,248,60
mav 28,252,60
s_waitcnt vmcnt(17)
mvv 16,116,164
buffer_load_dwordx4 v[128:131], v90, s[36:39], s49 offen offset:1024 sc1
mvv 20,120,164
s_waitcnt vmcnt(17)
mvv 24,116,168
s_xor_b32 s50, s50, 0x10000
s_sub_u32 s54, s54, 32768
mvv 28,120,168
s_waitcnt vmcnt(15)
mvv 16,116,172
mvv 20,120,172
s_waitcnt vmcnt(14)
v_xor_b32 v80, 0x10000, v80
mvv 24,116,176
s_waitcnt vmcnt(13)
s_add_u32 s49, s49, 313344
mvv 28,120,176
s_barrier
mvv 16,116,180
R 32,0
mvv 20,120,180
s_waitcnt vmcnt(12)
mvv 24,116,184
mvv 28,120,184
s_waitcnt vmcnt(11)
mvv 16,116,188
buffer_load_dwordx4 v[132:135], v86, s[36:39], s49 offen sc1
mvv 20,120,188
s_waitcnt vmcnt(11)
R 36,1024
mvv 24,116,192
mvv 28,120,192
s_waitcnt vmcnt(10)
mvv 16,116,196
buffer_load_dwordx4 v[136:139], v86, s[36:39], s49 offen offset:1024 sc1
mvv 20,120,196
s_waitcnt vmcnt(10)
R 40,2048
mvv 24,116,200
mvv 28,120,200
s_waitcnt lgkmcnt(1)
v_mfma_f32_16x16x32_bf16 v[0:3], a[0:3], v[32:35], v[100:103]
buffer_load_dwordx4 v[140:143], v87, s[36:39], s49 offen sc1
v_mfma_f32_16x16x32_bf16 v[4:7], a[4:7], v[32:35], v[104:107]
R 44,3072
v_cvt_pk_bf16_f32 v64, v16, v17
v_mfma_f32_16x16x32_bf16 v[8:11], a[8:11], v[32:35], v[108:111]
v_cvt_pk_bf16_f32 v65, v18, v19
v_cvt_pk_bf16_f32 v66, v20, v21
v_mfma_f32_16x16x32_bf16 v[12:15], a[12:15], v[32:35], v[112:115]
v_cvt_pk_bf16_f32 v67, v22, v23
v_cvt_pk_bf16_f32 v68, v24, v25
mav 0,16,36
buffer_load_dwordx4 v[144:147], v87, s[36:39], s49 offen offset:1024 sc1
mav 4,20,36
R 48,4096
v_cvt_pk_bf16_f32 v69, v26, v27
mav 8,24,36
v_cvt_pk_bf16_f32 v70, v28, v29
v_cvt_pk_bf16_f32 v71, v30, v31
mav 12,28,36
s_waitcnt lgkmcnt(1)
mav 0,32,40
buffer_load_dwordx4 v[148:151], v88, s[36:39], s49 offen sc1
mav 4,36,40
R 52,5120
mav 8,40,40
mav 12,44,40
mav 0,48,44
buffer_load_dwordx4 v[152:155], v88, s[36:39], s49 offen offset:1024 sc1
mav 4,52,44
R 56,6144
mav 8,56,44
mav 12,60,44
s_waitcnt lgkmcnt(1)
mav 0,64,48
buffer_load_dwordx4 v[156:159], v89, s[36:39], s49 offen sc1
mav 4,68,48
R 60,7168
mav 8,72,48
mav 12,76,48
mav 0,80,52
buffer_load_dwordx4 v[160:163], v89, s[36:39], s49 offen offset:1024 sc1
mav 4,84,52
R 32,8192
mav 8,88,52
s_add_u32 m0, s50, 0
mav 12,92,52
s_waitcnt lgkmcnt(1)
ds_write_b128 v94, v[64:67] offset:0
mav 0,96,56
D 83,0
mav 4,100,56
R 36,9216
mav 8,104,56
ds_write_b128 v95, v[68:71] offset:0
mav 12,108,56
ds_read_b128 v[64:67], v96 offset:0
mav 0,112,60
D 83,1024
mav 4,116,60
R 40,10240
mav 8,120,60
ds_read_b128 v[68:71], v96 offset:1024
mav 12,124,60
s_waitcnt lgkmcnt(4)
mav 0,128,32
R 44,11264
mav 4,132,32
mav 8,136,32
mav 12,140,32
mav 0,144,36
R 48,12288
mav 4,148,36
mav 8,152,36
mav 12,156,36
s_waitcnt lgkmcnt(1)
mav 0,160,40
R 52,13312
mav 4,164,40
mav 8,168,40
mav 12,172,40
mav 0,176,44
D 83,2048
mav 4,180,44
R 56,14336
mav 8,184,44
mav 12,188,44
s_waitcnt lgkmcnt(1)
mav 0,192,48
D 83,3072
mav 4,196,48
R 60,15360
mav 8,200,48
mav 12,204,48
mav 0,208,52
buffer_store_dwordx4 v[64:67], v91, s[44:47], s54 offen sc1
mav 4,212,52
mav 8,216,52
mav 12,220,52
s_waitcnt lgkmcnt(0)
mav 0,224,56
buffer_store_dwordx4 v[68:71], v92, s[44:47], s54 offen sc1
mav 4,228,56
mav 8,232,56
mav 12,236,56
mav 0,240,60
buffer_load_dwordx4 v[164:167], v90, s[36:39], s49 offen sc1
mav 4,244,60
mav 8,248,60
mav 12,252,60
s_waitcnt vmcnt(16)
mvv 0,116,124
buffer_load_dwordx4 v[168:171], v90, s[36:39], s49 offen offset:1024 sc1
mvv 4,120,124
s_waitcnt vmcnt(16)
mvv 8,116,128
s_sub_u32 s54, s54, 32768
mvv 12,120,128
s_waitcnt vmcnt(15)
mvv 0,116,132
mvv 4,120,132
s_waitcnt vmcnt(14)
mvv 8,116,136
s_waitcnt vmcnt(13)
s_add_u32 s49, s49, 313344
mvv 12,120,136
s_barrier
mvv 0,116,140
R 32,32768
mvv 4,120,140
s_waitcnt vmcnt(12)
mvv 8,116,144
mvv 12,120,144
s_waitcnt vmcnt(11)
mvv 0,116,148
buffer_load_dwordx4 v[172:175], v86, s[36:39], s49 offen sc1
mvv 4,120,148
s_waitcnt vmcnt(11)
R 36,33792
mvv 8,116,152
mvv 12,120,152
s_waitcnt vmcnt(10)
mvv 0,116,156
buffer_load_dwordx4 v[176:179], v86, s[36:39], s49 offen offset:1024 sc1
mvv 4,120,156
s_waitcnt vmcnt(10)
R 40,34816
mvv 8,116,160
mvv 12,120,160
s_waitcnt lgkmcnt(1)
v_mfma_f32_16x16x32_bf16 v[16:19], a[0:3], v[32:35], v[100:103]
buffer_load_dwordx4 v[180:183], v87, s[36:39], s49 offen sc1
v_mfma_f32_16x16x32_bf16 v[20:23], a[4:7], v[32:35], v[104:107]
R 44,35840
v_cvt_pk_bf16_f32 v64, v0, v1
v_mfma_f32_16x16x32_bf16 v[24:27], a[8:11], v[32:35], v[108:111]
v_cvt_pk_bf16_f32 v65, v2, v3
v_cvt_pk_bf16_f32 v66, v4, v5
v_mfma_f32_16x16x32_bf16 v[28:31], a[12:15], v[32:35], v[112:115]
v_cvt_pk_bf16_f32 v67, v6, v7
v_cvt_pk_bf16_f32 v68, v8, v9
mav 16,16,36
buffer_load_dwordx4 v[184:187], v87, s[36:39], s49 offen offset:1024 sc1
mav 20,20,36
R 48,36864
v_cvt_pk_bf16_f32 v69, v10, v11
mav 24,24,36
v_cvt_pk_bf16_f32 v70, v12, v13
v_cvt_pk_bf16_f32 v71, v14, v15
mav 28,28,36
s_waitcnt lgkmcnt(1)
mav 16,32,40
buffer_load_dwordx4 v[188:191], v88, s[36:39], s49 offen sc1
mav 20,36,40
R 52,37888
mav 24,40,40
mav 28,44,40
mav 16,48,44
buffer_load_dwordx4 v[192:195], v88, s[36:39], s49 offen offset:1024 sc1
mav 20,52,44
R 56,38912
mav 24,56,44
mav 28,60,44
s_waitcnt lgkmcnt(1)
mav 16,64,48
buffer_load_dwordx4 v[196:199], v89, s[36:39], s49 offen sc1
mav 20,68,48
R 60,39936
mav 24,72,48
mav 28,76,48
mav 16,80,52
buffer_load_dwordx4 v[200:203], v89, s[36:39], s49 offen offset:1024 sc1
mav 20,84,52
R 32,40960
mav 24,88,52
s_add_u32 m0, s50, 32768
mav 28,92,52
s_waitcnt lgkmcnt(1)
ds_write_b128 v94, v[64:67] offset:0
mav 16,96,56
D 83,0
mav 20,100,56
R 36,41984
mav 24,104,56
ds_write_b128 v95, v[68:71] offset:0
mav 28,108,56
ds_read_b128 v[64:67], v96 offset:0
mav 16,112,60
D 83,1024
mav 20,116,60
R 40,43008
mav 24,120,60
ds_read_b128 v[68:71], v96 offset:1024
mav 28,124,60
s_waitcnt lgkmcnt(4)
mav 16,128,32
R 44,44032
mav 20,132,32
mav 24,136,32
mav 28,140,32
mav 16,144,36
R 48,45056
mav 20,148,36
mav 24,152,36
mav 28,156,36
s_waitcnt lgkmcnt(1)
mav 16,160,40
s_mov_b32 s60, -12
.p2align 6
LR1_2:
mav 20,164,40
R 52,46080
mav 24,168,40
mav 28,172,40
mav 16,176,44
D 83,2048
mav 20,180,44
R 56,47104
mav 24,184,44
mav 28,188,44
s_waitcnt lgkmcnt(1)
mav 16,192,48
D 83,3072
mav 20,196,48
R 60,48128
mav 24,200,48
mav 28,204,48
mav 16,208,52
buffer_store_dwordx4 v[64:67], v91, s[44:47], s54 offen sc1
mav 20,212,52
mav 24,216,52
mav 28,220,52
s_waitcnt lgkmcnt(0)
mav 16,224,56
buffer_store_dwordx4 v[68:71], v92, s[44:47], s54 offen sc1
mav 20,228,56
mav 24,232,56
mav 28,236,56
mav 16,240,60
buffer_load_dwordx4 v[124:127], v90, s[36:39], s49 offen sc1
mav 20,244,60
mav 24,248,60
mav 28,252,60
s_waitcnt vmcnt(16)
mvv 16,116,164
buffer_load_dwordx4 v[128:131], v90, s[36:39], s49 offen offset:1024 sc1
mvv 20,120,164
s_waitcnt vmcnt(16)
mvv 24,116,168
s_xor_b32 s50, s50, 0x10000
s_sub_u32 s54, s54, 32768
mvv 28,120,168
s_waitcnt vmcnt(15)
mvv 16,116,172
mvv 20,120,172
s_waitcnt vmcnt(14)
v_xor_b32 v80, 0x10000, v80
mvv 24,116,176
s_waitcnt vmcnt(13)
s_add_u32 s49, s49, 313344
mvv 28,120,176
s_barrier
mvv 16,116,180
R 32,0
mvv 20,120,180
s_waitcnt vmcnt(12)
mvv 24,116,184
mvv 28,120,184
s_waitcnt vmcnt(11)
mvv 16,116,188
buffer_load_dwordx4 v[132:135], v86, s[36:39], s49 offen sc1
mvv 20,120,188
s_waitcnt vmcnt(11)
R 36,1024
mvv 24,116,192
mvv 28,120,192
s_waitcnt vmcnt(10)
mvv 16,116,196
buffer_load_dwordx4 v[136:139], v86, s[36:39], s49 offen offset:1024 sc1
mvv 20,120,196
s_waitcnt vmcnt(10)
R 40,2048
mvv 24,116,200
mvv 28,120,200
s_waitcnt lgkmcnt(1)
v_mfma_f32_16x16x32_bf16 v[0:3], a[0:3], v[32:35], v[100:103]
buffer_load_dwordx4 v[140:143], v87, s[36:39], s49 offen sc1
v_mfma_f32_16x16x32_bf16 v[4:7], a[4:7], v[32:35], v[104:107]
R 44,3072
v_cvt_pk_bf16_f32 v64, v16, v17
v_mfma_f32_16x16x32_bf16 v[8:11], a[8:11], v[32:35], v[108:111]
v_cvt_pk_bf16_f32 v65, v18, v19
v_cvt_pk_bf16_f32 v66, v20, v21
v_mfma_f32_16x16x32_bf16 v[12:15], a[12:15], v[32:35], v[112:115]
v_cvt_pk_bf16_f32 v67, v22, v23
v_cvt_pk_bf16_f32 v68, v24, v25
mav 0,16,36
buffer_load_dwordx4 v[144:147], v87, s[36:39], s49 offen offset:1024 sc1
mav 4,20,36
R 48,4096
v_cvt_pk_bf16_f32 v69, v26, v27
mav 8,24,36
v_cvt_pk_bf16_f32 v70, v28, v29
v_cvt_pk_bf16_f32 v71, v30, v31
mav 12,28,36
s_waitcnt lgkmcnt(1)
mav 0,32,40
buffer_load_dwordx4 v[148:151], v88, s[36:39], s49 offen sc1
mav 4,36,40
R 52,5120
mav 8,40,40
mav 12,44,40
mav 0,48,44
buffer_load_dwordx4 v[152:155], v88, s[36:39], s49 offen offset:1024 sc1
mav 4,52,44
R 56,6144
mav 8,56,44
mav 12,60,44
s_waitcnt lgkmcnt(1)
mav 0,64,48
buffer_load_dwordx4 v[156:159], v89, s[36:39], s49 offen sc1
mav 4,68,48
R 60,7168
mav 8,72,48
mav 12,76,48
mav 0,80,52
buffer_load_dwordx4 v[160:163], v89, s[36:39], s49 offen offset:1024 sc1
mav 4,84,52
R 32,8192
mav 8,88,52
s_add_u32 m0, s50, 0
mav 12,92,52
s_waitcnt lgkmcnt(1)
ds_write_b128 v94, v[64:67] offset:0
mav 0,96,56
D 83,0
mav 4,100,56
R 36,9216
mav 8,104,56
LsR10:
s_cbranch_execz LsR14
ds_write_b128 v95, v[68:71] offset:0
mav 12,108,56
ds_read_b128 v[64:67], v96 offset:0
mav 0,112,60
D 83,1024
mav 4,116,60
R 40,10240
mav 8,120,60
ds_read_b128 v[68:71], v96 offset:1024
mav 12,124,60
s_waitcnt lgkmcnt(4)
mav 0,128,32
R 44,11264
mav 4,132,32
mav 8,136,32
mav 12,140,32
mav 0,144,36
R 48,12288
mav 4,148,36
mav 8,152,36
mav 12,156,36
s_waitcnt lgkmcnt(1)
mav 0,160,40
R 52,13312
mav 4,164,40
mav 8,168,40
mav 12,172,40
mav 0,176,44
D 83,2048
mav 4,180,44
R 56,14336
mav 8,184,44
mav 12,188,44
s_waitcnt lgkmcnt(1)
mav 0,192,48
D 83,3072
mav 4,196,48
R 60,15360
mav 8,200,48
mav 12,204,48
mav 0,208,52
buffer_store_dwordx4 v[64:67], v91, s[44:47], s54 offen sc1
mav 4,212,52
mav 8,216,52
mav 12,220,52
s_waitcnt lgkmcnt(0)
mav 0,224,56
buffer_store_dwordx4 v[68:71], v92, s[44:47], s54 offen sc1
mav 4,228,56
mav 8,232,56
mav 12,236,56
mav 0,240,60
buffer_load_dwordx4 v[164:167], v90, s[36:39], s49 offen sc1
mav 4,244,60
mav 8,248,60
mav 12,252,60
s_waitcnt vmcnt(16)
mvv 0,116,124
buffer_load_dwordx4 v[168:171], v90, s[36:39], s49 offen offset:1024 sc1
mvv 4,120,124
s_waitcnt vmcnt(16)
mvv 8,116,128
s_sub_u32 s54, s54, 32768
mvv 12,120,128
s_waitcnt vmcnt(15)
mvv 0,116,132
mvv 4,120,132
s_waitcnt vmcnt(14)
mvv 8,116,136
s_waitcnt vmcnt(13)
s_add_u32 s49, s49, 313344
mvv 12,120,136
s_barrier
mvv 0,116,140
R 32,32768
mvv 4,120,140
s_waitcnt vmcnt(12)
mvv 8,116,144
mvv 12,120,144
s_waitcnt vmcnt(11)
mvv 0,116,148
buffer_load_dwordx4 v[172:175], v86, s[36:39], s49 offen sc1
mvv 4,120,148
s_waitcnt vmcnt(11)
R 36,33792
mvv 8,116,152
mvv 12,120,152
s_waitcnt vmcnt(10)
mvv 0,116,156
buffer_load_dwordx4 v[176:179], v86, s[36:39], s49 offen offset:1024 sc1
mvv 4,120,156
s_waitcnt vmcnt(10)
R 40,34816
mvv 8,116,160
mvv 12,120,160
s_waitcnt lgkmcnt(1)
v_mfma_f32_16x16x32_bf16 v[16:19], a[0:3], v[32:35], v[100:103]
buffer_load_dwordx4 v[180:183], v87, s[36:39], s49 offen sc1
v_mfma_f32_16x16x32_bf16 v[20:23], a[4:7], v[32:35], v[104:107]
R 44,35840
v_cvt_pk_bf16_f32 v64, v0, v1
v_mfma_f32_16x16x32_bf16 v[24:27], a[8:11], v[32:35], v[108:111]
v_cvt_pk_bf16_f32 v65, v2, v3
v_cvt_pk_bf16_f32 v66, v4, v5
v_mfma_f32_16x16x32_bf16 v[28:31], a[12:15], v[32:35], v[112:115]
v_cvt_pk_bf16_f32 v67, v6, v7
v_cvt_pk_bf16_f32 v68, v8, v9
mav 16,16,36
buffer_load_dwordx4 v[184:187], v87, s[36:39], s49 offen offset:1024 sc1
mav 20,20,36
R 48,36864
v_cvt_pk_bf16_f32 v69, v10, v11
mav 24,24,36
v_cvt_pk_bf16_f32 v70, v12, v13
v_cvt_pk_bf16_f32 v71, v14, v15
mav 28,28,36
s_waitcnt lgkmcnt(1)
mav 16,32,40
buffer_load_dwordx4 v[188:191], v88, s[36:39], s49 offen sc1
mav 20,36,40
R 52,37888
mav 24,40,40
mav 28,44,40
mav 16,48,44
buffer_load_dwordx4 v[192:195], v88, s[36:39], s49 offen offset:1024 sc1
mav 20,52,44
R 56,38912
mav 24,56,44
mav 28,60,44
s_waitcnt lgkmcnt(1)
mav 16,64,48
buffer_load_dwordx4 v[196:199], v89, s[36:39], s49 offen sc1
mav 20,68,48
R 60,39936
mav 24,72,48
mav 28,76,48
mav 16,80,52
buffer_load_dwordx4 v[200:203], v89, s[36:39], s49 offen offset:1024 sc1
mav 20,84,52
R 32,40960
mav 24,88,52
s_add_u32 m0, s50, 32768
mav 28,92,52
s_waitcnt lgkmcnt(1)
ds_write_b128 v94, v[64:67] offset:0
mav 16,96,56
D 83,0
mav 20,100,56
R 36,41984
mav 24,104,56
ds_write_b128 v95, v[68:71] offset:0
mav 28,108,56
ds_read_b128 v[64:67], v96 offset:0
mav 16,112,60
D 83,1024
mav 20,116,60
R 40,43008
mav 24,120,60
ds_read_b128 v[68:71], v96 offset:1024
mav 28,124,60
s_waitcnt lgkmcnt(4)
mav 16,128,32
R 44,44032
mav 20,132,32
mav 24,136,32
mav 28,140,32
mav 16,144,36
R 48,45056
mav 20,148,36
mav 24,152,36
mav 28,156,36
s_waitcnt lgkmcnt(1)
s_add_u32 s60, s60, 1
mav 16,160,40
s_cbranch_scc0 LR1_2
mav 20,164,40
R 52,46080
mav 24,168,40
mav 28,172,40
mav 16,176,44
D 83,2048
mav 20,180,44
R 56,47104
mav 24,184,44
mav 28,188,44
s_waitcnt lgkmcnt(1)
mav 16,192,48
D 83,3072
mav 20,196,48
R 60,48128
mav 24,200,48
mav 28,204,48
mav 16,208,52
buffer_store_dwordx4 v[64:67], v91, s[44:47], s54 offen sc1
mav 20,212,52
mav 24,216,52
mav 28,220,52
s_waitcnt lgkmcnt(0)
mav 16,224,56
buffer_store_dwordx4 v[68:71], v92, s[44:47], s54 offen sc1
mav 20,228,56
mav 24,232,56
mav 28,236,56
mav 16,240,60
buffer_load_dwordx4 v[124:127], v90, s[36:39], s49 offen sc1
mav 20,244,60
mav 24,248,60
mav 28,252,60
s_waitcnt vmcnt(16)
mvv 16,116,164
buffer_load_dwordx4 v[128:131], v90, s[36:39], s49 offen offset:1024 sc1
mvv 20,120,164
s_waitcnt vmcnt(16)
mvv 24,116,168
s_sub_u32 s54, s54, 32768
mvv 28,120,168
s_waitcnt vmcnt(15)
mvv 16,116,172
mvv 20,120,172
s_waitcnt vmcnt(14)
v_xor_b32 v80, 0x10000, v80
mvv 24,116,176
s_waitcnt vmcnt(13)
s_add_u32 s49, s49, 313344
mvv 28,120,176
s_barrier
mvv 16,116,180
R 32,0
mvv 20,120,180
s_waitcnt vmcnt(12)
mvv 24,116,184
mvv 28,120,184
s_waitcnt vmcnt(11)
mvv 16,116,188
buffer_load_dwordx4 v[132:135], v86, s[36:39], s49 offen sc1
mvv 20,120,188
s_waitcnt vmcnt(11)
R 36,1024
mvv 24,116,192
mvv 28,120,192
s_waitcnt vmcnt(10)
mvv 16,116,196
buffer_load_dwordx4 v[136:139], v86, s[36:39], s49 offen offset:1024 sc1
mvv 20,120,196
s_waitcnt vmcnt(10)
R 40,2048
mvv 24,116,200
mvv 28,120,200
s_waitcnt lgkmcnt(1)
v_mfma_f32_16x16x32_bf16 v[0:3], a[0:3], v[32:35], v[100:103]
buffer_load_dwordx4 v[140:143], v87, s[36:39], s49 offen sc1
v_mfma_f32_16x16x32_bf16 v[4:7], a[4:7], v[32:35], v[104:107]
R 44,3072
v_cvt_pk_bf16_f32 v64, v16, v17
v_mfma_f32_16x16x32_bf16 v[8:11], a[8:11], v[32:35], v[108:111]
v_cvt_pk_bf16_f32 v65, v18, v19
v_cvt_pk_bf16_f32 v66, v20, v21
v_mfma_f32_16x16x32_bf16 v[12:15], a[12:15], v[32:35], v[112:115]
v_cvt_pk_bf16_f32 v67, v22, v23
v_cvt_pk_bf16_f32 v68, v24, v25
mav 0,16,36
buffer_load_dwordx4 v[144:147], v87, s[36:39], s49 offen offset:1024 sc1
mav 4,20,36
R 48,4096
v_cvt_pk_bf16_f32 v69, v26, v27
mav 8,24,36
v_cvt_pk_bf16_f32 v70, v28, v29
v_cvt_pk_bf16_f32 v71, v30, v31
mav 12,28,36
s_waitcnt lgkmcnt(1)
mav 0,32,40
buffer_load_dwordx4 v[148:151], v88, s[36:39], s49 offen sc1
mav 4,36,40
R 52,5120
mav 8,40,40
mav 12,44,40
mav 0,48,44
buffer_load_dwordx4 v[152:155], v88, s[36:39], s49 offen offset:1024 sc1
mav 4,52,44
R 56,6144
mav 8,56,44
mav 12,60,44
s_waitcnt lgkmcnt(1)
mav 0,64,48
buffer_load_dwordx4 v[156:159], v89, s[36:39], s49 offen sc1
mav 4,68,48
R 60,7168
mav 8,72,48
mav 12,76,48
mav 0,80,52
buffer_load_dwordx4 v[160:163], v89, s[36:39], s49 offen offset:1024 sc1
mav 4,84,52
R 32,8192
mav 8,88,52
mav 12,92,52
s_waitcnt lgkmcnt(1)
ds_write_b128 v94, v[64:67] offset:0
mav 0,96,56
R 36,9216
mav 4,100,56
ds_write_b128 v95, v[68:71] offset:0
mav 8,104,56
ds_read_b128 v[64:67], v96 offset:0
mav 12,108,56
ds_read_b128 v[68:71], v96 offset:1024
mav 0,112,60
R 40,10240
mav 4,116,60
mav 8,120,60
mav 12,124,60
s_waitcnt lgkmcnt(3)
mav 0,128,32
R 44,11264
mav 4,132,32
mav 8,136,32
mav 12,140,32
mav 0,144,36
R 48,12288
mav 4,148,36
mav 8,152,36
mav 12,156,36
s_waitcnt lgkmcnt(1)
mav 0,160,40
R 52,13312
mav 4,164,40
mav 8,168,40
mav 12,172,40
mav 0,176,44
R 56,14336
mav 4,180,44
mav 8,184,44
mav 12,188,44
s_waitcnt lgkmcnt(1)
mav 0,192,48
R 60,15360
mav 4,196,48
mav 8,200,48
mav 12,204,48
mav 0,208,52
buffer_store_dwordx4 v[64:67], v91, s[44:47], s54 offen sc1
mav 4,212,52
mav 8,216,52
mav 12,220,52
s_waitcnt lgkmcnt(0)
mav 0,224,56
buffer_store_dwordx4 v[68:71], v92, s[44:47], s54 offen sc1
mav 4,228,56
mav 8,232,56
mav 12,236,56
mav 0,240,60
buffer_load_dwordx4 v[164:167], v90, s[36:39], s49 offen sc1
mav 4,244,60
mav 8,248,60
mav 12,252,60
s_waitcnt vmcnt(12)
mvv 0,116,124
buffer_load_dwordx4 v[168:171], v90, s[36:39], s49 offen offset:1024 sc1
mvv 4,120,124
s_waitcnt vmcnt(12)
mvv 8,116,128
s_sub_u32 s54, s54, 32768
mvv 12,120,128
s_waitcnt vmcnt(11)
mvv 0,116,132
mvv 4,120,132
s_waitcnt vmcnt(10)
mvv 8,116,136
s_waitcnt vmcnt(9)
s_add_u32 s49, s49, 313344
mvv 12,120,136
s_barrier
mvv 0,116,140
R 32,32768
mvv 4,120,140
s_waitcnt vmcnt(8)
mvv 8,116,144
mvv 12,120,144
s_waitcnt vmcnt(7)
mvv 0,116,148
buffer_load_dwordx4 v[172:175], v86, s[36:39], s49 offen sc1
mvv 4,120,148
s_waitcnt vmcnt(7)
R 36,33792
mvv 8,116,152
mvv 12,120,152
s_waitcnt vmcnt(6)
mvv 0,116,156
buffer_load_dwordx4 v[176:179], v86, s[36:39], s49 offen offset:1024 sc1
mvv 4,120,156
s_waitcnt vmcnt(6)
R 40,34816
mvv 8,116,160
mvv 12,120,160
s_waitcnt lgkmcnt(1)
v_mfma_f32_16x16x32_bf16 v[16:19], a[0:3], v[32:35], v[100:103]
buffer_load_dwordx4 v[180:183], v87, s[36:39], s49 offen sc1
v_mfma_f32_16x16x32_bf16 v[20:23], a[4:7], v[32:35], v[104:107]
R 44,35840
v_cvt_pk_bf16_f32 v64, v0, v1
v_mfma_f32_16x16x32_bf16 v[24:27], a[8:11], v[32:35], v[108:111]
v_cvt_pk_bf16_f32 v65, v2, v3
v_cvt_pk_bf16_f32 v66, v4, v5
v_mfma_f32_16x16x32_bf16 v[28:31], a[12:15], v[32:35], v[112:115]
v_cvt_pk_bf16_f32 v67, v6, v7
v_cvt_pk_bf16_f32 v68, v8, v9
mav 16,16,36
buffer_load_dwordx4 v[184:187], v87, s[36:39], s49 offen offset:1024 sc1
mav 20,20,36
R 48,36864
v_cvt_pk_bf16_f32 v69, v10, v11
mav 24,24,36
ds_write_b128 v94, v[64:67] offset:0
mav 28,28,36
s_waitcnt lgkmcnt(2)
v_cvt_pk_bf16_f32 v70, v12, v13
v_cvt_pk_bf16_f32 v71, v14, v15
mav 16,32,40
buffer_load_dwordx4 v[188:191], v88, s[36:39], s49 offen sc1
mav 20,36,40
R 52,37888
mav 24,40,40
ds_write_b128 v95, v[68:71] offset:0
mav 28,44,40
ds_read_b128 v[64:67], v96 offset:0
mav 16,48,44
buffer_load_dwordx4 v[192:195], v88, s[36:39], s49 offen offset:1024 sc1
mav 20,52,44
R 56,38912
mav 24,56,44
ds_read_b128 v[68:71], v96 offset:1024
mav 28,60,44
s_waitcnt lgkmcnt(4)
mav 16,64,48
buffer_load_dwordx4 v[196:199], v89, s[36:39], s49 offen sc1
mav 20,68,48
R 60,39936
mav 24,72,48
s_waitcnt lgkmcnt(3)
mav 28,76,48
mav 16,80,52
buffer_store_dwordx4 v[64:67], v91, s[44:47], s54 offen sc1
buffer_load_dwordx4 v[200:203], v89, s[36:39], s49 offen offset:1024 sc1
mav 20,84,52
s_waitcnt lgkmcnt(1)
R 32,40960
mav 24,88,52
mav 28,92,52
)"
R"(
mav 16,96,56
buffer_store_dwordx4 v[68:71], v92, s[44:47], s54 offen sc1
R 36,41984
mav 20,100,56
mav 24,104,56
mav 28,108,56
s_waitcnt lgkmcnt(1)
mav 16,112,60
R 40,43008
mav 20,116,60
mav 24,120,60
mav 28,124,60
mav 16,128,32
R 44,44032
mav 20,132,32
mav 24,136,32
mav 28,140,32
s_waitcnt lgkmcnt(1)
mav 16,144,36
R 48,45056
mav 20,148,36
mav 24,152,36
mav 28,156,36
mav 16,160,40
mav 20,164,40
R 52,46080
mav 24,168,40
mav 28,172,40
s_waitcnt lgkmcnt(1)
mav 16,176,44
R 56,47104
mav 20,180,44
mav 24,184,44
mav 28,188,44
mav 16,192,48
R 60,48128
mav 20,196,48
mav 24,200,48
mav 28,204,48
s_waitcnt lgkmcnt(1)
mav 16,208,52
mav 20,212,52
mav 24,216,52
mav 28,220,52
mav 16,224,56
mav 20,228,56
mav 24,232,56
mav 28,236,56
s_waitcnt lgkmcnt(0)
mav 16,240,60
mav 20,244,60
mav 24,248,60
mav 28,252,60
s_waitcnt vmcnt(11)
mvv 16,116,164
mvv 20,120,164
s_waitcnt vmcnt(10)
mvv 24,116,168
s_sub_u32 s54, s54, 32768
mvv 28,120,168
s_waitcnt vmcnt(9)
mvv 16,116,172
mvv 20,120,172
s_waitcnt vmcnt(8)
mvv 24,116,176
mvv 28,120,176
s_waitcnt vmcnt(7)
mvv 16,116,180
mvv 20,120,180
s_waitcnt vmcnt(6)
mvv 24,116,184
mvv 28,120,184
s_waitcnt vmcnt(5)
mvv 16,116,188
mvv 20,120,188
s_waitcnt vmcnt(4)
mvv 24,116,192
mvv 28,120,192
s_waitcnt vmcnt(3)
mvv 16,116,196
mvv 20,120,196
s_waitcnt vmcnt(1)
mvv 24,116,200
mvv 28,120,200
s_nop 3
v_cvt_pk_bf16_f32 v64, v16, v17
v_cvt_pk_bf16_f32 v65, v18, v19
v_cvt_pk_bf16_f32 v66, v20, v21
v_cvt_pk_bf16_f32 v67, v22, v23
ds_write_b128 v94, v[64:67] offset:0
v_cvt_pk_bf16_f32 v68, v24, v25
v_cvt_pk_bf16_f32 v69, v26, v27
v_cvt_pk_bf16_f32 v70, v28, v29
v_cvt_pk_bf16_f32 v71, v30, v31
ds_write_b128 v95, v[68:71] offset:0
ds_read_b128 v[64:67], v96 offset:0
ds_read_b128 v[68:71], v96 offset:1024
s_waitcnt lgkmcnt(0)
buffer_store_dwordx4 v[64:67], v91, s[44:47], s54 offen sc1
buffer_store_dwordx4 v[68:71], v92, s[44:47], s54 offen sc1
s_endpgm
LvR2:
LsR11:
s_cbranch_execz LsR15
s_add_u32 s49, s49, 313344
s_barrier
s_add_u32 m0, s50, 0
LsR12:
s_cbranch_execz LsR16
s_nop 0
D 83,0
D 83,1024
D 83,2048
D 83,3072
s_add_u32 s49, s49, 313344
s_barrier
s_add_u32 m0, s50, 32768
s_nop 0
D 83,0
D 83,1024
D 83,2048
D 83,3072
buffer_load_dwordx4 v[124:127], v90, s[36:39], s49 offen sc1
buffer_load_dwordx4 v[128:131], v90, s[36:39], s49 offen offset:1024 sc1
s_xor_b32 s50, s50, 0x10000
s_waitcnt vmcnt(6)
s_add_u32 s49, s49, 313344
s_barrier
R 32,0
buffer_load_dwordx4 v[132:135], v86, s[36:39], s49 offen sc1
R 36,1024
buffer_load_dwordx4 v[136:139], v86, s[36:39], s49 offen offset:1024 sc1
R 40,2048
buffer_load_dwordx4 v[140:143], v87, s[36:39], s49 offen sc1
s_waitcnt lgkmcnt(1)
v_mfma_f32_16x16x32_bf16 v[0:3], a[0:3], v[32:35], v[100:103]
R 44,3072
v_mfma_f32_16x16x32_bf16 v[4:7], a[4:7], v[32:35], v[104:107]
v_mfma_f32_16x16x32_bf16 v[8:11], a[8:11], v[32:35], v[108:111]
buffer_load_dwordx4 v[144:147], v87, s[36:39], s49 offen offset:1024 sc1
v_mfma_f32_16x16x32_bf16 v[12:15], a[12:15], v[32:35], v[112:115]
mav 0,16,36
R 48,4096
mav 4,20,36
mav 8,24,36
buffer_load_dwordx4 v[148:151], v88, s[36:39], s49 offen sc1
mav 12,28,36
s_waitcnt lgkmcnt(1)
mav 0,32,40
R 52,5120
mav 4,36,40
mav 8,40,40
buffer_load_dwordx4 v[152:155], v88, s[36:39], s49 offen offset:1024 sc1
mav 12,44,40
mav 0,48,44
R 56,6144
mav 4,52,44
mav 8,56,44
buffer_load_dwordx4 v[156:159], v89, s[36:39], s49 offen sc1
mav 12,60,44
s_waitcnt lgkmcnt(1)
mav 0,64,48
R 60,7168
mav 4,68,48
mav 8,72,48
buffer_load_dwordx4 v[160:163], v89, s[36:39], s49 offen offset:1024 sc1
mav 12,76,48
mav 0,80,52
R 32,8192
mav 4,84,52
mav 8,88,52
s_add_u32 m0, s50, 0
mav 12,92,52
s_waitcnt lgkmcnt(1)
mav 0,96,56
R 36,9216
mav 4,100,56
mav 8,104,56
D 83,0
mav 12,108,56
mav 0,112,60
R 40,10240
mav 4,116,60
mav 8,120,60
D 83,1024
mav 12,124,60
s_waitcnt lgkmcnt(1)
mav 0,128,32
R 44,11264
mav 4,132,32
mav 8,136,32
mav 12,140,32
mav 0,144,36
R 48,12288
mav 4,148,36
mav 8,152,36
mav 12,156,36
s_waitcnt lgkmcnt(1)
mav 0,160,40
R 52,13312
mav 4,164,40
mav 8,168,40
mav 12,172,40
mav 0,176,44
R 56,14336
mav 4,180,44
mav 8,184,44
D 83,2048
mav 12,188,44
s_waitcnt lgkmcnt(1)
mav 0,192,48
R 60,15360
mav 4,196,48
mav 8,200,48
D 83,3072
mav 12,204,48
mav 0,208,52
mav 4,212,52
mav 8,216,52
mav 12,220,52
s_waitcnt lgkmcnt(0)
mav 0,224,56
mav 4,228,56
mav 8,232,56
mav 12,236,56
mav 0,240,60
mav 4,244,60
mav 8,248,60
buffer_load_dwordx4 v[164:167], v90, s[36:39], s49 offen sc1
mav 12,252,60
s_waitcnt vmcnt(14)
mvv 0,116,124
mvv 4,120,124
s_waitcnt vmcnt(13)
mvv 8,116,128
buffer_load_dwordx4 v[168:171], v90, s[36:39], s49 offen offset:1024 sc1
mvv 12,120,128
s_waitcnt vmcnt(13)
mvv 0,116,132
mvv 4,120,132
s_waitcnt vmcnt(12)
mvv 8,116,136
s_waitcnt vmcnt(11)
s_add_u32 s49, s49, 313344
mvv 12,120,136
s_barrier
mvv 0,116,140
R 32,32768
mvv 4,120,140
s_waitcnt vmcnt(10)
mvv 8,116,144
buffer_load_dwordx4 v[172:175], v86, s[36:39], s49 offen sc1
mvv 12,120,144
s_waitcnt vmcnt(10)
mvv 0,116,148
R 36,33792
mvv 4,120,148
s_waitcnt vmcnt(9)
mvv 8,116,152
buffer_load_dwordx4 v[176:179], v86, s[36:39], s49 offen offset:1024 sc1
mvv 12,120,152
s_waitcnt vmcnt(9)
mvv 0,116,156
R 40,34816
mvv 4,120,156
s_waitcnt vmcnt(8)
mvv 8,116,160
buffer_load_dwordx4 v[180:183], v87, s[36:39], s49 offen sc1
mvv 12,120,160
s_waitcnt lgkmcnt(1)
v_mfma_f32_16x16x32_bf16 v[16:19], a[0:3], v[32:35], v[100:103]
R 44,35840
v_cvt_pk_bf16_f32 v64, v0, v1
v_mfma_f32_16x16x32_bf16 v[20:23], a[4:7], v[32:35], v[104:107]
v_cvt_pk_bf16_f32 v65, v2, v3
v_cvt_pk_bf16_f32 v66, v4, v5
v_mfma_f32_16x16x32_bf16 v[24:27], a[8:11], v[32:35], v[108:111]
buffer_load_dwordx4 v[184:187], v87, s[36:39], s49 offen offset:1024 sc1
v_mfma_f32_16x16x32_bf16 v[28:31], a[12:15], v[32:35], v[112:115]
v_cvt_pk_bf16_f32 v67, v6, v7
v_cvt_pk_bf16_f32 v68, v8, v9
mav 16,16,36
R 48,36864
v_cvt_pk_bf16_f32 v69, v10, v11
mav 20,20,36
v_cvt_pk_bf16_f32 v70, v12, v13
v_cvt_pk_bf16_f32 v71, v14, v15
mav 24,24,36
buffer_load_dwordx4 v[188:191], v88, s[36:39], s49 offen sc1
mav 28,28,36
s_waitcnt lgkmcnt(1)
v_mul_f32 v75, v0, v0
v_add_f32 v74, v1, v0
mav 16,32,40
R 52,37888
v_fmac_f32 v75, v1, v1
mav 20,36,40
v_add_f32 v74, v2, v74
v_fmac_f32 v75, v2, v2
mav 24,40,40
buffer_load_dwordx4 v[192:195], v88, s[36:39], s49 offen offset:1024 sc1
mav 28,44,40
v_add_f32 v74, v3, v74
v_fmac_f32 v75, v3, v3
mav 16,48,44
R 56,38912
v_add_f32 v74, v4, v74
mav 20,52,44
v_fmac_f32 v75, v4, v4
v_add_f32 v74, v5, v74
mav 24,56,44
buffer_load_dwordx4 v[196:199], v89, s[36:39], s49 offen sc1
mav 28,60,44
s_waitcnt lgkmcnt(1)
v_fmac_f32 v75, v5, v5
v_add_f32 v74, v6, v74
mav 16,64,48
R 60,39936
v_fmac_f32 v75, v6, v6
mav 20,68,48
v_add_f32 v74, v7, v74
v_fmac_f32 v75, v7, v7
mav 24,72,48
buffer_load_dwordx4 v[200:203], v89, s[36:39], s49 offen offset:1024 sc1
mav 28,76,48
v_add_f32 v74, v8, v74
v_fmac_f32 v75, v8, v8
mav 16,80,52
R 32,40960
v_add_f32 v74, v9, v74
mav 20,84,52
v_fmac_f32 v75, v9, v9
v_add_f32 v74, v10, v74
mav 24,88,52
s_add_u32 m0, s50, 32768
v_fmac_f32 v75, v10, v10
v_add_f32 v74, v11, v74
mav 28,92,52
s_waitcnt lgkmcnt(1)
v_fmac_f32 v75, v11, v11
v_add_f32 v74, v12, v74
mav 16,96,56
R 36,41984
v_fmac_f32 v75, v12, v12
mav 20,100,56
v_add_f32 v74, v13, v74
v_fmac_f32 v75, v13, v13
mav 24,104,56
D 83,0
mav 28,108,56
ds_write_b128 v94, v[64:67] offset:0
mav 16,112,60
R 40,43008
v_add_f32 v74, v14, v74
mav 20,116,60
ds_write_b128 v95, v[68:71] offset:0
mav 24,120,60
D 83,1024
mav 28,124,60
s_waitcnt lgkmcnt(3)
ds_read_b128 v[64:67], v96 offset:0
v_fmac_f32 v75, v14, v14
mav 16,128,32
R 44,44032
v_add_f32 v74, v15, v74
mav 20,132,32
ds_read_b128 v[68:71], v96 offset:1024
v_fmac_f32 v75, v15, v15
mav 24,136,32
mav 28,140,32
v_permlane32_swap_b32 v74, v75
mav 16,144,36
R 48,45056
v_add_f32 v74, v74, v75
mav 20,148,36
mav 24,152,36
mav 28,156,36
s_waitcnt lgkmcnt(1)
mav 16,160,40
mav 20,164,40
R 52,46080
mav 24,168,40
mav 28,172,40
mav 16,176,44
R 56,47104
mav 20,180,44
mav 24,184,44
D 83,2048
mav 28,188,44
s_waitcnt lgkmcnt(1)
mav 16,192,48
R 60,48128
mav 20,196,48
mav 24,200,48
D 83,3072
mav 28,204,48
mav 16,208,52
mav 20,212,52
mav 24,216,52
buffer_store_dwordx4 v[64:67], v91, s[36:39], s54 offen sc1
mav 28,220,52
s_waitcnt lgkmcnt(0)
mav 16,224,56
mav 20,228,56
mav 24,232,56
buffer_store_dwordx4 v[68:71], v92, s[36:39], s54 offen sc1
mav 28,236,56
mav 16,240,60
mav 20,244,60
mav 24,248,60
buffer_load_dwordx4 v[124:127], v90, s[36:39], s49 offen sc1
mav 28,252,60
s_waitcnt vmcnt(16)
mvv 16,116,164
mvv 20,120,164
s_waitcnt vmcnt(15)
mvv 24,116,168
buffer_load_dwordx4 v[128:131], v90, s[36:39], s49 offen offset:1024 sc1
mvv 28,120,168
s_waitcnt vmcnt(15)
s_xor_b32 s50, s50, 0x10000
s_sub_u32 s54, s54, 32768
mvv 16,116,172
mvv 20,120,172
s_waitcnt vmcnt(14)
v_xor_b32 v80, 0x10000, v80
mvv 24,116,176
s_waitcnt vmcnt(13)
s_add_u32 s49, s49, 313344
mvv 28,120,176
s_barrier
mvv 16,116,180
R 32,0
mvv 20,120,180
s_waitcnt vmcnt(12)
mvv 24,116,184
buffer_load_dwordx4 v[132:135], v86, s[36:39], s49 offen sc1
mvv 28,120,184
s_waitcnt vmcnt(12)
mvv 16,116,188
R 36,1024
mvv 20,120,188
s_waitcnt vmcnt(11)
mvv 24,116,192
buffer_load_dwordx4 v[136:139], v86, s[36:39], s49 offen offset:1024 sc1
mvv 28,120,192
s_waitcnt vmcnt(11)
mvv 16,116,196
R 40,2048
mvv 20,120,196
s_waitcnt vmcnt(10)
mvv 24,116,200
buffer_load_dwordx4 v[140:143], v87, s[36:39], s49 offen sc1
mvv 28,120,200
s_waitcnt lgkmcnt(1)
v_mfma_f32_16x16x32_bf16 v[0:3], a[0:3], v[32:35], v[100:103]
R 44,3072
v_cvt_pk_bf16_f32 v64, v16, v17
v_mfma_f32_16x16x32_bf16 v[4:7], a[4:7], v[32:35], v[104:107]
v_cvt_pk_bf16_f32 v65, v18, v19
v_cvt_pk_bf16_f32 v66, v20, v21
v_mfma_f32_16x16x32_bf16 v[8:11], a[8:11], v[32:35], v[108:111]
buffer_load_dwordx4 v[144:147], v87, s[36:39], s49 offen offset:1024 sc1
v_mfma_f32_16x16x32_bf16 v[12:15], a[12:15], v[32:35], v[112:115]
v_cvt_pk_bf16_f32 v67, v22, v23
v_cvt_pk_bf16_f32 v68, v24, v25
mav 0,16,36
R 48,4096
v_cvt_pk_bf16_f32 v69, v26, v27
mav 4,20,36
v_cvt_pk_bf16_f32 v70, v28, v29
v_cvt_pk_bf16_f32 v71, v30, v31
mav 8,24,36
buffer_load_dwordx4 v[148:151], v88, s[36:39], s49 offen sc1
mav 12,28,36
s_waitcnt lgkmcnt(1)
v_mul_f32 v75, v16, v16
v_fmac_f32 v75, v17, v17
mav 0,32,40
R 52,5120
v_fmac_f32 v75, v18, v18
mav 4,36,40
v_fmac_f32 v75, v19, v19
v_fmac_f32 v75, v20, v20
mav 8,40,40
buffer_load_dwordx4 v[152:155], v88, s[36:39], s49 offen offset:1024 sc1
mav 12,44,40
v_fmac_f32 v75, v21, v21
v_fmac_f32 v75, v22, v22
mav 0,48,44
R 56,6144
v_fmac_f32 v75, v23, v23
mav 4,52,44
v_fmac_f32 v75, v24, v24
v_fmac_f32 v75, v25, v25
mav 8,56,44
buffer_load_dwordx4 v[156:159], v89, s[36:39], s49 offen sc1
mav 12,60,44
s_waitcnt lgkmcnt(1)
v_fmac_f32 v75, v26, v26
v_fmac_f32 v75, v27, v27
mav 0,64,48
R 60,7168
v_fmac_f32 v75, v28, v28
mav 4,68,48
v_fmac_f32 v75, v29, v29
v_fmac_f32 v75, v30, v30
mav 8,72,48
buffer_load_dwordx4 v[160:163], v89, s[36:39], s49 offen offset:1024 sc1
mav 12,76,48
v_fmac_f32 v75, v31, v31
mav 0,80,52
R 32,8192
mav 4,84,52
mav 8,88,52
s_add_u32 m0, s50, 0
mav 12,92,52
s_waitcnt lgkmcnt(1)
mav 0,96,56
R 36,9216
mav 4,100,56
mav 8,104,56
D 83,0
mav 12,108,56
ds_write_b32 v97, v74 offset:0
mav 0,112,60
R 40,10240
v_add_f32 v74, v17, v16
mav 4,116,60
ds_write_b128 v94, v[64:67] offset:0
mav 8,120,60
D 83,1024
mav 12,124,60
s_waitcnt lgkmcnt(3)
ds_write_b128 v95, v[68:71] offset:0
mav 0,128,32
R 44,11264
v_add_f32 v74, v18, v74
mav 4,132,32
ds_read_b128 v[64:67], v96 offset:0
v_add_f32 v74, v19, v74
mav 8,136,32
ds_read_b128 v[68:71], v96 offset:1024
v_add_f32 v74, v20, v74
mav 12,140,32
v_add_f32 v74, v21, v74
v_add_f32 v74, v22, v74
mav 0,144,36
R 48,12288
v_add_f32 v74, v23, v74
mav 4,148,36
v_add_f32 v74, v24, v74
v_add_f32 v74, v25, v74
mav 8,152,36
v_add_f32 v74, v26, v74
v_add_f32 v74, v27, v74
mav 12,156,36
s_waitcnt lgkmcnt(2)
v_add_f32 v74, v28, v74
v_add_f32 v74, v29, v74
mav 0,160,40
R 52,13312
v_add_f32 v74, v30, v74
mav 4,164,40
v_add_f32 v74, v31, v74
v_add_u32 v72, 1, v72
mav 8,168,40
v_permlane32_swap_b32 v74, v75
mav 12,172,40
v_add_f32 v74, v74, v75
mav 0,176,44
R 56,14336
mav 4,180,44
mav 8,184,44
D 83,2048
mav 12,188,44
s_waitcnt lgkmcnt(1)
mav 0,192,48
R 60,15360
mav 4,196,48
mav 8,200,48
D 83,3072
mav 12,204,48
mav 0,208,52
mav 4,212,52
mav 8,216,52
buffer_store_dwordx4 v[64:67], v91, s[36:39], s54 offen sc1
mav 12,220,52
s_waitcnt lgkmcnt(0)
mav 0,224,56
mav 4,228,56
mav 8,232,56
buffer_store_dwordx4 v[68:71], v92, s[36:39], s54 offen sc1
mav 12,236,56
mav 0,240,60
mav 4,244,60
mav 8,248,60
buffer_load_dwordx4 v[164:167], v90, s[36:39], s49 offen sc1
mav 12,252,60
s_waitcnt vmcnt(16)
mvv 0,116,124
buffer_store_dword v72, v81, s[36:39], s52 offen sc1
mvv 4,120,124
s_waitcnt vmcnt(16)
mvv 8,116,128
buffer_load_dwordx4 v[168:171], v90, s[36:39], s49 offen offset:1024 sc1
mvv 12,120,128
s_waitcnt vmcnt(16)
s_sub_u32 s54, s54, 32768
mvv 0,116,132
mvv 4,120,132
s_waitcnt vmcnt(15)
mvv 8,116,136
s_waitcnt vmcnt(14)
s_add_u32 s49, s49, 313344
mvv 12,120,136
s_barrier
mvv 0,116,140
R 32,32768
mvv 4,120,140
s_waitcnt vmcnt(13)
mvv 8,116,144
buffer_load_dwordx4 v[172:175], v86, s[36:39], s49 offen sc1
mvv 12,120,144
s_waitcnt vmcnt(13)
mvv 0,116,148
R 36,33792
mvv 4,120,148
s_waitcnt vmcnt(12)
mvv 8,116,152
buffer_load_dwordx4 v[176:179], v86, s[36:39], s49 offen offset:1024 sc1
mvv 12,120,152
s_waitcnt vmcnt(12)
mvv 0,116,156
R 40,34816
mvv 4,120,156
s_waitcnt vmcnt(11)
mvv 8,116,160
buffer_load_dwordx4 v[180:183], v87, s[36:39], s49 offen sc1
mvv 12,120,160
s_waitcnt lgkmcnt(1)
v_mfma_f32_16x16x32_bf16 v[16:19], a[0:3], v[32:35], v[100:103]
R 44,35840
v_cvt_pk_bf16_f32 v64, v0, v1
v_mfma_f32_16x16x32_bf16 v[20:23], a[4:7], v[32:35], v[104:107]
v_cvt_pk_bf16_f32 v65, v2, v3
v_cvt_pk_bf16_f32 v66, v4, v5
v_mfma_f32_16x16x32_bf16 v[24:27], a[8:11], v[32:35], v[108:111]
buffer_load_dwordx4 v[184:187], v87, s[36:39], s49 offen offset:1024 sc1
v_mfma_f32_16x16x32_bf16 v[28:31], a[12:15], v[32:35], v[112:115]
v_cvt_pk_bf16_f32 v67, v6, v7
v_cvt_pk_bf16_f32 v68, v8, v9
mav 16,16,36
R 48,36864
v_cvt_pk_bf16_f32 v69, v10, v11
mav 20,20,36
v_cvt_pk_bf16_f32 v70, v12, v13
v_cvt_pk_bf16_f32 v71, v14, v15
mav 24,24,36
buffer_load_dwordx4 v[188:191], v88, s[36:39], s49 offen sc1
mav 28,28,36
s_waitcnt lgkmcnt(1)
v_mul_f32 v75, v0, v0
v_fmac_f32 v75, v1, v1
mav 16,32,40
R 52,37888
v_fmac_f32 v75, v2, v2
mav 20,36,40
v_fmac_f32 v75, v3, v3
v_fmac_f32 v75, v4, v4
mav 24,40,40
buffer_load_dwordx4 v[192:195], v88, s[36:39], s49 offen offset:1024 sc1
mav 28,44,40
v_fmac_f32 v75, v5, v5
v_fmac_f32 v75, v6, v6
mav 16,48,44
R 56,38912
v_fmac_f32 v75, v7, v7
mav 20,52,44
v_fmac_f32 v75, v8, v8
v_fmac_f32 v75, v9, v9
mav 24,56,44
buffer_load_dwordx4 v[196:199], v89, s[36:39], s49 offen sc1
mav 28,60,44
s_waitcnt lgkmcnt(1)
v_fmac_f32 v75, v10, v10
v_fmac_f32 v75, v11, v11
mav 16,64,48
R 60,39936
v_fmac_f32 v75, v12, v12
mav 20,68,48
v_fmac_f32 v75, v13, v13
v_fmac_f32 v75, v14, v14
mav 24,72,48
buffer_load_dwordx4 v[200:203], v89, s[36:39], s49 offen offset:1024 sc1
LsR13:
s_cbranch_execz LsR17
mav 28,76,48
v_fmac_f32 v75, v15, v15
mav 16,80,52
R 32,40960
mav 20,84,52
mav 24,88,52
s_add_u32 m0, s50, 32768
mav 28,92,52
s_waitcnt lgkmcnt(1)
mav 16,96,56
R 36,41984
mav 20,100,56
mav 24,104,56
D 83,0
mav 28,108,56
ds_write_b32 v97, v74 offset:1024
mav 16,112,60
R 40,43008
v_add_f32 v74, v1, v0
mav 20,116,60
ds_write_b128 v94, v[64:67] offset:0
mav 24,120,60
D 83,1024
mav 28,124,60
s_waitcnt lgkmcnt(3)
ds_write_b128 v95, v[68:71] offset:0
mav 16,128,32
R 44,44032
v_add_f32 v74, v2, v74
mav 20,132,32
ds_read_b128 v[64:67], v96 offset:0
v_add_f32 v74, v3, v74
mav 24,136,32
ds_read_b128 v[68:71], v96 offset:1024
v_add_f32 v74, v4, v74
mav 28,140,32
v_add_f32 v74, v5, v74
v_add_f32 v74, v6, v74
mav 16,144,36
R 48,45056
v_add_f32 v74, v7, v74
mav 20,148,36
v_add_f32 v74, v8, v74
v_add_f32 v74, v9, v74
mav 24,152,36
v_add_f32 v74, v10, v74
v_add_f32 v74, v11, v74
mav 28,156,36
s_waitcnt lgkmcnt(2)
v_add_f32 v74, v12, v74
mav 16,160,40
s_mov_b32 s60, -14
.p2align 6
LR2_1:
mav 20,164,40
R 52,46080
v_add_f32 v74, v13, v74
mav 24,168,40
v_add_f32 v74, v14, v74
v_add_f32 v74, v15, v74
mav 28,172,40
v_add_u32 v72, 1, v72
mav 16,176,44
R 56,47104
mav 20,180,44
v_permlane32_swap_b32 v74, v75
mav 24,184,44
D 83,2048
mav 28,188,44
s_waitcnt lgkmcnt(1)
v_add_f32 v74, v74, v75
mav 16,192,48
R 60,48128
mav 20,196,48
mav 24,200,48
D 83,3072
mav 28,204,48
mav 16,208,52
mav 20,212,52
mav 24,216,52
buffer_store_dwordx4 v[64:67], v91, s[36:39], s54 offen sc1
mav 28,220,52
s_waitcnt lgkmcnt(0)
mav 16,224,56
mav 20,228,56
mav 24,232,56
buffer_store_dwordx4 v[68:71], v92, s[36:39], s54 offen sc1
mav 28,236,56
mav 16,240,60
mav 20,244,60
mav 24,248,60
buffer_load_dwordx4 v[124:127], v90, s[36:39], s49 offen sc1
mav 28,252,60
s_waitcnt vmcnt(17)
mvv 16,116,164
buffer_store_dword v72, v81, s[36:39], s52 offen sc1
mvv 20,120,164
s_waitcnt vmcnt(16)
mvv 24,116,168
buffer_load_dwordx4 v[128:131], v90, s[36:39], s49 offen offset:1024 sc1
mvv 28,120,168
s_waitcnt vmcnt(16)
s_xor_b32 s50, s50, 0x10000
s_sub_u32 s54, s54, 32768
mvv 16,116,172
mvv 20,120,172
s_waitcnt vmcnt(15)
v_xor_b32 v80, 0x10000, v80
mvv 24,116,176
s_waitcnt vmcnt(14)
s_add_u32 s49, s49, 313344
mvv 28,120,176
s_barrier
mvv 16,116,180
R 32,0
mvv 20,120,180
s_waitcnt vmcnt(13)
mvv 24,116,184
buffer_load_dwordx4 v[132:135], v86, s[36:39], s49 offen sc1
mvv 28,120,184
s_waitcnt vmcnt(13)
mvv 16,116,188
R 36,1024
mvv 20,120,188
s_waitcnt vmcnt(12)
mvv 24,116,192
buffer_load_dwordx4 v[136:139], v86, s[36:39], s49 offen offset:1024 sc1
mvv 28,120,192
s_waitcnt vmcnt(12)
mvv 16,116,196
R 40,2048
mvv 20,120,196
s_waitcnt vmcnt(11)
mvv 24,116,200
buffer_load_dwordx4 v[140:143], v87, s[36:39], s49 offen sc1
mvv 28,120,200
s_waitcnt lgkmcnt(1)
v_mfma_f32_16x16x32_bf16 v[0:3], a[0:3], v[32:35], v[100:103]
R 44,3072
v_cvt_pk_bf16_f32 v64, v16, v17
v_mfma_f32_16x16x32_bf16 v[4:7], a[4:7], v[32:35], v[104:107]
v_cvt_pk_bf16_f32 v65, v18, v19
v_cvt_pk_bf16_f32 v66, v20, v21
v_mfma_f32_16x16x32_bf16 v[8:11], a[8:11], v[32:35], v[108:111]
buffer_load_dwordx4 v[144:147], v87, s[36:39], s49 offen offset:1024 sc1
v_mfma_f32_16x16x32_bf16 v[12:15], a[12:15], v[32:35], v[112:115]
v_cvt_pk_bf16_f32 v67, v22, v23
v_cvt_pk_bf16_f32 v68, v24, v25
mav 0,16,36
R 48,4096
v_cvt_pk_bf16_f32 v69, v26, v27
mav 4,20,36
v_cvt_pk_bf16_f32 v70, v28, v29
v_cvt_pk_bf16_f32 v71, v30, v31
mav 8,24,36
buffer_load_dwordx4 v[148:151], v88, s[36:39], s49 offen sc1
mav 12,28,36
s_waitcnt lgkmcnt(1)
v_mul_f32 v75, v16, v16
v_fmac_f32 v75, v17, v17
mav 0,32,40
R 52,5120
v_fmac_f32 v75, v18, v18
mav 4,36,40
v_fmac_f32 v75, v19, v19
v_fmac_f32 v75, v20, v20
mav 8,40,40
buffer_load_dwordx4 v[152:155], v88, s[36:39], s49 offen offset:1024 sc1
mav 12,44,40
v_fmac_f32 v75, v21, v21
v_fmac_f32 v75, v22, v22
mav 0,48,44
R 56,6144
v_fmac_f32 v75, v23, v23
mav 4,52,44
v_fmac_f32 v75, v24, v24
v_fmac_f32 v75, v25, v25
mav 8,56,44
buffer_load_dwordx4 v[156:159], v89, s[36:39], s49 offen sc1
mav 12,60,44
s_waitcnt lgkmcnt(1)
v_fmac_f32 v75, v26, v26
v_fmac_f32 v75, v27, v27
mav 0,64,48
R 60,7168
v_fmac_f32 v75, v28, v28
mav 4,68,48
v_fmac_f32 v75, v29, v29
v_fmac_f32 v75, v30, v30
mav 8,72,48
buffer_load_dwordx4 v[160:163], v89, s[36:39], s49 offen offset:1024 sc1
mav 12,76,48
v_fmac_f32 v75, v31, v31
mav 0,80,52
R 32,8192
mav 4,84,52
mav 8,88,52
s_add_u32 m0, s50, 0
mav 12,92,52
s_waitcnt lgkmcnt(1)
mav 0,96,56
R 36,9216
mav 4,100,56
mav 8,104,56
D 83,0
mav 12,108,56
ds_write_b32 v97, v74 offset:0
mav 0,112,60
R 40,10240
v_add_f32 v74, v17, v16
mav 4,116,60
ds_write_b128 v94, v[64:67] offset:0
mav 8,120,60
D 83,1024
mav 12,124,60
s_waitcnt lgkmcnt(3)
ds_write_b128 v95, v[68:71] offset:0
mav 0,128,32
R 44,11264
v_add_f32 v74, v18, v74
mav 4,132,32
ds_read_b128 v[64:67], v96 offset:0
v_add_f32 v74, v19, v74
mav 8,136,32
ds_read_b128 v[68:71], v96 offset:1024
v_add_f32 v74, v20, v74
mav 12,140,32
v_add_f32 v74, v21, v74
v_add_f32 v74, v22, v74
mav 0,144,36
R 48,12288
v_add_f32 v74, v23, v74
mav 4,148,36
v_add_f32 v74, v24, v74
v_add_f32 v74, v25, v74
mav 8,152,36
v_add_f32 v74, v26, v74
v_add_f32 v74, v27, v74
mav 12,156,36
s_waitcnt lgkmcnt(2)
v_add_f32 v74, v28, v74
v_add_f32 v74, v29, v74
mav 0,160,40
R 52,13312
v_add_f32 v74, v30, v74
mav 4,164,40
v_add_f32 v74, v31, v74
v_add_u32 v72, 1, v72
mav 8,168,40
v_permlane32_swap_b32 v74, v75
mav 12,172,40
v_add_f32 v74, v74, v75
mav 0,176,44
R 56,14336
mav 4,180,44
mav 8,184,44
D 83,2048
mav 12,188,44
s_waitcnt lgkmcnt(1)
mav 0,192,48
R 60,15360
mav 4,196,48
mav 8,200,48
D 83,3072
mav 12,204,48
mav 0,208,52
mav 4,212,52
mav 8,216,52
buffer_store_dwordx4 v[64:67], v91, s[36:39], s54 offen sc1
mav 12,220,52
s_waitcnt lgkmcnt(0)
mav 0,224,56
mav 4,228,56
mav 8,232,56
buffer_store_dwordx4 v[68:71], v92, s[36:39], s54 offen sc1
mav 12,236,56
mav 0,240,60
mav 4,244,60
mav 8,248,60
buffer_load_dwordx4 v[164:167], v90, s[36:39], s49 offen sc1
mav 12,252,60
s_waitcnt vmcnt(17)
mvv 0,116,124
buffer_store_dword v72, v81, s[36:39], s52 offen sc1
mvv 4,120,124
s_waitcnt vmcnt(16)
mvv 8,116,128
buffer_load_dwordx4 v[168:171], v90, s[36:39], s49 offen offset:1024 sc1
mvv 12,120,128
s_waitcnt vmcnt(16)
s_sub_u32 s54, s54, 32768
mvv 0,116,132
mvv 4,120,132
s_waitcnt vmcnt(15)
mvv 8,116,136
s_waitcnt vmcnt(14)
s_add_u32 s49, s49, 313344
mvv 12,120,136
s_barrier
mvv 0,116,140
R 32,32768
mvv 4,120,140
s_waitcnt vmcnt(13)
mvv 8,116,144
buffer_load_dwordx4 v[172:175], v86, s[36:39], s49 offen sc1
mvv 12,120,144
s_waitcnt vmcnt(13)
mvv 0,116,148
R 36,33792
mvv 4,120,148
s_waitcnt vmcnt(12)
mvv 8,116,152
buffer_load_dwordx4 v[176:179], v86, s[36:39], s49 offen offset:1024 sc1
mvv 12,120,152
s_waitcnt vmcnt(12)
mvv 0,116,156
R 40,34816
mvv 4,120,156
s_waitcnt vmcnt(11)
mvv 8,116,160
buffer_load_dwordx4 v[180:183], v87, s[36:39], s49 offen sc1
mvv 12,120,160
s_waitcnt lgkmcnt(1)
v_mfma_f32_16x16x32_bf16 v[16:19], a[0:3], v[32:35], v[100:103]
R 44,35840
v_cvt_pk_bf16_f32 v64, v0, v1
v_mfma_f32_16x16x32_bf16 v[20:23], a[4:7], v[32:35], v[104:107]
v_cvt_pk_bf16_f32 v65, v2, v3
v_cvt_pk_bf16_f32 v66, v4, v5
v_mfma_f32_16x16x32_bf16 v[24:27], a[8:11], v[32:35], v[108:111]
buffer_load_dwordx4 v[184:187], v87, s[36:39], s49 offen offset:1024 sc1
v_mfma_f32_16x16x32_bf16 v[28:31], a[12:15], v[32:35], v[112:115]
v_cvt_pk_bf16_f32 v67, v6, v7
v_cvt_pk_bf16_f32 v68, v8, v9
mav 16,16,36
R 48,36864
v_cvt_pk_bf16_f32 v69, v10, v11
mav 20,20,36
v_cvt_pk_bf16_f32 v70, v12, v13
v_cvt_pk_bf16_f32 v71, v14, v15
mav 24,24,36
buffer_load_dwordx4 v[188:191], v88, s[36:39], s49 offen sc1
mav 28,28,36
s_waitcnt lgkmcnt(1)
v_mul_f32 v75, v0, v0
v_fmac_f32 v75, v1, v1
mav 16,32,40
R 52,37888
v_fmac_f32 v75, v2, v2
mav 20,36,40
v_fmac_f32 v75, v3, v3
v_fmac_f32 v75, v4, v4
mav 24,40,40
buffer_load_dwordx4 v[192:195], v88, s[36:39], s49 offen offset:1024 sc1
mav 28,44,40
v_fmac_f32 v75, v5, v5
v_fmac_f32 v75, v6, v6
mav 16,48,44
R 56,38912
v_fmac_f32 v75, v7, v7
mav 20,52,44
v_fmac_f32 v75, v8, v8
v_fmac_f32 v75, v9, v9
mav 24,56,44
buffer_load_dwordx4 v[196:199], v89, s[36:39], s49 offen sc1
mav 28,60,44
s_waitcnt lgkmcnt(1)
v_fmac_f32 v75, v10, v10
v_fmac_f32 v75, v11, v11
mav 16,64,48
R 60,39936
v_fmac_f32 v75, v12, v12
mav 20,68,48
v_fmac_f32 v75, v13, v13
v_fmac_f32 v75, v14, v14
mav 24,72,48
buffer_load_dwordx4 v[200:203], v89, s[36:39], s49 offen offset:1024 sc1
mav 28,76,48
v_fmac_f32 v75, v15, v15
mav 16,80,52
R 32,40960
mav 20,84,52
mav 24,88,52
s_add_u32 m0, s50, 32768
mav 28,92,52
s_waitcnt lgkmcnt(1)
mav 16,96,56
R 36,41984
mav 20,100,56
mav 24,104,56
D 83,0
mav 28,108,56
ds_write_b32 v97, v74 offset:1024
mav 16,112,60
R 40,43008
v_add_f32 v74, v1, v0
mav 20,116,60
ds_write_b128 v94, v[64:67] offset:0
mav 24,120,60
D 83,1024
mav 28,124,60
s_waitcnt lgkmcnt(3)
ds_write_b128 v95, v[68:71] offset:0
mav 16,128,32
R 44,44032
v_add_f32 v74, v2, v74
mav 20,132,32
ds_read_b128 v[64:67], v96 offset:0
v_add_f32 v74, v3, v74
mav 24,136,32
ds_read_b128 v[68:71], v96 offset:1024
v_add_f32 v74, v4, v74
mav 28,140,32
v_add_f32 v74, v5, v74
v_add_f32 v74, v6, v74
mav 16,144,36
R 48,45056
v_add_f32 v74, v7, v74
mav 20,148,36
v_add_f32 v74, v8, v74
v_add_f32 v74, v9, v74
mav 24,152,36
v_add_f32 v74, v10, v74
v_add_f32 v74, v11, v74
mav 28,156,36
s_waitcnt lgkmcnt(2)
s_add_u32 s60, s60, 1
v_add_f32 v74, v12, v74
mav 16,160,40
s_cbranch_scc0 LR2_1
mav 20,164,40
R 52,46080
v_add_f32 v74, v13, v74
mav 24,168,40
v_add_f32 v74, v14, v74
v_add_f32 v74, v15, v74
mav 28,172,40
v_add_u32 v72, 1, v72
mav 16,176,44
R 56,47104
mav 20,180,44
v_permlane32_swap_b32 v74, v75
mav 24,184,44
D 83,2048
mav 28,188,44
s_waitcnt lgkmcnt(1)
v_add_f32 v74, v74, v75
mav 16,192,48
R 60,48128
mav 20,196,48
mav 24,200,48
D 83,3072
mav 28,204,48
mav 16,208,52
mav 20,212,52
mav 24,216,52
buffer_store_dwordx4 v[64:67], v91, s[36:39], s54 offen sc1
mav 28,220,52
s_waitcnt lgkmcnt(0)
mav 16,224,56
mav 20,228,56
mav 24,232,56
buffer_store_dwordx4 v[68:71], v92, s[36:39], s54 offen sc1
mav 28,236,56
mav 16,240,60
mav 20,244,60
mav 24,248,60
buffer_load_dwordx4 v[124:127], v90, s[36:39], s49 offen sc1
mav 28,252,60
s_waitcnt vmcnt(17)
mvv 16,116,164
buffer_store_dword v72, v81, s[36:39], s52 offen sc1
mvv 20,120,164
s_waitcnt vmcnt(16)
mvv 24,116,168
buffer_load_dwordx4 v[128:131], v90, s[36:39], s49 offen offset:1024 sc1
mvv 28,120,168
s_waitcnt vmcnt(16)
s_xor_b32 s50, s50, 0x10000
s_sub_u32 s54, s54, 32768
mvv 16,116,172
mvv 20,120,172
s_waitcnt vmcnt(15)
v_xor_b32 v80, 0x10000, v80
mvv 24,116,176
s_waitcnt vmcnt(14)
s_add_u32 s49, s49, 313344
mvv 28,120,176
s_barrier
mvv 16,116,180
R 32,0
mvv 20,120,180
s_waitcnt vmcnt(13)
mvv 24,116,184
buffer_load_dwordx4 v[132:135], v86, s[36:39], s49 offen sc1
mvv 28,120,184
s_waitcnt vmcnt(13)
mvv 16,116,188
R 36,1024
mvv 20,120,188
s_waitcnt vmcnt(12)
mvv 24,116,192
buffer_load_dwordx4 v[136:139], v86, s[36:39], s49 offen offset:1024 sc1
mvv 28,120,192
s_waitcnt vmcnt(12)
mvv 16,116,196
R 40,2048
mvv 20,120,196
s_waitcnt vmcnt(11)
mvv 24,116,200
buffer_load_dwordx4 v[140:143], v87, s[36:39], s49 offen sc1
mvv 28,120,200
s_waitcnt lgkmcnt(1)
v_mfma_f32_16x16x32_bf16 v[0:3], a[0:3], v[32:35], v[100:103]
R 44,3072
v_cvt_pk_bf16_f32 v64, v16, v17
v_mfma_f32_16x16x32_bf16 v[4:7], a[4:7], v[32:35], v[104:107]
v_cvt_pk_bf16_f32 v65, v18, v19
v_cvt_pk_bf16_f32 v66, v20, v21
v_mfma_f32_16x16x32_bf16 v[8:11], a[8:11], v[32:35], v[108:111]
buffer_load_dwordx4 v[144:147], v87, s[36:39], s49 offen offset:1024 sc1
v_mfma_f32_16x16x32_bf16 v[12:15], a[12:15], v[32:35], v[112:115]
v_cvt_pk_bf16_f32 v67, v22, v23
v_cvt_pk_bf16_f32 v68, v24, v25
mav 0,16,36
R 48,4096
v_cvt_pk_bf16_f32 v69, v26, v27
mav 4,20,36
v_cvt_pk_bf16_f32 v70, v28, v29
v_cvt_pk_bf16_f32 v71, v30, v31
mav 8,24,36
buffer_load_dwordx4 v[148:151], v88, s[36:39], s49 offen sc1
mav 12,28,36
s_waitcnt lgkmcnt(1)
v_mul_f32 v75, v16, v16
v_fmac_f32 v75, v17, v17
mav 0,32,40
R 52,5120
v_fmac_f32 v75, v18, v18
mav 4,36,40
v_fmac_f32 v75, v19, v19
v_fmac_f32 v75, v20, v20
mav 8,40,40
buffer_load_dwordx4 v[152:155], v88, s[36:39], s49 offen offset:1024 sc1
mav 12,44,40
v_fmac_f32 v75, v21, v21
v_fmac_f32 v75, v22, v22
mav 0,48,44
R 56,6144
v_fmac_f32 v75, v23, v23
mav 4,52,44
v_fmac_f32 v75, v24, v24
v_fmac_f32 v75, v25, v25
mav 8,56,44
buffer_load_dwordx4 v[156:159], v89, s[36:39], s49 offen sc1
mav 12,60,44
s_waitcnt lgkmcnt(1)
v_fmac_f32 v75, v26, v26
v_fmac_f32 v75, v27, v27
mav 0,64,48
R 60,7168
v_fmac_f32 v75, v28, v28
mav 4,68,48
v_fmac_f32 v75, v29, v29
v_fmac_f32 v75, v30, v30
mav 8,72,48
buffer_load_dwordx4 v[160:163], v89, s[36:39], s49 offen offset:1024 sc1
mav 12,76,48
v_fmac_f32 v75, v31, v31
mav 0,80,52
R 32,8192
mav 4,84,52
mav 8,88,52
s_add_u32 m0, s50, 0
mav 12,92,52
s_waitcnt lgkmcnt(1)
mav 0,96,56
R 36,9216
mav 4,100,56
mav 8,104,56
D 83,0
mav 12,108,56
ds_write_b32 v97, v74 offset:0
mav 0,112,60
R 40,10240
v_add_f32 v74, v17, v16
mav 4,116,60
ds_write_b128 v94, v[64:67] offset:0
mav 8,120,60
D 83,1024
mav 12,124,60
s_waitcnt lgkmcnt(3)
ds_write_b128 v95, v[68:71] offset:0
mav 0,128,32
LsR14:
s_cbranch_execz LsR18
R 44,11264
v_add_f32 v74, v18, v74
mav 4,132,32
ds_read_b128 v[64:67], v96 offset:0
v_add_f32 v74, v19, v74
mav 8,136,32
ds_read_b128 v[68:71], v96 offset:1024
v_add_f32 v74, v20, v74
mav 12,140,32
v_add_f32 v74, v21, v74
v_add_f32 v74, v22, v74
mav 0,144,36
R 48,12288
v_add_f32 v74, v23, v74
mav 4,148,36
v_add_f32 v74, v24, v74
v_add_f32 v74, v25, v74
mav 8,152,36
v_add_f32 v74, v26, v74
v_add_f32 v74, v27, v74
mav 12,156,36
s_waitcnt lgkmcnt(2)
v_add_f32 v74, v28, v74
v_add_f32 v74, v29, v74
mav 0,160,40
R 52,13312
v_add_f32 v74, v30, v74
mav 4,164,40
v_add_f32 v74, v31, v74
v_add_u32 v72, 1, v72
mav 8,168,40
v_permlane32_swap_b32 v74, v75
mav 12,172,40
v_add_f32 v74, v74, v75
mav 0,176,44
R 56,14336
mav 4,180,44
mav 8,184,44
D 83,2048
mav 12,188,44
s_waitcnt lgkmcnt(1)
mav 0,192,48
R 60,15360
mav 4,196,48
mav 8,200,48
D 83,3072
mav 12,204,48
mav 0,208,52
mav 4,212,52
mav 8,216,52
buffer_store_dwordx4 v[64:67], v91, s[36:39], s54 offen sc1
mav 12,220,52
s_waitcnt lgkmcnt(0)
mav 0,224,56
mav 4,228,56
mav 8,232,56
buffer_store_dwordx4 v[68:71], v92, s[36:39], s54 offen sc1
mav 12,236,56
mav 0,240,60
mav 4,244,60
mav 8,248,60
buffer_load_dwordx4 v[164:167], v90, s[36:39], s49 offen sc1
mav 12,252,60
s_waitcnt vmcnt(17)
mvv 0,116,124
buffer_store_dword v72, v81, s[36:39], s52 offen sc1
mvv 4,120,124
s_waitcnt vmcnt(16)
mvv 8,116,128
buffer_load_dwordx4 v[168:171], v90, s[36:39], s49 offen offset:1024 sc1
mvv 12,120,128
s_waitcnt vmcnt(16)
s_sub_u32 s54, s54, 3178496
mvv 0,116,132
mvv 4,120,132
s_waitcnt vmcnt(15)
mvv 8,116,136
s_waitcnt vmcnt(14)
s_add_u32 s49, s49, 313344
mvv 12,120,136
s_barrier
mvv 0,116,140
R 32,32768
mvv 4,120,140
s_waitcnt vmcnt(13)
mvv 8,116,144
buffer_load_dwordx4 v[172:175], v86, s[36:39], s49 offen sc1
mvv 12,120,144
s_waitcnt vmcnt(13)
mvv 0,116,148
R 36,33792
mvv 4,120,148
s_waitcnt vmcnt(12)
mvv 8,116,152
buffer_load_dwordx4 v[176:179], v86, s[36:39], s49 offen offset:1024 sc1
mvv 12,120,152
s_waitcnt vmcnt(12)
mvv 0,116,156
R 40,34816
mvv 4,120,156
s_waitcnt vmcnt(11)
mvv 8,116,160
buffer_load_dwordx4 v[180:183], v87, s[36:39], s49 offen sc1
mvv 12,120,160
s_waitcnt lgkmcnt(1)
v_mfma_f32_16x16x32_bf16 v[16:19], a[0:3], v[32:35], v[100:103]
R 44,35840
v_cvt_pk_bf16_f32 v64, v0, v1
v_mfma_f32_16x16x32_bf16 v[20:23], a[4:7], v[32:35], v[104:107]
v_cvt_pk_bf16_f32 v65, v2, v3
v_cvt_pk_bf16_f32 v66, v4, v5
v_mfma_f32_16x16x32_bf16 v[24:27], a[8:11], v[32:35], v[108:111]
buffer_load_dwordx4 v[184:187], v87, s[36:39], s49 offen offset:1024 sc1
v_mfma_f32_16x16x32_bf16 v[28:31], a[12:15], v[32:35], v[112:115]
v_cvt_pk_bf16_f32 v67, v6, v7
v_cvt_pk_bf16_f32 v68, v8, v9
mav 16,16,36
R 48,36864
v_cvt_pk_bf16_f32 v69, v10, v11
mav 20,20,36
v_cvt_pk_bf16_f32 v70, v12, v13
v_cvt_pk_bf16_f32 v71, v14, v15
mav 24,24,36
buffer_load_dwordx4 v[188:191], v88, s[36:39], s49 offen sc1
mav 28,28,36
s_waitcnt lgkmcnt(1)
mav 16,32,40
R 52,37888
mav 20,36,40
mav 24,40,40
buffer_load_dwordx4 v[192:195], v88, s[36:39], s49 offen offset:1024 sc1
mav 28,44,40
mav 16,48,44
R 56,38912
mav 20,52,44
mav 24,56,44
buffer_load_dwordx4 v[196:199], v89, s[36:39], s49 offen sc1
mav 28,60,44
s_waitcnt lgkmcnt(1)
mav 16,64,48
R 60,39936
mav 20,68,48
mav 24,72,48
buffer_load_dwordx4 v[200:203], v89, s[36:39], s49 offen offset:1024 sc1
mav 28,76,48
mav 16,80,52
R 32,40960
mav 20,84,52
mav 24,88,52
s_add_u32 m0, s50, 32768
mav 28,92,52
s_waitcnt lgkmcnt(1)
mav 16,96,56
R 36,41984
mav 20,100,56
mav 24,104,56
D 83,0
mav 28,108,56
ds_write_b32 v97, v74 offset:1024
mav 16,112,60
R 40,43008
mav 20,116,60
ds_write_b128 v94, v[64:67] offset:0
mav 24,120,60
D 83,1024
mav 28,124,60
s_waitcnt lgkmcnt(3)
ds_write_b128 v95, v[68:71] offset:0
mav 16,128,32
R 44,44032
mav 20,132,32
ds_read_b128 v[64:67], v96 offset:0
mav 24,136,32
ds_read_b128 v[68:71], v96 offset:1024
mav 28,140,32
mav 16,144,36
R 48,45056
mav 20,148,36
mav 24,152,36
mav 28,156,36
s_waitcnt lgkmcnt(2)
mav 16,160,40
mav 20,164,40
R 52,46080
v_add_u32 v72, 69, v72
mav 24,168,40
mav 28,172,40
mav 16,176,44
R 56,47104
mav 20,180,44
mav 24,184,44
D 83,2048
mav 28,188,44
s_waitcnt lgkmcnt(1)
mav 16,192,48
R 60,48128
mav 20,196,48
mav 24,200,48
D 83,3072
mav 28,204,48
mav 16,208,52
mav 20,212,52
mav 24,216,52
buffer_store_dwordx4 v[64:67], v91, s[44:47], s54 offen sc1
mav 28,220,52
s_waitcnt lgkmcnt(0)
mav 16,224,56
mav 20,228,56
mav 24,232,56
buffer_store_dwordx4 v[68:71], v92, s[44:47], s54 offen sc1
mav 28,236,56
mav 16,240,60
mav 20,244,60
mav 24,248,60
buffer_load_dwordx4 v[124:127], v90, s[36:39], s49 offen sc1
mav 28,252,60
s_waitcnt vmcnt(17)
mvv 16,116,164
buffer_store_dword v72, v81, s[36:39], s52 offen sc1
mvv 20,120,164
s_waitcnt vmcnt(16)
mvv 24,116,168
buffer_load_dwordx4 v[128:131], v90, s[36:39], s49 offen offset:1024 sc1
mvv 28,120,168
s_waitcnt vmcnt(16)
s_xor_b32 s50, s50, 0x10000
s_sub_u32 s54, s54, 32768
mvv 16,116,172
mvv 20,120,172
s_waitcnt vmcnt(15)
v_xor_b32 v80, 0x10000, v80
mvv 24,116,176
s_waitcnt vmcnt(14)
s_add_u32 s49, s49, 313344
mvv 28,120,176
s_barrier
mvv 16,116,180
R 32,0
mvv 20,120,180
s_waitcnt vmcnt(13)
mvv 24,116,184
buffer_load_dwordx4 v[132:135], v86, s[36:39], s49 offen sc1
mvv 28,120,184
s_waitcnt vmcnt(13)
mvv 16,116,188
R 36,1024
mvv 20,120,188
s_waitcnt vmcnt(12)
mvv 24,116,192
buffer_load_dwordx4 v[136:139], v86, s[36:39], s49 offen offset:1024 sc1
mvv 28,120,192
s_waitcnt vmcnt(12)
mvv 16,116,196
R 40,2048
mvv 20,120,196
s_waitcnt vmcnt(11)
mvv 24,116,200
buffer_load_dwordx4 v[140:143], v87, s[36:39], s49 offen sc1
mvv 28,120,200
s_waitcnt lgkmcnt(1)
v_mfma_f32_16x16x32_bf16 v[0:3], a[0:3], v[32:35], v[100:103]
R 44,3072
v_cvt_pk_bf16_f32 v64, v16, v17
v_mfma_f32_16x16x32_bf16 v[4:7], a[4:7], v[32:35], v[104:107]
v_cvt_pk_bf16_f32 v65, v18, v19
v_cvt_pk_bf16_f32 v66, v20, v21
v_mfma_f32_16x16x32_bf16 v[8:11], a[8:11], v[32:35], v[108:111]
buffer_load_dwordx4 v[144:147], v87, s[36:39], s49 offen offset:1024 sc1
v_mfma_f32_16x16x32_bf16 v[12:15], a[12:15], v[32:35], v[112:115]
v_cvt_pk_bf16_f32 v67, v22, v23
v_cvt_pk_bf16_f32 v68, v24, v25
mav 0,16,36
R 48,4096
v_cvt_pk_bf16_f32 v69, v26, v27
mav 4,20,36
v_cvt_pk_bf16_f32 v70, v28, v29
v_cvt_pk_bf16_f32 v71, v30, v31
mav 8,24,36
buffer_load_dwordx4 v[148:151], v88, s[36:39], s49 offen sc1
mav 12,28,36
s_waitcnt lgkmcnt(1)
mav 0,32,40
R 52,5120
mav 4,36,40
mav 8,40,40
buffer_load_dwordx4 v[152:155], v88, s[36:39], s49 offen offset:1024 sc1
mav 12,44,40
mav 0,48,44
R 56,6144
mav 4,52,44
mav 8,56,44
buffer_load_dwordx4 v[156:159], v89, s[36:39], s49 offen sc1
mav 12,60,44
s_waitcnt lgkmcnt(1)
mav 0,64,48
R 60,7168
mav 4,68,48
mav 8,72,48
buffer_load_dwordx4 v[160:163], v89, s[36:39], s49 offen offset:1024 sc1
mav 12,76,48
mav 0,80,52
R 32,8192
mav 4,84,52
mav 8,88,52
s_add_u32 m0, s50, 0
mav 12,92,52
s_waitcnt lgkmcnt(1)
mav 0,96,56
R 36,9216
mav 4,100,56
mav 8,104,56
D 83,0
mav 12,108,56
ds_write_b128 v94, v[64:67] offset:0
mav 0,112,60
R 40,10240
mav 4,116,60
ds_write_b128 v95, v[68:71] offset:0
mav 8,120,60
D 83,1024
mav 12,124,60
s_waitcnt lgkmcnt(3)
ds_read_b128 v[64:67], v96 offset:0
mav 0,128,32
R 44,11264
mav 4,132,32
ds_read_b128 v[68:71], v96 offset:1024
mav 8,136,32
mav 12,140,32
mav 0,144,36
R 48,12288
mav 4,148,36
mav 8,152,36
mav 12,156,36
s_waitcnt lgkmcnt(1)
mav 0,160,40
R 52,13312
mav 4,164,40
mav 8,168,40
mav 12,172,40
mav 0,176,44
R 56,14336
mav 4,180,44
mav 8,184,44
D 83,2048
mav 12,188,44
s_waitcnt lgkmcnt(1)
mav 0,192,48
R 60,15360
mav 4,196,48
mav 8,200,48
D 83,3072
mav 12,204,48
mav 0,208,52
mav 4,212,52
mav 8,216,52
buffer_store_dwordx4 v[64:67], v91, s[44:47], s54 offen sc1
mav 12,220,52
s_waitcnt lgkmcnt(0)
mav 0,224,56
mav 4,228,56
mav 8,232,56
buffer_store_dwordx4 v[68:71], v92, s[44:47], s54 offen sc1
mav 12,236,56
mav 0,240,60
mav 4,244,60
mav 8,248,60
buffer_load_dwordx4 v[164:167], v90, s[36:39], s49 offen sc1
mav 12,252,60
s_waitcnt vmcnt(17)
mvv 0,116,124
mvv 4,120,124
s_waitcnt vmcnt(15)
mvv 8,116,128
buffer_load_dwordx4 v[168:171], v90, s[36:39], s49 offen offset:1024 sc1
mvv 12,120,128
s_waitcnt vmcnt(15)
s_sub_u32 s54, s54, 32768
mvv 0,116,132
mvv 4,120,132
s_waitcnt vmcnt(14)
mvv 8,116,136
s_waitcnt vmcnt(13)
s_add_u32 s49, s49, 313344
mvv 12,120,136
s_barrier
mvv 0,116,140
R 32,32768
mvv 4,120,140
s_waitcnt vmcnt(12)
mvv 8,116,144
buffer_load_dwordx4 v[172:175], v86, s[36:39], s49 offen sc1
mvv 12,120,144
s_waitcnt vmcnt(12)
mvv 0,116,148
R 36,33792
mvv 4,120,148
s_waitcnt vmcnt(11)
mvv 8,116,152
buffer_load_dwordx4 v[176:179], v86, s[36:39], s49 offen offset:1024 sc1
mvv 12,120,152
s_waitcnt vmcnt(11)
mvv 0,116,156
R 40,34816
mvv 4,120,156
s_waitcnt vmcnt(10)
mvv 8,116,160
buffer_load_dwordx4 v[180:183], v87, s[36:39], s49 offen sc1
mvv 12,120,160
s_waitcnt lgkmcnt(1)
v_mfma_f32_16x16x32_bf16 v[16:19], a[0:3], v[32:35], v[100:103]
R 44,35840
v_cvt_pk_bf16_f32 v64, v0, v1
v_mfma_f32_16x16x32_bf16 v[20:23], a[4:7], v[32:35], v[104:107]
v_cvt_pk_bf16_f32 v65, v2, v3
v_cvt_pk_bf16_f32 v66, v4, v5
v_mfma_f32_16x16x32_bf16 v[24:27], a[8:11], v[32:35], v[108:111]
buffer_load_dwordx4 v[184:187], v87, s[36:39], s49 offen offset:1024 sc1
v_mfma_f32_16x16x32_bf16 v[28:31], a[12:15], v[32:35], v[112:115]
v_cvt_pk_bf16_f32 v67, v6, v7
v_cvt_pk_bf16_f32 v68, v8, v9
mav 16,16,36
R 48,36864
v_cvt_pk_bf16_f32 v69, v10, v11
mav 20,20,36
v_cvt_pk_bf16_f32 v70, v12, v13
v_cvt_pk_bf16_f32 v71, v14, v15
mav 24,24,36
buffer_load_dwordx4 v[188:191], v88, s[36:39], s49 offen sc1
mav 28,28,36
s_waitcnt lgkmcnt(1)
mav 16,32,40
R 52,37888
mav 20,36,40
mav 24,40,40
buffer_load_dwordx4 v[192:195], v88, s[36:39], s49 offen offset:1024 sc1
mav 28,44,40
mav 16,48,44
R 56,38912
mav 20,52,44
mav 24,56,44
buffer_load_dwordx4 v[196:199], v89, s[36:39], s49 offen sc1
mav 28,60,44
s_waitcnt lgkmcnt(1)
mav 16,64,48
R 60,39936
mav 20,68,48
mav 24,72,48
buffer_load_dwordx4 v[200:203], v89, s[36:39], s49 offen offset:1024 sc1
mav 28,76,48
mav 16,80,52
R 32,40960
mav 20,84,52
mav 24,88,52
s_add_u32 m0, s50, 32768
mav 28,92,52
s_waitcnt lgkmcnt(1)
mav 16,96,56
R 36,41984
mav 20,100,56
mav 24,104,56
D 83,0
mav 28,108,56
ds_write_b128 v94, v[64:67] offset:0
mav 16,112,60
R 40,43008
mav 20,116,60
ds_write_b128 v95, v[68:71] offset:0
mav 24,120,60
D 83,1024
mav 28,124,60
s_waitcnt lgkmcnt(3)
ds_read_b128 v[64:67], v96 offset:0
mav 16,128,32
R 44,44032
mav 20,132,32
ds_read_b128 v[68:71], v96 offset:1024
mav 24,136,32
mav 28,140,32
mav 16,144,36
R 48,45056
mav 20,148,36
mav 24,152,36
mav 28,156,36
s_waitcnt lgkmcnt(1)
mav 16,160,40
s_mov_b32 s60, -13
.p2align 6
LR2_2:
mav 20,164,40
R 52,46080
mav 24,168,40
mav 28,172,40
mav 16,176,44
R 56,47104
mav 20,180,44
mav 24,184,44
D 83,2048
mav 28,188,44
s_waitcnt lgkmcnt(1)
mav 16,192,48
R 60,48128
mav 20,196,48
mav 24,200,48
D 83,3072
mav 28,204,48
mav 16,208,52
mav 20,212,52
mav 24,216,52
buffer_store_dwordx4 v[64:67], v91, s[44:47], s54 offen sc1
mav 28,220,52
s_waitcnt lgkmcnt(0)
mav 16,224,56
mav 20,228,56
mav 24,232,56
buffer_store_dwordx4 v[68:71], v92, s[44:47], s54 offen sc1
mav 28,236,56
mav 16,240,60
mav 20,244,60
mav 24,248,60
buffer_load_dwordx4 v[124:127], v90, s[36:39], s49 offen sc1
mav 28,252,60
s_waitcnt vmcnt(16)
mvv 16,116,164
mvv 20,120,164
s_waitcnt vmcnt(15)
mvv 24,116,168
buffer_load_dwordx4 v[128:131], v90, s[36:39], s49 offen offset:1024 sc1
mvv 28,120,168
s_waitcnt vmcnt(15)
s_xor_b32 s50, s50, 0x10000
s_sub_u32 s54, s54, 32768
mvv 16,116,172
mvv 20,120,172
s_waitcnt vmcnt(14)
v_xor_b32 v80, 0x10000, v80
mvv 24,116,176
s_waitcnt vmcnt(13)
s_add_u32 s49, s49, 313344
mvv 28,120,176
s_barrier
mvv 16,116,180
R 32,0
mvv 20,120,180
s_waitcnt vmcnt(12)
mvv 24,116,184
buffer_load_dwordx4 v[132:135], v86, s[36:39], s49 offen sc1
mvv 28,120,184
s_waitcnt vmcnt(12)
mvv 16,116,188
LsR15:
s_cbranch_execz LsR19
R 36,1024
mvv 20,120,188
s_waitcnt vmcnt(11)
mvv 24,116,192
buffer_load_dwordx4 v[136:139], v86, s[36:39], s49 offen offset:1024 sc1
mvv 28,120,192
s_waitcnt vmcnt(11)
mvv 16,116,196
R 40,2048
mvv 20,120,196
s_waitcnt vmcnt(10)
mvv 24,116,200
buffer_load_dwordx4 v[140:143], v87, s[36:39], s49 offen sc1
mvv 28,120,200
s_waitcnt lgkmcnt(1)
v_mfma_f32_16x16x32_bf16 v[0:3], a[0:3], v[32:35], v[100:103]
R 44,3072
v_cvt_pk_bf16_f32 v64, v16, v17
v_mfma_f32_16x16x32_bf16 v[4:7], a[4:7], v[32:35], v[104:107]
v_cvt_pk_bf16_f32 v65, v18, v19
v_cvt_pk_bf16_f32 v66, v20, v21
v_mfma_f32_16x16x32_bf16 v[8:11], a[8:11], v[32:35], v[108:111]
buffer_load_dwordx4 v[144:147], v87, s[36:39], s49 offen offset:1024 sc1
v_mfma_f32_16x16x32_bf16 v[12:15], a[12:15], v[32:35], v[112:115]
v_cvt_pk_bf16_f32 v67, v22, v23
v_cvt_pk_bf16_f32 v68, v24, v25
mav 0,16,36
R 48,4096
v_cvt_pk_bf16_f32 v69, v26, v27
mav 4,20,36
v_cvt_pk_bf16_f32 v70, v28, v29
v_cvt_pk_bf16_f32 v71, v30, v31
mav 8,24,36
buffer_load_dwordx4 v[148:151], v88, s[36:39], s49 offen sc1
mav 12,28,36
s_waitcnt lgkmcnt(1)
mav 0,32,40
R 52,5120
mav 4,36,40
mav 8,40,40
buffer_load_dwordx4 v[152:155], v88, s[36:39], s49 offen offset:1024 sc1
mav 12,44,40
mav 0,48,44
R 56,6144
mav 4,52,44
mav 8,56,44
buffer_load_dwordx4 v[156:159], v89, s[36:39], s49 offen sc1
mav 12,60,44
s_waitcnt lgkmcnt(1)
mav 0,64,48
R 60,7168
mav 4,68,48
mav 8,72,48
buffer_load_dwordx4 v[160:163], v89, s[36:39], s49 offen offset:1024 sc1
mav 12,76,48
mav 0,80,52
R 32,8192
mav 4,84,52
mav 8,88,52
s_add_u32 m0, s50, 0
mav 12,92,52
s_waitcnt lgkmcnt(1)
mav 0,96,56
R 36,9216
mav 4,100,56
mav 8,104,56
D 83,0
mav 12,108,56
ds_write_b128 v94, v[64:67] offset:0
mav 0,112,60
R 40,10240
mav 4,116,60
ds_write_b128 v95, v[68:71] offset:0
mav 8,120,60
D 83,1024
mav 12,124,60
s_waitcnt lgkmcnt(3)
ds_read_b128 v[64:67], v96 offset:0
mav 0,128,32
R 44,11264
mav 4,132,32
ds_read_b128 v[68:71], v96 offset:1024
mav 8,136,32
mav 12,140,32
mav 0,144,36
R 48,12288
mav 4,148,36
mav 8,152,36
mav 12,156,36
s_waitcnt lgkmcnt(1)
mav 0,160,40
R 52,13312
mav 4,164,40
mav 8,168,40
mav 12,172,40
mav 0,176,44
R 56,14336
mav 4,180,44
mav 8,184,44
D 83,2048
mav 12,188,44
s_waitcnt lgkmcnt(1)
mav 0,192,48
R 60,15360
mav 4,196,48
mav 8,200,48
D 83,3072
mav 12,204,48
mav 0,208,52
mav 4,212,52
mav 8,216,52
buffer_store_dwordx4 v[64:67], v91, s[44:47], s54 offen sc1
mav 12,220,52
s_waitcnt lgkmcnt(0)
mav 0,224,56
mav 4,228,56
mav 8,232,56
buffer_store_dwordx4 v[68:71], v92, s[44:47], s54 offen sc1
mav 12,236,56
mav 0,240,60
mav 4,244,60
mav 8,248,60
buffer_load_dwordx4 v[164:167], v90, s[36:39], s49 offen sc1
mav 12,252,60
s_waitcnt vmcnt(16)
mvv 0,116,124
mvv 4,120,124
s_waitcnt vmcnt(15)
mvv 8,116,128
buffer_load_dwordx4 v[168:171], v90, s[36:39], s49 offen offset:1024 sc1
mvv 12,120,128
s_waitcnt vmcnt(15)
s_sub_u32 s54, s54, 32768
mvv 0,116,132
mvv 4,120,132
s_waitcnt vmcnt(14)
mvv 8,116,136
s_waitcnt vmcnt(13)
s_add_u32 s49, s49, 313344
mvv 12,120,136
s_barrier
mvv 0,116,140
R 32,32768
mvv 4,120,140
s_waitcnt vmcnt(12)
mvv 8,116,144
buffer_load_dwordx4 v[172:175], v86, s[36:39], s49 offen sc1
mvv 12,120,144
s_waitcnt vmcnt(12)
mvv 0,116,148
R 36,33792
mvv 4,120,148
s_waitcnt vmcnt(11)
mvv 8,116,152
buffer_load_dwordx4 v[176:179], v86, s[36:39], s49 offen offset:1024 sc1
)"
R"(
mvv 12,120,152
s_waitcnt vmcnt(11)
mvv 0,116,156
R 40,34816
mvv 4,120,156
s_waitcnt vmcnt(10)
mvv 8,116,160
buffer_load_dwordx4 v[180:183], v87, s[36:39], s49 offen sc1
mvv 12,120,160
s_waitcnt lgkmcnt(1)
v_mfma_f32_16x16x32_bf16 v[16:19], a[0:3], v[32:35], v[100:103]
R 44,35840
v_cvt_pk_bf16_f32 v64, v0, v1
v_mfma_f32_16x16x32_bf16 v[20:23], a[4:7], v[32:35], v[104:107]
v_cvt_pk_bf16_f32 v65, v2, v3
v_cvt_pk_bf16_f32 v66, v4, v5
v_mfma_f32_16x16x32_bf16 v[24:27], a[8:11], v[32:35], v[108:111]
buffer_load_dwordx4 v[184:187], v87, s[36:39], s49 offen offset:1024 sc1
v_mfma_f32_16x16x32_bf16 v[28:31], a[12:15], v[32:35], v[112:115]
v_cvt_pk_bf16_f32 v67, v6, v7
v_cvt_pk_bf16_f32 v68, v8, v9
mav 16,16,36
R 48,36864
v_cvt_pk_bf16_f32 v69, v10, v11
mav 20,20,36
v_cvt_pk_bf16_f32 v70, v12, v13
v_cvt_pk_bf16_f32 v71, v14, v15
mav 24,24,36
buffer_load_dwordx4 v[188:191], v88, s[36:39], s49 offen sc1
mav 28,28,36
s_waitcnt lgkmcnt(1)
mav 16,32,40
R 52,37888
mav 20,36,40
mav 24,40,40
buffer_load_dwordx4 v[192:195], v88, s[36:39], s49 offen offset:1024 sc1
mav 28,44,40
mav 16,48,44
R 56,38912
mav 20,52,44
mav 24,56,44
buffer_load_dwordx4 v[196:199], v89, s[36:39], s49 offen sc1
mav 28,60,44
s_waitcnt lgkmcnt(1)
mav 16,64,48
R 60,39936
mav 20,68,48
mav 24,72,48
buffer_load_dwordx4 v[200:203], v89, s[36:39], s49 offen offset:1024 sc1
mav 28,76,48
mav 16,80,52
R 32,40960
mav 20,84,52
mav 24,88,52
s_add_u32 m0, s50, 32768
mav 28,92,52
s_waitcnt lgkmcnt(1)
mav 16,96,56
R 36,41984
mav 20,100,56
mav 24,104,56
D 83,0
mav 28,108,56
ds_write_b128 v94, v[64:67] offset:0
mav 16,112,60
R 40,43008
mav 20,116,60
ds_write_b128 v95, v[68:71] offset:0
mav 24,120,60
D 83,1024
mav 28,124,60
s_waitcnt lgkmcnt(3)
ds_read_b128 v[64:67], v96 offset:0
mav 16,128,32
R 44,44032
mav 20,132,32
ds_read_b128 v[68:71], v96 offset:1024
mav 24,136,32
mav 28,140,32
mav 16,144,36
R 48,45056
mav 20,148,36
mav 24,152,36
mav 28,156,36
s_waitcnt lgkmcnt(1)
s_add_u32 s60, s60, 1
mav 16,160,40
s_cbranch_scc0 LR2_2
mav 20,164,40
R 52,46080
mav 24,168,40
mav 28,172,40
mav 16,176,44
R 56,47104
mav 20,180,44
mav 24,184,44
D 83,2048
mav 28,188,44
s_waitcnt lgkmcnt(1)
mav 16,192,48
R 60,48128
mav 20,196,48
mav 24,200,48
D 83,3072
mav 28,204,48
mav 16,208,52
mav 20,212,52
mav 24,216,52
buffer_store_dwordx4 v[64:67], v91, s[44:47], s54 offen sc1
mav 28,220,52
s_waitcnt lgkmcnt(0)
mav 16,224,56
mav 20,228,56
mav 24,232,56
buffer_store_dwordx4 v[68:71], v92, s[44:47], s54 offen sc1
mav 28,236,56
mav 16,240,60
mav 20,244,60
mav 24,248,60
buffer_load_dwordx4 v[124:127], v90, s[36:39], s49 offen sc1
mav 28,252,60
s_waitcnt vmcnt(16)
mvv 16,116,164
mvv 20,120,164
s_waitcnt vmcnt(15)
mvv 24,116,168
buffer_load_dwordx4 v[128:131], v90, s[36:39], s49 offen offset:1024 sc1
mvv 28,120,168
s_waitcnt vmcnt(15)
s_sub_u32 s54, s54, 32768
mvv 16,116,172
mvv 20,120,172
s_waitcnt vmcnt(14)
v_xor_b32 v80, 0x10000, v80
mvv 24,116,176
s_waitcnt vmcnt(13)
s_add_u32 s49, s49, 313344
mvv 28,120,176
s_barrier
mvv 16,116,180
R 32,0
mvv 20,120,180
s_waitcnt vmcnt(12)
mvv 24,116,184
buffer_load_dwordx4 v[132:135], v86, s[36:39], s49 offen sc1
mvv 28,120,184
s_waitcnt vmcnt(12)
mvv 16,116,188
R 36,1024
mvv 20,120,188
s_waitcnt vmcnt(11)
mvv 24,116,192
buffer_load_dwordx4 v[136:139], v86, s[36:39], s49 offen offset:1024 sc1
mvv 28,120,192
s_waitcnt vmcnt(11)
mvv 16,116,196
R 40,2048
mvv 20,120,196
s_waitcnt vmcnt(10)
mvv 24,116,200
buffer_load_dwordx4 v[140:143], v87, s[36:39], s49 offen sc1
mvv 28,120,200
s_waitcnt lgkmcnt(1)
v_mfma_f32_16x16x32_bf16 v[0:3], a[0:3], v[32:35], v[100:103]
R 44,3072
v_cvt_pk_bf16_f32 v64, v16, v17
v_mfma_f32_16x16x32_bf16 v[4:7], a[4:7], v[32:35], v[104:107]
v_cvt_pk_bf16_f32 v65, v18, v19
v_cvt_pk_bf16_f32 v66, v20, v21
v_mfma_f32_16x16x32_bf16 v[8:11], a[8:11], v[32:35], v[108:111]
buffer_load_dwordx4 v[144:147], v87, s[36:39], s49 offen offset:1024 sc1
v_mfma_f32_16x16x32_bf16 v[12:15], a[12:15], v[32:35], v[112:115]
v_cvt_pk_bf16_f32 v67, v22, v23
v_cvt_pk_bf16_f32 v68, v24, v25
mav 0,16,36
R 48,4096
v_cvt_pk_bf16_f32 v69, v26, v27
mav 4,20,36
v_cvt_pk_bf16_f32 v70, v28, v29
v_cvt_pk_bf16_f32 v71, v30, v31
mav 8,24,36
buffer_load_dwordx4 v[148:151], v88, s[36:39], s49 offen sc1
mav 12,28,36
s_waitcnt lgkmcnt(1)
mav 0,32,40
R 52,5120
mav 4,36,40
mav 8,40,40
buffer_load_dwordx4 v[152:155], v88, s[36:39], s49 offen offset:1024 sc1
mav 12,44,40
mav 0,48,44
R 56,6144
mav 4,52,44
mav 8,56,44
buffer_load_dwordx4 v[156:159], v89, s[36:39], s49 offen sc1
mav 12,60,44
s_waitcnt lgkmcnt(1)
mav 0,64,48
R 60,7168
mav 4,68,48
mav 8,72,48
buffer_load_dwordx4 v[160:163], v89, s[36:39], s49 offen offset:1024 sc1
mav 12,76,48
mav 0,80,52
R 32,8192
mav 4,84,52
mav 8,88,52
mav 12,92,52
s_waitcnt lgkmcnt(1)
mav 0,96,56
R 36,9216
mav 4,100,56
mav 8,104,56
mav 12,108,56
ds_write_b128 v94, v[64:67] offset:0
mav 0,112,60
R 40,10240
mav 4,116,60
ds_write_b128 v95, v[68:71] offset:0
mav 8,120,60
ds_read_b128 v[64:67], v96 offset:0
mav 12,124,60
s_waitcnt lgkmcnt(4)
ds_read_b128 v[68:71], v96 offset:1024
mav 0,128,32
R 44,11264
mav 4,132,32
mav 8,136,32
mav 12,140,32
mav 0,144,36
R 48,12288
mav 4,148,36
mav 8,152,36
mav 12,156,36
s_waitcnt lgkmcnt(1)
mav 0,160,40
R 52,13312
mav 4,164,40
mav 8,168,40
mav 12,172,40
mav 0,176,44
R 56,14336
mav 4,180,44
mav 8,184,44
mav 12,188,44
s_waitcnt lgkmcnt(1)
mav 0,192,48
R 60,15360
mav 4,196,48
mav 8,200,48
mav 12,204,48
mav 0,208,52
mav 4,212,52
mav 8,216,52
buffer_store_dwordx4 v[64:67], v91, s[44:47], s54 offen sc1
mav 12,220,52
s_waitcnt lgkmcnt(0)
mav 0,224,56
mav 4,228,56
mav 8,232,56
buffer_store_dwordx4 v[68:71], v92, s[44:47], s54 offen sc1
mav 12,236,56
mav 0,240,60
mav 4,244,60
mav 8,248,60
buffer_load_dwordx4 v[164:167], v90, s[36:39], s49 offen sc1
mav 12,252,60
s_waitcnt vmcnt(12)
mvv 0,116,124
mvv 4,120,124
s_waitcnt vmcnt(11)
mvv 8,116,128
buffer_load_dwordx4 v[168:171], v90, s[36:39], s49 offen offset:1024 sc1
mvv 12,120,128
s_waitcnt vmcnt(11)
s_sub_u32 s54, s54, 32768
mvv 0,116,132
mvv 4,120,132
s_waitcnt vmcnt(10)
mvv 8,116,136
s_waitcnt vmcnt(9)
s_add_u32 s49, s49, 313344
mvv 12,120,136
s_barrier
mvv 0,116,140
R 32,32768
mvv 4,120,140
s_waitcnt vmcnt(8)
mvv 8,116,144
buffer_load_dwordx4 v[172:175], v86, s[36:39], s49 offen sc1
mvv 12,120,144
s_waitcnt vmcnt(8)
mvv 0,116,148
R 36,33792
mvv 4,120,148
s_waitcnt vmcnt(7)
mvv 8,116,152
buffer_load_dwordx4 v[176:179], v86, s[36:39], s49 offen offset:1024 sc1
mvv 12,120,152
s_waitcnt vmcnt(7)
mvv 0,116,156
R 40,34816
mvv 4,120,156
s_waitcnt vmcnt(6)
mvv 8,116,160
buffer_load_dwordx4 v[180:183], v87, s[36:39], s49 offen sc1
mvv 12,120,160
s_waitcnt lgkmcnt(1)
v_mfma_f32_16x16x32_bf16 v[16:19], a[0:3], v[32:35], v[100:103]
R 44,35840
v_cvt_pk_bf16_f32 v64, v0, v1
v_mfma_f32_16x16x32_bf16 v[20:23], a[4:7], v[32:35], v[104:107]
v_cvt_pk_bf16_f32 v65, v2, v3
v_cvt_pk_bf16_f32 v66, v4, v5
v_mfma_f32_16x16x32_bf16 v[24:27], a[8:11], v[32:35], v[108:111]
buffer_load_dwordx4 v[184:187], v87, s[36:39], s49 offen offset:1024 sc1
v_mfma_f32_16x16x32_bf16 v[28:31], a[12:15], v[32:35], v[112:115]
v_cvt_pk_bf16_f32 v67, v6, v7
v_cvt_pk_bf16_f32 v68, v8, v9
mav 16,16,36
R 48,36864
v_cvt_pk_bf16_f32 v69, v10, v11
mav 20,20,36
ds_write_b128 v94, v[64:67] offset:0
mav 24,24,36
buffer_load_dwordx4 v[188:191], v88, s[36:39], s49 offen sc1
mav 28,28,36
s_waitcnt lgkmcnt(2)
v_cvt_pk_bf16_f32 v70, v12, v13
v_cvt_pk_bf16_f32 v71, v14, v15
mav 16,32,40
R 52,37888
mav 20,36,40
ds_write_b128 v95, v[68:71] offset:0
mav 24,40,40
buffer_load_dwordx4 v[192:195], v88, s[36:39], s49 offen offset:1024 sc1
mav 28,44,40
ds_read_b128 v[64:67], v96 offset:0
mav 16,48,44
R 56,38912
mav 20,52,44
ds_read_b128 v[68:71], v96 offset:1024
mav 24,56,44
buffer_load_dwordx4 v[196:199], v89, s[36:39], s49 offen sc1
mav 28,60,44
s_waitcnt lgkmcnt(3)
mav 16,64,48
R 60,39936
mav 20,68,48
mav 24,72,48
s_waitcnt lgkmcnt(2)
buffer_load_dwordx4 v[200:203], v89, s[36:39], s49 offen offset:1024 sc1
mav 28,76,48
mav 16,80,52
s_waitcnt lgkmcnt(1)
R 32,40960
mav 20,84,52
mav 24,88,52
buffer_store_dwordx4 v[64:67], v91, s[44:47], s54 offen sc1
mav 28,92,52
mav 16,96,56
R 36,41984
mav 20,100,56
mav 24,104,56
buffer_store_dwordx4 v[68:71], v92, s[44:47], s54 offen sc1
mav 28,108,56
s_waitcnt lgkmcnt(1)
mav 16,112,60
R 40,43008
mav 20,116,60
mav 24,120,60
mav 28,124,60
mav 16,128,32
R 44,44032
mav 20,132,32
mav 24,136,32
mav 28,140,32
s_waitcnt lgkmcnt(1)
mav 16,144,36
R 48,45056
mav 20,148,36
mav 24,152,36
mav 28,156,36
mav 16,160,40
mav 20,164,40
R 52,46080
mav 24,168,40
mav 28,172,40
s_waitcnt lgkmcnt(1)
mav 16,176,44
R 56,47104
mav 20,180,44
LsR16:
s_cbranch_execz LsR20
mav 24,184,44
mav 28,188,44
mav 16,192,48
R 60,48128
mav 20,196,48
mav 24,200,48
mav 28,204,48
s_waitcnt lgkmcnt(1)
mav 16,208,52
mav 20,212,52
mav 24,216,52
mav 28,220,52
mav 16,224,56
mav 20,228,56
mav 24,232,56
mav 28,236,56
s_waitcnt lgkmcnt(0)
mav 16,240,60
mav 20,244,60
mav 24,248,60
mav 28,252,60
s_waitcnt vmcnt(11)
mvv 16,116,164
mvv 20,120,164
s_waitcnt vmcnt(10)
mvv 24,116,168
s_sub_u32 s54, s54, 32768
mvv 28,120,168
s_waitcnt vmcnt(9)
mvv 16,116,172
mvv 20,120,172
s_waitcnt vmcnt(8)
mvv 24,116,176
mvv 28,120,176
s_waitcnt vmcnt(7)
mvv 16,116,180
mvv 20,120,180
s_waitcnt vmcnt(6)
mvv 24,116,184
mvv 28,120,184
s_waitcnt vmcnt(5)
mvv 16,116,188
mvv 20,120,188
s_waitcnt vmcnt(4)
mvv 24,116,192
mvv 28,120,192
s_waitcnt vmcnt(3)
mvv 16,116,196
mvv 20,120,196
s_waitcnt vmcnt(2)
mvv 24,116,200
mvv 28,120,200
s_nop 3
v_cvt_pk_bf16_f32 v64, v16, v17
v_cvt_pk_bf16_f32 v65, v18, v19
v_cvt_pk_bf16_f32 v66, v20, v21
v_cvt_pk_bf16_f32 v67, v22, v23
ds_write_b128 v94, v[64:67] offset:0
v_cvt_pk_bf16_f32 v68, v24, v25
v_cvt_pk_bf16_f32 v69, v26, v27
v_cvt_pk_bf16_f32 v70, v28, v29
v_cvt_pk_bf16_f32 v71, v30, v31
ds_write_b128 v95, v[68:71] offset:0
ds_read_b128 v[64:67], v96 offset:0
ds_read_b128 v[68:71], v96 offset:1024
s_waitcnt lgkmcnt(0)
buffer_store_dwordx4 v[64:67], v91, s[44:47], s54 offen sc1
buffer_store_dwordx4 v[68:71], v92, s[44:47], s54 offen sc1
s_endpgm
LvR3:
LsR17:
s_cbranch_execz LsR21
s_add_u32 s49, s49, 313344
s_barrier
s_add_u32 m0, s50, 0
s_nop 0
D 83,0
D 83,1024
D 83,2048
D 83,3072
s_add_u32 s49, s49, 313344
s_barrier
s_add_u32 m0, s50, 32768
s_nop 0
D 83,0
D 83,1024
D 83,2048
D 83,3072
buffer_load_dwordx4 v[124:127], v90, s[36:39], s49 offen sc1
buffer_load_dwordx4 v[128:131], v90, s[36:39], s49 offen offset:1024 sc1
s_xor_b32 s50, s50, 0x10000
s_waitcnt vmcnt(6)
s_add_u32 s49, s49, 313344
s_barrier
R 32,0
buffer_load_dwordx4 v[132:135], v86, s[36:39], s49 offen sc1
R 36,1024
buffer_load_dwordx4 v[136:139], v86, s[36:39], s49 offen offset:1024 sc1
R 40,2048
buffer_load_dwordx4 v[140:143], v87, s[36:39], s49 offen sc1
s_waitcnt lgkmcnt(1)
v_mfma_f32_16x16x32_bf16 v[0:3], a[0:3], v[32:35], v[100:103]
R 44,3072
v_mfma_f32_16x16x32_bf16 v[4:7], a[4:7], v[32:35], v[104:107]
v_mfma_f32_16x16x32_bf16 v[8:11], a[8:11], v[32:35], v[108:111]
v_mfma_f32_16x16x32_bf16 v[12:15], a[12:15], v[32:35], v[112:115]
buffer_load_dwordx4 v[144:147], v87, s[36:39], s49 offen offset:1024 sc1
mav 0,16,36
R 48,4096
mav 4,20,36
mav 8,24,36
mav 12,28,36
s_waitcnt lgkmcnt(1)
buffer_load_dwordx4 v[148:151], v88, s[36:39], s49 offen sc1
mav 0,32,40
R 52,5120
mav 4,36,40
mav 8,40,40
mav 12,44,40
buffer_load_dwordx4 v[152:155], v88, s[36:39], s49 offen offset:1024 sc1
mav 0,48,44
R 56,6144
mav 4,52,44
mav 8,56,44
mav 12,60,44
s_waitcnt lgkmcnt(1)
buffer_load_dwordx4 v[156:159], v89, s[36:39], s49 offen sc1
mav 0,64,48
R 60,7168
mav 4,68,48
mav 8,72,48
mav 12,76,48
buffer_load_dwordx4 v[160:163], v89, s[36:39], s49 offen offset:1024 sc1
mav 0,80,52
R 32,8192
mav 4,84,52
mav 8,88,52
s_add_u32 m0, s50, 0
mav 12,92,52
s_waitcnt lgkmcnt(1)
mav 0,96,56
R 36,9216
mav 4,100,56
mav 8,104,56
mav 12,108,56
D 83,0
mav 0,112,60
R 40,10240
mav 4,116,60
mav 8,120,60
mav 12,124,60
s_waitcnt lgkmcnt(1)
D 83,1024
mav 0,128,32
R 44,11264
mav 4,132,32
mav 8,136,32
mav 12,140,32
mav 0,144,36
R 48,12288
mav 4,148,36
mav 8,152,36
mav 12,156,36
s_waitcnt lgkmcnt(1)
mav 0,160,40
R 52,13312
mav 4,164,40
mav 8,168,40
mav 12,172,40
mav 0,176,44
R 56,14336
mav 4,180,44
mav 8,184,44
mav 12,188,44
s_waitcnt lgkmcnt(1)
D 83,2048
mav 0,192,48
R 60,15360
mav 4,196,48
mav 8,200,48
mav 12,204,48
D 83,3072
mav 0,208,52
mav 4,212,52
mav 8,216,52
mav 12,220,52
s_waitcnt lgkmcnt(0)
mav 0,224,56
mav 4,228,56
mav 8,232,56
mav 12,236,56
mav 0,240,60
mav 4,244,60
mav 8,248,60
mav 12,252,60
s_waitcnt vmcnt(13)
buffer_load_dwordx4 v[164:167], v90, s[36:39], s49 offen sc1
mvv 0,116,124
mvv 4,120,124
s_waitcnt vmcnt(13)
mvv 8,116,128
mvv 12,120,128
s_waitcnt vmcnt(12)
buffer_load_dwordx4 v[168:171], v90, s[36:39], s49 offen offset:1024 sc1
mvv 0,116,132
mvv 4,120,132
s_waitcnt vmcnt(12)
mvv 8,116,136
s_waitcnt vmcnt(11)
s_add_u32 s49, s49, 313344
mvv 12,120,136
s_barrier
mvv 0,116,140
R 32,32768
mvv 4,120,140
s_waitcnt vmcnt(10)
mvv 8,116,144
mvv 12,120,144
s_waitcnt vmcnt(9)
buffer_load_dwordx4 v[172:175], v86, s[36:39], s49 offen sc1
mvv 0,116,148
R 36,33792
mvv 4,120,148
s_waitcnt vmcnt(9)
mvv 8,116,152
mvv 12,120,152
s_waitcnt vmcnt(8)
buffer_load_dwordx4 v[176:179], v86, s[36:39], s49 offen offset:1024 sc1
mvv 0,116,156
R 40,34816
mvv 4,120,156
s_waitcnt vmcnt(8)
mvv 8,116,160
mvv 12,120,160
s_waitcnt lgkmcnt(1)
buffer_load_dwordx4 v[180:183], v87, s[36:39], s49 offen sc1
v_mfma_f32_16x16x32_bf16 v[16:19], a[0:3], v[32:35], v[100:103]
R 44,35840
v_cvt_pk_bf16_f32 v64, v0, v1
v_mfma_f32_16x16x32_bf16 v[20:23], a[4:7], v[32:35], v[104:107]
v_cvt_pk_bf16_f32 v65, v2, v3
v_cvt_pk_bf16_f32 v66, v4, v5
v_mfma_f32_16x16x32_bf16 v[24:27], a[8:11], v[32:35], v[108:111]
v_cvt_pk_bf16_f32 v67, v6, v7
v_cvt_pk_bf16_f32 v68, v8, v9
v_mfma_f32_16x16x32_bf16 v[28:31], a[12:15], v[32:35], v[112:115]
buffer_load_dwordx4 v[184:187], v87, s[36:39], s49 offen offset:1024 sc1
mav 16,16,36
R 48,36864
v_cvt_pk_bf16_f32 v69, v10, v11
mav 20,20,36
v_cvt_pk_bf16_f32 v70, v12, v13
v_cvt_pk_bf16_f32 v71, v14, v15
mav 24,24,36
v_mul_f32 v75, v0, v0
v_add_f32 v74, v1, v0
mav 28,28,36
s_waitcnt lgkmcnt(1)
buffer_load_dwordx4 v[188:191], v88, s[36:39], s49 offen sc1
mav 16,32,40
R 52,37888
v_fmac_f32 v75, v1, v1
mav 20,36,40
v_add_f32 v74, v2, v74
v_fmac_f32 v75, v2, v2
mav 24,40,40
v_add_f32 v74, v3, v74
v_fmac_f32 v75, v3, v3
mav 28,44,40
buffer_load_dwordx4 v[192:195], v88, s[36:39], s49 offen offset:1024 sc1
mav 16,48,44
R 56,38912
v_add_f32 v74, v4, v74
mav 20,52,44
v_fmac_f32 v75, v4, v4
v_add_f32 v74, v5, v74
mav 24,56,44
v_fmac_f32 v75, v5, v5
v_add_f32 v74, v6, v74
mav 28,60,44
s_waitcnt lgkmcnt(1)
buffer_load_dwordx4 v[196:199], v89, s[36:39], s49 offen sc1
mav 16,64,48
R 60,39936
v_fmac_f32 v75, v6, v6
mav 20,68,48
v_add_f32 v74, v7, v74
v_fmac_f32 v75, v7, v7
mav 24,72,48
v_add_f32 v74, v8, v74
v_fmac_f32 v75, v8, v8
mav 28,76,48
buffer_load_dwordx4 v[200:203], v89, s[36:39], s49 offen offset:1024 sc1
mav 16,80,52
R 32,40960
v_add_f32 v74, v9, v74
mav 20,84,52
v_fmac_f32 v75, v9, v9
v_add_f32 v74, v10, v74
mav 24,88,52
s_add_u32 m0, s50, 32768
v_fmac_f32 v75, v10, v10
v_add_f32 v74, v11, v74
mav 28,92,52
s_waitcnt lgkmcnt(1)
v_fmac_f32 v75, v11, v11
v_add_f32 v74, v12, v74
mav 16,96,56
R 36,41984
v_fmac_f32 v75, v12, v12
mav 20,100,56
v_add_f32 v74, v13, v74
v_fmac_f32 v75, v13, v13
mav 24,104,56
v_add_f32 v74, v14, v74
v_fmac_f32 v75, v14, v14
mav 28,108,56
D 83,0
mav 16,112,60
R 40,43008
v_add_f32 v74, v15, v74
mav 20,116,60
v_fmac_f32 v75, v15, v15
mav 24,120,60
mav 28,124,60
s_waitcnt lgkmcnt(1)
D 83,1024
mav 16,128,32
R 44,44032
mav 20,132,32
ds_write_b128 v94, v[64:67] offset:0
mav 24,136,32
ds_write_b128 v95, v[68:71] offset:0
mav 28,140,32
ds_read_b128 v[64:67], v96 offset:0
mav 16,144,36
R 48,45056
mav 20,148,36
ds_read_b128 v[68:71], v96 offset:1024
mav 24,152,36
v_permlane32_swap_b32 v74, v75
mav 28,156,36
s_waitcnt lgkmcnt(4)
v_add_f32 v74, v74, v75
mav 16,160,40
mav 20,164,40
R 52,46080
mav 24,168,40
mav 28,172,40
mav 16,176,44
R 56,47104
mav 20,180,44
mav 24,184,44
mav 28,188,44
s_waitcnt lgkmcnt(1)
D 83,2048
mav 16,192,48
R 60,48128
mav 20,196,48
mav 24,200,48
mav 28,204,48
D 83,3072
mav 16,208,52
mav 20,212,52
mav 24,216,52
mav 28,220,52
buffer_store_dwordx4 v[64:67], v91, s[36:39], s54 offen sc1
s_waitcnt lgkmcnt(0)
mav 16,224,56
mav 20,228,56
mav 24,232,56
mav 28,236,56
buffer_store_dwordx4 v[68:71], v92, s[36:39], s54 offen sc1
mav 16,240,60
mav 20,244,60
mav 24,248,60
mav 28,252,60
s_waitcnt vmcnt(15)
buffer_load_dwordx4 v[124:127], v90, s[36:39], s49 offen sc1
mvv 16,116,164
mvv 20,120,164
s_waitcnt vmcnt(15)
mvv 24,116,168
s_xor_b32 s50, s50, 0x10000
s_sub_u32 s54, s54, 32768
mvv 28,120,168
s_waitcnt vmcnt(14)
buffer_load_dwordx4 v[128:131], v90, s[36:39], s49 offen offset:1024 sc1
mvv 16,116,172
mvv 20,120,172
s_waitcnt vmcnt(14)
v_xor_b32 v80, 0x10000, v80
mvv 24,116,176
s_waitcnt vmcnt(13)
s_add_u32 s49, s49, 313344
mvv 28,120,176
s_barrier
mvv 16,116,180
R 32,0
mvv 20,120,180
s_waitcnt vmcnt(12)
mvv 24,116,184
mvv 28,120,184
s_waitcnt vmcnt(11)
buffer_load_dwordx4 v[132:135], v86, s[36:39], s49 offen sc1
mvv 16,116,188
R 36,1024
mvv 20,120,188
s_waitcnt vmcnt(11)
mvv 24,116,192
mvv 28,120,192
s_waitcnt vmcnt(10)
buffer_load_dwordx4 v[136:139], v86, s[36:39], s49 offen offset:1024 sc1
mvv 16,116,196
R 40,2048
mvv 20,120,196
s_waitcnt vmcnt(10)
mvv 24,116,200
mvv 28,120,200
s_waitcnt lgkmcnt(1)
buffer_load_dwordx4 v[140:143], v87, s[36:39], s49 offen sc1
v_mfma_f32_16x16x32_bf16 v[0:3], a[0:3], v[32:35], v[100:103]
R 44,3072
v_cvt_pk_bf16_f32 v64, v16, v17
v_mfma_f32_16x16x32_bf16 v[4:7], a[4:7], v[32:35], v[104:107]
v_cvt_pk_bf16_f32 v65, v18, v19
v_cvt_pk_bf16_f32 v66, v20, v21
v_mfma_f32_16x16x32_bf16 v[8:11], a[8:11], v[32:35], v[108:111]
v_cvt_pk_bf16_f32 v67, v22, v23
v_cvt_pk_bf16_f32 v68, v24, v25
v_mfma_f32_16x16x32_bf16 v[12:15], a[12:15], v[32:35], v[112:115]
buffer_load_dwordx4 v[144:147], v87, s[36:39], s49 offen offset:1024 sc1
mav 0,16,36
R 48,4096
v_cvt_pk_bf16_f32 v69, v26, v27
mav 4,20,36
v_cvt_pk_bf16_f32 v70, v28, v29
v_cvt_pk_bf16_f32 v71, v30, v31
mav 8,24,36
v_mul_f32 v75, v16, v16
v_fmac_f32 v75, v17, v17
mav 12,28,36
s_waitcnt lgkmcnt(1)
buffer_load_dwordx4 v[148:151], v88, s[36:39], s49 offen sc1
mav 0,32,40
R 52,5120
v_fmac_f32 v75, v18, v18
mav 4,36,40
v_fmac_f32 v75, v19, v19
v_fmac_f32 v75, v20, v20
mav 8,40,40
v_fmac_f32 v75, v21, v21
v_fmac_f32 v75, v22, v22
mav 12,44,40
buffer_load_dwordx4 v[152:155], v88, s[36:39], s49 offen offset:1024 sc1
mav 0,48,44
R 56,6144
v_fmac_f32 v75, v23, v23
mav 4,52,44
v_fmac_f32 v75, v24, v24
v_fmac_f32 v75, v25, v25
mav 8,56,44
v_fmac_f32 v75, v26, v26
v_fmac_f32 v75, v27, v27
mav 12,60,44
s_waitcnt lgkmcnt(1)
buffer_load_dwordx4 v[156:159], v89, s[36:39], s49 offen sc1
mav 0,64,48
R 60,7168
v_fmac_f32 v75, v28, v28
mav 4,68,48
v_fmac_f32 v75, v29, v29
v_fmac_f32 v75, v30, v30
mav 8,72,48
v_fmac_f32 v75, v31, v31
mav 12,76,48
buffer_load_dwordx4 v[160:163], v89, s[36:39], s49 offen offset:1024 sc1
mav 0,80,52
R 32,8192
mav 4,84,52
mav 8,88,52
s_add_u32 m0, s50, 0
mav 12,92,52
s_waitcnt lgkmcnt(1)
mav 0,96,56
R 36,9216
mav 4,100,56
mav 8,104,56
mav 12,108,56
D 83,0
mav 0,112,60
R 40,10240
mav 4,116,60
mav 8,120,60
ds_write_b32 v97, v74 offset:0
mav 12,124,60
s_waitcnt lgkmcnt(2)
D 83,1024
mav 0,128,32
R 44,11264
v_add_f32 v74, v17, v16
mav 4,132,32
ds_write_b128 v94, v[64:67] offset:0
mav 8,136,32
ds_write_b128 v95, v[68:71] offset:0
mav 12,140,32
ds_read_b128 v[64:67], v96 offset:0
v_add_f32 v74, v18, v74
mav 0,144,36
R 48,12288
v_add_f32 v74, v19, v74
mav 4,148,36
ds_read_b128 v[68:71], v96 offset:1024
v_add_f32 v74, v20, v74
mav 8,152,36
v_add_f32 v74, v21, v74
v_add_f32 v74, v22, v74
mav 12,156,36
s_waitcnt lgkmcnt(4)
v_add_f32 v74, v23, v74
v_add_f32 v74, v24, v74
mav 0,160,40
R 52,13312
v_add_f32 v74, v25, v74
mav 4,164,40
v_add_f32 v74, v26, v74
v_add_f32 v74, v27, v74
mav 8,168,40
v_add_f32 v74, v28, v74
v_add_f32 v74, v29, v74
mav 12,172,40
v_add_f32 v74, v30, v74
v_add_f32 v74, v31, v74
mav 0,176,44
R 56,14336
v_add_u32 v72, 1, v72
mav 4,180,44
v_permlane32_swap_b32 v74, v75
mav 8,184,44
v_add_f32 v74, v74, v75
mav 12,188,44
s_waitcnt lgkmcnt(1)
D 83,2048
mav 0,192,48
R 60,15360
mav 4,196,48
mav 8,200,48
mav 12,204,48
D 83,3072
mav 0,208,52
mav 4,212,52
mav 8,216,52
mav 12,220,52
buffer_store_dwordx4 v[64:67], v91, s[36:39], s54 offen sc1
s_waitcnt lgkmcnt(0)
mav 0,224,56
mav 4,228,56
mav 8,232,56
mav 12,236,56
buffer_store_dwordx4 v[68:71], v92, s[36:39], s54 offen sc1
mav 0,240,60
mav 4,244,60
mav 8,248,60
mav 12,252,60
s_waitcnt vmcnt(15)
buffer_load_dwordx4 v[164:167], v90, s[36:39], s49 offen sc1
mvv 0,116,124
buffer_store_dword v72, v81, s[36:39], s52 offen sc1
mvv 4,120,124
s_waitcnt vmcnt(16)
mvv 8,116,128
s_sub_u32 s54, s54, 32768
mvv 12,120,128
s_waitcnt vmcnt(15)
buffer_load_dwordx4 v[168:171], v90, s[36:39], s49 offen offset:1024 sc1
mvv 0,116,132
mvv 4,120,132
s_waitcnt vmcnt(15)
mvv 8,116,136
s_waitcnt vmcnt(14)
s_add_u32 s49, s49, 313344
mvv 12,120,136
s_barrier
mvv 0,116,140
R 32,32768
mvv 4,120,140
LsR18:
s_cbranch_execz LwR
s_waitcnt vmcnt(13)
mvv 8,116,144
mvv 12,120,144
s_waitcnt vmcnt(12)
buffer_load_dwordx4 v[172:175], v86, s[36:39], s49 offen sc1
mvv 0,116,148
R 36,33792
mvv 4,120,148
s_waitcnt vmcnt(12)
mvv 8,116,152
mvv 12,120,152
s_waitcnt vmcnt(11)
buffer_load_dwordx4 v[176:179], v86, s[36:39], s49 offen offset:1024 sc1
mvv 0,116,156
R 40,34816
mvv 4,120,156
s_waitcnt vmcnt(11)
mvv 8,116,160
mvv 12,120,160
s_waitcnt lgkmcnt(1)
buffer_load_dwordx4 v[180:183], v87, s[36:39], s49 offen sc1
v_mfma_f32_16x16x32_bf16 v[16:19], a[0:3], v[32:35], v[100:103]
R 44,35840
v_cvt_pk_bf16_f32 v64, v0, v1
v_mfma_f32_16x16x32_bf16 v[20:23], a[4:7], v[32:35], v[104:107]
v_cvt_pk_bf16_f32 v65, v2, v3
v_cvt_pk_bf16_f32 v66, v4, v5
v_mfma_f32_16x16x32_bf16 v[24:27], a[8:11], v[32:35], v[108:111]
v_cvt_pk_bf16_f32 v67, v6, v7
v_cvt_pk_bf16_f32 v68, v8, v9
v_mfma_f32_16x16x32_bf16 v[28:31], a[12:15], v[32:35], v[112:115]
buffer_load_dwordx4 v[184:187], v87, s[36:39], s49 offen offset:1024 sc1
mav 16,16,36
R 48,36864
v_cvt_pk_bf16_f32 v69, v10, v11
mav 20,20,36
v_cvt_pk_bf16_f32 v70, v12, v13
v_cvt_pk_bf16_f32 v71, v14, v15
mav 24,24,36
v_mul_f32 v75, v0, v0
v_fmac_f32 v75, v1, v1
mav 28,28,36
s_waitcnt lgkmcnt(1)
buffer_load_dwordx4 v[188:191], v88, s[36:39], s49 offen sc1
mav 16,32,40
R 52,37888
v_fmac_f32 v75, v2, v2
mav 20,36,40
v_fmac_f32 v75, v3, v3
v_fmac_f32 v75, v4, v4
mav 24,40,40
v_fmac_f32 v75, v5, v5
v_fmac_f32 v75, v6, v6
mav 28,44,40
buffer_load_dwordx4 v[192:195], v88, s[36:39], s49 offen offset:1024 sc1
mav 16,48,44
R 56,38912
v_fmac_f32 v75, v7, v7
mav 20,52,44
v_fmac_f32 v75, v8, v8
v_fmac_f32 v75, v9, v9
mav 24,56,44
v_fmac_f32 v75, v10, v10
v_fmac_f32 v75, v11, v11
mav 28,60,44
s_waitcnt lgkmcnt(1)
buffer_load_dwordx4 v[196:199], v89, s[36:39], s49 offen sc1
mav 16,64,48
R 60,39936
v_fmac_f32 v75, v12, v12
mav 20,68,48
v_fmac_f32 v75, v13, v13
v_fmac_f32 v75, v14, v14
mav 24,72,48
v_fmac_f32 v75, v15, v15
mav 28,76,48
buffer_load_dwordx4 v[200:203], v89, s[36:39], s49 offen offset:1024 sc1
mav 16,80,52
R 32,40960
mav 20,84,52
mav 24,88,52
s_add_u32 m0, s50, 32768
mav 28,92,52
s_waitcnt lgkmcnt(1)
mav 16,96,56
R 36,41984
mav 20,100,56
mav 24,104,56
mav 28,108,56
D 83,0
mav 16,112,60
R 40,43008
mav 20,116,60
mav 24,120,60
ds_write_b32 v97, v74 offset:1024
mav 28,124,60
s_waitcnt lgkmcnt(2)
D 83,1024
mav 16,128,32
R 44,44032
v_add_f32 v74, v1, v0
mav 20,132,32
ds_write_b128 v94, v[64:67] offset:0
mav 24,136,32
ds_write_b128 v95, v[68:71] offset:0
mav 28,140,32
ds_read_b128 v[64:67], v96 offset:0
v_add_f32 v74, v2, v74
mav 16,144,36
R 48,45056
v_add_f32 v74, v3, v74
mav 20,148,36
ds_read_b128 v[68:71], v96 offset:1024
v_add_f32 v74, v4, v74
mav 24,152,36
v_add_f32 v74, v5, v74
v_add_f32 v74, v6, v74
mav 28,156,36
s_waitcnt lgkmcnt(4)
v_add_f32 v74, v7, v74
mav 16,160,40
s_mov_b32 s60, -14
.p2align 6
LR3_1:
mav 20,164,40
R 52,46080
v_add_f32 v74, v8, v74
mav 24,168,40
v_add_f32 v74, v9, v74
v_add_f32 v74, v10, v74
mav 28,172,40
v_add_f32 v74, v11, v74
v_add_f32 v74, v12, v74
mav 16,176,44
R 56,47104
v_add_f32 v74, v13, v74
mav 20,180,44
v_add_f32 v74, v14, v74
v_add_f32 v74, v15, v74
mav 24,184,44
v_add_u32 v72, 1, v72
mav 28,188,44
s_waitcnt lgkmcnt(1)
D 83,2048
mav 16,192,48
R 60,48128
mav 20,196,48
v_permlane32_swap_b32 v74, v75
mav 24,200,48
v_add_f32 v74, v74, v75
mav 28,204,48
D 83,3072
mav 16,208,52
mav 20,212,52
mav 24,216,52
mav 28,220,52
buffer_store_dwordx4 v[64:67], v91, s[36:39], s54 offen sc1
s_waitcnt lgkmcnt(0)
mav 16,224,56
mav 20,228,56
mav 24,232,56
mav 28,236,56
buffer_store_dwordx4 v[68:71], v92, s[36:39], s54 offen sc1
mav 16,240,60
mav 20,244,60
mav 24,248,60
mav 28,252,60
s_waitcnt vmcnt(16)
buffer_load_dwordx4 v[124:127], v90, s[36:39], s49 offen sc1
mvv 16,116,164
buffer_store_dword v72, v81, s[36:39], s52 offen sc1
mvv 20,120,164
s_waitcnt vmcnt(16)
mvv 24,116,168
s_xor_b32 s50, s50, 0x10000
s_sub_u32 s54, s54, 32768
mvv 28,120,168
s_waitcnt vmcnt(15)
buffer_load_dwordx4 v[128:131], v90, s[36:39], s49 offen offset:1024 sc1
mvv 16,116,172
mvv 20,120,172
s_waitcnt vmcnt(15)
v_xor_b32 v80, 0x10000, v80
mvv 24,116,176
s_waitcnt vmcnt(14)
s_add_u32 s49, s49, 313344
mvv 28,120,176
s_barrier
mvv 16,116,180
R 32,0
mvv 20,120,180
s_waitcnt vmcnt(13)
mvv 24,116,184
mvv 28,120,184
s_waitcnt vmcnt(12)
buffer_load_dwordx4 v[132:135], v86, s[36:39], s49 offen sc1
mvv 16,116,188
R 36,1024
mvv 20,120,188
s_waitcnt vmcnt(12)
mvv 24,116,192
mvv 28,120,192
s_waitcnt vmcnt(11)
buffer_load_dwordx4 v[136:139], v86, s[36:39], s49 offen offset:1024 sc1
mvv 16,116,196
R 40,2048
mvv 20,120,196
s_waitcnt vmcnt(11)
mvv 24,116,200
mvv 28,120,200
s_waitcnt lgkmcnt(1)
buffer_load_dwordx4 v[140:143], v87, s[36:39], s49 offen sc1
v_mfma_f32_16x16x32_bf16 v[0:3], a[0:3], v[32:35], v[100:103]
R 44,3072
v_cvt_pk_bf16_f32 v64, v16, v17
v_mfma_f32_16x16x32_bf16 v[4:7], a[4:7], v[32:35], v[104:107]
v_cvt_pk_bf16_f32 v65, v18, v19
v_cvt_pk_bf16_f32 v66, v20, v21
v_mfma_f32_16x16x32_bf16 v[8:11], a[8:11], v[32:35], v[108:111]
v_cvt_pk_bf16_f32 v67, v22, v23
v_cvt_pk_bf16_f32 v68, v24, v25
v_mfma_f32_16x16x32_bf16 v[12:15], a[12:15], v[32:35], v[112:115]
buffer_load_dwordx4 v[144:147], v87, s[36:39], s49 offen offset:1024 sc1
mav 0,16,36
R 48,4096
v_cvt_pk_bf16_f32 v69, v26, v27
mav 4,20,36
v_cvt_pk_bf16_f32 v70, v28, v29
v_cvt_pk_bf16_f32 v71, v30, v31
mav 8,24,36
v_mul_f32 v75, v16, v16
v_fmac_f32 v75, v17, v17
mav 12,28,36
s_waitcnt lgkmcnt(1)
buffer_load_dwordx4 v[148:151], v88, s[36:39], s49 offen sc1
mav 0,32,40
R 52,5120
v_fmac_f32 v75, v18, v18
mav 4,36,40
v_fmac_f32 v75, v19, v19
v_fmac_f32 v75, v20, v20
mav 8,40,40
v_fmac_f32 v75, v21, v21
v_fmac_f32 v75, v22, v22
mav 12,44,40
buffer_load_dwordx4 v[152:155], v88, s[36:39], s49 offen offset:1024 sc1
mav 0,48,44
R 56,6144
v_fmac_f32 v75, v23, v23
mav 4,52,44
v_fmac_f32 v75, v24, v24
v_fmac_f32 v75, v25, v25
mav 8,56,44
v_fmac_f32 v75, v26, v26
v_fmac_f32 v75, v27, v27
mav 12,60,44
s_waitcnt lgkmcnt(1)
buffer_load_dwordx4 v[156:159], v89, s[36:39], s49 offen sc1
mav 0,64,48
R 60,7168
v_fmac_f32 v75, v28, v28
mav 4,68,48
v_fmac_f32 v75, v29, v29
v_fmac_f32 v75, v30, v30
mav 8,72,48
v_fmac_f32 v75, v31, v31
mav 12,76,48
buffer_load_dwordx4 v[160:163], v89, s[36:39], s49 offen offset:1024 sc1
mav 0,80,52
R 32,8192
mav 4,84,52
mav 8,88,52
s_add_u32 m0, s50, 0
mav 12,92,52
s_waitcnt lgkmcnt(1)
mav 0,96,56
R 36,9216
mav 4,100,56
mav 8,104,56
mav 12,108,56
D 83,0
mav 0,112,60
R 40,10240
mav 4,116,60
mav 8,120,60
ds_write_b32 v97, v74 offset:0
mav 12,124,60
s_waitcnt lgkmcnt(2)
D 83,1024
mav 0,128,32
R 44,11264
v_add_f32 v74, v17, v16
mav 4,132,32
ds_write_b128 v94, v[64:67] offset:0
mav 8,136,32
ds_write_b128 v95, v[68:71] offset:0
mav 12,140,32
ds_read_b128 v[64:67], v96 offset:0
v_add_f32 v74, v18, v74
mav 0,144,36
R 48,12288
v_add_f32 v74, v19, v74
mav 4,148,36
ds_read_b128 v[68:71], v96 offset:1024
v_add_f32 v74, v20, v74
mav 8,152,36
v_add_f32 v74, v21, v74
v_add_f32 v74, v22, v74
mav 12,156,36
s_waitcnt lgkmcnt(4)
v_add_f32 v74, v23, v74
v_add_f32 v74, v24, v74
mav 0,160,40
R 52,13312
v_add_f32 v74, v25, v74
mav 4,164,40
v_add_f32 v74, v26, v74
v_add_f32 v74, v27, v74
mav 8,168,40
v_add_f32 v74, v28, v74
v_add_f32 v74, v29, v74
mav 12,172,40
v_add_f32 v74, v30, v74
v_add_f32 v74, v31, v74
mav 0,176,44
R 56,14336
v_add_u32 v72, 1, v72
mav 4,180,44
v_permlane32_swap_b32 v74, v75
mav 8,184,44
v_add_f32 v74, v74, v75
mav 12,188,44
s_waitcnt lgkmcnt(1)
D 83,2048
mav 0,192,48
R 60,15360
mav 4,196,48
mav 8,200,48
mav 12,204,48
D 83,3072
mav 0,208,52
mav 4,212,52
mav 8,216,52
mav 12,220,52
buffer_store_dwordx4 v[64:67], v91, s[36:39], s54 offen sc1
s_waitcnt lgkmcnt(0)
mav 0,224,56
mav 4,228,56
mav 8,232,56
mav 12,236,56
buffer_store_dwordx4 v[68:71], v92, s[36:39], s54 offen sc1
mav 0,240,60
mav 4,244,60
mav 8,248,60
mav 12,252,60
s_waitcnt vmcnt(16)
buffer_load_dwordx4 v[164:167], v90, s[36:39], s49 offen sc1
mvv 0,116,124
buffer_store_dword v72, v81, s[36:39], s52 offen sc1
mvv 4,120,124
s_waitcnt vmcnt(16)
mvv 8,116,128
s_sub_u32 s54, s54, 32768
mvv 12,120,128
s_waitcnt vmcnt(15)
buffer_load_dwordx4 v[168:171], v90, s[36:39], s49 offen offset:1024 sc1
mvv 0,116,132
mvv 4,120,132
s_waitcnt vmcnt(15)
mvv 8,116,136
s_waitcnt vmcnt(14)
s_add_u32 s49, s49, 313344
mvv 12,120,136
s_barrier
mvv 0,116,140
R 32,32768
mvv 4,120,140
s_waitcnt vmcnt(13)
mvv 8,116,144
mvv 12,120,144
s_waitcnt vmcnt(12)
buffer_load_dwordx4 v[172:175], v86, s[36:39], s49 offen sc1
mvv 0,116,148
R 36,33792
mvv 4,120,148
s_waitcnt vmcnt(12)
mvv 8,116,152
mvv 12,120,152
s_waitcnt vmcnt(11)
buffer_load_dwordx4 v[176:179], v86, s[36:39], s49 offen offset:1024 sc1
mvv 0,116,156
R 40,34816
mvv 4,120,156
s_waitcnt vmcnt(11)
mvv 8,116,160
mvv 12,120,160
s_waitcnt lgkmcnt(1)
buffer_load_dwordx4 v[180:183], v87, s[36:39], s49 offen sc1
v_mfma_f32_16x16x32_bf16 v[16:19], a[0:3], v[32:35], v[100:103]
R 44,35840
v_cvt_pk_bf16_f32 v64, v0, v1
v_mfma_f32_16x16x32_bf16 v[20:23], a[4:7], v[32:35], v[104:107]
v_cvt_pk_bf16_f32 v65, v2, v3
v_cvt_pk_bf16_f32 v66, v4, v5
v_mfma_f32_16x16x32_bf16 v[24:27], a[8:11], v[32:35], v[108:111]
v_cvt_pk_bf16_f32 v67, v6, v7
v_cvt_pk_bf16_f32 v68, v8, v9
v_mfma_f32_16x16x32_bf16 v[28:31], a[12:15], v[32:35], v[112:115]
buffer_load_dwordx4 v[184:187], v87, s[36:39], s49 offen offset:1024 sc1
mav 16,16,36
R 48,36864
v_cvt_pk_bf16_f32 v69, v10, v11
mav 20,20,36
v_cvt_pk_bf16_f32 v70, v12, v13
v_cvt_pk_bf16_f32 v71, v14, v15
mav 24,24,36
v_mul_f32 v75, v0, v0
v_fmac_f32 v75, v1, v1
mav 28,28,36
s_waitcnt lgkmcnt(1)
buffer_load_dwordx4 v[188:191], v88, s[36:39], s49 offen sc1
mav 16,32,40
R 52,37888
v_fmac_f32 v75, v2, v2
mav 20,36,40
v_fmac_f32 v75, v3, v3
v_fmac_f32 v75, v4, v4
mav 24,40,40
v_fmac_f32 v75, v5, v5
v_fmac_f32 v75, v6, v6
mav 28,44,40
buffer_load_dwordx4 v[192:195], v88, s[36:39], s49 offen offset:1024 sc1
mav 16,48,44
R 56,38912
v_fmac_f32 v75, v7, v7
mav 20,52,44
v_fmac_f32 v75, v8, v8
v_fmac_f32 v75, v9, v9
mav 24,56,44
v_fmac_f32 v75, v10, v10
v_fmac_f32 v75, v11, v11
mav 28,60,44
s_waitcnt lgkmcnt(1)
buffer_load_dwordx4 v[196:199], v89, s[36:39], s49 offen sc1
mav 16,64,48
R 60,39936
v_fmac_f32 v75, v12, v12
mav 20,68,48
v_fmac_f32 v75, v13, v13
v_fmac_f32 v75, v14, v14
mav 24,72,48
v_fmac_f32 v75, v15, v15
mav 28,76,48
buffer_load_dwordx4 v[200:203], v89, s[36:39], s49 offen offset:1024 sc1
mav 16,80,52
R 32,40960
mav 20,84,52
mav 24,88,52
s_add_u32 m0, s50, 32768
mav 28,92,52
s_waitcnt lgkmcnt(1)
mav 16,96,56
R 36,41984
mav 20,100,56
mav 24,104,56
mav 28,108,56
D 83,0
mav 16,112,60
R 40,43008
mav 20,116,60
mav 24,120,60
ds_write_b32 v97, v74 offset:1024
mav 28,124,60
s_waitcnt lgkmcnt(2)
D 83,1024
mav 16,128,32
R 44,44032
v_add_f32 v74, v1, v0
mav 20,132,32
ds_write_b128 v94, v[64:67] offset:0
mav 24,136,32
ds_write_b128 v95, v[68:71] offset:0
mav 28,140,32
ds_read_b128 v[64:67], v96 offset:0
v_add_f32 v74, v2, v74
mav 16,144,36
R 48,45056
v_add_f32 v74, v3, v74
mav 20,148,36
ds_read_b128 v[68:71], v96 offset:1024
v_add_f32 v74, v4, v74
mav 24,152,36
v_add_f32 v74, v5, v74
v_add_f32 v74, v6, v74
mav 28,156,36
s_waitcnt lgkmcnt(4)
s_add_u32 s60, s60, 1
v_add_f32 v74, v7, v74
mav 16,160,40
s_cbranch_scc0 LR3_1
mav 20,164,40
R 52,46080
v_add_f32 v74, v8, v74
mav 24,168,40
v_add_f32 v74, v9, v74
v_add_f32 v74, v10, v74
mav 28,172,40
v_add_f32 v74, v11, v74
v_add_f32 v74, v12, v74
mav 16,176,44
R 56,47104
v_add_f32 v74, v13, v74
mav 20,180,44
v_add_f32 v74, v14, v74
v_add_f32 v74, v15, v74
mav 24,184,44
v_add_u32 v72, 1, v72
mav 28,188,44
s_waitcnt lgkmcnt(1)
D 83,2048
mav 16,192,48
R 60,48128
mav 20,196,48
v_permlane32_swap_b32 v74, v75
mav 24,200,48
v_add_f32 v74, v74, v75
mav 28,204,48
D 83,3072
mav 16,208,52
mav 20,212,52
mav 24,216,52
mav 28,220,52
buffer_store_dwordx4 v[64:67], v91, s[36:39], s54 offen sc1
s_waitcnt lgkmcnt(0)
mav 16,224,56
mav 20,228,56
mav 24,232,56
mav 28,236,56
buffer_store_dwordx4 v[68:71], v92, s[36:39], s54 offen sc1
mav 16,240,60
mav 20,244,60
mav 24,248,60
mav 28,252,60
s_waitcnt vmcnt(16)
buffer_load_dwordx4 v[124:127], v90, s[36:39], s49 offen sc1
mvv 16,116,164
buffer_store_dword v72, v81, s[36:39], s52 offen sc1
mvv 20,120,164
s_waitcnt vmcnt(16)
mvv 24,116,168
s_xor_b32 s50, s50, 0x10000
s_sub_u32 s54, s54, 32768
mvv 28,120,168
s_waitcnt vmcnt(15)
buffer_load_dwordx4 v[128:131], v90, s[36:39], s49 offen offset:1024 sc1
mvv 16,116,172
mvv 20,120,172
s_waitcnt vmcnt(15)
v_xor_b32 v80, 0x10000, v80
mvv 24,116,176
s_waitcnt vmcnt(14)
s_add_u32 s49, s49, 313344
mvv 28,120,176
s_barrier
mvv 16,116,180
R 32,0
mvv 20,120,180
s_waitcnt vmcnt(13)
mvv 24,116,184
mvv 28,120,184
s_waitcnt vmcnt(12)
buffer_load_dwordx4 v[132:135], v86, s[36:39], s49 offen sc1
mvv 16,116,188
R 36,1024
mvv 20,120,188
s_waitcnt vmcnt(12)
mvv 24,116,192
mvv 28,120,192
s_waitcnt vmcnt(11)
buffer_load_dwordx4 v[136:139], v86, s[36:39], s49 offen offset:1024 sc1
mvv 16,116,196
R 40,2048
mvv 20,120,196
s_waitcnt vmcnt(11)
mvv 24,116,200
mvv 28,120,200
s_waitcnt lgkmcnt(1)
buffer_load_dwordx4 v[140:143], v87, s[36:39], s49 offen sc1
v_mfma_f32_16x16x32_bf16 v[0:3], a[0:3], v[32:35], v[100:103]
R 44,3072
v_cvt_pk_bf16_f32 v64, v16, v17
LsR19:
s_cbranch_execz LwR
v_mfma_f32_16x16x32_bf16 v[4:7], a[4:7], v[32:35], v[104:107]
v_cvt_pk_bf16_f32 v65, v18, v19
v_cvt_pk_bf16_f32 v66, v20, v21
v_mfma_f32_16x16x32_bf16 v[8:11], a[8:11], v[32:35], v[108:111]
v_cvt_pk_bf16_f32 v67, v22, v23
v_cvt_pk_bf16_f32 v68, v24, v25
v_mfma_f32_16x16x32_bf16 v[12:15], a[12:15], v[32:35], v[112:115]
buffer_load_dwordx4 v[144:147], v87, s[36:39], s49 offen offset:1024 sc1
mav 0,16,36
R 48,4096
v_cvt_pk_bf16_f32 v69, v26, v27
mav 4,20,36
v_cvt_pk_bf16_f32 v70, v28, v29
v_cvt_pk_bf16_f32 v71, v30, v31
mav 8,24,36
v_mul_f32 v75, v16, v16
v_fmac_f32 v75, v17, v17
mav 12,28,36
s_waitcnt lgkmcnt(1)
buffer_load_dwordx4 v[148:151], v88, s[36:39], s49 offen sc1
mav 0,32,40
R 52,5120
v_fmac_f32 v75, v18, v18
mav 4,36,40
v_fmac_f32 v75, v19, v19
v_fmac_f32 v75, v20, v20
mav 8,40,40
v_fmac_f32 v75, v21, v21
v_fmac_f32 v75, v22, v22
mav 12,44,40
buffer_load_dwordx4 v[152:155], v88, s[36:39], s49 offen offset:1024 sc1
mav 0,48,44
R 56,6144
v_fmac_f32 v75, v23, v23
mav 4,52,44
v_fmac_f32 v75, v24, v24
v_fmac_f32 v75, v25, v25
mav 8,56,44
v_fmac_f32 v75, v26, v26
v_fmac_f32 v75, v27, v27
mav 12,60,44
s_waitcnt lgkmcnt(1)
buffer_load_dwordx4 v[156:159], v89, s[36:39], s49 offen sc1
mav 0,64,48
R 60,7168
v_fmac_f32 v75, v28, v28
mav 4,68,48
v_fmac_f32 v75, v29, v29
v_fmac_f32 v75, v30, v30
mav 8,72,48
v_fmac_f32 v75, v31, v31
mav 12,76,48
buffer_load_dwordx4 v[160:163], v89, s[36:39], s49 offen offset:1024 sc1
mav 0,80,52
R 32,8192
mav 4,84,52
mav 8,88,52
s_add_u32 m0, s50, 0
mav 12,92,52
s_waitcnt lgkmcnt(1)
mav 0,96,56
R 36,9216
mav 4,100,56
mav 8,104,56
mav 12,108,56
D 83,0
mav 0,112,60
R 40,10240
mav 4,116,60
mav 8,120,60
ds_write_b32 v97, v74 offset:0
mav 12,124,60
s_waitcnt lgkmcnt(2)
D 83,1024
mav 0,128,32
R 44,11264
v_add_f32 v74, v17, v16
mav 4,132,32
ds_write_b128 v94, v[64:67] offset:0
mav 8,136,32
ds_write_b128 v95, v[68:71] offset:0
mav 12,140,32
ds_read_b128 v[64:67], v96 offset:0
v_add_f32 v74, v18, v74
mav 0,144,36
R 48,12288
v_add_f32 v74, v19, v74
mav 4,148,36
ds_read_b128 v[68:71], v96 offset:1024
v_add_f32 v74, v20, v74
mav 8,152,36
v_add_f32 v74, v21, v74
v_add_f32 v74, v22, v74
mav 12,156,36
s_waitcnt lgkmcnt(4)
v_add_f32 v74, v23, v74
v_add_f32 v74, v24, v74
mav 0,160,40
R 52,13312
v_add_f32 v74, v25, v74
mav 4,164,40
v_add_f32 v74, v26, v74
v_add_f32 v74, v27, v74
mav 8,168,40
v_add_f32 v74, v28, v74
v_add_f32 v74, v29, v74
mav 12,172,40
v_add_f32 v74, v30, v74
v_add_f32 v74, v31, v74
mav 0,176,44
R 56,14336
v_add_u32 v72, 1, v72
mav 4,180,44
v_permlane32_swap_b32 v74, v75
mav 8,184,44
v_add_f32 v74, v74, v75
mav 12,188,44
s_waitcnt lgkmcnt(1)
D 83,2048
mav 0,192,48
R 60,15360
mav 4,196,48
mav 8,200,48
mav 12,204,48
D 83,3072
mav 0,208,52
mav 4,212,52
mav 8,216,52
mav 12,220,52
buffer_store_dwordx4 v[64:67], v91, s[36:39], s54 offen sc1
s_waitcnt lgkmcnt(0)
mav 0,224,56
mav 4,228,56
mav 8,232,56
mav 12,236,56
buffer_store_dwordx4 v[68:71], v92, s[36:39], s54 offen sc1
mav 0,240,60
mav 4,244,60
mav 8,248,60
mav 12,252,60
s_waitcnt vmcnt(16)
buffer_load_dwordx4 v[164:167], v90, s[36:39], s49 offen sc1
mvv 0,116,124
buffer_store_dword v72, v81, s[36:39], s52 offen sc1
mvv 4,120,124
s_waitcnt vmcnt(16)
mvv 8,116,128
s_sub_u32 s54, s54, 3178496
mvv 12,120,128
s_waitcnt vmcnt(15)
buffer_load_dwordx4 v[168:171], v90, s[36:39], s49 offen offset:1024 sc1
mvv 0,116,132
mvv 4,120,132
s_waitcnt vmcnt(15)
mvv 8,116,136
s_waitcnt vmcnt(14)
s_add_u32 s49, s49, 313344
mvv 12,120,136
s_barrier
mvv 0,116,140
R 32,32768
mvv 4,120,140
s_waitcnt vmcnt(13)
mvv 8,116,144
mvv 12,120,144
s_waitcnt vmcnt(12)
buffer_load_dwordx4 v[172:175], v86, s[36:39], s49 offen sc1
mvv 0,116,148
R 36,33792
mvv 4,120,148
s_waitcnt vmcnt(12)
mvv 8,116,152
mvv 12,120,152
s_waitcnt vmcnt(11)
buffer_load_dwordx4 v[176:179], v86, s[36:39], s49 offen offset:1024 sc1
mvv 0,116,156
R 40,34816
mvv 4,120,156
s_waitcnt vmcnt(11)
mvv 8,116,160
mvv 12,120,160
s_waitcnt lgkmcnt(1)
buffer_load_dwordx4 v[180:183], v87, s[36:39], s49 offen sc1
v_mfma_f32_16x16x32_bf16 v[16:19], a[0:3], v[32:35], v[100:103]
R 44,35840
v_cvt_pk_bf16_f32 v64, v0, v1
v_mfma_f32_16x16x32_bf16 v[20:23], a[4:7], v[32:35], v[104:107]
v_cvt_pk_bf16_f32 v65, v2, v3
v_cvt_pk_bf16_f32 v66, v4, v5
v_mfma_f32_16x16x32_bf16 v[24:27], a[8:11], v[32:35], v[108:111]
v_cvt_pk_bf16_f32 v67, v6, v7
v_cvt_pk_bf16_f32 v68, v8, v9
v_mfma_f32_16x16x32_bf16 v[28:31], a[12:15], v[32:35], v[112:115]
buffer_load_dwordx4 v[184:187], v87, s[36:39], s49 offen offset:1024 sc1
mav 16,16,36
R 48,36864
v_cvt_pk_bf16_f32 v69, v10, v11
mav 20,20,36
v_cvt_pk_bf16_f32 v70, v12, v13
v_cvt_pk_bf16_f32 v71, v14, v15
mav 24,24,36
mav 28,28,36
s_waitcnt lgkmcnt(1)
buffer_load_dwordx4 v[188:191], v88, s[36:39], s49 offen sc1
mav 16,32,40
R 52,37888
mav 20,36,40
mav 24,40,40
mav 28,44,40
buffer_load_dwordx4 v[192:195], v88, s[36:39], s49 offen offset:1024 sc1
mav 16,48,44
R 56,38912
mav 20,52,44
mav 24,56,44
mav 28,60,44
s_waitcnt lgkmcnt(1)
buffer_load_dwordx4 v[196:199], v89, s[36:39], s49 offen sc1
mav 16,64,48
R 60,39936
mav 20,68,48
mav 24,72,48
mav 28,76,48
buffer_load_dwordx4 v[200:203], v89, s[36:39], s49 offen offset:1024 sc1
mav 16,80,52
R 32,40960
mav 20,84,52
mav 24,88,52
s_add_u32 m0, s50, 32768
mav 28,92,52
s_waitcnt lgkmcnt(1)
mav 16,96,56
R 36,41984
mav 20,100,56
mav 24,104,56
mav 28,108,56
D 83,0
mav 16,112,60
R 40,43008
mav 20,116,60
mav 24,120,60
ds_write_b32 v97, v74 offset:1024
mav 28,124,60
s_waitcnt lgkmcnt(2)
D 83,1024
mav 16,128,32
R 44,44032
mav 20,132,32
ds_write_b128 v94, v[64:67] offset:0
mav 24,136,32
ds_write_b128 v95, v[68:71] offset:0
mav 28,140,32
ds_read_b128 v[64:67], v96 offset:0
mav 16,144,36
R 48,45056
mav 20,148,36
ds_read_b128 v[68:71], v96 offset:1024
mav 24,152,36
mav 28,156,36
s_waitcnt lgkmcnt(4)
mav 16,160,40
mav 20,164,40
R 52,46080
v_add_u32 v72, 69, v72
mav 24,168,40
mav 28,172,40
mav 16,176,44
R 56,47104
mav 20,180,44
mav 24,184,44
mav 28,188,44
s_waitcnt lgkmcnt(1)
D 83,2048
mav 16,192,48
R 60,48128
mav 20,196,48
mav 24,200,48
mav 28,204,48
D 83,3072
mav 16,208,52
mav 20,212,52
mav 24,216,52
mav 28,220,52
buffer_store_dwordx4 v[64:67], v91, s[44:47], s54 offen sc1
s_waitcnt lgkmcnt(0)
mav 16,224,56
mav 20,228,56
mav 24,232,56
mav 28,236,56
buffer_store_dwordx4 v[68:71], v92, s[44:47], s54 offen sc1
mav 16,240,60
mav 20,244,60
mav 24,248,60
mav 28,252,60
s_waitcnt vmcnt(16)
buffer_load_dwordx4 v[124:127], v90, s[36:39], s49 offen sc1
mvv 16,116,164
buffer_store_dword v72, v81, s[36:39], s52 offen sc1
mvv 20,120,164
s_waitcnt vmcnt(16)
mvv 24,116,168
s_xor_b32 s50, s50, 0x10000
s_sub_u32 s54, s54, 32768
mvv 28,120,168
s_waitcnt vmcnt(15)
buffer_load_dwordx4 v[128:131], v90, s[36:39], s49 offen offset:1024 sc1
mvv 16,116,172
mvv 20,120,172
s_waitcnt vmcnt(15)
v_xor_b32 v80, 0x10000, v80
mvv 24,116,176
s_waitcnt vmcnt(14)
s_add_u32 s49, s49, 313344
mvv 28,120,176
s_barrier
mvv 16,116,180
R 32,0
mvv 20,120,180
s_waitcnt vmcnt(13)
mvv 24,116,184
mvv 28,120,184
s_waitcnt vmcnt(12)
buffer_load_dwordx4 v[132:135], v86, s[36:39], s49 offen sc1
mvv 16,116,188
R 36,1024
mvv 20,120,188
s_waitcnt vmcnt(12)
mvv 24,116,192
mvv 28,120,192
s_waitcnt vmcnt(11)
buffer_load_dwordx4 v[136:139], v86, s[36:39], s49 offen offset:1024 sc1
mvv 16,116,196
R 40,2048
mvv 20,120,196
s_waitcnt vmcnt(11)
mvv 24,116,200
mvv 28,120,200
s_waitcnt lgkmcnt(1)
buffer_load_dwordx4 v[140:143], v87, s[36:39], s49 offen sc1
v_mfma_f32_16x16x32_bf16 v[0:3], a[0:3], v[32:35], v[100:103]
R 44,3072
v_cvt_pk_bf16_f32 v64, v16, v17
v_mfma_f32_16x16x32_bf16 v[4:7], a[4:7], v[32:35], v[104:107]
v_cvt_pk_bf16_f32 v65, v18, v19
v_cvt_pk_bf16_f32 v66, v20, v21
v_mfma_f32_16x16x32_bf16 v[8:11], a[8:11], v[32:35], v[108:111]
v_cvt_pk_bf16_f32 v67, v22, v23
v_cvt_pk_bf16_f32 v68, v24, v25
v_mfma_f32_16x16x32_bf16 v[12:15], a[12:15], v[32:35], v[112:115]
buffer_load_dwordx4 v[144:147], v87, s[36:39], s49 offen offset:1024 sc1
mav 0,16,36
R 48,4096
v_cvt_pk_bf16_f32 v69, v26, v27
mav 4,20,36
v_cvt_pk_bf16_f32 v70, v28, v29
v_cvt_pk_bf16_f32 v71, v30, v31
mav 8,24,36
mav 12,28,36
s_waitcnt lgkmcnt(1)
buffer_load_dwordx4 v[148:151], v88, s[36:39], s49 offen sc1
mav 0,32,40
R 52,5120
mav 4,36,40
mav 8,40,40
mav 12,44,40
buffer_load_dwordx4 v[152:155], v88, s[36:39], s49 offen offset:1024 sc1
mav 0,48,44
R 56,6144
mav 4,52,44
mav 8,56,44
mav 12,60,44
s_waitcnt lgkmcnt(1)
buffer_load_dwordx4 v[156:159], v89, s[36:39], s49 offen sc1
mav 0,64,48
R 60,7168
mav 4,68,48
mav 8,72,48
mav 12,76,48
buffer_load_dwordx4 v[160:163], v89, s[36:39], s49 offen offset:1024 sc1
mav 0,80,52
R 32,8192
mav 4,84,52
mav 8,88,52
s_add_u32 m0, s50, 0
mav 12,92,52
s_waitcnt lgkmcnt(1)
mav 0,96,56
R 36,9216
mav 4,100,56
mav 8,104,56
mav 12,108,56
D 83,0
mav 0,112,60
R 40,10240
mav 4,116,60
mav 8,120,60
mav 12,124,60
s_waitcnt lgkmcnt(1)
D 83,1024
mav 0,128,32
R 44,11264
mav 4,132,32
ds_write_b128 v94, v[64:67] offset:0
mav 8,136,32
ds_write_b128 v95, v[68:71] offset:0
mav 12,140,32
ds_read_b128 v[64:67], v96 offset:0
mav 0,144,36
R 48,12288
mav 4,148,36
ds_read_b128 v[68:71], v96 offset:1024
mav 8,152,36
mav 12,156,36
s_waitcnt lgkmcnt(4)
mav 0,160,40
R 52,13312
mav 4,164,40
mav 8,168,40
mav 12,172,40
mav 0,176,44
R 56,14336
mav 4,180,44
mav 8,184,44
mav 12,188,44
s_waitcnt lgkmcnt(1)
D 83,2048
)"
R"(
mav 0,192,48
R 60,15360
mav 4,196,48
mav 8,200,48
mav 12,204,48
D 83,3072
mav 0,208,52
mav 4,212,52
mav 8,216,52
mav 12,220,52
buffer_store_dwordx4 v[64:67], v91, s[44:47], s54 offen sc1
s_waitcnt lgkmcnt(0)
mav 0,224,56
mav 4,228,56
mav 8,232,56
mav 12,236,56
buffer_store_dwordx4 v[68:71], v92, s[44:47], s54 offen sc1
mav 0,240,60
mav 4,244,60
mav 8,248,60
mav 12,252,60
s_waitcnt vmcnt(16)
buffer_load_dwordx4 v[164:167], v90, s[36:39], s49 offen sc1
mvv 0,116,124
mvv 4,120,124
s_waitcnt vmcnt(15)
mvv 8,116,128
s_sub_u32 s54, s54, 32768
mvv 12,120,128
s_waitcnt vmcnt(14)
buffer_load_dwordx4 v[168:171], v90, s[36:39], s49 offen offset:1024 sc1
mvv 0,116,132
mvv 4,120,132
s_waitcnt vmcnt(14)
mvv 8,116,136
s_waitcnt vmcnt(13)
s_add_u32 s49, s49, 313344
mvv 12,120,136
s_barrier
mvv 0,116,140
R 32,32768
mvv 4,120,140
s_waitcnt vmcnt(12)
mvv 8,116,144
mvv 12,120,144
s_waitcnt vmcnt(11)
buffer_load_dwordx4 v[172:175], v86, s[36:39], s49 offen sc1
mvv 0,116,148
R 36,33792
mvv 4,120,148
s_waitcnt vmcnt(11)
mvv 8,116,152
mvv 12,120,152
s_waitcnt vmcnt(10)
buffer_load_dwordx4 v[176:179], v86, s[36:39], s49 offen offset:1024 sc1
mvv 0,116,156
R 40,34816
mvv 4,120,156
s_waitcnt vmcnt(10)
mvv 8,116,160
mvv 12,120,160
s_waitcnt lgkmcnt(1)
buffer_load_dwordx4 v[180:183], v87, s[36:39], s49 offen sc1
v_mfma_f32_16x16x32_bf16 v[16:19], a[0:3], v[32:35], v[100:103]
R 44,35840
v_cvt_pk_bf16_f32 v64, v0, v1
v_mfma_f32_16x16x32_bf16 v[20:23], a[4:7], v[32:35], v[104:107]
v_cvt_pk_bf16_f32 v65, v2, v3
v_cvt_pk_bf16_f32 v66, v4, v5
v_mfma_f32_16x16x32_bf16 v[24:27], a[8:11], v[32:35], v[108:111]
v_cvt_pk_bf16_f32 v67, v6, v7
v_cvt_pk_bf16_f32 v68, v8, v9
v_mfma_f32_16x16x32_bf16 v[28:31], a[12:15], v[32:35], v[112:115]
buffer_load_dwordx4 v[184:187], v87, s[36:39], s49 offen offset:1024 sc1
mav 16,16,36
R 48,36864
v_cvt_pk_bf16_f32 v69, v10, v11
mav 20,20,36
v_cvt_pk_bf16_f32 v70, v12, v13
v_cvt_pk_bf16_f32 v71, v14, v15
mav 24,24,36
mav 28,28,36
s_waitcnt lgkmcnt(1)
buffer_load_dwordx4 v[188:191], v88, s[36:39], s49 offen sc1
mav 16,32,40
R 52,37888
mav 20,36,40
mav 24,40,40
mav 28,44,40
buffer_load_dwordx4 v[192:195], v88, s[36:39], s49 offen offset:1024 sc1
mav 16,48,44
R 56,38912
mav 20,52,44
mav 24,56,44
mav 28,60,44
s_waitcnt lgkmcnt(1)
buffer_load_dwordx4 v[196:199], v89, s[36:39], s49 offen sc1
mav 16,64,48
R 60,39936
mav 20,68,48
mav 24,72,48
mav 28,76,48
buffer_load_dwordx4 v[200:203], v89, s[36:39], s49 offen offset:1024 sc1
mav 16,80,52
R 32,40960
mav 20,84,52
mav 24,88,52
s_add_u32 m0, s50, 32768
mav 28,92,52
s_waitcnt lgkmcnt(1)
mav 16,96,56
R 36,41984
mav 20,100,56
mav 24,104,56
mav 28,108,56
D 83,0
mav 16,112,60
R 40,43008
mav 20,116,60
mav 24,120,60
mav 28,124,60
s_waitcnt lgkmcnt(1)
D 83,1024
mav 16,128,32
R 44,44032
mav 20,132,32
ds_write_b128 v94, v[64:67] offset:0
mav 24,136,32
ds_write_b128 v95, v[68:71] offset:0
mav 28,140,32
ds_read_b128 v[64:67], v96 offset:0
mav 16,144,36
R 48,45056
mav 20,148,36
ds_read_b128 v[68:71], v96 offset:1024
LsR20:
s_cbranch_execz LwR
mav 24,152,36
mav 28,156,36
s_waitcnt lgkmcnt(4)
mav 16,160,40
s_mov_b32 s60, -13
.p2align 6
LR3_2:
mav 20,164,40
R 52,46080
mav 24,168,40
mav 28,172,40
mav 16,176,44
R 56,47104
mav 20,180,44
mav 24,184,44
mav 28,188,44
s_waitcnt lgkmcnt(1)
D 83,2048
mav 16,192,48
R 60,48128
mav 20,196,48
mav 24,200,48
mav 28,204,48
D 83,3072
mav 16,208,52
mav 20,212,52
mav 24,216,52
mav 28,220,52
buffer_store_dwordx4 v[64:67], v91, s[44:47], s54 offen sc1
s_waitcnt lgkmcnt(0)
mav 16,224,56
mav 20,228,56
mav 24,232,56
mav 28,236,56
buffer_store_dwordx4 v[68:71], v92, s[44:47], s54 offen sc1
mav 16,240,60
mav 20,244,60
mav 24,248,60
mav 28,252,60
s_waitcnt vmcnt(15)
buffer_load_dwordx4 v[124:127], v90, s[36:39], s49 offen sc1
mvv 16,116,164
mvv 20,120,164
s_waitcnt vmcnt(15)
mvv 24,116,168
s_xor_b32 s50, s50, 0x10000
s_sub_u32 s54, s54, 32768
mvv 28,120,168
s_waitcnt vmcnt(14)
buffer_load_dwordx4 v[128:131], v90, s[36:39], s49 offen offset:1024 sc1
mvv 16,116,172
mvv 20,120,172
s_waitcnt vmcnt(14)
v_xor_b32 v80, 0x10000, v80
mvv 24,116,176
s_waitcnt vmcnt(13)
s_add_u32 s49, s49, 313344
mvv 28,120,176
s_barrier
mvv 16,116,180
R 32,0
mvv 20,120,180
s_waitcnt vmcnt(12)
mvv 24,116,184
mvv 28,120,184
s_waitcnt vmcnt(11)
buffer_load_dwordx4 v[132:135], v86, s[36:39], s49 offen sc1
mvv 16,116,188
R 36,1024
mvv 20,120,188
s_waitcnt vmcnt(11)
mvv 24,116,192
mvv 28,120,192
s_waitcnt vmcnt(10)
buffer_load_dwordx4 v[136:139], v86, s[36:39], s49 offen offset:1024 sc1
mvv 16,116,196
R 40,2048
mvv 20,120,196
s_waitcnt vmcnt(10)
mvv 24,116,200
mvv 28,120,200
s_waitcnt lgkmcnt(1)
buffer_load_dwordx4 v[140:143], v87, s[36:39], s49 offen sc1
v_mfma_f32_16x16x32_bf16 v[0:3], a[0:3], v[32:35], v[100:103]
R 44,3072
v_cvt_pk_bf16_f32 v64, v16, v17
v_mfma_f32_16x16x32_bf16 v[4:7], a[4:7], v[32:35], v[104:107]
v_cvt_pk_bf16_f32 v65, v18, v19
v_cvt_pk_bf16_f32 v66, v20, v21
v_mfma_f32_16x16x32_bf16 v[8:11], a[8:11], v[32:35], v[108:111]
v_cvt_pk_bf16_f32 v67, v22, v23
v_cvt_pk_bf16_f32 v68, v24, v25
v_mfma_f32_16x16x32_bf16 v[12:15], a[12:15], v[32:35], v[112:115]
buffer_load_dwordx4 v[144:147], v87, s[36:39], s49 offen offset:1024 sc1
mav 0,16,36
R 48,4096
v_cvt_pk_bf16_f32 v69, v26, v27
mav 4,20,36
v_cvt_pk_bf16_f32 v70, v28, v29
v_cvt_pk_bf16_f32 v71, v30, v31
mav 8,24,36
mav 12,28,36
s_waitcnt lgkmcnt(1)
buffer_load_dwordx4 v[148:151], v88, s[36:39], s49 offen sc1
mav 0,32,40
R 52,5120
mav 4,36,40
mav 8,40,40
mav 12,44,40
buffer_load_dwordx4 v[152:155], v88, s[36:39], s49 offen offset:1024 sc1
mav 0,48,44
R 56,6144
mav 4,52,44
mav 8,56,44
mav 12,60,44
s_waitcnt lgkmcnt(1)
buffer_load_dwordx4 v[156:159], v89, s[36:39], s49 offen sc1
mav 0,64,48
R 60,7168
mav 4,68,48
mav 8,72,48
mav 12,76,48
buffer_load_dwordx4 v[160:163], v89, s[36:39], s49 offen offset:1024 sc1
mav 0,80,52
R 32,8192
mav 4,84,52
mav 8,88,52
s_add_u32 m0, s50, 0
mav 12,92,52
s_waitcnt lgkmcnt(1)
mav 0,96,56
R 36,9216
mav 4,100,56
mav 8,104,56
mav 12,108,56
D 83,0
mav 0,112,60
R 40,10240
mav 4,116,60
mav 8,120,60
mav 12,124,60
s_waitcnt lgkmcnt(1)
D 83,1024
mav 0,128,32
R 44,11264
mav 4,132,32
ds_write_b128 v94, v[64:67] offset:0
mav 8,136,32
ds_write_b128 v95, v[68:71] offset:0
mav 12,140,32
ds_read_b128 v[64:67], v96 offset:0
mav 0,144,36
R 48,12288
mav 4,148,36
ds_read_b128 v[68:71], v96 offset:1024
mav 8,152,36
mav 12,156,36
s_waitcnt lgkmcnt(4)
mav 0,160,40
R 52,13312
mav 4,164,40
mav 8,168,40
mav 12,172,40
mav 0,176,44
R 56,14336
mav 4,180,44
mav 8,184,44
mav 12,188,44
s_waitcnt lgkmcnt(1)
D 83,2048
mav 0,192,48
R 60,15360
mav 4,196,48
mav 8,200,48
mav 12,204,48
D 83,3072
mav 0,208,52
mav 4,212,52
mav 8,216,52
mav 12,220,52
buffer_store_dwordx4 v[64:67], v91, s[44:47], s54 offen sc1
s_waitcnt lgkmcnt(0)
mav 0,224,56
mav 4,228,56
mav 8,232,56
mav 12,236,56
buffer_store_dwordx4 v[68:71], v92, s[44:47], s54 offen sc1
mav 0,240,60
mav 4,244,60
mav 8,248,60
mav 12,252,60
s_waitcnt vmcnt(15)
buffer_load_dwordx4 v[164:167], v90, s[36:39], s49 offen sc1
mvv 0,116,124
mvv 4,120,124
s_waitcnt vmcnt(15)
mvv 8,116,128
s_sub_u32 s54, s54, 32768
mvv 12,120,128
s_waitcnt vmcnt(14)
buffer_load_dwordx4 v[168:171], v90, s[36:39], s49 offen offset:1024 sc1
mvv 0,116,132
mvv 4,120,132
s_waitcnt vmcnt(14)
mvv 8,116,136
s_waitcnt vmcnt(13)
s_add_u32 s49, s49, 313344
mvv 12,120,136
s_barrier
mvv 0,116,140
R 32,32768
mvv 4,120,140
s_waitcnt vmcnt(12)
mvv 8,116,144
mvv 12,120,144
s_waitcnt vmcnt(11)
buffer_load_dwordx4 v[172:175], v86, s[36:39], s49 offen sc1
mvv 0,116,148
R 36,33792
mvv 4,120,148
s_waitcnt vmcnt(11)
mvv 8,116,152
mvv 12,120,152
s_waitcnt vmcnt(10)
buffer_load_dwordx4 v[176:179], v86, s[36:39], s49 offen offset:1024 sc1
mvv 0,116,156
R 40,34816
mvv 4,120,156
s_waitcnt vmcnt(10)
mvv 8,116,160
mvv 12,120,160
s_waitcnt lgkmcnt(1)
buffer_load_dwordx4 v[180:183], v87, s[36:39], s49 offen sc1
v_mfma_f32_16x16x32_bf16 v[16:19], a[0:3], v[32:35], v[100:103]
R 44,35840
v_cvt_pk_bf16_f32 v64, v0, v1
v_mfma_f32_16x16x32_bf16 v[20:23], a[4:7], v[32:35], v[104:107]
v_cvt_pk_bf16_f32 v65, v2, v3
v_cvt_pk_bf16_f32 v66, v4, v5
v_mfma_f32_16x16x32_bf16 v[24:27], a[8:11], v[32:35], v[108:111]
v_cvt_pk_bf16_f32 v67, v6, v7
v_cvt_pk_bf16_f32 v68, v8, v9
v_mfma_f32_16x16x32_bf16 v[28:31], a[12:15], v[32:35], v[112:115]
buffer_load_dwordx4 v[184:187], v87, s[36:39], s49 offen offset:1024 sc1
mav 16,16,36
R 48,36864
v_cvt_pk_bf16_f32 v69, v10, v11
mav 20,20,36
v_cvt_pk_bf16_f32 v70, v12, v13
v_cvt_pk_bf16_f32 v71, v14, v15
mav 24,24,36
mav 28,28,36
s_waitcnt lgkmcnt(1)
buffer_load_dwordx4 v[188:191], v88, s[36:39], s49 offen sc1
mav 16,32,40
R 52,37888
mav 20,36,40
mav 24,40,40
mav 28,44,40
buffer_load_dwordx4 v[192:195], v88, s[36:39], s49 offen offset:1024 sc1
mav 16,48,44
R 56,38912
mav 20,52,44
mav 24,56,44
mav 28,60,44
s_waitcnt lgkmcnt(1)
buffer_load_dwordx4 v[196:199], v89, s[36:39], s49 offen sc1
mav 16,64,48
R 60,39936
mav 20,68,48
mav 24,72,48
mav 28,76,48
buffer_load_dwordx4 v[200:203], v89, s[36:39], s49 offen offset:1024 sc1
mav 16,80,52
R 32,40960
mav 20,84,52
mav 24,88,52
s_add_u32 m0, s50, 32768
mav 28,92,52
s_waitcnt lgkmcnt(1)
mav 16,96,56
R 36,41984
mav 20,100,56
mav 24,104,56
mav 28,108,56
D 83,0
mav 16,112,60
R 40,43008
mav 20,116,60
mav 24,120,60
mav 28,124,60
s_waitcnt lgkmcnt(1)
D 83,1024
mav 16,128,32
R 44,44032
mav 20,132,32
ds_write_b128 v94, v[64:67] offset:0
mav 24,136,32
ds_write_b128 v95, v[68:71] offset:0
mav 28,140,32
ds_read_b128 v[64:67], v96 offset:0
mav 16,144,36
R 48,45056
mav 20,148,36
ds_read_b128 v[68:71], v96 offset:1024
mav 24,152,36
mav 28,156,36
s_waitcnt lgkmcnt(4)
s_add_u32 s60, s60, 1
mav 16,160,40
s_cbranch_scc0 LR3_2
mav 20,164,40
R 52,46080
mav 24,168,40
mav 28,172,40
mav 16,176,44
R 56,47104
mav 20,180,44
mav 24,184,44
mav 28,188,44
s_waitcnt lgkmcnt(1)
D 83,2048
mav 16,192,48
R 60,48128
mav 20,196,48
mav 24,200,48
mav 28,204,48
D 83,3072
mav 16,208,52
mav 20,212,52
mav 24,216,52
mav 28,220,52
buffer_store_dwordx4 v[64:67], v91, s[44:47], s54 offen sc1
s_waitcnt lgkmcnt(0)
mav 16,224,56
mav 20,228,56
mav 24,232,56
mav 28,236,56
buffer_store_dwordx4 v[68:71], v92, s[44:47], s54 offen sc1
mav 16,240,60
mav 20,244,60
mav 24,248,60
mav 28,252,60
s_waitcnt vmcnt(15)
buffer_load_dwordx4 v[124:127], v90, s[36:39], s49 offen sc1
mvv 16,116,164
mvv 20,120,164
s_waitcnt vmcnt(15)
mvv 24,116,168
s_sub_u32 s54, s54, 32768
mvv 28,120,168
s_waitcnt vmcnt(14)
buffer_load_dwordx4 v[128:131], v90, s[36:39], s49 offen offset:1024 sc1
mvv 16,116,172
mvv 20,120,172
s_waitcnt vmcnt(14)
v_xor_b32 v80, 0x10000, v80
mvv 24,116,176
s_waitcnt vmcnt(13)
s_add_u32 s49, s49, 313344
mvv 28,120,176
s_barrier
mvv 16,116,180
R 32,0
mvv 20,120,180
s_waitcnt vmcnt(12)
mvv 24,116,184
mvv 28,120,184
s_waitcnt vmcnt(11)
buffer_load_dwordx4 v[132:135], v86, s[36:39], s49 offen sc1
mvv 16,116,188
R 36,1024
mvv 20,120,188
s_waitcnt vmcnt(11)
mvv 24,116,192
mvv 28,120,192
s_waitcnt vmcnt(10)
buffer_load_dwordx4 v[136:139], v86, s[36:39], s49 offen offset:1024 sc1
mvv 16,116,196
R 40,2048
mvv 20,120,196
s_waitcnt vmcnt(10)
mvv 24,116,200
mvv 28,120,200
s_waitcnt lgkmcnt(1)
buffer_load_dwordx4 v[140:143], v87, s[36:39], s49 offen sc1
v_mfma_f32_16x16x32_bf16 v[0:3], a[0:3], v[32:35], v[100:103]
R 44,3072
v_cvt_pk_bf16_f32 v64, v16, v17
v_mfma_f32_16x16x32_bf16 v[4:7], a[4:7], v[32:35], v[104:107]
v_cvt_pk_bf16_f32 v65, v18, v19
v_cvt_pk_bf16_f32 v66, v20, v21
v_mfma_f32_16x16x32_bf16 v[8:11], a[8:11], v[32:35], v[108:111]
v_cvt_pk_bf16_f32 v67, v22, v23
v_cvt_pk_bf16_f32 v68, v24, v25
v_mfma_f32_16x16x32_bf16 v[12:15], a[12:15], v[32:35], v[112:115]
buffer_load_dwordx4 v[144:147], v87, s[36:39], s49 offen offset:1024 sc1
mav 0,16,36
R 48,4096
v_cvt_pk_bf16_f32 v69, v26, v27
mav 4,20,36
v_cvt_pk_bf16_f32 v70, v28, v29
v_cvt_pk_bf16_f32 v71, v30, v31
mav 8,24,36
mav 12,28,36
s_waitcnt lgkmcnt(1)
buffer_load_dwordx4 v[148:151], v88, s[36:39], s49 offen sc1
mav 0,32,40
R 52,5120
mav 4,36,40
mav 8,40,40
mav 12,44,40
buffer_load_dwordx4 v[152:155], v88, s[36:39], s49 offen offset:1024 sc1
mav 0,48,44
R 56,6144
mav 4,52,44
mav 8,56,44
mav 12,60,44
s_waitcnt lgkmcnt(1)
buffer_load_dwordx4 v[156:159], v89, s[36:39], s49 offen sc1
mav 0,64,48
R 60,7168
mav 4,68,48
mav 8,72,48
mav 12,76,48
buffer_load_dwordx4 v[160:163], v89, s[36:39], s49 offen offset:1024 sc1
mav 0,80,52
R 32,8192
mav 4,84,52
mav 8,88,52
mav 12,92,52
s_waitcnt lgkmcnt(1)
mav 0,96,56
R 36,9216
mav 4,100,56
mav 8,104,56
mav 12,108,56
mav 0,112,60
R 40,10240
mav 4,116,60
mav 8,120,60
mav 12,124,60
s_waitcnt lgkmcnt(1)
ds_write_b128 v94, v[64:67] offset:0
mav 0,128,32
R 44,11264
mav 4,132,32
ds_write_b128 v95, v[68:71] offset:0
mav 8,136,32
ds_read_b128 v[64:67], v96 offset:0
mav 12,140,32
ds_read_b128 v[68:71], v96 offset:1024
mav 0,144,36
R 48,12288
mav 4,148,36
mav 8,152,36
mav 12,156,36
s_waitcnt lgkmcnt(3)
mav 0,160,40
R 52,13312
mav 4,164,40
mav 8,168,40
mav 12,172,40
mav 0,176,44
R 56,14336
mav 4,180,44
mav 8,184,44
mav 12,188,44
s_waitcnt lgkmcnt(1)
mav 0,192,48
R 60,15360
mav 4,196,48
mav 8,200,48
mav 12,204,48
mav 0,208,52
mav 4,212,52
mav 8,216,52
mav 12,220,52
buffer_store_dwordx4 v[64:67], v91, s[44:47], s54 offen sc1
s_waitcnt lgkmcnt(0)
mav 0,224,56
mav 4,228,56
mav 8,232,56
mav 12,236,56
buffer_store_dwordx4 v[68:71], v92, s[44:47], s54 offen sc1
mav 0,240,60
mav 4,244,60
mav 8,248,60
mav 12,252,60
s_waitcnt vmcnt(11)
buffer_load_dwordx4 v[164:167], v90, s[36:39], s49 offen sc1
mvv 0,116,124
mvv 4,120,124
s_waitcnt vmcnt(11)
mvv 8,116,128
s_sub_u32 s54, s54, 32768
mvv 12,120,128
s_waitcnt vmcnt(10)
buffer_load_dwordx4 v[168:171], v90, s[36:39], s49 offen offset:1024 sc1
mvv 0,116,132
mvv 4,120,132
s_waitcnt vmcnt(10)
mvv 8,116,136
s_waitcnt vmcnt(9)
s_add_u32 s49, s49, 313344
mvv 12,120,136
s_barrier
mvv 0,116,140
R 32,32768
mvv 4,120,140
s_waitcnt vmcnt(8)
mvv 8,116,144
mvv 12,120,144
s_waitcnt vmcnt(7)
buffer_load_dwordx4 v[172:175], v86, s[36:39], s49 offen sc1
mvv 0,116,148
R 36,33792
mvv 4,120,148
s_waitcnt vmcnt(7)
mvv 8,116,152
mvv 12,120,152
s_waitcnt vmcnt(6)
buffer_load_dwordx4 v[176:179], v86, s[36:39], s49 offen offset:1024 sc1
mvv 0,116,156
R 40,34816
mvv 4,120,156
s_waitcnt vmcnt(6)
mvv 8,116,160
mvv 12,120,160
s_waitcnt lgkmcnt(1)
buffer_load_dwordx4 v[180:183], v87, s[36:39], s49 offen sc1
v_mfma_f32_16x16x32_bf16 v[16:19], a[0:3], v[32:35], v[100:103]
R 44,35840
v_cvt_pk_bf16_f32 v64, v0, v1
v_mfma_f32_16x16x32_bf16 v[20:23], a[4:7], v[32:35], v[104:107]
v_cvt_pk_bf16_f32 v65, v2, v3
v_cvt_pk_bf16_f32 v66, v4, v5
v_mfma_f32_16x16x32_bf16 v[24:27], a[8:11], v[32:35], v[108:111]
v_cvt_pk_bf16_f32 v67, v6, v7
v_cvt_pk_bf16_f32 v68, v8, v9
v_mfma_f32_16x16x32_bf16 v[28:31], a[12:15], v[32:35], v[112:115]
buffer_load_dwordx4 v[184:187], v87, s[36:39], s49 offen offset:1024 sc1
mav 16,16,36
R 48,36864
v_cvt_pk_bf16_f32 v69, v10, v11
mav 20,20,36
ds_write_b128 v94, v[64:67] offset:0
mav 24,24,36
LsR21:
s_cbranch_execz LwR
v_cvt_pk_bf16_f32 v70, v12, v13
v_cvt_pk_bf16_f32 v71, v14, v15
mav 28,28,36
s_waitcnt lgkmcnt(2)
buffer_load_dwordx4 v[188:191], v88, s[36:39], s49 offen sc1
mav 16,32,40
R 52,37888
mav 20,36,40
ds_write_b128 v95, v[68:71] offset:0
mav 24,40,40
ds_read_b128 v[64:67], v96 offset:0
mav 28,44,40
buffer_load_dwordx4 v[192:195], v88, s[36:39], s49 offen offset:1024 sc1
mav 16,48,44
R 56,38912
mav 20,52,44
ds_read_b128 v[68:71], v96 offset:1024
mav 24,56,44
mav 28,60,44
s_waitcnt lgkmcnt(3)
buffer_load_dwordx4 v[196:199], v89, s[36:39], s49 offen sc1
mav 16,64,48
R 60,39936
mav 20,68,48
s_waitcnt lgkmcnt(3)
mav 24,72,48
mav 28,76,48
buffer_store_dwordx4 v[64:67], v91, s[44:47], s54 offen sc1
buffer_load_dwordx4 v[200:203], v89, s[36:39], s49 offen offset:1024 sc1
mav 16,80,52
s_waitcnt lgkmcnt(1)
R 32,40960
mav 20,84,52
mav 24,88,52
mav 28,92,52
buffer_store_dwordx4 v[68:71], v92, s[44:47], s54 offen sc1
mav 16,96,56
R 36,41984
mav 20,100,56
mav 24,104,56
mav 28,108,56
s_waitcnt lgkmcnt(1)
mav 16,112,60
R 40,43008
mav 20,116,60
mav 24,120,60
mav 28,124,60
mav 16,128,32
R 44,44032
mav 20,132,32
mav 24,136,32
mav 28,140,32
s_waitcnt lgkmcnt(1)
mav 16,144,36
R 48,45056
mav 20,148,36
mav 24,152,36
mav 28,156,36
mav 16,160,40
mav 20,164,40
R 52,46080
mav 24,168,40
mav 28,172,40
s_waitcnt lgkmcnt(1)
mav 16,176,44
R 56,47104
mav 20,180,44
mav 24,184,44
mav 28,188,44
mav 16,192,48
R 60,48128
mav 20,196,48
mav 24,200,48
mav 28,204,48
s_waitcnt lgkmcnt(1)
mav 16,208,52
mav 20,212,52
mav 24,216,52
mav 28,220,52
mav 16,224,56
mav 20,228,56
mav 24,232,56
mav 28,236,56
s_waitcnt lgkmcnt(0)
mav 16,240,60
mav 20,244,60
mav 24,248,60
mav 28,252,60
s_waitcnt vmcnt(11)
mvv 16,116,164
mvv 20,120,164
s_waitcnt vmcnt(10)
mvv 24,116,168
s_sub_u32 s54, s54, 32768
mvv 28,120,168
s_waitcnt vmcnt(9)
mvv 16,116,172
mvv 20,120,172
s_waitcnt vmcnt(8)
mvv 24,116,176
mvv 28,120,176
s_waitcnt vmcnt(7)
mvv 16,116,180
mvv 20,120,180
s_waitcnt vmcnt(6)
mvv 24,116,184
mvv 28,120,184
s_waitcnt vmcnt(5)
mvv 16,116,188
mvv 20,120,188
s_waitcnt vmcnt(4)
mvv 24,116,192
mvv 28,120,192
s_waitcnt vmcnt(3)
mvv 16,116,196
mvv 20,120,196
s_waitcnt vmcnt(1)
mvv 24,116,200
mvv 28,120,200
s_nop 3
v_cvt_pk_bf16_f32 v64, v16, v17
v_cvt_pk_bf16_f32 v65, v18, v19
v_cvt_pk_bf16_f32 v66, v20, v21
v_cvt_pk_bf16_f32 v67, v22, v23
ds_write_b128 v94, v[64:67] offset:0
v_cvt_pk_bf16_f32 v68, v24, v25
v_cvt_pk_bf16_f32 v69, v26, v27
v_cvt_pk_bf16_f32 v70, v28, v29
v_cvt_pk_bf16_f32 v71, v30, v31
ds_write_b128 v95, v[68:71] offset:0
ds_read_b128 v[64:67], v96 offset:0
ds_read_b128 v[68:71], v96 offset:1024
s_waitcnt lgkmcnt(0)
buffer_store_dwordx4 v[64:67], v91, s[44:47], s54 offen sc1
buffer_store_dwordx4 v[68:71], v92, s[44:47], s54 offen sc1
s_endpgm
Ct:
v_lshrrev_b32 v156, 5, v80
v_mul_u32_u24 v156, 96, v156
v_add_u32 v156, v80, v156
Dt:
s_mov_b32 s60, 4096
buffer_load_dwordx4 v[124:127], v156, s[36:39], s60 offen sc1
s_add_u32 s60, s60, 4096
buffer_load_dwordx4 v[128:131], v156, s[36:39], s60 offen sc1
s_add_u32 s60, s60, 4096
buffer_load_dwordx4 v[132:135], v156, s[36:39], s60 offen sc1
s_add_u32 s60, s60, 4096
buffer_load_dwordx4 v[136:139], v156, s[36:39], s60 offen sc1
s_add_u32 s60, s60, 4096
buffer_load_dwordx4 v[140:143], v156, s[36:39], s60 offen sc1
s_add_u32 s60, s60, 4096
buffer_load_dwordx4 v[144:147], v156, s[36:39], s60 offen sc1
s_add_u32 s60, s60, 4096
buffer_load_dwordx4 v[148:151], v156, s[36:39], s60 offen sc1
s_add_u32 s60, s60, 4096
buffer_load_dwordx4 v[152:155], v156, s[36:39], s60 offen sc1
s_add_u32 s60, s60, 4096
s_mov_b64 s[66:67], 0
s_waitcnt vmcnt(0)
v_sub_u32 v124, v124, v72
v_xor_b32 v125, v125, v73
v_cmp_lt_u64 vcc, s[62:63], v[124:125]
s_or_b64 s[66:67], s[66:67], vcc
v_sub_u32 v126, v126, v72
v_xor_b32 v127, v127, v73
v_cmp_lt_u64 vcc, s[62:63], v[126:127]
s_or_b64 s[66:67], s[66:67], vcc
v_sub_u32 v128, v128, v72
v_xor_b32 v129, v129, v73
v_cmp_lt_u64 vcc, s[62:63], v[128:129]
s_or_b64 s[66:67], s[66:67], vcc
v_sub_u32 v130, v130, v72
v_xor_b32 v131, v131, v73
v_cmp_lt_u64 vcc, s[62:63], v[130:131]
s_or_b64 s[66:67], s[66:67], vcc
v_sub_u32 v132, v132, v72
v_xor_b32 v133, v133, v73
v_cmp_lt_u64 vcc, s[62:63], v[132:133]
s_or_b64 s[66:67], s[66:67], vcc
v_sub_u32 v134, v134, v72
v_xor_b32 v135, v135, v73
v_cmp_lt_u64 vcc, s[62:63], v[134:135]
s_or_b64 s[66:67], s[66:67], vcc
v_sub_u32 v136, v136, v72
v_xor_b32 v137, v137, v73
v_cmp_lt_u64 vcc, s[62:63], v[136:137]
s_or_b64 s[66:67], s[66:67], vcc
v_sub_u32 v138, v138, v72
v_xor_b32 v139, v139, v73
v_cmp_lt_u64 vcc, s[62:63], v[138:139]
s_or_b64 s[66:67], s[66:67], vcc
v_sub_u32 v140, v140, v72
v_xor_b32 v141, v141, v73
v_cmp_lt_u64 vcc, s[62:63], v[140:141]
s_or_b64 s[66:67], s[66:67], vcc
v_sub_u32 v142, v142, v72
v_xor_b32 v143, v143, v73
v_cmp_lt_u64 vcc, s[62:63], v[142:143]
s_or_b64 s[66:67], s[66:67], vcc
v_sub_u32 v144, v144, v72
v_xor_b32 v145, v145, v73
v_cmp_lt_u64 vcc, s[62:63], v[144:145]
s_or_b64 s[66:67], s[66:67], vcc
v_sub_u32 v146, v146, v72
v_xor_b32 v147, v147, v73
v_cmp_lt_u64 vcc, s[62:63], v[146:147]
s_or_b64 s[66:67], s[66:67], vcc
v_sub_u32 v148, v148, v72
v_xor_b32 v149, v149, v73
v_cmp_lt_u64 vcc, s[62:63], v[148:149]
s_or_b64 s[66:67], s[66:67], vcc
v_sub_u32 v150, v150, v72
v_xor_b32 v151, v151, v73
v_cmp_lt_u64 vcc, s[62:63], v[150:151]
s_or_b64 s[66:67], s[66:67], vcc
v_sub_u32 v152, v152, v72
v_xor_b32 v153, v153, v73
v_cmp_lt_u64 vcc, s[62:63], v[152:153]
s_or_b64 s[66:67], s[66:67], vcc
v_sub_u32 v154, v154, v72
v_xor_b32 v155, v155, v73
v_cmp_lt_u64 vcc, s[62:63], v[154:155]
s_or_b64 s[66:67], s[66:67], vcc
s_cbranch_scc0 Et
s_cmp_eq_u32 s61, 0
s_cbranch_scc1 Et
s_sub_u32 s61, s61, 1
s_sleep 64
s_branch Dt
Et:
buffer_store_dword v73, v81, s[36:39], 0 offen sc1
s_branch Bt
Lfin:
.purgem mav
.purgem Ta
.purgem mva
.purgem mvv
.purgem Tv
.purgem R
.purgem D
.purgem D2
.purgem E
)"
    : "+{v[0:7]}"(v0), "+{v[80:95]}"(c0), "+{v[96:111]}"(c1), "+{v[112:127]}"(c2), "+{s[36:51]}"(s0), "+{s[52:59]}"(s1)
    :
    : "v8", "v9", "v10", "v11", "v12", "v13", "v14", "v15", "v16", "v17", "v18", "v19", "v20", "v21", "v22", "v23", "v24", "v25", "v26", "v27", "v28", "v29", "v30", "v31", "v32", "v33", "v34", "v35", "v36", "v37", "v38", "v39", "v40", "v41", "v42", "v43", "v44", "v45", "v46", "v47", "v48", "v49", "v50", "v51", "v52", "v53", "v54", "v55", "v56", "v57", "v58", "v59", "v60", "v61", "v62", "v63", "v64", "v65", "v66", "v67", "v68", "v69", "v70", "v71", "v72", "v73", "v74", "v75", "v76", "v77", "v78", "v79", "v128", "v129", "v130", "v131", "v132", "v133", "v134", "v135", "v136", "v137", "v138", "v139", "v140", "v141", "v142", "v143", "v144", "v145", "v146", "v147", "v148", "v149", "v150", "v151", "v152", "v153", "v154", "v155", "v156", "v157", "v158", "v159", "v160", "v161", "v162", "v163", "v164", "v165", "v166", "v167", "v168", "v169", "v170", "v171", "v172", "v173", "v174", "v175", "v176", "v177", "v178", "v179", "v180", "v181", "v182", "v183", "v184", "v185", "v186", "v187", "v188", "v189", "v190", "v191", "v192", "v193", "v194", "v195", "v196", "v197", "v198", "v199", "v200", "v201", "v202", "v203", "v204", "v205", "v206", "v207", "v208", "v209", "v210", "v211", "v212", "v213", "v214", "v215", "v216", "v217", "v218", "v219", "v220", "v221", "v222", "v223", "v224", "v225", "v226", "v227", "v228", "v229", "v230", "v231", "v232", "v233", "v234", "v235", "v236", "v237", "v238", "v239", "v240", "v241", "v242", "v243", "v244", "v245", "v246", "v247", "v248", "v249", "v250", "v251", "v252", "v253", "v254", "v255", "a0", "a1", "a2", "a3", "a4", "a5", "a6", "a7", "a8", "a9", "a10", "a11", "a12", "a13", "a14", "a15", "a16", "a17", "a18", "a19", "a20", "a21", "a22", "a23", "a24", "a25", "a26", "a27", "a28", "a29", "a30", "a31", "a32", "a33", "a34", "a35", "a36", "a37", "a38", "a39", "a40", "a41", "a42", "a43", "a44", "a45", "a46", "a47", "a48", "a49", "a50", "a51", "a52", "a53", "a54", "a55", "a56", "a57", "a58", "a59", "a60", "a61", "a62", "a63", "a64", "a65", "a66", "a67", "a68", "a69", "a70", "a71", "a72", "a73", "a74", "a75", "a76", "a77", "a78", "a79", "a80", "a81", "a82", "a83", "a84", "a85", "a86", "a87", "a88", "a89", "a90", "a91", "a92", "a93", "a94", "a95", "a96", "a97", "a98", "a99", "a100", "a101", "a102", "a103", "a104", "a105", "a106", "a107", "a108", "a109", "a110", "a111", "a112", "a113", "a114", "a115", "a116", "a117", "a118", "a119", "a120", "a121", "a122", "a123", "a124", "a125", "a126", "a127", "a128", "a129", "a130", "a131", "a132", "a133", "a134", "a135", "a136", "a137", "a138", "a139", "a140", "a141", "a142", "a143", "a144", "a145", "a146", "a147", "a148", "a149", "a150", "a151", "a152", "a153", "a154", "a155", "a156", "a157", "a158", "a159", "a160", "a161", "a162", "a163", "a164", "a165", "a166", "a167", "a168", "a169", "a170", "a171", "a172", "a173", "a174", "a175", "a176", "a177", "a178", "a179", "a180", "a181", "a182", "a183", "a184", "a185", "a186", "a187", "a188", "a189", "a190", "a191", "a192", "a193", "a194", "a195", "a196", "a197", "a198", "a199", "a200", "a201", "a202", "a203", "a204", "a205", "a206", "a207", "a208", "a209", "a210", "a211", "a212", "a213", "a214", "a215", "a216", "a217", "a218", "a219", "a220", "a221", "a222", "a223", "a224", "a225", "a226", "a227", "a228", "a229", "a230", "a231", "a232", "a233", "a234", "a235", "a236", "a237", "a238", "a239", "a240", "a241", "a242", "a243", "a244", "a245", "a246", "a247", "a248", "a249", "a250", "a251", "a252", "a253", "a254", "a255", "s60", "s61", "s62", "s63", "s64", "s65", "s66", "s67", "s68", "s69", "s70", "s71", "s72", "s73", "s74", "s75", "s76", "s77", "s78", "s79", "s80", "s81", "s82", "s83", "s84", "s85", "s86", "s87", "s88", "s89", "s90", "s91", "s92", "s93", "s94", "s95", "s96", "s97", "s98", "s99", "vcc", "scc", "memory");
}

extern "C" void kernel_launch(void* const* d_in, const int* in_sizes, int n_in, void* d_out, int out_size,
                              void* d_ws, size_t ws_size, hipStream_t stream) {
  Args m{};
  m.x = (const u16*)d_in[0]; m.wqkv = (const u16*)d_in[1]; m.wo = (const u16*)d_in[2]; m.w1 = (const u16*)d_in[4];
  m.b1 = (const u16*)d_in[5]; m.w2 = (const u16*)d_in[6]; m.b2 = (const u16*)d_in[7];
  m.out = (u16*)d_out; m.ws = (char*)d_ws;
  const unsigned tab[64] = {0x22022000u, 0x3814b010u, 0x26062404u, 0x781cf018u, 0x2a0a2808u, 0xb8943090u, 0x2e0e2c0cu, 0xf89c7098u, 0x62426040u, 0x3954b150u, 0x66466444u, 0x795cf158u, 0x6a4a6848u, 0xb9d431d0u, 0x6e4e6c4cu, 0xf9dc71d8u, 0xa282a080u, 0x3a15b211u, 0xa686a484u, 0x7a1df219u, 0xaa8aa888u, 0xba953291u, 0xae8eac8cu, 0xfa9d7299u, 0xe2c2e0c0u, 0x3b55b351u, 0xe6c6e4c4u, 0x7b5df359u, 0xeacae8c8u, 0xbbd533d1u, 0xeeceecccu, 0xfbdd73d9u, 0x23032101u, 0x7c16b412u, 0x27072505u, 0x3c1ef41au, 0x2b0b2909u, 0xfc963492u, 0x2f0f2d0du, 0xbc9e749au, 0x63436141u, 0x7d56b552u, 0x67476545u, 0x3d5ef55au, 0x6b4b6949u, 0xfdd635d2u, 0x6f4f6d4du, 0xbdde75dau, 0xa383a181u, 0x7e17b613u, 0xa787a585u, 0x3e1ff61bu, 0xab8ba989u, 0xfe973693u, 0xaf8fad8du, 0xbe9f769bu, 0xe3c3e1c1u, 0x7f57b753u, 0xe7c7e5c5u, 0x3f5ff75bu, 0xebcbe9c9u, 0xffd737d3u, 0xefcfedcdu, 0xbfdf77dbu};
  for (int i = 0; i < 64; ++i) m.tab[i] = tab[i];
  (void)hipFuncSetAttribute((const void*)df_kernel, hipFuncAttributeMaxDynamicSharedMemorySize, LDS_BYTES);
  hipLaunchKernelGGL(df_kernel, dim3(256), dim3(256), LDS_BYTES, stream, m);
}
```
